```python
import torch

def _softmax(x, axis=-1):
    return torch.softmax(x, dim=axis)
def _gelu(x):
    return torch.nn.functional.gelu(x, approximate="tanh")
def _silu(x):
    return torch.nn.functional.silu(x)
def _sigmoid(x):
    return torch.sigmoid(x)
def _layernorm(x, axis=-1, eps=1e-5):
    m = x.mean(dim=axis, keepdim=True)
    v = x.var(dim=axis, keepdim=True, unbiased=False)
    return (x - m) / torch.sqrt(v + eps)
def _scatter_add(tgt, idx, src, axis=0):
    return tgt.clone().index_add_(axis, idx, src)
def _segment_sum(src, seg, n):
    out = torch.zeros((n,) + src.shape[1:], dtype=src.dtype)
    return out.index_add_(0, seg, src)
def _pad(x, pw):
    flat = tuple(v for pair in reversed(pw) for v in pair)
    return torch.nn.functional.pad(x, flat)

kernel_name = 'recurrent_scan__mha_block_cached__moe_arch_mod__prompt'

def setup_inputs():

    _w = (lambda a: (lambda t: t.to(torch.bfloat16) if t.is_floating_point() else t)(torch.as_tensor(a)))
    def _A_setup():
        B, Sq, D = 1, 2048, 640
        H, Sk, Dh = 32, 2048, 128
        return {
            "x": _w((lambda *s: torch.randn(*s, dtype=torch.bfloat16) * 0.05)(B, Sq, D)),
            "k_cache": _w((lambda *s: torch.randn(*s, dtype=torch.bfloat16) * 1.6)(B, H, Sk, Dh)),
            "v_cache": _w((lambda *s: torch.randn(*s, dtype=torch.bfloat16) * 0.2)(B, H, Sk, Dh)),
            "wq": _w((lambda *s: torch.randn(*s, dtype=torch.bfloat16) * 1.6)(D, H * Dh) * (D ** -0.5)),
            "wo": _w((lambda *s: torch.randn(*s, dtype=torch.bfloat16) * 0.2)(H * Dh, D) * ((H * Dh) ** -0.5)),
        }
    _A_in = _A_setup()

    def _B_setup():
        B, S, D, F, C = 1, 2048, 640, 1728, 8
        return {
            "x": _w((lambda *s: torch.randn(*s, dtype=torch.bfloat16) * 0.05)(B, S, D)),
            "w_r": _w((lambda *s: torch.randn(*s, dtype=torch.bfloat16) * 0.2)(D)),
            "w_gate": _w((lambda *s: torch.randn(*s, dtype=torch.bfloat16) * 0.1)(D, F)),
            "w_up": _w((lambda *s: torch.randn(*s, dtype=torch.bfloat16) * 0.1)(D, F)),
            "w_down": _w((lambda *s: torch.randn(*s, dtype=torch.bfloat16) * 0.1)(F, D)),
        }
    _B_in = _B_setup()

    return {
        "A_x": _A_in["x"],
        "A_k_cache": _A_in["k_cache"],
        "A_v_cache": _A_in["v_cache"],
        "A_wq": _A_in["wq"],
        "A_wo": _A_in["wo"],
        "B_x": _B_in["x"],
        "B_w_r": _B_in["w_r"],
        "B_w_gate": _B_in["w_gate"],
        "B_w_up": _B_in["w_up"],
        "B_w_down": _B_in["w_down"],
    }

def reference(A_x, A_k_cache, A_v_cache, A_wq, A_wo, B_x, B_w_r, B_w_gate, B_w_up, B_w_down):
    def _A_ref(x, k_cache, v_cache, wq, wo):
        B, Sq, D = x.shape
        H, Dh = 32, 128
        q = (lambda x, *d: x.permute(*d))((x @ wq).reshape(B, Sq, H, Dh), 0, 2, 1, 3)
        scores = torch.einsum("bhqd,bhkd->bhqk", q, k_cache) * (Dh ** -0.5)
        p = _softmax(scores, axis=-1)
        o = torch.einsum("bhqk,bhkd->bhqd", p, v_cache)
        return (lambda x, *d: x.permute(*d))(o, 0, 2, 1, 3).reshape(B, Sq, H * Dh) @ wo

    def _B_ref(x, w_r, w_gate, w_up, w_down):
        B, S, D = x.shape
        C = 8
        r = x @ w_r
        idx = torch.argsort(-r, axis=-1)[:, :C]
        idx_e = (lambda x, n, axis: torch.repeat_interleave(x, n, dim=axis))(idx.reshape(B, C, 1), D, axis=2)
        x_sel = (lambda x, i, axis: torch.take_along_dim(x, i, dim=axis))(x, idx_e, axis=1)
        g = _silu(x_sel @ w_gate)
        u = x_sel @ w_up
        y = (g * u) @ w_down
        r_sel = _sigmoid((lambda x, i, axis: torch.take_along_dim(x, i, dim=axis))(r, idx, axis=1))
        y = y * r_sel.reshape(B, C, 1)
        b_off = (torch.arange(B) * S).reshape(B, 1)
        flat_idx = (idx + b_off).reshape(B * C)
        zero = (x * 0.0).reshape(B * S, D)
        upd = _scatter_add(zero, flat_idx, y.reshape(B * C, D), axis=0)
        return x + upd.reshape(B, S, D)

    x = A_x
    xn = _layernorm(x, axis=-1)
    s = _A_ref(xn, A_k_cache, A_v_cache, A_wq, A_wo)
    g = _silu(_B_ref(xn, B_w_r, B_w_gate, B_w_up, B_w_down))
    return x + s * g
```

```cpp
#include <hip/hip_runtime.h>
#include <stdint.h>

using f32x4  = __attribute__((ext_vector_type(4))) float;
using u32x4  = __attribute__((ext_vector_type(4))) unsigned;
using u32x2  = __attribute__((ext_vector_type(2))) unsigned;

constexpr int SEQ = 2048, DM = 640, NH = 32, DH = 128, HD = NH * DH, FF = 1728, TOPC = 8;

constexpr unsigned WS_FO  = 0;
constexpr unsigned WS_EP  = 32768;
constexpr unsigned WS_G1  = 36864;
constexpr unsigned WS_G2  = 53248;
constexpr unsigned WS_FH  = 69632;
constexpr unsigned WS_FY  = 73728;
constexpr unsigned WS_FG  = 77824;
constexpr unsigned WS_FR  = 81920;
constexpr unsigned WS_RK  = 86016;
constexpr unsigned WS_XN  = 131072;
constexpr unsigned WS_OF  = WS_XN + SEQ * DM * 2;
constexpr unsigned WS_H   = WS_OF + (18u << 20);
constexpr unsigned WS_Y   = WS_H + 32768;
constexpr unsigned WS_P   = WS_Y + 1048576;
constexpr unsigned WS_SPAN = 64u << 20;

#define CLOB_V "v0","v1","v2","v3","v4","v5","v6","v7","v8","v9","v10","v11","v12","v13","v14","v15","v16","v17","v18","v19","v20","v21","v22","v23","v24","v25","v26","v27","v28","v29","v30","v31","v32","v33","v34","v35","v36","v37","v38","v39","v40","v41","v42","v43","v44","v45","v46","v47","v48","v49","v50","v51","v52","v53","v54","v55","v56","v57","v58","v59","v60","v61","v62","v63","v64","v65","v66","v67","v68","v69","v70","v71","v72","v73","v74","v75","v76","v77","v78","v79","v80","v81","v82","v83","v84","v85","v86","v87","v88","v89","v90","v91","v92","v93","v94","v95","v96","v97","v98","v99","v100","v101","v102","v103","v104","v105","v106","v107","v108","v109","v110","v111","v112","v113","v114","v115","v116","v117","v118","v119","v120","v121","v122","v123","v124","v125","v126","v127","v128","v129","v130","v131","v132","v133","v134","v135","v136","v137","v138","v139","v140","v141","v142","v143","v144","v145","v146","v147","v148","v149","v150","v151","v152","v153","v154","v155","v156","v157","v158","v159","v160","v161","v162","v163","v164","v165","v166","v167","v168","v169","v170","v171","v172","v173","v174","v175","v176","v177","v178","v179","v180","v181","v182","v183","v184","v185","v186","v187","v188","v189","v190","v191","v192","v193","v194","v195","v196","v197","v198","v199","v200","v201","v202","v203","v204","v205","v206","v207","v208","v209","v210","v211","v212","v213","v214","v215","v216","v217","v218","v219","v220","v221","v222","v223","v224","v225","v226","v227","v228","v229","v230","v231","v232","v233","v234","v235","v236","v237","v238","v239","v240","v241","v242","v243","v244","v245","v246","v247","v248","v249","v250","v251","v252","v253","v254","v255"
#define CLOB_A "a0","a1","a2","a3","a4","a5","a6","a7","a8","a9","a10","a11","a12","a13","a14","a15","a16","a17","a18","a19","a20","a21","a22","a23","a24","a25","a26","a27","a28","a29","a30","a31","a32","a33","a34","a35","a36","a37","a38","a39","a40","a41","a42","a43","a44","a45","a46","a47","a48","a49","a50","a51","a52","a53","a54","a55","a56","a57","a58","a59","a60","a61","a62","a63","a64","a65","a66","a67","a68","a69","a70","a71","a72","a73","a74","a75","a76","a77","a78","a79","a80","a81","a82","a83","a84","a85","a86","a87","a88","a89","a90","a91","a92","a93","a94","a95","a96","a97","a98","a99","a100","a101","a102","a103","a104","a105","a106","a107","a108","a109","a110","a111","a112","a113","a114","a115","a116","a117","a118","a119","a120","a121","a122","a123","a124","a125","a126","a127","a128","a129","a130","a131","a132","a133","a134","a135","a136","a137","a138","a139","a140","a141","a142","a143","a144","a145","a146","a147","a148","a149","a150","a151","a152","a153","a154","a155","a156","a157","a158","a159","a160","a161","a162","a163","a164","a165","a166","a167","a168","a169","a170","a171","a172","a173","a174","a175","a176","a177","a178","a179","a180","a181","a182","a183","a184","a185","a186","a187","a188","a189","a190","a191","a192","a193","a194","a195","a196","a197","a198","a199","a200","a201","a202","a203","a204","a205","a206","a207","a208","a209","a210","a211","a212","a213","a214","a215","a216","a217","a218","a219","a220","a221","a222","a223","a224","a225","a226","a227","a228","a229","a230","a231","a232","a233","a234","a235","a236","a237","a238","a239","a240","a241","a242","a243","a244","a245","a246","a247","a248","a249","a250","a251","a252","a253","a254","a255"
#define CLOB_S "s8","s9","s10","s11","s12","s13","s14","s15","s16","s17","s18","s19","s20","s21","s22","s23","s24","s25","s26","s27","s28","s29","s30","s31","s32","s33","s34","s35","s36","s37","s38","s39","s40","s41","s42","s43","s44","s45","s46","s47","s48","s49","s50","s51","s52","s53","s54","s55","s56","s57","s58","s59","s60","s61","s62","s63","s64","s65","s66","s67","s68","s69","s70","s71","s72","s73","s74","s75","s76","s77","s78","s79","s80","s81","s82","s83","s84","s85","s86","s87","s88","s89","s90","s91","s92","s93","s94","s95","s96","s97","s98","s99"
__global__ __launch_bounds__(256, 1) __attribute__((aligned(4096))) void ka_fused(const void* wq, const void* x, void* ws, const void* Kc, const void* wr, const void* Vc, const void* wo, const void* wg, const void* wu, const void* wd, void* out) {
  extern __shared__ __attribute__((aligned(16))) char lds[];
  const unsigned w = __builtin_amdgcn_readfirstlane(threadIdx.x >> 6);
  const unsigned slds = __builtin_amdgcn_readfirstlane((unsigned)(size_t)lds);
  asm volatile(
R"ASM(
.macro MM a, b, c, d:vararg
v_mfma_f32_16x16x32_bf16 \a, \b, \c, \d
.endm
.macro TR a, b:vararg
ds_read_b64_tr_b16 \a, \b
.endm
.macro CV a, b, c:vararg
v_cvt_pk_bf16_f32 \a, \b, \c
.endm
.macro AR a, b:vararg
v_accvgpr_read_b32 \a, \b
.endm
.macro AW a, b:vararg
v_accvgpr_write_b32 \a, \b
.endm
.macro EX a, b:vararg
v_exp_f32 \a, \b
.endm
.macro RD a, b:vararg
ds_read_b128 \a, \b
.endm
s_branch LPG0_%=
.p2align 6
LPG0_%=:
s_load_dwordx2 s[16:17], %[ka], 0
s_load_dwordx2 s[32:33], %[ka], 8
s_load_dwordx2 s[20:21], %[ka], 16
s_load_dwordx2 s[8:9], %[ka], 24
s_load_dwordx2 s[12:13], %[ka], 40
s_load_dwordx2 s[24:25], %[ka], 48
s_load_dwordx2 s[44:45], %[ka], 32
s_load_dwordx2 s[28:29], %[ka], 72
s_load_dwordx2 s[40:41], %[ka], 80
s_lshl_b32 s77, %[swave], 2
s_and_b32 s77, s77, 8
s_add_u32 s77, s77, 56
s_load_dwordx2 s[36:37], %[ka], s77
v_mbcnt_lo_u32_b32 v127, -1, 0
v_mbcnt_hi_u32_b32 v127, -1, v127
v_lshlrev_b32 v248, 4, v127
s_lshr_b32 s77, %[sb], 3
s_lshl_b32 s77, s77, 2
s_add_u32 s77, s77, %[swave]
s_lshl_b32 s77, s77, 10
v_add_u32 v118, s77, v248
s_mov_b32 s77, LKEND_ka-LPC_%=-16
v_min_u32 v118, s77, v118
s_getpc_b64 s[98:99]
LPC_%=:
global_load_dwordx4 a[252:255], v118, s[98:99]
s_and_b32 s77, %[sb], 7
s_lshl_b32 s77, s77, 2
s_bfe_u32 s84, %[sb], 131075
s_add_u32 s49, s77, s84
s_lshr_b32 s50, %[sb], 5
s_lshl_b32 s76, %[sb], 2
s_add_u32 s76, s76, %[swave]
v_lshrrev_b32 v180, 5, v127
s_mul_i32 s82, s76, 2560
v_and_b32 v182, 496, v248
v_mul_u32_u24 v181, 1280, v180
v_add_u32 v168, v181, v182
v_lshrrev_b32 v210, 1, v182
v_add_u32 v181, v181, v210
s_lshl_b32 s77, %[swave], 12
s_add_u32 s65, s77, %[slds]
s_lshl_b32 s89, %[swave], 17
v_bfe_u32 v210, v127, 2, 3
v_and_b32 v212, 3, v127
v_lshlrev_b32 v213, 4, v212
v_lshl_add_u32 v213, v180, 6, v213
v_lshrrev_b32 v253, 3, v127
v_lshlrev_b32 v253, 13, v253
v_and_b32 v254, 112, v248
v_or_b32 v253, v253, v254
v_and_b32 v254, 16, v127
v_lshlrev_b32 v254, 2, v254
v_xor_b32 v253, v253, v254
v_add_u32 v254, 128, v253
s_mul_i32 s96, %[swave], 4112
s_add_u32 s96, s96, %[slds]
s_lshl_b32 s77, s50, 2
s_add_u32 s77, s77, %[swave]
s_mul_i32 s68, s77, 81920
s_and_b32 s62, %[sb], 1
s_mul_i32 s63, s62, 40960
s_add_u32 s68, s68, s63
s_lshl_b32 s63, s63, 1
s_mul_i32 s83, s77, 560128
s_add_u32 s83, s83, 2752512
s_lshl_b32 s93, s77, 9
s_lshl_b32 s94, %[swave], 13
s_add_u32 s94, s94, %[slds]
v_lshrrev_b32 v211, 3, v127
v_mul_u32_u24 v220, 1280, v211
v_lshrrev_b32 v212, 4, v127
v_and_b32 v211, 7, v127
v_xor_b32 v211, v211, v212
v_lshl_add_u32 v220, v211, 4, v220
v_xor_b32 v221, 64, v220
s_bitcmp1_b32 s84, 0
s_mov_b32 s77, 0x77552200
s_cselect_b32 s77, 0x77552200, s77
s_lshl_b32 s91, s50, 2
s_lshr_b32 s77, s77, s91
s_and_b32 s77, s77, 15
s_lshl_b32 s86, s77, 7
s_lshl_b32 s85, s77, 19
v_and_b32 v211, 15, v127
v_bfe_u32 v222, v127, 1, 3
v_xor_b32 v222, v222, v212
v_lshlrev_b32 v222, 4, v222
v_lshl_add_u32 v222, v211, 7, v222
v_xor_b32 v223, 64, v222
v_add_u32 v222, s94, v222
v_add_u32 v223, s94, v223
v_and_b32 v214, 1, v212
v_lshlrev_b32 v215, 5, v214
v_xor_b32 v215, v215, v213
v_lshl_add_u32 v235, v210, 8, v215
v_add_u32 v235, 128, v235
v_and_b32 v234, 1, v127
v_mul_u32_u24 v234, 43690, v234
v_add_u32 v234, 17476, v234
v_bfe_u32 v215, v127, 2, 2
v_and_b32 v216, 3, v127
v_lshlrev_b32 v252, 7, v215
v_lshl_add_u32 v252, v216, 4, v252
v_and_b32 v251, 8, v127
v_lshl_add_u32 v252, v251, 3, v252
v_mul_u32_u24 v251, 2056, v212
s_xor_b32 s77, %[swave], 0
s_and_b32 s91, s77, 1
s_lshl_b32 s91, s91, 6
s_lshr_b32 s77, s77, 1
s_lshl_b32 s77, s77, 10
s_add_u32 s77, s77, %[slds]
s_add_u32 s77, s77, 65536
v_xor_b32 v192, s91, v252
v_add3_u32 v192, s77, v192, v251
v_add_u32 v196, 49344, v192
s_xor_b32 s77, %[swave], 1
s_and_b32 s91, s77, 1
s_lshl_b32 s91, s91, 6
s_lshr_b32 s77, s77, 1
s_lshl_b32 s77, s77, 10
s_add_u32 s77, s77, %[slds]
s_add_u32 s77, s77, 65536
v_xor_b32 v193, s91, v252
v_add3_u32 v193, s77, v193, v251
v_add_u32 v197, 49344, v193
s_xor_b32 s77, %[swave], 2
s_and_b32 s91, s77, 1
s_lshl_b32 s91, s91, 6
s_lshr_b32 s77, s77, 1
s_lshl_b32 s77, s77, 10
s_add_u32 s77, s77, %[slds]
s_add_u32 s77, s77, 65536
v_xor_b32 v194, s91, v252
v_add3_u32 v194, s77, v194, v251
v_add_u32 v198, 49344, v194
s_xor_b32 s77, %[swave], 3
s_and_b32 s91, s77, 1
s_lshl_b32 s91, s91, 6
s_lshr_b32 s77, s77, 1
s_lshl_b32 s77, s77, 10
s_add_u32 s77, s77, %[slds]
s_add_u32 s77, s77, 65536
v_xor_b32 v195, s91, v252
v_add3_u32 v195, s77, v195, v251
v_add_u32 v199, 49344, v195
v_mov_b32 v236, 0x3f803f80
v_mov_b32 v184, 0
v_mov_b32 v237, 0x3f803f80
v_mov_b32 v185, 0
v_mov_b32 v238, 0x3f803f80
v_mov_b32 v188, 0
v_mov_b32 v239, 0x3f803f80
v_mov_b32 v189, 0
s_lshl_b32 s90, s49, 8
s_waitcnt lgkmcnt(0)
s_add_u32 s16, s16, s90
s_addc_u32 s17, s17, 0
s_sub_u32 s18, 5242880, s90
s_mov_b32 s19, 131072
s_mov_b32 s34, 2621440
s_mov_b32 s35, 131072
s_mov_b32 s22, 67108864
s_mov_b32 s23, 131072
s_add_u32 s87, s89, s85
s_add_u32 s85, s85, 524288
s_cmp_ge_u32 s85, 5242880
s_cselect_b32 s85, 0, s85
s_add_u32 m0, s96, 65536
s_nop 0
buffer_load_dwordx4 v253, s[16:19], s87 offen offset:0 lds
s_add_u32 m0, s96, 66560
s_nop 0
buffer_load_dwordx4 v254, s[16:19], s87 offen offset:0 lds
s_add_u32 s87, s87, 65536
s_add_u32 m0, s96, 67592
s_nop 0
buffer_load_dwordx4 v253, s[16:19], s87 offen offset:0 lds
s_add_u32 m0, s96, 68616
s_nop 0
buffer_load_dwordx4 v254, s[16:19], s87 offen offset:0 lds
s_add_u32 s88, s68, s86
s_add_u32 s86, s86, 128
s_cmp_ge_u32 s86, 1280
s_cselect_b32 s86, 0, s86
s_add_u32 m0, s94, 0
s_add_u32 s95, s88, 0
buffer_load_dwordx4 v220, s[32:35], s95 offen offset:0 lds
s_add_u32 s95, s88, 9216
buffer_load_dwordx4 v221, s[32:35], s95 offen offset:1024 lds
s_add_u32 s95, s88, 18432
buffer_load_dwordx4 v220, s[32:35], s95 offen offset:2048 lds
s_add_u32 s95, s88, 27648
buffer_load_dwordx4 v221, s[32:35], s95 offen offset:3072 lds
s_mov_b32 s66, 32768
buffer_load_dword v169, off, s[20:23], s66 sc1
buffer_load_dwordx4 v[128:131], v168, s[32:35], s82 offen offset:0
buffer_load_dwordx4 v[132:135], v168, s[32:35], s82 offen offset:512
buffer_load_dwordx2 v[136:137], v181, s[32:35], s82 offen offset:1024
s_add_u32 s82, s82, 131072
s_sub_u32 s88, s88, s63
s_add_u32 m0, s94, 4096
s_add_u32 s95, s88, 40960
buffer_load_dwordx4 v220, s[32:35], s95 offen offset:0 lds
s_add_u32 s95, s88, 50176
buffer_load_dwordx4 v221, s[32:35], s95 offen offset:1024 lds
s_add_u32 s95, s88, 59392
buffer_load_dwordx4 v220, s[32:35], s95 offen offset:2048 lds
s_add_u32 s95, s88, 68608
buffer_load_dwordx4 v221, s[32:35], s95 offen offset:3072 lds
s_getpc_b64 s[98:99]
LTA0_%=:
s_min_u32 s73, %[swave], 3
s_lshl_b32 s73, s73, 12
s_add_u32 s73, s73, LPG1_%=-LTA0_%=
s_add_u32 s98, s98, s73
s_addc_u32 s99, s99, 0
s_mov_b64 exec, 0
s_setpc_b64 s[98:99]
LTR0_%=:
s_mov_b64 exec, -1
s_add_u32 s87, s89, s85
s_add_u32 s85, s85, 524288
s_cmp_ge_u32 s85, 5242880
s_cselect_b32 s85, 0, s85
s_add_u32 m0, s96, 81984
s_nop 0
buffer_load_dwordx4 v253, s[16:19], s87 offen offset:0 lds
s_add_u32 m0, s96, 83008
s_nop 0
buffer_load_dwordx4 v254, s[16:19], s87 offen offset:0 lds
s_add_u32 s87, s87, 65536
s_add_u32 m0, s96, 84040
s_nop 0
buffer_load_dwordx4 v253, s[16:19], s87 offen offset:0 lds
s_add_u32 m0, s96, 85064
s_nop 0
buffer_load_dwordx4 v254, s[16:19], s87 offen offset:0 lds
s_add_u32 s88, s68, s86
s_add_u32 s86, s86, 128
s_cmp_ge_u32 s86, 1280
s_cselect_b32 s86, 0, s86
s_add_u32 m0, s94, 32768
s_add_u32 s95, s88, 0
buffer_load_dwordx4 v220, s[32:35], s95 offen offset:0 lds
s_add_u32 s95, s88, 9216
buffer_load_dwordx4 v221, s[32:35], s95 offen offset:1024 lds
s_add_u32 s95, s88, 18432
buffer_load_dwordx4 v220, s[32:35], s95 offen offset:2048 lds
s_add_u32 s95, s88, 27648
buffer_load_dwordx4 v221, s[32:35], s95 offen offset:3072 lds
s_sub_u32 s88, s88, s63
s_add_u32 m0, s94, 36864
s_add_u32 s95, s88, 40960
buffer_load_dwordx4 v220, s[32:35], s95 offen offset:0 lds
s_add_u32 s95, s88, 50176
buffer_load_dwordx4 v221, s[32:35], s95 offen offset:1024 lds
s_add_u32 s95, s88, 59392
buffer_load_dwordx4 v220, s[32:35], s95 offen offset:2048 lds
s_add_u32 s95, s88, 68608
buffer_load_dwordx4 v221, s[32:35], s95 offen offset:3072 lds
s_waitcnt vmcnt(20)
RD a[128:131],v222 offset:0
RD a[132:135],v223 offset:0
RD a[136:139],v222 offset:2048
RD a[140:143],v223 offset:2048
s_barrier
TR a[0:1],v192 offset:0
TR a[2:3],v192 offset:512
TR a[4:5],v192 offset:8224
TR a[6:7],v192 offset:8736
TR a[8:9],v192 offset:8
TR a[10:11],v192 offset:520
TR a[12:13],v192 offset:8232
TR a[14:15],v192 offset:8744
TR a[16:17],v193 offset:0
TR a[18:19],v193 offset:512
TR a[20:21],v193 offset:8224
TR a[22:23],v193 offset:8736
TR a[24:25],v193 offset:8
TR a[26:27],v193 offset:520
TR a[28:29],v193 offset:8232
TR a[30:31],v193 offset:8744
s_waitcnt lgkmcnt(15)
s_waitcnt lgkmcnt(12)
MM v[0:3],a[0:3],a[128:131],0
s_add_u32 s87, s89, s85
TR a[32:33],v194 offset:0
TR a[34:35],v194 offset:512
MM v[0:3],a[4:7],a[132:135],v[0:3]
s_add_u32 s85, s85, 524288
TR a[36:37],v194 offset:8224
TR a[38:39],v194 offset:8736
MM v[8:11],a[4:7],a[140:143],0
s_waitcnt vmcnt(16)
TR a[40:41],v194 offset:8
TR a[42:43],v194 offset:520
MM v[8:11],a[0:3],a[136:139],v[8:11]
s_waitcnt lgkmcnt(14)
TR a[44:45],v194 offset:8232
TR a[46:47],v194 offset:8744
s_cmp_ge_u32 s85, 5242880
s_cselect_b32 s85, 0, s85
MM v[4:7],a[8:11],a[128:131],0
s_add_u32 m0, s96, 98432
TR a[48:49],v195 offset:0
TR a[50:51],v195 offset:512
v_readfirstlane_b32 s69, v169
MM v[4:7],a[12:15],a[132:135],v[4:7]
s_lshl_b32 s90, s76, 1
TR a[52:53],v195 offset:8224
TR a[54:55],v195 offset:8736
MM v[12:15],a[12:15],a[140:143],0
s_and_b32 s77, s90, 15
TR a[56:57],v195 offset:8
TR a[58:59],v195 offset:520
buffer_load_dwordx4 v253, s[16:19], s87 offen offset:0 lds
MM v[12:15],a[8:11],a[136:139],v[12:15]
s_add_u32 m0, s96, 99456
TR a[60:61],v195 offset:8232
TR a[62:63],v195 offset:8744
v_lshlrev_b32 v148, 16, v128
v_smfmac_f32_16x16x64_bf16 v[184:187], v[236:239], a[0:7], v234
s_lshl_b32 s77, s77, 1
v_and_b32 v149, 0xffff0000, v128
v_smfmac_f32_16x16x64_bf16 v[188:191], v[236:239], a[8:15], v234
s_waitcnt lgkmcnt(15)
v_lshlrev_b32 v150, 16, v129
MM v[32:35],a[16:19],a[128:131],0
s_bfe_u32 s91, s90, 65540
buffer_load_dwordx4 v254, s[16:19], s87 offen offset:0 lds
MM v[32:35],a[20:23],a[132:135],v[32:35]
s_add_u32 s87, s87, 65536
s_add_u32 m0, s96, 100488
v_and_b32 v151, 0xffff0000, v129
MM v[40:43],a[20:23],a[140:143],0
s_add_u32 s77, s77, s91
buffer_load_dwordx4 v253, s[16:19], s87 offen offset:0 lds
MM v[40:43],a[16:19],a[136:139],v[40:43]
s_add_u32 m0, s96, 101512
v_lshlrev_b32 v152, 16, v130
v_and_b32 v153, 0xffff0000, v130
MM v[36:39],a[24:27],a[128:131],0
s_and_b32 s91, s90, 0xffffffe0
v_lshlrev_b32 v154, 16, v131
v_and_b32 v155, 0xffff0000, v131
MM v[36:39],a[28:31],a[132:135],v[36:39]
s_add_u32 s77, s77, s91
v_lshlrev_b32 v156, 16, v132
v_and_b32 v157, 0xffff0000, v132
MM v[44:47],a[28:31],a[140:143],0
s_lshl_b32 s77, s77, 3
buffer_load_dwordx4 v254, s[16:19], s87 offen offset:0 lds
MM v[44:47],a[24:27],a[136:139],v[44:47]
s_waitcnt lgkmcnt(12)
s_waitcnt vmcnt(16)
RD a[144:147],v222 offset:4096
v_lshlrev_b32 v158, 16, v133
MM v[64:67],a[32:35],a[128:131],0
s_add_u32 s97, s77, 36864
RD a[148:151],v223 offset:4096
RD a[152:155],v222 offset:6144
MM v[64:67],a[36:39],a[132:135],v[64:67]
s_sub_u32 s79, 65535, s90
RD a[156:159],v223 offset:6144
v_and_b32 v159, 0xffff0000, v133
MM v[72:75],a[36:39],a[140:143],0
s_lshl_b32 s78, s90, 2
s_waitcnt vmcnt(12)
s_barrier
v_lshlrev_b32 v160, 16, v134
MM v[72:75],a[32:35],a[136:139],v[72:75]
s_waitcnt lgkmcnt(12)
global_load_dwordx4 v[138:141], v182, s[44:45] offset:0
MM v[68:71],a[40:43],a[128:131],0
s_add_u32 s78, s78, 53248
v_and_b32 v161, 0xffff0000, v134
v_lshlrev_b32 v162, 16, v135
MM v[68:71],a[44:47],a[132:135],v[68:71]
s_lshl_b32 s80, s76, 2
v_and_b32 v163, 0xffff0000, v135
v_lshlrev_b32 v164, 16, v136
MM v[76:79],a[44:47],a[140:143],0
s_add_u32 s80, s80, 81920
global_load_dwordx4 v[142:145], v182, s[44:45] offset:512
MM v[76:79],a[40:43],a[136:139],v[76:79]
s_waitcnt lgkmcnt(8)
v_lshrrev_b32 v182, 1, v182
v_and_b32 v165, 0xffff0000, v136
MM v[96:99],a[48:51],a[128:131],0
global_load_dwordx2 v[146:147], v182, s[44:45] offset:1024
MM v[96:99],a[52:55],a[132:135],v[96:99]
s_waitcnt lgkmcnt(0)
s_add_u32 s88, s68, s86
s_add_u32 s86, s86, 128
v_lshlrev_b32 v166, 16, v137
MM v[104:107],a[52:55],a[140:143],0
s_cmp_ge_u32 s86, 1280
s_cselect_b32 s86, 0, s86
s_add_u32 m0, s94, 0
MM v[104:107],a[48:51],a[136:139],v[104:107]
)ASM"
R"ASM(
s_add_u32 s95, s88, 0
buffer_load_dwordx4 v220, s[32:35], s95 offen offset:0 lds
MM v[100:103],a[56:59],a[128:131],0
s_add_u32 s95, s88, 9216
v_and_b32 v167, 0xffff0000, v137
s_add_u32 s69, s69, 1
MM v[100:103],a[60:63],a[132:135],v[100:103]
v_add_f32 v170, v148, v149
v_add_f32 v171, v150, v151
MM v[108:111],a[60:63],a[140:143],0
buffer_load_dwordx4 v221, s[32:35], s95 offen offset:1024 lds
MM v[108:111],a[56:59],a[136:139],v[108:111]
s_add_u32 s95, s88, 18432
v_add_f32 v170, v170, v152
v_add_f32 v171, v171, v153
MM v[16:19],a[0:3],a[144:147],0
v_add_f32 v170, v170, v154
v_add_f32 v171, v171, v155
MM v[16:19],a[4:7],a[148:151],v[16:19]
v_add_f32 v170, v170, v156
v_add_f32 v171, v171, v157
MM v[24:27],a[4:7],a[156:159],0
buffer_load_dwordx4 v220, s[32:35], s95 offen offset:2048 lds
MM v[24:27],a[0:3],a[152:155],v[24:27]
s_add_u32 s95, s88, 27648
v_add_f32 v170, v170, v158
v_add_f32 v171, v171, v159
MM v[20:23],a[8:11],a[144:147],0
TR a[0:1],v192 offset:16448
TR a[2:3],v192 offset:16960
MM v[20:23],a[12:15],a[148:151],v[20:23]
TR a[4:5],v192 offset:24672
TR a[6:7],v192 offset:25184
v_add_f32 v170, v170, v160
MM v[28:31],a[12:15],a[156:159],0
buffer_load_dwordx4 v221, s[32:35], s95 offen offset:3072 lds
MM v[28:31],a[8:11],a[152:155],v[28:31]
s_sub_u32 s88, s88, s63
s_add_u32 m0, s94, 4096
s_add_u32 s95, s88, 40960
MM v[48:51],a[16:19],a[144:147],0
TR a[8:9],v192 offset:16456
TR a[10:11],v192 offset:16968
v_add_f32 v171, v171, v161
MM v[48:51],a[20:23],a[148:151],v[48:51]
TR a[12:13],v192 offset:24680
TR a[14:15],v192 offset:25192
MM v[56:59],a[20:23],a[156:159],0
buffer_load_dwordx4 v220, s[32:35], s95 offen offset:0 lds
MM v[56:59],a[16:19],a[152:155],v[56:59]
s_add_u32 s95, s88, 50176
v_add_f32 v170, v170, v162
v_add_f32 v171, v171, v163
MM v[52:55],a[24:27],a[144:147],0
TR a[16:17],v193 offset:16448
TR a[18:19],v193 offset:16960
MM v[52:55],a[28:31],a[148:151],v[52:55]
TR a[20:21],v193 offset:24672
TR a[22:23],v193 offset:25184
v_add_f32 v170, v170, v164
MM v[60:63],a[28:31],a[156:159],0
buffer_load_dwordx4 v221, s[32:35], s95 offen offset:1024 lds
MM v[60:63],a[24:27],a[152:155],v[60:63]
s_add_u32 s95, s88, 59392
v_add_f32 v171, v171, v165
v_add_f32 v170, v170, v166
MM v[80:83],a[32:35],a[144:147],0
TR a[24:25],v193 offset:16456
TR a[26:27],v193 offset:16968
MM v[80:83],a[36:39],a[148:151],v[80:83]
TR a[28:29],v193 offset:24680
TR a[30:31],v193 offset:25192
v_add_f32 v171, v171, v167
MM v[88:91],a[36:39],a[156:159],0
buffer_load_dwordx4 v220, s[32:35], s95 offen offset:2048 lds
MM v[88:91],a[32:35],a[152:155],v[88:91]
s_add_u32 s95, s88, 68608
v_add_f32 v172, v170, v171
v_lshlrev_b32 v232, 11, v180
MM v[84:87],a[40:43],a[144:147],0
TR a[32:33],v194 offset:16448
TR a[34:35],v194 offset:16960
MM v[84:87],a[44:47],a[148:151],v[84:87]
TR a[36:37],v194 offset:24672
TR a[38:39],v194 offset:25184
MM v[92:95],a[44:47],a[156:159],0
buffer_load_dwordx4 v221, s[32:35], s95 offen offset:3072 lds
v_add_f32_dpp v172, v172, v172 row_ror:8 row_mask:0xf bank_mask:0xf
MM v[92:95],a[40:43],a[152:155],v[92:95]
s_add_u32 s87, s89, s85
s_add_u32 s85, s85, 524288
v_add_f32_dpp v172, v172, v172 row_ror:4 row_mask:0xf bank_mask:0xf
MM v[112:115],a[48:51],a[144:147],0
TR a[40:41],v194 offset:16456
TR a[42:43],v194 offset:16968
MM v[112:115],a[52:55],a[148:151],v[112:115]
TR a[44:45],v194 offset:24680
TR a[46:47],v194 offset:25192
s_cmp_ge_u32 s85, 5242880
s_cselect_b32 s85, 0, s85
MM v[120:123],a[52:55],a[156:159],0
s_add_u32 m0, s96, 114880
s_nop 0
buffer_load_dwordx4 v253, s[16:19], s87 offen offset:0 lds
v_add_f32_dpp v172, v172, v172 row_ror:2 row_mask:0xf bank_mask:0xf
MM v[120:123],a[48:51],a[152:155],v[120:123]
s_add_u32 m0, s96, 115904
v_add_f32_dpp v172, v172, v172 row_ror:1 row_mask:0xf bank_mask:0xf
v_mov_b32 v173, v172
MM v[116:119],a[56:59],a[144:147],0
TR a[48:49],v195 offset:16448
TR a[50:51],v195 offset:16960
MM v[116:119],a[60:63],a[148:151],v[116:119]
TR a[52:53],v195 offset:24672
TR a[54:55],v195 offset:25184
MM v[124:127],a[60:63],a[156:159],0
buffer_load_dwordx4 v254, s[16:19], s87 offen offset:0 lds
v_permlane16_swap_b32 v172, v173
MM v[124:127],a[56:59],a[152:155],v[124:127]
s_add_u32 s87, s87, 65536
s_add_u32 m0, s96, 116936
v_add_f32 v172, v172, v173
buffer_load_dwordx4 v253, s[16:19], s87 offen offset:0 lds
s_add_u32 m0, s96, 117960
s_nop 0
buffer_load_dwordx4 v254, s[16:19], s87 offen offset:0 lds
s_waitcnt vmcnt(19)
RD a[160:163],v222 offset:32768
RD a[164:167],v223 offset:32768
RD a[168:171],v222 offset:34816
RD a[172:175],v223 offset:34816
RD a[176:179],v222 offset:36864
RD a[180:183],v223 offset:36864
RD a[184:187],v222 offset:38912
RD a[188:191],v223 offset:38912
s_waitcnt vmcnt(15) lgkmcnt(0)
s_barrier
MM v[0:3],a[0:3],a[160:163],v[0:3]
TR a[56:57],v195 offset:16456
TR a[58:59],v195 offset:16968
MM v[0:3],a[4:7],a[164:167],v[0:3]
TR a[60:61],v195 offset:24680
TR a[62:63],v195 offset:25192
v_mul_f32 v172, 0x3acccccd, v172
MM v[8:11],a[4:7],a[172:175],v[8:11]
v_sub_f32 v176, v148, v172
v_sub_f32 v177, v149, v172
MM v[8:11],a[0:3],a[168:171],v[8:11]
s_add_u32 s88, s68, s86
s_add_u32 s86, s86, 128
MM v[16:19],a[0:3],a[176:179],v[16:19]
s_cmp_ge_u32 s86, 1280
s_cselect_b32 s86, 0, s86
s_add_u32 m0, s94, 32768
v_mul_f32 v170, v176, v176
MM v[16:19],a[4:7],a[180:183],v[16:19]
s_add_u32 s95, s88, 0
buffer_load_dwordx4 v220, s[32:35], s95 offen offset:0 lds
MM v[24:27],a[4:7],a[188:191],v[24:27]
s_add_u32 s95, s88, 9216
v_mul_f32 v171, v177, v177
v_sub_f32 v176, v150, v172
MM v[24:27],a[0:3],a[184:187],v[24:27]
buffer_load_dwordx4 v221, s[32:35], s95 offen offset:1024 lds
MM v[4:7],a[8:11],a[160:163],v[4:7]
s_add_u32 s95, s88, 18432
v_sub_f32 v177, v151, v172
v_fma_f32 v170, v176, v176, v170
MM v[4:7],a[12:15],a[164:167],v[4:7]
v_fma_f32 v171, v177, v177, v171
v_sub_f32 v176, v152, v172
MM v[12:15],a[12:15],a[172:175],v[12:15]
v_sub_f32 v177, v153, v172
v_fma_f32 v170, v176, v176, v170
MM v[12:15],a[8:11],a[168:171],v[12:15]
buffer_load_dwordx4 v220, s[32:35], s95 offen offset:2048 lds
MM v[20:23],a[8:11],a[176:179],v[20:23]
s_add_u32 s95, s88, 27648
v_fma_f32 v171, v177, v177, v171
v_sub_f32 v176, v154, v172
MM v[20:23],a[12:15],a[180:183],v[20:23]
v_sub_f32 v177, v155, v172
v_fma_f32 v170, v176, v176, v170
MM v[28:31],a[12:15],a[188:191],v[28:31]
v_fma_f32 v171, v177, v177, v171
v_sub_f32 v176, v156, v172
MM v[28:31],a[8:11],a[184:187],v[28:31]
buffer_load_dwordx4 v221, s[32:35], s95 offen offset:3072 lds
v_smfmac_f32_16x16x64_bf16 v[184:187], v[236:239], a[0:7], v234
s_sub_u32 s88, s88, s63
s_add_u32 m0, s94, 36864
v_sub_f32 v177, v157, v172
v_smfmac_f32_16x16x64_bf16 v[188:191], v[236:239], a[8:15], v234
s_add_u32 s95, s88, 40960
v_fma_f32 v170, v176, v176, v170
MM v[32:35],a[16:19],a[160:163],v[32:35]
TR a[0:1],v192 offset:32896
TR a[2:3],v192 offset:33408
MM v[32:35],a[20:23],a[164:167],v[32:35]
TR a[4:5],v192 offset:41120
TR a[6:7],v192 offset:41632
v_fma_f32 v171, v177, v177, v171
MM v[40:43],a[20:23],a[172:175],v[40:43]
TR a[8:9],v192 offset:32904
TR a[10:11],v192 offset:33416
MM v[40:43],a[16:19],a[168:171],v[40:43]
TR a[12:13],v192 offset:41128
TR a[14:15],v192 offset:41640
buffer_load_dwordx4 v220, s[32:35], s95 offen offset:0 lds
MM v[48:51],a[16:19],a[176:179],v[48:51]
s_add_u32 s95, s88, 50176
v_sub_f32 v176, v158, v172
v_sub_f32 v177, v159, v172
MM v[48:51],a[20:23],a[180:183],v[48:51]
buffer_load_dwordx4 v221, s[32:35], s95 offen offset:1024 lds
LPG1_%=:
s_cbranch_execz LPG5_%=
MM v[56:59],a[20:23],a[188:191],v[56:59]
s_add_u32 s95, s88, 59392
v_fma_f32 v170, v176, v176, v170
v_fma_f32 v171, v177, v177, v171
MM v[56:59],a[16:19],a[184:187],v[56:59]
v_sub_f32 v176, v160, v172
v_sub_f32 v177, v161, v172
MM v[36:39],a[24:27],a[160:163],v[36:39]
TR a[16:17],v193 offset:32896
TR a[18:19],v193 offset:33408
MM v[36:39],a[28:31],a[164:167],v[36:39]
TR a[20:21],v193 offset:41120
TR a[22:23],v193 offset:41632
v_fma_f32 v170, v176, v176, v170
MM v[44:47],a[28:31],a[172:175],v[44:47]
buffer_load_dwordx4 v220, s[32:35], s95 offen offset:2048 lds
MM v[44:47],a[24:27],a[168:171],v[44:47]
s_add_u32 s95, s88, 68608
v_fma_f32 v171, v177, v177, v171
v_sub_f32 v176, v162, v172
MM v[52:55],a[24:27],a[176:179],v[52:55]
v_sub_f32 v177, v163, v172
v_fma_f32 v170, v176, v176, v170
MM v[52:55],a[28:31],a[180:183],v[52:55]
buffer_load_dwordx4 v221, s[32:35], s95 offen offset:3072 lds
MM v[60:63],a[28:31],a[188:191],v[60:63]
s_add_u32 s87, s89, s85
s_add_u32 s85, s85, 524288
v_fma_f32 v171, v177, v177, v171
MM v[60:63],a[24:27],a[184:187],v[60:63]
s_cmp_ge_u32 s85, 5242880
s_cselect_b32 s85, 0, s85
s_add_u32 m0, s96, 131328
MM v[64:67],a[32:35],a[160:163],v[64:67]
TR a[24:25],v193 offset:32904
TR a[26:27],v193 offset:33416
v_sub_f32 v176, v164, v172
MM v[64:67],a[36:39],a[164:167],v[64:67]
TR a[28:29],v193 offset:41128
TR a[30:31],v193 offset:41640
MM v[72:75],a[36:39],a[172:175],v[72:75]
buffer_load_dwordx4 v253, s[16:19], s87 offen offset:0 lds
MM v[72:75],a[32:35],a[168:171],v[72:75]
s_add_u32 m0, s96, 132352
v_sub_f32 v177, v165, v172
v_fma_f32 v170, v176, v176, v170
MM v[80:83],a[32:35],a[176:179],v[80:83]
v_fma_f32 v171, v177, v177, v171
v_sub_f32 v176, v166, v172
MM v[80:83],a[36:39],a[180:183],v[80:83]
buffer_load_dwordx4 v254, s[16:19], s87 offen offset:0 lds
MM v[88:91],a[36:39],a[188:191],v[88:91]
s_add_u32 s87, s87, 65536
s_add_u32 m0, s96, 133384
v_sub_f32 v177, v167, v172
MM v[88:91],a[32:35],a[184:187],v[88:91]
v_fma_f32 v170, v176, v176, v170
v_fma_f32 v171, v177, v177, v171
MM v[68:71],a[40:43],a[160:163],v[68:71]
TR a[32:33],v194 offset:32896
TR a[34:35],v194 offset:33408
MM v[68:71],a[44:47],a[164:167],v[68:71]
TR a[36:37],v194 offset:41120
TR a[38:39],v194 offset:41632
v_add_f32 v170, v170, v171
MM v[76:79],a[44:47],a[172:175],v[76:79]
buffer_load_dwordx4 v253, s[16:19], s87 offen offset:0 lds
MM v[76:79],a[40:43],a[168:171],v[76:79]
s_add_u32 m0, s96, 134408
v_add_f32_dpp v170, v170, v170 row_ror:8 row_mask:0xf bank_mask:0xf
v_lshl_add_u32 v232, v214, 8, v232
MM v[84:87],a[40:43],a[176:179],v[84:87]
v_add_f32_dpp v170, v170, v170 row_ror:4 row_mask:0xf bank_mask:0xf
v_lshl_add_u32 v232, v215, 6, v232
MM v[84:87],a[44:47],a[180:183],v[84:87]
buffer_load_dwordx4 v254, s[16:19], s87 offen offset:0 lds
MM v[92:95],a[44:47],a[188:191],v[92:95]
s_waitcnt vmcnt(16)
RD a[192:195],v222 offset:0
v_add_f32_dpp v170, v170, v170 row_ror:2 row_mask:0xf bank_mask:0xf
MM v[92:95],a[40:43],a[184:187],v[92:95]
RD a[196:199],v223 offset:0
v_add_f32_dpp v170, v170, v170 row_ror:1 row_mask:0xf bank_mask:0xf
MM v[96:99],a[48:51],a[160:163],v[96:99]
TR a[40:41],v194 offset:32904
TR a[42:43],v194 offset:33416
MM v[96:99],a[52:55],a[164:167],v[96:99]
TR a[44:45],v194 offset:41128
TR a[46:47],v194 offset:41640
v_mov_b32 v173, v170
MM v[104:107],a[52:55],a[172:175],v[104:107]
RD a[200:203],v222 offset:2048
v_permlane16_swap_b32 v170, v173
MM v[104:107],a[48:51],a[168:171],v[104:107]
RD a[204:207],v223 offset:2048
v_add_f32 v170, v170, v173
MM v[112:115],a[48:51],a[176:179],v[112:115]
RD a[208:211],v222 offset:4096
v_mul_f32 v170, 0x3acccccd, v170
MM v[112:115],a[52:55],a[180:183],v[112:115]
RD a[212:215],v223 offset:4096
CV v178,v172,v170
MM v[120:123],a[52:55],a[188:191],v[120:123]
RD a[216:219],v222 offset:6144
v_lshlrev_b32 v174, 16, v178
MM v[120:123],a[48:51],a[184:187],v[120:123]
s_waitcnt lgkmcnt(15)
RD a[220:223],v223 offset:6144
v_and_b32 v170, 0xffff0000, v178
MM v[100:103],a[56:59],a[160:163],v[100:103]
TR a[48:49],v195 offset:32896
TR a[50:51],v195 offset:33408
MM v[100:103],a[60:63],a[164:167],v[100:103]
TR a[52:53],v195 offset:41120
TR a[54:55],v195 offset:41632
v_add_f32 v170, 0x3727c5ac, v170
MM v[108:111],a[60:63],a[172:175],v[108:111]
CV v170,v170,v170
v_and_b32 v170, 0xffff0000, v170
MM v[108:111],a[56:59],a[168:171],v[108:111]
s_waitcnt lgkmcnt(4)
s_add_u32 s88, s68, s86
s_add_u32 s86, s86, 128
MM v[116:119],a[56:59],a[176:179],v[116:119]
s_cmp_ge_u32 s86, 1280
s_cselect_b32 s86, 0, s86
s_add_u32 m0, s94, 0
v_sqrt_f32 v170, v170
MM v[116:119],a[60:63],a[180:183],v[116:119]
)ASM"
R"ASM(
s_add_u32 s95, s88, 0
buffer_load_dwordx4 v220, s[32:35], s95 offen offset:0 lds
MM v[124:127],a[60:63],a[188:191],v[124:127]
s_add_u32 s95, s88, 9216
v_mov_b32 v179, s69
CV v178,v174,v170
MM v[124:127],a[56:59],a[184:187],v[124:127]
buffer_load_dwordx4 v221, s[32:35], s95 offen offset:1024 lds
s_waitcnt vmcnt(14)
s_barrier
MM v[0:3],a[0:3],a[192:195],v[0:3]
s_add_u32 s95, s88, 18432
TR a[56:57],v195 offset:32904
TR a[58:59],v195 offset:33416
v_and_b32 v170, 0xffff0000, v178
MM v[0:3],a[4:7],a[196:199],v[0:3]
TR a[60:61],v195 offset:41128
TR a[62:63],v195 offset:41640
MM v[8:11],a[4:7],a[204:207],v[8:11]
v_rcp_f32 v175, v170
MM v[8:11],a[0:3],a[200:203],v[8:11]
buffer_load_dwordx4 v220, s[32:35], s95 offen offset:2048 lds
MM v[16:19],a[0:3],a[208:211],v[16:19]
s_add_u32 s95, s88, 27648
v_lshlrev_b32 v173, 4, v180
v_lshl_add_u32 v232, v216, 3, v232
MM v[16:19],a[4:7],a[212:215],v[16:19]
s_mov_b32 exec_lo, 1
s_mov_b32 exec_hi, 1
buffer_store_dwordx2 v[178:179], v173, s[20:23], s97 offen offset:0 sc1
s_mov_b64 exec, -1
MM v[24:27],a[4:7],a[220:223],v[24:27]
v_sub_f32 v148, v148, v174
v_sub_f32 v149, v149, v174
MM v[24:27],a[0:3],a[216:219],v[24:27]
buffer_load_dwordx4 v221, s[32:35], s95 offen offset:3072 lds
MM v[4:7],a[8:11],a[192:195],v[4:7]
s_sub_u32 s88, s88, s63
s_add_u32 m0, s94, 4096
s_add_u32 s95, s88, 40960
v_sub_f32 v150, v150, v174
MM v[4:7],a[12:15],a[196:199],v[4:7]
v_sub_f32 v151, v151, v174
v_sub_f32 v152, v152, v174
MM v[12:15],a[12:15],a[204:207],v[12:15]
v_sub_f32 v153, v153, v174
v_sub_f32 v154, v154, v174
MM v[12:15],a[8:11],a[200:203],v[12:15]
buffer_load_dwordx4 v220, s[32:35], s95 offen offset:0 lds
MM v[20:23],a[8:11],a[208:211],v[20:23]
s_add_u32 s95, s88, 50176
v_sub_f32 v155, v155, v174
v_sub_f32 v156, v156, v174
MM v[20:23],a[12:15],a[212:215],v[20:23]
v_sub_f32 v157, v157, v174
v_sub_f32 v158, v158, v174
MM v[28:31],a[12:15],a[220:223],v[28:31]
v_sub_f32 v159, v159, v174
v_sub_f32 v160, v160, v174
MM v[28:31],a[8:11],a[216:219],v[28:31]
buffer_load_dwordx4 v221, s[32:35], s95 offen offset:1024 lds
v_smfmac_f32_16x16x64_bf16 v[184:187], v[236:239], a[0:7], v234
s_add_u32 s95, s88, 59392
v_sub_f32 v161, v161, v174
v_smfmac_f32_16x16x64_bf16 v[188:191], v[236:239], a[8:15], v234
v_sub_f32 v162, v162, v174
MM v[32:35],a[16:19],a[192:195],v[32:35]
TR a[0:1],v196 offset:0
TR a[2:3],v196 offset:512
MM v[32:35],a[20:23],a[196:199],v[32:35]
TR a[4:5],v196 offset:8224
TR a[6:7],v196 offset:8736
v_sub_f32 v163, v163, v174
MM v[40:43],a[20:23],a[204:207],v[40:43]
TR a[8:9],v196 offset:8
TR a[10:11],v196 offset:520
MM v[40:43],a[16:19],a[200:203],v[40:43]
TR a[12:13],v196 offset:8232
TR a[14:15],v196 offset:8744
buffer_load_dwordx4 v220, s[32:35], s95 offen offset:2048 lds
MM v[48:51],a[16:19],a[208:211],v[48:51]
s_add_u32 s95, s88, 68608
v_sub_f32 v164, v164, v174
v_sub_f32 v165, v165, v174
MM v[48:51],a[20:23],a[212:215],v[48:51]
buffer_load_dwordx4 v221, s[32:35], s95 offen offset:3072 lds
MM v[56:59],a[20:23],a[220:223],v[56:59]
s_add_u32 s87, s89, s85
s_add_u32 s85, s85, 524288
v_sub_f32 v166, v166, v174
MM v[56:59],a[16:19],a[216:219],v[56:59]
s_cmp_ge_u32 s85, 5242880
s_cselect_b32 s85, 0, s85
s_add_u32 m0, s96, 65536
MM v[36:39],a[24:27],a[192:195],v[36:39]
TR a[16:17],v197 offset:0
TR a[18:19],v197 offset:512
v_sub_f32 v167, v167, v174
MM v[36:39],a[28:31],a[196:199],v[36:39]
TR a[20:21],v197 offset:8224
TR a[22:23],v197 offset:8736
MM v[44:47],a[28:31],a[204:207],v[44:47]
buffer_load_dwordx4 v253, s[16:19], s87 offen offset:0 lds
MM v[44:47],a[24:27],a[200:203],v[44:47]
s_add_u32 m0, s96, 66560
CV v128,v148,v149
CV v129,v150,v151
MM v[52:55],a[24:27],a[208:211],v[52:55]
CV v130,v152,v153
CV v131,v154,v155
MM v[52:55],a[28:31],a[212:215],v[52:55]
buffer_load_dwordx4 v254, s[16:19], s87 offen offset:0 lds
MM v[60:63],a[28:31],a[220:223],v[60:63]
s_add_u32 s87, s87, 65536
s_add_u32 m0, s96, 67592
CV v132,v156,v157
MM v[60:63],a[24:27],a[216:219],v[60:63]
CV v133,v158,v159
CV v134,v160,v161
MM v[64:67],a[32:35],a[192:195],v[64:67]
TR a[24:25],v197 offset:8
TR a[26:27],v197 offset:520
MM v[64:67],a[36:39],a[196:199],v[64:67]
TR a[28:29],v197 offset:8232
TR a[30:31],v197 offset:8744
CV v135,v162,v163
MM v[72:75],a[36:39],a[204:207],v[72:75]
buffer_load_dwordx4 v253, s[16:19], s87 offen offset:0 lds
MM v[72:75],a[32:35],a[200:203],v[72:75]
s_add_u32 m0, s96, 68616
CV v136,v164,v165
CV v137,v166,v167
MM v[80:83],a[32:35],a[208:211],v[80:83]
v_lshlrev_b32 v148, 16, v128
v_and_b32 v149, 0xffff0000, v128
MM v[80:83],a[36:39],a[212:215],v[80:83]
buffer_load_dwordx4 v254, s[16:19], s87 offen offset:0 lds
MM v[88:91],a[36:39],a[220:223],v[88:91]
s_waitcnt vmcnt(17)
RD a[224:227],v222 offset:32768
v_lshlrev_b32 v150, 16, v129
MM v[88:91],a[32:35],a[216:219],v[88:91]
RD a[228:231],v223 offset:32768
v_and_b32 v151, 0xffff0000, v129
MM v[68:71],a[40:43],a[192:195],v[68:71]
TR a[32:33],v198 offset:0
TR a[34:35],v198 offset:512
MM v[68:71],a[44:47],a[196:199],v[68:71]
TR a[36:37],v198 offset:8224
TR a[38:39],v198 offset:8736
v_lshlrev_b32 v152, 16, v130
MM v[76:79],a[44:47],a[204:207],v[76:79]
RD a[232:235],v222 offset:34816
v_and_b32 v153, 0xffff0000, v130
MM v[76:79],a[40:43],a[200:203],v[76:79]
RD a[236:239],v223 offset:34816
v_lshlrev_b32 v154, 16, v131
MM v[84:87],a[40:43],a[208:211],v[84:87]
RD a[240:243],v222 offset:36864
v_and_b32 v155, 0xffff0000, v131
MM v[84:87],a[44:47],a[212:215],v[84:87]
RD a[244:247],v223 offset:36864
v_lshlrev_b32 v156, 16, v132
MM v[92:95],a[44:47],a[220:223],v[92:95]
RD a[248:251],v222 offset:38912
v_and_b32 v157, 0xffff0000, v132
MM v[92:95],a[40:43],a[216:219],v[92:95]
s_waitcnt lgkmcnt(15)
RD a[252:255],v223 offset:38912
v_lshlrev_b32 v158, 16, v133
MM v[96:99],a[48:51],a[192:195],v[96:99]
TR a[40:41],v198 offset:8
TR a[42:43],v198 offset:520
MM v[96:99],a[52:55],a[196:199],v[96:99]
TR a[44:45],v198 offset:8232
TR a[46:47],v198 offset:8744
v_and_b32 v159, 0xffff0000, v133
MM v[104:107],a[52:55],a[204:207],v[104:107]
v_lshlrev_b32 v160, 16, v134
v_and_b32 v161, 0xffff0000, v134
MM v[104:107],a[48:51],a[200:203],v[104:107]
s_waitcnt lgkmcnt(4)
s_add_u32 s88, s68, s86
s_add_u32 s86, s86, 128
MM v[112:115],a[48:51],a[208:211],v[112:115]
s_cmp_ge_u32 s86, 1280
s_cselect_b32 s86, 0, s86
s_add_u32 m0, s94, 32768
v_lshlrev_b32 v162, 16, v135
MM v[112:115],a[52:55],a[212:215],v[112:115]
s_add_u32 s95, s88, 0
buffer_load_dwordx4 v220, s[32:35], s95 offen offset:0 lds
MM v[120:123],a[52:55],a[220:223],v[120:123]
s_add_u32 s95, s88, 9216
v_and_b32 v163, 0xffff0000, v135
v_lshlrev_b32 v164, 16, v136
MM v[120:123],a[48:51],a[216:219],v[120:123]
buffer_load_dwordx4 v221, s[32:35], s95 offen offset:1024 lds
MM v[100:103],a[56:59],a[192:195],v[100:103]
s_add_u32 s95, s88, 18432
TR a[48:49],v199 offset:0
TR a[50:51],v199 offset:512
v_and_b32 v165, 0xffff0000, v136
MM v[100:103],a[60:63],a[196:199],v[100:103]
TR a[52:53],v199 offset:8224
TR a[54:55],v199 offset:8736
MM v[108:111],a[60:63],a[204:207],v[108:111]
v_lshlrev_b32 v166, 16, v137
v_and_b32 v167, 0xffff0000, v137
MM v[108:111],a[56:59],a[200:203],v[108:111]
buffer_load_dwordx4 v220, s[32:35], s95 offen offset:2048 lds
MM v[116:119],a[56:59],a[208:211],v[116:119]
s_add_u32 s95, s88, 27648
v_mul_f32 v148, v148, v175
v_mul_f32 v149, v149, v175
MM v[116:119],a[60:63],a[212:215],v[116:119]
v_mul_f32 v150, v150, v175
v_mul_f32 v151, v151, v175
MM v[124:127],a[60:63],a[220:223],v[124:127]
v_mul_f32 v152, v152, v175
v_mul_f32 v153, v153, v175
MM v[124:127],a[56:59],a[216:219],v[124:127]
buffer_load_dwordx4 v221, s[32:35], s95 offen offset:3072 lds
s_waitcnt vmcnt(17)
s_barrier
MM v[0:3],a[0:3],a[224:227],v[0:3]
s_sub_u32 s88, s88, s63
TR a[56:57],v199 offset:8
TR a[58:59],v199 offset:520
v_mul_f32 v154, v154, v175
MM v[0:3],a[4:7],a[228:231],v[0:3]
s_add_u32 m0, s94, 36864
TR a[60:61],v199 offset:8232
TR a[62:63],v199 offset:8744
MM v[8:11],a[4:7],a[236:239],v[8:11]
s_add_u32 s95, s88, 40960
v_mul_f32 v155, v155, v175
v_mul_f32 v156, v156, v175
MM v[8:11],a[0:3],a[232:235],v[8:11]
buffer_load_dwordx4 v220, s[32:35], s95 offen offset:0 lds
MM v[16:19],a[0:3],a[240:243],v[16:19]
s_add_u32 s95, s88, 50176
v_mul_f32 v157, v157, v175
v_mul_f32 v158, v158, v175
MM v[16:19],a[4:7],a[244:247],v[16:19]
v_mul_f32 v159, v159, v175
v_mul_f32 v160, v160, v175
MM v[24:27],a[4:7],a[252:255],v[24:27]
v_mul_f32 v161, v161, v175
v_mul_f32 v162, v162, v175
MM v[24:27],a[0:3],a[248:251],v[24:27]
buffer_load_dwordx4 v221, s[32:35], s95 offen offset:1024 lds
MM v[4:7],a[8:11],a[224:227],v[4:7]
s_add_u32 s95, s88, 59392
v_mul_f32 v163, v163, v175
v_mul_f32 v164, v164, v175
MM v[4:7],a[12:15],a[228:231],v[4:7]
v_mul_f32 v165, v165, v175
v_mul_f32 v166, v166, v175
MM v[12:15],a[12:15],a[236:239],v[12:15]
v_mul_f32 v167, v167, v175
CV v128,v148,v149
MM v[12:15],a[8:11],a[232:235],v[12:15]
buffer_load_dwordx4 v220, s[32:35], s95 offen offset:2048 lds
MM v[20:23],a[8:11],a[240:243],v[20:23]
s_add_u32 s95, s88, 68608
CV v129,v150,v151
CV v130,v152,v153
MM v[20:23],a[12:15],a[244:247],v[20:23]
CV v131,v154,v155
CV v132,v156,v157
MM v[28:31],a[12:15],a[252:255],v[28:31]
CV v133,v158,v159
CV v134,v160,v161
MM v[28:31],a[8:11],a[248:251],v[28:31]
buffer_load_dwordx4 v221, s[32:35], s95 offen offset:3072 lds
v_smfmac_f32_16x16x64_bf16 v[184:187], v[236:239], a[0:7], v234
s_add_u32 s87, s89, s85
s_add_u32 s85, s85, 524288
CV v135,v162,v163
v_smfmac_f32_16x16x64_bf16 v[188:191], v[236:239], a[8:15], v234
s_cmp_ge_u32 s85, 5242880
s_cselect_b32 s85, 0, s85
MM v[32:35],a[16:19],a[224:227],v[32:35]
s_add_u32 m0, s96, 81984
TR a[0:1],v196 offset:16448
TR a[2:3],v196 offset:16960
MM v[32:35],a[20:23],a[228:231],v[32:35]
TR a[4:5],v196 offset:24672
TR a[6:7],v196 offset:25184
CV v136,v164,v165
MM v[40:43],a[20:23],a[236:239],v[40:43]
TR a[8:9],v196 offset:16456
TR a[10:11],v196 offset:16968
MM v[40:43],a[16:19],a[232:235],v[40:43]
TR a[12:13],v196 offset:24680
TR a[14:15],v196 offset:25192
buffer_load_dwordx4 v253, s[16:19], s87 offen offset:0 lds
MM v[48:51],a[16:19],a[240:243],v[48:51]
s_add_u32 m0, s96, 83008
CV v137,v166,v167
v_lshl_add_u32 v232, v214, 5, v232
MM v[48:51],a[20:23],a[244:247],v[48:51]
buffer_load_dwordx4 v254, s[16:19], s87 offen offset:0 lds
MM v[56:59],a[20:23],a[252:255],v[56:59]
s_add_u32 s87, s87, 65536
s_add_u32 m0, s96, 84040
v_xor_b32 v233, 32, v232
MM v[56:59],a[16:19],a[248:251],v[56:59]
s_add_u32 s77, %[slds], 65536
v_add_u32 v232, s77, v232
v_add_u32 v233, s77, v233
MM v[36:39],a[24:27],a[224:227],v[36:39]
TR a[16:17],v197 offset:16448
TR a[18:19],v197 offset:16960
MM v[36:39],a[28:31],a[228:231],v[36:39]
TR a[20:21],v197 offset:24672
TR a[22:23],v197 offset:25184
MM v[44:47],a[28:31],a[236:239],v[44:47]
buffer_load_dwordx4 v253, s[16:19], s87 offen offset:0 lds
MM v[44:47],a[24:27],a[232:235],v[44:47]
s_add_u32 m0, s96, 85064
v_lshlrev_b32 v214, 8, v211
v_add_u32 v214, %[slds], v214
MM v[52:55],a[24:27],a[240:243],v[52:55]
v_xor_b32 v215, v211, v212
s_lshl_b32 s77, %[swave], 2
MM v[52:55],a[28:31],a[244:247],v[52:55]
buffer_load_dwordx4 v254, s[16:19], s87 offen offset:0 lds
MM v[60:63],a[28:31],a[252:255],v[60:63]
s_waitcnt vmcnt(16)
RD a[128:131],v222 offset:0
v_xor_b32 v213, s77, v215
MM v[60:63],a[24:27],a[248:251],v[60:63]
RD a[132:135],v223 offset:0
v_xor_b32 v216, 0, v213
MM v[64:67],a[32:35],a[224:227],v[64:67]
TR a[24:25],v197 offset:16456
TR a[26:27],v197 offset:16968
MM v[64:67],a[36:39],a[228:231],v[64:67]
TR a[28:29],v197 offset:24680
TR a[30:31],v197 offset:25192
MM v[72:75],a[36:39],a[236:239],v[72:75]
RD a[136:139],v222 offset:2048
buffer_store_dwordx4 v[128:131], v168, s[20:23], s82 offen offset:0 sc1
v_lshl_add_u32 v224, v216, 4, v214
MM v[72:75],a[32:35],a[232:235],v[72:75]
RD a[140:143],v223 offset:2048
v_xor_b32 v216, 4, v213
MM v[80:83],a[32:35],a[240:243],v[80:83]
RD a[144:147],v222 offset:4096
v_lshl_add_u32 v225, v216, 4, v214
MM v[80:83],a[36:39],a[244:247],v[80:83]
RD a[148:151],v223 offset:4096
v_xor_b32 v216, 8, v213
MM v[88:91],a[36:39],a[252:255],v[88:91]
RD a[152:155],v222 offset:6144
)ASM"
R"ASM(
v_lshl_add_u32 v226, v216, 4, v214
MM v[88:91],a[32:35],a[248:251],v[88:91]
s_waitcnt lgkmcnt(15)
RD a[156:159],v223 offset:6144
buffer_store_dwordx4 v[132:135], v168, s[20:23], s82 offen offset:512 sc1
v_xor_b32 v216, 12, v213
MM v[68:71],a[40:43],a[224:227],v[68:71]
TR a[32:33],v198 offset:16448
TR a[34:35],v198 offset:16960
MM v[68:71],a[44:47],a[228:231],v[68:71]
TR a[36:37],v198 offset:24672
TR a[38:39],v198 offset:25184
MM v[76:79],a[44:47],a[236:239],v[76:79]
buffer_store_dwordx2 v[136:137], v181, s[20:23], s82 offen offset:1024 sc1
MM v[76:79],a[40:43],a[232:235],v[76:79]
s_waitcnt lgkmcnt(4)
s_add_u32 s88, s68, s86
s_add_u32 s86, s86, 128
MM v[84:87],a[40:43],a[240:243],v[84:87]
s_cmp_ge_u32 s86, 1280
s_cselect_b32 s86, 0, s86
MM v[84:87],a[44:47],a[244:247],v[84:87]
s_add_u32 m0, s94, 0
s_add_u32 s95, s88, 0
v_lshlrev_b32 v148, 16, v128
MM v[92:95],a[44:47],a[252:255],v[92:95]
buffer_load_dwordx4 v220, s[32:35], s95 offen offset:0 lds
MM v[92:95],a[40:43],a[248:251],v[92:95]
s_add_u32 s95, s88, 9216
v_and_b32 v149, 0xffff0000, v128
v_lshlrev_b32 v176, 16, v138
MM v[96:99],a[48:51],a[224:227],v[96:99]
TR a[40:41],v198 offset:16456
TR a[42:43],v198 offset:16968
MM v[96:99],a[52:55],a[228:231],v[96:99]
TR a[44:45],v198 offset:24680
TR a[46:47],v198 offset:25192
v_and_b32 v177, 0xffff0000, v138
MM v[104:107],a[52:55],a[236:239],v[104:107]
buffer_load_dwordx4 v221, s[32:35], s95 offen offset:1024 lds
MM v[104:107],a[48:51],a[232:235],v[104:107]
s_add_u32 s95, s88, 18432
v_mul_f32 v170, v148, v176
v_mul_f32 v171, v149, v177
MM v[112:115],a[48:51],a[240:243],v[112:115]
buffer_load_dwordx4 v220, s[32:35], s95 offen offset:2048 lds
MM v[112:115],a[52:55],a[244:247],v[112:115]
s_add_u32 s95, s88, 27648
v_lshlrev_b32 v148, 16, v129
v_and_b32 v149, 0xffff0000, v129
MM v[120:123],a[52:55],a[252:255],v[120:123]
v_lshlrev_b32 v176, 16, v139
v_and_b32 v177, 0xffff0000, v139
MM v[120:123],a[48:51],a[248:251],v[120:123]
v_fma_f32 v170, v148, v176, v170
v_fma_f32 v171, v149, v177, v171
MM v[100:103],a[56:59],a[224:227],v[100:103]
TR a[48:49],v199 offset:16448
TR a[50:51],v199 offset:16960
MM v[100:103],a[60:63],a[228:231],v[100:103]
TR a[52:53],v199 offset:24672
TR a[54:55],v199 offset:25184
v_lshlrev_b32 v148, 16, v130
MM v[108:111],a[60:63],a[236:239],v[108:111]
buffer_load_dwordx4 v221, s[32:35], s95 offen offset:3072 lds
MM v[108:111],a[56:59],a[232:235],v[108:111]
s_sub_u32 s88, s88, s63
s_add_u32 m0, s94, 4096
s_add_u32 s95, s88, 40960
MM v[116:119],a[56:59],a[240:243],v[116:119]
buffer_load_dwordx4 v220, s[32:35], s95 offen offset:0 lds
v_and_b32 v149, 0xffff0000, v130
MM v[116:119],a[60:63],a[244:247],v[116:119]
s_add_u32 s95, s88, 50176
v_lshlrev_b32 v176, 16, v140
v_and_b32 v177, 0xffff0000, v140
MM v[124:127],a[60:63],a[252:255],v[124:127]
v_fma_f32 v170, v148, v176, v170
v_fma_f32 v171, v149, v177, v171
MM v[124:127],a[56:59],a[248:251],v[124:127]
v_lshlrev_b32 v148, 16, v131
v_and_b32 v149, 0xffff0000, v131
s_waitcnt vmcnt(20)
s_barrier
MM v[0:3],a[0:3],a[128:131],v[0:3]
TR a[56:57],v199 offset:16456
TR a[58:59],v199 offset:16968
MM v[0:3],a[4:7],a[132:135],v[0:3]
s_nop 0
LPG2_%=:
s_cbranch_execz LPG6_%=
TR a[60:61],v199 offset:24680
TR a[62:63],v199 offset:25192
v_lshlrev_b32 v176, 16, v141
MM v[8:11],a[4:7],a[140:143],v[8:11]
buffer_load_dwordx4 v221, s[32:35], s95 offen offset:1024 lds
MM v[8:11],a[0:3],a[136:139],v[8:11]
s_add_u32 s95, s88, 59392
v_and_b32 v177, 0xffff0000, v141
v_fma_f32 v170, v148, v176, v170
MM v[16:19],a[0:3],a[144:147],v[16:19]
buffer_load_dwordx4 v220, s[32:35], s95 offen offset:2048 lds
MM v[16:19],a[4:7],a[148:151],v[16:19]
s_add_u32 s95, s88, 68608
v_fma_f32 v171, v149, v177, v171
v_lshlrev_b32 v148, 16, v132
MM v[24:27],a[4:7],a[156:159],v[24:27]
v_and_b32 v149, 0xffff0000, v132
v_lshlrev_b32 v176, 16, v142
MM v[24:27],a[0:3],a[152:155],v[24:27]
v_and_b32 v177, 0xffff0000, v142
v_fma_f32 v170, v148, v176, v170
MM v[4:7],a[8:11],a[128:131],v[4:7]
buffer_load_dwordx4 v221, s[32:35], s95 offen offset:3072 lds
MM v[4:7],a[12:15],a[132:135],v[4:7]
s_add_u32 s87, s89, s85
s_add_u32 s85, s85, 524288
v_fma_f32 v171, v149, v177, v171
MM v[12:15],a[12:15],a[140:143],v[12:15]
s_cmp_ge_u32 s85, 5242880
s_cselect_b32 s85, 0, s85
s_add_u32 m0, s96, 98432
MM v[12:15],a[8:11],a[136:139],v[12:15]
v_lshlrev_b32 v148, 16, v133
v_and_b32 v149, 0xffff0000, v133
MM v[20:23],a[8:11],a[144:147],v[20:23]
buffer_load_dwordx4 v253, s[16:19], s87 offen offset:0 lds
MM v[20:23],a[12:15],a[148:151],v[20:23]
s_add_u32 m0, s96, 99456
v_lshlrev_b32 v176, 16, v143
v_and_b32 v177, 0xffff0000, v143
MM v[28:31],a[12:15],a[156:159],v[28:31]
v_fma_f32 v170, v148, v176, v170
v_fma_f32 v171, v149, v177, v171
MM v[28:31],a[8:11],a[152:155],v[28:31]
v_lshlrev_b32 v148, 16, v134
v_and_b32 v149, 0xffff0000, v134
v_smfmac_f32_16x16x64_bf16 v[184:187], v[236:239], a[0:7], v234
v_lshlrev_b32 v176, 16, v144
v_smfmac_f32_16x16x64_bf16 v[188:191], v[236:239], a[8:15], v234
v_and_b32 v177, 0xffff0000, v144
MM v[32:35],a[16:19],a[128:131],v[32:35]
TR a[0:1],v192 offset:0
TR a[2:3],v192 offset:512
buffer_load_dwordx4 v254, s[16:19], s87 offen offset:0 lds
MM v[32:35],a[20:23],a[132:135],v[32:35]
s_add_u32 s87, s87, 65536
TR a[4:5],v192 offset:8224
TR a[6:7],v192 offset:8736
v_fma_f32 v170, v148, v176, v170
MM v[40:43],a[20:23],a[140:143],v[40:43]
s_add_u32 m0, s96, 100488
TR a[8:9],v192 offset:8
TR a[10:11],v192 offset:520
MM v[40:43],a[16:19],a[136:139],v[40:43]
TR a[12:13],v192 offset:8232
TR a[14:15],v192 offset:8744
MM v[48:51],a[16:19],a[144:147],v[48:51]
buffer_load_dwordx4 v253, s[16:19], s87 offen offset:0 lds
v_fma_f32 v171, v149, v177, v171
MM v[48:51],a[20:23],a[148:151],v[48:51]
s_add_u32 m0, s96, 101512
v_lshlrev_b32 v148, 16, v135
v_and_b32 v149, 0xffff0000, v135
MM v[56:59],a[20:23],a[156:159],v[56:59]
buffer_load_dwordx4 v254, s[16:19], s87 offen offset:0 lds
MM v[56:59],a[16:19],a[152:155],v[56:59]
s_waitcnt vmcnt(19)
RD a[160:163],v222 offset:32768
v_lshlrev_b32 v176, 16, v145
MM v[36:39],a[24:27],a[128:131],v[36:39]
TR a[16:17],v193 offset:0
TR a[18:19],v193 offset:512
MM v[36:39],a[28:31],a[132:135],v[36:39]
TR a[20:21],v193 offset:8224
TR a[22:23],v193 offset:8736
v_and_b32 v177, 0xffff0000, v145
MM v[44:47],a[28:31],a[140:143],v[44:47]
RD a[164:167],v223 offset:32768
v_fma_f32 v170, v148, v176, v170
MM v[44:47],a[24:27],a[136:139],v[44:47]
RD a[168:171],v222 offset:34816
v_fma_f32 v171, v149, v177, v171
MM v[52:55],a[24:27],a[144:147],v[52:55]
RD a[172:175],v223 offset:34816
v_lshlrev_b32 v148, 16, v136
MM v[52:55],a[28:31],a[148:151],v[52:55]
RD a[176:179],v222 offset:36864
v_and_b32 v149, 0xffff0000, v136
MM v[60:63],a[28:31],a[156:159],v[60:63]
RD a[180:183],v223 offset:36864
v_lshlrev_b32 v176, 16, v146
MM v[60:63],a[24:27],a[152:155],v[60:63]
s_waitcnt lgkmcnt(15)
RD a[184:187],v222 offset:38912
v_and_b32 v177, 0xffff0000, v146
MM v[64:67],a[32:35],a[128:131],v[64:67]
TR a[24:25],v193 offset:8
TR a[26:27],v193 offset:520
MM v[64:67],a[36:39],a[132:135],v[64:67]
TR a[28:29],v193 offset:8232
TR a[30:31],v193 offset:8744
v_fma_f32 v170, v148, v176, v170
MM v[72:75],a[36:39],a[140:143],v[72:75]
RD a[188:191],v223 offset:38912
v_fma_f32 v171, v149, v177, v171
MM v[72:75],a[32:35],a[136:139],v[72:75]
v_lshlrev_b32 v148, 16, v137
v_and_b32 v149, 0xffff0000, v137
MM v[80:83],a[32:35],a[144:147],v[80:83]
v_lshlrev_b32 v176, 16, v147
v_and_b32 v177, 0xffff0000, v147
MM v[80:83],a[36:39],a[148:151],v[80:83]
v_fma_f32 v170, v148, v176, v170
v_fma_f32 v171, v149, v177, v171
MM v[88:91],a[36:39],a[156:159],v[88:91]
s_waitcnt lgkmcnt(0)
s_add_u32 s88, s68, s86
s_add_u32 s86, s86, 128
MM v[88:91],a[32:35],a[152:155],v[88:91]
s_cmp_ge_u32 s86, 1280
s_cselect_b32 s86, 0, s86
s_add_u32 m0, s94, 32768
v_add_f32 v170, v170, v171
MM v[68:71],a[40:43],a[128:131],v[68:71]
s_add_u32 s95, s88, 0
TR a[32:33],v194 offset:0
TR a[34:35],v194 offset:512
MM v[68:71],a[44:47],a[132:135],v[68:71]
TR a[36:37],v194 offset:8224
TR a[38:39],v194 offset:8736
MM v[76:79],a[44:47],a[140:143],v[76:79]
buffer_load_dwordx4 v220, s[32:35], s95 offen offset:0 lds
v_add_f32_dpp v170, v170, v170 row_ror:8 row_mask:0xf bank_mask:0xf
MM v[76:79],a[40:43],a[136:139],v[76:79]
s_add_u32 s95, s88, 9216
v_add_f32_dpp v170, v170, v170 row_ror:4 row_mask:0xf bank_mask:0xf
v_lshl_add_u32 v227, v216, 4, v214
MM v[84:87],a[40:43],a[144:147],v[84:87]
buffer_load_dwordx4 v221, s[32:35], s95 offen offset:1024 lds
MM v[84:87],a[44:47],a[148:151],v[84:87]
s_add_u32 s95, s88, 18432
v_add_f32_dpp v170, v170, v170 row_ror:2 row_mask:0xf bank_mask:0xf
v_lshlrev_b32 v214, 8, v212
MM v[92:95],a[44:47],a[156:159],v[92:95]
v_add_f32_dpp v170, v170, v170 row_ror:1 row_mask:0xf bank_mask:0xf
v_mov_b32 v173, v170
MM v[92:95],a[40:43],a[152:155],v[92:95]
s_nop 0
v_permlane16_swap_b32 v170, v173
v_add_f32 v170, v170, v173
MM v[96:99],a[48:51],a[128:131],v[96:99]
TR a[40:41],v194 offset:8
TR a[42:43],v194 offset:520
MM v[96:99],a[52:55],a[132:135],v[96:99]
TR a[44:45],v194 offset:8232
TR a[46:47],v194 offset:8744
CV v170,v170,v170
MM v[104:107],a[52:55],a[140:143],v[104:107]
buffer_load_dwordx4 v220, s[32:35], s95 offen offset:2048 lds
MM v[104:107],a[48:51],a[136:139],v[104:107]
s_add_u32 s95, s88, 27648
v_ashrrev_i32 v171, 31, v170
v_or_b32 v171, 0x80000000, v171
MM v[112:115],a[48:51],a[144:147],v[112:115]
buffer_load_dwordx4 v221, s[32:35], s95 offen offset:3072 lds
MM v[112:115],a[52:55],a[148:151],v[112:115]
s_sub_u32 s88, s88, s63
s_add_u32 m0, s94, 36864
s_add_u32 s95, s88, 40960
v_xor_b32 v170, v170, v171
MM v[120:123],a[52:55],a[156:159],v[120:123]
v_and_b32 v170, 0xffff0000, v170
v_sub_u32 v171, s79, v180
MM v[120:123],a[48:51],a[152:155],v[120:123]
v_or_b32 v170, v170, v171
v_lshlrev_b32 v173, 2, v180
MM v[100:103],a[56:59],a[128:131],v[100:103]
TR a[48:49],v195 offset:0
TR a[50:51],v195 offset:512
MM v[100:103],a[60:63],a[132:135],v[100:103]
TR a[52:53],v195 offset:8224
TR a[54:55],v195 offset:8736
MM v[108:111],a[60:63],a[140:143],v[108:111]
buffer_load_dwordx4 v220, s[32:35], s95 offen offset:0 lds
s_mov_b32 exec_lo, 1
s_mov_b32 exec_hi, 1
buffer_store_dword v170, v173, s[20:23], s78 offen offset:0 sc1
s_mov_b64 exec, -1
MM v[108:111],a[56:59],a[136:139],v[108:111]
s_add_u32 s95, s88, 50176
v_mov_b32 v171, s69
v_xor_b32 v216, 0, v215
MM v[116:119],a[56:59],a[144:147],v[116:119]
buffer_load_dwordx4 v221, s[32:35], s95 offen offset:1024 lds
MM v[116:119],a[60:63],a[148:151],v[116:119]
s_add_u32 s95, s88, 59392
v_lshl_add_u32 v228, v216, 4, v214
v_xor_b32 v216, 4, v215
MM v[124:127],a[60:63],a[156:159],v[124:127]
v_lshl_add_u32 v229, v216, 4, v214
v_xor_b32 v216, 8, v215
MM v[124:127],a[56:59],a[152:155],v[124:127]
v_lshl_add_u32 v230, v216, 4, v214
v_xor_b32 v216, 12, v215
s_waitcnt vmcnt(22)
s_barrier
MM v[0:3],a[0:3],a[160:163],v[0:3]
TR a[56:57],v195 offset:8
TR a[58:59],v195 offset:520
MM v[0:3],a[4:7],a[164:167],v[0:3]
TR a[60:61],v195 offset:8232
TR a[62:63],v195 offset:8744
MM v[8:11],a[4:7],a[172:175],v[8:11]
buffer_load_dwordx4 v220, s[32:35], s95 offen offset:2048 lds
MM v[8:11],a[0:3],a[168:171],v[8:11]
s_add_u32 s95, s88, 68608
v_lshl_add_u32 v231, v216, 4, v214
s_lshl_b32 s90, s49, 19
MM v[16:19],a[0:3],a[176:179],v[16:19]
buffer_load_dwordx4 v221, s[32:35], s95 offen offset:3072 lds
MM v[16:19],a[4:7],a[180:183],v[16:19]
s_add_u32 s87, s89, s85
s_add_u32 s8, s8, s90
s_addc_u32 s9, s9, 0
MM v[24:27],a[4:7],a[188:191],v[24:27]
s_add_u32 s85, s85, 524288
s_cmp_ge_u32 s85, 5242880
s_cselect_b32 s85, 0, s85
MM v[24:27],a[0:3],a[184:187],v[24:27]
s_add_u32 m0, s96, 114880
s_mov_b32 s10, 524288
s_mov_b32 s11, 131072
MM v[4:7],a[8:11],a[160:163],v[4:7]
buffer_load_dwordx4 v253, s[16:19], s87 offen offset:0 lds
MM v[4:7],a[12:15],a[164:167],v[4:7]
s_add_u32 m0, s96, 115904
s_add_u32 s12, s12, s90
s_addc_u32 s13, s13, 0
MM v[12:15],a[12:15],a[172:175],v[12:15]
s_mov_b32 s14, 525312
s_mov_b32 s15, 131072
MM v[12:15],a[8:11],a[168:171],v[12:15]
s_sub_u32 s12, s12, 1024
s_subb_u32 s13, s13, 0
s_lshr_b32 s90, s49, 3
MM v[20:23],a[8:11],a[176:179],v[20:23]
)ASM"
R"ASM(
s_mul_i32 s90, s90, 1310720
buffer_load_dwordx4 v254, s[16:19], s87 offen offset:0 lds
MM v[20:23],a[12:15],a[180:183],v[20:23]
s_add_u32 s87, s87, 65536
s_add_u32 m0, s96, 116936
s_mul_i32 s77, s84, 320
MM v[28:31],a[12:15],a[188:191],v[28:31]
s_add_u32 s90, s90, s77
s_add_u32 s24, s24, s90
s_addc_u32 s25, s25, 0
MM v[28:31],a[8:11],a[184:187],v[28:31]
s_sub_u32 s26, 5242880, s90
s_mov_b32 s27, 131072
s_mov_b32 s38, 2211840
v_smfmac_f32_16x16x64_bf16 v[184:187], v[236:239], a[0:7], v234
s_mov_b32 s39, 131072
s_mov_b32 s42, 2621440
v_smfmac_f32_16x16x64_bf16 v[188:191], v[236:239], a[8:15], v234
s_mov_b32 s43, 131072
s_mov_b32 s71, 0x4e800000
MM v[32:35],a[16:19],a[160:163],v[32:35]
s_mov_b32 s72, 0x3e0293ee
TR a[0:1],v192 offset:16448
TR a[2:3],v192 offset:16960
buffer_load_dwordx4 v253, s[16:19], s87 offen offset:0 lds
MM v[32:35],a[20:23],a[164:167],v[32:35]
s_add_u32 m0, s96, 117960
TR a[4:5],v192 offset:24672
TR a[6:7],v192 offset:25184
MM v[40:43],a[20:23],a[172:175],v[40:43]
s_lshl_b32 s64, %[swave], 12
TR a[8:9],v192 offset:16456
TR a[10:11],v192 offset:16968
MM v[40:43],a[16:19],a[168:171],v[40:43]
TR a[12:13],v192 offset:24680
TR a[14:15],v192 offset:25192
MM v[48:51],a[16:19],a[176:179],v[48:51]
buffer_load_dwordx4 v254, s[16:19], s87 offen offset:0 lds
MM v[48:51],a[20:23],a[180:183],v[48:51]
s_waitcnt vmcnt(17)
RD a[192:195],v222 offset:0
MM v[56:59],a[20:23],a[188:191],v[56:59]
RD a[196:199],v223 offset:0
MM v[56:59],a[16:19],a[184:187],v[56:59]
RD a[200:203],v222 offset:2048
MM v[36:39],a[24:27],a[160:163],v[36:39]
TR a[16:17],v193 offset:16448
TR a[18:19],v193 offset:16960
MM v[36:39],a[28:31],a[164:167],v[36:39]
TR a[20:21],v193 offset:24672
TR a[22:23],v193 offset:25184
MM v[44:47],a[28:31],a[172:175],v[44:47]
RD a[204:207],v223 offset:2048
MM v[44:47],a[24:27],a[168:171],v[44:47]
RD a[208:211],v222 offset:4096
MM v[52:55],a[24:27],a[176:179],v[52:55]
RD a[212:215],v223 offset:4096
MM v[52:55],a[28:31],a[180:183],v[52:55]
RD a[216:219],v222 offset:6144
MM v[60:63],a[28:31],a[188:191],v[60:63]
RD a[220:223],v223 offset:6144
MM v[60:63],a[24:27],a[184:187],v[60:63]
s_waitcnt lgkmcnt(15)
MM v[64:67],a[32:35],a[160:163],v[64:67]
TR a[24:25],v193 offset:16456
TR a[26:27],v193 offset:16968
MM v[64:67],a[36:39],a[164:167],v[64:67]
TR a[28:29],v193 offset:24680
TR a[30:31],v193 offset:25192
MM v[72:75],a[36:39],a[172:175],v[72:75]
s_waitcnt lgkmcnt(4)
s_add_u32 s88, s68, s86
s_add_u32 s86, s86, 128
MM v[72:75],a[32:35],a[168:171],v[72:75]
s_cmp_ge_u32 s86, 1280
s_cselect_b32 s86, 0, s86
s_add_u32 m0, s94, 0
MM v[80:83],a[32:35],a[176:179],v[80:83]
s_add_u32 s95, s88, 0
buffer_load_dwordx4 v220, s[32:35], s95 offen offset:0 lds
MM v[80:83],a[36:39],a[180:183],v[80:83]
s_add_u32 s95, s88, 9216
MM v[88:91],a[36:39],a[188:191],v[88:91]
buffer_load_dwordx4 v221, s[32:35], s95 offen offset:1024 lds
MM v[88:91],a[32:35],a[184:187],v[88:91]
s_add_u32 s95, s88, 18432
MM v[68:71],a[40:43],a[160:163],v[68:71]
TR a[32:33],v194 offset:16448
TR a[34:35],v194 offset:16960
MM v[68:71],a[44:47],a[164:167],v[68:71]
TR a[36:37],v194 offset:24672
TR a[38:39],v194 offset:25184
MM v[76:79],a[44:47],a[172:175],v[76:79]
buffer_load_dwordx4 v220, s[32:35], s95 offen offset:2048 lds
MM v[76:79],a[40:43],a[168:171],v[76:79]
s_add_u32 s95, s88, 27648
MM v[84:87],a[40:43],a[176:179],v[84:87]
MM v[84:87],a[44:47],a[180:183],v[84:87]
MM v[92:95],a[44:47],a[188:191],v[92:95]
buffer_load_dwordx4 v221, s[32:35], s95 offen offset:3072 lds
MM v[92:95],a[40:43],a[184:187],v[92:95]
s_sub_u32 s88, s88, s63
s_add_u32 m0, s94, 4096
s_add_u32 s95, s88, 40960
MM v[96:99],a[48:51],a[160:163],v[96:99]
TR a[40:41],v194 offset:16456
TR a[42:43],v194 offset:16968
MM v[96:99],a[52:55],a[164:167],v[96:99]
TR a[44:45],v194 offset:24680
TR a[46:47],v194 offset:25192
MM v[104:107],a[52:55],a[172:175],v[104:107]
buffer_load_dwordx4 v220, s[32:35], s95 offen offset:0 lds
MM v[104:107],a[48:51],a[168:171],v[104:107]
s_add_u32 s95, s88, 50176
MM v[112:115],a[48:51],a[176:179],v[112:115]
MM v[112:115],a[52:55],a[180:183],v[112:115]
MM v[120:123],a[52:55],a[188:191],v[120:123]
buffer_load_dwordx4 v221, s[32:35], s95 offen offset:1024 lds
MM v[120:123],a[48:51],a[184:187],v[120:123]
s_add_u32 s95, s88, 59392
MM v[100:103],a[56:59],a[160:163],v[100:103]
TR a[48:49],v195 offset:16448
TR a[50:51],v195 offset:16960
MM v[100:103],a[60:63],a[164:167],v[100:103]
TR a[52:53],v195 offset:24672
TR a[54:55],v195 offset:25184
MM v[108:111],a[60:63],a[172:175],v[108:111]
buffer_load_dwordx4 v220, s[32:35], s95 offen offset:2048 lds
MM v[108:111],a[56:59],a[168:171],v[108:111]
s_add_u32 s95, s88, 68608
MM v[116:119],a[56:59],a[176:179],v[116:119]
MM v[116:119],a[60:63],a[180:183],v[116:119]
MM v[124:127],a[60:63],a[188:191],v[124:127]
buffer_load_dwordx4 v221, s[32:35], s95 offen offset:3072 lds
MM v[124:127],a[56:59],a[184:187],v[124:127]
s_add_u32 s87, s89, s85
s_add_u32 s85, s85, 524288
s_waitcnt vmcnt(21)
s_barrier
MM v[0:3],a[0:3],a[192:195],v[0:3]
TR a[56:57],v195 offset:16456
TR a[58:59],v195 offset:16968
MM v[0:3],a[4:7],a[196:199],v[0:3]
TR a[60:61],v195 offset:24680
TR a[62:63],v195 offset:25192
s_cmp_ge_u32 s85, 5242880
s_cselect_b32 s85, 0, s85
MM v[8:11],a[4:7],a[204:207],v[8:11]
s_add_u32 m0, s96, 131328
s_nop 0
buffer_load_dwordx4 v253, s[16:19], s87 offen offset:0 lds
v_and_b32 v208, 240, v248
MM v[8:11],a[0:3],a[200:203],v[8:11]
s_add_u32 m0, s96, 132352
s_lshl_b32 s62, s62, 8
s_add_u32 s93, s93, 36864
MM v[16:19],a[0:3],a[208:211],v[16:19]
s_add_u32 s62, s62, s93
s_xor_b32 s93, s62, 256
MM v[16:19],a[4:7],a[212:215],v[16:19]
MM v[24:27],a[4:7],a[220:223],v[24:27]
buffer_load_dwordx4 v254, s[16:19], s87 offen offset:0 lds
MM v[24:27],a[0:3],a[216:219],v[24:27]
s_add_u32 s87, s87, 65536
s_add_u32 m0, s96, 133384
MM v[4:7],a[8:11],a[192:195],v[4:7]
MM v[4:7],a[12:15],a[196:199],v[4:7]
MM v[12:15],a[12:15],a[204:207],v[12:15]
buffer_load_dwordx4 v253, s[16:19], s87 offen offset:0 lds
MM v[12:15],a[8:11],a[200:203],v[12:15]
s_add_u32 m0, s96, 134408
MM v[20:23],a[8:11],a[208:211],v[20:23]
MM v[20:23],a[12:15],a[212:215],v[20:23]
MM v[28:31],a[12:15],a[220:223],v[28:31]
buffer_load_dwordx4 v254, s[16:19], s87 offen offset:0 lds
MM v[28:31],a[8:11],a[216:219],v[28:31]
s_waitcnt vmcnt(16)
RD a[224:227],v222 offset:32768
v_smfmac_f32_16x16x64_bf16 v[184:187], v[236:239], a[0:7], v234
RD a[228:231],v223 offset:32768
v_smfmac_f32_16x16x64_bf16 v[188:191], v[236:239], a[8:15], v234
RD a[232:235],v222 offset:34816
MM v[32:35],a[16:19],a[192:195],v[32:35]
TR a[0:1],v192 offset:32896
TR a[2:3],v192 offset:33408
MM v[32:35],a[20:23],a[196:199],v[32:35]
TR a[4:5],v192 offset:41120
TR a[6:7],v192 offset:41632
MM v[40:43],a[20:23],a[204:207],v[40:43]
TR a[8:9],v192 offset:32904
TR a[10:11],v192 offset:33416
RD a[236:239],v223 offset:34816
buffer_load_dwordx4 v[200:203], v208, s[20:23], s62 offen offset:0 sc1
MM v[40:43],a[16:19],a[200:203],v[40:43]
TR a[12:13],v192 offset:41128
TR a[14:15],v192 offset:41640
MM v[48:51],a[16:19],a[208:211],v[48:51]
buffer_load_dwordx4 v[204:207], v208, s[20:23], s93 offen offset:0 sc1
MM v[48:51],a[20:23],a[212:215],v[48:51]
RD a[240:243],v222 offset:36864
MM v[56:59],a[20:23],a[220:223],v[56:59]
RD a[244:247],v223 offset:36864
MM v[56:59],a[16:19],a[216:219],v[56:59]
s_waitcnt lgkmcnt(15)
RD a[248:251],v222 offset:38912
MM v[36:39],a[24:27],a[192:195],v[36:39]
TR a[16:17],v193 offset:32896
TR a[18:19],v193 offset:33408
MM v[36:39],a[28:31],a[196:199],v[36:39]
TR a[20:21],v193 offset:41120
TR a[22:23],v193 offset:41632
MM v[44:47],a[28:31],a[204:207],v[44:47]
RD a[252:255],v223 offset:38912
MM v[44:47],a[24:27],a[200:203],v[44:47]
MM v[52:55],a[24:27],a[208:211],v[52:55]
MM v[52:55],a[28:31],a[212:215],v[52:55]
MM v[60:63],a[28:31],a[220:223],v[60:63]
s_waitcnt lgkmcnt(0)
s_add_u32 s88, s68, s86
s_add_u32 s86, s86, 128
MM v[60:63],a[24:27],a[216:219],v[60:63]
s_cmp_ge_u32 s86, 1280
s_cselect_b32 s86, 0, s86
s_add_u32 m0, s94, 32768
MM v[64:67],a[32:35],a[192:195],v[64:67]
s_add_u32 s95, s88, 0
TR a[24:25],v193 offset:32904
TR a[26:27],v193 offset:33416
MM v[64:67],a[36:39],a[196:199],v[64:67]
TR a[28:29],v193 offset:41128
TR a[30:31],v193 offset:41640
MM v[72:75],a[36:39],a[204:207],v[72:75]
buffer_load_dwordx4 v220, s[32:35], s95 offen offset:0 lds
MM v[72:75],a[32:35],a[200:203],v[72:75]
s_add_u32 s95, s88, 9216
MM v[80:83],a[32:35],a[208:211],v[80:83]
buffer_load_dwordx4 v221, s[32:35], s95 offen offset:1024 lds
MM v[80:83],a[36:39],a[212:215],v[80:83]
s_add_u32 s95, s88, 18432
MM v[88:91],a[36:39],a[220:223],v[88:91]
MM v[88:91],a[32:35],a[216:219],v[88:91]
MM v[68:71],a[40:43],a[192:195],v[68:71]
TR a[32:33],v194 offset:32896
TR a[34:35],v194 offset:33408
MM v[68:71],a[44:47],a[196:199],v[68:71]
TR a[36:37],v194 offset:41120
TR a[38:39],v194 offset:41632
MM v[76:79],a[44:47],a[204:207],v[76:79]
buffer_load_dwordx4 v220, s[32:35], s95 offen offset:2048 lds
MM v[76:79],a[40:43],a[200:203],v[76:79]
s_add_u32 s95, s88, 27648
MM v[84:87],a[40:43],a[208:211],v[84:87]
buffer_load_dwordx4 v221, s[32:35], s95 offen offset:3072 lds
MM v[84:87],a[44:47],a[212:215],v[84:87]
s_waitcnt vmcnt(10)
RD a[128:131],v222 offset:0
MM v[92:95],a[44:47],a[220:223],v[92:95]
RD a[132:135],v223 offset:0
MM v[92:95],a[40:43],a[216:219],v[92:95]
RD a[136:139],v222 offset:2048
MM v[96:99],a[48:51],a[192:195],v[96:99]
TR a[40:41],v194 offset:32904
TR a[42:43],v194 offset:33416
MM v[96:99],a[52:55],a[196:199],v[96:99]
TR a[44:45],v194 offset:41128
TR a[46:47],v194 offset:41640
MM v[104:107],a[52:55],a[204:207],v[104:107]
RD a[140:143],v223 offset:2048
MM v[104:107],a[48:51],a[200:203],v[104:107]
RD a[144:147],v222 offset:4096
MM v[112:115],a[48:51],a[208:211],v[112:115]
RD a[148:151],v223 offset:4096
MM v[112:115],a[52:55],a[212:215],v[112:115]
RD a[152:155],v222 offset:6144
MM v[120:123],a[52:55],a[220:223],v[120:123]
RD a[156:159],v223 offset:6144
MM v[120:123],a[48:51],a[216:219],v[120:123]
s_sub_u32 s88, s88, s63
s_add_u32 m0, s94, 36864
s_add_u32 s95, s88, 40960
MM v[100:103],a[56:59],a[192:195],v[100:103]
TR a[48:49],v195 offset:32896
TR a[50:51],v195 offset:33408
MM v[100:103],a[60:63],a[196:199],v[100:103]
TR a[52:53],v195 offset:41120
TR a[54:55],v195 offset:41632
buffer_load_dwordx4 v220, s[32:35], s95 offen offset:0 lds
MM v[108:111],a[60:63],a[204:207],v[108:111]
s_add_u32 s95, s88, 50176
MM v[108:111],a[56:59],a[200:203],v[108:111]
buffer_load_dwordx4 v221, s[32:35], s95 offen offset:1024 lds
MM v[116:119],a[56:59],a[208:211],v[116:119]
s_add_u32 s95, s88, 59392
MM v[116:119],a[60:63],a[212:215],v[116:119]
MM v[124:127],a[60:63],a[220:223],v[124:127]
LPG3_%=:
s_cbranch_execz LPG7_%=
MM v[124:127],a[56:59],a[216:219],v[124:127]
buffer_load_dwordx4 v220, s[32:35], s95 offen offset:2048 lds
s_add_u32 s95, s88, 68608
buffer_load_dwordx4 v221, s[32:35], s95 offen offset:3072 lds
s_waitcnt lgkmcnt(4)
s_waitcnt vmcnt(10)
s_barrier
MM v[0:3],a[0:3],a[224:227],v[0:3]
s_add_u32 s77, s64, 0
TR a[56:57],v195 offset:32904
TR a[58:59],v195 offset:33416
MM v[0:3],a[4:7],a[228:231],v[0:3]
s_add_u32 m0, s65, 0
TR a[60:61],v195 offset:41128
TR a[62:63],v195 offset:41640
MM v[8:11],a[4:7],a[236:239],v[8:11]
s_waitcnt vmcnt(8)
TR a[64:65],v196 offset:0
TR a[66:67],v196 offset:512
MM v[8:11],a[0:3],a[232:235],v[8:11]
s_lshl_b32 s76, %[swave], 7
v_lshrrev_b32 v253, 8, v248
v_lshlrev_b32 v253, 5, v253
MM v[16:19],a[0:3],a[240:243],v[16:19]
s_add_u32 s76, s76, %[slds]
TR a[68:69],v196 offset:8224
TR a[70:71],v196 offset:8736
v_xor_b32 v201, s69, v201
MM v[16:19],a[4:7],a[244:247],v[16:19]
buffer_load_dwordx4 v228, s[8:11], s77 offen offset:0 lds
s_mov_b64 exec, 1
buffer_store_dword v171, off, s[20:23], s80 sc1
s_mov_b64 exec, -1
v_add_u32 v253, s76, v253
v_add_u32 v253, 151552, v253
MM v[24:27],a[4:7],a[252:255],v[24:27]
TR a[72:73],v196 offset:16448
TR a[74:75],v196 offset:16960
MM v[24:27],a[0:3],a[248:251],v[24:27]
TR a[76:77],v196 offset:24672
TR a[78:79],v196 offset:25184
v_xor_b32 v203, s69, v203
MM v[4:7],a[8:11],a[224:227],v[4:7]
TR a[80:81],v196 offset:8
)ASM"
R"ASM(
TR a[82:83],v196 offset:520
MM v[4:7],a[12:15],a[228:231],v[4:7]
TR a[84:85],v196 offset:8232
TR a[86:87],v196 offset:8744
buffer_load_dwordx4 v229, s[8:11], s77 offen offset:1024 lds
MM v[12:15],a[12:15],a[236:239],v[12:15]
TR a[88:89],v196 offset:16456
TR a[90:91],v196 offset:16968
v_xor_b32 v205, s69, v205
MM v[12:15],a[8:11],a[232:235],v[12:15]
TR a[92:93],v196 offset:24680
TR a[94:95],v196 offset:25192
MM v[20:23],a[8:11],a[240:243],v[20:23]
TR a[96:97],v197 offset:0
TR a[98:99],v197 offset:512
MM v[20:23],a[12:15],a[244:247],v[20:23]
TR a[100:101],v197 offset:8224
TR a[102:103],v197 offset:8736
buffer_load_dwordx4 v230, s[8:11], s77 offen offset:2048 lds
v_xor_b32 v207, s69, v207
MM v[28:31],a[12:15],a[252:255],v[28:31]
TR a[104:105],v197 offset:16448
TR a[106:107],v197 offset:16960
MM v[28:31],a[8:11],a[248:251],v[28:31]
TR a[108:109],v197 offset:24672
TR a[110:111],v197 offset:25184
v_smfmac_f32_16x16x64_bf16 v[184:187], v[236:239], a[0:7], v234
TR a[112:113],v197 offset:8
v_or3_b32 v209, v201, v203, v205
v_smfmac_f32_16x16x64_bf16 v[188:191], v[236:239], a[8:15], v234
TR a[114:115],v197 offset:520
buffer_load_dwordx4 v231, s[8:11], s77 offen offset:3072 lds
MM v[32:35],a[16:19],a[224:227],v[32:35]
s_add_u32 s77, s64, 16384
TR a[116:117],v197 offset:8232
TR a[118:119],v197 offset:8744
MM v[32:35],a[20:23],a[228:231],v[32:35]
s_add_u32 m0, s65, 16384
TR a[120:121],v197 offset:16456
TR a[122:123],v197 offset:16968
v_or_b32 v209, v209, v207
MM v[40:43],a[20:23],a[236:239],v[40:43]
TR a[124:125],v197 offset:24680
TR a[126:127],v197 offset:25192
MM v[40:43],a[16:19],a[232:235],v[40:43]
TR a[0:1],v198 offset:0
TR a[2:3],v198 offset:512
MM v[48:51],a[16:19],a[240:243],v[48:51]
TR a[4:5],v198 offset:8224
TR a[6:7],v198 offset:8736
v_cmp_ne_u32 vcc, 0, v209
s_cbranch_vccnz LRG1o_%=
LRG1ob_%=:
MM v[48:51],a[20:23],a[244:247],v[48:51]
TR a[8:9],v198 offset:16448
TR a[10:11],v198 offset:16960
buffer_load_dwordx4 v228, s[8:11], s77 offen offset:0 lds
MM v[56:59],a[20:23],a[252:255],v[56:59]
TR a[12:13],v198 offset:24672
TR a[14:15],v198 offset:25184
MM v[56:59],a[16:19],a[248:251],v[56:59]
v_lshlrev_b32 v240, 16, v200
v_and_b32 v200, 0xffff0000, v200
MM v[36:39],a[24:27],a[224:227],v[36:39]
v_rcp_f32 v244, v200
MM v[36:39],a[28:31],a[228:231],v[36:39]
v_xor_b32 v240, 0x80000000, v240
v_mul_f32 v244, s72, v244
MM v[44:47],a[28:31],a[236:239],v[44:47]
v_lshlrev_b32 v241, 16, v202
v_and_b32 v202, 0xffff0000, v202
MM v[44:47],a[24:27],a[232:235],v[44:47]
v_rcp_f32 v245, v202
MM v[52:55],a[24:27],a[240:243],v[52:55]
v_xor_b32 v241, 0x80000000, v241
v_mul_f32 v245, s72, v245
MM v[52:55],a[28:31],a[244:247],v[52:55]
buffer_load_dwordx4 v229, s[8:11], s77 offen offset:1024 lds
MM v[60:63],a[28:31],a[252:255],v[60:63]
v_lshlrev_b32 v242, 16, v204
v_and_b32 v204, 0xffff0000, v204
MM v[60:63],a[24:27],a[248:251],v[60:63]
v_rcp_f32 v246, v204
MM v[64:67],a[32:35],a[224:227],v[64:67]
TR a[16:17],v198 offset:8
TR a[18:19],v198 offset:520
MM v[64:67],a[36:39],a[228:231],v[64:67]
TR a[20:21],v198 offset:8232
TR a[22:23],v198 offset:8744
v_xor_b32 v242, 0x80000000, v242
MM v[72:75],a[36:39],a[236:239],v[72:75]
TR a[24:25],v198 offset:16456
TR a[26:27],v198 offset:16968
MM v[72:75],a[32:35],a[232:235],v[72:75]
TR a[28:29],v198 offset:24680
TR a[30:31],v198 offset:25192
MM v[80:83],a[32:35],a[240:243],v[80:83]
v_mul_f32 v246, s72, v246
v_lshlrev_b32 v243, 16, v206
MM v[80:83],a[36:39],a[244:247],v[80:83]
v_and_b32 v206, 0xffff0000, v206
MM v[88:91],a[36:39],a[252:255],v[88:91]
v_rcp_f32 v247, v206
MM v[88:91],a[32:35],a[248:251],v[88:91]
s_waitcnt lgkmcnt(15)
buffer_load_dwordx4 v230, s[8:11], s77 offen offset:2048 lds
v_smfmac_f32_16x16x64_bf16 v[184:187], v[236:239], a[64:71], v234
v_xor_b32 v243, 0x80000000, v243
v_smfmac_f32_16x16x64_bf16 v[184:187], v[236:239], a[72:79], v234
v_mul_f32 v247, s72, v247
v_smfmac_f32_16x16x64_bf16 v[188:191], v[236:239], a[80:87], v234
v_smfmac_f32_16x16x64_bf16 v[188:191], v[236:239], a[88:95], v234
MM v[68:71],a[40:43],a[224:227],v[68:71]
v_bfe_u32 v183, v248, 6, 2
v_lshlrev_b32 v183, 5, v183
MM v[68:71],a[44:47],a[228:231],v[68:71]
v_bfe_u32 v182, v248, 4, 2
v_lshl_add_u32 v183, v182, 2, v183
MM v[76:79],a[44:47],a[236:239],v[76:79]
v_add_u32 v183, s76, v183
v_add_u32 v183, 151552, v183
MM v[76:79],a[40:43],a[232:235],v[76:79]
MM v[84:87],a[40:43],a[240:243],v[84:87]
MM v[84:87],a[44:47],a[244:247],v[84:87]
buffer_load_dwordx4 v231, s[8:11], s77 offen offset:3072 lds
MM v[92:95],a[44:47],a[252:255],v[92:95]
s_waitcnt vmcnt(9)
RD a[160:163],v222 offset:32768
MM v[92:95],a[40:43],a[248:251],v[92:95]
v_add_f32 v184, v184, v185
v_add_f32 v188, v188, v189
MM v[96:99],a[48:51],a[224:227],v[96:99]
TR a[32:33],v199 offset:0
TR a[34:35],v199 offset:512
MM v[96:99],a[52:55],a[228:231],v[96:99]
RD a[164:167],v223 offset:32768
ds_write_b32 v183, v184 offset:0
ds_write_b32 v183, v188 offset:16
MM v[104:107],a[52:55],a[236:239],v[104:107]
TR a[36:37],v199 offset:8224
TR a[38:39],v199 offset:8736
MM v[104:107],a[48:51],a[232:235],v[104:107]
TR a[40:41],v199 offset:16448
TR a[42:43],v199 offset:16960
MM v[112:115],a[48:51],a[240:243],v[112:115]
TR a[44:45],v199 offset:24672
TR a[46:47],v199 offset:25184
RD a[168:171],v222 offset:34816
MM v[112:115],a[52:55],a[244:247],v[112:115]
RD a[172:175],v223 offset:34816
MM v[120:123],a[52:55],a[252:255],v[120:123]
RD a[176:179],v222 offset:36864
MM v[120:123],a[48:51],a[248:251],v[120:123]
RD a[180:183],v223 offset:36864
MM v[100:103],a[56:59],a[224:227],v[100:103]
RD a[184:187],v222 offset:38912
MM v[100:103],a[60:63],a[228:231],v[100:103]
RD a[188:191],v223 offset:38912
MM v[108:111],a[60:63],a[236:239],v[108:111]
s_add_u32 s77, s64, 0
s_add_u32 s81, s77, 896
s_add_u32 m0, s65, 65536
MM v[108:111],a[56:59],a[232:235],v[108:111]
buffer_load_dwordx4 v235, s[12:15], s81 offen offset:0 lds
MM v[116:119],a[56:59],a[240:243],v[116:119]
MM v[116:119],a[60:63],a[244:247],v[116:119]
MM v[124:127],a[60:63],a[252:255],v[124:127]
MM v[124:127],a[56:59],a[248:251],v[124:127]
s_waitcnt lgkmcnt(0)
s_barrier
MM v[0:3],a[64:67],a[128:131],v[0:3]
s_memrealtime s[52:53]
TR a[48:49],v199 offset:8
TR a[50:51],v199 offset:520
MM v[0:3],a[68:71],a[132:135],v[0:3]
s_memtime s[54:55]
TR a[52:53],v199 offset:8232
TR a[54:55],v199 offset:8744
MM v[0:3],a[72:75],a[160:163],v[0:3]
TR a[56:57],v199 offset:16456
TR a[58:59],v199 offset:16968
MM v[0:3],a[76:79],a[164:167],v[0:3]
TR a[60:61],v199 offset:24680
TR a[62:63],v199 offset:25192
buffer_load_dwordx4 v235, s[12:15], s77 offen offset:1024 lds
MM v[8:11],a[76:79],a[172:175],v[8:11]
v_xor_b32 v151, 0, v253
v_xor_b32 v143, 0, v253
MM v[8:11],a[72:75],a[168:171],v[8:11]
RD v[144:147],v151 offset:0
RD v[148:151],v151 offset:16
MM v[8:11],a[68:71],a[140:143],v[8:11]
RD v[136:139],v143 offset:0
RD v[140:143],v143 offset:16
MM v[8:11],a[64:67],a[136:139],v[8:11]
MM v[4:7],a[80:83],a[128:131],v[4:7]
MM v[4:7],a[84:87],a[132:135],v[4:7]
s_waitcnt lgkmcnt(2)
MM v[4:7],a[88:91],a[160:163],v[4:7]
s_waitcnt lgkmcnt(0)
MM v[4:7],a[92:95],a[164:167],v[4:7]
buffer_load_dwordx4 v235, s[12:15], s81 offen offset:2048 lds
MM v[12:15],a[92:95],a[172:175],v[12:15]
MM v[12:15],a[88:91],a[168:171],v[12:15]
MM v[12:15],a[84:87],a[140:143],v[12:15]
MM v[12:15],a[80:83],a[136:139],v[12:15]
MM v[32:35],a[96:99],a[128:131],v[32:35]
MM v[32:35],a[100:103],a[132:135],v[32:35]
MM v[32:35],a[104:107],a[160:163],v[32:35]
MM v[32:35],a[108:111],a[164:167],v[32:35]
v_fma_f32 v0, v240, v144, v0
v_fma_f32 v1, v240, v145, v1
v_fma_f32 v2, v240, v146, v2
MM v[40:43],a[108:111],a[172:175],v[40:43]
v_fma_f32 v3, v240, v147, v3
v_fma_f32 v4, v240, v148, v4
MM v[40:43],a[104:107],a[168:171],v[40:43]
buffer_load_dwordx4 v235, s[12:15], s77 offen offset:3072 lds
v_fma_f32 v5, v240, v149, v5
v_fma_f32 v6, v240, v150, v6
MM v[40:43],a[100:103],a[140:143],v[40:43]
s_add_u32 s77, s64, 32768
s_add_u32 m0, s65, 32768
v_fma_f32 v7, v240, v151, v7
v_mul_f32 v0, v244, v0
v_mul_f32 v1, v244, v1
MM v[40:43],a[96:99],a[136:139],v[40:43]
v_mul_f32 v2, v244, v2
v_mul_f32 v3, v244, v3
MM v[36:39],a[112:115],a[128:131],v[36:39]
v_mul_f32 v4, v244, v4
v_mul_f32 v5, v244, v5
MM v[36:39],a[116:119],a[132:135],v[36:39]
v_mul_f32 v6, v244, v6
v_mul_f32 v7, v244, v7
CV v160,v0,v1
MM v[36:39],a[120:123],a[160:163],v[36:39]
CV v161,v2,v3
CV v162,v4,v5
MM v[36:39],a[124:127],a[164:167],v[36:39]
CV v163,v6,v7
v_fma_f32 v8, v241, v144, v8
MM v[44:47],a[124:127],a[172:175],v[44:47]
v_fma_f32 v9, v241, v145, v9
v_fma_f32 v10, v241, v146, v10
v_fma_f32 v11, v241, v147, v11
MM v[44:47],a[120:123],a[168:171],v[44:47]
v_fma_f32 v12, v241, v148, v12
v_fma_f32 v13, v241, v149, v13
MM v[44:47],a[116:119],a[140:143],v[44:47]
v_fma_f32 v14, v241, v150, v14
v_fma_f32 v15, v241, v151, v15
v_mul_f32 v8, v245, v8
MM v[44:47],a[112:115],a[136:139],v[44:47]
buffer_load_dwordx4 v228, s[8:11], s77 offen offset:0 lds
v_mul_f32 v9, v245, v9
v_mul_f32 v10, v245, v10
MM v[64:67],a[0:3],a[128:131],v[64:67]
v_mul_f32 v11, v245, v11
v_mul_f32 v12, v245, v12
MM v[64:67],a[4:7],a[132:135],v[64:67]
v_mul_f32 v13, v245, v13
v_mul_f32 v14, v245, v14
v_mul_f32 v15, v245, v15
MM v[64:67],a[8:11],a[160:163],v[64:67]
CV v164,v8,v9
CV v165,v10,v11
MM v[64:67],a[12:15],a[164:167],v[64:67]
CV v166,v12,v13
CV v167,v14,v15
MM v[72:75],a[12:15],a[172:175],v[72:75]
v_xor_b32 v159, 128, v253
MM v[72:75],a[8:11],a[168:171],v[72:75]
RD v[152:155],v159 offset:0
RD v[156:159],v159 offset:16
MM v[72:75],a[4:7],a[140:143],v[72:75]
MM v[72:75],a[0:3],a[136:139],v[72:75]
MM v[68:71],a[16:19],a[128:131],v[68:71]
MM v[68:71],a[20:23],a[132:135],v[68:71]
s_waitcnt lgkmcnt(0)
buffer_load_dwordx4 v229, s[8:11], s77 offen offset:1024 lds
v_fma_f32 v32, v240, v152, v32
v_fma_f32 v33, v240, v153, v33
v_fma_f32 v34, v240, v154, v34
v_fma_f32 v35, v240, v155, v35
v_fma_f32 v36, v240, v156, v36
v_fma_f32 v37, v240, v157, v37
v_fma_f32 v38, v240, v158, v38
v_fma_f32 v39, v240, v159, v39
v_mul_f32 v32, v244, v32
v_mul_f32 v33, v244, v33
v_mul_f32 v34, v244, v34
MM v[68:71],a[24:27],a[160:163],v[68:71]
v_mul_f32 v35, v244, v35
v_mul_f32 v36, v244, v36
MM v[68:71],a[28:31],a[164:167],v[68:71]
v_mul_f32 v37, v244, v37
v_mul_f32 v38, v244, v38
v_mul_f32 v39, v244, v39
MM v[76:79],a[28:31],a[172:175],v[76:79]
CV v176,v32,v33
CV v177,v34,v35
MM v[76:79],a[24:27],a[168:171],v[76:79]
CV v178,v36,v37
CV v179,v38,v39
MM v[76:79],a[20:23],a[140:143],v[76:79]
v_fma_f32 v40, v241, v152, v40
v_fma_f32 v41, v241, v153, v41
v_fma_f32 v42, v241, v154, v42
MM v[76:79],a[16:19],a[136:139],v[76:79]
v_fma_f32 v43, v241, v155, v43
v_fma_f32 v44, v241, v156, v44
MM v[96:99],a[32:35],a[128:131],v[96:99]
v_fma_f32 v45, v241, v157, v45
v_fma_f32 v46, v241, v158, v46
MM v[96:99],a[36:39],a[132:135],v[96:99]
v_fma_f32 v47, v241, v159, v47
v_mul_f32 v40, v245, v40
v_mul_f32 v41, v245, v41
MM v[96:99],a[40:43],a[160:163],v[96:99]
v_mul_f32 v42, v245, v42
v_mul_f32 v43, v245, v43
MM v[96:99],a[44:47],a[164:167],v[96:99]
buffer_load_dwordx4 v230, s[8:11], s77 offen offset:2048 lds
v_mul_f32 v44, v245, v44
v_mul_f32 v45, v245, v45
MM v[104:107],a[44:47],a[172:175],v[104:107]
v_mul_f32 v46, v245, v46
v_mul_f32 v47, v245, v47
CV v180,v40,v41
MM v[104:107],a[40:43],a[168:171],v[104:107]
CV v181,v42,v43
CV v182,v44,v45
MM v[104:107],a[36:39],a[140:143],v[104:107]
CV v183,v46,v47
v_xor_b32 v151, 256, v253
MM v[104:107],a[32:35],a[136:139],v[104:107]
RD v[144:147],v151 offset:0
RD v[148:151],v151 offset:16
MM v[100:103],a[48:51],a[128:131],v[100:103]
MM v[100:103],a[52:55],a[132:135],v[100:103]
MM v[100:103],a[56:59],a[160:163],v[100:103]
MM v[100:103],a[60:63],a[164:167],v[100:103]
s_waitcnt lgkmcnt(0)
v_fma_f32 v64, v240, v144, v64
v_fma_f32 v65, v240, v145, v65
v_fma_f32 v66, v240, v146, v66
v_fma_f32 v67, v240, v147, v67
v_fma_f32 v68, v240, v148, v68
v_fma_f32 v69, v240, v149, v69
v_fma_f32 v70, v240, v150, v70
v_fma_f32 v71, v240, v151, v71
v_mul_f32 v64, v244, v64
v_mul_f32 v65, v244, v65
MM v[108:111],a[60:63],a[172:175],v[108:111]
v_mul_f32 v66, v244, v66
v_mul_f32 v67, v244, v67
)ASM"
R"ASM(
MM v[108:111],a[56:59],a[168:171],v[108:111]
buffer_load_dwordx4 v231, s[8:11], s77 offen offset:3072 lds
v_mul_f32 v68, v244, v68
v_mul_f32 v69, v244, v69
MM v[108:111],a[52:55],a[140:143],v[108:111]
s_add_u32 s77, s64, 16384
s_add_u32 s81, s77, 896
s_add_u32 m0, s65, 81920
v_mul_f32 v70, v244, v70
v_mul_f32 v71, v244, v71
CV v192,v64,v65
MM v[108:111],a[48:51],a[136:139],v[108:111]
CV v193,v66,v67
CV v194,v68,v69
MM v[16:19],a[64:67],a[144:147],v[16:19]
CV v195,v70,v71
v_fma_f32 v72, v241, v144, v72
MM v[16:19],a[68:71],a[148:151],v[16:19]
v_fma_f32 v73, v241, v145, v73
v_fma_f32 v74, v241, v146, v74
v_fma_f32 v75, v241, v147, v75
MM v[16:19],a[72:75],a[176:179],v[16:19]
v_fma_f32 v76, v241, v148, v76
v_fma_f32 v77, v241, v149, v77
MM v[16:19],a[76:79],a[180:183],v[16:19]
v_fma_f32 v78, v241, v150, v78
v_fma_f32 v79, v241, v151, v79
v_mul_f32 v72, v245, v72
MM v[24:27],a[76:79],a[188:191],v[24:27]
v_mul_f32 v73, v245, v73
v_mul_f32 v74, v245, v74
MM v[24:27],a[72:75],a[184:187],v[24:27]
v_mul_f32 v75, v245, v75
v_mul_f32 v76, v245, v76
MM v[24:27],a[68:71],a[156:159],v[24:27]
s_waitcnt vmcnt(12)
s_barrier
RD a[192:195],v224 offset:0
v_mul_f32 v77, v245, v77
v_mul_f32 v78, v245, v78
v_mul_f32 v79, v245, v79
MM v[24:27],a[64:67],a[152:155],v[24:27]
buffer_load_dwordx4 v235, s[12:15], s81 offen offset:0 lds
CV v196,v72,v73
CV v197,v74,v75
MM v[20:23],a[80:83],a[144:147],v[20:23]
RD a[196:199],v225 offset:0
RD a[200:203],v226 offset:0
CV v198,v76,v77
CV v199,v78,v79
MM v[20:23],a[84:87],a[148:151],v[20:23]
RD a[204:207],v227 offset:0
RD a[208:211],v224 offset:4096
v_xor_b32 v159, 384, v253
MM v[20:23],a[88:91],a[176:179],v[20:23]
RD a[212:215],v225 offset:4096
RD a[216:219],v226 offset:4096
RD v[152:155],v159 offset:0
RD v[156:159],v159 offset:16
MM v[20:23],a[92:95],a[180:183],v[20:23]
RD a[220:223],v227 offset:4096
RD a[224:227],v224 offset:8192
MM v[28:31],a[92:95],a[188:191],v[28:31]
RD a[228:231],v225 offset:8192
RD a[232:235],v226 offset:8192
MM v[28:31],a[88:91],a[184:187],v[28:31]
RD a[236:239],v227 offset:8192
RD a[240:243],v224 offset:12288
MM v[28:31],a[84:87],a[156:159],v[28:31]
s_waitcnt lgkmcnt(6)
RD a[244:247],v225 offset:12288
RD a[248:251],v226 offset:12288
v_fma_f32 v96, v240, v152, v96
v_fma_f32 v97, v240, v153, v97
v_fma_f32 v98, v240, v154, v98
v_fma_f32 v99, v240, v155, v99
v_fma_f32 v100, v240, v156, v100
v_fma_f32 v101, v240, v157, v101
v_fma_f32 v102, v240, v158, v102
v_fma_f32 v103, v240, v159, v103
v_mul_f32 v96, v244, v96
v_mul_f32 v97, v244, v97
v_mul_f32 v98, v244, v98
MM v[28:31],a[80:83],a[152:155],v[28:31]
RD a[252:255],v227 offset:12288
v_mul_f32 v99, v244, v99
v_mul_f32 v100, v244, v100
MM v[48:51],a[96:99],a[144:147],v[48:51]
v_mul_f32 v101, v244, v101
v_mul_f32 v102, v244, v102
v_mul_f32 v103, v244, v103
MM v[48:51],a[100:103],a[148:151],v[48:51]
buffer_load_dwordx4 v235, s[12:15], s77 offen offset:1024 lds
CV v208,v96,v97
CV v209,v98,v99
MM v[48:51],a[104:107],a[176:179],v[48:51]
CV v210,v100,v101
CV v211,v102,v103
MM v[48:51],a[108:111],a[180:183],v[48:51]
v_fma_f32 v104, v241, v152, v104
v_fma_f32 v105, v241, v153, v105
v_fma_f32 v106, v241, v154, v106
MM v[56:59],a[108:111],a[188:191],v[56:59]
v_fma_f32 v107, v241, v155, v107
v_fma_f32 v108, v241, v156, v108
MM v[56:59],a[104:107],a[184:187],v[56:59]
v_fma_f32 v109, v241, v157, v109
v_fma_f32 v110, v241, v158, v110
MM v[56:59],a[100:103],a[156:159],v[56:59]
v_fma_f32 v111, v241, v159, v111
v_mul_f32 v104, v245, v104
v_mul_f32 v105, v245, v105
MM v[56:59],a[96:99],a[152:155],v[56:59]
v_mul_f32 v106, v245, v106
v_mul_f32 v107, v245, v107
MM v[52:55],a[112:115],a[144:147],v[52:55]
v_mul_f32 v108, v245, v108
v_mul_f32 v109, v245, v109
MM v[52:55],a[116:119],a[148:151],v[52:55]
v_mul_f32 v110, v245, v110
v_mul_f32 v111, v245, v111
CV v212,v104,v105
MM v[52:55],a[120:123],a[176:179],v[52:55]
CV v213,v106,v107
CV v214,v108,v109
CV v215,v110,v111
MM v[52:55],a[124:127],a[180:183],v[52:55]
buffer_load_dwordx4 v235, s[12:15], s81 offen offset:2048 lds
s_waitcnt lgkmcnt(0)
MM v[0:3],a[192:195],v[160:163],0
v_mov_b32 v244, 0
v_mov_b32 v144, 0
MM v[0:3],a[196:199],v[176:179],v[0:3]
v_mov_b32 v145, 0
v_mov_b32 v245, 0
MM v[0:3],a[200:203],v[192:195],v[0:3]
v_mov_b32 v148, 0
v_mov_b32 v149, 0
MM v[0:3],a[204:207],v[208:211],v[0:3]
v_fma_f32 v16, v242, v136, v16
v_fma_f32 v17, v242, v137, v17
MM v[8:11],a[192:195],v[164:167],0
v_fma_f32 v18, v242, v138, v18
v_fma_f32 v19, v242, v139, v19
MM v[8:11],a[196:199],v[180:183],v[8:11]
v_fma_f32 v20, v242, v140, v20
v_fma_f32 v21, v242, v141, v21
MM v[8:11],a[200:203],v[196:199],v[8:11]
v_fma_f32 v22, v242, v142, v22
v_fma_f32 v23, v242, v143, v23
MM v[8:11],a[204:207],v[212:215],v[8:11]
v_max3_f32 v146, v0, v1, v2
v_max_f32 v146, v146, v3
MM v[4:7],a[208:211],v[160:163],0
v_mov_b32 v147, v146
v_mul_f32 v16, v246, v16
MM v[4:7],a[212:215],v[176:179],v[4:7]
buffer_load_dwordx4 v235, s[12:15], s77 offen offset:3072 lds
MM v[4:7],a[216:219],v[192:195],v[4:7]
s_add_u32 s77, s64, 32768
s_add_u32 s81, s77, 896
s_add_u32 m0, s65, 98304
MM v[4:7],a[220:223],v[208:211],v[4:7]
v_permlane16_swap_b32 v146, v147
v_max_f32 v146, v146, v147
MM v[12:15],a[208:211],v[164:167],0
s_waitcnt vmcnt(8)
s_barrier
RD a[192:195],v224 offset:16384
MM v[12:15],a[212:215],v[180:183],v[12:15]
RD a[196:199],v225 offset:16384
RD a[200:203],v226 offset:16384
v_mov_b32 v147, v146
MM v[12:15],a[216:219],v[196:199],v[12:15]
RD a[204:207],v227 offset:16384
v_permlane32_swap_b32 v146, v147
v_max_f32 v146, v146, v147
LPG4_%=:
s_cbranch_execz LPG8_%=
MM v[12:15],a[220:223],v[212:215],v[12:15]
v_sub_f32 v128, 0, v146
v_sub_f32 v129, 0, v146
v_sub_f32 v130, 0, v146
MM v[32:35],a[224:227],v[160:163],0
RD a[208:211],v224 offset:20480
RD a[212:215],v225 offset:20480
v_sub_f32 v131, 0, v146
v_max3_f32 v150, v8, v9, v10
MM v[32:35],a[228:231],v[176:179],v[32:35]
RD a[216:219],v226 offset:20480
RD a[220:223],v227 offset:20480
v_max_f32 v150, v150, v11
v_mov_b32 v151, v150
MM v[32:35],a[232:235],v[192:195],v[32:35]
s_nop 0
v_permlane16_swap_b32 v150, v151
v_max_f32 v150, v150, v151
v_mov_b32 v151, v150
MM v[32:35],a[236:239],v[208:211],v[32:35]
buffer_load_dwordx4 v235, s[12:15], s81 offen offset:0 lds
v_permlane32_swap_b32 v150, v151
v_max_f32 v150, v150, v151
v_sub_f32 v0, v0, v146
v_sub_f32 v132, 0, v150
MM v[40:43],a[224:227],v[164:167],0
v_sub_f32 v133, 0, v150
v_sub_f32 v134, 0, v150
v_sub_f32 v135, 0, v150
v_sub_f32 v1, v1, v146
EX v0,v0
MM v[40:43],a[228:231],v[180:183],v[40:43]
EX v1,v1
MM v[40:43],a[232:235],v[196:199],v[40:43]
v_sub_f32 v2, v2, v146
v_sub_f32 v3, v3, v146
MM v[40:43],a[236:239],v[212:215],v[40:43]
EX v2,v2
MM v[36:39],a[240:243],v[160:163],v[128:131]
RD a[224:227],v224 offset:24576
RD a[228:231],v225 offset:24576
EX v3,v3
MM v[36:39],a[244:247],v[176:179],v[36:39]
RD a[232:235],v226 offset:24576
RD a[236:239],v227 offset:24576
CV v0,v0,v1
v_sub_f32 v4, v4, v146
MM v[36:39],a[248:251],v[192:195],v[36:39]
v_sub_f32 v5, v5, v146
v_mul_f32 v17, v246, v17
EX v4,v4
MM v[36:39],a[252:255],v[208:211],v[36:39]
EX v5,v5
MM v[44:47],a[240:243],v[164:167],v[132:135]
CV v1,v2,v3
v_sub_f32 v6, v6, v146
MM v[44:47],a[244:247],v[180:183],v[44:47]
buffer_load_dwordx4 v235, s[12:15], s77 offen offset:1024 lds
v_sub_f32 v7, v7, v146
MM v[44:47],a[248:251],v[196:199],v[44:47]
EX v6,v6
EX v7,v7
MM v[44:47],a[252:255],v[212:215],v[44:47]
CV v2,v4,v5
v_sub_f32 v8, v8, v150
MM v[60:63],a[124:127],a[188:191],v[60:63]
RD a[240:243],v224 offset:28672
RD a[244:247],v225 offset:28672
v_sub_f32 v9, v9, v150
MM v[60:63],a[120:123],a[184:187],v[60:63]
RD a[248:251],v226 offset:28672
RD a[252:255],v227 offset:28672
EX v8,v8
MM v[60:63],a[116:119],a[156:159],v[60:63]
EX v9,v9
MM v[60:63],a[112:115],a[152:155],v[60:63]
CV v3,v6,v7
v_sub_f32 v10, v10, v150
MM v[80:83],a[0:3],a[144:147],v[80:83]
v_sub_f32 v11, v11, v150
v_mul_f32 v18, v246, v18
EX v10,v10
MM v[80:83],a[4:7],a[148:151],v[80:83]
EX v11,v11
MM v[80:83],a[8:11],a[176:179],v[80:83]
CV v8,v8,v9
v_sub_f32 v12, v12, v150
MM v[80:83],a[12:15],a[180:183],v[80:83]
buffer_load_dwordx4 v235, s[12:15], s81 offen offset:2048 lds
v_sub_f32 v13, v13, v150
EX v12,v12
MM v[88:91],a[12:15],a[188:191],v[88:91]
EX v13,v13
MM v[88:91],a[8:11],a[184:187],v[88:91]
CV v9,v10,v11
v_sub_f32 v14, v14, v150
MM v[88:91],a[4:7],a[156:159],v[88:91]
v_sub_f32 v15, v15, v150
v_mul_f32 v19, v246, v19
EX v14,v14
MM v[88:91],a[0:3],a[152:155],v[88:91]
EX v15,v15
MM v[84:87],a[16:19],a[144:147],v[84:87]
CV v10,v12,v13
v_sub_f32 v32, v32, v146
MM v[84:87],a[20:23],a[148:151],v[84:87]
v_sub_f32 v33, v33, v146
v_mul_f32 v20, v246, v20
MM v[84:87],a[24:27],a[176:179],v[84:87]
EX v32,v32
MM v[84:87],a[28:31],a[180:183],v[84:87]
EX v33,v33
MM v[92:95],a[28:31],a[188:191],v[92:95]
CV v11,v14,v15
v_sub_f32 v34, v34, v146
v_sub_f32 v35, v35, v146
MM v[92:95],a[24:27],a[184:187],v[92:95]
buffer_load_dwordx4 v235, s[12:15], s77 offen offset:3072 lds
EX v34,v34
MM v[92:95],a[20:23],a[156:159],v[92:95]
s_add_u32 s77, s64, 49152
s_add_u32 m0, s65, 49152
v_mul_f32 v21, v246, v21
EX v35,v35
MM v[92:95],a[16:19],a[152:155],v[92:95]
buffer_load_dwordx4 v228, s[8:11], s77 offen offset:0 lds
CV v4,v32,v33
EX v36,v36
MM v[112:115],a[32:35],a[144:147],v[112:115]
EX v37,v37
MM v[112:115],a[36:39],a[148:151],v[112:115]
CV v5,v34,v35
v_mul_f32 v22, v246, v22
MM v[112:115],a[40:43],a[176:179],v[112:115]
EX v38,v38
EX v39,v39
MM v[112:115],a[44:47],a[180:183],v[112:115]
buffer_load_dwordx4 v229, s[8:11], s77 offen offset:1024 lds
CV v6,v36,v37
MM v[120:123],a[44:47],a[188:191],v[120:123]
v_sub_f32 v40, v40, v150
v_sub_f32 v41, v41, v150
MM v[120:123],a[40:43],a[184:187],v[120:123]
EX v40,v40
MM v[120:123],a[36:39],a[156:159],v[120:123]
EX v41,v41
MM v[120:123],a[32:35],a[152:155],v[120:123]
buffer_load_dwordx4 v230, s[8:11], s77 offen offset:2048 lds
CV v7,v38,v39
v_sub_f32 v42, v42, v150
MM v[116:119],a[48:51],a[144:147],v[116:119]
v_sub_f32 v43, v43, v150
v_mul_f32 v23, v246, v23
EX v42,v42
MM v[116:119],a[52:55],a[148:151],v[116:119]
EX v43,v43
MM v[116:119],a[56:59],a[176:179],v[116:119]
CV v12,v40,v41
CV v168,v16,v17
EX v44,v44
MM v[116:119],a[60:63],a[180:183],v[116:119]
buffer_load_dwordx4 v231, s[8:11], s77 offen offset:3072 lds
EX v45,v45
MM v[124:127],a[60:63],a[188:191],v[124:127]
s_add_u32 s77, s64, 49152
s_add_u32 s81, s77, 896
s_add_u32 m0, s65, 114688
CV v13,v42,v43
MM v[124:127],a[56:59],a[184:187],v[124:127]
EX v46,v46
MM v[124:127],a[52:55],a[156:159],v[124:127]
EX v47,v47
CV v14,v44,v45
CV v15,v46,v47
MM v[124:127],a[48:51],a[152:155],v[124:127]
buffer_load_dwordx4 v235, s[12:15], s81 offen offset:0 lds
v_smfmac_f32_16x16x64_bf16 v[144:147], v[236:239], v[0:7], v234
CV v169,v18,v19
v_smfmac_f32_16x16x64_bf16 v[148:151], v[236:239], v[8:15], v234
s_waitcnt lgkmcnt(0)
CV v170,v20,v21
buffer_load_dwordx4 v235, s[12:15], s77 offen offset:1024 lds
buffer_load_dwordx4 v235, s[12:15], s81 offen offset:2048 lds
buffer_load_dwordx4 v235, s[12:15], s77 offen offset:3072 lds
s_add_u32 s64, s64, 49152
MM v[64:67],a[192:195],v[160:163],v[128:131]
s_add_u32 s64, s64, 16384
TR a[128:129],v232 offset:0
CV v171,v22,v23
MM v[64:67],a[196:199],v[176:179],v[64:67]
s_add_u32 s81, s64, 896
TR a[130:131],v232 offset:4096
v_fma_f32 v24, v243, v136, v24
MM v[64:67],a[200:203],v[192:195],v[64:67]
TR a[132:133],v232 offset:8192
v_fma_f32 v25, v243, v137, v25
MM v[64:67],a[204:207],v[208:211],v[64:67]
v_max3_f32 v249, v144, v145, v148
v_max_f32 v249, v249, v149
MM v[68:71],a[220:223],v[208:211],v[128:131]
TR a[134:135],v232 offset:12288
v_cmp_lt_f32 vcc, s71, v249
MM v[68:71],a[216:219],v[192:195],v[68:71]
s_cbranch_vccnz LSITE0_%=
LBACK0_%=:
v_add_f32 v244, v144, v145
v_fma_f32 v26, v243, v138, v26
MM v[68:71],a[212:215],v[176:179],v[68:71]
TR a[136:137],v233 offset:0
v_add_f32 v245, v148, v149
v_fma_f32 v27, v243, v139, v27
MM v[68:71],a[208:211],v[160:163],v[68:71]
TR a[138:139],v233 offset:4096
v_fma_f32 v28, v243, v140, v28
MM v[76:79],a[208:211],v[164:167],v[132:135]
TR a[140:141],v233 offset:8192
TR a[142:143],v233 offset:12288
)ASM"
R"ASM(
v_fma_f32 v29, v243, v141, v29
MM v[76:79],a[212:215],v[180:183],v[76:79]
TR a[144:145],v232 offset:512
v_fma_f32 v30, v243, v142, v30
MM v[76:79],a[216:219],v[196:199],v[76:79]
TR a[146:147],v232 offset:4608
v_fma_f32 v31, v243, v143, v31
v_mul_f32 v24, v247, v24
MM v[76:79],a[220:223],v[212:215],v[76:79]
TR a[148:149],v232 offset:8704
v_mul_f32 v25, v247, v25
MM v[72:75],a[204:207],v[212:215],v[132:135]
TR a[150:151],v232 offset:12800
EX v64,v64
v_mul_f32 v26, v247, v26
MM v[72:75],a[200:203],v[196:199],v[72:75]
TR a[152:153],v233 offset:512
TR a[154:155],v233 offset:4608
v_mul_f32 v27, v247, v27
EX v65,v65
MM v[72:75],a[196:199],v[180:183],v[72:75]
TR a[156:157],v233 offset:8704
v_mul_f32 v28, v247, v28
MM v[72:75],a[192:195],v[164:167],v[72:75]
TR a[158:159],v233 offset:12800
v_mul_f32 v29, v247, v29
EX v66,v66
MM v[96:99],a[224:227],v[160:163],v[128:131]
TR a[160:161],v232 offset:1024
v_mul_f32 v30, v247, v30
v_mul_f32 v31, v247, v31
EX v67,v67
MM v[96:99],a[228:231],v[176:179],v[96:99]
TR a[162:163],v232 offset:5120
TR a[164:165],v232 offset:9216
CV v172,v24,v25
MM v[96:99],a[232:235],v[192:195],v[96:99]
TR a[166:167],v232 offset:13312
CV v173,v26,v27
EX v68,v68
MM v[96:99],a[236:239],v[208:211],v[96:99]
TR a[168:169],v233 offset:1024
CV v174,v28,v29
MM v[100:103],a[252:255],v[208:211],v[128:131]
TR a[170:171],v233 offset:5120
CV v175,v30,v31
EX v69,v69
MM v[100:103],a[248:251],v[192:195],v[100:103]
TR a[172:173],v233 offset:9216
TR a[174:175],v233 offset:13312
v_xor_b32 v143, 128, v253
MM v[100:103],a[244:247],v[176:179],v[100:103]
RD v[136:139],v143 offset:0
TR a[176:177],v232 offset:1536
RD v[140:143],v143 offset:16
EX v70,v70
MM v[100:103],a[240:243],v[160:163],v[100:103]
TR a[178:179],v232 offset:5632
CV v64,v64,v65
EX v71,v71
MM v[108:111],a[240:243],v[164:167],v[132:135]
TR a[180:181],v232 offset:9728
TR a[182:183],v232 offset:13824
MM v[108:111],a[244:247],v[180:183],v[108:111]
EX v72,v72
MM v[108:111],a[248:251],v[196:199],v[108:111]
s_waitcnt lgkmcnt(3)
TR a[184:185],v233 offset:1536
TR a[186:187],v233 offset:5632
TR a[188:189],v233 offset:9728
TR a[190:191],v233 offset:13824
v_fma_f32 v48, v242, v136, v48
v_fma_f32 v49, v242, v137, v49
v_fma_f32 v50, v242, v138, v50
v_fma_f32 v51, v242, v139, v51
EX v73,v73
MM v[108:111],a[252:255],v[212:215],v[108:111]
v_fma_f32 v52, v242, v140, v52
CV v65,v66,v67
MM v[104:107],a[236:239],v[212:215],v[132:135]
v_fma_f32 v53, v242, v141, v53
v_fma_f32 v54, v242, v142, v54
EX v74,v74
MM v[104:107],a[232:235],v[196:199],v[104:107]
v_fma_f32 v55, v242, v143, v55
CV v66,v68,v69
MM v[104:107],a[228:231],v[180:183],v[104:107]
EX v75,v75
v_mul_f32 v48, v246, v48
MM v[104:107],a[224:227],v[164:167],v[104:107]
s_waitcnt vmcnt(12) lgkmcnt(0)
s_barrier
v_mul_f32 v49, v246, v49
EX v76,v76
MM a[0:3],a[128:131],v[0:3],0
s_add_u32 m0, s65, 0
v_mul_f32 v50, v246, v50
CV v67,v70,v71
MM a[0:3],a[132:135],v[4:7],a[0:3]
v_mul_f32 v51, v246, v51
v_mul_f32 v52, v246, v52
EX v77,v77
MM a[8:11],a[132:135],v[12:15],0
v_mul_f32 v53, v246, v53
CV v72,v72,v73
MM a[8:11],a[128:131],v[8:11],a[8:11]
EX v78,v78
v_mul_f32 v54, v246, v54
MM a[12:15],a[136:139],v[8:11],0
v_mul_f32 v55, v246, v55
CV v184,v48,v49
EX v79,v79
MM a[12:15],a[140:143],v[12:15],a[12:15]
CV v185,v50,v51
CV v73,v74,v75
MM a[4:7],a[140:143],v[4:7],0
EX v96,v96
MM a[4:7],a[136:139],v[0:3],a[4:7]
CV v186,v52,v53
CV v187,v54,v55
MM a[32:35],a[144:147],v[0:3],0
RD a[192:195],v224 offset:32768
EX v97,v97
v_fma_f32 v56, v243, v136, v56
MM a[32:35],a[148:151],v[4:7],a[32:35]
buffer_load_dwordx4 v228, s[8:11], s64 offen offset:0 lds
v_fma_f32 v57, v243, v137, v57
EX v98,v98
MM a[40:43],a[148:151],v[12:15],0
RD a[196:199],v225 offset:32768
v_fma_f32 v58, v243, v138, v58
MM a[40:43],a[144:147],v[8:11],a[40:43]
RD a[200:203],v226 offset:32768
v_fma_f32 v59, v243, v139, v59
CV v74,v76,v77
EX v99,v99
MM a[44:47],a[152:155],v[8:11],0
v_fma_f32 v60, v243, v140, v60
v_fma_f32 v61, v243, v141, v61
MM a[44:47],a[156:159],v[12:15],a[44:47]
buffer_load_dwordx4 v229, s[8:11], s64 offen offset:1024 lds
RD a[204:207],v227 offset:32768
EX v100,v100
v_fma_f32 v62, v243, v142, v62
MM a[36:39],a[156:159],v[4:7],0
EX v101,v101
v_fma_f32 v63, v243, v143, v63
CV v75,v78,v79
MM a[36:39],a[152:155],v[0:3],a[36:39]
RD a[208:211],v224 offset:36864
v_mul_f32 v56, v247, v56
MM a[64:67],a[160:163],v[0:3],0
RD a[212:215],v225 offset:36864
v_mul_f32 v57, v247, v57
EX v102,v102
MM a[64:67],a[164:167],v[4:7],a[64:67]
buffer_load_dwordx4 v230, s[8:11], s64 offen offset:2048 lds
v_mul_f32 v58, v247, v58
CV v68,v96,v97
MM a[72:75],a[164:167],v[12:15],0
EX v103,v103
v_mul_f32 v59, v247, v59
MM a[72:75],a[160:163],v[8:11],a[72:75]
RD a[216:219],v226 offset:36864
v_mul_f32 v60, v247, v60
EX v104,v104
v_mul_f32 v61, v247, v61
MM a[76:79],a[168:171],v[8:11],0
RD a[220:223],v227 offset:36864
buffer_load_dwordx4 v231, s[8:11], s64 offen offset:3072 lds
v_mul_f32 v62, v247, v62
MM a[76:79],a[172:175],v[12:15],a[76:79]
s_add_u32 m0, s65, 65536
EX v105,v105
v_mul_f32 v63, v247, v63
CV v69,v98,v99
MM a[68:71],a[172:175],v[4:7],0
RD a[224:227],v224 offset:40960
CV v188,v56,v57
MM a[68:71],a[168:171],v[0:3],a[68:71]
EX v106,v106
CV v189,v58,v59
MM a[96:99],a[176:179],v[0:3],0
RD a[228:231],v225 offset:40960
buffer_load_dwordx4 v235, s[12:15], s81 offen offset:0 lds
CV v190,v60,v61
EX v107,v107
CV v70,v100,v101
CV v191,v62,v63
MM a[96:99],a[180:183],v[4:7],a[96:99]
RD a[232:235],v226 offset:40960
v_xor_b32 v143, 256, v253
MM a[104:107],a[180:183],v[12:15],0
RD v[136:139],v143 offset:0
EX v108,v108
MM a[104:107],a[176:179],v[8:11],a[104:107]
RD v[140:143],v143 offset:16
CV v71,v102,v103
MM a[108:111],a[184:187],v[8:11],0
RD a[236:239],v227 offset:40960
buffer_load_dwordx4 v235, s[12:15], s64 offen offset:1024 lds
EX v109,v109
MM a[108:111],a[188:191],v[12:15],a[108:111]
EX v110,v110
MM a[100:103],a[188:191],v[4:7],0
RD a[240:243],v224 offset:45056
RD a[244:247],v225 offset:45056
RD a[248:251],v226 offset:45056
RD a[252:255],v227 offset:45056
EX v111,v111
MM a[100:103],a[184:187],v[0:3],a[100:103]
s_waitcnt lgkmcnt(5)
v_fma_f32 v80, v242, v136, v80
v_fma_f32 v81, v242, v137, v81
v_fma_f32 v82, v242, v138, v82
v_fma_f32 v83, v242, v139, v83
CV v76,v104,v105
v_smfmac_f32_16x16x64_bf16 v[144:147], v[236:239], v[64:71], v234
buffer_load_dwordx4 v235, s[12:15], s81 offen offset:2048 lds
CV v77,v106,v107
CV v78,v108,v109
CV v79,v110,v111
v_fma_f32 v84, v242, v140, v84
v_fma_f32 v85, v242, v141, v85
v_smfmac_f32_16x16x64_bf16 v[148:151], v[236:239], v[72:79], v234
s_waitcnt lgkmcnt(0)
v_fma_f32 v86, v242, v142, v86
buffer_load_dwordx4 v235, s[12:15], s64 offen offset:3072 lds
MM v[0:3],a[192:195],v[160:163],v[128:131]
s_add_u32 s64, s64, 16384
v_fma_f32 v87, v242, v143, v87
v_mul_f32 v80, v246, v80
MM v[0:3],a[196:199],v[176:179],v[0:3]
s_add_u32 s81, s64, 896
v_mul_f32 v81, v246, v81
v_mul_f32 v82, v246, v82
MM v[0:3],a[200:203],v[192:195],v[0:3]
TR a[128:129],v232 offset:16384
v_mul_f32 v83, v246, v83
MM v[0:3],a[204:207],v[208:211],v[0:3]
v_max3_f32 v249, v144, v145, v148
v_max_f32 v249, v249, v149
MM v[4:7],a[220:223],v[208:211],v[128:131]
TR a[130:131],v232 offset:20480
TR a[132:133],v232 offset:24576
v_cmp_lt_f32 vcc, s71, v249
v_mul_f32 v84, v246, v84
MM v[4:7],a[216:219],v[192:195],v[4:7]
s_cbranch_vccnz LSITE1_%=
LBACK1_%=:
TR a[134:135],v232 offset:28672
v_add_f32 v244, v144, v145
v_mul_f32 v85, v246, v85
MM v[4:7],a[212:215],v[176:179],v[4:7]
TR a[136:137],v233 offset:16384
v_add_f32 v245, v148, v149
v_mul_f32 v86, v246, v86
MM v[4:7],a[208:211],v[160:163],v[4:7]
TR a[138:139],v233 offset:20480
v_mul_f32 v87, v246, v87
MM v[12:15],a[208:211],v[164:167],v[132:135]
TR a[140:141],v233 offset:24576
TR a[142:143],v233 offset:28672
CV v200,v80,v81
MM v[12:15],a[212:215],v[180:183],v[12:15]
TR a[144:145],v232 offset:16896
CV v201,v82,v83
CV v202,v84,v85
MM v[12:15],a[216:219],v[196:199],v[12:15]
TR a[146:147],v232 offset:20992
CV v203,v86,v87
MM v[12:15],a[220:223],v[212:215],v[12:15]
TR a[148:149],v232 offset:25088
v_fma_f32 v88, v243, v136, v88
MM v[8:11],a[204:207],v[212:215],v[132:135]
TR a[150:151],v232 offset:29184
EX v0,v0
v_fma_f32 v89, v243, v137, v89
MM v[8:11],a[200:203],v[196:199],v[8:11]
TR a[152:153],v233 offset:16896
TR a[154:155],v233 offset:20992
v_fma_f32 v90, v243, v138, v90
EX v1,v1
MM v[8:11],a[196:199],v[180:183],v[8:11]
TR a[156:157],v233 offset:25088
v_fma_f32 v91, v243, v139, v91
v_fma_f32 v92, v243, v140, v92
MM v[8:11],a[192:195],v[164:167],v[8:11]
TR a[158:159],v233 offset:29184
EX v2,v2
v_fma_f32 v93, v243, v141, v93
MM v[32:35],a[224:227],v[160:163],v[128:131]
TR a[160:161],v232 offset:17408
v_fma_f32 v94, v243, v142, v94
EX v3,v3
MM v[32:35],a[228:231],v[176:179],v[32:35]
TR a[162:163],v232 offset:21504
TR a[164:165],v232 offset:25600
v_fma_f32 v95, v243, v143, v95
MM v[32:35],a[232:235],v[192:195],v[32:35]
TR a[166:167],v232 offset:29696
v_mul_f32 v88, v247, v88
EX v4,v4
MM v[32:35],a[236:239],v[208:211],v[32:35]
TR a[168:169],v233 offset:17408
v_mul_f32 v89, v247, v89
MM v[36:39],a[252:255],v[208:211],v[128:131]
TR a[170:171],v233 offset:21504
v_mul_f32 v90, v247, v90
EX v5,v5
v_mul_f32 v91, v247, v91
MM v[36:39],a[248:251],v[192:195],v[36:39]
TR a[172:173],v233 offset:25600
TR a[174:175],v233 offset:29696
v_mul_f32 v92, v247, v92
MM v[36:39],a[244:247],v[176:179],v[36:39]
TR a[176:177],v232 offset:17920
EX v6,v6
v_mul_f32 v93, v247, v93
MM v[36:39],a[240:243],v[160:163],v[36:39]
TR a[178:179],v232 offset:22016
v_mul_f32 v94, v247, v94
EX v7,v7
MM v[44:47],a[240:243],v[164:167],v[132:135]
TR a[180:181],v232 offset:26112
v_mul_f32 v95, v247, v95
CV v0,v0,v1
MM v[44:47],a[244:247],v[180:183],v[44:47]
TR a[182:183],v232 offset:30208
CV v204,v88,v89
EX v8,v8
CV v205,v90,v91
MM v[44:47],a[248:251],v[196:199],v[44:47]
TR a[184:185],v233 offset:17920
TR a[186:187],v233 offset:22016
TR a[188:189],v233 offset:26112
TR a[190:191],v233 offset:30208
CV v206,v92,v93
EX v9,v9
MM v[44:47],a[252:255],v[212:215],v[44:47]
CV v207,v94,v95
CV v1,v2,v3
MM v[40:43],a[236:239],v[212:215],v[132:135]
EX v10,v10
v_xor_b32 v143, 384, v253
MM v[40:43],a[232:235],v[196:199],v[40:43]
RD v[136:139],v143 offset:0
CV v2,v4,v5
MM v[40:43],a[228:231],v[180:183],v[40:43]
RD v[140:143],v143 offset:16
EX v11,v11
MM v[40:43],a[224:227],v[164:167],v[40:43]
s_waitcnt vmcnt(12) lgkmcnt(0)
s_barrier
CV v3,v6,v7
EX v12,v12
MM a[0:3],a[128:131],v[64:67],a[0:3]
s_add_u32 m0, s65, 16384
EX v13,v13
MM a[0:3],a[132:135],v[68:71],a[0:3]
CV v8,v8,v9
CV v9,v10,v11
MM a[8:11],a[132:135],v[76:79],a[8:11]
v_fma_f32 v112, v242, v136, v112
v_fma_f32 v113, v242, v137, v113
v_fma_f32 v114, v242, v138, v114
v_fma_f32 v115, v242, v139, v115
v_fma_f32 v116, v242, v140, v116
MM a[8:11],a[128:131],v[72:75],a[8:11]
EX v14,v14
v_fma_f32 v117, v242, v141, v117
MM a[12:15],a[136:139],v[72:75],a[12:15]
v_fma_f32 v118, v242, v142, v118
v_fma_f32 v119, v242, v143, v119
EX v15,v15
MM a[12:15],a[140:143],v[76:79],a[12:15]
v_mul_f32 v112, v246, v112
v_mul_f32 v113, v246, v113
MM a[4:7],a[140:143],v[68:71],a[4:7]
EX v32,v32
MM a[4:7],a[136:139],v[64:67],a[4:7]
RD a[192:195],v224 offset:49152
v_mul_f32 v114, v246, v114
MM a[32:35],a[144:147],v[64:67],a[32:35]
EX v33,v33
v_mul_f32 v115, v246, v115
MM a[32:35],a[148:151],v[68:71],a[32:35]
RD a[196:199],v225 offset:49152
buffer_load_dwordx4 v228, s[8:11], s64 offen offset:0 lds
v_mul_f32 v116, v246, v116
EX v34,v34
MM a[40:43],a[148:151],v[76:79],a[40:43]
v_mul_f32 v117, v246, v117
CV v10,v12,v13
LPG5_%=:
s_cbranch_execz LPG9_%=
MM a[40:43],a[144:147],v[72:75],a[40:43]
RD a[200:203],v226 offset:49152
v_mul_f32 v118, v246, v118
EX v35,v35
MM a[44:47],a[152:155],v[72:75],a[44:47]
v_mul_f32 v119, v246, v119
CV v216,v112,v113
MM a[44:47],a[156:159],v[76:79],a[44:47]
buffer_load_dwordx4 v229, s[8:11], s64 offen offset:1024 lds
RD a[204:207],v227 offset:49152
EX v36,v36
CV v217,v114,v115
MM a[36:39],a[156:159],v[68:71],a[36:39]
CV v218,v116,v117
CV v11,v14,v15
EX v37,v37
MM a[36:39],a[152:155],v[64:67],a[36:39]
RD a[208:211],v224 offset:53248
CV v219,v118,v119
)ASM"
R"ASM(
MM a[64:67],a[160:163],v[64:67],a[64:67]
v_fma_f32 v120, v243, v136, v120
v_fma_f32 v121, v243, v137, v121
EX v38,v38
MM a[64:67],a[164:167],v[68:71],a[64:67]
buffer_load_dwordx4 v230, s[8:11], s64 offen offset:2048 lds
RD a[212:215],v225 offset:53248
v_fma_f32 v122, v243, v138, v122
MM a[72:75],a[164:167],v[76:79],a[72:75]
EX v39,v39
CV v4,v32,v33
v_fma_f32 v123, v243, v139, v123
MM a[72:75],a[160:163],v[72:75],a[72:75]
RD a[216:219],v226 offset:53248
v_fma_f32 v124, v243, v140, v124
EX v40,v40
MM a[76:79],a[168:171],v[72:75],a[76:79]
RD a[220:223],v227 offset:53248
buffer_load_dwordx4 v231, s[8:11], s64 offen offset:3072 lds
v_fma_f32 v125, v243, v141, v125
MM a[76:79],a[172:175],v[76:79],a[76:79]
s_add_u32 m0, s65, 81920
v_fma_f32 v126, v243, v142, v126
CV v5,v34,v35
EX v41,v41
MM a[68:71],a[172:175],v[68:71],a[68:71]
RD a[224:227],v224 offset:57344
v_fma_f32 v127, v243, v143, v127
MM a[68:71],a[168:171],v[64:67],a[68:71]
v_mul_f32 v120, v247, v120
v_mul_f32 v121, v247, v121
EX v42,v42
MM a[96:99],a[176:179],v[64:67],a[96:99]
RD a[228:231],v225 offset:57344
buffer_load_dwordx4 v235, s[12:15], s81 offen offset:0 lds
EX v43,v43
v_mul_f32 v122, v247, v122
CV v6,v36,v37
MM a[96:99],a[180:183],v[68:71],a[96:99]
RD a[232:235],v226 offset:57344
v_mul_f32 v123, v247, v123
MM a[104:107],a[180:183],v[76:79],a[104:107]
v_mul_f32 v124, v247, v124
CV v7,v38,v39
EX v44,v44
MM a[104:107],a[176:179],v[72:75],a[104:107]
RD a[236:239],v227 offset:57344
v_mul_f32 v125, v247, v125
MM a[108:111],a[184:187],v[72:75],a[108:111]
buffer_load_dwordx4 v235, s[12:15], s64 offen offset:1024 lds
EX v45,v45
v_mul_f32 v126, v247, v126
v_mul_f32 v127, v247, v127
MM a[108:111],a[188:191],v[76:79],a[108:111]
EX v46,v46
CV v220,v120,v121
MM a[100:103],a[188:191],v[68:71],a[100:103]
RD a[240:243],v224 offset:61440
RD a[244:247],v225 offset:61440
RD a[248:251],v226 offset:61440
RD a[252:255],v227 offset:61440
CV v221,v122,v123
EX v47,v47
CV v222,v124,v125
CV v223,v126,v127
MM a[100:103],a[184:187],v[64:67],a[100:103]
buffer_load_dwordx4 v235, s[12:15], s81 offen offset:2048 lds
CV v12,v40,v41
v_smfmac_f32_16x16x64_bf16 v[144:147], v[236:239], v[0:7], v234
CV v13,v42,v43
CV v14,v44,v45
CV v15,v46,v47
s_nop 1
v_smfmac_f32_16x16x64_bf16 v[148:151], v[236:239], v[8:15], v234
s_waitcnt lgkmcnt(0)
buffer_load_dwordx4 v235, s[12:15], s64 offen offset:3072 lds
MM v[64:67],a[192:195],v[160:163],v[128:131]
s_add_u32 s64, s64, 16384
TR a[128:129],v232 offset:32768
TR a[130:131],v232 offset:36864
MM v[64:67],a[196:199],v[176:179],v[64:67]
s_add_u32 s81, s64, 896
TR a[132:133],v232 offset:40960
TR a[134:135],v232 offset:45056
MM v[64:67],a[200:203],v[192:195],v[64:67]
TR a[136:137],v233 offset:32768
TR a[138:139],v233 offset:36864
MM v[64:67],a[204:207],v[208:211],v[64:67]
v_max3_f32 v249, v144, v145, v148
v_max_f32 v249, v249, v149
MM v[68:71],a[220:223],v[208:211],v[128:131]
TR a[140:141],v233 offset:40960
v_cmp_lt_f32 vcc, s71, v249
MM v[68:71],a[216:219],v[192:195],v[68:71]
s_cbranch_vccnz LSITE2_%=
LBACK2_%=:
TR a[142:143],v233 offset:45056
v_add_f32 v244, v144, v145
MM v[68:71],a[212:215],v[176:179],v[68:71]
s_lshl_b32 s90, %[swave], 10
s_add_u32 s90, s90, 81920
v_add_f32 v245, v148, v149
MM v[68:71],a[208:211],v[160:163],v[68:71]
buffer_load_dwordx4 v[240:243], v248, s[20:23], s90 offen offset:0 sc1
MM v[76:79],a[208:211],v[164:167],v[132:135]
TR a[144:145],v232 offset:33280
TR a[146:147],v232 offset:37376
MM v[76:79],a[212:215],v[180:183],v[76:79]
EX v64,v64
MM v[76:79],a[216:219],v[196:199],v[76:79]
TR a[148:149],v232 offset:41472
TR a[150:151],v232 offset:45568
MM v[76:79],a[220:223],v[212:215],v[76:79]
EX v65,v65
MM v[72:75],a[204:207],v[212:215],v[132:135]
TR a[152:153],v233 offset:33280
CV v64,v64,v65
MM v[72:75],a[200:203],v[196:199],v[72:75]
EX v66,v66
MM v[72:75],a[196:199],v[180:183],v[72:75]
TR a[154:155],v233 offset:37376
TR a[156:157],v233 offset:41472
MM v[72:75],a[192:195],v[164:167],v[72:75]
EX v67,v67
MM v[96:99],a[224:227],v[160:163],v[128:131]
TR a[158:159],v233 offset:45568
CV v65,v66,v67
MM v[96:99],a[228:231],v[176:179],v[96:99]
EX v68,v68
MM v[96:99],a[232:235],v[192:195],v[96:99]
TR a[160:161],v232 offset:33792
TR a[162:163],v232 offset:37888
MM v[96:99],a[236:239],v[208:211],v[96:99]
EX v69,v69
MM v[100:103],a[252:255],v[208:211],v[128:131]
TR a[164:165],v232 offset:41984
TR a[166:167],v232 offset:46080
MM v[100:103],a[248:251],v[192:195],v[100:103]
EX v70,v70
MM v[100:103],a[244:247],v[176:179],v[100:103]
TR a[168:169],v233 offset:33792
CV v66,v68,v69
MM v[100:103],a[240:243],v[160:163],v[100:103]
EX v71,v71
MM v[108:111],a[240:243],v[164:167],v[132:135]
TR a[170:171],v233 offset:37888
TR a[172:173],v233 offset:41984
MM v[108:111],a[244:247],v[180:183],v[108:111]
EX v72,v72
MM v[108:111],a[248:251],v[196:199],v[108:111]
TR a[174:175],v233 offset:46080
TR a[176:177],v232 offset:34304
MM v[108:111],a[252:255],v[212:215],v[108:111]
EX v73,v73
MM v[104:107],a[236:239],v[212:215],v[132:135]
TR a[178:179],v232 offset:38400
CV v67,v70,v71
MM v[104:107],a[232:235],v[196:199],v[104:107]
TR a[180:181],v232 offset:42496
TR a[182:183],v232 offset:46592
MM v[104:107],a[228:231],v[180:183],v[104:107]
EX v74,v74
MM v[104:107],a[224:227],v[164:167],v[104:107]
s_memrealtime s[56:57]
EX v75,v75
MM v[80:83],a[192:195],v[168:171],0
s_memtime s[58:59]
TR a[184:185],v233 offset:34304
TR a[186:187],v233 offset:38400
MM v[80:83],a[196:199],v[184:187],v[80:83]
EX v76,v76
MM v[80:83],a[200:203],v[200:203],v[80:83]
TR a[188:189],v233 offset:42496
TR a[190:191],v233 offset:46592
MM v[80:83],a[204:207],v[216:219],v[80:83]
EX v77,v77
MM v[88:91],a[192:195],v[172:175],0
EX v78,v78
MM v[88:91],a[196:199],v[188:191],v[88:91]
EX v79,v79
MM v[88:91],a[200:203],v[204:207],v[88:91]
EX v96,v96
MM v[88:91],a[204:207],v[220:223],v[88:91]
s_waitcnt vmcnt(13) lgkmcnt(0)
s_barrier
CV v72,v72,v73
MM a[0:3],a[128:131],v[0:3],a[0:3]
s_sub_u32 s52, s56, s52
s_sub_u32 s54, s58, s54
v_cvt_f32_u32 v250, s52
MM a[0:3],a[132:135],v[4:7],a[0:3]
s_add_u32 m0, s65, 32768
v_cvt_f32_u32 v251, s54
CV v73,v74,v75
MM a[8:11],a[132:135],v[12:15],a[8:11]
v_rcp_f32 v251, v251
MM a[8:11],a[128:131],v[8:11],a[8:11]
v_mul_f32 v250, 0x47abe000, v250
s_lshl_b32 s52, %[swave], 11
MM a[12:15],a[136:139],v[8:11],a[12:15]
v_mul_f32 v250, v250, v251
s_lshl_b32 s54, %[sb], 3
EX v97,v97
MM a[12:15],a[140:143],v[12:15],a[12:15]
v_cvt_u32_f32 v250, v250
s_add_u32 s52, s52, s54
v_max3_f32 v154, v80, v81, v82
MM a[4:7],a[140:143],v[4:7],a[4:7]
v_add_u32 v250, s56, v250
v_mov_b32 v251, s69
v_max_f32 v154, v154, v83
EX v98,v98
MM a[4:7],a[136:139],v[0:3],a[4:7]
s_add_u32 s52, s52, 86016
s_mov_b64 exec, 1
buffer_store_dwordx2 v[250:251], off, s[20:23], s52 sc1
s_mov_b64 exec, -1
s_nop 0
v_mov_b32 v155, v154
MM a[32:35],a[144:147],v[0:3],a[32:35]
RD a[192:195],v224 offset:0
v_permlane16_swap_b32 v154, v155
EX v99,v99
CV v74,v76,v77
v_max_f32 v154, v154, v155
MM a[32:35],a[148:151],v[4:7],a[32:35]
buffer_load_dwordx4 v228, s[8:11], s64 offen offset:0 lds
v_mov_b32 v155, v154
MM a[40:43],a[148:151],v[12:15],a[40:43]
RD a[196:199],v225 offset:0
v_permlane32_swap_b32 v154, v155
EX v100,v100
MM a[40:43],a[144:147],v[8:11],a[40:43]
RD a[200:203],v226 offset:0
v_max_f32 v154, v154, v155
CV v75,v78,v79
MM a[44:47],a[152:155],v[8:11],a[44:47]
v_sub_f32 v136, 0, v154
v_sub_f32 v137, 0, v154
EX v101,v101
MM a[44:47],a[156:159],v[12:15],a[44:47]
buffer_load_dwordx4 v229, s[8:11], s64 offen offset:1024 lds
RD a[204:207],v227 offset:0
MM a[36:39],a[156:159],v[4:7],a[36:39]
v_sub_f32 v138, 0, v154
v_sub_f32 v139, 0, v154
EX v102,v102
MM a[36:39],a[152:155],v[0:3],a[36:39]
RD a[208:211],v224 offset:4096
v_mov_b32 v246, 0
MM a[64:67],a[160:163],v[0:3],a[64:67]
EX v103,v103
v_mov_b32 v152, 0
CV v68,v96,v97
MM a[64:67],a[164:167],v[4:7],a[64:67]
buffer_load_dwordx4 v230, s[8:11], s64 offen offset:2048 lds
RD a[212:215],v225 offset:4096
v_mov_b32 v153, 0
MM a[72:75],a[164:167],v[12:15],a[72:75]
EX v104,v104
v_max3_f32 v158, v88, v89, v90
MM a[72:75],a[160:163],v[8:11],a[72:75]
RD a[216:219],v226 offset:4096
v_max_f32 v158, v158, v91
CV v69,v98,v99
MM a[76:79],a[168:171],v[8:11],a[76:79]
buffer_load_dwordx4 v231, s[8:11], s64 offen offset:3072 lds
RD a[220:223],v227 offset:4096
EX v105,v105
v_mov_b32 v159, v158
MM a[76:79],a[172:175],v[12:15],a[76:79]
s_add_u32 m0, s65, 98304
v_permlane16_swap_b32 v158, v159
v_max_f32 v158, v158, v159
MM a[68:71],a[172:175],v[4:7],a[68:71]
RD a[224:227],v224 offset:8192
EX v106,v106
v_mov_b32 v159, v158
MM a[68:71],a[168:171],v[0:3],a[68:71]
s_nop 0
v_permlane32_swap_b32 v158, v159
CV v70,v100,v101
EX v107,v107
MM a[96:99],a[176:179],v[0:3],a[96:99]
RD a[228:231],v225 offset:8192
buffer_load_dwordx4 v235, s[12:15], s81 offen offset:0 lds
v_max_f32 v158, v158, v159
MM a[96:99],a[180:183],v[4:7],a[96:99]
v_sub_f32 v140, 0, v158
CV v71,v102,v103
EX v108,v108
MM a[104:107],a[180:183],v[12:15],a[104:107]
RD a[232:235],v226 offset:8192
v_sub_f32 v141, 0, v158
MM a[104:107],a[176:179],v[8:11],a[104:107]
v_sub_f32 v142, 0, v158
v_sub_f32 v143, 0, v158
EX v109,v109
MM a[108:111],a[184:187],v[8:11],a[108:111]
RD a[236:239],v227 offset:8192
buffer_load_dwordx4 v235, s[12:15], s64 offen offset:1024 lds
v_mov_b32 v247, 0
v_mov_b32 v156, 0
v_mov_b32 v157, 0
MM a[108:111],a[188:191],v[12:15],a[108:111]
EX v110,v110
MM a[100:103],a[188:191],v[4:7],a[100:103]
RD a[240:243],v224 offset:12288
RD a[244:247],v225 offset:12288
RD a[248:251],v226 offset:12288
RD a[252:255],v227 offset:12288
EX v111,v111
MM a[100:103],a[184:187],v[0:3],a[100:103]
buffer_load_dwordx4 v235, s[12:15], s81 offen offset:2048 lds
CV v76,v104,v105
v_smfmac_f32_16x16x64_bf16 v[144:147], v[236:239], v[64:71], v234
CV v77,v106,v107
CV v78,v108,v109
CV v79,v110,v111
s_nop 1
v_smfmac_f32_16x16x64_bf16 v[148:151], v[236:239], v[72:79], v234
s_waitcnt lgkmcnt(0)
buffer_load_dwordx4 v235, s[12:15], s64 offen offset:3072 lds
MM v[0:3],a[192:195],v[160:163],v[128:131]
s_add_u32 s64, s64, 16384
TR a[128:129],v232 offset:49152
s_add_u32 s81, s64, 896
MM v[0:3],a[196:199],v[176:179],v[0:3]
TR a[130:131],v232 offset:53248
TR a[132:133],v232 offset:57344
MM v[0:3],a[200:203],v[192:195],v[0:3]
TR a[134:135],v232 offset:61440
TR a[136:137],v233 offset:49152
MM v[0:3],a[204:207],v[208:211],v[0:3]
v_max3_f32 v249, v144, v145, v148
v_max_f32 v249, v249, v149
MM v[4:7],a[220:223],v[208:211],v[128:131]
TR a[138:139],v233 offset:53248
v_cmp_lt_f32 vcc, s71, v249
MM v[4:7],a[216:219],v[192:195],v[4:7]
s_cbranch_vccnz LSITE3_%=
LBACK3_%=:
TR a[140:141],v233 offset:57344
v_add_f32 v244, v144, v145
MM v[4:7],a[212:215],v[176:179],v[4:7]
TR a[142:143],v233 offset:61440
v_add_f32 v245, v148, v149
MM v[4:7],a[208:211],v[160:163],v[4:7]
TR a[144:145],v232 offset:49664
TR a[146:147],v232 offset:53760
MM v[12:15],a[208:211],v[164:167],v[132:135]
EX v0,v0
MM v[12:15],a[212:215],v[180:183],v[12:15]
EX v1,v1
MM v[12:15],a[216:219],v[196:199],v[12:15]
TR a[148:149],v232 offset:57856
CV v0,v0,v1
MM v[12:15],a[220:223],v[212:215],v[12:15]
EX v2,v2
MM v[8:11],a[204:207],v[212:215],v[132:135]
EX v3,v3
MM v[8:11],a[200:203],v[196:199],v[8:11]
TR a[150:151],v232 offset:61952
CV v1,v2,v3
MM v[8:11],a[196:199],v[180:183],v[8:11]
EX v4,v4
MM v[8:11],a[192:195],v[164:167],v[8:11]
EX v5,v5
MM v[16:19],a[192:195],v[168:171],v[136:139]
TR a[152:153],v233 offset:49664
CV v2,v4,v5
MM v[16:19],a[196:199],v[184:187],v[16:19]
EX v6,v6
MM v[16:19],a[200:203],v[200:203],v[16:19]
EX v7,v7
MM v[16:19],a[204:207],v[216:219],v[16:19]
TR a[154:155],v233 offset:53760
CV v3,v6,v7
MM v[20:23],a[220:223],v[216:219],v[136:139]
s_waitcnt vmcnt(9)
v_xor_b32 v240, s69, v240
v_xor_b32 v241, s69, v241
MM v[20:23],a[216:219],v[200:203],v[20:23]
v_xor_b32 v242, s69, v242
v_xor_b32 v243, s69, v243
MM v[20:23],a[212:215],v[184:187],v[20:23]
v_or3_b32 v252, v240, v241, v242
v_or_b32 v252, v252, v243
EX v8,v8
MM v[20:23],a[208:211],v[168:171],v[20:23]
v_cmp_ne_u32 vcc, 0, v252
s_cbranch_vccnz LRFR_%=
LRFRb_%=:
EX v9,v9
MM v[28:31],a[208:211],v[172:175],v[140:143]
s_lshl_b32 s90, %[swave], 11
)ASM"
R"ASM(
s_add_u32 s78, s90, %[slds]
s_add_u32 s78, s78, 155648
MM v[28:31],a[212:215],v[188:191],v[28:31]
s_add_u32 s90, s90, 53248
s_mov_b32 m0, s78
CV v8,v8,v9
EX v10,v10
MM v[28:31],a[216:219],v[204:207],v[28:31]
buffer_load_dwordx4 v248, s[20:23], s90 offen offset:0 sc1 lds
EX v11,v11
MM v[28:31],a[220:223],v[220:223],v[28:31]
buffer_load_dwordx4 v248, s[20:23], s90 offen offset:1024 sc1 lds
EX v12,v12
MM v[24:27],a[204:207],v[220:223],v[140:143]
EX v13,v13
MM v[24:27],a[200:203],v[204:207],v[24:27]
EX v14,v14
MM v[24:27],a[196:199],v[188:191],v[24:27]
TR a[156:157],v233 offset:57856
CV v9,v10,v11
MM v[24:27],a[192:195],v[172:175],v[24:27]
EX v15,v15
MM v[32:35],a[224:227],v[160:163],v[128:131]
EX v16,v16
MM v[32:35],a[228:231],v[176:179],v[32:35]
TR a[158:159],v233 offset:61952
TR a[160:161],v232 offset:50176
MM v[32:35],a[232:235],v[192:195],v[32:35]
EX v17,v17
MM v[32:35],a[236:239],v[208:211],v[32:35]
EX v18,v18
MM v[36:39],a[252:255],v[208:211],v[128:131]
TR a[162:163],v232 offset:54272
CV v10,v12,v13
MM v[36:39],a[248:251],v[192:195],v[36:39]
EX v19,v19
MM v[36:39],a[244:247],v[176:179],v[36:39]
EX v20,v20
MM v[36:39],a[240:243],v[160:163],v[36:39]
TR a[164:165],v232 offset:58368
CV v11,v14,v15
MM v[44:47],a[240:243],v[164:167],v[132:135]
EX v21,v21
MM v[44:47],a[244:247],v[180:183],v[44:47]
TR a[166:167],v232 offset:62464
EX v22,v22
MM v[44:47],a[248:251],v[196:199],v[44:47]
EX v23,v23
MM v[44:47],a[252:255],v[212:215],v[44:47]
TR a[168:169],v233 offset:50176
CV v16,v16,v17
MM v[40:43],a[236:239],v[212:215],v[132:135]
EX v24,v24
MM v[40:43],a[232:235],v[196:199],v[40:43]
TR a[170:171],v233 offset:54272
EX v25,v25
MM v[40:43],a[228:231],v[180:183],v[40:43]
TR a[172:173],v233 offset:58368
CV v17,v18,v19
EX v26,v26
MM v[40:43],a[224:227],v[164:167],v[40:43]
TR a[174:175],v233 offset:62464
CV v18,v20,v21
MM v[48:51],a[224:227],v[168:171],v[136:139]
EX v27,v27
MM v[48:51],a[228:231],v[184:187],v[48:51]
EX v28,v28
MM v[48:51],a[232:235],v[200:203],v[48:51]
TR a[176:177],v232 offset:50688
EX v29,v29
MM v[48:51],a[236:239],v[216:219],v[48:51]
TR a[178:179],v232 offset:54784
CV v19,v22,v23
MM v[52:55],a[252:255],v[216:219],v[136:139]
EX v30,v30
MM v[52:55],a[248:251],v[200:203],v[52:55]
EX v31,v31
MM v[52:55],a[244:247],v[184:187],v[52:55]
TR a[180:181],v232 offset:58880
CV v24,v24,v25
MM v[52:55],a[240:243],v[168:171],v[52:55]
EX v32,v32
MM v[60:63],a[240:243],v[172:175],v[140:143]
TR a[182:183],v232 offset:62976
EX v33,v33
CV v25,v26,v27
MM v[60:63],a[244:247],v[188:191],v[60:63]
EX v34,v34
MM v[60:63],a[248:251],v[204:207],v[60:63]
TR a[184:185],v233 offset:50688
TR a[186:187],v233 offset:54784
TR a[188:189],v233 offset:58880
TR a[190:191],v233 offset:62976
MM v[60:63],a[252:255],v[220:223],v[60:63]
EX v35,v35
CV v26,v28,v29
MM v[56:59],a[236:239],v[220:223],v[140:143]
EX v36,v36
MM v[56:59],a[232:235],v[204:207],v[56:59]
EX v37,v37
CV v27,v30,v31
MM v[56:59],a[228:231],v[188:191],v[56:59]
EX v38,v38
MM v[56:59],a[224:227],v[172:175],v[56:59]
s_waitcnt lgkmcnt(0)
s_barrier
CV v4,v32,v33
MM a[0:3],a[128:131],v[64:67],a[0:3]
s_add_u32 m0, s65, 49152
EX v39,v39
MM a[0:3],a[132:135],v[68:71],a[0:3]
EX v40,v40
MM a[8:11],a[132:135],v[76:79],a[8:11]
CV v5,v34,v35
CV v6,v36,v37
MM a[8:11],a[128:131],v[72:75],a[8:11]
EX v41,v41
MM a[12:15],a[136:139],v[72:75],a[12:15]
EX v42,v42
MM a[12:15],a[140:143],v[76:79],a[12:15]
EX v43,v43
MM a[4:7],a[140:143],v[68:71],a[4:7]
EX v44,v44
MM a[4:7],a[136:139],v[64:67],a[4:7]
RD a[192:195],v224 offset:16384
CV v7,v38,v39
MM a[32:35],a[144:147],v[64:67],a[32:35]
EX v45,v45
MM a[32:35],a[148:151],v[68:71],a[32:35]
buffer_load_dwordx4 v228, s[8:11], s64 offen offset:0 lds
MM a[40:43],a[148:151],v[76:79],a[40:43]
RD a[196:199],v225 offset:16384
EX v46,v46
MM a[40:43],a[144:147],v[72:75],a[40:43]
EX v47,v47
CV v12,v40,v41
MM a[44:47],a[152:155],v[72:75],a[44:47]
RD a[200:203],v226 offset:16384
EX v48,v48
MM a[44:47],a[156:159],v[76:79],a[44:47]
buffer_load_dwordx4 v229, s[8:11], s64 offen offset:1024 lds
MM a[36:39],a[156:159],v[68:71],a[36:39]
RD a[204:207],v227 offset:16384
EX v49,v49
CV v13,v42,v43
MM a[36:39],a[152:155],v[64:67],a[36:39]
EX v50,v50
MM a[64:67],a[160:163],v[64:67],a[64:67]
RD a[208:211],v224 offset:20480
EX v51,v51
CV v14,v44,v45
MM a[64:67],a[164:167],v[68:71],a[64:67]
buffer_load_dwordx4 v230, s[8:11], s64 offen offset:2048 lds
MM a[72:75],a[164:167],v[76:79],a[72:75]
RD a[212:215],v225 offset:20480
EX v52,v52
MM a[72:75],a[160:163],v[72:75],a[72:75]
EX v53,v53
CV v15,v46,v47
MM a[76:79],a[168:171],v[72:75],a[76:79]
RD a[216:219],v226 offset:20480
CV v20,v48,v49
EX v54,v54
MM a[76:79],a[172:175],v[76:79],a[76:79]
buffer_load_dwordx4 v231, s[8:11], s64 offen offset:3072 lds
MM a[68:71],a[172:175],v[68:71],a[68:71]
s_add_u32 m0, s65, 114688
RD a[220:223],v227 offset:20480
EX v55,v55
MM a[68:71],a[168:171],v[64:67],a[68:71]
EX v56,v56
MM a[96:99],a[176:179],v[64:67],a[96:99]
RD a[224:227],v224 offset:24576
CV v21,v50,v51
EX v57,v57
MM a[96:99],a[180:183],v[68:71],a[96:99]
RD a[228:231],v225 offset:24576
CV v22,v52,v53
MM a[104:107],a[180:183],v[76:79],a[104:107]
buffer_load_dwordx4 v235, s[12:15], s81 offen offset:0 lds
EX v58,v58
MM a[104:107],a[176:179],v[72:75],a[104:107]
EX v59,v59
MM a[108:111],a[184:187],v[72:75],a[108:111]
RD a[232:235],v226 offset:24576
CV v23,v54,v55
MM a[108:111],a[188:191],v[76:79],a[108:111]
EX v60,v60
MM a[100:103],a[188:191],v[68:71],a[100:103]
RD a[236:239],v227 offset:24576
buffer_load_dwordx4 v235, s[12:15], s64 offen offset:1024 lds
EX v61,v61
MM a[100:103],a[184:187],v[64:67],a[100:103]
EX v62,v62
v_smfmac_f32_16x16x64_bf16 v[144:147], v[236:239], v[0:7], v234
s_nop 0
LPG6_%=:
s_cbranch_execz LPG10_%=
RD a[240:243],v224 offset:28672
RD a[244:247],v225 offset:28672
RD a[248:251],v226 offset:28672
RD a[252:255],v227 offset:28672
EX v63,v63
v_smfmac_f32_16x16x64_bf16 v[148:151], v[236:239], v[8:15], v234
CV v28,v56,v57
v_smfmac_f32_16x16x64_bf16 v[152:155], v[236:239], v[16:23], v234
buffer_load_dwordx4 v235, s[12:15], s81 offen offset:2048 lds
CV v29,v58,v59
CV v30,v60,v61
CV v31,v62,v63
s_nop 1
v_smfmac_f32_16x16x64_bf16 v[156:159], v[236:239], v[24:31], v234
s_waitcnt lgkmcnt(0)
buffer_load_dwordx4 v235, s[12:15], s64 offen offset:3072 lds
MM v[64:67],a[192:195],v[160:163],v[128:131]
s_add_u32 s64, s64, 16384
TR a[128:129],v232 offset:0
s_add_u32 s81, s64, 896
MM v[64:67],a[196:199],v[176:179],v[64:67]
TR a[130:131],v232 offset:4096
TR a[132:133],v232 offset:8192
MM v[64:67],a[200:203],v[192:195],v[64:67]
v_max3_f32 v249, v144, v145, v148
v_max3_f32 v249, v249, v149, v152
MM v[64:67],a[204:207],v[208:211],v[64:67]
v_max3_f32 v249, v249, v153, v156
v_max_f32 v249, v249, v157
MM v[68:71],a[220:223],v[208:211],v[128:131]
TR a[134:135],v232 offset:12288
v_cmp_lt_f32 vcc, s71, v249
MM v[68:71],a[216:219],v[192:195],v[68:71]
s_cbranch_vccnz LSITE4_%=
LBACK4_%=:
TR a[136:137],v233 offset:0
v_add_f32 v244, v144, v145
MM v[68:71],a[212:215],v[176:179],v[68:71]
v_add_f32 v245, v148, v149
v_add_f32 v246, v152, v153
MM v[68:71],a[208:211],v[160:163],v[68:71]
TR a[138:139],v233 offset:4096
v_add_f32 v247, v156, v157
MM v[76:79],a[208:211],v[164:167],v[132:135]
s_cmp_lg_u32 %[sb], 0
s_cbranch_scc1 LEP_%=
v_mov_b32 v250, s69
s_mov_b32 s76, 32768
s_mov_b64 exec, 1
buffer_store_dword v250, off, s[20:23], s76 sc1
s_mov_b64 exec, -1
s_nop 1
LEP_%=:
MM v[76:79],a[212:215],v[180:183],v[76:79]
s_lshl_b32 s76, s50, 1
EX v64,v64
MM v[76:79],a[216:219],v[196:199],v[76:79]
s_bfe_u32 s77, s49, 65538
EX v65,v65
MM v[76:79],a[220:223],v[212:215],v[76:79]
s_add_u32 s76, s76, s77
TR a[140:141],v233 offset:8192
CV v64,v64,v65
MM v[72:75],a[204:207],v[212:215],v[132:135]
s_lshl_b32 s76, s76, 2
EX v66,v66
MM v[72:75],a[200:203],v[196:199],v[72:75]
s_add_u32 s76, s76, s84
TR a[142:143],v233 offset:12288
TR a[144:145],v232 offset:512
MM v[72:75],a[196:199],v[180:183],v[72:75]
s_lshl_b32 s76, s76, 2
EX v67,v67
MM v[72:75],a[192:195],v[164:167],v[72:75]
s_lshr_b32 s77, s49, 3
TR a[146:147],v232 offset:4608
CV v65,v66,v67
MM v[80:83],a[192:195],v[168:171],v[136:139]
s_add_u32 s76, s76, s77
EX v68,v68
MM v[80:83],a[196:199],v[184:187],v[80:83]
s_mul_i32 s95, s76, 33792
TR a[148:149],v232 offset:8704
TR a[150:151],v232 offset:12800
MM v[80:83],a[200:203],v[200:203],v[80:83]
s_lshl_b32 s76, %[swave], 13
EX v69,v69
MM v[80:83],a[204:207],v[216:219],v[80:83]
s_add_u32 s95, s95, s76
TR a[152:153],v233 offset:512
CV v66,v68,v69
MM v[84:87],a[220:223],v[216:219],v[136:139]
s_add_u32 s95, s95, 22708224
TR a[154:155],v233 offset:4608
v_mov_b32 v240, -1
MM v[84:87],a[216:219],v[200:203],v[84:87]
s_mov_b32 s67, s95
EX v70,v70
MM v[84:87],a[212:215],v[184:187],v[84:87]
s_add_u32 s87, s95, 6144
buffer_store_dword v240, v248, s[20:23], s87 offen offset:12 sc1
MM v[84:87],a[208:211],v[168:171],v[84:87]
EX v71,v71
MM v[92:95],a[208:211],v[172:175],v[140:143]
EX v72,v72
MM v[92:95],a[212:215],v[188:191],v[92:95]
TR a[156:157],v233 offset:8704
CV v67,v70,v71
MM v[92:95],a[216:219],v[204:207],v[92:95]
EX v73,v73
MM v[92:95],a[220:223],v[220:223],v[92:95]
EX v74,v74
MM v[88:91],a[204:207],v[220:223],v[140:143]
s_add_u32 s87, s95, 7168
buffer_store_dword v240, v248, s[20:23], s87 offen offset:12 sc1
MM v[88:91],a[200:203],v[204:207],v[88:91]
TR a[158:159],v233 offset:12800
CV v72,v72,v73
MM v[88:91],a[196:199],v[188:191],v[88:91]
TR a[160:161],v232 offset:1024
v_lshrrev_b32 v34, 1, v248
MM v[88:91],a[192:195],v[172:175],v[88:91]
s_mov_b32 s90, 86016
buffer_load_dwordx2 v[32:33], v34, s[20:23], s90 offen offset:0 sc1
MM v[96:99],a[224:227],v[160:163],v[128:131]
EX v75,v75
MM v[96:99],a[228:231],v[176:179],v[96:99]
EX v76,v76
MM v[96:99],a[232:235],v[192:195],v[96:99]
TR a[162:163],v232 offset:5120
CV v73,v74,v75
MM v[96:99],a[236:239],v[208:211],v[96:99]
EX v77,v77
MM v[100:103],a[252:255],v[208:211],v[128:131]
TR a[164:165],v232 offset:9216
TR a[166:167],v232 offset:13312
MM v[100:103],a[248:251],v[192:195],v[100:103]
EX v78,v78
MM v[100:103],a[244:247],v[176:179],v[100:103]
EX v79,v79
MM v[100:103],a[240:243],v[160:163],v[100:103]
TR a[168:169],v233 offset:1024
CV v74,v76,v77
MM v[108:111],a[240:243],v[164:167],v[132:135]
EX v80,v80
MM v[108:111],a[244:247],v[180:183],v[108:111]
TR a[170:171],v233 offset:5120
CV v75,v78,v79
MM v[108:111],a[248:251],v[196:199],v[108:111]
EX v81,v81
MM v[108:111],a[252:255],v[212:215],v[108:111]
TR a[172:173],v233 offset:9216
TR a[174:175],v233 offset:13312
MM v[104:107],a[236:239],v[212:215],v[132:135]
EX v82,v82
MM v[104:107],a[232:235],v[196:199],v[104:107]
EX v83,v83
MM v[104:107],a[228:231],v[180:183],v[104:107]
TR a[176:177],v232 offset:1536
CV v80,v80,v81
MM v[104:107],a[224:227],v[164:167],v[104:107]
EX v84,v84
MM v[112:115],a[224:227],v[168:171],v[136:139]
TR a[178:179],v232 offset:5632
CV v81,v82,v83
MM v[112:115],a[228:231],v[184:187],v[112:115]
EX v85,v85
MM v[112:115],a[232:235],v[200:203],v[112:115]
TR a[180:181],v232 offset:9728
TR a[182:183],v232 offset:13824
MM v[112:115],a[236:239],v[216:219],v[112:115]
EX v86,v86
MM v[116:119],a[252:255],v[216:219],v[136:139]
EX v87,v87
MM v[116:119],a[248:251],v[200:203],v[116:119]
TR a[184:185],v233 offset:1536
CV v82,v84,v85
MM v[116:119],a[244:247],v[184:187],v[116:119]
EX v88,v88
MM v[116:119],a[240:243],v[168:171],v[116:119]
TR a[186:187],v233 offset:5632
CV v83,v86,v87
MM v[124:127],a[240:243],v[172:175],v[140:143]
EX v89,v89
MM v[124:127],a[244:247],v[188:191],v[124:127]
TR a[188:189],v233 offset:9728
TR a[190:191],v233 offset:13824
MM v[124:127],a[248:251],v[204:207],v[124:127]
EX v90,v90
MM v[124:127],a[252:255],v[220:223],v[124:127]
EX v91,v91
MM v[120:123],a[236:239],v[220:223],v[140:143]
EX v92,v92
MM v[120:123],a[232:235],v[204:207],v[120:123]
EX v93,v93
MM v[120:123],a[228:231],v[188:191],v[120:123]
EX v94,v94
MM v[120:123],a[224:227],v[172:175],v[120:123]
s_waitcnt vmcnt(11) lgkmcnt(0)
s_barrier
MM a[0:3],a[128:131],v[0:3],a[0:3]
s_add_u32 m0, s65, 0
CV v88,v88,v89
CV v89,v90,v91
MM a[0:3],a[132:135],v[4:7],a[0:3]
RD a[192:195],v224 offset:32768
CV v90,v92,v93
)ASM"
R"ASM(
MM a[8:11],a[132:135],v[12:15],a[8:11]
EX v95,v95
MM a[8:11],a[128:131],v[8:11],a[8:11]
RD a[196:199],v225 offset:32768
CV v91,v94,v95
MM a[16:19],a[128:131],v[16:19],0
EX v96,v96
MM a[16:19],a[132:135],v[20:23],a[16:19]
buffer_load_dwordx4 v228, s[8:11], s64 offen offset:0 lds
MM a[24:27],a[132:135],v[28:31],0
EX v97,v97
MM a[24:27],a[128:131],v[24:27],a[24:27]
RD a[200:203],v226 offset:32768
CV v68,v96,v97
MM a[28:31],a[136:139],v[24:27],0
EX v98,v98
MM a[28:31],a[140:143],v[28:31],a[28:31]
EX v99,v99
MM a[20:23],a[140:143],v[20:23],0
RD a[204:207],v227 offset:32768
CV v69,v98,v99
MM a[20:23],a[136:139],v[16:19],a[20:23]
buffer_load_dwordx4 v229, s[8:11], s64 offen offset:1024 lds
MM a[12:15],a[136:139],v[8:11],a[12:15]
EX v100,v100
MM a[12:15],a[140:143],v[12:15],a[12:15]
EX v101,v101
MM a[4:7],a[140:143],v[4:7],a[4:7]
RD a[208:211],v224 offset:36864
CV v70,v100,v101
MM a[4:7],a[136:139],v[0:3],a[4:7]
EX v102,v102
MM a[32:35],a[144:147],v[0:3],a[32:35]
EX v103,v103
MM a[32:35],a[148:151],v[4:7],a[32:35]
RD a[212:215],v225 offset:36864
CV v71,v102,v103
MM a[40:43],a[148:151],v[12:15],a[40:43]
buffer_load_dwordx4 v230, s[8:11], s64 offen offset:2048 lds
MM a[40:43],a[144:147],v[8:11],a[40:43]
EX v104,v104
MM a[48:51],a[144:147],v[16:19],0
EX v105,v105
MM a[48:51],a[148:151],v[20:23],a[48:51]
RD a[216:219],v226 offset:36864
CV v76,v104,v105
MM a[56:59],a[148:151],v[28:31],0
EX v106,v106
MM a[56:59],a[144:147],v[24:27],a[56:59]
EX v107,v107
MM a[60:63],a[152:155],v[24:27],0
RD a[220:223],v227 offset:36864
CV v77,v106,v107
MM a[60:63],a[156:159],v[28:31],a[60:63]
buffer_load_dwordx4 v231, s[8:11], s64 offen offset:3072 lds
MM a[52:55],a[156:159],v[20:23],0
s_add_u32 m0, s65, 65536
EX v108,v108
MM a[52:55],a[152:155],v[16:19],a[52:55]
EX v109,v109
MM a[44:47],a[152:155],v[8:11],a[44:47]
RD a[224:227],v224 offset:40960
CV v78,v108,v109
MM a[44:47],a[156:159],v[12:15],a[44:47]
EX v110,v110
MM a[36:39],a[156:159],v[4:7],a[36:39]
buffer_load_dwordx4 v235, s[12:15], s81 offen offset:0 lds
MM a[36:39],a[152:155],v[0:3],a[36:39]
EX v111,v111
MM a[64:67],a[160:163],v[0:3],a[64:67]
s_waitcnt vmcnt(5)
v_cmp_ne_u32 vcc, s69, v33
s_cbranch_vccnz LRRK_%=
LRRKb_%=:
MM a[64:67],a[164:167],v[4:7],a[64:67]
v_add_u32_dpp v32, v32, v32 row_ror:8 row_mask:0xf bank_mask:0xf
CV v79,v110,v111
MM a[72:75],a[164:167],v[12:15],a[72:75]
s_and_b32 s76, %[sb], 7
RD a[228:231],v225 offset:40960
v_mov_b32 v35, v32
MM a[72:75],a[160:163],v[8:11],a[72:75]
s_nop 0
v_permlane16_swap_b32 v32, v35
v_add_u32 v32, v32, v35
MM a[80:83],a[160:163],v[16:19],0
RD a[232:235],v226 offset:40960
v_mov_b32 v35, v32
MM a[80:83],a[164:167],v[20:23],a[80:83]
s_nop 0
v_permlane32_swap_b32 v32, v35
v_add_u32 v32, v32, v35
MM a[88:91],a[164:167],v[28:31],0
RD a[236:239],v227 offset:40960
v_readlane_b32 s77, v32, s76
MM a[88:91],a[160:163],v[24:27],a[88:91]
s_bfm_b32 s76, s76, 0
EX v112,v112
MM a[92:95],a[168:171],v[24:27],0
v_cmp_gt_u32 s[60:61], s77, v32
v_cmp_eq_u32 s[62:63], s77, v32
MM a[92:95],a[172:175],v[28:31],a[92:95]
s_and_b32 s62, s62, s76
EX v113,v113
MM a[84:87],a[172:175],v[20:23],0
s_or_b32 s60, s60, s62
EX v114,v114
MM a[84:87],a[168:171],v[16:19],a[84:87]
s_and_b32 s60, s60, 255
EX v115,v115
MM a[76:79],a[168:171],v[8:11],a[76:79]
s_bcnt1_i32_b32 s51, s60
EX v116,v116
MM a[76:79],a[172:175],v[12:15],a[76:79]
s_lshr_b32 s16, %[sb], 3
EX v117,v117
MM a[68:71],a[172:175],v[4:7],a[68:71]
s_lshl_b32 s76, s51, 5
CV v84,v112,v113
CV v85,v114,v115
MM a[68:71],a[168:171],v[0:3],a[68:71]
s_add_u32 s16, s16, s76
EX v118,v118
MM a[96:99],a[176:179],v[0:3],a[96:99]
EX v119,v119
MM a[96:99],a[180:183],v[4:7],a[96:99]
CV v86,v116,v117
CV v87,v118,v119
MM a[104:107],a[180:183],v[12:15],a[104:107]
EX v120,v120
MM a[104:107],a[176:179],v[8:11],a[104:107]
buffer_load_dwordx4 v235, s[12:15], s64 offen offset:1024 lds
MM a[112:115],a[176:179],v[16:19],0
EX v121,v121
MM a[112:115],a[180:183],v[20:23],a[112:115]
RD a[240:243],v224 offset:45056
CV v92,v120,v121
MM a[120:123],a[180:183],v[28:31],0
EX v122,v122
MM a[120:123],a[176:179],v[24:27],a[120:123]
EX v123,v123
MM a[124:127],a[184:187],v[24:27],0
buffer_load_dwordx4 v235, s[12:15], s81 offen offset:2048 lds
MM a[124:127],a[188:191],v[28:31],a[124:127]
EX v124,v124
MM a[116:119],a[188:191],v[20:23],0
RD a[244:247],v225 offset:45056
CV v93,v122,v123
MM a[116:119],a[184:187],v[16:19],a[116:119]
EX v125,v125
MM a[108:111],a[184:187],v[8:11],a[108:111]
RD a[248:251],v226 offset:45056
CV v94,v124,v125
MM a[108:111],a[188:191],v[12:15],a[108:111]
EX v126,v126
MM a[100:103],a[188:191],v[4:7],a[100:103]
EX v127,v127
MM a[100:103],a[184:187],v[0:3],a[100:103]
buffer_load_dwordx4 v235, s[12:15], s64 offen offset:3072 lds
v_smfmac_f32_16x16x64_bf16 v[144:147], v[236:239], v[64:71], v234
RD a[252:255],v227 offset:45056
v_smfmac_f32_16x16x64_bf16 v[148:151], v[236:239], v[72:79], v234
CV v95,v126,v127
v_smfmac_f32_16x16x64_bf16 v[152:155], v[236:239], v[80:87], v234
s_nop 0
v_smfmac_f32_16x16x64_bf16 v[156:159], v[236:239], v[88:95], v234
s_waitcnt lgkmcnt(0)
s_cmp_lt_u32 s51, 4
s_cbranch_scc1 LLAR_%=
s_mov_b32 s70, 6
LLAP_%=:
MM v[0:3],a[192:195],v[160:163],v[128:131]
s_add_u32 s64, s64, 16384
TR a[128:129],v232 offset:16384
s_add_u32 s81, s64, 896
MM v[0:3],a[196:199],v[176:179],v[0:3]
TR a[130:131],v232 offset:20480
TR a[132:133],v232 offset:24576
MM v[0:3],a[200:203],v[192:195],v[0:3]
v_max3_f32 v249, v144, v145, v148
v_max3_f32 v249, v249, v149, v152
MM v[0:3],a[204:207],v[208:211],v[0:3]
v_max3_f32 v249, v249, v153, v156
v_max_f32 v249, v249, v157
MM v[4:7],a[220:223],v[208:211],v[128:131]
TR a[134:135],v232 offset:28672
v_cmp_lt_f32 vcc, s71, v249
MM v[4:7],a[216:219],v[192:195],v[4:7]
s_cbranch_vccnz LSITE5_%=
LBACK5_%=:
TR a[136:137],v233 offset:16384
v_add_f32 v244, v144, v145
MM v[4:7],a[212:215],v[176:179],v[4:7]
v_add_f32 v245, v148, v149
v_add_f32 v246, v152, v153
MM v[4:7],a[208:211],v[160:163],v[4:7]
TR a[138:139],v233 offset:20480
v_add_f32 v247, v156, v157
MM v[12:15],a[208:211],v[164:167],v[132:135]
EX v0,v0
MM v[12:15],a[212:215],v[180:183],v[12:15]
TR a[140:141],v233 offset:24576
TR a[142:143],v233 offset:28672
MM v[12:15],a[216:219],v[196:199],v[12:15]
EX v1,v1
MM v[12:15],a[220:223],v[212:215],v[12:15]
TR a[144:145],v232 offset:16896
CV v0,v0,v1
MM v[8:11],a[204:207],v[212:215],v[132:135]
EX v2,v2
MM v[8:11],a[200:203],v[196:199],v[8:11]
EX v3,v3
MM v[8:11],a[196:199],v[180:183],v[8:11]
TR a[146:147],v232 offset:20992
CV v1,v2,v3
MM v[8:11],a[192:195],v[164:167],v[8:11]
EX v4,v4
MM v[16:19],a[192:195],v[168:171],v[136:139]
TR a[148:149],v232 offset:25088
TR a[150:151],v232 offset:29184
MM v[16:19],a[196:199],v[184:187],v[16:19]
EX v5,v5
MM v[16:19],a[200:203],v[200:203],v[16:19]
TR a[152:153],v233 offset:16896
CV v2,v4,v5
MM v[16:19],a[204:207],v[216:219],v[16:19]
EX v6,v6
MM v[20:23],a[220:223],v[216:219],v[136:139]
s_cmp_lg_u32 s70, 3
s_cbranch_scc1 LWW_%=
v_lshlrev_b32 v96, 3, v248
v_min_u32 v96, 4992, v96
s_andn2_b32 s90, 1, %[sb]
s_lshl_b32 s90, s90, 3
s_add_u32 s90, s90, s50
s_lshl_b32 s90, s90, 2
s_add_u32 s90, s90, s84
s_lshl_b32 s90, s90, 2
s_add_u32 s90, s90, %[swave]
s_mul_i32 s90, s90, 5120
s_mul_i32 s91, s84, 320
s_sub_u32 s90, s90, s91
s_add_u32 s91, %[slds], 135168
s_mov_b32 m0, s91
s_nop 0
buffer_load_dword v96, s[24:27], s90 offen offset:0 lds
LWW_%=:
MM v[20:23],a[216:219],v[200:203],v[20:23]
EX v7,v7
MM v[20:23],a[212:215],v[184:187],v[20:23]
TR a[154:155],v233 offset:20992
CV v3,v6,v7
MM v[20:23],a[208:211],v[168:171],v[20:23]
EX v8,v8
MM v[28:31],a[208:211],v[172:175],v[140:143]
TR a[156:157],v233 offset:25088
TR a[158:159],v233 offset:29184
MM v[28:31],a[212:215],v[188:191],v[28:31]
EX v9,v9
MM v[28:31],a[216:219],v[204:207],v[28:31]
TR a[160:161],v232 offset:17408
CV v8,v8,v9
MM v[28:31],a[220:223],v[220:223],v[28:31]
EX v10,v10
MM v[24:27],a[204:207],v[220:223],v[140:143]
EX v11,v11
MM v[24:27],a[200:203],v[204:207],v[24:27]
TR a[162:163],v232 offset:21504
CV v9,v10,v11
MM v[24:27],a[196:199],v[188:191],v[24:27]
EX v12,v12
MM v[24:27],a[192:195],v[172:175],v[24:27]
TR a[164:165],v232 offset:25600
TR a[166:167],v232 offset:29696
MM v[32:35],a[224:227],v[160:163],v[128:131]
EX v13,v13
MM v[32:35],a[228:231],v[176:179],v[32:35]
TR a[168:169],v233 offset:17408
CV v10,v12,v13
MM v[32:35],a[232:235],v[192:195],v[32:35]
EX v14,v14
MM v[32:35],a[236:239],v[208:211],v[32:35]
EX v15,v15
MM v[36:39],a[252:255],v[208:211],v[128:131]
TR a[170:171],v233 offset:21504
CV v11,v14,v15
MM v[36:39],a[248:251],v[192:195],v[36:39]
EX v16,v16
MM v[36:39],a[244:247],v[176:179],v[36:39]
TR a[172:173],v233 offset:25600
TR a[174:175],v233 offset:29696
MM v[36:39],a[240:243],v[160:163],v[36:39]
EX v17,v17
MM v[44:47],a[240:243],v[164:167],v[132:135]
TR a[176:177],v232 offset:17920
CV v16,v16,v17
MM v[44:47],a[244:247],v[180:183],v[44:47]
EX v18,v18
MM v[44:47],a[248:251],v[196:199],v[44:47]
TR a[178:179],v232 offset:22016
TR a[180:181],v232 offset:26112
MM v[44:47],a[252:255],v[212:215],v[44:47]
EX v19,v19
MM v[40:43],a[236:239],v[212:215],v[132:135]
TR a[182:183],v232 offset:30208
CV v17,v18,v19
MM v[40:43],a[232:235],v[196:199],v[40:43]
EX v20,v20
MM v[40:43],a[228:231],v[180:183],v[40:43]
EX v21,v21
MM v[40:43],a[224:227],v[164:167],v[40:43]
TR a[184:185],v233 offset:17920
CV v18,v20,v21
MM v[48:51],a[224:227],v[168:171],v[136:139]
EX v22,v22
MM v[48:51],a[228:231],v[184:187],v[48:51]
TR a[186:187],v233 offset:22016
TR a[188:189],v233 offset:26112
MM v[48:51],a[232:235],v[200:203],v[48:51]
EX v23,v23
MM v[48:51],a[236:239],v[216:219],v[48:51]
TR a[190:191],v233 offset:30208
CV v19,v22,v23
MM v[52:55],a[252:255],v[216:219],v[136:139]
EX v24,v24
MM v[52:55],a[248:251],v[200:203],v[52:55]
EX v25,v25
MM v[52:55],a[244:247],v[184:187],v[52:55]
EX v26,v26
MM v[52:55],a[240:243],v[168:171],v[52:55]
EX v27,v27
MM v[60:63],a[240:243],v[172:175],v[140:143]
EX v28,v28
MM v[60:63],a[244:247],v[188:191],v[60:63]
EX v29,v29
MM v[60:63],a[248:251],v[204:207],v[60:63]
EX v30,v30
MM v[60:63],a[252:255],v[220:223],v[60:63]
EX v31,v31
MM v[56:59],a[236:239],v[220:223],v[140:143]
EX v32,v32
MM v[56:59],a[232:235],v[204:207],v[56:59]
EX v33,v33
MM v[56:59],a[228:231],v[188:191],v[56:59]
EX v34,v34
MM v[56:59],a[224:227],v[172:175],v[56:59]
s_waitcnt vmcnt(12) lgkmcnt(0)
s_barrier
MM a[0:3],a[128:131],v[64:67],a[0:3]
s_add_u32 m0, s65, 16384
CV v24,v24,v25
CV v25,v26,v27
MM a[0:3],a[132:135],v[68:71],a[0:3]
RD a[192:195],v224 offset:49152
CV v26,v28,v29
MM a[8:11],a[132:135],v[76:79],a[8:11]
RD a[196:199],v225 offset:49152
CV v27,v30,v31
MM a[8:11],a[128:131],v[72:75],a[8:11]
buffer_load_dwordx4 v228, s[8:11], s64 offen offset:0 lds
MM a[16:19],a[128:131],v[80:83],a[16:19]
RD a[200:203],v226 offset:49152
CV v4,v32,v33
MM a[16:19],a[132:135],v[84:87],a[16:19]
buffer_load_dwordx4 v229, s[8:11], s64 offen offset:1024 lds
MM a[24:27],a[132:135],v[92:95],a[24:27]
RD a[204:207],v227 offset:49152
RD a[208:211],v224 offset:53248
MM a[24:27],a[128:131],v[88:91],a[24:27]
EX v35,v35
MM a[28:31],a[136:139],v[88:91],a[28:31]
RD a[212:215],v225 offset:53248
CV v5,v34,v35
MM a[28:31],a[140:143],v[92:95],a[28:31]
EX v36,v36
MM a[20:23],a[140:143],v[84:87],a[20:23]
buffer_load_dwordx4 v230, s[8:11], s64 offen offset:2048 lds
MM a[20:23],a[136:139],v[80:83],a[20:23]
EX v37,v37
MM a[12:15],a[136:139],v[72:75],a[12:15]
RD a[216:219],v226 offset:53248
CV v6,v36,v37
MM a[12:15],a[140:143],v[76:79],a[12:15]
EX v38,v38
MM a[4:7],a[140:143],v[68:71],a[4:7]
EX v39,v39
MM a[4:7],a[136:139],v[64:67],a[4:7]
RD a[220:223],v227 offset:53248
CV v7,v38,v39
MM a[32:35],a[144:147],v[64:67],a[32:35]
EX v40,v40
MM a[32:35],a[148:151],v[68:71],a[32:35]
buffer_load_dwordx4 v231, s[8:11], s64 offen offset:3072 lds
MM a[40:43],a[148:151],v[76:79],a[40:43]
s_add_u32 m0, s65, 81920
EX v41,v41
MM a[40:43],a[144:147],v[72:75],a[40:43]
RD a[224:227],v224 offset:57344
CV v12,v40,v41
MM a[48:51],a[144:147],v[80:83],a[48:51]
EX v42,v42
MM a[48:51],a[148:151],v[84:87],a[48:51]
EX v43,v43
LPG7_%=:
s_cbranch_execz LPG11_%=
MM a[56:59],a[148:151],v[92:95],a[56:59]
)ASM"
R"ASM(
RD a[228:231],v225 offset:57344
CV v13,v42,v43
MM a[56:59],a[144:147],v[88:91],a[56:59]
EX v44,v44
MM a[60:63],a[152:155],v[88:91],a[60:63]
buffer_load_dwordx4 v235, s[12:15], s81 offen offset:0 lds
MM a[60:63],a[156:159],v[92:95],a[60:63]
EX v45,v45
MM a[52:55],a[156:159],v[84:87],a[52:55]
RD a[232:235],v226 offset:57344
CV v14,v44,v45
MM a[52:55],a[152:155],v[80:83],a[52:55]
EX v46,v46
MM a[44:47],a[152:155],v[72:75],a[44:47]
EX v47,v47
MM a[44:47],a[156:159],v[76:79],a[44:47]
RD a[236:239],v227 offset:57344
CV v15,v46,v47
MM a[36:39],a[156:159],v[68:71],a[36:39]
buffer_load_dwordx4 v235, s[12:15], s64 offen offset:1024 lds
MM a[36:39],a[152:155],v[64:67],a[36:39]
EX v48,v48
MM a[64:67],a[160:163],v[64:67],a[64:67]
EX v49,v49
MM a[64:67],a[164:167],v[68:71],a[64:67]
RD a[240:243],v224 offset:61440
CV v20,v48,v49
MM a[72:75],a[164:167],v[76:79],a[72:75]
EX v50,v50
MM a[72:75],a[160:163],v[72:75],a[72:75]
RD a[244:247],v225 offset:61440
RD a[248:251],v226 offset:61440
MM a[80:83],a[160:163],v[80:83],a[80:83]
EX v51,v51
MM a[80:83],a[164:167],v[84:87],a[80:83]
RD a[252:255],v227 offset:61440
CV v21,v50,v51
MM a[88:91],a[164:167],v[92:95],a[88:91]
EX v52,v52
MM a[88:91],a[160:163],v[88:91],a[88:91]
buffer_load_dwordx4 v235, s[12:15], s81 offen offset:2048 lds
MM a[92:95],a[168:171],v[88:91],a[92:95]
EX v53,v53
MM a[92:95],a[172:175],v[92:95],a[92:95]
EX v54,v54
MM a[84:87],a[172:175],v[84:87],a[84:87]
buffer_load_dwordx4 v235, s[12:15], s64 offen offset:3072 lds
MM a[84:87],a[168:171],v[80:83],a[84:87]
EX v55,v55
MM a[76:79],a[168:171],v[72:75],a[76:79]
EX v56,v56
MM a[76:79],a[172:175],v[76:79],a[76:79]
EX v57,v57
MM a[68:71],a[172:175],v[68:71],a[68:71]
EX v58,v58
MM a[68:71],a[168:171],v[64:67],a[68:71]
EX v59,v59
MM a[96:99],a[176:179],v[64:67],a[96:99]
EX v60,v60
MM a[96:99],a[180:183],v[68:71],a[96:99]
EX v61,v61
MM a[104:107],a[180:183],v[76:79],a[104:107]
EX v62,v62
MM a[104:107],a[176:179],v[72:75],a[104:107]
EX v63,v63
MM a[112:115],a[176:179],v[80:83],a[112:115]
CV v22,v52,v53
CV v23,v54,v55
MM a[112:115],a[180:183],v[84:87],a[112:115]
CV v28,v56,v57
CV v29,v58,v59
MM a[120:123],a[180:183],v[92:95],a[120:123]
CV v30,v60,v61
CV v31,v62,v63
MM a[120:123],a[176:179],v[88:91],a[120:123]
MM a[124:127],a[184:187],v[88:91],a[124:127]
MM a[124:127],a[188:191],v[92:95],a[124:127]
MM a[116:119],a[188:191],v[84:87],a[116:119]
MM a[116:119],a[184:187],v[80:83],a[116:119]
MM a[108:111],a[184:187],v[72:75],a[108:111]
MM a[108:111],a[188:191],v[76:79],a[108:111]
MM a[100:103],a[188:191],v[68:71],a[100:103]
MM a[100:103],a[184:187],v[64:67],a[100:103]
v_smfmac_f32_16x16x64_bf16 v[144:147], v[236:239], v[0:7], v234
v_smfmac_f32_16x16x64_bf16 v[148:151], v[236:239], v[8:15], v234
v_smfmac_f32_16x16x64_bf16 v[152:155], v[236:239], v[16:23], v234
v_smfmac_f32_16x16x64_bf16 v[156:159], v[236:239], v[24:31], v234
s_waitcnt lgkmcnt(0)
MM v[64:67],a[192:195],v[160:163],v[128:131]
s_add_u32 s64, s64, 16384
TR a[128:129],v232 offset:32768
s_add_u32 s81, s64, 896
MM v[64:67],a[196:199],v[176:179],v[64:67]
TR a[130:131],v232 offset:36864
TR a[132:133],v232 offset:40960
MM v[64:67],a[200:203],v[192:195],v[64:67]
v_max3_f32 v249, v144, v145, v148
v_max3_f32 v249, v249, v149, v152
MM v[64:67],a[204:207],v[208:211],v[64:67]
v_max3_f32 v249, v249, v153, v156
v_max_f32 v249, v249, v157
MM v[68:71],a[220:223],v[208:211],v[128:131]
TR a[134:135],v232 offset:45056
v_cmp_lt_f32 vcc, s71, v249
MM v[68:71],a[216:219],v[192:195],v[68:71]
s_cbranch_vccnz LSITE6_%=
LBACK6_%=:
TR a[136:137],v233 offset:32768
v_add_f32 v244, v144, v145
MM v[68:71],a[212:215],v[176:179],v[68:71]
v_add_f32 v245, v148, v149
v_add_f32 v246, v152, v153
MM v[68:71],a[208:211],v[160:163],v[68:71]
TR a[138:139],v233 offset:36864
v_add_f32 v247, v156, v157
MM v[76:79],a[208:211],v[164:167],v[132:135]
EX v64,v64
MM v[76:79],a[212:215],v[180:183],v[76:79]
TR a[140:141],v233 offset:40960
TR a[142:143],v233 offset:45056
MM v[76:79],a[216:219],v[196:199],v[76:79]
EX v65,v65
MM v[76:79],a[220:223],v[212:215],v[76:79]
TR a[144:145],v232 offset:33280
CV v64,v64,v65
MM v[72:75],a[204:207],v[212:215],v[132:135]
EX v66,v66
MM v[72:75],a[200:203],v[196:199],v[72:75]
EX v67,v67
MM v[72:75],a[196:199],v[180:183],v[72:75]
TR a[146:147],v232 offset:37376
CV v65,v66,v67
MM v[72:75],a[192:195],v[164:167],v[72:75]
EX v68,v68
MM v[80:83],a[192:195],v[168:171],v[136:139]
TR a[148:149],v232 offset:41472
TR a[150:151],v232 offset:45568
MM v[80:83],a[196:199],v[184:187],v[80:83]
EX v69,v69
MM v[80:83],a[200:203],v[200:203],v[80:83]
TR a[152:153],v233 offset:33280
CV v66,v68,v69
MM v[80:83],a[204:207],v[216:219],v[80:83]
EX v70,v70
MM v[84:87],a[220:223],v[216:219],v[136:139]
EX v71,v71
MM v[84:87],a[216:219],v[200:203],v[84:87]
TR a[154:155],v233 offset:37376
CV v67,v70,v71
MM v[84:87],a[212:215],v[184:187],v[84:87]
EX v72,v72
MM v[84:87],a[208:211],v[168:171],v[84:87]
TR a[156:157],v233 offset:41472
TR a[158:159],v233 offset:45568
MM v[92:95],a[208:211],v[172:175],v[140:143]
EX v73,v73
MM v[92:95],a[212:215],v[188:191],v[92:95]
TR a[160:161],v232 offset:33792
CV v72,v72,v73
MM v[92:95],a[216:219],v[204:207],v[92:95]
EX v74,v74
MM v[92:95],a[220:223],v[220:223],v[92:95]
EX v75,v75
MM v[88:91],a[204:207],v[220:223],v[140:143]
TR a[162:163],v232 offset:37888
CV v73,v74,v75
MM v[88:91],a[200:203],v[204:207],v[88:91]
TR a[164:165],v232 offset:41984
TR a[166:167],v232 offset:46080
MM v[88:91],a[196:199],v[188:191],v[88:91]
EX v76,v76
MM v[88:91],a[192:195],v[172:175],v[88:91]
EX v77,v77
MM v[96:99],a[224:227],v[160:163],v[128:131]
TR a[168:169],v233 offset:33792
CV v74,v76,v77
MM v[96:99],a[228:231],v[176:179],v[96:99]
EX v78,v78
MM v[96:99],a[232:235],v[192:195],v[96:99]
TR a[170:171],v233 offset:37888
TR a[172:173],v233 offset:41984
MM v[96:99],a[236:239],v[208:211],v[96:99]
EX v79,v79
MM v[100:103],a[252:255],v[208:211],v[128:131]
TR a[174:175],v233 offset:46080
CV v75,v78,v79
MM v[100:103],a[248:251],v[192:195],v[100:103]
EX v80,v80
MM v[100:103],a[244:247],v[176:179],v[100:103]
EX v81,v81
MM v[100:103],a[240:243],v[160:163],v[100:103]
TR a[176:177],v232 offset:34304
CV v80,v80,v81
MM v[108:111],a[240:243],v[164:167],v[132:135]
buffer_store_dword v240, v248, s[20:23], s67 offen offset:12 sc1
MM v[108:111],a[244:247],v[180:183],v[108:111]
s_add_u32 s67, s67, 1024
EX v82,v82
MM v[108:111],a[248:251],v[196:199],v[108:111]
TR a[178:179],v232 offset:38400
TR a[180:181],v232 offset:42496
MM v[108:111],a[252:255],v[212:215],v[108:111]
EX v83,v83
MM v[104:107],a[236:239],v[212:215],v[132:135]
TR a[182:183],v232 offset:46592
CV v81,v82,v83
MM v[104:107],a[232:235],v[196:199],v[104:107]
EX v84,v84
MM v[104:107],a[228:231],v[180:183],v[104:107]
EX v85,v85
MM v[104:107],a[224:227],v[164:167],v[104:107]
TR a[184:185],v233 offset:34304
CV v82,v84,v85
MM v[112:115],a[224:227],v[168:171],v[136:139]
EX v86,v86
MM v[112:115],a[228:231],v[184:187],v[112:115]
TR a[186:187],v233 offset:38400
TR a[188:189],v233 offset:42496
MM v[112:115],a[232:235],v[200:203],v[112:115]
EX v87,v87
MM v[112:115],a[236:239],v[216:219],v[112:115]
TR a[190:191],v233 offset:46592
CV v83,v86,v87
MM v[116:119],a[252:255],v[216:219],v[136:139]
EX v88,v88
MM v[116:119],a[248:251],v[200:203],v[116:119]
EX v89,v89
MM v[116:119],a[244:247],v[184:187],v[116:119]
EX v90,v90
MM v[116:119],a[240:243],v[168:171],v[116:119]
EX v91,v91
MM v[124:127],a[240:243],v[172:175],v[140:143]
EX v92,v92
MM v[124:127],a[244:247],v[188:191],v[124:127]
EX v93,v93
MM v[124:127],a[248:251],v[204:207],v[124:127]
EX v94,v94
MM v[124:127],a[252:255],v[220:223],v[124:127]
EX v95,v95
MM v[120:123],a[236:239],v[220:223],v[140:143]
EX v96,v96
MM v[120:123],a[232:235],v[204:207],v[120:123]
EX v97,v97
MM v[120:123],a[228:231],v[188:191],v[120:123]
EX v98,v98
MM v[120:123],a[224:227],v[172:175],v[120:123]
s_waitcnt vmcnt(12) lgkmcnt(0)
s_barrier
MM a[0:3],a[128:131],v[0:3],a[0:3]
s_add_u32 m0, s65, 32768
CV v88,v88,v89
CV v89,v90,v91
MM a[0:3],a[132:135],v[4:7],a[0:3]
RD a[192:195],v224 offset:0
CV v90,v92,v93
MM a[8:11],a[132:135],v[12:15],a[8:11]
RD a[196:199],v225 offset:0
CV v91,v94,v95
MM a[8:11],a[128:131],v[8:11],a[8:11]
buffer_load_dwordx4 v228, s[8:11], s64 offen offset:0 lds
MM a[16:19],a[128:131],v[16:19],a[16:19]
RD a[200:203],v226 offset:0
CV v68,v96,v97
MM a[16:19],a[132:135],v[20:23],a[16:19]
buffer_load_dwordx4 v229, s[8:11], s64 offen offset:1024 lds
MM a[24:27],a[132:135],v[28:31],a[24:27]
RD a[204:207],v227 offset:0
RD a[208:211],v224 offset:4096
MM a[24:27],a[128:131],v[24:27],a[24:27]
EX v99,v99
MM a[28:31],a[136:139],v[24:27],a[28:31]
RD a[212:215],v225 offset:4096
CV v69,v98,v99
MM a[28:31],a[140:143],v[28:31],a[28:31]
EX v100,v100
MM a[20:23],a[140:143],v[20:23],a[20:23]
buffer_load_dwordx4 v230, s[8:11], s64 offen offset:2048 lds
MM a[20:23],a[136:139],v[16:19],a[20:23]
EX v101,v101
MM a[12:15],a[136:139],v[8:11],a[12:15]
RD a[216:219],v226 offset:4096
CV v70,v100,v101
MM a[12:15],a[140:143],v[12:15],a[12:15]
EX v102,v102
MM a[4:7],a[140:143],v[4:7],a[4:7]
EX v103,v103
MM a[4:7],a[136:139],v[0:3],a[4:7]
RD a[220:223],v227 offset:4096
CV v71,v102,v103
MM a[32:35],a[144:147],v[0:3],a[32:35]
EX v104,v104
MM a[32:35],a[148:151],v[4:7],a[32:35]
buffer_load_dwordx4 v231, s[8:11], s64 offen offset:3072 lds
MM a[40:43],a[148:151],v[12:15],a[40:43]
s_add_u32 m0, s65, 98304
EX v105,v105
MM a[40:43],a[144:147],v[8:11],a[40:43]
RD a[224:227],v224 offset:8192
CV v76,v104,v105
MM a[48:51],a[144:147],v[16:19],a[48:51]
EX v106,v106
MM a[48:51],a[148:151],v[20:23],a[48:51]
EX v107,v107
MM a[56:59],a[148:151],v[28:31],a[56:59]
RD a[228:231],v225 offset:8192
CV v77,v106,v107
MM a[56:59],a[144:147],v[24:27],a[56:59]
EX v108,v108
MM a[60:63],a[152:155],v[24:27],a[60:63]
buffer_load_dwordx4 v235, s[12:15], s81 offen offset:0 lds
MM a[60:63],a[156:159],v[28:31],a[60:63]
EX v109,v109
MM a[52:55],a[156:159],v[20:23],a[52:55]
RD a[232:235],v226 offset:8192
CV v78,v108,v109
MM a[52:55],a[152:155],v[16:19],a[52:55]
EX v110,v110
MM a[44:47],a[152:155],v[8:11],a[44:47]
EX v111,v111
MM a[44:47],a[156:159],v[12:15],a[44:47]
RD a[236:239],v227 offset:8192
CV v79,v110,v111
MM a[36:39],a[156:159],v[4:7],a[36:39]
buffer_load_dwordx4 v235, s[12:15], s64 offen offset:1024 lds
MM a[36:39],a[152:155],v[0:3],a[36:39]
EX v112,v112
MM a[64:67],a[160:163],v[0:3],a[64:67]
EX v113,v113
MM a[64:67],a[164:167],v[4:7],a[64:67]
RD a[240:243],v224 offset:12288
CV v84,v112,v113
MM a[72:75],a[164:167],v[12:15],a[72:75]
EX v114,v114
MM a[72:75],a[160:163],v[8:11],a[72:75]
RD a[244:247],v225 offset:12288
RD a[248:251],v226 offset:12288
MM a[80:83],a[160:163],v[16:19],a[80:83]
EX v115,v115
MM a[80:83],a[164:167],v[20:23],a[80:83]
RD a[252:255],v227 offset:12288
CV v85,v114,v115
MM a[88:91],a[164:167],v[28:31],a[88:91]
EX v116,v116
MM a[88:91],a[160:163],v[24:27],a[88:91]
buffer_load_dwordx4 v235, s[12:15], s81 offen offset:2048 lds
MM a[92:95],a[168:171],v[24:27],a[92:95]
EX v117,v117
MM a[92:95],a[172:175],v[28:31],a[92:95]
EX v118,v118
MM a[84:87],a[172:175],v[20:23],a[84:87]
buffer_load_dwordx4 v235, s[12:15], s64 offen offset:3072 lds
MM a[84:87],a[168:171],v[16:19],a[84:87]
EX v119,v119
MM a[76:79],a[168:171],v[8:11],a[76:79]
EX v120,v120
MM a[76:79],a[172:175],v[12:15],a[76:79]
EX v121,v121
MM a[68:71],a[172:175],v[4:7],a[68:71]
EX v122,v122
MM a[68:71],a[168:171],v[0:3],a[68:71]
EX v123,v123
MM a[96:99],a[176:179],v[0:3],a[96:99]
EX v124,v124
MM a[96:99],a[180:183],v[4:7],a[96:99]
EX v125,v125
MM a[104:107],a[180:183],v[12:15],a[104:107]
EX v126,v126
MM a[104:107],a[176:179],v[8:11],a[104:107]
EX v127,v127
MM a[112:115],a[176:179],v[16:19],a[112:115]
CV v86,v116,v117
CV v87,v118,v119
MM a[112:115],a[180:183],v[20:23],a[112:115]
CV v92,v120,v121
CV v93,v122,v123
MM a[120:123],a[180:183],v[28:31],a[120:123]
CV v94,v124,v125
CV v95,v126,v127
MM a[120:123],a[176:179],v[24:27],a[120:123]
MM a[124:127],a[184:187],v[24:27],a[124:127]
MM a[124:127],a[188:191],v[28:31],a[124:127]
MM a[116:119],a[188:191],v[20:23],a[116:119]
MM a[116:119],a[184:187],v[16:19],a[116:119]
MM a[108:111],a[184:187],v[8:11],a[108:111]
)ASM"
R"ASM(
MM a[108:111],a[188:191],v[12:15],a[108:111]
MM a[100:103],a[188:191],v[4:7],a[100:103]
MM a[100:103],a[184:187],v[0:3],a[100:103]
v_smfmac_f32_16x16x64_bf16 v[144:147], v[236:239], v[64:71], v234
v_smfmac_f32_16x16x64_bf16 v[148:151], v[236:239], v[72:79], v234
v_smfmac_f32_16x16x64_bf16 v[152:155], v[236:239], v[80:87], v234
v_smfmac_f32_16x16x64_bf16 v[156:159], v[236:239], v[88:95], v234
s_waitcnt lgkmcnt(0)
MM v[0:3],a[192:195],v[160:163],v[128:131]
s_add_u32 s64, s64, 16384
TR a[128:129],v232 offset:49152
s_add_u32 s81, s64, 896
MM v[0:3],a[196:199],v[176:179],v[0:3]
TR a[130:131],v232 offset:53248
TR a[132:133],v232 offset:57344
MM v[0:3],a[200:203],v[192:195],v[0:3]
v_max3_f32 v249, v144, v145, v148
v_max3_f32 v249, v249, v149, v152
MM v[0:3],a[204:207],v[208:211],v[0:3]
v_max3_f32 v249, v249, v153, v156
v_max_f32 v249, v249, v157
MM v[4:7],a[220:223],v[208:211],v[128:131]
TR a[134:135],v232 offset:61440
v_cmp_lt_f32 vcc, s71, v249
MM v[4:7],a[216:219],v[192:195],v[4:7]
s_cbranch_vccnz LSITE7_%=
LBACK7_%=:
TR a[136:137],v233 offset:49152
v_add_f32 v244, v144, v145
MM v[4:7],a[212:215],v[176:179],v[4:7]
v_add_f32 v245, v148, v149
v_add_f32 v246, v152, v153
MM v[4:7],a[208:211],v[160:163],v[4:7]
TR a[138:139],v233 offset:53248
v_add_f32 v247, v156, v157
MM v[12:15],a[208:211],v[164:167],v[132:135]
EX v0,v0
MM v[12:15],a[212:215],v[180:183],v[12:15]
TR a[140:141],v233 offset:57344
TR a[142:143],v233 offset:61440
MM v[12:15],a[216:219],v[196:199],v[12:15]
EX v1,v1
MM v[12:15],a[220:223],v[212:215],v[12:15]
TR a[144:145],v232 offset:49664
CV v0,v0,v1
MM v[8:11],a[204:207],v[212:215],v[132:135]
EX v2,v2
MM v[8:11],a[200:203],v[196:199],v[8:11]
EX v3,v3
MM v[8:11],a[196:199],v[180:183],v[8:11]
TR a[146:147],v232 offset:53760
CV v1,v2,v3
MM v[8:11],a[192:195],v[164:167],v[8:11]
EX v4,v4
MM v[16:19],a[192:195],v[168:171],v[136:139]
TR a[148:149],v232 offset:57856
TR a[150:151],v232 offset:61952
MM v[16:19],a[196:199],v[184:187],v[16:19]
EX v5,v5
MM v[16:19],a[200:203],v[200:203],v[16:19]
TR a[152:153],v233 offset:49664
CV v2,v4,v5
MM v[16:19],a[204:207],v[216:219],v[16:19]
EX v6,v6
MM v[20:23],a[220:223],v[216:219],v[136:139]
s_cmp_lg_u32 s70, 2
s_cbranch_scc1 LIP_%=
s_lshr_b32 s77, %[sb], 3
s_lshl_b32 s77, s77, 2
s_add_u32 s77, s77, %[swave]
s_lshl_b32 s77, s77, 9
s_add_u32 s77, s77, LLAJ_%=-LPDP_%=
v_lshrrev_b32 v253, 1, v248
v_add_u32 v253, s77, v253
s_mov_b32 s77, LKEND_ka-LPDP_%=-16
v_min_u32 v253, s77, v253
s_getpc_b64 s[98:99]
LPDP_%=:
global_load_dword v253, v253, s[98:99]
LIP_%=:
MM v[20:23],a[216:219],v[200:203],v[20:23]
EX v7,v7
MM v[20:23],a[212:215],v[184:187],v[20:23]
TR a[154:155],v233 offset:53760
CV v3,v6,v7
MM v[20:23],a[208:211],v[168:171],v[20:23]
EX v8,v8
MM v[28:31],a[208:211],v[172:175],v[140:143]
TR a[156:157],v233 offset:57856
TR a[158:159],v233 offset:61952
MM v[28:31],a[212:215],v[188:191],v[28:31]
EX v9,v9
MM v[28:31],a[216:219],v[204:207],v[28:31]
TR a[160:161],v232 offset:50176
CV v8,v8,v9
MM v[28:31],a[220:223],v[220:223],v[28:31]
EX v10,v10
MM v[24:27],a[204:207],v[220:223],v[140:143]
EX v11,v11
MM v[24:27],a[200:203],v[204:207],v[24:27]
TR a[162:163],v232 offset:54272
CV v9,v10,v11
MM v[24:27],a[196:199],v[188:191],v[24:27]
EX v12,v12
MM v[24:27],a[192:195],v[172:175],v[24:27]
TR a[164:165],v232 offset:58368
TR a[166:167],v232 offset:62464
MM v[32:35],a[224:227],v[160:163],v[128:131]
EX v13,v13
MM v[32:35],a[228:231],v[176:179],v[32:35]
TR a[168:169],v233 offset:50176
CV v10,v12,v13
MM v[32:35],a[232:235],v[192:195],v[32:35]
EX v14,v14
MM v[32:35],a[236:239],v[208:211],v[32:35]
EX v15,v15
MM v[36:39],a[252:255],v[208:211],v[128:131]
TR a[170:171],v233 offset:54272
CV v11,v14,v15
MM v[36:39],a[248:251],v[192:195],v[36:39]
EX v16,v16
MM v[36:39],a[244:247],v[176:179],v[36:39]
TR a[172:173],v233 offset:58368
TR a[174:175],v233 offset:62464
MM v[36:39],a[240:243],v[160:163],v[36:39]
EX v17,v17
MM v[44:47],a[240:243],v[164:167],v[132:135]
TR a[176:177],v232 offset:50688
CV v16,v16,v17
MM v[44:47],a[244:247],v[180:183],v[44:47]
EX v18,v18
MM v[44:47],a[248:251],v[196:199],v[44:47]
TR a[178:179],v232 offset:54784
TR a[180:181],v232 offset:58880
MM v[44:47],a[252:255],v[212:215],v[44:47]
EX v19,v19
MM v[40:43],a[236:239],v[212:215],v[132:135]
TR a[182:183],v232 offset:62976
CV v17,v18,v19
MM v[40:43],a[232:235],v[196:199],v[40:43]
EX v20,v20
MM v[40:43],a[228:231],v[180:183],v[40:43]
EX v21,v21
MM v[40:43],a[224:227],v[164:167],v[40:43]
TR a[184:185],v233 offset:50688
CV v18,v20,v21
MM v[48:51],a[224:227],v[168:171],v[136:139]
EX v22,v22
MM v[48:51],a[228:231],v[184:187],v[48:51]
TR a[186:187],v233 offset:54784
TR a[188:189],v233 offset:58880
MM v[48:51],a[232:235],v[200:203],v[48:51]
EX v23,v23
MM v[48:51],a[236:239],v[216:219],v[48:51]
TR a[190:191],v233 offset:62976
CV v19,v22,v23
MM v[52:55],a[252:255],v[216:219],v[136:139]
EX v24,v24
MM v[52:55],a[248:251],v[200:203],v[52:55]
EX v25,v25
MM v[52:55],a[244:247],v[184:187],v[52:55]
EX v26,v26
MM v[52:55],a[240:243],v[168:171],v[52:55]
EX v27,v27
MM v[60:63],a[240:243],v[172:175],v[140:143]
EX v28,v28
MM v[60:63],a[244:247],v[188:191],v[60:63]
EX v29,v29
MM v[60:63],a[248:251],v[204:207],v[60:63]
EX v30,v30
MM v[60:63],a[252:255],v[220:223],v[60:63]
EX v31,v31
MM v[56:59],a[236:239],v[220:223],v[140:143]
EX v32,v32
MM v[56:59],a[232:235],v[204:207],v[56:59]
EX v33,v33
MM v[56:59],a[228:231],v[188:191],v[56:59]
EX v34,v34
MM v[56:59],a[224:227],v[172:175],v[56:59]
s_waitcnt vmcnt(12) lgkmcnt(0)
s_barrier
MM a[0:3],a[128:131],v[64:67],a[0:3]
s_add_u32 m0, s65, 49152
CV v24,v24,v25
CV v25,v26,v27
MM a[0:3],a[132:135],v[68:71],a[0:3]
RD a[192:195],v224 offset:16384
CV v26,v28,v29
MM a[8:11],a[132:135],v[76:79],a[8:11]
RD a[196:199],v225 offset:16384
CV v27,v30,v31
MM a[8:11],a[128:131],v[72:75],a[8:11]
buffer_load_dwordx4 v228, s[8:11], s64 offen offset:0 lds
MM a[16:19],a[128:131],v[80:83],a[16:19]
RD a[200:203],v226 offset:16384
CV v4,v32,v33
MM a[16:19],a[132:135],v[84:87],a[16:19]
buffer_load_dwordx4 v229, s[8:11], s64 offen offset:1024 lds
MM a[24:27],a[132:135],v[92:95],a[24:27]
RD a[204:207],v227 offset:16384
RD a[208:211],v224 offset:20480
MM a[24:27],a[128:131],v[88:91],a[24:27]
EX v35,v35
MM a[28:31],a[136:139],v[88:91],a[28:31]
RD a[212:215],v225 offset:20480
s_nop 0
LPG8_%=:
s_cbranch_execz LPG12_%=
CV v5,v34,v35
MM a[28:31],a[140:143],v[92:95],a[28:31]
EX v36,v36
MM a[20:23],a[140:143],v[84:87],a[20:23]
buffer_load_dwordx4 v230, s[8:11], s64 offen offset:2048 lds
MM a[20:23],a[136:139],v[80:83],a[20:23]
EX v37,v37
MM a[12:15],a[136:139],v[72:75],a[12:15]
RD a[216:219],v226 offset:20480
CV v6,v36,v37
MM a[12:15],a[140:143],v[76:79],a[12:15]
EX v38,v38
MM a[4:7],a[140:143],v[68:71],a[4:7]
EX v39,v39
MM a[4:7],a[136:139],v[64:67],a[4:7]
RD a[220:223],v227 offset:20480
CV v7,v38,v39
MM a[32:35],a[144:147],v[64:67],a[32:35]
EX v40,v40
MM a[32:35],a[148:151],v[68:71],a[32:35]
buffer_load_dwordx4 v231, s[8:11], s64 offen offset:3072 lds
MM a[40:43],a[148:151],v[76:79],a[40:43]
s_add_u32 m0, s65, 114688
EX v41,v41
MM a[40:43],a[144:147],v[72:75],a[40:43]
RD a[224:227],v224 offset:24576
CV v12,v40,v41
MM a[48:51],a[144:147],v[80:83],a[48:51]
EX v42,v42
MM a[48:51],a[148:151],v[84:87],a[48:51]
EX v43,v43
MM a[56:59],a[148:151],v[92:95],a[56:59]
RD a[228:231],v225 offset:24576
CV v13,v42,v43
MM a[56:59],a[144:147],v[88:91],a[56:59]
EX v44,v44
MM a[60:63],a[152:155],v[88:91],a[60:63]
buffer_load_dwordx4 v235, s[12:15], s81 offen offset:0 lds
MM a[60:63],a[156:159],v[92:95],a[60:63]
EX v45,v45
MM a[52:55],a[156:159],v[84:87],a[52:55]
RD a[232:235],v226 offset:24576
CV v14,v44,v45
MM a[52:55],a[152:155],v[80:83],a[52:55]
EX v46,v46
MM a[44:47],a[152:155],v[72:75],a[44:47]
EX v47,v47
MM a[44:47],a[156:159],v[76:79],a[44:47]
RD a[236:239],v227 offset:24576
CV v15,v46,v47
MM a[36:39],a[156:159],v[68:71],a[36:39]
buffer_load_dwordx4 v235, s[12:15], s64 offen offset:1024 lds
MM a[36:39],a[152:155],v[64:67],a[36:39]
EX v48,v48
MM a[64:67],a[160:163],v[64:67],a[64:67]
EX v49,v49
MM a[64:67],a[164:167],v[68:71],a[64:67]
RD a[240:243],v224 offset:28672
CV v20,v48,v49
MM a[72:75],a[164:167],v[76:79],a[72:75]
EX v50,v50
MM a[72:75],a[160:163],v[72:75],a[72:75]
RD a[244:247],v225 offset:28672
RD a[248:251],v226 offset:28672
MM a[80:83],a[160:163],v[80:83],a[80:83]
EX v51,v51
MM a[80:83],a[164:167],v[84:87],a[80:83]
RD a[252:255],v227 offset:28672
CV v21,v50,v51
MM a[88:91],a[164:167],v[92:95],a[88:91]
EX v52,v52
MM a[88:91],a[160:163],v[88:91],a[88:91]
buffer_load_dwordx4 v235, s[12:15], s81 offen offset:2048 lds
MM a[92:95],a[168:171],v[88:91],a[92:95]
EX v53,v53
MM a[92:95],a[172:175],v[92:95],a[92:95]
EX v54,v54
MM a[84:87],a[172:175],v[84:87],a[84:87]
buffer_load_dwordx4 v235, s[12:15], s64 offen offset:3072 lds
MM a[84:87],a[168:171],v[80:83],a[84:87]
EX v55,v55
MM a[76:79],a[168:171],v[72:75],a[76:79]
EX v56,v56
MM a[76:79],a[172:175],v[76:79],a[76:79]
EX v57,v57
MM a[68:71],a[172:175],v[68:71],a[68:71]
EX v58,v58
MM a[68:71],a[168:171],v[64:67],a[68:71]
EX v59,v59
MM a[96:99],a[176:179],v[64:67],a[96:99]
EX v60,v60
MM a[96:99],a[180:183],v[68:71],a[96:99]
EX v61,v61
MM a[104:107],a[180:183],v[76:79],a[104:107]
EX v62,v62
MM a[104:107],a[176:179],v[72:75],a[104:107]
EX v63,v63
MM a[112:115],a[176:179],v[80:83],a[112:115]
CV v22,v52,v53
CV v23,v54,v55
MM a[112:115],a[180:183],v[84:87],a[112:115]
CV v28,v56,v57
CV v29,v58,v59
MM a[120:123],a[180:183],v[92:95],a[120:123]
CV v30,v60,v61
CV v31,v62,v63
MM a[120:123],a[176:179],v[88:91],a[120:123]
MM a[124:127],a[184:187],v[88:91],a[124:127]
MM a[124:127],a[188:191],v[92:95],a[124:127]
MM a[116:119],a[188:191],v[84:87],a[116:119]
MM a[116:119],a[184:187],v[80:83],a[116:119]
MM a[108:111],a[184:187],v[72:75],a[108:111]
MM a[108:111],a[188:191],v[76:79],a[108:111]
MM a[100:103],a[188:191],v[68:71],a[100:103]
MM a[100:103],a[184:187],v[64:67],a[100:103]
v_smfmac_f32_16x16x64_bf16 v[144:147], v[236:239], v[0:7], v234
v_smfmac_f32_16x16x64_bf16 v[148:151], v[236:239], v[8:15], v234
v_smfmac_f32_16x16x64_bf16 v[152:155], v[236:239], v[16:23], v234
v_smfmac_f32_16x16x64_bf16 v[156:159], v[236:239], v[24:31], v234
s_waitcnt lgkmcnt(0)
MM v[64:67],a[192:195],v[160:163],v[128:131]
s_add_u32 s64, s64, 16384
TR a[128:129],v232 offset:0
s_and_b32 s64, s64, 524287
MM v[64:67],a[196:199],v[176:179],v[64:67]
s_add_u32 s81, s64, 896
TR a[130:131],v232 offset:4096
TR a[132:133],v232 offset:8192
MM v[64:67],a[200:203],v[192:195],v[64:67]
v_max3_f32 v249, v144, v145, v148
v_max3_f32 v249, v249, v149, v152
MM v[64:67],a[204:207],v[208:211],v[64:67]
v_max3_f32 v249, v249, v153, v156
v_max_f32 v249, v249, v157
MM v[68:71],a[220:223],v[208:211],v[128:131]
TR a[134:135],v232 offset:12288
v_cmp_lt_f32 vcc, s71, v249
MM v[68:71],a[216:219],v[192:195],v[68:71]
s_cbranch_vccnz LSITE8_%=
LBACK8_%=:
TR a[136:137],v233 offset:0
v_add_f32 v244, v144, v145
MM v[68:71],a[212:215],v[176:179],v[68:71]
v_add_f32 v245, v148, v149
v_add_f32 v246, v152, v153
MM v[68:71],a[208:211],v[160:163],v[68:71]
TR a[138:139],v233 offset:4096
v_add_f32 v247, v156, v157
MM v[76:79],a[208:211],v[164:167],v[132:135]
EX v64,v64
MM v[76:79],a[212:215],v[180:183],v[76:79]
TR a[140:141],v233 offset:8192
TR a[142:143],v233 offset:12288
MM v[76:79],a[216:219],v[196:199],v[76:79]
EX v65,v65
MM v[76:79],a[220:223],v[212:215],v[76:79]
TR a[144:145],v232 offset:512
CV v64,v64,v65
MM v[72:75],a[204:207],v[212:215],v[132:135]
EX v66,v66
MM v[72:75],a[200:203],v[196:199],v[72:75]
EX v67,v67
MM v[72:75],a[196:199],v[180:183],v[72:75]
TR a[146:147],v232 offset:4608
CV v65,v66,v67
MM v[72:75],a[192:195],v[164:167],v[72:75]
EX v68,v68
MM v[80:83],a[192:195],v[168:171],v[136:139]
TR a[148:149],v232 offset:8704
TR a[150:151],v232 offset:12800
MM v[80:83],a[196:199],v[184:187],v[80:83]
EX v69,v69
MM v[80:83],a[200:203],v[200:203],v[80:83]
TR a[152:153],v233 offset:512
CV v66,v68,v69
MM v[80:83],a[204:207],v[216:219],v[80:83]
EX v70,v70
MM v[84:87],a[220:223],v[216:219],v[136:139]
s_cmp_lg_u32 s70, 2
s_cbranch_scc1 LWV_%=
v_lshlrev_b32 v32, 3, v248
)ASM"
R"ASM(
v_min_u32 v32, 4992, v32
s_and_b32 s90, 1, %[sb]
s_lshl_b32 s90, s90, 3
s_add_u32 s90, s90, s50
s_lshl_b32 s90, s90, 2
s_add_u32 s90, s90, s84
s_lshl_b32 s90, s90, 2
s_add_u32 s90, s90, %[swave]
s_mul_i32 s90, s90, 5120
s_mul_i32 s91, s84, 320
s_sub_u32 s90, s90, s91
s_add_u32 s91, %[slds], 135168
s_mov_b32 m0, s91
s_nop 0
buffer_load_dword v32, s[24:27], s90 offen offset:0 lds
LWV_%=:
MM v[84:87],a[216:219],v[200:203],v[84:87]
EX v71,v71
MM v[84:87],a[212:215],v[184:187],v[84:87]
TR a[154:155],v233 offset:4608
CV v67,v70,v71
MM v[84:87],a[208:211],v[168:171],v[84:87]
EX v72,v72
MM v[92:95],a[208:211],v[172:175],v[140:143]
TR a[156:157],v233 offset:8704
TR a[158:159],v233 offset:12800
MM v[92:95],a[212:215],v[188:191],v[92:95]
EX v73,v73
MM v[92:95],a[216:219],v[204:207],v[92:95]
TR a[160:161],v232 offset:1024
CV v72,v72,v73
MM v[92:95],a[220:223],v[220:223],v[92:95]
EX v74,v74
MM v[88:91],a[204:207],v[220:223],v[140:143]
EX v75,v75
MM v[88:91],a[200:203],v[204:207],v[88:91]
TR a[162:163],v232 offset:5120
CV v73,v74,v75
MM v[88:91],a[196:199],v[188:191],v[88:91]
EX v76,v76
MM v[88:91],a[192:195],v[172:175],v[88:91]
TR a[164:165],v232 offset:9216
TR a[166:167],v232 offset:13312
MM v[96:99],a[224:227],v[160:163],v[128:131]
EX v77,v77
MM v[96:99],a[228:231],v[176:179],v[96:99]
TR a[168:169],v233 offset:1024
CV v74,v76,v77
MM v[96:99],a[232:235],v[192:195],v[96:99]
EX v78,v78
MM v[96:99],a[236:239],v[208:211],v[96:99]
EX v79,v79
MM v[100:103],a[252:255],v[208:211],v[128:131]
TR a[170:171],v233 offset:5120
CV v75,v78,v79
MM v[100:103],a[248:251],v[192:195],v[100:103]
EX v80,v80
MM v[100:103],a[244:247],v[176:179],v[100:103]
TR a[172:173],v233 offset:9216
TR a[174:175],v233 offset:13312
MM v[100:103],a[240:243],v[160:163],v[100:103]
EX v81,v81
MM v[108:111],a[240:243],v[164:167],v[132:135]
TR a[176:177],v232 offset:1536
CV v80,v80,v81
MM v[108:111],a[244:247],v[180:183],v[108:111]
EX v82,v82
MM v[108:111],a[248:251],v[196:199],v[108:111]
TR a[178:179],v232 offset:5632
TR a[180:181],v232 offset:9728
MM v[108:111],a[252:255],v[212:215],v[108:111]
EX v83,v83
MM v[104:107],a[236:239],v[212:215],v[132:135]
TR a[182:183],v232 offset:13824
CV v81,v82,v83
MM v[104:107],a[232:235],v[196:199],v[104:107]
EX v84,v84
MM v[104:107],a[228:231],v[180:183],v[104:107]
EX v85,v85
MM v[104:107],a[224:227],v[164:167],v[104:107]
TR a[184:185],v233 offset:1536
CV v82,v84,v85
MM v[112:115],a[224:227],v[168:171],v[136:139]
EX v86,v86
MM v[112:115],a[228:231],v[184:187],v[112:115]
TR a[186:187],v233 offset:5632
TR a[188:189],v233 offset:9728
MM v[112:115],a[232:235],v[200:203],v[112:115]
EX v87,v87
MM v[112:115],a[236:239],v[216:219],v[112:115]
TR a[190:191],v233 offset:13824
CV v83,v86,v87
MM v[116:119],a[252:255],v[216:219],v[136:139]
EX v88,v88
MM v[116:119],a[248:251],v[200:203],v[116:119]
EX v89,v89
MM v[116:119],a[244:247],v[184:187],v[116:119]
EX v90,v90
MM v[116:119],a[240:243],v[168:171],v[116:119]
EX v91,v91
MM v[124:127],a[240:243],v[172:175],v[140:143]
EX v92,v92
MM v[124:127],a[244:247],v[188:191],v[124:127]
EX v93,v93
MM v[124:127],a[248:251],v[204:207],v[124:127]
EX v94,v94
MM v[124:127],a[252:255],v[220:223],v[124:127]
EX v95,v95
MM v[120:123],a[236:239],v[220:223],v[140:143]
EX v96,v96
MM v[120:123],a[232:235],v[204:207],v[120:123]
EX v97,v97
MM v[120:123],a[228:231],v[188:191],v[120:123]
s_cmp_lg_u32 s70, 1
s_cbranch_scc1 LTP_%=
s_getpc_b64 s[98:99]
LTA1_%=:
s_min_u32 s73, %[swave], 3
s_lshl_b32 s73, s73, 12
s_add_u32 s73, s73, LPG22_%=-LTA1_%=
s_add_u32 s98, s98, s73
s_addc_u32 s99, s99, 0
s_mov_b64 exec, 0
s_setpc_b64 s[98:99]
LTR1_%=:
s_mov_b64 exec, -1
LTP_%=:
MM v[120:123],a[224:227],v[172:175],v[120:123]
s_waitcnt vmcnt(12) lgkmcnt(0)
s_barrier
MM a[0:3],a[128:131],v[0:3],a[0:3]
s_add_u32 m0, s65, 0
CV v88,v88,v89
CV v89,v90,v91
MM a[0:3],a[132:135],v[4:7],a[0:3]
RD a[192:195],v224 offset:32768
CV v90,v92,v93
MM a[8:11],a[132:135],v[12:15],a[8:11]
RD a[196:199],v225 offset:32768
CV v91,v94,v95
MM a[8:11],a[128:131],v[8:11],a[8:11]
buffer_load_dwordx4 v228, s[8:11], s64 offen offset:0 lds
MM a[16:19],a[128:131],v[16:19],a[16:19]
RD a[200:203],v226 offset:32768
CV v68,v96,v97
MM a[16:19],a[132:135],v[20:23],a[16:19]
RD a[204:207],v227 offset:32768
RD a[208:211],v224 offset:36864
MM a[24:27],a[132:135],v[28:31],a[24:27]
EX v98,v98
MM a[24:27],a[128:131],v[24:27],a[24:27]
buffer_load_dwordx4 v229, s[8:11], s64 offen offset:1024 lds
MM a[28:31],a[136:139],v[24:27],a[28:31]
EX v99,v99
MM a[28:31],a[140:143],v[28:31],a[28:31]
RD a[212:215],v225 offset:36864
CV v69,v98,v99
MM a[20:23],a[140:143],v[20:23],a[20:23]
EX v100,v100
MM a[20:23],a[136:139],v[16:19],a[20:23]
EX v101,v101
MM a[12:15],a[136:139],v[8:11],a[12:15]
RD a[216:219],v226 offset:36864
CV v70,v100,v101
MM a[12:15],a[140:143],v[12:15],a[12:15]
buffer_load_dwordx4 v230, s[8:11], s64 offen offset:2048 lds
MM a[4:7],a[140:143],v[4:7],a[4:7]
EX v102,v102
MM a[4:7],a[136:139],v[0:3],a[4:7]
EX v103,v103
MM a[32:35],a[144:147],v[0:3],a[32:35]
RD a[220:223],v227 offset:36864
CV v71,v102,v103
MM a[32:35],a[148:151],v[4:7],a[32:35]
EX v104,v104
MM a[40:43],a[148:151],v[12:15],a[40:43]
EX v105,v105
MM a[40:43],a[144:147],v[8:11],a[40:43]
RD a[224:227],v224 offset:40960
CV v76,v104,v105
MM a[48:51],a[144:147],v[16:19],a[48:51]
buffer_load_dwordx4 v231, s[8:11], s64 offen offset:3072 lds
MM a[48:51],a[148:151],v[20:23],a[48:51]
s_add_u32 m0, s65, 65536
EX v106,v106
MM a[56:59],a[148:151],v[28:31],a[56:59]
EX v107,v107
MM a[56:59],a[144:147],v[24:27],a[56:59]
RD a[228:231],v225 offset:40960
CV v77,v106,v107
MM a[60:63],a[152:155],v[24:27],a[60:63]
EX v108,v108
MM a[60:63],a[156:159],v[28:31],a[60:63]
buffer_load_dwordx4 v235, s[12:15], s81 offen offset:0 lds
MM a[52:55],a[156:159],v[20:23],a[52:55]
EX v109,v109
MM a[52:55],a[152:155],v[16:19],a[52:55]
RD a[232:235],v226 offset:40960
CV v78,v108,v109
MM a[44:47],a[152:155],v[8:11],a[44:47]
EX v110,v110
MM a[44:47],a[156:159],v[12:15],a[44:47]
EX v111,v111
MM a[36:39],a[156:159],v[4:7],a[36:39]
RD a[236:239],v227 offset:40960
CV v79,v110,v111
MM a[36:39],a[152:155],v[0:3],a[36:39]
EX v112,v112
MM a[64:67],a[160:163],v[0:3],a[64:67]
buffer_load_dwordx4 v235, s[12:15], s64 offen offset:1024 lds
MM a[64:67],a[164:167],v[4:7],a[64:67]
EX v113,v113
MM a[72:75],a[164:167],v[12:15],a[72:75]
RD a[240:243],v224 offset:45056
CV v84,v112,v113
MM a[72:75],a[160:163],v[8:11],a[72:75]
EX v114,v114
MM a[80:83],a[160:163],v[16:19],a[80:83]
EX v115,v115
MM a[80:83],a[164:167],v[20:23],a[80:83]
RD a[244:247],v225 offset:45056
CV v85,v114,v115
MM a[88:91],a[164:167],v[28:31],a[88:91]
EX v116,v116
MM a[88:91],a[160:163],v[24:27],a[88:91]
buffer_load_dwordx4 v235, s[12:15], s81 offen offset:2048 lds
MM a[92:95],a[168:171],v[24:27],a[92:95]
EX v117,v117
MM a[92:95],a[172:175],v[28:31],a[92:95]
RD a[248:251],v226 offset:45056
CV v86,v116,v117
MM a[84:87],a[172:175],v[20:23],a[84:87]
EX v118,v118
MM a[84:87],a[168:171],v[16:19],a[84:87]
EX v119,v119
MM a[76:79],a[168:171],v[8:11],a[76:79]
RD a[252:255],v227 offset:45056
CV v87,v118,v119
MM a[76:79],a[172:175],v[12:15],a[76:79]
EX v120,v120
MM a[68:71],a[172:175],v[4:7],a[68:71]
buffer_load_dwordx4 v235, s[12:15], s64 offen offset:3072 lds
MM a[68:71],a[168:171],v[0:3],a[68:71]
EX v121,v121
MM a[96:99],a[176:179],v[0:3],a[96:99]
EX v122,v122
MM a[96:99],a[180:183],v[4:7],a[96:99]
EX v123,v123
MM a[104:107],a[180:183],v[12:15],a[104:107]
EX v124,v124
MM a[104:107],a[176:179],v[8:11],a[104:107]
EX v125,v125
MM a[112:115],a[176:179],v[16:19],a[112:115]
EX v126,v126
MM a[112:115],a[180:183],v[20:23],a[112:115]
EX v127,v127
MM a[120:123],a[180:183],v[28:31],a[120:123]
CV v92,v120,v121
CV v93,v122,v123
MM a[120:123],a[176:179],v[24:27],a[120:123]
CV v94,v124,v125
CV v95,v126,v127
MM a[124:127],a[184:187],v[24:27],a[124:127]
MM a[124:127],a[188:191],v[28:31],a[124:127]
MM a[116:119],a[188:191],v[20:23],a[116:119]
MM a[116:119],a[184:187],v[16:19],a[116:119]
MM a[108:111],a[184:187],v[8:11],a[108:111]
MM a[108:111],a[188:191],v[12:15],a[108:111]
MM a[100:103],a[188:191],v[4:7],a[100:103]
MM a[100:103],a[184:187],v[0:3],a[100:103]
v_smfmac_f32_16x16x64_bf16 v[144:147], v[236:239], v[64:71], v234
v_smfmac_f32_16x16x64_bf16 v[148:151], v[236:239], v[72:79], v234
v_smfmac_f32_16x16x64_bf16 v[152:155], v[236:239], v[80:87], v234
v_smfmac_f32_16x16x64_bf16 v[156:159], v[236:239], v[88:95], v234
s_waitcnt lgkmcnt(0)
s_sub_u32 s70, s70, 1
s_cmp_lg_u32 s70, 0
s_cbranch_scc1 LLAP_%=
s_branch LLAJ_%=
LLAR_%=:
MM v[0:3],a[192:195],v[160:163],v[128:131]
s_add_u32 s64, s64, 16384
TR a[128:129],v232 offset:16384
s_add_u32 s81, s64, 896
MM v[0:3],a[196:199],v[176:179],v[0:3]
TR a[130:131],v232 offset:20480
TR a[132:133],v232 offset:24576
MM v[0:3],a[200:203],v[192:195],v[0:3]
v_max3_f32 v249, v144, v145, v148
v_max3_f32 v249, v249, v149, v152
MM v[0:3],a[204:207],v[208:211],v[0:3]
v_max3_f32 v249, v249, v153, v156
v_max_f32 v249, v249, v157
MM v[4:7],a[220:223],v[208:211],v[128:131]
TR a[134:135],v232 offset:28672
v_cmp_lt_f32 vcc, s71, v249
MM v[4:7],a[216:219],v[192:195],v[4:7]
s_cbranch_vccnz LSITE9_%=
LBACK9_%=:
TR a[136:137],v233 offset:16384
v_add_f32 v244, v144, v145
MM v[4:7],a[212:215],v[176:179],v[4:7]
s_lshl_b32 s17, s16, 2
v_add_f32 v245, v148, v149
v_add_f32 v246, v152, v153
MM v[4:7],a[208:211],v[160:163],v[4:7]
s_add_u32 s17, s17, %[swave]
TR a[138:139],v233 offset:20480
v_add_f32 v247, v156, v157
MM v[12:15],a[208:211],v[164:167],v[132:135]
s_sub_u32 s90, s16, 108
EX v0,v0
MM v[12:15],a[212:215],v[180:183],v[12:15]
s_min_u32 s90, s90, s16
TR a[140:141],v233 offset:24576
TR a[142:143],v233 offset:28672
MM v[12:15],a[216:219],v[196:199],v[12:15]
s_lshl_b32 s48, s90, 5
EX v1,v1
MM v[12:15],a[220:223],v[212:215],v[12:15]
s_add_u32 s76, %[slds], 155648
v_add_u32 v250, s76, v248
CV v0,v0,v1
MM v[8:11],a[204:207],v[212:215],v[132:135]
s_lshl_b32 s77, s17, 4
EX v2,v2
MM v[8:11],a[200:203],v[196:199],v[8:11]
s_add_u32 s76, s76, s77
TR a[144:145],v232 offset:16896
v_mov_b32 v251, s76
MM v[8:11],a[196:199],v[180:183],v[8:11]
RD v[124:127],v251 offset:0
RD v[96:99],v250 offset:0
MM v[8:11],a[192:195],v[164:167],v[8:11]
RD v[100:103],v250 offset:1024
RD v[104:107],v250 offset:2048
MM v[16:19],a[192:195],v[168:171],v[136:139]
RD v[108:111],v250 offset:3072
RD v[112:115],v250 offset:4096
MM v[16:19],a[196:199],v[184:187],v[16:19]
RD v[116:119],v250 offset:5120
RD v[120:123],v250 offset:6144
MM v[16:19],a[200:203],v[200:203],v[16:19]
EX v3,v3
MM v[16:19],a[204:207],v[216:219],v[16:19]
EX v4,v4
MM v[20:23],a[220:223],v[216:219],v[136:139]
s_waitcnt lgkmcnt(7)
v_readfirstlane_b32 s52, v124
v_readfirstlane_b32 s53, v125
MM v[20:23],a[216:219],v[200:203],v[20:23]
v_readfirstlane_b32 s54, v126
v_readfirstlane_b32 s55, v127
EX v5,v5
MM v[20:23],a[212:215],v[184:187],v[20:23]
RD v[124:127],v250 offset:7168
TR a[146:147],v232 offset:20992
MM v[20:23],a[208:211],v[168:171],v[20:23]
s_waitcnt lgkmcnt(2)
v_max3_u32 v253, v96, v97, v98
v_max3_u32 v251, v99, v100, v101
EX v6,v6
MM v[28:31],a[208:211],v[172:175],v[140:143]
v_max3_u32 v253, v253, v102, v103
v_max3_u32 v251, v251, v104, v105
MM v[28:31],a[212:215],v[188:191],v[28:31]
TR a[148:149],v232 offset:25088
v_max3_u32 v253, v253, v106, v107
v_max3_u32 v251, v251, v108, v109
EX v7,v7
MM v[28:31],a[216:219],v[204:207],v[28:31]
v_max3_u32 v253, v253, v110, v111
v_max3_u32 v251, v251, v112, v113
MM v[28:31],a[220:223],v[220:223],v[28:31]
TR a[150:151],v232 offset:29184
v_max3_u32 v253, v253, v114, v115
v_max3_u32 v251, v251, v116, v117
EX v8,v8
MM v[24:27],a[204:207],v[220:223],v[140:143]
v_max3_u32 v253, v253, v118, v119
v_max3_u32 v251, v251, v120, v121
MM v[24:27],a[200:203],v[204:207],v[24:27]
TR a[152:153],v233 offset:16896
v_max3_u32 v253, v253, v122, v123
EX v9,v9
CV v1,v2,v3
MM v[24:27],a[196:199],v[188:191],v[24:27]
s_waitcnt lgkmcnt(4)
TR a[154:155],v233 offset:20992
v_max3_u32 v251, v251, v124, v125
v_max3_u32 v253, v253, v126, v127
MM v[24:27],a[192:195],v[172:175],v[24:27]
v_max_u32 v253, v253, v251
v_cmp_lt_u32 s[60:61], s52, v253
EX v10,v10
)ASM"
R"ASM(
MM v[32:35],a[224:227],v[160:163],v[128:131]
TR a[156:157],v233 offset:25088
v_cmp_lt_u32 s[62:63], s53, v253
v_cmp_lt_u32 s[56:57], s54, v253
EX v11,v11
CV v2,v4,v5
MM v[32:35],a[228:231],v[176:179],v[32:35]
TR a[158:159],v233 offset:29184
v_cmp_lt_u32 s[58:59], s55, v253
MM v[32:35],a[232:235],v[192:195],v[32:35]
EX v12,v12
MM v[32:35],a[236:239],v[208:211],v[32:35]
s_bcnt1_i32_b64 s60, s[60:61]
TR a[160:161],v232 offset:17408
CV v3,v6,v7
MM v[36:39],a[252:255],v[208:211],v[128:131]
s_bcnt1_i32_b64 s62, s[62:63]
EX v13,v13
MM v[36:39],a[248:251],v[192:195],v[36:39]
s_bcnt1_i32_b64 s56, s[56:57]
TR a[162:163],v232 offset:21504
CV v8,v8,v9
MM v[36:39],a[244:247],v[176:179],v[36:39]
s_bcnt1_i32_b64 s58, s[58:59]
EX v14,v14
MM v[36:39],a[240:243],v[160:163],v[36:39]
s_min_u32 s76, s60, s62
TR a[164:165],v232 offset:25600
TR a[166:167],v232 offset:29696
s_nop 0
LPG9_%=:
s_cbranch_execz LTR0_%=
MM v[44:47],a[240:243],v[164:167],v[132:135]
s_min_u32 s77, s56, s58
EX v15,v15
MM v[44:47],a[244:247],v[180:183],v[44:47]
s_min_u32 s76, s76, s77
s_cmp_lt_u32 s76, 8
s_cbranch_scc1 LRK_%=
LRKb_%=:
MM v[44:47],a[248:251],v[196:199],v[44:47]
v_lshrrev_b32 v252, 6, v248
v_mul_u32_u24 v252, 3456, v252
EX v16,v16
MM v[44:47],a[252:255],v[212:215],v[44:47]
TR a[168:169],v233 offset:17408
v_and_b32 v251, 48, v248
v_add_u32 v252, v252, v251
EX v17,v17
MM v[40:43],a[236:239],v[212:215],v[132:135]
s_and_b32 s90, %[swave], 1
TR a[170:171],v233 offset:21504
CV v9,v10,v11
MM v[40:43],a[232:235],v[196:199],v[40:43]
s_mul_i32 s90, s90, 276480
EX v18,v18
MM v[40:43],a[228:231],v[180:183],v[40:43]
s_bfe_u32 s91, s48, 131077
TR a[172:173],v233 offset:25600
CV v10,v12,v13
MM v[40:43],a[224:227],v[164:167],v[40:43]
s_mul_i32 s91, s91, 552960
EX v19,v19
MM v[48:51],a[224:227],v[168:171],v[136:139]
s_add_u32 s90, s90, s91
TR a[174:175],v233 offset:29696
CV v11,v14,v15
MM v[48:51],a[228:231],v[184:187],v[48:51]
s_and_b32 s91, s48, 0xffffff80
EX v20,v20
MM v[48:51],a[232:235],v[200:203],v[48:51]
s_add_u32 s90, s90, s91
TR a[176:177],v232 offset:17920
TR a[178:179],v232 offset:22016
MM v[48:51],a[236:239],v[216:219],v[48:51]
s_and_b32 s91, s48, 16
EX v21,v21
MM v[52:55],a[252:255],v[216:219],v[136:139]
s_lshl_b32 s91, s91, 2
EX v22,v22
MM v[52:55],a[248:251],v[200:203],v[52:55]
s_add_u32 s66, s90, s91
TR a[180:181],v232 offset:26112
CV v16,v16,v17
MM v[52:55],a[244:247],v[184:187],v[52:55]
s_mul_i32 s91, %[swave], 5120
EX v23,v23
MM v[52:55],a[240:243],v[168:171],v[52:55]
s_add_u32 s91, s91, %[slds]
TR a[182:183],v232 offset:30208
CV v17,v18,v19
MM v[60:63],a[240:243],v[172:175],v[140:143]
s_add_u32 s67, s91, 131072
EX v24,v24
MM v[60:63],a[244:247],v[188:191],v[60:63]
TR a[184:185],v233 offset:17920
TR a[186:187],v233 offset:22016
MM v[60:63],a[248:251],v[204:207],v[60:63]
TR a[188:189],v233 offset:26112
TR a[190:191],v233 offset:30208
MM v[60:63],a[252:255],v[220:223],v[60:63]
EX v25,v25
MM v[56:59],a[236:239],v[220:223],v[140:143]
EX v26,v26
MM v[56:59],a[232:235],v[204:207],v[56:59]
EX v27,v27
MM v[56:59],a[228:231],v[188:191],v[56:59]
EX v28,v28
MM v[56:59],a[224:227],v[172:175],v[56:59]
s_waitcnt lgkmcnt(0)
s_barrier
MM a[0:3],a[128:131],v[64:67],a[0:3]
s_add_u32 m0, s65, 16384
CV v18,v20,v21
CV v19,v22,v23
MM a[0:3],a[132:135],v[68:71],a[0:3]
EX v29,v29
MM a[8:11],a[132:135],v[76:79],a[8:11]
CV v24,v24,v25
CV v25,v26,v27
MM a[8:11],a[128:131],v[72:75],a[8:11]
EX v30,v30
MM a[16:19],a[128:131],v[80:83],a[16:19]
EX v31,v31
MM a[16:19],a[132:135],v[84:87],a[16:19]
RD a[192:195],v224 offset:49152
CV v26,v28,v29
MM a[24:27],a[132:135],v[92:95],a[24:27]
EX v32,v32
MM a[24:27],a[128:131],v[88:91],a[24:27]
EX v33,v33
MM a[28:31],a[136:139],v[88:91],a[28:31]
RD a[196:199],v225 offset:49152
CV v27,v30,v31
MM a[28:31],a[140:143],v[92:95],a[28:31]
EX v34,v34
MM a[20:23],a[140:143],v[84:87],a[20:23]
buffer_load_dwordx4 v228, s[8:11], s64 offen offset:0 lds
MM a[20:23],a[136:139],v[80:83],a[20:23]
EX v35,v35
MM a[12:15],a[136:139],v[72:75],a[12:15]
RD a[200:203],v226 offset:49152
CV v4,v32,v33
MM a[12:15],a[140:143],v[76:79],a[12:15]
EX v36,v36
MM a[4:7],a[140:143],v[68:71],a[4:7]
EX v37,v37
MM a[4:7],a[136:139],v[64:67],a[4:7]
RD a[204:207],v227 offset:49152
CV v5,v34,v35
MM a[32:35],a[144:147],v[64:67],a[32:35]
EX v38,v38
MM a[32:35],a[148:151],v[68:71],a[32:35]
buffer_load_dwordx4 v229, s[8:11], s64 offen offset:1024 lds
MM a[40:43],a[148:151],v[76:79],a[40:43]
EX v39,v39
MM a[40:43],a[144:147],v[72:75],a[40:43]
RD a[208:211],v224 offset:53248
CV v6,v36,v37
MM a[48:51],a[144:147],v[80:83],a[48:51]
EX v40,v40
MM a[48:51],a[148:151],v[84:87],a[48:51]
RD a[212:215],v225 offset:53248
CV v7,v38,v39
MM a[56:59],a[148:151],v[92:95],a[56:59]
EX v41,v41
MM a[56:59],a[144:147],v[88:91],a[56:59]
buffer_load_dwordx4 v230, s[8:11], s64 offen offset:2048 lds
MM a[60:63],a[152:155],v[88:91],a[60:63]
EX v42,v42
MM a[60:63],a[156:159],v[92:95],a[60:63]
EX v43,v43
MM a[52:55],a[156:159],v[84:87],a[52:55]
RD a[216:219],v226 offset:53248
CV v12,v40,v41
MM a[52:55],a[152:155],v[80:83],a[52:55]
EX v44,v44
MM a[44:47],a[152:155],v[72:75],a[44:47]
RD a[220:223],v227 offset:53248
CV v13,v42,v43
MM a[44:47],a[156:159],v[76:79],a[44:47]
EX v45,v45
MM a[36:39],a[156:159],v[68:71],a[36:39]
buffer_load_dwordx4 v231, s[8:11], s64 offen offset:3072 lds
MM a[36:39],a[152:155],v[64:67],a[36:39]
s_add_u32 m0, s65, 81920
EX v46,v46
MM a[64:67],a[160:163],v[64:67],a[64:67]
RD a[224:227],v224 offset:57344
CV v14,v44,v45
MM a[64:67],a[164:167],v[68:71],a[64:67]
EX v47,v47
MM a[72:75],a[164:167],v[76:79],a[72:75]
EX v48,v48
MM a[72:75],a[160:163],v[72:75],a[72:75]
RD a[228:231],v225 offset:57344
CV v15,v46,v47
MM a[80:83],a[160:163],v[80:83],a[80:83]
s_add_u32 m0, s67, 0
s_add_u32 s85, s66, 0
buffer_load_dwordx4 v252, s[36:39], s85 offen offset:0 lds
MM a[80:83],a[164:167],v[84:87],a[80:83]
EX v49,v49
MM a[88:91],a[164:167],v[92:95],a[88:91]
EX v50,v50
MM a[88:91],a[160:163],v[88:91],a[88:91]
s_add_u32 m0, s65, 81920
s_nop 0
buffer_load_dwordx4 v235, s[12:15], s81 offen offset:0 lds
MM a[92:95],a[168:171],v[88:91],a[92:95]
EX v51,v51
MM a[92:95],a[172:175],v[92:95],a[92:95]
CV v20,v48,v49
CV v21,v50,v51
MM a[84:87],a[172:175],v[84:87],a[84:87]
EX v52,v52
MM a[84:87],a[168:171],v[80:83],a[84:87]
RD a[232:235],v226 offset:57344
RD a[236:239],v227 offset:57344
MM a[76:79],a[168:171],v[72:75],a[76:79]
EX v53,v53
MM a[76:79],a[172:175],v[76:79],a[76:79]
RD a[240:243],v224 offset:61440
CV v22,v52,v53
MM a[68:71],a[172:175],v[68:71],a[68:71]
EX v54,v54
MM a[68:71],a[168:171],v[64:67],a[68:71]
buffer_load_dwordx4 v235, s[12:15], s64 offen offset:1024 lds
MM a[96:99],a[176:179],v[64:67],a[96:99]
EX v55,v55
MM a[96:99],a[180:183],v[68:71],a[96:99]
EX v56,v56
MM a[104:107],a[180:183],v[76:79],a[104:107]
RD a[244:247],v225 offset:61440
CV v23,v54,v55
MM a[104:107],a[176:179],v[72:75],a[104:107]
EX v57,v57
MM a[112:115],a[176:179],v[80:83],a[112:115]
RD a[248:251],v226 offset:61440
CV v28,v56,v57
MM a[112:115],a[180:183],v[84:87],a[112:115]
EX v58,v58
MM a[120:123],a[180:183],v[92:95],a[120:123]
buffer_load_dwordx4 v235, s[12:15], s81 offen offset:2048 lds
MM a[120:123],a[176:179],v[88:91],a[120:123]
EX v59,v59
MM a[124:127],a[184:187],v[88:91],a[124:127]
RD a[252:255],v227 offset:61440
CV v29,v58,v59
MM a[124:127],a[188:191],v[92:95],a[124:127]
EX v60,v60
MM a[116:119],a[188:191],v[84:87],a[116:119]
EX v61,v61
MM a[116:119],a[184:187],v[80:83],a[116:119]
buffer_load_dwordx4 v235, s[12:15], s64 offen offset:3072 lds
MM a[108:111],a[184:187],v[72:75],a[108:111]
EX v62,v62
MM a[108:111],a[188:191],v[76:79],a[108:111]
EX v63,v63
MM a[100:103],a[188:191],v[68:71],a[100:103]
CV v30,v60,v61
CV v31,v62,v63
MM a[100:103],a[184:187],v[64:67],a[100:103]
v_smfmac_f32_16x16x64_bf16 v[144:147], v[236:239], v[0:7], v234
v_smfmac_f32_16x16x64_bf16 v[148:151], v[236:239], v[8:15], v234
v_smfmac_f32_16x16x64_bf16 v[152:155], v[236:239], v[16:23], v234
v_smfmac_f32_16x16x64_bf16 v[156:159], v[236:239], v[24:31], v234
s_waitcnt lgkmcnt(0)
MM v[64:67],a[192:195],v[160:163],v[128:131]
s_add_u32 s64, s64, 16384
TR a[128:129],v232 offset:32768
s_add_u32 s81, s64, 896
MM v[64:67],a[196:199],v[176:179],v[64:67]
TR a[130:131],v232 offset:36864
TR a[132:133],v232 offset:40960
MM v[64:67],a[200:203],v[192:195],v[64:67]
v_max3_f32 v249, v144, v145, v148
v_max3_f32 v249, v249, v149, v152
MM v[64:67],a[204:207],v[208:211],v[64:67]
v_max3_f32 v249, v249, v153, v156
v_max_f32 v249, v249, v157
MM v[68:71],a[220:223],v[208:211],v[128:131]
TR a[134:135],v232 offset:45056
v_cmp_lt_f32 vcc, s71, v249
MM v[68:71],a[216:219],v[192:195],v[68:71]
s_cbranch_vccnz LSITE10_%=
LBACK10_%=:
TR a[136:137],v233 offset:32768
v_add_f32 v244, v144, v145
MM v[68:71],a[212:215],v[176:179],v[68:71]
v_add_f32 v245, v148, v149
v_add_f32 v246, v152, v153
MM v[68:71],a[208:211],v[160:163],v[68:71]
TR a[138:139],v233 offset:36864
v_add_f32 v247, v156, v157
MM v[76:79],a[208:211],v[164:167],v[132:135]
s_add_u32 m0, s67, 1024
s_add_u32 s85, s66, 55296
buffer_load_dwordx4 v252, s[36:39], s85 offen offset:0 lds
MM v[76:79],a[212:215],v[180:183],v[76:79]
EX v64,v64
MM v[76:79],a[216:219],v[196:199],v[76:79]
EX v65,v65
MM v[76:79],a[220:223],v[212:215],v[76:79]
TR a[140:141],v233 offset:40960
CV v64,v64,v65
MM v[72:75],a[204:207],v[212:215],v[132:135]
EX v66,v66
MM v[72:75],a[200:203],v[196:199],v[72:75]
TR a[142:143],v233 offset:45056
TR a[144:145],v232 offset:33280
MM v[72:75],a[196:199],v[180:183],v[72:75]
EX v67,v67
MM v[72:75],a[192:195],v[164:167],v[72:75]
TR a[146:147],v232 offset:37376
CV v65,v66,v67
MM v[80:83],a[192:195],v[168:171],v[136:139]
EX v68,v68
MM v[80:83],a[196:199],v[184:187],v[80:83]
TR a[148:149],v232 offset:41472
TR a[150:151],v232 offset:45568
MM v[80:83],a[200:203],v[200:203],v[80:83]
EX v69,v69
MM v[80:83],a[204:207],v[216:219],v[80:83]
TR a[152:153],v233 offset:33280
CV v66,v68,v69
MM v[84:87],a[220:223],v[216:219],v[136:139]
EX v70,v70
MM v[84:87],a[216:219],v[200:203],v[84:87]
EX v71,v71
MM v[84:87],a[212:215],v[184:187],v[84:87]
TR a[154:155],v233 offset:37376
CV v67,v70,v71
MM v[84:87],a[208:211],v[168:171],v[84:87]
EX v72,v72
MM v[92:95],a[208:211],v[172:175],v[140:143]
TR a[156:157],v233 offset:41472
TR a[158:159],v233 offset:45568
MM v[92:95],a[212:215],v[188:191],v[92:95]
EX v73,v73
MM v[92:95],a[216:219],v[204:207],v[92:95]
TR a[160:161],v232 offset:33792
CV v72,v72,v73
MM v[92:95],a[220:223],v[220:223],v[92:95]
EX v74,v74
MM v[88:91],a[204:207],v[220:223],v[140:143]
EX v75,v75
MM v[88:91],a[200:203],v[204:207],v[88:91]
TR a[162:163],v232 offset:37888
CV v73,v74,v75
MM v[88:91],a[196:199],v[188:191],v[88:91]
EX v76,v76
MM v[88:91],a[192:195],v[172:175],v[88:91]
TR a[164:165],v232 offset:41984
TR a[166:167],v232 offset:46080
MM v[96:99],a[224:227],v[160:163],v[128:131]
EX v77,v77
MM v[96:99],a[228:231],v[176:179],v[96:99]
TR a[168:169],v233 offset:33792
CV v74,v76,v77
MM v[96:99],a[232:235],v[192:195],v[96:99]
EX v78,v78
MM v[96:99],a[236:239],v[208:211],v[96:99]
EX v79,v79
MM v[100:103],a[252:255],v[208:211],v[128:131]
TR a[170:171],v233 offset:37888
CV v75,v78,v79
MM v[100:103],a[248:251],v[192:195],v[100:103]
EX v80,v80
MM v[100:103],a[244:247],v[176:179],v[100:103]
TR a[172:173],v233 offset:41984
TR a[174:175],v233 offset:46080
MM v[100:103],a[240:243],v[160:163],v[100:103]
EX v81,v81
MM v[108:111],a[240:243],v[164:167],v[132:135]
s_add_u32 m0, s67, 2048
s_add_u32 s85, s66, 110592
buffer_load_dwordx4 v252, s[36:39], s85 offen offset:0 lds
MM v[108:111],a[244:247],v[180:183],v[108:111]
TR a[176:177],v232 offset:34304
CV v80,v80,v81
MM v[108:111],a[248:251],v[196:199],v[108:111]
EX v82,v82
MM v[108:111],a[252:255],v[212:215],v[108:111]
EX v83,v83
MM v[104:107],a[236:239],v[212:215],v[132:135]
TR a[178:179],v232 offset:38400
CV v81,v82,v83
MM v[104:107],a[232:235],v[196:199],v[104:107]
EX v84,v84
MM v[104:107],a[228:231],v[180:183],v[104:107]
TR a[180:181],v232 offset:42496
TR a[182:183],v232 offset:46592
MM v[104:107],a[224:227],v[164:167],v[104:107]
EX v85,v85
MM v[112:115],a[224:227],v[168:171],v[136:139]
)ASM"
R"ASM(
TR a[184:185],v233 offset:34304
CV v82,v84,v85
MM v[112:115],a[228:231],v[184:187],v[112:115]
EX v86,v86
MM v[112:115],a[232:235],v[200:203],v[112:115]
EX v87,v87
MM v[112:115],a[236:239],v[216:219],v[112:115]
TR a[186:187],v233 offset:38400
CV v83,v86,v87
MM v[116:119],a[252:255],v[216:219],v[136:139]
EX v88,v88
MM v[116:119],a[248:251],v[200:203],v[116:119]
TR a[188:189],v233 offset:42496
TR a[190:191],v233 offset:46592
MM v[116:119],a[244:247],v[184:187],v[116:119]
EX v89,v89
MM v[116:119],a[240:243],v[168:171],v[116:119]
EX v90,v90
MM v[124:127],a[240:243],v[172:175],v[140:143]
EX v91,v91
MM v[124:127],a[244:247],v[188:191],v[124:127]
EX v92,v92
MM v[124:127],a[248:251],v[204:207],v[124:127]
EX v93,v93
MM v[124:127],a[252:255],v[220:223],v[124:127]
EX v94,v94
MM v[120:123],a[236:239],v[220:223],v[140:143]
EX v95,v95
MM v[120:123],a[232:235],v[204:207],v[120:123]
EX v96,v96
MM v[120:123],a[228:231],v[188:191],v[120:123]
EX v97,v97
MM v[120:123],a[224:227],v[172:175],v[120:123]
s_waitcnt vmcnt(15) lgkmcnt(0)
s_barrier
MM a[0:3],a[128:131],v[0:3],a[0:3]
s_add_u32 m0, s65, 32768
CV v88,v88,v89
CV v89,v90,v91
MM a[0:3],a[132:135],v[4:7],a[0:3]
RD a[192:195],v224 offset:0
CV v90,v92,v93
MM a[8:11],a[132:135],v[12:15],a[8:11]
RD a[196:199],v225 offset:0
CV v91,v94,v95
MM a[8:11],a[128:131],v[8:11],a[8:11]
buffer_load_dwordx4 v228, s[8:11], s64 offen offset:0 lds
MM a[16:19],a[128:131],v[16:19],a[16:19]
RD a[200:203],v226 offset:0
CV v68,v96,v97
MM a[16:19],a[132:135],v[20:23],a[16:19]
buffer_load_dwordx4 v229, s[8:11], s64 offen offset:1024 lds
MM a[24:27],a[132:135],v[28:31],a[24:27]
EX v98,v98
MM a[24:27],a[128:131],v[24:27],a[24:27]
RD a[204:207],v227 offset:0
RD a[208:211],v224 offset:4096
MM a[28:31],a[136:139],v[24:27],a[28:31]
EX v99,v99
MM a[28:31],a[140:143],v[28:31],a[28:31]
RD a[212:215],v225 offset:4096
CV v69,v98,v99
MM a[20:23],a[140:143],v[20:23],a[20:23]
EX v100,v100
MM a[20:23],a[136:139],v[16:19],a[20:23]
EX v101,v101
MM a[12:15],a[136:139],v[8:11],a[12:15]
RD a[216:219],v226 offset:4096
CV v70,v100,v101
MM a[12:15],a[140:143],v[12:15],a[12:15]
buffer_load_dwordx4 v230, s[8:11], s64 offen offset:2048 lds
MM a[4:7],a[140:143],v[4:7],a[4:7]
EX v102,v102
MM a[4:7],a[136:139],v[0:3],a[4:7]
EX v103,v103
MM a[32:35],a[144:147],v[0:3],a[32:35]
RD a[220:223],v227 offset:4096
CV v71,v102,v103
MM a[32:35],a[148:151],v[4:7],a[32:35]
EX v104,v104
MM a[40:43],a[148:151],v[12:15],a[40:43]
EX v105,v105
MM a[40:43],a[144:147],v[8:11],a[40:43]
RD a[224:227],v224 offset:8192
CV v76,v104,v105
MM a[48:51],a[144:147],v[16:19],a[48:51]
buffer_load_dwordx4 v231, s[8:11], s64 offen offset:3072 lds
MM a[48:51],a[148:151],v[20:23],a[48:51]
s_add_u32 m0, s65, 98304
EX v106,v106
MM a[56:59],a[148:151],v[28:31],a[56:59]
EX v107,v107
MM a[56:59],a[144:147],v[24:27],a[56:59]
RD a[228:231],v225 offset:8192
CV v77,v106,v107
MM a[60:63],a[152:155],v[24:27],a[60:63]
EX v108,v108
MM a[60:63],a[156:159],v[28:31],a[60:63]
buffer_load_dwordx4 v235, s[12:15], s81 offen offset:0 lds
MM a[52:55],a[156:159],v[20:23],a[52:55]
EX v109,v109
MM a[52:55],a[152:155],v[16:19],a[52:55]
RD a[232:235],v226 offset:8192
CV v78,v108,v109
MM a[44:47],a[152:155],v[8:11],a[44:47]
EX v110,v110
MM a[44:47],a[156:159],v[12:15],a[44:47]
EX v111,v111
MM a[36:39],a[156:159],v[4:7],a[36:39]
RD a[236:239],v227 offset:8192
CV v79,v110,v111
MM a[36:39],a[152:155],v[0:3],a[36:39]
EX v112,v112
MM a[64:67],a[160:163],v[0:3],a[64:67]
buffer_load_dwordx4 v235, s[12:15], s64 offen offset:1024 lds
MM a[64:67],a[164:167],v[4:7],a[64:67]
EX v113,v113
MM a[72:75],a[164:167],v[12:15],a[72:75]
RD a[240:243],v224 offset:12288
CV v84,v112,v113
MM a[72:75],a[160:163],v[8:11],a[72:75]
EX v114,v114
MM a[80:83],a[160:163],v[16:19],a[80:83]
s_add_u32 m0, s67, 3072
s_add_u32 s85, s66, 165888
buffer_load_dwordx4 v252, s[36:39], s85 offen offset:0 lds
MM a[80:83],a[164:167],v[20:23],a[80:83]
EX v115,v115
MM a[88:91],a[164:167],v[28:31],a[88:91]
RD a[244:247],v225 offset:12288
CV v85,v114,v115
MM a[88:91],a[160:163],v[24:27],a[88:91]
EX v116,v116
MM a[92:95],a[168:171],v[24:27],a[92:95]
EX v117,v117
MM a[92:95],a[172:175],v[28:31],a[92:95]
RD a[248:251],v226 offset:12288
CV v86,v116,v117
MM a[84:87],a[172:175],v[20:23],a[84:87]
s_add_u32 m0, s65, 98304
s_nop 0
buffer_load_dwordx4 v235, s[12:15], s81 offen offset:2048 lds
MM a[84:87],a[168:171],v[16:19],a[84:87]
EX v118,v118
MM a[76:79],a[168:171],v[8:11],a[76:79]
EX v119,v119
MM a[76:79],a[172:175],v[12:15],a[76:79]
RD a[252:255],v227 offset:12288
CV v87,v118,v119
MM a[68:71],a[172:175],v[4:7],a[68:71]
EX v120,v120
MM a[68:71],a[168:171],v[0:3],a[68:71]
EX v121,v121
MM a[96:99],a[176:179],v[0:3],a[96:99]
buffer_load_dwordx4 v235, s[12:15], s64 offen offset:3072 lds
MM a[96:99],a[180:183],v[4:7],a[96:99]
EX v122,v122
MM a[104:107],a[180:183],v[12:15],a[104:107]
EX v123,v123
MM a[104:107],a[176:179],v[8:11],a[104:107]
EX v124,v124
MM a[112:115],a[176:179],v[16:19],a[112:115]
EX v125,v125
MM a[112:115],a[180:183],v[20:23],a[112:115]
EX v126,v126
MM a[120:123],a[180:183],v[28:31],a[120:123]
EX v127,v127
MM a[120:123],a[176:179],v[24:27],a[120:123]
CV v92,v120,v121
CV v93,v122,v123
MM a[124:127],a[184:187],v[24:27],a[124:127]
CV v94,v124,v125
CV v95,v126,v127
MM a[124:127],a[188:191],v[28:31],a[124:127]
MM a[116:119],a[188:191],v[20:23],a[116:119]
MM a[116:119],a[184:187],v[16:19],a[116:119]
MM a[108:111],a[184:187],v[8:11],a[108:111]
MM a[108:111],a[188:191],v[12:15],a[108:111]
MM a[100:103],a[188:191],v[4:7],a[100:103]
MM a[100:103],a[184:187],v[0:3],a[100:103]
v_smfmac_f32_16x16x64_bf16 v[144:147], v[236:239], v[64:71], v234
v_smfmac_f32_16x16x64_bf16 v[148:151], v[236:239], v[72:79], v234
v_smfmac_f32_16x16x64_bf16 v[152:155], v[236:239], v[80:87], v234
v_smfmac_f32_16x16x64_bf16 v[156:159], v[236:239], v[88:95], v234
s_waitcnt lgkmcnt(0)
MM v[0:3],a[192:195],v[160:163],v[128:131]
s_add_u32 s64, s64, 16384
TR a[128:129],v232 offset:49152
s_add_u32 s81, s64, 896
MM v[0:3],a[196:199],v[176:179],v[0:3]
TR a[130:131],v232 offset:53248
TR a[132:133],v232 offset:57344
MM v[0:3],a[200:203],v[192:195],v[0:3]
v_max3_f32 v249, v144, v145, v148
v_max3_f32 v249, v249, v149, v152
MM v[0:3],a[204:207],v[208:211],v[0:3]
v_max3_f32 v249, v249, v153, v156
v_max_f32 v249, v249, v157
MM v[4:7],a[220:223],v[208:211],v[128:131]
TR a[134:135],v232 offset:61440
v_cmp_lt_f32 vcc, s71, v249
MM v[4:7],a[216:219],v[192:195],v[4:7]
s_cbranch_vccnz LSITE11_%=
LBACK11_%=:
TR a[136:137],v233 offset:49152
v_add_f32 v244, v144, v145
MM v[4:7],a[212:215],v[176:179],v[4:7]
v_add_f32 v245, v148, v149
v_add_f32 v246, v152, v153
MM v[4:7],a[208:211],v[160:163],v[4:7]
TR a[138:139],v233 offset:53248
v_add_f32 v247, v156, v157
MM v[12:15],a[208:211],v[164:167],v[132:135]
s_add_u32 m0, s67, 4096
s_add_u32 s85, s66, 221184
buffer_load_dwordx4 v252, s[36:39], s85 offen offset:0 lds
MM v[12:15],a[212:215],v[180:183],v[12:15]
EX v0,v0
MM v[12:15],a[216:219],v[196:199],v[12:15]
EX v1,v1
MM v[12:15],a[220:223],v[212:215],v[12:15]
TR a[140:141],v233 offset:57344
CV v0,v0,v1
MM v[8:11],a[204:207],v[212:215],v[132:135]
EX v2,v2
MM v[8:11],a[200:203],v[196:199],v[8:11]
TR a[142:143],v233 offset:61440
s_nop 0
LPG10_%=:
s_cbranch_execz LTR0_%=
TR a[144:145],v232 offset:49664
MM v[8:11],a[196:199],v[180:183],v[8:11]
EX v3,v3
MM v[8:11],a[192:195],v[164:167],v[8:11]
TR a[146:147],v232 offset:53760
CV v1,v2,v3
MM v[16:19],a[192:195],v[168:171],v[136:139]
EX v4,v4
MM v[16:19],a[196:199],v[184:187],v[16:19]
TR a[148:149],v232 offset:57856
TR a[150:151],v232 offset:61952
MM v[16:19],a[200:203],v[200:203],v[16:19]
EX v5,v5
MM v[16:19],a[204:207],v[216:219],v[16:19]
TR a[152:153],v233 offset:49664
CV v2,v4,v5
MM v[20:23],a[220:223],v[216:219],v[136:139]
EX v6,v6
MM v[20:23],a[216:219],v[200:203],v[20:23]
EX v7,v7
MM v[20:23],a[212:215],v[184:187],v[20:23]
TR a[154:155],v233 offset:53760
CV v3,v6,v7
MM v[20:23],a[208:211],v[168:171],v[20:23]
EX v8,v8
MM v[28:31],a[208:211],v[172:175],v[140:143]
TR a[156:157],v233 offset:57856
TR a[158:159],v233 offset:61952
MM v[28:31],a[212:215],v[188:191],v[28:31]
EX v9,v9
MM v[28:31],a[216:219],v[204:207],v[28:31]
TR a[160:161],v232 offset:50176
CV v8,v8,v9
MM v[28:31],a[220:223],v[220:223],v[28:31]
EX v10,v10
MM v[24:27],a[204:207],v[220:223],v[140:143]
EX v11,v11
MM v[24:27],a[200:203],v[204:207],v[24:27]
TR a[162:163],v232 offset:54272
CV v9,v10,v11
MM v[24:27],a[196:199],v[188:191],v[24:27]
EX v12,v12
MM v[24:27],a[192:195],v[172:175],v[24:27]
TR a[164:165],v232 offset:58368
TR a[166:167],v232 offset:62464
MM v[32:35],a[224:227],v[160:163],v[128:131]
EX v13,v13
MM v[32:35],a[228:231],v[176:179],v[32:35]
TR a[168:169],v233 offset:50176
CV v10,v12,v13
MM v[32:35],a[232:235],v[192:195],v[32:35]
EX v14,v14
MM v[32:35],a[236:239],v[208:211],v[32:35]
EX v15,v15
MM v[36:39],a[252:255],v[208:211],v[128:131]
TR a[170:171],v233 offset:54272
CV v11,v14,v15
MM v[36:39],a[248:251],v[192:195],v[36:39]
EX v16,v16
MM v[36:39],a[244:247],v[176:179],v[36:39]
TR a[172:173],v233 offset:58368
TR a[174:175],v233 offset:62464
MM v[36:39],a[240:243],v[160:163],v[36:39]
EX v17,v17
MM v[44:47],a[240:243],v[164:167],v[132:135]
TR a[176:177],v232 offset:50688
CV v16,v16,v17
MM v[44:47],a[244:247],v[180:183],v[44:47]
EX v18,v18
MM v[44:47],a[248:251],v[196:199],v[44:47]
TR a[178:179],v232 offset:54784
TR a[180:181],v232 offset:58880
MM v[44:47],a[252:255],v[212:215],v[44:47]
EX v19,v19
MM v[40:43],a[236:239],v[212:215],v[132:135]
TR a[182:183],v232 offset:62976
CV v17,v18,v19
MM v[40:43],a[232:235],v[196:199],v[40:43]
EX v20,v20
MM v[40:43],a[228:231],v[180:183],v[40:43]
EX v21,v21
MM v[40:43],a[224:227],v[164:167],v[40:43]
TR a[184:185],v233 offset:50688
CV v18,v20,v21
MM v[48:51],a[224:227],v[168:171],v[136:139]
EX v22,v22
MM v[48:51],a[228:231],v[184:187],v[48:51]
TR a[186:187],v233 offset:54784
TR a[188:189],v233 offset:58880
MM v[48:51],a[232:235],v[200:203],v[48:51]
EX v23,v23
MM v[48:51],a[236:239],v[216:219],v[48:51]
TR a[190:191],v233 offset:62976
CV v19,v22,v23
MM v[52:55],a[252:255],v[216:219],v[136:139]
EX v24,v24
MM v[52:55],a[248:251],v[200:203],v[52:55]
EX v25,v25
MM v[52:55],a[244:247],v[184:187],v[52:55]
EX v26,v26
MM v[52:55],a[240:243],v[168:171],v[52:55]
EX v27,v27
MM v[60:63],a[240:243],v[172:175],v[140:143]
EX v28,v28
MM v[60:63],a[244:247],v[188:191],v[60:63]
EX v29,v29
MM v[60:63],a[248:251],v[204:207],v[60:63]
EX v30,v30
MM v[60:63],a[252:255],v[220:223],v[60:63]
EX v31,v31
MM v[56:59],a[236:239],v[220:223],v[140:143]
EX v32,v32
MM v[56:59],a[232:235],v[204:207],v[56:59]
EX v33,v33
MM v[56:59],a[228:231],v[188:191],v[56:59]
EX v34,v34
MM v[56:59],a[224:227],v[172:175],v[56:59]
s_waitcnt vmcnt(17) lgkmcnt(0)
s_barrier
MM a[0:3],a[128:131],v[64:67],a[0:3]
s_mov_b32 s86, 94208
v_bfe_u32 v251, v248, 4, 3
v_lshlrev_b32 v251, 3, v251
buffer_load_dwordx2 v[252:253], v251, s[20:23], s86 offen offset:0 sc1
MM a[0:3],a[132:135],v[68:71],a[0:3]
s_add_u32 m0, s65, 49152
CV v24,v24,v25
CV v25,v26,v27
MM a[8:11],a[132:135],v[76:79],a[8:11]
RD a[192:195],v224 offset:16384
CV v26,v28,v29
MM a[8:11],a[128:131],v[72:75],a[8:11]
RD a[196:199],v225 offset:16384
CV v27,v30,v31
MM a[16:19],a[128:131],v[80:83],a[16:19]
buffer_load_dwordx4 v228, s[8:11], s64 offen offset:0 lds
MM a[16:19],a[132:135],v[84:87],a[16:19]
RD a[200:203],v226 offset:16384
CV v4,v32,v33
MM a[24:27],a[132:135],v[92:95],a[24:27]
buffer_load_dwordx4 v229, s[8:11], s64 offen offset:1024 lds
MM a[24:27],a[128:131],v[88:91],a[24:27]
RD a[204:207],v227 offset:16384
RD a[208:211],v224 offset:20480
MM a[28:31],a[136:139],v[88:91],a[28:31]
EX v35,v35
MM a[28:31],a[140:143],v[92:95],a[28:31]
RD a[212:215],v225 offset:20480
CV v5,v34,v35
MM a[20:23],a[140:143],v[84:87],a[20:23]
EX v36,v36
MM a[20:23],a[136:139],v[80:83],a[20:23]
EX v37,v37
MM a[12:15],a[136:139],v[72:75],a[12:15]
RD a[216:219],v226 offset:20480
CV v6,v36,v37
MM a[12:15],a[140:143],v[76:79],a[12:15]
buffer_load_dwordx4 v230, s[8:11], s64 offen offset:2048 lds
MM a[4:7],a[140:143],v[68:71],a[4:7]
EX v38,v38
MM a[4:7],a[136:139],v[64:67],a[4:7]
)ASM"
R"ASM(
EX v39,v39
MM a[32:35],a[144:147],v[64:67],a[32:35]
RD a[220:223],v227 offset:20480
CV v7,v38,v39
MM a[32:35],a[148:151],v[68:71],a[32:35]
EX v40,v40
MM a[40:43],a[148:151],v[76:79],a[40:43]
EX v41,v41
MM a[40:43],a[144:147],v[72:75],a[40:43]
RD a[224:227],v224 offset:24576
CV v12,v40,v41
MM a[48:51],a[144:147],v[80:83],a[48:51]
buffer_load_dwordx4 v231, s[8:11], s64 offen offset:3072 lds
MM a[48:51],a[148:151],v[84:87],a[48:51]
s_add_u32 m0, s65, 114688
EX v42,v42
MM a[56:59],a[148:151],v[92:95],a[56:59]
EX v43,v43
MM a[56:59],a[144:147],v[88:91],a[56:59]
RD a[228:231],v225 offset:24576
CV v13,v42,v43
MM a[60:63],a[152:155],v[88:91],a[60:63]
EX v44,v44
MM a[60:63],a[156:159],v[92:95],a[60:63]
buffer_load_dwordx4 v235, s[12:15], s81 offen offset:0 lds
MM a[52:55],a[156:159],v[84:87],a[52:55]
EX v45,v45
MM a[52:55],a[152:155],v[80:83],a[52:55]
RD a[232:235],v226 offset:24576
CV v14,v44,v45
MM a[44:47],a[152:155],v[72:75],a[44:47]
EX v46,v46
MM a[44:47],a[156:159],v[76:79],a[44:47]
EX v47,v47
MM a[36:39],a[156:159],v[68:71],a[36:39]
RD a[236:239],v227 offset:24576
CV v15,v46,v47
MM a[36:39],a[152:155],v[64:67],a[36:39]
EX v48,v48
MM a[64:67],a[160:163],v[64:67],a[64:67]
buffer_load_dwordx4 v235, s[12:15], s64 offen offset:1024 lds
MM a[64:67],a[164:167],v[68:71],a[64:67]
EX v49,v49
MM a[72:75],a[164:167],v[76:79],a[72:75]
RD a[240:243],v224 offset:28672
CV v20,v48,v49
MM a[72:75],a[160:163],v[72:75],a[72:75]
EX v50,v50
MM a[80:83],a[160:163],v[80:83],a[80:83]
EX v51,v51
MM a[80:83],a[164:167],v[84:87],a[80:83]
RD a[244:247],v225 offset:28672
CV v21,v50,v51
MM a[88:91],a[164:167],v[92:95],a[88:91]
EX v52,v52
MM a[88:91],a[160:163],v[88:91],a[88:91]
buffer_load_dwordx4 v235, s[12:15], s81 offen offset:2048 lds
MM a[92:95],a[168:171],v[88:91],a[92:95]
EX v53,v53
MM a[92:95],a[172:175],v[92:95],a[92:95]
RD a[248:251],v226 offset:28672
CV v22,v52,v53
MM a[84:87],a[172:175],v[84:87],a[84:87]
EX v54,v54
MM a[84:87],a[168:171],v[80:83],a[84:87]
EX v55,v55
MM a[76:79],a[168:171],v[72:75],a[76:79]
RD a[252:255],v227 offset:28672
CV v23,v54,v55
MM a[76:79],a[172:175],v[76:79],a[76:79]
EX v56,v56
MM a[68:71],a[172:175],v[68:71],a[68:71]
buffer_load_dwordx4 v235, s[12:15], s64 offen offset:3072 lds
MM a[68:71],a[168:171],v[64:67],a[68:71]
EX v57,v57
MM a[96:99],a[176:179],v[64:67],a[96:99]
EX v58,v58
MM a[96:99],a[180:183],v[68:71],a[96:99]
EX v59,v59
MM a[104:107],a[180:183],v[76:79],a[104:107]
EX v60,v60
MM a[104:107],a[176:179],v[72:75],a[104:107]
EX v61,v61
MM a[112:115],a[176:179],v[80:83],a[112:115]
EX v62,v62
MM a[112:115],a[180:183],v[84:87],a[112:115]
EX v63,v63
MM a[120:123],a[180:183],v[92:95],a[120:123]
CV v28,v56,v57
CV v29,v58,v59
MM a[120:123],a[176:179],v[88:91],a[120:123]
CV v30,v60,v61
CV v31,v62,v63
MM a[124:127],a[184:187],v[88:91],a[124:127]
MM a[124:127],a[188:191],v[92:95],a[124:127]
MM a[116:119],a[188:191],v[84:87],a[116:119]
MM a[116:119],a[184:187],v[80:83],a[116:119]
MM a[108:111],a[184:187],v[72:75],a[108:111]
MM a[108:111],a[188:191],v[76:79],a[108:111]
MM a[100:103],a[188:191],v[68:71],a[100:103]
MM a[100:103],a[184:187],v[64:67],a[100:103]
v_smfmac_f32_16x16x64_bf16 v[144:147], v[236:239], v[0:7], v234
v_smfmac_f32_16x16x64_bf16 v[148:151], v[236:239], v[8:15], v234
v_smfmac_f32_16x16x64_bf16 v[152:155], v[236:239], v[16:23], v234
v_smfmac_f32_16x16x64_bf16 v[156:159], v[236:239], v[24:31], v234
s_waitcnt lgkmcnt(0)
MM v[64:67],a[192:195],v[160:163],v[128:131]
s_add_u32 s64, s64, 16384
TR a[128:129],v232 offset:0
s_add_u32 s81, s64, 896
MM v[64:67],a[196:199],v[176:179],v[64:67]
TR a[130:131],v232 offset:4096
TR a[132:133],v232 offset:8192
MM v[64:67],a[200:203],v[192:195],v[64:67]
v_max3_f32 v249, v144, v145, v148
v_max3_f32 v249, v249, v149, v152
MM v[64:67],a[204:207],v[208:211],v[64:67]
v_max3_f32 v249, v249, v153, v156
v_max_f32 v249, v249, v157
MM v[68:71],a[220:223],v[208:211],v[128:131]
TR a[134:135],v232 offset:12288
v_cmp_lt_f32 vcc, s71, v249
MM v[68:71],a[216:219],v[192:195],v[68:71]
s_cbranch_vccnz LSITE12_%=
LBACK12_%=:
TR a[136:137],v233 offset:0
v_add_f32 v244, v144, v145
MM v[68:71],a[212:215],v[176:179],v[68:71]
s_waitcnt vmcnt(8)
v_add_f32 v245, v148, v149
v_add_f32 v246, v152, v153
MM v[68:71],a[208:211],v[160:163],v[68:71]
TR a[138:139],v233 offset:4096
v_add_f32 v247, v156, v157
MM v[76:79],a[208:211],v[164:167],v[132:135]
v_cmp_ne_u32 vcc, s69, v253
s_cbranch_vccnz LRIX_%=
LRIXb_%=:
MM v[76:79],a[212:215],v[180:183],v[76:79]
s_and_b32 s90, s17, 7
v_and_b32 v253, 65535, v252
v_readlane_b32 s19, v252, s90
MM v[76:79],a[216:219],v[196:199],v[76:79]
v_sub_u32 v253, 65535, v253
v_mul_u32_u24 v253, 1280, v253
MM v[76:79],a[220:223],v[212:215],v[76:79]
v_lshrrev_b32 v252, 7, v248
v_lshl_add_u32 v253, v252, 4, v253
MM v[72:75],a[204:207],v[212:215],v[132:135]
s_mul_i32 s91, %[swave], 3072
EX v64,v64
MM v[72:75],a[200:203],v[196:199],v[72:75]
s_add_u32 s91, s91, %[slds]
EX v65,v65
MM v[72:75],a[196:199],v[180:183],v[72:75]
s_bfe_u32 s90, s48, 131077
EX v66,v66
MM v[72:75],a[192:195],v[164:167],v[72:75]
s_mul_i32 s90, s90, 320
TR a[140:141],v233 offset:8192
TR a[142:143],v233 offset:12288
MM v[80:83],a[192:195],v[168:171],v[136:139]
s_add_u32 s90, s90, 131072
s_add_u32 m0, s91, 151552
s_nop 0
buffer_load_dwordx4 v253, s[20:23], s90 offen offset:0 sc1 lds
MM v[80:83],a[196:199],v[184:187],v[80:83]
EX v67,v67
MM v[80:83],a[200:203],v[200:203],v[80:83]
s_add_u32 m0, s91, 152448
s_nop 0
buffer_load_dwordx4 v253, s[20:23], s90 offen offset:128 sc1 lds
MM v[80:83],a[204:207],v[216:219],v[80:83]
EX v68,v68
MM v[84:87],a[220:223],v[216:219],v[136:139]
s_add_u32 m0, s91, 153344
s_nop 0
buffer_load_dwordx4 v253, s[20:23], s90 offen offset:256 sc1 lds
MM v[84:87],a[216:219],v[200:203],v[84:87]
TR a[144:145],v232 offset:512
EX v69,v69
MM v[84:87],a[212:215],v[184:187],v[84:87]
s_lshr_b32 s77, %[sb], 3
TR a[146:147],v232 offset:4608
CV v64,v64,v65
MM v[84:87],a[208:211],v[168:171],v[84:87]
s_lshl_b32 s77, s77, 2
EX v70,v70
MM v[92:95],a[208:211],v[172:175],v[140:143]
s_add_u32 s77, s77, %[swave]
TR a[148:149],v232 offset:8704
TR a[150:151],v232 offset:12800
MM v[92:95],a[212:215],v[188:191],v[92:95]
s_lshl_b32 s77, s77, 9
EX v71,v71
MM v[92:95],a[216:219],v[204:207],v[92:95]
s_add_u32 s77, s77, 0
v_lshrrev_b32 v253, 1, v248
v_add_u32 v253, s77, v253
MM v[92:95],a[220:223],v[220:223],v[92:95]
s_mov_b32 s77, LKEND_ka-LPD0_%=-16
v_min_u32 v253, s77, v253
CV v65,v66,v67
EX v72,v72
MM v[88:91],a[204:207],v[220:223],v[140:143]
s_getpc_b64 s[98:99]
LPD0_%=:
global_load_dword v253, v253, s[98:99]
MM v[88:91],a[200:203],v[204:207],v[88:91]
s_add_u32 s67, s95, 1024
TR a[152:153],v233 offset:512
EX v73,v73
MM v[88:91],a[196:199],v[188:191],v[88:91]
s_mov_b32 s70, 5
TR a[154:155],v233 offset:4608
CV v66,v68,v69
MM v[88:91],a[192:195],v[172:175],v[88:91]
EX v74,v74
MM v[96:99],a[224:227],v[160:163],v[128:131]
TR a[156:157],v233 offset:8704
TR a[158:159],v233 offset:12800
EX v75,v75
MM v[96:99],a[228:231],v[176:179],v[96:99]
TR a[160:161],v232 offset:1024
CV v67,v70,v71
MM v[96:99],a[232:235],v[192:195],v[96:99]
EX v76,v76
MM v[96:99],a[236:239],v[208:211],v[96:99]
EX v77,v77
MM v[100:103],a[252:255],v[208:211],v[128:131]
TR a[162:163],v232 offset:5120
CV v72,v72,v73
MM v[100:103],a[248:251],v[192:195],v[100:103]
EX v78,v78
MM v[100:103],a[244:247],v[176:179],v[100:103]
TR a[164:165],v232 offset:9216
CV v73,v74,v75
MM v[100:103],a[240:243],v[160:163],v[100:103]
EX v79,v79
MM v[108:111],a[240:243],v[164:167],v[132:135]
TR a[166:167],v232 offset:13312
TR a[168:169],v233 offset:1024
MM v[108:111],a[244:247],v[180:183],v[108:111]
EX v80,v80
MM v[108:111],a[248:251],v[196:199],v[108:111]
EX v81,v81
MM v[108:111],a[252:255],v[212:215],v[108:111]
TR a[170:171],v233 offset:5120
CV v74,v76,v77
MM v[104:107],a[236:239],v[212:215],v[132:135]
EX v82,v82
MM v[104:107],a[232:235],v[196:199],v[104:107]
TR a[172:173],v233 offset:9216
CV v75,v78,v79
MM v[104:107],a[228:231],v[180:183],v[104:107]
EX v83,v83
MM v[104:107],a[224:227],v[164:167],v[104:107]
TR a[174:175],v233 offset:13312
TR a[176:177],v232 offset:1536
MM v[112:115],a[224:227],v[168:171],v[136:139]
EX v84,v84
MM v[112:115],a[228:231],v[184:187],v[112:115]
TR a[178:179],v232 offset:5632
CV v80,v80,v81
MM v[112:115],a[232:235],v[200:203],v[112:115]
EX v85,v85
MM v[112:115],a[236:239],v[216:219],v[112:115]
TR a[180:181],v232 offset:9728
CV v81,v82,v83
MM v[116:119],a[252:255],v[216:219],v[136:139]
EX v86,v86
MM v[116:119],a[248:251],v[200:203],v[116:119]
TR a[182:183],v232 offset:13824
TR a[184:185],v233 offset:1536
MM v[116:119],a[244:247],v[184:187],v[116:119]
EX v87,v87
MM v[116:119],a[240:243],v[168:171],v[116:119]
EX v88,v88
MM v[124:127],a[240:243],v[172:175],v[140:143]
TR a[186:187],v233 offset:5632
CV v82,v84,v85
MM v[124:127],a[244:247],v[188:191],v[124:127]
EX v89,v89
MM v[124:127],a[248:251],v[204:207],v[124:127]
TR a[188:189],v233 offset:9728
TR a[190:191],v233 offset:13824
MM v[124:127],a[252:255],v[220:223],v[124:127]
EX v90,v90
MM v[120:123],a[236:239],v[220:223],v[140:143]
CV v83,v86,v87
CV v88,v88,v89
MM v[120:123],a[232:235],v[204:207],v[120:123]
EX v91,v91
MM v[120:123],a[228:231],v[188:191],v[120:123]
EX v92,v92
MM v[120:123],a[224:227],v[172:175],v[120:123]
s_waitcnt lgkmcnt(0)
s_barrier
MM a[0:3],a[128:131],v[0:3],a[0:3]
s_add_u32 m0, s65, 0
EX v93,v93
MM a[0:3],a[132:135],v[4:7],a[0:3]
CV v89,v90,v91
CV v90,v92,v93
MM a[8:11],a[132:135],v[12:15],a[8:11]
EX v94,v94
MM a[8:11],a[128:131],v[8:11],a[8:11]
EX v95,v95
MM a[16:19],a[128:131],v[16:19],a[16:19]
RD a[192:195],v224 offset:32768
CV v91,v94,v95
MM a[16:19],a[132:135],v[20:23],a[16:19]
EX v96,v96
MM a[24:27],a[132:135],v[28:31],a[24:27]
buffer_load_dwordx4 v228, s[8:11], s64 offen offset:0 lds
MM a[24:27],a[128:131],v[24:27],a[24:27]
EX v97,v97
MM a[28:31],a[136:139],v[24:27],a[28:31]
RD a[196:199],v225 offset:32768
CV v68,v96,v97
MM a[28:31],a[140:143],v[28:31],a[28:31]
EX v98,v98
MM a[20:23],a[140:143],v[20:23],a[20:23]
EX v99,v99
MM a[20:23],a[136:139],v[16:19],a[20:23]
RD a[200:203],v226 offset:32768
CV v69,v98,v99
MM a[12:15],a[136:139],v[8:11],a[12:15]
EX v100,v100
MM a[12:15],a[140:143],v[12:15],a[12:15]
buffer_load_dwordx4 v229, s[8:11], s64 offen offset:1024 lds
MM a[4:7],a[140:143],v[4:7],a[4:7]
EX v101,v101
MM a[4:7],a[136:139],v[0:3],a[4:7]
RD a[204:207],v227 offset:32768
CV v70,v100,v101
MM a[32:35],a[144:147],v[0:3],a[32:35]
EX v102,v102
MM a[32:35],a[148:151],v[4:7],a[32:35]
EX v103,v103
MM a[40:43],a[148:151],v[12:15],a[40:43]
RD a[208:211],v224 offset:36864
CV v71,v102,v103
MM a[40:43],a[144:147],v[8:11],a[40:43]
EX v104,v104
MM a[48:51],a[144:147],v[16:19],a[48:51]
buffer_load_dwordx4 v230, s[8:11], s64 offen offset:2048 lds
MM a[48:51],a[148:151],v[20:23],a[48:51]
EX v105,v105
MM a[56:59],a[148:151],v[28:31],a[56:59]
RD a[212:215],v225 offset:36864
CV v76,v104,v105
MM a[56:59],a[144:147],v[24:27],a[56:59]
EX v106,v106
MM a[60:63],a[152:155],v[24:27],a[60:63]
EX v107,v107
MM a[60:63],a[156:159],v[28:31],a[60:63]
RD a[216:219],v226 offset:36864
CV v77,v106,v107
MM a[52:55],a[156:159],v[20:23],a[52:55]
EX v108,v108
MM a[52:55],a[152:155],v[16:19],a[52:55]
buffer_load_dwordx4 v231, s[8:11], s64 offen offset:3072 lds
MM a[44:47],a[152:155],v[8:11],a[44:47]
s_add_u32 m0, s65, 65536
EX v109,v109
MM a[44:47],a[156:159],v[12:15],a[44:47]
RD a[220:223],v227 offset:36864
CV v78,v108,v109
MM a[36:39],a[156:159],v[4:7],a[36:39]
EX v110,v110
MM a[36:39],a[152:155],v[0:3],a[36:39]
RD a[224:227],v224 offset:40960
RD a[228:231],v225 offset:40960
MM a[64:67],a[160:163],v[0:3],a[64:67]
EX v111,v111
MM a[64:67],a[164:167],v[4:7],a[64:67]
RD a[232:235],v226 offset:40960
CV v79,v110,v111
MM a[72:75],a[164:167],v[12:15],a[72:75]
EX v112,v112
MM a[72:75],a[160:163],v[8:11],a[72:75]
buffer_load_dwordx4 v235, s[12:15], s81 offen offset:0 lds
MM a[80:83],a[160:163],v[16:19],a[80:83]
EX v113,v113
MM a[80:83],a[164:167],v[20:23],a[80:83]
RD a[236:239],v227 offset:40960
CV v84,v112,v113
MM a[88:91],a[164:167],v[28:31],a[88:91]
EX v114,v114
MM a[88:91],a[160:163],v[24:27],a[88:91]
)ASM"
R"ASM(
EX v115,v115
MM a[92:95],a[168:171],v[24:27],a[92:95]
RD a[240:243],v224 offset:45056
CV v85,v114,v115
MM a[92:95],a[172:175],v[28:31],a[92:95]
EX v116,v116
MM a[84:87],a[172:175],v[20:23],a[84:87]
buffer_load_dwordx4 v235, s[12:15], s64 offen offset:1024 lds
MM a[84:87],a[168:171],v[16:19],a[84:87]
EX v117,v117
MM a[76:79],a[168:171],v[8:11],a[76:79]
RD a[244:247],v225 offset:45056
CV v86,v116,v117
MM a[76:79],a[172:175],v[12:15],a[76:79]
EX v118,v118
MM a[68:71],a[172:175],v[4:7],a[68:71]
EX v119,v119
MM a[68:71],a[168:171],v[0:3],a[68:71]
RD a[248:251],v226 offset:45056
CV v87,v118,v119
MM a[96:99],a[176:179],v[0:3],a[96:99]
EX v120,v120
MM a[96:99],a[180:183],v[4:7],a[96:99]
buffer_load_dwordx4 v235, s[12:15], s81 offen offset:2048 lds
MM a[104:107],a[180:183],v[12:15],a[104:107]
EX v121,v121
MM a[104:107],a[176:179],v[8:11],a[104:107]
RD a[252:255],v227 offset:45056
CV v92,v120,v121
MM a[112:115],a[176:179],v[16:19],a[112:115]
EX v122,v122
MM a[112:115],a[180:183],v[20:23],a[112:115]
EX v123,v123
MM a[120:123],a[180:183],v[28:31],a[120:123]
s_add_u32 s87, s95, 0
buffer_store_dword v240, v248, s[20:23], s87 offen offset:12 sc1
MM a[120:123],a[176:179],v[24:27],a[120:123]
EX v124,v124
MM a[124:127],a[184:187],v[24:27],a[124:127]
buffer_load_dwordx4 v235, s[12:15], s64 offen offset:3072 lds
s_nop 0
LPG11_%=:
s_cbranch_execz LTR0_%=
MM a[124:127],a[188:191],v[28:31],a[124:127]
EX v125,v125
MM a[116:119],a[188:191],v[20:23],a[116:119]
EX v126,v126
MM a[116:119],a[184:187],v[16:19],a[116:119]
EX v127,v127
MM a[108:111],a[184:187],v[8:11],a[108:111]
CV v93,v122,v123
CV v94,v124,v125
MM a[108:111],a[188:191],v[12:15],a[108:111]
CV v95,v126,v127
MM a[100:103],a[188:191],v[4:7],a[100:103]
MM a[100:103],a[184:187],v[0:3],a[100:103]
v_smfmac_f32_16x16x64_bf16 v[144:147], v[236:239], v[64:71], v234
v_smfmac_f32_16x16x64_bf16 v[148:151], v[236:239], v[72:79], v234
v_smfmac_f32_16x16x64_bf16 v[152:155], v[236:239], v[80:87], v234
v_smfmac_f32_16x16x64_bf16 v[156:159], v[236:239], v[88:95], v234
s_waitcnt lgkmcnt(0)
s_cmp_ge_u32 s16, 108
s_cbranch_scc1 LLAP_%=
MM v[0:3],a[192:195],v[160:163],v[128:131]
s_add_u32 s64, s64, 16384
TR a[128:129],v232 offset:16384
s_add_u32 s81, s64, 896
MM v[0:3],a[196:199],v[176:179],v[0:3]
TR a[130:131],v232 offset:20480
TR a[132:133],v232 offset:24576
MM v[0:3],a[200:203],v[192:195],v[0:3]
v_max3_f32 v249, v144, v145, v148
v_max3_f32 v249, v249, v149, v152
MM v[0:3],a[204:207],v[208:211],v[0:3]
v_max3_f32 v249, v249, v153, v156
v_max_f32 v249, v249, v157
MM v[4:7],a[220:223],v[208:211],v[128:131]
TR a[134:135],v232 offset:28672
v_cmp_lt_f32 vcc, s71, v249
MM v[4:7],a[216:219],v[192:195],v[4:7]
s_cbranch_vccnz LSITE13_%=
LBACK13_%=:
TR a[136:137],v233 offset:16384
v_add_f32 v244, v144, v145
MM v[4:7],a[212:215],v[176:179],v[4:7]
v_add_f32 v245, v148, v149
v_add_f32 v246, v152, v153
MM v[4:7],a[208:211],v[160:163],v[4:7]
v_add_f32 v247, v156, v157
v_lshrrev_b32 v127, 8, v248
MM v[12:15],a[208:211],v[164:167],v[132:135]
v_lshlrev_b32 v124, 9, v127
v_and_b32 v126, 192, v248
MM v[12:15],a[212:215],v[180:183],v[12:15]
v_add_u32 v124, v124, v126
v_bfe_u32 v126, v248, 4, 2
MM v[12:15],a[216:219],v[196:199],v[12:15]
TR a[138:139],v233 offset:20480
v_lshl_add_u32 v124, v126, 3, v124
MM v[12:15],a[220:223],v[212:215],v[12:15]
s_lshr_b32 s76, %[swave], 1
EX v0,v0
MM v[8:11],a[204:207],v[212:215],v[132:135]
s_mul_i32 s76, s76, 10240
EX v1,v1
MM v[8:11],a[200:203],v[196:199],v[8:11]
s_and_b32 s77, %[swave], 1
TR a[140:141],v233 offset:24576
TR a[142:143],v233 offset:28672
MM v[8:11],a[196:199],v[180:183],v[8:11]
s_lshl_b32 s77, s77, 5
EX v2,v2
MM v[8:11],a[192:195],v[164:167],v[8:11]
s_add_u32 s76, s76, s77
EX v3,v3
MM v[16:19],a[192:195],v[168:171],v[136:139]
s_add_u32 s76, s76, %[slds]
TR a[144:145],v232 offset:16896
CV v0,v0,v1
MM v[16:19],a[196:199],v[184:187],v[16:19]
s_add_u32 s76, s76, 131072
v_add_u32 v124, s76, v124
v_bfe_u32 v126, v248, 4, 3
MM v[16:19],a[200:203],v[200:203],v[16:19]
v_lshl_add_u32 v126, v127, 3, v126
v_lshlrev_b32 v126, 4, v126
MM v[16:19],a[204:207],v[216:219],v[16:19]
s_mul_i32 s76, %[swave], 3072
EX v4,v4
MM v[20:23],a[220:223],v[216:219],v[136:139]
s_add_u32 s76, s76, %[slds]
EX v5,v5
MM v[20:23],a[216:219],v[200:203],v[20:23]
s_add_u32 s76, s76, 151552
TR a[146:147],v232 offset:20992
v_add_u32 v125, s76, v126
MM v[20:23],a[212:215],v[184:187],v[20:23]
s_lshr_b32 s76, s48, 7
EX v6,v6
MM v[20:23],a[208:211],v[168:171],v[20:23]
s_lshl_b32 s76, s76, 5
TR a[148:149],v232 offset:25088
CV v1,v2,v3
MM v[28:31],a[208:211],v[172:175],v[140:143]
s_and_b32 s77, s48, 16
EX v7,v7
MM v[28:31],a[212:215],v[188:191],v[28:31]
s_add_u32 s76, s76, s77
TR a[150:151],v232 offset:29184
TR a[152:153],v233 offset:16896
MM v[28:31],a[216:219],v[204:207],v[28:31]
s_bfe_u32 s77, s48, 131077
EX v8,v8
MM v[28:31],a[220:223],v[220:223],v[28:31]
s_lshl_b32 s77, s77, 2
EX v9,v9
MM v[24:27],a[204:207],v[220:223],v[140:143]
s_add_u32 s76, s76, s77
TR a[154:155],v233 offset:20992
CV v2,v4,v5
MM v[24:27],a[200:203],v[204:207],v[24:27]
s_add_u32 s76, s76, %[swave]
EX v10,v10
MM v[24:27],a[196:199],v[188:191],v[24:27]
s_lshl_b32 s97, s76, 2
TR a[156:157],v233 offset:25088
CV v3,v6,v7
MM v[24:27],a[192:195],v[172:175],v[24:27]
s_add_u32 s97, s97, 77824
EX v11,v11
MM v[32:35],a[224:227],v[160:163],v[128:131]
s_lshl_b32 s76, s76, 9
TR a[158:159],v233 offset:29184
TR a[160:161],v232 offset:17408
MM v[32:35],a[228:231],v[176:179],v[32:35]
s_add_u32 s76, s76, 22183936
EX v12,v12
MM v[32:35],a[232:235],v[192:195],v[32:35]
TR a[162:163],v232 offset:21504
CV v8,v8,v9
MM v[32:35],a[236:239],v[208:211],v[32:35]
EX v13,v13
MM v[36:39],a[252:255],v[208:211],v[128:131]
TR a[164:165],v232 offset:25600
CV v9,v10,v11
MM v[36:39],a[248:251],v[192:195],v[36:39]
EX v14,v14
MM v[36:39],a[244:247],v[176:179],v[36:39]
EX v15,v15
MM v[36:39],a[240:243],v[160:163],v[36:39]
TR a[166:167],v232 offset:29696
TR a[168:169],v233 offset:17408
MM v[44:47],a[240:243],v[164:167],v[132:135]
EX v16,v16
MM v[44:47],a[244:247],v[180:183],v[44:47]
TR a[170:171],v233 offset:21504
CV v10,v12,v13
MM v[44:47],a[248:251],v[196:199],v[44:47]
EX v17,v17
MM v[44:47],a[252:255],v[212:215],v[44:47]
TR a[172:173],v233 offset:25600
CV v11,v14,v15
MM v[40:43],a[236:239],v[212:215],v[132:135]
EX v18,v18
MM v[40:43],a[232:235],v[196:199],v[40:43]
TR a[174:175],v233 offset:29696
TR a[176:177],v232 offset:17920
MM v[40:43],a[228:231],v[180:183],v[40:43]
EX v19,v19
MM v[40:43],a[224:227],v[164:167],v[40:43]
EX v20,v20
MM v[48:51],a[224:227],v[168:171],v[136:139]
TR a[178:179],v232 offset:22016
CV v16,v16,v17
MM v[48:51],a[228:231],v[184:187],v[48:51]
EX v21,v21
MM v[48:51],a[232:235],v[200:203],v[48:51]
TR a[180:181],v232 offset:26112
CV v17,v18,v19
MM v[48:51],a[236:239],v[216:219],v[48:51]
EX v22,v22
MM v[52:55],a[252:255],v[216:219],v[136:139]
TR a[182:183],v232 offset:30208
CV v18,v20,v21
MM v[52:55],a[248:251],v[200:203],v[52:55]
EX v23,v23
MM v[52:55],a[244:247],v[184:187],v[52:55]
TR a[184:185],v233 offset:17920
TR a[186:187],v233 offset:22016
MM v[52:55],a[240:243],v[168:171],v[52:55]
EX v24,v24
MM v[60:63],a[240:243],v[172:175],v[140:143]
EX v25,v25
MM v[60:63],a[244:247],v[188:191],v[60:63]
TR a[188:189],v233 offset:26112
CV v19,v22,v23
MM v[60:63],a[248:251],v[204:207],v[60:63]
TR a[190:191],v233 offset:30208
CV v24,v24,v25
MM v[60:63],a[252:255],v[220:223],v[60:63]
EX v26,v26
MM v[56:59],a[236:239],v[220:223],v[140:143]
EX v27,v27
MM v[56:59],a[232:235],v[204:207],v[56:59]
EX v28,v28
MM v[56:59],a[228:231],v[188:191],v[56:59]
s_getpc_b64 s[98:99]
LTA2_%=:
s_min_u32 s73, %[swave], 3
s_lshl_b32 s73, s73, 12
s_add_u32 s73, s73, LPG13_%=-LTA2_%=
s_add_u32 s98, s98, s73
s_addc_u32 s99, s99, 0
s_mov_b64 exec, 0
s_setpc_b64 s[98:99]
LTR2_%=:
s_mov_b64 exec, -1
CV v25,v26,v27
MM v[56:59],a[224:227],v[172:175],v[56:59]
s_waitcnt vmcnt(17) lgkmcnt(0)
s_barrier
MM a[0:3],a[128:131],v[64:67],a[0:3]
s_add_u32 m0, s65, 16384
EX v29,v29
MM a[0:3],a[132:135],v[68:71],a[0:3]
EX v30,v30
MM a[8:11],a[132:135],v[76:79],a[8:11]
RD a[192:195],v224 offset:49152
CV v26,v28,v29
MM a[8:11],a[128:131],v[72:75],a[8:11]
EX v31,v31
MM a[16:19],a[128:131],v[80:83],a[16:19]
s_waitcnt vmcnt(10)
TR v[96:97],v124 offset:0
TR v[98:99],v124 offset:256
MM a[16:19],a[132:135],v[84:87],a[16:19]
RD v[108:111],v125 offset:0
TR v[100:101],v124 offset:2048
MM a[24:27],a[132:135],v[92:95],a[24:27]
TR v[102:103],v124 offset:2304
RD v[112:115],v125 offset:512
MM a[24:27],a[128:131],v[88:91],a[24:27]
TR v[104:105],v124 offset:4096
TR v[106:107],v124 offset:4352
MM a[28:31],a[136:139],v[88:91],a[28:31]
RD v[116:119],v125 offset:1024
CV v27,v30,v31
EX v32,v32
MM a[28:31],a[140:143],v[92:95],a[28:31]
EX v33,v33
MM a[20:23],a[140:143],v[84:87],a[20:23]
EX v34,v34
MM a[20:23],a[136:139],v[80:83],a[20:23]
buffer_load_dwordx4 v228, s[8:11], s64 offen offset:0 lds
MM a[12:15],a[136:139],v[72:75],a[12:15]
EX v35,v35
MM a[12:15],a[140:143],v[76:79],a[12:15]
EX v36,v36
MM a[4:7],a[140:143],v[68:71],a[4:7]
RD a[196:199],v225 offset:49152
CV v4,v32,v33
MM a[4:7],a[136:139],v[64:67],a[4:7]
EX v37,v37
MM a[32:35],a[144:147],v[64:67],a[32:35]
RD a[200:203],v226 offset:49152
CV v5,v34,v35
MM a[32:35],a[148:151],v[68:71],a[32:35]
s_waitcnt lgkmcnt(2)
EX v38,v38
MM a[40:43],a[148:151],v[76:79],a[40:43]
buffer_load_dwordx4 v229, s[8:11], s64 offen offset:1024 lds
MM v[120:123],v[96:99],v[108:111],0
MM v[120:123],v[100:103],v[112:115],v[120:123]
MM v[120:123],v[104:107],v[116:119],v[120:123]
MM a[40:43],a[144:147],v[72:75],a[40:43]
TR v[96:97],v124 offset:6144
TR v[98:99],v124 offset:6400
MM a[48:51],a[144:147],v[80:83],a[48:51]
RD v[108:111],v125 offset:1536
TR v[100:101],v124 offset:8192
EX v39,v39
MM a[48:51],a[148:151],v[84:87],a[48:51]
TR v[102:103],v124 offset:8448
RD v[112:115],v125 offset:2048
MM a[56:59],a[148:151],v[92:95],a[56:59]
EX v40,v40
MM a[56:59],a[144:147],v[88:91],a[56:59]
EX v41,v41
MM a[60:63],a[152:155],v[88:91],a[60:63]
RD a[204:207],v227 offset:49152
CV v6,v36,v37
MM a[60:63],a[156:159],v[92:95],a[60:63]
EX v42,v42
MM a[52:55],a[156:159],v[84:87],a[52:55]
RD a[208:211],v224 offset:53248
CV v7,v38,v39
MM a[52:55],a[152:155],v[80:83],a[52:55]
EX v43,v43
MM a[44:47],a[152:155],v[72:75],a[44:47]
buffer_load_dwordx4 v230, s[8:11], s64 offen offset:2048 lds
MM a[44:47],a[156:159],v[76:79],a[44:47]
EX v44,v44
MM a[36:39],a[156:159],v[68:71],a[36:39]
s_waitcnt lgkmcnt(2)
EX v45,v45
MM a[36:39],a[152:155],v[64:67],a[36:39]
RD a[212:215],v225 offset:53248
CV v12,v40,v41
MM v[120:123],v[96:99],v[108:111],v[120:123]
MM v[120:123],v[100:103],v[112:115],v[120:123]
MM a[64:67],a[160:163],v[64:67],a[64:67]
EX v46,v46
MM a[64:67],a[164:167],v[68:71],a[64:67]
RD a[216:219],v226 offset:53248
CV v13,v42,v43
MM a[72:75],a[164:167],v[76:79],a[72:75]
EX v47,v47
MM a[72:75],a[160:163],v[72:75],a[72:75]
buffer_load_dwordx4 v231, s[8:11], s64 offen offset:3072 lds
MM a[80:83],a[160:163],v[80:83],a[80:83]
s_add_u32 m0, s65, 81920
EX v48,v48
MM a[80:83],a[164:167],v[84:87],a[80:83]
RD a[220:223],v227 offset:53248
CV v14,v44,v45
MM a[88:91],a[164:167],v[92:95],a[88:91]
s_mov_b32 exec_lo, 0x00ff00ff
s_mov_b32 exec_hi, 0x00ff00ff
buffer_store_dwordx4 v[120:123], v126, s[20:23], s76 offen offset:0 sc1
s_mov_b64 exec, -1
s_nop 0
MM a[88:91],a[160:163],v[88:91],a[88:91]
EX v49,v49
MM a[92:95],a[168:171],v[88:91],a[92:95]
EX v50,v50
MM a[92:95],a[172:175],v[92:95],a[92:95]
RD a[224:227],v224 offset:57344
CV v15,v46,v47
MM a[84:87],a[172:175],v[84:87],a[84:87]
EX v51,v51
MM a[84:87],a[168:171],v[80:83],a[84:87]
EX v52,v52
MM a[76:79],a[168:171],v[72:75],a[76:79]
buffer_load_dwordx4 v235, s[12:15], s81 offen offset:0 lds
MM a[76:79],a[172:175],v[76:79],a[76:79]
EX v53,v53
MM a[68:71],a[172:175],v[68:71],a[68:71]
RD a[228:231],v225 offset:57344
CV v20,v48,v49
MM a[68:71],a[168:171],v[64:67],a[68:71]
EX v54,v54
MM a[96:99],a[176:179],v[64:67],a[96:99]
RD a[232:235],v226 offset:57344
CV v21,v50,v51
MM a[96:99],a[180:183],v[68:71],a[96:99]
EX v55,v55
MM a[104:107],a[180:183],v[76:79],a[104:107]
RD a[236:239],v227 offset:57344
)ASM"
R"ASM(
CV v22,v52,v53
MM a[104:107],a[176:179],v[72:75],a[104:107]
EX v56,v56
MM a[112:115],a[176:179],v[80:83],a[112:115]
buffer_load_dwordx4 v235, s[12:15], s64 offen offset:1024 lds
MM a[112:115],a[180:183],v[84:87],a[112:115]
EX v57,v57
MM a[120:123],a[180:183],v[92:95],a[120:123]
EX v58,v58
MM a[120:123],a[176:179],v[88:91],a[120:123]
RD a[240:243],v224 offset:61440
CV v23,v54,v55
MM a[124:127],a[184:187],v[88:91],a[124:127]
EX v59,v59
MM a[124:127],a[188:191],v[92:95],a[124:127]
RD a[244:247],v225 offset:61440
CV v28,v56,v57
MM a[116:119],a[188:191],v[84:87],a[116:119]
EX v60,v60
MM a[116:119],a[184:187],v[80:83],a[116:119]
buffer_load_dwordx4 v235, s[12:15], s81 offen offset:2048 lds
MM a[108:111],a[184:187],v[72:75],a[108:111]
EX v61,v61
MM a[108:111],a[188:191],v[76:79],a[108:111]
RD a[248:251],v226 offset:61440
CV v29,v58,v59
MM a[100:103],a[188:191],v[68:71],a[100:103]
EX v62,v62
MM a[100:103],a[184:187],v[64:67],a[100:103]
EX v63,v63
v_smfmac_f32_16x16x64_bf16 v[144:147], v[236:239], v[0:7], v234
RD a[252:255],v227 offset:61440
v_smfmac_f32_16x16x64_bf16 v[148:151], v[236:239], v[8:15], v234
CV v30,v60,v61
v_smfmac_f32_16x16x64_bf16 v[152:155], v[236:239], v[16:23], v234
CV v31,v62,v63
s_nop 1
v_smfmac_f32_16x16x64_bf16 v[156:159], v[236:239], v[24:31], v234
s_waitcnt lgkmcnt(0)
buffer_load_dwordx4 v235, s[12:15], s64 offen offset:3072 lds
MM v[64:67],a[192:195],v[160:163],v[128:131]
s_add_u32 s64, s64, 16384
TR a[128:129],v232 offset:32768
s_add_u32 s81, s64, 896
MM v[64:67],a[196:199],v[176:179],v[64:67]
TR a[130:131],v232 offset:36864
TR a[132:133],v232 offset:40960
MM v[64:67],a[200:203],v[192:195],v[64:67]
v_max3_f32 v249, v144, v145, v148
v_max3_f32 v249, v249, v149, v152
MM v[64:67],a[204:207],v[208:211],v[64:67]
v_max3_f32 v249, v249, v153, v156
v_max_f32 v249, v249, v157
MM v[68:71],a[220:223],v[208:211],v[128:131]
TR a[134:135],v232 offset:45056
v_cmp_lt_f32 vcc, s71, v249
MM v[68:71],a[216:219],v[192:195],v[68:71]
s_cbranch_vccnz LSITE14_%=
LBACK14_%=:
TR a[136:137],v233 offset:32768
v_add_f32 v244, v144, v145
MM v[68:71],a[212:215],v[176:179],v[68:71]
s_add_u32 s48, s48, 16
v_add_f32 v245, v148, v149
v_add_f32 v246, v152, v153
MM v[68:71],a[208:211],v[160:163],v[68:71]
v_add_f32 v247, v156, v157
v_lshrrev_b32 v252, 6, v248
MM v[76:79],a[208:211],v[164:167],v[132:135]
v_mul_u32_u24 v252, 3456, v252
v_and_b32 v251, 48, v248
MM v[76:79],a[212:215],v[180:183],v[76:79]
TR a[138:139],v233 offset:36864
v_add_u32 v252, v252, v251
MM v[76:79],a[216:219],v[196:199],v[76:79]
s_and_b32 s90, %[swave], 1
EX v64,v64
MM v[76:79],a[220:223],v[212:215],v[76:79]
s_mul_i32 s90, s90, 276480
EX v65,v65
MM v[72:75],a[204:207],v[212:215],v[132:135]
s_bfe_u32 s91, s48, 131077
TR a[140:141],v233 offset:40960
CV v64,v64,v65
MM v[72:75],a[200:203],v[196:199],v[72:75]
s_mul_i32 s91, s91, 552960
EX v66,v66
MM v[72:75],a[196:199],v[180:183],v[72:75]
s_add_u32 s90, s90, s91
TR a[142:143],v233 offset:45056
TR a[144:145],v232 offset:33280
MM v[72:75],a[192:195],v[164:167],v[72:75]
s_and_b32 s91, s48, 0xffffff80
EX v67,v67
MM v[80:83],a[192:195],v[168:171],v[136:139]
s_add_u32 s90, s90, s91
EX v68,v68
MM v[80:83],a[196:199],v[184:187],v[80:83]
s_and_b32 s91, s48, 16
TR a[146:147],v232 offset:37376
CV v65,v66,v67
MM v[80:83],a[200:203],v[200:203],v[80:83]
s_lshl_b32 s91, s91, 2
EX v69,v69
MM v[80:83],a[204:207],v[216:219],v[80:83]
s_add_u32 s66, s90, s91
TR a[148:149],v232 offset:41472
CV v66,v68,v69
MM v[84:87],a[220:223],v[216:219],v[136:139]
s_mul_i32 s91, %[swave], 5120
EX v70,v70
MM v[84:87],a[216:219],v[200:203],v[84:87]
s_add_u32 s91, s91, %[slds]
TR a[150:151],v232 offset:45568
TR a[152:153],v233 offset:33280
MM v[84:87],a[212:215],v[184:187],v[84:87]
s_add_u32 s67, s91, 131072
EX v71,v71
MM v[84:87],a[208:211],v[168:171],v[84:87]
s_and_b32 s18, s17, 255
TR a[154:155],v233 offset:37376
CV v67,v70,v71
MM v[92:95],a[208:211],v[172:175],v[140:143]
s_sub_u32 s90, s18, 40
s_cmp_ge_u32 s18, 216
s_cselect_b32 s18, s90, s18
MM v[92:95],a[212:215],v[188:191],v[92:95]
s_lshl_b32 s18, s18, 4
EX v72,v72
MM v[92:95],a[216:219],v[204:207],v[92:95]
s_sub_u32 s90, s16, 90
EX v73,v73
MM v[92:95],a[220:223],v[220:223],v[92:95]
s_min_u32 s47, s90, s16
TR a[156:157],v233 offset:41472
TR a[158:159],v233 offset:45568
MM v[88:91],a[204:207],v[220:223],v[140:143]
s_mul_i32 s90, s47, 205
EX v74,v74
MM v[88:91],a[200:203],v[204:207],v[88:91]
s_lshr_b32 s46, s90, 11
TR a[160:161],v232 offset:33792
CV v72,v72,v73
MM v[88:91],a[196:199],v[188:191],v[88:91]
s_mul_i32 s90, s46, 244480
EX v75,v75
MM v[88:91],a[192:195],v[172:175],v[88:91]
s_lshl_b32 s91, s47, 7
EX v76,v76
MM v[96:99],a[224:227],v[160:163],v[128:131]
s_add_u32 s90, s90, s91
s_add_u32 s28, s28, s90
s_addc_u32 s29, s29, 0
MM v[96:99],a[228:231],v[176:179],v[96:99]
s_sub_u32 s30, 2211840, s90
TR a[162:163],v232 offset:37888
CV v73,v74,v75
MM v[96:99],a[232:235],v[192:195],v[96:99]
s_mov_b32 s31, 131072
EX v77,v77
MM v[96:99],a[236:239],v[208:211],v[96:99]
TR a[164:165],v232 offset:41984
TR a[166:167],v232 offset:46080
MM v[100:103],a[252:255],v[208:211],v[128:131]
EX v78,v78
MM v[100:103],a[248:251],v[192:195],v[100:103]
EX v79,v79
MM v[100:103],a[244:247],v[176:179],v[100:103]
TR a[168:169],v233 offset:33792
CV v74,v76,v77
MM v[100:103],a[240:243],v[160:163],v[100:103]
EX v80,v80
MM v[108:111],a[240:243],v[164:167],v[132:135]
TR a[170:171],v233 offset:37888
CV v75,v78,v79
MM v[108:111],a[244:247],v[180:183],v[108:111]
EX v81,v81
MM v[108:111],a[248:251],v[196:199],v[108:111]
TR a[172:173],v233 offset:41984
TR a[174:175],v233 offset:46080
MM v[108:111],a[252:255],v[212:215],v[108:111]
EX v82,v82
MM v[104:107],a[236:239],v[212:215],v[132:135]
TR a[176:177],v232 offset:34304
CV v80,v80,v81
MM v[104:107],a[232:235],v[196:199],v[104:107]
EX v83,v83
MM v[104:107],a[228:231],v[180:183],v[104:107]
TR a[178:179],v232 offset:38400
CV v81,v82,v83
MM v[104:107],a[224:227],v[164:167],v[104:107]
EX v84,v84
MM v[112:115],a[224:227],v[168:171],v[136:139]
EX v85,v85
MM v[112:115],a[228:231],v[184:187],v[112:115]
TR a[180:181],v232 offset:42496
TR a[182:183],v232 offset:46592
MM v[112:115],a[232:235],v[200:203],v[112:115]
EX v86,v86
MM v[112:115],a[236:239],v[216:219],v[112:115]
TR a[184:185],v233 offset:34304
CV v82,v84,v85
MM v[116:119],a[252:255],v[216:219],v[136:139]
EX v87,v87
MM v[116:119],a[248:251],v[200:203],v[116:119]
TR a[186:187],v233 offset:38400
CV v83,v86,v87
MM v[116:119],a[244:247],v[184:187],v[116:119]
EX v88,v88
MM v[116:119],a[240:243],v[168:171],v[116:119]
TR a[188:189],v233 offset:42496
TR a[190:191],v233 offset:46592
MM v[124:127],a[240:243],v[172:175],v[140:143]
EX v89,v89
MM v[124:127],a[244:247],v[188:191],v[124:127]
EX v90,v90
MM v[124:127],a[248:251],v[204:207],v[124:127]
EX v91,v91
MM v[124:127],a[252:255],v[220:223],v[124:127]
EX v92,v92
MM v[120:123],a[236:239],v[220:223],v[140:143]
EX v93,v93
MM v[120:123],a[232:235],v[204:207],v[120:123]
EX v94,v94
MM v[120:123],a[228:231],v[188:191],v[120:123]
EX v95,v95
MM v[120:123],a[224:227],v[172:175],v[120:123]
s_waitcnt vmcnt(14) lgkmcnt(0)
s_barrier
MM a[0:3],a[128:131],v[0:3],a[0:3]
s_add_u32 m0, s65, 32768
CV v88,v88,v89
CV v89,v90,v91
MM a[0:3],a[132:135],v[4:7],a[0:3]
RD a[192:195],v224 offset:0
CV v90,v92,v93
MM a[8:11],a[132:135],v[12:15],a[8:11]
RD a[196:199],v225 offset:0
CV v91,v94,v95
MM a[8:11],a[128:131],v[8:11],a[8:11]
buffer_load_dwordx4 v228, s[8:11], s64 offen offset:0 lds
MM a[16:19],a[128:131],v[16:19],a[16:19]
EX v96,v96
MM a[16:19],a[132:135],v[20:23],a[16:19]
EX v97,v97
MM a[24:27],a[132:135],v[28:31],a[24:27]
RD a[200:203],v226 offset:0
CV v68,v96,v97
MM a[24:27],a[128:131],v[24:27],a[24:27]
LPG12_%=:
s_cbranch_execz LTR0_%=
EX v98,v98
MM a[28:31],a[136:139],v[24:27],a[28:31]
buffer_load_dwordx4 v229, s[8:11], s64 offen offset:1024 lds
MM a[28:31],a[140:143],v[28:31],a[28:31]
EX v99,v99
MM a[20:23],a[140:143],v[20:23],a[20:23]
RD a[204:207],v227 offset:0
CV v69,v98,v99
MM a[20:23],a[136:139],v[16:19],a[20:23]
EX v100,v100
MM a[12:15],a[136:139],v[8:11],a[12:15]
EX v101,v101
MM a[12:15],a[140:143],v[12:15],a[12:15]
RD a[208:211],v224 offset:4096
CV v70,v100,v101
MM a[4:7],a[140:143],v[4:7],a[4:7]
EX v102,v102
MM a[4:7],a[136:139],v[0:3],a[4:7]
buffer_load_dwordx4 v230, s[8:11], s64 offen offset:2048 lds
MM a[32:35],a[144:147],v[0:3],a[32:35]
EX v103,v103
MM a[32:35],a[148:151],v[4:7],a[32:35]
RD a[212:215],v225 offset:4096
CV v71,v102,v103
MM a[40:43],a[148:151],v[12:15],a[40:43]
EX v104,v104
MM a[40:43],a[144:147],v[8:11],a[40:43]
RD a[216:219],v226 offset:4096
RD a[220:223],v227 offset:4096
MM a[48:51],a[144:147],v[16:19],a[48:51]
EX v105,v105
MM a[48:51],a[148:151],v[20:23],a[48:51]
RD a[224:227],v224 offset:8192
CV v76,v104,v105
MM a[56:59],a[148:151],v[28:31],a[56:59]
EX v106,v106
MM a[56:59],a[144:147],v[24:27],a[56:59]
EX v107,v107
MM a[60:63],a[152:155],v[24:27],a[60:63]
RD a[228:231],v225 offset:8192
CV v77,v106,v107
MM a[60:63],a[156:159],v[28:31],a[60:63]
buffer_load_dwordx4 v231, s[8:11], s64 offen offset:3072 lds
MM a[52:55],a[156:159],v[20:23],a[52:55]
s_add_u32 m0, s65, 98304
EX v108,v108
MM a[52:55],a[152:155],v[16:19],a[52:55]
EX v109,v109
MM a[44:47],a[152:155],v[8:11],a[44:47]
RD a[232:235],v226 offset:8192
CV v78,v108,v109
MM a[44:47],a[156:159],v[12:15],a[44:47]
EX v110,v110
MM a[36:39],a[156:159],v[4:7],a[36:39]
buffer_load_dwordx4 v235, s[12:15], s81 offen offset:0 lds
MM a[36:39],a[152:155],v[0:3],a[36:39]
EX v111,v111
MM a[64:67],a[160:163],v[0:3],a[64:67]
RD a[236:239],v227 offset:8192
CV v79,v110,v111
MM a[64:67],a[164:167],v[4:7],a[64:67]
EX v112,v112
MM a[72:75],a[164:167],v[12:15],a[72:75]
EX v113,v113
MM a[72:75],a[160:163],v[8:11],a[72:75]
RD a[240:243],v224 offset:12288
CV v84,v112,v113
MM a[80:83],a[160:163],v[16:19],a[80:83]
s_waitcnt vmcnt(9)
v_mov_b32 v250, s69
s_mov_b64 exec, 1
buffer_store_dword v250, off, s[20:23], s97 offset:0 sc1
s_mov_b64 exec, -1
MM a[80:83],a[164:167],v[20:23],a[80:83]
s_add_u32 m0, s67, 0
s_add_u32 s85, s66, 0
buffer_load_dwordx4 v252, s[36:39], s85 offen offset:0 lds
MM a[88:91],a[164:167],v[28:31],a[88:91]
EX v114,v114
MM a[88:91],a[160:163],v[24:27],a[88:91]
EX v115,v115
MM a[92:95],a[168:171],v[24:27],a[92:95]
RD a[244:247],v225 offset:12288
CV v85,v114,v115
MM a[92:95],a[172:175],v[28:31],a[92:95]
EX v116,v116
MM a[84:87],a[172:175],v[20:23],a[84:87]
s_add_u32 m0, s65, 98304
s_nop 0
buffer_load_dwordx4 v235, s[12:15], s64 offen offset:1024 lds
MM a[84:87],a[168:171],v[16:19],a[84:87]
EX v117,v117
MM a[76:79],a[168:171],v[8:11],a[76:79]
RD a[248:251],v226 offset:12288
CV v86,v116,v117
MM a[76:79],a[172:175],v[12:15],a[76:79]
EX v118,v118
MM a[68:71],a[172:175],v[4:7],a[68:71]
EX v119,v119
MM a[68:71],a[168:171],v[0:3],a[68:71]
RD a[252:255],v227 offset:12288
CV v87,v118,v119
MM a[96:99],a[176:179],v[0:3],a[96:99]
EX v120,v120
MM a[96:99],a[180:183],v[4:7],a[96:99]
buffer_load_dwordx4 v235, s[12:15], s81 offen offset:2048 lds
MM a[104:107],a[180:183],v[12:15],a[104:107]
EX v121,v121
MM a[104:107],a[176:179],v[8:11],a[104:107]
EX v122,v122
MM a[112:115],a[176:179],v[16:19],a[112:115]
EX v123,v123
MM a[112:115],a[180:183],v[20:23],a[112:115]
buffer_load_dwordx4 v235, s[12:15], s64 offen offset:3072 lds
MM a[120:123],a[180:183],v[28:31],a[120:123]
EX v124,v124
MM a[120:123],a[176:179],v[24:27],a[120:123]
EX v125,v125
MM a[124:127],a[184:187],v[24:27],a[124:127]
EX v126,v126
MM a[124:127],a[188:191],v[28:31],a[124:127]
EX v127,v127
MM a[116:119],a[188:191],v[20:23],a[116:119]
CV v92,v120,v121
CV v93,v122,v123
MM a[116:119],a[184:187],v[16:19],a[116:119]
CV v94,v124,v125
CV v95,v126,v127
MM a[108:111],a[184:187],v[8:11],a[108:111]
MM a[108:111],a[188:191],v[12:15],a[108:111]
MM a[100:103],a[188:191],v[4:7],a[100:103]
MM a[100:103],a[184:187],v[0:3],a[100:103]
v_smfmac_f32_16x16x64_bf16 v[144:147], v[236:239], v[64:71], v234
v_smfmac_f32_16x16x64_bf16 v[148:151], v[236:239], v[72:79], v234
v_smfmac_f32_16x16x64_bf16 v[152:155], v[236:239], v[80:87], v234
v_smfmac_f32_16x16x64_bf16 v[156:159], v[236:239], v[88:95], v234
)ASM"
R"ASM(
s_waitcnt lgkmcnt(0)
MM v[0:3],a[192:195],v[160:163],v[128:131]
s_add_u32 s64, s64, 16384
TR a[128:129],v232 offset:49152
s_add_u32 s81, s64, 896
MM v[0:3],a[196:199],v[176:179],v[0:3]
TR a[130:131],v232 offset:53248
TR a[132:133],v232 offset:57344
MM v[0:3],a[200:203],v[192:195],v[0:3]
v_max3_f32 v249, v144, v145, v148
v_max3_f32 v249, v249, v149, v152
MM v[0:3],a[204:207],v[208:211],v[0:3]
v_max3_f32 v249, v249, v153, v156
v_max_f32 v249, v249, v157
MM v[4:7],a[220:223],v[208:211],v[128:131]
TR a[134:135],v232 offset:61440
v_cmp_lt_f32 vcc, s71, v249
MM v[4:7],a[216:219],v[192:195],v[4:7]
s_cbranch_vccnz LSITE15_%=
LBACK15_%=:
TR a[136:137],v233 offset:49152
v_add_f32 v244, v144, v145
MM v[4:7],a[212:215],v[176:179],v[4:7]
v_add_f32 v245, v148, v149
v_add_f32 v246, v152, v153
MM v[4:7],a[208:211],v[160:163],v[4:7]
TR a[138:139],v233 offset:53248
v_add_f32 v247, v156, v157
MM v[12:15],a[208:211],v[164:167],v[132:135]
s_add_u32 m0, s67, 1024
s_add_u32 s85, s66, 55296
buffer_load_dwordx4 v252, s[36:39], s85 offen offset:0 lds
MM v[12:15],a[212:215],v[180:183],v[12:15]
EX v0,v0
MM v[12:15],a[216:219],v[196:199],v[12:15]
EX v1,v1
MM v[12:15],a[220:223],v[212:215],v[12:15]
TR a[140:141],v233 offset:57344
CV v0,v0,v1
MM v[8:11],a[204:207],v[212:215],v[132:135]
EX v2,v2
MM v[8:11],a[200:203],v[196:199],v[8:11]
TR a[142:143],v233 offset:61440
TR a[144:145],v232 offset:49664
MM v[8:11],a[196:199],v[180:183],v[8:11]
EX v3,v3
MM v[8:11],a[192:195],v[164:167],v[8:11]
TR a[146:147],v232 offset:53760
CV v1,v2,v3
MM v[16:19],a[192:195],v[168:171],v[136:139]
EX v4,v4
MM v[16:19],a[196:199],v[184:187],v[16:19]
TR a[148:149],v232 offset:57856
TR a[150:151],v232 offset:61952
MM v[16:19],a[200:203],v[200:203],v[16:19]
EX v5,v5
MM v[16:19],a[204:207],v[216:219],v[16:19]
TR a[152:153],v233 offset:49664
CV v2,v4,v5
MM v[20:23],a[220:223],v[216:219],v[136:139]
EX v6,v6
MM v[20:23],a[216:219],v[200:203],v[20:23]
EX v7,v7
MM v[20:23],a[212:215],v[184:187],v[20:23]
TR a[154:155],v233 offset:53760
CV v3,v6,v7
MM v[20:23],a[208:211],v[168:171],v[20:23]
EX v8,v8
MM v[28:31],a[208:211],v[172:175],v[140:143]
TR a[156:157],v233 offset:57856
TR a[158:159],v233 offset:61952
MM v[28:31],a[212:215],v[188:191],v[28:31]
EX v9,v9
MM v[28:31],a[216:219],v[204:207],v[28:31]
TR a[160:161],v232 offset:50176
CV v8,v8,v9
MM v[28:31],a[220:223],v[220:223],v[28:31]
EX v10,v10
MM v[24:27],a[204:207],v[220:223],v[140:143]
EX v11,v11
MM v[24:27],a[200:203],v[204:207],v[24:27]
TR a[162:163],v232 offset:54272
CV v9,v10,v11
MM v[24:27],a[196:199],v[188:191],v[24:27]
EX v12,v12
MM v[24:27],a[192:195],v[172:175],v[24:27]
TR a[164:165],v232 offset:58368
TR a[166:167],v232 offset:62464
MM v[32:35],a[224:227],v[160:163],v[128:131]
EX v13,v13
MM v[32:35],a[228:231],v[176:179],v[32:35]
TR a[168:169],v233 offset:50176
CV v10,v12,v13
MM v[32:35],a[232:235],v[192:195],v[32:35]
EX v14,v14
MM v[32:35],a[236:239],v[208:211],v[32:35]
EX v15,v15
MM v[36:39],a[252:255],v[208:211],v[128:131]
TR a[170:171],v233 offset:54272
CV v11,v14,v15
MM v[36:39],a[248:251],v[192:195],v[36:39]
EX v16,v16
MM v[36:39],a[244:247],v[176:179],v[36:39]
TR a[172:173],v233 offset:58368
TR a[174:175],v233 offset:62464
MM v[36:39],a[240:243],v[160:163],v[36:39]
EX v17,v17
MM v[44:47],a[240:243],v[164:167],v[132:135]
s_add_u32 m0, s67, 2048
s_add_u32 s85, s66, 110592
buffer_load_dwordx4 v252, s[36:39], s85 offen offset:0 lds
MM v[44:47],a[244:247],v[180:183],v[44:47]
TR a[176:177],v232 offset:50688
CV v16,v16,v17
MM v[44:47],a[248:251],v[196:199],v[44:47]
EX v18,v18
MM v[44:47],a[252:255],v[212:215],v[44:47]
EX v19,v19
MM v[40:43],a[236:239],v[212:215],v[132:135]
TR a[178:179],v232 offset:54784
CV v17,v18,v19
MM v[40:43],a[232:235],v[196:199],v[40:43]
EX v20,v20
MM v[40:43],a[228:231],v[180:183],v[40:43]
TR a[180:181],v232 offset:58880
TR a[182:183],v232 offset:62976
MM v[40:43],a[224:227],v[164:167],v[40:43]
EX v21,v21
MM v[48:51],a[224:227],v[168:171],v[136:139]
TR a[184:185],v233 offset:50688
CV v18,v20,v21
MM v[48:51],a[228:231],v[184:187],v[48:51]
EX v22,v22
MM v[48:51],a[232:235],v[200:203],v[48:51]
EX v23,v23
MM v[48:51],a[236:239],v[216:219],v[48:51]
TR a[186:187],v233 offset:54784
CV v19,v22,v23
MM v[52:55],a[252:255],v[216:219],v[136:139]
EX v24,v24
MM v[52:55],a[248:251],v[200:203],v[52:55]
TR a[188:189],v233 offset:58880
TR a[190:191],v233 offset:62976
MM v[52:55],a[244:247],v[184:187],v[52:55]
EX v25,v25
MM v[52:55],a[240:243],v[168:171],v[52:55]
EX v26,v26
MM v[60:63],a[240:243],v[172:175],v[140:143]
EX v27,v27
MM v[60:63],a[244:247],v[188:191],v[60:63]
EX v28,v28
MM v[60:63],a[248:251],v[204:207],v[60:63]
EX v29,v29
MM v[60:63],a[252:255],v[220:223],v[60:63]
EX v30,v30
MM v[56:59],a[236:239],v[220:223],v[140:143]
EX v31,v31
MM v[56:59],a[232:235],v[204:207],v[56:59]
EX v32,v32
MM v[56:59],a[228:231],v[188:191],v[56:59]
EX v33,v33
MM v[56:59],a[224:227],v[172:175],v[56:59]
s_waitcnt lgkmcnt(0)
s_barrier
MM a[0:3],a[128:131],v[64:67],a[0:3]
s_add_u32 m0, s65, 49152
CV v24,v24,v25
CV v25,v26,v27
MM a[0:3],a[132:135],v[68:71],a[0:3]
RD a[192:195],v224 offset:16384
CV v26,v28,v29
MM a[8:11],a[132:135],v[76:79],a[8:11]
RD a[196:199],v225 offset:16384
CV v27,v30,v31
MM a[8:11],a[128:131],v[72:75],a[8:11]
buffer_load_dwordx4 v228, s[8:11], s64 offen offset:0 lds
MM a[16:19],a[128:131],v[80:83],a[16:19]
s_add_u32 m0, s67, 3072
s_add_u32 s85, s66, 165888
buffer_load_dwordx4 v252, s[36:39], s85 offen offset:0 lds
MM a[16:19],a[132:135],v[84:87],a[16:19]
RD a[200:203],v226 offset:16384
CV v4,v32,v33
MM a[24:27],a[132:135],v[92:95],a[24:27]
EX v34,v34
MM a[24:27],a[128:131],v[88:91],a[24:27]
s_add_u32 m0, s65, 49152
s_nop 0
buffer_load_dwordx4 v229, s[8:11], s64 offen offset:1024 lds
MM a[28:31],a[136:139],v[88:91],a[28:31]
RD a[204:207],v227 offset:16384
RD a[208:211],v224 offset:20480
MM a[28:31],a[140:143],v[92:95],a[28:31]
EX v35,v35
MM a[20:23],a[140:143],v[84:87],a[20:23]
RD a[212:215],v225 offset:20480
CV v5,v34,v35
MM a[20:23],a[136:139],v[80:83],a[20:23]
EX v36,v36
MM a[12:15],a[136:139],v[72:75],a[12:15]
EX v37,v37
MM a[12:15],a[140:143],v[76:79],a[12:15]
RD a[216:219],v226 offset:20480
CV v6,v36,v37
MM a[4:7],a[140:143],v[68:71],a[4:7]
EX v38,v38
MM a[4:7],a[136:139],v[64:67],a[4:7]
buffer_load_dwordx4 v230, s[8:11], s64 offen offset:2048 lds
MM a[32:35],a[144:147],v[64:67],a[32:35]
EX v39,v39
MM a[32:35],a[148:151],v[68:71],a[32:35]
RD a[220:223],v227 offset:20480
CV v7,v38,v39
MM a[40:43],a[148:151],v[76:79],a[40:43]
EX v40,v40
MM a[40:43],a[144:147],v[72:75],a[40:43]
EX v41,v41
MM a[48:51],a[144:147],v[80:83],a[48:51]
RD a[224:227],v224 offset:24576
CV v12,v40,v41
MM a[48:51],a[148:151],v[84:87],a[48:51]
EX v42,v42
MM a[56:59],a[148:151],v[92:95],a[56:59]
buffer_load_dwordx4 v231, s[8:11], s64 offen offset:3072 lds
MM a[56:59],a[144:147],v[88:91],a[56:59]
s_add_u32 m0, s65, 114688
EX v43,v43
MM a[60:63],a[152:155],v[88:91],a[60:63]
RD a[228:231],v225 offset:24576
CV v13,v42,v43
MM a[60:63],a[156:159],v[92:95],a[60:63]
EX v44,v44
MM a[52:55],a[156:159],v[84:87],a[52:55]
EX v45,v45
MM a[52:55],a[152:155],v[80:83],a[52:55]
RD a[232:235],v226 offset:24576
CV v14,v44,v45
MM a[44:47],a[152:155],v[72:75],a[44:47]
EX v46,v46
MM a[44:47],a[156:159],v[76:79],a[44:47]
buffer_load_dwordx4 v235, s[12:15], s81 offen offset:0 lds
MM a[36:39],a[156:159],v[68:71],a[36:39]
EX v47,v47
MM a[36:39],a[152:155],v[64:67],a[36:39]
RD a[236:239],v227 offset:24576
CV v15,v46,v47
MM a[64:67],a[160:163],v[64:67],a[64:67]
EX v48,v48
MM a[64:67],a[164:167],v[68:71],a[64:67]
EX v49,v49
MM a[72:75],a[164:167],v[76:79],a[72:75]
RD a[240:243],v224 offset:28672
CV v20,v48,v49
MM a[72:75],a[160:163],v[72:75],a[72:75]
buffer_load_dwordx4 v235, s[12:15], s64 offen offset:1024 lds
MM a[80:83],a[160:163],v[80:83],a[80:83]
EX v50,v50
MM a[80:83],a[164:167],v[84:87],a[80:83]
EX v51,v51
MM a[88:91],a[164:167],v[92:95],a[88:91]
RD a[244:247],v225 offset:28672
CV v21,v50,v51
MM a[88:91],a[160:163],v[88:91],a[88:91]
EX v52,v52
MM a[92:95],a[168:171],v[88:91],a[92:95]
EX v53,v53
MM a[92:95],a[172:175],v[92:95],a[92:95]
RD a[248:251],v226 offset:28672
CV v22,v52,v53
MM a[84:87],a[172:175],v[84:87],a[84:87]
buffer_load_dwordx4 v235, s[12:15], s81 offen offset:2048 lds
MM a[84:87],a[168:171],v[80:83],a[84:87]
EX v54,v54
MM a[76:79],a[168:171],v[72:75],a[76:79]
EX v55,v55
MM a[76:79],a[172:175],v[76:79],a[76:79]
RD a[252:255],v227 offset:28672
CV v23,v54,v55
MM a[68:71],a[172:175],v[68:71],a[68:71]
EX v56,v56
MM a[68:71],a[168:171],v[64:67],a[68:71]
EX v57,v57
MM a[96:99],a[176:179],v[64:67],a[96:99]
buffer_load_dwordx4 v235, s[12:15], s64 offen offset:3072 lds
MM a[96:99],a[180:183],v[68:71],a[96:99]
EX v58,v58
MM a[104:107],a[180:183],v[76:79],a[104:107]
EX v59,v59
MM a[104:107],a[176:179],v[72:75],a[104:107]
EX v60,v60
MM a[112:115],a[176:179],v[80:83],a[112:115]
EX v61,v61
MM a[112:115],a[180:183],v[84:87],a[112:115]
EX v62,v62
MM a[120:123],a[180:183],v[92:95],a[120:123]
s_add_u32 m0, s67, 4096
s_add_u32 s85, s66, 221184
buffer_load_dwordx4 v252, s[36:39], s85 offen offset:0 lds
MM a[120:123],a[176:179],v[88:91],a[120:123]
EX v63,v63
MM a[124:127],a[184:187],v[88:91],a[124:127]
CV v28,v56,v57
CV v29,v58,v59
MM a[124:127],a[188:191],v[92:95],a[124:127]
CV v30,v60,v61
CV v31,v62,v63
MM a[116:119],a[188:191],v[84:87],a[116:119]
MM a[116:119],a[184:187],v[80:83],a[116:119]
MM a[108:111],a[184:187],v[72:75],a[108:111]
MM a[108:111],a[188:191],v[76:79],a[108:111]
MM a[100:103],a[188:191],v[68:71],a[100:103]
MM a[100:103],a[184:187],v[64:67],a[100:103]
v_smfmac_f32_16x16x64_bf16 v[144:147], v[236:239], v[0:7], v234
v_smfmac_f32_16x16x64_bf16 v[148:151], v[236:239], v[8:15], v234
v_smfmac_f32_16x16x64_bf16 v[152:155], v[236:239], v[16:23], v234
v_smfmac_f32_16x16x64_bf16 v[156:159], v[236:239], v[24:31], v234
s_waitcnt lgkmcnt(0)
MM v[64:67],a[192:195],v[160:163],v[128:131]
s_add_u32 s64, s64, 16384
TR a[128:129],v232 offset:0
s_add_u32 s81, s64, 896
MM v[64:67],a[196:199],v[176:179],v[64:67]
TR a[130:131],v232 offset:4096
TR a[132:133],v232 offset:8192
MM v[64:67],a[200:203],v[192:195],v[64:67]
v_max3_f32 v249, v144, v145, v148
v_max3_f32 v249, v249, v149, v152
MM v[64:67],a[204:207],v[208:211],v[64:67]
v_max3_f32 v249, v249, v153, v156
v_max_f32 v249, v249, v157
MM v[68:71],a[220:223],v[208:211],v[128:131]
TR a[134:135],v232 offset:12288
v_cmp_lt_f32 vcc, s71, v249
MM v[68:71],a[216:219],v[192:195],v[68:71]
s_cbranch_vccnz LSITE16_%=
LBACK16_%=:
TR a[136:137],v233 offset:0
v_add_f32 v244, v144, v145
MM v[68:71],a[212:215],v[176:179],v[68:71]
v_add_f32 v245, v148, v149
v_add_f32 v246, v152, v153
MM v[68:71],a[208:211],v[160:163],v[68:71]
TR a[138:139],v233 offset:4096
v_add_f32 v247, v156, v157
MM v[76:79],a[208:211],v[164:167],v[132:135]
EX v64,v64
MM v[76:79],a[212:215],v[180:183],v[76:79]
TR a[140:141],v233 offset:8192
TR a[142:143],v233 offset:12288
MM v[76:79],a[216:219],v[196:199],v[76:79]
EX v65,v65
MM v[76:79],a[220:223],v[212:215],v[76:79]
TR a[144:145],v232 offset:512
CV v64,v64,v65
MM v[72:75],a[204:207],v[212:215],v[132:135]
EX v66,v66
MM v[72:75],a[200:203],v[196:199],v[72:75]
EX v67,v67
MM v[72:75],a[196:199],v[180:183],v[72:75]
TR a[146:147],v232 offset:4608
CV v65,v66,v67
MM v[72:75],a[192:195],v[164:167],v[72:75]
EX v68,v68
MM v[80:83],a[192:195],v[168:171],v[136:139]
TR a[148:149],v232 offset:8704
TR a[150:151],v232 offset:12800
MM v[80:83],a[196:199],v[184:187],v[80:83]
EX v69,v69
MM v[80:83],a[200:203],v[200:203],v[80:83]
TR a[152:153],v233 offset:512
CV v66,v68,v69
MM v[80:83],a[204:207],v[216:219],v[80:83]
EX v70,v70
MM v[84:87],a[220:223],v[216:219],v[136:139]
s_add_u32 s87, s95, 1024
buffer_store_dword v240, v248, s[20:23], s87 offen offset:12 sc1
MM v[84:87],a[216:219],v[200:203],v[84:87]
EX v71,v71
MM v[84:87],a[212:215],v[184:187],v[84:87]
TR a[154:155],v233 offset:4608
CV v67,v70,v71
MM v[84:87],a[208:211],v[168:171],v[84:87]
EX v72,v72
MM v[92:95],a[208:211],v[172:175],v[140:143]
TR a[156:157],v233 offset:8704
TR a[158:159],v233 offset:12800
MM v[92:95],a[212:215],v[188:191],v[92:95]
)ASM"
R"ASM(
EX v73,v73
MM v[92:95],a[216:219],v[204:207],v[92:95]
TR a[160:161],v232 offset:1024
CV v72,v72,v73
MM v[92:95],a[220:223],v[220:223],v[92:95]
EX v74,v74
MM v[88:91],a[204:207],v[220:223],v[140:143]
EX v75,v75
MM v[88:91],a[200:203],v[204:207],v[88:91]
TR a[162:163],v232 offset:5120
CV v73,v74,v75
MM v[88:91],a[196:199],v[188:191],v[88:91]
EX v76,v76
MM v[88:91],a[192:195],v[172:175],v[88:91]
TR a[164:165],v232 offset:9216
TR a[166:167],v232 offset:13312
MM v[96:99],a[224:227],v[160:163],v[128:131]
EX v77,v77
MM v[96:99],a[228:231],v[176:179],v[96:99]
TR a[168:169],v233 offset:1024
CV v74,v76,v77
MM v[96:99],a[232:235],v[192:195],v[96:99]
EX v78,v78
MM v[96:99],a[236:239],v[208:211],v[96:99]
EX v79,v79
MM v[100:103],a[252:255],v[208:211],v[128:131]
TR a[170:171],v233 offset:5120
CV v75,v78,v79
MM v[100:103],a[248:251],v[192:195],v[100:103]
EX v80,v80
MM v[100:103],a[244:247],v[176:179],v[100:103]
TR a[172:173],v233 offset:9216
TR a[174:175],v233 offset:13312
MM v[100:103],a[240:243],v[160:163],v[100:103]
EX v81,v81
MM v[108:111],a[240:243],v[164:167],v[132:135]
TR a[176:177],v232 offset:1536
CV v80,v80,v81
MM v[108:111],a[244:247],v[180:183],v[108:111]
EX v82,v82
MM v[108:111],a[248:251],v[196:199],v[108:111]
TR a[178:179],v232 offset:5632
TR a[180:181],v232 offset:9728
MM v[108:111],a[252:255],v[212:215],v[108:111]
EX v83,v83
MM v[104:107],a[236:239],v[212:215],v[132:135]
TR a[182:183],v232 offset:13824
CV v81,v82,v83
MM v[104:107],a[232:235],v[196:199],v[104:107]
EX v84,v84
MM v[104:107],a[228:231],v[180:183],v[104:107]
EX v85,v85
MM v[104:107],a[224:227],v[164:167],v[104:107]
TR a[184:185],v233 offset:1536
CV v82,v84,v85
MM v[112:115],a[224:227],v[168:171],v[136:139]
EX v86,v86
MM v[112:115],a[228:231],v[184:187],v[112:115]
TR a[186:187],v233 offset:5632
TR a[188:189],v233 offset:9728
MM v[112:115],a[232:235],v[200:203],v[112:115]
EX v87,v87
s_nop 0
LPG13_%=:
s_cbranch_execz LTR2_%=
MM v[112:115],a[236:239],v[216:219],v[112:115]
TR a[190:191],v233 offset:13824
CV v83,v86,v87
MM v[116:119],a[252:255],v[216:219],v[136:139]
EX v88,v88
MM v[116:119],a[248:251],v[200:203],v[116:119]
EX v89,v89
MM v[116:119],a[244:247],v[184:187],v[116:119]
EX v90,v90
MM v[116:119],a[240:243],v[168:171],v[116:119]
EX v91,v91
MM v[124:127],a[240:243],v[172:175],v[140:143]
EX v92,v92
MM v[124:127],a[244:247],v[188:191],v[124:127]
EX v93,v93
MM v[124:127],a[248:251],v[204:207],v[124:127]
EX v94,v94
MM v[124:127],a[252:255],v[220:223],v[124:127]
EX v95,v95
MM v[120:123],a[236:239],v[220:223],v[140:143]
EX v96,v96
MM v[120:123],a[232:235],v[204:207],v[120:123]
EX v97,v97
MM v[120:123],a[228:231],v[188:191],v[120:123]
EX v98,v98
MM v[120:123],a[224:227],v[172:175],v[120:123]
s_waitcnt vmcnt(19) lgkmcnt(0)
s_barrier
MM a[0:3],a[128:131],v[0:3],a[0:3]
s_add_u32 m0, s65, 0
CV v88,v88,v89
CV v89,v90,v91
MM a[0:3],a[132:135],v[4:7],a[0:3]
RD a[192:195],v224 offset:32768
CV v90,v92,v93
MM a[8:11],a[132:135],v[12:15],a[8:11]
RD a[196:199],v225 offset:32768
CV v91,v94,v95
MM a[8:11],a[128:131],v[8:11],a[8:11]
buffer_load_dwordx4 v228, s[8:11], s64 offen offset:0 lds
MM a[16:19],a[128:131],v[16:19],a[16:19]
RD a[200:203],v226 offset:32768
CV v68,v96,v97
MM a[16:19],a[132:135],v[20:23],a[16:19]
buffer_load_dwordx4 v229, s[8:11], s64 offen offset:1024 lds
MM a[24:27],a[132:135],v[28:31],a[24:27]
RD a[204:207],v227 offset:32768
RD a[208:211],v224 offset:36864
MM a[24:27],a[128:131],v[24:27],a[24:27]
EX v99,v99
MM a[28:31],a[136:139],v[24:27],a[28:31]
RD a[212:215],v225 offset:36864
CV v69,v98,v99
MM a[28:31],a[140:143],v[28:31],a[28:31]
EX v100,v100
MM a[20:23],a[140:143],v[20:23],a[20:23]
buffer_load_dwordx4 v230, s[8:11], s64 offen offset:2048 lds
MM a[20:23],a[136:139],v[16:19],a[20:23]
EX v101,v101
MM a[12:15],a[136:139],v[8:11],a[12:15]
RD a[216:219],v226 offset:36864
CV v70,v100,v101
MM a[12:15],a[140:143],v[12:15],a[12:15]
EX v102,v102
MM a[4:7],a[140:143],v[4:7],a[4:7]
EX v103,v103
MM a[4:7],a[136:139],v[0:3],a[4:7]
RD a[220:223],v227 offset:36864
CV v71,v102,v103
MM a[32:35],a[144:147],v[0:3],a[32:35]
EX v104,v104
MM a[32:35],a[148:151],v[4:7],a[32:35]
buffer_load_dwordx4 v231, s[8:11], s64 offen offset:3072 lds
MM a[40:43],a[148:151],v[12:15],a[40:43]
s_add_u32 m0, s65, 65536
EX v105,v105
MM a[40:43],a[144:147],v[8:11],a[40:43]
RD a[224:227],v224 offset:40960
CV v76,v104,v105
MM a[48:51],a[144:147],v[16:19],a[48:51]
EX v106,v106
MM a[48:51],a[148:151],v[20:23],a[48:51]
EX v107,v107
MM a[56:59],a[148:151],v[28:31],a[56:59]
RD a[228:231],v225 offset:40960
CV v77,v106,v107
MM a[56:59],a[144:147],v[24:27],a[56:59]
EX v108,v108
MM a[60:63],a[152:155],v[24:27],a[60:63]
buffer_load_dwordx4 v235, s[12:15], s81 offen offset:0 lds
MM a[60:63],a[156:159],v[28:31],a[60:63]
EX v109,v109
MM a[52:55],a[156:159],v[20:23],a[52:55]
s_add_u32 s87, s95, 2048
buffer_store_dword v240, v248, s[20:23], s87 offen offset:12 sc1
MM a[52:55],a[152:155],v[16:19],a[52:55]
RD a[232:235],v226 offset:40960
CV v78,v108,v109
MM a[44:47],a[152:155],v[8:11],a[44:47]
EX v110,v110
MM a[44:47],a[156:159],v[12:15],a[44:47]
EX v111,v111
MM a[36:39],a[156:159],v[4:7],a[36:39]
RD a[236:239],v227 offset:40960
CV v79,v110,v111
MM a[36:39],a[152:155],v[0:3],a[36:39]
EX v112,v112
MM a[64:67],a[160:163],v[0:3],a[64:67]
buffer_load_dwordx4 v235, s[12:15], s64 offen offset:1024 lds
MM a[64:67],a[164:167],v[4:7],a[64:67]
EX v113,v113
MM a[72:75],a[164:167],v[12:15],a[72:75]
RD a[240:243],v224 offset:45056
CV v84,v112,v113
MM a[72:75],a[160:163],v[8:11],a[72:75]
EX v114,v114
MM a[80:83],a[160:163],v[16:19],a[80:83]
EX v115,v115
MM a[80:83],a[164:167],v[20:23],a[80:83]
RD a[244:247],v225 offset:45056
CV v85,v114,v115
MM a[88:91],a[164:167],v[28:31],a[88:91]
EX v116,v116
MM a[88:91],a[160:163],v[24:27],a[88:91]
buffer_load_dwordx4 v235, s[12:15], s81 offen offset:2048 lds
MM a[92:95],a[168:171],v[24:27],a[92:95]
EX v117,v117
MM a[92:95],a[172:175],v[28:31],a[92:95]
RD a[248:251],v226 offset:45056
CV v86,v116,v117
MM a[84:87],a[172:175],v[20:23],a[84:87]
EX v118,v118
MM a[84:87],a[168:171],v[16:19],a[84:87]
EX v119,v119
MM a[76:79],a[168:171],v[8:11],a[76:79]
RD a[252:255],v227 offset:45056
CV v87,v118,v119
MM a[76:79],a[172:175],v[12:15],a[76:79]
EX v120,v120
MM a[68:71],a[172:175],v[4:7],a[68:71]
buffer_load_dwordx4 v235, s[12:15], s64 offen offset:3072 lds
MM a[68:71],a[168:171],v[0:3],a[68:71]
EX v121,v121
MM a[96:99],a[176:179],v[0:3],a[96:99]
EX v122,v122
MM a[96:99],a[180:183],v[4:7],a[96:99]
EX v123,v123
MM a[104:107],a[180:183],v[12:15],a[104:107]
EX v124,v124
MM a[104:107],a[176:179],v[8:11],a[104:107]
EX v125,v125
MM a[112:115],a[176:179],v[16:19],a[112:115]
EX v126,v126
MM a[112:115],a[180:183],v[20:23],a[112:115]
EX v127,v127
MM a[120:123],a[180:183],v[28:31],a[120:123]
CV v92,v120,v121
CV v93,v122,v123
MM a[120:123],a[176:179],v[24:27],a[120:123]
CV v94,v124,v125
CV v95,v126,v127
MM a[124:127],a[184:187],v[24:27],a[124:127]
MM a[124:127],a[188:191],v[28:31],a[124:127]
MM a[116:119],a[188:191],v[20:23],a[116:119]
MM a[116:119],a[184:187],v[16:19],a[116:119]
MM a[108:111],a[184:187],v[8:11],a[108:111]
MM a[108:111],a[188:191],v[12:15],a[108:111]
MM a[100:103],a[188:191],v[4:7],a[100:103]
MM a[100:103],a[184:187],v[0:3],a[100:103]
v_smfmac_f32_16x16x64_bf16 v[144:147], v[236:239], v[64:71], v234
v_smfmac_f32_16x16x64_bf16 v[148:151], v[236:239], v[72:79], v234
v_smfmac_f32_16x16x64_bf16 v[152:155], v[236:239], v[80:87], v234
v_smfmac_f32_16x16x64_bf16 v[156:159], v[236:239], v[88:95], v234
s_waitcnt lgkmcnt(0)
MM v[0:3],a[192:195],v[160:163],v[128:131]
s_add_u32 s64, s64, 16384
TR a[128:129],v232 offset:16384
s_add_u32 s81, s64, 896
MM v[0:3],a[196:199],v[176:179],v[0:3]
TR a[130:131],v232 offset:20480
TR a[132:133],v232 offset:24576
MM v[0:3],a[200:203],v[192:195],v[0:3]
v_max3_f32 v249, v144, v145, v148
v_max3_f32 v249, v249, v149, v152
MM v[0:3],a[204:207],v[208:211],v[0:3]
v_max3_f32 v249, v249, v153, v156
v_max_f32 v249, v249, v157
MM v[4:7],a[220:223],v[208:211],v[128:131]
TR a[134:135],v232 offset:28672
v_cmp_lt_f32 vcc, s71, v249
MM v[4:7],a[216:219],v[192:195],v[4:7]
s_cbranch_vccnz LSITE17_%=
LBACK17_%=:
TR a[136:137],v233 offset:16384
v_add_f32 v244, v144, v145
MM v[4:7],a[212:215],v[176:179],v[4:7]
v_add_f32 v245, v148, v149
v_add_f32 v246, v152, v153
MM v[4:7],a[208:211],v[160:163],v[4:7]
v_add_f32 v247, v156, v157
v_lshrrev_b32 v127, 8, v248
MM v[12:15],a[208:211],v[164:167],v[132:135]
v_lshlrev_b32 v124, 9, v127
v_and_b32 v126, 192, v248
MM v[12:15],a[212:215],v[180:183],v[12:15]
v_add_u32 v124, v124, v126
v_bfe_u32 v126, v248, 4, 2
MM v[12:15],a[216:219],v[196:199],v[12:15]
TR a[138:139],v233 offset:20480
v_lshl_add_u32 v124, v126, 3, v124
MM v[12:15],a[220:223],v[212:215],v[12:15]
s_lshr_b32 s76, %[swave], 1
EX v0,v0
MM v[8:11],a[204:207],v[212:215],v[132:135]
s_mul_i32 s76, s76, 10240
EX v1,v1
MM v[8:11],a[200:203],v[196:199],v[8:11]
s_and_b32 s77, %[swave], 1
TR a[140:141],v233 offset:24576
TR a[142:143],v233 offset:28672
MM v[8:11],a[196:199],v[180:183],v[8:11]
s_lshl_b32 s77, s77, 5
EX v2,v2
MM v[8:11],a[192:195],v[164:167],v[8:11]
s_add_u32 s76, s76, s77
EX v3,v3
MM v[16:19],a[192:195],v[168:171],v[136:139]
s_add_u32 s76, s76, %[slds]
TR a[144:145],v232 offset:16896
CV v0,v0,v1
MM v[16:19],a[196:199],v[184:187],v[16:19]
s_add_u32 s76, s76, 131072
v_add_u32 v124, s76, v124
v_bfe_u32 v126, v248, 4, 3
MM v[16:19],a[200:203],v[200:203],v[16:19]
v_lshl_add_u32 v126, v127, 3, v126
v_lshlrev_b32 v126, 4, v126
MM v[16:19],a[204:207],v[216:219],v[16:19]
s_mul_i32 s76, %[swave], 3072
EX v4,v4
MM v[20:23],a[220:223],v[216:219],v[136:139]
s_add_u32 s76, s76, %[slds]
EX v5,v5
MM v[20:23],a[216:219],v[200:203],v[20:23]
s_add_u32 s76, s76, 151552
TR a[146:147],v232 offset:20992
v_add_u32 v125, s76, v126
MM v[20:23],a[212:215],v[184:187],v[20:23]
s_lshr_b32 s76, s48, 7
EX v6,v6
MM v[20:23],a[208:211],v[168:171],v[20:23]
s_lshl_b32 s76, s76, 5
TR a[148:149],v232 offset:25088
CV v1,v2,v3
MM v[28:31],a[208:211],v[172:175],v[140:143]
s_and_b32 s77, s48, 16
EX v7,v7
MM v[28:31],a[212:215],v[188:191],v[28:31]
s_add_u32 s76, s76, s77
TR a[150:151],v232 offset:29184
TR a[152:153],v233 offset:16896
MM v[28:31],a[216:219],v[204:207],v[28:31]
s_bfe_u32 s77, s48, 131077
EX v8,v8
MM v[28:31],a[220:223],v[220:223],v[28:31]
s_lshl_b32 s77, s77, 2
EX v9,v9
MM v[24:27],a[204:207],v[220:223],v[140:143]
s_add_u32 s76, s76, s77
TR a[154:155],v233 offset:20992
CV v2,v4,v5
MM v[24:27],a[200:203],v[204:207],v[24:27]
s_add_u32 s76, s76, %[swave]
EX v10,v10
MM v[24:27],a[196:199],v[188:191],v[24:27]
s_lshl_b32 s97, s76, 2
TR a[156:157],v233 offset:25088
CV v3,v6,v7
MM v[24:27],a[192:195],v[172:175],v[24:27]
s_add_u32 s97, s97, 77824
EX v11,v11
MM v[32:35],a[224:227],v[160:163],v[128:131]
s_lshl_b32 s76, s76, 9
TR a[158:159],v233 offset:29184
TR a[160:161],v232 offset:17408
MM v[32:35],a[228:231],v[176:179],v[32:35]
s_add_u32 s76, s76, 22183936
EX v12,v12
MM v[32:35],a[232:235],v[192:195],v[32:35]
TR a[162:163],v232 offset:21504
CV v8,v8,v9
MM v[32:35],a[236:239],v[208:211],v[32:35]
EX v13,v13
MM v[36:39],a[252:255],v[208:211],v[128:131]
TR a[164:165],v232 offset:25600
CV v9,v10,v11
MM v[36:39],a[248:251],v[192:195],v[36:39]
EX v14,v14
MM v[36:39],a[244:247],v[176:179],v[36:39]
EX v15,v15
MM v[36:39],a[240:243],v[160:163],v[36:39]
TR a[166:167],v232 offset:29696
TR a[168:169],v233 offset:17408
MM v[44:47],a[240:243],v[164:167],v[132:135]
EX v16,v16
MM v[44:47],a[244:247],v[180:183],v[44:47]
TR a[170:171],v233 offset:21504
CV v10,v12,v13
MM v[44:47],a[248:251],v[196:199],v[44:47]
EX v17,v17
MM v[44:47],a[252:255],v[212:215],v[44:47]
TR a[172:173],v233 offset:25600
CV v11,v14,v15
MM v[40:43],a[236:239],v[212:215],v[132:135]
EX v18,v18
MM v[40:43],a[232:235],v[196:199],v[40:43]
TR a[174:175],v233 offset:29696
TR a[176:177],v232 offset:17920
MM v[40:43],a[228:231],v[180:183],v[40:43]
EX v19,v19
MM v[40:43],a[224:227],v[164:167],v[40:43]
EX v20,v20
MM v[48:51],a[224:227],v[168:171],v[136:139]
TR a[178:179],v232 offset:22016
CV v16,v16,v17
MM v[48:51],a[228:231],v[184:187],v[48:51]
)ASM"
R"ASM(
EX v21,v21
MM v[48:51],a[232:235],v[200:203],v[48:51]
TR a[180:181],v232 offset:26112
CV v17,v18,v19
MM v[48:51],a[236:239],v[216:219],v[48:51]
EX v22,v22
MM v[52:55],a[252:255],v[216:219],v[136:139]
TR a[182:183],v232 offset:30208
CV v18,v20,v21
MM v[52:55],a[248:251],v[200:203],v[52:55]
EX v23,v23
MM v[52:55],a[244:247],v[184:187],v[52:55]
TR a[184:185],v233 offset:17920
TR a[186:187],v233 offset:22016
MM v[52:55],a[240:243],v[168:171],v[52:55]
EX v24,v24
MM v[60:63],a[240:243],v[172:175],v[140:143]
EX v25,v25
MM v[60:63],a[244:247],v[188:191],v[60:63]
TR a[188:189],v233 offset:26112
CV v19,v22,v23
MM v[60:63],a[248:251],v[204:207],v[60:63]
TR a[190:191],v233 offset:30208
CV v24,v24,v25
MM v[60:63],a[252:255],v[220:223],v[60:63]
EX v26,v26
MM v[56:59],a[236:239],v[220:223],v[140:143]
EX v27,v27
MM v[56:59],a[232:235],v[204:207],v[56:59]
EX v28,v28
MM v[56:59],a[228:231],v[188:191],v[56:59]
EX v29,v29
MM v[56:59],a[224:227],v[172:175],v[56:59]
s_waitcnt vmcnt(10) lgkmcnt(0)
s_barrier
MM a[0:3],a[128:131],v[64:67],a[0:3]
s_add_u32 m0, s65, 16384
CV v25,v26,v27
CV v26,v28,v29
MM a[0:3],a[132:135],v[68:71],a[0:3]
EX v30,v30
MM a[8:11],a[132:135],v[76:79],a[8:11]
EX v31,v31
MM a[8:11],a[128:131],v[72:75],a[8:11]
RD a[192:195],v224 offset:49152
CV v27,v30,v31
MM a[16:19],a[128:131],v[80:83],a[16:19]
TR v[96:97],v124 offset:0
TR v[98:99],v124 offset:256
MM a[16:19],a[132:135],v[84:87],a[16:19]
RD v[108:111],v125 offset:0
TR v[100:101],v124 offset:2048
MM a[24:27],a[132:135],v[92:95],a[24:27]
TR v[102:103],v124 offset:2304
RD v[112:115],v125 offset:512
MM a[24:27],a[128:131],v[88:91],a[24:27]
TR v[104:105],v124 offset:4096
TR v[106:107],v124 offset:4352
MM a[28:31],a[136:139],v[88:91],a[28:31]
RD v[116:119],v125 offset:1024
RD a[196:199],v225 offset:49152
EX v32,v32
MM a[28:31],a[140:143],v[92:95],a[28:31]
EX v33,v33
MM a[20:23],a[140:143],v[84:87],a[20:23]
EX v34,v34
MM a[20:23],a[136:139],v[80:83],a[20:23]
buffer_load_dwordx4 v228, s[8:11], s64 offen offset:0 lds
MM a[12:15],a[136:139],v[72:75],a[12:15]
EX v35,v35
MM a[12:15],a[140:143],v[76:79],a[12:15]
EX v36,v36
MM a[4:7],a[140:143],v[68:71],a[4:7]
RD a[200:203],v226 offset:49152
CV v4,v32,v33
MM a[4:7],a[136:139],v[64:67],a[4:7]
EX v37,v37
MM a[32:35],a[144:147],v[64:67],a[32:35]
RD a[204:207],v227 offset:49152
CV v5,v34,v35
MM a[32:35],a[148:151],v[68:71],a[32:35]
s_waitcnt lgkmcnt(3)
EX v38,v38
MM a[40:43],a[148:151],v[76:79],a[40:43]
buffer_load_dwordx4 v229, s[8:11], s64 offen offset:1024 lds
MM v[120:123],v[96:99],v[108:111],0
MM v[120:123],v[100:103],v[112:115],v[120:123]
MM v[120:123],v[104:107],v[116:119],v[120:123]
MM a[40:43],a[144:147],v[72:75],a[40:43]
TR v[96:97],v124 offset:6144
TR v[98:99],v124 offset:6400
MM a[48:51],a[144:147],v[80:83],a[48:51]
RD v[108:111],v125 offset:1536
TR v[100:101],v124 offset:8192
EX v39,v39
MM a[48:51],a[148:151],v[84:87],a[48:51]
TR v[102:103],v124 offset:8448
RD v[112:115],v125 offset:2048
MM a[56:59],a[148:151],v[92:95],a[56:59]
EX v40,v40
MM a[56:59],a[144:147],v[88:91],a[56:59]
EX v41,v41
MM a[60:63],a[152:155],v[88:91],a[60:63]
CV v6,v36,v37
CV v7,v38,v39
MM a[60:63],a[156:159],v[92:95],a[60:63]
EX v42,v42
MM a[52:55],a[156:159],v[84:87],a[52:55]
RD a[208:211],v224 offset:53248
RD a[212:215],v225 offset:53248
MM a[52:55],a[152:155],v[80:83],a[52:55]
EX v43,v43
MM a[44:47],a[152:155],v[72:75],a[44:47]
buffer_load_dwordx4 v230, s[8:11], s64 offen offset:2048 lds
MM a[44:47],a[156:159],v[76:79],a[44:47]
EX v44,v44
MM a[36:39],a[156:159],v[68:71],a[36:39]
s_waitcnt lgkmcnt(2)
EX v45,v45
MM a[36:39],a[152:155],v[64:67],a[36:39]
CV v12,v40,v41
CV v13,v42,v43
MM v[120:123],v[96:99],v[108:111],v[120:123]
MM v[120:123],v[100:103],v[112:115],v[120:123]
MM a[64:67],a[160:163],v[64:67],a[64:67]
EX v46,v46
MM a[64:67],a[164:167],v[68:71],a[64:67]
RD a[216:219],v226 offset:53248
CV v14,v44,v45
MM a[72:75],a[164:167],v[76:79],a[72:75]
EX v47,v47
MM a[72:75],a[160:163],v[72:75],a[72:75]
buffer_load_dwordx4 v231, s[8:11], s64 offen offset:3072 lds
MM a[80:83],a[160:163],v[80:83],a[80:83]
s_add_u32 m0, s65, 81920
EX v48,v48
MM a[80:83],a[164:167],v[84:87],a[80:83]
RD a[220:223],v227 offset:53248
CV v15,v46,v47
MM a[88:91],a[164:167],v[92:95],a[88:91]
s_mov_b32 exec_lo, 0x00ff00ff
s_mov_b32 exec_hi, 0x00ff00ff
buffer_store_dwordx4 v[120:123], v126, s[20:23], s76 offen offset:0 sc1
s_mov_b64 exec, -1
s_nop 0
MM a[88:91],a[160:163],v[88:91],a[88:91]
EX v49,v49
MM a[92:95],a[168:171],v[88:91],a[92:95]
EX v50,v50
MM a[92:95],a[172:175],v[92:95],a[92:95]
EX v51,v51
MM a[84:87],a[172:175],v[84:87],a[84:87]
RD a[224:227],v224 offset:57344
CV v20,v48,v49
MM a[84:87],a[168:171],v[80:83],a[84:87]
EX v52,v52
MM a[76:79],a[168:171],v[72:75],a[76:79]
buffer_load_dwordx4 v235, s[12:15], s81 offen offset:0 lds
MM a[76:79],a[172:175],v[76:79],a[76:79]
EX v53,v53
MM a[68:71],a[172:175],v[68:71],a[68:71]
RD a[228:231],v225 offset:57344
CV v21,v50,v51
MM a[68:71],a[168:171],v[64:67],a[68:71]
EX v54,v54
MM a[96:99],a[176:179],v[64:67],a[96:99]
RD a[232:235],v226 offset:57344
CV v22,v52,v53
MM a[96:99],a[180:183],v[68:71],a[96:99]
EX v55,v55
MM a[104:107],a[180:183],v[76:79],a[104:107]
EX v56,v56
MM a[104:107],a[176:179],v[72:75],a[104:107]
buffer_load_dwordx4 v235, s[12:15], s64 offen offset:1024 lds
MM a[112:115],a[176:179],v[80:83],a[112:115]
EX v57,v57
MM a[112:115],a[180:183],v[84:87],a[112:115]
RD a[236:239],v227 offset:57344
CV v23,v54,v55
MM a[120:123],a[180:183],v[92:95],a[120:123]
EX v58,v58
MM a[120:123],a[176:179],v[88:91],a[120:123]
RD a[240:243],v224 offset:61440
CV v28,v56,v57
MM a[124:127],a[184:187],v[88:91],a[124:127]
EX v59,v59
MM a[124:127],a[188:191],v[92:95],a[124:127]
buffer_load_dwordx4 v235, s[12:15], s81 offen offset:2048 lds
MM a[116:119],a[188:191],v[84:87],a[116:119]
EX v60,v60
MM a[116:119],a[184:187],v[80:83],a[116:119]
RD a[244:247],v225 offset:61440
CV v29,v58,v59
MM a[108:111],a[184:187],v[72:75],a[108:111]
EX v61,v61
MM a[108:111],a[188:191],v[76:79],a[108:111]
EX v62,v62
MM a[100:103],a[188:191],v[68:71],a[100:103]
RD a[248:251],v226 offset:61440
CV v30,v60,v61
MM a[100:103],a[184:187],v[64:67],a[100:103]
EX v63,v63
v_smfmac_f32_16x16x64_bf16 v[144:147], v[236:239], v[0:7], v234
RD a[252:255],v227 offset:61440
v_smfmac_f32_16x16x64_bf16 v[148:151], v[236:239], v[8:15], v234
CV v31,v62,v63
v_smfmac_f32_16x16x64_bf16 v[152:155], v[236:239], v[16:23], v234
s_nop 0
v_smfmac_f32_16x16x64_bf16 v[156:159], v[236:239], v[24:31], v234
s_waitcnt lgkmcnt(0)
buffer_load_dwordx4 v235, s[12:15], s64 offen offset:3072 lds
MM v[64:67],a[192:195],v[160:163],v[128:131]
s_add_u32 s64, s64, 16384
TR a[128:129],v232 offset:32768
s_add_u32 s81, s64, 896
MM v[64:67],a[196:199],v[176:179],v[64:67]
TR a[130:131],v232 offset:36864
TR a[132:133],v232 offset:40960
MM v[64:67],a[200:203],v[192:195],v[64:67]
v_max3_f32 v249, v144, v145, v148
v_max3_f32 v249, v249, v149, v152
MM v[64:67],a[204:207],v[208:211],v[64:67]
v_max3_f32 v249, v249, v153, v156
v_max_f32 v249, v249, v157
MM v[68:71],a[220:223],v[208:211],v[128:131]
TR a[134:135],v232 offset:45056
v_cmp_lt_f32 vcc, s71, v249
MM v[68:71],a[216:219],v[192:195],v[68:71]
s_cbranch_vccnz LSITE18_%=
LBACK18_%=:
TR a[136:137],v233 offset:32768
v_add_f32 v244, v144, v145
MM v[68:71],a[212:215],v[176:179],v[68:71]
v_add_f32 v245, v148, v149
v_add_f32 v246, v152, v153
MM v[68:71],a[208:211],v[160:163],v[68:71]
TR a[138:139],v233 offset:36864
v_add_f32 v247, v156, v157
MM v[76:79],a[208:211],v[164:167],v[132:135]
EX v64,v64
MM v[76:79],a[212:215],v[180:183],v[76:79]
TR a[140:141],v233 offset:40960
TR a[142:143],v233 offset:45056
MM v[76:79],a[216:219],v[196:199],v[76:79]
EX v65,v65
MM v[76:79],a[220:223],v[212:215],v[76:79]
TR a[144:145],v232 offset:33280
CV v64,v64,v65
MM v[72:75],a[204:207],v[212:215],v[132:135]
EX v66,v66
MM v[72:75],a[200:203],v[196:199],v[72:75]
EX v67,v67
MM v[72:75],a[196:199],v[180:183],v[72:75]
TR a[146:147],v232 offset:37376
CV v65,v66,v67
MM v[72:75],a[192:195],v[164:167],v[72:75]
EX v68,v68
MM v[80:83],a[192:195],v[168:171],v[136:139]
TR a[148:149],v232 offset:41472
TR a[150:151],v232 offset:45568
MM v[80:83],a[196:199],v[184:187],v[80:83]
EX v69,v69
MM v[80:83],a[200:203],v[200:203],v[80:83]
TR a[152:153],v233 offset:33280
CV v66,v68,v69
MM v[80:83],a[204:207],v[216:219],v[80:83]
LPG14_%=:
s_cbranch_execz LTR2_%=
EX v70,v70
MM v[84:87],a[220:223],v[216:219],v[136:139]
EX v71,v71
MM v[84:87],a[216:219],v[200:203],v[84:87]
TR a[154:155],v233 offset:37376
CV v67,v70,v71
MM v[84:87],a[212:215],v[184:187],v[84:87]
EX v72,v72
MM v[84:87],a[208:211],v[168:171],v[84:87]
TR a[156:157],v233 offset:41472
TR a[158:159],v233 offset:45568
MM v[92:95],a[208:211],v[172:175],v[140:143]
EX v73,v73
MM v[92:95],a[212:215],v[188:191],v[92:95]
TR a[160:161],v232 offset:33792
CV v72,v72,v73
MM v[92:95],a[216:219],v[204:207],v[92:95]
EX v74,v74
MM v[92:95],a[220:223],v[220:223],v[92:95]
EX v75,v75
MM v[88:91],a[204:207],v[220:223],v[140:143]
TR a[162:163],v232 offset:37888
CV v73,v74,v75
MM v[88:91],a[200:203],v[204:207],v[88:91]
TR a[164:165],v232 offset:41984
TR a[166:167],v232 offset:46080
MM v[88:91],a[196:199],v[188:191],v[88:91]
EX v76,v76
MM v[88:91],a[192:195],v[172:175],v[88:91]
EX v77,v77
MM v[96:99],a[224:227],v[160:163],v[128:131]
TR a[168:169],v233 offset:33792
CV v74,v76,v77
MM v[96:99],a[228:231],v[176:179],v[96:99]
EX v78,v78
MM v[96:99],a[232:235],v[192:195],v[96:99]
TR a[170:171],v233 offset:37888
TR a[172:173],v233 offset:41984
MM v[96:99],a[236:239],v[208:211],v[96:99]
EX v79,v79
MM v[100:103],a[252:255],v[208:211],v[128:131]
TR a[174:175],v233 offset:46080
CV v75,v78,v79
MM v[100:103],a[248:251],v[192:195],v[100:103]
EX v80,v80
MM v[100:103],a[244:247],v[176:179],v[100:103]
EX v81,v81
MM v[100:103],a[240:243],v[160:163],v[100:103]
TR a[176:177],v232 offset:34304
CV v80,v80,v81
MM v[108:111],a[240:243],v[164:167],v[132:135]
v_lshrrev_b32 v253, 7, v248
v_mul_u32_u24 v253, 1280, v253
MM v[108:111],a[244:247],v[180:183],v[108:111]
v_and_b32 v252, 112, v248
v_add_u32 v253, v253, v252
MM v[108:111],a[248:251],v[196:199],v[108:111]
s_mul_i32 s90, %[swave], 61440
EX v82,v82
MM v[108:111],a[252:255],v[212:215],v[108:111]
s_mul_i32 s91, %[swave], 6144
EX v83,v83
MM v[104:107],a[236:239],v[212:215],v[132:135]
s_add_u32 s91, s91, %[slds]
TR a[178:179],v232 offset:38400
CV v81,v82,v83
MM v[104:107],a[232:235],v[196:199],v[104:107]
EX v84,v84
MM v[104:107],a[228:231],v[180:183],v[104:107]
TR a[180:181],v232 offset:42496
TR a[182:183],v232 offset:46592
MM v[104:107],a[224:227],v[164:167],v[104:107]
EX v85,v85
MM v[112:115],a[224:227],v[168:171],v[136:139]
TR a[184:185],v233 offset:34304
CV v82,v84,v85
MM v[112:115],a[228:231],v[184:187],v[112:115]
EX v86,v86
MM v[112:115],a[232:235],v[200:203],v[112:115]
EX v87,v87
MM v[112:115],a[236:239],v[216:219],v[112:115]
TR a[186:187],v233 offset:38400
CV v83,v86,v87
MM v[116:119],a[252:255],v[216:219],v[136:139]
EX v88,v88
MM v[116:119],a[248:251],v[200:203],v[116:119]
TR a[188:189],v233 offset:42496
TR a[190:191],v233 offset:46592
MM v[116:119],a[244:247],v[184:187],v[116:119]
EX v89,v89
MM v[116:119],a[240:243],v[168:171],v[116:119]
EX v90,v90
MM v[124:127],a[240:243],v[172:175],v[140:143]
EX v91,v91
MM v[124:127],a[244:247],v[188:191],v[124:127]
EX v92,v92
MM v[124:127],a[248:251],v[204:207],v[124:127]
EX v93,v93
MM v[124:127],a[252:255],v[220:223],v[124:127]
EX v94,v94
MM v[120:123],a[236:239],v[220:223],v[140:143]
EX v95,v95
MM v[120:123],a[232:235],v[204:207],v[120:123]
EX v96,v96
MM v[120:123],a[228:231],v[188:191],v[120:123]
EX v97,v97
MM v[120:123],a[224:227],v[172:175],v[120:123]
s_waitcnt vmcnt(14) lgkmcnt(0)
s_barrier
MM a[0:3],a[128:131],v[0:3],a[0:3]
s_add_u32 m0, s65, 32768
CV v88,v88,v89
CV v89,v90,v91
MM a[0:3],a[132:135],v[4:7],a[0:3]
RD a[192:195],v224 offset:0
CV v90,v92,v93
MM a[8:11],a[132:135],v[12:15],a[8:11]
RD a[196:199],v225 offset:0
CV v91,v94,v95
MM a[8:11],a[128:131],v[8:11],a[8:11]
buffer_load_dwordx4 v228, s[8:11], s64 offen offset:0 lds
MM a[16:19],a[128:131],v[16:19],a[16:19]
)ASM"
R"ASM(
RD a[200:203],v226 offset:0
CV v68,v96,v97
MM a[16:19],a[132:135],v[20:23],a[16:19]
buffer_load_dwordx4 v229, s[8:11], s64 offen offset:1024 lds
MM a[24:27],a[132:135],v[28:31],a[24:27]
EX v98,v98
MM a[24:27],a[128:131],v[24:27],a[24:27]
RD a[204:207],v227 offset:0
RD a[208:211],v224 offset:4096
MM a[28:31],a[136:139],v[24:27],a[28:31]
EX v99,v99
MM a[28:31],a[140:143],v[28:31],a[28:31]
RD a[212:215],v225 offset:4096
CV v69,v98,v99
MM a[20:23],a[140:143],v[20:23],a[20:23]
EX v100,v100
MM a[20:23],a[136:139],v[16:19],a[20:23]
EX v101,v101
MM a[12:15],a[136:139],v[8:11],a[12:15]
RD a[216:219],v226 offset:4096
CV v70,v100,v101
MM a[12:15],a[140:143],v[12:15],a[12:15]
buffer_load_dwordx4 v230, s[8:11], s64 offen offset:2048 lds
MM a[4:7],a[140:143],v[4:7],a[4:7]
EX v102,v102
MM a[4:7],a[136:139],v[0:3],a[4:7]
EX v103,v103
MM a[32:35],a[144:147],v[0:3],a[32:35]
RD a[220:223],v227 offset:4096
CV v71,v102,v103
MM a[32:35],a[148:151],v[4:7],a[32:35]
EX v104,v104
MM a[40:43],a[148:151],v[12:15],a[40:43]
EX v105,v105
MM a[40:43],a[144:147],v[8:11],a[40:43]
RD a[224:227],v224 offset:8192
CV v76,v104,v105
MM a[48:51],a[144:147],v[16:19],a[48:51]
buffer_load_dwordx4 v231, s[8:11], s64 offen offset:3072 lds
MM a[48:51],a[148:151],v[20:23],a[48:51]
s_add_u32 m0, s65, 98304
EX v106,v106
MM a[56:59],a[148:151],v[28:31],a[56:59]
EX v107,v107
MM a[56:59],a[144:147],v[24:27],a[56:59]
RD a[228:231],v225 offset:8192
CV v77,v106,v107
MM a[60:63],a[152:155],v[24:27],a[60:63]
EX v108,v108
MM a[60:63],a[156:159],v[28:31],a[60:63]
buffer_load_dwordx4 v235, s[12:15], s81 offen offset:0 lds
MM a[52:55],a[156:159],v[20:23],a[52:55]
EX v109,v109
MM a[52:55],a[152:155],v[16:19],a[52:55]
RD a[232:235],v226 offset:8192
CV v78,v108,v109
MM a[44:47],a[152:155],v[8:11],a[44:47]
EX v110,v110
MM a[44:47],a[156:159],v[12:15],a[44:47]
EX v111,v111
MM a[36:39],a[156:159],v[4:7],a[36:39]
RD a[236:239],v227 offset:8192
CV v79,v110,v111
MM a[36:39],a[152:155],v[0:3],a[36:39]
EX v112,v112
MM a[64:67],a[160:163],v[0:3],a[64:67]
buffer_load_dwordx4 v235, s[12:15], s64 offen offset:1024 lds
MM a[64:67],a[164:167],v[4:7],a[64:67]
EX v113,v113
MM a[72:75],a[164:167],v[12:15],a[72:75]
RD a[240:243],v224 offset:12288
CV v84,v112,v113
MM a[72:75],a[160:163],v[8:11],a[72:75]
EX v114,v114
MM a[80:83],a[160:163],v[16:19],a[80:83]
s_waitcnt vmcnt(10)
v_mov_b32 v250, s69
s_mov_b64 exec, 1
buffer_store_dword v250, off, s[20:23], s97 offset:0 sc1
s_mov_b64 exec, -1
MM a[80:83],a[164:167],v[20:23],a[80:83]
s_add_u32 m0, s91, 139264
s_add_u32 s77, s90, 0
buffer_load_dwordx4 v253, s[28:31], s77 offen offset:0 lds
MM a[88:91],a[164:167],v[28:31],a[88:91]
EX v115,v115
MM a[88:91],a[160:163],v[24:27],a[88:91]
RD a[244:247],v225 offset:12288
CV v85,v114,v115
MM a[92:95],a[168:171],v[24:27],a[92:95]
EX v116,v116
MM a[92:95],a[172:175],v[28:31],a[92:95]
EX v117,v117
MM a[84:87],a[172:175],v[20:23],a[84:87]
RD a[248:251],v226 offset:12288
CV v86,v116,v117
MM a[84:87],a[168:171],v[16:19],a[84:87]
EX v118,v118
MM a[76:79],a[168:171],v[8:11],a[76:79]
s_add_u32 m0, s65, 98304
s_nop 0
buffer_load_dwordx4 v235, s[12:15], s81 offen offset:2048 lds
MM a[76:79],a[172:175],v[12:15],a[76:79]
EX v119,v119
MM a[68:71],a[172:175],v[4:7],a[68:71]
RD a[252:255],v227 offset:12288
CV v87,v118,v119
MM a[68:71],a[168:171],v[0:3],a[68:71]
EX v120,v120
MM a[96:99],a[176:179],v[0:3],a[96:99]
EX v121,v121
MM a[96:99],a[180:183],v[4:7],a[96:99]
buffer_load_dwordx4 v235, s[12:15], s64 offen offset:3072 lds
MM a[104:107],a[180:183],v[12:15],a[104:107]
EX v122,v122
MM a[104:107],a[176:179],v[8:11],a[104:107]
EX v123,v123
MM a[112:115],a[176:179],v[16:19],a[112:115]
EX v124,v124
MM a[112:115],a[180:183],v[20:23],a[112:115]
EX v125,v125
MM a[120:123],a[180:183],v[28:31],a[120:123]
EX v126,v126
MM a[120:123],a[176:179],v[24:27],a[120:123]
EX v127,v127
MM a[124:127],a[184:187],v[24:27],a[124:127]
CV v92,v120,v121
CV v93,v122,v123
MM a[124:127],a[188:191],v[28:31],a[124:127]
CV v94,v124,v125
CV v95,v126,v127
MM a[116:119],a[188:191],v[20:23],a[116:119]
MM a[116:119],a[184:187],v[16:19],a[116:119]
MM a[108:111],a[184:187],v[8:11],a[108:111]
MM a[108:111],a[188:191],v[12:15],a[108:111]
MM a[100:103],a[188:191],v[4:7],a[100:103]
MM a[100:103],a[184:187],v[0:3],a[100:103]
v_smfmac_f32_16x16x64_bf16 v[144:147], v[236:239], v[64:71], v234
v_smfmac_f32_16x16x64_bf16 v[148:151], v[236:239], v[72:79], v234
v_smfmac_f32_16x16x64_bf16 v[152:155], v[236:239], v[80:87], v234
v_smfmac_f32_16x16x64_bf16 v[156:159], v[236:239], v[88:95], v234
s_waitcnt lgkmcnt(0)
MM v[0:3],a[192:195],v[160:163],v[128:131]
s_add_u32 s64, s64, 16384
TR a[128:129],v232 offset:49152
s_add_u32 s81, s64, 896
MM v[0:3],a[196:199],v[176:179],v[0:3]
TR a[130:131],v232 offset:53248
TR a[132:133],v232 offset:57344
MM v[0:3],a[200:203],v[192:195],v[0:3]
v_max3_f32 v249, v144, v145, v148
v_max3_f32 v249, v249, v149, v152
MM v[0:3],a[204:207],v[208:211],v[0:3]
v_max3_f32 v249, v249, v153, v156
v_max_f32 v249, v249, v157
MM v[4:7],a[220:223],v[208:211],v[128:131]
TR a[134:135],v232 offset:61440
v_cmp_lt_f32 vcc, s71, v249
MM v[4:7],a[216:219],v[192:195],v[4:7]
s_cbranch_vccnz LSITE19_%=
LBACK19_%=:
TR a[136:137],v233 offset:49152
v_add_f32 v244, v144, v145
MM v[4:7],a[212:215],v[176:179],v[4:7]
v_add_f32 v245, v148, v149
v_add_f32 v246, v152, v153
MM v[4:7],a[208:211],v[160:163],v[4:7]
TR a[138:139],v233 offset:53248
v_add_f32 v247, v156, v157
MM v[12:15],a[208:211],v[164:167],v[132:135]
s_add_u32 m0, s91, 140288
s_add_u32 s77, s90, 10240
buffer_load_dwordx4 v253, s[28:31], s77 offen offset:0 lds
MM v[12:15],a[212:215],v[180:183],v[12:15]
EX v0,v0
MM v[12:15],a[216:219],v[196:199],v[12:15]
EX v1,v1
MM v[12:15],a[220:223],v[212:215],v[12:15]
TR a[140:141],v233 offset:57344
CV v0,v0,v1
MM v[8:11],a[204:207],v[212:215],v[132:135]
EX v2,v2
MM v[8:11],a[200:203],v[196:199],v[8:11]
TR a[142:143],v233 offset:61440
TR a[144:145],v232 offset:49664
MM v[8:11],a[196:199],v[180:183],v[8:11]
EX v3,v3
MM v[8:11],a[192:195],v[164:167],v[8:11]
TR a[146:147],v232 offset:53760
CV v1,v2,v3
MM v[16:19],a[192:195],v[168:171],v[136:139]
EX v4,v4
MM v[16:19],a[196:199],v[184:187],v[16:19]
TR a[148:149],v232 offset:57856
TR a[150:151],v232 offset:61952
MM v[16:19],a[200:203],v[200:203],v[16:19]
EX v5,v5
MM v[16:19],a[204:207],v[216:219],v[16:19]
TR a[152:153],v233 offset:49664
CV v2,v4,v5
MM v[20:23],a[220:223],v[216:219],v[136:139]
EX v6,v6
MM v[20:23],a[216:219],v[200:203],v[20:23]
EX v7,v7
MM v[20:23],a[212:215],v[184:187],v[20:23]
TR a[154:155],v233 offset:53760
CV v3,v6,v7
MM v[20:23],a[208:211],v[168:171],v[20:23]
EX v8,v8
MM v[28:31],a[208:211],v[172:175],v[140:143]
TR a[156:157],v233 offset:57856
TR a[158:159],v233 offset:61952
MM v[28:31],a[212:215],v[188:191],v[28:31]
EX v9,v9
MM v[28:31],a[216:219],v[204:207],v[28:31]
TR a[160:161],v232 offset:50176
CV v8,v8,v9
MM v[28:31],a[220:223],v[220:223],v[28:31]
EX v10,v10
MM v[24:27],a[204:207],v[220:223],v[140:143]
EX v11,v11
MM v[24:27],a[200:203],v[204:207],v[24:27]
TR a[162:163],v232 offset:54272
CV v9,v10,v11
MM v[24:27],a[196:199],v[188:191],v[24:27]
EX v12,v12
MM v[24:27],a[192:195],v[172:175],v[24:27]
TR a[164:165],v232 offset:58368
TR a[166:167],v232 offset:62464
MM v[32:35],a[224:227],v[160:163],v[128:131]
EX v13,v13
MM v[32:35],a[228:231],v[176:179],v[32:35]
TR a[168:169],v233 offset:50176
CV v10,v12,v13
MM v[32:35],a[232:235],v[192:195],v[32:35]
EX v14,v14
MM v[32:35],a[236:239],v[208:211],v[32:35]
EX v15,v15
MM v[36:39],a[252:255],v[208:211],v[128:131]
TR a[170:171],v233 offset:54272
CV v11,v14,v15
MM v[36:39],a[248:251],v[192:195],v[36:39]
EX v16,v16
MM v[36:39],a[244:247],v[176:179],v[36:39]
TR a[172:173],v233 offset:58368
TR a[174:175],v233 offset:62464
MM v[36:39],a[240:243],v[160:163],v[36:39]
EX v17,v17
MM v[44:47],a[240:243],v[164:167],v[132:135]
s_add_u32 m0, s91, 141312
s_add_u32 s77, s90, 20480
buffer_load_dwordx4 v253, s[28:31], s77 offen offset:0 lds
MM v[44:47],a[244:247],v[180:183],v[44:47]
TR a[176:177],v232 offset:50688
CV v16,v16,v17
MM v[44:47],a[248:251],v[196:199],v[44:47]
EX v18,v18
MM v[44:47],a[252:255],v[212:215],v[44:47]
EX v19,v19
MM v[40:43],a[236:239],v[212:215],v[132:135]
TR a[178:179],v232 offset:54784
CV v17,v18,v19
MM v[40:43],a[232:235],v[196:199],v[40:43]
EX v20,v20
MM v[40:43],a[228:231],v[180:183],v[40:43]
TR a[180:181],v232 offset:58880
TR a[182:183],v232 offset:62976
MM v[40:43],a[224:227],v[164:167],v[40:43]
EX v21,v21
MM v[48:51],a[224:227],v[168:171],v[136:139]
TR a[184:185],v233 offset:50688
CV v18,v20,v21
MM v[48:51],a[228:231],v[184:187],v[48:51]
EX v22,v22
MM v[48:51],a[232:235],v[200:203],v[48:51]
EX v23,v23
MM v[48:51],a[236:239],v[216:219],v[48:51]
TR a[186:187],v233 offset:54784
CV v19,v22,v23
MM v[52:55],a[252:255],v[216:219],v[136:139]
EX v24,v24
MM v[52:55],a[248:251],v[200:203],v[52:55]
TR a[188:189],v233 offset:58880
TR a[190:191],v233 offset:62976
MM v[52:55],a[244:247],v[184:187],v[52:55]
EX v25,v25
MM v[52:55],a[240:243],v[168:171],v[52:55]
EX v26,v26
MM v[60:63],a[240:243],v[172:175],v[140:143]
EX v27,v27
MM v[60:63],a[244:247],v[188:191],v[60:63]
EX v28,v28
MM v[60:63],a[248:251],v[204:207],v[60:63]
EX v29,v29
MM v[60:63],a[252:255],v[220:223],v[60:63]
EX v30,v30
MM v[56:59],a[236:239],v[220:223],v[140:143]
EX v31,v31
MM v[56:59],a[232:235],v[204:207],v[56:59]
EX v32,v32
MM v[56:59],a[228:231],v[188:191],v[56:59]
EX v33,v33
MM v[56:59],a[224:227],v[172:175],v[56:59]
s_waitcnt lgkmcnt(0)
s_barrier
MM a[0:3],a[128:131],v[64:67],a[0:3]
s_add_u32 m0, s65, 49152
CV v24,v24,v25
CV v25,v26,v27
MM a[0:3],a[132:135],v[68:71],a[0:3]
RD a[192:195],v224 offset:16384
CV v26,v28,v29
MM a[8:11],a[132:135],v[76:79],a[8:11]
RD a[196:199],v225 offset:16384
CV v27,v30,v31
MM a[8:11],a[128:131],v[72:75],a[8:11]
buffer_load_dwordx4 v228, s[8:11], s64 offen offset:0 lds
MM a[16:19],a[128:131],v[80:83],a[16:19]
s_add_u32 m0, s91, 142336
s_add_u32 s77, s90, 30720
buffer_load_dwordx4 v253, s[28:31], s77 offen offset:0 lds
MM a[16:19],a[132:135],v[84:87],a[16:19]
RD a[200:203],v226 offset:16384
CV v4,v32,v33
MM a[24:27],a[132:135],v[92:95],a[24:27]
EX v34,v34
MM a[24:27],a[128:131],v[88:91],a[24:27]
s_add_u32 m0, s65, 49152
s_nop 0
buffer_load_dwordx4 v229, s[8:11], s64 offen offset:1024 lds
MM a[28:31],a[136:139],v[88:91],a[28:31]
RD a[204:207],v227 offset:16384
RD a[208:211],v224 offset:20480
MM a[28:31],a[140:143],v[92:95],a[28:31]
EX v35,v35
MM a[20:23],a[140:143],v[84:87],a[20:23]
RD a[212:215],v225 offset:20480
CV v5,v34,v35
MM a[20:23],a[136:139],v[80:83],a[20:23]
EX v36,v36
MM a[12:15],a[136:139],v[72:75],a[12:15]
EX v37,v37
MM a[12:15],a[140:143],v[76:79],a[12:15]
RD a[216:219],v226 offset:20480
CV v6,v36,v37
MM a[4:7],a[140:143],v[68:71],a[4:7]
EX v38,v38
MM a[4:7],a[136:139],v[64:67],a[4:7]
buffer_load_dwordx4 v230, s[8:11], s64 offen offset:2048 lds
MM a[32:35],a[144:147],v[64:67],a[32:35]
EX v39,v39
MM a[32:35],a[148:151],v[68:71],a[32:35]
RD a[220:223],v227 offset:20480
CV v7,v38,v39
MM a[40:43],a[148:151],v[76:79],a[40:43]
EX v40,v40
MM a[40:43],a[144:147],v[72:75],a[40:43]
EX v41,v41
MM a[48:51],a[144:147],v[80:83],a[48:51]
RD a[224:227],v224 offset:24576
CV v12,v40,v41
MM a[48:51],a[148:151],v[84:87],a[48:51]
EX v42,v42
MM a[56:59],a[148:151],v[92:95],a[56:59]
buffer_load_dwordx4 v231, s[8:11], s64 offen offset:3072 lds
MM a[56:59],a[144:147],v[88:91],a[56:59]
s_add_u32 m0, s65, 114688
EX v43,v43
MM a[60:63],a[152:155],v[88:91],a[60:63]
RD a[228:231],v225 offset:24576
CV v13,v42,v43
MM a[60:63],a[156:159],v[92:95],a[60:63]
EX v44,v44
MM a[52:55],a[156:159],v[84:87],a[52:55]
EX v45,v45
MM a[52:55],a[152:155],v[80:83],a[52:55]
RD a[232:235],v226 offset:24576
CV v14,v44,v45
MM a[44:47],a[152:155],v[72:75],a[44:47]
EX v46,v46
MM a[44:47],a[156:159],v[76:79],a[44:47]
buffer_load_dwordx4 v235, s[12:15], s81 offen offset:0 lds
MM a[36:39],a[156:159],v[68:71],a[36:39]
EX v47,v47
MM a[36:39],a[152:155],v[64:67],a[36:39]
RD a[236:239],v227 offset:24576
CV v15,v46,v47
MM a[64:67],a[160:163],v[64:67],a[64:67]
EX v48,v48
MM a[64:67],a[164:167],v[68:71],a[64:67]
)ASM"
R"ASM(
EX v49,v49
MM a[72:75],a[164:167],v[76:79],a[72:75]
RD a[240:243],v224 offset:28672
CV v20,v48,v49
MM a[72:75],a[160:163],v[72:75],a[72:75]
buffer_load_dwordx4 v235, s[12:15], s64 offen offset:1024 lds
MM a[80:83],a[160:163],v[80:83],a[80:83]
s_add_u32 m0, s91, 143360
s_add_u32 s77, s90, 40960
buffer_load_dwordx4 v253, s[28:31], s77 offen offset:0 lds
MM a[80:83],a[164:167],v[84:87],a[80:83]
EX v50,v50
MM a[88:91],a[164:167],v[92:95],a[88:91]
EX v51,v51
MM a[88:91],a[160:163],v[88:91],a[88:91]
RD a[244:247],v225 offset:28672
CV v21,v50,v51
MM a[92:95],a[168:171],v[88:91],a[92:95]
EX v52,v52
MM a[92:95],a[172:175],v[92:95],a[92:95]
EX v53,v53
MM a[84:87],a[172:175],v[84:87],a[84:87]
RD a[248:251],v226 offset:28672
CV v22,v52,v53
MM a[84:87],a[168:171],v[80:83],a[84:87]
EX v54,v54
MM a[76:79],a[168:171],v[72:75],a[76:79]
s_add_u32 m0, s65, 114688
s_nop 0
buffer_load_dwordx4 v235, s[12:15], s81 offen offset:2048 lds
MM a[76:79],a[172:175],v[76:79],a[76:79]
EX v55,v55
MM a[68:71],a[172:175],v[68:71],a[68:71]
RD a[252:255],v227 offset:28672
CV v23,v54,v55
MM a[68:71],a[168:171],v[64:67],a[68:71]
EX v56,v56
MM a[96:99],a[176:179],v[64:67],a[96:99]
EX v57,v57
MM a[96:99],a[180:183],v[68:71],a[96:99]
buffer_load_dwordx4 v235, s[12:15], s64 offen offset:3072 lds
MM a[104:107],a[180:183],v[76:79],a[104:107]
EX v58,v58
MM a[104:107],a[176:179],v[72:75],a[104:107]
EX v59,v59
MM a[112:115],a[176:179],v[80:83],a[112:115]
EX v60,v60
MM a[112:115],a[180:183],v[84:87],a[112:115]
EX v61,v61
MM a[120:123],a[180:183],v[92:95],a[120:123]
s_add_u32 m0, s91, 144384
s_add_u32 s77, s90, 51200
buffer_load_dwordx4 v253, s[28:31], s77 offen offset:0 lds
MM a[120:123],a[176:179],v[88:91],a[120:123]
EX v62,v62
MM a[124:127],a[184:187],v[88:91],a[124:127]
EX v63,v63
MM a[124:127],a[188:191],v[92:95],a[124:127]
CV v28,v56,v57
CV v29,v58,v59
MM a[116:119],a[188:191],v[84:87],a[116:119]
CV v30,v60,v61
CV v31,v62,v63
MM a[116:119],a[184:187],v[80:83],a[116:119]
MM a[108:111],a[184:187],v[72:75],a[108:111]
MM a[108:111],a[188:191],v[76:79],a[108:111]
MM a[100:103],a[188:191],v[68:71],a[100:103]
MM a[100:103],a[184:187],v[64:67],a[100:103]
v_smfmac_f32_16x16x64_bf16 v[144:147], v[236:239], v[0:7], v234
v_smfmac_f32_16x16x64_bf16 v[148:151], v[236:239], v[8:15], v234
v_smfmac_f32_16x16x64_bf16 v[152:155], v[236:239], v[16:23], v234
v_smfmac_f32_16x16x64_bf16 v[156:159], v[236:239], v[24:31], v234
s_waitcnt lgkmcnt(0)
s_nop 0
LPG15_%=:
s_cbranch_execz LTR2_%=
MM v[64:67],a[192:195],v[160:163],v[128:131]
s_add_u32 s64, s64, 16384
TR a[128:129],v232 offset:0
s_add_u32 s81, s64, 896
MM v[64:67],a[196:199],v[176:179],v[64:67]
TR a[130:131],v232 offset:4096
TR a[132:133],v232 offset:8192
MM v[64:67],a[200:203],v[192:195],v[64:67]
v_max3_f32 v249, v144, v145, v148
v_max3_f32 v249, v249, v149, v152
MM v[64:67],a[204:207],v[208:211],v[64:67]
v_max3_f32 v249, v249, v153, v156
v_max_f32 v249, v249, v157
MM v[68:71],a[220:223],v[208:211],v[128:131]
TR a[134:135],v232 offset:12288
v_cmp_lt_f32 vcc, s71, v249
MM v[68:71],a[216:219],v[192:195],v[68:71]
s_cbranch_vccnz LSITE20_%=
LBACK20_%=:
TR a[136:137],v233 offset:0
v_add_f32 v244, v144, v145
MM v[68:71],a[212:215],v[176:179],v[68:71]
v_add_f32 v245, v148, v149
v_add_f32 v246, v152, v153
MM v[68:71],a[208:211],v[160:163],v[68:71]
v_add_f32 v247, v156, v157
v_bfe_u32 v253, v248, 4, 4
MM v[76:79],a[208:211],v[164:167],v[132:135]
TR a[138:139],v233 offset:4096
v_lshlrev_b32 v253, 2, v253
MM v[76:79],a[212:215],v[180:183],v[76:79]
s_and_b32 s76, s18, 0xffffffc0
EX v64,v64
MM v[76:79],a[216:219],v[196:199],v[76:79]
s_add_u32 s76, s76, 77824
buffer_load_dword v252, v253, s[20:23], s76 offen offset:0 sc1
MM v[76:79],a[220:223],v[212:215],v[76:79]
EX v65,v65
MM v[72:75],a[204:207],v[212:215],v[132:135]
TR a[140:141],v233 offset:8192
CV v64,v64,v65
MM v[72:75],a[200:203],v[196:199],v[72:75]
EX v66,v66
MM v[72:75],a[196:199],v[180:183],v[72:75]
TR a[142:143],v233 offset:12288
TR a[144:145],v232 offset:512
MM v[72:75],a[192:195],v[164:167],v[72:75]
EX v67,v67
MM v[80:83],a[192:195],v[168:171],v[136:139]
EX v68,v68
MM v[80:83],a[196:199],v[184:187],v[80:83]
TR a[146:147],v232 offset:4608
CV v65,v66,v67
MM v[80:83],a[200:203],v[200:203],v[80:83]
EX v69,v69
MM v[80:83],a[204:207],v[216:219],v[80:83]
TR a[148:149],v232 offset:8704
CV v66,v68,v69
MM v[84:87],a[220:223],v[216:219],v[136:139]
EX v70,v70
MM v[84:87],a[216:219],v[200:203],v[84:87]
TR a[150:151],v232 offset:12800
TR a[152:153],v233 offset:512
MM v[84:87],a[212:215],v[184:187],v[84:87]
EX v71,v71
MM v[84:87],a[208:211],v[168:171],v[84:87]
TR a[154:155],v233 offset:4608
CV v67,v70,v71
MM v[92:95],a[208:211],v[172:175],v[140:143]
EX v72,v72
MM v[92:95],a[212:215],v[188:191],v[92:95]
EX v73,v73
MM v[92:95],a[216:219],v[204:207],v[92:95]
TR a[156:157],v233 offset:8704
CV v72,v72,v73
MM v[92:95],a[220:223],v[220:223],v[92:95]
EX v74,v74
MM v[88:91],a[204:207],v[220:223],v[140:143]
TR a[158:159],v233 offset:12800
TR a[160:161],v232 offset:1024
MM v[88:91],a[200:203],v[204:207],v[88:91]
EX v75,v75
MM v[88:91],a[196:199],v[188:191],v[88:91]
TR a[162:163],v232 offset:5120
CV v73,v74,v75
MM v[88:91],a[192:195],v[172:175],v[88:91]
EX v76,v76
MM v[96:99],a[224:227],v[160:163],v[128:131]
TR a[164:165],v232 offset:9216
TR a[166:167],v232 offset:13312
MM v[96:99],a[228:231],v[176:179],v[96:99]
EX v77,v77
MM v[96:99],a[232:235],v[192:195],v[96:99]
TR a[168:169],v233 offset:1024
CV v74,v76,v77
MM v[96:99],a[236:239],v[208:211],v[96:99]
EX v78,v78
MM v[100:103],a[252:255],v[208:211],v[128:131]
EX v79,v79
MM v[100:103],a[248:251],v[192:195],v[100:103]
TR a[170:171],v233 offset:5120
CV v75,v78,v79
MM v[100:103],a[244:247],v[176:179],v[100:103]
EX v80,v80
MM v[100:103],a[240:243],v[160:163],v[100:103]
TR a[172:173],v233 offset:9216
TR a[174:175],v233 offset:13312
MM v[108:111],a[240:243],v[164:167],v[132:135]
EX v81,v81
MM v[108:111],a[244:247],v[180:183],v[108:111]
TR a[176:177],v232 offset:1536
CV v80,v80,v81
MM v[108:111],a[248:251],v[196:199],v[108:111]
EX v82,v82
MM v[108:111],a[252:255],v[212:215],v[108:111]
EX v83,v83
MM v[104:107],a[236:239],v[212:215],v[132:135]
TR a[178:179],v232 offset:5632
CV v81,v82,v83
MM v[104:107],a[232:235],v[196:199],v[104:107]
EX v84,v84
MM v[104:107],a[228:231],v[180:183],v[104:107]
TR a[180:181],v232 offset:9728
TR a[182:183],v232 offset:13824
MM v[104:107],a[224:227],v[164:167],v[104:107]
EX v85,v85
MM v[112:115],a[224:227],v[168:171],v[136:139]
TR a[184:185],v233 offset:1536
CV v82,v84,v85
MM v[112:115],a[228:231],v[184:187],v[112:115]
EX v86,v86
MM v[112:115],a[232:235],v[200:203],v[112:115]
EX v87,v87
MM v[112:115],a[236:239],v[216:219],v[112:115]
TR a[186:187],v233 offset:5632
CV v83,v86,v87
MM v[116:119],a[252:255],v[216:219],v[136:139]
EX v88,v88
MM v[116:119],a[248:251],v[200:203],v[116:119]
TR a[188:189],v233 offset:9728
TR a[190:191],v233 offset:13824
MM v[116:119],a[244:247],v[184:187],v[116:119]
EX v89,v89
MM v[116:119],a[240:243],v[168:171],v[116:119]
EX v90,v90
MM v[124:127],a[240:243],v[172:175],v[140:143]
EX v91,v91
MM v[124:127],a[244:247],v[188:191],v[124:127]
EX v92,v92
MM v[124:127],a[248:251],v[204:207],v[124:127]
EX v93,v93
MM v[124:127],a[252:255],v[220:223],v[124:127]
EX v94,v94
MM v[120:123],a[236:239],v[220:223],v[140:143]
EX v95,v95
MM v[120:123],a[232:235],v[204:207],v[120:123]
EX v96,v96
MM v[120:123],a[228:231],v[188:191],v[120:123]
s_getpc_b64 s[98:99]
LTA3_%=:
s_min_u32 s73, %[swave], 3
s_lshl_b32 s73, s73, 12
s_add_u32 s73, s73, LPG17_%=-LTA3_%=
s_add_u32 s98, s98, s73
s_addc_u32 s99, s99, 0
s_mov_b64 exec, 0
s_setpc_b64 s[98:99]
LTR3_%=:
s_mov_b64 exec, -1
CV v88,v88,v89
MM v[120:123],a[224:227],v[172:175],v[120:123]
s_waitcnt vmcnt(20) lgkmcnt(0)
s_barrier
MM a[0:3],a[128:131],v[0:3],a[0:3]
s_add_u32 m0, s65, 0
CV v89,v90,v91
CV v90,v92,v93
MM a[0:3],a[132:135],v[4:7],a[0:3]
RD a[192:195],v224 offset:32768
CV v91,v94,v95
MM a[8:11],a[132:135],v[12:15],a[8:11]
buffer_load_dwordx4 v228, s[8:11], s64 offen offset:0 lds
MM a[8:11],a[128:131],v[8:11],a[8:11]
RD a[196:199],v225 offset:32768
RD a[200:203],v226 offset:32768
MM a[16:19],a[128:131],v[16:19],a[16:19]
EX v97,v97
MM a[16:19],a[132:135],v[20:23],a[16:19]
RD a[204:207],v227 offset:32768
CV v68,v96,v97
MM a[24:27],a[132:135],v[28:31],a[24:27]
buffer_load_dwordx4 v229, s[8:11], s64 offen offset:1024 lds
MM a[24:27],a[128:131],v[24:27],a[24:27]
EX v98,v98
MM a[28:31],a[136:139],v[24:27],a[28:31]
EX v99,v99
MM a[28:31],a[140:143],v[28:31],a[28:31]
RD a[208:211],v224 offset:36864
CV v69,v98,v99
MM a[20:23],a[140:143],v[20:23],a[20:23]
EX v100,v100
MM a[20:23],a[136:139],v[16:19],a[20:23]
EX v101,v101
MM a[12:15],a[136:139],v[8:11],a[12:15]
RD a[212:215],v225 offset:36864
CV v70,v100,v101
MM a[12:15],a[140:143],v[12:15],a[12:15]
buffer_load_dwordx4 v230, s[8:11], s64 offen offset:2048 lds
MM a[4:7],a[140:143],v[4:7],a[4:7]
EX v102,v102
MM a[4:7],a[136:139],v[0:3],a[4:7]
EX v103,v103
MM a[32:35],a[144:147],v[0:3],a[32:35]
RD a[216:219],v226 offset:36864
CV v71,v102,v103
MM a[32:35],a[148:151],v[4:7],a[32:35]
EX v104,v104
MM a[40:43],a[148:151],v[12:15],a[40:43]
RD a[220:223],v227 offset:36864
RD a[224:227],v224 offset:40960
MM a[40:43],a[144:147],v[8:11],a[40:43]
EX v105,v105
MM a[48:51],a[144:147],v[16:19],a[48:51]
RD a[228:231],v225 offset:40960
CV v76,v104,v105
MM a[48:51],a[148:151],v[20:23],a[48:51]
EX v106,v106
MM a[56:59],a[148:151],v[28:31],a[56:59]
buffer_load_dwordx4 v231, s[8:11], s64 offen offset:3072 lds
MM a[56:59],a[144:147],v[24:27],a[56:59]
s_add_u32 m0, s65, 65536
EX v107,v107
MM a[60:63],a[152:155],v[24:27],a[60:63]
RD a[232:235],v226 offset:40960
CV v77,v106,v107
MM a[60:63],a[156:159],v[28:31],a[60:63]
EX v108,v108
MM a[52:55],a[156:159],v[20:23],a[52:55]
EX v109,v109
MM a[52:55],a[152:155],v[16:19],a[52:55]
RD a[236:239],v227 offset:40960
CV v78,v108,v109
MM a[44:47],a[152:155],v[8:11],a[44:47]
EX v110,v110
MM a[44:47],a[156:159],v[12:15],a[44:47]
buffer_load_dwordx4 v235, s[12:15], s81 offen offset:0 lds
MM a[36:39],a[156:159],v[4:7],a[36:39]
EX v111,v111
MM a[36:39],a[152:155],v[0:3],a[36:39]
RD a[240:243],v224 offset:45056
CV v79,v110,v111
MM a[64:67],a[160:163],v[0:3],a[64:67]
EX v112,v112
MM a[64:67],a[164:167],v[4:7],a[64:67]
EX v113,v113
MM a[72:75],a[164:167],v[12:15],a[72:75]
RD a[244:247],v225 offset:45056
CV v84,v112,v113
MM a[72:75],a[160:163],v[8:11],a[72:75]
buffer_load_dwordx4 v235, s[12:15], s64 offen offset:1024 lds
MM a[80:83],a[160:163],v[16:19],a[80:83]
EX v114,v114
MM a[80:83],a[164:167],v[20:23],a[80:83]
EX v115,v115
MM a[88:91],a[164:167],v[28:31],a[88:91]
RD a[248:251],v226 offset:45056
CV v85,v114,v115
MM a[88:91],a[160:163],v[24:27],a[88:91]
EX v116,v116
MM a[92:95],a[168:171],v[24:27],a[92:95]
EX v117,v117
MM a[92:95],a[172:175],v[28:31],a[92:95]
RD a[252:255],v227 offset:45056
CV v86,v116,v117
MM a[84:87],a[172:175],v[20:23],a[84:87]
buffer_load_dwordx4 v235, s[12:15], s81 offen offset:2048 lds
MM a[84:87],a[168:171],v[16:19],a[84:87]
EX v118,v118
MM a[76:79],a[168:171],v[8:11],a[76:79]
EX v119,v119
MM a[76:79],a[172:175],v[12:15],a[76:79]
CV v87,v118,v119
MM a[68:71],a[172:175],v[4:7],a[68:71]
EX v120,v120
MM a[68:71],a[168:171],v[0:3],a[68:71]
EX v121,v121
MM a[96:99],a[176:179],v[0:3],a[96:99]
buffer_load_dwordx4 v235, s[12:15], s64 offen offset:3072 lds
MM a[96:99],a[180:183],v[4:7],a[96:99]
EX v122,v122
MM a[104:107],a[180:183],v[12:15],a[104:107]
EX v123,v123
MM a[104:107],a[176:179],v[8:11],a[104:107]
EX v124,v124
MM a[112:115],a[176:179],v[16:19],a[112:115]
EX v125,v125
MM a[112:115],a[180:183],v[20:23],a[112:115]
EX v126,v126
MM a[120:123],a[180:183],v[28:31],a[120:123]
s_waitcnt vmcnt(8)
v_cmp_ne_u32 vcc, s69, v252
s_cbranch_vccnz LRFG_%=
LRFGb_%=:
MM a[120:123],a[176:179],v[24:27],a[120:123]
v_lshrrev_b32 v253, 8, v248
v_lshlrev_b32 v253, 10, v253
MM a[124:127],a[184:187],v[24:27],a[124:127]
v_and_b32 v252, 240, v248
v_add_u32 v253, v253, v252
MM a[124:127],a[188:191],v[28:31],a[124:127]
s_lshr_b32 s76, s18, 6
EX v127,v127
MM a[116:119],a[188:191],v[20:23],a[116:119]
s_lshl_b32 s76, s76, 13
)ASM"
R"ASM(
CV v92,v120,v121
CV v93,v122,v123
MM a[116:119],a[184:187],v[16:19],a[116:119]
s_bfe_u32 s90, s18, 131076
CV v94,v124,v125
CV v95,v126,v127
MM a[108:111],a[184:187],v[8:11],a[108:111]
s_lshl_b32 s90, s90, 8
s_add_u32 s76, s76, s90
s_add_u32 s76, s76, 22183936
MM a[108:111],a[188:191],v[12:15],a[108:111]
s_lshl_b32 s90, %[swave], 11
s_add_u32 s90, s90, %[slds]
MM a[100:103],a[188:191],v[4:7],a[100:103]
s_add_u32 m0, s90, 131072
s_nop 0
buffer_load_dwordx4 v253, s[20:23], s76 offen offset:0 sc1 lds
MM a[100:103],a[184:187],v[0:3],a[100:103]
s_add_u32 m0, s90, 132096
s_add_u32 s76, s76, 4096
buffer_load_dwordx4 v253, s[20:23], s76 offen offset:0 sc1 lds
v_smfmac_f32_16x16x64_bf16 v[144:147], v[236:239], v[64:71], v234
s_add_u32 s67, s95, 3072
s_mov_b32 s70, 3
v_smfmac_f32_16x16x64_bf16 v[148:151], v[236:239], v[72:79], v234
v_smfmac_f32_16x16x64_bf16 v[152:155], v[236:239], v[80:87], v234
v_smfmac_f32_16x16x64_bf16 v[156:159], v[236:239], v[88:95], v234
s_waitcnt lgkmcnt(0)
s_cmp_ge_u32 s16, 90
s_cbranch_scc1 LLAP_%=
MM v[0:3],a[192:195],v[160:163],v[128:131]
s_add_u32 s64, s64, 16384
TR a[128:129],v232 offset:16384
s_add_u32 s81, s64, 896
MM v[0:3],a[196:199],v[176:179],v[0:3]
TR a[130:131],v232 offset:20480
TR a[132:133],v232 offset:24576
MM v[0:3],a[200:203],v[192:195],v[0:3]
v_max3_f32 v249, v144, v145, v148
v_max3_f32 v249, v249, v149, v152
MM v[0:3],a[204:207],v[208:211],v[0:3]
v_max3_f32 v249, v249, v153, v156
v_max_f32 v249, v249, v157
MM v[4:7],a[220:223],v[208:211],v[128:131]
TR a[134:135],v232 offset:28672
v_cmp_lt_f32 vcc, s71, v249
MM v[4:7],a[216:219],v[192:195],v[4:7]
s_cbranch_vccnz LSITE21_%=
LBACK21_%=:
TR a[136:137],v233 offset:16384
v_add_f32 v244, v144, v145
MM v[4:7],a[212:215],v[176:179],v[4:7]
v_add_f32 v245, v148, v149
v_add_f32 v246, v152, v153
MM v[4:7],a[208:211],v[160:163],v[4:7]
TR a[138:139],v233 offset:20480
v_add_f32 v247, v156, v157
MM v[12:15],a[208:211],v[164:167],v[132:135]
EX v0,v0
MM v[12:15],a[212:215],v[180:183],v[12:15]
TR a[140:141],v233 offset:24576
TR a[142:143],v233 offset:28672
MM v[12:15],a[216:219],v[196:199],v[12:15]
EX v1,v1
MM v[12:15],a[220:223],v[212:215],v[12:15]
TR a[144:145],v232 offset:16896
CV v0,v0,v1
MM v[8:11],a[204:207],v[212:215],v[132:135]
EX v2,v2
MM v[8:11],a[200:203],v[196:199],v[8:11]
EX v3,v3
MM v[8:11],a[196:199],v[180:183],v[8:11]
TR a[146:147],v232 offset:20992
CV v1,v2,v3
MM v[8:11],a[192:195],v[164:167],v[8:11]
EX v4,v4
MM v[16:19],a[192:195],v[168:171],v[136:139]
TR a[148:149],v232 offset:25088
TR a[150:151],v232 offset:29184
MM v[16:19],a[196:199],v[184:187],v[16:19]
EX v5,v5
MM v[16:19],a[200:203],v[200:203],v[16:19]
TR a[152:153],v233 offset:16896
CV v2,v4,v5
MM v[16:19],a[204:207],v[216:219],v[16:19]
EX v6,v6
MM v[20:23],a[220:223],v[216:219],v[136:139]
s_add_u32 s87, s95, 3072
buffer_store_dword v240, v248, s[20:23], s87 offen offset:12 sc1
MM v[20:23],a[216:219],v[200:203],v[20:23]
EX v7,v7
MM v[20:23],a[212:215],v[184:187],v[20:23]
TR a[154:155],v233 offset:20992
CV v3,v6,v7
MM v[20:23],a[208:211],v[168:171],v[20:23]
EX v8,v8
MM v[28:31],a[208:211],v[172:175],v[140:143]
TR a[156:157],v233 offset:25088
TR a[158:159],v233 offset:29184
MM v[28:31],a[212:215],v[188:191],v[28:31]
EX v9,v9
MM v[28:31],a[216:219],v[204:207],v[28:31]
TR a[160:161],v232 offset:17408
CV v8,v8,v9
MM v[28:31],a[220:223],v[220:223],v[28:31]
EX v10,v10
MM v[24:27],a[204:207],v[220:223],v[140:143]
EX v11,v11
MM v[24:27],a[200:203],v[204:207],v[24:27]
TR a[162:163],v232 offset:21504
CV v9,v10,v11
MM v[24:27],a[196:199],v[188:191],v[24:27]
EX v12,v12
MM v[24:27],a[192:195],v[172:175],v[24:27]
TR a[164:165],v232 offset:25600
TR a[166:167],v232 offset:29696
MM v[32:35],a[224:227],v[160:163],v[128:131]
EX v13,v13
MM v[32:35],a[228:231],v[176:179],v[32:35]
TR a[168:169],v233 offset:17408
CV v10,v12,v13
MM v[32:35],a[232:235],v[192:195],v[32:35]
EX v14,v14
MM v[32:35],a[236:239],v[208:211],v[32:35]
EX v15,v15
MM v[36:39],a[252:255],v[208:211],v[128:131]
TR a[170:171],v233 offset:21504
CV v11,v14,v15
MM v[36:39],a[248:251],v[192:195],v[36:39]
EX v16,v16
MM v[36:39],a[244:247],v[176:179],v[36:39]
TR a[172:173],v233 offset:25600
TR a[174:175],v233 offset:29696
MM v[36:39],a[240:243],v[160:163],v[36:39]
EX v17,v17
MM v[44:47],a[240:243],v[164:167],v[132:135]
TR a[176:177],v232 offset:17920
CV v16,v16,v17
MM v[44:47],a[244:247],v[180:183],v[44:47]
EX v18,v18
MM v[44:47],a[248:251],v[196:199],v[44:47]
TR a[178:179],v232 offset:22016
TR a[180:181],v232 offset:26112
MM v[44:47],a[252:255],v[212:215],v[44:47]
EX v19,v19
MM v[40:43],a[236:239],v[212:215],v[132:135]
TR a[182:183],v232 offset:30208
CV v17,v18,v19
MM v[40:43],a[232:235],v[196:199],v[40:43]
EX v20,v20
MM v[40:43],a[228:231],v[180:183],v[40:43]
EX v21,v21
MM v[40:43],a[224:227],v[164:167],v[40:43]
TR a[184:185],v233 offset:17920
CV v18,v20,v21
MM v[48:51],a[224:227],v[168:171],v[136:139]
EX v22,v22
MM v[48:51],a[228:231],v[184:187],v[48:51]
TR a[186:187],v233 offset:22016
TR a[188:189],v233 offset:26112
MM v[48:51],a[232:235],v[200:203],v[48:51]
EX v23,v23
MM v[48:51],a[236:239],v[216:219],v[48:51]
TR a[190:191],v233 offset:30208
CV v19,v22,v23
MM v[52:55],a[252:255],v[216:219],v[136:139]
EX v24,v24
MM v[52:55],a[248:251],v[200:203],v[52:55]
EX v25,v25
MM v[52:55],a[244:247],v[184:187],v[52:55]
EX v26,v26
MM v[52:55],a[240:243],v[168:171],v[52:55]
EX v27,v27
MM v[60:63],a[240:243],v[172:175],v[140:143]
EX v28,v28
MM v[60:63],a[244:247],v[188:191],v[60:63]
EX v29,v29
MM v[60:63],a[248:251],v[204:207],v[60:63]
EX v30,v30
MM v[60:63],a[252:255],v[220:223],v[60:63]
EX v31,v31
MM v[56:59],a[236:239],v[220:223],v[140:143]
EX v32,v32
MM v[56:59],a[232:235],v[204:207],v[56:59]
EX v33,v33
MM v[56:59],a[228:231],v[188:191],v[56:59]
EX v34,v34
MM v[56:59],a[224:227],v[172:175],v[56:59]
s_waitcnt lgkmcnt(0)
s_barrier
MM a[0:3],a[128:131],v[64:67],a[0:3]
s_add_u32 m0, s65, 16384
CV v24,v24,v25
CV v25,v26,v27
MM a[0:3],a[132:135],v[68:71],a[0:3]
RD a[192:195],v224 offset:49152
CV v26,v28,v29
MM a[8:11],a[132:135],v[76:79],a[8:11]
RD a[196:199],v225 offset:49152
CV v27,v30,v31
MM a[8:11],a[128:131],v[72:75],a[8:11]
buffer_load_dwordx4 v228, s[8:11], s64 offen offset:0 lds
MM a[16:19],a[128:131],v[80:83],a[16:19]
RD a[200:203],v226 offset:49152
CV v4,v32,v33
MM a[16:19],a[132:135],v[84:87],a[16:19]
buffer_load_dwordx4 v229, s[8:11], s64 offen offset:1024 lds
MM a[24:27],a[132:135],v[92:95],a[24:27]
RD a[204:207],v227 offset:49152
RD a[208:211],v224 offset:53248
MM a[24:27],a[128:131],v[88:91],a[24:27]
EX v35,v35
MM a[28:31],a[136:139],v[88:91],a[28:31]
RD a[212:215],v225 offset:53248
CV v5,v34,v35
MM a[28:31],a[140:143],v[92:95],a[28:31]
EX v36,v36
MM a[20:23],a[140:143],v[84:87],a[20:23]
buffer_load_dwordx4 v230, s[8:11], s64 offen offset:2048 lds
MM a[20:23],a[136:139],v[80:83],a[20:23]
EX v37,v37
MM a[12:15],a[136:139],v[72:75],a[12:15]
RD a[216:219],v226 offset:53248
CV v6,v36,v37
MM a[12:15],a[140:143],v[76:79],a[12:15]
EX v38,v38
MM a[4:7],a[140:143],v[68:71],a[4:7]
EX v39,v39
MM a[4:7],a[136:139],v[64:67],a[4:7]
RD a[220:223],v227 offset:53248
CV v7,v38,v39
MM a[32:35],a[144:147],v[64:67],a[32:35]
EX v40,v40
MM a[32:35],a[148:151],v[68:71],a[32:35]
buffer_load_dwordx4 v231, s[8:11], s64 offen offset:3072 lds
MM a[40:43],a[148:151],v[76:79],a[40:43]
s_add_u32 m0, s65, 81920
EX v41,v41
MM a[40:43],a[144:147],v[72:75],a[40:43]
RD a[224:227],v224 offset:57344
CV v12,v40,v41
MM a[48:51],a[144:147],v[80:83],a[48:51]
EX v42,v42
MM a[48:51],a[148:151],v[84:87],a[48:51]
EX v43,v43
MM a[56:59],a[148:151],v[92:95],a[56:59]
RD a[228:231],v225 offset:57344
CV v13,v42,v43
MM a[56:59],a[144:147],v[88:91],a[56:59]
EX v44,v44
MM a[60:63],a[152:155],v[88:91],a[60:63]
buffer_load_dwordx4 v235, s[12:15], s81 offen offset:0 lds
MM a[60:63],a[156:159],v[92:95],a[60:63]
EX v45,v45
MM a[52:55],a[156:159],v[84:87],a[52:55]
RD a[232:235],v226 offset:57344
CV v14,v44,v45
MM a[52:55],a[152:155],v[80:83],a[52:55]
EX v46,v46
MM a[44:47],a[152:155],v[72:75],a[44:47]
EX v47,v47
MM a[44:47],a[156:159],v[76:79],a[44:47]
RD a[236:239],v227 offset:57344
CV v15,v46,v47
MM a[36:39],a[156:159],v[68:71],a[36:39]
buffer_load_dwordx4 v235, s[12:15], s64 offen offset:1024 lds
MM a[36:39],a[152:155],v[64:67],a[36:39]
EX v48,v48
MM a[64:67],a[160:163],v[64:67],a[64:67]
EX v49,v49
MM a[64:67],a[164:167],v[68:71],a[64:67]
RD a[240:243],v224 offset:61440
CV v20,v48,v49
MM a[72:75],a[164:167],v[76:79],a[72:75]
EX v50,v50
MM a[72:75],a[160:163],v[72:75],a[72:75]
RD a[244:247],v225 offset:61440
RD a[248:251],v226 offset:61440
MM a[80:83],a[160:163],v[80:83],a[80:83]
EX v51,v51
MM a[80:83],a[164:167],v[84:87],a[80:83]
RD a[252:255],v227 offset:61440
s_nop 0
LPG16_%=:
s_cbranch_execz LTR2_%=
CV v21,v50,v51
MM a[88:91],a[164:167],v[92:95],a[88:91]
EX v52,v52
MM a[88:91],a[160:163],v[88:91],a[88:91]
buffer_load_dwordx4 v235, s[12:15], s81 offen offset:2048 lds
MM a[92:95],a[168:171],v[88:91],a[92:95]
EX v53,v53
MM a[92:95],a[172:175],v[92:95],a[92:95]
EX v54,v54
MM a[84:87],a[172:175],v[84:87],a[84:87]
buffer_load_dwordx4 v235, s[12:15], s64 offen offset:3072 lds
MM a[84:87],a[168:171],v[80:83],a[84:87]
EX v55,v55
MM a[76:79],a[168:171],v[72:75],a[76:79]
EX v56,v56
MM a[76:79],a[172:175],v[76:79],a[76:79]
EX v57,v57
MM a[68:71],a[172:175],v[68:71],a[68:71]
EX v58,v58
MM a[68:71],a[168:171],v[64:67],a[68:71]
EX v59,v59
MM a[96:99],a[176:179],v[64:67],a[96:99]
EX v60,v60
MM a[96:99],a[180:183],v[68:71],a[96:99]
EX v61,v61
MM a[104:107],a[180:183],v[76:79],a[104:107]
EX v62,v62
MM a[104:107],a[176:179],v[72:75],a[104:107]
EX v63,v63
MM a[112:115],a[176:179],v[80:83],a[112:115]
CV v22,v52,v53
CV v23,v54,v55
MM a[112:115],a[180:183],v[84:87],a[112:115]
CV v28,v56,v57
CV v29,v58,v59
MM a[120:123],a[180:183],v[92:95],a[120:123]
CV v30,v60,v61
CV v31,v62,v63
MM a[120:123],a[176:179],v[88:91],a[120:123]
MM a[124:127],a[184:187],v[88:91],a[124:127]
MM a[124:127],a[188:191],v[92:95],a[124:127]
MM a[116:119],a[188:191],v[84:87],a[116:119]
MM a[116:119],a[184:187],v[80:83],a[116:119]
MM a[108:111],a[184:187],v[72:75],a[108:111]
MM a[108:111],a[188:191],v[76:79],a[108:111]
MM a[100:103],a[188:191],v[68:71],a[100:103]
MM a[100:103],a[184:187],v[64:67],a[100:103]
v_smfmac_f32_16x16x64_bf16 v[144:147], v[236:239], v[0:7], v234
v_smfmac_f32_16x16x64_bf16 v[148:151], v[236:239], v[8:15], v234
v_smfmac_f32_16x16x64_bf16 v[152:155], v[236:239], v[16:23], v234
v_smfmac_f32_16x16x64_bf16 v[156:159], v[236:239], v[24:31], v234
s_waitcnt lgkmcnt(0)
MM v[64:67],a[192:195],v[160:163],v[128:131]
s_add_u32 s64, s64, 16384
TR a[128:129],v232 offset:32768
s_add_u32 s81, s64, 896
MM v[64:67],a[196:199],v[176:179],v[64:67]
TR a[130:131],v232 offset:36864
TR a[132:133],v232 offset:40960
MM v[64:67],a[200:203],v[192:195],v[64:67]
v_max3_f32 v249, v144, v145, v148
v_max3_f32 v249, v249, v149, v152
MM v[64:67],a[204:207],v[208:211],v[64:67]
v_max3_f32 v249, v249, v153, v156
v_max_f32 v249, v249, v157
MM v[68:71],a[220:223],v[208:211],v[128:131]
TR a[134:135],v232 offset:45056
v_cmp_lt_f32 vcc, s71, v249
MM v[68:71],a[216:219],v[192:195],v[68:71]
s_cbranch_vccnz LSITE22_%=
LBACK22_%=:
TR a[136:137],v233 offset:32768
v_add_f32 v244, v144, v145
MM v[68:71],a[212:215],v[176:179],v[68:71]
v_add_f32 v245, v148, v149
v_add_f32 v246, v152, v153
MM v[68:71],a[208:211],v[160:163],v[68:71]
v_add_f32 v247, v156, v157
v_lshrrev_b32 v43, 7, v248
MM v[76:79],a[208:211],v[164:167],v[132:135]
v_bfe_u32 v42, v248, 6, 1
v_lshl_add_u32 v40, v42, 3, v43
MM v[76:79],a[212:215],v[180:183],v[76:79]
v_lshlrev_b32 v40, 4, v40
v_bfe_u32 v42, v248, 4, 2
MM v[76:79],a[216:219],v[196:199],v[76:79]
TR a[138:139],v233 offset:36864
v_lshl_add_u32 v40, v42, 2, v40
MM v[76:79],a[220:223],v[212:215],v[76:79]
s_lshl_b32 s76, %[swave], 11
EX v64,v64
MM v[72:75],a[204:207],v[212:215],v[132:135]
s_add_u32 s76, s76, %[slds]
EX v65,v65
MM v[72:75],a[200:203],v[196:199],v[72:75]
s_add_u32 s76, s76, 131072
TR a[140:141],v233 offset:40960
v_add_u32 v40, s76, v40
MM v[72:75],a[196:199],v[180:183],v[72:75]
s_waitcnt vmcnt(9)
)ASM"
R"ASM(
ds_read_b32 v32, v40 offset:0
ds_read_b32 v36, v40 offset:256
MM v[72:75],a[192:195],v[164:167],v[72:75]
ds_read_b32 v33, v40 offset:512
ds_read_b32 v37, v40 offset:768
MM v[80:83],a[192:195],v[168:171],v[136:139]
ds_read_b32 v34, v40 offset:1024
ds_read_b32 v38, v40 offset:1280
MM v[80:83],a[196:199],v[184:187],v[80:83]
ds_read_b32 v35, v40 offset:1536
ds_read_b32 v39, v40 offset:1792
EX v66,v66
MM v[80:83],a[200:203],v[200:203],v[80:83]
v_mul_u32_u24 v41, 3456, v43
v_bfe_u32 v42, v248, 4, 3
EX v67,v67
MM v[80:83],a[204:207],v[216:219],v[80:83]
TR a[142:143],v233 offset:45056
v_lshl_add_u32 v41, v42, 1, v41
EX v68,v68
MM v[84:87],a[220:223],v[216:219],v[136:139]
s_add_u32 s76, s18, 21626880
TR a[144:145],v232 offset:33280
CV v64,v64,v65
MM v[84:87],a[216:219],v[200:203],v[84:87]
EX v69,v69
MM v[84:87],a[212:215],v[184:187],v[84:87]
TR a[146:147],v232 offset:37376
TR a[148:149],v232 offset:41472
MM v[84:87],a[208:211],v[168:171],v[84:87]
EX v70,v70
MM v[92:95],a[208:211],v[172:175],v[140:143]
EX v71,v71
MM v[92:95],a[212:215],v[188:191],v[92:95]
TR a[150:151],v232 offset:45568
CV v65,v66,v67
MM v[92:95],a[216:219],v[204:207],v[92:95]
EX v72,v72
MM v[92:95],a[220:223],v[220:223],v[92:95]
TR a[152:153],v233 offset:33280
CV v66,v68,v69
MM v[88:91],a[204:207],v[220:223],v[140:143]
s_waitcnt lgkmcnt(6)
v_add_f32 v32, v32, v33
v_add_f32 v36, v36, v37
MM v[88:91],a[200:203],v[204:207],v[88:91]
v_add_f32 v34, v34, v35
v_add_f32 v38, v38, v39
EX v73,v73
MM v[88:91],a[196:199],v[188:191],v[88:91]
TR a[154:155],v233 offset:37376
v_add_f32 v32, v32, v34
v_add_f32 v36, v36, v38
MM v[88:91],a[192:195],v[172:175],v[88:91]
CV v42,v32,v36
v_lshlrev_b32 v32, 16, v42
EX v74,v74
MM v[96:99],a[224:227],v[160:163],v[128:131]
TR a[156:157],v233 offset:41472
v_and_b32 v36, 0xffff0000, v42
v_mul_f32 v42, 0xbfb8aa3b, v32
EX v75,v75
MM v[96:99],a[228:231],v[176:179],v[96:99]
EX v42,v42
MM v[96:99],a[232:235],v[192:195],v[96:99]
TR a[158:159],v233 offset:45568
v_add_f32 v42, 1.0, v42
EX v76,v76
MM v[96:99],a[236:239],v[208:211],v[96:99]
v_rcp_f32 v42, v42
MM v[100:103],a[252:255],v[208:211],v[128:131]
TR a[160:161],v232 offset:33792
v_mul_f32 v42, v32, v42
CV v42,v42,v42
EX v77,v77
CV v67,v70,v71
MM v[100:103],a[248:251],v[192:195],v[100:103]
v_lshlrev_b32 v42, 16, v42
v_mul_f32 v32, v42, v36
MM v[100:103],a[244:247],v[176:179],v[100:103]
TR a[162:163],v232 offset:37888
CV v43,v32,v32
EX v78,v78
MM v[100:103],a[240:243],v[160:163],v[100:103]
buffer_store_short v43, v41, s[20:23], s76 offen offset:0 sc1
s_nop 1
TR a[164:165],v232 offset:41984
MM v[108:111],a[240:243],v[164:167],v[132:135]
EX v79,v79
CV v72,v72,v73
MM v[108:111],a[244:247],v[180:183],v[108:111]
TR a[166:167],v232 offset:46080
CV v73,v74,v75
MM v[108:111],a[248:251],v[196:199],v[108:111]
EX v80,v80
MM v[108:111],a[252:255],v[212:215],v[108:111]
TR a[168:169],v233 offset:33792
TR a[170:171],v233 offset:37888
EX v81,v81
MM v[104:107],a[236:239],v[212:215],v[132:135]
TR a[172:173],v233 offset:41984
CV v74,v76,v77
MM v[104:107],a[232:235],v[196:199],v[104:107]
EX v82,v82
MM v[104:107],a[228:231],v[180:183],v[104:107]
EX v83,v83
MM v[104:107],a[224:227],v[164:167],v[104:107]
TR a[174:175],v233 offset:46080
CV v75,v78,v79
MM v[112:115],a[224:227],v[168:171],v[136:139]
EX v84,v84
MM v[112:115],a[228:231],v[184:187],v[112:115]
TR a[176:177],v232 offset:34304
TR a[178:179],v232 offset:38400
MM v[112:115],a[232:235],v[200:203],v[112:115]
EX v85,v85
MM v[112:115],a[236:239],v[216:219],v[112:115]
TR a[180:181],v232 offset:42496
CV v80,v80,v81
MM v[116:119],a[252:255],v[216:219],v[136:139]
EX v86,v86
MM v[116:119],a[248:251],v[200:203],v[116:119]
TR a[182:183],v232 offset:46592
CV v81,v82,v83
MM v[116:119],a[244:247],v[184:187],v[116:119]
EX v87,v87
MM v[116:119],a[240:243],v[168:171],v[116:119]
TR a[184:185],v233 offset:34304
CV v82,v84,v85
MM v[124:127],a[240:243],v[172:175],v[140:143]
EX v88,v88
MM v[124:127],a[244:247],v[188:191],v[124:127]
TR a[186:187],v233 offset:38400
TR a[188:189],v233 offset:42496
MM v[124:127],a[248:251],v[204:207],v[124:127]
TR a[190:191],v233 offset:46592
CV v83,v86,v87
MM v[124:127],a[252:255],v[220:223],v[124:127]
EX v89,v89
MM v[120:123],a[236:239],v[220:223],v[140:143]
EX v90,v90
MM v[120:123],a[232:235],v[204:207],v[120:123]
EX v91,v91
MM v[120:123],a[228:231],v[188:191],v[120:123]
EX v92,v92
MM v[120:123],a[224:227],v[172:175],v[120:123]
s_waitcnt lgkmcnt(0)
s_barrier
MM a[0:3],a[128:131],v[0:3],a[0:3]
s_add_u32 m0, s65, 32768
CV v88,v88,v89
CV v89,v90,v91
MM a[0:3],a[132:135],v[4:7],a[0:3]
EX v93,v93
MM a[8:11],a[132:135],v[12:15],a[8:11]
EX v94,v94
MM a[8:11],a[128:131],v[8:11],a[8:11]
EX v95,v95
MM a[16:19],a[128:131],v[16:19],a[16:19]
RD a[192:195],v224 offset:0
CV v90,v92,v93
MM a[16:19],a[132:135],v[20:23],a[16:19]
EX v96,v96
MM a[24:27],a[132:135],v[28:31],a[24:27]
RD a[196:199],v225 offset:0
CV v91,v94,v95
MM a[24:27],a[128:131],v[24:27],a[24:27]
EX v97,v97
MM a[28:31],a[136:139],v[24:27],a[28:31]
buffer_load_dwordx4 v228, s[8:11], s64 offen offset:0 lds
MM a[28:31],a[140:143],v[28:31],a[28:31]
EX v98,v98
MM a[20:23],a[140:143],v[20:23],a[20:23]
EX v99,v99
MM a[20:23],a[136:139],v[16:19],a[20:23]
RD a[200:203],v226 offset:0
CV v68,v96,v97
MM a[12:15],a[136:139],v[8:11],a[12:15]
EX v100,v100
MM a[12:15],a[140:143],v[12:15],a[12:15]
RD a[204:207],v227 offset:0
CV v69,v98,v99
MM a[4:7],a[140:143],v[4:7],a[4:7]
EX v101,v101
MM a[4:7],a[136:139],v[0:3],a[4:7]
buffer_load_dwordx4 v229, s[8:11], s64 offen offset:1024 lds
MM a[32:35],a[144:147],v[0:3],a[32:35]
EX v102,v102
MM a[32:35],a[148:151],v[4:7],a[32:35]
RD a[208:211],v224 offset:4096
CV v70,v100,v101
MM a[40:43],a[148:151],v[12:15],a[40:43]
EX v103,v103
MM a[40:43],a[144:147],v[8:11],a[40:43]
RD a[212:215],v225 offset:4096
CV v71,v102,v103
MM a[48:51],a[144:147],v[16:19],a[48:51]
EX v104,v104
MM a[48:51],a[148:151],v[20:23],a[48:51]
EX v105,v105
MM a[56:59],a[148:151],v[28:31],a[56:59]
buffer_load_dwordx4 v230, s[8:11], s64 offen offset:2048 lds
MM a[56:59],a[144:147],v[24:27],a[56:59]
EX v106,v106
MM a[60:63],a[152:155],v[24:27],a[60:63]
RD a[216:219],v226 offset:4096
CV v76,v104,v105
MM a[60:63],a[156:159],v[28:31],a[60:63]
EX v107,v107
MM a[52:55],a[156:159],v[20:23],a[52:55]
RD a[220:223],v227 offset:4096
CV v77,v106,v107
MM a[52:55],a[152:155],v[16:19],a[52:55]
EX v108,v108
MM a[44:47],a[152:155],v[8:11],a[44:47]
EX v109,v109
MM a[44:47],a[156:159],v[12:15],a[44:47]
buffer_load_dwordx4 v231, s[8:11], s64 offen offset:3072 lds
MM a[36:39],a[156:159],v[4:7],a[36:39]
s_add_u32 m0, s65, 98304
RD a[224:227],v224 offset:8192
CV v78,v108,v109
MM a[36:39],a[152:155],v[0:3],a[36:39]
EX v110,v110
MM a[64:67],a[160:163],v[0:3],a[64:67]
EX v111,v111
MM a[64:67],a[164:167],v[4:7],a[64:67]
RD a[228:231],v225 offset:8192
CV v79,v110,v111
MM a[72:75],a[164:167],v[12:15],a[72:75]
EX v112,v112
MM a[72:75],a[160:163],v[8:11],a[72:75]
buffer_load_dwordx4 v235, s[12:15], s81 offen offset:0 lds
MM a[80:83],a[160:163],v[16:19],a[80:83]
EX v113,v113
MM a[80:83],a[164:167],v[20:23],a[80:83]
RD a[232:235],v226 offset:8192
CV v84,v112,v113
MM a[88:91],a[164:167],v[28:31],a[88:91]
EX v114,v114
MM a[88:91],a[160:163],v[24:27],a[88:91]
EX v115,v115
MM a[92:95],a[168:171],v[24:27],a[92:95]
RD a[236:239],v227 offset:8192
CV v85,v114,v115
MM a[92:95],a[172:175],v[28:31],a[92:95]
EX v116,v116
MM a[84:87],a[172:175],v[20:23],a[84:87]
buffer_load_dwordx4 v235, s[12:15], s64 offen offset:1024 lds
MM a[84:87],a[168:171],v[16:19],a[84:87]
EX v117,v117
MM a[76:79],a[168:171],v[8:11],a[76:79]
RD a[240:243],v224 offset:12288
CV v86,v116,v117
MM a[76:79],a[172:175],v[12:15],a[76:79]
EX v118,v118
MM a[68:71],a[172:175],v[4:7],a[68:71]
EX v119,v119
MM a[68:71],a[168:171],v[0:3],a[68:71]
RD a[244:247],v225 offset:12288
CV v87,v118,v119
MM a[96:99],a[176:179],v[0:3],a[96:99]
EX v120,v120
MM a[96:99],a[180:183],v[4:7],a[96:99]
buffer_load_dwordx4 v235, s[12:15], s81 offen offset:2048 lds
MM a[104:107],a[180:183],v[12:15],a[104:107]
EX v121,v121
MM a[104:107],a[176:179],v[8:11],a[104:107]
RD a[248:251],v226 offset:12288
CV v92,v120,v121
MM a[112:115],a[176:179],v[16:19],a[112:115]
EX v122,v122
MM a[112:115],a[180:183],v[20:23],a[112:115]
EX v123,v123
MM a[120:123],a[180:183],v[28:31],a[120:123]
RD a[252:255],v227 offset:12288
CV v93,v122,v123
MM a[120:123],a[176:179],v[24:27],a[120:123]
EX v124,v124
MM a[124:127],a[184:187],v[24:27],a[124:127]
buffer_load_dwordx4 v235, s[12:15], s64 offen offset:3072 lds
MM a[124:127],a[188:191],v[28:31],a[124:127]
EX v125,v125
MM a[116:119],a[188:191],v[20:23],a[116:119]
EX v126,v126
MM a[116:119],a[184:187],v[16:19],a[116:119]
EX v127,v127
MM a[108:111],a[184:187],v[8:11],a[108:111]
CV v94,v124,v125
CV v95,v126,v127
MM a[108:111],a[188:191],v[12:15],a[108:111]
MM a[100:103],a[188:191],v[4:7],a[100:103]
MM a[100:103],a[184:187],v[0:3],a[100:103]
v_smfmac_f32_16x16x64_bf16 v[144:147], v[236:239], v[64:71], v234
v_smfmac_f32_16x16x64_bf16 v[148:151], v[236:239], v[72:79], v234
v_smfmac_f32_16x16x64_bf16 v[152:155], v[236:239], v[80:87], v234
v_smfmac_f32_16x16x64_bf16 v[156:159], v[236:239], v[88:95], v234
s_waitcnt lgkmcnt(0)
MM v[0:3],a[192:195],v[160:163],v[128:131]
s_add_u32 s64, s64, 16384
TR a[128:129],v232 offset:49152
s_add_u32 s81, s64, 896
MM v[0:3],a[196:199],v[176:179],v[0:3]
TR a[130:131],v232 offset:53248
TR a[132:133],v232 offset:57344
MM v[0:3],a[200:203],v[192:195],v[0:3]
v_max3_f32 v249, v144, v145, v148
v_max3_f32 v249, v249, v149, v152
MM v[0:3],a[204:207],v[208:211],v[0:3]
v_max3_f32 v249, v249, v153, v156
v_max_f32 v249, v249, v157
MM v[4:7],a[220:223],v[208:211],v[128:131]
TR a[134:135],v232 offset:61440
v_cmp_lt_f32 vcc, s71, v249
MM v[4:7],a[216:219],v[192:195],v[4:7]
s_cbranch_vccnz LSITE23_%=
LBACK23_%=:
TR a[136:137],v233 offset:49152
v_add_f32 v244, v144, v145
MM v[4:7],a[212:215],v[176:179],v[4:7]
v_add_f32 v245, v148, v149
v_add_f32 v246, v152, v153
MM v[4:7],a[208:211],v[160:163],v[4:7]
TR a[138:139],v233 offset:53248
v_add_f32 v247, v156, v157
MM v[12:15],a[208:211],v[164:167],v[132:135]
EX v0,v0
MM v[12:15],a[212:215],v[180:183],v[12:15]
TR a[140:141],v233 offset:57344
TR a[142:143],v233 offset:61440
MM v[12:15],a[216:219],v[196:199],v[12:15]
EX v1,v1
MM v[12:15],a[220:223],v[212:215],v[12:15]
TR a[144:145],v232 offset:49664
CV v0,v0,v1
MM v[8:11],a[204:207],v[212:215],v[132:135]
EX v2,v2
MM v[8:11],a[200:203],v[196:199],v[8:11]
EX v3,v3
MM v[8:11],a[196:199],v[180:183],v[8:11]
TR a[146:147],v232 offset:53760
CV v1,v2,v3
MM v[8:11],a[192:195],v[164:167],v[8:11]
EX v4,v4
MM v[16:19],a[192:195],v[168:171],v[136:139]
TR a[148:149],v232 offset:57856
TR a[150:151],v232 offset:61952
MM v[16:19],a[196:199],v[184:187],v[16:19]
EX v5,v5
MM v[16:19],a[200:203],v[200:203],v[16:19]
TR a[152:153],v233 offset:49664
CV v2,v4,v5
MM v[16:19],a[204:207],v[216:219],v[16:19]
EX v6,v6
MM v[20:23],a[220:223],v[216:219],v[136:139]
EX v7,v7
MM v[20:23],a[216:219],v[200:203],v[20:23]
TR a[154:155],v233 offset:53760
CV v3,v6,v7
MM v[20:23],a[212:215],v[184:187],v[20:23]
EX v8,v8
MM v[20:23],a[208:211],v[168:171],v[20:23]
TR a[156:157],v233 offset:57856
TR a[158:159],v233 offset:61952
MM v[28:31],a[208:211],v[172:175],v[140:143]
EX v9,v9
MM v[28:31],a[212:215],v[188:191],v[28:31]
TR a[160:161],v232 offset:50176
CV v8,v8,v9
MM v[28:31],a[216:219],v[204:207],v[28:31]
EX v10,v10
MM v[28:31],a[220:223],v[220:223],v[28:31]
EX v11,v11
MM v[24:27],a[204:207],v[220:223],v[140:143]
TR a[162:163],v232 offset:54272
CV v9,v10,v11
MM v[24:27],a[200:203],v[204:207],v[24:27]
TR a[164:165],v232 offset:58368
TR a[166:167],v232 offset:62464
MM v[24:27],a[196:199],v[188:191],v[24:27]
EX v12,v12
MM v[24:27],a[192:195],v[172:175],v[24:27]
EX v13,v13
MM v[32:35],a[224:227],v[160:163],v[128:131]
TR a[168:169],v233 offset:50176
CV v10,v12,v13
MM v[32:35],a[228:231],v[176:179],v[32:35]
EX v14,v14
MM v[32:35],a[232:235],v[192:195],v[32:35]
TR a[170:171],v233 offset:54272
TR a[172:173],v233 offset:58368
MM v[32:35],a[236:239],v[208:211],v[32:35]
EX v15,v15
MM v[36:39],a[252:255],v[208:211],v[128:131]
)ASM"
R"ASM(
TR a[174:175],v233 offset:62464
CV v11,v14,v15
MM v[36:39],a[248:251],v[192:195],v[36:39]
EX v16,v16
MM v[36:39],a[244:247],v[176:179],v[36:39]
EX v17,v17
MM v[36:39],a[240:243],v[160:163],v[36:39]
TR a[176:177],v232 offset:50688
CV v16,v16,v17
MM v[44:47],a[240:243],v[164:167],v[132:135]
EX v18,v18
MM v[44:47],a[244:247],v[180:183],v[44:47]
TR a[178:179],v232 offset:54784
TR a[180:181],v232 offset:58880
MM v[44:47],a[248:251],v[196:199],v[44:47]
EX v19,v19
MM v[44:47],a[252:255],v[212:215],v[44:47]
TR a[182:183],v232 offset:62976
CV v17,v18,v19
MM v[40:43],a[236:239],v[212:215],v[132:135]
EX v20,v20
MM v[40:43],a[232:235],v[196:199],v[40:43]
EX v21,v21
MM v[40:43],a[228:231],v[180:183],v[40:43]
TR a[184:185],v233 offset:50688
CV v18,v20,v21
MM v[40:43],a[224:227],v[164:167],v[40:43]
EX v22,v22
MM v[48:51],a[224:227],v[168:171],v[136:139]
TR a[186:187],v233 offset:54784
TR a[188:189],v233 offset:58880
MM v[48:51],a[228:231],v[184:187],v[48:51]
EX v23,v23
MM v[48:51],a[232:235],v[200:203],v[48:51]
TR a[190:191],v233 offset:62976
CV v19,v22,v23
MM v[48:51],a[236:239],v[216:219],v[48:51]
EX v24,v24
MM v[52:55],a[252:255],v[216:219],v[136:139]
EX v25,v25
MM v[52:55],a[248:251],v[200:203],v[52:55]
EX v26,v26
MM v[52:55],a[244:247],v[184:187],v[52:55]
EX v27,v27
MM v[52:55],a[240:243],v[168:171],v[52:55]
EX v28,v28
MM v[60:63],a[240:243],v[172:175],v[140:143]
EX v29,v29
MM v[60:63],a[244:247],v[188:191],v[60:63]
EX v30,v30
MM v[60:63],a[248:251],v[204:207],v[60:63]
EX v31,v31
MM v[60:63],a[252:255],v[220:223],v[60:63]
EX v32,v32
MM v[56:59],a[236:239],v[220:223],v[140:143]
EX v33,v33
MM v[56:59],a[232:235],v[204:207],v[56:59]
EX v34,v34
MM v[56:59],a[228:231],v[188:191],v[56:59]
EX v35,v35
MM v[56:59],a[224:227],v[172:175],v[56:59]
s_waitcnt vmcnt(13) lgkmcnt(0)
s_barrier
MM a[0:3],a[128:131],v[64:67],a[0:3]
s_add_u32 m0, s65, 49152
CV v24,v24,v25
CV v25,v26,v27
MM a[0:3],a[132:135],v[68:71],a[0:3]
RD a[192:195],v224 offset:16384
CV v26,v28,v29
MM a[8:11],a[132:135],v[76:79],a[8:11]
RD a[196:199],v225 offset:16384
CV v27,v30,v31
MM a[8:11],a[128:131],v[72:75],a[8:11]
buffer_load_dwordx4 v228, s[8:11], s64 offen offset:0 lds
MM a[16:19],a[128:131],v[80:83],a[16:19]
RD a[200:203],v226 offset:16384
CV v4,v32,v33
MM a[16:19],a[132:135],v[84:87],a[16:19]
RD a[204:207],v227 offset:16384
CV v5,v34,v35
MM a[24:27],a[132:135],v[92:95],a[24:27]
buffer_load_dwordx4 v229, s[8:11], s64 offen offset:1024 lds
MM a[24:27],a[128:131],v[88:91],a[24:27]
RD a[208:211],v224 offset:20480
RD a[212:215],v225 offset:20480
MM a[28:31],a[136:139],v[88:91],a[28:31]
buffer_load_dwordx4 v230, s[8:11], s64 offen offset:2048 lds
MM a[28:31],a[140:143],v[92:95],a[28:31]
EX v36,v36
MM a[20:23],a[140:143],v[84:87],a[20:23]
EX v37,v37
MM a[20:23],a[136:139],v[80:83],a[20:23]
RD a[216:219],v226 offset:20480
CV v6,v36,v37
MM a[12:15],a[136:139],v[72:75],a[12:15]
EX v38,v38
s_nop 0
LPG17_%=:
s_cbranch_execz LTR3_%=
MM a[12:15],a[140:143],v[76:79],a[12:15]
RD a[220:223],v227 offset:20480
RD a[224:227],v224 offset:24576
MM a[4:7],a[140:143],v[68:71],a[4:7]
EX v39,v39
MM a[4:7],a[136:139],v[64:67],a[4:7]
RD a[228:231],v225 offset:24576
CV v7,v38,v39
MM a[32:35],a[144:147],v[64:67],a[32:35]
EX v40,v40
MM a[32:35],a[148:151],v[68:71],a[32:35]
buffer_load_dwordx4 v231, s[8:11], s64 offen offset:3072 lds
MM a[40:43],a[148:151],v[76:79],a[40:43]
s_add_u32 m0, s65, 114688
EX v41,v41
MM a[40:43],a[144:147],v[72:75],a[40:43]
RD a[232:235],v226 offset:24576
CV v12,v40,v41
MM a[48:51],a[144:147],v[80:83],a[48:51]
EX v42,v42
MM a[48:51],a[148:151],v[84:87],a[48:51]
EX v43,v43
MM a[56:59],a[148:151],v[92:95],a[56:59]
RD a[236:239],v227 offset:24576
CV v13,v42,v43
MM a[56:59],a[144:147],v[88:91],a[56:59]
EX v44,v44
MM a[60:63],a[152:155],v[88:91],a[60:63]
buffer_load_dwordx4 v235, s[12:15], s81 offen offset:0 lds
MM a[60:63],a[156:159],v[92:95],a[60:63]
EX v45,v45
MM a[52:55],a[156:159],v[84:87],a[52:55]
RD a[240:243],v224 offset:28672
CV v14,v44,v45
MM a[52:55],a[152:155],v[80:83],a[52:55]
EX v46,v46
MM a[44:47],a[152:155],v[72:75],a[44:47]
EX v47,v47
MM a[44:47],a[156:159],v[76:79],a[44:47]
RD a[244:247],v225 offset:28672
CV v15,v46,v47
MM a[36:39],a[156:159],v[68:71],a[36:39]
buffer_load_dwordx4 v235, s[12:15], s64 offen offset:1024 lds
MM a[36:39],a[152:155],v[64:67],a[36:39]
EX v48,v48
MM a[64:67],a[160:163],v[64:67],a[64:67]
EX v49,v49
MM a[64:67],a[164:167],v[68:71],a[64:67]
RD a[248:251],v226 offset:28672
CV v20,v48,v49
MM a[72:75],a[164:167],v[76:79],a[72:75]
EX v50,v50
MM a[72:75],a[160:163],v[72:75],a[72:75]
EX v51,v51
MM a[80:83],a[160:163],v[80:83],a[80:83]
RD a[252:255],v227 offset:28672
CV v21,v50,v51
MM a[80:83],a[164:167],v[84:87],a[80:83]
buffer_load_dwordx4 v235, s[12:15], s81 offen offset:2048 lds
MM a[88:91],a[164:167],v[92:95],a[88:91]
EX v52,v52
MM a[88:91],a[160:163],v[88:91],a[88:91]
EX v53,v53
MM a[92:95],a[168:171],v[88:91],a[92:95]
EX v54,v54
MM a[92:95],a[172:175],v[92:95],a[92:95]
buffer_load_dwordx4 v235, s[12:15], s64 offen offset:3072 lds
MM a[84:87],a[172:175],v[84:87],a[84:87]
EX v55,v55
MM a[84:87],a[168:171],v[80:83],a[84:87]
EX v56,v56
MM a[76:79],a[168:171],v[72:75],a[76:79]
EX v57,v57
MM a[76:79],a[172:175],v[76:79],a[76:79]
EX v58,v58
MM a[68:71],a[172:175],v[68:71],a[68:71]
EX v59,v59
MM a[68:71],a[168:171],v[64:67],a[68:71]
EX v60,v60
MM a[96:99],a[176:179],v[64:67],a[96:99]
EX v61,v61
MM a[96:99],a[180:183],v[68:71],a[96:99]
EX v62,v62
MM a[104:107],a[180:183],v[76:79],a[104:107]
EX v63,v63
MM a[104:107],a[176:179],v[72:75],a[104:107]
CV v22,v52,v53
CV v23,v54,v55
MM a[112:115],a[176:179],v[80:83],a[112:115]
CV v28,v56,v57
CV v29,v58,v59
MM a[112:115],a[180:183],v[84:87],a[112:115]
CV v30,v60,v61
CV v31,v62,v63
MM a[120:123],a[180:183],v[92:95],a[120:123]
s_lshr_b32 s91, s18, 2
s_add_u32 s91, s91, 69632
s_waitcnt vmcnt(16)
MM a[120:123],a[176:179],v[88:91],a[120:123]
v_mov_b32 v250, s69
s_mov_b64 exec, 1
buffer_store_dword v250, off, s[20:23], s91 offset:0 sc1
s_mov_b64 exec, -1
MM a[124:127],a[184:187],v[88:91],a[124:127]
v_lshlrev_b32 v96, 3, v248
v_min_u32 v96, 4992, v96
MM a[124:127],a[188:191],v[92:95],a[124:127]
s_andn2_b32 s90, 1, %[sb]
s_lshl_b32 s90, s90, 3
s_add_u32 s90, s90, s50
MM a[116:119],a[188:191],v[84:87],a[116:119]
s_lshl_b32 s90, s90, 2
s_add_u32 s90, s90, s84
s_lshl_b32 s90, s90, 2
MM a[116:119],a[184:187],v[80:83],a[116:119]
s_add_u32 s90, s90, %[swave]
s_mul_i32 s90, s90, 5120
s_mul_i32 s91, s84, 320
MM a[108:111],a[184:187],v[72:75],a[108:111]
s_sub_u32 s90, s90, s91
s_add_u32 s91, %[slds], 135168
MM a[108:111],a[188:191],v[76:79],a[108:111]
s_mov_b32 m0, s91
s_nop 0
buffer_load_dword v96, s[24:27], s90 offen offset:0 lds
MM a[100:103],a[188:191],v[68:71],a[100:103]
s_lshr_b32 s77, %[sb], 3
s_lshl_b32 s77, s77, 2
s_add_u32 s77, s77, %[swave]
MM a[100:103],a[184:187],v[64:67],a[100:103]
s_lshl_b32 s77, s77, 9
s_add_u32 s77, s77, 0
v_lshrrev_b32 v253, 1, v248
v_smfmac_f32_16x16x64_bf16 v[144:147], v[236:239], v[0:7], v234
v_add_u32 v253, s77, v253
v_smfmac_f32_16x16x64_bf16 v[148:151], v[236:239], v[8:15], v234
s_mov_b32 s77, LKEND_ka-LPD1_%=-16
v_min_u32 v253, s77, v253
v_smfmac_f32_16x16x64_bf16 v[152:155], v[236:239], v[16:23], v234
v_smfmac_f32_16x16x64_bf16 v[156:159], v[236:239], v[24:31], v234
s_waitcnt lgkmcnt(0)
s_getpc_b64 s[98:99]
LPD1_%=:
global_load_dword v253, v253, s[98:99]
MM v[64:67],a[192:195],v[160:163],v[128:131]
s_add_u32 s64, s64, 16384
TR a[128:129],v232 offset:0
s_add_u32 s81, s64, 896
MM v[64:67],a[196:199],v[176:179],v[64:67]
TR a[130:131],v232 offset:4096
TR a[132:133],v232 offset:8192
MM v[64:67],a[200:203],v[192:195],v[64:67]
v_max3_f32 v249, v144, v145, v148
v_max3_f32 v249, v249, v149, v152
MM v[64:67],a[204:207],v[208:211],v[64:67]
v_max3_f32 v249, v249, v153, v156
v_max_f32 v249, v249, v157
MM v[68:71],a[220:223],v[208:211],v[128:131]
TR a[134:135],v232 offset:12288
v_cmp_lt_f32 vcc, s71, v249
MM v[68:71],a[216:219],v[192:195],v[68:71]
s_cbranch_vccnz LSITE24_%=
LBACK24_%=:
TR a[136:137],v233 offset:0
v_add_f32 v244, v144, v145
MM v[68:71],a[212:215],v[176:179],v[68:71]
v_add_f32 v245, v148, v149
v_add_f32 v246, v152, v153
MM v[68:71],a[208:211],v[160:163],v[68:71]
TR a[138:139],v233 offset:4096
v_add_f32 v247, v156, v157
MM v[76:79],a[208:211],v[164:167],v[132:135]
EX v64,v64
MM v[76:79],a[212:215],v[180:183],v[76:79]
TR a[140:141],v233 offset:8192
TR a[142:143],v233 offset:12288
MM v[76:79],a[216:219],v[196:199],v[76:79]
EX v65,v65
MM v[76:79],a[220:223],v[212:215],v[76:79]
TR a[144:145],v232 offset:512
CV v64,v64,v65
MM v[72:75],a[204:207],v[212:215],v[132:135]
EX v66,v66
MM v[72:75],a[200:203],v[196:199],v[72:75]
EX v67,v67
MM v[72:75],a[196:199],v[180:183],v[72:75]
TR a[146:147],v232 offset:4608
CV v65,v66,v67
MM v[72:75],a[192:195],v[164:167],v[72:75]
EX v68,v68
MM v[80:83],a[192:195],v[168:171],v[136:139]
TR a[148:149],v232 offset:8704
TR a[150:151],v232 offset:12800
MM v[80:83],a[196:199],v[184:187],v[80:83]
EX v69,v69
MM v[80:83],a[200:203],v[200:203],v[80:83]
TR a[152:153],v233 offset:512
CV v66,v68,v69
MM v[80:83],a[204:207],v[216:219],v[80:83]
EX v70,v70
MM v[84:87],a[220:223],v[216:219],v[136:139]
s_add_u32 s87, s95, 4096
buffer_store_dword v240, v248, s[20:23], s87 offen offset:12 sc1
MM v[84:87],a[216:219],v[200:203],v[84:87]
EX v71,v71
MM v[84:87],a[212:215],v[184:187],v[84:87]
TR a[154:155],v233 offset:4608
CV v67,v70,v71
MM v[84:87],a[208:211],v[168:171],v[84:87]
EX v72,v72
MM v[92:95],a[208:211],v[172:175],v[140:143]
TR a[156:157],v233 offset:8704
TR a[158:159],v233 offset:12800
MM v[92:95],a[212:215],v[188:191],v[92:95]
EX v73,v73
MM v[92:95],a[216:219],v[204:207],v[92:95]
TR a[160:161],v232 offset:1024
CV v72,v72,v73
MM v[92:95],a[220:223],v[220:223],v[92:95]
EX v74,v74
MM v[88:91],a[204:207],v[220:223],v[140:143]
EX v75,v75
MM v[88:91],a[200:203],v[204:207],v[88:91]
TR a[162:163],v232 offset:5120
CV v73,v74,v75
MM v[88:91],a[196:199],v[188:191],v[88:91]
EX v76,v76
MM v[88:91],a[192:195],v[172:175],v[88:91]
TR a[164:165],v232 offset:9216
TR a[166:167],v232 offset:13312
MM v[96:99],a[224:227],v[160:163],v[128:131]
EX v77,v77
MM v[96:99],a[228:231],v[176:179],v[96:99]
TR a[168:169],v233 offset:1024
CV v74,v76,v77
MM v[96:99],a[232:235],v[192:195],v[96:99]
EX v78,v78
MM v[96:99],a[236:239],v[208:211],v[96:99]
EX v79,v79
MM v[100:103],a[252:255],v[208:211],v[128:131]
TR a[170:171],v233 offset:5120
CV v75,v78,v79
MM v[100:103],a[248:251],v[192:195],v[100:103]
EX v80,v80
MM v[100:103],a[244:247],v[176:179],v[100:103]
TR a[172:173],v233 offset:9216
TR a[174:175],v233 offset:13312
MM v[100:103],a[240:243],v[160:163],v[100:103]
EX v81,v81
MM v[108:111],a[240:243],v[164:167],v[132:135]
TR a[176:177],v232 offset:1536
CV v80,v80,v81
MM v[108:111],a[244:247],v[180:183],v[108:111]
EX v82,v82
MM v[108:111],a[248:251],v[196:199],v[108:111]
TR a[178:179],v232 offset:5632
TR a[180:181],v232 offset:9728
MM v[108:111],a[252:255],v[212:215],v[108:111]
EX v83,v83
MM v[104:107],a[236:239],v[212:215],v[132:135]
TR a[182:183],v232 offset:13824
CV v81,v82,v83
MM v[104:107],a[232:235],v[196:199],v[104:107]
EX v84,v84
MM v[104:107],a[228:231],v[180:183],v[104:107]
EX v85,v85
MM v[104:107],a[224:227],v[164:167],v[104:107]
TR a[184:185],v233 offset:1536
CV v82,v84,v85
MM v[112:115],a[224:227],v[168:171],v[136:139]
EX v86,v86
MM v[112:115],a[228:231],v[184:187],v[112:115]
TR a[186:187],v233 offset:5632
TR a[188:189],v233 offset:9728
MM v[112:115],a[232:235],v[200:203],v[112:115]
EX v87,v87
MM v[112:115],a[236:239],v[216:219],v[112:115]
TR a[190:191],v233 offset:13824
CV v83,v86,v87
MM v[116:119],a[252:255],v[216:219],v[136:139]
EX v88,v88
MM v[116:119],a[248:251],v[200:203],v[116:119]
EX v89,v89
MM v[116:119],a[244:247],v[184:187],v[116:119]
EX v90,v90
MM v[116:119],a[240:243],v[168:171],v[116:119]
EX v91,v91
MM v[124:127],a[240:243],v[172:175],v[140:143]
EX v92,v92
MM v[124:127],a[244:247],v[188:191],v[124:127]
EX v93,v93
MM v[124:127],a[248:251],v[204:207],v[124:127]
EX v94,v94
MM v[124:127],a[252:255],v[220:223],v[124:127]
EX v95,v95
MM v[120:123],a[236:239],v[220:223],v[140:143]
)ASM"
R"ASM(
EX v96,v96
MM v[120:123],a[232:235],v[204:207],v[120:123]
EX v97,v97
MM v[120:123],a[228:231],v[188:191],v[120:123]
EX v98,v98
MM v[120:123],a[224:227],v[172:175],v[120:123]
s_waitcnt vmcnt(16) lgkmcnt(0)
s_barrier
MM a[0:3],a[128:131],v[0:3],a[0:3]
s_add_u32 m0, s65, 0
CV v88,v88,v89
CV v89,v90,v91
MM a[0:3],a[132:135],v[4:7],a[0:3]
RD a[192:195],v224 offset:32768
CV v90,v92,v93
MM a[8:11],a[132:135],v[12:15],a[8:11]
RD a[196:199],v225 offset:32768
CV v91,v94,v95
MM a[8:11],a[128:131],v[8:11],a[8:11]
buffer_load_dwordx4 v228, s[8:11], s64 offen offset:0 lds
MM a[16:19],a[128:131],v[16:19],a[16:19]
RD a[200:203],v226 offset:32768
CV v68,v96,v97
MM a[16:19],a[132:135],v[20:23],a[16:19]
buffer_load_dwordx4 v229, s[8:11], s64 offen offset:1024 lds
MM a[24:27],a[132:135],v[28:31],a[24:27]
RD a[204:207],v227 offset:32768
RD a[208:211],v224 offset:36864
MM a[24:27],a[128:131],v[24:27],a[24:27]
EX v99,v99
MM a[28:31],a[136:139],v[24:27],a[28:31]
RD a[212:215],v225 offset:36864
CV v69,v98,v99
MM a[28:31],a[140:143],v[28:31],a[28:31]
EX v100,v100
MM a[20:23],a[140:143],v[20:23],a[20:23]
buffer_load_dwordx4 v230, s[8:11], s64 offen offset:2048 lds
MM a[20:23],a[136:139],v[16:19],a[20:23]
EX v101,v101
MM a[12:15],a[136:139],v[8:11],a[12:15]
RD a[216:219],v226 offset:36864
CV v70,v100,v101
MM a[12:15],a[140:143],v[12:15],a[12:15]
EX v102,v102
MM a[4:7],a[140:143],v[4:7],a[4:7]
EX v103,v103
MM a[4:7],a[136:139],v[0:3],a[4:7]
RD a[220:223],v227 offset:36864
CV v71,v102,v103
MM a[32:35],a[144:147],v[0:3],a[32:35]
EX v104,v104
MM a[32:35],a[148:151],v[4:7],a[32:35]
buffer_load_dwordx4 v231, s[8:11], s64 offen offset:3072 lds
MM a[40:43],a[148:151],v[12:15],a[40:43]
s_add_u32 m0, s65, 65536
EX v105,v105
MM a[40:43],a[144:147],v[8:11],a[40:43]
RD a[224:227],v224 offset:40960
CV v76,v104,v105
MM a[48:51],a[144:147],v[16:19],a[48:51]
EX v106,v106
MM a[48:51],a[148:151],v[20:23],a[48:51]
EX v107,v107
MM a[56:59],a[148:151],v[28:31],a[56:59]
RD a[228:231],v225 offset:40960
CV v77,v106,v107
MM a[56:59],a[144:147],v[24:27],a[56:59]
EX v108,v108
MM a[60:63],a[152:155],v[24:27],a[60:63]
buffer_load_dwordx4 v235, s[12:15], s81 offen offset:0 lds
MM a[60:63],a[156:159],v[28:31],a[60:63]
EX v109,v109
MM a[52:55],a[156:159],v[20:23],a[52:55]
RD a[232:235],v226 offset:40960
CV v78,v108,v109
MM a[52:55],a[152:155],v[16:19],a[52:55]
EX v110,v110
MM a[44:47],a[152:155],v[8:11],a[44:47]
EX v111,v111
MM a[44:47],a[156:159],v[12:15],a[44:47]
RD a[236:239],v227 offset:40960
CV v79,v110,v111
MM a[36:39],a[156:159],v[4:7],a[36:39]
buffer_load_dwordx4 v235, s[12:15], s64 offen offset:1024 lds
MM a[36:39],a[152:155],v[0:3],a[36:39]
EX v112,v112
MM a[64:67],a[160:163],v[0:3],a[64:67]
EX v113,v113
MM a[64:67],a[164:167],v[4:7],a[64:67]
RD a[240:243],v224 offset:45056
CV v84,v112,v113
MM a[72:75],a[164:167],v[12:15],a[72:75]
EX v114,v114
MM a[72:75],a[160:163],v[8:11],a[72:75]
RD a[244:247],v225 offset:45056
RD a[248:251],v226 offset:45056
MM a[80:83],a[160:163],v[16:19],a[80:83]
EX v115,v115
MM a[80:83],a[164:167],v[20:23],a[80:83]
RD a[252:255],v227 offset:45056
CV v85,v114,v115
MM a[88:91],a[164:167],v[28:31],a[88:91]
EX v116,v116
MM a[88:91],a[160:163],v[24:27],a[88:91]
buffer_load_dwordx4 v235, s[12:15], s81 offen offset:2048 lds
MM a[92:95],a[168:171],v[24:27],a[92:95]
EX v117,v117
MM a[92:95],a[172:175],v[28:31],a[92:95]
EX v118,v118
MM a[84:87],a[172:175],v[20:23],a[84:87]
buffer_load_dwordx4 v235, s[12:15], s64 offen offset:3072 lds
MM a[84:87],a[168:171],v[16:19],a[84:87]
EX v119,v119
MM a[76:79],a[168:171],v[8:11],a[76:79]
EX v120,v120
MM a[76:79],a[172:175],v[12:15],a[76:79]
EX v121,v121
MM a[68:71],a[172:175],v[4:7],a[68:71]
EX v122,v122
MM a[68:71],a[168:171],v[0:3],a[68:71]
EX v123,v123
MM a[96:99],a[176:179],v[0:3],a[96:99]
EX v124,v124
MM a[96:99],a[180:183],v[4:7],a[96:99]
EX v125,v125
MM a[104:107],a[180:183],v[12:15],a[104:107]
EX v126,v126
MM a[104:107],a[176:179],v[8:11],a[104:107]
EX v127,v127
MM a[112:115],a[176:179],v[16:19],a[112:115]
CV v86,v116,v117
CV v87,v118,v119
MM a[112:115],a[180:183],v[20:23],a[112:115]
CV v92,v120,v121
CV v93,v122,v123
MM a[120:123],a[180:183],v[28:31],a[120:123]
CV v94,v124,v125
CV v95,v126,v127
MM a[120:123],a[176:179],v[24:27],a[120:123]
MM a[124:127],a[184:187],v[24:27],a[124:127]
MM a[124:127],a[188:191],v[28:31],a[124:127]
MM a[116:119],a[188:191],v[20:23],a[116:119]
MM a[116:119],a[184:187],v[16:19],a[116:119]
MM a[108:111],a[184:187],v[8:11],a[108:111]
MM a[108:111],a[188:191],v[12:15],a[108:111]
MM a[100:103],a[188:191],v[4:7],a[100:103]
MM a[100:103],a[184:187],v[0:3],a[100:103]
v_smfmac_f32_16x16x64_bf16 v[144:147], v[236:239], v[64:71], v234
v_smfmac_f32_16x16x64_bf16 v[148:151], v[236:239], v[72:79], v234
v_smfmac_f32_16x16x64_bf16 v[152:155], v[236:239], v[80:87], v234
v_smfmac_f32_16x16x64_bf16 v[156:159], v[236:239], v[88:95], v234
s_waitcnt lgkmcnt(0)
MM v[0:3],a[192:195],v[160:163],v[128:131]
s_add_u32 s64, s64, 16384
TR a[128:129],v232 offset:16384
s_add_u32 s81, s64, 896
MM v[0:3],a[196:199],v[176:179],v[0:3]
TR a[130:131],v232 offset:20480
TR a[132:133],v232 offset:24576
MM v[0:3],a[200:203],v[192:195],v[0:3]
v_max3_f32 v249, v144, v145, v148
v_max3_f32 v249, v249, v149, v152
MM v[0:3],a[204:207],v[208:211],v[0:3]
v_max3_f32 v249, v249, v153, v156
v_max_f32 v249, v249, v157
MM v[4:7],a[220:223],v[208:211],v[128:131]
TR a[134:135],v232 offset:28672
v_cmp_lt_f32 vcc, s71, v249
MM v[4:7],a[216:219],v[192:195],v[4:7]
s_cbranch_vccnz LSITE25_%=
LBACK25_%=:
TR a[136:137],v233 offset:16384
v_add_f32 v244, v144, v145
MM v[4:7],a[212:215],v[176:179],v[4:7]
s_waitcnt vmcnt(9)
v_add_f32 v245, v148, v149
v_add_f32 v246, v152, v153
MM v[4:7],a[208:211],v[160:163],v[4:7]
s_mul_i32 s76, s46, 96
TR a[138:139],v233 offset:20480
v_add_f32 v247, v156, v157
MM v[12:15],a[208:211],v[164:167],v[132:135]
s_add_u32 s76, s76, 69632
v_lshrrev_b32 v253, 2, v248
v_min_u32 v253, 92, v253
MM v[12:15],a[212:215],v[180:183],v[12:15]
buffer_load_dword v252, v253, s[20:23], s76 offen offset:0 sc1
MM v[12:15],a[216:219],v[196:199],v[12:15]
EX v0,v0
MM v[12:15],a[220:223],v[212:215],v[12:15]
EX v1,v1
MM v[8:11],a[204:207],v[212:215],v[132:135]
TR a[140:141],v233 offset:24576
CV v0,v0,v1
MM v[8:11],a[200:203],v[196:199],v[8:11]
EX v2,v2
MM v[8:11],a[196:199],v[180:183],v[8:11]
TR a[142:143],v233 offset:28672
TR a[144:145],v232 offset:16896
MM v[8:11],a[192:195],v[164:167],v[8:11]
EX v3,v3
MM v[16:19],a[192:195],v[168:171],v[136:139]
EX v4,v4
MM v[16:19],a[196:199],v[184:187],v[16:19]
TR a[146:147],v232 offset:20992
CV v1,v2,v3
MM v[16:19],a[200:203],v[200:203],v[16:19]
EX v5,v5
MM v[16:19],a[204:207],v[216:219],v[16:19]
TR a[148:149],v232 offset:25088
CV v2,v4,v5
MM v[20:23],a[220:223],v[216:219],v[136:139]
EX v6,v6
MM v[20:23],a[216:219],v[200:203],v[20:23]
TR a[150:151],v232 offset:29184
TR a[152:153],v233 offset:16896
MM v[20:23],a[212:215],v[184:187],v[20:23]
EX v7,v7
MM v[20:23],a[208:211],v[168:171],v[20:23]
TR a[154:155],v233 offset:20992
CV v3,v6,v7
MM v[28:31],a[208:211],v[172:175],v[140:143]
EX v8,v8
MM v[28:31],a[212:215],v[188:191],v[28:31]
EX v9,v9
MM v[28:31],a[216:219],v[204:207],v[28:31]
TR a[156:157],v233 offset:25088
CV v8,v8,v9
MM v[28:31],a[220:223],v[220:223],v[28:31]
EX v10,v10
MM v[24:27],a[204:207],v[220:223],v[140:143]
TR a[158:159],v233 offset:29184
TR a[160:161],v232 offset:17408
MM v[24:27],a[200:203],v[204:207],v[24:27]
EX v11,v11
MM v[24:27],a[196:199],v[188:191],v[24:27]
TR a[162:163],v232 offset:21504
CV v9,v10,v11
MM v[24:27],a[192:195],v[172:175],v[24:27]
EX v12,v12
MM v[32:35],a[224:227],v[160:163],v[128:131]
TR a[164:165],v232 offset:25600
TR a[166:167],v232 offset:29696
MM v[32:35],a[228:231],v[176:179],v[32:35]
EX v13,v13
MM v[32:35],a[232:235],v[192:195],v[32:35]
TR a[168:169],v233 offset:17408
CV v10,v12,v13
MM v[32:35],a[236:239],v[208:211],v[32:35]
EX v14,v14
MM v[36:39],a[252:255],v[208:211],v[128:131]
EX v15,v15
MM v[36:39],a[248:251],v[192:195],v[36:39]
TR a[170:171],v233 offset:21504
CV v11,v14,v15
MM v[36:39],a[244:247],v[176:179],v[36:39]
EX v16,v16
MM v[36:39],a[240:243],v[160:163],v[36:39]
TR a[172:173],v233 offset:25600
TR a[174:175],v233 offset:29696
MM v[44:47],a[240:243],v[164:167],v[132:135]
EX v17,v17
MM v[44:47],a[244:247],v[180:183],v[44:47]
TR a[176:177],v232 offset:17920
CV v16,v16,v17
MM v[44:47],a[248:251],v[196:199],v[44:47]
EX v18,v18
MM v[44:47],a[252:255],v[212:215],v[44:47]
EX v19,v19
MM v[40:43],a[236:239],v[212:215],v[132:135]
TR a[178:179],v232 offset:22016
CV v17,v18,v19
MM v[40:43],a[232:235],v[196:199],v[40:43]
EX v20,v20
MM v[40:43],a[228:231],v[180:183],v[40:43]
TR a[180:181],v232 offset:26112
TR a[182:183],v232 offset:30208
MM v[40:43],a[224:227],v[164:167],v[40:43]
EX v21,v21
MM v[48:51],a[224:227],v[168:171],v[136:139]
TR a[184:185],v233 offset:17920
CV v18,v20,v21
MM v[48:51],a[228:231],v[184:187],v[48:51]
EX v22,v22
MM v[48:51],a[232:235],v[200:203],v[48:51]
EX v23,v23
MM v[48:51],a[236:239],v[216:219],v[48:51]
TR a[186:187],v233 offset:22016
CV v19,v22,v23
MM v[52:55],a[252:255],v[216:219],v[136:139]
EX v24,v24
MM v[52:55],a[248:251],v[200:203],v[52:55]
TR a[188:189],v233 offset:26112
TR a[190:191],v233 offset:30208
MM v[52:55],a[244:247],v[184:187],v[52:55]
EX v25,v25
MM v[52:55],a[240:243],v[168:171],v[52:55]
EX v26,v26
MM v[60:63],a[240:243],v[172:175],v[140:143]
EX v27,v27
LPG18_%=:
s_cbranch_execz LTR3_%=
MM v[60:63],a[244:247],v[188:191],v[60:63]
EX v28,v28
MM v[60:63],a[248:251],v[204:207],v[60:63]
EX v29,v29
MM v[60:63],a[252:255],v[220:223],v[60:63]
EX v30,v30
MM v[56:59],a[236:239],v[220:223],v[140:143]
EX v31,v31
MM v[56:59],a[232:235],v[204:207],v[56:59]
EX v32,v32
MM v[56:59],a[228:231],v[188:191],v[56:59]
EX v33,v33
MM v[56:59],a[224:227],v[172:175],v[56:59]
s_waitcnt lgkmcnt(0)
s_barrier
MM a[0:3],a[128:131],v[64:67],a[0:3]
s_add_u32 m0, s65, 16384
CV v24,v24,v25
CV v25,v26,v27
MM a[0:3],a[132:135],v[68:71],a[0:3]
RD a[192:195],v224 offset:49152
CV v26,v28,v29
MM a[8:11],a[132:135],v[76:79],a[8:11]
RD a[196:199],v225 offset:49152
CV v27,v30,v31
MM a[8:11],a[128:131],v[72:75],a[8:11]
buffer_load_dwordx4 v228, s[8:11], s64 offen offset:0 lds
MM a[16:19],a[128:131],v[80:83],a[16:19]
RD a[200:203],v226 offset:49152
CV v4,v32,v33
MM a[16:19],a[132:135],v[84:87],a[16:19]
buffer_load_dwordx4 v229, s[8:11], s64 offen offset:1024 lds
MM a[24:27],a[132:135],v[92:95],a[24:27]
EX v34,v34
MM a[24:27],a[128:131],v[88:91],a[24:27]
RD a[204:207],v227 offset:49152
RD a[208:211],v224 offset:53248
MM a[28:31],a[136:139],v[88:91],a[28:31]
EX v35,v35
MM a[28:31],a[140:143],v[92:95],a[28:31]
RD a[212:215],v225 offset:53248
CV v5,v34,v35
MM a[20:23],a[140:143],v[84:87],a[20:23]
EX v36,v36
MM a[20:23],a[136:139],v[80:83],a[20:23]
EX v37,v37
MM a[12:15],a[136:139],v[72:75],a[12:15]
RD a[216:219],v226 offset:53248
CV v6,v36,v37
MM a[12:15],a[140:143],v[76:79],a[12:15]
buffer_load_dwordx4 v230, s[8:11], s64 offen offset:2048 lds
MM a[4:7],a[140:143],v[68:71],a[4:7]
EX v38,v38
MM a[4:7],a[136:139],v[64:67],a[4:7]
EX v39,v39
MM a[32:35],a[144:147],v[64:67],a[32:35]
RD a[220:223],v227 offset:53248
CV v7,v38,v39
MM a[32:35],a[148:151],v[68:71],a[32:35]
EX v40,v40
MM a[40:43],a[148:151],v[76:79],a[40:43]
EX v41,v41
MM a[40:43],a[144:147],v[72:75],a[40:43]
RD a[224:227],v224 offset:57344
CV v12,v40,v41
MM a[48:51],a[144:147],v[80:83],a[48:51]
buffer_load_dwordx4 v231, s[8:11], s64 offen offset:3072 lds
MM a[48:51],a[148:151],v[84:87],a[48:51]
s_add_u32 m0, s65, 81920
EX v42,v42
MM a[56:59],a[148:151],v[92:95],a[56:59]
EX v43,v43
MM a[56:59],a[144:147],v[88:91],a[56:59]
RD a[228:231],v225 offset:57344
CV v13,v42,v43
MM a[60:63],a[152:155],v[88:91],a[60:63]
EX v44,v44
MM a[60:63],a[156:159],v[92:95],a[60:63]
buffer_load_dwordx4 v235, s[12:15], s81 offen offset:0 lds
MM a[52:55],a[156:159],v[84:87],a[52:55]
EX v45,v45
MM a[52:55],a[152:155],v[80:83],a[52:55]
RD a[232:235],v226 offset:57344
CV v14,v44,v45
MM a[44:47],a[152:155],v[72:75],a[44:47]
EX v46,v46
MM a[44:47],a[156:159],v[76:79],a[44:47]
EX v47,v47
MM a[36:39],a[156:159],v[68:71],a[36:39]
)ASM"
R"ASM(
RD a[236:239],v227 offset:57344
CV v15,v46,v47
MM a[36:39],a[152:155],v[64:67],a[36:39]
EX v48,v48
MM a[64:67],a[160:163],v[64:67],a[64:67]
buffer_load_dwordx4 v235, s[12:15], s64 offen offset:1024 lds
MM a[64:67],a[164:167],v[68:71],a[64:67]
EX v49,v49
MM a[72:75],a[164:167],v[76:79],a[72:75]
RD a[240:243],v224 offset:61440
CV v20,v48,v49
MM a[72:75],a[160:163],v[72:75],a[72:75]
EX v50,v50
MM a[80:83],a[160:163],v[80:83],a[80:83]
EX v51,v51
MM a[80:83],a[164:167],v[84:87],a[80:83]
RD a[244:247],v225 offset:61440
CV v21,v50,v51
MM a[88:91],a[164:167],v[92:95],a[88:91]
EX v52,v52
MM a[88:91],a[160:163],v[88:91],a[88:91]
buffer_load_dwordx4 v235, s[12:15], s81 offen offset:2048 lds
MM a[92:95],a[168:171],v[88:91],a[92:95]
EX v53,v53
MM a[92:95],a[172:175],v[92:95],a[92:95]
RD a[248:251],v226 offset:61440
CV v22,v52,v53
MM a[84:87],a[172:175],v[84:87],a[84:87]
EX v54,v54
MM a[84:87],a[168:171],v[80:83],a[84:87]
EX v55,v55
MM a[76:79],a[168:171],v[72:75],a[76:79]
RD a[252:255],v227 offset:61440
CV v23,v54,v55
MM a[76:79],a[172:175],v[76:79],a[76:79]
EX v56,v56
MM a[68:71],a[172:175],v[68:71],a[68:71]
buffer_load_dwordx4 v235, s[12:15], s64 offen offset:3072 lds
MM a[68:71],a[168:171],v[64:67],a[68:71]
EX v57,v57
MM a[96:99],a[176:179],v[64:67],a[96:99]
EX v58,v58
MM a[96:99],a[180:183],v[68:71],a[96:99]
EX v59,v59
MM a[104:107],a[180:183],v[76:79],a[104:107]
EX v60,v60
MM a[104:107],a[176:179],v[72:75],a[104:107]
EX v61,v61
MM a[112:115],a[176:179],v[80:83],a[112:115]
EX v62,v62
MM a[112:115],a[180:183],v[84:87],a[112:115]
EX v63,v63
MM a[120:123],a[180:183],v[92:95],a[120:123]
s_waitcnt vmcnt(8)
v_cmp_ne_u32 vcc, s69, v252
s_cbranch_vccnz LRFH_%=
LRFHb_%=:
MM a[120:123],a[176:179],v[88:91],a[120:123]
v_bfe_u32 v253, v248, 4, 3
v_mul_u32_u24 v253, 3456, v253
MM a[124:127],a[184:187],v[88:91],a[124:127]
v_lshrrev_b32 v252, 7, v248
v_lshl_add_u32 v253, v252, 4, v253
MM a[124:127],a[188:191],v[92:95],a[124:127]
s_min_u32 s90, %[swave], 2
CV v28,v56,v57
CV v29,v58,v59
MM a[116:119],a[188:191],v[84:87],a[116:119]
s_mul_i32 s76, s46, 384
CV v30,v60,v61
CV v31,v62,v63
MM a[116:119],a[184:187],v[80:83],a[116:119]
s_lshl_b32 s91, s90, 7
s_add_u32 s76, s76, s91
s_add_u32 s76, s76, 21626880
MM a[108:111],a[184:187],v[72:75],a[108:111]
s_lshl_b32 s90, s90, 10
s_add_u32 s90, s90, %[slds]
MM a[108:111],a[188:191],v[76:79],a[108:111]
s_add_u32 m0, s90, 131072
s_nop 0
buffer_load_dwordx4 v253, s[20:23], s76 offen offset:0 sc1 lds
MM a[100:103],a[188:191],v[68:71],a[100:103]
MM a[100:103],a[184:187],v[64:67],a[100:103]
v_smfmac_f32_16x16x64_bf16 v[144:147], v[236:239], v[0:7], v234
v_smfmac_f32_16x16x64_bf16 v[148:151], v[236:239], v[8:15], v234
v_smfmac_f32_16x16x64_bf16 v[152:155], v[236:239], v[16:23], v234
v_smfmac_f32_16x16x64_bf16 v[156:159], v[236:239], v[24:31], v234
s_waitcnt lgkmcnt(0)
MM v[64:67],a[192:195],v[160:163],v[128:131]
s_add_u32 s64, s64, 16384
TR a[128:129],v232 offset:32768
s_add_u32 s81, s64, 896
MM v[64:67],a[196:199],v[176:179],v[64:67]
TR a[130:131],v232 offset:36864
TR a[132:133],v232 offset:40960
MM v[64:67],a[200:203],v[192:195],v[64:67]
v_max3_f32 v249, v144, v145, v148
v_max3_f32 v249, v249, v149, v152
MM v[64:67],a[204:207],v[208:211],v[64:67]
v_max3_f32 v249, v249, v153, v156
v_max_f32 v249, v249, v157
MM v[68:71],a[220:223],v[208:211],v[128:131]
TR a[134:135],v232 offset:45056
v_cmp_lt_f32 vcc, s71, v249
MM v[68:71],a[216:219],v[192:195],v[68:71]
s_cbranch_vccnz LSITE26_%=
LBACK26_%=:
TR a[136:137],v233 offset:32768
v_add_f32 v244, v144, v145
MM v[68:71],a[212:215],v[176:179],v[68:71]
v_add_f32 v245, v148, v149
v_add_f32 v246, v152, v153
MM v[68:71],a[208:211],v[160:163],v[68:71]
TR a[138:139],v233 offset:36864
v_add_f32 v247, v156, v157
MM v[76:79],a[208:211],v[164:167],v[132:135]
EX v64,v64
MM v[76:79],a[212:215],v[180:183],v[76:79]
TR a[140:141],v233 offset:40960
TR a[142:143],v233 offset:45056
MM v[76:79],a[216:219],v[196:199],v[76:79]
EX v65,v65
MM v[76:79],a[220:223],v[212:215],v[76:79]
TR a[144:145],v232 offset:33280
CV v64,v64,v65
MM v[72:75],a[204:207],v[212:215],v[132:135]
EX v66,v66
MM v[72:75],a[200:203],v[196:199],v[72:75]
EX v67,v67
MM v[72:75],a[196:199],v[180:183],v[72:75]
TR a[146:147],v232 offset:37376
CV v65,v66,v67
MM v[72:75],a[192:195],v[164:167],v[72:75]
EX v68,v68
MM v[80:83],a[192:195],v[168:171],v[136:139]
TR a[148:149],v232 offset:41472
TR a[150:151],v232 offset:45568
MM v[80:83],a[196:199],v[184:187],v[80:83]
EX v69,v69
MM v[80:83],a[200:203],v[200:203],v[80:83]
TR a[152:153],v233 offset:33280
CV v66,v68,v69
MM v[80:83],a[204:207],v[216:219],v[80:83]
EX v70,v70
MM v[84:87],a[220:223],v[216:219],v[136:139]
EX v71,v71
MM v[84:87],a[216:219],v[200:203],v[84:87]
TR a[154:155],v233 offset:37376
CV v67,v70,v71
MM v[84:87],a[212:215],v[184:187],v[84:87]
EX v72,v72
MM v[84:87],a[208:211],v[168:171],v[84:87]
TR a[156:157],v233 offset:41472
TR a[158:159],v233 offset:45568
MM v[92:95],a[208:211],v[172:175],v[140:143]
EX v73,v73
MM v[92:95],a[212:215],v[188:191],v[92:95]
TR a[160:161],v232 offset:33792
CV v72,v72,v73
MM v[92:95],a[216:219],v[204:207],v[92:95]
EX v74,v74
MM v[92:95],a[220:223],v[220:223],v[92:95]
EX v75,v75
MM v[88:91],a[204:207],v[220:223],v[140:143]
TR a[162:163],v232 offset:37888
CV v73,v74,v75
MM v[88:91],a[200:203],v[204:207],v[88:91]
TR a[164:165],v232 offset:41984
TR a[166:167],v232 offset:46080
MM v[88:91],a[196:199],v[188:191],v[88:91]
EX v76,v76
MM v[88:91],a[192:195],v[172:175],v[88:91]
EX v77,v77
MM v[96:99],a[224:227],v[160:163],v[128:131]
TR a[168:169],v233 offset:33792
CV v74,v76,v77
MM v[96:99],a[228:231],v[176:179],v[96:99]
EX v78,v78
MM v[96:99],a[232:235],v[192:195],v[96:99]
TR a[170:171],v233 offset:37888
TR a[172:173],v233 offset:41984
MM v[96:99],a[236:239],v[208:211],v[96:99]
EX v79,v79
MM v[100:103],a[252:255],v[208:211],v[128:131]
TR a[174:175],v233 offset:46080
CV v75,v78,v79
MM v[100:103],a[248:251],v[192:195],v[100:103]
EX v80,v80
MM v[100:103],a[244:247],v[176:179],v[100:103]
EX v81,v81
MM v[100:103],a[240:243],v[160:163],v[100:103]
TR a[176:177],v232 offset:34304
CV v80,v80,v81
MM v[108:111],a[240:243],v[164:167],v[132:135]
EX v82,v82
MM v[108:111],a[244:247],v[180:183],v[108:111]
TR a[178:179],v232 offset:38400
TR a[180:181],v232 offset:42496
MM v[108:111],a[248:251],v[196:199],v[108:111]
EX v83,v83
MM v[108:111],a[252:255],v[212:215],v[108:111]
TR a[182:183],v232 offset:46592
CV v81,v82,v83
MM v[104:107],a[236:239],v[212:215],v[132:135]
EX v84,v84
MM v[104:107],a[232:235],v[196:199],v[104:107]
EX v85,v85
MM v[104:107],a[228:231],v[180:183],v[104:107]
TR a[184:185],v233 offset:34304
CV v82,v84,v85
MM v[104:107],a[224:227],v[164:167],v[104:107]
EX v86,v86
MM v[112:115],a[224:227],v[168:171],v[136:139]
TR a[186:187],v233 offset:38400
TR a[188:189],v233 offset:42496
MM v[112:115],a[228:231],v[184:187],v[112:115]
EX v87,v87
MM v[112:115],a[232:235],v[200:203],v[112:115]
TR a[190:191],v233 offset:46592
CV v83,v86,v87
MM v[112:115],a[236:239],v[216:219],v[112:115]
EX v88,v88
MM v[116:119],a[252:255],v[216:219],v[136:139]
EX v89,v89
MM v[116:119],a[248:251],v[200:203],v[116:119]
EX v90,v90
MM v[116:119],a[244:247],v[184:187],v[116:119]
EX v91,v91
MM v[116:119],a[240:243],v[168:171],v[116:119]
EX v92,v92
MM v[124:127],a[240:243],v[172:175],v[140:143]
EX v93,v93
MM v[124:127],a[244:247],v[188:191],v[124:127]
EX v94,v94
MM v[124:127],a[248:251],v[204:207],v[124:127]
EX v95,v95
MM v[124:127],a[252:255],v[220:223],v[124:127]
EX v96,v96
MM v[120:123],a[236:239],v[220:223],v[140:143]
EX v97,v97
MM v[120:123],a[232:235],v[204:207],v[120:123]
EX v98,v98
MM v[120:123],a[228:231],v[188:191],v[120:123]
s_getpc_b64 s[98:99]
LTA4_%=:
s_min_u32 s73, %[swave], 0
s_lshl_b32 s73, s73, 12
s_add_u32 s73, s73, LPG21_%=-LTA4_%=
s_add_u32 s98, s98, s73
s_addc_u32 s99, s99, 0
s_mov_b64 exec, 0
s_setpc_b64 s[98:99]
LTR4_%=:
s_mov_b64 exec, -1
CV v88,v88,v89
MM v[120:123],a[224:227],v[172:175],v[120:123]
s_waitcnt lgkmcnt(0)
s_barrier
MM a[0:3],a[128:131],v[0:3],a[0:3]
s_add_u32 m0, s65, 32768
CV v89,v90,v91
CV v90,v92,v93
MM a[0:3],a[132:135],v[4:7],a[0:3]
RD a[192:195],v224 offset:0
CV v91,v94,v95
MM a[8:11],a[132:135],v[12:15],a[8:11]
buffer_load_dwordx4 v228, s[8:11], s64 offen offset:0 lds
MM a[8:11],a[128:131],v[8:11],a[8:11]
RD a[196:199],v225 offset:0
CV v68,v96,v97
MM a[16:19],a[128:131],v[16:19],a[16:19]
RD a[200:203],v226 offset:0
RD a[204:207],v227 offset:0
MM a[16:19],a[132:135],v[20:23],a[16:19]
buffer_load_dwordx4 v229, s[8:11], s64 offen offset:1024 lds
MM a[24:27],a[132:135],v[28:31],a[24:27]
RD a[208:211],v224 offset:4096
RD a[212:215],v225 offset:4096
MM a[24:27],a[128:131],v[24:27],a[24:27]
EX v99,v99
MM a[28:31],a[136:139],v[24:27],a[28:31]
RD a[216:219],v226 offset:4096
CV v69,v98,v99
MM a[28:31],a[140:143],v[28:31],a[28:31]
EX v100,v100
MM a[20:23],a[140:143],v[20:23],a[20:23]
buffer_load_dwordx4 v230, s[8:11], s64 offen offset:2048 lds
MM a[20:23],a[136:139],v[16:19],a[20:23]
EX v101,v101
MM a[12:15],a[136:139],v[8:11],a[12:15]
RD a[220:223],v227 offset:4096
CV v70,v100,v101
MM a[12:15],a[140:143],v[12:15],a[12:15]
EX v102,v102
MM a[4:7],a[140:143],v[4:7],a[4:7]
EX v103,v103
MM a[4:7],a[136:139],v[0:3],a[4:7]
RD a[224:227],v224 offset:8192
CV v71,v102,v103
MM a[32:35],a[144:147],v[0:3],a[32:35]
EX v104,v104
MM a[32:35],a[148:151],v[4:7],a[32:35]
buffer_load_dwordx4 v231, s[8:11], s64 offen offset:3072 lds
MM a[40:43],a[148:151],v[12:15],a[40:43]
s_add_u32 m0, s65, 98304
EX v105,v105
MM a[40:43],a[144:147],v[8:11],a[40:43]
RD a[228:231],v225 offset:8192
CV v76,v104,v105
MM a[48:51],a[144:147],v[16:19],a[48:51]
EX v106,v106
MM a[48:51],a[148:151],v[20:23],a[48:51]
EX v107,v107
MM a[56:59],a[148:151],v[28:31],a[56:59]
RD a[232:235],v226 offset:8192
CV v77,v106,v107
MM a[56:59],a[144:147],v[24:27],a[56:59]
EX v108,v108
MM a[60:63],a[152:155],v[24:27],a[60:63]
buffer_load_dwordx4 v235, s[12:15], s81 offen offset:0 lds
MM a[60:63],a[156:159],v[28:31],a[60:63]
EX v109,v109
MM a[52:55],a[156:159],v[20:23],a[52:55]
RD a[236:239],v227 offset:8192
CV v78,v108,v109
MM a[52:55],a[152:155],v[16:19],a[52:55]
EX v110,v110
MM a[44:47],a[152:155],v[8:11],a[44:47]
EX v111,v111
MM a[44:47],a[156:159],v[12:15],a[44:47]
RD a[240:243],v224 offset:12288
CV v79,v110,v111
MM a[36:39],a[156:159],v[4:7],a[36:39]
buffer_load_dwordx4 v235, s[12:15], s64 offen offset:1024 lds
MM a[36:39],a[152:155],v[0:3],a[36:39]
EX v112,v112
MM a[64:67],a[160:163],v[0:3],a[64:67]
EX v113,v113
MM a[64:67],a[164:167],v[4:7],a[64:67]
RD a[244:247],v225 offset:12288
CV v84,v112,v113
MM a[72:75],a[164:167],v[12:15],a[72:75]
EX v114,v114
MM a[72:75],a[160:163],v[8:11],a[72:75]
EX v115,v115
MM a[80:83],a[160:163],v[16:19],a[80:83]
RD a[248:251],v226 offset:12288
CV v85,v114,v115
MM a[80:83],a[164:167],v[20:23],a[80:83]
buffer_load_dwordx4 v235, s[12:15], s81 offen offset:2048 lds
MM a[88:91],a[164:167],v[28:31],a[88:91]
EX v116,v116
MM a[88:91],a[160:163],v[24:27],a[88:91]
EX v117,v117
MM a[92:95],a[168:171],v[24:27],a[92:95]
RD a[252:255],v227 offset:12288
CV v86,v116,v117
MM a[92:95],a[172:175],v[28:31],a[92:95]
EX v118,v118
MM a[84:87],a[172:175],v[20:23],a[84:87]
buffer_load_dwordx4 v235, s[12:15], s64 offen offset:3072 lds
MM a[84:87],a[168:171],v[16:19],a[84:87]
EX v119,v119
MM a[76:79],a[168:171],v[8:11],a[76:79]
CV v87,v118,v119
MM a[76:79],a[172:175],v[12:15],a[76:79]
EX v120,v120
MM a[68:71],a[172:175],v[4:7],a[68:71]
EX v121,v121
MM a[68:71],a[168:171],v[0:3],a[68:71]
EX v122,v122
MM a[96:99],a[176:179],v[0:3],a[96:99]
EX v123,v123
MM a[96:99],a[180:183],v[4:7],a[96:99]
EX v124,v124
MM a[104:107],a[180:183],v[12:15],a[104:107]
EX v125,v125
MM a[104:107],a[176:179],v[8:11],a[104:107]
EX v126,v126
MM a[112:115],a[176:179],v[16:19],a[112:115]
EX v127,v127
MM a[112:115],a[180:183],v[20:23],a[112:115]
CV v92,v120,v121
CV v93,v122,v123
MM a[120:123],a[180:183],v[28:31],a[120:123]
CV v94,v124,v125
CV v95,v126,v127
MM a[120:123],a[176:179],v[24:27],a[120:123]
MM a[124:127],a[184:187],v[24:27],a[124:127]
MM a[124:127],a[188:191],v[28:31],a[124:127]
MM a[116:119],a[188:191],v[20:23],a[116:119]
)ASM"
R"ASM(
MM a[116:119],a[184:187],v[16:19],a[116:119]
MM a[108:111],a[184:187],v[8:11],a[108:111]
MM a[108:111],a[188:191],v[12:15],a[108:111]
MM a[100:103],a[188:191],v[4:7],a[100:103]
MM a[100:103],a[184:187],v[0:3],a[100:103]
v_smfmac_f32_16x16x64_bf16 v[144:147], v[236:239], v[64:71], v234
v_smfmac_f32_16x16x64_bf16 v[148:151], v[236:239], v[72:79], v234
v_smfmac_f32_16x16x64_bf16 v[152:155], v[236:239], v[80:87], v234
v_smfmac_f32_16x16x64_bf16 v[156:159], v[236:239], v[88:95], v234
s_waitcnt lgkmcnt(0)
MM v[0:3],a[192:195],v[160:163],v[128:131]
s_add_u32 s64, s64, 16384
TR a[128:129],v232 offset:49152
s_add_u32 s81, s64, 896
MM v[0:3],a[196:199],v[176:179],v[0:3]
TR a[130:131],v232 offset:53248
TR a[132:133],v232 offset:57344
MM v[0:3],a[200:203],v[192:195],v[0:3]
v_max3_f32 v249, v144, v145, v148
v_max3_f32 v249, v249, v149, v152
MM v[0:3],a[204:207],v[208:211],v[0:3]
v_max3_f32 v249, v249, v153, v156
v_max_f32 v249, v249, v157
MM v[4:7],a[220:223],v[208:211],v[128:131]
TR a[134:135],v232 offset:61440
v_cmp_lt_f32 vcc, s71, v249
MM v[4:7],a[216:219],v[192:195],v[4:7]
s_cbranch_vccnz LSITE27_%=
LBACK27_%=:
TR a[136:137],v233 offset:49152
v_add_f32 v244, v144, v145
MM v[4:7],a[212:215],v[176:179],v[4:7]
v_add_f32 v245, v148, v149
v_add_f32 v246, v152, v153
MM v[4:7],a[208:211],v[160:163],v[4:7]
v_add_f32 v247, v156, v157
v_lshrrev_b32 v125, 8, v248
MM v[12:15],a[208:211],v[164:167],v[132:135]
v_lshlrev_b32 v124, 10, v125
v_bfe_u32 v126, v248, 6, 2
MM v[12:15],a[212:215],v[180:183],v[12:15]
v_lshl_add_u32 v124, v126, 7, v124
v_bfe_u32 v126, v248, 4, 2
MM v[12:15],a[216:219],v[196:199],v[12:15]
TR a[138:139],v233 offset:53248
v_lshl_add_u32 v124, v126, 3, v124
MM v[12:15],a[220:223],v[212:215],v[12:15]
s_lshl_b32 s91, %[swave], 5
EX v0,v0
MM v[8:11],a[204:207],v[212:215],v[132:135]
s_add_u32 s91, s91, %[slds]
EX v1,v1
MM v[8:11],a[200:203],v[196:199],v[8:11]
s_add_u32 s91, s91, 139264
v_add_u32 v124, s91, v124
v_and_b32 v126, 112, v248
MM v[8:11],a[196:199],v[180:183],v[8:11]
s_add_u32 s91, %[slds], 131072
v_lshl_add_u32 v126, v125, 7, v126
v_add_u32 v126, s91, v126
MM v[8:11],a[192:195],v[164:167],v[8:11]
v_bfe_u32 v127, v248, 4, 3
v_mul_u32_u24 v127, 2560, v127
MM v[16:19],a[192:195],v[168:171],v[136:139]
TR a[140:141],v233 offset:57344
v_lshl_add_u32 v127, v125, 4, v127
MM v[16:19],a[196:199],v[184:187],v[16:19]
s_lshl_b32 s90, %[swave], 6
EX v2,v2
MM v[16:19],a[200:203],v[200:203],v[16:19]
s_lshl_b32 s91, s47, 8
EX v3,v3
MM v[16:19],a[204:207],v[216:219],v[16:19]
s_add_u32 s90, s90, s91
EX v4,v4
MM v[20:23],a[220:223],v[216:219],v[136:139]
s_mul_i32 s91, s46, 17920
TR a[142:143],v233 offset:61440
TR a[144:145],v232 offset:49664
MM v[20:23],a[216:219],v[200:203],v[20:23]
s_add_u32 s90, s90, s91
EX v5,v5
MM v[20:23],a[212:215],v[184:187],v[20:23]
s_add_u32 s90, s90, 21659648
TR a[146:147],v232 offset:53760
CV v0,v0,v1
MM v[20:23],a[208:211],v[168:171],v[20:23]
EX v6,v6
MM v[28:31],a[208:211],v[172:175],v[140:143]
TR a[148:149],v232 offset:57856
TR a[150:151],v232 offset:61952
MM v[28:31],a[212:215],v[188:191],v[28:31]
EX v7,v7
MM v[28:31],a[216:219],v[204:207],v[28:31]
EX v8,v8
MM v[28:31],a[220:223],v[220:223],v[28:31]
TR a[152:153],v233 offset:49664
CV v1,v2,v3
MM v[24:27],a[204:207],v[220:223],v[140:143]
EX v9,v9
MM v[24:27],a[200:203],v[204:207],v[24:27]
TR a[154:155],v233 offset:53760
CV v2,v4,v5
MM v[24:27],a[196:199],v[188:191],v[24:27]
EX v10,v10
MM v[24:27],a[192:195],v[172:175],v[24:27]
TR a[156:157],v233 offset:57856
CV v3,v6,v7
MM v[32:35],a[224:227],v[160:163],v[128:131]
EX v11,v11
MM v[32:35],a[228:231],v[176:179],v[32:35]
TR a[158:159],v233 offset:61952
TR a[160:161],v232 offset:50176
MM v[32:35],a[232:235],v[192:195],v[32:35]
EX v12,v12
MM v[32:35],a[236:239],v[208:211],v[32:35]
LPG19_%=:
s_cbranch_execz LTR3_%=
EX v13,v13
MM v[36:39],a[252:255],v[208:211],v[128:131]
TR a[162:163],v232 offset:54272
CV v8,v8,v9
MM v[36:39],a[248:251],v[192:195],v[36:39]
EX v14,v14
MM v[36:39],a[244:247],v[176:179],v[36:39]
TR a[164:165],v232 offset:58368
CV v9,v10,v11
MM v[36:39],a[240:243],v[160:163],v[36:39]
EX v15,v15
MM v[44:47],a[240:243],v[164:167],v[132:135]
TR a[166:167],v232 offset:62464
TR a[168:169],v233 offset:50176
MM v[44:47],a[244:247],v[180:183],v[44:47]
EX v16,v16
MM v[44:47],a[248:251],v[196:199],v[44:47]
EX v17,v17
MM v[44:47],a[252:255],v[212:215],v[44:47]
TR a[170:171],v233 offset:54272
CV v10,v12,v13
MM v[40:43],a[236:239],v[212:215],v[132:135]
EX v18,v18
MM v[40:43],a[232:235],v[196:199],v[40:43]
TR a[172:173],v233 offset:58368
CV v11,v14,v15
MM v[40:43],a[228:231],v[180:183],v[40:43]
EX v19,v19
MM v[40:43],a[224:227],v[164:167],v[40:43]
TR a[174:175],v233 offset:62464
TR a[176:177],v232 offset:50688
MM v[48:51],a[224:227],v[168:171],v[136:139]
EX v20,v20
MM v[48:51],a[228:231],v[184:187],v[48:51]
TR a[178:179],v232 offset:54784
CV v16,v16,v17
MM v[48:51],a[232:235],v[200:203],v[48:51]
EX v21,v21
MM v[48:51],a[236:239],v[216:219],v[48:51]
TR a[180:181],v232 offset:58880
CV v17,v18,v19
MM v[52:55],a[252:255],v[216:219],v[136:139]
EX v22,v22
MM v[52:55],a[248:251],v[200:203],v[52:55]
TR a[182:183],v232 offset:62976
TR a[184:185],v233 offset:50688
MM v[52:55],a[244:247],v[184:187],v[52:55]
EX v23,v23
MM v[52:55],a[240:243],v[168:171],v[52:55]
EX v24,v24
MM v[60:63],a[240:243],v[172:175],v[140:143]
TR a[186:187],v233 offset:54784
CV v18,v20,v21
MM v[60:63],a[244:247],v[188:191],v[60:63]
EX v25,v25
MM v[60:63],a[248:251],v[204:207],v[60:63]
TR a[188:189],v233 offset:58880
TR a[190:191],v233 offset:62976
MM v[60:63],a[252:255],v[220:223],v[60:63]
EX v26,v26
MM v[56:59],a[236:239],v[220:223],v[140:143]
CV v19,v22,v23
CV v24,v24,v25
MM v[56:59],a[232:235],v[204:207],v[56:59]
EX v27,v27
MM v[56:59],a[228:231],v[188:191],v[56:59]
EX v28,v28
MM v[56:59],a[224:227],v[172:175],v[56:59]
s_waitcnt vmcnt(8) lgkmcnt(0)
s_barrier
MM a[0:3],a[128:131],v[64:67],a[0:3]
s_add_u32 m0, s65, 49152
EX v29,v29
MM a[0:3],a[132:135],v[68:71],a[0:3]
CV v25,v26,v27
CV v26,v28,v29
MM a[8:11],a[132:135],v[76:79],a[8:11]
EX v30,v30
MM a[8:11],a[128:131],v[72:75],a[8:11]
EX v31,v31
MM a[16:19],a[128:131],v[80:83],a[16:19]
TR v[96:97],v124 offset:0
TR v[98:99],v124 offset:512
MM a[16:19],a[132:135],v[84:87],a[16:19]
RD v[108:111],v126 offset:0
TR v[100:101],v124 offset:4096
MM a[24:27],a[132:135],v[92:95],a[24:27]
TR v[102:103],v124 offset:4608
RD v[112:115],v126 offset:512
MM a[24:27],a[128:131],v[88:91],a[24:27]
TR v[104:105],v124 offset:8192
TR v[106:107],v124 offset:8704
MM a[28:31],a[136:139],v[88:91],a[28:31]
RD v[116:119],v126 offset:1024
RD a[192:195],v224 offset:16384
EX v32,v32
MM a[28:31],a[140:143],v[92:95],a[28:31]
EX v33,v33
MM a[20:23],a[140:143],v[84:87],a[20:23]
EX v34,v34
MM a[20:23],a[136:139],v[80:83],a[20:23]
RD a[196:199],v225 offset:16384
CV v27,v30,v31
MM a[12:15],a[136:139],v[72:75],a[12:15]
EX v35,v35
MM a[12:15],a[140:143],v[76:79],a[12:15]
buffer_load_dwordx4 v228, s[8:11], s64 offen offset:0 lds
MM a[4:7],a[140:143],v[68:71],a[4:7]
EX v36,v36
MM a[4:7],a[136:139],v[64:67],a[4:7]
EX v37,v37
MM a[32:35],a[144:147],v[64:67],a[32:35]
RD a[200:203],v226 offset:16384
CV v4,v32,v33
MM a[32:35],a[148:151],v[68:71],a[32:35]
EX v38,v38
MM a[40:43],a[148:151],v[76:79],a[40:43]
RD a[204:207],v227 offset:16384
CV v5,v34,v35
MM a[40:43],a[144:147],v[72:75],a[40:43]
EX v39,v39
MM a[48:51],a[144:147],v[80:83],a[48:51]
s_waitcnt lgkmcnt(4)
buffer_load_dwordx4 v229, s[8:11], s64 offen offset:1024 lds
MM a[48:51],a[148:151],v[84:87],a[48:51]
EX v40,v40
MM v[120:123],v[96:99],v[108:111],0
MM v[120:123],v[100:103],v[112:115],v[120:123]
MM v[120:123],v[104:107],v[116:119],v[120:123]
MM a[56:59],a[148:151],v[92:95],a[56:59]
TR v[96:97],v124 offset:12288
TR v[98:99],v124 offset:12800
MM a[56:59],a[144:147],v[88:91],a[56:59]
RD v[108:111],v126 offset:1536
TR v[100:101],v124 offset:16384
MM a[60:63],a[152:155],v[88:91],a[60:63]
TR v[102:103],v124 offset:16896
RD v[112:115],v126 offset:2048
EX v41,v41
MM a[60:63],a[156:159],v[92:95],a[60:63]
TR v[104:105],v124 offset:20480
TR v[106:107],v124 offset:20992
MM a[52:55],a[156:159],v[84:87],a[52:55]
RD v[116:119],v126 offset:2560
CV v6,v36,v37
EX v42,v42
MM a[52:55],a[152:155],v[80:83],a[52:55]
EX v43,v43
MM a[44:47],a[152:155],v[72:75],a[44:47]
RD a[208:211],v224 offset:20480
CV v7,v38,v39
MM a[44:47],a[156:159],v[76:79],a[44:47]
EX v44,v44
MM a[36:39],a[156:159],v[68:71],a[36:39]
buffer_load_dwordx4 v230, s[8:11], s64 offen offset:2048 lds
MM a[36:39],a[152:155],v[64:67],a[36:39]
EX v45,v45
MM a[64:67],a[160:163],v[64:67],a[64:67]
RD a[212:215],v225 offset:20480
CV v12,v40,v41
MM a[64:67],a[164:167],v[68:71],a[64:67]
EX v46,v46
MM a[72:75],a[164:167],v[76:79],a[72:75]
EX v47,v47
MM a[72:75],a[160:163],v[72:75],a[72:75]
RD a[216:219],v226 offset:20480
CV v13,v42,v43
MM a[80:83],a[160:163],v[80:83],a[80:83]
EX v48,v48
MM a[80:83],a[164:167],v[84:87],a[80:83]
buffer_load_dwordx4 v231, s[8:11], s64 offen offset:3072 lds
MM a[88:91],a[164:167],v[92:95],a[88:91]
s_waitcnt lgkmcnt(3)
EX v49,v49
MM a[88:91],a[160:163],v[88:91],a[88:91]
s_add_u32 m0, s65, 114688
RD a[220:223],v227 offset:20480
CV v14,v44,v45
MM v[120:123],v[96:99],v[108:111],v[120:123]
MM v[120:123],v[100:103],v[112:115],v[120:123]
MM v[120:123],v[104:107],v[116:119],v[120:123]
MM a[92:95],a[168:171],v[88:91],a[92:95]
EX v50,v50
MM a[92:95],a[172:175],v[92:95],a[92:95]
RD a[224:227],v224 offset:24576
CV v15,v46,v47
MM a[84:87],a[172:175],v[84:87],a[84:87]
EX v51,v51
MM a[84:87],a[168:171],v[80:83],a[84:87]
EX v52,v52
MM a[76:79],a[168:171],v[72:75],a[76:79]
buffer_load_dwordx4 v235, s[12:15], s81 offen offset:0 lds
MM a[76:79],a[172:175],v[76:79],a[76:79]
EX v53,v53
MM a[68:71],a[172:175],v[68:71],a[68:71]
s_mov_b32 exec_lo, 0x00ff00ff
s_mov_b32 exec_hi, 0x00ff00ff
buffer_store_dwordx4 v[120:123], v127, s[20:23], s90 offen offset:0 sc1
s_mov_b64 exec, -1
s_nop 0
MM a[68:71],a[168:171],v[64:67],a[68:71]
RD a[228:231],v225 offset:24576
CV v20,v48,v49
MM a[96:99],a[176:179],v[64:67],a[96:99]
EX v54,v54
MM a[96:99],a[180:183],v[68:71],a[96:99]
EX v55,v55
MM a[104:107],a[180:183],v[76:79],a[104:107]
RD a[232:235],v226 offset:24576
CV v21,v50,v51
MM a[104:107],a[176:179],v[72:75],a[104:107]
EX v56,v56
MM a[112:115],a[176:179],v[80:83],a[112:115]
RD a[236:239],v227 offset:24576
CV v22,v52,v53
MM a[112:115],a[180:183],v[84:87],a[112:115]
EX v57,v57
MM a[120:123],a[180:183],v[92:95],a[120:123]
buffer_load_dwordx4 v235, s[12:15], s64 offen offset:1024 lds
MM a[120:123],a[176:179],v[88:91],a[120:123]
EX v58,v58
MM a[124:127],a[184:187],v[88:91],a[124:127]
RD a[240:243],v224 offset:28672
CV v23,v54,v55
MM a[124:127],a[188:191],v[92:95],a[124:127]
EX v59,v59
MM a[116:119],a[188:191],v[84:87],a[116:119]
EX v60,v60
MM a[116:119],a[184:187],v[80:83],a[116:119]
RD a[244:247],v225 offset:28672
CV v28,v56,v57
MM a[108:111],a[184:187],v[72:75],a[108:111]
EX v61,v61
MM a[108:111],a[188:191],v[76:79],a[108:111]
buffer_load_dwordx4 v235, s[12:15], s81 offen offset:2048 lds
MM a[100:103],a[188:191],v[68:71],a[100:103]
EX v62,v62
MM a[100:103],a[184:187],v[64:67],a[100:103]
RD a[248:251],v226 offset:28672
CV v29,v58,v59
v_smfmac_f32_16x16x64_bf16 v[144:147], v[236:239], v[0:7], v234
RD a[252:255],v227 offset:28672
EX v63,v63
v_smfmac_f32_16x16x64_bf16 v[148:151], v[236:239], v[8:15], v234
CV v30,v60,v61
v_smfmac_f32_16x16x64_bf16 v[152:155], v[236:239], v[16:23], v234
CV v31,v62,v63
s_nop 1
v_smfmac_f32_16x16x64_bf16 v[156:159], v[236:239], v[24:31], v234
s_waitcnt lgkmcnt(0)
buffer_load_dwordx4 v235, s[12:15], s64 offen offset:3072 lds
MM v[64:67],a[192:195],v[160:163],v[128:131]
s_add_u32 s64, s64, 16384
TR a[128:129],v232 offset:0
s_add_u32 s81, s64, 896
MM v[64:67],a[196:199],v[176:179],v[64:67]
TR a[130:131],v232 offset:4096
TR a[132:133],v232 offset:8192
MM v[64:67],a[200:203],v[192:195],v[64:67]
v_max3_f32 v249, v144, v145, v148
v_max3_f32 v249, v249, v149, v152
MM v[64:67],a[204:207],v[208:211],v[64:67]
v_max3_f32 v249, v249, v153, v156
v_max_f32 v249, v249, v157
MM v[68:71],a[220:223],v[208:211],v[128:131]
)ASM"
R"ASM(
TR a[134:135],v232 offset:12288
v_cmp_lt_f32 vcc, s71, v249
MM v[68:71],a[216:219],v[192:195],v[68:71]
s_cbranch_vccnz LSITE28_%=
LBACK28_%=:
TR a[136:137],v233 offset:0
v_add_f32 v244, v144, v145
MM v[68:71],a[212:215],v[176:179],v[68:71]
v_add_f32 v245, v148, v149
v_add_f32 v246, v152, v153
MM v[68:71],a[208:211],v[160:163],v[68:71]
TR a[138:139],v233 offset:4096
v_add_f32 v247, v156, v157
MM v[76:79],a[208:211],v[164:167],v[132:135]
EX v64,v64
MM v[76:79],a[212:215],v[180:183],v[76:79]
TR a[140:141],v233 offset:8192
TR a[142:143],v233 offset:12288
MM v[76:79],a[216:219],v[196:199],v[76:79]
EX v65,v65
MM v[76:79],a[220:223],v[212:215],v[76:79]
TR a[144:145],v232 offset:512
CV v64,v64,v65
MM v[72:75],a[204:207],v[212:215],v[132:135]
EX v66,v66
MM v[72:75],a[200:203],v[196:199],v[72:75]
EX v67,v67
MM v[72:75],a[196:199],v[180:183],v[72:75]
TR a[146:147],v232 offset:4608
CV v65,v66,v67
MM v[72:75],a[192:195],v[164:167],v[72:75]
EX v68,v68
MM v[80:83],a[192:195],v[168:171],v[136:139]
TR a[148:149],v232 offset:8704
TR a[150:151],v232 offset:12800
MM v[80:83],a[196:199],v[184:187],v[80:83]
EX v69,v69
MM v[80:83],a[200:203],v[200:203],v[80:83]
TR a[152:153],v233 offset:512
CV v66,v68,v69
MM v[80:83],a[204:207],v[216:219],v[80:83]
EX v70,v70
MM v[84:87],a[220:223],v[216:219],v[136:139]
v_lshlrev_b32 v32, 3, v248
v_min_u32 v32, 4992, v32
MM v[84:87],a[216:219],v[200:203],v[84:87]
s_and_b32 s90, 1, %[sb]
EX v71,v71
MM v[84:87],a[212:215],v[184:187],v[84:87]
s_lshl_b32 s90, s90, 3
TR a[154:155],v233 offset:4608
CV v67,v70,v71
MM v[84:87],a[208:211],v[168:171],v[84:87]
s_add_u32 s90, s90, s50
EX v72,v72
MM v[92:95],a[208:211],v[172:175],v[140:143]
s_lshl_b32 s90, s90, 2
TR a[156:157],v233 offset:8704
TR a[158:159],v233 offset:12800
MM v[92:95],a[212:215],v[188:191],v[92:95]
s_add_u32 s90, s90, s84
EX v73,v73
MM v[92:95],a[216:219],v[204:207],v[92:95]
s_lshl_b32 s90, s90, 2
TR a[160:161],v232 offset:1024
CV v72,v72,v73
MM v[92:95],a[220:223],v[220:223],v[92:95]
s_add_u32 s90, s90, %[swave]
EX v74,v74
MM v[88:91],a[204:207],v[220:223],v[140:143]
s_mul_i32 s90, s90, 5120
EX v75,v75
MM v[88:91],a[200:203],v[204:207],v[88:91]
s_mul_i32 s91, s84, 320
TR a[162:163],v232 offset:5120
CV v73,v74,v75
MM v[88:91],a[196:199],v[188:191],v[88:91]
s_sub_u32 s90, s90, s91
EX v76,v76
MM v[88:91],a[192:195],v[172:175],v[88:91]
s_add_u32 s91, %[slds], 135168
s_mov_b32 m0, s91
s_nop 0
buffer_load_dword v32, s[24:27], s90 offen offset:0 lds
MM v[96:99],a[224:227],v[160:163],v[128:131]
TR a[164:165],v232 offset:9216
TR a[166:167],v232 offset:13312
MM v[96:99],a[228:231],v[176:179],v[96:99]
EX v77,v77
MM v[96:99],a[232:235],v[192:195],v[96:99]
TR a[168:169],v233 offset:1024
CV v74,v76,v77
MM v[96:99],a[236:239],v[208:211],v[96:99]
EX v78,v78
MM v[100:103],a[252:255],v[208:211],v[128:131]
EX v79,v79
MM v[100:103],a[248:251],v[192:195],v[100:103]
TR a[170:171],v233 offset:5120
CV v75,v78,v79
MM v[100:103],a[244:247],v[176:179],v[100:103]
EX v80,v80
MM v[100:103],a[240:243],v[160:163],v[100:103]
TR a[172:173],v233 offset:9216
TR a[174:175],v233 offset:13312
MM v[108:111],a[240:243],v[164:167],v[132:135]
s_add_u32 s67, s95, 5120
EX v81,v81
MM v[108:111],a[244:247],v[180:183],v[108:111]
s_mov_b32 s70, 1
TR a[176:177],v232 offset:1536
CV v80,v80,v81
MM v[108:111],a[248:251],v[196:199],v[108:111]
EX v82,v82
MM v[108:111],a[252:255],v[212:215],v[108:111]
EX v83,v83
MM v[104:107],a[236:239],v[212:215],v[132:135]
TR a[178:179],v232 offset:5632
CV v81,v82,v83
MM v[104:107],a[232:235],v[196:199],v[104:107]
EX v84,v84
MM v[104:107],a[228:231],v[180:183],v[104:107]
TR a[180:181],v232 offset:9728
TR a[182:183],v232 offset:13824
MM v[104:107],a[224:227],v[164:167],v[104:107]
EX v85,v85
MM v[112:115],a[224:227],v[168:171],v[136:139]
TR a[184:185],v233 offset:1536
CV v82,v84,v85
MM v[112:115],a[228:231],v[184:187],v[112:115]
EX v86,v86
MM v[112:115],a[232:235],v[200:203],v[112:115]
EX v87,v87
MM v[112:115],a[236:239],v[216:219],v[112:115]
TR a[186:187],v233 offset:5632
CV v83,v86,v87
MM v[116:119],a[252:255],v[216:219],v[136:139]
EX v88,v88
MM v[116:119],a[248:251],v[200:203],v[116:119]
TR a[188:189],v233 offset:9728
TR a[190:191],v233 offset:13824
MM v[116:119],a[244:247],v[184:187],v[116:119]
EX v89,v89
MM v[116:119],a[240:243],v[168:171],v[116:119]
EX v90,v90
MM v[124:127],a[240:243],v[172:175],v[140:143]
EX v91,v91
MM v[124:127],a[244:247],v[188:191],v[124:127]
EX v92,v92
MM v[124:127],a[248:251],v[204:207],v[124:127]
EX v93,v93
MM v[124:127],a[252:255],v[220:223],v[124:127]
EX v94,v94
MM v[120:123],a[236:239],v[220:223],v[140:143]
EX v95,v95
MM v[120:123],a[232:235],v[204:207],v[120:123]
EX v96,v96
MM v[120:123],a[228:231],v[188:191],v[120:123]
EX v97,v97
MM v[120:123],a[224:227],v[172:175],v[120:123]
s_waitcnt vmcnt(14) lgkmcnt(0)
s_barrier
MM a[0:3],a[128:131],v[0:3],a[0:3]
s_add_u32 m0, s65, 0
CV v88,v88,v89
CV v89,v90,v91
MM a[0:3],a[132:135],v[4:7],a[0:3]
RD a[192:195],v224 offset:32768
CV v90,v92,v93
MM a[8:11],a[132:135],v[12:15],a[8:11]
RD a[196:199],v225 offset:32768
CV v91,v94,v95
MM a[8:11],a[128:131],v[8:11],a[8:11]
buffer_load_dwordx4 v228, s[8:11], s64 offen offset:0 lds
MM a[16:19],a[128:131],v[16:19],a[16:19]
RD a[200:203],v226 offset:32768
CV v68,v96,v97
MM a[16:19],a[132:135],v[20:23],a[16:19]
buffer_load_dwordx4 v229, s[8:11], s64 offen offset:1024 lds
MM a[24:27],a[132:135],v[28:31],a[24:27]
EX v98,v98
MM a[24:27],a[128:131],v[24:27],a[24:27]
RD a[204:207],v227 offset:32768
RD a[208:211],v224 offset:36864
MM a[28:31],a[136:139],v[24:27],a[28:31]
EX v99,v99
MM a[28:31],a[140:143],v[28:31],a[28:31]
RD a[212:215],v225 offset:36864
CV v69,v98,v99
MM a[20:23],a[140:143],v[20:23],a[20:23]
EX v100,v100
MM a[20:23],a[136:139],v[16:19],a[20:23]
EX v101,v101
MM a[12:15],a[136:139],v[8:11],a[12:15]
RD a[216:219],v226 offset:36864
CV v70,v100,v101
MM a[12:15],a[140:143],v[12:15],a[12:15]
buffer_load_dwordx4 v230, s[8:11], s64 offen offset:2048 lds
MM a[4:7],a[140:143],v[4:7],a[4:7]
EX v102,v102
MM a[4:7],a[136:139],v[0:3],a[4:7]
EX v103,v103
MM a[32:35],a[144:147],v[0:3],a[32:35]
RD a[220:223],v227 offset:36864
CV v71,v102,v103
MM a[32:35],a[148:151],v[4:7],a[32:35]
EX v104,v104
MM a[40:43],a[148:151],v[12:15],a[40:43]
EX v105,v105
MM a[40:43],a[144:147],v[8:11],a[40:43]
RD a[224:227],v224 offset:40960
CV v76,v104,v105
MM a[48:51],a[144:147],v[16:19],a[48:51]
buffer_load_dwordx4 v231, s[8:11], s64 offen offset:3072 lds
MM a[48:51],a[148:151],v[20:23],a[48:51]
s_add_u32 m0, s65, 65536
EX v106,v106
MM a[56:59],a[148:151],v[28:31],a[56:59]
EX v107,v107
MM a[56:59],a[144:147],v[24:27],a[56:59]
RD a[228:231],v225 offset:40960
CV v77,v106,v107
MM a[60:63],a[152:155],v[24:27],a[60:63]
EX v108,v108
MM a[60:63],a[156:159],v[28:31],a[60:63]
buffer_load_dwordx4 v235, s[12:15], s81 offen offset:0 lds
MM a[52:55],a[156:159],v[20:23],a[52:55]
EX v109,v109
MM a[52:55],a[152:155],v[16:19],a[52:55]
RD a[232:235],v226 offset:40960
CV v78,v108,v109
MM a[44:47],a[152:155],v[8:11],a[44:47]
EX v110,v110
MM a[44:47],a[156:159],v[12:15],a[44:47]
EX v111,v111
MM a[36:39],a[156:159],v[4:7],a[36:39]
RD a[236:239],v227 offset:40960
CV v79,v110,v111
MM a[36:39],a[152:155],v[0:3],a[36:39]
EX v112,v112
MM a[64:67],a[160:163],v[0:3],a[64:67]
buffer_load_dwordx4 v235, s[12:15], s64 offen offset:1024 lds
MM a[64:67],a[164:167],v[4:7],a[64:67]
EX v113,v113
MM a[72:75],a[164:167],v[12:15],a[72:75]
RD a[240:243],v224 offset:45056
CV v84,v112,v113
MM a[72:75],a[160:163],v[8:11],a[72:75]
EX v114,v114
MM a[80:83],a[160:163],v[16:19],a[80:83]
EX v115,v115
MM a[80:83],a[164:167],v[20:23],a[80:83]
RD a[244:247],v225 offset:45056
CV v85,v114,v115
MM a[88:91],a[164:167],v[28:31],a[88:91]
EX v116,v116
MM a[88:91],a[160:163],v[24:27],a[88:91]
buffer_load_dwordx4 v235, s[12:15], s81 offen offset:2048 lds
MM a[92:95],a[168:171],v[24:27],a[92:95]
EX v117,v117
MM a[92:95],a[172:175],v[28:31],a[92:95]
RD a[248:251],v226 offset:45056
CV v86,v116,v117
MM a[84:87],a[172:175],v[20:23],a[84:87]
EX v118,v118
MM a[84:87],a[168:171],v[16:19],a[84:87]
EX v119,v119
MM a[76:79],a[168:171],v[8:11],a[76:79]
RD a[252:255],v227 offset:45056
CV v87,v118,v119
MM a[76:79],a[172:175],v[12:15],a[76:79]
EX v120,v120
MM a[68:71],a[172:175],v[4:7],a[68:71]
buffer_load_dwordx4 v235, s[12:15], s64 offen offset:3072 lds
MM a[68:71],a[168:171],v[0:3],a[68:71]
EX v121,v121
MM a[96:99],a[176:179],v[0:3],a[96:99]
EX v122,v122
MM a[96:99],a[180:183],v[4:7],a[96:99]
EX v123,v123
MM a[104:107],a[180:183],v[12:15],a[104:107]
EX v124,v124
MM a[104:107],a[176:179],v[8:11],a[104:107]
EX v125,v125
MM a[112:115],a[176:179],v[16:19],a[112:115]
EX v126,v126
MM a[112:115],a[180:183],v[20:23],a[112:115]
EX v127,v127
MM a[120:123],a[180:183],v[28:31],a[120:123]
s_lshl_b32 s91, s47, 2
CV v92,v120,v121
CV v93,v122,v123
MM a[120:123],a[176:179],v[24:27],a[120:123]
s_add_u32 s91, s91, %[swave]
CV v94,v124,v125
CV v95,v126,v127
MM a[124:127],a[184:187],v[24:27],a[124:127]
s_lshl_b32 s91, s91, 2
s_add_u32 s91, s91, 73728
s_waitcnt vmcnt(12)
MM a[124:127],a[188:191],v[28:31],a[124:127]
v_mov_b32 v250, s69
s_mov_b64 exec, 1
buffer_store_dword v250, off, s[20:23], s91 offset:0 sc1
s_mov_b64 exec, -1
MM a[116:119],a[188:191],v[20:23],a[116:119]
MM a[116:119],a[184:187],v[16:19],a[116:119]
MM a[108:111],a[184:187],v[8:11],a[108:111]
MM a[108:111],a[188:191],v[12:15],a[108:111]
MM a[100:103],a[188:191],v[4:7],a[100:103]
MM a[100:103],a[184:187],v[0:3],a[100:103]
v_smfmac_f32_16x16x64_bf16 v[144:147], v[236:239], v[64:71], v234
v_smfmac_f32_16x16x64_bf16 v[148:151], v[236:239], v[72:79], v234
v_smfmac_f32_16x16x64_bf16 v[152:155], v[236:239], v[80:87], v234
v_smfmac_f32_16x16x64_bf16 v[156:159], v[236:239], v[88:95], v234
s_waitcnt lgkmcnt(0)
s_cmp_ge_u32 s16, 20
s_cbranch_scc1 LLAP_%=
MM v[0:3],a[192:195],v[160:163],v[128:131]
s_add_u32 s64, s64, 16384
TR a[128:129],v232 offset:16384
s_add_u32 s81, s64, 896
MM v[0:3],a[196:199],v[176:179],v[0:3]
TR a[130:131],v232 offset:20480
TR a[132:133],v232 offset:24576
MM v[0:3],a[200:203],v[192:195],v[0:3]
LPG20_%=:
s_cbranch_execz LTR3_%=
v_max3_f32 v249, v144, v145, v148
v_max3_f32 v249, v249, v149, v152
MM v[0:3],a[204:207],v[208:211],v[0:3]
v_max3_f32 v249, v249, v153, v156
v_max_f32 v249, v249, v157
MM v[4:7],a[220:223],v[208:211],v[128:131]
TR a[134:135],v232 offset:28672
v_cmp_lt_f32 vcc, s71, v249
MM v[4:7],a[216:219],v[192:195],v[4:7]
s_cbranch_vccnz LSITE29_%=
LBACK29_%=:
TR a[136:137],v233 offset:16384
v_add_f32 v244, v144, v145
MM v[4:7],a[212:215],v[176:179],v[4:7]
v_add_f32 v245, v148, v149
v_add_f32 v246, v152, v153
MM v[4:7],a[208:211],v[160:163],v[4:7]
TR a[138:139],v233 offset:20480
v_add_f32 v247, v156, v157
MM v[12:15],a[208:211],v[164:167],v[132:135]
EX v0,v0
MM v[12:15],a[212:215],v[180:183],v[12:15]
TR a[140:141],v233 offset:24576
TR a[142:143],v233 offset:28672
MM v[12:15],a[216:219],v[196:199],v[12:15]
EX v1,v1
MM v[12:15],a[220:223],v[212:215],v[12:15]
TR a[144:145],v232 offset:16896
CV v0,v0,v1
MM v[8:11],a[204:207],v[212:215],v[132:135]
EX v2,v2
MM v[8:11],a[200:203],v[196:199],v[8:11]
EX v3,v3
MM v[8:11],a[196:199],v[180:183],v[8:11]
TR a[146:147],v232 offset:20992
CV v1,v2,v3
MM v[8:11],a[192:195],v[164:167],v[8:11]
EX v4,v4
MM v[16:19],a[192:195],v[168:171],v[136:139]
TR a[148:149],v232 offset:25088
TR a[150:151],v232 offset:29184
MM v[16:19],a[196:199],v[184:187],v[16:19]
EX v5,v5
MM v[16:19],a[200:203],v[200:203],v[16:19]
TR a[152:153],v233 offset:16896
CV v2,v4,v5
MM v[16:19],a[204:207],v[216:219],v[16:19]
EX v6,v6
MM v[20:23],a[220:223],v[216:219],v[136:139]
s_add_u32 s87, s95, 5120
buffer_store_dword v240, v248, s[20:23], s87 offen offset:12 sc1
MM v[20:23],a[216:219],v[200:203],v[20:23]
EX v7,v7
MM v[20:23],a[212:215],v[184:187],v[20:23]
TR a[154:155],v233 offset:20992
CV v3,v6,v7
MM v[20:23],a[208:211],v[168:171],v[20:23]
EX v8,v8
MM v[28:31],a[208:211],v[172:175],v[140:143]
TR a[156:157],v233 offset:25088
TR a[158:159],v233 offset:29184
MM v[28:31],a[212:215],v[188:191],v[28:31]
EX v9,v9
MM v[28:31],a[216:219],v[204:207],v[28:31]
TR a[160:161],v232 offset:17408
)ASM"
R"ASM(
CV v8,v8,v9
MM v[28:31],a[220:223],v[220:223],v[28:31]
EX v10,v10
MM v[24:27],a[204:207],v[220:223],v[140:143]
EX v11,v11
MM v[24:27],a[200:203],v[204:207],v[24:27]
TR a[162:163],v232 offset:21504
CV v9,v10,v11
MM v[24:27],a[196:199],v[188:191],v[24:27]
EX v12,v12
MM v[24:27],a[192:195],v[172:175],v[24:27]
TR a[164:165],v232 offset:25600
TR a[166:167],v232 offset:29696
MM v[32:35],a[224:227],v[160:163],v[128:131]
EX v13,v13
MM v[32:35],a[228:231],v[176:179],v[32:35]
TR a[168:169],v233 offset:17408
CV v10,v12,v13
MM v[32:35],a[232:235],v[192:195],v[32:35]
EX v14,v14
MM v[32:35],a[236:239],v[208:211],v[32:35]
EX v15,v15
MM v[36:39],a[252:255],v[208:211],v[128:131]
TR a[170:171],v233 offset:21504
CV v11,v14,v15
MM v[36:39],a[248:251],v[192:195],v[36:39]
EX v16,v16
MM v[36:39],a[244:247],v[176:179],v[36:39]
TR a[172:173],v233 offset:25600
TR a[174:175],v233 offset:29696
MM v[36:39],a[240:243],v[160:163],v[36:39]
EX v17,v17
MM v[44:47],a[240:243],v[164:167],v[132:135]
TR a[176:177],v232 offset:17920
CV v16,v16,v17
MM v[44:47],a[244:247],v[180:183],v[44:47]
EX v18,v18
MM v[44:47],a[248:251],v[196:199],v[44:47]
TR a[178:179],v232 offset:22016
TR a[180:181],v232 offset:26112
MM v[44:47],a[252:255],v[212:215],v[44:47]
EX v19,v19
MM v[40:43],a[236:239],v[212:215],v[132:135]
TR a[182:183],v232 offset:30208
CV v17,v18,v19
MM v[40:43],a[232:235],v[196:199],v[40:43]
EX v20,v20
MM v[40:43],a[228:231],v[180:183],v[40:43]
EX v21,v21
MM v[40:43],a[224:227],v[164:167],v[40:43]
TR a[184:185],v233 offset:17920
CV v18,v20,v21
MM v[48:51],a[224:227],v[168:171],v[136:139]
EX v22,v22
MM v[48:51],a[228:231],v[184:187],v[48:51]
TR a[186:187],v233 offset:22016
TR a[188:189],v233 offset:26112
MM v[48:51],a[232:235],v[200:203],v[48:51]
EX v23,v23
MM v[48:51],a[236:239],v[216:219],v[48:51]
TR a[190:191],v233 offset:30208
CV v19,v22,v23
MM v[52:55],a[252:255],v[216:219],v[136:139]
EX v24,v24
MM v[52:55],a[248:251],v[200:203],v[52:55]
EX v25,v25
MM v[52:55],a[244:247],v[184:187],v[52:55]
EX v26,v26
MM v[52:55],a[240:243],v[168:171],v[52:55]
EX v27,v27
MM v[60:63],a[240:243],v[172:175],v[140:143]
EX v28,v28
MM v[60:63],a[244:247],v[188:191],v[60:63]
EX v29,v29
MM v[60:63],a[248:251],v[204:207],v[60:63]
EX v30,v30
MM v[60:63],a[252:255],v[220:223],v[60:63]
EX v31,v31
MM v[56:59],a[236:239],v[220:223],v[140:143]
EX v32,v32
MM v[56:59],a[232:235],v[204:207],v[56:59]
EX v33,v33
MM v[56:59],a[228:231],v[188:191],v[56:59]
EX v34,v34
MM v[56:59],a[224:227],v[172:175],v[56:59]
s_waitcnt lgkmcnt(0)
s_barrier
MM a[0:3],a[128:131],v[64:67],a[0:3]
s_add_u32 m0, s65, 16384
CV v24,v24,v25
CV v25,v26,v27
MM a[0:3],a[132:135],v[68:71],a[0:3]
RD a[192:195],v224 offset:49152
CV v26,v28,v29
MM a[8:11],a[132:135],v[76:79],a[8:11]
RD a[196:199],v225 offset:49152
CV v27,v30,v31
MM a[8:11],a[128:131],v[72:75],a[8:11]
buffer_load_dwordx4 v228, s[8:11], s64 offen offset:0 lds
MM a[16:19],a[128:131],v[80:83],a[16:19]
RD a[200:203],v226 offset:49152
CV v4,v32,v33
MM a[16:19],a[132:135],v[84:87],a[16:19]
buffer_load_dwordx4 v229, s[8:11], s64 offen offset:1024 lds
MM a[24:27],a[132:135],v[92:95],a[24:27]
RD a[204:207],v227 offset:49152
RD a[208:211],v224 offset:53248
MM a[24:27],a[128:131],v[88:91],a[24:27]
EX v35,v35
MM a[28:31],a[136:139],v[88:91],a[28:31]
RD a[212:215],v225 offset:53248
CV v5,v34,v35
MM a[28:31],a[140:143],v[92:95],a[28:31]
EX v36,v36
MM a[20:23],a[140:143],v[84:87],a[20:23]
buffer_load_dwordx4 v230, s[8:11], s64 offen offset:2048 lds
MM a[20:23],a[136:139],v[80:83],a[20:23]
EX v37,v37
MM a[12:15],a[136:139],v[72:75],a[12:15]
RD a[216:219],v226 offset:53248
CV v6,v36,v37
MM a[12:15],a[140:143],v[76:79],a[12:15]
EX v38,v38
MM a[4:7],a[140:143],v[68:71],a[4:7]
EX v39,v39
MM a[4:7],a[136:139],v[64:67],a[4:7]
RD a[220:223],v227 offset:53248
CV v7,v38,v39
MM a[32:35],a[144:147],v[64:67],a[32:35]
EX v40,v40
MM a[32:35],a[148:151],v[68:71],a[32:35]
buffer_load_dwordx4 v231, s[8:11], s64 offen offset:3072 lds
MM a[40:43],a[148:151],v[76:79],a[40:43]
s_add_u32 m0, s65, 81920
EX v41,v41
MM a[40:43],a[144:147],v[72:75],a[40:43]
RD a[224:227],v224 offset:57344
CV v12,v40,v41
MM a[48:51],a[144:147],v[80:83],a[48:51]
EX v42,v42
MM a[48:51],a[148:151],v[84:87],a[48:51]
EX v43,v43
MM a[56:59],a[148:151],v[92:95],a[56:59]
RD a[228:231],v225 offset:57344
CV v13,v42,v43
MM a[56:59],a[144:147],v[88:91],a[56:59]
EX v44,v44
MM a[60:63],a[152:155],v[88:91],a[60:63]
buffer_load_dwordx4 v235, s[12:15], s81 offen offset:0 lds
MM a[60:63],a[156:159],v[92:95],a[60:63]
EX v45,v45
MM a[52:55],a[156:159],v[84:87],a[52:55]
RD a[232:235],v226 offset:57344
CV v14,v44,v45
MM a[52:55],a[152:155],v[80:83],a[52:55]
EX v46,v46
MM a[44:47],a[152:155],v[72:75],a[44:47]
EX v47,v47
MM a[44:47],a[156:159],v[76:79],a[44:47]
RD a[236:239],v227 offset:57344
CV v15,v46,v47
MM a[36:39],a[156:159],v[68:71],a[36:39]
buffer_load_dwordx4 v235, s[12:15], s64 offen offset:1024 lds
MM a[36:39],a[152:155],v[64:67],a[36:39]
EX v48,v48
MM a[64:67],a[160:163],v[64:67],a[64:67]
EX v49,v49
MM a[64:67],a[164:167],v[68:71],a[64:67]
RD a[240:243],v224 offset:61440
CV v20,v48,v49
MM a[72:75],a[164:167],v[76:79],a[72:75]
EX v50,v50
MM a[72:75],a[160:163],v[72:75],a[72:75]
RD a[244:247],v225 offset:61440
RD a[248:251],v226 offset:61440
MM a[80:83],a[160:163],v[80:83],a[80:83]
EX v51,v51
MM a[80:83],a[164:167],v[84:87],a[80:83]
RD a[252:255],v227 offset:61440
CV v21,v50,v51
MM a[88:91],a[164:167],v[92:95],a[88:91]
EX v52,v52
MM a[88:91],a[160:163],v[88:91],a[88:91]
buffer_load_dwordx4 v235, s[12:15], s81 offen offset:2048 lds
MM a[92:95],a[168:171],v[88:91],a[92:95]
EX v53,v53
MM a[92:95],a[172:175],v[92:95],a[92:95]
EX v54,v54
MM a[84:87],a[172:175],v[84:87],a[84:87]
buffer_load_dwordx4 v235, s[12:15], s64 offen offset:3072 lds
MM a[84:87],a[168:171],v[80:83],a[84:87]
EX v55,v55
MM a[76:79],a[168:171],v[72:75],a[76:79]
EX v56,v56
MM a[76:79],a[172:175],v[76:79],a[76:79]
EX v57,v57
MM a[68:71],a[172:175],v[68:71],a[68:71]
EX v58,v58
MM a[68:71],a[168:171],v[64:67],a[68:71]
EX v59,v59
MM a[96:99],a[176:179],v[64:67],a[96:99]
EX v60,v60
MM a[96:99],a[180:183],v[68:71],a[96:99]
EX v61,v61
MM a[104:107],a[180:183],v[76:79],a[104:107]
EX v62,v62
MM a[104:107],a[176:179],v[72:75],a[104:107]
EX v63,v63
MM a[112:115],a[176:179],v[80:83],a[112:115]
CV v22,v52,v53
CV v23,v54,v55
MM a[112:115],a[180:183],v[84:87],a[112:115]
CV v28,v56,v57
CV v29,v58,v59
MM a[120:123],a[180:183],v[92:95],a[120:123]
CV v30,v60,v61
CV v31,v62,v63
MM a[120:123],a[176:179],v[88:91],a[120:123]
MM a[124:127],a[184:187],v[88:91],a[124:127]
MM a[124:127],a[188:191],v[92:95],a[124:127]
MM a[116:119],a[188:191],v[84:87],a[116:119]
MM a[116:119],a[184:187],v[80:83],a[116:119]
MM a[108:111],a[184:187],v[72:75],a[108:111]
MM a[108:111],a[188:191],v[76:79],a[108:111]
MM a[100:103],a[188:191],v[68:71],a[100:103]
MM a[100:103],a[184:187],v[64:67],a[100:103]
v_smfmac_f32_16x16x64_bf16 v[144:147], v[236:239], v[0:7], v234
v_smfmac_f32_16x16x64_bf16 v[148:151], v[236:239], v[8:15], v234
v_smfmac_f32_16x16x64_bf16 v[152:155], v[236:239], v[16:23], v234
v_smfmac_f32_16x16x64_bf16 v[156:159], v[236:239], v[24:31], v234
s_waitcnt lgkmcnt(0)
MM v[64:67],a[192:195],v[160:163],v[128:131]
s_add_u32 s64, s64, 16384
TR a[128:129],v232 offset:32768
s_add_u32 s81, s64, 896
MM v[64:67],a[196:199],v[176:179],v[64:67]
TR a[130:131],v232 offset:36864
TR a[132:133],v232 offset:40960
MM v[64:67],a[200:203],v[192:195],v[64:67]
v_max3_f32 v249, v144, v145, v148
v_max3_f32 v249, v249, v149, v152
MM v[64:67],a[204:207],v[208:211],v[64:67]
v_max3_f32 v249, v249, v153, v156
v_max_f32 v249, v249, v157
MM v[68:71],a[220:223],v[208:211],v[128:131]
TR a[134:135],v232 offset:45056
v_cmp_lt_f32 vcc, s71, v249
MM v[68:71],a[216:219],v[192:195],v[68:71]
s_cbranch_vccnz LSITE30_%=
LBACK30_%=:
TR a[136:137],v233 offset:32768
v_add_f32 v244, v144, v145
MM v[68:71],a[212:215],v[176:179],v[68:71]
s_and_b32 s90, s17, 63
v_add_f32 v245, v148, v149
v_add_f32 v246, v152, v153
MM v[68:71],a[208:211],v[160:163],v[68:71]
TR a[138:139],v233 offset:36864
v_add_f32 v247, v156, v157
MM v[76:79],a[208:211],v[164:167],v[132:135]
s_cmp_lt_u32 s17, 80
s_cselect_b32 s17, s17, s90
s_cselect_b64 s[44:45], -1, 0
MM v[76:79],a[212:215],v[180:183],v[76:79]
v_lshrrev_b32 v253, 4, v248
v_min_u32 v253, 35, v253
MM v[76:79],a[216:219],v[196:199],v[76:79]
v_lshrrev_b32 v252, 2, v253
v_mul_u32_u24 v252, 144, v252
MM v[76:79],a[220:223],v[212:215],v[76:79]
TR a[140:141],v233 offset:40960
v_lshl_add_u32 v253, v253, 2, v252
MM v[72:75],a[204:207],v[212:215],v[132:135]
s_lshr_b32 s90, s17, 3
EX v64,v64
MM v[72:75],a[200:203],v[196:199],v[72:75]
s_lshl_b32 s76, s90, 4
EX v65,v65
MM v[72:75],a[196:199],v[180:183],v[72:75]
s_add_u32 s76, s76, 73728
buffer_load_dword v252, v253, s[20:23], s76 offen offset:0 sc1
MM v[72:75],a[192:195],v[164:167],v[72:75]
EX v66,v66
MM v[80:83],a[192:195],v[168:171],v[136:139]
EX v67,v67
MM v[80:83],a[196:199],v[184:187],v[80:83]
TR a[142:143],v233 offset:45056
TR a[144:145],v232 offset:33280
MM v[80:83],a[200:203],v[200:203],v[80:83]
EX v68,v68
MM v[80:83],a[204:207],v[216:219],v[80:83]
TR a[146:147],v232 offset:37376
CV v64,v64,v65
MM v[84:87],a[220:223],v[216:219],v[136:139]
EX v69,v69
MM v[84:87],a[216:219],v[200:203],v[84:87]
TR a[148:149],v232 offset:41472
CV v65,v66,v67
MM v[84:87],a[212:215],v[184:187],v[84:87]
EX v70,v70
MM v[84:87],a[208:211],v[168:171],v[84:87]
EX v71,v71
MM v[92:95],a[208:211],v[172:175],v[140:143]
TR a[150:151],v232 offset:45568
TR a[152:153],v233 offset:33280
MM v[92:95],a[212:215],v[188:191],v[92:95]
EX v72,v72
MM v[92:95],a[216:219],v[204:207],v[92:95]
TR a[154:155],v233 offset:37376
CV v66,v68,v69
MM v[92:95],a[220:223],v[220:223],v[92:95]
EX v73,v73
MM v[88:91],a[204:207],v[220:223],v[140:143]
TR a[156:157],v233 offset:41472
CV v67,v70,v71
MM v[88:91],a[200:203],v[204:207],v[88:91]
EX v74,v74
MM v[88:91],a[196:199],v[188:191],v[88:91]
TR a[158:159],v233 offset:45568
CV v72,v72,v73
MM v[88:91],a[192:195],v[172:175],v[88:91]
EX v75,v75
MM v[96:99],a[224:227],v[160:163],v[128:131]
TR a[160:161],v232 offset:33792
TR a[162:163],v232 offset:37888
MM v[96:99],a[228:231],v[176:179],v[96:99]
EX v76,v76
MM v[96:99],a[232:235],v[192:195],v[96:99]
EX v77,v77
MM v[96:99],a[236:239],v[208:211],v[96:99]
TR a[164:165],v232 offset:41984
CV v73,v74,v75
MM v[100:103],a[252:255],v[208:211],v[128:131]
EX v78,v78
MM v[100:103],a[248:251],v[192:195],v[100:103]
TR a[166:167],v232 offset:46080
CV v74,v76,v77
MM v[100:103],a[244:247],v[176:179],v[100:103]
EX v79,v79
MM v[100:103],a[240:243],v[160:163],v[100:103]
TR a[168:169],v233 offset:33792
TR a[170:171],v233 offset:37888
MM v[108:111],a[240:243],v[164:167],v[132:135]
EX v80,v80
MM v[108:111],a[244:247],v[180:183],v[108:111]
EX v81,v81
MM v[108:111],a[248:251],v[196:199],v[108:111]
TR a[172:173],v233 offset:41984
CV v75,v78,v79
MM v[108:111],a[252:255],v[212:215],v[108:111]
EX v82,v82
MM v[104:107],a[236:239],v[212:215],v[132:135]
TR a[174:175],v233 offset:46080
CV v80,v80,v81
MM v[104:107],a[232:235],v[196:199],v[104:107]
EX v83,v83
MM v[104:107],a[228:231],v[180:183],v[104:107]
TR a[176:177],v232 offset:34304
TR a[178:179],v232 offset:38400
MM v[104:107],a[224:227],v[164:167],v[104:107]
EX v84,v84
MM v[112:115],a[224:227],v[168:171],v[136:139]
EX v85,v85
MM v[112:115],a[228:231],v[184:187],v[112:115]
TR a[180:181],v232 offset:42496
CV v81,v82,v83
MM v[112:115],a[232:235],v[200:203],v[112:115]
EX v86,v86
MM v[112:115],a[236:239],v[216:219],v[112:115]
TR a[182:183],v232 offset:46592
CV v82,v84,v85
MM v[116:119],a[252:255],v[216:219],v[136:139]
EX v87,v87
MM v[116:119],a[248:251],v[200:203],v[116:119]
TR a[184:185],v233 offset:34304
TR a[186:187],v233 offset:38400
MM v[116:119],a[244:247],v[184:187],v[116:119]
EX v88,v88
MM v[116:119],a[240:243],v[168:171],v[116:119]
TR a[188:189],v233 offset:42496
CV v83,v86,v87
MM v[124:127],a[240:243],v[172:175],v[140:143]
EX v89,v89
MM v[124:127],a[244:247],v[188:191],v[124:127]
TR a[190:191],v233 offset:46592
CV v88,v88,v89
MM v[124:127],a[248:251],v[204:207],v[124:127]
)ASM"
R"ASM(
EX v90,v90
MM v[124:127],a[252:255],v[220:223],v[124:127]
EX v91,v91
MM v[120:123],a[236:239],v[220:223],v[140:143]
EX v92,v92
MM v[120:123],a[232:235],v[204:207],v[120:123]
EX v93,v93
MM v[120:123],a[228:231],v[188:191],v[120:123]
EX v94,v94
MM v[120:123],a[224:227],v[172:175],v[120:123]
s_waitcnt vmcnt(15) lgkmcnt(0)
s_barrier
MM a[0:3],a[128:131],v[0:3],a[0:3]
s_add_u32 m0, s65, 32768
CV v89,v90,v91
CV v90,v92,v93
MM a[0:3],a[132:135],v[4:7],a[0:3]
RD a[192:195],v224 offset:0
RD a[196:199],v225 offset:0
MM a[8:11],a[132:135],v[12:15],a[8:11]
EX v95,v95
MM a[8:11],a[128:131],v[8:11],a[8:11]
RD a[200:203],v226 offset:0
CV v91,v94,v95
MM a[16:19],a[128:131],v[16:19],a[16:19]
EX v96,v96
MM a[16:19],a[132:135],v[20:23],a[16:19]
buffer_load_dwordx4 v228, s[8:11], s64 offen offset:0 lds
MM a[24:27],a[132:135],v[28:31],a[24:27]
EX v97,v97
MM a[24:27],a[128:131],v[24:27],a[24:27]
RD a[204:207],v227 offset:0
CV v68,v96,v97
MM a[28:31],a[136:139],v[24:27],a[28:31]
EX v98,v98
MM a[28:31],a[140:143],v[28:31],a[28:31]
EX v99,v99
MM a[20:23],a[140:143],v[20:23],a[20:23]
RD a[208:211],v224 offset:4096
CV v69,v98,v99
MM a[20:23],a[136:139],v[16:19],a[20:23]
buffer_load_dwordx4 v229, s[8:11], s64 offen offset:1024 lds
MM a[12:15],a[136:139],v[8:11],a[12:15]
EX v100,v100
MM a[12:15],a[140:143],v[12:15],a[12:15]
EX v101,v101
MM a[4:7],a[140:143],v[4:7],a[4:7]
RD a[212:215],v225 offset:4096
CV v70,v100,v101
MM a[4:7],a[136:139],v[0:3],a[4:7]
EX v102,v102
MM a[32:35],a[144:147],v[0:3],a[32:35]
EX v103,v103
MM a[32:35],a[148:151],v[4:7],a[32:35]
RD a[216:219],v226 offset:4096
CV v71,v102,v103
MM a[40:43],a[148:151],v[12:15],a[40:43]
buffer_load_dwordx4 v230, s[8:11], s64 offen offset:2048 lds
MM a[40:43],a[144:147],v[8:11],a[40:43]
EX v104,v104
MM a[48:51],a[144:147],v[16:19],a[48:51]
EX v105,v105
MM a[48:51],a[148:151],v[20:23],a[48:51]
RD a[220:223],v227 offset:4096
CV v76,v104,v105
MM a[56:59],a[148:151],v[28:31],a[56:59]
EX v106,v106
MM a[56:59],a[144:147],v[24:27],a[56:59]
EX v107,v107
MM a[60:63],a[152:155],v[24:27],a[60:63]
RD a[224:227],v224 offset:8192
CV v77,v106,v107
MM a[60:63],a[156:159],v[28:31],a[60:63]
buffer_load_dwordx4 v231, s[8:11], s64 offen offset:3072 lds
MM a[52:55],a[156:159],v[20:23],a[52:55]
s_add_u32 m0, s65, 98304
EX v108,v108
MM a[52:55],a[152:155],v[16:19],a[52:55]
EX v109,v109
MM a[44:47],a[152:155],v[8:11],a[44:47]
RD a[228:231],v225 offset:8192
CV v78,v108,v109
MM a[44:47],a[156:159],v[12:15],a[44:47]
EX v110,v110
MM a[36:39],a[156:159],v[4:7],a[36:39]
buffer_load_dwordx4 v235, s[12:15], s81 offen offset:0 lds
MM a[36:39],a[152:155],v[0:3],a[36:39]
EX v111,v111
MM a[64:67],a[160:163],v[0:3],a[64:67]
RD a[232:235],v226 offset:8192
CV v79,v110,v111
MM a[64:67],a[164:167],v[4:7],a[64:67]
EX v112,v112
MM a[72:75],a[164:167],v[12:15],a[72:75]
EX v113,v113
MM a[72:75],a[160:163],v[8:11],a[72:75]
RD a[236:239],v227 offset:8192
CV v84,v112,v113
MM a[80:83],a[160:163],v[16:19],a[80:83]
EX v114,v114
MM a[80:83],a[164:167],v[20:23],a[80:83]
buffer_load_dwordx4 v235, s[12:15], s64 offen offset:1024 lds
MM a[88:91],a[164:167],v[28:31],a[88:91]
EX v115,v115
MM a[88:91],a[160:163],v[24:27],a[88:91]
RD a[240:243],v224 offset:12288
CV v85,v114,v115
MM a[92:95],a[168:171],v[24:27],a[92:95]
EX v116,v116
MM a[92:95],a[172:175],v[28:31],a[92:95]
EX v117,v117
MM a[84:87],a[172:175],v[20:23],a[84:87]
RD a[244:247],v225 offset:12288
CV v86,v116,v117
MM a[84:87],a[168:171],v[16:19],a[84:87]
EX v118,v118
MM a[76:79],a[168:171],v[8:11],a[76:79]
buffer_load_dwordx4 v235, s[12:15], s81 offen offset:2048 lds
MM a[76:79],a[172:175],v[12:15],a[76:79]
EX v119,v119
MM a[68:71],a[172:175],v[4:7],a[68:71]
RD a[248:251],v226 offset:12288
CV v87,v118,v119
MM a[68:71],a[168:171],v[0:3],a[68:71]
EX v120,v120
MM a[96:99],a[176:179],v[0:3],a[96:99]
EX v121,v121
MM a[96:99],a[180:183],v[4:7],a[96:99]
RD a[252:255],v227 offset:12288
CV v92,v120,v121
MM a[104:107],a[180:183],v[12:15],a[104:107]
EX v122,v122
MM a[104:107],a[176:179],v[8:11],a[104:107]
buffer_load_dwordx4 v235, s[12:15], s64 offen offset:3072 lds
MM a[112:115],a[176:179],v[16:19],a[112:115]
EX v123,v123
MM a[112:115],a[180:183],v[20:23],a[112:115]
LPG21_%=:
s_cbranch_execz LTR4_%=
EX v124,v124
MM a[120:123],a[180:183],v[28:31],a[120:123]
EX v125,v125
MM a[120:123],a[176:179],v[24:27],a[120:123]
EX v126,v126
MM a[124:127],a[184:187],v[24:27],a[124:127]
EX v127,v127
MM a[124:127],a[188:191],v[28:31],a[124:127]
CV v93,v122,v123
CV v94,v124,v125
MM a[116:119],a[188:191],v[20:23],a[116:119]
CV v95,v126,v127
MM a[116:119],a[184:187],v[16:19],a[116:119]
MM a[108:111],a[184:187],v[8:11],a[108:111]
MM a[108:111],a[188:191],v[12:15],a[108:111]
MM a[100:103],a[188:191],v[4:7],a[100:103]
MM a[100:103],a[184:187],v[0:3],a[100:103]
v_smfmac_f32_16x16x64_bf16 v[144:147], v[236:239], v[64:71], v234
v_smfmac_f32_16x16x64_bf16 v[148:151], v[236:239], v[72:79], v234
v_smfmac_f32_16x16x64_bf16 v[152:155], v[236:239], v[80:87], v234
v_smfmac_f32_16x16x64_bf16 v[156:159], v[236:239], v[88:95], v234
s_waitcnt lgkmcnt(0)
MM v[0:3],a[192:195],v[160:163],v[128:131]
s_add_u32 s64, s64, 16384
TR a[128:129],v232 offset:49152
s_add_u32 s81, s64, 896
MM v[0:3],a[196:199],v[176:179],v[0:3]
TR a[130:131],v232 offset:53248
TR a[132:133],v232 offset:57344
MM v[0:3],a[200:203],v[192:195],v[0:3]
v_max3_f32 v249, v144, v145, v148
v_max3_f32 v249, v249, v149, v152
MM v[0:3],a[204:207],v[208:211],v[0:3]
v_max3_f32 v249, v249, v153, v156
v_max_f32 v249, v249, v157
MM v[4:7],a[220:223],v[208:211],v[128:131]
TR a[134:135],v232 offset:61440
v_cmp_lt_f32 vcc, s71, v249
MM v[4:7],a[216:219],v[192:195],v[4:7]
s_cbranch_vccnz LSITE31_%=
LBACK31_%=:
TR a[136:137],v233 offset:49152
v_add_f32 v244, v144, v145
MM v[4:7],a[212:215],v[176:179],v[4:7]
s_waitcnt vmcnt(8)
v_add_f32 v245, v148, v149
v_add_f32 v246, v152, v153
MM v[4:7],a[208:211],v[160:163],v[4:7]
TR a[138:139],v233 offset:53248
v_add_f32 v247, v156, v157
MM v[12:15],a[208:211],v[164:167],v[132:135]
v_cmp_ne_u32 vcc, s69, v252
s_cbranch_vccnz LRFY_%=
LRFYb_%=:
MM v[12:15],a[212:215],v[180:183],v[12:15]
v_lshrrev_b32 v109, 2, v248
v_lshrrev_b32 v110, 3, v248
MM v[12:15],a[216:219],v[196:199],v[12:15]
s_and_b32 s76, s17, 7
EX v0,v0
MM v[12:15],a[220:223],v[212:215],v[12:15]
s_mul_i32 s76, s76, 2560
EX v1,v1
MM v[8:11],a[204:207],v[212:215],v[132:135]
s_lshl_b32 s91, s90, 8
TR a[140:141],v233 offset:57344
CV v0,v0,v1
MM v[8:11],a[200:203],v[196:199],v[8:11]
s_add_u32 s76, s76, s91
EX v2,v2
MM v[8:11],a[196:199],v[180:183],v[8:11]
s_add_u32 s76, s76, 21659648
buffer_load_dword v96, v109, s[20:23], s76 offen offset:0 sc1
MM v[8:11],a[192:195],v[164:167],v[8:11]
s_add_u32 s76, s76, 20480
TR a[142:143],v233 offset:61440
TR a[144:145],v232 offset:49664
MM v[16:19],a[192:195],v[168:171],v[136:139]
buffer_load_dword v97, v109, s[20:23], s76 offen offset:0 sc1
MM v[16:19],a[196:199],v[184:187],v[16:19]
s_add_u32 s76, s76, 20480
EX v3,v3
MM v[16:19],a[200:203],v[200:203],v[16:19]
buffer_load_dword v98, v109, s[20:23], s76 offen offset:0 sc1
MM v[16:19],a[204:207],v[216:219],v[16:19]
s_add_u32 s76, s76, 20480
EX v4,v4
MM v[20:23],a[220:223],v[216:219],v[136:139]
buffer_load_dword v99, v109, s[20:23], s76 offen offset:0 sc1
MM v[20:23],a[216:219],v[200:203],v[20:23]
s_add_u32 s76, s76, 20480
EX v5,v5
MM v[20:23],a[212:215],v[184:187],v[20:23]
buffer_load_dword v100, v109, s[20:23], s76 offen offset:0 sc1
MM v[20:23],a[208:211],v[168:171],v[20:23]
s_add_u32 s76, s76, 20480
TR a[146:147],v232 offset:53760
EX v6,v6
MM v[28:31],a[208:211],v[172:175],v[140:143]
buffer_load_dword v101, v109, s[20:23], s76 offen offset:0 sc1
MM v[28:31],a[212:215],v[188:191],v[28:31]
s_add_u32 s76, s76, 20480
TR a[148:149],v232 offset:57856
EX v7,v7
MM v[28:31],a[216:219],v[204:207],v[28:31]
buffer_load_dword v102, v109, s[20:23], s76 offen offset:0 sc1
MM v[28:31],a[220:223],v[220:223],v[28:31]
s_add_u32 s76, s76, 20480
TR a[150:151],v232 offset:61952
EX v8,v8
MM v[24:27],a[204:207],v[220:223],v[140:143]
buffer_load_dword v103, v109, s[20:23], s76 offen offset:0 sc1
MM v[24:27],a[200:203],v[204:207],v[24:27]
s_add_u32 s76, s76, 20480
TR a[152:153],v233 offset:49664
EX v9,v9
CV v1,v2,v3
MM v[24:27],a[196:199],v[188:191],v[24:27]
buffer_load_dword v104, v109, s[20:23], s76 offen offset:0 sc1
TR a[154:155],v233 offset:53760
MM v[24:27],a[192:195],v[172:175],v[24:27]
s_and_b32 s76, s19, 65535
EX v10,v10
MM v[32:35],a[224:227],v[160:163],v[128:131]
s_sub_u32 s76, 65535, s76
TR a[156:157],v233 offset:57856
CV v2,v4,v5
EX v11,v11
MM v[32:35],a[228:231],v[176:179],v[32:35]
s_mul_i32 s76, s76, 1280
TR a[158:159],v233 offset:61952
CV v3,v6,v7
MM v[32:35],a[232:235],v[192:195],v[32:35]
s_lshl_b32 s91, s90, 7
EX v12,v12
MM v[32:35],a[236:239],v[208:211],v[32:35]
s_add_u32 s76, s76, s91
TR a[160:161],v232 offset:50176
TR a[162:163],v232 offset:54272
MM v[36:39],a[252:255],v[208:211],v[128:131]
s_add_u32 s76, s76, 131072
buffer_load_ushort v108, v110, s[20:23], s76 offen sc1
EX v13,v13
MM v[36:39],a[248:251],v[192:195],v[36:39]
s_ashr_i32 s91, s19, 31
EX v14,v14
MM v[36:39],a[244:247],v[176:179],v[36:39]
s_not_b32 s91, s91
TR a[164:165],v232 offset:58368
CV v8,v8,v9
MM v[36:39],a[240:243],v[160:163],v[36:39]
s_or_b32 s91, s91, 0x80000000
EX v15,v15
MM v[44:47],a[240:243],v[164:167],v[132:135]
s_xor_b32 s19, s19, s91
TR a[166:167],v232 offset:62464
CV v9,v10,v11
MM v[44:47],a[244:247],v[180:183],v[44:47]
s_and_b32 s19, s19, 0xffff0000
v_mov_b32 v111, 0xbfb8aa3b
v_mul_f32 v111, s19, v111
MM v[44:47],a[248:251],v[196:199],v[44:47]
EX v111,v111
EX v16,v16
MM v[44:47],a[252:255],v[212:215],v[44:47]
TR a[168:169],v233 offset:50176
v_add_f32 v111, 1.0, v111
EX v17,v17
MM v[40:43],a[236:239],v[212:215],v[132:135]
v_rcp_f32 v111, v111
MM v[40:43],a[232:235],v[196:199],v[40:43]
TR a[170:171],v233 offset:54272
CV v111,v111,v111
v_and_b32 v111, 0xffff0000, v111
EX v18,v18
MM v[40:43],a[228:231],v[180:183],v[40:43]
TR a[172:173],v233 offset:58368
CV v10,v12,v13
MM v[40:43],a[224:227],v[164:167],v[40:43]
EX v19,v19
MM v[48:51],a[224:227],v[168:171],v[136:139]
TR a[174:175],v233 offset:62464
CV v11,v14,v15
MM v[48:51],a[228:231],v[184:187],v[48:51]
EX v20,v20
MM v[48:51],a[232:235],v[200:203],v[48:51]
TR a[176:177],v232 offset:50688
TR a[178:179],v232 offset:54784
MM v[48:51],a[236:239],v[216:219],v[48:51]
EX v21,v21
MM v[52:55],a[252:255],v[216:219],v[136:139]
EX v22,v22
MM v[52:55],a[248:251],v[200:203],v[52:55]
TR a[180:181],v232 offset:58880
CV v16,v16,v17
MM v[52:55],a[244:247],v[184:187],v[52:55]
EX v23,v23
MM v[52:55],a[240:243],v[168:171],v[52:55]
TR a[182:183],v232 offset:62976
CV v17,v18,v19
MM v[60:63],a[240:243],v[172:175],v[140:143]
EX v24,v24
MM v[60:63],a[244:247],v[188:191],v[60:63]
TR a[184:185],v233 offset:50688
TR a[186:187],v233 offset:54784
MM v[60:63],a[248:251],v[204:207],v[60:63]
TR a[188:189],v233 offset:58880
TR a[190:191],v233 offset:62976
MM v[60:63],a[252:255],v[220:223],v[60:63]
EX v25,v25
MM v[56:59],a[236:239],v[220:223],v[140:143]
EX v26,v26
MM v[56:59],a[232:235],v[204:207],v[56:59]
EX v27,v27
MM v[56:59],a[228:231],v[188:191],v[56:59]
EX v28,v28
MM v[56:59],a[224:227],v[172:175],v[56:59]
s_waitcnt lgkmcnt(0)
s_barrier
MM a[0:3],a[128:131],v[64:67],a[0:3]
s_add_u32 m0, s65, 49152
CV v18,v20,v21
CV v19,v22,v23
MM a[0:3],a[132:135],v[68:71],a[0:3]
EX v29,v29
MM a[8:11],a[132:135],v[76:79],a[8:11]
CV v24,v24,v25
CV v25,v26,v27
MM a[8:11],a[128:131],v[72:75],a[8:11]
EX v30,v30
MM a[16:19],a[128:131],v[80:83],a[16:19]
EX v31,v31
MM a[16:19],a[132:135],v[84:87],a[16:19]
RD a[192:195],v224 offset:16384
CV v26,v28,v29
MM a[24:27],a[132:135],v[92:95],a[24:27]
EX v32,v32
MM a[24:27],a[128:131],v[88:91],a[24:27]
EX v33,v33
MM a[28:31],a[136:139],v[88:91],a[28:31]
RD a[196:199],v225 offset:16384
CV v27,v30,v31
MM a[28:31],a[140:143],v[92:95],a[28:31]
EX v34,v34
MM a[20:23],a[140:143],v[84:87],a[20:23]
buffer_load_dwordx4 v228, s[8:11], s64 offen offset:0 lds
MM a[20:23],a[136:139],v[80:83],a[20:23]
EX v35,v35
MM a[12:15],a[136:139],v[72:75],a[12:15]
RD a[200:203],v226 offset:16384
)ASM"
R"ASM(
CV v4,v32,v33
MM a[12:15],a[140:143],v[76:79],a[12:15]
EX v36,v36
MM a[4:7],a[140:143],v[68:71],a[4:7]
EX v37,v37
MM a[4:7],a[136:139],v[64:67],a[4:7]
RD a[204:207],v227 offset:16384
CV v5,v34,v35
MM a[32:35],a[144:147],v[64:67],a[32:35]
EX v38,v38
MM a[32:35],a[148:151],v[68:71],a[32:35]
buffer_load_dwordx4 v229, s[8:11], s64 offen offset:1024 lds
MM a[40:43],a[148:151],v[76:79],a[40:43]
EX v39,v39
MM a[40:43],a[144:147],v[72:75],a[40:43]
RD a[208:211],v224 offset:20480
CV v6,v36,v37
MM a[48:51],a[144:147],v[80:83],a[48:51]
EX v40,v40
MM a[48:51],a[148:151],v[84:87],a[48:51]
RD a[212:215],v225 offset:20480
CV v7,v38,v39
MM a[56:59],a[148:151],v[92:95],a[56:59]
EX v41,v41
MM a[56:59],a[144:147],v[88:91],a[56:59]
buffer_load_dwordx4 v230, s[8:11], s64 offen offset:2048 lds
MM a[60:63],a[152:155],v[88:91],a[60:63]
EX v42,v42
MM a[60:63],a[156:159],v[92:95],a[60:63]
EX v43,v43
MM a[52:55],a[156:159],v[84:87],a[52:55]
RD a[216:219],v226 offset:20480
CV v12,v40,v41
MM a[52:55],a[152:155],v[80:83],a[52:55]
EX v44,v44
MM a[44:47],a[152:155],v[72:75],a[44:47]
RD a[220:223],v227 offset:20480
CV v13,v42,v43
MM a[44:47],a[156:159],v[76:79],a[44:47]
EX v45,v45
MM a[36:39],a[156:159],v[68:71],a[36:39]
buffer_load_dwordx4 v231, s[8:11], s64 offen offset:3072 lds
MM a[36:39],a[152:155],v[64:67],a[36:39]
s_add_u32 m0, s65, 114688
EX v46,v46
MM a[64:67],a[160:163],v[64:67],a[64:67]
RD a[224:227],v224 offset:24576
CV v14,v44,v45
MM a[64:67],a[164:167],v[68:71],a[64:67]
EX v47,v47
MM a[72:75],a[164:167],v[76:79],a[72:75]
EX v48,v48
MM a[72:75],a[160:163],v[72:75],a[72:75]
RD a[228:231],v225 offset:24576
CV v15,v46,v47
MM a[80:83],a[160:163],v[80:83],a[80:83]
EX v49,v49
MM a[80:83],a[164:167],v[84:87],a[80:83]
buffer_load_dwordx4 v235, s[12:15], s81 offen offset:0 lds
MM a[88:91],a[164:167],v[92:95],a[88:91]
EX v50,v50
MM a[88:91],a[160:163],v[88:91],a[88:91]
RD a[232:235],v226 offset:24576
CV v20,v48,v49
MM a[92:95],a[168:171],v[88:91],a[92:95]
EX v51,v51
MM a[92:95],a[172:175],v[92:95],a[92:95]
RD a[236:239],v227 offset:24576
CV v21,v50,v51
MM a[84:87],a[172:175],v[84:87],a[84:87]
EX v52,v52
MM a[84:87],a[168:171],v[80:83],a[84:87]
EX v53,v53
MM a[76:79],a[168:171],v[72:75],a[76:79]
RD a[240:243],v224 offset:28672
CV v22,v52,v53
MM a[76:79],a[172:175],v[76:79],a[76:79]
EX v54,v54
MM a[68:71],a[172:175],v[68:71],a[68:71]
s_waitcnt vmcnt(5)
v_add_f32 v96, v96, v100
v_add_f32 v97, v97, v101
MM a[68:71],a[168:171],v[64:67],a[68:71]
v_add_f32 v98, v98, v102
v_add_f32 v99, v99, v103
MM a[96:99],a[176:179],v[64:67],a[96:99]
v_add_f32 v96, v96, v98
v_add_f32 v97, v97, v99
MM a[96:99],a[180:183],v[68:71],a[96:99]
v_add_f32 v96, v96, v104
v_lshlrev_b32 v108, 16, v108
MM a[104:107],a[180:183],v[76:79],a[104:107]
v_add_f32 v96, v96, v97
CV v96,v96,v96
EX v55,v55
MM a[104:107],a[176:179],v[72:75],a[104:107]
v_and_b32 v96, 0xffff0000, v96
v_mul_f32 v96, v96, v111
MM a[112:115],a[176:179],v[80:83],a[112:115]
CV v96,v96,v96
v_and_b32 v96, 0xffff0000, v96
EX v56,v56
MM a[112:115],a[180:183],v[84:87],a[112:115]
v_add_f32 v96, v96, v108
CV v96,v96,v96
MM a[120:123],a[180:183],v[92:95],a[120:123]
s_mov_b64 exec, s[44:45]
buffer_store_short v96, v110, s[20:23], s76 offen offset:0 sc1
s_mov_b64 exec, -1
s_nop 0
EX v57,v57
MM a[120:123],a[176:179],v[88:91],a[120:123]
EX v58,v58
MM a[124:127],a[184:187],v[88:91],a[124:127]
buffer_load_dwordx4 v235, s[12:15], s64 offen offset:1024 lds
MM a[124:127],a[188:191],v[92:95],a[124:127]
EX v59,v59
MM a[116:119],a[188:191],v[84:87],a[116:119]
CV v23,v54,v55
CV v28,v56,v57
MM a[116:119],a[184:187],v[80:83],a[116:119]
EX v60,v60
MM a[108:111],a[184:187],v[72:75],a[108:111]
EX v61,v61
MM a[108:111],a[188:191],v[76:79],a[108:111]
buffer_load_dwordx4 v235, s[12:15], s81 offen offset:2048 lds
MM a[100:103],a[188:191],v[68:71],a[100:103]
EX v62,v62
MM a[100:103],a[184:187],v[64:67],a[100:103]
RD a[244:247],v225 offset:28672
CV v29,v58,v59
v_smfmac_f32_16x16x64_bf16 v[144:147], v[236:239], v[0:7], v234
RD a[248:251],v226 offset:28672
RD a[252:255],v227 offset:28672
EX v63,v63
v_smfmac_f32_16x16x64_bf16 v[148:151], v[236:239], v[8:15], v234
CV v30,v60,v61
v_smfmac_f32_16x16x64_bf16 v[152:155], v[236:239], v[16:23], v234
CV v31,v62,v63
s_nop 1
v_smfmac_f32_16x16x64_bf16 v[156:159], v[236:239], v[24:31], v234
s_waitcnt lgkmcnt(0)
buffer_load_dwordx4 v235, s[12:15], s64 offen offset:3072 lds
MM v[64:67],a[192:195],v[160:163],v[128:131]
s_sub_u32 s64, s64, 507904
TR a[128:129],v232 offset:0
s_add_u32 s81, s64, 896
MM v[64:67],a[196:199],v[176:179],v[64:67]
TR a[130:131],v232 offset:4096
TR a[132:133],v232 offset:8192
MM v[64:67],a[200:203],v[192:195],v[64:67]
v_max3_f32 v249, v144, v145, v148
v_max3_f32 v249, v249, v149, v152
MM v[64:67],a[204:207],v[208:211],v[64:67]
v_max3_f32 v249, v249, v153, v156
v_max_f32 v249, v249, v157
MM v[68:71],a[220:223],v[208:211],v[128:131]
TR a[134:135],v232 offset:12288
v_cmp_lt_f32 vcc, s71, v249
MM v[68:71],a[216:219],v[192:195],v[68:71]
s_cbranch_vccnz LSITE32_%=
LBACK32_%=:
TR a[136:137],v233 offset:0
v_add_f32 v244, v144, v145
MM v[68:71],a[212:215],v[176:179],v[68:71]
v_add_f32 v245, v148, v149
v_add_f32 v246, v152, v153
MM v[68:71],a[208:211],v[160:163],v[68:71]
TR a[138:139],v233 offset:4096
v_add_f32 v247, v156, v157
MM v[76:79],a[208:211],v[164:167],v[132:135]
EX v64,v64
MM v[76:79],a[212:215],v[180:183],v[76:79]
TR a[140:141],v233 offset:8192
TR a[142:143],v233 offset:12288
MM v[76:79],a[216:219],v[196:199],v[76:79]
EX v65,v65
MM v[76:79],a[220:223],v[212:215],v[76:79]
TR a[144:145],v232 offset:512
CV v64,v64,v65
MM v[72:75],a[204:207],v[212:215],v[132:135]
EX v66,v66
MM v[72:75],a[200:203],v[196:199],v[72:75]
EX v67,v67
MM v[72:75],a[196:199],v[180:183],v[72:75]
TR a[146:147],v232 offset:4608
CV v65,v66,v67
MM v[72:75],a[192:195],v[164:167],v[72:75]
EX v68,v68
MM v[80:83],a[192:195],v[168:171],v[136:139]
TR a[148:149],v232 offset:8704
TR a[150:151],v232 offset:12800
MM v[80:83],a[196:199],v[184:187],v[80:83]
EX v69,v69
MM v[80:83],a[200:203],v[200:203],v[80:83]
TR a[152:153],v233 offset:512
CV v66,v68,v69
MM v[80:83],a[204:207],v[216:219],v[80:83]
EX v70,v70
MM v[84:87],a[220:223],v[216:219],v[136:139]
EX v71,v71
MM v[84:87],a[216:219],v[200:203],v[84:87]
TR a[154:155],v233 offset:4608
CV v67,v70,v71
MM v[84:87],a[212:215],v[184:187],v[84:87]
EX v72,v72
MM v[84:87],a[208:211],v[168:171],v[84:87]
TR a[156:157],v233 offset:8704
TR a[158:159],v233 offset:12800
MM v[92:95],a[208:211],v[172:175],v[140:143]
EX v73,v73
MM v[92:95],a[212:215],v[188:191],v[92:95]
TR a[160:161],v232 offset:1024
CV v72,v72,v73
MM v[92:95],a[216:219],v[204:207],v[92:95]
EX v74,v74
MM v[92:95],a[220:223],v[220:223],v[92:95]
EX v75,v75
MM v[88:91],a[204:207],v[220:223],v[140:143]
TR a[162:163],v232 offset:5120
CV v73,v74,v75
MM v[88:91],a[200:203],v[204:207],v[88:91]
TR a[164:165],v232 offset:9216
TR a[166:167],v232 offset:13312
MM v[88:91],a[196:199],v[188:191],v[88:91]
EX v76,v76
MM v[88:91],a[192:195],v[172:175],v[88:91]
EX v77,v77
MM v[96:99],a[224:227],v[160:163],v[128:131]
TR a[168:169],v233 offset:1024
CV v74,v76,v77
MM v[96:99],a[228:231],v[176:179],v[96:99]
EX v78,v78
MM v[96:99],a[232:235],v[192:195],v[96:99]
TR a[170:171],v233 offset:5120
TR a[172:173],v233 offset:9216
MM v[96:99],a[236:239],v[208:211],v[96:99]
EX v79,v79
MM v[100:103],a[252:255],v[208:211],v[128:131]
TR a[174:175],v233 offset:13312
CV v75,v78,v79
MM v[100:103],a[248:251],v[192:195],v[100:103]
EX v80,v80
MM v[100:103],a[244:247],v[176:179],v[100:103]
EX v81,v81
MM v[100:103],a[240:243],v[160:163],v[100:103]
TR a[176:177],v232 offset:1536
CV v80,v80,v81
MM v[108:111],a[240:243],v[164:167],v[132:135]
EX v82,v82
MM v[108:111],a[244:247],v[180:183],v[108:111]
TR a[178:179],v232 offset:5632
TR a[180:181],v232 offset:9728
MM v[108:111],a[248:251],v[196:199],v[108:111]
EX v83,v83
MM v[108:111],a[252:255],v[212:215],v[108:111]
TR a[182:183],v232 offset:13824
CV v81,v82,v83
MM v[104:107],a[236:239],v[212:215],v[132:135]
EX v84,v84
MM v[104:107],a[232:235],v[196:199],v[104:107]
EX v85,v85
MM v[104:107],a[228:231],v[180:183],v[104:107]
TR a[184:185],v233 offset:1536
CV v82,v84,v85
MM v[104:107],a[224:227],v[164:167],v[104:107]
EX v86,v86
MM v[112:115],a[224:227],v[168:171],v[136:139]
TR a[186:187],v233 offset:5632
TR a[188:189],v233 offset:9728
MM v[112:115],a[228:231],v[184:187],v[112:115]
EX v87,v87
MM v[112:115],a[232:235],v[200:203],v[112:115]
TR a[190:191],v233 offset:13824
CV v83,v86,v87
MM v[112:115],a[236:239],v[216:219],v[112:115]
EX v88,v88
MM v[116:119],a[252:255],v[216:219],v[136:139]
EX v89,v89
MM v[116:119],a[248:251],v[200:203],v[116:119]
EX v90,v90
MM v[116:119],a[244:247],v[184:187],v[116:119]
EX v91,v91
MM v[116:119],a[240:243],v[168:171],v[116:119]
EX v92,v92
MM v[124:127],a[240:243],v[172:175],v[140:143]
EX v93,v93
MM v[124:127],a[244:247],v[188:191],v[124:127]
EX v94,v94
MM v[124:127],a[248:251],v[204:207],v[124:127]
EX v95,v95
MM v[124:127],a[252:255],v[220:223],v[124:127]
EX v96,v96
MM v[120:123],a[236:239],v[220:223],v[140:143]
EX v97,v97
MM v[120:123],a[232:235],v[204:207],v[120:123]
EX v98,v98
MM v[120:123],a[228:231],v[188:191],v[120:123]
EX v99,v99
MM v[120:123],a[224:227],v[172:175],v[120:123]
s_waitcnt lgkmcnt(0)
s_barrier
MM a[0:3],a[128:131],v[0:3],a[0:3]
s_add_u32 m0, s65, 0
CV v88,v88,v89
CV v89,v90,v91
MM a[0:3],a[132:135],v[4:7],a[0:3]
RD a[192:195],v224 offset:32768
CV v90,v92,v93
MM a[8:11],a[132:135],v[12:15],a[8:11]
RD a[196:199],v225 offset:32768
CV v91,v94,v95
MM a[8:11],a[128:131],v[8:11],a[8:11]
buffer_load_dwordx4 v228, s[8:11], s64 offen offset:0 lds
MM a[16:19],a[128:131],v[16:19],a[16:19]
RD a[200:203],v226 offset:32768
CV v68,v96,v97
MM a[16:19],a[132:135],v[20:23],a[16:19]
RD a[204:207],v227 offset:32768
CV v69,v98,v99
MM a[24:27],a[132:135],v[28:31],a[24:27]
buffer_load_dwordx4 v229, s[8:11], s64 offen offset:1024 lds
MM a[24:27],a[128:131],v[24:27],a[24:27]
RD a[208:211],v224 offset:36864
RD a[212:215],v225 offset:36864
MM a[28:31],a[136:139],v[24:27],a[28:31]
buffer_load_dwordx4 v230, s[8:11], s64 offen offset:2048 lds
MM a[28:31],a[140:143],v[28:31],a[28:31]
EX v100,v100
MM a[20:23],a[140:143],v[20:23],a[20:23]
EX v101,v101
MM a[20:23],a[136:139],v[16:19],a[20:23]
RD a[216:219],v226 offset:36864
CV v70,v100,v101
MM a[12:15],a[136:139],v[8:11],a[12:15]
EX v102,v102
MM a[12:15],a[140:143],v[12:15],a[12:15]
RD a[220:223],v227 offset:36864
RD a[224:227],v224 offset:40960
MM a[4:7],a[140:143],v[4:7],a[4:7]
EX v103,v103
MM a[4:7],a[136:139],v[0:3],a[4:7]
RD a[228:231],v225 offset:40960
CV v71,v102,v103
MM a[32:35],a[144:147],v[0:3],a[32:35]
EX v104,v104
LPG22_%=:
s_cbranch_execz LPG26_%=
MM a[32:35],a[148:151],v[4:7],a[32:35]
buffer_load_dwordx4 v231, s[8:11], s64 offen offset:3072 lds
MM a[40:43],a[148:151],v[12:15],a[40:43]
s_add_u32 m0, s65, 65536
EX v105,v105
MM a[40:43],a[144:147],v[8:11],a[40:43]
RD a[232:235],v226 offset:40960
CV v76,v104,v105
MM a[48:51],a[144:147],v[16:19],a[48:51]
EX v106,v106
MM a[48:51],a[148:151],v[20:23],a[48:51]
EX v107,v107
MM a[56:59],a[148:151],v[28:31],a[56:59]
RD a[236:239],v227 offset:40960
CV v77,v106,v107
MM a[56:59],a[144:147],v[24:27],a[56:59]
EX v108,v108
MM a[60:63],a[152:155],v[24:27],a[60:63]
buffer_load_dwordx4 v235, s[12:15], s81 offen offset:0 lds
MM a[60:63],a[156:159],v[28:31],a[60:63]
EX v109,v109
MM a[52:55],a[156:159],v[20:23],a[52:55]
RD a[240:243],v224 offset:45056
CV v78,v108,v109
MM a[52:55],a[152:155],v[16:19],a[52:55]
EX v110,v110
MM a[44:47],a[152:155],v[8:11],a[44:47]
EX v111,v111
MM a[44:47],a[156:159],v[12:15],a[44:47]
RD a[244:247],v225 offset:45056
CV v79,v110,v111
MM a[36:39],a[156:159],v[4:7],a[36:39]
buffer_load_dwordx4 v235, s[12:15], s64 offen offset:1024 lds
MM a[36:39],a[152:155],v[0:3],a[36:39]
EX v112,v112
MM a[64:67],a[160:163],v[0:3],a[64:67]
EX v113,v113
MM a[64:67],a[164:167],v[4:7],a[64:67]
RD a[248:251],v226 offset:45056
CV v84,v112,v113
MM a[72:75],a[164:167],v[12:15],a[72:75]
EX v114,v114
MM a[72:75],a[160:163],v[8:11],a[72:75]
EX v115,v115
MM a[80:83],a[160:163],v[16:19],a[80:83]
)ASM"
R"ASM(
RD a[252:255],v227 offset:45056
CV v85,v114,v115
MM a[80:83],a[164:167],v[20:23],a[80:83]
buffer_load_dwordx4 v235, s[12:15], s81 offen offset:2048 lds
MM a[88:91],a[164:167],v[28:31],a[88:91]
EX v116,v116
MM a[88:91],a[160:163],v[24:27],a[88:91]
EX v117,v117
MM a[92:95],a[168:171],v[24:27],a[92:95]
EX v118,v118
MM a[92:95],a[172:175],v[28:31],a[92:95]
buffer_load_dwordx4 v235, s[12:15], s64 offen offset:3072 lds
MM a[84:87],a[172:175],v[20:23],a[84:87]
EX v119,v119
MM a[84:87],a[168:171],v[16:19],a[84:87]
EX v120,v120
MM a[76:79],a[168:171],v[8:11],a[76:79]
EX v121,v121
MM a[76:79],a[172:175],v[12:15],a[76:79]
EX v122,v122
MM a[68:71],a[172:175],v[4:7],a[68:71]
EX v123,v123
MM a[68:71],a[168:171],v[0:3],a[68:71]
EX v124,v124
MM a[96:99],a[176:179],v[0:3],a[96:99]
EX v125,v125
MM a[96:99],a[180:183],v[4:7],a[96:99]
EX v126,v126
MM a[104:107],a[180:183],v[12:15],a[104:107]
EX v127,v127
MM a[104:107],a[176:179],v[8:11],a[104:107]
CV v86,v116,v117
CV v87,v118,v119
MM a[112:115],a[176:179],v[16:19],a[112:115]
CV v92,v120,v121
CV v93,v122,v123
MM a[112:115],a[180:183],v[20:23],a[112:115]
CV v94,v124,v125
CV v95,v126,v127
MM a[120:123],a[180:183],v[28:31],a[120:123]
s_lshl_b32 s91, s17, 2
s_add_u32 s91, s91, 94464
s_and_b64 s[44:45], s[44:45], 1
MM a[120:123],a[176:179],v[24:27],a[120:123]
s_waitcnt vmcnt(11)
v_mov_b32 v250, s69
s_mov_b64 exec, s[44:45]
buffer_store_dword v250, off, s[20:23], s91 sc1
s_mov_b64 exec, -1
MM a[124:127],a[184:187],v[24:27],a[124:127]
MM a[124:127],a[188:191],v[28:31],a[124:127]
MM a[116:119],a[188:191],v[20:23],a[116:119]
MM a[116:119],a[184:187],v[16:19],a[116:119]
MM a[108:111],a[184:187],v[8:11],a[108:111]
MM a[108:111],a[188:191],v[12:15],a[108:111]
MM a[100:103],a[188:191],v[4:7],a[100:103]
MM a[100:103],a[184:187],v[0:3],a[100:103]
v_smfmac_f32_16x16x64_bf16 v[144:147], v[236:239], v[64:71], v234
v_smfmac_f32_16x16x64_bf16 v[148:151], v[236:239], v[72:79], v234
v_smfmac_f32_16x16x64_bf16 v[152:155], v[236:239], v[80:87], v234
v_smfmac_f32_16x16x64_bf16 v[156:159], v[236:239], v[88:95], v234
s_waitcnt lgkmcnt(0)
LLAJ_%=:
MM v[0:3],a[192:195],v[160:163],v[128:131]
s_add_u32 s64, s64, 16384
TR a[128:129],v232 offset:16384
s_add_u32 s81, s64, 896
MM v[0:3],a[196:199],v[176:179],v[0:3]
TR a[130:131],v232 offset:20480
TR a[132:133],v232 offset:24576
MM v[0:3],a[200:203],v[192:195],v[0:3]
v_max3_f32 v249, v144, v145, v148
v_max3_f32 v249, v249, v149, v152
MM v[0:3],a[204:207],v[208:211],v[0:3]
v_max3_f32 v249, v249, v153, v156
v_max_f32 v249, v249, v157
MM v[4:7],a[220:223],v[208:211],v[128:131]
TR a[134:135],v232 offset:28672
v_cmp_lt_f32 vcc, s71, v249
MM v[4:7],a[216:219],v[192:195],v[4:7]
s_cbranch_vccnz LSITE33_%=
LBACK33_%=:
TR a[136:137],v233 offset:16384
v_add_f32 v244, v144, v145
MM v[4:7],a[212:215],v[176:179],v[4:7]
v_add_f32 v245, v148, v149
v_add_f32 v246, v152, v153
MM v[4:7],a[208:211],v[160:163],v[4:7]
v_add_f32 v247, v156, v157
v_bfe_u32 v242, v248, 6, 4
MM v[12:15],a[208:211],v[164:167],v[132:135]
v_mul_u32_u24 v242, 1280, v242
v_and_b32 v241, 48, v248
MM v[12:15],a[212:215],v[180:183],v[12:15]
v_bfe_u32 v243, v248, 8, 1
v_lshlrev_b32 v243, 5, v243
MM v[12:15],a[216:219],v[196:199],v[12:15]
v_xor_b32 v241, v241, v243
v_add_u32 v241, v241, v242
MM v[12:15],a[220:223],v[212:215],v[12:15]
v_lshrrev_b32 v242, 3, v248
v_xor_b32 v243, v242, v248
MM v[8:11],a[204:207],v[212:215],v[132:135]
v_and_b32 v243, 96, v243
v_and_or_b32 v243, v248, 16, v243
MM v[8:11],a[200:203],v[196:199],v[8:11]
s_and_b32 s66, s49, 8
EX v0,v0
MM v[8:11],a[196:199],v[180:183],v[8:11]
s_lshl_b32 s66, s66, 3
v_lshrrev_b32 v242, 7, v248
v_xor_b32 v243, s66, v243
MM v[8:11],a[192:195],v[164:167],v[8:11]
v_mul_u32_u24 v242, 1280, v242
v_add_u32 v240, v242, v243
EX v1,v1
MM v[16:19],a[192:195],v[168:171],v[136:139]
s_mul_i32 s88, %[swave], 40960
EX v2,v2
MM v[16:19],a[196:199],v[184:187],v[16:19]
s_and_b32 s66, s84, 1
TR a[138:139],v233 offset:20480
TR a[140:141],v233 offset:24576
MM v[16:19],a[200:203],v[200:203],v[16:19]
s_lshl_b32 s86, s66, 6
EX v3,v3
MM v[16:19],a[204:207],v[216:219],v[16:19]
s_add_u32 s86, s86, s88
EX v4,v4
MM v[20:23],a[220:223],v[216:219],v[136:139]
s_xor_b32 s66, s66, 1
TR a[142:143],v233 offset:28672
TR a[144:145],v232 offset:16896
MM v[20:23],a[216:219],v[200:203],v[20:23]
s_lshl_b32 s66, s66, 8
EX v5,v5
MM v[20:23],a[212:215],v[184:187],v[20:23]
s_add_u32 s88, s88, s66
TR a[146:147],v232 offset:20992
CV v0,v0,v1
MM v[20:23],a[208:211],v[168:171],v[20:23]
s_and_b32 s66, s49, 16
EX v6,v6
MM v[28:31],a[208:211],v[172:175],v[140:143]
s_lshl_b32 s66, s66, 3
TR a[148:149],v232 offset:25088
TR a[150:151],v232 offset:29184
MM v[28:31],a[212:215],v[188:191],v[28:31]
s_sub_u32 s87, s86, s66
EX v7,v7
MM v[28:31],a[216:219],v[204:207],v[28:31]
s_add_u32 s87, s87, 128
EX v8,v8
MM v[28:31],a[220:223],v[220:223],v[28:31]
s_add_u32 s86, s86, s66
TR a[152:153],v233 offset:16896
CV v1,v2,v3
MM v[24:27],a[204:207],v[220:223],v[140:143]
s_and_b32 s94, s49, 7
EX v9,v9
MM v[24:27],a[200:203],v[204:207],v[24:27]
s_lshl_b32 s89, %[swave], 11
TR a[154:155],v233 offset:20992
CV v2,v4,v5
MM v[24:27],a[196:199],v[188:191],v[24:27]
s_add_u32 s89, s89, %[slds]
EX v10,v10
MM v[24:27],a[192:195],v[172:175],v[24:27]
s_xor_b32 s66, s94, 4
TR a[156:157],v233 offset:25088
CV v3,v6,v7
MM v[32:35],a[224:227],v[160:163],v[128:131]
s_mul_i32 s66, s66, 163840
EX v11,v11
MM v[32:35],a[228:231],v[176:179],v[32:35]
s_add_u32 s66, s66, s88
s_add_u32 m0, s89, 131072
s_nop 0
buffer_load_dwordx4 v241, s[24:27], s66 offen offset:0 lds
MM v[32:35],a[232:235],v[192:195],v[32:35]
s_add_u32 s66, s66, 19456
TR a[158:159],v233 offset:29184
TR a[160:161],v232 offset:17408
EX v12,v12
MM v[32:35],a[236:239],v[208:211],v[32:35]
EX v13,v13
MM v[36:39],a[252:255],v[208:211],v[128:131]
TR a[162:163],v232 offset:21504
CV v8,v8,v9
MM v[36:39],a[248:251],v[192:195],v[36:39]
EX v14,v14
MM v[36:39],a[244:247],v[176:179],v[36:39]
TR a[164:165],v232 offset:25600
CV v9,v10,v11
MM v[36:39],a[240:243],v[160:163],v[36:39]
EX v15,v15
MM v[44:47],a[240:243],v[164:167],v[132:135]
TR a[166:167],v232 offset:29696
TR a[168:169],v233 offset:17408
MM v[44:47],a[244:247],v[180:183],v[44:47]
EX v16,v16
MM v[44:47],a[248:251],v[196:199],v[44:47]
s_add_u32 m0, s89, 131072
s_nop 0
buffer_load_dwordx4 v241, s[24:27], s66 offen offset:1024 lds
MM v[44:47],a[252:255],v[212:215],v[44:47]
EX v17,v17
MM v[40:43],a[236:239],v[212:215],v[132:135]
TR a[170:171],v233 offset:21504
CV v10,v12,v13
MM v[40:43],a[232:235],v[196:199],v[40:43]
EX v18,v18
MM v[40:43],a[228:231],v[180:183],v[40:43]
TR a[172:173],v233 offset:25600
TR a[174:175],v233 offset:29696
MM v[40:43],a[224:227],v[164:167],v[40:43]
EX v19,v19
MM v[48:51],a[224:227],v[168:171],v[136:139]
TR a[176:177],v232 offset:17920
CV v11,v14,v15
MM v[48:51],a[228:231],v[184:187],v[48:51]
EX v20,v20
MM v[48:51],a[232:235],v[200:203],v[48:51]
s_lshl_b32 s60, s50, 2
EX v21,v21
MM v[48:51],a[236:239],v[216:219],v[48:51]
s_add_u32 s60, s60, %[swave]
TR a[178:179],v232 offset:22016
CV v16,v16,v17
MM v[52:55],a[252:255],v[216:219],v[136:139]
s_lshl_b32 s60, s60, 1
EX v22,v22
MM v[52:55],a[248:251],v[200:203],v[52:55]
s_bfe_u32 s61, s49, 65538
TR a[180:181],v232 offset:26112
TR a[182:183],v232 offset:30208
MM v[52:55],a[244:247],v[184:187],v[52:55]
s_xor_b32 s61, s61, 1
EX v23,v23
MM v[52:55],a[240:243],v[168:171],v[52:55]
s_add_u32 s60, s60, s61
TR a[184:185],v233 offset:17920
CV v17,v18,v19
MM v[60:63],a[240:243],v[172:175],v[140:143]
s_mul_i32 s18, s60, 40960
EX v24,v24
MM v[60:63],a[244:247],v[188:191],v[60:63]
s_mul_i32 s60, s84, 320
TR a[186:187],v233 offset:22016
CV v18,v20,v21
MM v[60:63],a[248:251],v[204:207],v[60:63]
s_add_u32 s18, s18, s60
TR a[188:189],v233 offset:26112
TR a[190:191],v233 offset:30208
MM v[60:63],a[252:255],v[220:223],v[60:63]
s_lshr_b32 s62, s49, 3
EX v25,v25
MM v[56:59],a[236:239],v[220:223],v[140:143]
s_mul_i32 s63, s62, 10240
EX v26,v26
MM v[56:59],a[232:235],v[204:207],v[56:59]
s_add_u32 s63, s63, s18
EX v27,v27
MM v[56:59],a[228:231],v[188:191],v[56:59]
s_and_b32 s60, s84, 1
CV v19,v22,v23
CV v24,v24,v25
MM v[56:59],a[224:227],v[172:175],v[56:59]
s_waitcnt vmcnt(14) lgkmcnt(0)
s_barrier
MM a[0:3],a[128:131],v[64:67],a[0:3]
s_add_u32 s61, s60, s62
EX v28,v28
MM a[0:3],a[132:135],v[68:71],a[0:3]
s_lshl_b32 s61, s61, 6
EX v29,v29
MM a[8:11],a[132:135],v[76:79],a[8:11]
s_add_u32 s18, s18, s61
EX v30,v30
MM a[8:11],a[128:131],v[72:75],a[8:11]
s_xor_b32 s60, s60, 1
CV v25,v26,v27
CV v26,v28,v29
MM a[16:19],a[128:131],v[80:83],a[16:19]
s_lshl_b32 s60, s60, 8
EX v31,v31
MM a[16:19],a[132:135],v[84:87],a[16:19]
s_add_u32 s63, s63, s60
EX v32,v32
MM a[24:27],a[132:135],v[92:95],a[24:27]
s_mul_i32 s60, s62, 33792
RD a[192:195],v224 offset:49152
CV v27,v30,v31
MM a[24:27],a[128:131],v[88:91],a[24:27]
s_sub_u32 s60, s95, s60
EX v33,v33
MM a[28:31],a[136:139],v[88:91],a[28:31]
s_xor_b32 s61, s62, 1
s_add_u32 m0, s65, 16384
RD a[196:199],v225 offset:49152
MM a[28:31],a[140:143],v[92:95],a[28:31]
s_mul_i32 s61, s61, 33792
EX v34,v34
MM a[20:23],a[140:143],v[84:87],a[20:23]
s_add_u32 s61, s61, s60
buffer_load_dwordx4 v228, s[8:11], s64 offen offset:0 lds
MM a[20:23],a[136:139],v[80:83],a[20:23]
s_add_u32 s36, s61, 2048
EX v35,v35
MM a[12:15],a[136:139],v[72:75],a[12:15]
s_xor_b32 s61, s62, 2
RD a[200:203],v226 offset:49152
CV v4,v32,v33
MM a[12:15],a[140:143],v[76:79],a[12:15]
s_mul_i32 s61, s61, 33792
EX v36,v36
MM a[4:7],a[140:143],v[68:71],a[4:7]
s_add_u32 s61, s61, s60
EX v37,v37
MM a[4:7],a[136:139],v[64:67],a[4:7]
s_add_u32 s37, s61, 4096
CV v5,v34,v35
MM a[32:35],a[144:147],v[64:67],a[32:35]
s_xor_b32 s61, s62, 3
EX v38,v38
MM a[32:35],a[148:151],v[68:71],a[32:35]
s_mul_i32 s61, s61, 33792
buffer_load_dwordx4 v229, s[8:11], s64 offen offset:1024 lds
MM a[40:43],a[148:151],v[76:79],a[40:43]
s_add_u32 s61, s61, s60
EX v39,v39
MM a[40:43],a[144:147],v[72:75],a[40:43]
s_add_u32 s38, s61, 6144
RD a[204:207],v227 offset:49152
CV v6,v36,v37
MM a[48:51],a[144:147],v[80:83],a[48:51]
s_lshl_b32 s61, s62, 9
EX v40,v40
MM a[48:51],a[148:151],v[84:87],a[48:51]
s_add_u32 s39, s60, s61
RD a[208:211],v224 offset:53248
CV v7,v38,v39
MM a[56:59],a[148:151],v[92:95],a[56:59]
s_mov_b32 s17, 15
EX v41,v41
MM a[56:59],a[144:147],v[88:91],a[56:59]
s_mov_b32 s58, 0
RD a[212:215],v225 offset:53248
MM a[60:63],a[152:155],v[88:91],a[60:63]
s_lshl_b32 s60, s50, 1
EX v42,v42
MM a[60:63],a[156:159],v[92:95],a[60:63]
s_bfe_u32 s61, s49, 65538
buffer_load_dwordx4 v230, s[8:11], s64 offen offset:2048 lds
MM a[52:55],a[156:159],v[84:87],a[52:55]
s_add_u32 s60, s60, s61
EX v43,v43
MM a[52:55],a[152:155],v[80:83],a[52:55]
s_lshl_b32 s60, s60, 2
RD a[216:219],v226 offset:53248
CV v12,v40,v41
MM a[44:47],a[152:155],v[72:75],a[44:47]
s_add_u32 s60, s60, s84
EX v44,v44
MM a[44:47],a[156:159],v[76:79],a[44:47]
s_lshl_b32 s60, s60, 2
EX v45,v45
MM a[36:39],a[156:159],v[68:71],a[36:39]
s_add_u32 s68, s60, %[swave]
CV v13,v42,v43
MM a[36:39],a[152:155],v[64:67],a[36:39]
s_lshl_b32 s68, s68, 6
EX v46,v46
MM a[64:67],a[160:163],v[64:67],a[64:67]
s_add_u32 s68, s68, 16384
buffer_load_dwordx4 v231, s[8:11], s64 offen offset:3072 lds
MM a[64:67],a[164:167],v[68:71],a[64:67]
s_lshr_b32 s61, s49, 3
EX v47,v47
MM a[72:75],a[164:167],v[76:79],a[72:75]
s_lshl_b32 s19, s61, 4
RD a[220:223],v227 offset:53248
CV v14,v44,v45
MM a[72:75],a[160:163],v[72:75],a[72:75]
s_add_u32 s19, s19, s68
EX v48,v48
MM a[80:83],a[160:163],v[80:83],a[80:83]
s_lshr_b32 s96, s49, 2
RD a[224:227],v224 offset:57344
CV v15,v46,v47
MM a[80:83],a[164:167],v[84:87],a[80:83]
s_lshl_b32 s97, s50, 3
EX v49,v49
MM a[88:91],a[164:167],v[92:95],a[88:91]
s_add_u32 s96, s96, s97
EX v50,v50
MM a[88:91],a[160:163],v[88:91],a[88:91]
s_lshl_b32 s96, s96, 2
s_add_u32 m0, s65, 81920
CV v20,v48,v49
MM a[92:95],a[168:171],v[88:91],a[92:95]
s_add_u32 s96, s96, %[swave]
EX v51,v51
MM a[92:95],a[172:175],v[92:95],a[92:95]
s_lshl_b32 s96, s96, 6
buffer_load_dwordx4 v235, s[12:15], s81 offen offset:0 lds
MM a[84:87],a[172:175],v[84:87],a[84:87]
s_add_u32 s96, s96, 0
RD a[228:231],v225 offset:57344
)ASM"
R"ASM(
CV v21,v50,v51
MM a[84:87],a[168:171],v[80:83],a[84:87]
s_xor_b32 s44, s96, 256
EX v52,v52
MM a[76:79],a[168:171],v[72:75],a[76:79]
s_xor_b32 s66, s94, 5
EX v53,v53
MM a[76:79],a[172:175],v[76:79],a[76:79]
s_mul_i32 s66, s66, 163840
RD a[232:235],v226 offset:57344
MM a[68:71],a[172:175],v[68:71],a[68:71]
s_add_u32 s66, s66, s88
s_add_u32 m0, s89, 139264
s_nop 0
buffer_load_dwordx4 v241, s[24:27], s66 offen offset:0 lds
MM a[68:71],a[168:171],v[64:67],a[68:71]
s_add_u32 s66, s66, 19456
EX v54,v54
MM a[96:99],a[176:179],v[64:67],a[96:99]
EX v55,v55
MM a[96:99],a[180:183],v[68:71],a[96:99]
RD a[236:239],v227 offset:57344
CV v22,v52,v53
MM a[104:107],a[180:183],v[76:79],a[104:107]
EX v56,v56
MM a[104:107],a[176:179],v[72:75],a[104:107]
s_add_u32 m0, s65, 81920
s_nop 0
buffer_load_dwordx4 v235, s[12:15], s64 offen offset:1024 lds
MM a[112:115],a[176:179],v[80:83],a[112:115]
EX v57,v57
MM a[112:115],a[180:183],v[84:87],a[112:115]
RD a[240:243],v224 offset:61440
CV v23,v54,v55
MM a[120:123],a[180:183],v[92:95],a[120:123]
EX v58,v58
MM a[120:123],a[176:179],v[88:91],a[120:123]
s_add_u32 m0, s89, 139264
s_nop 0
buffer_load_dwordx4 v241, s[24:27], s66 offen offset:1024 lds
MM a[124:127],a[184:187],v[88:91],a[124:127]
EX v59,v59
MM a[124:127],a[188:191],v[92:95],a[124:127]
RD a[244:247],v225 offset:61440
CV v28,v56,v57
MM a[116:119],a[188:191],v[84:87],a[116:119]
EX v60,v60
MM a[116:119],a[184:187],v[80:83],a[116:119]
s_add_u32 m0, s65, 81920
s_nop 0
buffer_load_dwordx4 v235, s[12:15], s81 offen offset:2048 lds
MM a[108:111],a[184:187],v[72:75],a[108:111]
EX v61,v61
MM a[108:111],a[188:191],v[76:79],a[108:111]
RD a[248:251],v226 offset:61440
CV v29,v58,v59
MM a[100:103],a[188:191],v[68:71],a[100:103]
EX v62,v62
MM a[100:103],a[184:187],v[64:67],a[100:103]
EX v63,v63
v_smfmac_f32_16x16x64_bf16 v[144:147], v[236:239], v[0:7], v234
RD a[252:255],v227 offset:61440
v_smfmac_f32_16x16x64_bf16 v[148:151], v[236:239], v[8:15], v234
CV v30,v60,v61
v_smfmac_f32_16x16x64_bf16 v[152:155], v[236:239], v[16:23], v234
CV v31,v62,v63
s_nop 1
v_smfmac_f32_16x16x64_bf16 v[156:159], v[236:239], v[24:31], v234
s_waitcnt lgkmcnt(0)
buffer_load_dwordx4 v235, s[12:15], s64 offen offset:3072 lds
MM v[64:67],a[192:195],v[160:163],v[128:131]
s_add_u32 s64, s64, 16384
TR a[128:129],v232 offset:32768
s_add_u32 s81, s64, 896
MM v[64:67],a[196:199],v[176:179],v[64:67]
TR a[130:131],v232 offset:36864
TR a[132:133],v232 offset:40960
MM v[64:67],a[200:203],v[192:195],v[64:67]
v_max3_f32 v249, v144, v145, v148
v_max3_f32 v249, v249, v149, v152
MM v[64:67],a[204:207],v[208:211],v[64:67]
v_max3_f32 v249, v249, v153, v156
v_max_f32 v249, v249, v157
MM v[68:71],a[220:223],v[208:211],v[128:131]
TR a[134:135],v232 offset:45056
v_cmp_lt_f32 vcc, s71, v249
MM v[68:71],a[216:219],v[192:195],v[68:71]
s_cbranch_vccnz LSITE34_%=
LBACK34_%=:
TR a[136:137],v233 offset:32768
v_add_f32 v244, v144, v145
MM v[68:71],a[212:215],v[176:179],v[68:71]
v_add_f32 v245, v148, v149
v_add_f32 v246, v152, v153
MM v[68:71],a[208:211],v[160:163],v[68:71]
v_add_f32 v247, v156, v157
v_and_b32 v32, 496, v248
MM v[76:79],a[208:211],v[164:167],v[132:135]
buffer_load_dword v242, off, s[20:23], s96 sc1
MM v[76:79],a[212:215],v[180:183],v[76:79]
buffer_load_dword v243, off, s[20:23], s19 sc1
v_mul_u32_u24 v32, 80, v32
MM v[76:79],a[216:219],v[196:199],v[76:79]
s_xor_b32 s66, s94, 6
EX v64,v64
MM v[76:79],a[220:223],v[212:215],v[76:79]
s_mul_i32 s66, s66, 163840
TR a[138:139],v233 offset:36864
TR a[140:141],v233 offset:40960
MM v[72:75],a[204:207],v[212:215],v[132:135]
s_add_u32 s66, s66, s88
s_add_u32 m0, s89, 147456
s_nop 0
buffer_load_dwordx4 v241, s[24:27], s66 offen offset:0 lds
MM v[72:75],a[200:203],v[196:199],v[72:75]
s_add_u32 s66, s66, 19456
EX v65,v65
MM v[72:75],a[196:199],v[180:183],v[72:75]
s_sub_u32 s60, s63, s18
TR a[142:143],v233 offset:45056
v_add_u32 v32, s18, v32
MM v[72:75],a[192:195],v[164:167],v[72:75]
s_mov_b32 s61, 131072
s_mov_b32 exec_lo, 0
v_add_u32 v32, s60, v32
s_mov_b64 exec, -1
MM v[80:83],a[192:195],v[168:171],v[136:139]
s_mov_b32 exec_hi, 0xff
buffer_load_dword v36, v32, s[32:35], 0 offen offset:0
s_mov_b64 exec, -1
EX v66,v66
MM v[80:83],a[196:199],v[184:187],v[80:83]
EX v67,v67
MM v[80:83],a[200:203],v[200:203],v[80:83]
EX v68,v68
MM v[80:83],a[204:207],v[216:219],v[80:83]
s_mov_b32 exec_hi, 0xff
buffer_load_dword v37, v32, s[20:23], s61 offen offset:0 sc1
s_mov_b64 exec, -1
MM v[84:87],a[220:223],v[216:219],v[136:139]
TR a[144:145],v232 offset:33280
CV v64,v64,v65
MM v[84:87],a[216:219],v[200:203],v[84:87]
s_add_u32 m0, s89, 147456
s_nop 0
buffer_load_dwordx4 v241, s[24:27], s66 offen offset:1024 lds
EX v69,v69
MM v[84:87],a[212:215],v[184:187],v[84:87]
TR a[146:147],v232 offset:37376
TR a[148:149],v232 offset:41472
MM v[84:87],a[208:211],v[168:171],v[84:87]
EX v70,v70
MM v[92:95],a[208:211],v[172:175],v[140:143]
EX v71,v71
MM v[92:95],a[212:215],v[188:191],v[92:95]
TR a[150:151],v232 offset:45568
CV v65,v66,v67
MM v[92:95],a[216:219],v[204:207],v[92:95]
EX v72,v72
MM v[92:95],a[220:223],v[220:223],v[92:95]
TR a[152:153],v233 offset:33280
CV v66,v68,v69
MM v[88:91],a[204:207],v[220:223],v[140:143]
EX v73,v73
MM v[88:91],a[200:203],v[204:207],v[88:91]
s_xor_b32 s66, s94, 7
LPG23_%=:
s_cbranch_execz LPG27_%=
TR a[154:155],v233 offset:37376
TR a[156:157],v233 offset:41472
MM v[88:91],a[196:199],v[188:191],v[88:91]
s_mul_i32 s66, s66, 163840
EX v74,v74
MM v[88:91],a[192:195],v[172:175],v[88:91]
s_add_u32 s66, s66, s88
s_add_u32 m0, s89, 155648
s_nop 0
buffer_load_dwordx4 v241, s[24:27], s66 offen offset:0 lds
MM v[96:99],a[224:227],v[160:163],v[128:131]
s_add_u32 s66, s66, 19456
EX v75,v75
MM v[96:99],a[228:231],v[176:179],v[96:99]
TR a[158:159],v233 offset:45568
CV v67,v70,v71
MM v[96:99],a[232:235],v[192:195],v[96:99]
EX v76,v76
MM v[96:99],a[236:239],v[208:211],v[96:99]
TR a[160:161],v232 offset:33792
TR a[162:163],v232 offset:37888
MM v[100:103],a[252:255],v[208:211],v[128:131]
EX v77,v77
MM v[100:103],a[248:251],v[192:195],v[100:103]
EX v78,v78
MM v[100:103],a[244:247],v[176:179],v[100:103]
TR a[164:165],v232 offset:41984
CV v72,v72,v73
MM v[100:103],a[240:243],v[160:163],v[100:103]
EX v79,v79
MM v[108:111],a[240:243],v[164:167],v[132:135]
s_add_u32 m0, s89, 155648
s_nop 0
buffer_load_dwordx4 v241, s[24:27], s66 offen offset:1024 lds
MM v[108:111],a[244:247],v[180:183],v[108:111]
TR a[166:167],v232 offset:46080
CV v73,v74,v75
MM v[108:111],a[248:251],v[196:199],v[108:111]
EX v80,v80
MM v[108:111],a[252:255],v[212:215],v[108:111]
TR a[168:169],v233 offset:33792
TR a[170:171],v233 offset:37888
EX v81,v81
MM v[104:107],a[236:239],v[212:215],v[132:135]
TR a[172:173],v233 offset:41984
CV v74,v76,v77
MM v[104:107],a[232:235],v[196:199],v[104:107]
EX v82,v82
MM v[104:107],a[228:231],v[180:183],v[104:107]
EX v83,v83
MM v[104:107],a[224:227],v[164:167],v[104:107]
TR a[174:175],v233 offset:46080
CV v75,v78,v79
MM v[112:115],a[224:227],v[168:171],v[136:139]
EX v84,v84
MM v[112:115],a[228:231],v[184:187],v[112:115]
TR a[176:177],v232 offset:34304
TR a[178:179],v232 offset:38400
MM v[112:115],a[232:235],v[200:203],v[112:115]
EX v85,v85
MM v[112:115],a[236:239],v[216:219],v[112:115]
TR a[180:181],v232 offset:42496
CV v80,v80,v81
MM v[116:119],a[252:255],v[216:219],v[136:139]
EX v86,v86
MM v[116:119],a[248:251],v[200:203],v[116:119]
TR a[182:183],v232 offset:46592
CV v81,v82,v83
MM v[116:119],a[244:247],v[184:187],v[116:119]
EX v87,v87
MM v[116:119],a[240:243],v[168:171],v[116:119]
TR a[184:185],v233 offset:34304
CV v82,v84,v85
MM v[124:127],a[240:243],v[172:175],v[140:143]
EX v88,v88
MM v[124:127],a[244:247],v[188:191],v[124:127]
TR a[186:187],v233 offset:38400
TR a[188:189],v233 offset:42496
MM v[124:127],a[248:251],v[204:207],v[124:127]
TR a[190:191],v233 offset:46592
CV v83,v86,v87
EX v89,v89
MM v[124:127],a[252:255],v[220:223],v[124:127]
EX v90,v90
MM v[120:123],a[236:239],v[220:223],v[140:143]
EX v91,v91
MM v[120:123],a[232:235],v[204:207],v[120:123]
EX v92,v92
MM v[120:123],a[228:231],v[188:191],v[120:123]
EX v93,v93
MM v[120:123],a[224:227],v[172:175],v[120:123]
s_waitcnt vmcnt(24) lgkmcnt(0)
s_barrier
MM a[0:3],a[128:131],v[0:3],a[0:3]
s_add_u32 m0, s65, 32768
CV v88,v88,v89
CV v89,v90,v91
MM a[0:3],a[132:135],v[4:7],a[0:3]
EX v94,v94
MM a[8:11],a[132:135],v[12:15],a[8:11]
RD a[192:195],v224 offset:0
CV v90,v92,v93
MM a[8:11],a[128:131],v[8:11],a[8:11]
EX v95,v95
MM a[16:19],a[128:131],v[16:19],a[16:19]
RD a[196:199],v225 offset:0
CV v91,v94,v95
MM a[16:19],a[132:135],v[20:23],a[16:19]
EX v96,v96
MM a[24:27],a[132:135],v[28:31],a[24:27]
buffer_load_dwordx4 v228, s[8:11], s64 offen offset:0 lds
MM a[24:27],a[128:131],v[24:27],a[24:27]
EX v97,v97
MM a[28:31],a[136:139],v[24:27],a[28:31]
EX v98,v98
MM a[28:31],a[140:143],v[28:31],a[28:31]
RD a[200:203],v226 offset:0
CV v68,v96,v97
MM a[20:23],a[140:143],v[20:23],a[20:23]
EX v99,v99
MM a[20:23],a[136:139],v[16:19],a[20:23]
TR a[128:129],v232 offset:49152
CV v69,v98,v99
MM a[12:15],a[136:139],v[8:11],a[12:15]
EX v100,v100
MM a[12:15],a[140:143],v[12:15],a[12:15]
TR a[130:131],v232 offset:53248
RD a[204:207],v227 offset:0
MM a[4:7],a[140:143],v[4:7],a[4:7]
EX v101,v101
MM a[4:7],a[136:139],v[0:3],a[4:7]
TR a[132:133],v232 offset:57344
CV v70,v100,v101
MM a[32:35],a[144:147],v[0:3],a[32:35]
EX v102,v102
MM a[32:35],a[148:151],v[4:7],a[32:35]
buffer_load_dwordx4 v229, s[8:11], s64 offen offset:1024 lds
MM a[40:43],a[148:151],v[12:15],a[40:43]
TR a[134:135],v232 offset:61440
TR a[136:137],v233 offset:49152
MM a[40:43],a[144:147],v[8:11],a[40:43]
EX v103,v103
MM a[48:51],a[144:147],v[16:19],a[48:51]
EX v104,v104
MM a[48:51],a[148:151],v[20:23],a[48:51]
TR a[138:139],v233 offset:53248
RD a[208:211],v224 offset:4096
MM a[56:59],a[148:151],v[28:31],a[56:59]
EX v105,v105
MM a[56:59],a[144:147],v[24:27],a[56:59]
TR a[140:141],v233 offset:57344
CV v71,v102,v103
MM a[60:63],a[152:155],v[24:27],a[60:63]
EX v106,v106
MM a[60:63],a[156:159],v[28:31],a[60:63]
TR a[142:143],v233 offset:61440
RD a[212:215],v225 offset:4096
MM a[52:55],a[156:159],v[20:23],a[52:55]
EX v107,v107
MM a[52:55],a[152:155],v[16:19],a[52:55]
buffer_load_dwordx4 v230, s[8:11], s64 offen offset:2048 lds
MM a[44:47],a[152:155],v[8:11],a[44:47]
TR a[144:145],v232 offset:49664
CV v76,v104,v105
MM a[44:47],a[156:159],v[12:15],a[44:47]
EX v108,v108
MM a[36:39],a[156:159],v[4:7],a[36:39]
TR a[146:147],v232 offset:53760
TR a[148:149],v232 offset:57856
MM a[36:39],a[152:155],v[0:3],a[36:39]
EX v109,v109
MM a[64:67],a[160:163],v[0:3],a[64:67]
EX v110,v110
MM a[64:67],a[164:167],v[4:7],a[64:67]
TR a[150:151],v232 offset:61952
CV v77,v106,v107
MM a[72:75],a[164:167],v[12:15],a[72:75]
EX v111,v111
MM a[72:75],a[160:163],v[8:11],a[72:75]
RD a[216:219],v226 offset:4096
TR a[152:153],v233 offset:49664
MM a[80:83],a[160:163],v[16:19],a[80:83]
EX v112,v112
MM a[80:83],a[164:167],v[20:23],a[80:83]
buffer_load_dwordx4 v231, s[8:11], s64 offen offset:3072 lds
MM a[88:91],a[164:167],v[28:31],a[88:91]
s_add_u32 m0, s65, 98304
EX v113,v113
MM a[88:91],a[160:163],v[24:27],a[88:91]
TR a[154:155],v233 offset:53760
CV v78,v108,v109
MM a[92:95],a[168:171],v[24:27],a[92:95]
EX v114,v114
MM a[92:95],a[172:175],v[28:31],a[92:95]
TR a[156:157],v233 offset:57856
RD a[220:223],v227 offset:4096
MM a[84:87],a[172:175],v[20:23],a[84:87]
EX v115,v115
MM a[84:87],a[168:171],v[16:19],a[84:87]
TR a[158:159],v233 offset:61952
CV v79,v110,v111
MM a[76:79],a[168:171],v[8:11],a[76:79]
EX v116,v116
MM a[76:79],a[172:175],v[12:15],a[76:79]
TR a[160:161],v232 offset:50176
RD a[224:227],v224 offset:8192
MM a[68:71],a[172:175],v[4:7],a[68:71]
EX v117,v117
MM a[68:71],a[168:171],v[0:3],a[68:71]
TR a[162:163],v232 offset:54272
TR a[164:165],v232 offset:58368
MM a[96:99],a[176:179],v[0:3],a[96:99]
EX v118,v118
MM a[96:99],a[180:183],v[4:7],a[96:99]
buffer_load_dwordx4 v235, s[12:15], s81 offen offset:0 lds
MM a[104:107],a[180:183],v[12:15],a[104:107]
TR a[166:167],v232 offset:62464
EX v119,v119
CV v84,v112,v113
MM a[104:107],a[176:179],v[8:11],a[104:107]
RD a[228:231],v225 offset:8192
)ASM"
R"ASM(
TR a[168:169],v233 offset:50176
MM a[112:115],a[176:179],v[16:19],a[112:115]
EX v120,v120
MM a[112:115],a[180:183],v[20:23],a[112:115]
TR a[170:171],v233 offset:54272
CV v85,v114,v115
MM a[120:123],a[180:183],v[28:31],a[120:123]
s_waitcnt vmcnt(8)
EX v121,v121
MM a[120:123],a[176:179],v[24:27],a[120:123]
s_lshr_b32 s77, %[sb], 3
RD a[232:235],v226 offset:8192
TR a[172:173],v233 offset:58368
MM a[124:127],a[184:187],v[24:27],a[124:127]
s_lshl_b32 s77, s77, 2
EX v122,v122
MM a[124:127],a[188:191],v[28:31],a[124:127]
s_add_u32 s77, s77, %[swave]
TR a[174:175],v233 offset:62464
CV v86,v116,v117
MM a[116:119],a[188:191],v[20:23],a[116:119]
s_lshl_b32 s77, s77, 9
buffer_load_dwordx4 v235, s[12:15], s64 offen offset:1024 lds
EX v123,v123
MM a[116:119],a[184:187],v[16:19],a[116:119]
s_add_u32 s77, s77, 0
TR a[176:177],v232 offset:50688
v_lshrrev_b32 v253, 1, v248
v_add_u32 v253, s77, v253
MM a[108:111],a[184:187],v[8:11],a[108:111]
s_mov_b32 s77, LKEND_ka-LPD2_%=-16
RD a[236:239],v227 offset:8192
v_min_u32 v253, s77, v253
EX v124,v124
CV v87,v118,v119
MM a[108:111],a[188:191],v[12:15],a[108:111]
s_getpc_b64 s[98:99]
LPD2_%=:
global_load_dword v253, v253, s[98:99]
TR a[178:179],v232 offset:54784
EX v125,v125
MM a[100:103],a[188:191],v[4:7],a[100:103]
TR a[180:181],v232 offset:58880
RD a[240:243],v224 offset:12288
MM a[100:103],a[184:187],v[0:3],a[100:103]
EX v126,v126
v_smfmac_f32_16x16x64_bf16 v[144:147], v[236:239], v[64:71], v234
RD a[244:247],v225 offset:12288
RD a[248:251],v226 offset:12288
RD a[252:255],v227 offset:12288
EX v127,v127
v_smfmac_f32_16x16x64_bf16 v[148:151], v[236:239], v[72:79], v234
TR a[182:183],v232 offset:62976
CV v92,v120,v121
v_smfmac_f32_16x16x64_bf16 v[152:155], v[236:239], v[80:87], v234
buffer_load_dwordx4 v235, s[12:15], s81 offen offset:2048 lds
CV v93,v122,v123
CV v94,v124,v125
CV v95,v126,v127
s_nop 1
v_smfmac_f32_16x16x64_bf16 v[156:159], v[236:239], v[88:95], v234
TR a[184:185],v233 offset:50688
TR a[186:187],v233 offset:54784
TR a[188:189],v233 offset:58880
TR a[190:191],v233 offset:62976
s_waitcnt lgkmcnt(5)
buffer_load_dwordx4 v235, s[12:15], s64 offen offset:3072 lds
MM v[16:19],a[192:195],v[168:171],v[136:139]
s_add_u32 s64, s64, 16384
s_add_u32 s81, s64, 896
MM v[16:19],a[196:199],v[184:187],v[16:19]
MM v[16:19],a[200:203],v[200:203],v[16:19]
v_max3_f32 v249, v144, v145, v148
v_max3_f32 v249, v249, v149, v152
MM v[16:19],a[204:207],v[216:219],v[16:19]
v_max3_f32 v249, v249, v153, v156
v_max_f32 v249, v249, v157
MM v[20:23],a[220:223],v[216:219],v[136:139]
v_cmp_lt_f32 vcc, s71, v249
MM v[20:23],a[216:219],v[200:203],v[20:23]
s_cbranch_vccnz LSITE35_%=
LBACK35_%=:
v_add_f32 v244, v144, v145
MM v[20:23],a[212:215],v[184:187],v[20:23]
v_add_f32 v245, v148, v149
v_add_f32 v246, v152, v153
MM v[20:23],a[208:211],v[168:171],v[20:23]
s_waitcnt lgkmcnt(0)
v_add_f32 v247, v156, v157
MM a[0:3],a[128:131],v[64:67],a[0:3]
EX v16,v16
MM a[0:3],a[132:135],v[68:71],a[0:3]
v_rcp_f32 v145, v244
MM a[8:11],a[132:135],v[76:79],a[8:11]
EX v17,v17
MM a[8:11],a[128:131],v[72:75],a[8:11]
EX v18,v18
MM a[12:15],a[136:139],v[72:75],a[12:15]
v_rcp_f32 v149, v245
MM a[12:15],a[140:143],v[76:79],a[12:15]
s_mul_i32 s78, s49, 17408
s_add_u32 s78, s83, s78
CV v16,v16,v17
MM a[4:7],a[140:143],v[68:71],a[4:7]
s_add_u32 s78, s78, 0
EX v19,v19
MM a[4:7],a[136:139],v[64:67],a[4:7]
s_add_u32 s79, s78, 4096
EX v20,v20
MM a[32:35],a[144:147],v[64:67],a[32:35]
EX v21,v21
MM a[32:35],a[148:151],v[68:71],a[32:35]
EX v22,v22
MM a[40:43],a[148:151],v[76:79],a[40:43]
EX v23,v23
MM a[40:43],a[144:147],v[72:75],a[40:43]
AR v0,a0
AR v1,a1
MM a[44:47],a[152:155],v[72:75],a[44:47]
AR v2,a2
AR v3,a3
MM a[44:47],a[156:159],v[76:79],a[44:47]
AR v4,a4
AR v5,a5
MM a[36:39],a[156:159],v[68:71],a[36:39]
AR v6,a6
AR v7,a7
v_mul_f32 v0, v0, v145
MM a[36:39],a[152:155],v[64:67],a[36:39]
v_mul_f32 v1, v1, v145
v_mul_f32 v2, v2, v145
MM a[64:67],a[160:163],v[64:67],a[64:67]
v_mul_f32 v3, v3, v145
v_mul_f32 v4, v4, v145
v_mul_f32 v5, v5, v145
MM a[64:67],a[164:167],v[68:71],a[64:67]
v_mul_f32 v6, v6, v145
v_mul_f32 v7, v7, v145
MM a[72:75],a[164:167],v[76:79],a[72:75]
CV v160,v0,v1
CV v161,v2,v3
CV v162,v4,v5
MM a[72:75],a[160:163],v[72:75],a[72:75]
CV v163,v6,v7
CV v17,v18,v19
MM a[76:79],a[168:171],v[72:75],a[76:79]
buffer_store_dwordx4 v[160:163], v248, s[20:23], s78 offen offset:0 sc1
AR v8,a8
AR v9,a9
MM a[76:79],a[172:175],v[76:79],a[76:79]
AR v10,a10
AR v11,a11
MM a[68:71],a[172:175],v[68:71],a[68:71]
AR v12,a12
AR v13,a13
MM a[68:71],a[168:171],v[64:67],a[68:71]
AR v14,a14
AR v15,a15
v_mul_f32 v8, v8, v149
MM a[96:99],a[176:179],v[64:67],a[96:99]
v_mul_f32 v9, v9, v149
v_mul_f32 v10, v10, v149
MM a[96:99],a[180:183],v[68:71],a[96:99]
v_mul_f32 v11, v11, v149
v_mul_f32 v12, v12, v149
v_mul_f32 v13, v13, v149
MM a[104:107],a[180:183],v[76:79],a[104:107]
v_mul_f32 v14, v14, v149
v_mul_f32 v15, v15, v149
MM a[104:107],a[176:179],v[72:75],a[104:107]
CV v164,v8,v9
CV v165,v10,v11
CV v166,v12,v13
MM a[108:111],a[184:187],v[72:75],a[108:111]
CV v167,v14,v15
CV v18,v20,v21
MM a[108:111],a[188:191],v[76:79],a[108:111]
buffer_store_dwordx4 v[164:167], v248, s[20:23], s79 offen offset:0 sc1
AR v32,a32
MM a[100:103],a[188:191],v[68:71],a[100:103]
AR v33,a33
AR v34,a34
AR v35,a35
MM a[100:103],a[184:187],v[64:67],a[100:103]
AR v36,a36
AR v37,a37
MM v[28:31],a[208:211],v[172:175],v[140:143]
AR v38,a38
AR v39,a39
v_mul_f32 v32, v32, v145
MM v[28:31],a[212:215],v[188:191],v[28:31]
v_mul_f32 v33, v33, v145
v_mul_f32 v34, v34, v145
MM v[28:31],a[216:219],v[204:207],v[28:31]
v_mul_f32 v35, v35, v145
v_mul_f32 v36, v36, v145
v_mul_f32 v37, v37, v145
MM v[28:31],a[220:223],v[220:223],v[28:31]
v_mul_f32 v38, v38, v145
v_mul_f32 v39, v39, v145
MM v[24:27],a[204:207],v[220:223],v[140:143]
CV v176,v32,v33
CV v177,v34,v35
CV v178,v36,v37
MM v[24:27],a[200:203],v[204:207],v[24:27]
CV v179,v38,v39
CV v19,v22,v23
MM v[24:27],a[196:199],v[188:191],v[24:27]
buffer_store_dwordx4 v[176:179], v248, s[20:23], s78 offen offset:1024 sc1
AR v40,a40
AR v41,a41
MM v[24:27],a[192:195],v[172:175],v[24:27]
AR v42,a42
AR v43,a43
MM v[48:51],a[224:227],v[168:171],v[136:139]
AR v44,a44
AR v45,a45
AR v46,a46
MM v[48:51],a[228:231],v[184:187],v[48:51]
AR v47,a47
v_mul_f32 v40, v40, v149
MM v[48:51],a[232:235],v[200:203],v[48:51]
v_mul_f32 v41, v41, v149
v_mul_f32 v42, v42, v149
MM v[48:51],a[236:239],v[216:219],v[48:51]
v_mul_f32 v43, v43, v149
v_mul_f32 v44, v44, v149
v_mul_f32 v45, v45, v149
MM v[52:55],a[252:255],v[216:219],v[136:139]
EX v24,v24
EX v25,v25
EX v26,v26
EX v27,v27
EX v28,v28
EX v29,v29
v_mul_f32 v46, v46, v149
EX v30,v30
v_mul_f32 v47, v47, v149
MM v[52:55],a[248:251],v[200:203],v[52:55]
CV v180,v40,v41
CV v181,v42,v43
CV v182,v44,v45
EX v31,v31
CV v24,v24,v25
MM v[52:55],a[244:247],v[184:187],v[52:55]
CV v183,v46,v47
CV v25,v26,v27
MM v[52:55],a[240:243],v[168:171],v[52:55]
buffer_store_dwordx4 v[180:183], v248, s[20:23], s79 offen offset:1024 sc1
AR v0,a64
AR v1,a65
MM v[60:63],a[240:243],v[172:175],v[140:143]
AR v2,a66
AR v3,a67
EX v48,v48
MM v[60:63],a[244:247],v[188:191],v[60:63]
AR v4,a68
AR v5,a69
MM v[60:63],a[248:251],v[204:207],v[60:63]
AR v6,a70
AR v7,a71
v_mul_f32 v0, v0, v145
MM v[60:63],a[252:255],v[220:223],v[60:63]
v_mul_f32 v1, v1, v145
v_mul_f32 v2, v2, v145
EX v49,v49
MM v[56:59],a[236:239],v[220:223],v[140:143]
v_mul_f32 v3, v3, v145
v_mul_f32 v4, v4, v145
v_mul_f32 v5, v5, v145
MM v[56:59],a[232:235],v[204:207],v[56:59]
v_mul_f32 v6, v6, v145
v_mul_f32 v7, v7, v145
EX v50,v50
MM v[56:59],a[228:231],v[188:191],v[56:59]
CV v192,v0,v1
CV v193,v2,v3
CV v194,v4,v5
CV v26,v28,v29
MM v[56:59],a[224:227],v[172:175],v[56:59]
s_waitcnt lgkmcnt(0)
s_barrier
CV v195,v6,v7
MM a[16:19],a[128:131],v[80:83],a[16:19]
s_add_u32 m0, s65, 49152
buffer_store_dwordx4 v[192:195], v248, s[20:23], s78 offen offset:2048 sc1
AR v8,a72
EX v51,v51
AR v9,a73
MM a[16:19],a[132:135],v[84:87],a[16:19]
AR v10,a74
AR v11,a75
MM a[24:27],a[132:135],v[92:95],a[24:27]
AR v12,a76
AR v13,a77
AR v14,a78
MM a[24:27],a[128:131],v[88:91],a[24:27]
AR v15,a79
v_mul_f32 v8, v8, v149
EX v52,v52
CV v27,v30,v31
MM a[28:31],a[136:139],v[88:91],a[28:31]
v_mul_f32 v9, v9, v149
v_mul_f32 v10, v10, v149
v_mul_f32 v11, v11, v149
MM a[28:31],a[140:143],v[92:95],a[28:31]
v_mul_f32 v12, v12, v149
v_mul_f32 v13, v13, v149
EX v53,v53
MM a[20:23],a[140:143],v[84:87],a[20:23]
v_mul_f32 v14, v14, v149
v_mul_f32 v15, v15, v149
MM a[20:23],a[136:139],v[80:83],a[20:23]
CV v196,v8,v9
CV v197,v10,v11
CV v198,v12,v13
MM a[48:51],a[144:147],v[80:83],a[48:51]
RD a[192:195],v224 offset:16384
CV v199,v14,v15
EX v54,v54
MM a[48:51],a[148:151],v[84:87],a[48:51]
buffer_store_dwordx4 v[196:199], v248, s[20:23], s79 offen offset:2048 sc1
AR v32,a96
AR v33,a97
CV v20,v48,v49
MM a[56:59],a[148:151],v[92:95],a[56:59]
buffer_load_dwordx4 v228, s[8:11], s64 offen offset:0 lds
RD a[196:199],v225 offset:16384
AR v34,a98
AR v35,a99
EX v55,v55
MM a[56:59],a[144:147],v[88:91],a[56:59]
RD a[200:203],v226 offset:16384
AR v36,a100
AR v37,a101
AR v38,a102
MM a[60:63],a[152:155],v[88:91],a[60:63]
AR v39,a103
v_mul_f32 v32, v32, v145
MM a[60:63],a[156:159],v[92:95],a[60:63]
buffer_load_dwordx4 v229, s[8:11], s64 offen offset:1024 lds
RD a[204:207],v227 offset:16384
v_mul_f32 v33, v33, v145
v_mul_f32 v34, v34, v145
EX v56,v56
MM a[52:55],a[156:159],v[84:87],a[52:55]
v_mul_f32 v35, v35, v145
v_mul_f32 v36, v36, v145
v_mul_f32 v37, v37, v145
CV v21,v50,v51
MM a[52:55],a[152:155],v[80:83],a[52:55]
RD a[208:211],v224 offset:20480
v_mul_f32 v38, v38, v145
v_mul_f32 v39, v39, v145
MM a[80:83],a[160:163],v[80:83],a[80:83]
CV v208,v32,v33
CV v209,v34,v35
EX v57,v57
CV v210,v36,v37
MM a[80:83],a[164:167],v[84:87],a[80:83]
RD a[212:215],v225 offset:20480
buffer_load_dwordx4 v230, s[8:11], s64 offen offset:2048 lds
CV v211,v38,v39
MM a[88:91],a[164:167],v[92:95],a[88:91]
EX v58,v58
MM a[88:91],a[160:163],v[88:91],a[88:91]
buffer_store_dwordx4 v[208:211], v248, s[20:23], s78 offen offset:3072 sc1
RD a[216:219],v226 offset:20480
AR v40,a104
AR v41,a105
AR v42,a106
AR v43,a107
CV v22,v52,v53
MM a[92:95],a[168:171],v[88:91],a[92:95]
RD a[220:223],v227 offset:20480
AR v44,a108
s_nop 0
LPG24_%=:
s_cbranch_execz LPG28_%=
AR v45,a109
AR v46,a110
MM a[92:95],a[172:175],v[92:95],a[92:95]
AR v47,a111
v_mul_f32 v40, v40, v149
EX v59,v59
MM a[84:87],a[172:175],v[84:87],a[84:87]
buffer_load_dwordx4 v231, s[8:11], s64 offen offset:3072 lds
RD a[224:227],v224 offset:24576
v_mul_f32 v41, v41, v149
v_mul_f32 v42, v42, v149
v_mul_f32 v43, v43, v149
MM a[84:87],a[168:171],v[80:83],a[84:87]
s_add_u32 m0, s65, 114688
v_mul_f32 v44, v44, v149
v_mul_f32 v45, v45, v149
MM a[112:115],a[176:179],v[80:83],a[112:115]
RD a[228:231],v225 offset:24576
v_mul_f32 v46, v46, v149
v_mul_f32 v47, v47, v149
EX v60,v60
CV v23,v54,v55
CV v212,v40,v41
MM a[112:115],a[180:183],v[84:87],a[112:115]
buffer_load_dwordx4 v235, s[12:15], s81 offen offset:0 lds
CV v213,v42,v43
CV v214,v44,v45
MM a[120:123],a[180:183],v[92:95],a[120:123]
RD a[232:235],v226 offset:24576
CV v215,v46,v47
EX v61,v61
MM a[120:123],a[176:179],v[88:91],a[120:123]
RD a[236:239],v227 offset:24576
CV v28,v56,v57
MM a[124:127],a[184:187],v[88:91],a[124:127]
buffer_store_dwordx4 v[212:215], v248, s[20:23], s79 offen offset:3072 sc1
MM a[124:127],a[188:191],v[92:95],a[124:127]
EX v62,v62
MM a[116:119],a[188:191],v[84:87],a[116:119]
RD a[240:243],v224 offset:28672
RD a[244:247],v225 offset:28672
RD a[248:251],v226 offset:28672
RD a[252:255],v227 offset:28672
EX v63,v63
MM a[116:119],a[184:187],v[80:83],a[116:119]
buffer_load_dwordx4 v235, s[12:15], s64 offen offset:1024 lds
v_smfmac_f32_16x16x64_bf16 v[152:155], v[236:239], v[16:23], v234
CV v29,v58,v59
CV v30,v60,v61
CV v31,v62,v63
s_nop 1
v_smfmac_f32_16x16x64_bf16 v[156:159], v[236:239], v[24:31], v234
s_waitcnt lgkmcnt(0)
buffer_load_dwordx4 v235, s[12:15], s81 offen offset:2048 lds
buffer_load_dwordx4 v235, s[12:15], s64 offen offset:3072 lds
MM v[80:83],a[192:195],v[168:171],v[136:139]
TR a[128:129],v232 offset:0
TR a[130:131],v232 offset:4096
)ASM"
R"ASM(
MM v[80:83],a[196:199],v[184:187],v[80:83]
TR a[132:133],v232 offset:8192
TR a[134:135],v232 offset:12288
MM v[80:83],a[200:203],v[200:203],v[80:83]
TR a[136:137],v233 offset:0
TR a[138:139],v233 offset:4096
MM v[80:83],a[204:207],v[216:219],v[80:83]
v_max3_f32 v249, v152, v153, v156
v_max_f32 v249, v249, v157
MM v[84:87],a[220:223],v[216:219],v[136:139]
TR a[140:141],v233 offset:8192
v_cmp_lt_f32 vcc, s71, v249
MM v[84:87],a[216:219],v[200:203],v[84:87]
s_cbranch_vccnz LSITE36_%=
LBACK36_%=:
TR a[142:143],v233 offset:12288
v_add_f32 v246, v152, v153
MM v[84:87],a[212:215],v[184:187],v[84:87]
TR a[144:145],v232 offset:512
v_add_f32 v247, v156, v157
MM v[84:87],a[208:211],v[168:171],v[84:87]
TR a[146:147],v232 offset:4608
TR a[148:149],v232 offset:8704
MM v[92:95],a[208:211],v[172:175],v[140:143]
TR a[150:151],v232 offset:12800
TR a[152:153],v233 offset:512
MM v[92:95],a[212:215],v[188:191],v[92:95]
EX v80,v80
MM v[92:95],a[216:219],v[204:207],v[92:95]
EX v81,v81
MM v[92:95],a[220:223],v[220:223],v[92:95]
TR a[154:155],v233 offset:4608
TR a[156:157],v233 offset:8704
MM v[88:91],a[204:207],v[220:223],v[140:143]
TR a[158:159],v233 offset:12800
CV v80,v80,v81
MM v[88:91],a[200:203],v[204:207],v[88:91]
EX v82,v82
MM v[88:91],a[196:199],v[188:191],v[88:91]
TR a[160:161],v232 offset:1024
TR a[162:163],v232 offset:5120
MM v[88:91],a[192:195],v[172:175],v[88:91]
EX v83,v83
MM v[112:115],a[224:227],v[168:171],v[136:139]
TR a[164:165],v232 offset:9216
TR a[166:167],v232 offset:13312
MM v[112:115],a[228:231],v[184:187],v[112:115]
EX v84,v84
MM v[112:115],a[232:235],v[200:203],v[112:115]
TR a[168:169],v233 offset:1024
TR a[170:171],v233 offset:5120
MM v[112:115],a[236:239],v[216:219],v[112:115]
EX v85,v85
MM v[116:119],a[252:255],v[216:219],v[136:139]
TR a[172:173],v233 offset:9216
TR a[174:175],v233 offset:13312
MM v[116:119],a[248:251],v[200:203],v[116:119]
EX v86,v86
MM v[116:119],a[244:247],v[184:187],v[116:119]
TR a[176:177],v232 offset:1536
TR a[178:179],v232 offset:5632
MM v[116:119],a[240:243],v[168:171],v[116:119]
EX v87,v87
MM v[124:127],a[240:243],v[172:175],v[140:143]
TR a[180:181],v232 offset:9728
TR a[182:183],v232 offset:13824
MM v[124:127],a[244:247],v[188:191],v[124:127]
EX v88,v88
MM v[124:127],a[248:251],v[204:207],v[124:127]
TR a[184:185],v233 offset:1536
TR a[186:187],v233 offset:5632
TR a[188:189],v233 offset:9728
TR a[190:191],v233 offset:13824
EX v89,v89
MM v[124:127],a[252:255],v[220:223],v[124:127]
CV v81,v82,v83
CV v82,v84,v85
MM v[120:123],a[236:239],v[220:223],v[140:143]
EX v90,v90
MM v[120:123],a[232:235],v[204:207],v[120:123]
EX v91,v91
MM v[120:123],a[228:231],v[188:191],v[120:123]
CV v83,v86,v87
CV v88,v88,v89
MM v[120:123],a[224:227],v[172:175],v[120:123]
s_waitcnt vmcnt(21) lgkmcnt(0)
s_barrier
CV v89,v90,v91
EX v92,v92
MM a[16:19],a[128:131],v[16:19],a[16:19]
s_xor_b32 s66, s94, 4
EX v93,v93
MM a[16:19],a[132:135],v[20:23],a[16:19]
s_mul_i32 s66, s66, 163840
EX v94,v94
MM a[24:27],a[132:135],v[28:31],a[24:27]
s_add_u32 s66, s66, s87
EX v95,v95
MM a[24:27],a[128:131],v[24:27],a[24:27]
s_add_u32 m0, s65, 65536
RD a[192:195],v224 offset:32768
CV v90,v92,v93
MM a[28:31],a[136:139],v[24:27],a[28:31]
EX v112,v112
MM a[28:31],a[140:143],v[28:31],a[28:31]
buffer_load_dwordx4 v240, s[24:27], s66 offen offset:0 lds
MM a[20:23],a[140:143],v[20:23],a[20:23]
s_add_u32 s66, s66, 9216
EX v113,v113
MM a[20:23],a[136:139],v[16:19],a[20:23]
RD a[196:199],v225 offset:32768
CV v91,v94,v95
MM a[48:51],a[144:147],v[16:19],a[48:51]
EX v114,v114
MM a[48:51],a[148:151],v[20:23],a[48:51]
RD a[200:203],v226 offset:32768
RD a[204:207],v227 offset:32768
MM a[56:59],a[148:151],v[28:31],a[56:59]
EX v115,v115
MM a[56:59],a[144:147],v[24:27],a[56:59]
buffer_load_dwordx4 v240, s[24:27], s66 offen offset:1024 lds
MM a[60:63],a[152:155],v[24:27],a[60:63]
s_add_u32 s66, s66, 9216
EX v116,v116
MM a[60:63],a[156:159],v[28:31],a[60:63]
RD a[208:211],v224 offset:36864
CV v84,v112,v113
MM a[52:55],a[156:159],v[20:23],a[52:55]
EX v117,v117
MM a[52:55],a[152:155],v[16:19],a[52:55]
buffer_load_dwordx4 v240, s[24:27], s66 offen offset:2048 lds
MM a[80:83],a[160:163],v[16:19],a[80:83]
s_add_u32 s66, s66, 9216
EX v118,v118
MM a[80:83],a[164:167],v[20:23],a[80:83]
RD a[212:215],v225 offset:36864
RD a[216:219],v226 offset:36864
MM a[88:91],a[164:167],v[28:31],a[88:91]
s_lshl_b32 s97, s84, 2
s_add_u32 s97, s97, s96
s_add_u32 s97, s97, 0
EX v119,v119
MM a[88:91],a[160:163],v[24:27],a[88:91]
s_waitcnt vmcnt(6)
v_mov_b32 v242, s69
s_mov_b64 exec, 1
buffer_store_dword v242, off, s[20:23], s97 offset:0 sc1
s_mov_b64 exec, -1
buffer_load_dwordx4 v240, s[24:27], s66 offen offset:3072 lds
EX v120,v120
MM a[92:95],a[168:171],v[24:27],a[92:95]
s_xor_b32 s66, s94, 4
RD a[220:223],v227 offset:36864
CV v85,v114,v115
MM a[92:95],a[172:175],v[28:31],a[92:95]
s_mul_i32 s66, s66, 163840
EX v121,v121
MM a[84:87],a[172:175],v[20:23],a[84:87]
s_add_u32 s66, s66, s86
RD a[224:227],v224 offset:40960
CV v86,v116,v117
MM a[84:87],a[168:171],v[16:19],a[84:87]
s_add_u32 m0, s65, 0
EX v122,v122
MM a[112:115],a[176:179],v[16:19],a[112:115]
buffer_load_dwordx4 v240, s[24:27], s66 offen offset:0 lds
RD a[228:231],v225 offset:40960
EX v123,v123
MM a[112:115],a[180:183],v[20:23],a[112:115]
s_add_u32 s66, s66, 9216
RD a[232:235],v226 offset:40960
CV v87,v118,v119
MM a[120:123],a[180:183],v[28:31],a[120:123]
EX v124,v124
MM a[120:123],a[176:179],v[24:27],a[120:123]
buffer_load_dwordx4 v240, s[24:27], s66 offen offset:1024 lds
MM a[124:127],a[184:187],v[24:27],a[124:127]
s_add_u32 s66, s66, 9216
RD a[236:239],v227 offset:40960
EX v125,v125
MM a[124:127],a[188:191],v[28:31],a[124:127]
EX v126,v126
MM a[116:119],a[188:191],v[20:23],a[116:119]
RD a[240:243],v224 offset:45056
RD a[244:247],v225 offset:45056
RD a[248:251],v226 offset:45056
RD a[252:255],v227 offset:45056
EX v127,v127
MM a[116:119],a[184:187],v[16:19],a[116:119]
buffer_load_dwordx4 v240, s[24:27], s66 offen offset:2048 lds
CV v92,v120,v121
v_smfmac_f32_16x16x64_bf16 v[152:155], v[236:239], v[80:87], v234
s_add_u32 s66, s66, 9216
CV v93,v122,v123
CV v94,v124,v125
CV v95,v126,v127
s_nop 1
v_smfmac_f32_16x16x64_bf16 v[156:159], v[236:239], v[88:95], v234
s_waitcnt lgkmcnt(0)
buffer_load_dwordx4 v240, s[24:27], s66 offen offset:3072 lds
v_lshrrev_b32 v145, 9, v248
v_mul_u32_u24 v145, 1536, v145
v_sub_u32 v244, v232, v145
v_sub_u32 v235, v233, v145
v_add_u32 v244, 65536, v244
v_add_u32 v235, 65536, v235
v_and_b32 v145, 960, v248
v_lshlrev_b32 v145, 1, v145
v_bfe_u32 v146, v248, 7, 2
v_lshl_add_u32 v145, v146, 5, v145
v_bfe_u32 v146, v248, 4, 2
v_lshl_add_u32 v145, v146, 3, v145
v_add_u32 v228, %[slds], v145
v_xor_b32 v229, 32, v228
v_xor_b32 v230, 64, v228
v_xor_b32 v231, 96, v228
v_add_u32 v144, 65536, v228
v_add_u32 v145, 65536, v229
v_add_u32 v146, 65536, v230
v_add_u32 v147, 65536, v231
MM v[16:19],a[192:195],v[168:171],v[136:139]
TR a[128:129],v232 offset:16384
TR a[130:131],v232 offset:20480
MM v[16:19],a[196:199],v[184:187],v[16:19]
TR a[132:133],v232 offset:24576
TR a[134:135],v232 offset:28672
MM v[16:19],a[200:203],v[200:203],v[16:19]
TR a[136:137],v233 offset:16384
TR a[138:139],v233 offset:20480
MM v[16:19],a[204:207],v[216:219],v[16:19]
v_max3_f32 v249, v152, v153, v156
v_max_f32 v249, v249, v157
MM v[20:23],a[220:223],v[216:219],v[136:139]
TR a[140:141],v233 offset:24576
v_cmp_lt_f32 vcc, s71, v249
MM v[20:23],a[216:219],v[200:203],v[20:23]
s_cbranch_vccnz LSITE37_%=
LBACK37_%=:
TR a[142:143],v233 offset:28672
v_add_f32 v246, v152, v153
MM v[20:23],a[212:215],v[184:187],v[20:23]
TR a[144:145],v232 offset:16896
v_add_f32 v247, v156, v157
MM v[20:23],a[208:211],v[168:171],v[20:23]
TR a[146:147],v232 offset:20992
TR a[148:149],v232 offset:25088
MM v[28:31],a[208:211],v[172:175],v[140:143]
TR a[150:151],v232 offset:29184
TR a[152:153],v233 offset:16896
MM v[28:31],a[212:215],v[188:191],v[28:31]
EX v16,v16
MM v[28:31],a[216:219],v[204:207],v[28:31]
v_lshrrev_b32 v98, 1, v248
v_min_u32 v98, 312, v98
MM v[28:31],a[220:223],v[220:223],v[28:31]
s_mov_b32 s62, 94464
buffer_load_dwordx2 v[96:97], v98, s[20:23], s62 offen offset:0 sc1
MM v[24:27],a[204:207],v[220:223],v[140:143]
EX v17,v17
MM v[24:27],a[200:203],v[204:207],v[24:27]
TR a[154:155],v233 offset:20992
TR a[156:157],v233 offset:25088
MM v[24:27],a[196:199],v[188:191],v[24:27]
TR a[158:159],v233 offset:29184
TR a[160:161],v232 offset:17408
MM v[24:27],a[192:195],v[172:175],v[24:27]
EX v18,v18
MM v[48:51],a[224:227],v[168:171],v[136:139]
EX v19,v19
MM v[48:51],a[228:231],v[184:187],v[48:51]
TR a[162:163],v232 offset:21504
TR a[164:165],v232 offset:25600
MM v[48:51],a[232:235],v[200:203],v[48:51]
TR a[166:167],v232 offset:29696
TR a[168:169],v233 offset:17408
EX v20,v20
MM v[48:51],a[236:239],v[216:219],v[48:51]
TR a[170:171],v233 offset:21504
TR a[172:173],v233 offset:25600
MM v[52:55],a[252:255],v[216:219],v[136:139]
EX v21,v21
MM v[52:55],a[248:251],v[200:203],v[52:55]
TR a[174:175],v233 offset:29696
CV v16,v16,v17
MM v[52:55],a[244:247],v[184:187],v[52:55]
TR a[176:177],v232 offset:17920
EX v22,v22
MM v[52:55],a[240:243],v[168:171],v[52:55]
TR a[178:179],v232 offset:22016
TR a[180:181],v232 offset:26112
EX v23,v23
MM v[60:63],a[240:243],v[172:175],v[140:143]
TR a[182:183],v232 offset:30208
CV v17,v18,v19
MM v[60:63],a[244:247],v[188:191],v[60:63]
EX v24,v24
MM v[60:63],a[248:251],v[204:207],v[60:63]
TR a[184:185],v233 offset:17920
TR a[186:187],v233 offset:22016
TR a[188:189],v233 offset:26112
TR a[190:191],v233 offset:30208
EX v25,v25
MM v[60:63],a[252:255],v[220:223],v[60:63]
EX v26,v26
MM v[56:59],a[236:239],v[220:223],v[140:143]
EX v27,v27
MM v[56:59],a[232:235],v[204:207],v[56:59]
EX v28,v28
MM v[56:59],a[228:231],v[188:191],v[56:59]
EX v29,v29
CV v18,v20,v21
MM v[56:59],a[224:227],v[172:175],v[56:59]
s_waitcnt lgkmcnt(0)
s_barrier
CV v19,v22,v23
MM a[16:19],a[128:131],v[80:83],a[16:19]
s_xor_b32 s66, s94, 5
CV v24,v24,v25
CV v25,v26,v27
MM a[16:19],a[132:135],v[84:87],a[16:19]
s_mul_i32 s66, s66, 163840
EX v30,v30
MM a[24:27],a[132:135],v[92:95],a[24:27]
s_add_u32 s66, s66, s87
v_lshrrev_b32 v243, 2, v248
v_min_u32 v243, 12, v243
MM a[24:27],a[128:131],v[88:91],a[24:27]
s_add_u32 m0, s65, 81920
buffer_load_dword v242, v243, s[20:23], s44 offen offset:0 sc1
MM a[28:31],a[136:139],v[88:91],a[28:31]
EX v31,v31
MM a[28:31],a[140:143],v[92:95],a[28:31]
EX v48,v48
MM a[20:23],a[140:143],v[84:87],a[20:23]
RD a[192:195],v224 offset:49152
CV v26,v28,v29
MM a[20:23],a[136:139],v[80:83],a[20:23]
EX v49,v49
MM a[48:51],a[144:147],v[80:83],a[48:51]
buffer_load_dwordx4 v240, s[24:27], s66 offen offset:0 lds
MM a[48:51],a[148:151],v[84:87],a[48:51]
s_add_u32 s66, s66, 9216
EX v50,v50
MM a[56:59],a[148:151],v[92:95],a[56:59]
RD a[196:199],v225 offset:49152
RD a[200:203],v226 offset:49152
MM a[56:59],a[144:147],v[88:91],a[56:59]
EX v51,v51
MM a[60:63],a[152:155],v[88:91],a[60:63]
buffer_load_dwordx4 v240, s[24:27], s66 offen offset:1024 lds
MM a[60:63],a[156:159],v[92:95],a[60:63]
s_add_u32 s66, s66, 9216
RD a[204:207],v227 offset:49152
EX v52,v52
MM a[52:55],a[156:159],v[84:87],a[52:55]
EX v53,v53
CV v27,v30,v31
MM a[52:55],a[152:155],v[80:83],a[52:55]
RD a[208:211],v224 offset:53248
RD a[212:215],v225 offset:53248
buffer_load_dwordx4 v240, s[24:27], s66 offen offset:2048 lds
MM a[80:83],a[160:163],v[80:83],a[80:83]
s_add_u32 s66, s66, 9216
EX v54,v54
MM a[80:83],a[164:167],v[84:87],a[80:83]
RD a[216:219],v226 offset:53248
CV v20,v48,v49
MM a[88:91],a[164:167],v[92:95],a[88:91]
EX v55,v55
MM a[88:91],a[160:163],v[88:91],a[88:91]
buffer_load_dwordx4 v240, s[24:27], s66 offen offset:3072 lds
EX v56,v56
MM a[92:95],a[168:171],v[88:91],a[92:95]
s_xor_b32 s66, s94, 5
RD a[220:223],v227 offset:53248
CV v21,v50,v51
MM a[92:95],a[172:175],v[92:95],a[92:95]
s_mul_i32 s66, s66, 163840
EX v57,v57
MM a[84:87],a[172:175],v[84:87],a[84:87]
s_add_u32 s66, s66, s86
RD a[224:227],v224 offset:57344
CV v22,v52,v53
MM a[84:87],a[168:171],v[80:83],a[84:87]
s_add_u32 m0, s65, 16384
EX v58,v58
MM a[112:115],a[176:179],v[80:83],a[112:115]
buffer_load_dwordx4 v240, s[24:27], s66 offen offset:0 lds
)ASM"
R"ASM(
RD a[228:231],v225 offset:57344
EX v59,v59
MM a[112:115],a[180:183],v[84:87],a[112:115]
s_add_u32 s66, s66, 9216
RD a[232:235],v226 offset:57344
CV v23,v54,v55
MM a[120:123],a[180:183],v[92:95],a[120:123]
EX v60,v60
MM a[120:123],a[176:179],v[88:91],a[120:123]
buffer_load_dwordx4 v240, s[24:27], s66 offen offset:1024 lds
MM a[124:127],a[184:187],v[88:91],a[124:127]
s_add_u32 s66, s66, 9216
RD a[236:239],v227 offset:57344
EX v61,v61
MM a[124:127],a[188:191],v[92:95],a[124:127]
EX v62,v62
MM a[116:119],a[188:191],v[84:87],a[116:119]
RD a[240:243],v224 offset:61440
RD a[244:247],v225 offset:61440
RD a[248:251],v226 offset:61440
RD a[252:255],v227 offset:61440
EX v63,v63
MM a[116:119],a[184:187],v[80:83],a[116:119]
buffer_load_dwordx4 v240, s[24:27], s66 offen offset:2048 lds
CV v28,v56,v57
v_smfmac_f32_16x16x64_bf16 v[152:155], v[236:239], v[16:23], v234
s_add_u32 s66, s66, 9216
CV v29,v58,v59
CV v30,v60,v61
CV v31,v62,v63
s_nop 1
v_smfmac_f32_16x16x64_bf16 v[156:159], v[236:239], v[24:31], v234
s_waitcnt lgkmcnt(0)
buffer_load_dwordx4 v240, s[24:27], s66 offen offset:3072 lds
MM v[80:83],a[192:195],v[168:171],v[136:139]
TR a[128:129],v232 offset:32768
TR a[130:131],v232 offset:36864
MM v[80:83],a[196:199],v[184:187],v[80:83]
TR a[132:133],v232 offset:40960
TR a[134:135],v232 offset:45056
MM v[80:83],a[200:203],v[200:203],v[80:83]
TR a[136:137],v233 offset:32768
TR a[138:139],v233 offset:36864
MM v[80:83],a[204:207],v[216:219],v[80:83]
v_max3_f32 v249, v152, v153, v156
v_max_f32 v249, v249, v157
MM v[84:87],a[220:223],v[216:219],v[136:139]
TR a[140:141],v233 offset:40960
v_cmp_lt_f32 vcc, s71, v249
MM v[84:87],a[216:219],v[200:203],v[84:87]
s_cbranch_vccnz LSITE38_%=
LBACK38_%=:
TR a[142:143],v233 offset:45056
v_add_f32 v246, v152, v153
MM v[84:87],a[212:215],v[184:187],v[84:87]
TR a[144:145],v232 offset:33280
v_add_f32 v247, v156, v157
MM v[84:87],a[208:211],v[168:171],v[84:87]
TR a[146:147],v232 offset:37376
TR a[148:149],v232 offset:41472
MM v[92:95],a[208:211],v[172:175],v[140:143]
s_waitcnt vmcnt(8)
v_cmp_ne_u32 vcc, s69, v242
s_cbranch_vccnz LRPO0_%=
LRPO0b_%=:
MM v[92:95],a[212:215],v[188:191],v[92:95]
s_xor_b32 s92, s49, 4
s_mul_i32 s92, s92, 17408
s_add_u32 s92, s83, s92
MM v[92:95],a[216:219],v[204:207],v[92:95]
buffer_load_dwordx4 a[0:3], v248, s[20:23], s92 offen offset:0 sc1
MM v[92:95],a[220:223],v[220:223],v[92:95]
buffer_load_dwordx4 a[32:35], v248, s[20:23], s92 offen offset:1024 sc1
MM v[88:91],a[204:207],v[220:223],v[140:143]
TR a[150:151],v232 offset:45568
v_xor_b32 v96, s69, v96
v_xor_b32 v97, s69, v97
EX v80,v80
MM v[88:91],a[200:203],v[204:207],v[88:91]
TR a[152:153],v233 offset:33280
TR a[154:155],v233 offset:37376
v_or_b32 v99, v96, v97
EX v81,v81
MM v[88:91],a[196:199],v[188:191],v[88:91]
buffer_load_dwordx4 a[64:67], v248, s[20:23], s92 offen offset:2048 sc1
TR a[156:157],v233 offset:41472
MM v[88:91],a[192:195],v[172:175],v[88:91]
TR a[158:159],v233 offset:45568
v_cmp_ne_u32 vcc, 0, v99
s_cbranch_vccnz LRPW_%=
LRPWb_%=:
EX v82,v82
MM v[112:115],a[224:227],v[168:171],v[136:139]
TR a[160:161],v232 offset:33792
TR a[162:163],v232 offset:37888
EX v83,v83
MM v[112:115],a[228:231],v[184:187],v[112:115]
buffer_load_dwordx4 a[96:99], v248, s[20:23], s92 offen offset:3072 sc1
TR a[164:165],v232 offset:41984
MM v[112:115],a[232:235],v[200:203],v[112:115]
s_add_u32 s92, s92, 4096
TR a[166:167],v232 offset:46080
TR a[168:169],v233 offset:33792
EX v84,v84
MM v[112:115],a[236:239],v[216:219],v[112:115]
TR a[170:171],v233 offset:37888
TR a[172:173],v233 offset:41984
MM v[116:119],a[252:255],v[216:219],v[136:139]
buffer_load_dwordx4 a[4:7], v248, s[20:23], s92 offen offset:0 sc1
EX v85,v85
MM v[116:119],a[248:251],v[200:203],v[116:119]
TR a[174:175],v233 offset:46080
CV v80,v80,v81
MM v[116:119],a[244:247],v[184:187],v[116:119]
TR a[176:177],v232 offset:34304
EX v86,v86
MM v[116:119],a[240:243],v[168:171],v[116:119]
buffer_load_dwordx4 a[36:39], v248, s[20:23], s92 offen offset:1024 sc1
TR a[178:179],v232 offset:38400
EX v87,v87
MM v[124:127],a[240:243],v[172:175],v[140:143]
TR a[180:181],v232 offset:42496
TR a[182:183],v232 offset:46592
MM v[124:127],a[244:247],v[188:191],v[124:127]
EX v88,v88
MM v[124:127],a[248:251],v[204:207],v[124:127]
buffer_load_dwordx4 a[68:71], v248, s[20:23], s92 offen offset:2048 sc1
TR a[184:185],v233 offset:34304
TR a[186:187],v233 offset:38400
TR a[188:189],v233 offset:42496
TR a[190:191],v233 offset:46592
EX v89,v89
MM v[124:127],a[252:255],v[220:223],v[124:127]
EX v90,v90
CV v81,v82,v83
MM v[120:123],a[236:239],v[220:223],v[140:143]
EX v91,v91
MM v[120:123],a[232:235],v[204:207],v[120:123]
buffer_load_dwordx4 a[100:103], v248, s[20:23], s92 offen offset:3072 sc1
MM v[120:123],a[228:231],v[188:191],v[120:123]
EX v92,v92
CV v82,v84,v85
MM v[120:123],a[224:227],v[172:175],v[120:123]
s_waitcnt lgkmcnt(0)
s_barrier
CV v83,v86,v87
MM a[16:19],a[128:131],v[16:19],a[16:19]
LPG25_%=:
s_cbranch_execz LPG29_%=
s_xor_b32 s66, s94, 6
EX v93,v93
MM a[16:19],a[132:135],v[20:23],a[16:19]
s_mul_i32 s66, s66, 163840
CV v88,v88,v89
CV v89,v90,v91
MM a[24:27],a[132:135],v[28:31],a[24:27]
s_add_u32 s66, s66, s87
EX v94,v94
MM a[24:27],a[128:131],v[24:27],a[24:27]
s_add_u32 m0, s65, 98304
EX v95,v95
MM a[28:31],a[136:139],v[24:27],a[28:31]
EX v112,v112
MM a[28:31],a[140:143],v[28:31],a[28:31]
CV v90,v92,v93
CV v91,v94,v95
MM a[20:23],a[140:143],v[20:23],a[20:23]
buffer_load_dwordx4 v240, s[24:27], s66 offen offset:0 lds
MM a[20:23],a[136:139],v[16:19],a[20:23]
s_add_u32 s66, s66, 9216
EX v113,v113
MM a[48:51],a[144:147],v[16:19],a[48:51]
EX v114,v114
MM a[48:51],a[148:151],v[20:23],a[48:51]
buffer_load_dwordx4 v240, s[24:27], s66 offen offset:1024 lds
MM a[56:59],a[148:151],v[28:31],a[56:59]
s_add_u32 s66, s66, 9216
EX v115,v115
MM a[56:59],a[144:147],v[24:27],a[56:59]
TR a[128:129],v232 offset:49152
CV v84,v112,v113
MM a[60:63],a[152:155],v[24:27],a[60:63]
EX v116,v116
MM a[60:63],a[156:159],v[28:31],a[60:63]
TR a[130:131],v232 offset:53248
TR a[132:133],v232 offset:57344
MM a[52:55],a[156:159],v[20:23],a[52:55]
EX v117,v117
MM a[52:55],a[152:155],v[16:19],a[52:55]
TR a[134:135],v232 offset:61440
TR a[136:137],v233 offset:49152
buffer_load_dwordx4 v240, s[24:27], s66 offen offset:2048 lds
MM a[80:83],a[160:163],v[16:19],a[80:83]
s_add_u32 s66, s66, 9216
TR a[138:139],v233 offset:53248
TR a[140:141],v233 offset:57344
EX v118,v118
MM a[80:83],a[164:167],v[20:23],a[80:83]
TR a[142:143],v233 offset:61440
TR a[144:145],v232 offset:49664
MM a[88:91],a[164:167],v[28:31],a[88:91]
s_xor_b32 s92, s49, 5
s_mul_i32 s92, s92, 17408
s_add_u32 s92, s83, s92
EX v119,v119
MM a[88:91],a[160:163],v[24:27],a[88:91]
buffer_load_dwordx4 a[8:11], v248, s[20:23], s92 offen offset:0 sc1
TR a[146:147],v232 offset:53760
EX v120,v120
MM a[92:95],a[168:171],v[24:27],a[92:95]
TR a[148:149],v232 offset:57856
TR a[150:151],v232 offset:61952
buffer_load_dwordx4 v240, s[24:27], s66 offen offset:3072 lds
MM a[92:95],a[172:175],v[28:31],a[92:95]
s_xor_b32 s66, s94, 6
TR a[152:153],v233 offset:49664
EX v121,v121
CV v85,v114,v115
MM a[84:87],a[172:175],v[20:23],a[84:87]
s_mul_i32 s66, s66, 163840
TR a[154:155],v233 offset:53760
TR a[156:157],v233 offset:57856
MM a[84:87],a[168:171],v[16:19],a[84:87]
s_add_u32 s66, s66, s86
buffer_load_dwordx4 a[40:43], v248, s[20:23], s92 offen offset:1024 sc1
TR a[158:159],v233 offset:61952
s_add_u32 m0, s65, 32768
EX v122,v122
MM a[112:115],a[176:179],v[16:19],a[112:115]
TR a[160:161],v232 offset:50176
CV v86,v116,v117
EX v123,v123
MM a[112:115],a[180:183],v[20:23],a[112:115]
TR a[162:163],v232 offset:54272
TR a[164:165],v232 offset:58368
MM a[120:123],a[180:183],v[28:31],a[120:123]
buffer_load_dwordx4 a[72:75], v248, s[20:23], s92 offen offset:2048 sc1
TR a[166:167],v232 offset:62464
EX v124,v124
MM a[120:123],a[176:179],v[24:27],a[120:123]
TR a[168:169],v233 offset:50176
TR a[170:171],v233 offset:54272
CV v87,v118,v119
MM a[124:127],a[184:187],v[24:27],a[124:127]
TR a[172:173],v233 offset:58368
EX v125,v125
MM a[124:127],a[188:191],v[28:31],a[124:127]
buffer_load_dwordx4 a[104:107], v248, s[20:23], s92 offen offset:3072 sc1
TR a[174:175],v233 offset:62464
EX v126,v126
MM a[116:119],a[188:191],v[20:23],a[116:119]
TR a[176:177],v232 offset:50688
TR a[178:179],v232 offset:54784
EX v127,v127
MM a[116:119],a[184:187],v[16:19],a[116:119]
s_add_u32 s92, s92, 4096
TR a[180:181],v232 offset:58880
CV v92,v120,v121
v_smfmac_f32_16x16x64_bf16 v[152:155], v[236:239], v[80:87], v234
buffer_load_dwordx4 v240, s[24:27], s66 offen offset:0 lds
s_add_u32 s66, s66, 9216
TR a[182:183],v232 offset:62976
CV v93,v122,v123
CV v94,v124,v125
CV v95,v126,v127
s_nop 1
v_smfmac_f32_16x16x64_bf16 v[156:159], v[236:239], v[88:95], v234
TR a[184:185],v233 offset:50688
TR a[186:187],v233 offset:54784
TR a[188:189],v233 offset:58880
TR a[190:191],v233 offset:62976
s_waitcnt lgkmcnt(0)
buffer_load_dwordx4 v240, s[24:27], s66 offen offset:1024 lds
s_add_u32 s66, s66, 9216
buffer_load_dwordx4 v240, s[24:27], s66 offen offset:2048 lds
s_add_u32 s66, s66, 9216
buffer_load_dwordx4 v240, s[24:27], s66 offen offset:3072 lds
s_nop 6
v_max3_f32 v249, v152, v153, v156
v_max_f32 v249, v249, v157
v_cmp_lt_f32 vcc, s71, v249
s_cbranch_vccnz LSITE39_%=
LBACK39_%=:
v_add_f32 v246, v152, v153
v_add_f32 v247, v156, v157
s_waitcnt lgkmcnt(0)
MM a[16:19],a[128:131],v[80:83],a[16:19]
s_barrier
MM a[16:19],a[132:135],v[84:87],a[16:19]
buffer_load_dwordx4 a[12:15], v248, s[20:23], s92 offen offset:0 sc1
MM a[24:27],a[132:135],v[92:95],a[24:27]
v_rcp_f32 v153, v246
MM a[24:27],a[128:131],v[88:91],a[24:27]
v_rcp_f32 v157, v247
MM a[28:31],a[136:139],v[88:91],a[28:31]
s_mul_i32 s78, s49, 17408
s_add_u32 s78, s83, s78
s_add_u32 s78, s78, 8192
MM a[28:31],a[140:143],v[92:95],a[28:31]
s_add_u32 s79, s78, 4096
buffer_load_dwordx4 a[44:47], v248, s[20:23], s92 offen offset:1024 sc1
MM a[20:23],a[140:143],v[84:87],a[20:23]
MM a[20:23],a[136:139],v[80:83],a[20:23]
MM a[48:51],a[144:147],v[80:83],a[48:51]
TR a[128:129],v244 offset:0
TR a[130:131],v244 offset:1024
MM a[48:51],a[148:151],v[84:87],a[48:51]
buffer_load_dwordx4 a[76:79], v248, s[20:23], s92 offen offset:2048 sc1
MM a[56:59],a[148:151],v[92:95],a[56:59]
TR a[132:133],v244 offset:2048
TR a[134:135],v244 offset:3072
MM a[56:59],a[144:147],v[88:91],a[56:59]
AR v16,a16
AR v17,a17
MM a[60:63],a[152:155],v[88:91],a[60:63]
TR a[136:137],v244 offset:4096
TR a[138:139],v244 offset:5120
MM a[60:63],a[156:159],v[92:95],a[60:63]
buffer_load_dwordx4 a[108:111], v248, s[20:23], s92 offen offset:3072 sc1
MM a[52:55],a[156:159],v[84:87],a[52:55]
TR a[140:141],v244 offset:6144
TR a[142:143],v244 offset:7168
AR v18,a18
MM a[52:55],a[152:155],v[80:83],a[52:55]
AR v19,a19
AR v20,a20
MM a[80:83],a[160:163],v[80:83],a[80:83]
TR a[144:145],v235 offset:0
TR a[146:147],v235 offset:1024
AR v21,a21
MM a[80:83],a[164:167],v[84:87],a[80:83]
AR v22,a22
AR v23,a23
MM a[88:91],a[164:167],v[92:95],a[88:91]
TR a[148:149],v235 offset:2048
TR a[150:151],v235 offset:3072
v_mul_f32 v16, v16, v153
MM a[88:91],a[160:163],v[88:91],a[88:91]
v_mul_f32 v17, v17, v153
v_mul_f32 v18, v18, v153
MM a[92:95],a[168:171],v[88:91],a[92:95]
TR a[152:153],v235 offset:4096
TR a[154:155],v235 offset:5120
v_mul_f32 v19, v19, v153
v_mul_f32 v20, v20, v153
MM a[92:95],a[172:175],v[92:95],a[92:95]
s_xor_b32 s66, s94, 7
v_mul_f32 v21, v21, v153
v_mul_f32 v22, v22, v153
MM a[84:87],a[172:175],v[84:87],a[84:87]
s_mul_i32 s66, s66, 163840
TR a[156:157],v235 offset:6144
TR a[158:159],v235 offset:7168
v_mul_f32 v23, v23, v153
MM a[84:87],a[168:171],v[80:83],a[84:87]
s_add_u32 s66, s66, s87
CV v168,v16,v17
CV v169,v18,v19
MM a[112:115],a[176:179],v[80:83],a[112:115]
s_add_u32 m0, s65, 114688
TR a[160:161],v144 offset:0
TR a[162:163],v144 offset:2048
CV v170,v20,v21
MM a[112:115],a[180:183],v[84:87],a[112:115]
buffer_load_dwordx4 v240, s[24:27], s66 offen offset:0 lds
CV v171,v22,v23
MM a[120:123],a[180:183],v[92:95],a[120:123]
s_add_u32 s66, s66, 9216
TR a[164:165],v144 offset:4096
TR a[166:167],v144 offset:6144
MM a[120:123],a[176:179],v[88:91],a[120:123]
)ASM"
R"ASM(
buffer_store_dwordx4 v[168:171], v248, s[20:23], s78 offen offset:0 sc1
AR v24,a24
AR v25,a25
MM a[124:127],a[184:187],v[88:91],a[124:127]
TR a[168:169],v144 offset:8192
TR a[170:171],v144 offset:10240
AR v26,a26
AR v27,a27
MM a[124:127],a[188:191],v[92:95],a[124:127]
AR v28,a28
AR v29,a29
MM a[116:119],a[188:191],v[84:87],a[116:119]
TR a[172:173],v144 offset:12288
TR a[174:175],v144 offset:14336
AR v30,a30
MM a[116:119],a[184:187],v[80:83],a[116:119]
buffer_load_dwordx4 v240, s[24:27], s66 offen offset:1024 lds
AR v31,a31
v_mul_f32 v24, v24, v157
s_waitcnt vmcnt(23)
s_waitcnt lgkmcnt(15)
MM v[128:131],a[128:131],a[0:3],0
TR a[176:177],v145 offset:0
TR a[178:179],v145 offset:2048
v_mul_f32 v25, v25, v157
MM v[128:131],a[132:135],a[32:35],v[128:131]
s_add_u32 s66, s66, 9216
v_bfe_u32 v249, v248, 6, 2
v_lshlrev_b32 v249, 8, v249
v_mul_f32 v26, v26, v157
v_mul_f32 v27, v27, v157
MM v[128:131],a[136:139],a[64:67],v[128:131]
TR a[180:181],v145 offset:4096
TR a[182:183],v145 offset:6144
v_mul_f32 v28, v28, v157
MM v[128:131],a[140:143],a[96:99],v[128:131]
s_waitcnt vmcnt(19)
v_lshrrev_b32 v250, 8, v248
v_lshl_add_u32 v249, v250, 6, v249
v_mul_f32 v29, v29, v157
v_mul_f32 v30, v30, v157
MM v[136:139],a[140:143],a[100:103],0
TR a[184:185],v145 offset:8192
TR a[186:187],v145 offset:10240
v_mul_f32 v31, v31, v157
CV v172,v24,v25
MM v[136:139],a[136:139],a[68:71],v[136:139]
buffer_load_dwordx4 v240, s[24:27], s66 offen offset:2048 lds
v_and_b32 v251, 48, v248
v_add_u32 v249, v249, v251
CV v173,v26,v27
MM v[136:139],a[132:135],a[36:39],v[136:139]
s_add_u32 s66, s66, 9216
TR a[188:189],v145 offset:12288
TR a[190:191],v145 offset:14336
CV v174,v28,v29
CV v175,v30,v31
MM v[136:139],a[128:131],a[4:7],v[136:139]
s_waitcnt lgkmcnt(15)
buffer_store_dwordx4 v[172:175], v248, s[20:23], s79 offen offset:0 sc1
MM v[140:143],a[144:147],a[4:7],0
s_lshr_b32 s60, s49, 3
TR a[128:129],v146 offset:0
TR a[130:131],v146 offset:2048
AR v48,a48
MM v[140:143],a[148:151],a[36:39],v[140:143]
AR v49,a49
AR v50,a50
MM v[140:143],a[152:155],a[68:71],v[140:143]
TR a[132:133],v146 offset:4096
TR a[134:135],v146 offset:6144
AR v51,a51
MM v[140:143],a[156:159],a[100:103],v[140:143]
buffer_load_dwordx4 v240, s[24:27], s66 offen offset:3072 lds
AR v52,a52
AR v53,a53
MM v[132:135],a[156:159],a[96:99],0
TR a[136:137],v146 offset:8192
TR a[138:139],v146 offset:10240
AR v54,a54
MM v[132:135],a[152:155],a[64:67],v[132:135]
s_xor_b32 s66, s94, 7
AR v55,a55
v_mul_f32 v48, v48, v153
MM v[132:135],a[148:151],a[32:35],v[132:135]
s_mul_i32 s66, s66, 163840
TR a[140:141],v146 offset:12288
TR a[142:143],v146 offset:14336
v_mul_f32 v49, v49, v153
v_mul_f32 v50, v50, v153
MM v[132:135],a[144:147],a[0:3],v[132:135]
s_waitcnt lgkmcnt(15)
v_mul_f32 v51, v51, v153
v_mul_f32 v52, v52, v153
MM v[64:67],a[160:163],a[0:3],0
s_xor_b32 s92, s49, 6
TR a[144:145],v147 offset:0
TR a[146:147],v147 offset:2048
v_mul_f32 v53, v53, v153
MM v[64:67],a[164:167],a[32:35],v[64:67]
s_mul_i32 s92, s92, 17408
s_add_u32 s92, s83, s92
v_mul_f32 v54, v54, v153
v_mul_f32 v55, v55, v153
MM v[64:67],a[168:171],a[64:67],v[64:67]
s_add_u32 s66, s66, s86
TR a[148:149],v147 offset:4096
TR a[150:151],v147 offset:6144
CV v184,v48,v49
MM v[64:67],a[172:175],a[96:99],v[64:67]
s_add_u32 m0, s65, 49152
buffer_load_dwordx4 v[160:163], v248, s[20:23], s92 offen offset:0 sc1
CV v185,v50,v51
CV v186,v52,v53
MM v[72:75],a[172:175],a[100:103],0
TR a[152:153],v147 offset:8192
TR a[154:155],v147 offset:10240
CV v187,v54,v55
MM v[72:75],a[168:171],a[68:71],v[72:75]
buffer_store_dwordx4 v[184:187], v248, s[20:23], s78 offen offset:1024 sc1
MM v[72:75],a[164:167],a[36:39],v[72:75]
TR a[156:157],v147 offset:12288
TR a[158:159],v147 offset:14336
AR v56,a56
AR v57,a57
MM v[72:75],a[160:163],a[4:7],v[72:75]
s_waitcnt lgkmcnt(15)
AR v58,a58
AR v59,a59
MM v[76:79],a[176:179],a[4:7],0
TR a[160:161],v228 offset:0
TR a[162:163],v228 offset:2048
AR v60,a60
MM v[76:79],a[180:183],a[36:39],v[76:79]
buffer_load_dwordx4 v[176:179], v248, s[20:23], s92 offen offset:1024 sc1
AR v61,a61
AR v62,a62
MM v[76:79],a[184:187],a[68:71],v[76:79]
TR a[164:165],v228 offset:4096
TR a[166:167],v228 offset:6144
AR v63,a63
MM v[76:79],a[188:191],a[100:103],v[76:79]
v_mul_f32 v56, v56, v157
v_mul_f32 v57, v57, v157
MM v[68:71],a[188:191],a[96:99],0
TR a[168:169],v228 offset:8192
TR a[170:171],v228 offset:10240
v_mul_f32 v58, v58, v157
MM v[68:71],a[184:187],a[64:67],v[68:71]
buffer_load_dwordx4 v[192:195], v248, s[20:23], s92 offen offset:2048 sc1
v_mul_f32 v59, v59, v157
v_mul_f32 v60, v60, v157
MM v[68:71],a[180:183],a[32:35],v[68:71]
TR a[172:173],v228 offset:12288
TR a[174:175],v228 offset:14336
v_mul_f32 v61, v61, v157
v_mul_f32 v62, v62, v157
MM v[68:71],a[176:179],a[0:3],v[68:71]
s_waitcnt lgkmcnt(15)
v_mul_f32 v63, v63, v157
CV v188,v56,v57
MM v[96:99],a[128:131],a[0:3],0
TR a[176:177],v229 offset:0
TR a[178:179],v229 offset:2048
CV v189,v58,v59
MM v[96:99],a[132:135],a[32:35],v[96:99]
buffer_load_dwordx4 v[208:211], v248, s[20:23], s92 offen offset:3072 sc1
CV v190,v60,v61
CV v191,v62,v63
MM v[96:99],a[136:139],a[64:67],v[96:99]
TR a[180:181],v229 offset:4096
TR a[182:183],v229 offset:6144
MM v[96:99],a[140:143],a[96:99],v[96:99]
s_add_u32 s92, s92, 4096
v_xor_b32 v253, s60, v250
v_and_b32 v252, 240, v248
MM v[104:107],a[140:143],a[100:103],0
TR a[184:185],v229 offset:8192
TR a[186:187],v229 offset:10240
MM v[104:107],a[136:139],a[68:71],v[104:107]
buffer_store_dwordx4 v[188:191], v248, s[20:23], s79 offen offset:1024 sc1
AR v16,a80
AR v17,a81
AR v18,a82
AR v19,a83
MM v[104:107],a[132:135],a[36:39],v[104:107]
TR a[188:189],v229 offset:12288
TR a[190:191],v229 offset:14336
AR v20,a84
AR v21,a85
MM v[104:107],a[128:131],a[4:7],v[104:107]
s_waitcnt lgkmcnt(15)
AR v22,a86
AR v23,a87
MM v[108:111],a[144:147],a[4:7],0
TR a[128:129],v230 offset:0
TR a[130:131],v230 offset:2048
v_mul_f32 v16, v16, v153
MM v[108:111],a[148:151],a[36:39],v[108:111]
buffer_load_dwordx4 v[164:167], v248, s[20:23], s92 offen offset:0 sc1
v_mul_f32 v17, v17, v153
MM v[108:111],a[152:155],a[68:71],v[108:111]
TR a[132:133],v230 offset:4096
TR a[134:135],v230 offset:6144
v_mul_f32 v18, v18, v153
v_mul_f32 v19, v19, v153
MM v[108:111],a[156:159],a[100:103],v[108:111]
v_mul_f32 v20, v20, v153
v_mul_f32 v21, v21, v153
MM v[100:103],a[156:159],a[96:99],0
TR a[136:137],v230 offset:8192
TR a[138:139],v230 offset:10240
v_mul_f32 v22, v22, v153
MM v[100:103],a[152:155],a[64:67],v[100:103]
buffer_load_dwordx4 v[180:183], v248, s[20:23], s92 offen offset:1024 sc1
v_mul_f32 v23, v23, v153
CV v200,v16,v17
MM v[100:103],a[148:151],a[32:35],v[100:103]
TR a[140:141],v230 offset:12288
TR a[142:143],v230 offset:14336
CV v201,v18,v19
CV v202,v20,v21
MM v[100:103],a[144:147],a[0:3],v[100:103]
s_waitcnt lgkmcnt(15)
CV v203,v22,v23
v_lshl_add_u32 v232, v250, 2, v252
MM v[0:3],a[160:163],a[0:3],0
s_add_u32 s61, s65, 131072
TR a[144:145],v231 offset:0
TR a[146:147],v231 offset:2048
MM v[0:3],a[164:167],a[32:35],v[0:3]
buffer_store_dwordx4 v[200:203], v248, s[20:23], s78 offen offset:2048 sc1
AR v24,a88
MM v[0:3],a[168:171],a[64:67],v[0:3]
TR a[148:149],v231 offset:4096
TR a[150:151],v231 offset:6144
AR v25,a89
AR v26,a90
MM v[0:3],a[172:175],a[96:99],v[0:3]
AR v27,a91
AR v28,a92
MM v[8:11],a[172:175],a[100:103],0
TR a[152:153],v231 offset:8192
TR a[154:155],v231 offset:10240
AR v29,a93
MM v[8:11],a[168:171],a[68:71],v[8:11]
buffer_load_dwordx4 v[196:199], v248, s[20:23], s92 offen offset:2048 sc1
AR v30,a94
AR v31,a95
MM v[8:11],a[164:167],a[36:39],v[8:11]
TR a[156:157],v231 offset:12288
TR a[158:159],v231 offset:14336
v_mul_f32 v24, v24, v157
v_mul_f32 v25, v25, v157
MM v[8:11],a[160:163],a[4:7],v[8:11]
s_waitcnt lgkmcnt(15)
v_mul_f32 v26, v26, v157
v_mul_f32 v27, v27, v157
s_barrier
MM v[12:15],a[176:179],a[4:7],0
TR a[160:161],v244 offset:8192
TR a[162:163],v244 offset:9216
v_mul_f32 v28, v28, v157
MM v[12:15],a[180:183],a[36:39],v[12:15]
buffer_load_dwordx4 v[212:215], v248, s[20:23], s92 offen offset:3072 sc1
v_mul_f32 v29, v29, v157
MM v[12:15],a[184:187],a[68:71],v[12:15]
TR a[164:165],v244 offset:10240
TR a[166:167],v244 offset:11264
v_mul_f32 v30, v30, v157
v_mul_f32 v31, v31, v157
MM v[12:15],a[188:191],a[100:103],v[12:15]
CV v204,v24,v25
CV v205,v26,v27
MM v[4:7],a[188:191],a[96:99],0
TR a[168:169],v244 offset:12288
TR a[170:171],v244 offset:13312
CV v206,v28,v29
MM v[4:7],a[184:187],a[64:67],v[4:7]
buffer_load_dwordx4 v240, s[24:27], s66 offen offset:0 lds
CV v207,v30,v31
MM v[4:7],a[180:183],a[32:35],v[4:7]
s_add_u32 s66, s66, 9216
TR a[172:173],v244 offset:14336
TR a[174:175],v244 offset:15360
MM v[4:7],a[176:179],a[0:3],v[4:7]
s_waitcnt lgkmcnt(15)
v_mul_u32_u24 v224, 5, v252
v_lshl_add_u32 v224, v250, 3, v224
MM v[32:35],a[128:131],a[0:3],0
TR a[176:177],v235 offset:8192
TR a[178:179],v235 offset:9216
MM v[32:35],a[132:135],a[32:35],v[32:35]
buffer_store_dwordx4 v[204:207], v248, s[20:23], s79 offen offset:2048 sc1
AR v48,a112
AR v49,a113
AR v50,a114
AR v51,a115
AR v52,a116
MM v[32:35],a[136:139],a[64:67],v[32:35]
TR a[180:181],v235 offset:10240
TR a[182:183],v235 offset:11264
AR v53,a117
AR v54,a118
MM v[32:35],a[140:143],a[96:99],v[32:35]
AR v55,a119
v_mul_f32 v48, v48, v153
MM v[40:43],a[140:143],a[100:103],0
TR a[184:185],v235 offset:12288
TR a[186:187],v235 offset:13312
v_mul_f32 v49, v49, v153
MM v[40:43],a[136:139],a[68:71],v[40:43]
buffer_load_dwordx4 v240, s[24:27], s66 offen offset:1024 lds
v_mul_f32 v50, v50, v153
v_mul_f32 v51, v51, v153
MM v[40:43],a[132:135],a[36:39],v[40:43]
s_add_u32 s66, s66, 9216
TR a[188:189],v235 offset:14336
TR a[190:191],v235 offset:15360
v_mul_f32 v52, v52, v153
MM v[40:43],a[128:131],a[4:7],v[40:43]
s_waitcnt lgkmcnt(15)
v_mul_f32 v53, v53, v153
v_mul_f32 v54, v54, v153
MM v[44:47],a[144:147],a[4:7],0
TR a[128:129],v144 offset:16384
TR a[130:131],v144 offset:18432
v_mul_f32 v55, v55, v153
CV v216,v48,v49
MM v[44:47],a[148:151],a[36:39],v[44:47]
buffer_load_dwordx4 v240, s[24:27], s66 offen offset:2048 lds
CV v217,v50,v51
MM v[44:47],a[152:155],a[68:71],v[44:47]
s_add_u32 s66, s66, 9216
TR a[132:133],v144 offset:20480
TR a[134:135],v144 offset:22528
CV v218,v52,v53
CV v219,v54,v55
MM v[44:47],a[156:159],a[100:103],v[44:47]
v_add_u32 v224, s61, v224
v_mul_u32_u24 v254, 1280, v250
MM v[36:39],a[156:159],a[96:99],0
TR a[136:137],v144 offset:24576
TR a[138:139],v144 offset:26624
MM v[36:39],a[152:155],a[64:67],v[36:39]
buffer_store_dwordx4 v[216:219], v248, s[20:23], s78 offen offset:3072 sc1
AR v56,a120
AR v57,a121
AR v58,a122
MM v[36:39],a[148:151],a[32:35],v[36:39]
TR a[140:141],v144 offset:28672
LPG26_%=:
s_cbranch_execz LTR1_%=
TR a[142:143],v144 offset:30720
AR v59,a123
MM v[36:39],a[144:147],a[0:3],v[36:39]
s_waitcnt lgkmcnt(15)
AR v60,a124
AR v61,a125
s_waitcnt vmcnt(30)
MM v[128:131],a[160:163],a[8:11],v[128:131]
TR a[144:145],v145 offset:16384
TR a[146:147],v145 offset:18432
AR v62,a126
AR v63,a127
MM v[128:131],a[164:167],a[40:43],v[128:131]
buffer_load_dwordx4 v240, s[24:27], s66 offen offset:3072 lds
v_mul_f32 v56, v56, v157
MM v[128:131],a[168:171],a[72:75],v[128:131]
TR a[148:149],v145 offset:20480
TR a[150:151],v145 offset:22528
v_mul_f32 v57, v57, v157
v_mul_f32 v58, v58, v157
MM v[128:131],a[172:175],a[104:107],v[128:131]
s_waitcnt vmcnt(23)
v_mul_f32 v59, v59, v157
v_mul_f32 v60, v60, v157
MM v[136:139],a[172:175],a[108:111],v[136:139]
s_xor_b32 s66, s94, 0
TR a[152:153],v145 offset:24576
TR a[154:155],v145 offset:26624
v_mul_f32 v61, v61, v157
MM v[136:139],a[168:171],a[76:79],v[136:139]
s_mul_i32 s66, s66, 163840
v_mul_f32 v62, v62, v157
v_mul_f32 v63, v63, v157
MM v[136:139],a[164:167],a[44:47],v[136:139]
s_add_u32 s66, s66, s88
TR a[156:157],v145 offset:28672
TR a[158:159],v145 offset:30720
CV v220,v56,v57
MM v[136:139],a[160:163],a[12:15],v[136:139]
s_waitcnt lgkmcnt(15)
CV v221,v58,v59
CV v222,v60,v61
MM v[140:143],a[176:179],a[12:15],v[140:143]
s_add_u32 m0, s89, 131072
TR a[160:161],v146 offset:16384
TR a[162:163],v146 offset:18432
CV v223,v62,v63
MM v[140:143],a[180:183],a[44:47],v[140:143]
)ASM"
R"ASM(
buffer_store_dwordx4 v[220:223], v248, s[20:23], s79 offen offset:3072 sc1
MM v[140:143],a[184:187],a[76:79],v[140:143]
TR a[164:165],v146 offset:20480
TR a[166:167],v146 offset:22528
MM v[140:143],a[188:191],a[108:111],v[140:143]
buffer_load_dwordx4 v241, s[24:27], s66 offen offset:0 lds
MM v[132:135],a[188:191],a[104:107],v[132:135]
s_add_u32 s66, s66, 19456
TR a[168:169],v146 offset:24576
TR a[170:171],v146 offset:26624
MM v[132:135],a[184:187],a[72:75],v[132:135]
v_mul_u32_u24 v250, 33792, v253
v_add_u32 v250, v250, v252
MM v[132:135],a[180:183],a[40:43],v[132:135]
TR a[172:173],v146 offset:28672
TR a[174:175],v146 offset:30720
MM v[132:135],a[176:179],a[8:11],v[132:135]
s_waitcnt lgkmcnt(15)
buffer_load_dwordx4 v241, s[24:27], s66 offen offset:1024 lds
MM v[64:67],a[128:131],a[8:11],v[64:67]
s_xor_b32 s92, s49, 7
TR a[176:177],v147 offset:16384
TR a[178:179],v147 offset:18432
MM v[64:67],a[132:135],a[40:43],v[64:67]
s_mul_i32 s92, s92, 17408
s_add_u32 s92, s83, s92
v_lshlrev_b32 v253, 4, v253
MM v[64:67],a[136:139],a[72:75],v[64:67]
s_xor_b32 s66, s94, 0
TR a[180:181],v147 offset:20480
TR a[182:183],v147 offset:22528
MM v[64:67],a[140:143],a[104:107],v[64:67]
s_mul_i32 s66, s66, 163840
buffer_load_dwordx4 a[192:195], v248, s[20:23], s92 offen offset:0 sc1
MM v[72:75],a[140:143],a[108:111],v[72:75]
s_add_u32 s66, s66, s87
TR a[184:185],v147 offset:24576
TR a[186:187],v147 offset:26624
MM v[72:75],a[136:139],a[76:79],v[72:75]
s_add_u32 m0, s65, 65536
v_bfe_u32 v252, v248, 4, 3
v_lshl_add_u32 v254, v252, 3, v254
MM v[72:75],a[132:135],a[44:47],v[72:75]
TR a[188:189],v147 offset:28672
TR a[190:191],v147 offset:30720
MM v[72:75],a[128:131],a[12:15],v[72:75]
s_waitcnt lgkmcnt(15)
buffer_load_dwordx4 a[208:211], v248, s[20:23], s92 offen offset:1024 sc1
MM v[76:79],a[144:147],a[12:15],v[76:79]
TR a[128:129],v228 offset:16384
TR a[130:131],v228 offset:18432
MM v[76:79],a[148:151],a[44:47],v[76:79]
v_bfe_u32 v252, v248, 7, 1
v_mul_u32_u24 v252, 5120, v252
MM v[76:79],a[152:155],a[76:79],v[76:79]
TR a[132:133],v228 offset:20480
TR a[134:135],v228 offset:22528
MM v[76:79],a[156:159],a[108:111],v[76:79]
buffer_load_dwordx4 a[224:227], v248, s[20:23], s92 offen offset:2048 sc1
MM v[68:71],a[156:159],a[104:107],v[68:71]
TR a[136:137],v228 offset:24576
TR a[138:139],v228 offset:26624
MM v[68:71],a[152:155],a[72:75],v[68:71]
v_add_u32 v254, v254, v252
v_lshrrev_b32 v252, 6, v248
MM v[68:71],a[148:151],a[40:43],v[68:71]
TR a[140:141],v228 offset:28672
TR a[142:143],v228 offset:30720
MM v[68:71],a[144:147],a[8:11],v[68:71]
s_waitcnt lgkmcnt(15)
buffer_load_dwordx4 a[240:243], v248, s[20:23], s92 offen offset:3072 sc1
MM v[96:99],a[160:163],a[8:11],v[96:99]
TR a[144:145],v229 offset:16384
TR a[146:147],v229 offset:18432
MM v[96:99],a[164:167],a[40:43],v[96:99]
s_add_u32 s92, s92, 4096
v_mul_u32_u24 v225, 80, v252
v_add3_u32 v225, v225, v251, s61
MM v[96:99],a[168:171],a[72:75],v[96:99]
TR a[148:149],v229 offset:20480
TR a[150:151],v229 offset:22528
MM v[96:99],a[172:175],a[104:107],v[96:99]
buffer_load_dwordx4 a[196:199], v248, s[20:23], s92 offen offset:0 sc1
MM v[104:107],a[172:175],a[108:111],v[104:107]
TR a[152:153],v229 offset:24576
TR a[154:155],v229 offset:26624
MM v[104:107],a[168:171],a[76:79],v[104:107]
v_mul_u32_u24 v252, 1280, v252
v_add_u32 v251, v251, v252
MM v[104:107],a[164:167],a[44:47],v[104:107]
TR a[156:157],v229 offset:28672
TR a[158:159],v229 offset:30720
MM v[104:107],a[160:163],a[12:15],v[104:107]
s_waitcnt lgkmcnt(15)
buffer_load_dwordx4 a[212:215], v248, s[20:23], s92 offen offset:1024 sc1
MM v[108:111],a[176:179],a[12:15],v[108:111]
TR a[160:161],v230 offset:16384
TR a[162:163],v230 offset:18432
MM v[108:111],a[180:183],a[44:47],v[108:111]
MM v[108:111],a[184:187],a[76:79],v[108:111]
TR a[164:165],v230 offset:20480
TR a[166:167],v230 offset:22528
MM v[108:111],a[188:191],a[108:111],v[108:111]
buffer_load_dwordx4 a[228:231], v248, s[20:23], s92 offen offset:2048 sc1
MM v[100:103],a[188:191],a[104:107],v[100:103]
TR a[168:169],v230 offset:24576
TR a[170:171],v230 offset:26624
MM v[100:103],a[184:187],a[72:75],v[100:103]
MM v[100:103],a[180:183],a[40:43],v[100:103]
TR a[172:173],v230 offset:28672
TR a[174:175],v230 offset:30720
MM v[100:103],a[176:179],a[8:11],v[100:103]
s_waitcnt lgkmcnt(15)
buffer_load_dwordx4 a[244:247], v248, s[20:23], s92 offen offset:3072 sc1
MM v[0:3],a[128:131],a[8:11],v[0:3]
TR a[176:177],v231 offset:16384
TR a[178:179],v231 offset:18432
MM v[0:3],a[132:135],a[40:43],v[0:3]
MM v[0:3],a[136:139],a[72:75],v[0:3]
TR a[180:181],v231 offset:20480
TR a[182:183],v231 offset:22528
MM v[0:3],a[140:143],a[104:107],v[0:3]
buffer_load_dwordx4 v240, s[24:27], s66 offen offset:0 lds
MM v[8:11],a[140:143],a[108:111],v[8:11]
s_add_u32 s66, s66, 9216
TR a[184:185],v231 offset:24576
TR a[186:187],v231 offset:26624
MM v[8:11],a[136:139],a[76:79],v[8:11]
MM v[8:11],a[132:135],a[44:47],v[8:11]
TR a[188:189],v231 offset:28672
TR a[190:191],v231 offset:30720
MM v[8:11],a[128:131],a[12:15],v[8:11]
s_waitcnt lgkmcnt(15)
buffer_load_dwordx4 v240, s[24:27], s66 offen offset:1024 lds
s_barrier
MM v[12:15],a[144:147],a[12:15],v[12:15]
s_add_u32 s66, s66, 9216
TR a[128:129],v244 offset:16384
TR a[130:131],v244 offset:17408
MM v[12:15],a[148:151],a[44:47],v[12:15]
MM v[12:15],a[152:155],a[76:79],v[12:15]
TR a[132:133],v244 offset:18432
TR a[134:135],v244 offset:19456
MM v[12:15],a[156:159],a[108:111],v[12:15]
buffer_load_dwordx4 v240, s[24:27], s66 offen offset:2048 lds
MM v[4:7],a[156:159],a[104:107],v[4:7]
s_add_u32 s66, s66, 9216
TR a[136:137],v244 offset:20480
TR a[138:139],v244 offset:21504
MM v[4:7],a[152:155],a[72:75],v[4:7]
MM v[4:7],a[148:151],a[40:43],v[4:7]
TR a[140:141],v244 offset:22528
TR a[142:143],v244 offset:23552
MM v[4:7],a[144:147],a[8:11],v[4:7]
s_waitcnt lgkmcnt(15)
buffer_load_dwordx4 v240, s[24:27], s66 offen offset:3072 lds
MM v[32:35],a[160:163],a[8:11],v[32:35]
TR a[144:145],v235 offset:16384
TR a[146:147],v235 offset:17408
MM v[32:35],a[164:167],a[40:43],v[32:35]
s_xor_b32 s66, s94, 0
s_mul_i32 s66, s66, 163840
s_add_u32 s66, s66, s86
MM v[32:35],a[168:171],a[72:75],v[32:35]
s_add_u32 m0, s65, 0
TR a[148:149],v235 offset:18432
TR a[150:151],v235 offset:19456
MM v[32:35],a[172:175],a[104:107],v[32:35]
buffer_load_dwordx4 v240, s[24:27], s66 offen offset:0 lds
MM v[40:43],a[172:175],a[108:111],v[40:43]
s_add_u32 s66, s66, 9216
TR a[152:153],v235 offset:20480
TR a[154:155],v235 offset:21504
MM v[40:43],a[168:171],a[76:79],v[40:43]
MM v[40:43],a[164:167],a[44:47],v[40:43]
TR a[156:157],v235 offset:22528
TR a[158:159],v235 offset:23552
MM v[40:43],a[160:163],a[12:15],v[40:43]
s_waitcnt lgkmcnt(15)
buffer_load_dwordx4 v240, s[24:27], s66 offen offset:1024 lds
MM v[44:47],a[176:179],a[12:15],v[44:47]
s_lshl_b32 s97, s84, 2
TR a[160:161],v144 offset:32768
TR a[162:163],v144 offset:34816
MM v[44:47],a[180:183],a[44:47],v[44:47]
s_add_u32 s97, s97, s96
s_add_u32 s97, s97, 16
s_waitcnt vmcnt(16)
MM v[44:47],a[184:187],a[76:79],v[44:47]
s_add_u32 s66, s66, 9216
TR a[164:165],v144 offset:36864
TR a[166:167],v144 offset:38912
MM v[44:47],a[188:191],a[108:111],v[44:47]
v_mov_b32 v242, s69
s_mov_b64 exec, 1
buffer_store_dword v242, off, s[20:23], s97 offset:0 sc1
s_mov_b64 exec, -1
MM v[36:39],a[188:191],a[104:107],v[36:39]
TR a[168:169],v144 offset:40960
TR a[170:171],v144 offset:43008
MM v[36:39],a[184:187],a[72:75],v[36:39]
buffer_load_dwordx4 v240, s[24:27], s66 offen offset:2048 lds
MM v[36:39],a[180:183],a[40:43],v[36:39]
s_add_u32 s66, s66, 9216
TR a[172:173],v144 offset:45056
TR a[174:175],v144 offset:47104
MM v[36:39],a[176:179],a[8:11],v[36:39]
s_waitcnt lgkmcnt(15)
MM v[128:131],a[128:131],v[160:163],v[128:131]
TR a[176:177],v145 offset:32768
TR a[178:179],v145 offset:34816
MM v[128:131],a[132:135],v[176:179],v[128:131]
buffer_load_dwordx4 v240, s[24:27], s66 offen offset:3072 lds
MM v[128:131],a[136:139],v[192:195],v[128:131]
TR a[180:181],v145 offset:36864
TR a[182:183],v145 offset:38912
MM v[128:131],a[140:143],v[208:211],v[128:131]
s_xor_b32 s66, s94, 1
s_mul_i32 s66, s66, 163840
s_add_u32 s66, s66, s88
MM v[136:139],a[140:143],v[212:215],v[136:139]
s_add_u32 m0, s89, 139264
TR a[184:185],v145 offset:40960
TR a[186:187],v145 offset:43008
MM v[136:139],a[136:139],v[196:199],v[136:139]
buffer_load_dwordx4 v241, s[24:27], s66 offen offset:0 lds
MM v[136:139],a[132:135],v[180:183],v[136:139]
s_add_u32 s66, s66, 19456
TR a[188:189],v145 offset:45056
TR a[190:191],v145 offset:47104
MM v[136:139],a[128:131],v[164:167],v[136:139]
s_waitcnt lgkmcnt(15)
MM v[140:143],a[144:147],v[164:167],v[140:143]
TR a[128:129],v146 offset:32768
TR a[130:131],v146 offset:34816
MM v[140:143],a[148:151],v[180:183],v[140:143]
buffer_load_dwordx4 v241, s[24:27], s66 offen offset:1024 lds
MM v[140:143],a[152:155],v[196:199],v[140:143]
TR a[132:133],v146 offset:36864
TR a[134:135],v146 offset:38912
MM v[140:143],a[156:159],v[212:215],v[140:143]
s_xor_b32 s66, s94, 1
s_mul_i32 s66, s66, 163840
s_add_u32 s66, s66, s87
MM v[132:135],a[156:159],v[208:211],v[132:135]
s_add_u32 m0, s65, 81920
TR a[136:137],v146 offset:40960
TR a[138:139],v146 offset:43008
MM v[132:135],a[152:155],v[192:195],v[132:135]
buffer_load_dwordx4 v240, s[24:27], s66 offen offset:0 lds
MM v[132:135],a[148:151],v[176:179],v[132:135]
s_add_u32 s66, s66, 9216
TR a[140:141],v146 offset:45056
TR a[142:143],v146 offset:47104
MM v[132:135],a[144:147],v[160:163],v[132:135]
s_waitcnt lgkmcnt(15)
MM v[64:67],a[160:163],v[160:163],v[64:67]
TR a[144:145],v147 offset:32768
TR a[146:147],v147 offset:34816
MM v[64:67],a[164:167],v[176:179],v[64:67]
buffer_load_dwordx4 v240, s[24:27], s66 offen offset:1024 lds
MM v[64:67],a[168:171],v[192:195],v[64:67]
s_add_u32 s66, s66, 9216
TR a[148:149],v147 offset:36864
TR a[150:151],v147 offset:38912
MM v[64:67],a[172:175],v[208:211],v[64:67]
MM v[72:75],a[172:175],v[212:215],v[72:75]
TR a[152:153],v147 offset:40960
TR a[154:155],v147 offset:43008
MM v[72:75],a[168:171],v[196:199],v[72:75]
buffer_load_dwordx4 v240, s[24:27], s66 offen offset:2048 lds
MM v[72:75],a[164:167],v[180:183],v[72:75]
s_add_u32 s66, s66, 9216
TR a[156:157],v147 offset:45056
TR a[158:159],v147 offset:47104
MM v[72:75],a[160:163],v[164:167],v[72:75]
s_waitcnt lgkmcnt(15)
MM v[76:79],a[176:179],v[164:167],v[76:79]
TR a[160:161],v228 offset:32768
TR a[162:163],v228 offset:34816
MM v[76:79],a[180:183],v[180:183],v[76:79]
buffer_load_dwordx4 v240, s[24:27], s66 offen offset:3072 lds
MM v[76:79],a[184:187],v[196:199],v[76:79]
TR a[164:165],v228 offset:36864
TR a[166:167],v228 offset:38912
MM v[76:79],a[188:191],v[212:215],v[76:79]
MM v[68:71],a[188:191],v[208:211],v[68:71]
TR a[168:169],v228 offset:40960
TR a[170:171],v228 offset:43008
MM v[68:71],a[184:187],v[192:195],v[68:71]
MM v[68:71],a[180:183],v[176:179],v[68:71]
TR a[172:173],v228 offset:45056
TR a[174:175],v228 offset:47104
MM v[68:71],a[176:179],v[160:163],v[68:71]
s_waitcnt lgkmcnt(15)
MM v[96:99],a[128:131],v[160:163],v[96:99]
s_add_u32 s80, s18, 131072
TR a[176:177],v229 offset:32768
TR a[178:179],v229 offset:34816
MM v[96:99],a[132:135],v[176:179],v[96:99]
buffer_load_dwordx4 v[80:83], v251, s[20:23], s80 offen offset:0 sc1
MM v[96:99],a[136:139],v[192:195],v[96:99]
TR a[180:181],v229 offset:36864
TR a[182:183],v229 offset:38912
MM v[96:99],a[140:143],v[208:211],v[96:99]
v_mov_b32 v237, 0x3f80
v_and_b32 v236, 16, v248
MM v[104:107],a[140:143],v[212:215],v[104:107]
TR a[184:185],v229 offset:40960
TR a[186:187],v229 offset:43008
MM v[104:107],a[136:139],v[196:199],v[104:107]
s_add_u32 s80, s18, 151552
buffer_load_dwordx4 v[84:87], v251, s[20:23], s80 offen offset:0 sc1
MM v[104:107],a[132:135],v[180:183],v[104:107]
TR a[188:189],v229 offset:45056
TR a[190:191],v229 offset:47104
MM v[104:107],a[128:131],v[164:167],v[104:107]
s_waitcnt lgkmcnt(15)
v_lshlrev_b32 v236, v236, v237
s_mov_b32 s28, 0x30003
MM v[108:111],a[144:147],v[164:167],v[108:111]
s_mov_b32 s29, s28
TR a[128:129],v230 offset:32768
TR a[130:131],v230 offset:34816
MM v[108:111],a[148:151],v[180:183],v[108:111]
)ASM"
R"ASM(
s_add_u32 s80, s63, 131072
v_lshrrev_b32 v243, 2, v248
v_min_u32 v243, 12, v243
MM v[108:111],a[152:155],v[196:199],v[108:111]
TR a[132:133],v230 offset:36864
TR a[134:135],v230 offset:38912
MM v[108:111],a[156:159],v[212:215],v[108:111]
buffer_load_dword v242, v243, s[20:23], s96 offen offset:16 sc1
buffer_load_dwordx2 v[150:151], v254, s[20:23], s80 offen offset:0 sc1
MM v[100:103],a[156:159],v[208:211],v[100:103]
TR a[136:137],v230 offset:40960
TR a[138:139],v230 offset:43008
MM v[100:103],a[152:155],v[192:195],v[100:103]
v_cndmask_b32 v112, 0, v236, s[28:29]
s_lshl_b64 s[28:29], s[28:29], 2
MM v[100:103],a[148:151],v[176:179],v[100:103]
TR a[140:141],v230 offset:45056
TR a[142:143],v230 offset:47104
MM v[100:103],a[144:147],v[160:163],v[100:103]
s_waitcnt lgkmcnt(15)
v_cndmask_b32 v113, 0, v236, s[28:29]
s_lshl_b64 s[28:29], s[28:29], 2
MM v[0:3],a[160:163],v[160:163],v[0:3]
TR a[144:145],v231 offset:32768
TR a[146:147],v231 offset:34816
MM v[0:3],a[164:167],v[176:179],v[0:3]
v_cndmask_b32 v114, 0, v236, s[28:29]
s_lshl_b64 s[28:29], s[28:29], 2
MM v[0:3],a[168:171],v[192:195],v[0:3]
TR a[148:149],v231 offset:36864
TR a[150:151],v231 offset:38912
MM v[0:3],a[172:175],v[208:211],v[0:3]
v_cndmask_b32 v115, 0, v236, s[28:29]
s_lshl_b64 s[28:29], s[28:29], 2
MM v[8:11],a[172:175],v[212:215],v[8:11]
TR a[152:153],v231 offset:40960
TR a[154:155],v231 offset:43008
MM v[8:11],a[168:171],v[196:199],v[8:11]
v_cndmask_b32 v116, 0, v236, s[28:29]
s_lshl_b64 s[28:29], s[28:29], 2
MM v[8:11],a[164:167],v[180:183],v[8:11]
TR a[156:157],v231 offset:45056
TR a[158:159],v231 offset:47104
MM v[8:11],a[160:163],v[164:167],v[8:11]
s_waitcnt lgkmcnt(15)
v_cndmask_b32 v117, 0, v236, s[28:29]
s_lshl_b64 s[28:29], s[28:29], 2
s_barrier
MM v[12:15],a[176:179],v[164:167],v[12:15]
s_xor_b32 s66, s94, 2
TR a[160:161],v244 offset:24576
TR a[162:163],v244 offset:25600
MM v[12:15],a[180:183],v[180:183],v[12:15]
s_mul_i32 s66, s66, 163840
v_cndmask_b32 v118, 0, v236, s[28:29]
s_lshl_b64 s[28:29], s[28:29], 2
MM v[12:15],a[184:187],v[196:199],v[12:15]
s_add_u32 s66, s66, s88
TR a[164:165],v244 offset:26624
TR a[166:167],v244 offset:27648
MM v[12:15],a[188:191],v[212:215],v[12:15]
s_add_u32 m0, s89, 147456
v_cndmask_b32 v119, 0, v236, s[28:29]
s_mov_b32 s28, 0x300003
MM v[4:7],a[188:191],v[208:211],v[4:7]
s_mov_b32 s29, 0x30000300
TR a[168:169],v244 offset:28672
TR a[170:171],v244 offset:29696
MM v[4:7],a[184:187],v[192:195],v[4:7]
buffer_load_dwordx4 v241, s[24:27], s66 offen offset:0 lds
MM v[4:7],a[180:183],v[176:179],v[4:7]
s_add_u32 s66, s66, 19456
TR a[172:173],v244 offset:30720
TR a[174:175],v244 offset:31744
MM v[4:7],a[176:179],v[160:163],v[4:7]
s_waitcnt lgkmcnt(15)
v_mov_b32 v120, 0
v_mov_b32 v121, 0
MM v[32:35],a[128:131],v[160:163],v[32:35]
TR a[176:177],v235 offset:24576
TR a[178:179],v235 offset:25600
MM v[32:35],a[132:135],v[176:179],v[32:35]
buffer_load_dwordx4 v241, s[24:27], s66 offen offset:1024 lds
MM v[32:35],a[136:139],v[192:195],v[32:35]
TR a[180:181],v235 offset:26624
TR a[182:183],v235 offset:27648
MM v[32:35],a[140:143],v[208:211],v[32:35]
s_xor_b32 s66, s94, 2
v_mov_b32 v124, 0
v_mov_b32 v125, 0
MM v[40:43],a[140:143],v[212:215],v[40:43]
s_mul_i32 s66, s66, 163840
TR a[184:185],v235 offset:28672
TR a[186:187],v235 offset:29696
MM v[40:43],a[136:139],v[196:199],v[40:43]
s_add_u32 s66, s66, s87
v_cndmask_b32 v122, 0, v236, s[28:29]
s_lshl_b64 s[28:29], s[28:29], 2
MM v[40:43],a[132:135],v[180:183],v[40:43]
s_add_u32 m0, s65, 98304
TR a[188:189],v235 offset:30720
TR a[190:191],v235 offset:31744
MM v[40:43],a[128:131],v[164:167],v[40:43]
s_waitcnt lgkmcnt(15)
buffer_load_dwordx4 v240, s[24:27], s66 offen offset:0 lds
MM v[44:47],a[144:147],v[164:167],v[44:47]
s_add_u32 s66, s66, 9216
TR a[128:129],v144 offset:49152
TR a[130:131],v144 offset:51200
MM v[44:47],a[148:151],v[180:183],v[44:47]
v_cndmask_b32 v123, 0, v236, s[28:29]
MM v[44:47],a[152:155],v[196:199],v[44:47]
TR a[132:133],v144 offset:53248
TR a[134:135],v144 offset:55296
MM v[44:47],a[156:159],v[212:215],v[44:47]
buffer_load_dwordx4 v240, s[24:27], s66 offen offset:1024 lds
MM v[36:39],a[156:159],v[208:211],v[36:39]
s_add_u32 s66, s66, 9216
TR a[136:137],v144 offset:57344
TR a[138:139],v144 offset:59392
MM v[36:39],a[152:155],v[192:195],v[36:39]
MM v[36:39],a[148:151],v[176:179],v[36:39]
TR a[140:141],v144 offset:61440
TR a[142:143],v144 offset:63488
MM v[36:39],a[144:147],v[160:163],v[36:39]
s_waitcnt lgkmcnt(15)
buffer_load_dwordx4 v240, s[24:27], s66 offen offset:2048 lds
s_waitcnt vmcnt(28)
MM v[128:131],a[160:163],a[192:195],v[128:131]
s_add_u32 s80, s18, 0
TR a[144:145],v145 offset:49152
TR a[146:147],v145 offset:51200
MM v[128:131],a[164:167],a[208:211],v[128:131]
s_add_u32 s66, s66, 9216
buffer_load_dwordx4 v[88:91], v251, s[32:35], s80 offen offset:0
MM v[128:131],a[168:171],a[224:227],v[128:131]
TR a[148:149],v145 offset:53248
TR a[150:151],v145 offset:55296
MM v[128:131],a[172:175],a[240:243],v[128:131]
s_waitcnt vmcnt(25)
MM v[136:139],a[172:175],a[244:247],v[136:139]
TR a[152:153],v145 offset:57344
TR a[154:155],v145 offset:59392
MM v[136:139],a[168:171],a[228:231],v[136:139]
s_add_u32 s80, s18, 20480
buffer_load_dwordx4 v[92:95], v251, s[32:35], s80 offen offset:0
MM v[136:139],a[164:167],a[212:215],v[136:139]
TR a[156:157],v145 offset:61440
TR a[158:159],v145 offset:63488
MM v[136:139],a[160:163],a[196:199],v[136:139]
s_waitcnt lgkmcnt(15)
MM v[140:143],a[176:179],a[196:199],v[140:143]
TR a[160:161],v146 offset:49152
TR a[162:163],v146 offset:51200
MM v[140:143],a[180:183],a[212:215],v[140:143]
s_add_u32 s80, s63, 0
buffer_load_dwordx2 v[126:127], v254, s[32:35], s80 offen offset:0
MM v[140:143],a[184:187],a[228:231],v[140:143]
TR a[164:165],v146 offset:53248
TR a[166:167],v146 offset:55296
MM v[140:143],a[188:191],a[244:247],v[140:143]
buffer_load_dwordx4 v240, s[24:27], s66 offen offset:3072 lds
MM v[132:135],a[188:191],a[240:243],v[132:135]
TR a[168:169],v146 offset:57344
TR a[170:171],v146 offset:59392
MM v[132:135],a[184:187],a[224:227],v[132:135]
MM v[132:135],a[180:183],a[208:211],v[132:135]
TR a[172:173],v146 offset:61440
TR a[174:175],v146 offset:63488
MM v[132:135],a[176:179],a[192:195],v[132:135]
s_waitcnt lgkmcnt(15)
MM v[64:67],a[128:131],a[192:195],v[64:67]
s_waitcnt vmcnt(10)
TR a[176:177],v147 offset:49152
TR a[178:179],v147 offset:51200
MM v[64:67],a[132:135],a[208:211],v[64:67]
v_cmp_ne_u32 vcc, s69, v242
s_cbranch_vccnz LRPO1_%=
LRPO1b_%=:
MM v[64:67],a[136:139],a[224:227],v[64:67]
s_xor_b32 s92, s49, 1
TR a[180:181],v147 offset:53248
TR a[182:183],v147 offset:55296
MM v[64:67],a[140:143],a[240:243],v[64:67]
LPG27_%=:
s_cbranch_execz LTR1_%=
s_mul_i32 s92, s92, 17408
s_add_u32 s92, s83, s92
s_add_u32 s92, s92, 8192
MM v[72:75],a[140:143],a[244:247],v[72:75]
TR a[184:185],v147 offset:57344
TR a[186:187],v147 offset:59392
MM v[72:75],a[136:139],a[228:231],v[72:75]
buffer_load_dwordx4 a[16:19], v248, s[20:23], s92 offen offset:0 sc1
MM v[72:75],a[132:135],a[212:215],v[72:75]
TR a[188:189],v147 offset:61440
TR a[190:191],v147 offset:63488
MM v[72:75],a[128:131],a[196:199],v[72:75]
s_waitcnt lgkmcnt(15)
buffer_load_dwordx4 a[48:51], v248, s[20:23], s92 offen offset:1024 sc1
MM v[76:79],a[144:147],a[196:199],v[76:79]
TR a[128:129],v228 offset:49152
TR a[130:131],v228 offset:51200
MM v[76:79],a[148:151],a[212:215],v[76:79]
MM v[76:79],a[152:155],a[228:231],v[76:79]
TR a[132:133],v228 offset:53248
TR a[134:135],v228 offset:55296
MM v[76:79],a[156:159],a[244:247],v[76:79]
buffer_load_dwordx4 a[80:83], v248, s[20:23], s92 offen offset:2048 sc1
MM v[68:71],a[156:159],a[240:243],v[68:71]
TR a[136:137],v228 offset:57344
TR a[138:139],v228 offset:59392
MM v[68:71],a[152:155],a[224:227],v[68:71]
MM v[68:71],a[148:151],a[208:211],v[68:71]
TR a[140:141],v228 offset:61440
TR a[142:143],v228 offset:63488
MM v[68:71],a[144:147],a[192:195],v[68:71]
s_waitcnt lgkmcnt(15)
buffer_load_dwordx4 a[112:115], v248, s[20:23], s92 offen offset:3072 sc1
MM v[96:99],a[160:163],a[192:195],v[96:99]
s_add_u32 s92, s92, 4096
TR a[144:145],v229 offset:49152
TR a[146:147],v229 offset:51200
MM v[96:99],a[164:167],a[208:211],v[96:99]
MM v[96:99],a[168:171],a[224:227],v[96:99]
TR a[148:149],v229 offset:53248
TR a[150:151],v229 offset:55296
MM v[96:99],a[172:175],a[240:243],v[96:99]
buffer_load_dwordx4 a[20:23], v248, s[20:23], s92 offen offset:0 sc1
MM v[104:107],a[172:175],a[244:247],v[104:107]
TR a[152:153],v229 offset:57344
TR a[154:155],v229 offset:59392
MM v[104:107],a[168:171],a[228:231],v[104:107]
MM v[104:107],a[164:167],a[212:215],v[104:107]
TR a[156:157],v229 offset:61440
TR a[158:159],v229 offset:63488
MM v[104:107],a[160:163],a[196:199],v[104:107]
s_waitcnt lgkmcnt(15)
buffer_load_dwordx4 a[52:55], v248, s[20:23], s92 offen offset:1024 sc1
MM v[108:111],a[176:179],a[196:199],v[108:111]
TR a[160:161],v230 offset:49152
TR a[162:163],v230 offset:51200
MM v[108:111],a[180:183],a[212:215],v[108:111]
MM v[108:111],a[184:187],a[228:231],v[108:111]
TR a[164:165],v230 offset:53248
TR a[166:167],v230 offset:55296
MM v[108:111],a[188:191],a[244:247],v[108:111]
buffer_load_dwordx4 a[84:87], v248, s[20:23], s92 offen offset:2048 sc1
MM v[100:103],a[188:191],a[240:243],v[100:103]
TR a[168:169],v230 offset:57344
TR a[170:171],v230 offset:59392
MM v[100:103],a[184:187],a[224:227],v[100:103]
MM v[100:103],a[180:183],a[208:211],v[100:103]
TR a[172:173],v230 offset:61440
TR a[174:175],v230 offset:63488
MM v[100:103],a[176:179],a[192:195],v[100:103]
s_waitcnt lgkmcnt(15)
buffer_load_dwordx4 a[116:119], v248, s[20:23], s92 offen offset:3072 sc1
MM v[0:3],a[128:131],a[192:195],v[0:3]
TR a[176:177],v231 offset:49152
TR a[178:179],v231 offset:51200
MM v[0:3],a[132:135],a[208:211],v[0:3]
MM v[0:3],a[136:139],a[224:227],v[0:3]
TR a[180:181],v231 offset:53248
TR a[182:183],v231 offset:55296
MM v[0:3],a[140:143],a[240:243],v[0:3]
MM v[8:11],a[140:143],a[244:247],v[8:11]
TR a[184:185],v231 offset:57344
TR a[186:187],v231 offset:59392
MM v[8:11],a[136:139],a[228:231],v[8:11]
MM v[8:11],a[132:135],a[212:215],v[8:11]
TR a[188:189],v231 offset:61440
TR a[190:191],v231 offset:63488
MM v[8:11],a[128:131],a[196:199],v[8:11]
s_waitcnt lgkmcnt(15)
s_barrier
MM v[12:15],a[144:147],a[196:199],v[12:15]
s_xor_b32 s66, s94, 3
TR a[128:129],v244 offset:0
TR a[130:131],v244 offset:1024
MM v[12:15],a[148:151],a[212:215],v[12:15]
s_mul_i32 s66, s66, 163840
s_add_u32 s66, s66, s88
s_add_u32 m0, s89, 155648
MM v[12:15],a[152:155],a[228:231],v[12:15]
TR a[132:133],v244 offset:2048
TR a[134:135],v244 offset:3072
MM v[12:15],a[156:159],a[244:247],v[12:15]
buffer_load_dwordx4 v241, s[24:27], s66 offen offset:0 lds
MM v[4:7],a[156:159],a[240:243],v[4:7]
s_add_u32 s66, s66, 19456
TR a[136:137],v244 offset:4096
TR a[138:139],v244 offset:5120
MM v[4:7],a[152:155],a[224:227],v[4:7]
MM v[4:7],a[148:151],a[208:211],v[4:7]
TR a[140:141],v244 offset:6144
TR a[142:143],v244 offset:7168
MM v[4:7],a[144:147],a[192:195],v[4:7]
s_waitcnt lgkmcnt(15)
buffer_load_dwordx4 v241, s[24:27], s66 offen offset:1024 lds
MM v[32:35],a[160:163],a[192:195],v[32:35]
TR a[144:145],v235 offset:0
TR a[146:147],v235 offset:1024
MM v[32:35],a[164:167],a[208:211],v[32:35]
s_xor_b32 s66, s94, 3
s_mul_i32 s66, s66, 163840
s_add_u32 s66, s66, s87
MM v[32:35],a[168:171],a[224:227],v[32:35]
s_add_u32 m0, s65, 114688
TR a[148:149],v235 offset:2048
TR a[150:151],v235 offset:3072
MM v[32:35],a[172:175],a[240:243],v[32:35]
buffer_load_dwordx4 v240, s[24:27], s66 offen offset:0 lds
MM v[40:43],a[172:175],a[244:247],v[40:43]
s_add_u32 s66, s66, 9216
TR a[152:153],v235 offset:4096
TR a[154:155],v235 offset:5120
MM v[40:43],a[168:171],a[228:231],v[40:43]
MM v[40:43],a[164:167],a[212:215],v[40:43]
TR a[156:157],v235 offset:6144
TR a[158:159],v235 offset:7168
MM v[40:43],a[160:163],a[196:199],v[40:43]
s_waitcnt lgkmcnt(15)
buffer_load_dwordx4 v240, s[24:27], s66 offen offset:1024 lds
MM v[44:47],a[176:179],a[196:199],v[44:47]
s_add_u32 s66, s66, 9216
TR a[160:161],v144 offset:0
TR a[162:163],v144 offset:2048
MM v[44:47],a[180:183],a[212:215],v[44:47]
)ASM"
R"ASM(
MM v[44:47],a[184:187],a[228:231],v[44:47]
TR a[164:165],v144 offset:4096
TR a[166:167],v144 offset:6144
MM v[44:47],a[188:191],a[244:247],v[44:47]
buffer_load_dwordx4 v240, s[24:27], s66 offen offset:2048 lds
MM v[36:39],a[188:191],a[240:243],v[36:39]
s_add_u32 s66, s66, 9216
TR a[168:169],v144 offset:8192
TR a[170:171],v144 offset:10240
MM v[36:39],a[184:187],a[224:227],v[36:39]
MM v[36:39],a[180:183],a[208:211],v[36:39]
TR a[172:173],v144 offset:12288
TR a[174:175],v144 offset:14336
MM v[36:39],a[176:179],a[192:195],v[36:39]
s_waitcnt lgkmcnt(15)
buffer_load_dwordx4 v240, s[24:27], s66 offen offset:3072 lds
MM v[128:131],a[128:131],v[168:171],v[128:131]
TR a[176:177],v145 offset:0
TR a[178:179],v145 offset:2048
MM v[128:131],a[132:135],v[184:187],v[128:131]
MM v[128:131],a[136:139],v[200:203],v[128:131]
TR a[180:181],v145 offset:4096
TR a[182:183],v145 offset:6144
MM v[128:131],a[140:143],v[216:219],v[128:131]
MM v[136:139],a[140:143],v[220:223],v[136:139]
TR a[184:185],v145 offset:8192
TR a[186:187],v145 offset:10240
MM v[136:139],a[136:139],v[204:207],v[136:139]
MM v[136:139],a[132:135],v[188:191],v[136:139]
TR a[188:189],v145 offset:12288
TR a[190:191],v145 offset:14336
MM v[136:139],a[128:131],v[172:175],v[136:139]
s_waitcnt lgkmcnt(15)
MM v[140:143],a[144:147],v[172:175],v[140:143]
s_xor_b32 s92, s49, 2
TR a[128:129],v146 offset:0
TR a[130:131],v146 offset:2048
MM v[140:143],a[148:151],v[188:191],v[140:143]
s_mul_i32 s92, s92, 17408
s_add_u32 s92, s83, s92
s_add_u32 s92, s92, 8192
MM v[140:143],a[152:155],v[204:207],v[140:143]
TR a[132:133],v146 offset:4096
TR a[134:135],v146 offset:6144
MM v[140:143],a[156:159],v[220:223],v[140:143]
buffer_load_dwordx4 a[24:27], v248, s[20:23], s92 offen offset:0 sc1
MM v[132:135],a[156:159],v[216:219],v[132:135]
TR a[136:137],v146 offset:8192
TR a[138:139],v146 offset:10240
MM v[132:135],a[152:155],v[200:203],v[132:135]
buffer_load_dwordx4 a[56:59], v248, s[20:23], s92 offen offset:1024 sc1
MM v[132:135],a[148:151],v[184:187],v[132:135]
TR a[140:141],v146 offset:12288
TR a[142:143],v146 offset:14336
MM v[132:135],a[144:147],v[168:171],v[132:135]
s_waitcnt lgkmcnt(15)
MM v[64:67],a[160:163],v[168:171],v[64:67]
TR a[144:145],v147 offset:0
TR a[146:147],v147 offset:2048
MM v[64:67],a[164:167],v[184:187],v[64:67]
buffer_load_dwordx4 a[88:91], v248, s[20:23], s92 offen offset:2048 sc1
MM v[64:67],a[168:171],v[200:203],v[64:67]
TR a[148:149],v147 offset:4096
TR a[150:151],v147 offset:6144
MM v[64:67],a[172:175],v[216:219],v[64:67]
MM v[72:75],a[172:175],v[220:223],v[72:75]
TR a[152:153],v147 offset:8192
TR a[154:155],v147 offset:10240
MM v[72:75],a[168:171],v[204:207],v[72:75]
buffer_load_dwordx4 a[120:123], v248, s[20:23], s92 offen offset:3072 sc1
MM v[72:75],a[164:167],v[188:191],v[72:75]
s_add_u32 s92, s92, 4096
TR a[156:157],v147 offset:12288
TR a[158:159],v147 offset:14336
MM v[72:75],a[160:163],v[172:175],v[72:75]
s_waitcnt lgkmcnt(15)
MM v[76:79],a[176:179],v[172:175],v[76:79]
TR a[160:161],v230 offset:0
TR a[162:163],v230 offset:2048
MM v[76:79],a[180:183],v[188:191],v[76:79]
buffer_load_dwordx4 a[28:31], v248, s[20:23], s92 offen offset:0 sc1
MM v[76:79],a[184:187],v[204:207],v[76:79]
TR a[164:165],v230 offset:4096
TR a[166:167],v230 offset:6144
MM v[76:79],a[188:191],v[220:223],v[76:79]
MM v[68:71],a[188:191],v[216:219],v[68:71]
TR a[168:169],v230 offset:8192
TR a[170:171],v230 offset:10240
MM v[68:71],a[184:187],v[200:203],v[68:71]
buffer_load_dwordx4 a[60:63], v248, s[20:23], s92 offen offset:1024 sc1
MM v[68:71],a[180:183],v[184:187],v[68:71]
TR a[172:173],v230 offset:12288
TR a[174:175],v230 offset:14336
MM v[68:71],a[176:179],v[168:171],v[68:71]
s_waitcnt lgkmcnt(15)
MM v[96:99],a[128:131],v[168:171],v[96:99]
TR a[176:177],v231 offset:0
TR a[178:179],v231 offset:2048
MM v[96:99],a[132:135],v[184:187],v[96:99]
buffer_load_dwordx4 a[92:95], v248, s[20:23], s92 offen offset:2048 sc1
MM v[96:99],a[136:139],v[200:203],v[96:99]
TR a[180:181],v231 offset:4096
TR a[182:183],v231 offset:6144
MM v[96:99],a[140:143],v[216:219],v[96:99]
MM v[104:107],a[140:143],v[220:223],v[104:107]
TR a[184:185],v231 offset:8192
TR a[186:187],v231 offset:10240
MM v[104:107],a[136:139],v[204:207],v[104:107]
buffer_load_dwordx4 a[124:127], v248, s[20:23], s92 offen offset:3072 sc1
MM v[104:107],a[132:135],v[188:191],v[104:107]
s_xor_b32 s92, s49, 3
TR a[188:189],v231 offset:12288
TR a[190:191],v231 offset:14336
MM v[104:107],a[128:131],v[172:175],v[104:107]
s_waitcnt lgkmcnt(15)
s_mul_i32 s92, s92, 17408
s_add_u32 s92, s83, s92
s_barrier
MM v[108:111],a[144:147],v[172:175],v[108:111]
s_add_u32 s92, s92, 8192
TR a[128:129],v244 offset:8192
TR a[130:131],v244 offset:9216
MM v[108:111],a[148:151],v[188:191],v[108:111]
buffer_load_dwordx4 a[200:203], v248, s[20:23], s92 offen offset:0 sc1
MM v[108:111],a[152:155],v[204:207],v[108:111]
TR a[132:133],v244 offset:10240
TR a[134:135],v244 offset:11264
MM v[108:111],a[156:159],v[220:223],v[108:111]
MM v[100:103],a[156:159],v[216:219],v[100:103]
TR a[136:137],v244 offset:12288
TR a[138:139],v244 offset:13312
MM v[100:103],a[152:155],v[200:203],v[100:103]
buffer_load_dwordx4 a[216:219], v248, s[20:23], s92 offen offset:1024 sc1
MM v[100:103],a[148:151],v[184:187],v[100:103]
TR a[140:141],v244 offset:14336
TR a[142:143],v244 offset:15360
MM v[100:103],a[144:147],v[168:171],v[100:103]
s_waitcnt lgkmcnt(15)
MM v[32:35],a[160:163],v[168:171],v[32:35]
TR a[144:145],v235 offset:8192
TR a[146:147],v235 offset:9216
MM v[32:35],a[164:167],v[184:187],v[32:35]
buffer_load_dwordx4 a[232:235], v248, s[20:23], s92 offen offset:2048 sc1
MM v[32:35],a[168:171],v[200:203],v[32:35]
TR a[148:149],v235 offset:10240
TR a[150:151],v235 offset:11264
MM v[32:35],a[172:175],v[216:219],v[32:35]
MM v[40:43],a[172:175],v[220:223],v[40:43]
TR a[152:153],v235 offset:12288
TR a[154:155],v235 offset:13312
MM v[40:43],a[168:171],v[204:207],v[40:43]
buffer_load_dwordx4 a[248:251], v248, s[20:23], s92 offen offset:3072 sc1
MM v[40:43],a[164:167],v[188:191],v[40:43]
s_add_u32 s92, s92, 4096
TR a[156:157],v235 offset:14336
TR a[158:159],v235 offset:15360
MM v[40:43],a[160:163],v[172:175],v[40:43]
s_waitcnt lgkmcnt(15)
MM v[44:47],a[176:179],v[172:175],v[44:47]
TR a[160:161],v144 offset:16384
TR a[162:163],v144 offset:18432
MM v[44:47],a[180:183],v[188:191],v[44:47]
buffer_load_dwordx4 a[204:207], v248, s[20:23], s92 offen offset:0 sc1
MM v[44:47],a[184:187],v[204:207],v[44:47]
TR a[164:165],v144 offset:20480
TR a[166:167],v144 offset:22528
MM v[44:47],a[188:191],v[220:223],v[44:47]
MM v[36:39],a[188:191],v[216:219],v[36:39]
TR a[168:169],v144 offset:24576
TR a[170:171],v144 offset:26624
MM v[36:39],a[184:187],v[200:203],v[36:39]
buffer_load_dwordx4 a[220:223], v248, s[20:23], s92 offen offset:1024 sc1
MM v[36:39],a[180:183],v[184:187],v[36:39]
TR a[172:173],v144 offset:28672
TR a[174:175],v144 offset:30720
MM v[36:39],a[176:179],v[168:171],v[36:39]
s_waitcnt lgkmcnt(15)
s_waitcnt vmcnt(24)
MM v[128:131],a[128:131],a[16:19],v[128:131]
TR a[176:177],v145 offset:16384
TR a[178:179],v145 offset:18432
MM v[128:131],a[132:135],a[48:51],v[128:131]
buffer_load_dwordx4 a[236:239], v248, s[20:23], s92 offen offset:2048 sc1
MM v[128:131],a[136:139],a[80:83],v[128:131]
TR a[180:181],v145 offset:20480
TR a[182:183],v145 offset:22528
MM v[128:131],a[140:143],a[112:115],v[128:131]
s_waitcnt vmcnt(21)
MM v[136:139],a[140:143],a[116:119],v[136:139]
TR a[184:185],v145 offset:24576
TR a[186:187],v145 offset:26624
MM v[136:139],a[136:139],a[84:87],v[136:139]
buffer_load_dwordx4 a[252:255], v248, s[20:23], s92 offen offset:3072 sc1
MM v[136:139],a[132:135],a[52:55],v[136:139]
TR a[188:189],v145 offset:28672
TR a[190:191],v145 offset:30720
MM v[136:139],a[128:131],a[20:23],v[136:139]
s_waitcnt lgkmcnt(15)
MM v[140:143],a[144:147],a[20:23],v[140:143]
TR a[128:129],v146 offset:16384
TR a[130:131],v146 offset:18432
MM v[140:143],a[148:151],a[52:55],v[140:143]
MM v[140:143],a[152:155],a[84:87],v[140:143]
TR a[132:133],v146 offset:20480
TR a[134:135],v146 offset:22528
MM v[140:143],a[156:159],a[116:119],v[140:143]
MM v[132:135],a[156:159],a[112:115],v[132:135]
TR a[136:137],v146 offset:24576
TR a[138:139],v146 offset:26624
MM v[132:135],a[152:155],a[80:83],v[132:135]
MM v[132:135],a[148:151],a[48:51],v[132:135]
TR a[140:141],v146 offset:28672
TR a[142:143],v146 offset:30720
MM v[132:135],a[144:147],a[16:19],v[132:135]
s_waitcnt lgkmcnt(15)
MM v[64:67],a[160:163],a[16:19],v[64:67]
TR a[144:145],v147 offset:16384
TR a[146:147],v147 offset:18432
MM v[64:67],a[164:167],a[48:51],v[64:67]
MM v[64:67],a[168:171],a[80:83],v[64:67]
TR a[148:149],v147 offset:20480
TR a[150:151],v147 offset:22528
MM v[64:67],a[172:175],a[112:115],v[64:67]
MM v[72:75],a[172:175],a[116:119],v[72:75]
TR a[152:153],v147 offset:24576
TR a[154:155],v147 offset:26624
MM v[72:75],a[168:171],a[84:87],v[72:75]
MM v[72:75],a[164:167],a[52:55],v[72:75]
TR a[156:157],v147 offset:28672
TR a[158:159],v147 offset:30720
MM v[72:75],a[160:163],a[20:23],v[72:75]
s_waitcnt lgkmcnt(15)
s_waitcnt vmcnt(16)
s_barrier
MM v[76:79],a[176:179],a[20:23],v[76:79]
s_xor_b32 s66, s94, 1
TR a[160:161],v244 offset:16384
TR a[162:163],v244 offset:17408
MM v[76:79],a[180:183],a[52:55],v[76:79]
s_mul_i32 s66, s66, 163840
s_add_u32 s66, s66, s86
s_add_u32 m0, s65, 16384
MM v[76:79],a[184:187],a[84:87],v[76:79]
TR a[164:165],v244 offset:18432
TR a[166:167],v244 offset:19456
MM v[76:79],a[188:191],a[116:119],v[76:79]
buffer_load_dwordx4 v240, s[24:27], s66 offen offset:0 lds
MM v[68:71],a[188:191],a[112:115],v[68:71]
s_add_u32 s66, s66, 9216
TR a[168:169],v244 offset:20480
TR a[170:171],v244 offset:21504
MM v[68:71],a[184:187],a[80:83],v[68:71]
MM v[68:71],a[180:183],a[48:51],v[68:71]
TR a[172:173],v244 offset:22528
TR a[174:175],v244 offset:23552
MM v[68:71],a[176:179],a[16:19],v[68:71]
s_waitcnt lgkmcnt(15)
buffer_load_dwordx4 v240, s[24:27], s66 offen offset:1024 lds
MM v[96:99],a[128:131],a[16:19],v[96:99]
s_add_u32 s66, s66, 9216
TR a[176:177],v244 offset:24576
TR a[178:179],v244 offset:25600
MM v[96:99],a[132:135],a[48:51],v[96:99]
MM v[96:99],a[136:139],a[80:83],v[96:99]
TR a[180:181],v244 offset:26624
TR a[182:183],v244 offset:27648
MM v[96:99],a[140:143],a[112:115],v[96:99]
buffer_load_dwordx4 v240, s[24:27], s66 offen offset:2048 lds
MM v[104:107],a[140:143],a[116:119],v[104:107]
s_add_u32 s66, s66, 9216
TR a[184:185],v244 offset:28672
TR a[186:187],v244 offset:29696
MM v[104:107],a[136:139],a[84:87],v[104:107]
MM v[104:107],a[132:135],a[52:55],v[104:107]
TR a[188:189],v244 offset:30720
TR a[190:191],v244 offset:31744
MM v[104:107],a[128:131],a[20:23],v[104:107]
s_waitcnt lgkmcnt(15)
buffer_load_dwordx4 v240, s[24:27], s66 offen offset:3072 lds
MM v[108:111],a[144:147],a[20:23],v[108:111]
TR a[128:129],v235 offset:16384
TR a[130:131],v235 offset:17408
MM v[108:111],a[148:151],a[52:55],v[108:111]
s_xor_b32 s66, s94, 2
s_mul_i32 s66, s66, 163840
s_add_u32 s66, s66, s86
MM v[108:111],a[152:155],a[84:87],v[108:111]
s_add_u32 m0, s65, 32768
TR a[132:133],v235 offset:18432
TR a[134:135],v235 offset:19456
MM v[108:111],a[156:159],a[116:119],v[108:111]
buffer_load_dwordx4 v240, s[24:27], s66 offen offset:0 lds
MM v[100:103],a[156:159],a[112:115],v[100:103]
s_add_u32 s66, s66, 9216
TR a[136:137],v235 offset:20480
TR a[138:139],v235 offset:21504
MM v[100:103],a[152:155],a[80:83],v[100:103]
MM v[100:103],a[148:151],a[48:51],v[100:103]
TR a[140:141],v235 offset:22528
TR a[142:143],v235 offset:23552
MM v[100:103],a[144:147],a[16:19],v[100:103]
s_waitcnt lgkmcnt(15)
buffer_load_dwordx4 v240, s[24:27], s66 offen offset:1024 lds
s_waitcnt vmcnt(18)
MM v[128:131],a[160:163],a[24:27],v[128:131]
s_add_u32 s66, s66, 9216
TR a[144:145],v235 offset:24576
TR a[146:147],v235 offset:25600
MM v[128:131],a[164:167],a[56:59],v[128:131]
MM v[128:131],a[168:171],a[88:91],v[128:131]
TR a[148:149],v235 offset:26624
TR a[150:151],v235 offset:27648
MM v[128:131],a[172:175],a[120:123],v[128:131]
s_waitcnt vmcnt(14)
buffer_load_dwordx4 v240, s[24:27], s66 offen offset:2048 lds
)ASM"
R"ASM(
MM v[136:139],a[172:175],a[124:127],v[136:139]
s_add_u32 s66, s66, 9216
TR a[152:153],v235 offset:28672
TR a[154:155],v235 offset:29696
MM v[136:139],a[168:171],a[92:95],v[136:139]
MM v[136:139],a[164:167],a[60:63],v[136:139]
TR a[156:157],v235 offset:30720
TR a[158:159],v235 offset:31744
MM v[136:139],a[160:163],a[28:31],v[136:139]
s_waitcnt lgkmcnt(15)
buffer_load_dwordx4 v240, s[24:27], s66 offen offset:3072 lds
s_waitcnt vmcnt(12)
MM v[128:131],a[176:179],a[200:203],v[128:131]
s_memrealtime s[56:57]
TR a[160:161],v144 offset:32768
TR a[162:163],v144 offset:34816
MM v[128:131],a[180:183],a[216:219],v[128:131]
s_xor_b32 s66, s94, 3
s_mul_i32 s66, s66, 163840
s_add_u32 s66, s66, s86
MM v[128:131],a[184:187],a[232:235],v[128:131]
s_add_u32 m0, s65, 49152
TR a[164:165],v144 offset:36864
TR a[166:167],v144 offset:38912
MM v[128:131],a[188:191],a[248:251],v[128:131]
s_waitcnt vmcnt(8)
buffer_load_dwordx4 v240, s[24:27], s66 offen offset:0 lds
MM v[136:139],a[188:191],a[252:255],v[136:139]
s_add_u32 s66, s66, 9216
TR a[168:169],v144 offset:40960
TR a[170:171],v144 offset:43008
MM v[136:139],a[184:187],a[236:239],v[136:139]
MM v[136:139],a[180:183],a[220:223],v[136:139]
TR a[172:173],v144 offset:45056
TR a[174:175],v144 offset:47104
MM v[136:139],a[176:179],a[204:207],v[136:139]
s_waitcnt lgkmcnt(0)
buffer_load_dwordx4 v240, s[24:27], s66 offen offset:1024 lds
MM v[140:143],a[128:131],a[28:31],v[140:143]
s_add_u32 s66, s66, 9216
TR a[176:177],v144 offset:49152
TR a[178:179],v144 offset:51200
MM v[140:143],a[132:135],a[60:63],v[140:143]
v_mov_b32 v158, s56
v_mov_b32 v159, s69
MM v[140:143],a[136:139],a[92:95],v[140:143]
TR a[180:181],v144 offset:53248
TR a[182:183],v144 offset:55296
MM v[140:143],a[140:143],a[124:127],v[140:143]
s_add_u32 s67, s95, 0
CV v128,v128,v129
CV v129,v130,v131
MM v[132:135],a[140:143],a[120:123],v[132:135]
buffer_load_dwordx4 v240, s[24:27], s66 offen offset:2048 lds
s_mov_b64 exec, 1
buffer_store_dwordx2 v[158:159], off, s[20:23], s19 offset:8 sc1
s_mov_b64 exec, -1
s_nop 0
TR a[184:185],v144 offset:57344
TR a[186:187],v144 offset:59392
MM v[132:135],a[136:139],a[88:91],v[132:135]
s_add_u32 s66, s66, 9216
CV v136,v136,v137
CV v137,v138,v139
MM v[132:135],a[132:135],a[56:59],v[132:135]
TR a[188:189],v144 offset:61440
TR a[190:191],v144 offset:63488
MM v[132:135],a[128:131],a[24:27],v[132:135]
buffer_load_dwordx4 v240, s[24:27], s66 offen offset:3072 lds
MM v[132:135],a[144:147],a[200:203],v[132:135]
TR a[128:129],v145 offset:32768
TR a[130:131],v145 offset:34816
MM v[132:135],a[148:151],a[216:219],v[132:135]
MM v[132:135],a[152:155],a[232:235],v[132:135]
TR a[132:133],v145 offset:36864
TR a[134:135],v145 offset:38912
MM v[132:135],a[156:159],a[248:251],v[132:135]
MM v[140:143],a[156:159],a[252:255],v[140:143]
TR a[136:137],v145 offset:40960
TR a[138:139],v145 offset:43008
MM v[140:143],a[152:155],a[236:239],v[140:143]
LPG28_%=:
s_cbranch_execz LTR1_%=
MM v[140:143],a[148:151],a[220:223],v[140:143]
TR a[140:141],v145 offset:45056
TR a[142:143],v145 offset:47104
MM v[140:143],a[144:147],a[204:207],v[140:143]
CV v130,v132,v133
CV v131,v134,v135
MM v[72:75],a[160:163],a[28:31],v[72:75]
TR a[144:145],v145 offset:49152
TR a[146:147],v145 offset:51200
MM v[72:75],a[164:167],a[60:63],v[72:75]
v_min_u32 v131, -2, v131
MM v[72:75],a[168:171],a[92:95],v[72:75]
TR a[148:149],v145 offset:53248
TR a[150:151],v145 offset:55296
MM v[72:75],a[172:175],a[124:127],v[72:75]
buffer_store_dwordx4 v[128:131], v249, s[20:23], s67 offen offset:0 sc1
MM v[64:67],a[172:175],a[120:123],v[64:67]
TR a[152:153],v145 offset:57344
TR a[154:155],v145 offset:59392
MM v[64:67],a[168:171],a[88:91],v[64:67]
CV v138,v140,v141
CV v139,v142,v143
MM v[64:67],a[164:167],a[56:59],v[64:67]
TR a[156:157],v145 offset:61440
TR a[158:159],v145 offset:63488
MM v[64:67],a[160:163],a[24:27],v[64:67]
s_waitcnt lgkmcnt(15)
v_min_u32 v139, -2, v139
MM v[64:67],a[176:179],a[200:203],v[64:67]
TR a[160:161],v146 offset:32768
TR a[162:163],v146 offset:34816
MM v[64:67],a[180:183],a[216:219],v[64:67]
buffer_store_dwordx4 v[136:139], v249, s[20:23], s67 offen offset:1024 sc1
MM v[64:67],a[184:187],a[232:235],v[64:67]
TR a[164:165],v146 offset:36864
TR a[166:167],v146 offset:38912
MM v[64:67],a[188:191],a[248:251],v[64:67]
MM v[72:75],a[188:191],a[252:255],v[72:75]
TR a[168:169],v146 offset:40960
TR a[170:171],v146 offset:43008
MM v[72:75],a[184:187],a[236:239],v[72:75]
MM v[72:75],a[180:183],a[220:223],v[72:75]
TR a[172:173],v146 offset:45056
TR a[174:175],v146 offset:47104
MM v[72:75],a[176:179],a[204:207],v[72:75]
s_waitcnt lgkmcnt(15)
MM v[76:79],a[128:131],a[28:31],v[76:79]
TR a[176:177],v146 offset:49152
TR a[178:179],v146 offset:51200
MM v[76:79],a[132:135],a[60:63],v[76:79]
MM v[76:79],a[136:139],a[92:95],v[76:79]
TR a[180:181],v146 offset:53248
TR a[182:183],v146 offset:55296
MM v[76:79],a[140:143],a[124:127],v[76:79]
s_add_u32 s67, s95, 4096
CV v64,v64,v65
CV v65,v66,v67
MM v[68:71],a[140:143],a[120:123],v[68:71]
TR a[184:185],v146 offset:57344
TR a[186:187],v146 offset:59392
MM v[68:71],a[136:139],a[88:91],v[68:71]
CV v72,v72,v73
CV v73,v74,v75
MM v[68:71],a[132:135],a[56:59],v[68:71]
TR a[188:189],v146 offset:61440
TR a[190:191],v146 offset:63488
MM v[68:71],a[128:131],a[24:27],v[68:71]
s_waitcnt lgkmcnt(15)
MM v[68:71],a[144:147],a[200:203],v[68:71]
TR a[128:129],v147 offset:32768
TR a[130:131],v147 offset:34816
MM v[68:71],a[148:151],a[216:219],v[68:71]
buffer_load_dwordx2 v[148:149], v253, s[20:23], s68 offen offset:8 sc1
MM v[68:71],a[152:155],a[232:235],v[68:71]
TR a[132:133],v147 offset:36864
TR a[134:135],v147 offset:38912
MM v[68:71],a[156:159],a[248:251],v[68:71]
MM v[76:79],a[156:159],a[252:255],v[76:79]
TR a[136:137],v147 offset:40960
TR a[138:139],v147 offset:43008
MM v[76:79],a[152:155],a[236:239],v[76:79]
MM v[76:79],a[148:151],a[220:223],v[76:79]
TR a[140:141],v147 offset:45056
TR a[142:143],v147 offset:47104
MM v[76:79],a[144:147],a[204:207],v[76:79]
s_waitcnt lgkmcnt(15)
CV v66,v68,v69
CV v67,v70,v71
s_waitcnt vmcnt(3)
s_barrier
MM v[104:107],a[160:163],a[28:31],v[104:107]
TR a[144:145],v147 offset:49152
TR a[146:147],v147 offset:51200
MM v[104:107],a[164:167],a[60:63],v[104:107]
v_min_u32 v67, -2, v67
MM v[104:107],a[168:171],a[92:95],v[104:107]
TR a[148:149],v147 offset:53248
TR a[150:151],v147 offset:55296
MM v[104:107],a[172:175],a[124:127],v[104:107]
buffer_store_dwordx4 v[64:67], v248, s[20:23], s67 offen offset:0 sc1
MM v[96:99],a[172:175],a[120:123],v[96:99]
TR a[152:153],v147 offset:57344
TR a[154:155],v147 offset:59392
MM v[96:99],a[168:171],a[88:91],v[96:99]
CV v74,v76,v77
CV v75,v78,v79
MM v[96:99],a[164:167],a[56:59],v[96:99]
TR a[156:157],v147 offset:61440
TR a[158:159],v147 offset:63488
MM v[96:99],a[160:163],a[24:27],v[96:99]
s_waitcnt lgkmcnt(15)
v_min_u32 v75, -2, v75
MM v[96:99],a[176:179],a[200:203],v[96:99]
TR a[160:161],v230 offset:16384
TR a[162:163],v230 offset:18432
MM v[96:99],a[180:183],a[216:219],v[96:99]
buffer_store_dwordx4 v[72:75], v248, s[20:23], s67 offen offset:1024 sc1
MM v[96:99],a[184:187],a[232:235],v[96:99]
TR a[164:165],v230 offset:20480
TR a[166:167],v230 offset:22528
MM v[96:99],a[188:191],a[248:251],v[96:99]
MM v[104:107],a[188:191],a[252:255],v[104:107]
TR a[168:169],v230 offset:24576
TR a[170:171],v230 offset:26624
MM v[104:107],a[184:187],a[236:239],v[104:107]
MM v[104:107],a[180:183],a[220:223],v[104:107]
TR a[172:173],v230 offset:28672
TR a[174:175],v230 offset:30720
MM v[104:107],a[176:179],a[204:207],v[104:107]
s_waitcnt lgkmcnt(15)
MM v[108:111],a[128:131],a[28:31],v[108:111]
TR a[176:177],v230 offset:32768
TR a[178:179],v230 offset:34816
MM v[108:111],a[132:135],a[60:63],v[108:111]
MM v[108:111],a[136:139],a[92:95],v[108:111]
TR a[180:181],v230 offset:36864
TR a[182:183],v230 offset:38912
MM v[108:111],a[140:143],a[124:127],v[108:111]
s_add_u32 s67, s95, 4096
CV v96,v96,v97
CV v97,v98,v99
MM v[100:103],a[140:143],a[120:123],v[100:103]
TR a[184:185],v230 offset:40960
TR a[186:187],v230 offset:43008
MM v[100:103],a[136:139],a[88:91],v[100:103]
CV v104,v104,v105
CV v105,v106,v107
MM v[100:103],a[132:135],a[56:59],v[100:103]
TR a[188:189],v230 offset:45056
TR a[190:191],v230 offset:47104
MM v[100:103],a[128:131],a[24:27],v[100:103]
s_waitcnt lgkmcnt(15)
MM v[100:103],a[144:147],a[200:203],v[100:103]
TR a[128:129],v230 offset:49152
TR a[130:131],v230 offset:51200
MM v[100:103],a[148:151],a[216:219],v[100:103]
MM v[100:103],a[152:155],a[232:235],v[100:103]
TR a[132:133],v230 offset:53248
TR a[134:135],v230 offset:55296
MM v[100:103],a[156:159],a[248:251],v[100:103]
MM v[108:111],a[156:159],a[252:255],v[108:111]
TR a[136:137],v230 offset:57344
TR a[138:139],v230 offset:59392
MM v[108:111],a[152:155],a[236:239],v[108:111]
MM v[108:111],a[148:151],a[220:223],v[108:111]
TR a[140:141],v230 offset:61440
TR a[142:143],v230 offset:63488
MM v[108:111],a[144:147],a[204:207],v[108:111]
s_waitcnt lgkmcnt(15)
CV v98,v100,v101
CV v99,v102,v103
MM v[40:43],a[160:163],a[20:23],v[40:43]
TR a[144:145],v231 offset:16384
TR a[146:147],v231 offset:18432
MM v[40:43],a[164:167],a[52:55],v[40:43]
v_min_u32 v99, -2, v99
v_lshlrev_b32 v136, 16, v150
MM v[40:43],a[168:171],a[84:87],v[40:43]
TR a[148:149],v231 offset:20480
TR a[150:151],v231 offset:22528
MM v[40:43],a[172:175],a[116:119],v[40:43]
buffer_store_dwordx4 v[96:99], v248, s[20:23], s67 offen offset:2048 sc1
MM v[32:35],a[172:175],a[112:115],v[32:35]
TR a[152:153],v231 offset:24576
TR a[154:155],v231 offset:26624
MM v[32:35],a[168:171],a[80:83],v[32:35]
CV v106,v108,v109
CV v107,v110,v111
MM v[32:35],a[164:167],a[48:51],v[32:35]
TR a[156:157],v231 offset:28672
TR a[158:159],v231 offset:30720
MM v[32:35],a[160:163],a[16:19],v[32:35]
s_waitcnt lgkmcnt(15)
v_min_u32 v107, -2, v107
v_and_b32 v137, 0xffff0000, v150
MM v[32:35],a[176:179],a[24:27],v[32:35]
TR a[160:161],v231 offset:32768
TR a[162:163],v231 offset:34816
MM v[32:35],a[180:183],a[56:59],v[32:35]
buffer_store_dwordx4 v[104:107], v248, s[20:23], s67 offen offset:3072 sc1
MM v[32:35],a[184:187],a[88:91],v[32:35]
TR a[164:165],v231 offset:36864
TR a[166:167],v231 offset:38912
MM v[32:35],a[188:191],a[120:123],v[32:35]
v_lshlrev_b32 v138, 16, v151
v_and_b32 v139, 0xffff0000, v151
MM v[40:43],a[188:191],a[124:127],v[40:43]
TR a[168:169],v231 offset:40960
TR a[170:171],v231 offset:43008
MM v[40:43],a[184:187],a[92:95],v[40:43]
v_mul_f32 v140, 0xbfb8aa3b, v136
v_mul_f32 v141, 0xbfb8aa3b, v137
MM v[40:43],a[180:183],a[60:63],v[40:43]
TR a[172:173],v231 offset:45056
TR a[174:175],v231 offset:47104
MM v[40:43],a[176:179],a[28:31],v[40:43]
s_waitcnt lgkmcnt(15)
v_mul_f32 v142, 0xbfb8aa3b, v138
v_mul_f32 v143, 0xbfb8aa3b, v139
MM v[40:43],a[128:131],a[204:207],v[40:43]
TR a[176:177],v231 offset:49152
TR a[178:179],v231 offset:51200
MM v[40:43],a[132:135],a[220:223],v[40:43]
EX v140,v140
MM v[40:43],a[136:139],a[236:239],v[40:43]
TR a[180:181],v231 offset:53248
TR a[182:183],v231 offset:55296
MM v[40:43],a[140:143],a[252:255],v[40:43]
EX v141,v141
MM v[32:35],a[140:143],a[248:251],v[32:35]
TR a[184:185],v231 offset:57344
TR a[186:187],v231 offset:59392
MM v[32:35],a[136:139],a[232:235],v[32:35]
EX v142,v142
MM v[32:35],a[132:135],a[216:219],v[32:35]
TR a[188:189],v231 offset:61440
TR a[190:191],v231 offset:63488
MM v[32:35],a[128:131],a[200:203],v[32:35]
s_waitcnt lgkmcnt(15)
EX v143,v143
MM v[36:39],a[144:147],a[16:19],v[36:39]
TR a[128:129],v228 offset:0
TR a[130:131],v228 offset:2048
MM v[36:39],a[148:151],a[48:51],v[36:39]
v_add_f32 v140, 1.0, v140
v_add_f32 v141, 1.0, v141
MM v[36:39],a[152:155],a[80:83],v[36:39]
TR a[132:133],v228 offset:4096
TR a[134:135],v228 offset:6144
MM v[36:39],a[156:159],a[112:115],v[36:39]
s_add_u32 s67, s95, 0
CV v32,v32,v33
CV v33,v34,v35
MM v[44:47],a[156:159],a[116:119],v[44:47]
TR a[136:137],v228 offset:8192
TR a[138:139],v228 offset:10240
MM v[44:47],a[152:155],a[84:87],v[44:47]
CV v40,v40,v41
CV v41,v42,v43
MM v[44:47],a[148:151],a[52:55],v[44:47]
TR a[140:141],v228 offset:12288
TR a[142:143],v228 offset:14336
MM v[44:47],a[144:147],a[20:23],v[44:47]
)ASM"
R"ASM(
s_waitcnt lgkmcnt(15)
v_add_f32 v142, 1.0, v142
v_add_f32 v143, 1.0, v143
MM v[44:47],a[160:163],a[28:31],v[44:47]
s_waitcnt vmcnt(4)
TR a[144:145],v228 offset:16384
TR a[146:147],v228 offset:18432
MM v[44:47],a[164:167],a[60:63],v[44:47]
v_cmp_eq_u32 s[8:9], s69, v149
v_subrev_u32 v226, s56, v148
MM v[44:47],a[168:171],a[92:95],v[44:47]
TR a[148:149],v228 offset:20480
TR a[150:151],v228 offset:22528
MM v[44:47],a[172:175],a[124:127],v[44:47]
v_rcp_f32 v140, v140
MM v[36:39],a[172:175],a[120:123],v[36:39]
TR a[152:153],v228 offset:24576
TR a[154:155],v228 offset:26624
v_cndmask_b32 v226, 4.0, v226, s[8:9]
MM v[36:39],a[168:171],a[88:91],v[36:39]
v_readlane_b32 s13, v226, 16
v_readlane_b32 s14, v226, 32
MM v[36:39],a[164:167],a[56:59],v[36:39]
TR a[156:157],v228 offset:28672
TR a[158:159],v228 offset:30720
MM v[36:39],a[160:163],a[24:27],v[36:39]
s_waitcnt lgkmcnt(15)
v_rcp_f32 v141, v141
v_readlane_b32 s15, v226, 48
MM v[36:39],a[176:179],a[200:203],v[36:39]
s_max_i32 s12, s13, s14
TR a[160:161],v228 offset:32768
TR a[162:163],v228 offset:34816
MM v[36:39],a[180:183],a[216:219],v[36:39]
s_max_i32 s12, s12, s15
v_rcp_f32 v142, v142
MM v[36:39],a[184:187],a[232:235],v[36:39]
s_max_i32 s12, s12, 0
TR a[164:165],v228 offset:36864
TR a[166:167],v228 offset:38912
MM v[36:39],a[188:191],a[248:251],v[36:39]
buffer_load_dwordx2 v[148:149], v253, s[20:23], s68 offen offset:8 sc1
MM v[44:47],a[188:191],a[252:255],v[44:47]
TR a[168:169],v228 offset:40960
TR a[170:171],v228 offset:43008
MM v[44:47],a[184:187],a[236:239],v[44:47]
v_rcp_f32 v143, v143
MM v[44:47],a[180:183],a[220:223],v[44:47]
TR a[172:173],v228 offset:45056
TR a[174:175],v228 offset:47104
MM v[44:47],a[176:179],a[204:207],v[44:47]
s_waitcnt lgkmcnt(15)
CV v34,v36,v37
CV v35,v38,v39
v_min_u32 v35, -2, v35
buffer_store_dwordx4 v[32:35], v248, s[20:23], s67 offen offset:2048 sc1
MM v[8:11],a[128:131],v[172:175],v[8:11]
TR a[176:177],v228 offset:49152
TR a[178:179],v228 offset:51200
MM v[8:11],a[132:135],v[188:191],v[8:11]
v_mul_f32 v140, v136, v140
v_mul_f32 v141, v137, v141
MM v[8:11],a[136:139],v[204:207],v[8:11]
TR a[180:181],v228 offset:53248
TR a[182:183],v228 offset:55296
MM v[8:11],a[140:143],v[220:223],v[8:11]
CV v42,v44,v45
CV v43,v46,v47
v_min_u32 v43, -2, v43
buffer_store_dwordx4 v[40:43], v248, s[20:23], s67 offen offset:3072 sc1
MM v[0:3],a[140:143],v[216:219],v[0:3]
TR a[184:185],v228 offset:57344
TR a[186:187],v228 offset:59392
MM v[0:3],a[136:139],v[200:203],v[0:3]
v_mul_f32 v142, v138, v142
v_mul_f32 v143, v139, v143
MM v[0:3],a[132:135],v[184:187],v[0:3]
TR a[188:189],v228 offset:61440
TR a[190:191],v228 offset:63488
MM v[0:3],a[128:131],v[168:171],v[0:3]
s_waitcnt lgkmcnt(15)
CV v150,v140,v141
CV v151,v142,v143
MM v[0:3],a[144:147],a[16:19],v[0:3]
TR a[128:129],v229 offset:0
TR a[130:131],v229 offset:2048
MM v[0:3],a[148:151],a[48:51],v[0:3]
s_cmp_le_i32 s12, 76
s_cselect_b32 s60, 1, 0
s_cselect_b64 s[8:9], -1, 0
MM v[0:3],a[152:155],a[80:83],v[0:3]
s_or_b32 s58, s58, s60
TR a[132:133],v229 offset:4096
TR a[134:135],v229 offset:6144
MM v[0:3],a[156:159],a[112:115],v[0:3]
s_mov_b64 exec, s[8:9]
buffer_load_dwordx4 v[56:59], v250, s[20:23], s39 offen offset:0 sc1
s_mov_b64 exec, -1
MM v[8:11],a[156:159],a[116:119],v[8:11]
TR a[136:137],v229 offset:8192
TR a[138:139],v229 offset:10240
MM v[8:11],a[152:155],a[84:87],v[8:11]
s_mov_b64 exec, s[8:9]
buffer_load_dwordx4 v[60:63], v250, s[20:23], s39 offen offset:256 sc1
s_mov_b64 exec, -1
MM v[8:11],a[148:151],a[52:55],v[8:11]
TR a[140:141],v229 offset:12288
TR a[142:143],v229 offset:14336
MM v[8:11],a[144:147],a[20:23],v[8:11]
s_waitcnt lgkmcnt(15)
v_lshlrev_b32 v160, 16, v80
v_and_b32 v161, 0xffff0000, v80
MM v[8:11],a[160:163],a[28:31],v[8:11]
TR a[144:145],v229 offset:16384
TR a[146:147],v229 offset:18432
MM v[8:11],a[164:167],a[60:63],v[8:11]
s_cmp_le_i32 s14, 51
s_cselect_b32 s60, 4, 0
s_cselect_b64 s[8:9], -1, 0
MM v[8:11],a[168:171],a[92:95],v[8:11]
s_or_b32 s58, s58, s60
TR a[148:149],v229 offset:20480
TR a[150:151],v229 offset:22528
MM v[8:11],a[172:175],a[124:127],v[8:11]
s_mov_b64 exec, s[8:9]
buffer_load_dwordx4 v[24:27], v248, s[20:23], s37 offen offset:0 sc1
s_mov_b64 exec, -1
MM v[0:3],a[172:175],a[120:123],v[0:3]
TR a[152:153],v229 offset:24576
TR a[154:155],v229 offset:26624
MM v[0:3],a[168:171],a[88:91],v[0:3]
s_mov_b64 exec, s[8:9]
buffer_load_dwordx4 v[28:31], v248, s[20:23], s37 offen offset:1024 sc1
s_mov_b64 exec, -1
MM v[0:3],a[164:167],a[56:59],v[0:3]
TR a[156:157],v229 offset:28672
TR a[158:159],v229 offset:30720
MM v[0:3],a[160:163],a[24:27],v[0:3]
s_waitcnt lgkmcnt(15)
v_lshlrev_b32 v162, 16, v81
v_and_b32 v163, 0xffff0000, v81
MM v[0:3],a[176:179],a[200:203],v[0:3]
TR a[160:161],v229 offset:32768
TR a[162:163],v229 offset:34816
MM v[0:3],a[180:183],a[216:219],v[0:3]
s_cmp_le_i32 s15, 25
s_cselect_b32 s60, 8, 0
s_cselect_b64 s[8:9], -1, 0
MM v[0:3],a[184:187],a[232:235],v[0:3]
s_or_b32 s58, s58, s60
TR a[164:165],v229 offset:36864
TR a[166:167],v229 offset:38912
MM v[0:3],a[188:191],a[248:251],v[0:3]
s_mov_b64 exec, s[8:9]
buffer_load_dwordx4 v[48:51], v248, s[20:23], s38 offen offset:0 sc1
s_mov_b64 exec, -1
MM v[8:11],a[188:191],a[252:255],v[8:11]
TR a[168:169],v229 offset:40960
TR a[170:171],v229 offset:43008
MM v[8:11],a[184:187],a[236:239],v[8:11]
s_mov_b64 exec, s[8:9]
buffer_load_dwordx4 v[52:55], v248, s[20:23], s38 offen offset:1024 sc1
s_mov_b64 exec, -1
MM v[8:11],a[180:183],a[220:223],v[8:11]
TR a[172:173],v229 offset:45056
TR a[174:175],v229 offset:47104
MM v[8:11],a[176:179],a[204:207],v[8:11]
s_waitcnt lgkmcnt(15)
v_lshlrev_b32 v164, 16, v82
v_and_b32 v165, 0xffff0000, v82
MM v[12:15],a[128:131],v[172:175],v[12:15]
TR a[176:177],v229 offset:49152
TR a[178:179],v229 offset:51200
MM v[12:15],a[132:135],v[188:191],v[12:15]
v_lshlrev_b32 v166, 16, v83
v_and_b32 v167, 0xffff0000, v83
MM v[12:15],a[136:139],v[204:207],v[12:15]
TR a[180:181],v229 offset:53248
TR a[182:183],v229 offset:55296
MM v[12:15],a[140:143],v[220:223],v[12:15]
v_mul_f32 v136, 0xbfb8aa3b, v160
v_mul_f32 v137, 0xbfb8aa3b, v161
MM v[4:7],a[140:143],v[216:219],v[4:7]
TR a[184:185],v229 offset:57344
TR a[186:187],v229 offset:59392
MM v[4:7],a[136:139],v[200:203],v[4:7]
v_mul_f32 v138, 0xbfb8aa3b, v162
v_mul_f32 v139, 0xbfb8aa3b, v163
MM v[4:7],a[132:135],v[184:187],v[4:7]
TR a[188:189],v229 offset:61440
TR a[190:191],v229 offset:63488
MM v[4:7],a[128:131],v[168:171],v[4:7]
s_waitcnt lgkmcnt(15)
v_mul_f32 v140, 0xbfb8aa3b, v164
v_mul_f32 v141, 0xbfb8aa3b, v165
MM v[4:7],a[144:147],a[16:19],v[4:7]
s_cmp_le_i32 s13, -8
s_cselect_b32 s60, 2, 0
s_cselect_b64 s[8:9], -1, 0
MM v[4:7],a[148:151],a[48:51],v[4:7]
s_or_b32 s58, s58, s60
s_mov_b64 exec, s[8:9]
buffer_load_dwordx4 v[16:19], v248, s[20:23], s36 offen offset:0 sc1
s_mov_b64 exec, -1
MM v[4:7],a[152:155],a[80:83],v[4:7]
v_mul_f32 v142, 0xbfb8aa3b, v166
v_mul_f32 v143, 0xbfb8aa3b, v167
MM v[4:7],a[156:159],a[112:115],v[4:7]
s_mov_b64 exec, s[8:9]
buffer_load_dwordx4 v[20:23], v248, s[20:23], s36 offen offset:1024 sc1
s_mov_b64 exec, -1
MM v[12:15],a[156:159],a[116:119],v[12:15]
EX v136,v136
MM v[12:15],a[152:155],a[84:87],v[12:15]
EX v137,v137
MM v[12:15],a[148:151],a[52:55],v[12:15]
EX v138,v138
MM v[12:15],a[144:147],a[20:23],v[12:15]
s_waitcnt lgkmcnt(8)
EX v139,v139
MM v[12:15],a[160:163],a[28:31],v[12:15]
EX v140,v140
MM v[12:15],a[164:167],a[60:63],v[12:15]
EX v141,v141
MM v[12:15],a[168:171],a[92:95],v[12:15]
EX v142,v142
MM v[12:15],a[172:175],a[124:127],v[12:15]
EX v143,v143
MM v[4:7],a[172:175],a[120:123],v[4:7]
v_add_f32 v136, 1.0, v136
v_add_f32 v137, 1.0, v137
MM v[4:7],a[168:171],a[88:91],v[4:7]
v_add_f32 v138, 1.0, v138
v_add_f32 v139, 1.0, v139
MM v[4:7],a[164:167],a[56:59],v[4:7]
v_add_f32 v140, 1.0, v140
v_add_f32 v141, 1.0, v141
MM v[4:7],a[160:163],a[24:27],v[4:7]
s_waitcnt lgkmcnt(0)
v_add_f32 v142, 1.0, v142
v_add_f32 v143, 1.0, v143
MM v[4:7],a[176:179],a[200:203],v[4:7]
v_rcp_f32 v136, v136
MM v[4:7],a[180:183],a[216:219],v[4:7]
v_rcp_f32 v137, v137
MM v[4:7],a[184:187],a[232:235],v[4:7]
v_rcp_f32 v138, v138
MM v[4:7],a[188:191],a[248:251],v[4:7]
v_rcp_f32 v139, v139
MM v[12:15],a[188:191],a[252:255],v[12:15]
v_rcp_f32 v140, v140
MM v[12:15],a[184:187],a[236:239],v[12:15]
v_rcp_f32 v141, v141
MM v[12:15],a[180:183],a[220:223],v[12:15]
v_rcp_f32 v142, v142
MM v[12:15],a[176:179],a[204:207],v[12:15]
v_rcp_f32 v143, v143
s_memrealtime s[54:55]
s_cmp_eq_u32 s58, 15
s_cbranch_scc1 LSK1_%=
s_memrealtime s[98:99]
s_andn2_b32 s91, s17, s58
s_mov_b32 s77, 0x7fffffff
s_waitcnt lgkmcnt(0)
s_sub_u32 s76, s98, s54
s_bitcmp0_b32 s91, 1
s_cbranch_scc1 LSM11_%=
s_sub_u32 s90, s13, s76
s_sub_u32 s90, s90, 12
s_cmp_gt_i32 s90, 0
s_cbranch_scc1 LSM11_%=
buffer_load_dwordx4 v[16:19], v248, s[20:23], s36 offen offset:0 sc1
buffer_load_dwordx4 v[20:23], v248, s[20:23], s36 offen offset:1024 sc1
s_bitset1_b32 s58, 1
LSM11_%=:
s_bitcmp0_b32 s91, 3
s_cbranch_scc1 LSM13_%=
s_sub_u32 s90, s15, s76
s_sub_u32 s90, s90, 63
s_cmp_gt_i32 s90, 0
s_cbranch_scc1 LSM13_%=
buffer_load_dwordx4 v[48:51], v248, s[20:23], s38 offen offset:0 sc1
buffer_load_dwordx4 v[52:55], v248, s[20:23], s38 offen offset:1024 sc1
s_bitset1_b32 s58, 3
LSM13_%=:
s_bitcmp0_b32 s91, 2
s_cbranch_scc1 LSM12_%=
s_sub_u32 s90, s14, s76
s_sub_u32 s90, s90, 97
s_cmp_gt_i32 s90, 0
s_cbranch_scc1 LSM12_%=
buffer_load_dwordx4 v[24:27], v248, s[20:23], s37 offen offset:0 sc1
buffer_load_dwordx4 v[28:31], v248, s[20:23], s37 offen offset:1024 sc1
s_bitset1_b32 s58, 2
LSM12_%=:
s_bitcmp0_b32 s91, 0
s_cbranch_scc1 LSM10_%=
s_sub_u32 s90, s12, s76
s_sub_u32 s90, s90, 131
s_cmp_gt_i32 s90, 0
s_cbranch_scc1 LSM10_%=
buffer_load_dwordx4 v[56:59], v250, s[20:23], s39 offen offset:0 sc1
buffer_load_dwordx4 v[60:63], v250, s[20:23], s39 offen offset:256 sc1
s_bitset1_b32 s58, 0
LSM10_%=:
LSK1_%=:
v_mul_f32 v136, v160, v136
v_mul_f32 v137, v161, v137
v_mul_f32 v138, v162, v138
v_mul_f32 v139, v163, v139
v_mul_f32 v140, v164, v140
v_mul_f32 v141, v165, v141
v_mul_f32 v142, v166, v142
v_mul_f32 v143, v167, v143
CV v80,v136,v137
CV v81,v138,v139
CV v82,v140,v141
CV v83,v142,v143
v_lshlrev_b32 v160, 16, v80
v_and_b32 v161, 0xffff0000, v80
v_lshlrev_b32 v162, 16, v81
v_and_b32 v163, 0xffff0000, v81
v_lshlrev_b32 v164, 16, v82
v_and_b32 v165, 0xffff0000, v82
v_lshlrev_b32 v166, 16, v83
v_and_b32 v167, 0xffff0000, v83
v_lshlrev_b32 v176, 16, v84
v_and_b32 v177, 0xffff0000, v84
v_lshlrev_b32 v178, 16, v85
v_and_b32 v179, 0xffff0000, v85
v_lshlrev_b32 v180, 16, v86
v_and_b32 v181, 0xffff0000, v86
v_lshlrev_b32 v182, 16, v87
v_and_b32 v183, 0xffff0000, v87
LPG29_%=:
s_cbranch_execz LTR1_%=
v_mul_f32 v136, 0xbfb8aa3b, v176
v_mul_f32 v137, 0xbfb8aa3b, v177
v_mul_f32 v138, 0xbfb8aa3b, v178
v_mul_f32 v139, 0xbfb8aa3b, v179
v_mul_f32 v140, 0xbfb8aa3b, v180
v_mul_f32 v141, 0xbfb8aa3b, v181
v_mul_f32 v142, 0xbfb8aa3b, v182
v_mul_f32 v143, 0xbfb8aa3b, v183
EX v136,v136
s_waitcnt vmcnt(10)
v_cmp_eq_u32 s[8:9], s69, v149
v_subrev_u32 v226, s56, v148
s_nop 0
v_cndmask_b32 v226, 4.0, v226, s[8:9]
s_nop 0
v_readlane_b32 s13, v226, 16
v_readlane_b32 s14, v226, 32
v_readlane_b32 s15, v226, 48
s_max_i32 s12, s13, s14
s_max_i32 s12, s12, s15
s_max_i32 s12, s12, 0
s_cmp_eq_u32 s58, 15
s_cbranch_scc1 LSK2_%=
s_memrealtime s[98:99]
s_andn2_b32 s91, s17, s58
s_mov_b32 s77, 0x7fffffff
s_waitcnt lgkmcnt(0)
s_sub_u32 s76, s98, s54
s_bitcmp0_b32 s91, 1
s_cbranch_scc1 LSM21_%=
s_sub_u32 s90, s13, s76
s_sub_u32 s90, s90, 12
s_cmp_gt_i32 s90, 0
s_cbranch_scc1 LSM21_%=
buffer_load_dwordx4 v[16:19], v248, s[20:23], s36 offen offset:0 sc1
buffer_load_dwordx4 v[20:23], v248, s[20:23], s36 offen offset:1024 sc1
s_bitset1_b32 s58, 1
LSM21_%=:
s_bitcmp0_b32 s91, 3
s_cbranch_scc1 LSM23_%=
s_sub_u32 s90, s15, s76
s_sub_u32 s90, s90, 63
s_cmp_gt_i32 s90, 0
s_cbranch_scc1 LSM23_%=
buffer_load_dwordx4 v[48:51], v248, s[20:23], s38 offen offset:0 sc1
buffer_load_dwordx4 v[52:55], v248, s[20:23], s38 offen offset:1024 sc1
s_bitset1_b32 s58, 3
LSM23_%=:
s_bitcmp0_b32 s91, 2
s_cbranch_scc1 LSM22_%=
s_sub_u32 s90, s14, s76
)ASM"
R"ASM(
s_sub_u32 s90, s90, 97
s_cmp_gt_i32 s90, 0
s_cbranch_scc1 LSM22_%=
buffer_load_dwordx4 v[24:27], v248, s[20:23], s37 offen offset:0 sc1
buffer_load_dwordx4 v[28:31], v248, s[20:23], s37 offen offset:1024 sc1
s_bitset1_b32 s58, 2
LSM22_%=:
s_bitcmp0_b32 s91, 0
s_cbranch_scc1 LSM20_%=
s_sub_u32 s90, s12, s76
s_sub_u32 s90, s90, 131
s_cmp_gt_i32 s90, 0
s_cbranch_scc1 LSM20_%=
buffer_load_dwordx4 v[56:59], v250, s[20:23], s39 offen offset:0 sc1
buffer_load_dwordx4 v[60:63], v250, s[20:23], s39 offen offset:256 sc1
s_bitset1_b32 s58, 0
LSM20_%=:
LSK2_%=:
EX v137,v137
EX v138,v138
EX v139,v139
EX v140,v140
EX v141,v141
EX v142,v142
EX v143,v143
v_add_f32 v136, 1.0, v136
v_add_f32 v137, 1.0, v137
v_add_f32 v138, 1.0, v138
v_add_f32 v139, 1.0, v139
v_add_f32 v140, 1.0, v140
v_add_f32 v141, 1.0, v141
v_add_f32 v142, 1.0, v142
v_add_f32 v143, 1.0, v143
v_rcp_f32 v136, v136
v_rcp_f32 v137, v137
v_rcp_f32 v138, v138
v_rcp_f32 v139, v139
v_rcp_f32 v140, v140
v_rcp_f32 v141, v141
v_rcp_f32 v142, v142
v_rcp_f32 v143, v143
s_cmp_eq_u32 s58, 15
s_cbranch_scc1 LSK3_%=
s_memrealtime s[98:99]
s_andn2_b32 s91, s17, s58
s_mov_b32 s77, 0x7fffffff
s_waitcnt lgkmcnt(0)
s_sub_u32 s76, s98, s54
s_bitcmp0_b32 s91, 1
s_cbranch_scc1 LSM31_%=
s_sub_u32 s90, s13, s76
s_sub_u32 s90, s90, 12
s_cmp_gt_i32 s90, 0
s_cbranch_scc1 LSM31_%=
buffer_load_dwordx4 v[16:19], v248, s[20:23], s36 offen offset:0 sc1
buffer_load_dwordx4 v[20:23], v248, s[20:23], s36 offen offset:1024 sc1
s_bitset1_b32 s58, 1
LSM31_%=:
s_bitcmp0_b32 s91, 3
s_cbranch_scc1 LSM33_%=
s_sub_u32 s90, s15, s76
s_sub_u32 s90, s90, 63
s_cmp_gt_i32 s90, 0
s_cbranch_scc1 LSM33_%=
buffer_load_dwordx4 v[48:51], v248, s[20:23], s38 offen offset:0 sc1
buffer_load_dwordx4 v[52:55], v248, s[20:23], s38 offen offset:1024 sc1
s_bitset1_b32 s58, 3
LSM33_%=:
s_bitcmp0_b32 s91, 2
s_cbranch_scc1 LSM32_%=
s_sub_u32 s90, s14, s76
s_sub_u32 s90, s90, 97
s_cmp_gt_i32 s90, 0
s_cbranch_scc1 LSM32_%=
buffer_load_dwordx4 v[24:27], v248, s[20:23], s37 offen offset:0 sc1
buffer_load_dwordx4 v[28:31], v248, s[20:23], s37 offen offset:1024 sc1
s_bitset1_b32 s58, 2
LSM32_%=:
s_bitcmp0_b32 s91, 0
s_cbranch_scc1 LSM30_%=
s_sub_u32 s90, s12, s76
s_sub_u32 s90, s90, 131
s_cmp_gt_i32 s90, 0
s_cbranch_scc1 LSM30_%=
buffer_load_dwordx4 v[56:59], v250, s[20:23], s39 offen offset:0 sc1
buffer_load_dwordx4 v[60:63], v250, s[20:23], s39 offen offset:256 sc1
s_bitset1_b32 s58, 0
LSM30_%=:
LSK3_%=:
v_mul_f32 v136, v176, v136
v_mul_f32 v137, v177, v137
v_mul_f32 v138, v178, v138
v_mul_f32 v139, v179, v139
v_mul_f32 v140, v180, v140
v_mul_f32 v141, v181, v141
v_mul_f32 v142, v182, v142
v_mul_f32 v143, v183, v143
CV v84,v136,v137
CV v85,v138,v139
CV v86,v140,v141
CV v87,v142,v143
v_lshlrev_b32 v176, 16, v84
v_and_b32 v177, 0xffff0000, v84
v_lshlrev_b32 v178, 16, v85
v_and_b32 v179, 0xffff0000, v85
v_lshlrev_b32 v180, 16, v86
v_and_b32 v181, 0xffff0000, v86
v_lshlrev_b32 v182, 16, v87
v_and_b32 v183, 0xffff0000, v87
v_lshlrev_b32 v192, 16, v88
v_and_b32 v193, 0xffff0000, v88
v_lshlrev_b32 v194, 16, v89
v_and_b32 v195, 0xffff0000, v89
v_lshlrev_b32 v196, 16, v90
v_and_b32 v197, 0xffff0000, v90
v_lshlrev_b32 v198, 16, v91
v_and_b32 v199, 0xffff0000, v91
v_lshlrev_b32 v208, 16, v92
v_and_b32 v209, 0xffff0000, v92
v_lshlrev_b32 v210, 16, v93
v_and_b32 v211, 0xffff0000, v93
v_lshlrev_b32 v212, 16, v94
v_and_b32 v213, 0xffff0000, v94
v_lshlrev_b32 v214, 16, v95
v_and_b32 v215, 0xffff0000, v95
s_mov_b32 s73, 30000
s_cmp_lg_u32 s58, 15
s_cbranch_scc1 LSI_%=
s_waitcnt vmcnt(0)
v_max3_u32 v236, v59, v63, v19
v_max3_u32 v237, v23, v27, v31
v_max3_u32 v236, v236, v51, v55
v_max_u32 v236, v236, v237
v_cmp_eq_u32 vcc, -1, v236
s_cbranch_vccnz LSI_%=
LSF_%=:
s_waitcnt lgkmcnt(0)
MM v[152:155],v[112:115],v[56:59],0
MM v[152:155],v[116:119],v[60:63],v[152:155]
MM v[0:3],v[122:125],v[24:27],v[0:3]
v_lshlrev_b32 v128, 16, v150
v_and_b32 v129, 0xffff0000, v150
MM v[0:3],v[122:125],v[48:51],v[0:3]
v_lshlrev_b32 v130, 16, v151
v_and_b32 v131, 0xffff0000, v151
MM v[0:3],v[122:125],v[16:19],v[0:3]
v_lshlrev_b32 v132, 16, v126
v_and_b32 v133, 0xffff0000, v126
MM v[4:7],v[120:123],v[24:27],v[4:7]
v_lshlrev_b32 v134, 16, v127
v_and_b32 v135, 0xffff0000, v127
MM v[4:7],v[120:123],v[48:51],v[4:7]
CV v236,v152,v153
CV v237,v154,v155
MM v[4:7],v[120:123],v[16:19],v[4:7]
ds_bpermute_b32 v236, v232, v236
ds_bpermute_b32 v237, v232, v237
MM v[8:11],v[122:125],v[28:31],v[8:11]
MM v[8:11],v[122:125],v[52:55],v[8:11]
MM v[8:11],v[122:125],v[20:23],v[8:11]
s_add_u32 s80, s18, 20480
MM v[12:15],v[120:123],v[28:31],v[12:15]
MM v[12:15],v[120:123],v[52:55],v[12:15]
CV v32,v0,v1
CV v33,v2,v3
MM v[12:15],v[120:123],v[20:23],v[12:15]
CV v34,v4,v5
CV v35,v6,v7
ds_write2_b64 v224, v[32:33], v[34:35] offset0:0 offset1:4
RD v[40:43],v225 offset:0
CV v36,v8,v9
CV v37,v10,v11
s_nop 1
CV v38,v12,v13
CV v39,v14,v15
ds_write2_b64 v224, v[36:37], v[38:39] offset0:160 offset1:164
RD v[44:47],v225 offset:1280
s_waitcnt lgkmcnt(4)
v_lshlrev_b32 v152, 16, v236
v_and_b32 v153, 0xffff0000, v236
v_lshlrev_b32 v154, 16, v237
v_and_b32 v155, 0xffff0000, v237
v_pk_mul_f32 v[152:153], v[152:153], v[128:129]
v_pk_mul_f32 v[154:155], v[154:155], v[130:131]
CV v236,v152,v153
CV v237,v154,v155
v_lshlrev_b32 v152, 16, v236
v_and_b32 v153, 0xffff0000, v236
v_lshlrev_b32 v154, 16, v237
v_and_b32 v155, 0xffff0000, v237
v_pk_add_f32 v[152:153], v[152:153], v[132:133]
v_pk_add_f32 v[154:155], v[154:155], v[134:135]
CV v238,v152,v153
CV v239,v154,v155
buffer_store_dwordx2 v[238:239], v254, s[40:43], s63 offen offset:0 nt
s_nop 0
s_waitcnt lgkmcnt(2)
v_lshlrev_b32 v0, 16, v40
v_and_b32 v1, 0xffff0000, v40
v_lshlrev_b32 v2, 16, v41
v_and_b32 v3, 0xffff0000, v41
v_lshlrev_b32 v4, 16, v42
v_and_b32 v5, 0xffff0000, v42
v_lshlrev_b32 v6, 16, v43
v_and_b32 v7, 0xffff0000, v43
v_pk_mul_f32 v[0:1], v[0:1], v[160:161]
v_pk_mul_f32 v[2:3], v[2:3], v[162:163]
v_pk_mul_f32 v[4:5], v[4:5], v[164:165]
v_pk_mul_f32 v[6:7], v[6:7], v[166:167]
CV v40,v0,v1
CV v41,v2,v3
CV v42,v4,v5
CV v43,v6,v7
v_lshlrev_b32 v0, 16, v40
v_and_b32 v1, 0xffff0000, v40
v_lshlrev_b32 v2, 16, v41
v_and_b32 v3, 0xffff0000, v41
v_lshlrev_b32 v4, 16, v42
v_and_b32 v5, 0xffff0000, v42
v_lshlrev_b32 v6, 16, v43
v_and_b32 v7, 0xffff0000, v43
v_pk_add_f32 v[0:1], v[0:1], v[192:193]
v_pk_add_f32 v[2:3], v[2:3], v[194:195]
v_pk_add_f32 v[4:5], v[4:5], v[196:197]
v_pk_add_f32 v[6:7], v[6:7], v[198:199]
CV v40,v0,v1
CV v41,v2,v3
CV v42,v4,v5
CV v43,v6,v7
buffer_store_dwordx4 v[40:43], v251, s[40:43], s18 offen offset:0 nt
s_waitcnt lgkmcnt(0)
v_lshlrev_b32 v8, 16, v44
v_and_b32 v9, 0xffff0000, v44
v_lshlrev_b32 v10, 16, v45
v_and_b32 v11, 0xffff0000, v45
v_lshlrev_b32 v12, 16, v46
v_and_b32 v13, 0xffff0000, v46
v_lshlrev_b32 v14, 16, v47
v_and_b32 v15, 0xffff0000, v47
v_pk_mul_f32 v[8:9], v[8:9], v[176:177]
v_pk_mul_f32 v[10:11], v[10:11], v[178:179]
v_pk_mul_f32 v[12:13], v[12:13], v[180:181]
v_pk_mul_f32 v[14:15], v[14:15], v[182:183]
CV v44,v8,v9
CV v45,v10,v11
CV v46,v12,v13
CV v47,v14,v15
v_lshlrev_b32 v8, 16, v44
v_and_b32 v9, 0xffff0000, v44
v_lshlrev_b32 v10, 16, v45
v_and_b32 v11, 0xffff0000, v45
v_lshlrev_b32 v12, 16, v46
v_and_b32 v13, 0xffff0000, v46
v_lshlrev_b32 v14, 16, v47
v_and_b32 v15, 0xffff0000, v47
v_pk_add_f32 v[8:9], v[8:9], v[208:209]
v_pk_add_f32 v[10:11], v[10:11], v[210:211]
v_pk_add_f32 v[12:13], v[12:13], v[212:213]
v_pk_add_f32 v[14:15], v[14:15], v[214:215]
CV v44,v8,v9
CV v45,v10,v11
CV v46,v12,v13
CV v47,v14,v15
buffer_store_dwordx4 v[44:47], v251, s[40:43], s80 offen offset:0 nt
s_branch LSE_%=
LSI_%=:
s_memrealtime s[98:99]
s_andn2_b32 s91, s17, s58
s_mov_b32 s77, 0x7fffffff
s_waitcnt lgkmcnt(0)
s_sub_u32 s76, s98, s54
s_bitcmp0_b32 s91, 1
s_cbranch_scc1 LSM1_%=
s_sub_u32 s90, s13, s76
s_sub_u32 s90, s90, 12
s_cmp_gt_i32 s90, 0
s_cbranch_scc1 LSN1_%=
buffer_load_dwordx4 v[16:19], v248, s[20:23], s36 offen offset:0 sc1
buffer_load_dwordx4 v[20:23], v248, s[20:23], s36 offen offset:1024 sc1
s_bitset1_b32 s58, 1
s_branch LSM1_%=
LSN1_%=:
s_min_i32 s77, s77, s90
LSM1_%=:
s_bitcmp0_b32 s91, 3
s_cbranch_scc1 LSM3_%=
s_sub_u32 s90, s15, s76
s_sub_u32 s90, s90, 63
s_cmp_gt_i32 s90, 0
s_cbranch_scc1 LSN3_%=
buffer_load_dwordx4 v[48:51], v248, s[20:23], s38 offen offset:0 sc1
buffer_load_dwordx4 v[52:55], v248, s[20:23], s38 offen offset:1024 sc1
s_bitset1_b32 s58, 3
s_branch LSM3_%=
LSN3_%=:
s_min_i32 s77, s77, s90
LSM3_%=:
s_bitcmp0_b32 s91, 2
s_cbranch_scc1 LSM2_%=
s_sub_u32 s90, s14, s76
s_sub_u32 s90, s90, 97
s_cmp_gt_i32 s90, 0
s_cbranch_scc1 LSN2_%=
buffer_load_dwordx4 v[24:27], v248, s[20:23], s37 offen offset:0 sc1
buffer_load_dwordx4 v[28:31], v248, s[20:23], s37 offen offset:1024 sc1
s_bitset1_b32 s58, 2
s_branch LSM2_%=
LSN2_%=:
s_min_i32 s77, s77, s90
LSM2_%=:
s_bitcmp0_b32 s91, 0
s_cbranch_scc1 LSM0_%=
s_sub_u32 s90, s12, s76
s_sub_u32 s90, s90, 131
s_cmp_gt_i32 s90, 0
s_cbranch_scc1 LSN0_%=
buffer_load_dwordx4 v[56:59], v250, s[20:23], s39 offen offset:0 sc1
buffer_load_dwordx4 v[60:63], v250, s[20:23], s39 offen offset:256 sc1
s_bitset1_b32 s58, 0
s_branch LSM0_%=
LSN0_%=:
s_min_i32 s77, s77, s90
LSM0_%=:
s_cmp_lt_i32 s77, 100
s_cbranch_scc1 LSI_%=
buffer_load_dwordx2 v[148:149], v253, s[20:23], s68 offen offset:8 sc1
s_waitcnt vmcnt(1)
s_bitcmp0_b32 s58, 1
s_cbranch_scc1 LSB1_%=
v_max_u32 v237, v19, v23
v_cmp_eq_u32 vcc, -1, v237
s_cbranch_vccnz LSB1_%=
s_bitset0_b32 s17, 1
LSB1_%=:
s_bitcmp0_b32 s58, 3
s_cbranch_scc1 LSB3_%=
v_max_u32 v239, v51, v55
v_cmp_eq_u32 vcc, -1, v239
s_cbranch_vccnz LSB3_%=
s_bitset0_b32 s17, 3
LSB3_%=:
s_bitcmp0_b32 s58, 2
s_cbranch_scc1 LSB2_%=
v_max_u32 v238, v27, v31
v_cmp_eq_u32 vcc, -1, v238
s_cbranch_vccnz LSB2_%=
s_bitset0_b32 s17, 2
LSB2_%=:
s_bitcmp0_b32 s58, 0
s_cbranch_scc1 LSB0_%=
v_max_u32 v236, v59, v63
v_cmp_eq_u32 vcc, -1, v236
s_cbranch_vccnz LSB0_%=
s_bitset0_b32 s17, 0
LSB0_%=:
s_cmp_eq_u32 s17, 0
s_cbranch_scc1 LSF_%=
s_and_b32 s76, s17, 30
s_cbranch_scc1 LSO_%=
MM v[0:3],v[122:125],v[24:27],v[0:3]
MM v[0:3],v[122:125],v[48:51],v[0:3]
MM v[0:3],v[122:125],v[16:19],v[0:3]
MM v[4:7],v[120:123],v[24:27],v[4:7]
MM v[4:7],v[120:123],v[48:51],v[4:7]
MM v[4:7],v[120:123],v[16:19],v[4:7]
MM v[8:11],v[122:125],v[28:31],v[8:11]
MM v[8:11],v[122:125],v[52:55],v[8:11]
MM v[8:11],v[122:125],v[20:23],v[8:11]
s_add_u32 s80, s18, 20480
MM v[12:15],v[120:123],v[28:31],v[12:15]
MM v[12:15],v[120:123],v[52:55],v[12:15]
CV v32,v0,v1
CV v33,v2,v3
MM v[12:15],v[120:123],v[20:23],v[12:15]
CV v34,v4,v5
CV v35,v6,v7
ds_write2_b64 v224, v[32:33], v[34:35] offset0:0 offset1:4
RD v[40:43],v225 offset:0
CV v36,v8,v9
CV v37,v10,v11
s_nop 1
CV v38,v12,v13
CV v39,v14,v15
ds_write2_b64 v224, v[36:37], v[38:39] offset0:160 offset1:164
RD v[44:47],v225 offset:1280
s_waitcnt lgkmcnt(2)
v_lshlrev_b32 v0, 16, v40
v_and_b32 v1, 0xffff0000, v40
v_lshlrev_b32 v2, 16, v41
v_and_b32 v3, 0xffff0000, v41
v_lshlrev_b32 v4, 16, v42
v_and_b32 v5, 0xffff0000, v42
v_lshlrev_b32 v6, 16, v43
v_and_b32 v7, 0xffff0000, v43
v_pk_mul_f32 v[0:1], v[0:1], v[160:161]
v_pk_mul_f32 v[2:3], v[2:3], v[162:163]
v_pk_mul_f32 v[4:5], v[4:5], v[164:165]
v_pk_mul_f32 v[6:7], v[6:7], v[166:167]
CV v40,v0,v1
CV v41,v2,v3
CV v42,v4,v5
CV v43,v6,v7
v_lshlrev_b32 v0, 16, v40
v_and_b32 v1, 0xffff0000, v40
v_lshlrev_b32 v2, 16, v41
v_and_b32 v3, 0xffff0000, v41
v_lshlrev_b32 v4, 16, v42
v_and_b32 v5, 0xffff0000, v42
v_lshlrev_b32 v6, 16, v43
v_and_b32 v7, 0xffff0000, v43
v_pk_add_f32 v[0:1], v[0:1], v[192:193]
v_pk_add_f32 v[2:3], v[2:3], v[194:195]
v_pk_add_f32 v[4:5], v[4:5], v[196:197]
v_pk_add_f32 v[6:7], v[6:7], v[198:199]
CV v40,v0,v1
CV v41,v2,v3
CV v42,v4,v5
CV v43,v6,v7
buffer_store_dwordx4 v[40:43], v251, s[40:43], s18 offen offset:0 nt
s_waitcnt lgkmcnt(0)
v_lshlrev_b32 v8, 16, v44
v_and_b32 v9, 0xffff0000, v44
v_lshlrev_b32 v10, 16, v45
v_and_b32 v11, 0xffff0000, v45
v_lshlrev_b32 v12, 16, v46
v_and_b32 v13, 0xffff0000, v46
v_lshlrev_b32 v14, 16, v47
v_and_b32 v15, 0xffff0000, v47
v_pk_mul_f32 v[8:9], v[8:9], v[176:177]
v_pk_mul_f32 v[10:11], v[10:11], v[178:179]
v_pk_mul_f32 v[12:13], v[12:13], v[180:181]
)ASM"
R"ASM(
v_pk_mul_f32 v[14:15], v[14:15], v[182:183]
CV v44,v8,v9
CV v45,v10,v11
CV v46,v12,v13
CV v47,v14,v15
v_lshlrev_b32 v8, 16, v44
v_and_b32 v9, 0xffff0000, v44
v_lshlrev_b32 v10, 16, v45
v_and_b32 v11, 0xffff0000, v45
v_lshlrev_b32 v12, 16, v46
v_and_b32 v13, 0xffff0000, v46
v_lshlrev_b32 v14, 16, v47
v_and_b32 v15, 0xffff0000, v47
v_pk_add_f32 v[8:9], v[8:9], v[208:209]
v_pk_add_f32 v[10:11], v[10:11], v[210:211]
v_pk_add_f32 v[12:13], v[12:13], v[212:213]
v_pk_add_f32 v[14:15], v[14:15], v[214:215]
CV v44,v8,v9
CV v45,v10,v11
CV v46,v12,v13
CV v47,v14,v15
buffer_store_dwordx4 v[44:47], v251, s[40:43], s80 offen offset:0 nt
s_or_b32 s17, s17, 16
LSO_%=:
s_and_b32 s76, s17, 33
s_cbranch_scc1 LSP_%=
MM v[152:155],v[112:115],v[56:59],0
MM v[152:155],v[116:119],v[60:63],v[152:155]
v_lshlrev_b32 v128, 16, v150
v_and_b32 v129, 0xffff0000, v150
v_lshlrev_b32 v130, 16, v151
v_and_b32 v131, 0xffff0000, v151
v_lshlrev_b32 v132, 16, v126
v_and_b32 v133, 0xffff0000, v126
v_lshlrev_b32 v134, 16, v127
v_and_b32 v135, 0xffff0000, v127
CV v236,v152,v153
CV v237,v154,v155
ds_bpermute_b32 v236, v232, v236
ds_bpermute_b32 v237, v232, v237
s_waitcnt lgkmcnt(0)
v_lshlrev_b32 v152, 16, v236
v_and_b32 v153, 0xffff0000, v236
v_lshlrev_b32 v154, 16, v237
v_and_b32 v155, 0xffff0000, v237
v_pk_mul_f32 v[152:153], v[152:153], v[128:129]
v_pk_mul_f32 v[154:155], v[154:155], v[130:131]
CV v236,v152,v153
CV v237,v154,v155
v_lshlrev_b32 v152, 16, v236
v_and_b32 v153, 0xffff0000, v236
v_lshlrev_b32 v154, 16, v237
v_and_b32 v155, 0xffff0000, v237
v_pk_add_f32 v[152:153], v[152:153], v[132:133]
v_pk_add_f32 v[154:155], v[154:155], v[134:135]
CV v238,v152,v153
CV v239,v154,v155
buffer_store_dwordx2 v[238:239], v254, s[40:43], s63 offen offset:0 nt
s_nop 0
s_or_b32 s17, s17, 32
LSP_%=:
s_cmp_eq_u32 s17, 48
s_cbranch_scc1 LSE_%=
s_mov_b32 s58, 0
s_waitcnt vmcnt(0)
v_cmp_eq_u32 s[8:9], s69, v149
v_subrev_u32 v226, s56, v148
s_nop 0
v_cndmask_b32 v226, 4.0, v226, s[8:9]
s_nop 0
v_readlane_b32 s13, v226, 16
v_readlane_b32 s14, v226, 32
v_readlane_b32 s15, v226, 48
s_max_i32 s12, s13, s14
s_max_i32 s12, s12, s15
s_max_i32 s12, s12, 0
s_sub_u32 s73, s73, 1
s_cbranch_scc0 LSI_%=
LSE_%=:
s_waitcnt vmcnt(0)
s_endpgm
LRG1o_%=:
s_mov_b32 s70, 30000
LRG1ol_%=:
s_sleep 2
buffer_load_dwordx4 v[200:203], v208, s[20:23], s62 offen offset:0 sc1
buffer_load_dwordx4 v[204:207], v208, s[20:23], s93 offen offset:0 sc1
s_waitcnt vmcnt(0)
v_xor_b32 v201, s69, v201
v_xor_b32 v203, s69, v203
v_xor_b32 v205, s69, v205
v_xor_b32 v207, s69, v207
v_or3_b32 v209, v201, v203, v205
v_or_b32 v209, v209, v207
v_cmp_ne_u32 vcc, 0, v209
s_cbranch_vccz LRG1ob_%=
s_sub_u32 s70, s70, 1
s_cbranch_scc0 LRG1ol_%=
s_branch LRG1ob_%=
LRFR_%=:
s_mov_b32 s91, 30000
LRFRl_%=:
s_sleep 2
buffer_load_dwordx4 v[240:243], v248, s[20:23], s90 offen offset:0 sc1
s_waitcnt vmcnt(0)
v_xor_b32 v240, s69, v240
v_xor_b32 v241, s69, v241
v_xor_b32 v242, s69, v242
v_xor_b32 v243, s69, v243
v_or3_b32 v252, v240, v241, v242
v_or_b32 v252, v252, v243
v_cmp_ne_u32 vcc, 0, v252
s_cbranch_vccz LRFRb_%=
s_sub_u32 s91, s91, 1
s_cbranch_scc0 LRFRl_%=
s_branch LRFRb_%=
LRRK_%=:
s_mov_b32 s91, 30000
LRRKl_%=:
s_sleep 2
buffer_load_dwordx2 v[32:33], v34, s[20:23], s90 offen offset:0 sc1
s_waitcnt vmcnt(0)
v_cmp_ne_u32 vcc, s69, v33
s_cbranch_vccz LRRKb_%=
s_sub_u32 s91, s91, 1
s_cbranch_scc0 LRRKl_%=
s_branch LRRKb_%=
LRK_%=:
s_cmp_ge_u32 s60, 8
s_cbranch_scc1 LRK0_%=
v_mov_b32 v253, 0
v_cmp_lt_u32 vcc, s52, v96
v_cmp_lt_u32 s[98:99], s52, v97
s_nop 0
v_addc_co_u32 v253, vcc, 0, v253, vcc
v_addc_co_u32 v253, s[98:99], 0, v253, s[98:99]
v_cmp_lt_u32 vcc, s52, v98
v_cmp_lt_u32 s[98:99], s52, v99
s_nop 0
v_addc_co_u32 v253, vcc, 0, v253, vcc
v_addc_co_u32 v253, s[98:99], 0, v253, s[98:99]
v_cmp_lt_u32 vcc, s52, v100
v_cmp_lt_u32 s[98:99], s52, v101
s_nop 0
v_addc_co_u32 v253, vcc, 0, v253, vcc
v_addc_co_u32 v253, s[98:99], 0, v253, s[98:99]
v_cmp_lt_u32 vcc, s52, v102
v_cmp_lt_u32 s[98:99], s52, v103
s_nop 0
v_addc_co_u32 v253, vcc, 0, v253, vcc
v_addc_co_u32 v253, s[98:99], 0, v253, s[98:99]
v_cmp_lt_u32 vcc, s52, v104
v_cmp_lt_u32 s[98:99], s52, v105
s_nop 0
v_addc_co_u32 v253, vcc, 0, v253, vcc
v_addc_co_u32 v253, s[98:99], 0, v253, s[98:99]
v_cmp_lt_u32 vcc, s52, v106
v_cmp_lt_u32 s[98:99], s52, v107
s_nop 0
v_addc_co_u32 v253, vcc, 0, v253, vcc
v_addc_co_u32 v253, s[98:99], 0, v253, s[98:99]
v_cmp_lt_u32 vcc, s52, v108
v_cmp_lt_u32 s[98:99], s52, v109
s_nop 0
v_addc_co_u32 v253, vcc, 0, v253, vcc
v_addc_co_u32 v253, s[98:99], 0, v253, s[98:99]
v_cmp_lt_u32 vcc, s52, v110
v_cmp_lt_u32 s[98:99], s52, v111
s_nop 0
v_addc_co_u32 v253, vcc, 0, v253, vcc
v_addc_co_u32 v253, s[98:99], 0, v253, s[98:99]
v_cmp_lt_u32 vcc, s52, v112
v_cmp_lt_u32 s[98:99], s52, v113
s_nop 0
v_addc_co_u32 v253, vcc, 0, v253, vcc
v_addc_co_u32 v253, s[98:99], 0, v253, s[98:99]
v_cmp_lt_u32 vcc, s52, v114
v_cmp_lt_u32 s[98:99], s52, v115
s_nop 0
v_addc_co_u32 v253, vcc, 0, v253, vcc
v_addc_co_u32 v253, s[98:99], 0, v253, s[98:99]
v_cmp_lt_u32 vcc, s52, v116
v_cmp_lt_u32 s[98:99], s52, v117
s_nop 0
v_addc_co_u32 v253, vcc, 0, v253, vcc
v_addc_co_u32 v253, s[98:99], 0, v253, s[98:99]
v_cmp_lt_u32 vcc, s52, v118
v_cmp_lt_u32 s[98:99], s52, v119
s_nop 0
v_addc_co_u32 v253, vcc, 0, v253, vcc
v_addc_co_u32 v253, s[98:99], 0, v253, s[98:99]
v_cmp_lt_u32 vcc, s52, v120
v_cmp_lt_u32 s[98:99], s52, v121
s_nop 0
v_addc_co_u32 v253, vcc, 0, v253, vcc
v_addc_co_u32 v253, s[98:99], 0, v253, s[98:99]
v_cmp_lt_u32 vcc, s52, v122
v_cmp_lt_u32 s[98:99], s52, v123
s_nop 0
v_addc_co_u32 v253, vcc, 0, v253, vcc
v_addc_co_u32 v253, s[98:99], 0, v253, s[98:99]
v_cmp_lt_u32 vcc, s52, v124
v_cmp_lt_u32 s[98:99], s52, v125
s_nop 0
v_addc_co_u32 v253, vcc, 0, v253, vcc
v_addc_co_u32 v253, s[98:99], 0, v253, s[98:99]
v_cmp_lt_u32 vcc, s52, v126
v_cmp_lt_u32 s[98:99], s52, v127
s_nop 0
v_addc_co_u32 v253, vcc, 0, v253, vcc
v_addc_co_u32 v253, s[98:99], 0, v253, s[98:99]
s_nop 1
v_add_u32_dpp v253, v253, v253 row_ror:8 row_mask:0xf bank_mask:0xf
s_nop 1
v_add_u32_dpp v253, v253, v253 row_ror:4 row_mask:0xf bank_mask:0xf
s_nop 1
v_add_u32_dpp v253, v253, v253 row_ror:2 row_mask:0xf bank_mask:0xf
s_nop 1
v_add_u32_dpp v253, v253, v253 row_ror:1 row_mask:0xf bank_mask:0xf
v_mov_b32 v250, v253
s_nop 1
v_permlane16_swap_b32 v253, v250
v_add_u32 v253, v253, v250
v_mov_b32 v250, v253
s_nop 1
v_permlane32_swap_b32 v253, v250
v_add_u32 v253, v253, v250
v_cmp_gt_u32 vcc, 8, v253
v_mov_b32 v250, s52
v_mov_b32 v251, s69
v_lshlrev_b32 v253, 3, v253
s_mov_b32 s76, 94208
s_cbranch_vccz LRK0_%=
s_mov_b64 exec, 1
buffer_store_dwordx2 v[250:251], v253, s[20:23], s76 offen offset:0 sc1
s_mov_b64 exec, -1
s_nop 1
LRK0_%=:
s_cmp_ge_u32 s62, 8
s_cbranch_scc1 LRK1_%=
v_mov_b32 v253, 0
v_cmp_lt_u32 vcc, s53, v96
v_cmp_lt_u32 s[98:99], s53, v97
s_nop 0
v_addc_co_u32 v253, vcc, 0, v253, vcc
v_addc_co_u32 v253, s[98:99], 0, v253, s[98:99]
v_cmp_lt_u32 vcc, s53, v98
v_cmp_lt_u32 s[98:99], s53, v99
s_nop 0
v_addc_co_u32 v253, vcc, 0, v253, vcc
v_addc_co_u32 v253, s[98:99], 0, v253, s[98:99]
v_cmp_lt_u32 vcc, s53, v100
v_cmp_lt_u32 s[98:99], s53, v101
s_nop 0
v_addc_co_u32 v253, vcc, 0, v253, vcc
v_addc_co_u32 v253, s[98:99], 0, v253, s[98:99]
v_cmp_lt_u32 vcc, s53, v102
v_cmp_lt_u32 s[98:99], s53, v103
s_nop 0
v_addc_co_u32 v253, vcc, 0, v253, vcc
v_addc_co_u32 v253, s[98:99], 0, v253, s[98:99]
v_cmp_lt_u32 vcc, s53, v104
v_cmp_lt_u32 s[98:99], s53, v105
s_nop 0
v_addc_co_u32 v253, vcc, 0, v253, vcc
v_addc_co_u32 v253, s[98:99], 0, v253, s[98:99]
v_cmp_lt_u32 vcc, s53, v106
v_cmp_lt_u32 s[98:99], s53, v107
s_nop 0
v_addc_co_u32 v253, vcc, 0, v253, vcc
v_addc_co_u32 v253, s[98:99], 0, v253, s[98:99]
v_cmp_lt_u32 vcc, s53, v108
v_cmp_lt_u32 s[98:99], s53, v109
s_nop 0
v_addc_co_u32 v253, vcc, 0, v253, vcc
v_addc_co_u32 v253, s[98:99], 0, v253, s[98:99]
v_cmp_lt_u32 vcc, s53, v110
v_cmp_lt_u32 s[98:99], s53, v111
s_nop 0
v_addc_co_u32 v253, vcc, 0, v253, vcc
v_addc_co_u32 v253, s[98:99], 0, v253, s[98:99]
v_cmp_lt_u32 vcc, s53, v112
v_cmp_lt_u32 s[98:99], s53, v113
s_nop 0
v_addc_co_u32 v253, vcc, 0, v253, vcc
v_addc_co_u32 v253, s[98:99], 0, v253, s[98:99]
v_cmp_lt_u32 vcc, s53, v114
v_cmp_lt_u32 s[98:99], s53, v115
s_nop 0
v_addc_co_u32 v253, vcc, 0, v253, vcc
v_addc_co_u32 v253, s[98:99], 0, v253, s[98:99]
v_cmp_lt_u32 vcc, s53, v116
v_cmp_lt_u32 s[98:99], s53, v117
s_nop 0
v_addc_co_u32 v253, vcc, 0, v253, vcc
v_addc_co_u32 v253, s[98:99], 0, v253, s[98:99]
v_cmp_lt_u32 vcc, s53, v118
v_cmp_lt_u32 s[98:99], s53, v119
s_nop 0
v_addc_co_u32 v253, vcc, 0, v253, vcc
v_addc_co_u32 v253, s[98:99], 0, v253, s[98:99]
v_cmp_lt_u32 vcc, s53, v120
v_cmp_lt_u32 s[98:99], s53, v121
s_nop 0
v_addc_co_u32 v253, vcc, 0, v253, vcc
v_addc_co_u32 v253, s[98:99], 0, v253, s[98:99]
v_cmp_lt_u32 vcc, s53, v122
v_cmp_lt_u32 s[98:99], s53, v123
s_nop 0
v_addc_co_u32 v253, vcc, 0, v253, vcc
v_addc_co_u32 v253, s[98:99], 0, v253, s[98:99]
v_cmp_lt_u32 vcc, s53, v124
v_cmp_lt_u32 s[98:99], s53, v125
s_nop 0
v_addc_co_u32 v253, vcc, 0, v253, vcc
v_addc_co_u32 v253, s[98:99], 0, v253, s[98:99]
v_cmp_lt_u32 vcc, s53, v126
v_cmp_lt_u32 s[98:99], s53, v127
s_nop 0
v_addc_co_u32 v253, vcc, 0, v253, vcc
v_addc_co_u32 v253, s[98:99], 0, v253, s[98:99]
s_nop 1
v_add_u32_dpp v253, v253, v253 row_ror:8 row_mask:0xf bank_mask:0xf
s_nop 1
v_add_u32_dpp v253, v253, v253 row_ror:4 row_mask:0xf bank_mask:0xf
s_nop 1
v_add_u32_dpp v253, v253, v253 row_ror:2 row_mask:0xf bank_mask:0xf
s_nop 1
v_add_u32_dpp v253, v253, v253 row_ror:1 row_mask:0xf bank_mask:0xf
v_mov_b32 v250, v253
s_nop 1
v_permlane16_swap_b32 v253, v250
v_add_u32 v253, v253, v250
v_mov_b32 v250, v253
s_nop 1
v_permlane32_swap_b32 v253, v250
v_add_u32 v253, v253, v250
v_cmp_gt_u32 vcc, 8, v253
v_mov_b32 v250, s53
v_mov_b32 v251, s69
v_lshlrev_b32 v253, 3, v253
s_mov_b32 s76, 94208
s_cbranch_vccz LRK1_%=
s_mov_b64 exec, 1
buffer_store_dwordx2 v[250:251], v253, s[20:23], s76 offen offset:0 sc1
s_mov_b64 exec, -1
s_nop 1
LRK1_%=:
s_cmp_ge_u32 s56, 8
s_cbranch_scc1 LRK2_%=
v_mov_b32 v253, 0
v_cmp_lt_u32 vcc, s54, v96
v_cmp_lt_u32 s[98:99], s54, v97
s_nop 0
v_addc_co_u32 v253, vcc, 0, v253, vcc
v_addc_co_u32 v253, s[98:99], 0, v253, s[98:99]
v_cmp_lt_u32 vcc, s54, v98
v_cmp_lt_u32 s[98:99], s54, v99
s_nop 0
v_addc_co_u32 v253, vcc, 0, v253, vcc
v_addc_co_u32 v253, s[98:99], 0, v253, s[98:99]
v_cmp_lt_u32 vcc, s54, v100
v_cmp_lt_u32 s[98:99], s54, v101
s_nop 0
v_addc_co_u32 v253, vcc, 0, v253, vcc
v_addc_co_u32 v253, s[98:99], 0, v253, s[98:99]
v_cmp_lt_u32 vcc, s54, v102
v_cmp_lt_u32 s[98:99], s54, v103
s_nop 0
v_addc_co_u32 v253, vcc, 0, v253, vcc
v_addc_co_u32 v253, s[98:99], 0, v253, s[98:99]
v_cmp_lt_u32 vcc, s54, v104
v_cmp_lt_u32 s[98:99], s54, v105
s_nop 0
v_addc_co_u32 v253, vcc, 0, v253, vcc
v_addc_co_u32 v253, s[98:99], 0, v253, s[98:99]
v_cmp_lt_u32 vcc, s54, v106
v_cmp_lt_u32 s[98:99], s54, v107
s_nop 0
v_addc_co_u32 v253, vcc, 0, v253, vcc
v_addc_co_u32 v253, s[98:99], 0, v253, s[98:99]
v_cmp_lt_u32 vcc, s54, v108
v_cmp_lt_u32 s[98:99], s54, v109
s_nop 0
v_addc_co_u32 v253, vcc, 0, v253, vcc
v_addc_co_u32 v253, s[98:99], 0, v253, s[98:99]
v_cmp_lt_u32 vcc, s54, v110
v_cmp_lt_u32 s[98:99], s54, v111
s_nop 0
v_addc_co_u32 v253, vcc, 0, v253, vcc
v_addc_co_u32 v253, s[98:99], 0, v253, s[98:99]
v_cmp_lt_u32 vcc, s54, v112
v_cmp_lt_u32 s[98:99], s54, v113
s_nop 0
v_addc_co_u32 v253, vcc, 0, v253, vcc
v_addc_co_u32 v253, s[98:99], 0, v253, s[98:99]
v_cmp_lt_u32 vcc, s54, v114
v_cmp_lt_u32 s[98:99], s54, v115
s_nop 0
v_addc_co_u32 v253, vcc, 0, v253, vcc
v_addc_co_u32 v253, s[98:99], 0, v253, s[98:99]
v_cmp_lt_u32 vcc, s54, v116
v_cmp_lt_u32 s[98:99], s54, v117
s_nop 0
v_addc_co_u32 v253, vcc, 0, v253, vcc
v_addc_co_u32 v253, s[98:99], 0, v253, s[98:99]
v_cmp_lt_u32 vcc, s54, v118
v_cmp_lt_u32 s[98:99], s54, v119
s_nop 0
v_addc_co_u32 v253, vcc, 0, v253, vcc
v_addc_co_u32 v253, s[98:99], 0, v253, s[98:99]
v_cmp_lt_u32 vcc, s54, v120
v_cmp_lt_u32 s[98:99], s54, v121
s_nop 0
v_addc_co_u32 v253, vcc, 0, v253, vcc
v_addc_co_u32 v253, s[98:99], 0, v253, s[98:99]
)ASM"
R"ASM(
v_cmp_lt_u32 vcc, s54, v122
v_cmp_lt_u32 s[98:99], s54, v123
s_nop 0
v_addc_co_u32 v253, vcc, 0, v253, vcc
v_addc_co_u32 v253, s[98:99], 0, v253, s[98:99]
v_cmp_lt_u32 vcc, s54, v124
v_cmp_lt_u32 s[98:99], s54, v125
s_nop 0
v_addc_co_u32 v253, vcc, 0, v253, vcc
v_addc_co_u32 v253, s[98:99], 0, v253, s[98:99]
v_cmp_lt_u32 vcc, s54, v126
v_cmp_lt_u32 s[98:99], s54, v127
s_nop 0
v_addc_co_u32 v253, vcc, 0, v253, vcc
v_addc_co_u32 v253, s[98:99], 0, v253, s[98:99]
s_nop 1
v_add_u32_dpp v253, v253, v253 row_ror:8 row_mask:0xf bank_mask:0xf
s_nop 1
v_add_u32_dpp v253, v253, v253 row_ror:4 row_mask:0xf bank_mask:0xf
s_nop 1
v_add_u32_dpp v253, v253, v253 row_ror:2 row_mask:0xf bank_mask:0xf
s_nop 1
v_add_u32_dpp v253, v253, v253 row_ror:1 row_mask:0xf bank_mask:0xf
v_mov_b32 v250, v253
s_nop 1
v_permlane16_swap_b32 v253, v250
v_add_u32 v253, v253, v250
v_mov_b32 v250, v253
s_nop 1
v_permlane32_swap_b32 v253, v250
v_add_u32 v253, v253, v250
v_cmp_gt_u32 vcc, 8, v253
v_mov_b32 v250, s54
v_mov_b32 v251, s69
v_lshlrev_b32 v253, 3, v253
s_mov_b32 s76, 94208
s_cbranch_vccz LRK2_%=
s_mov_b64 exec, 1
buffer_store_dwordx2 v[250:251], v253, s[20:23], s76 offen offset:0 sc1
s_mov_b64 exec, -1
s_nop 1
LRK2_%=:
s_cmp_ge_u32 s58, 8
s_cbranch_scc1 LRK3_%=
v_mov_b32 v253, 0
v_cmp_lt_u32 vcc, s55, v96
v_cmp_lt_u32 s[98:99], s55, v97
s_nop 0
v_addc_co_u32 v253, vcc, 0, v253, vcc
v_addc_co_u32 v253, s[98:99], 0, v253, s[98:99]
v_cmp_lt_u32 vcc, s55, v98
v_cmp_lt_u32 s[98:99], s55, v99
s_nop 0
v_addc_co_u32 v253, vcc, 0, v253, vcc
v_addc_co_u32 v253, s[98:99], 0, v253, s[98:99]
v_cmp_lt_u32 vcc, s55, v100
v_cmp_lt_u32 s[98:99], s55, v101
s_nop 0
v_addc_co_u32 v253, vcc, 0, v253, vcc
v_addc_co_u32 v253, s[98:99], 0, v253, s[98:99]
v_cmp_lt_u32 vcc, s55, v102
v_cmp_lt_u32 s[98:99], s55, v103
s_nop 0
v_addc_co_u32 v253, vcc, 0, v253, vcc
v_addc_co_u32 v253, s[98:99], 0, v253, s[98:99]
v_cmp_lt_u32 vcc, s55, v104
v_cmp_lt_u32 s[98:99], s55, v105
s_nop 0
v_addc_co_u32 v253, vcc, 0, v253, vcc
v_addc_co_u32 v253, s[98:99], 0, v253, s[98:99]
v_cmp_lt_u32 vcc, s55, v106
v_cmp_lt_u32 s[98:99], s55, v107
s_nop 0
v_addc_co_u32 v253, vcc, 0, v253, vcc
v_addc_co_u32 v253, s[98:99], 0, v253, s[98:99]
v_cmp_lt_u32 vcc, s55, v108
v_cmp_lt_u32 s[98:99], s55, v109
s_nop 0
v_addc_co_u32 v253, vcc, 0, v253, vcc
v_addc_co_u32 v253, s[98:99], 0, v253, s[98:99]
v_cmp_lt_u32 vcc, s55, v110
v_cmp_lt_u32 s[98:99], s55, v111
s_nop 0
v_addc_co_u32 v253, vcc, 0, v253, vcc
v_addc_co_u32 v253, s[98:99], 0, v253, s[98:99]
v_cmp_lt_u32 vcc, s55, v112
v_cmp_lt_u32 s[98:99], s55, v113
s_nop 0
v_addc_co_u32 v253, vcc, 0, v253, vcc
v_addc_co_u32 v253, s[98:99], 0, v253, s[98:99]
v_cmp_lt_u32 vcc, s55, v114
v_cmp_lt_u32 s[98:99], s55, v115
s_nop 0
v_addc_co_u32 v253, vcc, 0, v253, vcc
v_addc_co_u32 v253, s[98:99], 0, v253, s[98:99]
v_cmp_lt_u32 vcc, s55, v116
v_cmp_lt_u32 s[98:99], s55, v117
s_nop 0
v_addc_co_u32 v253, vcc, 0, v253, vcc
v_addc_co_u32 v253, s[98:99], 0, v253, s[98:99]
v_cmp_lt_u32 vcc, s55, v118
v_cmp_lt_u32 s[98:99], s55, v119
s_nop 0
v_addc_co_u32 v253, vcc, 0, v253, vcc
v_addc_co_u32 v253, s[98:99], 0, v253, s[98:99]
v_cmp_lt_u32 vcc, s55, v120
v_cmp_lt_u32 s[98:99], s55, v121
s_nop 0
v_addc_co_u32 v253, vcc, 0, v253, vcc
v_addc_co_u32 v253, s[98:99], 0, v253, s[98:99]
v_cmp_lt_u32 vcc, s55, v122
v_cmp_lt_u32 s[98:99], s55, v123
s_nop 0
v_addc_co_u32 v253, vcc, 0, v253, vcc
v_addc_co_u32 v253, s[98:99], 0, v253, s[98:99]
v_cmp_lt_u32 vcc, s55, v124
v_cmp_lt_u32 s[98:99], s55, v125
s_nop 0
v_addc_co_u32 v253, vcc, 0, v253, vcc
v_addc_co_u32 v253, s[98:99], 0, v253, s[98:99]
v_cmp_lt_u32 vcc, s55, v126
v_cmp_lt_u32 s[98:99], s55, v127
s_nop 0
v_addc_co_u32 v253, vcc, 0, v253, vcc
v_addc_co_u32 v253, s[98:99], 0, v253, s[98:99]
s_nop 1
v_add_u32_dpp v253, v253, v253 row_ror:8 row_mask:0xf bank_mask:0xf
s_nop 1
v_add_u32_dpp v253, v253, v253 row_ror:4 row_mask:0xf bank_mask:0xf
s_nop 1
v_add_u32_dpp v253, v253, v253 row_ror:2 row_mask:0xf bank_mask:0xf
s_nop 1
v_add_u32_dpp v253, v253, v253 row_ror:1 row_mask:0xf bank_mask:0xf
v_mov_b32 v250, v253
s_nop 1
v_permlane16_swap_b32 v253, v250
v_add_u32 v253, v253, v250
v_mov_b32 v250, v253
s_nop 1
v_permlane32_swap_b32 v253, v250
v_add_u32 v253, v253, v250
v_cmp_gt_u32 vcc, 8, v253
v_mov_b32 v250, s55
v_mov_b32 v251, s69
v_lshlrev_b32 v253, 3, v253
s_mov_b32 s76, 94208
s_cbranch_vccz LRK3_%=
s_mov_b64 exec, 1
buffer_store_dwordx2 v[250:251], v253, s[20:23], s76 offen offset:0 sc1
s_mov_b64 exec, -1
s_nop 1
LRK3_%=:
s_branch LRKb_%=
LRIX_%=:
s_mov_b32 s91, 30000
LRIXl_%=:
s_sleep 2
v_bfe_u32 v251, v248, 4, 3
v_lshlrev_b32 v251, 3, v251
buffer_load_dwordx2 v[252:253], v251, s[20:23], s86 offen offset:0 sc1
s_waitcnt vmcnt(0)
v_cmp_ne_u32 vcc, s69, v253
s_cbranch_vccz LRIXb_%=
s_sub_u32 s91, s91, 1
s_cbranch_scc0 LRIXl_%=
s_branch LRIXb_%=
LRFG_%=:
s_mov_b32 s91, 30000
LRFGl_%=:
s_sleep 2
buffer_load_dword v252, v253, s[20:23], s76 offen offset:0 sc1
s_waitcnt vmcnt(0)
v_cmp_ne_u32 vcc, s69, v252
s_cbranch_vccz LRFGb_%=
s_sub_u32 s91, s91, 1
s_cbranch_scc0 LRFGl_%=
s_branch LRFGb_%=
LRFH_%=:
s_mov_b32 s91, 30000
LRFHl_%=:
s_sleep 2
buffer_load_dword v252, v253, s[20:23], s76 offen offset:0 sc1
s_waitcnt vmcnt(0)
v_cmp_ne_u32 vcc, s69, v252
s_cbranch_vccz LRFHb_%=
s_sub_u32 s91, s91, 1
s_cbranch_scc0 LRFHl_%=
s_branch LRFHb_%=
LRFY_%=:
s_mov_b32 s91, 30000
LRFYl_%=:
s_sleep 2
buffer_load_dword v252, v253, s[20:23], s76 offen offset:0 sc1
s_waitcnt vmcnt(0)
v_cmp_ne_u32 vcc, s69, v252
s_cbranch_vccz LRFYb_%=
s_sub_u32 s91, s91, 1
s_cbranch_scc0 LRFYl_%=
s_branch LRFYb_%=
LRPO0_%=:
s_mov_b32 s97, 30000
LRPO0l_%=:
s_sleep 2
buffer_load_dword v242, v243, s[20:23], s44 offen offset:0 sc1
s_waitcnt vmcnt(0)
v_cmp_ne_u32 vcc, s69, v242
s_cbranch_vccz LRPO0b_%=
s_sub_u32 s97, s97, 1
s_cbranch_scc0 LRPO0l_%=
s_branch LRPO0b_%=
LRPW_%=:
s_mov_b32 s61, 30000
LRPWl_%=:
s_sleep 2
buffer_load_dwordx2 v[96:97], v98, s[20:23], s62 offen offset:0 sc1
s_waitcnt vmcnt(0)
v_xor_b32 v96, s69, v96
v_xor_b32 v97, s69, v97
v_or_b32 v99, v96, v97
v_cmp_ne_u32 vcc, 0, v99
s_cbranch_vccz LRPWb_%=
s_sub_u32 s61, s61, 1
s_cbranch_scc0 LRPWl_%=
s_branch LRPWb_%=
LRPO1_%=:
s_mov_b32 s97, 30000
LRPO1l_%=:
s_sleep 2
buffer_load_dword v242, v243, s[20:23], s96 offen offset:16 sc1
s_waitcnt vmcnt(0)
v_cmp_ne_u32 vcc, s69, v242
s_cbranch_vccz LRPO1b_%=
s_sub_u32 s97, s97, 1
s_cbranch_scc0 LRPO1l_%=
s_branch LRPO1b_%=
LKEND_ka:
LSITE0_%=:
s_mov_b32 s73, 0
s_mov_b32 s74, 0
s_mov_b32 s75, 16384
s_branch LRARE002_%=
LSITE2_%=:
s_mov_b32 s73, 2
s_mov_b32 s74, 32768
s_mov_b32 s75, 49152
s_branch LRARE002_%=
LRARE002_%=:
s_nop 15
s_waitcnt vmcnt(0) lgkmcnt(0)
v_add_u32 v249, s74, v224
RD a[192:195],v249 offset:0
RD a[208:211],v249 offset:4096
RD a[224:227],v249 offset:8192
RD a[240:243],v249 offset:12288
v_add_u32 v250, s74, v225
RD a[196:199],v250 offset:0
RD a[212:215],v250 offset:4096
RD a[228:231],v250 offset:8192
RD a[244:247],v250 offset:12288
v_add_u32 v251, s74, v226
RD a[200:203],v251 offset:0
RD a[216:219],v251 offset:4096
RD a[232:235],v251 offset:8192
RD a[248:251],v251 offset:12288
v_add_u32 v254, s74, v227
RD a[204:207],v254 offset:0
RD a[220:223],v254 offset:4096
RD a[236:239],v254 offset:8192
RD a[252:255],v254 offset:12288
s_waitcnt lgkmcnt(0)
MM v[0:3],a[192:195],v[160:163],v[128:131]
MM v[0:3],a[196:199],v[176:179],v[0:3]
MM v[0:3],a[200:203],v[192:195],v[0:3]
MM v[0:3],a[204:207],v[208:211],v[0:3]
MM v[4:7],a[220:223],v[208:211],v[128:131]
MM v[4:7],a[216:219],v[192:195],v[4:7]
MM v[4:7],a[212:215],v[176:179],v[4:7]
MM v[4:7],a[208:211],v[160:163],v[4:7]
MM v[12:15],a[208:211],v[164:167],v[132:135]
MM v[12:15],a[212:215],v[180:183],v[12:15]
MM v[12:15],a[216:219],v[196:199],v[12:15]
MM v[12:15],a[220:223],v[212:215],v[12:15]
MM v[8:11],a[204:207],v[212:215],v[132:135]
MM v[8:11],a[200:203],v[196:199],v[8:11]
MM v[8:11],a[196:199],v[180:183],v[8:11]
MM v[8:11],a[192:195],v[164:167],v[8:11]
MM v[32:35],a[224:227],v[160:163],v[128:131]
MM v[32:35],a[228:231],v[176:179],v[32:35]
MM v[32:35],a[232:235],v[192:195],v[32:35]
MM v[32:35],a[236:239],v[208:211],v[32:35]
MM v[36:39],a[252:255],v[208:211],v[128:131]
MM v[36:39],a[248:251],v[192:195],v[36:39]
MM v[36:39],a[244:247],v[176:179],v[36:39]
MM v[36:39],a[240:243],v[160:163],v[36:39]
MM v[44:47],a[240:243],v[164:167],v[132:135]
MM v[44:47],a[244:247],v[180:183],v[44:47]
MM v[44:47],a[248:251],v[196:199],v[44:47]
MM v[44:47],a[252:255],v[212:215],v[44:47]
MM v[40:43],a[236:239],v[212:215],v[132:135]
MM v[40:43],a[232:235],v[196:199],v[40:43]
MM v[40:43],a[228:231],v[180:183],v[40:43]
MM v[40:43],a[224:227],v[164:167],v[40:43]
s_nop 15
v_add_u32 v249, s75, v224
RD a[192:195],v249 offset:0
RD a[208:211],v249 offset:4096
RD a[224:227],v249 offset:8192
RD a[240:243],v249 offset:12288
v_add_u32 v250, s75, v225
RD a[196:199],v250 offset:0
RD a[212:215],v250 offset:4096
RD a[228:231],v250 offset:8192
RD a[244:247],v250 offset:12288
v_add_u32 v251, s75, v226
RD a[200:203],v251 offset:0
RD a[216:219],v251 offset:4096
RD a[232:235],v251 offset:8192
RD a[248:251],v251 offset:12288
v_add_u32 v254, s75, v227
RD a[204:207],v254 offset:0
RD a[220:223],v254 offset:4096
RD a[236:239],v254 offset:8192
RD a[252:255],v254 offset:12288
s_waitcnt lgkmcnt(0)
v_max3_f32 v249, v0, v1, v2
v_max3_f32 v251, v32, v33, v34
v_max3_f32 v249, v249, v3, v4
v_max3_f32 v251, v251, v35, v36
v_max3_f32 v249, v249, v5, v6
v_max3_f32 v251, v251, v37, v38
v_max3_f32 v249, v249, v7, v39
v_max_f32 v249, v249, v251
v_mov_b32 v251, v249
s_nop 1
v_permlane16_swap_b32 v249, v251
v_max_f32 v249, v249, v251
v_mov_b32 v251, v249
s_nop 1
v_permlane32_swap_b32 v249, v251
v_max_f32 v249, v249, v251
v_log_f32 v250, v244
s_nop 0
v_max3_f32 v249, 0, v249, v250
v_sub_f32 v250, 0, v249
EX v250,v250
v_sub_f32 v128, v128, v249
v_sub_f32 v129, v129, v249
v_sub_f32 v130, v130, v249
v_sub_f32 v131, v131, v249
v_sub_f32 v64, v64, v249
v_sub_f32 v0, v0, v249
v_sub_f32 v65, v65, v249
v_sub_f32 v1, v1, v249
v_sub_f32 v66, v66, v249
v_sub_f32 v2, v2, v249
v_sub_f32 v67, v67, v249
v_sub_f32 v3, v3, v249
v_sub_f32 v68, v68, v249
v_sub_f32 v4, v4, v249
v_sub_f32 v69, v69, v249
v_sub_f32 v5, v5, v249
v_sub_f32 v70, v70, v249
v_sub_f32 v6, v6, v249
v_sub_f32 v71, v71, v249
v_sub_f32 v7, v7, v249
v_sub_f32 v96, v96, v249
v_sub_f32 v32, v32, v249
v_sub_f32 v97, v97, v249
v_sub_f32 v33, v33, v249
v_sub_f32 v98, v98, v249
v_sub_f32 v34, v34, v249
v_sub_f32 v99, v99, v249
v_sub_f32 v35, v35, v249
v_sub_f32 v100, v100, v249
v_sub_f32 v36, v36, v249
v_sub_f32 v101, v101, v249
v_sub_f32 v37, v37, v249
v_sub_f32 v102, v102, v249
v_sub_f32 v38, v38, v249
v_sub_f32 v103, v103, v249
v_sub_f32 v39, v39, v249
v_mul_f32 v144, v244, v250
v_mov_b32 v145, 0
AR v254,a0
v_mul_f32 v254, v254, v250
AW a0,v254
AR v251,a1
v_mul_f32 v251, v251, v250
AW a1,v251
AR v254,a2
v_mul_f32 v254, v254, v250
AW a2,v254
AR v251,a3
v_mul_f32 v251, v251, v250
AW a3,v251
AR v254,a4
v_mul_f32 v254, v254, v250
AW a4,v254
AR v251,a5
v_mul_f32 v251, v251, v250
AW a5,v251
AR v254,a6
v_mul_f32 v254, v254, v250
AW a6,v254
AR v251,a7
v_mul_f32 v251, v251, v250
AW a7,v251
AR v254,a32
v_mul_f32 v254, v254, v250
AW a32,v254
AR v251,a33
v_mul_f32 v251, v251, v250
AW a33,v251
AR v254,a34
v_mul_f32 v254, v254, v250
AW a34,v254
AR v251,a35
v_mul_f32 v251, v251, v250
AW a35,v251
AR v254,a36
v_mul_f32 v254, v254, v250
AW a36,v254
AR v251,a37
v_mul_f32 v251, v251, v250
AW a37,v251
AR v254,a38
v_mul_f32 v254, v254, v250
AW a38,v254
AR v251,a39
v_mul_f32 v251, v251, v250
AW a39,v251
AR v254,a64
v_mul_f32 v254, v254, v250
AW a64,v254
AR v251,a65
v_mul_f32 v251, v251, v250
AW a65,v251
AR v254,a66
v_mul_f32 v254, v254, v250
AW a66,v254
AR v251,a67
v_mul_f32 v251, v251, v250
AW a67,v251
AR v254,a68
v_mul_f32 v254, v254, v250
AW a68,v254
AR v251,a69
v_mul_f32 v251, v251, v250
AW a69,v251
AR v254,a70
v_mul_f32 v254, v254, v250
AW a70,v254
AR v251,a71
v_mul_f32 v251, v251, v250
AW a71,v251
AR v254,a96
v_mul_f32 v254, v254, v250
AW a96,v254
AR v251,a97
v_mul_f32 v251, v251, v250
AW a97,v251
AR v254,a98
)ASM"
R"ASM(
v_mul_f32 v254, v254, v250
AW a98,v254
AR v251,a99
v_mul_f32 v251, v251, v250
AW a99,v251
AR v254,a100
v_mul_f32 v254, v254, v250
AW a100,v254
AR v251,a101
v_mul_f32 v251, v251, v250
AW a101,v251
AR v254,a102
v_mul_f32 v254, v254, v250
AW a102,v254
AR v251,a103
v_mul_f32 v251, v251, v250
AW a103,v251
v_max3_f32 v249, v8, v9, v10
v_max3_f32 v251, v40, v41, v42
v_max3_f32 v249, v249, v11, v12
v_max3_f32 v251, v251, v43, v44
v_max3_f32 v249, v249, v13, v14
v_max3_f32 v251, v251, v45, v46
v_max3_f32 v249, v249, v15, v47
v_max_f32 v249, v249, v251
v_mov_b32 v251, v249
s_nop 1
v_permlane16_swap_b32 v249, v251
v_max_f32 v249, v249, v251
v_mov_b32 v251, v249
s_nop 1
v_permlane32_swap_b32 v249, v251
v_max_f32 v249, v249, v251
v_log_f32 v250, v245
s_nop 0
v_max3_f32 v249, 0, v249, v250
v_sub_f32 v250, 0, v249
EX v250,v250
v_sub_f32 v132, v132, v249
v_sub_f32 v133, v133, v249
v_sub_f32 v134, v134, v249
v_sub_f32 v135, v135, v249
v_sub_f32 v72, v72, v249
v_sub_f32 v8, v8, v249
v_sub_f32 v73, v73, v249
v_sub_f32 v9, v9, v249
v_sub_f32 v74, v74, v249
v_sub_f32 v10, v10, v249
v_sub_f32 v75, v75, v249
v_sub_f32 v11, v11, v249
v_sub_f32 v76, v76, v249
v_sub_f32 v12, v12, v249
v_sub_f32 v77, v77, v249
v_sub_f32 v13, v13, v249
v_sub_f32 v78, v78, v249
v_sub_f32 v14, v14, v249
v_sub_f32 v79, v79, v249
v_sub_f32 v15, v15, v249
v_sub_f32 v104, v104, v249
v_sub_f32 v40, v40, v249
v_sub_f32 v105, v105, v249
v_sub_f32 v41, v41, v249
v_sub_f32 v106, v106, v249
v_sub_f32 v42, v42, v249
v_sub_f32 v107, v107, v249
v_sub_f32 v43, v43, v249
v_sub_f32 v108, v108, v249
v_sub_f32 v44, v44, v249
v_sub_f32 v109, v109, v249
v_sub_f32 v45, v45, v249
v_sub_f32 v110, v110, v249
v_sub_f32 v46, v46, v249
v_sub_f32 v111, v111, v249
v_sub_f32 v47, v47, v249
v_mul_f32 v148, v245, v250
v_mov_b32 v149, 0
AR v254,a8
v_mul_f32 v254, v254, v250
AW a8,v254
AR v251,a9
v_mul_f32 v251, v251, v250
AW a9,v251
AR v254,a10
v_mul_f32 v254, v254, v250
AW a10,v254
AR v251,a11
v_mul_f32 v251, v251, v250
AW a11,v251
AR v254,a12
v_mul_f32 v254, v254, v250
AW a12,v254
AR v251,a13
v_mul_f32 v251, v251, v250
AW a13,v251
AR v254,a14
v_mul_f32 v254, v254, v250
AW a14,v254
AR v251,a15
v_mul_f32 v251, v251, v250
AW a15,v251
AR v254,a40
v_mul_f32 v254, v254, v250
AW a40,v254
AR v251,a41
v_mul_f32 v251, v251, v250
AW a41,v251
AR v254,a42
v_mul_f32 v254, v254, v250
AW a42,v254
AR v251,a43
v_mul_f32 v251, v251, v250
AW a43,v251
AR v254,a44
v_mul_f32 v254, v254, v250
AW a44,v254
AR v251,a45
v_mul_f32 v251, v251, v250
AW a45,v251
AR v254,a46
v_mul_f32 v254, v254, v250
AW a46,v254
AR v251,a47
v_mul_f32 v251, v251, v250
AW a47,v251
AR v254,a72
v_mul_f32 v254, v254, v250
AW a72,v254
AR v251,a73
v_mul_f32 v251, v251, v250
AW a73,v251
AR v254,a74
v_mul_f32 v254, v254, v250
AW a74,v254
AR v251,a75
v_mul_f32 v251, v251, v250
AW a75,v251
AR v254,a76
v_mul_f32 v254, v254, v250
AW a76,v254
AR v251,a77
v_mul_f32 v251, v251, v250
AW a77,v251
AR v254,a78
v_mul_f32 v254, v254, v250
AW a78,v254
AR v251,a79
v_mul_f32 v251, v251, v250
AW a79,v251
AR v254,a104
v_mul_f32 v254, v254, v250
AW a104,v254
AR v251,a105
v_mul_f32 v251, v251, v250
AW a105,v251
AR v254,a106
v_mul_f32 v254, v254, v250
AW a106,v254
AR v251,a107
v_mul_f32 v251, v251, v250
AW a107,v251
AR v254,a108
v_mul_f32 v254, v254, v250
AW a108,v254
AR v251,a109
v_mul_f32 v251, v251, v250
AW a109,v251
AR v254,a110
v_mul_f32 v254, v254, v250
AW a110,v254
AR v251,a111
v_mul_f32 v251, v251, v250
AW a111,v251
EX v0,v0
EX v1,v1
EX v2,v2
EX v3,v3
EX v4,v4
EX v5,v5
EX v6,v6
EX v7,v7
EX v8,v8
EX v9,v9
EX v10,v10
EX v11,v11
EX v12,v12
EX v13,v13
EX v14,v14
EX v15,v15
EX v32,v32
EX v33,v33
EX v34,v34
EX v35,v35
EX v36,v36
EX v37,v37
EX v38,v38
EX v39,v39
EX v40,v40
EX v41,v41
EX v42,v42
EX v43,v43
EX v44,v44
EX v45,v45
EX v46,v46
EX v47,v47
CV v0,v0,v1
CV v1,v2,v3
CV v2,v4,v5
CV v3,v6,v7
CV v8,v8,v9
CV v9,v10,v11
CV v10,v12,v13
CV v11,v14,v15
CV v4,v32,v33
CV v5,v34,v35
CV v6,v36,v37
CV v7,v38,v39
CV v12,v40,v41
CV v13,v42,v43
CV v14,v44,v45
CV v15,v46,v47
v_smfmac_f32_16x16x64_bf16 v[144:147], v[236:239], v[0:7], v234
s_nop 0
v_smfmac_f32_16x16x64_bf16 v[148:151], v[236:239], v[8:15], v234
s_nop 9
s_cmp_eq_u32 s73, 0
s_cbranch_scc1 LBACK0_%=
s_cmp_eq_u32 s73, 2
s_cbranch_scc1 LBACK2_%=
s_endpgm
LSITE1_%=:
s_mov_b32 s73, 1
s_mov_b32 s74, 16384
s_mov_b32 s75, 32768
s_branch LRARE102_%=
LSITE3_%=:
s_mov_b32 s73, 3
s_mov_b32 s74, 49152
s_mov_b32 s75, 0
s_branch LRARE102_%=
LRARE102_%=:
s_nop 15
s_waitcnt vmcnt(0) lgkmcnt(0)
v_add_u32 v249, s74, v224
RD a[192:195],v249 offset:0
RD a[208:211],v249 offset:4096
RD a[224:227],v249 offset:8192
RD a[240:243],v249 offset:12288
v_add_u32 v250, s74, v225
RD a[196:199],v250 offset:0
RD a[212:215],v250 offset:4096
RD a[228:231],v250 offset:8192
RD a[244:247],v250 offset:12288
v_add_u32 v251, s74, v226
RD a[200:203],v251 offset:0
RD a[216:219],v251 offset:4096
RD a[232:235],v251 offset:8192
RD a[248:251],v251 offset:12288
v_add_u32 v254, s74, v227
RD a[204:207],v254 offset:0
RD a[220:223],v254 offset:4096
RD a[236:239],v254 offset:8192
RD a[252:255],v254 offset:12288
s_waitcnt lgkmcnt(0)
MM v[64:67],a[192:195],v[160:163],v[128:131]
MM v[64:67],a[196:199],v[176:179],v[64:67]
MM v[64:67],a[200:203],v[192:195],v[64:67]
MM v[64:67],a[204:207],v[208:211],v[64:67]
MM v[68:71],a[220:223],v[208:211],v[128:131]
MM v[68:71],a[216:219],v[192:195],v[68:71]
MM v[68:71],a[212:215],v[176:179],v[68:71]
MM v[68:71],a[208:211],v[160:163],v[68:71]
MM v[76:79],a[208:211],v[164:167],v[132:135]
MM v[76:79],a[212:215],v[180:183],v[76:79]
MM v[76:79],a[216:219],v[196:199],v[76:79]
MM v[76:79],a[220:223],v[212:215],v[76:79]
MM v[72:75],a[204:207],v[212:215],v[132:135]
MM v[72:75],a[200:203],v[196:199],v[72:75]
MM v[72:75],a[196:199],v[180:183],v[72:75]
MM v[72:75],a[192:195],v[164:167],v[72:75]
MM v[96:99],a[224:227],v[160:163],v[128:131]
MM v[96:99],a[228:231],v[176:179],v[96:99]
MM v[96:99],a[232:235],v[192:195],v[96:99]
MM v[96:99],a[236:239],v[208:211],v[96:99]
MM v[100:103],a[252:255],v[208:211],v[128:131]
MM v[100:103],a[248:251],v[192:195],v[100:103]
MM v[100:103],a[244:247],v[176:179],v[100:103]
MM v[100:103],a[240:243],v[160:163],v[100:103]
MM v[108:111],a[240:243],v[164:167],v[132:135]
MM v[108:111],a[244:247],v[180:183],v[108:111]
MM v[108:111],a[248:251],v[196:199],v[108:111]
MM v[108:111],a[252:255],v[212:215],v[108:111]
MM v[104:107],a[236:239],v[212:215],v[132:135]
MM v[104:107],a[232:235],v[196:199],v[104:107]
MM v[104:107],a[228:231],v[180:183],v[104:107]
MM v[104:107],a[224:227],v[164:167],v[104:107]
s_nop 15
v_add_u32 v249, s75, v224
RD a[192:195],v249 offset:0
RD a[208:211],v249 offset:4096
RD a[224:227],v249 offset:8192
RD a[240:243],v249 offset:12288
v_add_u32 v250, s75, v225
RD a[196:199],v250 offset:0
RD a[212:215],v250 offset:4096
RD a[228:231],v250 offset:8192
RD a[244:247],v250 offset:12288
v_add_u32 v251, s75, v226
RD a[200:203],v251 offset:0
RD a[216:219],v251 offset:4096
RD a[232:235],v251 offset:8192
RD a[248:251],v251 offset:12288
v_add_u32 v254, s75, v227
RD a[204:207],v254 offset:0
RD a[220:223],v254 offset:4096
RD a[236:239],v254 offset:8192
RD a[252:255],v254 offset:12288
s_waitcnt lgkmcnt(0)
v_max3_f32 v249, v64, v65, v66
v_max3_f32 v251, v96, v97, v98
v_max3_f32 v249, v249, v67, v68
v_max3_f32 v251, v251, v99, v100
v_max3_f32 v249, v249, v69, v70
v_max3_f32 v251, v251, v101, v102
v_max3_f32 v249, v249, v71, v103
v_max_f32 v249, v249, v251
v_mov_b32 v251, v249
s_nop 1
v_permlane16_swap_b32 v249, v251
v_max_f32 v249, v249, v251
v_mov_b32 v251, v249
s_nop 1
v_permlane32_swap_b32 v249, v251
v_max_f32 v249, v249, v251
v_log_f32 v250, v244
s_nop 0
v_max3_f32 v249, 0, v249, v250
v_sub_f32 v250, 0, v249
EX v250,v250
v_sub_f32 v128, v128, v249
v_sub_f32 v129, v129, v249
v_sub_f32 v130, v130, v249
v_sub_f32 v131, v131, v249
v_sub_f32 v0, v0, v249
v_sub_f32 v64, v64, v249
v_sub_f32 v1, v1, v249
v_sub_f32 v65, v65, v249
v_sub_f32 v2, v2, v249
v_sub_f32 v66, v66, v249
v_sub_f32 v3, v3, v249
v_sub_f32 v67, v67, v249
v_sub_f32 v4, v4, v249
v_sub_f32 v68, v68, v249
v_sub_f32 v5, v5, v249
v_sub_f32 v69, v69, v249
v_sub_f32 v6, v6, v249
v_sub_f32 v70, v70, v249
v_sub_f32 v7, v7, v249
v_sub_f32 v71, v71, v249
v_sub_f32 v32, v32, v249
v_sub_f32 v96, v96, v249
v_sub_f32 v33, v33, v249
v_sub_f32 v97, v97, v249
v_sub_f32 v34, v34, v249
v_sub_f32 v98, v98, v249
v_sub_f32 v35, v35, v249
v_sub_f32 v99, v99, v249
v_sub_f32 v36, v36, v249
v_sub_f32 v100, v100, v249
v_sub_f32 v37, v37, v249
v_sub_f32 v101, v101, v249
v_sub_f32 v38, v38, v249
v_sub_f32 v102, v102, v249
v_sub_f32 v39, v39, v249
v_sub_f32 v103, v103, v249
v_mul_f32 v144, v244, v250
v_mov_b32 v145, 0
AR v254,a0
v_mul_f32 v254, v254, v250
AW a0,v254
AR v251,a1
v_mul_f32 v251, v251, v250
AW a1,v251
AR v254,a2
v_mul_f32 v254, v254, v250
AW a2,v254
AR v251,a3
v_mul_f32 v251, v251, v250
AW a3,v251
AR v254,a4
v_mul_f32 v254, v254, v250
AW a4,v254
AR v251,a5
v_mul_f32 v251, v251, v250
AW a5,v251
AR v254,a6
v_mul_f32 v254, v254, v250
AW a6,v254
AR v251,a7
v_mul_f32 v251, v251, v250
AW a7,v251
AR v254,a32
v_mul_f32 v254, v254, v250
AW a32,v254
AR v251,a33
v_mul_f32 v251, v251, v250
AW a33,v251
AR v254,a34
v_mul_f32 v254, v254, v250
AW a34,v254
AR v251,a35
v_mul_f32 v251, v251, v250
AW a35,v251
AR v254,a36
v_mul_f32 v254, v254, v250
AW a36,v254
AR v251,a37
v_mul_f32 v251, v251, v250
AW a37,v251
AR v254,a38
v_mul_f32 v254, v254, v250
AW a38,v254
AR v251,a39
v_mul_f32 v251, v251, v250
AW a39,v251
AR v254,a64
v_mul_f32 v254, v254, v250
AW a64,v254
AR v251,a65
v_mul_f32 v251, v251, v250
AW a65,v251
AR v254,a66
v_mul_f32 v254, v254, v250
AW a66,v254
AR v251,a67
v_mul_f32 v251, v251, v250
AW a67,v251
AR v254,a68
v_mul_f32 v254, v254, v250
AW a68,v254
AR v251,a69
v_mul_f32 v251, v251, v250
AW a69,v251
AR v254,a70
v_mul_f32 v254, v254, v250
AW a70,v254
AR v251,a71
v_mul_f32 v251, v251, v250
AW a71,v251
AR v254,a96
v_mul_f32 v254, v254, v250
AW a96,v254
AR v251,a97
v_mul_f32 v251, v251, v250
AW a97,v251
AR v254,a98
v_mul_f32 v254, v254, v250
AW a98,v254
AR v251,a99
v_mul_f32 v251, v251, v250
AW a99,v251
AR v254,a100
v_mul_f32 v254, v254, v250
AW a100,v254
AR v251,a101
v_mul_f32 v251, v251, v250
AW a101,v251
AR v254,a102
v_mul_f32 v254, v254, v250
AW a102,v254
AR v251,a103
v_mul_f32 v251, v251, v250
AW a103,v251
v_max3_f32 v249, v72, v73, v74
v_max3_f32 v251, v104, v105, v106
v_max3_f32 v249, v249, v75, v76
v_max3_f32 v251, v251, v107, v108
v_max3_f32 v249, v249, v77, v78
v_max3_f32 v251, v251, v109, v110
v_max3_f32 v249, v249, v79, v111
v_max_f32 v249, v249, v251
v_mov_b32 v251, v249
s_nop 1
v_permlane16_swap_b32 v249, v251
v_max_f32 v249, v249, v251
v_mov_b32 v251, v249
s_nop 1
v_permlane32_swap_b32 v249, v251
v_max_f32 v249, v249, v251
v_log_f32 v250, v245
s_nop 0
v_max3_f32 v249, 0, v249, v250
v_sub_f32 v250, 0, v249
EX v250,v250
v_sub_f32 v132, v132, v249
v_sub_f32 v133, v133, v249
v_sub_f32 v134, v134, v249
v_sub_f32 v135, v135, v249
v_sub_f32 v8, v8, v249
v_sub_f32 v72, v72, v249
v_sub_f32 v9, v9, v249
v_sub_f32 v73, v73, v249
v_sub_f32 v10, v10, v249
v_sub_f32 v74, v74, v249
v_sub_f32 v11, v11, v249
v_sub_f32 v75, v75, v249
v_sub_f32 v12, v12, v249
v_sub_f32 v76, v76, v249
v_sub_f32 v13, v13, v249
v_sub_f32 v77, v77, v249
v_sub_f32 v14, v14, v249
v_sub_f32 v78, v78, v249
v_sub_f32 v15, v15, v249
v_sub_f32 v79, v79, v249
v_sub_f32 v40, v40, v249
v_sub_f32 v104, v104, v249
v_sub_f32 v41, v41, v249
v_sub_f32 v105, v105, v249
v_sub_f32 v42, v42, v249
v_sub_f32 v106, v106, v249
v_sub_f32 v43, v43, v249
v_sub_f32 v107, v107, v249
v_sub_f32 v44, v44, v249
v_sub_f32 v108, v108, v249
v_sub_f32 v45, v45, v249
v_sub_f32 v109, v109, v249
v_sub_f32 v46, v46, v249
v_sub_f32 v110, v110, v249
v_sub_f32 v47, v47, v249
v_sub_f32 v111, v111, v249
v_mul_f32 v148, v245, v250
v_mov_b32 v149, 0
AR v254,a8
v_mul_f32 v254, v254, v250
AW a8,v254
AR v251,a9
v_mul_f32 v251, v251, v250
AW a9,v251
AR v254,a10
v_mul_f32 v254, v254, v250
AW a10,v254
AR v251,a11
v_mul_f32 v251, v251, v250
AW a11,v251
AR v254,a12
v_mul_f32 v254, v254, v250
AW a12,v254
AR v251,a13
v_mul_f32 v251, v251, v250
)ASM"
R"ASM(
AW a13,v251
AR v254,a14
v_mul_f32 v254, v254, v250
AW a14,v254
AR v251,a15
v_mul_f32 v251, v251, v250
AW a15,v251
AR v254,a40
v_mul_f32 v254, v254, v250
AW a40,v254
AR v251,a41
v_mul_f32 v251, v251, v250
AW a41,v251
AR v254,a42
v_mul_f32 v254, v254, v250
AW a42,v254
AR v251,a43
v_mul_f32 v251, v251, v250
AW a43,v251
AR v254,a44
v_mul_f32 v254, v254, v250
AW a44,v254
AR v251,a45
v_mul_f32 v251, v251, v250
AW a45,v251
AR v254,a46
v_mul_f32 v254, v254, v250
AW a46,v254
AR v251,a47
v_mul_f32 v251, v251, v250
AW a47,v251
AR v254,a72
v_mul_f32 v254, v254, v250
AW a72,v254
AR v251,a73
v_mul_f32 v251, v251, v250
AW a73,v251
AR v254,a74
v_mul_f32 v254, v254, v250
AW a74,v254
AR v251,a75
v_mul_f32 v251, v251, v250
AW a75,v251
AR v254,a76
v_mul_f32 v254, v254, v250
AW a76,v254
AR v251,a77
v_mul_f32 v251, v251, v250
AW a77,v251
AR v254,a78
v_mul_f32 v254, v254, v250
AW a78,v254
AR v251,a79
v_mul_f32 v251, v251, v250
AW a79,v251
AR v254,a104
v_mul_f32 v254, v254, v250
AW a104,v254
AR v251,a105
v_mul_f32 v251, v251, v250
AW a105,v251
AR v254,a106
v_mul_f32 v254, v254, v250
AW a106,v254
AR v251,a107
v_mul_f32 v251, v251, v250
AW a107,v251
AR v254,a108
v_mul_f32 v254, v254, v250
AW a108,v254
AR v251,a109
v_mul_f32 v251, v251, v250
AW a109,v251
AR v254,a110
v_mul_f32 v254, v254, v250
AW a110,v254
AR v251,a111
v_mul_f32 v251, v251, v250
AW a111,v251
EX v64,v64
EX v65,v65
EX v66,v66
EX v67,v67
EX v68,v68
EX v69,v69
EX v70,v70
EX v71,v71
EX v72,v72
EX v73,v73
EX v74,v74
EX v75,v75
EX v76,v76
EX v77,v77
EX v78,v78
EX v79,v79
EX v96,v96
EX v97,v97
EX v98,v98
EX v99,v99
EX v100,v100
EX v101,v101
EX v102,v102
EX v103,v103
EX v104,v104
EX v105,v105
EX v106,v106
EX v107,v107
EX v108,v108
EX v109,v109
EX v110,v110
EX v111,v111
CV v64,v64,v65
CV v65,v66,v67
CV v66,v68,v69
CV v67,v70,v71
CV v72,v72,v73
CV v73,v74,v75
CV v74,v76,v77
CV v75,v78,v79
CV v68,v96,v97
CV v69,v98,v99
CV v70,v100,v101
CV v71,v102,v103
CV v76,v104,v105
CV v77,v106,v107
CV v78,v108,v109
CV v79,v110,v111
v_smfmac_f32_16x16x64_bf16 v[144:147], v[236:239], v[64:71], v234
s_nop 0
v_smfmac_f32_16x16x64_bf16 v[148:151], v[236:239], v[72:79], v234
s_nop 9
s_cmp_eq_u32 s73, 1
s_cbranch_scc1 LBACK1_%=
s_cmp_eq_u32 s73, 3
s_cbranch_scc1 LBACK3_%=
s_endpgm
LSITE4_%=:
s_mov_b32 s73, 4
s_mov_b32 s74, 0
s_mov_b32 s75, 16384
s_branch LRARE004_%=
LSITE6_%=:
s_mov_b32 s73, 6
s_mov_b32 s74, 32768
s_mov_b32 s75, 49152
s_branch LRARE004_%=
LSITE8_%=:
s_mov_b32 s73, 8
s_mov_b32 s74, 0
s_mov_b32 s75, 16384
s_branch LRARE004_%=
LSITE10_%=:
s_mov_b32 s73, 10
s_mov_b32 s74, 32768
s_mov_b32 s75, 49152
s_branch LRARE004_%=
LSITE12_%=:
s_mov_b32 s73, 12
s_mov_b32 s74, 0
s_mov_b32 s75, 16384
s_branch LRARE004_%=
LSITE14_%=:
s_mov_b32 s73, 14
s_mov_b32 s74, 32768
s_mov_b32 s75, 49152
s_branch LRARE004_%=
LSITE16_%=:
s_mov_b32 s73, 16
s_mov_b32 s74, 0
s_mov_b32 s75, 16384
s_branch LRARE004_%=
LSITE18_%=:
s_mov_b32 s73, 18
s_mov_b32 s74, 32768
s_mov_b32 s75, 49152
s_branch LRARE004_%=
LSITE20_%=:
s_mov_b32 s73, 20
s_mov_b32 s74, 0
s_mov_b32 s75, 16384
s_branch LRARE004_%=
LSITE22_%=:
s_mov_b32 s73, 22
s_mov_b32 s74, 32768
s_mov_b32 s75, 49152
s_branch LRARE004_%=
LSITE24_%=:
s_mov_b32 s73, 24
s_mov_b32 s74, 0
s_mov_b32 s75, 16384
s_branch LRARE004_%=
LSITE26_%=:
s_mov_b32 s73, 26
s_mov_b32 s74, 32768
s_mov_b32 s75, 49152
s_branch LRARE004_%=
LSITE28_%=:
s_mov_b32 s73, 28
s_mov_b32 s74, 0
s_mov_b32 s75, 16384
s_branch LRARE004_%=
LSITE30_%=:
s_mov_b32 s73, 30
s_mov_b32 s74, 32768
s_mov_b32 s75, 49152
s_branch LRARE004_%=
LSITE32_%=:
s_mov_b32 s73, 32
s_mov_b32 s74, 0
s_mov_b32 s75, 16384
s_branch LRARE004_%=
LSITE34_%=:
s_mov_b32 s73, 34
s_mov_b32 s74, 32768
s_mov_b32 s75, 49152
s_branch LRARE004_%=
LRARE004_%=:
s_nop 15
s_waitcnt vmcnt(0) lgkmcnt(0)
v_add_u32 v249, s74, v224
RD a[192:195],v249 offset:0
RD a[208:211],v249 offset:4096
RD a[224:227],v249 offset:8192
RD a[240:243],v249 offset:12288
v_add_u32 v250, s74, v225
RD a[196:199],v250 offset:0
RD a[212:215],v250 offset:4096
RD a[228:231],v250 offset:8192
RD a[244:247],v250 offset:12288
v_add_u32 v251, s74, v226
RD a[200:203],v251 offset:0
RD a[216:219],v251 offset:4096
RD a[232:235],v251 offset:8192
RD a[248:251],v251 offset:12288
v_add_u32 v254, s74, v227
RD a[204:207],v254 offset:0
RD a[220:223],v254 offset:4096
RD a[236:239],v254 offset:8192
RD a[252:255],v254 offset:12288
s_waitcnt lgkmcnt(0)
MM v[0:3],a[192:195],v[160:163],v[128:131]
MM v[0:3],a[196:199],v[176:179],v[0:3]
MM v[0:3],a[200:203],v[192:195],v[0:3]
MM v[0:3],a[204:207],v[208:211],v[0:3]
MM v[4:7],a[220:223],v[208:211],v[128:131]
MM v[4:7],a[216:219],v[192:195],v[4:7]
MM v[4:7],a[212:215],v[176:179],v[4:7]
MM v[4:7],a[208:211],v[160:163],v[4:7]
MM v[12:15],a[208:211],v[164:167],v[132:135]
MM v[12:15],a[212:215],v[180:183],v[12:15]
MM v[12:15],a[216:219],v[196:199],v[12:15]
MM v[12:15],a[220:223],v[212:215],v[12:15]
MM v[8:11],a[204:207],v[212:215],v[132:135]
MM v[8:11],a[200:203],v[196:199],v[8:11]
MM v[8:11],a[196:199],v[180:183],v[8:11]
MM v[8:11],a[192:195],v[164:167],v[8:11]
MM v[16:19],a[192:195],v[168:171],v[136:139]
MM v[16:19],a[196:199],v[184:187],v[16:19]
MM v[16:19],a[200:203],v[200:203],v[16:19]
MM v[16:19],a[204:207],v[216:219],v[16:19]
MM v[20:23],a[220:223],v[216:219],v[136:139]
MM v[20:23],a[216:219],v[200:203],v[20:23]
MM v[20:23],a[212:215],v[184:187],v[20:23]
MM v[20:23],a[208:211],v[168:171],v[20:23]
MM v[28:31],a[208:211],v[172:175],v[140:143]
MM v[28:31],a[212:215],v[188:191],v[28:31]
MM v[28:31],a[216:219],v[204:207],v[28:31]
MM v[28:31],a[220:223],v[220:223],v[28:31]
MM v[24:27],a[204:207],v[220:223],v[140:143]
MM v[24:27],a[200:203],v[204:207],v[24:27]
MM v[24:27],a[196:199],v[188:191],v[24:27]
MM v[24:27],a[192:195],v[172:175],v[24:27]
MM v[32:35],a[224:227],v[160:163],v[128:131]
MM v[32:35],a[228:231],v[176:179],v[32:35]
MM v[32:35],a[232:235],v[192:195],v[32:35]
MM v[32:35],a[236:239],v[208:211],v[32:35]
MM v[36:39],a[252:255],v[208:211],v[128:131]
MM v[36:39],a[248:251],v[192:195],v[36:39]
MM v[36:39],a[244:247],v[176:179],v[36:39]
MM v[36:39],a[240:243],v[160:163],v[36:39]
MM v[44:47],a[240:243],v[164:167],v[132:135]
MM v[44:47],a[244:247],v[180:183],v[44:47]
MM v[44:47],a[248:251],v[196:199],v[44:47]
MM v[44:47],a[252:255],v[212:215],v[44:47]
MM v[40:43],a[236:239],v[212:215],v[132:135]
MM v[40:43],a[232:235],v[196:199],v[40:43]
MM v[40:43],a[228:231],v[180:183],v[40:43]
MM v[40:43],a[224:227],v[164:167],v[40:43]
MM v[48:51],a[224:227],v[168:171],v[136:139]
MM v[48:51],a[228:231],v[184:187],v[48:51]
MM v[48:51],a[232:235],v[200:203],v[48:51]
MM v[48:51],a[236:239],v[216:219],v[48:51]
MM v[52:55],a[252:255],v[216:219],v[136:139]
MM v[52:55],a[248:251],v[200:203],v[52:55]
MM v[52:55],a[244:247],v[184:187],v[52:55]
MM v[52:55],a[240:243],v[168:171],v[52:55]
MM v[60:63],a[240:243],v[172:175],v[140:143]
MM v[60:63],a[244:247],v[188:191],v[60:63]
MM v[60:63],a[248:251],v[204:207],v[60:63]
MM v[60:63],a[252:255],v[220:223],v[60:63]
MM v[56:59],a[236:239],v[220:223],v[140:143]
MM v[56:59],a[232:235],v[204:207],v[56:59]
MM v[56:59],a[228:231],v[188:191],v[56:59]
MM v[56:59],a[224:227],v[172:175],v[56:59]
s_nop 15
v_add_u32 v249, s75, v224
RD a[192:195],v249 offset:0
RD a[208:211],v249 offset:4096
RD a[224:227],v249 offset:8192
RD a[240:243],v249 offset:12288
v_add_u32 v250, s75, v225
RD a[196:199],v250 offset:0
RD a[212:215],v250 offset:4096
RD a[228:231],v250 offset:8192
RD a[244:247],v250 offset:12288
v_add_u32 v251, s75, v226
RD a[200:203],v251 offset:0
RD a[216:219],v251 offset:4096
RD a[232:235],v251 offset:8192
RD a[248:251],v251 offset:12288
v_add_u32 v254, s75, v227
RD a[204:207],v254 offset:0
RD a[220:223],v254 offset:4096
RD a[236:239],v254 offset:8192
RD a[252:255],v254 offset:12288
s_waitcnt lgkmcnt(0)
v_max3_f32 v249, v0, v1, v2
v_max3_f32 v251, v32, v33, v34
v_max3_f32 v249, v249, v3, v4
v_max3_f32 v251, v251, v35, v36
v_max3_f32 v249, v249, v5, v6
v_max3_f32 v251, v251, v37, v38
v_max3_f32 v249, v249, v7, v39
v_max_f32 v249, v249, v251
v_mov_b32 v251, v249
s_nop 1
v_permlane16_swap_b32 v249, v251
v_max_f32 v249, v249, v251
v_mov_b32 v251, v249
s_nop 1
v_permlane32_swap_b32 v249, v251
v_max_f32 v249, v249, v251
v_log_f32 v250, v244
s_nop 0
v_max3_f32 v249, 0, v249, v250
v_sub_f32 v250, 0, v249
EX v250,v250
v_sub_f32 v128, v128, v249
v_sub_f32 v129, v129, v249
v_sub_f32 v130, v130, v249
v_sub_f32 v131, v131, v249
v_sub_f32 v64, v64, v249
v_sub_f32 v0, v0, v249
v_sub_f32 v65, v65, v249
v_sub_f32 v1, v1, v249
v_sub_f32 v66, v66, v249
v_sub_f32 v2, v2, v249
v_sub_f32 v67, v67, v249
v_sub_f32 v3, v3, v249
v_sub_f32 v68, v68, v249
v_sub_f32 v4, v4, v249
v_sub_f32 v69, v69, v249
v_sub_f32 v5, v5, v249
v_sub_f32 v70, v70, v249
v_sub_f32 v6, v6, v249
v_sub_f32 v71, v71, v249
v_sub_f32 v7, v7, v249
v_sub_f32 v96, v96, v249
v_sub_f32 v32, v32, v249
v_sub_f32 v97, v97, v249
v_sub_f32 v33, v33, v249
v_sub_f32 v98, v98, v249
v_sub_f32 v34, v34, v249
v_sub_f32 v99, v99, v249
v_sub_f32 v35, v35, v249
v_sub_f32 v100, v100, v249
v_sub_f32 v36, v36, v249
v_sub_f32 v101, v101, v249
v_sub_f32 v37, v37, v249
v_sub_f32 v102, v102, v249
v_sub_f32 v38, v38, v249
v_sub_f32 v103, v103, v249
v_sub_f32 v39, v39, v249
v_mul_f32 v144, v244, v250
v_mov_b32 v145, 0
AR v254,a0
v_mul_f32 v254, v254, v250
AW a0,v254
AR v251,a1
v_mul_f32 v251, v251, v250
AW a1,v251
AR v254,a2
v_mul_f32 v254, v254, v250
AW a2,v254
AR v251,a3
v_mul_f32 v251, v251, v250
AW a3,v251
AR v254,a4
v_mul_f32 v254, v254, v250
AW a4,v254
AR v251,a5
v_mul_f32 v251, v251, v250
AW a5,v251
AR v254,a6
v_mul_f32 v254, v254, v250
AW a6,v254
AR v251,a7
v_mul_f32 v251, v251, v250
AW a7,v251
AR v254,a32
v_mul_f32 v254, v254, v250
AW a32,v254
AR v251,a33
v_mul_f32 v251, v251, v250
AW a33,v251
AR v254,a34
v_mul_f32 v254, v254, v250
AW a34,v254
AR v251,a35
v_mul_f32 v251, v251, v250
AW a35,v251
AR v254,a36
v_mul_f32 v254, v254, v250
AW a36,v254
AR v251,a37
v_mul_f32 v251, v251, v250
AW a37,v251
AR v254,a38
v_mul_f32 v254, v254, v250
AW a38,v254
AR v251,a39
v_mul_f32 v251, v251, v250
AW a39,v251
AR v254,a64
v_mul_f32 v254, v254, v250
AW a64,v254
AR v251,a65
v_mul_f32 v251, v251, v250
AW a65,v251
AR v254,a66
v_mul_f32 v254, v254, v250
AW a66,v254
AR v251,a67
v_mul_f32 v251, v251, v250
AW a67,v251
AR v254,a68
v_mul_f32 v254, v254, v250
AW a68,v254
AR v251,a69
v_mul_f32 v251, v251, v250
AW a69,v251
AR v254,a70
v_mul_f32 v254, v254, v250
AW a70,v254
AR v251,a71
v_mul_f32 v251, v251, v250
AW a71,v251
AR v254,a96
v_mul_f32 v254, v254, v250
AW a96,v254
AR v251,a97
v_mul_f32 v251, v251, v250
AW a97,v251
AR v254,a98
v_mul_f32 v254, v254, v250
AW a98,v254
AR v251,a99
v_mul_f32 v251, v251, v250
AW a99,v251
AR v254,a100
v_mul_f32 v254, v254, v250
AW a100,v254
AR v251,a101
v_mul_f32 v251, v251, v250
AW a101,v251
AR v254,a102
v_mul_f32 v254, v254, v250
AW a102,v254
AR v251,a103
v_mul_f32 v251, v251, v250
AW a103,v251
v_max3_f32 v249, v8, v9, v10
v_max3_f32 v251, v40, v41, v42
v_max3_f32 v249, v249, v11, v12
v_max3_f32 v251, v251, v43, v44
v_max3_f32 v249, v249, v13, v14
v_max3_f32 v251, v251, v45, v46
v_max3_f32 v249, v249, v15, v47
v_max_f32 v249, v249, v251
v_mov_b32 v251, v249
s_nop 1
v_permlane16_swap_b32 v249, v251
v_max_f32 v249, v249, v251
v_mov_b32 v251, v249
s_nop 1
v_permlane32_swap_b32 v249, v251
v_max_f32 v249, v249, v251
v_log_f32 v250, v245
s_nop 0
v_max3_f32 v249, 0, v249, v250
v_sub_f32 v250, 0, v249
EX v250,v250
v_sub_f32 v132, v132, v249
v_sub_f32 v133, v133, v249
v_sub_f32 v134, v134, v249
v_sub_f32 v135, v135, v249
v_sub_f32 v72, v72, v249
v_sub_f32 v8, v8, v249
v_sub_f32 v73, v73, v249
v_sub_f32 v9, v9, v249
v_sub_f32 v74, v74, v249
v_sub_f32 v10, v10, v249
v_sub_f32 v75, v75, v249
v_sub_f32 v11, v11, v249
v_sub_f32 v76, v76, v249
v_sub_f32 v12, v12, v249
v_sub_f32 v77, v77, v249
v_sub_f32 v13, v13, v249
v_sub_f32 v78, v78, v249
v_sub_f32 v14, v14, v249
v_sub_f32 v79, v79, v249
v_sub_f32 v15, v15, v249
v_sub_f32 v104, v104, v249
v_sub_f32 v40, v40, v249
v_sub_f32 v105, v105, v249
v_sub_f32 v41, v41, v249
v_sub_f32 v106, v106, v249
v_sub_f32 v42, v42, v249
v_sub_f32 v107, v107, v249
)ASM"
R"ASM(
v_sub_f32 v43, v43, v249
v_sub_f32 v108, v108, v249
v_sub_f32 v44, v44, v249
v_sub_f32 v109, v109, v249
v_sub_f32 v45, v45, v249
v_sub_f32 v110, v110, v249
v_sub_f32 v46, v46, v249
v_sub_f32 v111, v111, v249
v_sub_f32 v47, v47, v249
v_mul_f32 v148, v245, v250
v_mov_b32 v149, 0
AR v254,a8
v_mul_f32 v254, v254, v250
AW a8,v254
AR v251,a9
v_mul_f32 v251, v251, v250
AW a9,v251
AR v254,a10
v_mul_f32 v254, v254, v250
AW a10,v254
AR v251,a11
v_mul_f32 v251, v251, v250
AW a11,v251
AR v254,a12
v_mul_f32 v254, v254, v250
AW a12,v254
AR v251,a13
v_mul_f32 v251, v251, v250
AW a13,v251
AR v254,a14
v_mul_f32 v254, v254, v250
AW a14,v254
AR v251,a15
v_mul_f32 v251, v251, v250
AW a15,v251
AR v254,a40
v_mul_f32 v254, v254, v250
AW a40,v254
AR v251,a41
v_mul_f32 v251, v251, v250
AW a41,v251
AR v254,a42
v_mul_f32 v254, v254, v250
AW a42,v254
AR v251,a43
v_mul_f32 v251, v251, v250
AW a43,v251
AR v254,a44
v_mul_f32 v254, v254, v250
AW a44,v254
AR v251,a45
v_mul_f32 v251, v251, v250
AW a45,v251
AR v254,a46
v_mul_f32 v254, v254, v250
AW a46,v254
AR v251,a47
v_mul_f32 v251, v251, v250
AW a47,v251
AR v254,a72
v_mul_f32 v254, v254, v250
AW a72,v254
AR v251,a73
v_mul_f32 v251, v251, v250
AW a73,v251
AR v254,a74
v_mul_f32 v254, v254, v250
AW a74,v254
AR v251,a75
v_mul_f32 v251, v251, v250
AW a75,v251
AR v254,a76
v_mul_f32 v254, v254, v250
AW a76,v254
AR v251,a77
v_mul_f32 v251, v251, v250
AW a77,v251
AR v254,a78
v_mul_f32 v254, v254, v250
AW a78,v254
AR v251,a79
v_mul_f32 v251, v251, v250
AW a79,v251
AR v254,a104
v_mul_f32 v254, v254, v250
AW a104,v254
AR v251,a105
v_mul_f32 v251, v251, v250
AW a105,v251
AR v254,a106
v_mul_f32 v254, v254, v250
AW a106,v254
AR v251,a107
v_mul_f32 v251, v251, v250
AW a107,v251
AR v254,a108
v_mul_f32 v254, v254, v250
AW a108,v254
AR v251,a109
v_mul_f32 v251, v251, v250
AW a109,v251
AR v254,a110
v_mul_f32 v254, v254, v250
AW a110,v254
AR v251,a111
v_mul_f32 v251, v251, v250
AW a111,v251
v_max3_f32 v249, v16, v17, v18
v_max3_f32 v251, v48, v49, v50
v_max3_f32 v249, v249, v19, v20
v_max3_f32 v251, v251, v51, v52
v_max3_f32 v249, v249, v21, v22
v_max3_f32 v251, v251, v53, v54
v_max3_f32 v249, v249, v23, v55
v_max_f32 v249, v249, v251
v_mov_b32 v251, v249
s_nop 1
v_permlane16_swap_b32 v249, v251
v_max_f32 v249, v249, v251
v_mov_b32 v251, v249
s_nop 1
v_permlane32_swap_b32 v249, v251
v_max_f32 v249, v249, v251
v_log_f32 v250, v246
s_nop 0
v_max3_f32 v249, 0, v249, v250
v_sub_f32 v250, 0, v249
EX v250,v250
v_sub_f32 v136, v136, v249
v_sub_f32 v137, v137, v249
v_sub_f32 v138, v138, v249
v_sub_f32 v139, v139, v249
v_sub_f32 v80, v80, v249
v_sub_f32 v16, v16, v249
v_sub_f32 v81, v81, v249
v_sub_f32 v17, v17, v249
v_sub_f32 v82, v82, v249
v_sub_f32 v18, v18, v249
v_sub_f32 v83, v83, v249
v_sub_f32 v19, v19, v249
v_sub_f32 v84, v84, v249
v_sub_f32 v20, v20, v249
v_sub_f32 v85, v85, v249
v_sub_f32 v21, v21, v249
v_sub_f32 v86, v86, v249
v_sub_f32 v22, v22, v249
v_sub_f32 v87, v87, v249
v_sub_f32 v23, v23, v249
v_sub_f32 v112, v112, v249
v_sub_f32 v48, v48, v249
v_sub_f32 v113, v113, v249
v_sub_f32 v49, v49, v249
v_sub_f32 v114, v114, v249
v_sub_f32 v50, v50, v249
v_sub_f32 v115, v115, v249
v_sub_f32 v51, v51, v249
v_sub_f32 v116, v116, v249
v_sub_f32 v52, v52, v249
v_sub_f32 v117, v117, v249
v_sub_f32 v53, v53, v249
v_sub_f32 v118, v118, v249
v_sub_f32 v54, v54, v249
v_sub_f32 v119, v119, v249
v_sub_f32 v55, v55, v249
v_mul_f32 v152, v246, v250
v_mov_b32 v153, 0
AR v254,a16
v_mul_f32 v254, v254, v250
AW a16,v254
AR v251,a17
v_mul_f32 v251, v251, v250
AW a17,v251
AR v254,a18
v_mul_f32 v254, v254, v250
AW a18,v254
AR v251,a19
v_mul_f32 v251, v251, v250
AW a19,v251
AR v254,a20
v_mul_f32 v254, v254, v250
AW a20,v254
AR v251,a21
v_mul_f32 v251, v251, v250
AW a21,v251
AR v254,a22
v_mul_f32 v254, v254, v250
AW a22,v254
AR v251,a23
v_mul_f32 v251, v251, v250
AW a23,v251
AR v254,a48
v_mul_f32 v254, v254, v250
AW a48,v254
AR v251,a49
v_mul_f32 v251, v251, v250
AW a49,v251
AR v254,a50
v_mul_f32 v254, v254, v250
AW a50,v254
AR v251,a51
v_mul_f32 v251, v251, v250
AW a51,v251
AR v254,a52
v_mul_f32 v254, v254, v250
AW a52,v254
AR v251,a53
v_mul_f32 v251, v251, v250
AW a53,v251
AR v254,a54
v_mul_f32 v254, v254, v250
AW a54,v254
AR v251,a55
v_mul_f32 v251, v251, v250
AW a55,v251
AR v254,a80
v_mul_f32 v254, v254, v250
AW a80,v254
AR v251,a81
v_mul_f32 v251, v251, v250
AW a81,v251
AR v254,a82
v_mul_f32 v254, v254, v250
AW a82,v254
AR v251,a83
v_mul_f32 v251, v251, v250
AW a83,v251
AR v254,a84
v_mul_f32 v254, v254, v250
AW a84,v254
AR v251,a85
v_mul_f32 v251, v251, v250
AW a85,v251
AR v254,a86
v_mul_f32 v254, v254, v250
AW a86,v254
AR v251,a87
v_mul_f32 v251, v251, v250
AW a87,v251
AR v254,a112
v_mul_f32 v254, v254, v250
AW a112,v254
AR v251,a113
v_mul_f32 v251, v251, v250
AW a113,v251
AR v254,a114
v_mul_f32 v254, v254, v250
AW a114,v254
AR v251,a115
v_mul_f32 v251, v251, v250
AW a115,v251
AR v254,a116
v_mul_f32 v254, v254, v250
AW a116,v254
AR v251,a117
v_mul_f32 v251, v251, v250
AW a117,v251
AR v254,a118
v_mul_f32 v254, v254, v250
AW a118,v254
AR v251,a119
v_mul_f32 v251, v251, v250
AW a119,v251
v_max3_f32 v249, v24, v25, v26
v_max3_f32 v251, v56, v57, v58
v_max3_f32 v249, v249, v27, v28
v_max3_f32 v251, v251, v59, v60
v_max3_f32 v249, v249, v29, v30
v_max3_f32 v251, v251, v61, v62
v_max3_f32 v249, v249, v31, v63
v_max_f32 v249, v249, v251
v_mov_b32 v251, v249
s_nop 1
v_permlane16_swap_b32 v249, v251
v_max_f32 v249, v249, v251
v_mov_b32 v251, v249
s_nop 1
v_permlane32_swap_b32 v249, v251
v_max_f32 v249, v249, v251
v_log_f32 v250, v247
s_nop 0
v_max3_f32 v249, 0, v249, v250
v_sub_f32 v250, 0, v249
EX v250,v250
v_sub_f32 v140, v140, v249
v_sub_f32 v141, v141, v249
v_sub_f32 v142, v142, v249
v_sub_f32 v143, v143, v249
v_sub_f32 v88, v88, v249
v_sub_f32 v24, v24, v249
v_sub_f32 v89, v89, v249
v_sub_f32 v25, v25, v249
v_sub_f32 v90, v90, v249
v_sub_f32 v26, v26, v249
v_sub_f32 v91, v91, v249
v_sub_f32 v27, v27, v249
v_sub_f32 v92, v92, v249
v_sub_f32 v28, v28, v249
v_sub_f32 v93, v93, v249
v_sub_f32 v29, v29, v249
v_sub_f32 v94, v94, v249
v_sub_f32 v30, v30, v249
v_sub_f32 v95, v95, v249
v_sub_f32 v31, v31, v249
v_sub_f32 v120, v120, v249
v_sub_f32 v56, v56, v249
v_sub_f32 v121, v121, v249
v_sub_f32 v57, v57, v249
v_sub_f32 v122, v122, v249
v_sub_f32 v58, v58, v249
v_sub_f32 v123, v123, v249
v_sub_f32 v59, v59, v249
v_sub_f32 v124, v124, v249
v_sub_f32 v60, v60, v249
v_sub_f32 v125, v125, v249
v_sub_f32 v61, v61, v249
v_sub_f32 v126, v126, v249
v_sub_f32 v62, v62, v249
v_sub_f32 v127, v127, v249
v_sub_f32 v63, v63, v249
v_mul_f32 v156, v247, v250
v_mov_b32 v157, 0
AR v254,a24
v_mul_f32 v254, v254, v250
AW a24,v254
AR v251,a25
v_mul_f32 v251, v251, v250
AW a25,v251
AR v254,a26
v_mul_f32 v254, v254, v250
AW a26,v254
AR v251,a27
v_mul_f32 v251, v251, v250
AW a27,v251
AR v254,a28
v_mul_f32 v254, v254, v250
AW a28,v254
AR v251,a29
v_mul_f32 v251, v251, v250
AW a29,v251
AR v254,a30
v_mul_f32 v254, v254, v250
AW a30,v254
AR v251,a31
v_mul_f32 v251, v251, v250
AW a31,v251
AR v254,a56
v_mul_f32 v254, v254, v250
AW a56,v254
AR v251,a57
v_mul_f32 v251, v251, v250
AW a57,v251
AR v254,a58
v_mul_f32 v254, v254, v250
AW a58,v254
AR v251,a59
v_mul_f32 v251, v251, v250
AW a59,v251
AR v254,a60
v_mul_f32 v254, v254, v250
AW a60,v254
AR v251,a61
v_mul_f32 v251, v251, v250
AW a61,v251
AR v254,a62
v_mul_f32 v254, v254, v250
AW a62,v254
AR v251,a63
v_mul_f32 v251, v251, v250
AW a63,v251
AR v254,a88
v_mul_f32 v254, v254, v250
AW a88,v254
AR v251,a89
v_mul_f32 v251, v251, v250
AW a89,v251
AR v254,a90
v_mul_f32 v254, v254, v250
AW a90,v254
AR v251,a91
v_mul_f32 v251, v251, v250
AW a91,v251
AR v254,a92
v_mul_f32 v254, v254, v250
AW a92,v254
AR v251,a93
v_mul_f32 v251, v251, v250
AW a93,v251
AR v254,a94
v_mul_f32 v254, v254, v250
AW a94,v254
AR v251,a95
v_mul_f32 v251, v251, v250
AW a95,v251
AR v254,a120
v_mul_f32 v254, v254, v250
AW a120,v254
AR v251,a121
v_mul_f32 v251, v251, v250
AW a121,v251
AR v254,a122
v_mul_f32 v254, v254, v250
AW a122,v254
AR v251,a123
v_mul_f32 v251, v251, v250
AW a123,v251
AR v254,a124
v_mul_f32 v254, v254, v250
AW a124,v254
AR v251,a125
v_mul_f32 v251, v251, v250
AW a125,v251
AR v254,a126
v_mul_f32 v254, v254, v250
AW a126,v254
AR v251,a127
v_mul_f32 v251, v251, v250
AW a127,v251
EX v0,v0
EX v1,v1
EX v2,v2
EX v3,v3
EX v4,v4
EX v5,v5
EX v6,v6
EX v7,v7
EX v8,v8
EX v9,v9
EX v10,v10
EX v11,v11
EX v12,v12
EX v13,v13
EX v14,v14
EX v15,v15
EX v16,v16
EX v17,v17
EX v18,v18
EX v19,v19
EX v20,v20
EX v21,v21
EX v22,v22
EX v23,v23
EX v24,v24
EX v25,v25
EX v26,v26
EX v27,v27
EX v28,v28
EX v29,v29
EX v30,v30
EX v31,v31
EX v32,v32
EX v33,v33
EX v34,v34
EX v35,v35
EX v36,v36
EX v37,v37
EX v38,v38
EX v39,v39
EX v40,v40
EX v41,v41
EX v42,v42
EX v43,v43
EX v44,v44
EX v45,v45
EX v46,v46
EX v47,v47
EX v48,v48
EX v49,v49
EX v50,v50
EX v51,v51
EX v52,v52
EX v53,v53
EX v54,v54
EX v55,v55
EX v56,v56
EX v57,v57
EX v58,v58
EX v59,v59
EX v60,v60
EX v61,v61
EX v62,v62
EX v63,v63
CV v0,v0,v1
CV v1,v2,v3
CV v2,v4,v5
CV v3,v6,v7
CV v8,v8,v9
CV v9,v10,v11
CV v10,v12,v13
CV v11,v14,v15
CV v16,v16,v17
CV v17,v18,v19
CV v18,v20,v21
CV v19,v22,v23
CV v24,v24,v25
CV v25,v26,v27
CV v26,v28,v29
CV v27,v30,v31
CV v4,v32,v33
CV v5,v34,v35
CV v6,v36,v37
CV v7,v38,v39
CV v12,v40,v41
CV v13,v42,v43
CV v14,v44,v45
CV v15,v46,v47
CV v20,v48,v49
CV v21,v50,v51
CV v22,v52,v53
CV v23,v54,v55
CV v28,v56,v57
CV v29,v58,v59
CV v30,v60,v61
CV v31,v62,v63
v_smfmac_f32_16x16x64_bf16 v[144:147], v[236:239], v[0:7], v234
v_smfmac_f32_16x16x64_bf16 v[148:151], v[236:239], v[8:15], v234
v_smfmac_f32_16x16x64_bf16 v[152:155], v[236:239], v[16:23], v234
v_smfmac_f32_16x16x64_bf16 v[156:159], v[236:239], v[24:31], v234
s_nop 9
s_cmp_eq_u32 s73, 4
s_cbranch_scc1 LBACK4_%=
s_cmp_eq_u32 s73, 6
s_cbranch_scc1 LBACK6_%=
s_cmp_eq_u32 s73, 8
s_cbranch_scc1 LBACK8_%=
s_cmp_eq_u32 s73, 10
s_cbranch_scc1 LBACK10_%=
s_cmp_eq_u32 s73, 12
s_cbranch_scc1 LBACK12_%=
s_cmp_eq_u32 s73, 14
s_cbranch_scc1 LBACK14_%=
s_cmp_eq_u32 s73, 16
s_cbranch_scc1 LBACK16_%=
s_cmp_eq_u32 s73, 18
s_cbranch_scc1 LBACK18_%=
s_cmp_eq_u32 s73, 20
s_cbranch_scc1 LBACK20_%=
s_cmp_eq_u32 s73, 22
s_cbranch_scc1 LBACK22_%=
s_cmp_eq_u32 s73, 24
s_cbranch_scc1 LBACK24_%=
s_cmp_eq_u32 s73, 26
s_cbranch_scc1 LBACK26_%=
s_cmp_eq_u32 s73, 28
s_cbranch_scc1 LBACK28_%=
s_cmp_eq_u32 s73, 30
s_cbranch_scc1 LBACK30_%=
s_cmp_eq_u32 s73, 32
s_cbranch_scc1 LBACK32_%=
s_cmp_eq_u32 s73, 34
s_cbranch_scc1 LBACK34_%=
s_endpgm
LSITE5_%=:
s_mov_b32 s73, 5
s_mov_b32 s74, 16384
s_mov_b32 s75, 32768
s_branch LRARE104_%=
LSITE7_%=:
s_mov_b32 s73, 7
s_mov_b32 s74, 49152
s_mov_b32 s75, 0
s_branch LRARE104_%=
LSITE9_%=:
s_mov_b32 s73, 9
s_mov_b32 s74, 16384
s_mov_b32 s75, 32768
s_branch LRARE104_%=
LSITE11_%=:
s_mov_b32 s73, 11
s_mov_b32 s74, 49152
s_mov_b32 s75, 0
s_branch LRARE104_%=
LSITE13_%=:
s_mov_b32 s73, 13
s_mov_b32 s74, 16384
s_mov_b32 s75, 32768
s_branch LRARE104_%=
LSITE15_%=:
s_mov_b32 s73, 15
s_mov_b32 s74, 49152
s_mov_b32 s75, 0
s_branch LRARE104_%=
LSITE17_%=:
s_mov_b32 s73, 17
s_mov_b32 s74, 16384
s_mov_b32 s75, 32768
s_branch LRARE104_%=
LSITE19_%=:
s_mov_b32 s73, 19
s_mov_b32 s74, 49152
s_mov_b32 s75, 0
s_branch LRARE104_%=
LSITE21_%=:
s_mov_b32 s73, 21
s_mov_b32 s74, 16384
s_mov_b32 s75, 32768
s_branch LRARE104_%=
LSITE23_%=:
s_mov_b32 s73, 23
s_mov_b32 s74, 49152
s_mov_b32 s75, 0
s_branch LRARE104_%=
LSITE25_%=:
s_mov_b32 s73, 25
s_mov_b32 s74, 16384
s_mov_b32 s75, 32768
s_branch LRARE104_%=
LSITE27_%=:
s_mov_b32 s73, 27
s_mov_b32 s74, 49152
s_mov_b32 s75, 0
s_branch LRARE104_%=
LSITE29_%=:
s_mov_b32 s73, 29
s_mov_b32 s74, 16384
s_mov_b32 s75, 32768
s_branch LRARE104_%=
LSITE31_%=:
s_mov_b32 s73, 31
s_mov_b32 s74, 49152
s_mov_b32 s75, 0
s_branch LRARE104_%=
LSITE33_%=:
s_mov_b32 s73, 33
s_mov_b32 s74, 16384
s_mov_b32 s75, 32768
s_branch LRARE104_%=
LSITE35_%=:
s_mov_b32 s73, 35
s_mov_b32 s74, 49152
s_mov_b32 s75, 0
s_branch LRARE104_%=
LRARE104_%=:
s_nop 15
s_waitcnt vmcnt(0) lgkmcnt(0)
v_add_u32 v249, s74, v224
RD a[192:195],v249 offset:0
RD a[208:211],v249 offset:4096
RD a[224:227],v249 offset:8192
RD a[240:243],v249 offset:12288
)ASM"
R"ASM(
v_add_u32 v250, s74, v225
RD a[196:199],v250 offset:0
RD a[212:215],v250 offset:4096
RD a[228:231],v250 offset:8192
RD a[244:247],v250 offset:12288
v_add_u32 v251, s74, v226
RD a[200:203],v251 offset:0
RD a[216:219],v251 offset:4096
RD a[232:235],v251 offset:8192
RD a[248:251],v251 offset:12288
v_add_u32 v254, s74, v227
RD a[204:207],v254 offset:0
RD a[220:223],v254 offset:4096
RD a[236:239],v254 offset:8192
RD a[252:255],v254 offset:12288
s_waitcnt lgkmcnt(0)
MM v[64:67],a[192:195],v[160:163],v[128:131]
MM v[64:67],a[196:199],v[176:179],v[64:67]
MM v[64:67],a[200:203],v[192:195],v[64:67]
MM v[64:67],a[204:207],v[208:211],v[64:67]
MM v[68:71],a[220:223],v[208:211],v[128:131]
MM v[68:71],a[216:219],v[192:195],v[68:71]
MM v[68:71],a[212:215],v[176:179],v[68:71]
MM v[68:71],a[208:211],v[160:163],v[68:71]
MM v[76:79],a[208:211],v[164:167],v[132:135]
MM v[76:79],a[212:215],v[180:183],v[76:79]
MM v[76:79],a[216:219],v[196:199],v[76:79]
MM v[76:79],a[220:223],v[212:215],v[76:79]
MM v[72:75],a[204:207],v[212:215],v[132:135]
MM v[72:75],a[200:203],v[196:199],v[72:75]
MM v[72:75],a[196:199],v[180:183],v[72:75]
MM v[72:75],a[192:195],v[164:167],v[72:75]
MM v[80:83],a[192:195],v[168:171],v[136:139]
MM v[80:83],a[196:199],v[184:187],v[80:83]
MM v[80:83],a[200:203],v[200:203],v[80:83]
MM v[80:83],a[204:207],v[216:219],v[80:83]
MM v[84:87],a[220:223],v[216:219],v[136:139]
MM v[84:87],a[216:219],v[200:203],v[84:87]
MM v[84:87],a[212:215],v[184:187],v[84:87]
MM v[84:87],a[208:211],v[168:171],v[84:87]
MM v[92:95],a[208:211],v[172:175],v[140:143]
MM v[92:95],a[212:215],v[188:191],v[92:95]
MM v[92:95],a[216:219],v[204:207],v[92:95]
MM v[92:95],a[220:223],v[220:223],v[92:95]
MM v[88:91],a[204:207],v[220:223],v[140:143]
MM v[88:91],a[200:203],v[204:207],v[88:91]
MM v[88:91],a[196:199],v[188:191],v[88:91]
MM v[88:91],a[192:195],v[172:175],v[88:91]
MM v[96:99],a[224:227],v[160:163],v[128:131]
MM v[96:99],a[228:231],v[176:179],v[96:99]
MM v[96:99],a[232:235],v[192:195],v[96:99]
MM v[96:99],a[236:239],v[208:211],v[96:99]
MM v[100:103],a[252:255],v[208:211],v[128:131]
MM v[100:103],a[248:251],v[192:195],v[100:103]
MM v[100:103],a[244:247],v[176:179],v[100:103]
MM v[100:103],a[240:243],v[160:163],v[100:103]
MM v[108:111],a[240:243],v[164:167],v[132:135]
MM v[108:111],a[244:247],v[180:183],v[108:111]
MM v[108:111],a[248:251],v[196:199],v[108:111]
MM v[108:111],a[252:255],v[212:215],v[108:111]
MM v[104:107],a[236:239],v[212:215],v[132:135]
MM v[104:107],a[232:235],v[196:199],v[104:107]
MM v[104:107],a[228:231],v[180:183],v[104:107]
MM v[104:107],a[224:227],v[164:167],v[104:107]
MM v[112:115],a[224:227],v[168:171],v[136:139]
MM v[112:115],a[228:231],v[184:187],v[112:115]
MM v[112:115],a[232:235],v[200:203],v[112:115]
MM v[112:115],a[236:239],v[216:219],v[112:115]
MM v[116:119],a[252:255],v[216:219],v[136:139]
MM v[116:119],a[248:251],v[200:203],v[116:119]
MM v[116:119],a[244:247],v[184:187],v[116:119]
MM v[116:119],a[240:243],v[168:171],v[116:119]
MM v[124:127],a[240:243],v[172:175],v[140:143]
MM v[124:127],a[244:247],v[188:191],v[124:127]
MM v[124:127],a[248:251],v[204:207],v[124:127]
MM v[124:127],a[252:255],v[220:223],v[124:127]
MM v[120:123],a[236:239],v[220:223],v[140:143]
MM v[120:123],a[232:235],v[204:207],v[120:123]
MM v[120:123],a[228:231],v[188:191],v[120:123]
MM v[120:123],a[224:227],v[172:175],v[120:123]
s_nop 15
v_add_u32 v249, s75, v224
RD a[192:195],v249 offset:0
RD a[208:211],v249 offset:4096
RD a[224:227],v249 offset:8192
RD a[240:243],v249 offset:12288
v_add_u32 v250, s75, v225
RD a[196:199],v250 offset:0
RD a[212:215],v250 offset:4096
RD a[228:231],v250 offset:8192
RD a[244:247],v250 offset:12288
v_add_u32 v251, s75, v226
RD a[200:203],v251 offset:0
RD a[216:219],v251 offset:4096
RD a[232:235],v251 offset:8192
RD a[248:251],v251 offset:12288
v_add_u32 v254, s75, v227
RD a[204:207],v254 offset:0
RD a[220:223],v254 offset:4096
RD a[236:239],v254 offset:8192
RD a[252:255],v254 offset:12288
s_waitcnt lgkmcnt(0)
v_max3_f32 v249, v64, v65, v66
v_max3_f32 v251, v96, v97, v98
v_max3_f32 v249, v249, v67, v68
v_max3_f32 v251, v251, v99, v100
v_max3_f32 v249, v249, v69, v70
v_max3_f32 v251, v251, v101, v102
v_max3_f32 v249, v249, v71, v103
v_max_f32 v249, v249, v251
v_mov_b32 v251, v249
s_nop 1
v_permlane16_swap_b32 v249, v251
v_max_f32 v249, v249, v251
v_mov_b32 v251, v249
s_nop 1
v_permlane32_swap_b32 v249, v251
v_max_f32 v249, v249, v251
v_log_f32 v250, v244
s_nop 0
v_max3_f32 v249, 0, v249, v250
v_sub_f32 v250, 0, v249
EX v250,v250
v_sub_f32 v128, v128, v249
v_sub_f32 v129, v129, v249
v_sub_f32 v130, v130, v249
v_sub_f32 v131, v131, v249
v_sub_f32 v0, v0, v249
v_sub_f32 v64, v64, v249
v_sub_f32 v1, v1, v249
v_sub_f32 v65, v65, v249
v_sub_f32 v2, v2, v249
v_sub_f32 v66, v66, v249
v_sub_f32 v3, v3, v249
v_sub_f32 v67, v67, v249
v_sub_f32 v4, v4, v249
v_sub_f32 v68, v68, v249
v_sub_f32 v5, v5, v249
v_sub_f32 v69, v69, v249
v_sub_f32 v6, v6, v249
v_sub_f32 v70, v70, v249
v_sub_f32 v7, v7, v249
v_sub_f32 v71, v71, v249
v_sub_f32 v32, v32, v249
v_sub_f32 v96, v96, v249
v_sub_f32 v33, v33, v249
v_sub_f32 v97, v97, v249
v_sub_f32 v34, v34, v249
v_sub_f32 v98, v98, v249
v_sub_f32 v35, v35, v249
v_sub_f32 v99, v99, v249
v_sub_f32 v36, v36, v249
v_sub_f32 v100, v100, v249
v_sub_f32 v37, v37, v249
v_sub_f32 v101, v101, v249
v_sub_f32 v38, v38, v249
v_sub_f32 v102, v102, v249
v_sub_f32 v39, v39, v249
v_sub_f32 v103, v103, v249
v_mul_f32 v144, v244, v250
v_mov_b32 v145, 0
AR v254,a0
v_mul_f32 v254, v254, v250
AW a0,v254
AR v251,a1
v_mul_f32 v251, v251, v250
AW a1,v251
AR v254,a2
v_mul_f32 v254, v254, v250
AW a2,v254
AR v251,a3
v_mul_f32 v251, v251, v250
AW a3,v251
AR v254,a4
v_mul_f32 v254, v254, v250
AW a4,v254
AR v251,a5
v_mul_f32 v251, v251, v250
AW a5,v251
AR v254,a6
v_mul_f32 v254, v254, v250
AW a6,v254
AR v251,a7
v_mul_f32 v251, v251, v250
AW a7,v251
AR v254,a32
v_mul_f32 v254, v254, v250
AW a32,v254
AR v251,a33
v_mul_f32 v251, v251, v250
AW a33,v251
AR v254,a34
v_mul_f32 v254, v254, v250
AW a34,v254
AR v251,a35
v_mul_f32 v251, v251, v250
AW a35,v251
AR v254,a36
v_mul_f32 v254, v254, v250
AW a36,v254
AR v251,a37
v_mul_f32 v251, v251, v250
AW a37,v251
AR v254,a38
v_mul_f32 v254, v254, v250
AW a38,v254
AR v251,a39
v_mul_f32 v251, v251, v250
AW a39,v251
AR v254,a64
v_mul_f32 v254, v254, v250
AW a64,v254
AR v251,a65
v_mul_f32 v251, v251, v250
AW a65,v251
AR v254,a66
v_mul_f32 v254, v254, v250
AW a66,v254
AR v251,a67
v_mul_f32 v251, v251, v250
AW a67,v251
AR v254,a68
v_mul_f32 v254, v254, v250
AW a68,v254
AR v251,a69
v_mul_f32 v251, v251, v250
AW a69,v251
AR v254,a70
v_mul_f32 v254, v254, v250
AW a70,v254
AR v251,a71
v_mul_f32 v251, v251, v250
AW a71,v251
AR v254,a96
v_mul_f32 v254, v254, v250
AW a96,v254
AR v251,a97
v_mul_f32 v251, v251, v250
AW a97,v251
AR v254,a98
v_mul_f32 v254, v254, v250
AW a98,v254
AR v251,a99
v_mul_f32 v251, v251, v250
AW a99,v251
AR v254,a100
v_mul_f32 v254, v254, v250
AW a100,v254
AR v251,a101
v_mul_f32 v251, v251, v250
AW a101,v251
AR v254,a102
v_mul_f32 v254, v254, v250
AW a102,v254
AR v251,a103
v_mul_f32 v251, v251, v250
AW a103,v251
v_max3_f32 v249, v72, v73, v74
v_max3_f32 v251, v104, v105, v106
v_max3_f32 v249, v249, v75, v76
v_max3_f32 v251, v251, v107, v108
v_max3_f32 v249, v249, v77, v78
v_max3_f32 v251, v251, v109, v110
v_max3_f32 v249, v249, v79, v111
v_max_f32 v249, v249, v251
v_mov_b32 v251, v249
s_nop 1
v_permlane16_swap_b32 v249, v251
v_max_f32 v249, v249, v251
v_mov_b32 v251, v249
s_nop 1
v_permlane32_swap_b32 v249, v251
v_max_f32 v249, v249, v251
v_log_f32 v250, v245
s_nop 0
v_max3_f32 v249, 0, v249, v250
v_sub_f32 v250, 0, v249
EX v250,v250
v_sub_f32 v132, v132, v249
v_sub_f32 v133, v133, v249
v_sub_f32 v134, v134, v249
v_sub_f32 v135, v135, v249
v_sub_f32 v8, v8, v249
v_sub_f32 v72, v72, v249
v_sub_f32 v9, v9, v249
v_sub_f32 v73, v73, v249
v_sub_f32 v10, v10, v249
v_sub_f32 v74, v74, v249
v_sub_f32 v11, v11, v249
v_sub_f32 v75, v75, v249
v_sub_f32 v12, v12, v249
v_sub_f32 v76, v76, v249
v_sub_f32 v13, v13, v249
v_sub_f32 v77, v77, v249
v_sub_f32 v14, v14, v249
v_sub_f32 v78, v78, v249
v_sub_f32 v15, v15, v249
v_sub_f32 v79, v79, v249
v_sub_f32 v40, v40, v249
v_sub_f32 v104, v104, v249
v_sub_f32 v41, v41, v249
v_sub_f32 v105, v105, v249
v_sub_f32 v42, v42, v249
v_sub_f32 v106, v106, v249
v_sub_f32 v43, v43, v249
v_sub_f32 v107, v107, v249
v_sub_f32 v44, v44, v249
v_sub_f32 v108, v108, v249
v_sub_f32 v45, v45, v249
v_sub_f32 v109, v109, v249
v_sub_f32 v46, v46, v249
v_sub_f32 v110, v110, v249
v_sub_f32 v47, v47, v249
v_sub_f32 v111, v111, v249
v_mul_f32 v148, v245, v250
v_mov_b32 v149, 0
AR v254,a8
v_mul_f32 v254, v254, v250
AW a8,v254
AR v251,a9
v_mul_f32 v251, v251, v250
AW a9,v251
AR v254,a10
v_mul_f32 v254, v254, v250
AW a10,v254
AR v251,a11
v_mul_f32 v251, v251, v250
AW a11,v251
AR v254,a12
v_mul_f32 v254, v254, v250
AW a12,v254
AR v251,a13
v_mul_f32 v251, v251, v250
AW a13,v251
AR v254,a14
v_mul_f32 v254, v254, v250
AW a14,v254
AR v251,a15
v_mul_f32 v251, v251, v250
AW a15,v251
AR v254,a40
v_mul_f32 v254, v254, v250
AW a40,v254
AR v251,a41
v_mul_f32 v251, v251, v250
AW a41,v251
AR v254,a42
v_mul_f32 v254, v254, v250
AW a42,v254
AR v251,a43
v_mul_f32 v251, v251, v250
AW a43,v251
AR v254,a44
v_mul_f32 v254, v254, v250
AW a44,v254
AR v251,a45
v_mul_f32 v251, v251, v250
AW a45,v251
AR v254,a46
v_mul_f32 v254, v254, v250
AW a46,v254
AR v251,a47
v_mul_f32 v251, v251, v250
AW a47,v251
AR v254,a72
v_mul_f32 v254, v254, v250
AW a72,v254
AR v251,a73
v_mul_f32 v251, v251, v250
AW a73,v251
AR v254,a74
v_mul_f32 v254, v254, v250
AW a74,v254
AR v251,a75
v_mul_f32 v251, v251, v250
AW a75,v251
AR v254,a76
v_mul_f32 v254, v254, v250
AW a76,v254
AR v251,a77
v_mul_f32 v251, v251, v250
AW a77,v251
AR v254,a78
v_mul_f32 v254, v254, v250
AW a78,v254
AR v251,a79
v_mul_f32 v251, v251, v250
AW a79,v251
AR v254,a104
v_mul_f32 v254, v254, v250
AW a104,v254
AR v251,a105
v_mul_f32 v251, v251, v250
AW a105,v251
AR v254,a106
v_mul_f32 v254, v254, v250
AW a106,v254
AR v251,a107
v_mul_f32 v251, v251, v250
AW a107,v251
AR v254,a108
v_mul_f32 v254, v254, v250
AW a108,v254
AR v251,a109
v_mul_f32 v251, v251, v250
AW a109,v251
AR v254,a110
v_mul_f32 v254, v254, v250
AW a110,v254
AR v251,a111
v_mul_f32 v251, v251, v250
AW a111,v251
v_max3_f32 v249, v80, v81, v82
v_max3_f32 v251, v112, v113, v114
v_max3_f32 v249, v249, v83, v84
v_max3_f32 v251, v251, v115, v116
v_max3_f32 v249, v249, v85, v86
v_max3_f32 v251, v251, v117, v118
v_max3_f32 v249, v249, v87, v119
v_max_f32 v249, v249, v251
v_mov_b32 v251, v249
s_nop 1
v_permlane16_swap_b32 v249, v251
v_max_f32 v249, v249, v251
v_mov_b32 v251, v249
s_nop 1
v_permlane32_swap_b32 v249, v251
v_max_f32 v249, v249, v251
v_log_f32 v250, v246
s_nop 0
v_max3_f32 v249, 0, v249, v250
v_sub_f32 v250, 0, v249
EX v250,v250
v_sub_f32 v136, v136, v249
v_sub_f32 v137, v137, v249
v_sub_f32 v138, v138, v249
v_sub_f32 v139, v139, v249
v_sub_f32 v16, v16, v249
v_sub_f32 v80, v80, v249
v_sub_f32 v17, v17, v249
v_sub_f32 v81, v81, v249
v_sub_f32 v18, v18, v249
v_sub_f32 v82, v82, v249
v_sub_f32 v19, v19, v249
v_sub_f32 v83, v83, v249
v_sub_f32 v20, v20, v249
v_sub_f32 v84, v84, v249
v_sub_f32 v21, v21, v249
v_sub_f32 v85, v85, v249
v_sub_f32 v22, v22, v249
v_sub_f32 v86, v86, v249
v_sub_f32 v23, v23, v249
v_sub_f32 v87, v87, v249
v_sub_f32 v48, v48, v249
v_sub_f32 v112, v112, v249
v_sub_f32 v49, v49, v249
v_sub_f32 v113, v113, v249
v_sub_f32 v50, v50, v249
v_sub_f32 v114, v114, v249
v_sub_f32 v51, v51, v249
v_sub_f32 v115, v115, v249
v_sub_f32 v52, v52, v249
v_sub_f32 v116, v116, v249
v_sub_f32 v53, v53, v249
v_sub_f32 v117, v117, v249
v_sub_f32 v54, v54, v249
v_sub_f32 v118, v118, v249
v_sub_f32 v55, v55, v249
v_sub_f32 v119, v119, v249
v_mul_f32 v152, v246, v250
v_mov_b32 v153, 0
AR v254,a16
v_mul_f32 v254, v254, v250
AW a16,v254
AR v251,a17
v_mul_f32 v251, v251, v250
AW a17,v251
AR v254,a18
v_mul_f32 v254, v254, v250
AW a18,v254
AR v251,a19
v_mul_f32 v251, v251, v250
AW a19,v251
AR v254,a20
v_mul_f32 v254, v254, v250
AW a20,v254
AR v251,a21
v_mul_f32 v251, v251, v250
)ASM"
R"ASM(
AW a21,v251
AR v254,a22
v_mul_f32 v254, v254, v250
AW a22,v254
AR v251,a23
v_mul_f32 v251, v251, v250
AW a23,v251
AR v254,a48
v_mul_f32 v254, v254, v250
AW a48,v254
AR v251,a49
v_mul_f32 v251, v251, v250
AW a49,v251
AR v254,a50
v_mul_f32 v254, v254, v250
AW a50,v254
AR v251,a51
v_mul_f32 v251, v251, v250
AW a51,v251
AR v254,a52
v_mul_f32 v254, v254, v250
AW a52,v254
AR v251,a53
v_mul_f32 v251, v251, v250
AW a53,v251
AR v254,a54
v_mul_f32 v254, v254, v250
AW a54,v254
AR v251,a55
v_mul_f32 v251, v251, v250
AW a55,v251
AR v254,a80
v_mul_f32 v254, v254, v250
AW a80,v254
AR v251,a81
v_mul_f32 v251, v251, v250
AW a81,v251
AR v254,a82
v_mul_f32 v254, v254, v250
AW a82,v254
AR v251,a83
v_mul_f32 v251, v251, v250
AW a83,v251
AR v254,a84
v_mul_f32 v254, v254, v250
AW a84,v254
AR v251,a85
v_mul_f32 v251, v251, v250
AW a85,v251
AR v254,a86
v_mul_f32 v254, v254, v250
AW a86,v254
AR v251,a87
v_mul_f32 v251, v251, v250
AW a87,v251
AR v254,a112
v_mul_f32 v254, v254, v250
AW a112,v254
AR v251,a113
v_mul_f32 v251, v251, v250
AW a113,v251
AR v254,a114
v_mul_f32 v254, v254, v250
AW a114,v254
AR v251,a115
v_mul_f32 v251, v251, v250
AW a115,v251
AR v254,a116
v_mul_f32 v254, v254, v250
AW a116,v254
AR v251,a117
v_mul_f32 v251, v251, v250
AW a117,v251
AR v254,a118
v_mul_f32 v254, v254, v250
AW a118,v254
AR v251,a119
v_mul_f32 v251, v251, v250
AW a119,v251
v_max3_f32 v249, v88, v89, v90
v_max3_f32 v251, v120, v121, v122
v_max3_f32 v249, v249, v91, v92
v_max3_f32 v251, v251, v123, v124
v_max3_f32 v249, v249, v93, v94
v_max3_f32 v251, v251, v125, v126
v_max3_f32 v249, v249, v95, v127
v_max_f32 v249, v249, v251
v_mov_b32 v251, v249
s_nop 1
v_permlane16_swap_b32 v249, v251
v_max_f32 v249, v249, v251
v_mov_b32 v251, v249
s_nop 1
v_permlane32_swap_b32 v249, v251
v_max_f32 v249, v249, v251
v_log_f32 v250, v247
s_nop 0
v_max3_f32 v249, 0, v249, v250
v_sub_f32 v250, 0, v249
EX v250,v250
v_sub_f32 v140, v140, v249
v_sub_f32 v141, v141, v249
v_sub_f32 v142, v142, v249
v_sub_f32 v143, v143, v249
v_sub_f32 v24, v24, v249
v_sub_f32 v88, v88, v249
v_sub_f32 v25, v25, v249
v_sub_f32 v89, v89, v249
v_sub_f32 v26, v26, v249
v_sub_f32 v90, v90, v249
v_sub_f32 v27, v27, v249
v_sub_f32 v91, v91, v249
v_sub_f32 v28, v28, v249
v_sub_f32 v92, v92, v249
v_sub_f32 v29, v29, v249
v_sub_f32 v93, v93, v249
v_sub_f32 v30, v30, v249
v_sub_f32 v94, v94, v249
v_sub_f32 v31, v31, v249
v_sub_f32 v95, v95, v249
v_sub_f32 v56, v56, v249
v_sub_f32 v120, v120, v249
v_sub_f32 v57, v57, v249
v_sub_f32 v121, v121, v249
v_sub_f32 v58, v58, v249
v_sub_f32 v122, v122, v249
v_sub_f32 v59, v59, v249
v_sub_f32 v123, v123, v249
v_sub_f32 v60, v60, v249
v_sub_f32 v124, v124, v249
v_sub_f32 v61, v61, v249
v_sub_f32 v125, v125, v249
v_sub_f32 v62, v62, v249
v_sub_f32 v126, v126, v249
v_sub_f32 v63, v63, v249
v_sub_f32 v127, v127, v249
v_mul_f32 v156, v247, v250
v_mov_b32 v157, 0
AR v254,a24
v_mul_f32 v254, v254, v250
AW a24,v254
AR v251,a25
v_mul_f32 v251, v251, v250
AW a25,v251
AR v254,a26
v_mul_f32 v254, v254, v250
AW a26,v254
AR v251,a27
v_mul_f32 v251, v251, v250
AW a27,v251
AR v254,a28
v_mul_f32 v254, v254, v250
AW a28,v254
AR v251,a29
v_mul_f32 v251, v251, v250
AW a29,v251
AR v254,a30
v_mul_f32 v254, v254, v250
AW a30,v254
AR v251,a31
v_mul_f32 v251, v251, v250
AW a31,v251
AR v254,a56
v_mul_f32 v254, v254, v250
AW a56,v254
AR v251,a57
v_mul_f32 v251, v251, v250
AW a57,v251
AR v254,a58
v_mul_f32 v254, v254, v250
AW a58,v254
AR v251,a59
v_mul_f32 v251, v251, v250
AW a59,v251
AR v254,a60
v_mul_f32 v254, v254, v250
AW a60,v254
AR v251,a61
v_mul_f32 v251, v251, v250
AW a61,v251
AR v254,a62
v_mul_f32 v254, v254, v250
AW a62,v254
AR v251,a63
v_mul_f32 v251, v251, v250
AW a63,v251
AR v254,a88
v_mul_f32 v254, v254, v250
AW a88,v254
AR v251,a89
v_mul_f32 v251, v251, v250
AW a89,v251
AR v254,a90
v_mul_f32 v254, v254, v250
AW a90,v254
AR v251,a91
v_mul_f32 v251, v251, v250
AW a91,v251
AR v254,a92
v_mul_f32 v254, v254, v250
AW a92,v254
AR v251,a93
v_mul_f32 v251, v251, v250
AW a93,v251
AR v254,a94
v_mul_f32 v254, v254, v250
AW a94,v254
AR v251,a95
v_mul_f32 v251, v251, v250
AW a95,v251
AR v254,a120
v_mul_f32 v254, v254, v250
AW a120,v254
AR v251,a121
v_mul_f32 v251, v251, v250
AW a121,v251
AR v254,a122
v_mul_f32 v254, v254, v250
AW a122,v254
AR v251,a123
v_mul_f32 v251, v251, v250
AW a123,v251
AR v254,a124
v_mul_f32 v254, v254, v250
AW a124,v254
AR v251,a125
v_mul_f32 v251, v251, v250
AW a125,v251
AR v254,a126
v_mul_f32 v254, v254, v250
AW a126,v254
AR v251,a127
v_mul_f32 v251, v251, v250
AW a127,v251
EX v64,v64
EX v65,v65
EX v66,v66
EX v67,v67
EX v68,v68
EX v69,v69
EX v70,v70
EX v71,v71
EX v72,v72
EX v73,v73
EX v74,v74
EX v75,v75
EX v76,v76
EX v77,v77
EX v78,v78
EX v79,v79
EX v80,v80
EX v81,v81
EX v82,v82
EX v83,v83
EX v84,v84
EX v85,v85
EX v86,v86
EX v87,v87
EX v88,v88
EX v89,v89
EX v90,v90
EX v91,v91
EX v92,v92
EX v93,v93
EX v94,v94
EX v95,v95
EX v96,v96
EX v97,v97
EX v98,v98
EX v99,v99
EX v100,v100
EX v101,v101
EX v102,v102
EX v103,v103
EX v104,v104
EX v105,v105
EX v106,v106
EX v107,v107
EX v108,v108
EX v109,v109
EX v110,v110
EX v111,v111
EX v112,v112
EX v113,v113
EX v114,v114
EX v115,v115
EX v116,v116
EX v117,v117
EX v118,v118
EX v119,v119
EX v120,v120
EX v121,v121
EX v122,v122
EX v123,v123
EX v124,v124
EX v125,v125
EX v126,v126
EX v127,v127
CV v64,v64,v65
CV v65,v66,v67
CV v66,v68,v69
CV v67,v70,v71
CV v72,v72,v73
CV v73,v74,v75
CV v74,v76,v77
CV v75,v78,v79
CV v80,v80,v81
CV v81,v82,v83
CV v82,v84,v85
CV v83,v86,v87
CV v88,v88,v89
CV v89,v90,v91
CV v90,v92,v93
CV v91,v94,v95
CV v68,v96,v97
CV v69,v98,v99
CV v70,v100,v101
CV v71,v102,v103
CV v76,v104,v105
CV v77,v106,v107
CV v78,v108,v109
CV v79,v110,v111
CV v84,v112,v113
CV v85,v114,v115
CV v86,v116,v117
CV v87,v118,v119
CV v92,v120,v121
CV v93,v122,v123
CV v94,v124,v125
CV v95,v126,v127
v_smfmac_f32_16x16x64_bf16 v[144:147], v[236:239], v[64:71], v234
v_smfmac_f32_16x16x64_bf16 v[148:151], v[236:239], v[72:79], v234
v_smfmac_f32_16x16x64_bf16 v[152:155], v[236:239], v[80:87], v234
v_smfmac_f32_16x16x64_bf16 v[156:159], v[236:239], v[88:95], v234
s_nop 9
s_cmp_eq_u32 s73, 5
s_cbranch_scc1 LBACK5_%=
s_cmp_eq_u32 s73, 7
s_cbranch_scc1 LBACK7_%=
s_cmp_eq_u32 s73, 9
s_cbranch_scc1 LBACK9_%=
s_cmp_eq_u32 s73, 11
s_cbranch_scc1 LBACK11_%=
s_cmp_eq_u32 s73, 13
s_cbranch_scc1 LBACK13_%=
s_cmp_eq_u32 s73, 15
s_cbranch_scc1 LBACK15_%=
s_cmp_eq_u32 s73, 17
s_cbranch_scc1 LBACK17_%=
s_cmp_eq_u32 s73, 19
s_cbranch_scc1 LBACK19_%=
s_cmp_eq_u32 s73, 21
s_cbranch_scc1 LBACK21_%=
s_cmp_eq_u32 s73, 23
s_cbranch_scc1 LBACK23_%=
s_cmp_eq_u32 s73, 25
s_cbranch_scc1 LBACK25_%=
s_cmp_eq_u32 s73, 27
s_cbranch_scc1 LBACK27_%=
s_cmp_eq_u32 s73, 29
s_cbranch_scc1 LBACK29_%=
s_cmp_eq_u32 s73, 31
s_cbranch_scc1 LBACK31_%=
s_cmp_eq_u32 s73, 33
s_cbranch_scc1 LBACK33_%=
s_cmp_eq_u32 s73, 35
s_cbranch_scc1 LBACK35_%=
s_endpgm
LSITE36_%=:
s_mov_b32 s73, 36
s_mov_b32 s74, 0
s_mov_b32 s75, 16384
s_branch LRARE022_%=
LSITE38_%=:
s_mov_b32 s73, 38
s_mov_b32 s74, 32768
s_mov_b32 s75, 49152
s_branch LRARE022_%=
LRARE022_%=:
s_nop 15
s_waitcnt vmcnt(0) lgkmcnt(0)
v_add_u32 v249, s74, v224
RD a[192:195],v249 offset:0
RD a[208:211],v249 offset:4096
RD a[224:227],v249 offset:8192
RD a[240:243],v249 offset:12288
v_add_u32 v250, s74, v225
RD a[196:199],v250 offset:0
RD a[212:215],v250 offset:4096
RD a[228:231],v250 offset:8192
RD a[244:247],v250 offset:12288
v_add_u32 v251, s74, v226
RD a[200:203],v251 offset:0
RD a[216:219],v251 offset:4096
RD a[232:235],v251 offset:8192
RD a[248:251],v251 offset:12288
v_add_u32 v254, s74, v227
RD a[204:207],v254 offset:0
RD a[220:223],v254 offset:4096
RD a[236:239],v254 offset:8192
RD a[252:255],v254 offset:12288
s_waitcnt lgkmcnt(0)
MM v[16:19],a[192:195],v[168:171],v[136:139]
MM v[16:19],a[196:199],v[184:187],v[16:19]
MM v[16:19],a[200:203],v[200:203],v[16:19]
MM v[16:19],a[204:207],v[216:219],v[16:19]
MM v[20:23],a[220:223],v[216:219],v[136:139]
MM v[20:23],a[216:219],v[200:203],v[20:23]
MM v[20:23],a[212:215],v[184:187],v[20:23]
MM v[20:23],a[208:211],v[168:171],v[20:23]
MM v[28:31],a[208:211],v[172:175],v[140:143]
MM v[28:31],a[212:215],v[188:191],v[28:31]
MM v[28:31],a[216:219],v[204:207],v[28:31]
MM v[28:31],a[220:223],v[220:223],v[28:31]
MM v[24:27],a[204:207],v[220:223],v[140:143]
MM v[24:27],a[200:203],v[204:207],v[24:27]
MM v[24:27],a[196:199],v[188:191],v[24:27]
MM v[24:27],a[192:195],v[172:175],v[24:27]
MM v[48:51],a[224:227],v[168:171],v[136:139]
MM v[48:51],a[228:231],v[184:187],v[48:51]
MM v[48:51],a[232:235],v[200:203],v[48:51]
MM v[48:51],a[236:239],v[216:219],v[48:51]
MM v[52:55],a[252:255],v[216:219],v[136:139]
MM v[52:55],a[248:251],v[200:203],v[52:55]
MM v[52:55],a[244:247],v[184:187],v[52:55]
MM v[52:55],a[240:243],v[168:171],v[52:55]
MM v[60:63],a[240:243],v[172:175],v[140:143]
MM v[60:63],a[244:247],v[188:191],v[60:63]
MM v[60:63],a[248:251],v[204:207],v[60:63]
MM v[60:63],a[252:255],v[220:223],v[60:63]
MM v[56:59],a[236:239],v[220:223],v[140:143]
MM v[56:59],a[232:235],v[204:207],v[56:59]
MM v[56:59],a[228:231],v[188:191],v[56:59]
MM v[56:59],a[224:227],v[172:175],v[56:59]
s_nop 15
v_add_u32 v249, s75, v224
RD a[192:195],v249 offset:0
RD a[208:211],v249 offset:4096
RD a[224:227],v249 offset:8192
RD a[240:243],v249 offset:12288
v_add_u32 v250, s75, v225
RD a[196:199],v250 offset:0
RD a[212:215],v250 offset:4096
RD a[228:231],v250 offset:8192
RD a[244:247],v250 offset:12288
v_add_u32 v251, s75, v226
RD a[200:203],v251 offset:0
RD a[216:219],v251 offset:4096
RD a[232:235],v251 offset:8192
RD a[248:251],v251 offset:12288
v_add_u32 v254, s75, v227
RD a[204:207],v254 offset:0
RD a[220:223],v254 offset:4096
RD a[236:239],v254 offset:8192
RD a[252:255],v254 offset:12288
s_waitcnt lgkmcnt(0)
v_max3_f32 v249, v16, v17, v18
v_max3_f32 v251, v48, v49, v50
v_max3_f32 v249, v249, v19, v20
v_max3_f32 v251, v251, v51, v52
v_max3_f32 v249, v249, v21, v22
v_max3_f32 v251, v251, v53, v54
v_max3_f32 v249, v249, v23, v55
v_max_f32 v249, v249, v251
v_mov_b32 v251, v249
s_nop 1
v_permlane16_swap_b32 v249, v251
v_max_f32 v249, v249, v251
v_mov_b32 v251, v249
s_nop 1
v_permlane32_swap_b32 v249, v251
v_max_f32 v249, v249, v251
v_log_f32 v250, v246
s_nop 0
v_max3_f32 v249, 0, v249, v250
v_sub_f32 v250, 0, v249
EX v250,v250
v_sub_f32 v136, v136, v249
v_sub_f32 v137, v137, v249
v_sub_f32 v138, v138, v249
v_sub_f32 v139, v139, v249
v_sub_f32 v80, v80, v249
v_sub_f32 v16, v16, v249
v_sub_f32 v81, v81, v249
v_sub_f32 v17, v17, v249
v_sub_f32 v82, v82, v249
v_sub_f32 v18, v18, v249
v_sub_f32 v83, v83, v249
v_sub_f32 v19, v19, v249
v_sub_f32 v84, v84, v249
v_sub_f32 v20, v20, v249
v_sub_f32 v85, v85, v249
v_sub_f32 v21, v21, v249
v_sub_f32 v86, v86, v249
v_sub_f32 v22, v22, v249
v_sub_f32 v87, v87, v249
v_sub_f32 v23, v23, v249
v_sub_f32 v112, v112, v249
v_sub_f32 v48, v48, v249
v_sub_f32 v113, v113, v249
v_sub_f32 v49, v49, v249
v_sub_f32 v114, v114, v249
v_sub_f32 v50, v50, v249
v_sub_f32 v115, v115, v249
v_sub_f32 v51, v51, v249
v_sub_f32 v116, v116, v249
v_sub_f32 v52, v52, v249
v_sub_f32 v117, v117, v249
v_sub_f32 v53, v53, v249
v_sub_f32 v118, v118, v249
v_sub_f32 v54, v54, v249
v_sub_f32 v119, v119, v249
v_sub_f32 v55, v55, v249
v_mul_f32 v152, v246, v250
v_mov_b32 v153, 0
AR v254,a16
v_mul_f32 v254, v254, v250
AW a16,v254
AR v251,a17
v_mul_f32 v251, v251, v250
AW a17,v251
AR v254,a18
v_mul_f32 v254, v254, v250
AW a18,v254
AR v251,a19
v_mul_f32 v251, v251, v250
AW a19,v251
AR v254,a20
v_mul_f32 v254, v254, v250
AW a20,v254
AR v251,a21
v_mul_f32 v251, v251, v250
AW a21,v251
AR v254,a22
v_mul_f32 v254, v254, v250
AW a22,v254
AR v251,a23
v_mul_f32 v251, v251, v250
AW a23,v251
AR v254,a48
v_mul_f32 v254, v254, v250
AW a48,v254
AR v251,a49
v_mul_f32 v251, v251, v250
AW a49,v251
AR v254,a50
v_mul_f32 v254, v254, v250
AW a50,v254
AR v251,a51
v_mul_f32 v251, v251, v250
AW a51,v251
AR v254,a52
v_mul_f32 v254, v254, v250
AW a52,v254
AR v251,a53
v_mul_f32 v251, v251, v250
AW a53,v251
AR v254,a54
v_mul_f32 v254, v254, v250
AW a54,v254
AR v251,a55
v_mul_f32 v251, v251, v250
AW a55,v251
AR v254,a80
v_mul_f32 v254, v254, v250
)ASM"
R"ASM(
AW a80,v254
AR v251,a81
v_mul_f32 v251, v251, v250
AW a81,v251
AR v254,a82
v_mul_f32 v254, v254, v250
AW a82,v254
AR v251,a83
v_mul_f32 v251, v251, v250
AW a83,v251
AR v254,a84
v_mul_f32 v254, v254, v250
AW a84,v254
AR v251,a85
v_mul_f32 v251, v251, v250
AW a85,v251
AR v254,a86
v_mul_f32 v254, v254, v250
AW a86,v254
AR v251,a87
v_mul_f32 v251, v251, v250
AW a87,v251
AR v254,a112
v_mul_f32 v254, v254, v250
AW a112,v254
AR v251,a113
v_mul_f32 v251, v251, v250
AW a113,v251
AR v254,a114
v_mul_f32 v254, v254, v250
AW a114,v254
AR v251,a115
v_mul_f32 v251, v251, v250
AW a115,v251
AR v254,a116
v_mul_f32 v254, v254, v250
AW a116,v254
AR v251,a117
v_mul_f32 v251, v251, v250
AW a117,v251
AR v254,a118
v_mul_f32 v254, v254, v250
AW a118,v254
AR v251,a119
v_mul_f32 v251, v251, v250
AW a119,v251
v_max3_f32 v249, v24, v25, v26
v_max3_f32 v251, v56, v57, v58
v_max3_f32 v249, v249, v27, v28
v_max3_f32 v251, v251, v59, v60
v_max3_f32 v249, v249, v29, v30
v_max3_f32 v251, v251, v61, v62
v_max3_f32 v249, v249, v31, v63
v_max_f32 v249, v249, v251
v_mov_b32 v251, v249
s_nop 1
v_permlane16_swap_b32 v249, v251
v_max_f32 v249, v249, v251
v_mov_b32 v251, v249
s_nop 1
v_permlane32_swap_b32 v249, v251
v_max_f32 v249, v249, v251
v_log_f32 v250, v247
s_nop 0
v_max3_f32 v249, 0, v249, v250
v_sub_f32 v250, 0, v249
EX v250,v250
v_sub_f32 v140, v140, v249
v_sub_f32 v141, v141, v249
v_sub_f32 v142, v142, v249
v_sub_f32 v143, v143, v249
v_sub_f32 v88, v88, v249
v_sub_f32 v24, v24, v249
v_sub_f32 v89, v89, v249
v_sub_f32 v25, v25, v249
v_sub_f32 v90, v90, v249
v_sub_f32 v26, v26, v249
v_sub_f32 v91, v91, v249
v_sub_f32 v27, v27, v249
v_sub_f32 v92, v92, v249
v_sub_f32 v28, v28, v249
v_sub_f32 v93, v93, v249
v_sub_f32 v29, v29, v249
v_sub_f32 v94, v94, v249
v_sub_f32 v30, v30, v249
v_sub_f32 v95, v95, v249
v_sub_f32 v31, v31, v249
v_sub_f32 v120, v120, v249
v_sub_f32 v56, v56, v249
v_sub_f32 v121, v121, v249
v_sub_f32 v57, v57, v249
v_sub_f32 v122, v122, v249
v_sub_f32 v58, v58, v249
v_sub_f32 v123, v123, v249
v_sub_f32 v59, v59, v249
v_sub_f32 v124, v124, v249
v_sub_f32 v60, v60, v249
v_sub_f32 v125, v125, v249
v_sub_f32 v61, v61, v249
v_sub_f32 v126, v126, v249
v_sub_f32 v62, v62, v249
v_sub_f32 v127, v127, v249
v_sub_f32 v63, v63, v249
v_mul_f32 v156, v247, v250
v_mov_b32 v157, 0
AR v254,a24
v_mul_f32 v254, v254, v250
AW a24,v254
AR v251,a25
v_mul_f32 v251, v251, v250
AW a25,v251
AR v254,a26
v_mul_f32 v254, v254, v250
AW a26,v254
AR v251,a27
v_mul_f32 v251, v251, v250
AW a27,v251
AR v254,a28
v_mul_f32 v254, v254, v250
AW a28,v254
AR v251,a29
v_mul_f32 v251, v251, v250
AW a29,v251
AR v254,a30
v_mul_f32 v254, v254, v250
AW a30,v254
AR v251,a31
v_mul_f32 v251, v251, v250
AW a31,v251
AR v254,a56
v_mul_f32 v254, v254, v250
AW a56,v254
AR v251,a57
v_mul_f32 v251, v251, v250
AW a57,v251
AR v254,a58
v_mul_f32 v254, v254, v250
AW a58,v254
AR v251,a59
v_mul_f32 v251, v251, v250
AW a59,v251
AR v254,a60
v_mul_f32 v254, v254, v250
AW a60,v254
AR v251,a61
v_mul_f32 v251, v251, v250
AW a61,v251
AR v254,a62
v_mul_f32 v254, v254, v250
AW a62,v254
AR v251,a63
v_mul_f32 v251, v251, v250
AW a63,v251
AR v254,a88
v_mul_f32 v254, v254, v250
AW a88,v254
AR v251,a89
v_mul_f32 v251, v251, v250
AW a89,v251
AR v254,a90
v_mul_f32 v254, v254, v250
AW a90,v254
AR v251,a91
v_mul_f32 v251, v251, v250
AW a91,v251
AR v254,a92
v_mul_f32 v254, v254, v250
AW a92,v254
AR v251,a93
v_mul_f32 v251, v251, v250
AW a93,v251
AR v254,a94
v_mul_f32 v254, v254, v250
AW a94,v254
AR v251,a95
v_mul_f32 v251, v251, v250
AW a95,v251
AR v254,a120
v_mul_f32 v254, v254, v250
AW a120,v254
AR v251,a121
v_mul_f32 v251, v251, v250
AW a121,v251
AR v254,a122
v_mul_f32 v254, v254, v250
AW a122,v254
AR v251,a123
v_mul_f32 v251, v251, v250
AW a123,v251
AR v254,a124
v_mul_f32 v254, v254, v250
AW a124,v254
AR v251,a125
v_mul_f32 v251, v251, v250
AW a125,v251
AR v254,a126
v_mul_f32 v254, v254, v250
AW a126,v254
AR v251,a127
v_mul_f32 v251, v251, v250
AW a127,v251
EX v16,v16
EX v17,v17
EX v18,v18
EX v19,v19
EX v20,v20
EX v21,v21
EX v22,v22
EX v23,v23
EX v24,v24
EX v25,v25
EX v26,v26
EX v27,v27
EX v28,v28
EX v29,v29
EX v30,v30
EX v31,v31
EX v48,v48
EX v49,v49
EX v50,v50
EX v51,v51
EX v52,v52
EX v53,v53
EX v54,v54
EX v55,v55
EX v56,v56
EX v57,v57
EX v58,v58
EX v59,v59
EX v60,v60
EX v61,v61
EX v62,v62
EX v63,v63
CV v16,v16,v17
CV v17,v18,v19
CV v18,v20,v21
CV v19,v22,v23
CV v24,v24,v25
CV v25,v26,v27
CV v26,v28,v29
CV v27,v30,v31
CV v20,v48,v49
CV v21,v50,v51
CV v22,v52,v53
CV v23,v54,v55
CV v28,v56,v57
CV v29,v58,v59
CV v30,v60,v61
CV v31,v62,v63
v_smfmac_f32_16x16x64_bf16 v[152:155], v[236:239], v[16:23], v234
s_nop 0
v_smfmac_f32_16x16x64_bf16 v[156:159], v[236:239], v[24:31], v234
s_nop 9
s_cmp_eq_u32 s73, 36
s_cbranch_scc1 LBACK36_%=
s_cmp_eq_u32 s73, 38
s_cbranch_scc1 LBACK38_%=
s_endpgm
LSITE37_%=:
s_mov_b32 s73, 37
s_mov_b32 s74, 16384
s_mov_b32 s75, 32768
s_branch LRARE122_%=
LSITE39_%=:
s_mov_b32 s73, 39
s_mov_b32 s74, 49152
s_mov_b32 s75, 0
s_branch LRARE122_%=
LRARE122_%=:
s_nop 15
s_waitcnt vmcnt(0) lgkmcnt(0)
v_add_u32 v249, s74, v224
RD a[192:195],v249 offset:0
RD a[208:211],v249 offset:4096
RD a[224:227],v249 offset:8192
RD a[240:243],v249 offset:12288
v_add_u32 v250, s74, v225
RD a[196:199],v250 offset:0
RD a[212:215],v250 offset:4096
RD a[228:231],v250 offset:8192
RD a[244:247],v250 offset:12288
v_add_u32 v251, s74, v226
RD a[200:203],v251 offset:0
RD a[216:219],v251 offset:4096
RD a[232:235],v251 offset:8192
RD a[248:251],v251 offset:12288
v_add_u32 v254, s74, v227
RD a[204:207],v254 offset:0
RD a[220:223],v254 offset:4096
RD a[236:239],v254 offset:8192
RD a[252:255],v254 offset:12288
s_waitcnt lgkmcnt(0)
MM v[80:83],a[192:195],v[168:171],v[136:139]
MM v[80:83],a[196:199],v[184:187],v[80:83]
MM v[80:83],a[200:203],v[200:203],v[80:83]
MM v[80:83],a[204:207],v[216:219],v[80:83]
MM v[84:87],a[220:223],v[216:219],v[136:139]
MM v[84:87],a[216:219],v[200:203],v[84:87]
MM v[84:87],a[212:215],v[184:187],v[84:87]
MM v[84:87],a[208:211],v[168:171],v[84:87]
MM v[92:95],a[208:211],v[172:175],v[140:143]
MM v[92:95],a[212:215],v[188:191],v[92:95]
MM v[92:95],a[216:219],v[204:207],v[92:95]
MM v[92:95],a[220:223],v[220:223],v[92:95]
MM v[88:91],a[204:207],v[220:223],v[140:143]
MM v[88:91],a[200:203],v[204:207],v[88:91]
MM v[88:91],a[196:199],v[188:191],v[88:91]
MM v[88:91],a[192:195],v[172:175],v[88:91]
MM v[112:115],a[224:227],v[168:171],v[136:139]
MM v[112:115],a[228:231],v[184:187],v[112:115]
MM v[112:115],a[232:235],v[200:203],v[112:115]
MM v[112:115],a[236:239],v[216:219],v[112:115]
MM v[116:119],a[252:255],v[216:219],v[136:139]
MM v[116:119],a[248:251],v[200:203],v[116:119]
MM v[116:119],a[244:247],v[184:187],v[116:119]
MM v[116:119],a[240:243],v[168:171],v[116:119]
MM v[124:127],a[240:243],v[172:175],v[140:143]
MM v[124:127],a[244:247],v[188:191],v[124:127]
MM v[124:127],a[248:251],v[204:207],v[124:127]
MM v[124:127],a[252:255],v[220:223],v[124:127]
MM v[120:123],a[236:239],v[220:223],v[140:143]
MM v[120:123],a[232:235],v[204:207],v[120:123]
MM v[120:123],a[228:231],v[188:191],v[120:123]
MM v[120:123],a[224:227],v[172:175],v[120:123]
s_nop 15
v_add_u32 v249, s75, v224
RD a[192:195],v249 offset:0
RD a[208:211],v249 offset:4096
RD a[224:227],v249 offset:8192
RD a[240:243],v249 offset:12288
v_add_u32 v250, s75, v225
RD a[196:199],v250 offset:0
RD a[212:215],v250 offset:4096
RD a[228:231],v250 offset:8192
RD a[244:247],v250 offset:12288
v_add_u32 v251, s75, v226
RD a[200:203],v251 offset:0
RD a[216:219],v251 offset:4096
RD a[232:235],v251 offset:8192
RD a[248:251],v251 offset:12288
v_add_u32 v254, s75, v227
RD a[204:207],v254 offset:0
RD a[220:223],v254 offset:4096
RD a[236:239],v254 offset:8192
RD a[252:255],v254 offset:12288
s_waitcnt lgkmcnt(0)
v_max3_f32 v249, v80, v81, v82
v_max3_f32 v251, v112, v113, v114
v_max3_f32 v249, v249, v83, v84
v_max3_f32 v251, v251, v115, v116
v_max3_f32 v249, v249, v85, v86
v_max3_f32 v251, v251, v117, v118
v_max3_f32 v249, v249, v87, v119
v_max_f32 v249, v249, v251
v_mov_b32 v251, v249
s_nop 1
v_permlane16_swap_b32 v249, v251
v_max_f32 v249, v249, v251
v_mov_b32 v251, v249
s_nop 1
v_permlane32_swap_b32 v249, v251
v_max_f32 v249, v249, v251
v_log_f32 v250, v246
s_nop 0
v_max3_f32 v249, 0, v249, v250
v_sub_f32 v250, 0, v249
EX v250,v250
v_sub_f32 v136, v136, v249
v_sub_f32 v137, v137, v249
v_sub_f32 v138, v138, v249
v_sub_f32 v139, v139, v249
v_sub_f32 v16, v16, v249
v_sub_f32 v80, v80, v249
v_sub_f32 v17, v17, v249
v_sub_f32 v81, v81, v249
v_sub_f32 v18, v18, v249
v_sub_f32 v82, v82, v249
v_sub_f32 v19, v19, v249
v_sub_f32 v83, v83, v249
v_sub_f32 v20, v20, v249
v_sub_f32 v84, v84, v249
v_sub_f32 v21, v21, v249
v_sub_f32 v85, v85, v249
v_sub_f32 v22, v22, v249
v_sub_f32 v86, v86, v249
v_sub_f32 v23, v23, v249
v_sub_f32 v87, v87, v249
v_sub_f32 v48, v48, v249
v_sub_f32 v112, v112, v249
v_sub_f32 v49, v49, v249
v_sub_f32 v113, v113, v249
v_sub_f32 v50, v50, v249
v_sub_f32 v114, v114, v249
v_sub_f32 v51, v51, v249
v_sub_f32 v115, v115, v249
v_sub_f32 v52, v52, v249
v_sub_f32 v116, v116, v249
v_sub_f32 v53, v53, v249
v_sub_f32 v117, v117, v249
v_sub_f32 v54, v54, v249
v_sub_f32 v118, v118, v249
v_sub_f32 v55, v55, v249
v_sub_f32 v119, v119, v249
v_mul_f32 v152, v246, v250
v_mov_b32 v153, 0
AR v254,a16
v_mul_f32 v254, v254, v250
AW a16,v254
AR v251,a17
v_mul_f32 v251, v251, v250
AW a17,v251
AR v254,a18
v_mul_f32 v254, v254, v250
AW a18,v254
AR v251,a19
v_mul_f32 v251, v251, v250
AW a19,v251
AR v254,a20
v_mul_f32 v254, v254, v250
AW a20,v254
AR v251,a21
v_mul_f32 v251, v251, v250
AW a21,v251
AR v254,a22
v_mul_f32 v254, v254, v250
AW a22,v254
AR v251,a23
v_mul_f32 v251, v251, v250
AW a23,v251
AR v254,a48
v_mul_f32 v254, v254, v250
AW a48,v254
AR v251,a49
v_mul_f32 v251, v251, v250
AW a49,v251
AR v254,a50
v_mul_f32 v254, v254, v250
AW a50,v254
AR v251,a51
v_mul_f32 v251, v251, v250
AW a51,v251
AR v254,a52
v_mul_f32 v254, v254, v250
AW a52,v254
AR v251,a53
v_mul_f32 v251, v251, v250
AW a53,v251
AR v254,a54
v_mul_f32 v254, v254, v250
AW a54,v254
AR v251,a55
v_mul_f32 v251, v251, v250
AW a55,v251
AR v254,a80
v_mul_f32 v254, v254, v250
AW a80,v254
AR v251,a81
v_mul_f32 v251, v251, v250
AW a81,v251
AR v254,a82
v_mul_f32 v254, v254, v250
AW a82,v254
AR v251,a83
v_mul_f32 v251, v251, v250
AW a83,v251
AR v254,a84
v_mul_f32 v254, v254, v250
AW a84,v254
AR v251,a85
v_mul_f32 v251, v251, v250
AW a85,v251
AR v254,a86
v_mul_f32 v254, v254, v250
AW a86,v254
AR v251,a87
v_mul_f32 v251, v251, v250
AW a87,v251
AR v254,a112
v_mul_f32 v254, v254, v250
AW a112,v254
AR v251,a113
v_mul_f32 v251, v251, v250
AW a113,v251
AR v254,a114
v_mul_f32 v254, v254, v250
AW a114,v254
AR v251,a115
v_mul_f32 v251, v251, v250
AW a115,v251
AR v254,a116
v_mul_f32 v254, v254, v250
AW a116,v254
AR v251,a117
v_mul_f32 v251, v251, v250
AW a117,v251
AR v254,a118
v_mul_f32 v254, v254, v250
AW a118,v254
AR v251,a119
v_mul_f32 v251, v251, v250
AW a119,v251
v_max3_f32 v249, v88, v89, v90
v_max3_f32 v251, v120, v121, v122
v_max3_f32 v249, v249, v91, v92
v_max3_f32 v251, v251, v123, v124
v_max3_f32 v249, v249, v93, v94
v_max3_f32 v251, v251, v125, v126
v_max3_f32 v249, v249, v95, v127
v_max_f32 v249, v249, v251
v_mov_b32 v251, v249
s_nop 1
v_permlane16_swap_b32 v249, v251
v_max_f32 v249, v249, v251
v_mov_b32 v251, v249
s_nop 1
v_permlane32_swap_b32 v249, v251
v_max_f32 v249, v249, v251
v_log_f32 v250, v247
s_nop 0
v_max3_f32 v249, 0, v249, v250
v_sub_f32 v250, 0, v249
EX v250,v250
v_sub_f32 v140, v140, v249
v_sub_f32 v141, v141, v249
v_sub_f32 v142, v142, v249
v_sub_f32 v143, v143, v249
v_sub_f32 v24, v24, v249
v_sub_f32 v88, v88, v249
v_sub_f32 v25, v25, v249
v_sub_f32 v89, v89, v249
v_sub_f32 v26, v26, v249
v_sub_f32 v90, v90, v249
v_sub_f32 v27, v27, v249
v_sub_f32 v91, v91, v249
v_sub_f32 v28, v28, v249
v_sub_f32 v92, v92, v249
v_sub_f32 v29, v29, v249
v_sub_f32 v93, v93, v249
v_sub_f32 v30, v30, v249
v_sub_f32 v94, v94, v249
v_sub_f32 v31, v31, v249
v_sub_f32 v95, v95, v249
v_sub_f32 v56, v56, v249
v_sub_f32 v120, v120, v249
v_sub_f32 v57, v57, v249
v_sub_f32 v121, v121, v249
v_sub_f32 v58, v58, v249
)ASM"
R"ASM(
v_sub_f32 v122, v122, v249
v_sub_f32 v59, v59, v249
v_sub_f32 v123, v123, v249
v_sub_f32 v60, v60, v249
v_sub_f32 v124, v124, v249
v_sub_f32 v61, v61, v249
v_sub_f32 v125, v125, v249
v_sub_f32 v62, v62, v249
v_sub_f32 v126, v126, v249
v_sub_f32 v63, v63, v249
v_sub_f32 v127, v127, v249
v_mul_f32 v156, v247, v250
v_mov_b32 v157, 0
AR v254,a24
v_mul_f32 v254, v254, v250
AW a24,v254
AR v251,a25
v_mul_f32 v251, v251, v250
AW a25,v251
AR v254,a26
v_mul_f32 v254, v254, v250
AW a26,v254
AR v251,a27
v_mul_f32 v251, v251, v250
AW a27,v251
AR v254,a28
v_mul_f32 v254, v254, v250
AW a28,v254
AR v251,a29
v_mul_f32 v251, v251, v250
AW a29,v251
AR v254,a30
v_mul_f32 v254, v254, v250
AW a30,v254
AR v251,a31
v_mul_f32 v251, v251, v250
AW a31,v251
AR v254,a56
v_mul_f32 v254, v254, v250
AW a56,v254
AR v251,a57
v_mul_f32 v251, v251, v250
AW a57,v251
AR v254,a58
v_mul_f32 v254, v254, v250
AW a58,v254
AR v251,a59
v_mul_f32 v251, v251, v250
AW a59,v251
AR v254,a60
v_mul_f32 v254, v254, v250
AW a60,v254
AR v251,a61
v_mul_f32 v251, v251, v250
AW a61,v251
AR v254,a62
v_mul_f32 v254, v254, v250
AW a62,v254
AR v251,a63
v_mul_f32 v251, v251, v250
AW a63,v251
AR v254,a88
v_mul_f32 v254, v254, v250
AW a88,v254
AR v251,a89
v_mul_f32 v251, v251, v250
AW a89,v251
AR v254,a90
v_mul_f32 v254, v254, v250
AW a90,v254
AR v251,a91
v_mul_f32 v251, v251, v250
AW a91,v251
AR v254,a92
v_mul_f32 v254, v254, v250
AW a92,v254
AR v251,a93
v_mul_f32 v251, v251, v250
AW a93,v251
AR v254,a94
v_mul_f32 v254, v254, v250
AW a94,v254
AR v251,a95
v_mul_f32 v251, v251, v250
AW a95,v251
AR v254,a120
v_mul_f32 v254, v254, v250
AW a120,v254
AR v251,a121
v_mul_f32 v251, v251, v250
AW a121,v251
AR v254,a122
v_mul_f32 v254, v254, v250
AW a122,v254
AR v251,a123
v_mul_f32 v251, v251, v250
AW a123,v251
AR v254,a124
v_mul_f32 v254, v254, v250
AW a124,v254
AR v251,a125
v_mul_f32 v251, v251, v250
AW a125,v251
AR v254,a126
v_mul_f32 v254, v254, v250
AW a126,v254
AR v251,a127
v_mul_f32 v251, v251, v250
AW a127,v251
EX v80,v80
EX v81,v81
EX v82,v82
EX v83,v83
EX v84,v84
EX v85,v85
EX v86,v86
EX v87,v87
EX v88,v88
EX v89,v89
EX v90,v90
EX v91,v91
EX v92,v92
EX v93,v93
EX v94,v94
EX v95,v95
EX v112,v112
EX v113,v113
EX v114,v114
EX v115,v115
EX v116,v116
EX v117,v117
EX v118,v118
EX v119,v119
EX v120,v120
EX v121,v121
EX v122,v122
EX v123,v123
EX v124,v124
EX v125,v125
EX v126,v126
EX v127,v127
CV v80,v80,v81
CV v81,v82,v83
CV v82,v84,v85
CV v83,v86,v87
CV v88,v88,v89
CV v89,v90,v91
CV v90,v92,v93
CV v91,v94,v95
CV v84,v112,v113
CV v85,v114,v115
CV v86,v116,v117
CV v87,v118,v119
CV v92,v120,v121
CV v93,v122,v123
CV v94,v124,v125
CV v95,v126,v127
v_smfmac_f32_16x16x64_bf16 v[152:155], v[236:239], v[80:87], v234
s_nop 0
v_smfmac_f32_16x16x64_bf16 v[156:159], v[236:239], v[88:95], v234
s_nop 9
s_cmp_eq_u32 s73, 37
s_cbranch_scc1 LBACK37_%=
s_cmp_eq_u32 s73, 39
s_cbranch_scc1 LBACK39_%=
s_endpgm
LEND_%=:
)ASM"

    :: [ka] "s"(__builtin_amdgcn_kernarg_segment_ptr()), [slds] "s"(slds), [swave] "s"(w), [sb] "s"(blockIdx.x)
    : "memory", "vcc", "scc", CLOB_V, CLOB_A, CLOB_S);
}
constexpr int DYN_LDS = 163840;

extern "C" void kernel_launch(void* const* d_in, const int* in_sizes, int n_in, void* d_out, int out_size, void* d_ws, size_t ws_size, hipStream_t stream) {
#if !defined(__HIP_DEVICE_COMPILE__)
  if (DYN_LDS) hipFuncSetAttribute((const void*)ka_fused, hipFuncAttributeMaxDynamicSharedMemorySize, DYN_LDS);
  ka_fused<<<256, 256, DYN_LDS, stream>>>(d_in[3], d_in[0], d_ws, d_in[1], d_in[6], d_in[2], d_in[4], d_in[7], d_in[8], d_in[9], d_out);
#endif
}
```

```cpp
#include <hip/hip_runtime.h>
#include <stdint.h>

using f32x4  = __attribute__((ext_vector_type(4))) float;
using u32x4  = __attribute__((ext_vector_type(4))) unsigned;
using u32x2  = __attribute__((ext_vector_type(2))) unsigned;

constexpr int SEQ = 2048, DM = 640, NH = 32, DH = 128, HD = NH * DH, FF = 1728, TOPC = 8;

constexpr unsigned WS_FO  = 0;
constexpr unsigned WS_EP  = 32768;
constexpr unsigned WS_G1  = 36864;
constexpr unsigned WS_G2  = 53248;
constexpr unsigned WS_FH  = 69632;
constexpr unsigned WS_FY  = 73728;
constexpr unsigned WS_FG  = 77824;
constexpr unsigned WS_FR  = 81920;
constexpr unsigned WS_RK  = 86016;
constexpr unsigned WS_XN  = 131072;
constexpr unsigned WS_OF  = WS_XN + SEQ * DM * 2;
constexpr unsigned WS_H   = WS_OF + (18u << 20);
constexpr unsigned WS_Y   = WS_H + 32768;
constexpr unsigned WS_P   = WS_Y + 1048576;
constexpr unsigned WS_SPAN = 64u << 20;

#define CLOB_V "v0","v1","v2","v3","v4","v5","v6","v7","v8","v9","v10","v11","v12","v13","v14","v15","v16","v17","v18","v19","v20","v21","v22","v23","v24","v25","v26","v27","v28","v29","v30","v31","v32","v33","v34","v35","v36","v37","v38","v39","v40","v41","v42","v43","v44","v45","v46","v47","v48","v49","v50","v51","v52","v53","v54","v55","v56","v57","v58","v59","v60","v61","v62","v63","v64","v65","v66","v67","v68","v69","v70","v71","v72","v73","v74","v75","v76","v77","v78","v79","v80","v81","v82","v83","v84","v85","v86","v87","v88","v89","v90","v91","v92","v93","v94","v95","v96","v97","v98","v99","v100","v101","v102","v103","v104","v105","v106","v107","v108","v109","v110","v111","v112","v113","v114","v115","v116","v117","v118","v119","v120","v121","v122","v123","v124","v125","v126","v127","v128","v129","v130","v131","v132","v133","v134","v135","v136","v137","v138","v139","v140","v141","v142","v143","v144","v145","v146","v147","v148","v149","v150","v151","v152","v153","v154","v155","v156","v157","v158","v159","v160","v161","v162","v163","v164","v165","v166","v167","v168","v169","v170","v171","v172","v173","v174","v175","v176","v177","v178","v179","v180","v181","v182","v183","v184","v185","v186","v187","v188","v189","v190","v191","v192","v193","v194","v195","v196","v197","v198","v199","v200","v201","v202","v203","v204","v205","v206","v207","v208","v209","v210","v211","v212","v213","v214","v215","v216","v217","v218","v219","v220","v221","v222","v223","v224","v225","v226","v227","v228","v229","v230","v231","v232","v233","v234","v235","v236","v237","v238","v239","v240","v241","v242","v243","v244","v245","v246","v247","v248","v249","v250","v251","v252","v253","v254","v255"
#define CLOB_A "a0","a1","a2","a3","a4","a5","a6","a7","a8","a9","a10","a11","a12","a13","a14","a15","a16","a17","a18","a19","a20","a21","a22","a23","a24","a25","a26","a27","a28","a29","a30","a31","a32","a33","a34","a35","a36","a37","a38","a39","a40","a41","a42","a43","a44","a45","a46","a47","a48","a49","a50","a51","a52","a53","a54","a55","a56","a57","a58","a59","a60","a61","a62","a63","a64","a65","a66","a67","a68","a69","a70","a71","a72","a73","a74","a75","a76","a77","a78","a79","a80","a81","a82","a83","a84","a85","a86","a87","a88","a89","a90","a91","a92","a93","a94","a95","a96","a97","a98","a99","a100","a101","a102","a103","a104","a105","a106","a107","a108","a109","a110","a111","a112","a113","a114","a115","a116","a117","a118","a119","a120","a121","a122","a123","a124","a125","a126","a127","a128","a129","a130","a131","a132","a133","a134","a135","a136","a137","a138","a139","a140","a141","a142","a143","a144","a145","a146","a147","a148","a149","a150","a151","a152","a153","a154","a155","a156","a157","a158","a159","a160","a161","a162","a163","a164","a165","a166","a167","a168","a169","a170","a171","a172","a173","a174","a175","a176","a177","a178","a179","a180","a181","a182","a183","a184","a185","a186","a187","a188","a189","a190","a191","a192","a193","a194","a195","a196","a197","a198","a199","a200","a201","a202","a203","a204","a205","a206","a207","a208","a209","a210","a211","a212","a213","a214","a215","a216","a217","a218","a219","a220","a221","a222","a223","a224","a225","a226","a227","a228","a229","a230","a231","a232","a233","a234","a235","a236","a237","a238","a239","a240","a241","a242","a243","a244","a245","a246","a247","a248","a249","a250","a251","a252","a253","a254","a255"
#define CLOB_S "s8","s9","s10","s11","s12","s13","s14","s15","s16","s17","s18","s19","s20","s21","s22","s23","s24","s25","s26","s27","s28","s29","s30","s31","s32","s33","s34","s35","s36","s37","s38","s39","s40","s41","s42","s43","s44","s45","s46","s47","s48","s49","s50","s51","s52","s53","s54","s55","s56","s57","s58","s59","s60","s61","s62","s63","s64","s65","s66","s67","s68","s69","s70","s71","s72","s73","s74","s75","s76","s77","s78","s79","s80","s81","s82","s83","s84","s85","s86","s87","s88","s89","s90","s91","s92","s93","s94","s95","s96","s97","s98","s99"
__global__ __launch_bounds__(256, 1) __attribute__((aligned(4096))) void ka_fused(const void* wq, const void* x, void* ws, const void* Kc, const void* wr, const void* Vc, const void* wo, const void* wg, const void* wu, const void* wd, void* out) {
  extern __shared__ __attribute__((aligned(16))) char lds[];
  const unsigned w = __builtin_amdgcn_readfirstlane(threadIdx.x >> 6);
  const unsigned slds = __builtin_amdgcn_readfirstlane((unsigned)(size_t)lds);
  asm volatile(
R"ASM(
.macro MM a, b, c, d:vararg
v_mfma_f32_16x16x32_bf16 \a, \b, \c, \d
.endm
.macro TR a, b:vararg
ds_read_b64_tr_b16 \a, \b
.endm
.macro CV a, b, c:vararg
v_cvt_pk_bf16_f32 \a, \b, \c
.endm
.macro AR a, b:vararg
v_accvgpr_read_b32 \a, \b
.endm
.macro AW a, b:vararg
v_accvgpr_write_b32 \a, \b
.endm
.macro EX a, b:vararg
v_exp_f32 \a, \b
.endm
.macro RD a, b:vararg
ds_read_b128 \a, \b
.endm
s_branch LPG0_%=
.p2align 6
LPG0_%=:
s_load_dwordx2 s[16:17], %[ka], 0
s_load_dwordx2 s[32:33], %[ka], 8
s_load_dwordx2 s[20:21], %[ka], 16
s_load_dwordx2 s[8:9], %[ka], 24
s_load_dwordx2 s[12:13], %[ka], 40
s_load_dwordx2 s[24:25], %[ka], 48
s_load_dwordx2 s[44:45], %[ka], 32
s_load_dwordx2 s[28:29], %[ka], 72
s_load_dwordx2 s[40:41], %[ka], 80
s_lshl_b32 s77, %[swave], 2
s_and_b32 s77, s77, 8
s_add_u32 s77, s77, 56
s_load_dwordx2 s[36:37], %[ka], s77
v_mbcnt_lo_u32_b32 v127, -1, 0
v_mbcnt_hi_u32_b32 v127, -1, v127
v_lshlrev_b32 v248, 4, v127
s_lshr_b32 s77, %[sb], 3
s_lshl_b32 s77, s77, 2
s_add_u32 s77, s77, %[swave]
s_lshl_b32 s77, s77, 10
v_add_u32 v118, s77, v248
s_mov_b32 s77, LKEND_ka-LPC_%=-16
v_min_u32 v118, s77, v118
s_getpc_b64 s[98:99]
LPC_%=:
global_load_dwordx4 a[252:255], v118, s[98:99]
s_and_b32 s77, %[sb], 7
s_lshl_b32 s77, s77, 2
s_bfe_u32 s84, %[sb], 131075
s_add_u32 s49, s77, s84
s_lshr_b32 s50, %[sb], 5
s_lshl_b32 s76, %[sb], 2
s_add_u32 s76, s76, %[swave]
v_lshrrev_b32 v180, 5, v127
s_mul_i32 s82, s76, 2560
v_and_b32 v182, 496, v248
v_mul_u32_u24 v181, 1280, v180
v_add_u32 v168, v181, v182
v_lshrrev_b32 v210, 1, v182
v_add_u32 v181, v181, v210
s_lshl_b32 s77, %[swave], 12
s_add_u32 s65, s77, %[slds]
s_lshl_b32 s89, %[swave], 17
v_bfe_u32 v210, v127, 2, 3
v_and_b32 v212, 3, v127
v_lshlrev_b32 v213, 4, v212
v_lshl_add_u32 v213, v180, 6, v213
v_lshrrev_b32 v253, 3, v127
v_lshlrev_b32 v253, 13, v253
v_and_b32 v254, 112, v248
v_or_b32 v253, v253, v254
v_and_b32 v254, 16, v127
v_lshlrev_b32 v254, 2, v254
v_xor_b32 v253, v253, v254
v_add_u32 v254, 128, v253
s_mul_i32 s96, %[swave], 4112
s_add_u32 s96, s96, %[slds]
s_lshl_b32 s77, s50, 2
s_add_u32 s77, s77, %[swave]
s_mul_i32 s68, s77, 81920
s_and_b32 s62, %[sb], 1
s_mul_i32 s63, s62, 40960
s_add_u32 s68, s68, s63
s_lshl_b32 s63, s63, 1
s_mul_i32 s83, s77, 560128
s_add_u32 s83, s83, 2752512
s_lshl_b32 s93, s77, 9
s_lshl_b32 s94, %[swave], 13
s_add_u32 s94, s94, %[slds]
v_lshrrev_b32 v211, 3, v127
v_mul_u32_u24 v220, 1280, v211
v_lshrrev_b32 v212, 4, v127
v_and_b32 v211, 7, v127
v_xor_b32 v211, v211, v212
v_lshl_add_u32 v220, v211, 4, v220
v_xor_b32 v221, 64, v220
s_bitcmp1_b32 s84, 0
s_mov_b32 s77, 0x77552200
s_cselect_b32 s77, 0x77552200, s77
s_lshl_b32 s91, s50, 2
s_lshr_b32 s77, s77, s91
s_and_b32 s77, s77, 15
s_lshl_b32 s86, s77, 7
s_lshl_b32 s85, s77, 19
v_and_b32 v211, 15, v127
v_bfe_u32 v222, v127, 1, 3
v_xor_b32 v222, v222, v212
v_lshlrev_b32 v222, 4, v222
v_lshl_add_u32 v222, v211, 7, v222
v_xor_b32 v223, 64, v222
v_add_u32 v222, s94, v222
v_add_u32 v223, s94, v223
v_and_b32 v214, 1, v212
v_lshlrev_b32 v215, 5, v214
v_xor_b32 v215, v215, v213
v_lshl_add_u32 v235, v210, 8, v215
v_add_u32 v235, 128, v235
v_and_b32 v234, 1, v127
v_mul_u32_u24 v234, 43690, v234
v_add_u32 v234, 17476, v234
v_bfe_u32 v215, v127, 2, 2
v_and_b32 v216, 3, v127
v_lshlrev_b32 v252, 7, v215
v_lshl_add_u32 v252, v216, 4, v252
v_and_b32 v251, 8, v127
v_lshl_add_u32 v252, v251, 3, v252
v_mul_u32_u24 v251, 2056, v212
s_xor_b32 s77, %[swave], 0
s_and_b32 s91, s77, 1
s_lshl_b32 s91, s91, 6
s_lshr_b32 s77, s77, 1
s_lshl_b32 s77, s77, 10
s_add_u32 s77, s77, %[slds]
s_add_u32 s77, s77, 65536
v_xor_b32 v192, s91, v252
v_add3_u32 v192, s77, v192, v251
v_add_u32 v196, 49344, v192
s_xor_b32 s77, %[swave], 1
s_and_b32 s91, s77, 1
s_lshl_b32 s91, s91, 6
s_lshr_b32 s77, s77, 1
s_lshl_b32 s77, s77, 10
s_add_u32 s77, s77, %[slds]
s_add_u32 s77, s77, 65536
v_xor_b32 v193, s91, v252
v_add3_u32 v193, s77, v193, v251
v_add_u32 v197, 49344, v193
s_xor_b32 s77, %[swave], 2
s_and_b32 s91, s77, 1
s_lshl_b32 s91, s91, 6
s_lshr_b32 s77, s77, 1
s_lshl_b32 s77, s77, 10
s_add_u32 s77, s77, %[slds]
s_add_u32 s77, s77, 65536
v_xor_b32 v194, s91, v252
v_add3_u32 v194, s77, v194, v251
v_add_u32 v198, 49344, v194
s_xor_b32 s77, %[swave], 3
s_and_b32 s91, s77, 1
s_lshl_b32 s91, s91, 6
s_lshr_b32 s77, s77, 1
s_lshl_b32 s77, s77, 10
s_add_u32 s77, s77, %[slds]
s_add_u32 s77, s77, 65536
v_xor_b32 v195, s91, v252
v_add3_u32 v195, s77, v195, v251
v_add_u32 v199, 49344, v195
v_mov_b32 v236, 0x3f803f80
v_mov_b32 v184, 0
v_mov_b32 v237, 0x3f803f80
v_mov_b32 v185, 0
v_mov_b32 v238, 0x3f803f80
v_mov_b32 v188, 0
v_mov_b32 v239, 0x3f803f80
v_mov_b32 v189, 0
s_lshl_b32 s90, s49, 8
s_waitcnt lgkmcnt(0)
s_add_u32 s16, s16, s90
s_addc_u32 s17, s17, 0
s_sub_u32 s18, 5242880, s90
s_mov_b32 s19, 131072
s_mov_b32 s34, 2621440
s_mov_b32 s35, 131072
s_mov_b32 s22, 67108864
s_mov_b32 s23, 131072
s_add_u32 s87, s89, s85
s_add_u32 s85, s85, 524288
s_cmp_ge_u32 s85, 5242880
s_cselect_b32 s85, 0, s85
s_add_u32 m0, s96, 65536
s_nop 0
buffer_load_dwordx4 v253, s[16:19], s87 offen offset:0 lds
s_add_u32 m0, s96, 66560
s_nop 0
buffer_load_dwordx4 v254, s[16:19], s87 offen offset:0 lds
s_add_u32 s87, s87, 65536
s_add_u32 m0, s96, 67592
s_nop 0
buffer_load_dwordx4 v253, s[16:19], s87 offen offset:0 lds
s_add_u32 m0, s96, 68616
s_nop 0
buffer_load_dwordx4 v254, s[16:19], s87 offen offset:0 lds
s_add_u32 s88, s68, s86
s_add_u32 s86, s86, 128
s_cmp_ge_u32 s86, 1280
s_cselect_b32 s86, 0, s86
s_add_u32 m0, s94, 0
s_add_u32 s95, s88, 0
buffer_load_dwordx4 v220, s[32:35], s95 offen offset:0 lds
s_add_u32 s95, s88, 9216
buffer_load_dwordx4 v221, s[32:35], s95 offen offset:1024 lds
s_add_u32 s95, s88, 18432
buffer_load_dwordx4 v220, s[32:35], s95 offen offset:2048 lds
s_add_u32 s95, s88, 27648
buffer_load_dwordx4 v221, s[32:35], s95 offen offset:3072 lds
s_mov_b32 s66, 32768
buffer_load_dword v169, off, s[20:23], s66 sc1
buffer_load_dwordx4 v[128:131], v168, s[32:35], s82 offen offset:0
buffer_load_dwordx4 v[132:135], v168, s[32:35], s82 offen offset:512
buffer_load_dwordx2 v[136:137], v181, s[32:35], s82 offen offset:1024
s_add_u32 s82, s82, 131072
s_sub_u32 s88, s88, s63
s_add_u32 m0, s94, 4096
s_add_u32 s95, s88, 40960
buffer_load_dwordx4 v220, s[32:35], s95 offen offset:0 lds
s_add_u32 s95, s88, 50176
buffer_load_dwordx4 v221, s[32:35], s95 offen offset:1024 lds
s_add_u32 s95, s88, 59392
buffer_load_dwordx4 v220, s[32:35], s95 offen offset:2048 lds
s_add_u32 s95, s88, 68608
buffer_load_dwordx4 v221, s[32:35], s95 offen offset:3072 lds
s_getpc_b64 s[98:99]
LTA0_%=:
s_min_u32 s73, %[swave], 3
s_lshl_b32 s73, s73, 12
s_add_u32 s73, s73, LPG1_%=-LTA0_%=
s_add_u32 s98, s98, s73
s_addc_u32 s99, s99, 0
s_mov_b64 exec, 0
s_setpc_b64 s[98:99]
LTR0_%=:
s_mov_b64 exec, -1
s_add_u32 s87, s89, s85
s_add_u32 s85, s85, 524288
s_cmp_ge_u32 s85, 5242880
s_cselect_b32 s85, 0, s85
s_add_u32 m0, s96, 81984
s_nop 0
buffer_load_dwordx4 v253, s[16:19], s87 offen offset:0 lds
s_add_u32 m0, s96, 83008
s_nop 0
buffer_load_dwordx4 v254, s[16:19], s87 offen offset:0 lds
s_add_u32 s87, s87, 65536
s_add_u32 m0, s96, 84040
s_nop 0
buffer_load_dwordx4 v253, s[16:19], s87 offen offset:0 lds
s_add_u32 m0, s96, 85064
s_nop 0
buffer_load_dwordx4 v254, s[16:19], s87 offen offset:0 lds
s_add_u32 s88, s68, s86
s_add_u32 s86, s86, 128
s_cmp_ge_u32 s86, 1280
s_cselect_b32 s86, 0, s86
s_add_u32 m0, s94, 32768
s_add_u32 s95, s88, 0
buffer_load_dwordx4 v220, s[32:35], s95 offen offset:0 lds
s_add_u32 s95, s88, 9216
buffer_load_dwordx4 v221, s[32:35], s95 offen offset:1024 lds
s_add_u32 s95, s88, 18432
buffer_load_dwordx4 v220, s[32:35], s95 offen offset:2048 lds
s_add_u32 s95, s88, 27648
buffer_load_dwordx4 v221, s[32:35], s95 offen offset:3072 lds
s_sub_u32 s88, s88, s63
s_add_u32 m0, s94, 36864
s_add_u32 s95, s88, 40960
buffer_load_dwordx4 v220, s[32:35], s95 offen offset:0 lds
s_add_u32 s95, s88, 50176
buffer_load_dwordx4 v221, s[32:35], s95 offen offset:1024 lds
s_add_u32 s95, s88, 59392
buffer_load_dwordx4 v220, s[32:35], s95 offen offset:2048 lds
s_add_u32 s95, s88, 68608
buffer_load_dwordx4 v221, s[32:35], s95 offen offset:3072 lds
s_waitcnt vmcnt(20)
RD a[128:131],v222 offset:0
RD a[132:135],v223 offset:0
RD a[136:139],v222 offset:2048
RD a[140:143],v223 offset:2048
s_barrier
TR a[0:1],v192 offset:0
TR a[2:3],v192 offset:512
TR a[4:5],v192 offset:8224
TR a[6:7],v192 offset:8736
TR a[8:9],v192 offset:8
TR a[10:11],v192 offset:520
TR a[12:13],v192 offset:8232
TR a[14:15],v192 offset:8744
TR a[16:17],v193 offset:0
TR a[18:19],v193 offset:512
TR a[20:21],v193 offset:8224
TR a[22:23],v193 offset:8736
TR a[24:25],v193 offset:8
TR a[26:27],v193 offset:520
TR a[28:29],v193 offset:8232
TR a[30:31],v193 offset:8744
s_waitcnt lgkmcnt(15)
s_waitcnt lgkmcnt(12)
MM v[0:3],a[0:3],a[128:131],0
s_add_u32 s87, s89, s85
TR a[32:33],v194 offset:0
TR a[34:35],v194 offset:512
MM v[0:3],a[4:7],a[132:135],v[0:3]
s_add_u32 s85, s85, 524288
TR a[36:37],v194 offset:8224
TR a[38:39],v194 offset:8736
MM v[8:11],a[4:7],a[140:143],0
s_waitcnt vmcnt(16)
TR a[40:41],v194 offset:8
TR a[42:43],v194 offset:520
MM v[8:11],a[0:3],a[136:139],v[8:11]
s_waitcnt lgkmcnt(14)
TR a[44:45],v194 offset:8232
TR a[46:47],v194 offset:8744
s_cmp_ge_u32 s85, 5242880
s_cselect_b32 s85, 0, s85
MM v[4:7],a[8:11],a[128:131],0
s_add_u32 m0, s96, 98432
v_max3_f32 v200, |v0|, |v1|, |v2|
v_max_f32 v200, v200, |v3|
v_readfirstlane_b32 s69, v169
MM v[4:7],a[12:15],a[132:135],v[4:7]
s_lshl_b32 s90, s76, 1
TR a[48:49],v195 offset:0
TR a[50:51],v195 offset:512
MM v[12:15],a[12:15],a[140:143],0
s_and_b32 s77, s90, 15
buffer_load_dwordx4 v253, s[16:19], s87 offen offset:0 lds
v_max3_f32 v201, |v8|, |v9|, |v10|
v_max_f32 v201, v201, |v11|
MM v[12:15],a[8:11],a[136:139],v[12:15]
s_add_u32 m0, s96, 99456
TR a[52:53],v195 offset:8224
v_min_f32 v200, v200, v201
v_and_b32 v200, 0x7f800000, v200
v_lshlrev_b32 v148, 16, v128
v_smfmac_f32_16x16x64_bf16 v[184:187], v[236:239], a[0:7], v234
s_lshl_b32 s77, s77, 1
v_add_u32 v200, 0x2c00000, v200
v_smfmac_f32_16x16x64_bf16 v[188:191], v[236:239], a[8:15], v234
s_waitcnt lgkmcnt(15)
v_min_u32 v200, 0x78400000, v200
v_mov_b32 v201, v200
v_mov_b32 v202, v200
v_mov_b32 v203, v200
s_nop 1
MM v[32:35],a[16:19],a[128:131],v[200:203]
s_bfe_u32 s91, s90, 65540
TR a[54:55],v195 offset:8736
buffer_load_dwordx4 v254, s[16:19], s87 offen offset:0 lds
v_mov_b32 v250, v200
v_and_b32 v149, 0xffff0000, v128
MM v[32:35],a[20:23],a[132:135],v[32:35]
s_add_u32 s87, s87, 65536
TR a[56:57],v195 offset:8
TR a[58:59],v195 offset:520
MM v[40:43],a[20:23],a[140:143],v[200:203]
s_add_u32 m0, s96, 100488
TR a[60:61],v195 offset:8232
TR a[62:63],v195 offset:8744
MM v[40:43],a[16:19],a[136:139],v[40:43]
s_waitcnt lgkmcnt(15)
buffer_load_dwordx4 v253, s[16:19], s87 offen offset:0 lds
v_lshlrev_b32 v150, 16, v129
MM v[36:39],a[24:27],a[128:131],v[200:203]
s_add_u32 m0, s96, 101512
v_and_b32 v151, 0xffff0000, v129
v_lshlrev_b32 v152, 16, v130
MM v[36:39],a[28:31],a[132:135],v[36:39]
s_add_u32 s77, s77, s91
v_and_b32 v153, 0xffff0000, v130
v_lshlrev_b32 v154, 16, v131
MM v[44:47],a[28:31],a[140:143],v[200:203]
s_and_b32 s91, s90, 0xffffffe0
buffer_load_dwordx4 v254, s[16:19], s87 offen offset:0 lds
MM v[44:47],a[24:27],a[136:139],v[44:47]
s_waitcnt lgkmcnt(12)
s_waitcnt vmcnt(16)
RD a[144:147],v222 offset:4096
v_and_b32 v155, 0xffff0000, v131
MM v[64:67],a[32:35],a[128:131],v[200:203]
s_add_u32 s77, s77, s91
RD a[148:151],v223 offset:4096
RD a[152:155],v222 offset:6144
MM v[64:67],a[36:39],a[132:135],v[64:67]
s_lshl_b32 s77, s77, 3
RD a[156:159],v223 offset:6144
v_lshlrev_b32 v156, 16, v132
MM v[72:75],a[36:39],a[140:143],v[200:203]
s_add_u32 s97, s77, 36864
s_waitcnt vmcnt(12)
s_barrier
v_and_b32 v157, 0xffff0000, v132
MM v[72:75],a[32:35],a[136:139],v[72:75]
s_waitcnt lgkmcnt(12)
global_load_dwordx4 v[138:141], v182, s[44:45] offset:0
MM v[68:71],a[40:43],a[128:131],v[200:203]
s_sub_u32 s79, 65535, s90
v_lshlrev_b32 v158, 16, v133
v_and_b32 v159, 0xffff0000, v133
MM v[68:71],a[44:47],a[132:135],v[68:71]
s_lshl_b32 s78, s90, 2
v_lshlrev_b32 v160, 16, v134
v_and_b32 v161, 0xffff0000, v134
MM v[76:79],a[44:47],a[140:143],v[200:203]
s_add_u32 s78, s78, 53248
global_load_dwordx4 v[142:145], v182, s[44:45] offset:512
MM v[76:79],a[40:43],a[136:139],v[76:79]
s_waitcnt lgkmcnt(8)
v_lshrrev_b32 v182, 1, v182
v_lshlrev_b32 v162, 16, v135
MM v[96:99],a[48:51],a[128:131],v[200:203]
s_lshl_b32 s80, s76, 2
global_load_dwordx2 v[146:147], v182, s[44:45] offset:1024
)ASM"
R"ASM(
MM v[96:99],a[52:55],a[132:135],v[96:99]
s_waitcnt lgkmcnt(0)
s_add_u32 s88, s68, s86
s_add_u32 s86, s86, 128
v_and_b32 v163, 0xffff0000, v135
MM v[104:107],a[52:55],a[140:143],v[200:203]
s_add_u32 s80, s80, 81920
s_cmp_ge_u32 s86, 1280
s_cselect_b32 s86, 0, s86
MM v[104:107],a[48:51],a[136:139],v[104:107]
s_add_u32 m0, s94, 0
s_add_u32 s95, s88, 0
v_lshlrev_b32 v164, 16, v136
MM v[100:103],a[56:59],a[128:131],v[200:203]
buffer_load_dwordx4 v220, s[32:35], s95 offen offset:0 lds
MM v[100:103],a[60:63],a[132:135],v[100:103]
s_add_u32 s95, s88, 9216
v_and_b32 v165, 0xffff0000, v136
v_lshlrev_b32 v166, 16, v137
MM v[108:111],a[60:63],a[140:143],v[200:203]
buffer_load_dwordx4 v221, s[32:35], s95 offen offset:1024 lds
MM v[108:111],a[56:59],a[136:139],v[108:111]
s_add_u32 s95, s88, 18432
v_and_b32 v167, 0xffff0000, v137
s_add_u32 s69, s69, 1
MM v[16:19],a[0:3],a[144:147],0
v_add_f32 v170, v148, v149
v_add_f32 v171, v150, v151
MM v[16:19],a[4:7],a[148:151],v[16:19]
v_add_f32 v170, v170, v152
v_add_f32 v171, v171, v153
MM v[24:27],a[4:7],a[156:159],0
buffer_load_dwordx4 v220, s[32:35], s95 offen offset:2048 lds
MM v[24:27],a[0:3],a[152:155],v[24:27]
s_add_u32 s95, s88, 27648
v_add_f32 v170, v170, v154
v_add_f32 v171, v171, v155
MM v[20:23],a[8:11],a[144:147],0
v_max3_f32 v204, |v16|, |v17|, |v18|
v_max_f32 v204, v204, |v19|
MM v[20:23],a[12:15],a[148:151],v[20:23]
TR a[0:1],v192 offset:16448
TR a[2:3],v192 offset:16960
v_add_f32 v170, v170, v156
MM v[28:31],a[12:15],a[156:159],0
v_max3_f32 v205, |v24|, |v25|, |v26|
v_max_f32 v205, v205, |v27|
MM v[28:31],a[8:11],a[152:155],v[28:31]
TR a[4:5],v192 offset:24672
v_min_f32 v204, v204, v205
v_and_b32 v204, 0x7f800000, v204
v_add_u32 v204, 0x2c00000, v204
v_min_u32 v204, 0x78400000, v204
v_mov_b32 v205, v204
v_mov_b32 v206, v204
v_mov_b32 v207, v204
s_nop 1
MM v[48:51],a[16:19],a[144:147],v[204:207]
TR a[6:7],v192 offset:25184
buffer_load_dwordx4 v221, s[32:35], s95 offen offset:3072 lds
v_mov_b32 v252, v204
v_add_f32 v171, v171, v157
MM v[48:51],a[20:23],a[148:151],v[48:51]
s_sub_u32 s88, s88, s63
TR a[8:9],v192 offset:16456
TR a[10:11],v192 offset:16968
MM v[56:59],a[20:23],a[156:159],v[204:207]
s_add_u32 m0, s94, 4096
TR a[12:13],v192 offset:24680
TR a[14:15],v192 offset:25192
MM v[56:59],a[16:19],a[152:155],v[56:59]
s_add_u32 s95, s88, 40960
buffer_load_dwordx4 v220, s[32:35], s95 offen offset:0 lds
v_add_f32 v170, v170, v158
MM v[52:55],a[24:27],a[144:147],v[204:207]
s_add_u32 s95, s88, 50176
TR a[16:17],v193 offset:16448
TR a[18:19],v193 offset:16960
MM v[52:55],a[28:31],a[148:151],v[52:55]
TR a[20:21],v193 offset:24672
TR a[22:23],v193 offset:25184
MM v[60:63],a[28:31],a[156:159],v[204:207]
buffer_load_dwordx4 v221, s[32:35], s95 offen offset:1024 lds
v_add_f32 v171, v171, v159
MM v[60:63],a[24:27],a[152:155],v[60:63]
s_add_u32 s95, s88, 59392
v_add_f32 v170, v170, v160
v_add_f32 v171, v171, v161
MM v[80:83],a[32:35],a[144:147],v[204:207]
TR a[24:25],v193 offset:16456
TR a[26:27],v193 offset:16968
MM v[80:83],a[36:39],a[148:151],v[80:83]
TR a[28:29],v193 offset:24680
TR a[30:31],v193 offset:25192
v_add_f32 v170, v170, v162
MM v[88:91],a[36:39],a[156:159],v[204:207]
buffer_load_dwordx4 v220, s[32:35], s95 offen offset:2048 lds
MM v[88:91],a[32:35],a[152:155],v[88:91]
s_add_u32 s95, s88, 68608
v_add_f32 v171, v171, v163
v_add_f32 v170, v170, v164
MM v[84:87],a[40:43],a[144:147],v[204:207]
TR a[32:33],v194 offset:16448
TR a[34:35],v194 offset:16960
MM v[84:87],a[44:47],a[148:151],v[84:87]
TR a[36:37],v194 offset:24672
TR a[38:39],v194 offset:25184
v_add_f32 v171, v171, v165
MM v[92:95],a[44:47],a[156:159],v[204:207]
buffer_load_dwordx4 v221, s[32:35], s95 offen offset:3072 lds
MM v[92:95],a[40:43],a[152:155],v[92:95]
s_add_u32 s87, s89, s85
s_add_u32 s85, s85, 524288
v_add_f32 v170, v170, v166
MM v[112:115],a[48:51],a[144:147],v[204:207]
TR a[40:41],v194 offset:16456
TR a[42:43],v194 offset:16968
MM v[112:115],a[52:55],a[148:151],v[112:115]
TR a[44:45],v194 offset:24680
TR a[46:47],v194 offset:25192
s_cmp_ge_u32 s85, 5242880
s_cselect_b32 s85, 0, s85
v_add_f32 v171, v171, v167
MM v[120:123],a[52:55],a[156:159],v[204:207]
s_add_u32 m0, s96, 114880
s_nop 0
buffer_load_dwordx4 v253, s[16:19], s87 offen offset:0 lds
MM v[120:123],a[48:51],a[152:155],v[120:123]
s_add_u32 m0, s96, 115904
v_add_f32 v172, v170, v171
v_lshlrev_b32 v232, 11, v180
MM v[116:119],a[56:59],a[144:147],v[204:207]
TR a[48:49],v195 offset:16448
TR a[50:51],v195 offset:16960
MM v[116:119],a[60:63],a[148:151],v[116:119]
TR a[52:53],v195 offset:24672
TR a[54:55],v195 offset:25184
MM v[124:127],a[60:63],a[156:159],v[204:207]
buffer_load_dwordx4 v254, s[16:19], s87 offen offset:0 lds
v_add_f32_dpp v172, v172, v172 row_ror:8 row_mask:0xf bank_mask:0xf
MM v[124:127],a[56:59],a[152:155],v[124:127]
s_add_u32 s87, s87, 65536
s_add_u32 m0, s96, 116936
v_add_f32_dpp v172, v172, v172 row_ror:4 row_mask:0xf bank_mask:0xf
buffer_load_dwordx4 v253, s[16:19], s87 offen offset:0 lds
s_add_u32 m0, s96, 117960
s_nop 0
buffer_load_dwordx4 v254, s[16:19], s87 offen offset:0 lds
s_waitcnt vmcnt(19)
RD a[160:163],v222 offset:32768
RD a[164:167],v223 offset:32768
RD a[168:171],v222 offset:34816
RD a[172:175],v223 offset:34816
RD a[176:179],v222 offset:36864
RD a[180:183],v223 offset:36864
RD a[184:187],v222 offset:38912
RD a[188:191],v223 offset:38912
s_waitcnt vmcnt(15) lgkmcnt(0)
s_barrier
MM v[0:3],a[0:3],a[160:163],v[0:3]
TR a[56:57],v195 offset:16456
TR a[58:59],v195 offset:16968
MM v[0:3],a[4:7],a[164:167],v[0:3]
TR a[60:61],v195 offset:24680
TR a[62:63],v195 offset:25192
MM v[8:11],a[4:7],a[172:175],v[8:11]
v_add_f32_dpp v172, v172, v172 row_ror:2 row_mask:0xf bank_mask:0xf
v_lshl_add_u32 v232, v214, 8, v232
MM v[8:11],a[0:3],a[168:171],v[8:11]
s_add_u32 s88, s68, s86
s_add_u32 s86, s86, 128
MM v[16:19],a[0:3],a[176:179],v[16:19]
s_cmp_ge_u32 s86, 1280
s_cselect_b32 s86, 0, s86
s_add_u32 m0, s94, 32768
MM v[16:19],a[4:7],a[180:183],v[16:19]
s_add_u32 s95, s88, 0
buffer_load_dwordx4 v220, s[32:35], s95 offen offset:0 lds
v_add_f32_dpp v172, v172, v172 row_ror:1 row_mask:0xf bank_mask:0xf
MM v[24:27],a[4:7],a[188:191],v[24:27]
s_add_u32 s95, s88, 9216
v_mov_b32 v173, v172
v_lshl_add_u32 v232, v215, 6, v232
MM v[24:27],a[0:3],a[184:187],v[24:27]
buffer_load_dwordx4 v221, s[32:35], s95 offen offset:1024 lds
MM v[4:7],a[8:11],a[160:163],v[4:7]
s_add_u32 s95, s88, 18432
v_permlane16_swap_b32 v172, v173
v_add_f32 v172, v172, v173
MM v[4:7],a[12:15],a[164:167],v[4:7]
v_mul_f32 v172, 0x3acccccd, v172
v_sub_f32 v176, v148, v172
MM v[12:15],a[12:15],a[172:175],v[12:15]
v_sub_f32 v177, v149, v172
v_mul_f32 v170, v176, v176
MM v[12:15],a[8:11],a[168:171],v[12:15]
buffer_load_dwordx4 v220, s[32:35], s95 offen offset:2048 lds
MM v[20:23],a[8:11],a[176:179],v[20:23]
s_add_u32 s95, s88, 27648
v_mul_f32 v171, v177, v177
v_sub_f32 v176, v150, v172
MM v[20:23],a[12:15],a[180:183],v[20:23]
v_sub_f32 v177, v151, v172
v_fma_f32 v170, v176, v176, v170
MM v[28:31],a[12:15],a[188:191],v[28:31]
v_fma_f32 v171, v177, v177, v171
v_sub_f32 v176, v152, v172
MM v[28:31],a[8:11],a[184:187],v[28:31]
buffer_load_dwordx4 v221, s[32:35], s95 offen offset:3072 lds
v_smfmac_f32_16x16x64_bf16 v[184:187], v[236:239], a[0:7], v234
s_sub_u32 s88, s88, s63
s_add_u32 m0, s94, 36864
v_sub_f32 v177, v153, v172
v_smfmac_f32_16x16x64_bf16 v[188:191], v[236:239], a[8:15], v234
s_add_u32 s95, s88, 40960
v_fma_f32 v170, v176, v176, v170
MM v[32:35],a[16:19],a[160:163],v[32:35]
TR a[0:1],v192 offset:32896
s_nop 0
LPG1_%=:
s_cbranch_execz LPG5_%=
TR a[2:3],v192 offset:33408
MM v[32:35],a[20:23],a[164:167],v[32:35]
TR a[4:5],v192 offset:41120
TR a[6:7],v192 offset:41632
v_fma_f32 v171, v177, v177, v171
MM v[40:43],a[20:23],a[172:175],v[40:43]
TR a[8:9],v192 offset:32904
TR a[10:11],v192 offset:33416
MM v[40:43],a[16:19],a[168:171],v[40:43]
TR a[12:13],v192 offset:41128
TR a[14:15],v192 offset:41640
buffer_load_dwordx4 v220, s[32:35], s95 offen offset:0 lds
MM v[48:51],a[16:19],a[176:179],v[48:51]
s_add_u32 s95, s88, 50176
v_sub_f32 v176, v154, v172
v_sub_f32 v177, v155, v172
MM v[48:51],a[20:23],a[180:183],v[48:51]
buffer_load_dwordx4 v221, s[32:35], s95 offen offset:1024 lds
MM v[56:59],a[20:23],a[188:191],v[56:59]
s_add_u32 s95, s88, 59392
v_fma_f32 v170, v176, v176, v170
v_fma_f32 v171, v177, v177, v171
MM v[56:59],a[16:19],a[184:187],v[56:59]
v_sub_f32 v176, v156, v172
v_sub_f32 v177, v157, v172
MM v[36:39],a[24:27],a[160:163],v[36:39]
TR a[16:17],v193 offset:32896
TR a[18:19],v193 offset:33408
MM v[36:39],a[28:31],a[164:167],v[36:39]
TR a[20:21],v193 offset:41120
TR a[22:23],v193 offset:41632
v_fma_f32 v170, v176, v176, v170
MM v[44:47],a[28:31],a[172:175],v[44:47]
buffer_load_dwordx4 v220, s[32:35], s95 offen offset:2048 lds
MM v[44:47],a[24:27],a[168:171],v[44:47]
s_add_u32 s95, s88, 68608
v_fma_f32 v171, v177, v177, v171
v_sub_f32 v176, v158, v172
MM v[52:55],a[24:27],a[176:179],v[52:55]
v_sub_f32 v177, v159, v172
v_fma_f32 v170, v176, v176, v170
MM v[52:55],a[28:31],a[180:183],v[52:55]
buffer_load_dwordx4 v221, s[32:35], s95 offen offset:3072 lds
MM v[60:63],a[28:31],a[188:191],v[60:63]
s_add_u32 s87, s89, s85
s_add_u32 s85, s85, 524288
v_fma_f32 v171, v177, v177, v171
MM v[60:63],a[24:27],a[184:187],v[60:63]
s_cmp_ge_u32 s85, 5242880
s_cselect_b32 s85, 0, s85
s_add_u32 m0, s96, 131328
MM v[64:67],a[32:35],a[160:163],v[64:67]
TR a[24:25],v193 offset:32904
TR a[26:27],v193 offset:33416
v_sub_f32 v176, v160, v172
MM v[64:67],a[36:39],a[164:167],v[64:67]
TR a[28:29],v193 offset:41128
TR a[30:31],v193 offset:41640
MM v[72:75],a[36:39],a[172:175],v[72:75]
buffer_load_dwordx4 v253, s[16:19], s87 offen offset:0 lds
MM v[72:75],a[32:35],a[168:171],v[72:75]
s_add_u32 m0, s96, 132352
v_sub_f32 v177, v161, v172
v_fma_f32 v170, v176, v176, v170
MM v[80:83],a[32:35],a[176:179],v[80:83]
v_fma_f32 v171, v177, v177, v171
v_sub_f32 v176, v162, v172
MM v[80:83],a[36:39],a[180:183],v[80:83]
buffer_load_dwordx4 v254, s[16:19], s87 offen offset:0 lds
MM v[88:91],a[36:39],a[188:191],v[88:91]
s_add_u32 s87, s87, 65536
s_add_u32 m0, s96, 133384
v_sub_f32 v177, v163, v172
MM v[88:91],a[32:35],a[184:187],v[88:91]
v_fma_f32 v170, v176, v176, v170
v_fma_f32 v171, v177, v177, v171
MM v[68:71],a[40:43],a[160:163],v[68:71]
TR a[32:33],v194 offset:32896
TR a[34:35],v194 offset:33408
MM v[68:71],a[44:47],a[164:167],v[68:71]
TR a[36:37],v194 offset:41120
TR a[38:39],v194 offset:41632
v_sub_f32 v176, v164, v172
MM v[76:79],a[44:47],a[172:175],v[76:79]
buffer_load_dwordx4 v253, s[16:19], s87 offen offset:0 lds
MM v[76:79],a[40:43],a[168:171],v[76:79]
s_add_u32 m0, s96, 134408
v_sub_f32 v177, v165, v172
v_fma_f32 v170, v176, v176, v170
MM v[84:87],a[40:43],a[176:179],v[84:87]
v_fma_f32 v171, v177, v177, v171
v_sub_f32 v176, v166, v172
MM v[84:87],a[44:47],a[180:183],v[84:87]
buffer_load_dwordx4 v254, s[16:19], s87 offen offset:0 lds
MM v[92:95],a[44:47],a[188:191],v[92:95]
s_waitcnt vmcnt(16)
RD a[192:195],v222 offset:0
v_sub_f32 v177, v167, v172
MM v[92:95],a[40:43],a[184:187],v[92:95]
RD a[196:199],v223 offset:0
v_fma_f32 v170, v176, v176, v170
MM v[96:99],a[48:51],a[160:163],v[96:99]
TR a[40:41],v194 offset:32904
TR a[42:43],v194 offset:33416
MM v[96:99],a[52:55],a[164:167],v[96:99]
TR a[44:45],v194 offset:41128
TR a[46:47],v194 offset:41640
v_fma_f32 v171, v177, v177, v171
MM v[104:107],a[52:55],a[172:175],v[104:107]
RD a[200:203],v222 offset:2048
v_add_f32 v170, v170, v171
MM v[104:107],a[48:51],a[168:171],v[104:107]
RD a[204:207],v223 offset:2048
v_add_f32_dpp v170, v170, v170 row_ror:8 row_mask:0xf bank_mask:0xf
MM v[112:115],a[48:51],a[176:179],v[112:115]
RD a[208:211],v222 offset:4096
v_add_f32_dpp v170, v170, v170 row_ror:4 row_mask:0xf bank_mask:0xf
MM v[112:115],a[52:55],a[180:183],v[112:115]
RD a[212:215],v223 offset:4096
v_add_f32_dpp v170, v170, v170 row_ror:2 row_mask:0xf bank_mask:0xf
MM v[120:123],a[52:55],a[188:191],v[120:123]
RD a[216:219],v222 offset:6144
v_add_f32_dpp v170, v170, v170 row_ror:1 row_mask:0xf bank_mask:0xf
MM v[120:123],a[48:51],a[184:187],v[120:123]
)ASM"
R"ASM(
s_waitcnt lgkmcnt(15)
RD a[220:223],v223 offset:6144
v_mov_b32 v173, v170
MM v[100:103],a[56:59],a[160:163],v[100:103]
TR a[48:49],v195 offset:32896
TR a[50:51],v195 offset:33408
MM v[100:103],a[60:63],a[164:167],v[100:103]
TR a[52:53],v195 offset:41120
TR a[54:55],v195 offset:41632
MM v[108:111],a[60:63],a[172:175],v[108:111]
v_permlane16_swap_b32 v170, v173
v_add_f32 v170, v170, v173
MM v[108:111],a[56:59],a[168:171],v[108:111]
s_waitcnt lgkmcnt(4)
s_add_u32 s88, s68, s86
s_add_u32 s86, s86, 128
MM v[116:119],a[56:59],a[176:179],v[116:119]
s_cmp_ge_u32 s86, 1280
s_cselect_b32 s86, 0, s86
s_add_u32 m0, s94, 0
v_mul_f32 v170, 0x3acccccd, v170
MM v[116:119],a[60:63],a[180:183],v[116:119]
s_add_u32 s95, s88, 0
buffer_load_dwordx4 v220, s[32:35], s95 offen offset:0 lds
MM v[124:127],a[60:63],a[188:191],v[124:127]
s_add_u32 s95, s88, 9216
CV v178,v172,v170
v_lshlrev_b32 v174, 16, v178
MM v[124:127],a[56:59],a[184:187],v[124:127]
buffer_load_dwordx4 v221, s[32:35], s95 offen offset:1024 lds
s_waitcnt vmcnt(14)
s_barrier
MM v[0:3],a[0:3],a[192:195],v[0:3]
s_add_u32 s95, s88, 18432
TR a[56:57],v195 offset:32904
TR a[58:59],v195 offset:33416
v_and_b32 v170, 0xffff0000, v178
MM v[0:3],a[4:7],a[196:199],v[0:3]
TR a[60:61],v195 offset:41128
TR a[62:63],v195 offset:41640
MM v[8:11],a[4:7],a[204:207],v[8:11]
v_add_f32 v170, 0x3727c5ac, v170
CV v170,v170,v170
MM v[8:11],a[0:3],a[200:203],v[8:11]
buffer_load_dwordx4 v220, s[32:35], s95 offen offset:2048 lds
MM v[16:19],a[0:3],a[208:211],v[16:19]
s_add_u32 s95, s88, 27648
v_and_b32 v170, 0xffff0000, v170
v_lshl_add_u32 v232, v216, 3, v232
MM v[16:19],a[4:7],a[212:215],v[16:19]
v_sqrt_f32 v170, v170
MM v[24:27],a[4:7],a[220:223],v[24:27]
v_mov_b32 v179, s69
CV v178,v174,v170
MM v[24:27],a[0:3],a[216:219],v[24:27]
buffer_load_dwordx4 v221, s[32:35], s95 offen offset:3072 lds
MM v[4:7],a[8:11],a[192:195],v[4:7]
s_sub_u32 s88, s88, s63
s_add_u32 m0, s94, 4096
s_add_u32 s95, s88, 40960
v_and_b32 v170, 0xffff0000, v178
MM v[4:7],a[12:15],a[196:199],v[4:7]
v_rcp_f32 v175, v170
MM v[12:15],a[12:15],a[204:207],v[12:15]
v_lshlrev_b32 v173, 4, v180
v_lshl_add_u32 v232, v214, 5, v232
MM v[12:15],a[8:11],a[200:203],v[12:15]
buffer_load_dwordx4 v220, s[32:35], s95 offen offset:0 lds
MM v[20:23],a[8:11],a[208:211],v[20:23]
s_add_u32 s95, s88, 50176
s_mov_b32 exec_lo, 1
s_mov_b32 exec_hi, 1
buffer_store_dwordx2 v[178:179], v173, s[20:23], s97 offen offset:0 sc1
s_mov_b64 exec, -1
MM v[20:23],a[12:15],a[212:215],v[20:23]
v_sub_f32 v148, v148, v174
v_sub_f32 v149, v149, v174
MM v[28:31],a[12:15],a[220:223],v[28:31]
v_sub_f32 v150, v150, v174
v_sub_f32 v151, v151, v174
MM v[28:31],a[8:11],a[216:219],v[28:31]
buffer_load_dwordx4 v221, s[32:35], s95 offen offset:1024 lds
v_smfmac_f32_16x16x64_bf16 v[184:187], v[236:239], a[0:7], v234
s_add_u32 s95, s88, 59392
v_sub_f32 v152, v152, v174
v_smfmac_f32_16x16x64_bf16 v[188:191], v[236:239], a[8:15], v234
v_sub_f32 v153, v153, v174
MM v[32:35],a[16:19],a[192:195],v[32:35]
TR a[0:1],v196 offset:0
TR a[2:3],v196 offset:512
MM v[32:35],a[20:23],a[196:199],v[32:35]
TR a[4:5],v196 offset:8224
TR a[6:7],v196 offset:8736
v_sub_f32 v154, v154, v174
MM v[40:43],a[20:23],a[204:207],v[40:43]
TR a[8:9],v196 offset:8
TR a[10:11],v196 offset:520
MM v[40:43],a[16:19],a[200:203],v[40:43]
TR a[12:13],v196 offset:8232
TR a[14:15],v196 offset:8744
buffer_load_dwordx4 v220, s[32:35], s95 offen offset:2048 lds
MM v[48:51],a[16:19],a[208:211],v[48:51]
s_add_u32 s95, s88, 68608
v_sub_f32 v155, v155, v174
v_sub_f32 v156, v156, v174
MM v[48:51],a[20:23],a[212:215],v[48:51]
buffer_load_dwordx4 v221, s[32:35], s95 offen offset:3072 lds
MM v[56:59],a[20:23],a[220:223],v[56:59]
s_add_u32 s87, s89, s85
s_add_u32 s85, s85, 524288
v_sub_f32 v157, v157, v174
MM v[56:59],a[16:19],a[216:219],v[56:59]
s_cmp_ge_u32 s85, 5242880
s_cselect_b32 s85, 0, s85
s_add_u32 m0, s96, 65536
MM v[36:39],a[24:27],a[192:195],v[36:39]
TR a[16:17],v197 offset:0
TR a[18:19],v197 offset:512
v_sub_f32 v158, v158, v174
MM v[36:39],a[28:31],a[196:199],v[36:39]
TR a[20:21],v197 offset:8224
TR a[22:23],v197 offset:8736
MM v[44:47],a[28:31],a[204:207],v[44:47]
buffer_load_dwordx4 v253, s[16:19], s87 offen offset:0 lds
MM v[44:47],a[24:27],a[200:203],v[44:47]
s_add_u32 m0, s96, 66560
v_sub_f32 v159, v159, v174
v_sub_f32 v160, v160, v174
MM v[52:55],a[24:27],a[208:211],v[52:55]
v_sub_f32 v161, v161, v174
v_sub_f32 v162, v162, v174
MM v[52:55],a[28:31],a[212:215],v[52:55]
buffer_load_dwordx4 v254, s[16:19], s87 offen offset:0 lds
MM v[60:63],a[28:31],a[220:223],v[60:63]
s_add_u32 s87, s87, 65536
s_add_u32 m0, s96, 67592
v_sub_f32 v163, v163, v174
MM v[60:63],a[24:27],a[216:219],v[60:63]
v_sub_f32 v164, v164, v174
v_sub_f32 v165, v165, v174
MM v[64:67],a[32:35],a[192:195],v[64:67]
TR a[24:25],v197 offset:8
TR a[26:27],v197 offset:520
MM v[64:67],a[36:39],a[196:199],v[64:67]
TR a[28:29],v197 offset:8232
TR a[30:31],v197 offset:8744
v_sub_f32 v166, v166, v174
MM v[72:75],a[36:39],a[204:207],v[72:75]
buffer_load_dwordx4 v253, s[16:19], s87 offen offset:0 lds
MM v[72:75],a[32:35],a[200:203],v[72:75]
s_add_u32 m0, s96, 68616
v_sub_f32 v167, v167, v174
CV v128,v148,v149
MM v[80:83],a[32:35],a[208:211],v[80:83]
CV v129,v150,v151
CV v130,v152,v153
MM v[80:83],a[36:39],a[212:215],v[80:83]
buffer_load_dwordx4 v254, s[16:19], s87 offen offset:0 lds
MM v[88:91],a[36:39],a[220:223],v[88:91]
s_waitcnt vmcnt(17)
RD a[224:227],v222 offset:32768
CV v131,v154,v155
MM v[88:91],a[32:35],a[216:219],v[88:91]
RD a[228:231],v223 offset:32768
CV v132,v156,v157
MM v[68:71],a[40:43],a[192:195],v[68:71]
TR a[32:33],v198 offset:0
TR a[34:35],v198 offset:512
MM v[68:71],a[44:47],a[196:199],v[68:71]
TR a[36:37],v198 offset:8224
TR a[38:39],v198 offset:8736
CV v133,v158,v159
MM v[76:79],a[44:47],a[204:207],v[76:79]
RD a[232:235],v222 offset:34816
CV v134,v160,v161
MM v[76:79],a[40:43],a[200:203],v[76:79]
RD a[236:239],v223 offset:34816
CV v135,v162,v163
MM v[84:87],a[40:43],a[208:211],v[84:87]
RD a[240:243],v222 offset:36864
CV v136,v164,v165
MM v[84:87],a[44:47],a[212:215],v[84:87]
RD a[244:247],v223 offset:36864
CV v137,v166,v167
MM v[92:95],a[44:47],a[220:223],v[92:95]
RD a[248:251],v222 offset:38912
v_lshlrev_b32 v148, 16, v128
MM v[92:95],a[40:43],a[216:219],v[92:95]
s_waitcnt lgkmcnt(15)
RD a[252:255],v223 offset:38912
v_and_b32 v149, 0xffff0000, v128
MM v[96:99],a[48:51],a[192:195],v[96:99]
TR a[40:41],v198 offset:8
TR a[42:43],v198 offset:520
MM v[96:99],a[52:55],a[196:199],v[96:99]
TR a[44:45],v198 offset:8232
TR a[46:47],v198 offset:8744
v_lshlrev_b32 v150, 16, v129
MM v[104:107],a[52:55],a[204:207],v[104:107]
v_and_b32 v151, 0xffff0000, v129
v_lshlrev_b32 v152, 16, v130
MM v[104:107],a[48:51],a[200:203],v[104:107]
s_waitcnt lgkmcnt(4)
s_add_u32 s88, s68, s86
s_add_u32 s86, s86, 128
MM v[112:115],a[48:51],a[208:211],v[112:115]
s_cmp_ge_u32 s86, 1280
s_cselect_b32 s86, 0, s86
s_add_u32 m0, s94, 32768
v_and_b32 v153, 0xffff0000, v130
MM v[112:115],a[52:55],a[212:215],v[112:115]
s_add_u32 s95, s88, 0
buffer_load_dwordx4 v220, s[32:35], s95 offen offset:0 lds
MM v[120:123],a[52:55],a[220:223],v[120:123]
s_add_u32 s95, s88, 9216
v_lshlrev_b32 v154, 16, v131
v_and_b32 v155, 0xffff0000, v131
MM v[120:123],a[48:51],a[216:219],v[120:123]
buffer_load_dwordx4 v221, s[32:35], s95 offen offset:1024 lds
MM v[100:103],a[56:59],a[192:195],v[100:103]
s_add_u32 s95, s88, 18432
TR a[48:49],v199 offset:0
TR a[50:51],v199 offset:512
v_lshlrev_b32 v156, 16, v132
MM v[100:103],a[60:63],a[196:199],v[100:103]
TR a[52:53],v199 offset:8224
TR a[54:55],v199 offset:8736
MM v[108:111],a[60:63],a[204:207],v[108:111]
v_and_b32 v157, 0xffff0000, v132
v_lshlrev_b32 v158, 16, v133
MM v[108:111],a[56:59],a[200:203],v[108:111]
buffer_load_dwordx4 v220, s[32:35], s95 offen offset:2048 lds
MM v[116:119],a[56:59],a[208:211],v[116:119]
s_add_u32 s95, s88, 27648
v_and_b32 v159, 0xffff0000, v133
v_lshlrev_b32 v160, 16, v134
MM v[116:119],a[60:63],a[212:215],v[116:119]
v_and_b32 v161, 0xffff0000, v134
v_lshlrev_b32 v162, 16, v135
MM v[124:127],a[60:63],a[220:223],v[124:127]
v_and_b32 v163, 0xffff0000, v135
v_lshlrev_b32 v164, 16, v136
MM v[124:127],a[56:59],a[216:219],v[124:127]
buffer_load_dwordx4 v221, s[32:35], s95 offen offset:3072 lds
s_waitcnt vmcnt(17)
s_barrier
MM v[0:3],a[0:3],a[224:227],v[0:3]
s_sub_u32 s88, s88, s63
TR a[56:57],v199 offset:8
TR a[58:59],v199 offset:520
v_and_b32 v165, 0xffff0000, v136
MM v[0:3],a[4:7],a[228:231],v[0:3]
s_add_u32 m0, s94, 36864
TR a[60:61],v199 offset:8232
TR a[62:63],v199 offset:8744
MM v[8:11],a[4:7],a[236:239],v[8:11]
s_add_u32 s95, s88, 40960
v_lshlrev_b32 v166, 16, v137
v_and_b32 v167, 0xffff0000, v137
MM v[8:11],a[0:3],a[232:235],v[8:11]
buffer_load_dwordx4 v220, s[32:35], s95 offen offset:0 lds
MM v[16:19],a[0:3],a[240:243],v[16:19]
s_add_u32 s95, s88, 50176
v_mul_f32 v148, v148, v175
v_mul_f32 v149, v149, v175
MM v[16:19],a[4:7],a[244:247],v[16:19]
v_mul_f32 v150, v150, v175
v_mul_f32 v151, v151, v175
MM v[24:27],a[4:7],a[252:255],v[24:27]
v_mul_f32 v152, v152, v175
v_mul_f32 v153, v153, v175
MM v[24:27],a[0:3],a[248:251],v[24:27]
buffer_load_dwordx4 v221, s[32:35], s95 offen offset:1024 lds
MM v[4:7],a[8:11],a[224:227],v[4:7]
s_add_u32 s95, s88, 59392
v_mul_f32 v154, v154, v175
v_mul_f32 v155, v155, v175
MM v[4:7],a[12:15],a[228:231],v[4:7]
v_mul_f32 v156, v156, v175
v_mul_f32 v157, v157, v175
MM v[12:15],a[12:15],a[236:239],v[12:15]
v_mul_f32 v158, v158, v175
v_mul_f32 v159, v159, v175
MM v[12:15],a[8:11],a[232:235],v[12:15]
buffer_load_dwordx4 v220, s[32:35], s95 offen offset:2048 lds
MM v[20:23],a[8:11],a[240:243],v[20:23]
s_add_u32 s95, s88, 68608
v_mul_f32 v160, v160, v175
v_mul_f32 v161, v161, v175
MM v[20:23],a[12:15],a[244:247],v[20:23]
v_mul_f32 v162, v162, v175
v_mul_f32 v163, v163, v175
MM v[28:31],a[12:15],a[252:255],v[28:31]
v_mul_f32 v164, v164, v175
v_mul_f32 v165, v165, v175
MM v[28:31],a[8:11],a[248:251],v[28:31]
buffer_load_dwordx4 v221, s[32:35], s95 offen offset:3072 lds
v_smfmac_f32_16x16x64_bf16 v[184:187], v[236:239], a[0:7], v234
s_add_u32 s87, s89, s85
s_add_u32 s85, s85, 524288
v_mul_f32 v166, v166, v175
v_smfmac_f32_16x16x64_bf16 v[188:191], v[236:239], a[8:15], v234
s_cmp_ge_u32 s85, 5242880
s_cselect_b32 s85, 0, s85
MM v[32:35],a[16:19],a[224:227],v[32:35]
s_add_u32 m0, s96, 81984
TR a[0:1],v196 offset:16448
TR a[2:3],v196 offset:16960
MM v[32:35],a[20:23],a[228:231],v[32:35]
TR a[4:5],v196 offset:24672
TR a[6:7],v196 offset:25184
v_mul_f32 v167, v167, v175
MM v[40:43],a[20:23],a[236:239],v[40:43]
TR a[8:9],v196 offset:16456
TR a[10:11],v196 offset:16968
MM v[40:43],a[16:19],a[232:235],v[40:43]
TR a[12:13],v196 offset:24680
TR a[14:15],v196 offset:25192
buffer_load_dwordx4 v253, s[16:19], s87 offen offset:0 lds
MM v[48:51],a[16:19],a[240:243],v[48:51]
s_add_u32 m0, s96, 83008
CV v128,v148,v149
CV v129,v150,v151
MM v[48:51],a[20:23],a[244:247],v[48:51]
buffer_load_dwordx4 v254, s[16:19], s87 offen offset:0 lds
MM v[56:59],a[20:23],a[252:255],v[56:59]
s_add_u32 s87, s87, 65536
s_add_u32 m0, s96, 84040
CV v130,v152,v153
MM v[56:59],a[16:19],a[248:251],v[56:59]
CV v131,v154,v155
CV v132,v156,v157
MM v[36:39],a[24:27],a[224:227],v[36:39]
TR a[16:17],v197 offset:16448
TR a[18:19],v197 offset:16960
MM v[36:39],a[28:31],a[228:231],v[36:39]
TR a[20:21],v197 offset:24672
TR a[22:23],v197 offset:25184
CV v133,v158,v159
MM v[44:47],a[28:31],a[236:239],v[44:47]
buffer_load_dwordx4 v253, s[16:19], s87 offen offset:0 lds
MM v[44:47],a[24:27],a[232:235],v[44:47]
s_add_u32 m0, s96, 85064
CV v134,v160,v161
CV v135,v162,v163
MM v[52:55],a[24:27],a[240:243],v[52:55]
CV v136,v164,v165
CV v137,v166,v167
MM v[52:55],a[28:31],a[244:247],v[52:55]
buffer_load_dwordx4 v254, s[16:19], s87 offen offset:0 lds
MM v[60:63],a[28:31],a[252:255],v[60:63]
s_waitcnt vmcnt(16)
RD a[128:131],v222 offset:0
v_xor_b32 v233, 32, v232
MM v[60:63],a[24:27],a[248:251],v[60:63]
s_add_u32 s77, %[slds], 65536
RD a[132:135],v223 offset:0
v_add_u32 v232, s77, v232
MM v[64:67],a[32:35],a[224:227],v[64:67]
TR a[24:25],v197 offset:16456
)ASM"
R"ASM(
TR a[26:27],v197 offset:16968
MM v[64:67],a[36:39],a[228:231],v[64:67]
TR a[28:29],v197 offset:24680
TR a[30:31],v197 offset:25192
MM v[72:75],a[36:39],a[236:239],v[72:75]
RD a[136:139],v222 offset:2048
v_add_u32 v233, s77, v233
MM v[72:75],a[32:35],a[232:235],v[72:75]
RD a[140:143],v223 offset:2048
buffer_store_dwordx4 v[128:131], v168, s[20:23], s82 offen offset:0 sc1
v_lshlrev_b32 v214, 8, v211
MM v[80:83],a[32:35],a[240:243],v[80:83]
RD a[144:147],v222 offset:4096
v_add_u32 v214, %[slds], v214
MM v[80:83],a[36:39],a[244:247],v[80:83]
RD a[148:151],v223 offset:4096
v_xor_b32 v215, v211, v212
MM v[88:91],a[36:39],a[252:255],v[88:91]
s_lshl_b32 s77, %[swave], 2
RD a[152:155],v222 offset:6144
v_xor_b32 v213, s77, v215
MM v[88:91],a[32:35],a[248:251],v[88:91]
s_waitcnt lgkmcnt(15)
RD a[156:159],v223 offset:6144
buffer_store_dwordx4 v[132:135], v168, s[20:23], s82 offen offset:512 sc1
v_xor_b32 v216, 0, v213
MM v[68:71],a[40:43],a[224:227],v[68:71]
TR a[32:33],v198 offset:16448
TR a[34:35],v198 offset:16960
MM v[68:71],a[44:47],a[228:231],v[68:71]
TR a[36:37],v198 offset:24672
TR a[38:39],v198 offset:25184
MM v[76:79],a[44:47],a[236:239],v[76:79]
buffer_store_dwordx2 v[136:137], v181, s[20:23], s82 offen offset:1024 sc1
MM v[76:79],a[40:43],a[232:235],v[76:79]
s_waitcnt lgkmcnt(4)
s_add_u32 s88, s68, s86
s_add_u32 s86, s86, 128
MM v[84:87],a[40:43],a[240:243],v[84:87]
s_cmp_ge_u32 s86, 1280
s_cselect_b32 s86, 0, s86
MM v[84:87],a[44:47],a[244:247],v[84:87]
s_add_u32 m0, s94, 0
s_add_u32 s95, s88, 0
v_lshlrev_b32 v148, 16, v128
MM v[92:95],a[44:47],a[252:255],v[92:95]
buffer_load_dwordx4 v220, s[32:35], s95 offen offset:0 lds
MM v[92:95],a[40:43],a[248:251],v[92:95]
s_add_u32 s95, s88, 9216
v_and_b32 v149, 0xffff0000, v128
v_lshlrev_b32 v176, 16, v138
MM v[96:99],a[48:51],a[224:227],v[96:99]
TR a[40:41],v198 offset:16456
TR a[42:43],v198 offset:16968
MM v[96:99],a[52:55],a[228:231],v[96:99]
TR a[44:45],v198 offset:24680
TR a[46:47],v198 offset:25192
v_and_b32 v177, 0xffff0000, v138
MM v[104:107],a[52:55],a[236:239],v[104:107]
buffer_load_dwordx4 v221, s[32:35], s95 offen offset:1024 lds
MM v[104:107],a[48:51],a[232:235],v[104:107]
s_add_u32 s95, s88, 18432
v_mul_f32 v170, v148, v176
v_mul_f32 v171, v149, v177
MM v[112:115],a[48:51],a[240:243],v[112:115]
buffer_load_dwordx4 v220, s[32:35], s95 offen offset:2048 lds
MM v[112:115],a[52:55],a[244:247],v[112:115]
s_add_u32 s95, s88, 27648
v_lshlrev_b32 v148, 16, v129
v_and_b32 v149, 0xffff0000, v129
MM v[120:123],a[52:55],a[252:255],v[120:123]
v_lshlrev_b32 v176, 16, v139
v_and_b32 v177, 0xffff0000, v139
MM v[120:123],a[48:51],a[248:251],v[120:123]
v_fma_f32 v170, v148, v176, v170
v_fma_f32 v171, v149, v177, v171
MM v[100:103],a[56:59],a[224:227],v[100:103]
TR a[48:49],v199 offset:16448
TR a[50:51],v199 offset:16960
MM v[100:103],a[60:63],a[228:231],v[100:103]
TR a[52:53],v199 offset:24672
TR a[54:55],v199 offset:25184
v_lshlrev_b32 v148, 16, v130
MM v[108:111],a[60:63],a[236:239],v[108:111]
buffer_load_dwordx4 v221, s[32:35], s95 offen offset:3072 lds
MM v[108:111],a[56:59],a[232:235],v[108:111]
s_sub_u32 s88, s88, s63
s_add_u32 m0, s94, 4096
s_add_u32 s95, s88, 40960
MM v[116:119],a[56:59],a[240:243],v[116:119]
LPG2_%=:
s_cbranch_execz LPG6_%=
buffer_load_dwordx4 v220, s[32:35], s95 offen offset:0 lds
v_and_b32 v149, 0xffff0000, v130
MM v[116:119],a[60:63],a[244:247],v[116:119]
s_add_u32 s95, s88, 50176
v_lshlrev_b32 v176, 16, v140
v_and_b32 v177, 0xffff0000, v140
MM v[124:127],a[60:63],a[252:255],v[124:127]
v_fma_f32 v170, v148, v176, v170
v_fma_f32 v171, v149, v177, v171
MM v[124:127],a[56:59],a[248:251],v[124:127]
v_lshlrev_b32 v148, 16, v131
v_and_b32 v149, 0xffff0000, v131
s_waitcnt vmcnt(20)
s_barrier
MM v[0:3],a[0:3],a[128:131],v[0:3]
TR a[56:57],v199 offset:16456
TR a[58:59],v199 offset:16968
MM v[0:3],a[4:7],a[132:135],v[0:3]
TR a[60:61],v199 offset:24680
TR a[62:63],v199 offset:25192
v_lshlrev_b32 v176, 16, v141
MM v[8:11],a[4:7],a[140:143],v[8:11]
buffer_load_dwordx4 v221, s[32:35], s95 offen offset:1024 lds
MM v[8:11],a[0:3],a[136:139],v[8:11]
s_add_u32 s95, s88, 59392
v_and_b32 v177, 0xffff0000, v141
v_fma_f32 v170, v148, v176, v170
MM v[16:19],a[0:3],a[144:147],v[16:19]
buffer_load_dwordx4 v220, s[32:35], s95 offen offset:2048 lds
MM v[16:19],a[4:7],a[148:151],v[16:19]
s_add_u32 s95, s88, 68608
v_fma_f32 v171, v149, v177, v171
v_lshlrev_b32 v148, 16, v132
MM v[24:27],a[4:7],a[156:159],v[24:27]
v_and_b32 v149, 0xffff0000, v132
v_lshlrev_b32 v176, 16, v142
MM v[24:27],a[0:3],a[152:155],v[24:27]
v_and_b32 v177, 0xffff0000, v142
v_fma_f32 v170, v148, v176, v170
MM v[4:7],a[8:11],a[128:131],v[4:7]
buffer_load_dwordx4 v221, s[32:35], s95 offen offset:3072 lds
MM v[4:7],a[12:15],a[132:135],v[4:7]
s_add_u32 s87, s89, s85
s_add_u32 s85, s85, 524288
v_fma_f32 v171, v149, v177, v171
MM v[12:15],a[12:15],a[140:143],v[12:15]
s_cmp_ge_u32 s85, 5242880
s_cselect_b32 s85, 0, s85
s_add_u32 m0, s96, 98432
MM v[12:15],a[8:11],a[136:139],v[12:15]
v_lshlrev_b32 v148, 16, v133
v_and_b32 v149, 0xffff0000, v133
MM v[20:23],a[8:11],a[144:147],v[20:23]
buffer_load_dwordx4 v253, s[16:19], s87 offen offset:0 lds
MM v[20:23],a[12:15],a[148:151],v[20:23]
s_add_u32 m0, s96, 99456
v_lshlrev_b32 v176, 16, v143
v_and_b32 v177, 0xffff0000, v143
MM v[28:31],a[12:15],a[156:159],v[28:31]
v_fma_f32 v170, v148, v176, v170
v_fma_f32 v171, v149, v177, v171
MM v[28:31],a[8:11],a[152:155],v[28:31]
v_lshlrev_b32 v148, 16, v134
v_and_b32 v149, 0xffff0000, v134
v_smfmac_f32_16x16x64_bf16 v[184:187], v[236:239], a[0:7], v234
v_lshlrev_b32 v176, 16, v144
v_smfmac_f32_16x16x64_bf16 v[188:191], v[236:239], a[8:15], v234
v_and_b32 v177, 0xffff0000, v144
MM v[32:35],a[16:19],a[128:131],v[32:35]
TR a[0:1],v192 offset:0
TR a[2:3],v192 offset:512
buffer_load_dwordx4 v254, s[16:19], s87 offen offset:0 lds
MM v[32:35],a[20:23],a[132:135],v[32:35]
s_add_u32 s87, s87, 65536
TR a[4:5],v192 offset:8224
TR a[6:7],v192 offset:8736
v_fma_f32 v170, v148, v176, v170
MM v[40:43],a[20:23],a[140:143],v[40:43]
s_add_u32 m0, s96, 100488
TR a[8:9],v192 offset:8
TR a[10:11],v192 offset:520
MM v[40:43],a[16:19],a[136:139],v[40:43]
TR a[12:13],v192 offset:8232
TR a[14:15],v192 offset:8744
MM v[48:51],a[16:19],a[144:147],v[48:51]
buffer_load_dwordx4 v253, s[16:19], s87 offen offset:0 lds
v_fma_f32 v171, v149, v177, v171
MM v[48:51],a[20:23],a[148:151],v[48:51]
s_add_u32 m0, s96, 101512
v_lshlrev_b32 v148, 16, v135
v_and_b32 v149, 0xffff0000, v135
MM v[56:59],a[20:23],a[156:159],v[56:59]
buffer_load_dwordx4 v254, s[16:19], s87 offen offset:0 lds
MM v[56:59],a[16:19],a[152:155],v[56:59]
s_waitcnt vmcnt(19)
RD a[160:163],v222 offset:32768
v_lshlrev_b32 v176, 16, v145
MM v[36:39],a[24:27],a[128:131],v[36:39]
TR a[16:17],v193 offset:0
TR a[18:19],v193 offset:512
MM v[36:39],a[28:31],a[132:135],v[36:39]
TR a[20:21],v193 offset:8224
TR a[22:23],v193 offset:8736
v_and_b32 v177, 0xffff0000, v145
MM v[44:47],a[28:31],a[140:143],v[44:47]
RD a[164:167],v223 offset:32768
v_fma_f32 v170, v148, v176, v170
MM v[44:47],a[24:27],a[136:139],v[44:47]
RD a[168:171],v222 offset:34816
v_fma_f32 v171, v149, v177, v171
MM v[52:55],a[24:27],a[144:147],v[52:55]
RD a[172:175],v223 offset:34816
v_lshlrev_b32 v148, 16, v136
MM v[52:55],a[28:31],a[148:151],v[52:55]
RD a[176:179],v222 offset:36864
v_and_b32 v149, 0xffff0000, v136
MM v[60:63],a[28:31],a[156:159],v[60:63]
RD a[180:183],v223 offset:36864
v_lshlrev_b32 v176, 16, v146
MM v[60:63],a[24:27],a[152:155],v[60:63]
s_waitcnt lgkmcnt(15)
RD a[184:187],v222 offset:38912
v_and_b32 v177, 0xffff0000, v146
MM v[64:67],a[32:35],a[128:131],v[64:67]
TR a[24:25],v193 offset:8
TR a[26:27],v193 offset:520
MM v[64:67],a[36:39],a[132:135],v[64:67]
TR a[28:29],v193 offset:8232
TR a[30:31],v193 offset:8744
v_fma_f32 v170, v148, v176, v170
MM v[72:75],a[36:39],a[140:143],v[72:75]
RD a[188:191],v223 offset:38912
v_fma_f32 v171, v149, v177, v171
MM v[72:75],a[32:35],a[136:139],v[72:75]
v_lshlrev_b32 v148, 16, v137
v_and_b32 v149, 0xffff0000, v137
MM v[80:83],a[32:35],a[144:147],v[80:83]
v_lshlrev_b32 v176, 16, v147
v_and_b32 v177, 0xffff0000, v147
MM v[80:83],a[36:39],a[148:151],v[80:83]
v_fma_f32 v170, v148, v176, v170
v_fma_f32 v171, v149, v177, v171
MM v[88:91],a[36:39],a[156:159],v[88:91]
s_waitcnt lgkmcnt(0)
s_add_u32 s88, s68, s86
s_add_u32 s86, s86, 128
MM v[88:91],a[32:35],a[152:155],v[88:91]
s_cmp_ge_u32 s86, 1280
s_cselect_b32 s86, 0, s86
s_add_u32 m0, s94, 32768
v_add_f32 v170, v170, v171
MM v[68:71],a[40:43],a[128:131],v[68:71]
s_add_u32 s95, s88, 0
TR a[32:33],v194 offset:0
TR a[34:35],v194 offset:512
MM v[68:71],a[44:47],a[132:135],v[68:71]
TR a[36:37],v194 offset:8224
TR a[38:39],v194 offset:8736
MM v[76:79],a[44:47],a[140:143],v[76:79]
buffer_load_dwordx4 v220, s[32:35], s95 offen offset:0 lds
v_add_f32_dpp v170, v170, v170 row_ror:8 row_mask:0xf bank_mask:0xf
MM v[76:79],a[40:43],a[136:139],v[76:79]
s_add_u32 s95, s88, 9216
v_add_f32_dpp v170, v170, v170 row_ror:4 row_mask:0xf bank_mask:0xf
v_lshl_add_u32 v224, v216, 4, v214
MM v[84:87],a[40:43],a[144:147],v[84:87]
buffer_load_dwordx4 v221, s[32:35], s95 offen offset:1024 lds
MM v[84:87],a[44:47],a[148:151],v[84:87]
s_add_u32 s95, s88, 18432
v_add_f32_dpp v170, v170, v170 row_ror:2 row_mask:0xf bank_mask:0xf
v_xor_b32 v216, 4, v213
MM v[92:95],a[44:47],a[156:159],v[92:95]
v_add_f32_dpp v170, v170, v170 row_ror:1 row_mask:0xf bank_mask:0xf
v_mov_b32 v173, v170
MM v[92:95],a[40:43],a[152:155],v[92:95]
s_nop 0
v_permlane16_swap_b32 v170, v173
v_add_f32 v170, v170, v173
MM v[96:99],a[48:51],a[128:131],v[96:99]
TR a[40:41],v194 offset:8
TR a[42:43],v194 offset:520
MM v[96:99],a[52:55],a[132:135],v[96:99]
TR a[44:45],v194 offset:8232
TR a[46:47],v194 offset:8744
CV v170,v170,v170
MM v[104:107],a[52:55],a[140:143],v[104:107]
buffer_load_dwordx4 v220, s[32:35], s95 offen offset:2048 lds
MM v[104:107],a[48:51],a[136:139],v[104:107]
s_add_u32 s95, s88, 27648
v_ashrrev_i32 v171, 31, v170
v_or_b32 v171, 0x80000000, v171
MM v[112:115],a[48:51],a[144:147],v[112:115]
buffer_load_dwordx4 v221, s[32:35], s95 offen offset:3072 lds
MM v[112:115],a[52:55],a[148:151],v[112:115]
s_sub_u32 s88, s88, s63
s_add_u32 m0, s94, 36864
s_add_u32 s95, s88, 40960
v_xor_b32 v170, v170, v171
MM v[120:123],a[52:55],a[156:159],v[120:123]
v_and_b32 v170, 0xffff0000, v170
v_sub_u32 v171, s79, v180
MM v[120:123],a[48:51],a[152:155],v[120:123]
v_or_b32 v170, v170, v171
v_lshlrev_b32 v173, 2, v180
MM v[100:103],a[56:59],a[128:131],v[100:103]
TR a[48:49],v195 offset:0
TR a[50:51],v195 offset:512
MM v[100:103],a[60:63],a[132:135],v[100:103]
TR a[52:53],v195 offset:8224
TR a[54:55],v195 offset:8736
MM v[108:111],a[60:63],a[140:143],v[108:111]
buffer_load_dwordx4 v220, s[32:35], s95 offen offset:0 lds
s_mov_b32 exec_lo, 1
s_mov_b32 exec_hi, 1
buffer_store_dword v170, v173, s[20:23], s78 offen offset:0 sc1
s_mov_b64 exec, -1
MM v[108:111],a[56:59],a[136:139],v[108:111]
s_add_u32 s95, s88, 50176
v_mov_b32 v171, s69
v_lshl_add_u32 v225, v216, 4, v214
MM v[116:119],a[56:59],a[144:147],v[116:119]
buffer_load_dwordx4 v221, s[32:35], s95 offen offset:1024 lds
MM v[116:119],a[60:63],a[148:151],v[116:119]
s_add_u32 s95, s88, 59392
v_xor_b32 v216, 8, v213
v_lshl_add_u32 v226, v216, 4, v214
MM v[124:127],a[60:63],a[156:159],v[124:127]
v_xor_b32 v216, 12, v213
v_lshl_add_u32 v227, v216, 4, v214
MM v[124:127],a[56:59],a[152:155],v[124:127]
v_lshlrev_b32 v214, 8, v212
v_xor_b32 v216, 0, v215
s_waitcnt vmcnt(22)
s_barrier
MM v[0:3],a[0:3],a[160:163],v[0:3]
TR a[56:57],v195 offset:8
TR a[58:59],v195 offset:520
MM v[0:3],a[4:7],a[164:167],v[0:3]
TR a[60:61],v195 offset:8232
TR a[62:63],v195 offset:8744
MM v[8:11],a[4:7],a[172:175],v[8:11]
buffer_load_dwordx4 v220, s[32:35], s95 offen offset:2048 lds
MM v[8:11],a[0:3],a[168:171],v[8:11]
s_add_u32 s95, s88, 68608
v_lshl_add_u32 v228, v216, 4, v214
v_xor_b32 v216, 4, v215
MM v[16:19],a[0:3],a[176:179],v[16:19]
buffer_load_dwordx4 v221, s[32:35], s95 offen offset:3072 lds
MM v[16:19],a[4:7],a[180:183],v[16:19]
s_add_u32 s87, s89, s85
)ASM"
R"ASM(
s_add_u32 s85, s85, 524288
v_lshl_add_u32 v229, v216, 4, v214
MM v[24:27],a[4:7],a[188:191],v[24:27]
s_cmp_ge_u32 s85, 5242880
s_cselect_b32 s85, 0, s85
s_add_u32 m0, s96, 114880
MM v[24:27],a[0:3],a[184:187],v[24:27]
v_xor_b32 v216, 8, v215
v_lshl_add_u32 v230, v216, 4, v214
MM v[4:7],a[8:11],a[160:163],v[4:7]
buffer_load_dwordx4 v253, s[16:19], s87 offen offset:0 lds
MM v[4:7],a[12:15],a[164:167],v[4:7]
s_add_u32 m0, s96, 115904
v_xor_b32 v216, 12, v215
v_lshl_add_u32 v231, v216, 4, v214
MM v[12:15],a[12:15],a[172:175],v[12:15]
s_lshl_b32 s90, s49, 19
s_add_u32 s8, s8, s90
s_addc_u32 s9, s9, 0
MM v[12:15],a[8:11],a[168:171],v[12:15]
s_mov_b32 s10, 524288
s_mov_b32 s11, 131072
MM v[20:23],a[8:11],a[176:179],v[20:23]
buffer_load_dwordx4 v254, s[16:19], s87 offen offset:0 lds
MM v[20:23],a[12:15],a[180:183],v[20:23]
s_add_u32 s87, s87, 65536
s_add_u32 s12, s12, s90
s_addc_u32 s13, s13, 0
MM v[28:31],a[12:15],a[188:191],v[28:31]
s_add_u32 m0, s96, 116936
s_mov_b32 s14, 525312
s_mov_b32 s15, 131072
MM v[28:31],a[8:11],a[184:187],v[28:31]
s_sub_u32 s12, s12, 1024
s_subb_u32 s13, s13, 0
s_lshr_b32 s90, s49, 3
v_smfmac_f32_16x16x64_bf16 v[184:187], v[236:239], a[0:7], v234
s_mul_i32 s90, s90, 1310720
s_mul_i32 s77, s84, 320
v_smfmac_f32_16x16x64_bf16 v[188:191], v[236:239], a[8:15], v234
s_add_u32 s90, s90, s77
MM v[32:35],a[16:19],a[160:163],v[32:35]
TR a[0:1],v192 offset:16448
TR a[2:3],v192 offset:16960
buffer_load_dwordx4 v253, s[16:19], s87 offen offset:0 lds
MM v[32:35],a[20:23],a[164:167],v[32:35]
s_add_u32 m0, s96, 117960
TR a[4:5],v192 offset:24672
TR a[6:7],v192 offset:25184
MM v[40:43],a[20:23],a[172:175],v[40:43]
TR a[8:9],v192 offset:16456
TR a[10:11],v192 offset:16968
MM v[40:43],a[16:19],a[168:171],v[40:43]
TR a[12:13],v192 offset:24680
TR a[14:15],v192 offset:25192
MM v[48:51],a[16:19],a[176:179],v[48:51]
buffer_load_dwordx4 v254, s[16:19], s87 offen offset:0 lds
MM v[48:51],a[20:23],a[180:183],v[48:51]
s_waitcnt vmcnt(17)
s_add_u32 s24, s24, s90
s_addc_u32 s25, s25, 0
MM v[56:59],a[20:23],a[188:191],v[56:59]
s_sub_u32 s26, 5242880, s90
RD a[192:195],v222 offset:0
s_mov_b32 s27, 131072
MM v[56:59],a[16:19],a[184:187],v[56:59]
s_mov_b32 s38, 2211840
RD a[196:199],v223 offset:0
s_mov_b32 s39, 131072
MM v[36:39],a[24:27],a[160:163],v[36:39]
s_mov_b32 s42, 2621440
TR a[16:17],v193 offset:16448
TR a[18:19],v193 offset:16960
MM v[36:39],a[28:31],a[164:167],v[36:39]
s_mov_b32 s43, 131072
TR a[20:21],v193 offset:24672
TR a[22:23],v193 offset:25184
MM v[44:47],a[28:31],a[172:175],v[44:47]
s_mov_b32 s71, 0x4e800000
RD a[200:203],v222 offset:2048
s_mov_b32 s72, 0x3e0293ee
MM v[44:47],a[24:27],a[168:171],v[44:47]
s_lshl_b32 s64, %[swave], 12
RD a[204:207],v223 offset:2048
MM v[52:55],a[24:27],a[176:179],v[52:55]
RD a[208:211],v222 offset:4096
MM v[52:55],a[28:31],a[180:183],v[52:55]
RD a[212:215],v223 offset:4096
MM v[60:63],a[28:31],a[188:191],v[60:63]
RD a[216:219],v222 offset:6144
MM v[60:63],a[24:27],a[184:187],v[60:63]
s_waitcnt lgkmcnt(15)
RD a[220:223],v223 offset:6144
MM v[64:67],a[32:35],a[160:163],v[64:67]
TR a[24:25],v193 offset:16456
TR a[26:27],v193 offset:16968
MM v[64:67],a[36:39],a[164:167],v[64:67]
TR a[28:29],v193 offset:24680
TR a[30:31],v193 offset:25192
MM v[72:75],a[36:39],a[172:175],v[72:75]
MM v[72:75],a[32:35],a[168:171],v[72:75]
s_waitcnt lgkmcnt(4)
s_add_u32 s88, s68, s86
s_add_u32 s86, s86, 128
MM v[80:83],a[32:35],a[176:179],v[80:83]
s_cmp_ge_u32 s86, 1280
s_cselect_b32 s86, 0, s86
s_add_u32 m0, s94, 0
MM v[80:83],a[36:39],a[180:183],v[80:83]
s_add_u32 s95, s88, 0
buffer_load_dwordx4 v220, s[32:35], s95 offen offset:0 lds
MM v[88:91],a[36:39],a[188:191],v[88:91]
s_add_u32 s95, s88, 9216
MM v[88:91],a[32:35],a[184:187],v[88:91]
buffer_load_dwordx4 v221, s[32:35], s95 offen offset:1024 lds
MM v[68:71],a[40:43],a[160:163],v[68:71]
s_add_u32 s95, s88, 18432
TR a[32:33],v194 offset:16448
TR a[34:35],v194 offset:16960
MM v[68:71],a[44:47],a[164:167],v[68:71]
TR a[36:37],v194 offset:24672
TR a[38:39],v194 offset:25184
MM v[76:79],a[44:47],a[172:175],v[76:79]
MM v[76:79],a[40:43],a[168:171],v[76:79]
buffer_load_dwordx4 v220, s[32:35], s95 offen offset:2048 lds
MM v[84:87],a[40:43],a[176:179],v[84:87]
s_add_u32 s95, s88, 27648
MM v[84:87],a[44:47],a[180:183],v[84:87]
MM v[92:95],a[44:47],a[188:191],v[92:95]
MM v[92:95],a[40:43],a[184:187],v[92:95]
buffer_load_dwordx4 v221, s[32:35], s95 offen offset:3072 lds
MM v[96:99],a[48:51],a[160:163],v[96:99]
s_sub_u32 s88, s88, s63
TR a[40:41],v194 offset:16456
TR a[42:43],v194 offset:16968
MM v[96:99],a[52:55],a[164:167],v[96:99]
s_add_u32 m0, s94, 4096
TR a[44:45],v194 offset:24680
TR a[46:47],v194 offset:25192
MM v[104:107],a[52:55],a[172:175],v[104:107]
s_add_u32 s95, s88, 40960
MM v[104:107],a[48:51],a[168:171],v[104:107]
buffer_load_dwordx4 v220, s[32:35], s95 offen offset:0 lds
MM v[112:115],a[48:51],a[176:179],v[112:115]
s_add_u32 s95, s88, 50176
MM v[112:115],a[52:55],a[180:183],v[112:115]
MM v[120:123],a[52:55],a[188:191],v[120:123]
MM v[120:123],a[48:51],a[184:187],v[120:123]
buffer_load_dwordx4 v221, s[32:35], s95 offen offset:1024 lds
MM v[100:103],a[56:59],a[160:163],v[100:103]
s_add_u32 s95, s88, 59392
TR a[48:49],v195 offset:16448
TR a[50:51],v195 offset:16960
MM v[100:103],a[60:63],a[164:167],v[100:103]
TR a[52:53],v195 offset:24672
TR a[54:55],v195 offset:25184
MM v[108:111],a[60:63],a[172:175],v[108:111]
MM v[108:111],a[56:59],a[168:171],v[108:111]
buffer_load_dwordx4 v220, s[32:35], s95 offen offset:2048 lds
MM v[116:119],a[56:59],a[176:179],v[116:119]
s_add_u32 s95, s88, 68608
MM v[116:119],a[60:63],a[180:183],v[116:119]
MM v[124:127],a[60:63],a[188:191],v[124:127]
MM v[124:127],a[56:59],a[184:187],v[124:127]
buffer_load_dwordx4 v221, s[32:35], s95 offen offset:3072 lds
s_waitcnt vmcnt(21)
s_barrier
MM v[0:3],a[0:3],a[192:195],v[0:3]
s_add_u32 s87, s89, s85
TR a[56:57],v195 offset:16456
TR a[58:59],v195 offset:16968
MM v[0:3],a[4:7],a[196:199],v[0:3]
s_add_u32 s85, s85, 524288
TR a[60:61],v195 offset:24680
TR a[62:63],v195 offset:25192
MM v[8:11],a[4:7],a[204:207],v[8:11]
s_cmp_ge_u32 s85, 5242880
s_cselect_b32 s85, 0, s85
s_add_u32 m0, s96, 131328
v_and_b32 v208, 240, v248
MM v[8:11],a[0:3],a[200:203],v[8:11]
s_lshl_b32 s62, s62, 8
buffer_load_dwordx4 v253, s[16:19], s87 offen offset:0 lds
MM v[16:19],a[0:3],a[208:211],v[16:19]
s_add_u32 m0, s96, 132352
s_add_u32 s93, s93, 36864
s_add_u32 s62, s62, s93
MM v[16:19],a[4:7],a[212:215],v[16:19]
s_xor_b32 s93, s62, 256
MM v[24:27],a[4:7],a[220:223],v[24:27]
MM v[24:27],a[0:3],a[216:219],v[24:27]
buffer_load_dwordx4 v254, s[16:19], s87 offen offset:0 lds
MM v[4:7],a[8:11],a[192:195],v[4:7]
s_add_u32 s87, s87, 65536
s_add_u32 m0, s96, 133384
MM v[4:7],a[12:15],a[196:199],v[4:7]
MM v[12:15],a[12:15],a[204:207],v[12:15]
MM v[12:15],a[8:11],a[200:203],v[12:15]
buffer_load_dwordx4 v253, s[16:19], s87 offen offset:0 lds
MM v[20:23],a[8:11],a[208:211],v[20:23]
s_add_u32 m0, s96, 134408
MM v[20:23],a[12:15],a[212:215],v[20:23]
MM v[28:31],a[12:15],a[220:223],v[28:31]
MM v[28:31],a[8:11],a[216:219],v[28:31]
buffer_load_dwordx4 v254, s[16:19], s87 offen offset:0 lds
v_smfmac_f32_16x16x64_bf16 v[184:187], v[236:239], a[0:7], v234
s_waitcnt vmcnt(16)
RD a[224:227],v222 offset:32768
v_smfmac_f32_16x16x64_bf16 v[188:191], v[236:239], a[8:15], v234
RD a[228:231],v223 offset:32768
MM v[32:35],a[16:19],a[192:195],v[32:35]
TR a[0:1],v192 offset:32896
TR a[2:3],v192 offset:33408
MM v[32:35],a[20:23],a[196:199],v[32:35]
TR a[4:5],v192 offset:41120
TR a[6:7],v192 offset:41632
MM v[40:43],a[20:23],a[204:207],v[40:43]
TR a[8:9],v192 offset:32904
TR a[10:11],v192 offset:33416
RD a[232:235],v222 offset:34816
MM v[40:43],a[16:19],a[200:203],v[40:43]
TR a[12:13],v192 offset:41128
TR a[14:15],v192 offset:41640
buffer_load_dwordx4 v[200:203], v208, s[20:23], s62 offen offset:0 sc1
MM v[48:51],a[16:19],a[208:211],v[48:51]
RD a[236:239],v223 offset:34816
MM v[48:51],a[20:23],a[212:215],v[48:51]
buffer_load_dwordx4 v[204:207], v208, s[20:23], s93 offen offset:0 sc1
MM v[56:59],a[20:23],a[220:223],v[56:59]
RD a[240:243],v222 offset:36864
MM v[56:59],a[16:19],a[216:219],v[56:59]
s_waitcnt lgkmcnt(15)
RD a[244:247],v223 offset:36864
MM v[36:39],a[24:27],a[192:195],v[36:39]
TR a[16:17],v193 offset:32896
TR a[18:19],v193 offset:33408
MM v[36:39],a[28:31],a[196:199],v[36:39]
TR a[20:21],v193 offset:41120
TR a[22:23],v193 offset:41632
MM v[44:47],a[28:31],a[204:207],v[44:47]
RD a[248:251],v222 offset:38912
MM v[44:47],a[24:27],a[200:203],v[44:47]
RD a[252:255],v223 offset:38912
MM v[52:55],a[24:27],a[208:211],v[52:55]
MM v[52:55],a[28:31],a[212:215],v[52:55]
MM v[60:63],a[28:31],a[220:223],v[60:63]
MM v[60:63],a[24:27],a[216:219],v[60:63]
s_waitcnt lgkmcnt(0)
s_add_u32 s88, s68, s86
s_add_u32 s86, s86, 128
MM v[64:67],a[32:35],a[192:195],v[64:67]
TR a[24:25],v193 offset:32904
TR a[26:27],v193 offset:33416
MM v[64:67],a[36:39],a[196:199],v[64:67]
TR a[28:29],v193 offset:41128
TR a[30:31],v193 offset:41640
s_cmp_ge_u32 s86, 1280
s_cselect_b32 s86, 0, s86
MM v[72:75],a[36:39],a[204:207],v[72:75]
s_add_u32 m0, s94, 32768
s_add_u32 s95, s88, 0
MM v[72:75],a[32:35],a[200:203],v[72:75]
buffer_load_dwordx4 v220, s[32:35], s95 offen offset:0 lds
MM v[80:83],a[32:35],a[208:211],v[80:83]
s_add_u32 s95, s88, 9216
MM v[80:83],a[36:39],a[212:215],v[80:83]
buffer_load_dwordx4 v221, s[32:35], s95 offen offset:1024 lds
MM v[88:91],a[36:39],a[220:223],v[88:91]
s_add_u32 s95, s88, 18432
MM v[88:91],a[32:35],a[216:219],v[88:91]
MM v[68:71],a[40:43],a[192:195],v[68:71]
TR a[32:33],v194 offset:32896
TR a[34:35],v194 offset:33408
MM v[68:71],a[44:47],a[196:199],v[68:71]
TR a[36:37],v194 offset:41120
TR a[38:39],v194 offset:41632
MM v[76:79],a[44:47],a[204:207],v[76:79]
buffer_load_dwordx4 v220, s[32:35], s95 offen offset:2048 lds
MM v[76:79],a[40:43],a[200:203],v[76:79]
s_add_u32 s95, s88, 27648
MM v[84:87],a[40:43],a[208:211],v[84:87]
MM v[84:87],a[44:47],a[212:215],v[84:87]
buffer_load_dwordx4 v221, s[32:35], s95 offen offset:3072 lds
MM v[92:95],a[44:47],a[220:223],v[92:95]
s_waitcnt vmcnt(10)
RD a[128:131],v222 offset:0
MM v[92:95],a[40:43],a[216:219],v[92:95]
RD a[132:135],v223 offset:0
MM v[96:99],a[48:51],a[192:195],v[96:99]
TR a[40:41],v194 offset:32904
TR a[42:43],v194 offset:33416
MM v[96:99],a[52:55],a[196:199],v[96:99]
TR a[44:45],v194 offset:41128
TR a[46:47],v194 offset:41640
MM v[104:107],a[52:55],a[204:207],v[104:107]
RD a[136:139],v222 offset:2048
MM v[104:107],a[48:51],a[200:203],v[104:107]
RD a[140:143],v223 offset:2048
MM v[112:115],a[48:51],a[208:211],v[112:115]
RD a[144:147],v222 offset:4096
MM v[112:115],a[52:55],a[212:215],v[112:115]
RD a[148:151],v223 offset:4096
MM v[120:123],a[52:55],a[220:223],v[120:123]
s_nop 0
LPG3_%=:
s_cbranch_execz LPG7_%=
RD a[152:155],v222 offset:6144
MM v[120:123],a[48:51],a[216:219],v[120:123]
RD a[156:159],v223 offset:6144
MM v[100:103],a[56:59],a[192:195],v[100:103]
s_sub_u32 s88, s88, s63
TR a[48:49],v195 offset:32896
TR a[50:51],v195 offset:33408
MM v[100:103],a[60:63],a[196:199],v[100:103]
s_add_u32 m0, s94, 36864
TR a[52:53],v195 offset:41120
TR a[54:55],v195 offset:41632
MM v[108:111],a[60:63],a[204:207],v[108:111]
s_add_u32 s95, s88, 40960
buffer_load_dwordx4 v220, s[32:35], s95 offen offset:0 lds
MM v[108:111],a[56:59],a[200:203],v[108:111]
s_add_u32 s95, s88, 50176
MM v[116:119],a[56:59],a[208:211],v[116:119]
buffer_load_dwordx4 v221, s[32:35], s95 offen offset:1024 lds
MM v[116:119],a[60:63],a[212:215],v[116:119]
s_add_u32 s95, s88, 59392
MM v[124:127],a[60:63],a[220:223],v[124:127]
MM v[124:127],a[56:59],a[216:219],v[124:127]
buffer_load_dwordx4 v220, s[32:35], s95 offen offset:2048 lds
s_add_u32 s95, s88, 68608
buffer_load_dwordx4 v221, s[32:35], s95 offen offset:3072 lds
s_waitcnt lgkmcnt(4)
s_waitcnt vmcnt(10)
s_barrier
MM v[0:3],a[0:3],a[224:227],v[0:3]
s_add_u32 s77, s64, 0
TR a[56:57],v195 offset:32904
TR a[58:59],v195 offset:33416
MM v[0:3],a[4:7],a[228:231],v[0:3]
s_add_u32 m0, s65, 0
TR a[60:61],v195 offset:41128
TR a[62:63],v195 offset:41640
)ASM"
R"ASM(
MM v[8:11],a[4:7],a[236:239],v[8:11]
s_waitcnt vmcnt(8)
TR a[64:65],v196 offset:0
TR a[66:67],v196 offset:512
MM v[8:11],a[0:3],a[232:235],v[8:11]
s_lshl_b32 s76, %[swave], 7
buffer_load_dwordx4 v228, s[8:11], s77 offen offset:0 lds
v_lshrrev_b32 v253, 8, v248
v_lshlrev_b32 v253, 5, v253
MM v[16:19],a[0:3],a[240:243],v[16:19]
s_add_u32 s76, s76, %[slds]
TR a[68:69],v196 offset:8224
TR a[70:71],v196 offset:8736
v_xor_b32 v201, s69, v201
MM v[16:19],a[4:7],a[244:247],v[16:19]
s_mov_b64 exec, 1
buffer_store_dword v171, off, s[20:23], s80 sc1
s_mov_b64 exec, -1
v_add_u32 v253, s76, v253
v_add_u32 v253, 151552, v253
MM v[24:27],a[4:7],a[252:255],v[24:27]
TR a[72:73],v196 offset:16448
TR a[74:75],v196 offset:16960
MM v[24:27],a[0:3],a[248:251],v[24:27]
TR a[76:77],v196 offset:24672
TR a[78:79],v196 offset:25184
buffer_load_dwordx4 v229, s[8:11], s77 offen offset:1024 lds
v_xor_b32 v203, s69, v203
MM v[4:7],a[8:11],a[224:227],v[4:7]
TR a[80:81],v196 offset:8
TR a[82:83],v196 offset:520
MM v[4:7],a[12:15],a[228:231],v[4:7]
TR a[84:85],v196 offset:8232
TR a[86:87],v196 offset:8744
MM v[12:15],a[12:15],a[236:239],v[12:15]
TR a[88:89],v196 offset:16456
TR a[90:91],v196 offset:16968
v_xor_b32 v205, s69, v205
MM v[12:15],a[8:11],a[232:235],v[12:15]
TR a[92:93],v196 offset:24680
TR a[94:95],v196 offset:25192
buffer_load_dwordx4 v230, s[8:11], s77 offen offset:2048 lds
MM v[20:23],a[8:11],a[240:243],v[20:23]
TR a[96:97],v197 offset:0
TR a[98:99],v197 offset:512
MM v[20:23],a[12:15],a[244:247],v[20:23]
TR a[100:101],v197 offset:8224
TR a[102:103],v197 offset:8736
v_xor_b32 v207, s69, v207
MM v[28:31],a[12:15],a[252:255],v[28:31]
TR a[104:105],v197 offset:16448
TR a[106:107],v197 offset:16960
MM v[28:31],a[8:11],a[248:251],v[28:31]
TR a[108:109],v197 offset:24672
TR a[110:111],v197 offset:25184
buffer_load_dwordx4 v231, s[8:11], s77 offen offset:3072 lds
v_smfmac_f32_16x16x64_bf16 v[184:187], v[236:239], a[0:7], v234
s_add_u32 s77, s64, 16384
TR a[112:113],v197 offset:8
v_or3_b32 v209, v201, v203, v205
v_smfmac_f32_16x16x64_bf16 v[188:191], v[236:239], a[8:15], v234
s_add_u32 m0, s65, 16384
TR a[114:115],v197 offset:520
MM v[32:35],a[16:19],a[224:227],v[32:35]
TR a[116:117],v197 offset:8232
TR a[118:119],v197 offset:8744
MM v[32:35],a[20:23],a[228:231],v[32:35]
TR a[120:121],v197 offset:16456
TR a[122:123],v197 offset:16968
v_or_b32 v209, v209, v207
MM v[40:43],a[20:23],a[236:239],v[40:43]
TR a[124:125],v197 offset:24680
TR a[126:127],v197 offset:25192
MM v[40:43],a[16:19],a[232:235],v[40:43]
TR a[0:1],v198 offset:0
TR a[2:3],v198 offset:512
buffer_load_dwordx4 v228, s[8:11], s77 offen offset:0 lds
MM v[48:51],a[16:19],a[240:243],v[48:51]
TR a[4:5],v198 offset:8224
TR a[6:7],v198 offset:8736
v_cmp_ne_u32 vcc, 0, v209
s_cbranch_vccnz LRG1o_%=
LRG1ob_%=:
MM v[48:51],a[20:23],a[244:247],v[48:51]
TR a[8:9],v198 offset:16448
TR a[10:11],v198 offset:16960
MM v[56:59],a[20:23],a[252:255],v[56:59]
TR a[12:13],v198 offset:24672
TR a[14:15],v198 offset:25184
MM v[56:59],a[16:19],a[248:251],v[56:59]
v_lshlrev_b32 v240, 16, v200
v_and_b32 v200, 0xffff0000, v200
MM v[36:39],a[24:27],a[224:227],v[36:39]
v_rcp_f32 v244, v200
MM v[36:39],a[28:31],a[228:231],v[36:39]
v_xor_b32 v240, 0x80000000, v240
v_mul_f32 v244, s72, v244
MM v[44:47],a[28:31],a[236:239],v[44:47]
v_lshlrev_b32 v241, 16, v202
v_and_b32 v202, 0xffff0000, v202
MM v[44:47],a[24:27],a[232:235],v[44:47]
buffer_load_dwordx4 v229, s[8:11], s77 offen offset:1024 lds
MM v[52:55],a[24:27],a[240:243],v[52:55]
v_rcp_f32 v245, v202
MM v[52:55],a[28:31],a[244:247],v[52:55]
v_xor_b32 v241, 0x80000000, v241
v_mul_f32 v245, s72, v245
MM v[60:63],a[28:31],a[252:255],v[60:63]
v_lshlrev_b32 v242, 16, v204
v_and_b32 v204, 0xffff0000, v204
MM v[60:63],a[24:27],a[248:251],v[60:63]
v_rcp_f32 v246, v204
MM v[64:67],a[32:35],a[224:227],v[64:67]
TR a[16:17],v198 offset:8
TR a[18:19],v198 offset:520
MM v[64:67],a[36:39],a[228:231],v[64:67]
TR a[20:21],v198 offset:8232
TR a[22:23],v198 offset:8744
v_xor_b32 v242, 0x80000000, v242
MM v[72:75],a[36:39],a[236:239],v[72:75]
TR a[24:25],v198 offset:16456
TR a[26:27],v198 offset:16968
MM v[72:75],a[32:35],a[232:235],v[72:75]
TR a[28:29],v198 offset:24680
TR a[30:31],v198 offset:25192
MM v[80:83],a[32:35],a[240:243],v[80:83]
v_mul_f32 v246, s72, v246
v_lshlrev_b32 v243, 16, v206
MM v[80:83],a[36:39],a[244:247],v[80:83]
buffer_load_dwordx4 v230, s[8:11], s77 offen offset:2048 lds
MM v[88:91],a[36:39],a[252:255],v[88:91]
v_and_b32 v206, 0xffff0000, v206
MM v[88:91],a[32:35],a[248:251],v[88:91]
s_waitcnt lgkmcnt(15)
v_rcp_f32 v247, v206
v_smfmac_f32_16x16x64_bf16 v[184:187], v[236:239], a[64:71], v234
v_xor_b32 v243, 0x80000000, v243
v_smfmac_f32_16x16x64_bf16 v[184:187], v[236:239], a[72:79], v234
v_mul_f32 v247, s72, v247
v_smfmac_f32_16x16x64_bf16 v[188:191], v[236:239], a[80:87], v234
v_smfmac_f32_16x16x64_bf16 v[188:191], v[236:239], a[88:95], v234
MM v[68:71],a[40:43],a[224:227],v[68:71]
v_bfe_u32 v183, v248, 6, 2
v_lshlrev_b32 v183, 5, v183
MM v[68:71],a[44:47],a[228:231],v[68:71]
v_bfe_u32 v182, v248, 4, 2
v_lshl_add_u32 v183, v182, 2, v183
MM v[76:79],a[44:47],a[236:239],v[76:79]
v_add_u32 v183, s76, v183
v_add_u32 v183, 151552, v183
MM v[76:79],a[40:43],a[232:235],v[76:79]
buffer_load_dwordx4 v231, s[8:11], s77 offen offset:3072 lds
MM v[84:87],a[40:43],a[240:243],v[84:87]
s_waitcnt vmcnt(9)
RD a[160:163],v222 offset:32768
MM v[84:87],a[44:47],a[244:247],v[84:87]
RD a[164:167],v223 offset:32768
MM v[92:95],a[44:47],a[252:255],v[92:95]
RD a[168:171],v222 offset:34816
MM v[92:95],a[40:43],a[248:251],v[92:95]
v_add_f32 v184, v184, v185
v_add_f32 v188, v188, v189
MM v[96:99],a[48:51],a[224:227],v[96:99]
TR a[32:33],v199 offset:0
TR a[34:35],v199 offset:512
MM v[96:99],a[52:55],a[228:231],v[96:99]
RD a[172:175],v223 offset:34816
ds_write_b32 v183, v184 offset:0
ds_write_b32 v183, v188 offset:16
MM v[104:107],a[52:55],a[236:239],v[104:107]
TR a[36:37],v199 offset:8224
TR a[38:39],v199 offset:8736
MM v[104:107],a[48:51],a[232:235],v[104:107]
TR a[40:41],v199 offset:16448
TR a[42:43],v199 offset:16960
MM v[112:115],a[48:51],a[240:243],v[112:115]
TR a[44:45],v199 offset:24672
TR a[46:47],v199 offset:25184
RD a[176:179],v222 offset:36864
MM v[112:115],a[52:55],a[244:247],v[112:115]
RD a[180:183],v223 offset:36864
MM v[120:123],a[52:55],a[252:255],v[120:123]
RD a[184:187],v222 offset:38912
MM v[120:123],a[48:51],a[248:251],v[120:123]
RD a[188:191],v223 offset:38912
MM v[100:103],a[56:59],a[224:227],v[100:103]
s_add_u32 s77, s64, 0
s_add_u32 s81, s77, 896
s_add_u32 m0, s65, 65536
MM v[100:103],a[60:63],a[228:231],v[100:103]
buffer_load_dwordx4 v235, s[12:15], s81 offen offset:0 lds
MM v[108:111],a[60:63],a[236:239],v[108:111]
MM v[108:111],a[56:59],a[232:235],v[108:111]
MM v[116:119],a[56:59],a[240:243],v[116:119]
MM v[116:119],a[60:63],a[244:247],v[116:119]
MM v[124:127],a[60:63],a[252:255],v[124:127]
MM v[124:127],a[56:59],a[248:251],v[124:127]
buffer_load_dwordx4 v235, s[12:15], s77 offen offset:1024 lds
s_waitcnt lgkmcnt(0)
s_barrier
MM v[0:3],a[64:67],a[128:131],v[0:3]
s_memrealtime s[52:53]
TR a[48:49],v199 offset:8
TR a[50:51],v199 offset:520
MM v[0:3],a[68:71],a[132:135],v[0:3]
s_memtime s[54:55]
TR a[52:53],v199 offset:8232
TR a[54:55],v199 offset:8744
MM v[0:3],a[72:75],a[160:163],v[0:3]
TR a[56:57],v199 offset:16456
TR a[58:59],v199 offset:16968
MM v[0:3],a[76:79],a[164:167],v[0:3]
TR a[60:61],v199 offset:24680
TR a[62:63],v199 offset:25192
MM v[8:11],a[76:79],a[172:175],v[8:11]
v_xor_b32 v151, 0, v253
v_xor_b32 v143, 0, v253
MM v[8:11],a[72:75],a[168:171],v[8:11]
RD v[144:147],v151 offset:0
RD v[148:151],v151 offset:16
MM v[8:11],a[68:71],a[140:143],v[8:11]
RD v[136:139],v143 offset:0
RD v[140:143],v143 offset:16
MM v[8:11],a[64:67],a[136:139],v[8:11]
MM v[4:7],a[80:83],a[128:131],v[4:7]
MM v[4:7],a[84:87],a[132:135],v[4:7]
s_waitcnt lgkmcnt(2)
buffer_load_dwordx4 v235, s[12:15], s81 offen offset:2048 lds
MM v[4:7],a[88:91],a[160:163],v[4:7]
s_waitcnt lgkmcnt(0)
MM v[4:7],a[92:95],a[164:167],v[4:7]
MM v[12:15],a[92:95],a[172:175],v[12:15]
MM v[12:15],a[88:91],a[168:171],v[12:15]
MM v[12:15],a[84:87],a[140:143],v[12:15]
MM v[12:15],a[80:83],a[136:139],v[12:15]
MM v[32:35],a[96:99],a[128:131],v[32:35]
MM v[32:35],a[100:103],a[132:135],v[32:35]
MM v[32:35],a[104:107],a[160:163],v[32:35]
MM v[32:35],a[108:111],a[164:167],v[32:35]
buffer_load_dwordx4 v235, s[12:15], s77 offen offset:3072 lds
v_fmac_f32 v0, v240, v144
v_fmac_f32 v1, v240, v145
v_fmac_f32 v2, v240, v146
MM v[40:43],a[108:111],a[172:175],v[40:43]
s_add_u32 s77, s64, 32768
s_add_u32 m0, s65, 32768
v_fmac_f32 v3, v240, v147
v_fmac_f32 v4, v240, v148
MM v[40:43],a[104:107],a[168:171],v[40:43]
v_fmac_f32 v5, v240, v149
v_fmac_f32 v6, v240, v150
MM v[40:43],a[100:103],a[140:143],v[40:43]
v_fmac_f32 v7, v240, v151
v_mul_f32 v0, v244, v0
v_mul_f32 v1, v244, v1
MM v[40:43],a[96:99],a[136:139],v[40:43]
v_mul_f32 v2, v244, v2
v_mul_f32 v3, v244, v3
MM v[36:39],a[112:115],a[128:131],v[36:39]
v_mul_f32 v4, v244, v4
v_mul_f32 v5, v244, v5
MM v[36:39],a[116:119],a[132:135],v[36:39]
v_mul_f32 v6, v244, v6
v_mul_f32 v7, v244, v7
CV v160,v0,v1
MM v[36:39],a[120:123],a[160:163],v[36:39]
CV v161,v2,v3
CV v162,v4,v5
MM v[36:39],a[124:127],a[164:167],v[36:39]
CV v163,v6,v7
v_fmac_f32 v8, v241, v144
MM v[44:47],a[124:127],a[172:175],v[44:47]
v_fmac_f32 v9, v241, v145
v_fmac_f32 v10, v241, v146
v_fmac_f32 v11, v241, v147
MM v[44:47],a[120:123],a[168:171],v[44:47]
buffer_load_dwordx4 v228, s[8:11], s77 offen offset:0 lds
v_fmac_f32 v12, v241, v148
v_fmac_f32 v13, v241, v149
MM v[44:47],a[116:119],a[140:143],v[44:47]
v_fmac_f32 v14, v241, v150
v_fmac_f32 v15, v241, v151
v_mul_f32 v8, v245, v8
MM v[44:47],a[112:115],a[136:139],v[44:47]
v_mul_f32 v9, v245, v9
v_mul_f32 v10, v245, v10
MM v[64:67],a[0:3],a[128:131],v[64:67]
v_mul_f32 v11, v245, v11
v_mul_f32 v12, v245, v12
MM v[64:67],a[4:7],a[132:135],v[64:67]
v_mul_f32 v13, v245, v13
v_mul_f32 v14, v245, v14
v_mul_f32 v15, v245, v15
MM v[64:67],a[8:11],a[160:163],v[64:67]
CV v164,v8,v9
CV v165,v10,v11
MM v[64:67],a[12:15],a[164:167],v[64:67]
CV v166,v12,v13
CV v167,v14,v15
MM v[72:75],a[12:15],a[172:175],v[72:75]
v_xor_b32 v159, 128, v253
MM v[72:75],a[8:11],a[168:171],v[72:75]
RD v[152:155],v159 offset:0
RD v[156:159],v159 offset:16
MM v[72:75],a[4:7],a[140:143],v[72:75]
MM v[72:75],a[0:3],a[136:139],v[72:75]
buffer_load_dwordx4 v229, s[8:11], s77 offen offset:1024 lds
MM v[68:71],a[16:19],a[128:131],v[68:71]
MM v[68:71],a[20:23],a[132:135],v[68:71]
s_waitcnt lgkmcnt(0)
v_fmac_f32 v32, v240, v152
v_fmac_f32 v33, v240, v153
v_fmac_f32 v34, v240, v154
v_fmac_f32 v35, v240, v155
v_fmac_f32 v36, v240, v156
v_fmac_f32 v37, v240, v157
v_fmac_f32 v38, v240, v158
v_fmac_f32 v39, v240, v159
v_mul_f32 v179, -v250, v244
v_fma_f32 v32, v244, v32, v179
v_fma_f32 v33, v244, v33, v179
v_fma_f32 v34, v244, v34, v179
MM v[68:71],a[24:27],a[160:163],v[68:71]
v_fma_f32 v35, v244, v35, v179
v_fma_f32 v36, v244, v36, v179
MM v[68:71],a[28:31],a[164:167],v[68:71]
v_fma_f32 v37, v244, v37, v179
v_fma_f32 v38, v244, v38, v179
v_fma_f32 v39, v244, v39, v179
MM v[76:79],a[28:31],a[172:175],v[76:79]
CV v176,v32,v33
CV v177,v34,v35
MM v[76:79],a[24:27],a[168:171],v[76:79]
CV v178,v36,v37
CV v179,v38,v39
v_fmac_f32 v40, v241, v152
MM v[76:79],a[20:23],a[140:143],v[76:79]
v_fmac_f32 v41, v241, v153
v_fmac_f32 v42, v241, v154
v_fmac_f32 v43, v241, v155
MM v[76:79],a[16:19],a[136:139],v[76:79]
v_fmac_f32 v44, v241, v156
v_fmac_f32 v45, v241, v157
MM v[96:99],a[32:35],a[128:131],v[96:99]
v_fmac_f32 v46, v241, v158
v_fmac_f32 v47, v241, v159
MM v[96:99],a[36:39],a[132:135],v[96:99]
buffer_load_dwordx4 v230, s[8:11], s77 offen offset:2048 lds
v_mul_f32 v183, -v250, v245
v_fma_f32 v40, v245, v40, v183
v_fma_f32 v41, v245, v41, v183
MM v[96:99],a[40:43],a[160:163],v[96:99]
v_fma_f32 v42, v245, v42, v183
v_fma_f32 v43, v245, v43, v183
MM v[96:99],a[44:47],a[164:167],v[96:99]
v_fma_f32 v44, v245, v44, v183
v_fma_f32 v45, v245, v45, v183
MM v[104:107],a[44:47],a[172:175],v[104:107]
)ASM"
R"ASM(
v_fma_f32 v46, v245, v46, v183
v_fma_f32 v47, v245, v47, v183
CV v180,v40,v41
MM v[104:107],a[40:43],a[168:171],v[104:107]
CV v181,v42,v43
CV v182,v44,v45
MM v[104:107],a[36:39],a[140:143],v[104:107]
CV v183,v46,v47
v_xor_b32 v151, 256, v253
MM v[104:107],a[32:35],a[136:139],v[104:107]
RD v[144:147],v151 offset:0
RD v[148:151],v151 offset:16
MM v[100:103],a[48:51],a[128:131],v[100:103]
MM v[100:103],a[52:55],a[132:135],v[100:103]
MM v[100:103],a[56:59],a[160:163],v[100:103]
MM v[100:103],a[60:63],a[164:167],v[100:103]
s_waitcnt lgkmcnt(0)
buffer_load_dwordx4 v231, s[8:11], s77 offen offset:3072 lds
v_fmac_f32 v64, v240, v144
v_fmac_f32 v65, v240, v145
v_fmac_f32 v66, v240, v146
v_fmac_f32 v67, v240, v147
v_fmac_f32 v68, v240, v148
v_fmac_f32 v69, v240, v149
v_fmac_f32 v70, v240, v150
v_fmac_f32 v71, v240, v151
v_mul_f32 v195, -v250, v244
v_fma_f32 v64, v244, v64, v195
v_fma_f32 v65, v244, v65, v195
MM v[108:111],a[60:63],a[172:175],v[108:111]
s_add_u32 s77, s64, 16384
s_add_u32 s81, s77, 896
s_add_u32 m0, s65, 81920
v_fma_f32 v66, v244, v66, v195
v_fma_f32 v67, v244, v67, v195
MM v[108:111],a[56:59],a[168:171],v[108:111]
v_fma_f32 v68, v244, v68, v195
v_fma_f32 v69, v244, v69, v195
MM v[108:111],a[52:55],a[140:143],v[108:111]
v_fma_f32 v70, v244, v70, v195
v_fma_f32 v71, v244, v71, v195
CV v192,v64,v65
MM v[108:111],a[48:51],a[136:139],v[108:111]
CV v193,v66,v67
CV v194,v68,v69
MM v[16:19],a[64:67],a[144:147],v[16:19]
CV v195,v70,v71
v_fmac_f32 v72, v241, v144
v_fmac_f32 v73, v241, v145
MM v[16:19],a[68:71],a[148:151],v[16:19]
v_fmac_f32 v74, v241, v146
v_fmac_f32 v75, v241, v147
v_fmac_f32 v76, v241, v148
MM v[16:19],a[72:75],a[176:179],v[16:19]
v_fmac_f32 v77, v241, v149
v_fmac_f32 v78, v241, v150
MM v[16:19],a[76:79],a[180:183],v[16:19]
v_fmac_f32 v79, v241, v151
v_mul_f32 v199, -v250, v245
v_fma_f32 v72, v245, v72, v199
MM v[24:27],a[76:79],a[188:191],v[24:27]
v_fma_f32 v73, v245, v73, v199
v_fma_f32 v74, v245, v74, v199
MM v[24:27],a[72:75],a[184:187],v[24:27]
buffer_load_dwordx4 v235, s[12:15], s81 offen offset:0 lds
v_fma_f32 v75, v245, v75, v199
v_fma_f32 v76, v245, v76, v199
MM v[24:27],a[68:71],a[156:159],v[24:27]
s_waitcnt vmcnt(13)
s_barrier
RD a[192:195],v224 offset:0
v_fma_f32 v77, v245, v77, v199
v_fma_f32 v78, v245, v78, v199
v_fma_f32 v79, v245, v79, v199
MM v[24:27],a[64:67],a[152:155],v[24:27]
RD a[196:199],v225 offset:0
RD a[200:203],v226 offset:0
CV v196,v72,v73
CV v197,v74,v75
MM v[20:23],a[80:83],a[144:147],v[20:23]
RD a[204:207],v227 offset:0
RD a[208:211],v224 offset:4096
CV v198,v76,v77
CV v199,v78,v79
MM v[20:23],a[84:87],a[148:151],v[20:23]
RD a[212:215],v225 offset:4096
RD a[216:219],v226 offset:4096
v_xor_b32 v159, 384, v253
MM v[20:23],a[88:91],a[176:179],v[20:23]
RD a[220:223],v227 offset:4096
RD a[224:227],v224 offset:8192
RD v[152:155],v159 offset:0
RD v[156:159],v159 offset:16
MM v[20:23],a[92:95],a[180:183],v[20:23]
RD a[228:231],v225 offset:8192
RD a[232:235],v226 offset:8192
MM v[28:31],a[92:95],a[188:191],v[28:31]
RD a[236:239],v227 offset:8192
RD a[240:243],v224 offset:12288
MM v[28:31],a[88:91],a[184:187],v[28:31]
RD a[244:247],v225 offset:12288
RD a[248:251],v226 offset:12288
MM v[28:31],a[84:87],a[156:159],v[28:31]
s_waitcnt lgkmcnt(6)
RD a[252:255],v227 offset:12288
v_fmac_f32 v96, v240, v152
v_fmac_f32 v97, v240, v153
v_fmac_f32 v98, v240, v154
v_fmac_f32 v99, v240, v155
v_fmac_f32 v100, v240, v156
v_fmac_f32 v101, v240, v157
v_fmac_f32 v102, v240, v158
v_fmac_f32 v103, v240, v159
v_mul_f32 v211, -v250, v244
v_fma_f32 v96, v244, v96, v211
v_fma_f32 v97, v244, v97, v211
v_fma_f32 v98, v244, v98, v211
MM v[28:31],a[80:83],a[152:155],v[28:31]
buffer_load_dwordx4 v235, s[12:15], s77 offen offset:1024 lds
v_fma_f32 v99, v244, v99, v211
v_fma_f32 v100, v244, v100, v211
MM v[48:51],a[96:99],a[144:147],v[48:51]
v_fma_f32 v101, v244, v101, v211
v_fma_f32 v102, v244, v102, v211
v_fma_f32 v103, v244, v103, v211
MM v[48:51],a[100:103],a[148:151],v[48:51]
CV v208,v96,v97
CV v209,v98,v99
MM v[48:51],a[104:107],a[176:179],v[48:51]
CV v210,v100,v101
CV v211,v102,v103
v_fmac_f32 v104, v241, v152
MM v[48:51],a[108:111],a[180:183],v[48:51]
v_fmac_f32 v105, v241, v153
v_fmac_f32 v106, v241, v154
v_fmac_f32 v107, v241, v155
MM v[56:59],a[108:111],a[188:191],v[56:59]
v_fmac_f32 v108, v241, v156
v_fmac_f32 v109, v241, v157
MM v[56:59],a[104:107],a[184:187],v[56:59]
v_fmac_f32 v110, v241, v158
v_fmac_f32 v111, v241, v159
MM v[56:59],a[100:103],a[156:159],v[56:59]
v_mul_f32 v215, -v250, v245
v_fma_f32 v104, v245, v104, v215
v_fma_f32 v105, v245, v105, v215
MM v[56:59],a[96:99],a[152:155],v[56:59]
v_fma_f32 v106, v245, v106, v215
v_fma_f32 v107, v245, v107, v215
MM v[52:55],a[112:115],a[144:147],v[52:55]
v_fma_f32 v108, v245, v108, v215
v_fma_f32 v109, v245, v109, v215
MM v[52:55],a[116:119],a[148:151],v[52:55]
buffer_load_dwordx4 v235, s[12:15], s81 offen offset:2048 lds
v_fma_f32 v110, v245, v110, v215
v_fma_f32 v111, v245, v111, v215
CV v212,v104,v105
MM v[52:55],a[120:123],a[176:179],v[52:55]
CV v213,v106,v107
CV v214,v108,v109
CV v215,v110,v111
MM v[52:55],a[124:127],a[180:183],v[52:55]
v_mov_b32 v241, 0
v_mov_b32 v244, 0
s_waitcnt lgkmcnt(0)
MM v[0:3],a[192:195],v[160:163],0
v_mov_b32 v144, 0
v_mov_b32 v145, 0
MM v[0:3],a[196:199],v[176:179],v[0:3]
v_mov_b32 v245, 0
v_mov_b32 v148, 0
MM v[0:3],a[200:203],v[192:195],v[0:3]
v_mov_b32 v149, 0
v_fmac_f32 v16, v242, v136
MM v[0:3],a[204:207],v[208:211],v[0:3]
v_fmac_f32 v17, v242, v137
v_fmac_f32 v18, v242, v138
MM v[8:11],a[192:195],v[164:167],0
v_fmac_f32 v19, v242, v139
v_fmac_f32 v20, v242, v140
MM v[8:11],a[196:199],v[180:183],v[8:11]
v_fmac_f32 v21, v242, v141
v_fmac_f32 v22, v242, v142
MM v[8:11],a[200:203],v[196:199],v[8:11]
v_fmac_f32 v23, v242, v143
v_mul_f32 v16, v246, v16
MM v[8:11],a[204:207],v[212:215],v[8:11]
buffer_load_dwordx4 v235, s[12:15], s77 offen offset:3072 lds
MM v[4:7],a[208:211],v[160:163],0
s_add_u32 s77, s64, 32768
s_add_u32 s81, s77, 896
s_add_u32 m0, s65, 98304
s_nop 0
LPG4_%=:
s_cbranch_execz LPG8_%=
MM v[4:7],a[212:215],v[176:179],v[4:7]
v_max3_f32 v146, v0, v1, v2
v_max_f32 v146, v146, v3
MM v[4:7],a[216:219],v[192:195],v[4:7]
v_mov_b32 v147, v146
v_mul_f32 v17, v246, v17
MM v[4:7],a[220:223],v[208:211],v[4:7]
v_permlane16_swap_b32 v146, v147
v_max_f32 v146, v146, v147
MM v[12:15],a[208:211],v[164:167],0
s_waitcnt vmcnt(8)
s_barrier
RD a[192:195],v224 offset:16384
MM v[12:15],a[212:215],v[180:183],v[12:15]
RD a[196:199],v225 offset:16384
RD a[200:203],v226 offset:16384
MM v[12:15],a[216:219],v[196:199],v[12:15]
RD a[204:207],v227 offset:16384
v_mov_b32 v147, v146
MM v[12:15],a[220:223],v[212:215],v[12:15]
s_nop 0
v_permlane32_swap_b32 v146, v147
v_max_f32 v146, v146, v147
v_sub_f32 v128, 0xc1600000, v146
v_sub_f32 v129, 0xc1600000, v146
MM v[32:35],a[224:227],v[160:163],0
RD a[208:211],v224 offset:20480
RD a[212:215],v225 offset:20480
v_sub_f32 v130, 0xc1600000, v146
v_sub_f32 v131, 0xc1600000, v146
v_max3_f32 v150, v8, v9, v10
MM v[32:35],a[228:231],v[176:179],v[32:35]
buffer_load_dwordx4 v235, s[12:15], s81 offen offset:0 lds
v_max_f32 v150, v150, v11
v_mov_b32 v151, v150
MM v[32:35],a[232:235],v[192:195],v[32:35]
RD a[216:219],v226 offset:20480
RD a[220:223],v227 offset:20480
v_permlane16_swap_b32 v150, v151
v_max_f32 v150, v150, v151
v_mov_b32 v151, v150
MM v[32:35],a[236:239],v[208:211],v[32:35]
s_nop 0
v_permlane32_swap_b32 v150, v151
v_max_f32 v150, v150, v151
v_add_f32 v0, v0, v128
v_sub_f32 v132, 0xc1600000, v150
MM v[40:43],a[224:227],v[164:167],0
v_sub_f32 v133, 0xc1600000, v150
v_sub_f32 v134, 0xc1600000, v150
v_sub_f32 v135, 0xc1600000, v150
v_add_f32 v1, v1, v128
EX v0,v0
MM v[40:43],a[228:231],v[180:183],v[40:43]
EX v1,v1
MM v[40:43],a[232:235],v[196:199],v[40:43]
v_add_f32 v2, v2, v128
v_add_f32 v3, v3, v128
MM v[40:43],a[236:239],v[212:215],v[40:43]
EX v2,v2
MM v[36:39],a[240:243],v[160:163],v[128:131]
RD a[224:227],v224 offset:24576
RD a[228:231],v225 offset:24576
EX v3,v3
MM v[36:39],a[244:247],v[176:179],v[36:39]
RD a[232:235],v226 offset:24576
RD a[236:239],v227 offset:24576
CV v0,v0,v1
v_add_f32 v4, v4, v128
MM v[36:39],a[248:251],v[192:195],v[36:39]
v_add_f32 v5, v5, v128
v_mul_f32 v18, v246, v18
EX v4,v4
MM v[36:39],a[252:255],v[208:211],v[36:39]
buffer_load_dwordx4 v235, s[12:15], s77 offen offset:1024 lds
EX v5,v5
MM v[44:47],a[240:243],v[164:167],v[132:135]
CV v1,v2,v3
v_add_f32 v6, v6, v128
MM v[44:47],a[244:247],v[180:183],v[44:47]
v_add_f32 v7, v7, v128
v_mul_f32 v19, v246, v19
MM v[44:47],a[248:251],v[196:199],v[44:47]
EX v6,v6
EX v7,v7
MM v[44:47],a[252:255],v[212:215],v[44:47]
CV v2,v4,v5
v_add_f32 v8, v8, v132
MM v[60:63],a[124:127],a[188:191],v[60:63]
RD a[240:243],v224 offset:28672
RD a[244:247],v225 offset:28672
v_add_f32 v9, v9, v132
MM v[60:63],a[120:123],a[184:187],v[60:63]
RD a[248:251],v226 offset:28672
RD a[252:255],v227 offset:28672
EX v8,v8
MM v[60:63],a[116:119],a[156:159],v[60:63]
EX v9,v9
MM v[60:63],a[112:115],a[152:155],v[60:63]
CV v3,v6,v7
v_add_f32 v10, v10, v132
MM v[80:83],a[0:3],a[144:147],v[80:83]
v_add_f32 v11, v11, v132
v_mul_f32 v20, v246, v20
EX v10,v10
MM v[80:83],a[4:7],a[148:151],v[80:83]
buffer_load_dwordx4 v235, s[12:15], s81 offen offset:2048 lds
EX v11,v11
MM v[80:83],a[8:11],a[176:179],v[80:83]
CV v8,v8,v9
v_add_f32 v12, v12, v132
MM v[80:83],a[12:15],a[180:183],v[80:83]
v_add_f32 v13, v13, v132
v_mul_f32 v21, v246, v21
EX v12,v12
MM v[88:91],a[12:15],a[188:191],v[88:91]
EX v13,v13
MM v[88:91],a[8:11],a[184:187],v[88:91]
CV v9,v10,v11
v_add_f32 v14, v14, v132
MM v[88:91],a[4:7],a[156:159],v[88:91]
v_add_f32 v15, v15, v132
v_mul_f32 v22, v246, v22
EX v14,v14
MM v[88:91],a[0:3],a[152:155],v[88:91]
EX v15,v15
MM v[84:87],a[16:19],a[144:147],v[84:87]
CV v10,v12,v13
v_add_f32 v32, v32, v128
MM v[84:87],a[20:23],a[148:151],v[84:87]
v_add_f32 v33, v33, v128
v_mul_f32 v23, v246, v23
MM v[84:87],a[24:27],a[176:179],v[84:87]
EX v32,v32
MM v[84:87],a[28:31],a[180:183],v[84:87]
buffer_load_dwordx4 v235, s[12:15], s77 offen offset:3072 lds
EX v33,v33
MM v[92:95],a[28:31],a[188:191],v[92:95]
s_add_u32 s77, s64, 49152
s_add_u32 m0, s65, 49152
CV v11,v14,v15
v_add_f32 v34, v34, v128
v_add_f32 v35, v35, v128
MM v[92:95],a[24:27],a[184:187],v[92:95]
buffer_load_dwordx4 v228, s[8:11], s77 offen offset:0 lds
EX v34,v34
MM v[92:95],a[20:23],a[156:159],v[92:95]
EX v35,v35
MM v[92:95],a[16:19],a[152:155],v[92:95]
CV v4,v32,v33
CV v168,v16,v17
EX v36,v36
MM v[112:115],a[32:35],a[144:147],v[112:115]
EX v37,v37
MM v[112:115],a[36:39],a[148:151],v[112:115]
buffer_load_dwordx4 v229, s[8:11], s77 offen offset:1024 lds
CV v5,v34,v35
MM v[112:115],a[40:43],a[176:179],v[112:115]
EX v38,v38
EX v39,v39
MM v[112:115],a[44:47],a[180:183],v[112:115]
CV v6,v36,v37
v_add_f32 v40, v40, v132
MM v[120:123],a[44:47],a[188:191],v[120:123]
v_add_f32 v41, v41, v132
CV v169,v18,v19
MM v[120:123],a[40:43],a[184:187],v[120:123]
buffer_load_dwordx4 v230, s[8:11], s77 offen offset:2048 lds
EX v40,v40
MM v[120:123],a[36:39],a[156:159],v[120:123]
EX v41,v41
MM v[120:123],a[32:35],a[152:155],v[120:123]
CV v7,v38,v39
v_add_f32 v42, v42, v132
MM v[116:119],a[48:51],a[144:147],v[116:119]
v_add_f32 v43, v43, v132
CV v170,v20,v21
EX v42,v42
MM v[116:119],a[52:55],a[148:151],v[116:119]
buffer_load_dwordx4 v231, s[8:11], s77 offen offset:3072 lds
EX v43,v43
MM v[116:119],a[56:59],a[176:179],v[116:119]
s_add_u32 s77, s64, 49152
s_add_u32 s81, s77, 896
s_add_u32 m0, s65, 114688
CV v12,v40,v41
EX v44,v44
MM v[116:119],a[60:63],a[180:183],v[116:119]
EX v45,v45
MM v[124:127],a[60:63],a[188:191],v[124:127]
CV v13,v42,v43
CV v171,v22,v23
MM v[124:127],a[56:59],a[184:187],v[124:127]
buffer_load_dwordx4 v235, s[12:15], s81 offen offset:0 lds
EX v46,v46
MM v[124:127],a[52:55],a[156:159],v[124:127]
EX v47,v47
CV v14,v44,v45
CV v15,v46,v47
MM v[124:127],a[48:51],a[152:155],v[124:127]
v_fmac_f32 v24, v243, v136
v_fmac_f32 v25, v243, v137
v_smfmac_f32_16x16x64_bf16 v[144:147], v[236:239], v[0:7], v234
v_fmac_f32 v26, v243, v138
v_smfmac_f32_16x16x64_bf16 v[148:151], v[236:239], v[8:15], v234
)ASM"
R"ASM(
s_waitcnt lgkmcnt(0)
v_fmac_f32 v27, v243, v139
buffer_load_dwordx4 v235, s[12:15], s77 offen offset:1024 lds
buffer_load_dwordx4 v235, s[12:15], s81 offen offset:2048 lds
buffer_load_dwordx4 v235, s[12:15], s77 offen offset:3072 lds
s_add_u32 s64, s64, 49152
MM v[64:67],a[192:195],v[160:163],v[128:131]
s_add_u32 s64, s64, 16384
TR a[128:129],v232 offset:0
v_fmac_f32 v28, v243, v140
MM v[64:67],a[196:199],v[176:179],v[64:67]
s_add_u32 s81, s64, 896
TR a[130:131],v232 offset:4096
v_fmac_f32 v29, v243, v141
MM v[64:67],a[200:203],v[192:195],v[64:67]
TR a[132:133],v232 offset:8192
v_fmac_f32 v30, v243, v142
MM v[64:67],a[204:207],v[208:211],v[64:67]
v_max3_f32 v249, v144, v145, v148
v_max_f32 v249, v249, v149
MM v[68:71],a[220:223],v[208:211],v[128:131]
TR a[134:135],v232 offset:12288
v_cmp_lt_f32 vcc, s71, v249
MM v[68:71],a[216:219],v[192:195],v[68:71]
s_cbranch_vccnz LSITE0_%=
LBACK0_%=:
v_add_f32 v244, v144, v145
v_fmac_f32 v31, v243, v143
MM v[68:71],a[212:215],v[176:179],v[68:71]
TR a[136:137],v233 offset:0
v_add_f32 v245, v148, v149
v_mul_f32 v24, v247, v24
MM v[68:71],a[208:211],v[160:163],v[68:71]
TR a[138:139],v233 offset:4096
v_mul_f32 v25, v247, v25
MM v[76:79],a[208:211],v[164:167],v[132:135]
TR a[140:141],v233 offset:8192
TR a[142:143],v233 offset:12288
v_mul_f32 v26, v247, v26
MM v[76:79],a[212:215],v[180:183],v[76:79]
TR a[144:145],v232 offset:512
v_mul_f32 v27, v247, v27
MM v[76:79],a[216:219],v[196:199],v[76:79]
s_waitcnt lgkmcnt(7)
TR a[146:147],v232 offset:4608
v_mul_f32 v28, v247, v28
MM v[76:79],a[220:223],v[212:215],v[76:79]
TR a[148:149],v232 offset:8704
v_mul_f32 v29, v247, v29
MM v[32:35],a[128:131],v[0:3],0
MM v[36:39],a[128:131],v[8:11],0
MM v[72:75],a[204:207],v[212:215],v[132:135]
TR a[150:151],v232 offset:12800
EX v64,v64
v_mul_f32 v30, v247, v30
MM v[72:75],a[200:203],v[196:199],v[72:75]
TR a[152:153],v233 offset:512
TR a[154:155],v233 offset:4608
v_mul_f32 v31, v247, v31
EX v65,v65
CV v172,v24,v25
MM v[72:75],a[196:199],v[180:183],v[72:75]
TR a[156:157],v233 offset:8704
CV v173,v26,v27
MM v[72:75],a[192:195],v[164:167],v[72:75]
TR a[158:159],v233 offset:12800
EX v66,v66
CV v174,v28,v29
MM v[96:99],a[224:227],v[160:163],v[128:131]
TR a[160:161],v232 offset:1024
v_max3_f32 v32, |v32|, |v33|, |v34|
v_max_f32 v32, v32, |v35|
CV v175,v30,v31
EX v67,v67
MM v[96:99],a[228:231],v[176:179],v[96:99]
TR a[162:163],v232 offset:5120
TR a[164:165],v232 offset:9216
v_bfe_u32 v32, v32, 23, 8
v_add_u32 v32, 8, v32
v_xor_b32 v143, 128, v253
MM v[96:99],a[232:235],v[192:195],v[96:99]
RD v[136:139],v143 offset:0
TR a[166:167],v232 offset:13312
v_min_u32 v32, 240, v32
v_lshl_or_b32 v241, v32, 0, v241
EX v68,v68
MM v[96:99],a[236:239],v[208:211],v[96:99]
RD v[140:143],v143 offset:16
TR a[168:169],v233 offset:1024
v_lshl_or_b32 v32, v32, 1, 1
v_lshlrev_b32 v32, 22, v32
MM v[100:103],a[252:255],v[208:211],v[128:131]
TR a[170:171],v233 offset:5120
AW a100,v32
AW a101,v32
EX v69,v69
MM v[100:103],a[248:251],v[192:195],v[100:103]
TR a[172:173],v233 offset:9216
TR a[174:175],v233 offset:13312
AW a102,v32
AW a103,v32
MM v[100:103],a[244:247],v[176:179],v[100:103]
TR a[176:177],v232 offset:1536
v_max3_f32 v36, |v36|, |v37|, |v38|
v_max_f32 v36, v36, |v39|
EX v70,v70
MM v[100:103],a[240:243],v[160:163],v[100:103]
s_waitcnt lgkmcnt(5)
TR a[178:179],v232 offset:5632
v_bfe_u32 v36, v36, 23, 8
v_add_u32 v36, 8, v36
v_fmac_f32 v48, v242, v136
v_fmac_f32 v49, v242, v137
v_fmac_f32 v50, v242, v138
v_fmac_f32 v51, v242, v139
v_fmac_f32 v52, v242, v140
EX v71,v71
v_fmac_f32 v53, v242, v141
MM v[108:111],a[240:243],v[164:167],v[132:135]
TR a[180:181],v232 offset:9728
v_min_u32 v36, 240, v36
v_lshl_or_b32 v241, v36, 8, v241
CV v64,v64,v65
v_fmac_f32 v54, v242, v142
MM v[108:111],a[244:247],v[180:183],v[108:111]
TR a[182:183],v232 offset:13824
v_lshl_or_b32 v36, v36, 1, 1
v_lshlrev_b32 v36, 22, v36
EX v72,v72
v_fmac_f32 v55, v242, v143
MM v[108:111],a[248:251],v[196:199],v[108:111]
TR a[184:185],v233 offset:1536
TR a[186:187],v233 offset:5632
TR a[188:189],v233 offset:9728
TR a[190:191],v233 offset:13824
AW a108,v36
AW a109,v36
v_mul_f32 v187, -v252, v246
EX v73,v73
MM v[108:111],a[252:255],v[212:215],v[108:111]
AW a110,v36
AW a111,v36
v_fma_f32 v48, v246, v48, v187
CV v65,v66,v67
MM v[104:107],a[236:239],v[212:215],v[132:135]
v_fma_f32 v49, v246, v49, v187
v_fma_f32 v50, v246, v50, v187
EX v74,v74
MM v[104:107],a[232:235],v[196:199],v[104:107]
v_fma_f32 v51, v246, v51, v187
CV v66,v68,v69
MM v[104:107],a[228:231],v[180:183],v[104:107]
EX v75,v75
v_fma_f32 v52, v246, v52, v187
MM v[104:107],a[224:227],v[164:167],v[104:107]
s_waitcnt vmcnt(12) lgkmcnt(0)
s_barrier
v_fma_f32 v53, v246, v53, v187
EX v76,v76
MM a[0:3],a[128:131],v[0:3],a[100:103]
s_add_u32 m0, s65, 0
v_fma_f32 v54, v246, v54, v187
CV v67,v70,v71
MM a[0:3],a[132:135],v[4:7],a[0:3]
EX v77,v77
v_fma_f32 v55, v246, v55, v187
MM a[8:11],a[132:135],v[12:15],a[108:111]
CV v184,v48,v49
CV v185,v50,v51
MM a[8:11],a[128:131],v[8:11],a[8:11]
EX v78,v78
MM a[12:15],a[136:139],v[8:11],a[108:111]
CV v186,v52,v53
CV v72,v72,v73
EX v79,v79
MM a[12:15],a[140:143],v[12:15],a[12:15]
CV v187,v54,v55
v_fmac_f32 v56, v243, v136
v_fmac_f32 v57, v243, v137
MM a[4:7],a[140:143],v[4:7],a[100:103]
EX v96,v96
v_fmac_f32 v58, v243, v138
MM a[4:7],a[136:139],v[0:3],a[4:7]
v_fmac_f32 v59, v243, v139
CV v73,v74,v75
MM a[32:35],a[144:147],v[0:3],a[100:103]
RD a[192:195],v224 offset:32768
EX v97,v97
v_fmac_f32 v60, v243, v140
MM a[32:35],a[148:151],v[4:7],a[32:35]
RD a[196:199],v225 offset:32768
buffer_load_dwordx4 v228, s[8:11], s64 offen offset:0 lds
v_fmac_f32 v61, v243, v141
EX v98,v98
MM a[40:43],a[148:151],v[12:15],a[108:111]
v_fmac_f32 v62, v243, v142
CV v74,v76,v77
MM a[40:43],a[144:147],v[8:11],a[40:43]
RD a[200:203],v226 offset:32768
v_fmac_f32 v63, v243, v143
EX v99,v99
MM a[44:47],a[152:155],v[8:11],a[108:111]
v_mul_f32 v191, -v252, v247
v_fma_f32 v56, v247, v56, v191
MM a[44:47],a[156:159],v[12:15],a[44:47]
buffer_load_dwordx4 v229, s[8:11], s64 offen offset:1024 lds
RD a[204:207],v227 offset:32768
EX v100,v100
v_fma_f32 v57, v247, v57, v191
MM a[36:39],a[156:159],v[4:7],a[100:103]
v_fma_f32 v58, v247, v58, v191
CV v75,v78,v79
EX v101,v101
MM a[36:39],a[152:155],v[0:3],a[36:39]
RD a[208:211],v224 offset:36864
v_fma_f32 v59, v247, v59, v191
MM a[64:67],a[160:163],v[0:3],a[100:103]
RD a[212:215],v225 offset:36864
v_fma_f32 v60, v247, v60, v191
EX v102,v102
MM a[64:67],a[164:167],v[4:7],a[64:67]
buffer_load_dwordx4 v230, s[8:11], s64 offen offset:2048 lds
v_fma_f32 v61, v247, v61, v191
CV v68,v96,v97
MM a[72:75],a[164:167],v[12:15],a[108:111]
EX v103,v103
v_fma_f32 v62, v247, v62, v191
MM a[72:75],a[160:163],v[8:11],a[72:75]
RD a[216:219],v226 offset:36864
v_fma_f32 v63, v247, v63, v191
EX v104,v104
CV v188,v56,v57
MM a[76:79],a[168:171],v[8:11],a[108:111]
RD a[220:223],v227 offset:36864
buffer_load_dwordx4 v231, s[8:11], s64 offen offset:3072 lds
CV v189,v58,v59
MM a[76:79],a[172:175],v[12:15],a[76:79]
s_add_u32 m0, s65, 65536
EX v105,v105
CV v190,v60,v61
CV v69,v98,v99
MM a[68:71],a[172:175],v[4:7],a[100:103]
RD a[224:227],v224 offset:40960
CV v191,v62,v63
MM a[68:71],a[168:171],v[0:3],a[68:71]
EX v106,v106
v_xor_b32 v143, 256, v253
MM a[96:99],a[176:179],v[0:3],a[100:103]
RD v[136:139],v143 offset:0
RD a[228:231],v225 offset:40960
buffer_load_dwordx4 v235, s[12:15], s81 offen offset:0 lds
EX v107,v107
CV v70,v100,v101
MM a[96:99],a[180:183],v[4:7],a[96:99]
RD v[140:143],v143 offset:16
RD a[232:235],v226 offset:40960
MM a[104:107],a[180:183],v[12:15],a[108:111]
EX v108,v108
MM a[104:107],a[176:179],v[8:11],a[104:107]
RD a[236:239],v227 offset:40960
CV v71,v102,v103
MM a[108:111],a[184:187],v[8:11],a[108:111]
buffer_load_dwordx4 v235, s[12:15], s64 offen offset:1024 lds
EX v109,v109
MM a[108:111],a[188:191],v[12:15],a[108:111]
s_waitcnt lgkmcnt(2)
v_fmac_f32 v80, v242, v136
v_fmac_f32 v81, v242, v137
v_fmac_f32 v82, v242, v138
v_fmac_f32 v83, v242, v139
v_fmac_f32 v84, v242, v140
EX v110,v110
v_fmac_f32 v85, v242, v141
MM a[100:103],a[188:191],v[4:7],a[100:103]
RD a[240:243],v224 offset:45056
RD a[244:247],v225 offset:45056
RD a[248:251],v226 offset:45056
RD a[252:255],v227 offset:45056
EX v111,v111
v_fmac_f32 v86, v242, v142
MM a[100:103],a[184:187],v[0:3],a[100:103]
v_fmac_f32 v87, v242, v143
CV v76,v104,v105
v_smfmac_f32_16x16x64_bf16 v[144:147], v[236:239], v[64:71], v234
buffer_load_dwordx4 v235, s[12:15], s81 offen offset:2048 lds
CV v77,v106,v107
CV v78,v108,v109
CV v79,v110,v111
v_mul_f32 v203, -v252, v246
s_nop 0
v_smfmac_f32_16x16x64_bf16 v[148:151], v[236:239], v[72:79], v234
s_waitcnt lgkmcnt(0)
v_fma_f32 v80, v246, v80, v203
buffer_load_dwordx4 v235, s[12:15], s64 offen offset:3072 lds
MM v[0:3],a[192:195],v[160:163],v[128:131]
s_add_u32 s64, s64, 16384
v_fma_f32 v81, v246, v81, v203
v_fma_f32 v82, v246, v82, v203
MM v[0:3],a[196:199],v[176:179],v[0:3]
s_add_u32 s81, s64, 896
v_fma_f32 v83, v246, v83, v203
v_fma_f32 v84, v246, v84, v203
MM v[0:3],a[200:203],v[192:195],v[0:3]
TR a[128:129],v232 offset:16384
v_fma_f32 v85, v246, v85, v203
MM v[0:3],a[204:207],v[208:211],v[0:3]
v_max3_f32 v249, v144, v145, v148
v_max_f32 v249, v249, v149
MM v[4:7],a[220:223],v[208:211],v[128:131]
TR a[130:131],v232 offset:20480
TR a[132:133],v232 offset:24576
v_cmp_lt_f32 vcc, s71, v249
v_fma_f32 v86, v246, v86, v203
MM v[4:7],a[216:219],v[192:195],v[4:7]
s_cbranch_vccnz LSITE1_%=
LBACK1_%=:
TR a[134:135],v232 offset:28672
v_add_f32 v244, v144, v145
v_fma_f32 v87, v246, v87, v203
MM v[4:7],a[212:215],v[176:179],v[4:7]
TR a[136:137],v233 offset:16384
v_add_f32 v245, v148, v149
CV v200,v80,v81
MM v[4:7],a[208:211],v[160:163],v[4:7]
TR a[138:139],v233 offset:20480
CV v201,v82,v83
MM v[12:15],a[208:211],v[164:167],v[132:135]
TR a[140:141],v233 offset:24576
TR a[142:143],v233 offset:28672
CV v202,v84,v85
MM v[12:15],a[212:215],v[180:183],v[12:15]
TR a[144:145],v232 offset:16896
CV v203,v86,v87
v_fmac_f32 v88, v243, v136
v_fmac_f32 v89, v243, v137
MM v[12:15],a[216:219],v[196:199],v[12:15]
TR a[146:147],v232 offset:20992
v_fmac_f32 v90, v243, v138
MM v[12:15],a[220:223],v[212:215],v[12:15]
TR a[148:149],v232 offset:25088
v_fmac_f32 v91, v243, v139
MM v[8:11],a[204:207],v[212:215],v[132:135]
TR a[150:151],v232 offset:29184
EX v0,v0
v_fmac_f32 v92, v243, v140
MM v[8:11],a[200:203],v[196:199],v[8:11]
TR a[152:153],v233 offset:16896
TR a[154:155],v233 offset:20992
v_fmac_f32 v93, v243, v141
EX v1,v1
MM v[8:11],a[196:199],v[180:183],v[8:11]
TR a[156:157],v233 offset:25088
v_fmac_f32 v94, v243, v142
MM v[8:11],a[192:195],v[164:167],v[8:11]
TR a[158:159],v233 offset:29184
v_fmac_f32 v95, v243, v143
EX v2,v2
v_mul_f32 v207, -v252, v247
MM v[32:35],a[224:227],v[160:163],v[128:131]
TR a[160:161],v232 offset:17408
v_fma_f32 v88, v247, v88, v207
EX v3,v3
MM v[32:35],a[228:231],v[176:179],v[32:35]
TR a[162:163],v232 offset:21504
TR a[164:165],v232 offset:25600
v_fma_f32 v89, v247, v89, v207
MM v[32:35],a[232:235],v[192:195],v[32:35]
TR a[166:167],v232 offset:29696
v_fma_f32 v90, v247, v90, v207
EX v4,v4
MM v[32:35],a[236:239],v[208:211],v[32:35]
TR a[168:169],v233 offset:17408
v_fma_f32 v91, v247, v91, v207
MM v[36:39],a[252:255],v[208:211],v[128:131]
TR a[170:171],v233 offset:21504
EX v5,v5
v_fma_f32 v92, v247, v92, v207
MM v[36:39],a[248:251],v[192:195],v[36:39]
TR a[172:173],v233 offset:25600
TR a[174:175],v233 offset:29696
v_fma_f32 v93, v247, v93, v207
MM v[36:39],a[244:247],v[176:179],v[36:39]
TR a[176:177],v232 offset:17920
EX v6,v6
v_fma_f32 v94, v247, v94, v207
v_fma_f32 v95, v247, v95, v207
MM v[36:39],a[240:243],v[160:163],v[36:39]
TR a[178:179],v232 offset:22016
CV v204,v88,v89
EX v7,v7
MM v[44:47],a[240:243],v[164:167],v[132:135]
TR a[180:181],v232 offset:26112
CV v205,v90,v91
CV v0,v0,v1
MM v[44:47],a[244:247],v[180:183],v[44:47]
TR a[182:183],v232 offset:30208
CV v206,v92,v93
EX v8,v8
MM v[44:47],a[248:251],v[196:199],v[44:47]
TR a[184:185],v233 offset:17920
TR a[186:187],v233 offset:22016
TR a[188:189],v233 offset:26112
TR a[190:191],v233 offset:30208
CV v207,v94,v95
)ASM"
R"ASM(
EX v9,v9
s_nop 0
LPG5_%=:
s_cbranch_execz LPG9_%=
MM v[44:47],a[252:255],v[212:215],v[44:47]
v_xor_b32 v143, 384, v253
CV v1,v2,v3
MM v[40:43],a[236:239],v[212:215],v[132:135]
RD v[136:139],v143 offset:0
RD v[140:143],v143 offset:16
EX v10,v10
MM v[40:43],a[232:235],v[196:199],v[40:43]
EX v11,v11
MM v[40:43],a[228:231],v[180:183],v[40:43]
EX v12,v12
CV v2,v4,v5
MM v[40:43],a[224:227],v[164:167],v[40:43]
s_waitcnt vmcnt(12) lgkmcnt(0)
s_barrier
CV v3,v6,v7
MM a[0:3],a[128:131],v[64:67],a[0:3]
v_fmac_f32 v112, v242, v136
v_fmac_f32 v113, v242, v137
v_fmac_f32 v114, v242, v138
v_fmac_f32 v115, v242, v139
v_fmac_f32 v116, v242, v140
MM a[0:3],a[132:135],v[68:71],a[0:3]
s_add_u32 m0, s65, 16384
EX v13,v13
v_fmac_f32 v117, v242, v141
MM a[8:11],a[132:135],v[76:79],a[8:11]
v_fmac_f32 v118, v242, v142
v_fmac_f32 v119, v242, v143
MM a[8:11],a[128:131],v[72:75],a[8:11]
EX v14,v14
MM a[12:15],a[136:139],v[72:75],a[12:15]
v_mul_f32 v219, -v252, v246
CV v8,v8,v9
EX v15,v15
MM a[12:15],a[140:143],v[76:79],a[12:15]
v_fma_f32 v112, v246, v112, v219
v_fma_f32 v113, v246, v113, v219
MM a[4:7],a[140:143],v[68:71],a[4:7]
EX v32,v32
MM a[4:7],a[136:139],v[64:67],a[4:7]
v_fma_f32 v114, v246, v114, v219
v_fma_f32 v115, v246, v115, v219
MM a[32:35],a[144:147],v[64:67],a[32:35]
RD a[192:195],v224 offset:49152
EX v33,v33
CV v9,v10,v11
v_fma_f32 v116, v246, v116, v219
MM a[32:35],a[148:151],v[68:71],a[32:35]
buffer_load_dwordx4 v228, s[8:11], s64 offen offset:0 lds
v_fma_f32 v117, v246, v117, v219
EX v34,v34
MM a[40:43],a[148:151],v[76:79],a[40:43]
RD a[196:199],v225 offset:49152
v_fma_f32 v118, v246, v118, v219
MM a[40:43],a[144:147],v[72:75],a[40:43]
RD a[200:203],v226 offset:49152
EX v35,v35
v_fma_f32 v119, v246, v119, v219
CV v10,v12,v13
MM a[44:47],a[152:155],v[72:75],a[44:47]
CV v216,v112,v113
CV v217,v114,v115
MM a[44:47],a[156:159],v[76:79],a[44:47]
buffer_load_dwordx4 v229, s[8:11], s64 offen offset:1024 lds
RD a[204:207],v227 offset:49152
EX v36,v36
MM a[36:39],a[156:159],v[68:71],a[36:39]
CV v218,v116,v117
CV v11,v14,v15
EX v37,v37
CV v219,v118,v119
v_fmac_f32 v120, v243, v136
MM a[36:39],a[152:155],v[64:67],a[36:39]
RD a[208:211],v224 offset:53248
v_fmac_f32 v121, v243, v137
MM a[64:67],a[160:163],v[64:67],a[64:67]
EX v38,v38
v_fmac_f32 v122, v243, v138
MM a[64:67],a[164:167],v[68:71],a[64:67]
buffer_load_dwordx4 v230, s[8:11], s64 offen offset:2048 lds
RD a[212:215],v225 offset:53248
v_fmac_f32 v123, v243, v139
MM a[72:75],a[164:167],v[76:79],a[72:75]
EX v39,v39
v_fmac_f32 v124, v243, v140
CV v4,v32,v33
MM a[72:75],a[160:163],v[72:75],a[72:75]
RD a[216:219],v226 offset:53248
v_fmac_f32 v125, v243, v141
EX v40,v40
MM a[76:79],a[168:171],v[72:75],a[76:79]
RD a[220:223],v227 offset:53248
buffer_load_dwordx4 v231, s[8:11], s64 offen offset:3072 lds
v_fmac_f32 v126, v243, v142
MM a[76:79],a[172:175],v[76:79],a[76:79]
s_add_u32 m0, s65, 81920
v_fmac_f32 v127, v243, v143
CV v5,v34,v35
EX v41,v41
v_mul_f32 v223, -v252, v247
MM a[68:71],a[172:175],v[68:71],a[68:71]
RD a[224:227],v224 offset:57344
v_fma_f32 v120, v247, v120, v223
MM a[68:71],a[168:171],v[64:67],a[68:71]
EX v42,v42
v_fma_f32 v121, v247, v121, v223
MM a[96:99],a[176:179],v[64:67],a[96:99]
RD a[228:231],v225 offset:57344
buffer_load_dwordx4 v235, s[12:15], s81 offen offset:0 lds
v_fma_f32 v122, v247, v122, v223
EX v43,v43
CV v6,v36,v37
MM a[96:99],a[180:183],v[68:71],a[96:99]
RD a[232:235],v226 offset:57344
v_fma_f32 v123, v247, v123, v223
MM a[104:107],a[180:183],v[76:79],a[104:107]
v_fma_f32 v124, v247, v124, v223
CV v7,v38,v39
EX v44,v44
MM a[104:107],a[176:179],v[72:75],a[104:107]
v_fma_f32 v125, v247, v125, v223
v_fma_f32 v126, v247, v126, v223
MM a[108:111],a[184:187],v[72:75],a[108:111]
RD a[236:239],v227 offset:57344
buffer_load_dwordx4 v235, s[12:15], s64 offen offset:1024 lds
EX v45,v45
v_fma_f32 v127, v247, v127, v223
MM a[108:111],a[188:191],v[76:79],a[108:111]
EX v46,v46
CV v220,v120,v121
MM a[100:103],a[188:191],v[68:71],a[100:103]
RD a[240:243],v224 offset:61440
RD a[244:247],v225 offset:61440
RD a[248:251],v226 offset:61440
RD a[252:255],v227 offset:61440
CV v221,v122,v123
EX v47,v47
CV v222,v124,v125
CV v223,v126,v127
MM a[100:103],a[184:187],v[64:67],a[100:103]
buffer_load_dwordx4 v235, s[12:15], s81 offen offset:2048 lds
CV v12,v40,v41
v_smfmac_f32_16x16x64_bf16 v[144:147], v[236:239], v[0:7], v234
CV v13,v42,v43
CV v14,v44,v45
CV v15,v46,v47
s_nop 1
v_smfmac_f32_16x16x64_bf16 v[148:151], v[236:239], v[8:15], v234
s_waitcnt lgkmcnt(0)
buffer_load_dwordx4 v235, s[12:15], s64 offen offset:3072 lds
MM v[64:67],a[192:195],v[160:163],v[128:131]
s_add_u32 s64, s64, 16384
TR a[128:129],v232 offset:32768
TR a[130:131],v232 offset:36864
MM v[64:67],a[196:199],v[176:179],v[64:67]
s_add_u32 s81, s64, 896
TR a[132:133],v232 offset:40960
TR a[134:135],v232 offset:45056
MM v[64:67],a[200:203],v[192:195],v[64:67]
TR a[136:137],v233 offset:32768
TR a[138:139],v233 offset:36864
MM v[64:67],a[204:207],v[208:211],v[64:67]
v_max3_f32 v249, v144, v145, v148
v_max_f32 v249, v249, v149
MM v[68:71],a[220:223],v[208:211],v[128:131]
TR a[140:141],v233 offset:40960
v_cmp_lt_f32 vcc, s71, v249
MM v[68:71],a[216:219],v[192:195],v[68:71]
s_cbranch_vccnz LSITE2_%=
LBACK2_%=:
TR a[142:143],v233 offset:45056
v_add_f32 v244, v144, v145
MM v[68:71],a[212:215],v[176:179],v[68:71]
TR a[144:145],v232 offset:33280
v_add_f32 v245, v148, v149
MM v[68:71],a[208:211],v[160:163],v[68:71]
TR a[146:147],v232 offset:37376
TR a[148:149],v232 offset:41472
MM v[76:79],a[208:211],v[164:167],v[132:135]
s_lshl_b32 s90, %[swave], 10
s_add_u32 s90, s90, 81920
TR a[150:151],v232 offset:45568
MM v[76:79],a[212:215],v[180:183],v[76:79]
buffer_load_dwordx4 v[112:115], v248, s[20:23], s90 offen offset:0 sc1
MM v[76:79],a[216:219],v[196:199],v[76:79]
EX v64,v64
MM v[76:79],a[220:223],v[212:215],v[76:79]
EX v65,v65
MM v[72:75],a[204:207],v[212:215],v[132:135]
TR a[152:153],v233 offset:33280
CV v64,v64,v65
MM v[72:75],a[200:203],v[196:199],v[72:75]
EX v66,v66
MM v[72:75],a[196:199],v[180:183],v[72:75]
TR a[154:155],v233 offset:37376
TR a[156:157],v233 offset:41472
MM v[72:75],a[192:195],v[164:167],v[72:75]
EX v67,v67
MM v[96:99],a[224:227],v[160:163],v[128:131]
TR a[158:159],v233 offset:45568
CV v65,v66,v67
MM v[96:99],a[228:231],v[176:179],v[96:99]
EX v68,v68
MM v[96:99],a[232:235],v[192:195],v[96:99]
TR a[160:161],v232 offset:33792
TR a[162:163],v232 offset:37888
MM v[96:99],a[236:239],v[208:211],v[96:99]
EX v69,v69
MM v[100:103],a[252:255],v[208:211],v[128:131]
TR a[164:165],v232 offset:41984
TR a[166:167],v232 offset:46080
MM v[100:103],a[248:251],v[192:195],v[100:103]
EX v70,v70
MM v[100:103],a[244:247],v[176:179],v[100:103]
TR a[168:169],v233 offset:33792
CV v66,v68,v69
MM v[100:103],a[240:243],v[160:163],v[100:103]
EX v71,v71
MM v[108:111],a[240:243],v[164:167],v[132:135]
TR a[170:171],v233 offset:37888
TR a[172:173],v233 offset:41984
MM v[108:111],a[244:247],v[180:183],v[108:111]
EX v72,v72
MM v[108:111],a[248:251],v[196:199],v[108:111]
TR a[174:175],v233 offset:46080
TR a[176:177],v232 offset:34304
MM v[108:111],a[252:255],v[212:215],v[108:111]
EX v73,v73
MM v[104:107],a[236:239],v[212:215],v[132:135]
TR a[178:179],v232 offset:38400
CV v67,v70,v71
MM v[104:107],a[232:235],v[196:199],v[104:107]
TR a[180:181],v232 offset:42496
TR a[182:183],v232 offset:46592
MM v[104:107],a[228:231],v[180:183],v[104:107]
EX v74,v74
MM v[104:107],a[224:227],v[164:167],v[104:107]
s_memrealtime s[56:57]
EX v75,v75
MM v[80:83],a[192:195],v[168:171],0
s_memtime s[58:59]
TR a[184:185],v233 offset:34304
TR a[186:187],v233 offset:38400
MM v[80:83],a[196:199],v[184:187],v[80:83]
EX v76,v76
MM v[80:83],a[200:203],v[200:203],v[80:83]
TR a[188:189],v233 offset:42496
TR a[190:191],v233 offset:46592
MM v[80:83],a[204:207],v[216:219],v[80:83]
EX v77,v77
MM v[88:91],a[192:195],v[172:175],0
EX v78,v78
MM v[88:91],a[196:199],v[188:191],v[88:91]
EX v79,v79
MM v[88:91],a[200:203],v[204:207],v[88:91]
EX v96,v96
MM v[88:91],a[204:207],v[220:223],v[88:91]
s_waitcnt vmcnt(13) lgkmcnt(0)
s_barrier
CV v72,v72,v73
MM a[0:3],a[128:131],v[0:3],a[0:3]
s_sub_u32 s52, s56, s52
s_sub_u32 s54, s58, s54
v_cvt_f32_u32 v250, s52
MM a[0:3],a[132:135],v[4:7],a[0:3]
s_add_u32 m0, s65, 32768
v_cvt_f32_u32 v251, s54
CV v73,v74,v75
MM a[8:11],a[132:135],v[12:15],a[8:11]
v_rcp_f32 v251, v251
MM a[8:11],a[128:131],v[8:11],a[8:11]
v_mul_f32 v250, 0x47abe000, v250
s_lshl_b32 s52, %[swave], 11
MM a[12:15],a[136:139],v[8:11],a[12:15]
v_mul_f32 v250, v250, v251
s_lshl_b32 s54, %[sb], 3
EX v97,v97
MM a[12:15],a[140:143],v[12:15],a[12:15]
v_cvt_u32_f32 v250, v250
s_add_u32 s52, s52, s54
v_max3_f32 v154, v80, v81, v82
MM a[4:7],a[140:143],v[4:7],a[4:7]
v_add_u32 v250, s56, v250
v_mov_b32 v251, s69
v_max_f32 v154, v154, v83
EX v98,v98
MM a[4:7],a[136:139],v[0:3],a[4:7]
s_add_u32 s52, s52, 86016
s_mov_b64 exec, 1
buffer_store_dwordx2 v[250:251], off, s[20:23], s52 sc1
s_mov_b64 exec, -1
s_nop 0
v_mov_b32 v155, v154
MM a[32:35],a[144:147],v[0:3],a[32:35]
RD a[192:195],v224 offset:0
v_permlane16_swap_b32 v154, v155
EX v99,v99
CV v74,v76,v77
v_max_f32 v154, v154, v155
MM a[32:35],a[148:151],v[4:7],a[32:35]
buffer_load_dwordx4 v228, s[8:11], s64 offen offset:0 lds
v_mov_b32 v155, v154
MM a[40:43],a[148:151],v[12:15],a[40:43]
RD a[196:199],v225 offset:0
v_permlane32_swap_b32 v154, v155
EX v100,v100
MM a[40:43],a[144:147],v[8:11],a[40:43]
RD a[200:203],v226 offset:0
v_max_f32 v154, v154, v155
CV v75,v78,v79
MM a[44:47],a[152:155],v[8:11],a[44:47]
v_sub_f32 v136, 0xc1600000, v154
v_sub_f32 v137, 0xc1600000, v154
EX v101,v101
MM a[44:47],a[156:159],v[12:15],a[44:47]
buffer_load_dwordx4 v229, s[8:11], s64 offen offset:1024 lds
RD a[204:207],v227 offset:0
MM a[36:39],a[156:159],v[4:7],a[36:39]
v_sub_f32 v138, 0xc1600000, v154
v_sub_f32 v139, 0xc1600000, v154
EX v102,v102
MM a[36:39],a[152:155],v[0:3],a[36:39]
RD a[208:211],v224 offset:4096
v_mov_b32 v246, 0
MM a[64:67],a[160:163],v[0:3],a[64:67]
EX v103,v103
v_mov_b32 v152, 0
CV v68,v96,v97
MM a[64:67],a[164:167],v[4:7],a[64:67]
buffer_load_dwordx4 v230, s[8:11], s64 offen offset:2048 lds
RD a[212:215],v225 offset:4096
v_mov_b32 v153, 0
MM a[72:75],a[164:167],v[12:15],a[72:75]
EX v104,v104
v_max3_f32 v158, v88, v89, v90
MM a[72:75],a[160:163],v[8:11],a[72:75]
RD a[216:219],v226 offset:4096
v_max_f32 v158, v158, v91
CV v69,v98,v99
MM a[76:79],a[168:171],v[8:11],a[76:79]
buffer_load_dwordx4 v231, s[8:11], s64 offen offset:3072 lds
RD a[220:223],v227 offset:4096
EX v105,v105
v_mov_b32 v159, v158
MM a[76:79],a[172:175],v[12:15],a[76:79]
s_add_u32 m0, s65, 98304
v_permlane16_swap_b32 v158, v159
v_max_f32 v158, v158, v159
MM a[68:71],a[172:175],v[4:7],a[68:71]
RD a[224:227],v224 offset:8192
EX v106,v106
v_mov_b32 v159, v158
MM a[68:71],a[168:171],v[0:3],a[68:71]
s_nop 0
v_permlane32_swap_b32 v158, v159
CV v70,v100,v101
EX v107,v107
MM a[96:99],a[176:179],v[0:3],a[96:99]
RD a[228:231],v225 offset:8192
buffer_load_dwordx4 v235, s[12:15], s81 offen offset:0 lds
v_max_f32 v158, v158, v159
MM a[96:99],a[180:183],v[4:7],a[96:99]
v_sub_f32 v140, 0xc1600000, v158
CV v71,v102,v103
EX v108,v108
MM a[104:107],a[180:183],v[12:15],a[104:107]
RD a[232:235],v226 offset:8192
v_sub_f32 v141, 0xc1600000, v158
MM a[104:107],a[176:179],v[8:11],a[104:107]
v_sub_f32 v142, 0xc1600000, v158
v_sub_f32 v143, 0xc1600000, v158
EX v109,v109
MM a[108:111],a[184:187],v[8:11],a[108:111]
RD a[236:239],v227 offset:8192
buffer_load_dwordx4 v235, s[12:15], s64 offen offset:1024 lds
v_mov_b32 v247, 0
v_mov_b32 v156, 0
v_mov_b32 v157, 0
MM a[108:111],a[188:191],v[12:15],a[108:111]
EX v110,v110
MM a[100:103],a[188:191],v[4:7],a[100:103]
RD a[240:243],v224 offset:12288
RD a[244:247],v225 offset:12288
RD a[248:251],v226 offset:12288
RD a[252:255],v227 offset:12288
EX v111,v111
MM a[100:103],a[184:187],v[0:3],a[100:103]
buffer_load_dwordx4 v235, s[12:15], s81 offen offset:2048 lds
CV v76,v104,v105
v_smfmac_f32_16x16x64_bf16 v[144:147], v[236:239], v[64:71], v234
CV v77,v106,v107
CV v78,v108,v109
CV v79,v110,v111
s_nop 1
v_smfmac_f32_16x16x64_bf16 v[148:151], v[236:239], v[72:79], v234
)ASM"
R"ASM(
s_waitcnt lgkmcnt(0)
buffer_load_dwordx4 v235, s[12:15], s64 offen offset:3072 lds
MM v[0:3],a[192:195],v[160:163],v[128:131]
s_add_u32 s64, s64, 16384
TR a[128:129],v232 offset:49152
s_add_u32 s81, s64, 896
MM v[0:3],a[196:199],v[176:179],v[0:3]
TR a[130:131],v232 offset:53248
TR a[132:133],v232 offset:57344
MM v[0:3],a[200:203],v[192:195],v[0:3]
TR a[134:135],v232 offset:61440
TR a[136:137],v233 offset:49152
MM v[0:3],a[204:207],v[208:211],v[0:3]
v_max3_f32 v249, v144, v145, v148
v_max_f32 v249, v249, v149
MM v[4:7],a[220:223],v[208:211],v[128:131]
TR a[138:139],v233 offset:53248
v_cmp_lt_f32 vcc, s71, v249
MM v[4:7],a[216:219],v[192:195],v[4:7]
s_cbranch_vccnz LSITE3_%=
LBACK3_%=:
TR a[140:141],v233 offset:57344
v_add_f32 v244, v144, v145
MM v[4:7],a[212:215],v[176:179],v[4:7]
TR a[142:143],v233 offset:61440
v_add_f32 v245, v148, v149
MM v[4:7],a[208:211],v[160:163],v[4:7]
TR a[144:145],v232 offset:49664
TR a[146:147],v232 offset:53760
MM v[12:15],a[208:211],v[164:167],v[132:135]
EX v0,v0
MM v[12:15],a[212:215],v[180:183],v[12:15]
EX v1,v1
MM v[12:15],a[216:219],v[196:199],v[12:15]
TR a[148:149],v232 offset:57856
CV v0,v0,v1
MM v[12:15],a[220:223],v[212:215],v[12:15]
EX v2,v2
MM v[8:11],a[204:207],v[212:215],v[132:135]
EX v3,v3
MM v[8:11],a[200:203],v[196:199],v[8:11]
TR a[150:151],v232 offset:61952
CV v1,v2,v3
MM v[8:11],a[196:199],v[180:183],v[8:11]
EX v4,v4
MM v[8:11],a[192:195],v[164:167],v[8:11]
EX v5,v5
MM v[16:19],a[192:195],v[168:171],v[136:139]
TR a[152:153],v233 offset:49664
CV v2,v4,v5
MM v[16:19],a[196:199],v[184:187],v[16:19]
EX v6,v6
MM v[16:19],a[200:203],v[200:203],v[16:19]
EX v7,v7
MM v[16:19],a[204:207],v[216:219],v[16:19]
TR a[154:155],v233 offset:53760
CV v3,v6,v7
MM v[20:23],a[220:223],v[216:219],v[136:139]
s_waitcnt vmcnt(9)
v_xor_b32 v112, s69, v112
v_xor_b32 v113, s69, v113
MM v[20:23],a[216:219],v[200:203],v[20:23]
v_xor_b32 v114, s69, v114
v_xor_b32 v115, s69, v115
MM v[20:23],a[212:215],v[184:187],v[20:23]
v_or3_b32 v252, v112, v113, v114
v_or_b32 v252, v252, v115
EX v8,v8
MM v[20:23],a[208:211],v[168:171],v[20:23]
v_cmp_ne_u32 vcc, 0, v252
s_cbranch_vccnz LRFR_%=
LRFRb_%=:
EX v9,v9
MM v[28:31],a[208:211],v[172:175],v[140:143]
s_lshl_b32 s90, %[swave], 11
s_add_u32 s78, s90, %[slds]
s_add_u32 s78, s78, 155648
MM v[28:31],a[212:215],v[188:191],v[28:31]
s_add_u32 s90, s90, 53248
s_mov_b32 m0, s78
CV v8,v8,v9
EX v10,v10
MM v[28:31],a[216:219],v[204:207],v[28:31]
buffer_load_dwordx4 v248, s[20:23], s90 offen offset:0 sc1 lds
EX v11,v11
MM v[28:31],a[220:223],v[220:223],v[28:31]
buffer_load_dwordx4 v248, s[20:23], s90 offen offset:1024 sc1 lds
EX v12,v12
MM v[24:27],a[204:207],v[220:223],v[140:143]
EX v13,v13
MM v[24:27],a[200:203],v[204:207],v[24:27]
EX v14,v14
MM v[24:27],a[196:199],v[188:191],v[24:27]
TR a[156:157],v233 offset:57856
CV v9,v10,v11
MM v[24:27],a[192:195],v[172:175],v[24:27]
EX v15,v15
MM v[32:35],a[224:227],v[160:163],v[128:131]
EX v16,v16
MM v[32:35],a[228:231],v[176:179],v[32:35]
TR a[158:159],v233 offset:61952
TR a[160:161],v232 offset:50176
MM v[32:35],a[232:235],v[192:195],v[32:35]
EX v17,v17
MM v[32:35],a[236:239],v[208:211],v[32:35]
EX v18,v18
MM v[36:39],a[252:255],v[208:211],v[128:131]
TR a[162:163],v232 offset:54272
CV v10,v12,v13
MM v[36:39],a[248:251],v[192:195],v[36:39]
EX v19,v19
MM v[36:39],a[244:247],v[176:179],v[36:39]
EX v20,v20
MM v[36:39],a[240:243],v[160:163],v[36:39]
TR a[164:165],v232 offset:58368
CV v11,v14,v15
MM v[44:47],a[240:243],v[164:167],v[132:135]
EX v21,v21
MM v[44:47],a[244:247],v[180:183],v[44:47]
TR a[166:167],v232 offset:62464
EX v22,v22
MM v[44:47],a[248:251],v[196:199],v[44:47]
EX v23,v23
MM v[44:47],a[252:255],v[212:215],v[44:47]
TR a[168:169],v233 offset:50176
CV v16,v16,v17
MM v[40:43],a[236:239],v[212:215],v[132:135]
EX v24,v24
MM v[40:43],a[232:235],v[196:199],v[40:43]
TR a[170:171],v233 offset:54272
EX v25,v25
MM v[40:43],a[228:231],v[180:183],v[40:43]
TR a[172:173],v233 offset:58368
CV v17,v18,v19
EX v26,v26
MM v[40:43],a[224:227],v[164:167],v[40:43]
TR a[174:175],v233 offset:62464
CV v18,v20,v21
MM v[48:51],a[224:227],v[168:171],v[136:139]
EX v27,v27
MM v[48:51],a[228:231],v[184:187],v[48:51]
EX v28,v28
MM v[48:51],a[232:235],v[200:203],v[48:51]
TR a[176:177],v232 offset:50688
EX v29,v29
MM v[48:51],a[236:239],v[216:219],v[48:51]
TR a[178:179],v232 offset:54784
CV v19,v22,v23
MM v[52:55],a[252:255],v[216:219],v[136:139]
EX v30,v30
MM v[52:55],a[248:251],v[200:203],v[52:55]
EX v31,v31
MM v[52:55],a[244:247],v[184:187],v[52:55]
TR a[180:181],v232 offset:58880
CV v24,v24,v25
MM v[52:55],a[240:243],v[168:171],v[52:55]
EX v32,v32
MM v[60:63],a[240:243],v[172:175],v[140:143]
TR a[182:183],v232 offset:62976
EX v33,v33
CV v25,v26,v27
MM v[60:63],a[244:247],v[188:191],v[60:63]
EX v34,v34
MM v[60:63],a[248:251],v[204:207],v[60:63]
TR a[184:185],v233 offset:50688
TR a[186:187],v233 offset:54784
TR a[188:189],v233 offset:58880
TR a[190:191],v233 offset:62976
MM v[60:63],a[252:255],v[220:223],v[60:63]
EX v35,v35
CV v26,v28,v29
MM v[56:59],a[236:239],v[220:223],v[140:143]
EX v36,v36
MM v[56:59],a[232:235],v[204:207],v[56:59]
EX v37,v37
CV v27,v30,v31
MM v[56:59],a[228:231],v[188:191],v[56:59]
EX v38,v38
MM v[56:59],a[224:227],v[172:175],v[56:59]
s_waitcnt lgkmcnt(0)
s_barrier
CV v4,v32,v33
MM a[0:3],a[128:131],v[64:67],a[0:3]
s_add_u32 m0, s65, 49152
EX v39,v39
MM a[0:3],a[132:135],v[68:71],a[0:3]
EX v40,v40
MM a[8:11],a[132:135],v[76:79],a[8:11]
CV v5,v34,v35
CV v6,v36,v37
MM a[8:11],a[128:131],v[72:75],a[8:11]
EX v41,v41
MM a[12:15],a[136:139],v[72:75],a[12:15]
EX v42,v42
MM a[12:15],a[140:143],v[76:79],a[12:15]
EX v43,v43
MM a[4:7],a[140:143],v[68:71],a[4:7]
EX v44,v44
MM a[4:7],a[136:139],v[64:67],a[4:7]
RD a[192:195],v224 offset:16384
CV v7,v38,v39
MM a[32:35],a[144:147],v[64:67],a[32:35]
EX v45,v45
MM a[32:35],a[148:151],v[68:71],a[32:35]
buffer_load_dwordx4 v228, s[8:11], s64 offen offset:0 lds
MM a[40:43],a[148:151],v[76:79],a[40:43]
RD a[196:199],v225 offset:16384
EX v46,v46
MM a[40:43],a[144:147],v[72:75],a[40:43]
EX v47,v47
CV v12,v40,v41
s_nop 0
LPG6_%=:
s_cbranch_execz LPG10_%=
MM a[44:47],a[152:155],v[72:75],a[44:47]
RD a[200:203],v226 offset:16384
EX v48,v48
MM a[44:47],a[156:159],v[76:79],a[44:47]
buffer_load_dwordx4 v229, s[8:11], s64 offen offset:1024 lds
MM a[36:39],a[156:159],v[68:71],a[36:39]
RD a[204:207],v227 offset:16384
EX v49,v49
CV v13,v42,v43
MM a[36:39],a[152:155],v[64:67],a[36:39]
EX v50,v50
MM a[64:67],a[160:163],v[64:67],a[64:67]
RD a[208:211],v224 offset:20480
EX v51,v51
CV v14,v44,v45
MM a[64:67],a[164:167],v[68:71],a[64:67]
buffer_load_dwordx4 v230, s[8:11], s64 offen offset:2048 lds
MM a[72:75],a[164:167],v[76:79],a[72:75]
RD a[212:215],v225 offset:20480
EX v52,v52
MM a[72:75],a[160:163],v[72:75],a[72:75]
EX v53,v53
CV v15,v46,v47
MM a[76:79],a[168:171],v[72:75],a[76:79]
RD a[216:219],v226 offset:20480
CV v20,v48,v49
EX v54,v54
MM a[76:79],a[172:175],v[76:79],a[76:79]
buffer_load_dwordx4 v231, s[8:11], s64 offen offset:3072 lds
MM a[68:71],a[172:175],v[68:71],a[68:71]
s_add_u32 m0, s65, 114688
RD a[220:223],v227 offset:20480
EX v55,v55
MM a[68:71],a[168:171],v[64:67],a[68:71]
EX v56,v56
MM a[96:99],a[176:179],v[64:67],a[96:99]
RD a[224:227],v224 offset:24576
CV v21,v50,v51
EX v57,v57
MM a[96:99],a[180:183],v[68:71],a[96:99]
RD a[228:231],v225 offset:24576
CV v22,v52,v53
MM a[104:107],a[180:183],v[76:79],a[104:107]
buffer_load_dwordx4 v235, s[12:15], s81 offen offset:0 lds
EX v58,v58
MM a[104:107],a[176:179],v[72:75],a[104:107]
EX v59,v59
MM a[108:111],a[184:187],v[72:75],a[108:111]
RD a[232:235],v226 offset:24576
CV v23,v54,v55
MM a[108:111],a[188:191],v[76:79],a[108:111]
EX v60,v60
MM a[100:103],a[188:191],v[68:71],a[100:103]
RD a[236:239],v227 offset:24576
buffer_load_dwordx4 v235, s[12:15], s64 offen offset:1024 lds
EX v61,v61
MM a[100:103],a[184:187],v[64:67],a[100:103]
EX v62,v62
v_smfmac_f32_16x16x64_bf16 v[144:147], v[236:239], v[0:7], v234
RD a[240:243],v224 offset:28672
RD a[244:247],v225 offset:28672
RD a[248:251],v226 offset:28672
RD a[252:255],v227 offset:28672
EX v63,v63
v_smfmac_f32_16x16x64_bf16 v[148:151], v[236:239], v[8:15], v234
CV v28,v56,v57
v_smfmac_f32_16x16x64_bf16 v[152:155], v[236:239], v[16:23], v234
buffer_load_dwordx4 v235, s[12:15], s81 offen offset:2048 lds
CV v29,v58,v59
CV v30,v60,v61
CV v31,v62,v63
s_nop 1
v_smfmac_f32_16x16x64_bf16 v[156:159], v[236:239], v[24:31], v234
s_waitcnt lgkmcnt(0)
buffer_load_dwordx4 v235, s[12:15], s64 offen offset:3072 lds
MM v[64:67],a[192:195],v[160:163],v[128:131]
s_add_u32 s64, s64, 16384
TR a[128:129],v232 offset:0
s_add_u32 s81, s64, 896
MM v[64:67],a[196:199],v[176:179],v[64:67]
TR a[130:131],v232 offset:4096
v_max3_f32 v249, v144, v145, v148
MM v[64:67],a[200:203],v[192:195],v[64:67]
TR a[132:133],v232 offset:8192
v_max3_f32 v249, v249, v149, v152
MM v[64:67],a[204:207],v[208:211],v[64:67]
TR a[134:135],v232 offset:12288
v_max3_f32 v249, v249, v153, v156
MM v[68:71],a[220:223],v[208:211],v[128:131]
TR a[136:137],v233 offset:0
v_max_f32 v249, v249, v157
MM v[68:71],a[216:219],v[192:195],v[68:71]
TR a[138:139],v233 offset:4096
v_cmp_lt_f32 vcc, s71, v249
MM v[68:71],a[212:215],v[176:179],v[68:71]
TR a[140:141],v233 offset:8192
TR a[142:143],v233 offset:12288
MM v[68:71],a[208:211],v[160:163],v[68:71]
s_cbranch_vccnz LSITE4_%=
LBACK4_%=:
MM v[76:79],a[208:211],v[164:167],v[132:135]
s_cmp_lg_u32 %[sb], 0
s_cbranch_scc1 LEP_%=
v_mov_b32 v250, s69
s_mov_b32 s76, 32768
s_mov_b64 exec, 1
buffer_store_dword v250, off, s[20:23], s76 sc1
s_mov_b64 exec, -1
s_nop 1
LEP_%=:
MM v[76:79],a[212:215],v[180:183],v[76:79]
s_lshl_b32 s76, s50, 1
EX v64,v64
MM v[76:79],a[216:219],v[196:199],v[76:79]
s_waitcnt lgkmcnt(6)
EX v65,v65
MM v[76:79],a[220:223],v[212:215],v[76:79]
s_bfe_u32 s77, s49, 65538
TR a[144:145],v232 offset:512
CV v64,v64,v65
MM v[48:51],a[128:131],v[16:19],0
MM v[52:55],a[128:131],v[24:27],0
MM v[72:75],a[204:207],v[212:215],v[132:135]
s_add_u32 s76, s76, s77
EX v66,v66
MM v[72:75],a[200:203],v[196:199],v[72:75]
s_lshl_b32 s76, s76, 2
EX v67,v67
MM v[72:75],a[196:199],v[180:183],v[72:75]
s_add_u32 s76, s76, s84
TR a[146:147],v232 offset:4608
CV v65,v66,v67
MM v[72:75],a[192:195],v[164:167],v[72:75]
s_lshl_b32 s76, s76, 2
EX v68,v68
MM v[80:83],a[192:195],v[168:171],v[136:139]
s_lshr_b32 s77, s49, 3
v_max3_f32 v48, |v48|, |v49|, |v50|
v_max_f32 v48, v48, |v51|
MM v[80:83],a[196:199],v[184:187],v[80:83]
s_add_u32 s76, s76, s77
v_bfe_u32 v48, v48, 23, 8
v_add_u32 v48, 8, v48
MM v[80:83],a[200:203],v[200:203],v[80:83]
s_mul_i32 s95, s76, 33792
v_min_u32 v48, 240, v48
v_lshl_or_b32 v241, v48, 16, v241
MM v[80:83],a[204:207],v[216:219],v[80:83]
s_lshl_b32 s76, %[swave], 13
v_lshl_or_b32 v48, v48, 1, 1
v_lshlrev_b32 v48, 22, v48
MM v[84:87],a[220:223],v[216:219],v[136:139]
s_add_u32 s95, s95, s76
AW a116,v48
AW a117,v48
MM v[84:87],a[216:219],v[200:203],v[84:87]
s_add_u32 s95, s95, 22708224
AW a118,v48
AW a119,v48
EX v69,v69
MM v[84:87],a[212:215],v[184:187],v[84:87]
v_max3_f32 v52, |v52|, |v53|, |v54|
v_max_f32 v52, v52, |v55|
MM v[84:87],a[208:211],v[168:171],v[84:87]
v_bfe_u32 v52, v52, 23, 8
v_add_u32 v52, 8, v52
EX v70,v70
v_mov_b32 v240, -1
MM v[92:95],a[208:211],v[172:175],v[140:143]
s_mov_b32 s67, s95
v_min_u32 v52, 240, v52
v_lshl_or_b32 v241, v52, 24, v241
MM v[92:95],a[212:215],v[188:191],v[92:95]
s_add_u32 s87, s95, 6144
TR a[148:149],v232 offset:8704
v_lshl_or_b32 v52, v52, 1, 1
v_lshlrev_b32 v52, 22, v52
EX v71,v71
MM v[92:95],a[216:219],v[204:207],v[92:95]
AW a124,v52
AW a125,v52
MM v[92:95],a[220:223],v[220:223],v[92:95]
TR a[150:151],v232 offset:12800
buffer_store_dword v240, v248, s[20:23], s87 offen offset:12 sc1
AW a126,v52
AW a127,v52
EX v72,v72
MM v[88:91],a[204:207],v[220:223],v[140:143]
TR a[152:153],v233 offset:512
TR a[154:155],v233 offset:4608
MM v[88:91],a[200:203],v[204:207],v[88:91]
EX v73,v73
MM v[88:91],a[196:199],v[188:191],v[88:91]
EX v74,v74
MM v[88:91],a[192:195],v[172:175],v[88:91]
TR a[156:157],v233 offset:8704
CV v66,v68,v69
MM v[96:99],a[224:227],v[160:163],v[128:131]
)ASM"
R"ASM(
EX v75,v75
MM v[96:99],a[228:231],v[176:179],v[96:99]
s_add_u32 s87, s95, 7168
buffer_store_dword v240, v248, s[20:23], s87 offen offset:12 sc1
MM v[96:99],a[232:235],v[192:195],v[96:99]
TR a[158:159],v233 offset:12800
v_lshrrev_b32 v34, 1, v248
EX v76,v76
MM v[96:99],a[236:239],v[208:211],v[96:99]
s_mov_b32 s90, 86016
buffer_load_dwordx2 v[32:33], v34, s[20:23], s90 offen offset:0 sc1
MM v[100:103],a[252:255],v[208:211],v[128:131]
TR a[160:161],v232 offset:1024
EX v77,v77
CV v67,v70,v71
MM v[100:103],a[248:251],v[192:195],v[100:103]
TR a[162:163],v232 offset:5120
CV v72,v72,v73
MM v[100:103],a[244:247],v[176:179],v[100:103]
EX v78,v78
MM v[100:103],a[240:243],v[160:163],v[100:103]
TR a[164:165],v232 offset:9216
TR a[166:167],v232 offset:13312
MM v[108:111],a[240:243],v[164:167],v[132:135]
EX v79,v79
MM v[108:111],a[244:247],v[180:183],v[108:111]
EX v80,v80
MM v[108:111],a[248:251],v[196:199],v[108:111]
TR a[168:169],v233 offset:1024
CV v73,v74,v75
MM v[108:111],a[252:255],v[212:215],v[108:111]
EX v81,v81
MM v[104:107],a[236:239],v[212:215],v[132:135]
TR a[170:171],v233 offset:5120
CV v74,v76,v77
MM v[104:107],a[232:235],v[196:199],v[104:107]
EX v82,v82
MM v[104:107],a[228:231],v[180:183],v[104:107]
TR a[172:173],v233 offset:9216
TR a[174:175],v233 offset:13312
MM v[104:107],a[224:227],v[164:167],v[104:107]
EX v83,v83
MM v[112:115],a[224:227],v[168:171],v[136:139]
TR a[176:177],v232 offset:1536
CV v75,v78,v79
MM v[112:115],a[228:231],v[184:187],v[112:115]
EX v84,v84
MM v[112:115],a[232:235],v[200:203],v[112:115]
EX v85,v85
MM v[112:115],a[236:239],v[216:219],v[112:115]
TR a[178:179],v232 offset:5632
CV v80,v80,v81
MM v[116:119],a[252:255],v[216:219],v[136:139]
EX v86,v86
MM v[116:119],a[248:251],v[200:203],v[116:119]
TR a[180:181],v232 offset:9728
TR a[182:183],v232 offset:13824
MM v[116:119],a[244:247],v[184:187],v[116:119]
EX v87,v87
MM v[116:119],a[240:243],v[168:171],v[116:119]
TR a[184:185],v233 offset:1536
CV v81,v82,v83
MM v[124:127],a[240:243],v[172:175],v[140:143]
EX v88,v88
MM v[124:127],a[244:247],v[188:191],v[124:127]
TR a[186:187],v233 offset:5632
CV v82,v84,v85
MM v[124:127],a[248:251],v[204:207],v[124:127]
TR a[188:189],v233 offset:9728
TR a[190:191],v233 offset:13824
EX v89,v89
MM v[124:127],a[252:255],v[220:223],v[124:127]
EX v90,v90
MM v[120:123],a[236:239],v[220:223],v[140:143]
CV v83,v86,v87
CV v88,v88,v89
MM v[120:123],a[232:235],v[204:207],v[120:123]
EX v91,v91
MM v[120:123],a[228:231],v[188:191],v[120:123]
EX v92,v92
MM v[120:123],a[224:227],v[172:175],v[120:123]
s_waitcnt vmcnt(11) lgkmcnt(0)
s_barrier
MM a[0:3],a[128:131],v[0:3],a[0:3]
s_add_u32 m0, s65, 0
EX v93,v93
MM a[0:3],a[132:135],v[4:7],a[0:3]
CV v89,v90,v91
CV v90,v92,v93
MM a[8:11],a[132:135],v[12:15],a[8:11]
EX v94,v94
MM a[8:11],a[128:131],v[8:11],a[8:11]
EX v95,v95
MM a[16:19],a[128:131],v[16:19],a[116:119]
RD a[192:195],v224 offset:32768
CV v91,v94,v95
MM a[16:19],a[132:135],v[20:23],a[16:19]
EX v96,v96
MM a[24:27],a[132:135],v[28:31],a[124:127]
buffer_load_dwordx4 v228, s[8:11], s64 offen offset:0 lds
MM a[24:27],a[128:131],v[24:27],a[24:27]
EX v97,v97
MM a[28:31],a[136:139],v[24:27],a[124:127]
RD a[196:199],v225 offset:32768
CV v68,v96,v97
MM a[28:31],a[140:143],v[28:31],a[28:31]
EX v98,v98
MM a[20:23],a[140:143],v[20:23],a[116:119]
EX v99,v99
MM a[20:23],a[136:139],v[16:19],a[20:23]
RD a[200:203],v226 offset:32768
CV v69,v98,v99
MM a[12:15],a[136:139],v[8:11],a[12:15]
EX v100,v100
MM a[12:15],a[140:143],v[12:15],a[12:15]
buffer_load_dwordx4 v229, s[8:11], s64 offen offset:1024 lds
MM a[4:7],a[140:143],v[4:7],a[4:7]
EX v101,v101
MM a[4:7],a[136:139],v[0:3],a[4:7]
RD a[204:207],v227 offset:32768
CV v70,v100,v101
MM a[32:35],a[144:147],v[0:3],a[32:35]
EX v102,v102
MM a[32:35],a[148:151],v[4:7],a[32:35]
EX v103,v103
MM a[40:43],a[148:151],v[12:15],a[40:43]
RD a[208:211],v224 offset:36864
CV v71,v102,v103
MM a[40:43],a[144:147],v[8:11],a[40:43]
EX v104,v104
MM a[48:51],a[144:147],v[16:19],a[116:119]
buffer_load_dwordx4 v230, s[8:11], s64 offen offset:2048 lds
MM a[48:51],a[148:151],v[20:23],a[48:51]
EX v105,v105
MM a[56:59],a[148:151],v[28:31],a[124:127]
RD a[212:215],v225 offset:36864
CV v76,v104,v105
MM a[56:59],a[144:147],v[24:27],a[56:59]
EX v106,v106
MM a[60:63],a[152:155],v[24:27],a[124:127]
EX v107,v107
MM a[60:63],a[156:159],v[28:31],a[60:63]
RD a[216:219],v226 offset:36864
CV v77,v106,v107
MM a[52:55],a[156:159],v[20:23],a[116:119]
EX v108,v108
MM a[52:55],a[152:155],v[16:19],a[52:55]
buffer_load_dwordx4 v231, s[8:11], s64 offen offset:3072 lds
MM a[44:47],a[152:155],v[8:11],a[44:47]
s_add_u32 m0, s65, 65536
EX v109,v109
MM a[44:47],a[156:159],v[12:15],a[44:47]
RD a[220:223],v227 offset:36864
CV v78,v108,v109
MM a[36:39],a[156:159],v[4:7],a[36:39]
EX v110,v110
MM a[36:39],a[152:155],v[0:3],a[36:39]
RD a[224:227],v224 offset:40960
RD a[228:231],v225 offset:40960
MM a[64:67],a[160:163],v[0:3],a[64:67]
s_waitcnt vmcnt(4)
v_cmp_ne_u32 vcc, s69, v33
s_cbranch_vccnz LRRK_%=
LRRKb_%=:
MM a[64:67],a[164:167],v[4:7],a[64:67]
RD a[232:235],v226 offset:40960
v_add_u32_dpp v32, v32, v32 row_ror:8 row_mask:0xf bank_mask:0xf
MM a[72:75],a[164:167],v[12:15],a[72:75]
s_and_b32 s76, %[sb], 7
RD a[236:239],v227 offset:40960
v_mov_b32 v35, v32
MM a[72:75],a[160:163],v[8:11],a[72:75]
s_nop 0
v_permlane16_swap_b32 v32, v35
v_add_u32 v32, v32, v35
EX v111,v111
MM a[80:83],a[160:163],v[16:19],a[116:119]
v_mov_b32 v35, v32
CV v79,v110,v111
MM a[80:83],a[164:167],v[20:23],a[80:83]
v_permlane32_swap_b32 v32, v35
v_add_u32 v32, v32, v35
EX v112,v112
MM a[88:91],a[164:167],v[28:31],a[124:127]
RD a[240:243],v224 offset:45056
v_readlane_b32 s77, v32, s76
MM a[88:91],a[160:163],v[24:27],a[88:91]
s_bfm_b32 s76, s76, 0
EX v113,v113
MM a[92:95],a[168:171],v[24:27],a[124:127]
v_cmp_gt_u32 s[60:61], s77, v32
v_cmp_eq_u32 s[62:63], s77, v32
MM a[92:95],a[172:175],v[28:31],a[92:95]
s_and_b32 s62, s62, s76
EX v114,v114
MM a[84:87],a[172:175],v[20:23],a[116:119]
s_or_b32 s60, s60, s62
EX v115,v115
MM a[84:87],a[168:171],v[16:19],a[84:87]
s_and_b32 s60, s60, 255
EX v116,v116
MM a[76:79],a[168:171],v[8:11],a[76:79]
s_bcnt1_i32_b32 s51, s60
s_cmp_lt_u32 s51, 4
s_cselect_b32 s82, 3, 99
MM a[76:79],a[172:175],v[12:15],a[76:79]
s_lshr_b32 s16, %[sb], 3
EX v117,v117
MM a[68:71],a[172:175],v[4:7],a[68:71]
s_lshl_b32 s76, s51, 5
buffer_load_dwordx4 v235, s[12:15], s81 offen offset:0 lds
MM a[68:71],a[168:171],v[0:3],a[68:71]
s_add_u32 s16, s16, s76
CV v84,v112,v113
CV v85,v114,v115
MM a[96:99],a[176:179],v[0:3],a[96:99]
EX v118,v118
MM a[96:99],a[180:183],v[4:7],a[96:99]
EX v119,v119
MM a[104:107],a[180:183],v[12:15],a[104:107]
CV v86,v116,v117
CV v87,v118,v119
MM a[104:107],a[176:179],v[8:11],a[104:107]
EX v120,v120
MM a[112:115],a[176:179],v[16:19],a[116:119]
buffer_load_dwordx4 v235, s[12:15], s64 offen offset:1024 lds
MM a[112:115],a[180:183],v[20:23],a[112:115]
EX v121,v121
MM a[120:123],a[180:183],v[28:31],a[124:127]
EX v122,v122
MM a[120:123],a[176:179],v[24:27],a[120:123]
EX v123,v123
MM a[124:127],a[184:187],v[24:27],a[124:127]
v_add_f32 v244, v144, v145
v_add_f32 v245, v148, v149
MM a[124:127],a[188:191],v[28:31],a[124:127]
v_add_f32 v246, v152, v153
v_add_f32 v247, v156, v157
MM a[116:119],a[188:191],v[20:23],a[116:119]
EX v124,v124
MM a[116:119],a[184:187],v[16:19],a[116:119]
RD a[244:247],v225 offset:45056
CV v92,v120,v121
MM a[108:111],a[184:187],v[8:11],a[108:111]
EX v125,v125
MM a[108:111],a[188:191],v[12:15],a[108:111]
buffer_load_dwordx4 v235, s[12:15], s81 offen offset:2048 lds
MM a[100:103],a[188:191],v[4:7],a[100:103]
EX v126,v126
MM a[100:103],a[184:187],v[0:3],a[100:103]
RD a[248:251],v226 offset:45056
CV v93,v122,v123
v_smfmac_f32_16x16x64_bf16 v[144:147], v[236:239], v[64:71], v234
RD a[252:255],v227 offset:45056
EX v127,v127
v_smfmac_f32_16x16x64_bf16 v[148:151], v[236:239], v[72:79], v234
CV v94,v124,v125
v_smfmac_f32_16x16x64_bf16 v[152:155], v[236:239], v[80:87], v234
CV v95,v126,v127
s_nop 1
v_smfmac_f32_16x16x64_bf16 v[156:159], v[236:239], v[88:95], v234
s_waitcnt lgkmcnt(0)
buffer_load_dwordx4 v235, s[12:15], s64 offen offset:3072 lds
s_cmp_lt_u32 s51, 4
s_cbranch_scc1 LLAR_%=
s_mov_b32 s70, 6
LLAP_%=:
MM v[0:3],a[192:195],v[160:163],v[128:131]
s_add_u32 s64, s64, 16384
TR a[128:129],v232 offset:16384
s_add_u32 s81, s64, 896
MM v[0:3],a[196:199],v[176:179],v[0:3]
TR a[130:131],v232 offset:20480
v_max3_f32 v249, v144, v145, v148
MM v[0:3],a[200:203],v[192:195],v[0:3]
TR a[132:133],v232 offset:24576
v_max3_f32 v249, v249, v149, v152
MM v[0:3],a[204:207],v[208:211],v[0:3]
TR a[134:135],v232 offset:28672
v_max3_f32 v249, v249, v153, v156
MM v[4:7],a[220:223],v[208:211],v[128:131]
TR a[136:137],v233 offset:16384
v_max_f32 v249, v249, v157
MM v[4:7],a[216:219],v[192:195],v[4:7]
TR a[138:139],v233 offset:20480
v_cmp_lt_f32 vcc, s71, v249
MM v[4:7],a[212:215],v[176:179],v[4:7]
TR a[140:141],v233 offset:24576
TR a[142:143],v233 offset:28672
MM v[4:7],a[208:211],v[160:163],v[4:7]
s_cbranch_vccnz LSITE5_%=
LBACK5_%=:
MM v[12:15],a[208:211],v[164:167],v[132:135]
EX v0,v0
MM v[12:15],a[212:215],v[180:183],v[12:15]
EX v1,v1
MM v[12:15],a[216:219],v[196:199],v[12:15]
TR a[144:145],v232 offset:16896
CV v0,v0,v1
MM v[12:15],a[220:223],v[212:215],v[12:15]
EX v2,v2
MM v[8:11],a[204:207],v[212:215],v[132:135]
EX v3,v3
MM v[8:11],a[200:203],v[196:199],v[8:11]
TR a[146:147],v232 offset:20992
CV v1,v2,v3
MM v[8:11],a[196:199],v[180:183],v[8:11]
EX v4,v4
MM v[8:11],a[192:195],v[164:167],v[8:11]
TR a[148:149],v232 offset:25088
TR a[150:151],v232 offset:29184
MM v[16:19],a[192:195],v[168:171],v[136:139]
EX v5,v5
MM v[16:19],a[196:199],v[184:187],v[16:19]
TR a[152:153],v233 offset:16896
CV v2,v4,v5
MM v[16:19],a[200:203],v[200:203],v[16:19]
EX v6,v6
MM v[16:19],a[204:207],v[216:219],v[16:19]
TR a[154:155],v233 offset:20992
TR a[156:157],v233 offset:25088
MM v[20:23],a[220:223],v[216:219],v[136:139]
s_cmp_lg_u32 s70, s82
s_cbranch_scc1 LWW_%=
v_lshlrev_b32 v96, 3, v248
v_min_u32 v96, 4992, v96
s_sub_u32 s91, 5242880, s26
s_sub_u32 s60, s24, s91
s_subb_u32 s61, s25, 0
s_mov_b32 s62, 5242880
s_mov_b32 s63, 131072
s_lshl_b32 s90, s16, 2
s_add_u32 s90, s90, %[swave]
s_mul_i32 s90, s90, 10240
s_add_u32 s90, s90, 0
s_add_u32 s91, %[slds], 135168
s_mov_b32 m0, s91
s_nop 0
buffer_load_dword v96, s[60:63], s90 offen offset:0 lds
LWW_%=:
MM v[20:23],a[216:219],v[200:203],v[20:23]
EX v7,v7
MM v[20:23],a[212:215],v[184:187],v[20:23]
TR a[158:159],v233 offset:29184
CV v3,v6,v7
MM v[20:23],a[208:211],v[168:171],v[20:23]
EX v8,v8
MM v[28:31],a[208:211],v[172:175],v[140:143]
EX v9,v9
MM v[28:31],a[212:215],v[188:191],v[28:31]
TR a[160:161],v232 offset:17408
CV v8,v8,v9
MM v[28:31],a[216:219],v[204:207],v[28:31]
EX v10,v10
MM v[28:31],a[220:223],v[220:223],v[28:31]
EX v11,v11
MM v[24:27],a[204:207],v[220:223],v[140:143]
TR a[162:163],v232 offset:21504
CV v9,v10,v11
MM v[24:27],a[200:203],v[204:207],v[24:27]
TR a[164:165],v232 offset:25600
TR a[166:167],v232 offset:29696
MM v[24:27],a[196:199],v[188:191],v[24:27]
EX v12,v12
MM v[24:27],a[192:195],v[172:175],v[24:27]
EX v13,v13
MM v[32:35],a[224:227],v[160:163],v[128:131]
TR a[168:169],v233 offset:17408
CV v10,v12,v13
MM v[32:35],a[228:231],v[176:179],v[32:35]
EX v14,v14
MM v[32:35],a[232:235],v[192:195],v[32:35]
TR a[170:171],v233 offset:21504
TR a[172:173],v233 offset:25600
MM v[32:35],a[236:239],v[208:211],v[32:35]
EX v15,v15
MM v[36:39],a[252:255],v[208:211],v[128:131]
TR a[174:175],v233 offset:29696
CV v11,v14,v15
MM v[36:39],a[248:251],v[192:195],v[36:39]
EX v16,v16
MM v[36:39],a[244:247],v[176:179],v[36:39]
EX v17,v17
MM v[36:39],a[240:243],v[160:163],v[36:39]
TR a[176:177],v232 offset:17920
CV v16,v16,v17
MM v[44:47],a[240:243],v[164:167],v[132:135]
EX v18,v18
MM v[44:47],a[244:247],v[180:183],v[44:47]
TR a[178:179],v232 offset:22016
TR a[180:181],v232 offset:26112
MM v[44:47],a[248:251],v[196:199],v[44:47]
EX v19,v19
MM v[44:47],a[252:255],v[212:215],v[44:47]
TR a[182:183],v232 offset:30208
CV v17,v18,v19
MM v[40:43],a[236:239],v[212:215],v[132:135]
EX v20,v20
MM v[40:43],a[232:235],v[196:199],v[40:43]
)ASM"
R"ASM(
EX v21,v21
MM v[40:43],a[228:231],v[180:183],v[40:43]
TR a[184:185],v233 offset:17920
CV v18,v20,v21
MM v[40:43],a[224:227],v[164:167],v[40:43]
EX v22,v22
MM v[48:51],a[224:227],v[168:171],v[136:139]
TR a[186:187],v233 offset:22016
LPG7_%=:
s_cbranch_execz LPG11_%=
TR a[188:189],v233 offset:26112
MM v[48:51],a[228:231],v[184:187],v[48:51]
EX v23,v23
MM v[48:51],a[232:235],v[200:203],v[48:51]
TR a[190:191],v233 offset:30208
CV v19,v22,v23
MM v[48:51],a[236:239],v[216:219],v[48:51]
EX v24,v24
MM v[52:55],a[252:255],v[216:219],v[136:139]
EX v25,v25
MM v[52:55],a[248:251],v[200:203],v[52:55]
EX v26,v26
MM v[52:55],a[244:247],v[184:187],v[52:55]
EX v27,v27
MM v[52:55],a[240:243],v[168:171],v[52:55]
EX v28,v28
MM v[60:63],a[240:243],v[172:175],v[140:143]
EX v29,v29
MM v[60:63],a[244:247],v[188:191],v[60:63]
EX v30,v30
MM v[60:63],a[248:251],v[204:207],v[60:63]
EX v31,v31
MM v[60:63],a[252:255],v[220:223],v[60:63]
EX v32,v32
MM v[56:59],a[236:239],v[220:223],v[140:143]
EX v33,v33
MM v[56:59],a[232:235],v[204:207],v[56:59]
EX v34,v34
MM v[56:59],a[228:231],v[188:191],v[56:59]
EX v35,v35
MM v[56:59],a[224:227],v[172:175],v[56:59]
s_waitcnt vmcnt(12) lgkmcnt(0)
s_barrier
MM a[0:3],a[128:131],v[64:67],a[0:3]
s_add_u32 m0, s65, 16384
CV v24,v24,v25
CV v25,v26,v27
MM a[0:3],a[132:135],v[68:71],a[0:3]
RD a[192:195],v224 offset:49152
CV v26,v28,v29
MM a[8:11],a[132:135],v[76:79],a[8:11]
RD a[196:199],v225 offset:49152
CV v27,v30,v31
MM a[8:11],a[128:131],v[72:75],a[8:11]
buffer_load_dwordx4 v228, s[8:11], s64 offen offset:0 lds
MM a[16:19],a[128:131],v[80:83],a[16:19]
RD a[200:203],v226 offset:49152
CV v4,v32,v33
MM a[16:19],a[132:135],v[84:87],a[16:19]
RD a[204:207],v227 offset:49152
CV v5,v34,v35
MM a[24:27],a[132:135],v[92:95],a[24:27]
buffer_load_dwordx4 v229, s[8:11], s64 offen offset:1024 lds
MM a[24:27],a[128:131],v[88:91],a[24:27]
RD a[208:211],v224 offset:53248
RD a[212:215],v225 offset:53248
MM a[28:31],a[136:139],v[88:91],a[28:31]
buffer_load_dwordx4 v230, s[8:11], s64 offen offset:2048 lds
MM a[28:31],a[140:143],v[92:95],a[28:31]
EX v36,v36
MM a[20:23],a[140:143],v[84:87],a[20:23]
EX v37,v37
MM a[20:23],a[136:139],v[80:83],a[20:23]
RD a[216:219],v226 offset:53248
CV v6,v36,v37
MM a[12:15],a[136:139],v[72:75],a[12:15]
EX v38,v38
MM a[12:15],a[140:143],v[76:79],a[12:15]
RD a[220:223],v227 offset:53248
RD a[224:227],v224 offset:57344
MM a[4:7],a[140:143],v[68:71],a[4:7]
EX v39,v39
MM a[4:7],a[136:139],v[64:67],a[4:7]
RD a[228:231],v225 offset:57344
CV v7,v38,v39
MM a[32:35],a[144:147],v[64:67],a[32:35]
EX v40,v40
MM a[32:35],a[148:151],v[68:71],a[32:35]
buffer_load_dwordx4 v231, s[8:11], s64 offen offset:3072 lds
MM a[40:43],a[148:151],v[76:79],a[40:43]
s_add_u32 m0, s65, 81920
EX v41,v41
MM a[40:43],a[144:147],v[72:75],a[40:43]
RD a[232:235],v226 offset:57344
CV v12,v40,v41
MM a[48:51],a[144:147],v[80:83],a[48:51]
EX v42,v42
MM a[48:51],a[148:151],v[84:87],a[48:51]
EX v43,v43
MM a[56:59],a[148:151],v[92:95],a[56:59]
RD a[236:239],v227 offset:57344
CV v13,v42,v43
MM a[56:59],a[144:147],v[88:91],a[56:59]
EX v44,v44
MM a[60:63],a[152:155],v[88:91],a[60:63]
buffer_load_dwordx4 v235, s[12:15], s81 offen offset:0 lds
MM a[60:63],a[156:159],v[92:95],a[60:63]
EX v45,v45
MM a[52:55],a[156:159],v[84:87],a[52:55]
RD a[240:243],v224 offset:61440
CV v14,v44,v45
MM a[52:55],a[152:155],v[80:83],a[52:55]
EX v46,v46
MM a[44:47],a[152:155],v[72:75],a[44:47]
EX v47,v47
MM a[44:47],a[156:159],v[76:79],a[44:47]
RD a[244:247],v225 offset:61440
CV v15,v46,v47
MM a[36:39],a[156:159],v[68:71],a[36:39]
buffer_load_dwordx4 v235, s[12:15], s64 offen offset:1024 lds
MM a[36:39],a[152:155],v[64:67],a[36:39]
EX v48,v48
MM a[64:67],a[160:163],v[64:67],a[64:67]
EX v49,v49
MM a[64:67],a[164:167],v[68:71],a[64:67]
RD a[248:251],v226 offset:61440
CV v20,v48,v49
MM a[72:75],a[164:167],v[76:79],a[72:75]
EX v50,v50
MM a[72:75],a[160:163],v[72:75],a[72:75]
EX v51,v51
MM a[80:83],a[160:163],v[80:83],a[80:83]
RD a[252:255],v227 offset:61440
CV v21,v50,v51
MM a[80:83],a[164:167],v[84:87],a[80:83]
buffer_load_dwordx4 v235, s[12:15], s81 offen offset:2048 lds
MM a[88:91],a[164:167],v[92:95],a[88:91]
EX v52,v52
MM a[88:91],a[160:163],v[88:91],a[88:91]
EX v53,v53
MM a[92:95],a[168:171],v[88:91],a[92:95]
EX v54,v54
MM a[92:95],a[172:175],v[92:95],a[92:95]
buffer_load_dwordx4 v235, s[12:15], s64 offen offset:3072 lds
MM a[84:87],a[172:175],v[84:87],a[84:87]
EX v55,v55
MM a[84:87],a[168:171],v[80:83],a[84:87]
EX v56,v56
MM a[76:79],a[168:171],v[72:75],a[76:79]
EX v57,v57
MM a[76:79],a[172:175],v[76:79],a[76:79]
EX v58,v58
MM a[68:71],a[172:175],v[68:71],a[68:71]
EX v59,v59
MM a[68:71],a[168:171],v[64:67],a[68:71]
EX v60,v60
MM a[96:99],a[176:179],v[64:67],a[96:99]
EX v61,v61
MM a[96:99],a[180:183],v[68:71],a[96:99]
EX v62,v62
MM a[104:107],a[180:183],v[76:79],a[104:107]
EX v63,v63
MM a[104:107],a[176:179],v[72:75],a[104:107]
CV v22,v52,v53
CV v23,v54,v55
MM a[112:115],a[176:179],v[80:83],a[112:115]
CV v28,v56,v57
CV v29,v58,v59
MM a[112:115],a[180:183],v[84:87],a[112:115]
CV v30,v60,v61
CV v31,v62,v63
MM a[120:123],a[180:183],v[92:95],a[120:123]
MM a[120:123],a[176:179],v[88:91],a[120:123]
MM a[124:127],a[184:187],v[88:91],a[124:127]
v_add_f32 v244, v144, v145
v_add_f32 v245, v148, v149
MM a[124:127],a[188:191],v[92:95],a[124:127]
v_add_f32 v246, v152, v153
v_add_f32 v247, v156, v157
MM a[116:119],a[188:191],v[84:87],a[116:119]
MM a[116:119],a[184:187],v[80:83],a[116:119]
MM a[108:111],a[184:187],v[72:75],a[108:111]
MM a[108:111],a[188:191],v[76:79],a[108:111]
MM a[100:103],a[188:191],v[68:71],a[100:103]
MM a[100:103],a[184:187],v[64:67],a[100:103]
v_smfmac_f32_16x16x64_bf16 v[144:147], v[236:239], v[0:7], v234
v_smfmac_f32_16x16x64_bf16 v[148:151], v[236:239], v[8:15], v234
v_smfmac_f32_16x16x64_bf16 v[152:155], v[236:239], v[16:23], v234
v_smfmac_f32_16x16x64_bf16 v[156:159], v[236:239], v[24:31], v234
s_waitcnt lgkmcnt(0)
MM v[64:67],a[192:195],v[160:163],v[128:131]
s_add_u32 s64, s64, 16384
TR a[128:129],v232 offset:32768
s_add_u32 s81, s64, 896
MM v[64:67],a[196:199],v[176:179],v[64:67]
TR a[130:131],v232 offset:36864
v_max3_f32 v249, v144, v145, v148
MM v[64:67],a[200:203],v[192:195],v[64:67]
TR a[132:133],v232 offset:40960
v_max3_f32 v249, v249, v149, v152
MM v[64:67],a[204:207],v[208:211],v[64:67]
TR a[134:135],v232 offset:45056
v_max3_f32 v249, v249, v153, v156
MM v[68:71],a[220:223],v[208:211],v[128:131]
TR a[136:137],v233 offset:32768
v_max_f32 v249, v249, v157
MM v[68:71],a[216:219],v[192:195],v[68:71]
TR a[138:139],v233 offset:36864
v_cmp_lt_f32 vcc, s71, v249
MM v[68:71],a[212:215],v[176:179],v[68:71]
TR a[140:141],v233 offset:40960
TR a[142:143],v233 offset:45056
MM v[68:71],a[208:211],v[160:163],v[68:71]
s_cbranch_vccnz LSITE6_%=
LBACK6_%=:
MM v[76:79],a[208:211],v[164:167],v[132:135]
EX v64,v64
MM v[76:79],a[212:215],v[180:183],v[76:79]
EX v65,v65
MM v[76:79],a[216:219],v[196:199],v[76:79]
TR a[144:145],v232 offset:33280
CV v64,v64,v65
MM v[76:79],a[220:223],v[212:215],v[76:79]
EX v66,v66
MM v[72:75],a[204:207],v[212:215],v[132:135]
EX v67,v67
MM v[72:75],a[200:203],v[196:199],v[72:75]
TR a[146:147],v232 offset:37376
CV v65,v66,v67
MM v[72:75],a[196:199],v[180:183],v[72:75]
EX v68,v68
MM v[72:75],a[192:195],v[164:167],v[72:75]
TR a[148:149],v232 offset:41472
TR a[150:151],v232 offset:45568
MM v[80:83],a[192:195],v[168:171],v[136:139]
EX v69,v69
MM v[80:83],a[196:199],v[184:187],v[80:83]
TR a[152:153],v233 offset:33280
CV v66,v68,v69
MM v[80:83],a[200:203],v[200:203],v[80:83]
EX v70,v70
MM v[80:83],a[204:207],v[216:219],v[80:83]
TR a[154:155],v233 offset:37376
TR a[156:157],v233 offset:41472
MM v[84:87],a[220:223],v[216:219],v[136:139]
EX v71,v71
MM v[84:87],a[216:219],v[200:203],v[84:87]
TR a[158:159],v233 offset:45568
CV v67,v70,v71
MM v[84:87],a[212:215],v[184:187],v[84:87]
EX v72,v72
MM v[84:87],a[208:211],v[168:171],v[84:87]
EX v73,v73
MM v[92:95],a[208:211],v[172:175],v[140:143]
TR a[160:161],v232 offset:33792
CV v72,v72,v73
MM v[92:95],a[212:215],v[188:191],v[92:95]
EX v74,v74
MM v[92:95],a[216:219],v[204:207],v[92:95]
TR a[162:163],v232 offset:37888
TR a[164:165],v232 offset:41984
MM v[92:95],a[220:223],v[220:223],v[92:95]
EX v75,v75
MM v[88:91],a[204:207],v[220:223],v[140:143]
TR a[166:167],v232 offset:46080
CV v73,v74,v75
MM v[88:91],a[200:203],v[204:207],v[88:91]
EX v76,v76
MM v[88:91],a[196:199],v[188:191],v[88:91]
EX v77,v77
MM v[88:91],a[192:195],v[172:175],v[88:91]
TR a[168:169],v233 offset:33792
CV v74,v76,v77
MM v[96:99],a[224:227],v[160:163],v[128:131]
EX v78,v78
MM v[96:99],a[228:231],v[176:179],v[96:99]
TR a[170:171],v233 offset:37888
TR a[172:173],v233 offset:41984
MM v[96:99],a[232:235],v[192:195],v[96:99]
EX v79,v79
MM v[96:99],a[236:239],v[208:211],v[96:99]
TR a[174:175],v233 offset:46080
CV v75,v78,v79
MM v[100:103],a[252:255],v[208:211],v[128:131]
EX v80,v80
MM v[100:103],a[248:251],v[192:195],v[100:103]
EX v81,v81
MM v[100:103],a[244:247],v[176:179],v[100:103]
TR a[176:177],v232 offset:34304
CV v80,v80,v81
MM v[100:103],a[240:243],v[160:163],v[100:103]
EX v82,v82
MM v[108:111],a[240:243],v[164:167],v[132:135]
buffer_store_dword v240, v248, s[20:23], s67 offen offset:12 sc1
MM v[108:111],a[244:247],v[180:183],v[108:111]
s_add_u32 s67, s67, 1024
TR a[178:179],v232 offset:38400
TR a[180:181],v232 offset:42496
MM v[108:111],a[248:251],v[196:199],v[108:111]
EX v83,v83
MM v[108:111],a[252:255],v[212:215],v[108:111]
TR a[182:183],v232 offset:46592
CV v81,v82,v83
MM v[104:107],a[236:239],v[212:215],v[132:135]
EX v84,v84
MM v[104:107],a[232:235],v[196:199],v[104:107]
EX v85,v85
MM v[104:107],a[228:231],v[180:183],v[104:107]
TR a[184:185],v233 offset:34304
CV v82,v84,v85
MM v[104:107],a[224:227],v[164:167],v[104:107]
EX v86,v86
MM v[112:115],a[224:227],v[168:171],v[136:139]
TR a[186:187],v233 offset:38400
TR a[188:189],v233 offset:42496
MM v[112:115],a[228:231],v[184:187],v[112:115]
EX v87,v87
MM v[112:115],a[232:235],v[200:203],v[112:115]
TR a[190:191],v233 offset:46592
CV v83,v86,v87
MM v[112:115],a[236:239],v[216:219],v[112:115]
EX v88,v88
MM v[116:119],a[252:255],v[216:219],v[136:139]
EX v89,v89
MM v[116:119],a[248:251],v[200:203],v[116:119]
EX v90,v90
MM v[116:119],a[244:247],v[184:187],v[116:119]
EX v91,v91
MM v[116:119],a[240:243],v[168:171],v[116:119]
EX v92,v92
MM v[124:127],a[240:243],v[172:175],v[140:143]
EX v93,v93
MM v[124:127],a[244:247],v[188:191],v[124:127]
EX v94,v94
MM v[124:127],a[248:251],v[204:207],v[124:127]
EX v95,v95
MM v[124:127],a[252:255],v[220:223],v[124:127]
EX v96,v96
MM v[120:123],a[236:239],v[220:223],v[140:143]
EX v97,v97
MM v[120:123],a[232:235],v[204:207],v[120:123]
EX v98,v98
MM v[120:123],a[228:231],v[188:191],v[120:123]
EX v99,v99
MM v[120:123],a[224:227],v[172:175],v[120:123]
s_waitcnt vmcnt(12) lgkmcnt(0)
s_barrier
MM a[0:3],a[128:131],v[0:3],a[0:3]
s_add_u32 m0, s65, 32768
CV v88,v88,v89
CV v89,v90,v91
MM a[0:3],a[132:135],v[4:7],a[0:3]
RD a[192:195],v224 offset:0
CV v90,v92,v93
MM a[8:11],a[132:135],v[12:15],a[8:11]
RD a[196:199],v225 offset:0
CV v91,v94,v95
MM a[8:11],a[128:131],v[8:11],a[8:11]
buffer_load_dwordx4 v228, s[8:11], s64 offen offset:0 lds
MM a[16:19],a[128:131],v[16:19],a[16:19]
RD a[200:203],v226 offset:0
CV v68,v96,v97
MM a[16:19],a[132:135],v[20:23],a[16:19]
RD a[204:207],v227 offset:0
CV v69,v98,v99
MM a[24:27],a[132:135],v[28:31],a[24:27]
buffer_load_dwordx4 v229, s[8:11], s64 offen offset:1024 lds
MM a[24:27],a[128:131],v[24:27],a[24:27]
RD a[208:211],v224 offset:4096
RD a[212:215],v225 offset:4096
MM a[28:31],a[136:139],v[24:27],a[28:31]
buffer_load_dwordx4 v230, s[8:11], s64 offen offset:2048 lds
MM a[28:31],a[140:143],v[28:31],a[28:31]
EX v100,v100
MM a[20:23],a[140:143],v[20:23],a[20:23]
EX v101,v101
MM a[20:23],a[136:139],v[16:19],a[20:23]
RD a[216:219],v226 offset:4096
CV v70,v100,v101
MM a[12:15],a[136:139],v[8:11],a[12:15]
EX v102,v102
MM a[12:15],a[140:143],v[12:15],a[12:15]
RD a[220:223],v227 offset:4096
RD a[224:227],v224 offset:8192
MM a[4:7],a[140:143],v[4:7],a[4:7]
EX v103,v103
MM a[4:7],a[136:139],v[0:3],a[4:7]
RD a[228:231],v225 offset:8192
)ASM"
R"ASM(
CV v71,v102,v103
MM a[32:35],a[144:147],v[0:3],a[32:35]
EX v104,v104
MM a[32:35],a[148:151],v[4:7],a[32:35]
buffer_load_dwordx4 v231, s[8:11], s64 offen offset:3072 lds
MM a[40:43],a[148:151],v[12:15],a[40:43]
s_add_u32 m0, s65, 98304
EX v105,v105
MM a[40:43],a[144:147],v[8:11],a[40:43]
RD a[232:235],v226 offset:8192
CV v76,v104,v105
MM a[48:51],a[144:147],v[16:19],a[48:51]
EX v106,v106
MM a[48:51],a[148:151],v[20:23],a[48:51]
EX v107,v107
MM a[56:59],a[148:151],v[28:31],a[56:59]
RD a[236:239],v227 offset:8192
CV v77,v106,v107
MM a[56:59],a[144:147],v[24:27],a[56:59]
EX v108,v108
MM a[60:63],a[152:155],v[24:27],a[60:63]
buffer_load_dwordx4 v235, s[12:15], s81 offen offset:0 lds
MM a[60:63],a[156:159],v[28:31],a[60:63]
EX v109,v109
MM a[52:55],a[156:159],v[20:23],a[52:55]
RD a[240:243],v224 offset:12288
CV v78,v108,v109
MM a[52:55],a[152:155],v[16:19],a[52:55]
EX v110,v110
MM a[44:47],a[152:155],v[8:11],a[44:47]
EX v111,v111
MM a[44:47],a[156:159],v[12:15],a[44:47]
RD a[244:247],v225 offset:12288
CV v79,v110,v111
MM a[36:39],a[156:159],v[4:7],a[36:39]
buffer_load_dwordx4 v235, s[12:15], s64 offen offset:1024 lds
MM a[36:39],a[152:155],v[0:3],a[36:39]
EX v112,v112
MM a[64:67],a[160:163],v[0:3],a[64:67]
EX v113,v113
MM a[64:67],a[164:167],v[4:7],a[64:67]
RD a[248:251],v226 offset:12288
CV v84,v112,v113
MM a[72:75],a[164:167],v[12:15],a[72:75]
EX v114,v114
MM a[72:75],a[160:163],v[8:11],a[72:75]
EX v115,v115
MM a[80:83],a[160:163],v[16:19],a[80:83]
RD a[252:255],v227 offset:12288
CV v85,v114,v115
MM a[80:83],a[164:167],v[20:23],a[80:83]
buffer_load_dwordx4 v235, s[12:15], s81 offen offset:2048 lds
MM a[88:91],a[164:167],v[28:31],a[88:91]
EX v116,v116
MM a[88:91],a[160:163],v[24:27],a[88:91]
EX v117,v117
MM a[92:95],a[168:171],v[24:27],a[92:95]
EX v118,v118
MM a[92:95],a[172:175],v[28:31],a[92:95]
buffer_load_dwordx4 v235, s[12:15], s64 offen offset:3072 lds
MM a[84:87],a[172:175],v[20:23],a[84:87]
EX v119,v119
MM a[84:87],a[168:171],v[16:19],a[84:87]
EX v120,v120
MM a[76:79],a[168:171],v[8:11],a[76:79]
EX v121,v121
MM a[76:79],a[172:175],v[12:15],a[76:79]
EX v122,v122
MM a[68:71],a[172:175],v[4:7],a[68:71]
EX v123,v123
MM a[68:71],a[168:171],v[0:3],a[68:71]
EX v124,v124
MM a[96:99],a[176:179],v[0:3],a[96:99]
EX v125,v125
MM a[96:99],a[180:183],v[4:7],a[96:99]
EX v126,v126
MM a[104:107],a[180:183],v[12:15],a[104:107]
EX v127,v127
MM a[104:107],a[176:179],v[8:11],a[104:107]
CV v86,v116,v117
CV v87,v118,v119
MM a[112:115],a[176:179],v[16:19],a[112:115]
CV v92,v120,v121
CV v93,v122,v123
MM a[112:115],a[180:183],v[20:23],a[112:115]
CV v94,v124,v125
CV v95,v126,v127
MM a[120:123],a[180:183],v[28:31],a[120:123]
MM a[120:123],a[176:179],v[24:27],a[120:123]
MM a[124:127],a[184:187],v[24:27],a[124:127]
v_add_f32 v244, v144, v145
v_add_f32 v245, v148, v149
MM a[124:127],a[188:191],v[28:31],a[124:127]
v_add_f32 v246, v152, v153
v_add_f32 v247, v156, v157
MM a[116:119],a[188:191],v[20:23],a[116:119]
MM a[116:119],a[184:187],v[16:19],a[116:119]
MM a[108:111],a[184:187],v[8:11],a[108:111]
MM a[108:111],a[188:191],v[12:15],a[108:111]
MM a[100:103],a[188:191],v[4:7],a[100:103]
MM a[100:103],a[184:187],v[0:3],a[100:103]
v_smfmac_f32_16x16x64_bf16 v[144:147], v[236:239], v[64:71], v234
v_smfmac_f32_16x16x64_bf16 v[148:151], v[236:239], v[72:79], v234
v_smfmac_f32_16x16x64_bf16 v[152:155], v[236:239], v[80:87], v234
v_smfmac_f32_16x16x64_bf16 v[156:159], v[236:239], v[88:95], v234
s_waitcnt lgkmcnt(0)
MM v[0:3],a[192:195],v[160:163],v[128:131]
s_add_u32 s64, s64, 16384
TR a[128:129],v232 offset:49152
s_add_u32 s81, s64, 896
MM v[0:3],a[196:199],v[176:179],v[0:3]
TR a[130:131],v232 offset:53248
v_max3_f32 v249, v144, v145, v148
MM v[0:3],a[200:203],v[192:195],v[0:3]
TR a[132:133],v232 offset:57344
v_max3_f32 v249, v249, v149, v152
MM v[0:3],a[204:207],v[208:211],v[0:3]
TR a[134:135],v232 offset:61440
v_max3_f32 v249, v249, v153, v156
MM v[4:7],a[220:223],v[208:211],v[128:131]
TR a[136:137],v233 offset:49152
v_max_f32 v249, v249, v157
MM v[4:7],a[216:219],v[192:195],v[4:7]
TR a[138:139],v233 offset:53248
v_cmp_lt_f32 vcc, s71, v249
MM v[4:7],a[212:215],v[176:179],v[4:7]
TR a[140:141],v233 offset:57344
TR a[142:143],v233 offset:61440
MM v[4:7],a[208:211],v[160:163],v[4:7]
s_cbranch_vccnz LSITE7_%=
LBACK7_%=:
MM v[12:15],a[208:211],v[164:167],v[132:135]
EX v0,v0
MM v[12:15],a[212:215],v[180:183],v[12:15]
EX v1,v1
MM v[12:15],a[216:219],v[196:199],v[12:15]
TR a[144:145],v232 offset:49664
CV v0,v0,v1
MM v[12:15],a[220:223],v[212:215],v[12:15]
EX v2,v2
MM v[8:11],a[204:207],v[212:215],v[132:135]
EX v3,v3
MM v[8:11],a[200:203],v[196:199],v[8:11]
TR a[146:147],v232 offset:53760
CV v1,v2,v3
MM v[8:11],a[196:199],v[180:183],v[8:11]
EX v4,v4
MM v[8:11],a[192:195],v[164:167],v[8:11]
TR a[148:149],v232 offset:57856
TR a[150:151],v232 offset:61952
MM v[16:19],a[192:195],v[168:171],v[136:139]
EX v5,v5
MM v[16:19],a[196:199],v[184:187],v[16:19]
TR a[152:153],v233 offset:49664
CV v2,v4,v5
MM v[16:19],a[200:203],v[200:203],v[16:19]
EX v6,v6
MM v[16:19],a[204:207],v[216:219],v[16:19]
TR a[154:155],v233 offset:53760
TR a[156:157],v233 offset:57856
MM v[20:23],a[220:223],v[216:219],v[136:139]
s_cmp_lg_u32 s70, 2
s_cbranch_scc1 LIP_%=
s_lshr_b32 s77, %[sb], 3
s_lshl_b32 s77, s77, 2
s_add_u32 s77, s77, %[swave]
s_lshl_b32 s77, s77, 9
s_add_u32 s77, s77, LLAJ_%=-LPDP_%=
v_lshrrev_b32 v253, 1, v248
v_add_u32 v253, s77, v253
s_mov_b32 s77, LKEND_ka-LPDP_%=-16
v_min_u32 v253, s77, v253
s_getpc_b64 s[98:99]
LPDP_%=:
global_load_dword v253, v253, s[98:99]
LIP_%=:
MM v[20:23],a[216:219],v[200:203],v[20:23]
EX v7,v7
MM v[20:23],a[212:215],v[184:187],v[20:23]
TR a[158:159],v233 offset:61952
CV v3,v6,v7
MM v[20:23],a[208:211],v[168:171],v[20:23]
EX v8,v8
MM v[28:31],a[208:211],v[172:175],v[140:143]
EX v9,v9
MM v[28:31],a[212:215],v[188:191],v[28:31]
TR a[160:161],v232 offset:50176
CV v8,v8,v9
MM v[28:31],a[216:219],v[204:207],v[28:31]
EX v10,v10
MM v[28:31],a[220:223],v[220:223],v[28:31]
EX v11,v11
MM v[24:27],a[204:207],v[220:223],v[140:143]
TR a[162:163],v232 offset:54272
CV v9,v10,v11
MM v[24:27],a[200:203],v[204:207],v[24:27]
TR a[164:165],v232 offset:58368
TR a[166:167],v232 offset:62464
MM v[24:27],a[196:199],v[188:191],v[24:27]
EX v12,v12
MM v[24:27],a[192:195],v[172:175],v[24:27]
EX v13,v13
MM v[32:35],a[224:227],v[160:163],v[128:131]
TR a[168:169],v233 offset:50176
CV v10,v12,v13
MM v[32:35],a[228:231],v[176:179],v[32:35]
EX v14,v14
MM v[32:35],a[232:235],v[192:195],v[32:35]
TR a[170:171],v233 offset:54272
TR a[172:173],v233 offset:58368
MM v[32:35],a[236:239],v[208:211],v[32:35]
EX v15,v15
MM v[36:39],a[252:255],v[208:211],v[128:131]
LPG8_%=:
s_cbranch_execz LPG12_%=
TR a[174:175],v233 offset:62464
CV v11,v14,v15
MM v[36:39],a[248:251],v[192:195],v[36:39]
EX v16,v16
MM v[36:39],a[244:247],v[176:179],v[36:39]
EX v17,v17
MM v[36:39],a[240:243],v[160:163],v[36:39]
TR a[176:177],v232 offset:50688
CV v16,v16,v17
MM v[44:47],a[240:243],v[164:167],v[132:135]
EX v18,v18
MM v[44:47],a[244:247],v[180:183],v[44:47]
TR a[178:179],v232 offset:54784
TR a[180:181],v232 offset:58880
MM v[44:47],a[248:251],v[196:199],v[44:47]
EX v19,v19
MM v[44:47],a[252:255],v[212:215],v[44:47]
TR a[182:183],v232 offset:62976
CV v17,v18,v19
MM v[40:43],a[236:239],v[212:215],v[132:135]
EX v20,v20
MM v[40:43],a[232:235],v[196:199],v[40:43]
EX v21,v21
MM v[40:43],a[228:231],v[180:183],v[40:43]
TR a[184:185],v233 offset:50688
CV v18,v20,v21
MM v[40:43],a[224:227],v[164:167],v[40:43]
EX v22,v22
MM v[48:51],a[224:227],v[168:171],v[136:139]
TR a[186:187],v233 offset:54784
TR a[188:189],v233 offset:58880
MM v[48:51],a[228:231],v[184:187],v[48:51]
EX v23,v23
MM v[48:51],a[232:235],v[200:203],v[48:51]
TR a[190:191],v233 offset:62976
CV v19,v22,v23
MM v[48:51],a[236:239],v[216:219],v[48:51]
EX v24,v24
MM v[52:55],a[252:255],v[216:219],v[136:139]
EX v25,v25
MM v[52:55],a[248:251],v[200:203],v[52:55]
EX v26,v26
MM v[52:55],a[244:247],v[184:187],v[52:55]
EX v27,v27
MM v[52:55],a[240:243],v[168:171],v[52:55]
EX v28,v28
MM v[60:63],a[240:243],v[172:175],v[140:143]
EX v29,v29
MM v[60:63],a[244:247],v[188:191],v[60:63]
EX v30,v30
MM v[60:63],a[248:251],v[204:207],v[60:63]
EX v31,v31
MM v[60:63],a[252:255],v[220:223],v[60:63]
EX v32,v32
MM v[56:59],a[236:239],v[220:223],v[140:143]
EX v33,v33
MM v[56:59],a[232:235],v[204:207],v[56:59]
EX v34,v34
MM v[56:59],a[228:231],v[188:191],v[56:59]
EX v35,v35
MM v[56:59],a[224:227],v[172:175],v[56:59]
s_waitcnt vmcnt(12) lgkmcnt(0)
s_barrier
MM a[0:3],a[128:131],v[64:67],a[0:3]
s_add_u32 m0, s65, 49152
CV v24,v24,v25
CV v25,v26,v27
MM a[0:3],a[132:135],v[68:71],a[0:3]
RD a[192:195],v224 offset:16384
CV v26,v28,v29
MM a[8:11],a[132:135],v[76:79],a[8:11]
RD a[196:199],v225 offset:16384
CV v27,v30,v31
MM a[8:11],a[128:131],v[72:75],a[8:11]
buffer_load_dwordx4 v228, s[8:11], s64 offen offset:0 lds
MM a[16:19],a[128:131],v[80:83],a[16:19]
RD a[200:203],v226 offset:16384
CV v4,v32,v33
MM a[16:19],a[132:135],v[84:87],a[16:19]
RD a[204:207],v227 offset:16384
CV v5,v34,v35
MM a[24:27],a[132:135],v[92:95],a[24:27]
buffer_load_dwordx4 v229, s[8:11], s64 offen offset:1024 lds
MM a[24:27],a[128:131],v[88:91],a[24:27]
RD a[208:211],v224 offset:20480
RD a[212:215],v225 offset:20480
MM a[28:31],a[136:139],v[88:91],a[28:31]
buffer_load_dwordx4 v230, s[8:11], s64 offen offset:2048 lds
MM a[28:31],a[140:143],v[92:95],a[28:31]
EX v36,v36
MM a[20:23],a[140:143],v[84:87],a[20:23]
EX v37,v37
MM a[20:23],a[136:139],v[80:83],a[20:23]
RD a[216:219],v226 offset:20480
CV v6,v36,v37
MM a[12:15],a[136:139],v[72:75],a[12:15]
EX v38,v38
MM a[12:15],a[140:143],v[76:79],a[12:15]
RD a[220:223],v227 offset:20480
RD a[224:227],v224 offset:24576
MM a[4:7],a[140:143],v[68:71],a[4:7]
EX v39,v39
MM a[4:7],a[136:139],v[64:67],a[4:7]
RD a[228:231],v225 offset:24576
CV v7,v38,v39
MM a[32:35],a[144:147],v[64:67],a[32:35]
EX v40,v40
MM a[32:35],a[148:151],v[68:71],a[32:35]
buffer_load_dwordx4 v231, s[8:11], s64 offen offset:3072 lds
MM a[40:43],a[148:151],v[76:79],a[40:43]
s_add_u32 m0, s65, 114688
EX v41,v41
MM a[40:43],a[144:147],v[72:75],a[40:43]
RD a[232:235],v226 offset:24576
CV v12,v40,v41
MM a[48:51],a[144:147],v[80:83],a[48:51]
EX v42,v42
MM a[48:51],a[148:151],v[84:87],a[48:51]
EX v43,v43
MM a[56:59],a[148:151],v[92:95],a[56:59]
RD a[236:239],v227 offset:24576
CV v13,v42,v43
MM a[56:59],a[144:147],v[88:91],a[56:59]
EX v44,v44
MM a[60:63],a[152:155],v[88:91],a[60:63]
buffer_load_dwordx4 v235, s[12:15], s81 offen offset:0 lds
MM a[60:63],a[156:159],v[92:95],a[60:63]
EX v45,v45
MM a[52:55],a[156:159],v[84:87],a[52:55]
RD a[240:243],v224 offset:28672
CV v14,v44,v45
MM a[52:55],a[152:155],v[80:83],a[52:55]
EX v46,v46
MM a[44:47],a[152:155],v[72:75],a[44:47]
EX v47,v47
MM a[44:47],a[156:159],v[76:79],a[44:47]
RD a[244:247],v225 offset:28672
CV v15,v46,v47
MM a[36:39],a[156:159],v[68:71],a[36:39]
buffer_load_dwordx4 v235, s[12:15], s64 offen offset:1024 lds
MM a[36:39],a[152:155],v[64:67],a[36:39]
EX v48,v48
MM a[64:67],a[160:163],v[64:67],a[64:67]
EX v49,v49
MM a[64:67],a[164:167],v[68:71],a[64:67]
RD a[248:251],v226 offset:28672
CV v20,v48,v49
MM a[72:75],a[164:167],v[76:79],a[72:75]
EX v50,v50
MM a[72:75],a[160:163],v[72:75],a[72:75]
EX v51,v51
MM a[80:83],a[160:163],v[80:83],a[80:83]
RD a[252:255],v227 offset:28672
CV v21,v50,v51
MM a[80:83],a[164:167],v[84:87],a[80:83]
buffer_load_dwordx4 v235, s[12:15], s81 offen offset:2048 lds
MM a[88:91],a[164:167],v[92:95],a[88:91]
EX v52,v52
MM a[88:91],a[160:163],v[88:91],a[88:91]
EX v53,v53
MM a[92:95],a[168:171],v[88:91],a[92:95]
EX v54,v54
MM a[92:95],a[172:175],v[92:95],a[92:95]
buffer_load_dwordx4 v235, s[12:15], s64 offen offset:3072 lds
MM a[84:87],a[172:175],v[84:87],a[84:87]
EX v55,v55
MM a[84:87],a[168:171],v[80:83],a[84:87]
EX v56,v56
MM a[76:79],a[168:171],v[72:75],a[76:79]
EX v57,v57
MM a[76:79],a[172:175],v[76:79],a[76:79]
EX v58,v58
MM a[68:71],a[172:175],v[68:71],a[68:71]
EX v59,v59
MM a[68:71],a[168:171],v[64:67],a[68:71]
EX v60,v60
MM a[96:99],a[176:179],v[64:67],a[96:99]
EX v61,v61
MM a[96:99],a[180:183],v[68:71],a[96:99]
)ASM"
R"ASM(
EX v62,v62
MM a[104:107],a[180:183],v[76:79],a[104:107]
EX v63,v63
MM a[104:107],a[176:179],v[72:75],a[104:107]
CV v22,v52,v53
CV v23,v54,v55
MM a[112:115],a[176:179],v[80:83],a[112:115]
CV v28,v56,v57
CV v29,v58,v59
MM a[112:115],a[180:183],v[84:87],a[112:115]
CV v30,v60,v61
CV v31,v62,v63
MM a[120:123],a[180:183],v[92:95],a[120:123]
MM a[120:123],a[176:179],v[88:91],a[120:123]
MM a[124:127],a[184:187],v[88:91],a[124:127]
v_add_f32 v244, v144, v145
v_add_f32 v245, v148, v149
MM a[124:127],a[188:191],v[92:95],a[124:127]
v_add_f32 v246, v152, v153
v_add_f32 v247, v156, v157
MM a[116:119],a[188:191],v[84:87],a[116:119]
MM a[116:119],a[184:187],v[80:83],a[116:119]
MM a[108:111],a[184:187],v[72:75],a[108:111]
MM a[108:111],a[188:191],v[76:79],a[108:111]
MM a[100:103],a[188:191],v[68:71],a[100:103]
MM a[100:103],a[184:187],v[64:67],a[100:103]
v_smfmac_f32_16x16x64_bf16 v[144:147], v[236:239], v[0:7], v234
v_smfmac_f32_16x16x64_bf16 v[148:151], v[236:239], v[8:15], v234
v_smfmac_f32_16x16x64_bf16 v[152:155], v[236:239], v[16:23], v234
v_smfmac_f32_16x16x64_bf16 v[156:159], v[236:239], v[24:31], v234
s_waitcnt lgkmcnt(0)
MM v[64:67],a[192:195],v[160:163],v[128:131]
s_add_u32 s64, s64, 16384
TR a[128:129],v232 offset:0
s_and_b32 s64, s64, 524287
MM v[64:67],a[196:199],v[176:179],v[64:67]
s_add_u32 s81, s64, 896
TR a[130:131],v232 offset:4096
v_max3_f32 v249, v144, v145, v148
MM v[64:67],a[200:203],v[192:195],v[64:67]
TR a[132:133],v232 offset:8192
v_max3_f32 v249, v249, v149, v152
MM v[64:67],a[204:207],v[208:211],v[64:67]
TR a[134:135],v232 offset:12288
v_max3_f32 v249, v249, v153, v156
MM v[68:71],a[220:223],v[208:211],v[128:131]
TR a[136:137],v233 offset:0
v_max_f32 v249, v249, v157
MM v[68:71],a[216:219],v[192:195],v[68:71]
TR a[138:139],v233 offset:4096
v_cmp_lt_f32 vcc, s71, v249
MM v[68:71],a[212:215],v[176:179],v[68:71]
TR a[140:141],v233 offset:8192
TR a[142:143],v233 offset:12288
MM v[68:71],a[208:211],v[160:163],v[68:71]
s_cbranch_vccnz LSITE8_%=
LBACK8_%=:
MM v[76:79],a[208:211],v[164:167],v[132:135]
EX v64,v64
MM v[76:79],a[212:215],v[180:183],v[76:79]
EX v65,v65
MM v[76:79],a[216:219],v[196:199],v[76:79]
TR a[144:145],v232 offset:512
CV v64,v64,v65
MM v[76:79],a[220:223],v[212:215],v[76:79]
EX v66,v66
MM v[72:75],a[204:207],v[212:215],v[132:135]
EX v67,v67
MM v[72:75],a[200:203],v[196:199],v[72:75]
TR a[146:147],v232 offset:4608
CV v65,v66,v67
MM v[72:75],a[196:199],v[180:183],v[72:75]
EX v68,v68
MM v[72:75],a[192:195],v[164:167],v[72:75]
TR a[148:149],v232 offset:8704
TR a[150:151],v232 offset:12800
MM v[80:83],a[192:195],v[168:171],v[136:139]
EX v69,v69
MM v[80:83],a[196:199],v[184:187],v[80:83]
TR a[152:153],v233 offset:512
CV v66,v68,v69
MM v[80:83],a[200:203],v[200:203],v[80:83]
EX v70,v70
MM v[80:83],a[204:207],v[216:219],v[80:83]
TR a[154:155],v233 offset:4608
TR a[156:157],v233 offset:8704
MM v[84:87],a[220:223],v[216:219],v[136:139]
s_cmp_lg_u32 s70, s82
s_cbranch_scc1 LWV_%=
v_lshlrev_b32 v32, 3, v248
v_min_u32 v32, 4992, v32
s_sub_u32 s91, 5242880, s26
s_sub_u32 s60, s24, s91
s_subb_u32 s61, s25, 0
s_mov_b32 s62, 5242880
s_mov_b32 s63, 131072
s_lshl_b32 s90, s16, 2
s_add_u32 s90, s90, %[swave]
s_mul_i32 s90, s90, 10240
s_add_u32 s90, s90, 5120
s_add_u32 s91, %[slds], 135168
s_mov_b32 m0, s91
s_nop 0
buffer_load_dword v32, s[60:63], s90 offen offset:0 lds
LWV_%=:
MM v[84:87],a[216:219],v[200:203],v[84:87]
EX v71,v71
MM v[84:87],a[212:215],v[184:187],v[84:87]
TR a[158:159],v233 offset:12800
CV v67,v70,v71
MM v[84:87],a[208:211],v[168:171],v[84:87]
EX v72,v72
MM v[92:95],a[208:211],v[172:175],v[140:143]
EX v73,v73
MM v[92:95],a[212:215],v[188:191],v[92:95]
TR a[160:161],v232 offset:1024
CV v72,v72,v73
MM v[92:95],a[216:219],v[204:207],v[92:95]
EX v74,v74
MM v[92:95],a[220:223],v[220:223],v[92:95]
EX v75,v75
MM v[88:91],a[204:207],v[220:223],v[140:143]
TR a[162:163],v232 offset:5120
CV v73,v74,v75
MM v[88:91],a[200:203],v[204:207],v[88:91]
TR a[164:165],v232 offset:9216
TR a[166:167],v232 offset:13312
MM v[88:91],a[196:199],v[188:191],v[88:91]
EX v76,v76
MM v[88:91],a[192:195],v[172:175],v[88:91]
EX v77,v77
MM v[96:99],a[224:227],v[160:163],v[128:131]
TR a[168:169],v233 offset:1024
CV v74,v76,v77
MM v[96:99],a[228:231],v[176:179],v[96:99]
EX v78,v78
MM v[96:99],a[232:235],v[192:195],v[96:99]
TR a[170:171],v233 offset:5120
TR a[172:173],v233 offset:9216
MM v[96:99],a[236:239],v[208:211],v[96:99]
EX v79,v79
MM v[100:103],a[252:255],v[208:211],v[128:131]
TR a[174:175],v233 offset:13312
CV v75,v78,v79
MM v[100:103],a[248:251],v[192:195],v[100:103]
EX v80,v80
MM v[100:103],a[244:247],v[176:179],v[100:103]
EX v81,v81
MM v[100:103],a[240:243],v[160:163],v[100:103]
TR a[176:177],v232 offset:1536
CV v80,v80,v81
MM v[108:111],a[240:243],v[164:167],v[132:135]
EX v82,v82
MM v[108:111],a[244:247],v[180:183],v[108:111]
TR a[178:179],v232 offset:5632
TR a[180:181],v232 offset:9728
MM v[108:111],a[248:251],v[196:199],v[108:111]
EX v83,v83
MM v[108:111],a[252:255],v[212:215],v[108:111]
TR a[182:183],v232 offset:13824
CV v81,v82,v83
MM v[104:107],a[236:239],v[212:215],v[132:135]
EX v84,v84
MM v[104:107],a[232:235],v[196:199],v[104:107]
EX v85,v85
MM v[104:107],a[228:231],v[180:183],v[104:107]
TR a[184:185],v233 offset:1536
CV v82,v84,v85
MM v[104:107],a[224:227],v[164:167],v[104:107]
EX v86,v86
MM v[112:115],a[224:227],v[168:171],v[136:139]
TR a[186:187],v233 offset:5632
TR a[188:189],v233 offset:9728
MM v[112:115],a[228:231],v[184:187],v[112:115]
EX v87,v87
MM v[112:115],a[232:235],v[200:203],v[112:115]
TR a[190:191],v233 offset:13824
CV v83,v86,v87
MM v[112:115],a[236:239],v[216:219],v[112:115]
EX v88,v88
MM v[116:119],a[252:255],v[216:219],v[136:139]
EX v89,v89
MM v[116:119],a[248:251],v[200:203],v[116:119]
EX v90,v90
MM v[116:119],a[244:247],v[184:187],v[116:119]
EX v91,v91
MM v[116:119],a[240:243],v[168:171],v[116:119]
EX v92,v92
MM v[124:127],a[240:243],v[172:175],v[140:143]
EX v93,v93
MM v[124:127],a[244:247],v[188:191],v[124:127]
EX v94,v94
MM v[124:127],a[248:251],v[204:207],v[124:127]
EX v95,v95
MM v[124:127],a[252:255],v[220:223],v[124:127]
EX v96,v96
MM v[120:123],a[236:239],v[220:223],v[140:143]
EX v97,v97
MM v[120:123],a[232:235],v[204:207],v[120:123]
EX v98,v98
MM v[120:123],a[228:231],v[188:191],v[120:123]
s_cmp_lg_u32 s70, 1
s_cbranch_scc1 LTP_%=
s_getpc_b64 s[98:99]
LTA1_%=:
s_min_u32 s73, %[swave], 3
s_lshl_b32 s73, s73, 12
s_add_u32 s73, s73, LPG23_%=-LTA1_%=
s_add_u32 s98, s98, s73
s_addc_u32 s99, s99, 0
s_mov_b64 exec, 0
s_setpc_b64 s[98:99]
LTR1_%=:
s_mov_b64 exec, -1
LTP_%=:
MM v[120:123],a[224:227],v[172:175],v[120:123]
s_waitcnt vmcnt(12) lgkmcnt(0)
s_barrier
MM a[0:3],a[128:131],v[0:3],a[0:3]
s_add_u32 m0, s65, 0
CV v88,v88,v89
CV v89,v90,v91
MM a[0:3],a[132:135],v[4:7],a[0:3]
RD a[192:195],v224 offset:32768
CV v90,v92,v93
MM a[8:11],a[132:135],v[12:15],a[8:11]
RD a[196:199],v225 offset:32768
CV v91,v94,v95
MM a[8:11],a[128:131],v[8:11],a[8:11]
buffer_load_dwordx4 v228, s[8:11], s64 offen offset:0 lds
MM a[16:19],a[128:131],v[16:19],a[16:19]
RD a[200:203],v226 offset:32768
CV v68,v96,v97
MM a[16:19],a[132:135],v[20:23],a[16:19]
RD a[204:207],v227 offset:32768
RD a[208:211],v224 offset:36864
MM a[24:27],a[132:135],v[28:31],a[24:27]
buffer_load_dwordx4 v229, s[8:11], s64 offen offset:1024 lds
MM a[24:27],a[128:131],v[24:27],a[24:27]
EX v99,v99
MM a[28:31],a[136:139],v[24:27],a[28:31]
RD a[212:215],v225 offset:36864
CV v69,v98,v99
MM a[28:31],a[140:143],v[28:31],a[28:31]
EX v100,v100
MM a[20:23],a[140:143],v[20:23],a[20:23]
buffer_load_dwordx4 v230, s[8:11], s64 offen offset:2048 lds
MM a[20:23],a[136:139],v[16:19],a[20:23]
EX v101,v101
MM a[12:15],a[136:139],v[8:11],a[12:15]
RD a[216:219],v226 offset:36864
CV v70,v100,v101
MM a[12:15],a[140:143],v[12:15],a[12:15]
EX v102,v102
MM a[4:7],a[140:143],v[4:7],a[4:7]
EX v103,v103
MM a[4:7],a[136:139],v[0:3],a[4:7]
RD a[220:223],v227 offset:36864
CV v71,v102,v103
MM a[32:35],a[144:147],v[0:3],a[32:35]
EX v104,v104
MM a[32:35],a[148:151],v[4:7],a[32:35]
buffer_load_dwordx4 v231, s[8:11], s64 offen offset:3072 lds
MM a[40:43],a[148:151],v[12:15],a[40:43]
s_add_u32 m0, s65, 65536
EX v105,v105
MM a[40:43],a[144:147],v[8:11],a[40:43]
RD a[224:227],v224 offset:40960
CV v76,v104,v105
MM a[48:51],a[144:147],v[16:19],a[48:51]
EX v106,v106
MM a[48:51],a[148:151],v[20:23],a[48:51]
EX v107,v107
MM a[56:59],a[148:151],v[28:31],a[56:59]
RD a[228:231],v225 offset:40960
CV v77,v106,v107
MM a[56:59],a[144:147],v[24:27],a[56:59]
EX v108,v108
MM a[60:63],a[152:155],v[24:27],a[60:63]
buffer_load_dwordx4 v235, s[12:15], s81 offen offset:0 lds
MM a[60:63],a[156:159],v[28:31],a[60:63]
EX v109,v109
MM a[52:55],a[156:159],v[20:23],a[52:55]
RD a[232:235],v226 offset:40960
CV v78,v108,v109
MM a[52:55],a[152:155],v[16:19],a[52:55]
EX v110,v110
MM a[44:47],a[152:155],v[8:11],a[44:47]
EX v111,v111
MM a[44:47],a[156:159],v[12:15],a[44:47]
RD a[236:239],v227 offset:40960
CV v79,v110,v111
MM a[36:39],a[156:159],v[4:7],a[36:39]
EX v112,v112
MM a[36:39],a[152:155],v[0:3],a[36:39]
buffer_load_dwordx4 v235, s[12:15], s64 offen offset:1024 lds
MM a[64:67],a[160:163],v[0:3],a[64:67]
EX v113,v113
MM a[64:67],a[164:167],v[4:7],a[64:67]
RD a[240:243],v224 offset:45056
CV v84,v112,v113
MM a[72:75],a[164:167],v[12:15],a[72:75]
EX v114,v114
MM a[72:75],a[160:163],v[8:11],a[72:75]
RD a[244:247],v225 offset:45056
RD a[248:251],v226 offset:45056
MM a[80:83],a[160:163],v[16:19],a[80:83]
EX v115,v115
MM a[80:83],a[164:167],v[20:23],a[80:83]
RD a[252:255],v227 offset:45056
CV v85,v114,v115
MM a[88:91],a[164:167],v[28:31],a[88:91]
EX v116,v116
MM a[88:91],a[160:163],v[24:27],a[88:91]
buffer_load_dwordx4 v235, s[12:15], s81 offen offset:2048 lds
MM a[92:95],a[168:171],v[24:27],a[92:95]
EX v117,v117
MM a[92:95],a[172:175],v[28:31],a[92:95]
EX v118,v118
MM a[84:87],a[172:175],v[20:23],a[84:87]
EX v119,v119
MM a[84:87],a[168:171],v[16:19],a[84:87]
buffer_load_dwordx4 v235, s[12:15], s64 offen offset:3072 lds
MM a[76:79],a[168:171],v[8:11],a[76:79]
EX v120,v120
MM a[76:79],a[172:175],v[12:15],a[76:79]
EX v121,v121
MM a[68:71],a[172:175],v[4:7],a[68:71]
EX v122,v122
MM a[68:71],a[168:171],v[0:3],a[68:71]
EX v123,v123
MM a[96:99],a[176:179],v[0:3],a[96:99]
EX v124,v124
MM a[96:99],a[180:183],v[4:7],a[96:99]
EX v125,v125
MM a[104:107],a[180:183],v[12:15],a[104:107]
EX v126,v126
MM a[104:107],a[176:179],v[8:11],a[104:107]
EX v127,v127
MM a[112:115],a[176:179],v[16:19],a[112:115]
CV v86,v116,v117
CV v87,v118,v119
MM a[112:115],a[180:183],v[20:23],a[112:115]
CV v92,v120,v121
CV v93,v122,v123
MM a[120:123],a[180:183],v[28:31],a[120:123]
CV v94,v124,v125
CV v95,v126,v127
MM a[120:123],a[176:179],v[24:27],a[120:123]
MM a[124:127],a[184:187],v[24:27],a[124:127]
v_add_f32 v244, v144, v145
v_add_f32 v245, v148, v149
MM a[124:127],a[188:191],v[28:31],a[124:127]
v_add_f32 v246, v152, v153
v_add_f32 v247, v156, v157
MM a[116:119],a[188:191],v[20:23],a[116:119]
MM a[116:119],a[184:187],v[16:19],a[116:119]
MM a[108:111],a[184:187],v[8:11],a[108:111]
MM a[108:111],a[188:191],v[12:15],a[108:111]
MM a[100:103],a[188:191],v[4:7],a[100:103]
MM a[100:103],a[184:187],v[0:3],a[100:103]
v_smfmac_f32_16x16x64_bf16 v[144:147], v[236:239], v[64:71], v234
v_smfmac_f32_16x16x64_bf16 v[148:151], v[236:239], v[72:79], v234
v_smfmac_f32_16x16x64_bf16 v[152:155], v[236:239], v[80:87], v234
v_smfmac_f32_16x16x64_bf16 v[156:159], v[236:239], v[88:95], v234
s_waitcnt lgkmcnt(0)
s_sub_u32 s70, s70, 1
s_cmp_lg_u32 s70, 0
s_cbranch_scc1 LLAP_%=
s_branch LLAJ_%=
s_nop 0
s_nop 0
s_nop 0
s_nop 0
s_nop 0
s_nop 0
s_nop 0
s_nop 0
s_nop 0
s_nop 0
s_nop 0
s_nop 0
s_nop 0
s_nop 0
s_nop 0
s_nop 0
s_nop 0
s_nop 0
s_nop 0
s_nop 0
s_nop 0
s_nop 0
s_nop 0
s_nop 0
s_nop 0
s_nop 0
s_nop 0
s_nop 0
s_nop 0
s_nop 0
s_nop 0
s_nop 0
s_nop 0
s_nop 0
s_nop 0
s_nop 0
s_nop 0
s_nop 0
s_nop 0
s_nop 0
s_nop 0
s_nop 0
s_nop 0
s_nop 0
s_nop 0
s_nop 0
s_nop 0
s_nop 0
s_nop 0
s_nop 0
s_nop 0
s_nop 0
s_nop 0
s_nop 0
s_nop 0
s_nop 0
s_nop 0
s_nop 0
s_nop 0
s_nop 0
s_nop 0
s_nop 0
s_nop 0
s_nop 0
s_nop 0
s_nop 0
s_nop 0
s_nop 0
s_nop 0
s_nop 0
)ASM"
R"ASM(
s_nop 0
s_nop 0
s_nop 0
s_nop 0
s_nop 0
s_nop 0
s_nop 0
s_nop 0
s_nop 0
s_nop 0
s_nop 0
s_nop 0
s_nop 0
s_nop 0
s_nop 0
s_nop 0
s_nop 0
s_nop 0
LPG9_%=:
s_cbranch_execz LTR0_%=
s_nop 0
s_nop 0
s_nop 0
s_nop 0
s_nop 0
s_nop 0
s_nop 0
s_nop 0
s_nop 0
s_nop 0
s_nop 0
s_nop 0
s_nop 0
s_nop 0
s_nop 0
s_nop 0
s_nop 0
s_nop 0
s_nop 0
s_nop 0
s_nop 0
s_nop 0
s_nop 0
s_nop 0
s_nop 0
s_nop 0
s_nop 0
s_nop 0
s_nop 0
s_nop 0
s_nop 0
s_nop 0
s_nop 0
s_nop 0
s_nop 0
s_nop 0
s_nop 0
s_nop 0
s_nop 0
s_nop 0
s_nop 0
s_nop 0
s_nop 0
s_nop 0
s_nop 0
s_nop 0
s_nop 0
s_nop 0
s_nop 0
s_nop 0
s_nop 0
s_nop 0
s_nop 0
s_nop 0
s_nop 0
s_nop 0
s_nop 0
s_nop 0
s_nop 0
s_nop 0
s_nop 0
s_nop 0
s_nop 0
s_nop 0
s_nop 0
s_nop 0
s_nop 0
s_nop 0
s_nop 0
s_nop 0
s_nop 0
s_nop 0
s_nop 0
s_nop 0
s_nop 0
s_nop 0
s_nop 0
s_nop 0
s_nop 0
s_nop 0
s_nop 0
s_nop 0
s_nop 0
s_nop 0
s_nop 0
s_nop 0
s_nop 0
s_nop 0
s_nop 0
s_nop 0
s_nop 0
s_nop 0
s_nop 0
s_nop 0
s_nop 0
s_nop 0
s_nop 0
s_nop 0
s_nop 0
s_nop 0
s_nop 0
s_nop 0
s_nop 0
s_nop 0
s_nop 0
s_nop 0
s_nop 0
s_nop 0
s_nop 0
s_nop 0
s_nop 0
s_nop 0
s_nop 0
s_nop 0
s_nop 0
s_nop 0
s_nop 0
s_nop 0
s_nop 0
s_nop 0
s_nop 0
s_nop 0
s_nop 0
s_nop 0
s_nop 0
s_nop 0
s_nop 0
s_nop 0
s_nop 0
s_nop 0
s_nop 0
s_nop 0
s_nop 0
s_nop 0
s_nop 0
s_nop 0
s_nop 0
s_nop 0
s_nop 0
s_nop 0
s_nop 0
s_nop 0
s_nop 0
s_nop 0
s_nop 0
s_nop 0
s_nop 0
s_nop 0
s_nop 0
s_nop 0
s_nop 0
s_nop 0
s_nop 0
s_nop 0
s_nop 0
s_nop 0
s_nop 0
s_nop 0
s_nop 0
s_nop 0
s_nop 0
s_nop 0
s_nop 0
s_nop 0
s_nop 0
s_nop 0
s_nop 0
s_nop 0
s_nop 0
s_nop 0
s_nop 0
s_nop 0
s_nop 0
s_nop 0
s_nop 0
s_nop 0
s_nop 0
s_nop 0
s_nop 0
s_nop 0
s_nop 0
s_nop 0
s_nop 0
s_nop 0
s_nop 0
s_nop 0
s_nop 0
s_nop 0
s_nop 0
s_nop 0
s_nop 0
s_nop 0
s_nop 0
s_nop 0
s_nop 0
s_nop 0
s_nop 0
s_nop 0
s_nop 0
s_nop 0
s_nop 0
s_nop 0
s_nop 0
s_nop 0
s_nop 0
s_nop 0
s_nop 0
s_nop 0
s_nop 0
s_nop 0
s_nop 0
s_nop 0
s_nop 0
s_nop 0
s_nop 0
s_nop 0
s_nop 0
s_nop 0
s_nop 0
s_nop 0
s_nop 0
s_nop 0
s_nop 0
s_nop 0
s_nop 0
s_nop 0
s_nop 0
s_nop 0
s_nop 0
s_nop 0
s_nop 0
s_nop 0
s_nop 0
s_nop 0
s_nop 0
s_nop 0
s_nop 0
s_nop 0
s_nop 0
s_nop 0
s_nop 0
s_nop 0
s_nop 0
s_nop 0
s_nop 0
s_nop 0
s_nop 0
s_nop 0
s_nop 0
s_nop 0
s_nop 0
s_nop 0
s_nop 0
s_nop 0
s_nop 0
s_nop 0
s_nop 0
s_nop 0
s_nop 0
s_nop 0
s_nop 0
s_nop 0
s_nop 0
s_nop 0
s_nop 0
s_nop 0
s_nop 0
s_nop 0
s_nop 0
s_nop 0
s_nop 0
s_nop 0
s_nop 0
s_nop 0
s_nop 0
s_nop 0
s_nop 0
s_nop 0
s_nop 0
s_nop 0
s_nop 0
s_nop 0
s_nop 0
s_nop 0
s_nop 0
s_nop 0
s_nop 0
s_nop 0
s_nop 0
s_nop 0
s_nop 0
s_nop 0
s_nop 0
s_nop 0
s_nop 0
s_nop 0
s_nop 0
s_nop 0
s_nop 0
s_nop 0
s_nop 0
s_nop 0
s_nop 0
s_nop 0
s_nop 0
s_nop 0
s_nop 0
s_nop 0
s_nop 0
s_nop 0
s_nop 0
s_nop 0
s_nop 0
s_nop 0
s_nop 0
s_nop 0
s_nop 0
s_nop 0
s_nop 0
s_nop 0
s_nop 0
s_nop 0
s_nop 0
s_nop 0
s_nop 0
s_nop 0
s_nop 0
s_nop 0
s_nop 0
s_nop 0
s_nop 0
s_nop 0
s_nop 0
s_nop 0
s_nop 0
s_nop 0
s_nop 0
s_nop 0
s_nop 0
s_nop 0
s_nop 0
s_nop 0
s_nop 0
s_nop 0
s_nop 0
s_nop 0
s_nop 0
s_nop 0
s_nop 0
s_nop 0
s_nop 0
s_nop 0
s_nop 0
s_nop 0
s_nop 0
s_nop 0
s_nop 0
s_nop 0
s_nop 0
s_nop 0
s_nop 0
s_nop 0
s_nop 0
s_nop 0
s_nop 0
s_nop 0
s_nop 0
s_nop 0
s_nop 0
s_nop 0
s_nop 0
s_nop 0
s_nop 0
s_nop 0
s_nop 0
s_nop 0
s_nop 0
s_nop 0
s_nop 0
s_nop 0
s_nop 0
s_nop 0
s_nop 0
s_nop 0
s_nop 0
s_nop 0
s_nop 0
s_nop 0
s_nop 0
s_nop 0
s_nop 0
s_nop 0
s_nop 0
s_nop 0
s_nop 0
s_nop 0
s_nop 0
s_nop 0
s_nop 0
s_nop 0
s_nop 0
s_nop 0
s_nop 0
s_nop 0
s_nop 0
s_nop 0
s_nop 0
s_nop 0
s_nop 0
s_nop 0
s_nop 0
s_nop 0
s_nop 0
s_nop 0
s_nop 0
s_nop 0
s_nop 0
s_nop 0
s_nop 0
s_nop 0
s_nop 0
s_nop 0
s_nop 0
s_nop 0
s_nop 0
s_nop 0
s_nop 0
s_nop 0
s_nop 0
s_nop 0
s_nop 0
s_nop 0
s_nop 0
s_nop 0
s_nop 0
s_nop 0
s_nop 0
s_nop 0
s_nop 0
s_nop 0
s_nop 0
s_nop 0
s_nop 0
s_nop 0
s_nop 0
s_nop 0
s_nop 0
s_nop 0
s_nop 0
s_nop 0
s_nop 0
s_nop 0
s_nop 0
s_nop 0
s_nop 0
s_nop 0
s_nop 0
s_nop 0
s_nop 0
s_nop 0
s_nop 0
s_nop 0
s_nop 0
s_nop 0
s_nop 0
s_nop 0
s_nop 0
s_nop 0
s_nop 0
s_nop 0
s_nop 0
s_nop 0
s_nop 0
s_nop 0
s_nop 0
s_nop 0
s_nop 0
s_nop 0
s_nop 0
s_nop 0
s_nop 0
s_nop 0
s_nop 0
s_nop 0
s_nop 0
s_nop 0
s_nop 0
s_nop 0
s_nop 0
s_nop 0
s_nop 0
s_nop 0
s_nop 0
s_nop 0
s_nop 0
s_nop 0
s_nop 0
s_nop 0
s_nop 0
s_nop 0
s_nop 0
s_nop 0
s_nop 0
s_nop 0
s_nop 0
s_nop 0
s_nop 0
s_nop 0
s_nop 0
s_nop 0
s_nop 0
s_nop 0
s_nop 0
s_nop 0
s_nop 0
s_nop 0
s_nop 0
s_nop 0
s_nop 0
s_nop 0
s_nop 0
s_nop 0
s_nop 0
s_nop 0
s_nop 0
s_nop 0
s_nop 0
s_nop 0
s_nop 0
s_nop 0
s_nop 0
s_nop 0
s_nop 0
s_nop 0
s_nop 0
s_nop 0
s_nop 0
s_nop 0
s_nop 0
s_nop 0
s_nop 0
s_nop 0
s_nop 0
s_nop 0
s_nop 0
s_nop 0
s_nop 0
s_nop 0
s_nop 0
s_nop 0
s_nop 0
s_nop 0
s_nop 0
s_nop 0
s_nop 0
s_nop 0
s_nop 0
s_nop 0
s_nop 0
s_nop 0
s_nop 0
s_nop 0
s_nop 0
s_nop 0
s_nop 0
s_nop 0
s_nop 0
s_nop 0
s_nop 0
s_nop 0
s_nop 0
s_nop 0
s_nop 0
s_nop 0
s_nop 0
s_nop 0
s_nop 0
s_nop 0
s_nop 0
s_nop 0
s_nop 0
s_nop 0
s_nop 0
s_nop 0
s_nop 0
s_nop 0
s_nop 0
s_nop 0
s_nop 0
s_nop 0
s_nop 0
s_nop 0
s_nop 0
s_nop 0
s_nop 0
s_nop 0
s_nop 0
LLAR_%=:
MM v[0:3],a[192:195],v[160:163],v[128:131]
s_add_u32 s64, s64, 16384
TR a[128:129],v232 offset:16384
s_add_u32 s81, s64, 896
MM v[0:3],a[196:199],v[176:179],v[0:3]
TR a[130:131],v232 offset:20480
v_max3_f32 v249, v144, v145, v148
MM v[0:3],a[200:203],v[192:195],v[0:3]
TR a[132:133],v232 offset:24576
v_max3_f32 v249, v249, v149, v152
MM v[0:3],a[204:207],v[208:211],v[0:3]
TR a[134:135],v232 offset:28672
v_max3_f32 v249, v249, v153, v156
MM v[4:7],a[220:223],v[208:211],v[128:131]
TR a[136:137],v233 offset:16384
v_max_f32 v249, v249, v157
MM v[4:7],a[216:219],v[192:195],v[4:7]
TR a[138:139],v233 offset:20480
v_cmp_lt_f32 vcc, s71, v249
MM v[4:7],a[212:215],v[176:179],v[4:7]
TR a[140:141],v233 offset:24576
TR a[142:143],v233 offset:28672
MM v[4:7],a[208:211],v[160:163],v[4:7]
s_cbranch_vccnz LSITE9_%=
LBACK9_%=:
MM v[12:15],a[208:211],v[164:167],v[132:135]
s_lshl_b32 s17, s16, 2
EX v0,v0
MM v[12:15],a[212:215],v[180:183],v[12:15]
s_add_u32 s17, s17, %[swave]
EX v1,v1
MM v[12:15],a[216:219],v[196:199],v[12:15]
s_sub_u32 s90, s16, 108
TR a[144:145],v232 offset:16896
CV v0,v0,v1
MM v[12:15],a[220:223],v[212:215],v[12:15]
s_min_u32 s90, s90, s16
EX v2,v2
MM v[8:11],a[204:207],v[212:215],v[132:135]
s_lshl_b32 s48, s90, 5
EX v3,v3
MM v[8:11],a[200:203],v[196:199],v[8:11]
s_add_u32 s76, %[slds], 155648
v_add_u32 v250, s76, v248
CV v1,v2,v3
MM v[8:11],a[196:199],v[180:183],v[8:11]
s_lshl_b32 s77, s17, 4
TR a[146:147],v232 offset:20992
TR a[148:149],v232 offset:25088
MM v[8:11],a[192:195],v[164:167],v[8:11]
s_add_u32 s76, s76, s77
TR a[150:151],v232 offset:29184
v_mov_b32 v251, s76
MM v[16:19],a[192:195],v[168:171],v[136:139]
RD v[124:127],v251 offset:0
RD v[96:99],v250 offset:0
MM v[16:19],a[196:199],v[184:187],v[16:19]
RD v[100:103],v250 offset:1024
RD v[104:107],v250 offset:2048
MM v[16:19],a[200:203],v[200:203],v[16:19]
RD v[108:111],v250 offset:3072
RD v[112:115],v250 offset:4096
MM v[16:19],a[204:207],v[216:219],v[16:19]
RD v[116:119],v250 offset:5120
RD v[120:123],v250 offset:6144
MM v[20:23],a[220:223],v[216:219],v[136:139]
EX v4,v4
MM v[20:23],a[216:219],v[200:203],v[20:23]
EX v5,v5
MM v[20:23],a[212:215],v[184:187],v[20:23]
s_waitcnt lgkmcnt(7)
v_readfirstlane_b32 s52, v124
v_readfirstlane_b32 s53, v125
MM v[20:23],a[208:211],v[168:171],v[20:23]
v_readfirstlane_b32 s54, v126
v_readfirstlane_b32 s55, v127
EX v6,v6
MM v[28:31],a[208:211],v[172:175],v[140:143]
RD v[124:127],v250 offset:7168
TR a[152:153],v233 offset:16896
MM v[28:31],a[212:215],v[188:191],v[28:31]
s_waitcnt lgkmcnt(2)
v_max3_u32 v253, v96, v97, v98
v_max3_u32 v251, v99, v100, v101
EX v7,v7
MM v[28:31],a[216:219],v[204:207],v[28:31]
v_max3_u32 v253, v253, v102, v103
v_max3_u32 v251, v251, v104, v105
MM v[28:31],a[220:223],v[220:223],v[28:31]
v_max3_u32 v253, v253, v106, v107
v_max3_u32 v251, v251, v108, v109
EX v8,v8
MM v[24:27],a[204:207],v[220:223],v[140:143]
v_max3_u32 v253, v253, v110, v111
v_max3_u32 v251, v251, v112, v113
MM v[24:27],a[200:203],v[204:207],v[24:27]
v_max3_u32 v253, v253, v114, v115
v_max3_u32 v251, v251, v116, v117
EX v9,v9
MM v[24:27],a[196:199],v[188:191],v[24:27]
TR a[154:155],v233 offset:20992
v_max3_u32 v253, v253, v118, v119
v_max3_u32 v251, v251, v120, v121
MM v[24:27],a[192:195],v[172:175],v[24:27]
v_max3_u32 v253, v253, v122, v123
CV v2,v4,v5
EX v10,v10
MM v[32:35],a[224:227],v[160:163],v[128:131]
s_waitcnt lgkmcnt(2)
TR a[156:157],v233 offset:25088
v_max3_u32 v251, v251, v124, v125
v_max3_u32 v253, v253, v126, v127
EX v11,v11
MM v[32:35],a[228:231],v[176:179],v[32:35]
v_max_u32 v253, v253, v251
v_cmp_lt_u32 s[60:61], s52, v253
MM v[32:35],a[232:235],v[192:195],v[32:35]
TR a[158:159],v233 offset:29184
v_cmp_lt_u32 s[62:63], s53, v253
v_cmp_lt_u32 s[56:57], s54, v253
EX v12,v12
MM v[32:35],a[236:239],v[208:211],v[32:35]
v_cmp_lt_u32 s[58:59], s55, v253
CV v3,v6,v7
MM v[36:39],a[252:255],v[208:211],v[128:131]
TR a[160:161],v232 offset:17408
EX v13,v13
MM v[36:39],a[248:251],v[192:195],v[36:39]
s_bcnt1_i32_b64 s60, s[60:61]
TR a[162:163],v232 offset:21504
CV v8,v8,v9
MM v[36:39],a[244:247],v[176:179],v[36:39]
s_bcnt1_i32_b64 s62, s[62:63]
EX v14,v14
MM v[36:39],a[240:243],v[160:163],v[36:39]
s_bcnt1_i32_b64 s56, s[56:57]
TR a[164:165],v232 offset:25600
TR a[166:167],v232 offset:29696
MM v[44:47],a[240:243],v[164:167],v[132:135]
s_bcnt1_i32_b64 s58, s[58:59]
EX v15,v15
MM v[44:47],a[244:247],v[180:183],v[44:47]
s_min_u32 s76, s60, s62
EX v16,v16
MM v[44:47],a[248:251],v[196:199],v[44:47]
s_min_u32 s77, s56, s58
TR a[168:169],v233 offset:17408
CV v9,v10,v11
MM v[44:47],a[252:255],v[212:215],v[44:47]
s_min_u32 s76, s76, s77
s_cmp_lt_u32 s76, 8
s_cbranch_scc1 LRK_%=
LRKb_%=:
EX v17,v17
MM v[40:43],a[236:239],v[212:215],v[132:135]
v_lshrrev_b32 v252, 6, v248
v_mul_u32_u24 v252, 3456, v252
MM v[40:43],a[232:235],v[196:199],v[40:43]
TR a[170:171],v233 offset:21504
v_and_b32 v251, 48, v248
v_add_u32 v252, v252, v251
EX v18,v18
MM v[40:43],a[228:231],v[180:183],v[40:43]
s_and_b32 s90, %[swave], 1
TR a[172:173],v233 offset:25600
CV v10,v12,v13
MM v[40:43],a[224:227],v[164:167],v[40:43]
s_mul_i32 s90, s90, 276480
EX v19,v19
MM v[48:51],a[224:227],v[168:171],v[136:139]
s_bfe_u32 s91, s48, 131077
TR a[174:175],v233 offset:29696
CV v11,v14,v15
MM v[48:51],a[228:231],v[184:187],v[48:51]
s_mul_i32 s91, s91, 552960
EX v20,v20
MM v[48:51],a[232:235],v[200:203],v[48:51]
s_add_u32 s90, s90, s91
TR a[176:177],v232 offset:17920
TR a[178:179],v232 offset:22016
MM v[48:51],a[236:239],v[216:219],v[48:51]
s_and_b32 s91, s48, 0xffffff80
EX v21,v21
MM v[52:55],a[252:255],v[216:219],v[136:139]
s_add_u32 s90, s90, s91
EX v22,v22
MM v[52:55],a[248:251],v[200:203],v[52:55]
s_and_b32 s91, s48, 16
TR a[180:181],v232 offset:26112
CV v16,v16,v17
MM v[52:55],a[244:247],v[184:187],v[52:55]
s_lshl_b32 s91, s91, 2
EX v23,v23
MM v[52:55],a[240:243],v[168:171],v[52:55]
s_add_u32 s66, s90, s91
TR a[182:183],v232 offset:30208
CV v17,v18,v19
MM v[60:63],a[240:243],v[172:175],v[140:143]
s_mul_i32 s91, %[swave], 5120
EX v24,v24
MM v[60:63],a[244:247],v[188:191],v[60:63]
s_add_u32 s91, s91, %[slds]
TR a[184:185],v233 offset:17920
TR a[186:187],v233 offset:22016
MM v[60:63],a[248:251],v[204:207],v[60:63]
s_add_u32 s67, s91, 131072
TR a[188:189],v233 offset:26112
TR a[190:191],v233 offset:30208
EX v25,v25
MM v[60:63],a[252:255],v[220:223],v[60:63]
EX v26,v26
MM v[56:59],a[236:239],v[220:223],v[140:143]
EX v27,v27
MM v[56:59],a[232:235],v[204:207],v[56:59]
EX v28,v28
MM v[56:59],a[228:231],v[188:191],v[56:59]
EX v29,v29
CV v18,v20,v21
MM v[56:59],a[224:227],v[172:175],v[56:59]
s_waitcnt lgkmcnt(0)
s_barrier
MM a[0:3],a[128:131],v[64:67],a[0:3]
s_add_u32 m0, s65, 16384
CV v19,v22,v23
CV v24,v24,v25
MM a[0:3],a[132:135],v[68:71],a[0:3]
CV v25,v26,v27
CV v26,v28,v29
MM a[8:11],a[132:135],v[76:79],a[8:11]
EX v30,v30
MM a[8:11],a[128:131],v[72:75],a[8:11]
EX v31,v31
MM a[16:19],a[128:131],v[80:83],a[16:19]
RD a[192:195],v224 offset:49152
CV v27,v30,v31
MM a[16:19],a[132:135],v[84:87],a[16:19]
EX v32,v32
MM a[24:27],a[132:135],v[92:95],a[24:27]
buffer_load_dwordx4 v228, s[8:11], s64 offen offset:0 lds
)ASM"
R"ASM(
MM a[24:27],a[128:131],v[88:91],a[24:27]
EX v33,v33
MM a[28:31],a[136:139],v[88:91],a[28:31]
RD a[196:199],v225 offset:49152
CV v4,v32,v33
MM a[28:31],a[140:143],v[92:95],a[28:31]
EX v34,v34
MM a[20:23],a[140:143],v[84:87],a[20:23]
EX v35,v35
MM a[20:23],a[136:139],v[80:83],a[20:23]
RD a[200:203],v226 offset:49152
CV v5,v34,v35
MM a[12:15],a[136:139],v[72:75],a[12:15]
EX v36,v36
s_nop 0
LPG10_%=:
s_cbranch_execz LTR0_%=
MM a[12:15],a[140:143],v[76:79],a[12:15]
buffer_load_dwordx4 v229, s[8:11], s64 offen offset:1024 lds
MM a[4:7],a[140:143],v[68:71],a[4:7]
EX v37,v37
MM a[4:7],a[136:139],v[64:67],a[4:7]
RD a[204:207],v227 offset:49152
CV v6,v36,v37
MM a[32:35],a[144:147],v[64:67],a[32:35]
EX v38,v38
MM a[32:35],a[148:151],v[68:71],a[32:35]
EX v39,v39
MM a[40:43],a[148:151],v[76:79],a[40:43]
RD a[208:211],v224 offset:53248
CV v7,v38,v39
MM a[40:43],a[144:147],v[72:75],a[40:43]
EX v40,v40
MM a[48:51],a[144:147],v[80:83],a[48:51]
buffer_load_dwordx4 v230, s[8:11], s64 offen offset:2048 lds
MM a[48:51],a[148:151],v[84:87],a[48:51]
EX v41,v41
MM a[56:59],a[148:151],v[92:95],a[56:59]
RD a[212:215],v225 offset:53248
CV v12,v40,v41
MM a[56:59],a[144:147],v[88:91],a[56:59]
EX v42,v42
MM a[60:63],a[152:155],v[88:91],a[60:63]
EX v43,v43
MM a[60:63],a[156:159],v[92:95],a[60:63]
RD a[216:219],v226 offset:53248
CV v13,v42,v43
MM a[52:55],a[156:159],v[84:87],a[52:55]
EX v44,v44
MM a[52:55],a[152:155],v[80:83],a[52:55]
buffer_load_dwordx4 v231, s[8:11], s64 offen offset:3072 lds
MM a[44:47],a[152:155],v[72:75],a[44:47]
s_add_u32 m0, s65, 81920
EX v45,v45
MM a[44:47],a[156:159],v[76:79],a[44:47]
RD a[220:223],v227 offset:53248
CV v14,v44,v45
MM a[36:39],a[156:159],v[68:71],a[36:39]
EX v46,v46
MM a[36:39],a[152:155],v[64:67],a[36:39]
RD a[224:227],v224 offset:57344
RD a[228:231],v225 offset:57344
MM a[64:67],a[160:163],v[64:67],a[64:67]
EX v47,v47
MM a[64:67],a[164:167],v[68:71],a[64:67]
RD a[232:235],v226 offset:57344
CV v15,v46,v47
MM a[72:75],a[164:167],v[76:79],a[72:75]
EX v48,v48
MM a[72:75],a[160:163],v[72:75],a[72:75]
buffer_load_dwordx4 v235, s[12:15], s81 offen offset:0 lds
MM a[80:83],a[160:163],v[80:83],a[80:83]
s_add_u32 m0, s67, 0
s_add_u32 s85, s66, 0
buffer_load_dwordx4 v252, s[36:39], s85 offen offset:0 lds
MM a[80:83],a[164:167],v[84:87],a[80:83]
EX v49,v49
MM a[88:91],a[164:167],v[92:95],a[88:91]
EX v50,v50
MM a[88:91],a[160:163],v[88:91],a[88:91]
RD a[236:239],v227 offset:57344
CV v20,v48,v49
MM a[92:95],a[168:171],v[88:91],a[92:95]
EX v51,v51
MM a[92:95],a[172:175],v[92:95],a[92:95]
RD a[240:243],v224 offset:61440
CV v21,v50,v51
MM a[84:87],a[172:175],v[84:87],a[84:87]
EX v52,v52
MM a[84:87],a[168:171],v[80:83],a[84:87]
EX v53,v53
MM a[76:79],a[168:171],v[72:75],a[76:79]
RD a[244:247],v225 offset:61440
CV v22,v52,v53
MM a[76:79],a[172:175],v[76:79],a[76:79]
EX v54,v54
MM a[68:71],a[172:175],v[68:71],a[68:71]
s_add_u32 m0, s65, 81920
s_nop 0
buffer_load_dwordx4 v235, s[12:15], s64 offen offset:1024 lds
MM a[68:71],a[168:171],v[64:67],a[68:71]
EX v55,v55
MM a[96:99],a[176:179],v[64:67],a[96:99]
RD a[248:251],v226 offset:61440
CV v23,v54,v55
MM a[96:99],a[180:183],v[68:71],a[96:99]
EX v56,v56
MM a[104:107],a[180:183],v[76:79],a[104:107]
EX v57,v57
MM a[104:107],a[176:179],v[72:75],a[104:107]
RD a[252:255],v227 offset:61440
CV v28,v56,v57
MM a[112:115],a[176:179],v[80:83],a[112:115]
buffer_load_dwordx4 v235, s[12:15], s81 offen offset:2048 lds
MM a[112:115],a[180:183],v[84:87],a[112:115]
EX v58,v58
MM a[120:123],a[180:183],v[92:95],a[120:123]
EX v59,v59
MM a[120:123],a[176:179],v[88:91],a[120:123]
EX v60,v60
MM a[124:127],a[184:187],v[88:91],a[124:127]
v_add_f32 v244, v144, v145
v_add_f32 v245, v148, v149
MM a[124:127],a[188:191],v[92:95],a[124:127]
v_add_f32 v246, v152, v153
v_add_f32 v247, v156, v157
MM a[116:119],a[188:191],v[84:87],a[116:119]
EX v61,v61
MM a[116:119],a[184:187],v[80:83],a[116:119]
buffer_load_dwordx4 v235, s[12:15], s64 offen offset:3072 lds
MM a[108:111],a[184:187],v[72:75],a[108:111]
EX v62,v62
MM a[108:111],a[188:191],v[76:79],a[108:111]
EX v63,v63
MM a[100:103],a[188:191],v[68:71],a[100:103]
CV v29,v58,v59
CV v30,v60,v61
MM a[100:103],a[184:187],v[64:67],a[100:103]
CV v31,v62,v63
v_smfmac_f32_16x16x64_bf16 v[144:147], v[236:239], v[0:7], v234
v_smfmac_f32_16x16x64_bf16 v[148:151], v[236:239], v[8:15], v234
v_smfmac_f32_16x16x64_bf16 v[152:155], v[236:239], v[16:23], v234
v_smfmac_f32_16x16x64_bf16 v[156:159], v[236:239], v[24:31], v234
s_waitcnt lgkmcnt(0)
MM v[64:67],a[192:195],v[160:163],v[128:131]
s_add_u32 s64, s64, 16384
TR a[128:129],v232 offset:32768
s_add_u32 s81, s64, 896
MM v[64:67],a[196:199],v[176:179],v[64:67]
TR a[130:131],v232 offset:36864
v_max3_f32 v249, v144, v145, v148
MM v[64:67],a[200:203],v[192:195],v[64:67]
TR a[132:133],v232 offset:40960
v_max3_f32 v249, v249, v149, v152
MM v[64:67],a[204:207],v[208:211],v[64:67]
TR a[134:135],v232 offset:45056
v_max3_f32 v249, v249, v153, v156
MM v[68:71],a[220:223],v[208:211],v[128:131]
TR a[136:137],v233 offset:32768
v_max_f32 v249, v249, v157
MM v[68:71],a[216:219],v[192:195],v[68:71]
TR a[138:139],v233 offset:36864
v_cmp_lt_f32 vcc, s71, v249
MM v[68:71],a[212:215],v[176:179],v[68:71]
TR a[140:141],v233 offset:40960
TR a[142:143],v233 offset:45056
MM v[68:71],a[208:211],v[160:163],v[68:71]
s_cbranch_vccnz LSITE10_%=
LBACK10_%=:
MM v[76:79],a[208:211],v[164:167],v[132:135]
s_add_u32 m0, s67, 1024
s_add_u32 s85, s66, 55296
buffer_load_dwordx4 v252, s[36:39], s85 offen offset:0 lds
MM v[76:79],a[212:215],v[180:183],v[76:79]
EX v64,v64
MM v[76:79],a[216:219],v[196:199],v[76:79]
EX v65,v65
MM v[76:79],a[220:223],v[212:215],v[76:79]
TR a[144:145],v232 offset:33280
CV v64,v64,v65
MM v[72:75],a[204:207],v[212:215],v[132:135]
EX v66,v66
MM v[72:75],a[200:203],v[196:199],v[72:75]
EX v67,v67
MM v[72:75],a[196:199],v[180:183],v[72:75]
TR a[146:147],v232 offset:37376
CV v65,v66,v67
MM v[72:75],a[192:195],v[164:167],v[72:75]
EX v68,v68
MM v[80:83],a[192:195],v[168:171],v[136:139]
TR a[148:149],v232 offset:41472
TR a[150:151],v232 offset:45568
MM v[80:83],a[196:199],v[184:187],v[80:83]
EX v69,v69
MM v[80:83],a[200:203],v[200:203],v[80:83]
TR a[152:153],v233 offset:33280
CV v66,v68,v69
MM v[80:83],a[204:207],v[216:219],v[80:83]
EX v70,v70
MM v[84:87],a[220:223],v[216:219],v[136:139]
EX v71,v71
MM v[84:87],a[216:219],v[200:203],v[84:87]
TR a[154:155],v233 offset:37376
CV v67,v70,v71
MM v[84:87],a[212:215],v[184:187],v[84:87]
EX v72,v72
MM v[84:87],a[208:211],v[168:171],v[84:87]
TR a[156:157],v233 offset:41472
TR a[158:159],v233 offset:45568
MM v[92:95],a[208:211],v[172:175],v[140:143]
EX v73,v73
MM v[92:95],a[212:215],v[188:191],v[92:95]
TR a[160:161],v232 offset:33792
CV v72,v72,v73
MM v[92:95],a[216:219],v[204:207],v[92:95]
EX v74,v74
MM v[92:95],a[220:223],v[220:223],v[92:95]
EX v75,v75
MM v[88:91],a[204:207],v[220:223],v[140:143]
TR a[162:163],v232 offset:37888
CV v73,v74,v75
MM v[88:91],a[200:203],v[204:207],v[88:91]
TR a[164:165],v232 offset:41984
TR a[166:167],v232 offset:46080
MM v[88:91],a[196:199],v[188:191],v[88:91]
EX v76,v76
MM v[88:91],a[192:195],v[172:175],v[88:91]
EX v77,v77
MM v[96:99],a[224:227],v[160:163],v[128:131]
TR a[168:169],v233 offset:33792
CV v74,v76,v77
MM v[96:99],a[228:231],v[176:179],v[96:99]
EX v78,v78
MM v[96:99],a[232:235],v[192:195],v[96:99]
TR a[170:171],v233 offset:37888
TR a[172:173],v233 offset:41984
MM v[96:99],a[236:239],v[208:211],v[96:99]
EX v79,v79
MM v[100:103],a[252:255],v[208:211],v[128:131]
TR a[174:175],v233 offset:46080
CV v75,v78,v79
MM v[100:103],a[248:251],v[192:195],v[100:103]
EX v80,v80
MM v[100:103],a[244:247],v[176:179],v[100:103]
EX v81,v81
MM v[100:103],a[240:243],v[160:163],v[100:103]
TR a[176:177],v232 offset:34304
CV v80,v80,v81
MM v[108:111],a[240:243],v[164:167],v[132:135]
s_add_u32 m0, s67, 2048
s_add_u32 s85, s66, 110592
buffer_load_dwordx4 v252, s[36:39], s85 offen offset:0 lds
MM v[108:111],a[244:247],v[180:183],v[108:111]
EX v82,v82
MM v[108:111],a[248:251],v[196:199],v[108:111]
TR a[178:179],v232 offset:38400
TR a[180:181],v232 offset:42496
MM v[108:111],a[252:255],v[212:215],v[108:111]
EX v83,v83
MM v[104:107],a[236:239],v[212:215],v[132:135]
TR a[182:183],v232 offset:46592
CV v81,v82,v83
MM v[104:107],a[232:235],v[196:199],v[104:107]
EX v84,v84
MM v[104:107],a[228:231],v[180:183],v[104:107]
EX v85,v85
MM v[104:107],a[224:227],v[164:167],v[104:107]
TR a[184:185],v233 offset:34304
CV v82,v84,v85
MM v[112:115],a[224:227],v[168:171],v[136:139]
EX v86,v86
MM v[112:115],a[228:231],v[184:187],v[112:115]
TR a[186:187],v233 offset:38400
TR a[188:189],v233 offset:42496
MM v[112:115],a[232:235],v[200:203],v[112:115]
EX v87,v87
MM v[112:115],a[236:239],v[216:219],v[112:115]
TR a[190:191],v233 offset:46592
CV v83,v86,v87
MM v[116:119],a[252:255],v[216:219],v[136:139]
EX v88,v88
MM v[116:119],a[248:251],v[200:203],v[116:119]
EX v89,v89
MM v[116:119],a[244:247],v[184:187],v[116:119]
EX v90,v90
MM v[116:119],a[240:243],v[168:171],v[116:119]
EX v91,v91
MM v[124:127],a[240:243],v[172:175],v[140:143]
EX v92,v92
MM v[124:127],a[244:247],v[188:191],v[124:127]
EX v93,v93
MM v[124:127],a[248:251],v[204:207],v[124:127]
EX v94,v94
MM v[124:127],a[252:255],v[220:223],v[124:127]
EX v95,v95
MM v[120:123],a[236:239],v[220:223],v[140:143]
EX v96,v96
MM v[120:123],a[232:235],v[204:207],v[120:123]
EX v97,v97
MM v[120:123],a[228:231],v[188:191],v[120:123]
EX v98,v98
MM v[120:123],a[224:227],v[172:175],v[120:123]
s_waitcnt vmcnt(15) lgkmcnt(0)
s_barrier
MM a[0:3],a[128:131],v[0:3],a[0:3]
s_add_u32 m0, s65, 32768
CV v88,v88,v89
CV v89,v90,v91
MM a[0:3],a[132:135],v[4:7],a[0:3]
RD a[192:195],v224 offset:0
CV v90,v92,v93
MM a[8:11],a[132:135],v[12:15],a[8:11]
RD a[196:199],v225 offset:0
CV v91,v94,v95
MM a[8:11],a[128:131],v[8:11],a[8:11]
buffer_load_dwordx4 v228, s[8:11], s64 offen offset:0 lds
MM a[16:19],a[128:131],v[16:19],a[16:19]
RD a[200:203],v226 offset:0
CV v68,v96,v97
MM a[16:19],a[132:135],v[20:23],a[16:19]
buffer_load_dwordx4 v229, s[8:11], s64 offen offset:1024 lds
MM a[24:27],a[132:135],v[28:31],a[24:27]
RD a[204:207],v227 offset:0
RD a[208:211],v224 offset:4096
MM a[24:27],a[128:131],v[24:27],a[24:27]
EX v99,v99
MM a[28:31],a[136:139],v[24:27],a[28:31]
RD a[212:215],v225 offset:4096
CV v69,v98,v99
MM a[28:31],a[140:143],v[28:31],a[28:31]
EX v100,v100
MM a[20:23],a[140:143],v[20:23],a[20:23]
buffer_load_dwordx4 v230, s[8:11], s64 offen offset:2048 lds
MM a[20:23],a[136:139],v[16:19],a[20:23]
EX v101,v101
MM a[12:15],a[136:139],v[8:11],a[12:15]
RD a[216:219],v226 offset:4096
CV v70,v100,v101
MM a[12:15],a[140:143],v[12:15],a[12:15]
EX v102,v102
MM a[4:7],a[140:143],v[4:7],a[4:7]
EX v103,v103
MM a[4:7],a[136:139],v[0:3],a[4:7]
RD a[220:223],v227 offset:4096
CV v71,v102,v103
MM a[32:35],a[144:147],v[0:3],a[32:35]
EX v104,v104
MM a[32:35],a[148:151],v[4:7],a[32:35]
buffer_load_dwordx4 v231, s[8:11], s64 offen offset:3072 lds
MM a[40:43],a[148:151],v[12:15],a[40:43]
s_add_u32 m0, s65, 98304
EX v105,v105
MM a[40:43],a[144:147],v[8:11],a[40:43]
RD a[224:227],v224 offset:8192
CV v76,v104,v105
MM a[48:51],a[144:147],v[16:19],a[48:51]
EX v106,v106
MM a[48:51],a[148:151],v[20:23],a[48:51]
EX v107,v107
MM a[56:59],a[148:151],v[28:31],a[56:59]
RD a[228:231],v225 offset:8192
CV v77,v106,v107
MM a[56:59],a[144:147],v[24:27],a[56:59]
EX v108,v108
MM a[60:63],a[152:155],v[24:27],a[60:63]
buffer_load_dwordx4 v235, s[12:15], s81 offen offset:0 lds
MM a[60:63],a[156:159],v[28:31],a[60:63]
EX v109,v109
MM a[52:55],a[156:159],v[20:23],a[52:55]
RD a[232:235],v226 offset:8192
CV v78,v108,v109
MM a[52:55],a[152:155],v[16:19],a[52:55]
EX v110,v110
MM a[44:47],a[152:155],v[8:11],a[44:47]
EX v111,v111
MM a[44:47],a[156:159],v[12:15],a[44:47]
RD a[236:239],v227 offset:8192
CV v79,v110,v111
MM a[36:39],a[156:159],v[4:7],a[36:39]
buffer_load_dwordx4 v235, s[12:15], s64 offen offset:1024 lds
MM a[36:39],a[152:155],v[0:3],a[36:39]
EX v112,v112
MM a[64:67],a[160:163],v[0:3],a[64:67]
EX v113,v113
MM a[64:67],a[164:167],v[4:7],a[64:67]
RD a[240:243],v224 offset:12288
CV v84,v112,v113
MM a[72:75],a[164:167],v[12:15],a[72:75]
)ASM"
R"ASM(
EX v114,v114
MM a[72:75],a[160:163],v[8:11],a[72:75]
RD a[244:247],v225 offset:12288
RD a[248:251],v226 offset:12288
MM a[80:83],a[160:163],v[16:19],a[80:83]
s_add_u32 m0, s67, 3072
s_add_u32 s85, s66, 165888
buffer_load_dwordx4 v252, s[36:39], s85 offen offset:0 lds
MM a[80:83],a[164:167],v[20:23],a[80:83]
EX v115,v115
MM a[88:91],a[164:167],v[28:31],a[88:91]
RD a[252:255],v227 offset:12288
CV v85,v114,v115
MM a[88:91],a[160:163],v[24:27],a[88:91]
EX v116,v116
MM a[92:95],a[168:171],v[24:27],a[92:95]
EX v117,v117
MM a[92:95],a[172:175],v[28:31],a[92:95]
s_add_u32 m0, s65, 98304
s_nop 0
buffer_load_dwordx4 v235, s[12:15], s81 offen offset:2048 lds
MM a[84:87],a[172:175],v[20:23],a[84:87]
EX v118,v118
MM a[84:87],a[168:171],v[16:19],a[84:87]
EX v119,v119
MM a[76:79],a[168:171],v[8:11],a[76:79]
buffer_load_dwordx4 v235, s[12:15], s64 offen offset:3072 lds
MM a[76:79],a[172:175],v[12:15],a[76:79]
EX v120,v120
MM a[68:71],a[172:175],v[4:7],a[68:71]
EX v121,v121
MM a[68:71],a[168:171],v[0:3],a[68:71]
EX v122,v122
MM a[96:99],a[176:179],v[0:3],a[96:99]
EX v123,v123
MM a[96:99],a[180:183],v[4:7],a[96:99]
EX v124,v124
MM a[104:107],a[180:183],v[12:15],a[104:107]
EX v125,v125
MM a[104:107],a[176:179],v[8:11],a[104:107]
EX v126,v126
MM a[112:115],a[176:179],v[16:19],a[112:115]
EX v127,v127
MM a[112:115],a[180:183],v[20:23],a[112:115]
CV v86,v116,v117
CV v87,v118,v119
MM a[120:123],a[180:183],v[28:31],a[120:123]
CV v92,v120,v121
CV v93,v122,v123
MM a[120:123],a[176:179],v[24:27],a[120:123]
CV v94,v124,v125
CV v95,v126,v127
MM a[124:127],a[184:187],v[24:27],a[124:127]
v_add_f32 v244, v144, v145
v_add_f32 v245, v148, v149
MM a[124:127],a[188:191],v[28:31],a[124:127]
v_add_f32 v246, v152, v153
v_add_f32 v247, v156, v157
MM a[116:119],a[188:191],v[20:23],a[116:119]
MM a[116:119],a[184:187],v[16:19],a[116:119]
MM a[108:111],a[184:187],v[8:11],a[108:111]
MM a[108:111],a[188:191],v[12:15],a[108:111]
MM a[100:103],a[188:191],v[4:7],a[100:103]
MM a[100:103],a[184:187],v[0:3],a[100:103]
v_smfmac_f32_16x16x64_bf16 v[144:147], v[236:239], v[64:71], v234
v_smfmac_f32_16x16x64_bf16 v[148:151], v[236:239], v[72:79], v234
v_smfmac_f32_16x16x64_bf16 v[152:155], v[236:239], v[80:87], v234
v_smfmac_f32_16x16x64_bf16 v[156:159], v[236:239], v[88:95], v234
s_waitcnt lgkmcnt(0)
MM v[0:3],a[192:195],v[160:163],v[128:131]
s_add_u32 s64, s64, 16384
TR a[128:129],v232 offset:49152
s_add_u32 s81, s64, 896
MM v[0:3],a[196:199],v[176:179],v[0:3]
TR a[130:131],v232 offset:53248
v_max3_f32 v249, v144, v145, v148
MM v[0:3],a[200:203],v[192:195],v[0:3]
TR a[132:133],v232 offset:57344
v_max3_f32 v249, v249, v149, v152
MM v[0:3],a[204:207],v[208:211],v[0:3]
TR a[134:135],v232 offset:61440
v_max3_f32 v249, v249, v153, v156
MM v[4:7],a[220:223],v[208:211],v[128:131]
TR a[136:137],v233 offset:49152
v_max_f32 v249, v249, v157
MM v[4:7],a[216:219],v[192:195],v[4:7]
TR a[138:139],v233 offset:53248
v_cmp_lt_f32 vcc, s71, v249
MM v[4:7],a[212:215],v[176:179],v[4:7]
TR a[140:141],v233 offset:57344
TR a[142:143],v233 offset:61440
MM v[4:7],a[208:211],v[160:163],v[4:7]
s_cbranch_vccnz LSITE11_%=
LBACK11_%=:
MM v[12:15],a[208:211],v[164:167],v[132:135]
s_add_u32 m0, s67, 4096
s_add_u32 s85, s66, 221184
buffer_load_dwordx4 v252, s[36:39], s85 offen offset:0 lds
MM v[12:15],a[212:215],v[180:183],v[12:15]
EX v0,v0
MM v[12:15],a[216:219],v[196:199],v[12:15]
EX v1,v1
MM v[12:15],a[220:223],v[212:215],v[12:15]
TR a[144:145],v232 offset:49664
CV v0,v0,v1
MM v[8:11],a[204:207],v[212:215],v[132:135]
EX v2,v2
MM v[8:11],a[200:203],v[196:199],v[8:11]
EX v3,v3
MM v[8:11],a[196:199],v[180:183],v[8:11]
TR a[146:147],v232 offset:53760
CV v1,v2,v3
MM v[8:11],a[192:195],v[164:167],v[8:11]
EX v4,v4
MM v[16:19],a[192:195],v[168:171],v[136:139]
TR a[148:149],v232 offset:57856
TR a[150:151],v232 offset:61952
MM v[16:19],a[196:199],v[184:187],v[16:19]
EX v5,v5
MM v[16:19],a[200:203],v[200:203],v[16:19]
TR a[152:153],v233 offset:49664
CV v2,v4,v5
MM v[16:19],a[204:207],v[216:219],v[16:19]
EX v6,v6
MM v[20:23],a[220:223],v[216:219],v[136:139]
EX v7,v7
MM v[20:23],a[216:219],v[200:203],v[20:23]
TR a[154:155],v233 offset:53760
CV v3,v6,v7
MM v[20:23],a[212:215],v[184:187],v[20:23]
EX v8,v8
MM v[20:23],a[208:211],v[168:171],v[20:23]
TR a[156:157],v233 offset:57856
TR a[158:159],v233 offset:61952
MM v[28:31],a[208:211],v[172:175],v[140:143]
EX v9,v9
MM v[28:31],a[212:215],v[188:191],v[28:31]
TR a[160:161],v232 offset:50176
CV v8,v8,v9
MM v[28:31],a[216:219],v[204:207],v[28:31]
EX v10,v10
MM v[28:31],a[220:223],v[220:223],v[28:31]
EX v11,v11
MM v[24:27],a[204:207],v[220:223],v[140:143]
TR a[162:163],v232 offset:54272
CV v9,v10,v11
MM v[24:27],a[200:203],v[204:207],v[24:27]
TR a[164:165],v232 offset:58368
TR a[166:167],v232 offset:62464
MM v[24:27],a[196:199],v[188:191],v[24:27]
EX v12,v12
MM v[24:27],a[192:195],v[172:175],v[24:27]
EX v13,v13
MM v[32:35],a[224:227],v[160:163],v[128:131]
TR a[168:169],v233 offset:50176
CV v10,v12,v13
MM v[32:35],a[228:231],v[176:179],v[32:35]
EX v14,v14
MM v[32:35],a[232:235],v[192:195],v[32:35]
TR a[170:171],v233 offset:54272
TR a[172:173],v233 offset:58368
MM v[32:35],a[236:239],v[208:211],v[32:35]
EX v15,v15
MM v[36:39],a[252:255],v[208:211],v[128:131]
TR a[174:175],v233 offset:62464
CV v11,v14,v15
MM v[36:39],a[248:251],v[192:195],v[36:39]
EX v16,v16
MM v[36:39],a[244:247],v[176:179],v[36:39]
EX v17,v17
MM v[36:39],a[240:243],v[160:163],v[36:39]
TR a[176:177],v232 offset:50688
CV v16,v16,v17
MM v[44:47],a[240:243],v[164:167],v[132:135]
EX v18,v18
MM v[44:47],a[244:247],v[180:183],v[44:47]
TR a[178:179],v232 offset:54784
TR a[180:181],v232 offset:58880
MM v[44:47],a[248:251],v[196:199],v[44:47]
EX v19,v19
MM v[44:47],a[252:255],v[212:215],v[44:47]
TR a[182:183],v232 offset:62976
CV v17,v18,v19
MM v[40:43],a[236:239],v[212:215],v[132:135]
EX v20,v20
MM v[40:43],a[232:235],v[196:199],v[40:43]
EX v21,v21
MM v[40:43],a[228:231],v[180:183],v[40:43]
TR a[184:185],v233 offset:50688
CV v18,v20,v21
MM v[40:43],a[224:227],v[164:167],v[40:43]
EX v22,v22
MM v[48:51],a[224:227],v[168:171],v[136:139]
TR a[186:187],v233 offset:54784
TR a[188:189],v233 offset:58880
MM v[48:51],a[228:231],v[184:187],v[48:51]
EX v23,v23
MM v[48:51],a[232:235],v[200:203],v[48:51]
TR a[190:191],v233 offset:62976
CV v19,v22,v23
MM v[48:51],a[236:239],v[216:219],v[48:51]
EX v24,v24
MM v[52:55],a[252:255],v[216:219],v[136:139]
EX v25,v25
MM v[52:55],a[248:251],v[200:203],v[52:55]
EX v26,v26
MM v[52:55],a[244:247],v[184:187],v[52:55]
EX v27,v27
MM v[52:55],a[240:243],v[168:171],v[52:55]
EX v28,v28
MM v[60:63],a[240:243],v[172:175],v[140:143]
EX v29,v29
MM v[60:63],a[244:247],v[188:191],v[60:63]
EX v30,v30
s_nop 0
LPG11_%=:
s_cbranch_execz LTR0_%=
MM v[60:63],a[248:251],v[204:207],v[60:63]
EX v31,v31
MM v[60:63],a[252:255],v[220:223],v[60:63]
EX v32,v32
MM v[56:59],a[236:239],v[220:223],v[140:143]
EX v33,v33
MM v[56:59],a[232:235],v[204:207],v[56:59]
EX v34,v34
MM v[56:59],a[228:231],v[188:191],v[56:59]
EX v35,v35
MM v[56:59],a[224:227],v[172:175],v[56:59]
s_waitcnt vmcnt(17) lgkmcnt(0)
s_barrier
MM a[0:3],a[128:131],v[64:67],a[0:3]
s_mov_b32 s86, 94208
v_bfe_u32 v251, v248, 4, 3
v_lshlrev_b32 v251, 3, v251
buffer_load_dwordx2 v[252:253], v251, s[20:23], s86 offen offset:0 sc1
MM a[0:3],a[132:135],v[68:71],a[0:3]
s_add_u32 m0, s65, 49152
CV v24,v24,v25
CV v25,v26,v27
MM a[8:11],a[132:135],v[76:79],a[8:11]
RD a[192:195],v224 offset:16384
CV v26,v28,v29
MM a[8:11],a[128:131],v[72:75],a[8:11]
RD a[196:199],v225 offset:16384
CV v27,v30,v31
MM a[16:19],a[128:131],v[80:83],a[16:19]
buffer_load_dwordx4 v228, s[8:11], s64 offen offset:0 lds
MM a[16:19],a[132:135],v[84:87],a[16:19]
RD a[200:203],v226 offset:16384
CV v4,v32,v33
MM a[24:27],a[132:135],v[92:95],a[24:27]
RD a[204:207],v227 offset:16384
CV v5,v34,v35
MM a[24:27],a[128:131],v[88:91],a[24:27]
buffer_load_dwordx4 v229, s[8:11], s64 offen offset:1024 lds
MM a[28:31],a[136:139],v[88:91],a[28:31]
RD a[208:211],v224 offset:20480
RD a[212:215],v225 offset:20480
MM a[28:31],a[140:143],v[92:95],a[28:31]
EX v36,v36
MM a[20:23],a[140:143],v[84:87],a[20:23]
buffer_load_dwordx4 v230, s[8:11], s64 offen offset:2048 lds
MM a[20:23],a[136:139],v[80:83],a[20:23]
EX v37,v37
MM a[12:15],a[136:139],v[72:75],a[12:15]
RD a[216:219],v226 offset:20480
CV v6,v36,v37
MM a[12:15],a[140:143],v[76:79],a[12:15]
EX v38,v38
MM a[4:7],a[140:143],v[68:71],a[4:7]
EX v39,v39
MM a[4:7],a[136:139],v[64:67],a[4:7]
RD a[220:223],v227 offset:20480
CV v7,v38,v39
MM a[32:35],a[144:147],v[64:67],a[32:35]
EX v40,v40
MM a[32:35],a[148:151],v[68:71],a[32:35]
buffer_load_dwordx4 v231, s[8:11], s64 offen offset:3072 lds
MM a[40:43],a[148:151],v[76:79],a[40:43]
s_add_u32 m0, s65, 114688
EX v41,v41
MM a[40:43],a[144:147],v[72:75],a[40:43]
RD a[224:227],v224 offset:24576
CV v12,v40,v41
MM a[48:51],a[144:147],v[80:83],a[48:51]
EX v42,v42
MM a[48:51],a[148:151],v[84:87],a[48:51]
EX v43,v43
MM a[56:59],a[148:151],v[92:95],a[56:59]
RD a[228:231],v225 offset:24576
CV v13,v42,v43
MM a[56:59],a[144:147],v[88:91],a[56:59]
EX v44,v44
MM a[60:63],a[152:155],v[88:91],a[60:63]
buffer_load_dwordx4 v235, s[12:15], s81 offen offset:0 lds
MM a[60:63],a[156:159],v[92:95],a[60:63]
EX v45,v45
MM a[52:55],a[156:159],v[84:87],a[52:55]
RD a[232:235],v226 offset:24576
CV v14,v44,v45
MM a[52:55],a[152:155],v[80:83],a[52:55]
EX v46,v46
MM a[44:47],a[152:155],v[72:75],a[44:47]
EX v47,v47
MM a[44:47],a[156:159],v[76:79],a[44:47]
RD a[236:239],v227 offset:24576
CV v15,v46,v47
MM a[36:39],a[156:159],v[68:71],a[36:39]
buffer_load_dwordx4 v235, s[12:15], s64 offen offset:1024 lds
MM a[36:39],a[152:155],v[64:67],a[36:39]
EX v48,v48
MM a[64:67],a[160:163],v[64:67],a[64:67]
EX v49,v49
MM a[64:67],a[164:167],v[68:71],a[64:67]
RD a[240:243],v224 offset:28672
CV v20,v48,v49
MM a[72:75],a[164:167],v[76:79],a[72:75]
EX v50,v50
MM a[72:75],a[160:163],v[72:75],a[72:75]
RD a[244:247],v225 offset:28672
RD a[248:251],v226 offset:28672
MM a[80:83],a[160:163],v[80:83],a[80:83]
EX v51,v51
MM a[80:83],a[164:167],v[84:87],a[80:83]
RD a[252:255],v227 offset:28672
CV v21,v50,v51
MM a[88:91],a[164:167],v[92:95],a[88:91]
EX v52,v52
MM a[88:91],a[160:163],v[88:91],a[88:91]
buffer_load_dwordx4 v235, s[12:15], s81 offen offset:2048 lds
MM a[92:95],a[168:171],v[88:91],a[92:95]
EX v53,v53
MM a[92:95],a[172:175],v[92:95],a[92:95]
EX v54,v54
MM a[84:87],a[172:175],v[84:87],a[84:87]
buffer_load_dwordx4 v235, s[12:15], s64 offen offset:3072 lds
MM a[84:87],a[168:171],v[80:83],a[84:87]
EX v55,v55
MM a[76:79],a[168:171],v[72:75],a[76:79]
EX v56,v56
MM a[76:79],a[172:175],v[76:79],a[76:79]
EX v57,v57
MM a[68:71],a[172:175],v[68:71],a[68:71]
EX v58,v58
MM a[68:71],a[168:171],v[64:67],a[68:71]
EX v59,v59
MM a[96:99],a[176:179],v[64:67],a[96:99]
EX v60,v60
MM a[96:99],a[180:183],v[68:71],a[96:99]
EX v61,v61
MM a[104:107],a[180:183],v[76:79],a[104:107]
EX v62,v62
MM a[104:107],a[176:179],v[72:75],a[104:107]
EX v63,v63
MM a[112:115],a[176:179],v[80:83],a[112:115]
CV v22,v52,v53
CV v23,v54,v55
MM a[112:115],a[180:183],v[84:87],a[112:115]
CV v28,v56,v57
CV v29,v58,v59
MM a[120:123],a[180:183],v[92:95],a[120:123]
CV v30,v60,v61
CV v31,v62,v63
MM a[120:123],a[176:179],v[88:91],a[120:123]
MM a[124:127],a[184:187],v[88:91],a[124:127]
v_add_f32 v244, v144, v145
v_add_f32 v245, v148, v149
MM a[124:127],a[188:191],v[92:95],a[124:127]
v_add_f32 v246, v152, v153
v_add_f32 v247, v156, v157
MM a[116:119],a[188:191],v[84:87],a[116:119]
MM a[116:119],a[184:187],v[80:83],a[116:119]
MM a[108:111],a[184:187],v[72:75],a[108:111]
MM a[108:111],a[188:191],v[76:79],a[108:111]
MM a[100:103],a[188:191],v[68:71],a[100:103]
MM a[100:103],a[184:187],v[64:67],a[100:103]
v_smfmac_f32_16x16x64_bf16 v[144:147], v[236:239], v[0:7], v234
v_smfmac_f32_16x16x64_bf16 v[148:151], v[236:239], v[8:15], v234
v_smfmac_f32_16x16x64_bf16 v[152:155], v[236:239], v[16:23], v234
v_smfmac_f32_16x16x64_bf16 v[156:159], v[236:239], v[24:31], v234
s_waitcnt lgkmcnt(0)
MM v[64:67],a[192:195],v[160:163],v[128:131]
s_add_u32 s64, s64, 16384
TR a[128:129],v232 offset:0
s_add_u32 s81, s64, 896
)ASM"
R"ASM(
MM v[64:67],a[196:199],v[176:179],v[64:67]
TR a[130:131],v232 offset:4096
v_max3_f32 v249, v144, v145, v148
MM v[64:67],a[200:203],v[192:195],v[64:67]
TR a[132:133],v232 offset:8192
v_max3_f32 v249, v249, v149, v152
MM v[64:67],a[204:207],v[208:211],v[64:67]
TR a[134:135],v232 offset:12288
v_max3_f32 v249, v249, v153, v156
MM v[68:71],a[220:223],v[208:211],v[128:131]
TR a[136:137],v233 offset:0
v_max_f32 v249, v249, v157
MM v[68:71],a[216:219],v[192:195],v[68:71]
TR a[138:139],v233 offset:4096
v_cmp_lt_f32 vcc, s71, v249
MM v[68:71],a[212:215],v[176:179],v[68:71]
TR a[140:141],v233 offset:8192
TR a[142:143],v233 offset:12288
MM v[68:71],a[208:211],v[160:163],v[68:71]
s_cbranch_vccnz LSITE12_%=
LBACK12_%=:
MM v[76:79],a[208:211],v[164:167],v[132:135]
s_waitcnt vmcnt(8)
v_cmp_ne_u32 vcc, s69, v253
s_cbranch_vccnz LRIX_%=
LRIXb_%=:
MM v[76:79],a[212:215],v[180:183],v[76:79]
s_and_b32 s90, s17, 7
v_and_b32 v253, 65535, v252
v_readlane_b32 s19, v252, s90
MM v[76:79],a[216:219],v[196:199],v[76:79]
v_sub_u32 v253, 65535, v253
v_mul_u32_u24 v253, 1280, v253
MM v[76:79],a[220:223],v[212:215],v[76:79]
v_lshrrev_b32 v252, 7, v248
v_lshl_add_u32 v253, v252, 4, v253
MM v[72:75],a[204:207],v[212:215],v[132:135]
s_mul_i32 s91, %[swave], 3072
EX v64,v64
MM v[72:75],a[200:203],v[196:199],v[72:75]
s_add_u32 s91, s91, %[slds]
EX v65,v65
MM v[72:75],a[196:199],v[180:183],v[72:75]
s_bfe_u32 s90, s48, 131077
EX v66,v66
MM v[72:75],a[192:195],v[164:167],v[72:75]
s_mul_i32 s90, s90, 320
EX v67,v67
MM v[80:83],a[192:195],v[168:171],v[136:139]
s_add_u32 s90, s90, 131072
s_add_u32 m0, s91, 151552
s_nop 0
buffer_load_dwordx4 v253, s[20:23], s90 offen offset:0 sc1 lds
MM v[80:83],a[196:199],v[184:187],v[80:83]
TR a[144:145],v232 offset:512
CV v64,v64,v65
MM v[80:83],a[200:203],v[200:203],v[80:83]
s_add_u32 m0, s91, 152448
s_nop 0
buffer_load_dwordx4 v253, s[20:23], s90 offen offset:128 sc1 lds
MM v[80:83],a[204:207],v[216:219],v[80:83]
EX v68,v68
MM v[84:87],a[220:223],v[216:219],v[136:139]
s_add_u32 m0, s91, 153344
s_nop 0
buffer_load_dwordx4 v253, s[20:23], s90 offen offset:256 sc1 lds
MM v[84:87],a[216:219],v[200:203],v[84:87]
EX v69,v69
MM v[84:87],a[212:215],v[184:187],v[84:87]
s_lshr_b32 s77, %[sb], 3
TR a[146:147],v232 offset:4608
TR a[148:149],v232 offset:8704
MM v[84:87],a[208:211],v[168:171],v[84:87]
s_lshl_b32 s77, s77, 2
EX v70,v70
MM v[92:95],a[208:211],v[172:175],v[140:143]
s_add_u32 s77, s77, %[swave]
EX v71,v71
MM v[92:95],a[212:215],v[188:191],v[92:95]
s_lshl_b32 s77, s77, 9
TR a[150:151],v232 offset:12800
CV v65,v66,v67
MM v[92:95],a[216:219],v[204:207],v[92:95]
s_add_u32 s77, s77, 0
v_lshrrev_b32 v253, 1, v248
v_add_u32 v253, s77, v253
MM v[92:95],a[220:223],v[220:223],v[92:95]
s_mov_b32 s77, LKEND_ka-LPD0_%=-16
TR a[152:153],v233 offset:512
v_min_u32 v253, s77, v253
EX v72,v72
MM v[88:91],a[204:207],v[220:223],v[140:143]
s_getpc_b64 s[98:99]
LPD0_%=:
global_load_dword v253, v253, s[98:99]
MM v[88:91],a[200:203],v[204:207],v[88:91]
s_add_u32 s67, s95, 1024
EX v73,v73
MM v[88:91],a[196:199],v[188:191],v[88:91]
s_mov_b32 s70, 5
TR a[154:155],v233 offset:4608
CV v66,v68,v69
MM v[88:91],a[192:195],v[172:175],v[88:91]
EX v74,v74
MM v[96:99],a[224:227],v[160:163],v[128:131]
TR a[156:157],v233 offset:8704
TR a[158:159],v233 offset:12800
EX v75,v75
MM v[96:99],a[228:231],v[176:179],v[96:99]
TR a[160:161],v232 offset:1024
CV v67,v70,v71
MM v[96:99],a[232:235],v[192:195],v[96:99]
EX v76,v76
MM v[96:99],a[236:239],v[208:211],v[96:99]
EX v77,v77
MM v[100:103],a[252:255],v[208:211],v[128:131]
TR a[162:163],v232 offset:5120
CV v72,v72,v73
MM v[100:103],a[248:251],v[192:195],v[100:103]
EX v78,v78
MM v[100:103],a[244:247],v[176:179],v[100:103]
TR a[164:165],v232 offset:9216
CV v73,v74,v75
MM v[100:103],a[240:243],v[160:163],v[100:103]
EX v79,v79
MM v[108:111],a[240:243],v[164:167],v[132:135]
TR a[166:167],v232 offset:13312
TR a[168:169],v233 offset:1024
MM v[108:111],a[244:247],v[180:183],v[108:111]
EX v80,v80
MM v[108:111],a[248:251],v[196:199],v[108:111]
EX v81,v81
MM v[108:111],a[252:255],v[212:215],v[108:111]
TR a[170:171],v233 offset:5120
CV v74,v76,v77
MM v[104:107],a[236:239],v[212:215],v[132:135]
EX v82,v82
MM v[104:107],a[232:235],v[196:199],v[104:107]
TR a[172:173],v233 offset:9216
CV v75,v78,v79
MM v[104:107],a[228:231],v[180:183],v[104:107]
EX v83,v83
MM v[104:107],a[224:227],v[164:167],v[104:107]
TR a[174:175],v233 offset:13312
TR a[176:177],v232 offset:1536
MM v[112:115],a[224:227],v[168:171],v[136:139]
EX v84,v84
MM v[112:115],a[228:231],v[184:187],v[112:115]
TR a[178:179],v232 offset:5632
CV v80,v80,v81
MM v[112:115],a[232:235],v[200:203],v[112:115]
EX v85,v85
MM v[112:115],a[236:239],v[216:219],v[112:115]
TR a[180:181],v232 offset:9728
CV v81,v82,v83
MM v[116:119],a[252:255],v[216:219],v[136:139]
EX v86,v86
MM v[116:119],a[248:251],v[200:203],v[116:119]
TR a[182:183],v232 offset:13824
TR a[184:185],v233 offset:1536
MM v[116:119],a[244:247],v[184:187],v[116:119]
EX v87,v87
MM v[116:119],a[240:243],v[168:171],v[116:119]
EX v88,v88
MM v[124:127],a[240:243],v[172:175],v[140:143]
TR a[186:187],v233 offset:5632
CV v82,v84,v85
MM v[124:127],a[244:247],v[188:191],v[124:127]
EX v89,v89
MM v[124:127],a[248:251],v[204:207],v[124:127]
TR a[188:189],v233 offset:9728
TR a[190:191],v233 offset:13824
MM v[124:127],a[252:255],v[220:223],v[124:127]
EX v90,v90
MM v[120:123],a[236:239],v[220:223],v[140:143]
CV v83,v86,v87
CV v88,v88,v89
MM v[120:123],a[232:235],v[204:207],v[120:123]
EX v91,v91
MM v[120:123],a[228:231],v[188:191],v[120:123]
EX v92,v92
MM v[120:123],a[224:227],v[172:175],v[120:123]
s_waitcnt lgkmcnt(0)
s_barrier
MM a[0:3],a[128:131],v[0:3],a[0:3]
s_add_u32 m0, s65, 0
EX v93,v93
MM a[0:3],a[132:135],v[4:7],a[0:3]
CV v89,v90,v91
CV v90,v92,v93
MM a[8:11],a[132:135],v[12:15],a[8:11]
EX v94,v94
MM a[8:11],a[128:131],v[8:11],a[8:11]
EX v95,v95
MM a[16:19],a[128:131],v[16:19],a[16:19]
RD a[192:195],v224 offset:32768
CV v91,v94,v95
MM a[16:19],a[132:135],v[20:23],a[16:19]
EX v96,v96
MM a[24:27],a[132:135],v[28:31],a[24:27]
buffer_load_dwordx4 v228, s[8:11], s64 offen offset:0 lds
MM a[24:27],a[128:131],v[24:27],a[24:27]
EX v97,v97
MM a[28:31],a[136:139],v[24:27],a[28:31]
RD a[196:199],v225 offset:32768
CV v68,v96,v97
MM a[28:31],a[140:143],v[28:31],a[28:31]
EX v98,v98
MM a[20:23],a[140:143],v[20:23],a[20:23]
EX v99,v99
MM a[20:23],a[136:139],v[16:19],a[20:23]
RD a[200:203],v226 offset:32768
CV v69,v98,v99
MM a[12:15],a[136:139],v[8:11],a[12:15]
EX v100,v100
MM a[12:15],a[140:143],v[12:15],a[12:15]
buffer_load_dwordx4 v229, s[8:11], s64 offen offset:1024 lds
MM a[4:7],a[140:143],v[4:7],a[4:7]
EX v101,v101
MM a[4:7],a[136:139],v[0:3],a[4:7]
RD a[204:207],v227 offset:32768
CV v70,v100,v101
MM a[32:35],a[144:147],v[0:3],a[32:35]
EX v102,v102
MM a[32:35],a[148:151],v[4:7],a[32:35]
EX v103,v103
MM a[40:43],a[148:151],v[12:15],a[40:43]
RD a[208:211],v224 offset:36864
CV v71,v102,v103
MM a[40:43],a[144:147],v[8:11],a[40:43]
EX v104,v104
MM a[48:51],a[144:147],v[16:19],a[48:51]
buffer_load_dwordx4 v230, s[8:11], s64 offen offset:2048 lds
MM a[48:51],a[148:151],v[20:23],a[48:51]
EX v105,v105
MM a[56:59],a[148:151],v[28:31],a[56:59]
RD a[212:215],v225 offset:36864
CV v76,v104,v105
MM a[56:59],a[144:147],v[24:27],a[56:59]
EX v106,v106
MM a[60:63],a[152:155],v[24:27],a[60:63]
EX v107,v107
MM a[60:63],a[156:159],v[28:31],a[60:63]
RD a[216:219],v226 offset:36864
CV v77,v106,v107
MM a[52:55],a[156:159],v[20:23],a[52:55]
EX v108,v108
MM a[52:55],a[152:155],v[16:19],a[52:55]
buffer_load_dwordx4 v231, s[8:11], s64 offen offset:3072 lds
MM a[44:47],a[152:155],v[8:11],a[44:47]
s_add_u32 m0, s65, 65536
EX v109,v109
MM a[44:47],a[156:159],v[12:15],a[44:47]
RD a[220:223],v227 offset:36864
CV v78,v108,v109
MM a[36:39],a[156:159],v[4:7],a[36:39]
EX v110,v110
MM a[36:39],a[152:155],v[0:3],a[36:39]
RD a[224:227],v224 offset:40960
RD a[228:231],v225 offset:40960
MM a[64:67],a[160:163],v[0:3],a[64:67]
EX v111,v111
MM a[64:67],a[164:167],v[4:7],a[64:67]
RD a[232:235],v226 offset:40960
CV v79,v110,v111
MM a[72:75],a[164:167],v[12:15],a[72:75]
EX v112,v112
MM a[72:75],a[160:163],v[8:11],a[72:75]
buffer_load_dwordx4 v235, s[12:15], s81 offen offset:0 lds
MM a[80:83],a[160:163],v[16:19],a[80:83]
EX v113,v113
MM a[80:83],a[164:167],v[20:23],a[80:83]
RD a[236:239],v227 offset:40960
CV v84,v112,v113
MM a[88:91],a[164:167],v[28:31],a[88:91]
EX v114,v114
MM a[88:91],a[160:163],v[24:27],a[88:91]
EX v115,v115
MM a[92:95],a[168:171],v[24:27],a[92:95]
RD a[240:243],v224 offset:45056
CV v85,v114,v115
MM a[92:95],a[172:175],v[28:31],a[92:95]
EX v116,v116
MM a[84:87],a[172:175],v[20:23],a[84:87]
buffer_load_dwordx4 v235, s[12:15], s64 offen offset:1024 lds
MM a[84:87],a[168:171],v[16:19],a[84:87]
EX v117,v117
MM a[76:79],a[168:171],v[8:11],a[76:79]
RD a[244:247],v225 offset:45056
CV v86,v116,v117
MM a[76:79],a[172:175],v[12:15],a[76:79]
EX v118,v118
MM a[68:71],a[172:175],v[4:7],a[68:71]
EX v119,v119
MM a[68:71],a[168:171],v[0:3],a[68:71]
RD a[248:251],v226 offset:45056
CV v87,v118,v119
MM a[96:99],a[176:179],v[0:3],a[96:99]
EX v120,v120
MM a[96:99],a[180:183],v[4:7],a[96:99]
buffer_load_dwordx4 v235, s[12:15], s81 offen offset:2048 lds
MM a[104:107],a[180:183],v[12:15],a[104:107]
EX v121,v121
MM a[104:107],a[176:179],v[8:11],a[104:107]
RD a[252:255],v227 offset:45056
CV v92,v120,v121
MM a[112:115],a[176:179],v[16:19],a[112:115]
EX v122,v122
MM a[112:115],a[180:183],v[20:23],a[112:115]
EX v123,v123
MM a[120:123],a[180:183],v[28:31],a[120:123]
s_add_u32 s87, s95, 0
buffer_store_dword v240, v248, s[20:23], s87 offen offset:12 sc1
MM a[120:123],a[176:179],v[24:27],a[120:123]
EX v124,v124
MM a[124:127],a[184:187],v[24:27],a[124:127]
v_add_f32 v244, v144, v145
v_add_f32 v245, v148, v149
MM a[124:127],a[188:191],v[28:31],a[124:127]
v_add_f32 v246, v152, v153
v_add_f32 v247, v156, v157
MM a[116:119],a[188:191],v[20:23],a[116:119]
EX v125,v125
MM a[116:119],a[184:187],v[16:19],a[116:119]
buffer_load_dwordx4 v235, s[12:15], s64 offen offset:3072 lds
MM a[108:111],a[184:187],v[8:11],a[108:111]
EX v126,v126
MM a[108:111],a[188:191],v[12:15],a[108:111]
EX v127,v127
MM a[100:103],a[188:191],v[4:7],a[100:103]
CV v93,v122,v123
CV v94,v124,v125
MM a[100:103],a[184:187],v[0:3],a[100:103]
CV v95,v126,v127
v_smfmac_f32_16x16x64_bf16 v[144:147], v[236:239], v[64:71], v234
v_smfmac_f32_16x16x64_bf16 v[148:151], v[236:239], v[72:79], v234
v_smfmac_f32_16x16x64_bf16 v[152:155], v[236:239], v[80:87], v234
v_smfmac_f32_16x16x64_bf16 v[156:159], v[236:239], v[88:95], v234
s_waitcnt lgkmcnt(0)
s_cmp_ge_u32 s16, 108
s_cbranch_scc1 LLAP_%=
MM v[0:3],a[192:195],v[160:163],v[128:131]
s_add_u32 s64, s64, 16384
TR a[128:129],v232 offset:16384
s_add_u32 s81, s64, 896
MM v[0:3],a[196:199],v[176:179],v[0:3]
TR a[130:131],v232 offset:20480
v_max3_f32 v249, v144, v145, v148
MM v[0:3],a[200:203],v[192:195],v[0:3]
TR a[132:133],v232 offset:24576
v_max3_f32 v249, v249, v149, v152
MM v[0:3],a[204:207],v[208:211],v[0:3]
TR a[134:135],v232 offset:28672
v_max3_f32 v249, v249, v153, v156
MM v[4:7],a[220:223],v[208:211],v[128:131]
TR a[136:137],v233 offset:16384
v_max_f32 v249, v249, v157
MM v[4:7],a[216:219],v[192:195],v[4:7]
TR a[138:139],v233 offset:20480
v_cmp_lt_f32 vcc, s71, v249
MM v[4:7],a[212:215],v[176:179],v[4:7]
TR a[140:141],v233 offset:24576
TR a[142:143],v233 offset:28672
MM v[4:7],a[208:211],v[160:163],v[4:7]
s_cbranch_vccnz LSITE13_%=
LBACK13_%=:
MM v[12:15],a[208:211],v[164:167],v[132:135]
v_lshrrev_b32 v127, 8, v248
v_lshlrev_b32 v124, 9, v127
MM v[12:15],a[212:215],v[180:183],v[12:15]
v_and_b32 v126, 192, v248
v_add_u32 v124, v124, v126
MM v[12:15],a[216:219],v[196:199],v[12:15]
v_bfe_u32 v126, v248, 4, 2
v_lshl_add_u32 v124, v126, 3, v124
MM v[12:15],a[220:223],v[212:215],v[12:15]
s_lshr_b32 s76, %[swave], 1
EX v0,v0
MM v[8:11],a[204:207],v[212:215],v[132:135]
s_mul_i32 s76, s76, 10240
EX v1,v1
MM v[8:11],a[200:203],v[196:199],v[8:11]
s_and_b32 s77, %[swave], 1
EX v2,v2
MM v[8:11],a[196:199],v[180:183],v[8:11]
s_lshl_b32 s77, s77, 5
TR a[144:145],v232 offset:16896
)ASM"
R"ASM(
CV v0,v0,v1
MM v[8:11],a[192:195],v[164:167],v[8:11]
s_add_u32 s76, s76, s77
EX v3,v3
MM v[16:19],a[192:195],v[168:171],v[136:139]
s_add_u32 s76, s76, %[slds]
EX v4,v4
MM v[16:19],a[196:199],v[184:187],v[16:19]
s_add_u32 s76, s76, 131072
v_add_u32 v124, s76, v124
v_bfe_u32 v126, v248, 4, 3
MM v[16:19],a[200:203],v[200:203],v[16:19]
v_lshl_add_u32 v126, v127, 3, v126
v_lshlrev_b32 v126, 4, v126
MM v[16:19],a[204:207],v[216:219],v[16:19]
s_mul_i32 s76, %[swave], 3072
EX v5,v5
MM v[20:23],a[220:223],v[216:219],v[136:139]
s_add_u32 s76, s76, %[slds]
TR a[146:147],v232 offset:20992
CV v1,v2,v3
MM v[20:23],a[216:219],v[200:203],v[20:23]
s_add_u32 s76, s76, 151552
TR a[148:149],v232 offset:25088
v_add_u32 v125, s76, v126
MM v[20:23],a[212:215],v[184:187],v[20:23]
s_lshr_b32 s76, s48, 7
EX v6,v6
MM v[20:23],a[208:211],v[168:171],v[20:23]
s_lshl_b32 s76, s76, 5
EX v7,v7
MM v[28:31],a[208:211],v[172:175],v[140:143]
s_and_b32 s77, s48, 16
TR a[150:151],v232 offset:29184
TR a[152:153],v233 offset:16896
MM v[28:31],a[212:215],v[188:191],v[28:31]
s_add_u32 s76, s76, s77
EX v8,v8
MM v[28:31],a[216:219],v[204:207],v[28:31]
s_bfe_u32 s77, s48, 131077
TR a[154:155],v233 offset:20992
CV v2,v4,v5
MM v[28:31],a[220:223],v[220:223],v[28:31]
LPG12_%=:
s_cbranch_execz LTR0_%=
s_lshl_b32 s77, s77, 2
EX v9,v9
MM v[24:27],a[204:207],v[220:223],v[140:143]
s_add_u32 s76, s76, s77
TR a[156:157],v233 offset:25088
CV v3,v6,v7
MM v[24:27],a[200:203],v[204:207],v[24:27]
s_add_u32 s76, s76, %[swave]
EX v10,v10
MM v[24:27],a[196:199],v[188:191],v[24:27]
s_lshl_b32 s97, s76, 2
TR a[158:159],v233 offset:29184
CV v8,v8,v9
MM v[24:27],a[192:195],v[172:175],v[24:27]
s_add_u32 s97, s97, 77824
EX v11,v11
MM v[32:35],a[224:227],v[160:163],v[128:131]
s_lshl_b32 s76, s76, 9
TR a[160:161],v232 offset:17408
TR a[162:163],v232 offset:21504
MM v[32:35],a[228:231],v[176:179],v[32:35]
s_add_u32 s76, s76, 22183936
EX v12,v12
MM v[32:35],a[232:235],v[192:195],v[32:35]
EX v13,v13
MM v[32:35],a[236:239],v[208:211],v[32:35]
TR a[164:165],v232 offset:25600
CV v9,v10,v11
MM v[36:39],a[252:255],v[208:211],v[128:131]
EX v14,v14
MM v[36:39],a[248:251],v[192:195],v[36:39]
TR a[166:167],v232 offset:29696
CV v10,v12,v13
MM v[36:39],a[244:247],v[176:179],v[36:39]
EX v15,v15
MM v[36:39],a[240:243],v[160:163],v[36:39]
TR a[168:169],v233 offset:17408
TR a[170:171],v233 offset:21504
MM v[44:47],a[240:243],v[164:167],v[132:135]
EX v16,v16
MM v[44:47],a[244:247],v[180:183],v[44:47]
EX v17,v17
MM v[44:47],a[248:251],v[196:199],v[44:47]
TR a[172:173],v233 offset:25600
CV v11,v14,v15
MM v[44:47],a[252:255],v[212:215],v[44:47]
EX v18,v18
MM v[40:43],a[236:239],v[212:215],v[132:135]
TR a[174:175],v233 offset:29696
CV v16,v16,v17
MM v[40:43],a[232:235],v[196:199],v[40:43]
EX v19,v19
MM v[40:43],a[228:231],v[180:183],v[40:43]
TR a[176:177],v232 offset:17920
TR a[178:179],v232 offset:22016
MM v[40:43],a[224:227],v[164:167],v[40:43]
EX v20,v20
MM v[48:51],a[224:227],v[168:171],v[136:139]
EX v21,v21
MM v[48:51],a[228:231],v[184:187],v[48:51]
TR a[180:181],v232 offset:26112
CV v17,v18,v19
MM v[48:51],a[232:235],v[200:203],v[48:51]
EX v22,v22
MM v[48:51],a[236:239],v[216:219],v[48:51]
TR a[182:183],v232 offset:30208
CV v18,v20,v21
MM v[52:55],a[252:255],v[216:219],v[136:139]
EX v23,v23
MM v[52:55],a[248:251],v[200:203],v[52:55]
TR a[184:185],v233 offset:17920
TR a[186:187],v233 offset:22016
MM v[52:55],a[244:247],v[184:187],v[52:55]
EX v24,v24
MM v[52:55],a[240:243],v[168:171],v[52:55]
TR a[188:189],v233 offset:26112
CV v19,v22,v23
MM v[60:63],a[240:243],v[172:175],v[140:143]
EX v25,v25
MM v[60:63],a[244:247],v[188:191],v[60:63]
TR a[190:191],v233 offset:30208
CV v24,v24,v25
MM v[60:63],a[248:251],v[204:207],v[60:63]
EX v26,v26
MM v[60:63],a[252:255],v[220:223],v[60:63]
EX v27,v27
MM v[56:59],a[236:239],v[220:223],v[140:143]
EX v28,v28
MM v[56:59],a[232:235],v[204:207],v[56:59]
EX v29,v29
MM v[56:59],a[228:231],v[188:191],v[56:59]
s_getpc_b64 s[98:99]
LTA2_%=:
s_min_u32 s73, %[swave], 3
s_lshl_b32 s73, s73, 12
s_add_u32 s73, s73, LPG13_%=-LTA2_%=
s_add_u32 s98, s98, s73
s_addc_u32 s99, s99, 0
s_mov_b64 exec, 0
s_setpc_b64 s[98:99]
LTR2_%=:
s_mov_b64 exec, -1
CV v25,v26,v27
MM v[56:59],a[224:227],v[172:175],v[56:59]
s_waitcnt vmcnt(17) lgkmcnt(0)
s_barrier
MM a[0:3],a[128:131],v[64:67],a[0:3]
s_add_u32 m0, s65, 16384
RD a[192:195],v224 offset:49152
CV v26,v28,v29
MM a[0:3],a[132:135],v[68:71],a[0:3]
EX v30,v30
MM a[8:11],a[132:135],v[76:79],a[8:11]
EX v31,v31
MM a[8:11],a[128:131],v[72:75],a[8:11]
RD a[196:199],v225 offset:49152
CV v27,v30,v31
MM a[16:19],a[128:131],v[80:83],a[16:19]
s_waitcnt vmcnt(10)
TR v[96:97],v124 offset:0
TR v[98:99],v124 offset:256
MM a[16:19],a[132:135],v[84:87],a[16:19]
RD v[108:111],v125 offset:0
TR v[100:101],v124 offset:2048
MM a[24:27],a[132:135],v[92:95],a[24:27]
TR v[102:103],v124 offset:2304
RD v[112:115],v125 offset:512
MM a[24:27],a[128:131],v[88:91],a[24:27]
TR v[104:105],v124 offset:4096
TR v[106:107],v124 offset:4352
MM a[28:31],a[136:139],v[88:91],a[28:31]
RD v[116:119],v125 offset:1024
RD a[200:203],v226 offset:49152
EX v32,v32
MM a[28:31],a[140:143],v[92:95],a[28:31]
EX v33,v33
MM a[20:23],a[140:143],v[84:87],a[20:23]
EX v34,v34
MM a[20:23],a[136:139],v[80:83],a[20:23]
buffer_load_dwordx4 v228, s[8:11], s64 offen offset:0 lds
MM a[12:15],a[136:139],v[72:75],a[12:15]
EX v35,v35
MM a[12:15],a[140:143],v[76:79],a[12:15]
EX v36,v36
MM a[4:7],a[140:143],v[68:71],a[4:7]
CV v4,v32,v33
CV v5,v34,v35
MM a[4:7],a[136:139],v[64:67],a[4:7]
EX v37,v37
MM a[32:35],a[144:147],v[64:67],a[32:35]
EX v38,v38
MM a[32:35],a[148:151],v[68:71],a[32:35]
s_waitcnt lgkmcnt(1)
buffer_load_dwordx4 v229, s[8:11], s64 offen offset:1024 lds
MM a[40:43],a[148:151],v[76:79],a[40:43]
EX v39,v39
MM v[120:123],v[96:99],v[108:111],0
MM v[120:123],v[100:103],v[112:115],v[120:123]
MM v[120:123],v[104:107],v[116:119],v[120:123]
MM a[40:43],a[144:147],v[72:75],a[40:43]
TR v[96:97],v124 offset:6144
TR v[98:99],v124 offset:6400
MM a[48:51],a[144:147],v[80:83],a[48:51]
RD v[108:111],v125 offset:1536
TR v[100:101],v124 offset:8192
MM a[48:51],a[148:151],v[84:87],a[48:51]
TR v[102:103],v124 offset:8448
RD v[112:115],v125 offset:2048
MM a[56:59],a[148:151],v[92:95],a[56:59]
EX v40,v40
MM a[56:59],a[144:147],v[88:91],a[56:59]
EX v41,v41
MM a[60:63],a[152:155],v[88:91],a[60:63]
RD a[204:207],v227 offset:49152
CV v6,v36,v37
MM a[60:63],a[156:159],v[92:95],a[60:63]
EX v42,v42
MM a[52:55],a[156:159],v[84:87],a[52:55]
RD a[208:211],v224 offset:53248
CV v7,v38,v39
MM a[52:55],a[152:155],v[80:83],a[52:55]
EX v43,v43
MM a[44:47],a[152:155],v[72:75],a[44:47]
buffer_load_dwordx4 v230, s[8:11], s64 offen offset:2048 lds
MM a[44:47],a[156:159],v[76:79],a[44:47]
EX v44,v44
MM a[36:39],a[156:159],v[68:71],a[36:39]
s_waitcnt lgkmcnt(2)
EX v45,v45
MM a[36:39],a[152:155],v[64:67],a[36:39]
RD a[212:215],v225 offset:53248
CV v12,v40,v41
MM v[120:123],v[96:99],v[108:111],v[120:123]
MM v[120:123],v[100:103],v[112:115],v[120:123]
MM a[64:67],a[160:163],v[64:67],a[64:67]
EX v46,v46
MM a[64:67],a[164:167],v[68:71],a[64:67]
RD a[216:219],v226 offset:53248
CV v13,v42,v43
MM a[72:75],a[164:167],v[76:79],a[72:75]
EX v47,v47
MM a[72:75],a[160:163],v[72:75],a[72:75]
buffer_load_dwordx4 v231, s[8:11], s64 offen offset:3072 lds
MM a[80:83],a[160:163],v[80:83],a[80:83]
s_add_u32 m0, s65, 81920
EX v48,v48
MM a[80:83],a[164:167],v[84:87],a[80:83]
RD a[220:223],v227 offset:53248
CV v14,v44,v45
MM a[88:91],a[164:167],v[92:95],a[88:91]
s_mov_b32 exec_lo, 0x00ff00ff
s_mov_b32 exec_hi, 0x00ff00ff
buffer_store_dwordx4 v[120:123], v126, s[20:23], s76 offen offset:0 sc1
s_mov_b64 exec, -1
s_nop 0
MM a[88:91],a[160:163],v[88:91],a[88:91]
EX v49,v49
MM a[92:95],a[168:171],v[88:91],a[92:95]
EX v50,v50
MM a[92:95],a[172:175],v[92:95],a[92:95]
RD a[224:227],v224 offset:57344
CV v15,v46,v47
MM a[84:87],a[172:175],v[84:87],a[84:87]
EX v51,v51
MM a[84:87],a[168:171],v[80:83],a[84:87]
EX v52,v52
MM a[76:79],a[168:171],v[72:75],a[76:79]
buffer_load_dwordx4 v235, s[12:15], s81 offen offset:0 lds
MM a[76:79],a[172:175],v[76:79],a[76:79]
EX v53,v53
MM a[68:71],a[172:175],v[68:71],a[68:71]
RD a[228:231],v225 offset:57344
CV v20,v48,v49
MM a[68:71],a[168:171],v[64:67],a[68:71]
EX v54,v54
MM a[96:99],a[176:179],v[64:67],a[96:99]
RD a[232:235],v226 offset:57344
CV v21,v50,v51
MM a[96:99],a[180:183],v[68:71],a[96:99]
EX v55,v55
MM a[104:107],a[180:183],v[76:79],a[104:107]
RD a[236:239],v227 offset:57344
CV v22,v52,v53
MM a[104:107],a[176:179],v[72:75],a[104:107]
EX v56,v56
MM a[112:115],a[176:179],v[80:83],a[112:115]
buffer_load_dwordx4 v235, s[12:15], s64 offen offset:1024 lds
MM a[112:115],a[180:183],v[84:87],a[112:115]
EX v57,v57
MM a[120:123],a[180:183],v[92:95],a[120:123]
EX v58,v58
MM a[120:123],a[176:179],v[88:91],a[120:123]
RD a[240:243],v224 offset:61440
CV v23,v54,v55
MM a[124:127],a[184:187],v[88:91],a[124:127]
v_add_f32 v244, v144, v145
v_add_f32 v245, v148, v149
MM a[124:127],a[188:191],v[92:95],a[124:127]
v_add_f32 v246, v152, v153
v_add_f32 v247, v156, v157
MM a[116:119],a[188:191],v[84:87],a[116:119]
EX v59,v59
MM a[116:119],a[184:187],v[80:83],a[116:119]
EX v60,v60
MM a[108:111],a[184:187],v[72:75],a[108:111]
EX v61,v61
MM a[108:111],a[188:191],v[76:79],a[108:111]
RD a[244:247],v225 offset:61440
CV v28,v56,v57
MM a[100:103],a[188:191],v[68:71],a[100:103]
buffer_load_dwordx4 v235, s[12:15], s81 offen offset:2048 lds
MM a[100:103],a[184:187],v[64:67],a[100:103]
EX v62,v62
v_smfmac_f32_16x16x64_bf16 v[144:147], v[236:239], v[0:7], v234
RD a[248:251],v226 offset:61440
RD a[252:255],v227 offset:61440
EX v63,v63
v_smfmac_f32_16x16x64_bf16 v[148:151], v[236:239], v[8:15], v234
CV v29,v58,v59
v_smfmac_f32_16x16x64_bf16 v[152:155], v[236:239], v[16:23], v234
CV v30,v60,v61
CV v31,v62,v63
s_nop 1
v_smfmac_f32_16x16x64_bf16 v[156:159], v[236:239], v[24:31], v234
s_waitcnt lgkmcnt(0)
buffer_load_dwordx4 v235, s[12:15], s64 offen offset:3072 lds
MM v[64:67],a[192:195],v[160:163],v[128:131]
s_add_u32 s64, s64, 16384
TR a[128:129],v232 offset:32768
s_add_u32 s81, s64, 896
MM v[64:67],a[196:199],v[176:179],v[64:67]
TR a[130:131],v232 offset:36864
v_max3_f32 v249, v144, v145, v148
MM v[64:67],a[200:203],v[192:195],v[64:67]
TR a[132:133],v232 offset:40960
v_max3_f32 v249, v249, v149, v152
MM v[64:67],a[204:207],v[208:211],v[64:67]
TR a[134:135],v232 offset:45056
v_max3_f32 v249, v249, v153, v156
MM v[68:71],a[220:223],v[208:211],v[128:131]
TR a[136:137],v233 offset:32768
v_max_f32 v249, v249, v157
MM v[68:71],a[216:219],v[192:195],v[68:71]
TR a[138:139],v233 offset:36864
v_cmp_lt_f32 vcc, s71, v249
MM v[68:71],a[212:215],v[176:179],v[68:71]
TR a[140:141],v233 offset:40960
TR a[142:143],v233 offset:45056
MM v[68:71],a[208:211],v[160:163],v[68:71]
s_cbranch_vccnz LSITE14_%=
LBACK14_%=:
MM v[76:79],a[208:211],v[164:167],v[132:135]
s_add_u32 s48, s48, 16
v_lshrrev_b32 v252, 6, v248
v_mul_u32_u24 v252, 3456, v252
MM v[76:79],a[212:215],v[180:183],v[76:79]
v_and_b32 v251, 48, v248
v_add_u32 v252, v252, v251
MM v[76:79],a[216:219],v[196:199],v[76:79]
s_and_b32 s90, %[swave], 1
EX v64,v64
MM v[76:79],a[220:223],v[212:215],v[76:79]
s_mul_i32 s90, s90, 276480
EX v65,v65
MM v[72:75],a[204:207],v[212:215],v[132:135]
s_bfe_u32 s91, s48, 131077
EX v66,v66
MM v[72:75],a[200:203],v[196:199],v[72:75]
s_mul_i32 s91, s91, 552960
TR a[144:145],v232 offset:33280
CV v64,v64,v65
MM v[72:75],a[196:199],v[180:183],v[72:75]
s_add_u32 s90, s90, s91
EX v67,v67
MM v[72:75],a[192:195],v[164:167],v[72:75]
s_and_b32 s91, s48, 0xffffff80
TR a[146:147],v232 offset:37376
CV v65,v66,v67
MM v[80:83],a[192:195],v[168:171],v[136:139]
s_add_u32 s90, s90, s91
EX v68,v68
MM v[80:83],a[196:199],v[184:187],v[80:83]
s_and_b32 s91, s48, 16
TR a[148:149],v232 offset:41472
TR a[150:151],v232 offset:45568
MM v[80:83],a[200:203],v[200:203],v[80:83]
s_lshl_b32 s91, s91, 2
EX v69,v69
MM v[80:83],a[204:207],v[216:219],v[80:83]
s_add_u32 s66, s90, s91
TR a[152:153],v233 offset:33280
CV v66,v68,v69
MM v[84:87],a[220:223],v[216:219],v[136:139]
s_mul_i32 s91, %[swave], 5120
EX v70,v70
)ASM"
R"ASM(
MM v[84:87],a[216:219],v[200:203],v[84:87]
s_add_u32 s91, s91, %[slds]
EX v71,v71
MM v[84:87],a[212:215],v[184:187],v[84:87]
s_add_u32 s67, s91, 131072
TR a[154:155],v233 offset:37376
CV v67,v70,v71
MM v[84:87],a[208:211],v[168:171],v[84:87]
s_and_b32 s18, s17, 255
EX v72,v72
MM v[92:95],a[208:211],v[172:175],v[140:143]
s_sub_u32 s90, s18, 40
s_cmp_ge_u32 s18, 216
s_cselect_b32 s18, s90, s18
MM v[92:95],a[212:215],v[188:191],v[92:95]
s_lshl_b32 s18, s18, 4
EX v73,v73
MM v[92:95],a[216:219],v[204:207],v[92:95]
s_sub_u32 s90, s16, 90
TR a[156:157],v233 offset:41472
CV v72,v72,v73
MM v[92:95],a[220:223],v[220:223],v[92:95]
s_min_u32 s47, s90, s16
EX v74,v74
MM v[88:91],a[204:207],v[220:223],v[140:143]
s_mul_i32 s90, s47, 205
TR a[158:159],v233 offset:45568
TR a[160:161],v232 offset:33792
MM v[88:91],a[200:203],v[204:207],v[88:91]
s_lshr_b32 s46, s90, 11
EX v75,v75
MM v[88:91],a[196:199],v[188:191],v[88:91]
s_mul_i32 s90, s46, 244480
TR a[162:163],v232 offset:37888
CV v73,v74,v75
MM v[88:91],a[192:195],v[172:175],v[88:91]
s_lshl_b32 s91, s47, 7
EX v76,v76
MM v[96:99],a[224:227],v[160:163],v[128:131]
s_add_u32 s90, s90, s91
s_add_u32 s28, s28, s90
s_addc_u32 s29, s29, 0
MM v[96:99],a[228:231],v[176:179],v[96:99]
s_sub_u32 s30, 2211840, s90
EX v77,v77
MM v[96:99],a[232:235],v[192:195],v[96:99]
s_mov_b32 s31, 131072
TR a[164:165],v232 offset:41984
CV v74,v76,v77
MM v[96:99],a[236:239],v[208:211],v[96:99]
EX v78,v78
MM v[100:103],a[252:255],v[208:211],v[128:131]
TR a[166:167],v232 offset:46080
TR a[168:169],v233 offset:33792
MM v[100:103],a[248:251],v[192:195],v[100:103]
EX v79,v79
MM v[100:103],a[244:247],v[176:179],v[100:103]
TR a[170:171],v233 offset:37888
CV v75,v78,v79
MM v[100:103],a[240:243],v[160:163],v[100:103]
EX v80,v80
MM v[108:111],a[240:243],v[164:167],v[132:135]
TR a[172:173],v233 offset:41984
TR a[174:175],v233 offset:46080
MM v[108:111],a[244:247],v[180:183],v[108:111]
EX v81,v81
MM v[108:111],a[248:251],v[196:199],v[108:111]
EX v82,v82
MM v[108:111],a[252:255],v[212:215],v[108:111]
TR a[176:177],v232 offset:34304
CV v80,v80,v81
MM v[104:107],a[236:239],v[212:215],v[132:135]
EX v83,v83
MM v[104:107],a[232:235],v[196:199],v[104:107]
TR a[178:179],v232 offset:38400
CV v81,v82,v83
MM v[104:107],a[228:231],v[180:183],v[104:107]
EX v84,v84
MM v[104:107],a[224:227],v[164:167],v[104:107]
TR a[180:181],v232 offset:42496
TR a[182:183],v232 offset:46592
MM v[112:115],a[224:227],v[168:171],v[136:139]
EX v85,v85
MM v[112:115],a[228:231],v[184:187],v[112:115]
TR a[184:185],v233 offset:34304
CV v82,v84,v85
MM v[112:115],a[232:235],v[200:203],v[112:115]
EX v86,v86
MM v[112:115],a[236:239],v[216:219],v[112:115]
EX v87,v87
MM v[116:119],a[252:255],v[216:219],v[136:139]
TR a[186:187],v233 offset:38400
CV v83,v86,v87
MM v[116:119],a[248:251],v[200:203],v[116:119]
EX v88,v88
MM v[116:119],a[244:247],v[184:187],v[116:119]
TR a[188:189],v233 offset:42496
TR a[190:191],v233 offset:46592
MM v[116:119],a[240:243],v[168:171],v[116:119]
EX v89,v89
MM v[124:127],a[240:243],v[172:175],v[140:143]
EX v90,v90
MM v[124:127],a[244:247],v[188:191],v[124:127]
EX v91,v91
MM v[124:127],a[248:251],v[204:207],v[124:127]
EX v92,v92
MM v[124:127],a[252:255],v[220:223],v[124:127]
EX v93,v93
MM v[120:123],a[236:239],v[220:223],v[140:143]
EX v94,v94
MM v[120:123],a[232:235],v[204:207],v[120:123]
EX v95,v95
MM v[120:123],a[228:231],v[188:191],v[120:123]
EX v96,v96
MM v[120:123],a[224:227],v[172:175],v[120:123]
s_waitcnt vmcnt(14) lgkmcnt(0)
s_barrier
MM a[0:3],a[128:131],v[0:3],a[0:3]
s_add_u32 m0, s65, 32768
CV v88,v88,v89
CV v89,v90,v91
MM a[0:3],a[132:135],v[4:7],a[0:3]
RD a[192:195],v224 offset:0
CV v90,v92,v93
MM a[8:11],a[132:135],v[12:15],a[8:11]
RD a[196:199],v225 offset:0
CV v91,v94,v95
MM a[8:11],a[128:131],v[8:11],a[8:11]
buffer_load_dwordx4 v228, s[8:11], s64 offen offset:0 lds
MM a[16:19],a[128:131],v[16:19],a[16:19]
RD a[200:203],v226 offset:0
RD a[204:207],v227 offset:0
MM a[16:19],a[132:135],v[20:23],a[16:19]
EX v97,v97
MM a[24:27],a[132:135],v[28:31],a[24:27]
RD a[208:211],v224 offset:4096
CV v68,v96,v97
MM a[24:27],a[128:131],v[24:27],a[24:27]
EX v98,v98
MM a[28:31],a[136:139],v[24:27],a[28:31]
buffer_load_dwordx4 v229, s[8:11], s64 offen offset:1024 lds
MM a[28:31],a[140:143],v[28:31],a[28:31]
EX v99,v99
MM a[20:23],a[140:143],v[20:23],a[20:23]
RD a[212:215],v225 offset:4096
CV v69,v98,v99
MM a[20:23],a[136:139],v[16:19],a[20:23]
EX v100,v100
MM a[12:15],a[136:139],v[8:11],a[12:15]
EX v101,v101
MM a[12:15],a[140:143],v[12:15],a[12:15]
RD a[216:219],v226 offset:4096
CV v70,v100,v101
MM a[4:7],a[140:143],v[4:7],a[4:7]
EX v102,v102
MM a[4:7],a[136:139],v[0:3],a[4:7]
buffer_load_dwordx4 v230, s[8:11], s64 offen offset:2048 lds
MM a[32:35],a[144:147],v[0:3],a[32:35]
EX v103,v103
MM a[32:35],a[148:151],v[4:7],a[32:35]
RD a[220:223],v227 offset:4096
CV v71,v102,v103
MM a[40:43],a[148:151],v[12:15],a[40:43]
EX v104,v104
MM a[40:43],a[144:147],v[8:11],a[40:43]
EX v105,v105
MM a[48:51],a[144:147],v[16:19],a[48:51]
RD a[224:227],v224 offset:8192
CV v76,v104,v105
MM a[48:51],a[148:151],v[20:23],a[48:51]
EX v106,v106
MM a[56:59],a[148:151],v[28:31],a[56:59]
buffer_load_dwordx4 v231, s[8:11], s64 offen offset:3072 lds
MM a[56:59],a[144:147],v[24:27],a[56:59]
s_add_u32 m0, s65, 98304
EX v107,v107
MM a[60:63],a[152:155],v[24:27],a[60:63]
RD a[228:231],v225 offset:8192
CV v77,v106,v107
MM a[60:63],a[156:159],v[28:31],a[60:63]
EX v108,v108
MM a[52:55],a[156:159],v[20:23],a[52:55]
EX v109,v109
MM a[52:55],a[152:155],v[16:19],a[52:55]
RD a[232:235],v226 offset:8192
CV v78,v108,v109
MM a[44:47],a[152:155],v[8:11],a[44:47]
EX v110,v110
MM a[44:47],a[156:159],v[12:15],a[44:47]
buffer_load_dwordx4 v235, s[12:15], s81 offen offset:0 lds
MM a[36:39],a[156:159],v[4:7],a[36:39]
EX v111,v111
MM a[36:39],a[152:155],v[0:3],a[36:39]
RD a[236:239],v227 offset:8192
CV v79,v110,v111
MM a[64:67],a[160:163],v[0:3],a[64:67]
EX v112,v112
MM a[64:67],a[164:167],v[4:7],a[64:67]
EX v113,v113
MM a[72:75],a[164:167],v[12:15],a[72:75]
RD a[240:243],v224 offset:12288
CV v84,v112,v113
MM a[72:75],a[160:163],v[8:11],a[72:75]
buffer_load_dwordx4 v235, s[12:15], s64 offen offset:1024 lds
MM a[80:83],a[160:163],v[16:19],a[80:83]
s_waitcnt vmcnt(10)
v_mov_b32 v250, s69
s_mov_b64 exec, 1
buffer_store_dword v250, off, s[20:23], s97 offset:0 sc1
s_mov_b64 exec, -1
MM a[80:83],a[164:167],v[20:23],a[80:83]
s_add_u32 m0, s67, 0
s_add_u32 s85, s66, 0
buffer_load_dwordx4 v252, s[36:39], s85 offen offset:0 lds
MM a[88:91],a[164:167],v[28:31],a[88:91]
EX v114,v114
MM a[88:91],a[160:163],v[24:27],a[88:91]
EX v115,v115
MM a[92:95],a[168:171],v[24:27],a[92:95]
RD a[244:247],v225 offset:12288
CV v85,v114,v115
MM a[92:95],a[172:175],v[28:31],a[92:95]
EX v116,v116
MM a[84:87],a[172:175],v[20:23],a[84:87]
EX v117,v117
MM a[84:87],a[168:171],v[16:19],a[84:87]
RD a[248:251],v226 offset:12288
CV v86,v116,v117
MM a[76:79],a[168:171],v[8:11],a[76:79]
EX v118,v118
MM a[76:79],a[172:175],v[12:15],a[76:79]
EX v119,v119
MM a[68:71],a[172:175],v[4:7],a[68:71]
RD a[252:255],v227 offset:12288
CV v87,v118,v119
MM a[68:71],a[168:171],v[0:3],a[68:71]
s_add_u32 m0, s65, 98304
s_nop 0
buffer_load_dwordx4 v235, s[12:15], s81 offen offset:2048 lds
MM a[96:99],a[176:179],v[0:3],a[96:99]
EX v120,v120
MM a[96:99],a[180:183],v[4:7],a[96:99]
EX v121,v121
MM a[104:107],a[180:183],v[12:15],a[104:107]
EX v122,v122
s_nop 0
LPG13_%=:
s_cbranch_execz LTR2_%=
MM a[104:107],a[176:179],v[8:11],a[104:107]
buffer_load_dwordx4 v235, s[12:15], s64 offen offset:3072 lds
MM a[112:115],a[176:179],v[16:19],a[112:115]
EX v123,v123
MM a[112:115],a[180:183],v[20:23],a[112:115]
EX v124,v124
MM a[120:123],a[180:183],v[28:31],a[120:123]
EX v125,v125
MM a[120:123],a[176:179],v[24:27],a[120:123]
EX v126,v126
MM a[124:127],a[184:187],v[24:27],a[124:127]
v_add_f32 v244, v144, v145
v_add_f32 v245, v148, v149
MM a[124:127],a[188:191],v[28:31],a[124:127]
v_add_f32 v246, v152, v153
v_add_f32 v247, v156, v157
MM a[116:119],a[188:191],v[20:23],a[116:119]
EX v127,v127
MM a[116:119],a[184:187],v[16:19],a[116:119]
CV v92,v120,v121
CV v93,v122,v123
MM a[108:111],a[184:187],v[8:11],a[108:111]
CV v94,v124,v125
CV v95,v126,v127
MM a[108:111],a[188:191],v[12:15],a[108:111]
MM a[100:103],a[188:191],v[4:7],a[100:103]
MM a[100:103],a[184:187],v[0:3],a[100:103]
v_smfmac_f32_16x16x64_bf16 v[144:147], v[236:239], v[64:71], v234
v_smfmac_f32_16x16x64_bf16 v[148:151], v[236:239], v[72:79], v234
v_smfmac_f32_16x16x64_bf16 v[152:155], v[236:239], v[80:87], v234
v_smfmac_f32_16x16x64_bf16 v[156:159], v[236:239], v[88:95], v234
s_waitcnt lgkmcnt(0)
MM v[0:3],a[192:195],v[160:163],v[128:131]
s_add_u32 s64, s64, 16384
TR a[128:129],v232 offset:49152
s_add_u32 s81, s64, 896
MM v[0:3],a[196:199],v[176:179],v[0:3]
TR a[130:131],v232 offset:53248
v_max3_f32 v249, v144, v145, v148
MM v[0:3],a[200:203],v[192:195],v[0:3]
TR a[132:133],v232 offset:57344
v_max3_f32 v249, v249, v149, v152
MM v[0:3],a[204:207],v[208:211],v[0:3]
TR a[134:135],v232 offset:61440
v_max3_f32 v249, v249, v153, v156
MM v[4:7],a[220:223],v[208:211],v[128:131]
TR a[136:137],v233 offset:49152
v_max_f32 v249, v249, v157
MM v[4:7],a[216:219],v[192:195],v[4:7]
TR a[138:139],v233 offset:53248
v_cmp_lt_f32 vcc, s71, v249
MM v[4:7],a[212:215],v[176:179],v[4:7]
TR a[140:141],v233 offset:57344
TR a[142:143],v233 offset:61440
MM v[4:7],a[208:211],v[160:163],v[4:7]
s_cbranch_vccnz LSITE15_%=
LBACK15_%=:
MM v[12:15],a[208:211],v[164:167],v[132:135]
s_add_u32 m0, s67, 1024
s_add_u32 s85, s66, 55296
buffer_load_dwordx4 v252, s[36:39], s85 offen offset:0 lds
MM v[12:15],a[212:215],v[180:183],v[12:15]
EX v0,v0
MM v[12:15],a[216:219],v[196:199],v[12:15]
EX v1,v1
MM v[12:15],a[220:223],v[212:215],v[12:15]
TR a[144:145],v232 offset:49664
CV v0,v0,v1
MM v[8:11],a[204:207],v[212:215],v[132:135]
EX v2,v2
MM v[8:11],a[200:203],v[196:199],v[8:11]
EX v3,v3
MM v[8:11],a[196:199],v[180:183],v[8:11]
TR a[146:147],v232 offset:53760
CV v1,v2,v3
MM v[8:11],a[192:195],v[164:167],v[8:11]
EX v4,v4
MM v[16:19],a[192:195],v[168:171],v[136:139]
TR a[148:149],v232 offset:57856
TR a[150:151],v232 offset:61952
MM v[16:19],a[196:199],v[184:187],v[16:19]
EX v5,v5
MM v[16:19],a[200:203],v[200:203],v[16:19]
TR a[152:153],v233 offset:49664
CV v2,v4,v5
MM v[16:19],a[204:207],v[216:219],v[16:19]
EX v6,v6
MM v[20:23],a[220:223],v[216:219],v[136:139]
EX v7,v7
MM v[20:23],a[216:219],v[200:203],v[20:23]
TR a[154:155],v233 offset:53760
CV v3,v6,v7
MM v[20:23],a[212:215],v[184:187],v[20:23]
EX v8,v8
MM v[20:23],a[208:211],v[168:171],v[20:23]
TR a[156:157],v233 offset:57856
TR a[158:159],v233 offset:61952
MM v[28:31],a[208:211],v[172:175],v[140:143]
EX v9,v9
MM v[28:31],a[212:215],v[188:191],v[28:31]
TR a[160:161],v232 offset:50176
CV v8,v8,v9
MM v[28:31],a[216:219],v[204:207],v[28:31]
EX v10,v10
MM v[28:31],a[220:223],v[220:223],v[28:31]
EX v11,v11
MM v[24:27],a[204:207],v[220:223],v[140:143]
TR a[162:163],v232 offset:54272
CV v9,v10,v11
MM v[24:27],a[200:203],v[204:207],v[24:27]
TR a[164:165],v232 offset:58368
TR a[166:167],v232 offset:62464
MM v[24:27],a[196:199],v[188:191],v[24:27]
EX v12,v12
MM v[24:27],a[192:195],v[172:175],v[24:27]
EX v13,v13
MM v[32:35],a[224:227],v[160:163],v[128:131]
TR a[168:169],v233 offset:50176
CV v10,v12,v13
MM v[32:35],a[228:231],v[176:179],v[32:35]
EX v14,v14
MM v[32:35],a[232:235],v[192:195],v[32:35]
TR a[170:171],v233 offset:54272
TR a[172:173],v233 offset:58368
MM v[32:35],a[236:239],v[208:211],v[32:35]
EX v15,v15
MM v[36:39],a[252:255],v[208:211],v[128:131]
TR a[174:175],v233 offset:62464
CV v11,v14,v15
MM v[36:39],a[248:251],v[192:195],v[36:39]
EX v16,v16
MM v[36:39],a[244:247],v[176:179],v[36:39]
EX v17,v17
MM v[36:39],a[240:243],v[160:163],v[36:39]
TR a[176:177],v232 offset:50688
CV v16,v16,v17
MM v[44:47],a[240:243],v[164:167],v[132:135]
s_add_u32 m0, s67, 2048
s_add_u32 s85, s66, 110592
buffer_load_dwordx4 v252, s[36:39], s85 offen offset:0 lds
MM v[44:47],a[244:247],v[180:183],v[44:47]
EX v18,v18
MM v[44:47],a[248:251],v[196:199],v[44:47]
TR a[178:179],v232 offset:54784
TR a[180:181],v232 offset:58880
)ASM"
R"ASM(
MM v[44:47],a[252:255],v[212:215],v[44:47]
EX v19,v19
MM v[40:43],a[236:239],v[212:215],v[132:135]
TR a[182:183],v232 offset:62976
CV v17,v18,v19
MM v[40:43],a[232:235],v[196:199],v[40:43]
EX v20,v20
MM v[40:43],a[228:231],v[180:183],v[40:43]
EX v21,v21
MM v[40:43],a[224:227],v[164:167],v[40:43]
TR a[184:185],v233 offset:50688
CV v18,v20,v21
MM v[48:51],a[224:227],v[168:171],v[136:139]
EX v22,v22
MM v[48:51],a[228:231],v[184:187],v[48:51]
TR a[186:187],v233 offset:54784
TR a[188:189],v233 offset:58880
MM v[48:51],a[232:235],v[200:203],v[48:51]
EX v23,v23
MM v[48:51],a[236:239],v[216:219],v[48:51]
TR a[190:191],v233 offset:62976
CV v19,v22,v23
MM v[52:55],a[252:255],v[216:219],v[136:139]
EX v24,v24
MM v[52:55],a[248:251],v[200:203],v[52:55]
EX v25,v25
MM v[52:55],a[244:247],v[184:187],v[52:55]
EX v26,v26
MM v[52:55],a[240:243],v[168:171],v[52:55]
EX v27,v27
MM v[60:63],a[240:243],v[172:175],v[140:143]
EX v28,v28
MM v[60:63],a[244:247],v[188:191],v[60:63]
EX v29,v29
MM v[60:63],a[248:251],v[204:207],v[60:63]
EX v30,v30
MM v[60:63],a[252:255],v[220:223],v[60:63]
EX v31,v31
MM v[56:59],a[236:239],v[220:223],v[140:143]
EX v32,v32
MM v[56:59],a[232:235],v[204:207],v[56:59]
EX v33,v33
MM v[56:59],a[228:231],v[188:191],v[56:59]
EX v34,v34
MM v[56:59],a[224:227],v[172:175],v[56:59]
s_waitcnt lgkmcnt(0)
s_barrier
MM a[0:3],a[128:131],v[64:67],a[0:3]
s_add_u32 m0, s65, 49152
CV v24,v24,v25
CV v25,v26,v27
MM a[0:3],a[132:135],v[68:71],a[0:3]
RD a[192:195],v224 offset:16384
CV v26,v28,v29
MM a[8:11],a[132:135],v[76:79],a[8:11]
RD a[196:199],v225 offset:16384
CV v27,v30,v31
MM a[8:11],a[128:131],v[72:75],a[8:11]
buffer_load_dwordx4 v228, s[8:11], s64 offen offset:0 lds
MM a[16:19],a[128:131],v[80:83],a[16:19]
s_add_u32 m0, s67, 3072
s_add_u32 s85, s66, 165888
buffer_load_dwordx4 v252, s[36:39], s85 offen offset:0 lds
MM a[16:19],a[132:135],v[84:87],a[16:19]
RD a[200:203],v226 offset:16384
CV v4,v32,v33
MM a[24:27],a[132:135],v[92:95],a[24:27]
RD a[204:207],v227 offset:16384
RD a[208:211],v224 offset:20480
MM a[24:27],a[128:131],v[88:91],a[24:27]
s_add_u32 m0, s65, 49152
s_nop 0
buffer_load_dwordx4 v229, s[8:11], s64 offen offset:1024 lds
MM a[28:31],a[136:139],v[88:91],a[28:31]
EX v35,v35
MM a[28:31],a[140:143],v[92:95],a[28:31]
RD a[212:215],v225 offset:20480
CV v5,v34,v35
MM a[20:23],a[140:143],v[84:87],a[20:23]
EX v36,v36
MM a[20:23],a[136:139],v[80:83],a[20:23]
EX v37,v37
MM a[12:15],a[136:139],v[72:75],a[12:15]
RD a[216:219],v226 offset:20480
CV v6,v36,v37
MM a[12:15],a[140:143],v[76:79],a[12:15]
buffer_load_dwordx4 v230, s[8:11], s64 offen offset:2048 lds
MM a[4:7],a[140:143],v[68:71],a[4:7]
EX v38,v38
MM a[4:7],a[136:139],v[64:67],a[4:7]
EX v39,v39
MM a[32:35],a[144:147],v[64:67],a[32:35]
RD a[220:223],v227 offset:20480
CV v7,v38,v39
MM a[32:35],a[148:151],v[68:71],a[32:35]
EX v40,v40
MM a[40:43],a[148:151],v[76:79],a[40:43]
EX v41,v41
MM a[40:43],a[144:147],v[72:75],a[40:43]
RD a[224:227],v224 offset:24576
CV v12,v40,v41
MM a[48:51],a[144:147],v[80:83],a[48:51]
buffer_load_dwordx4 v231, s[8:11], s64 offen offset:3072 lds
MM a[48:51],a[148:151],v[84:87],a[48:51]
s_add_u32 m0, s65, 114688
EX v42,v42
MM a[56:59],a[148:151],v[92:95],a[56:59]
EX v43,v43
MM a[56:59],a[144:147],v[88:91],a[56:59]
RD a[228:231],v225 offset:24576
CV v13,v42,v43
MM a[60:63],a[152:155],v[88:91],a[60:63]
EX v44,v44
MM a[60:63],a[156:159],v[92:95],a[60:63]
buffer_load_dwordx4 v235, s[12:15], s81 offen offset:0 lds
MM a[52:55],a[156:159],v[84:87],a[52:55]
EX v45,v45
MM a[52:55],a[152:155],v[80:83],a[52:55]
RD a[232:235],v226 offset:24576
CV v14,v44,v45
MM a[44:47],a[152:155],v[72:75],a[44:47]
EX v46,v46
MM a[44:47],a[156:159],v[76:79],a[44:47]
EX v47,v47
MM a[36:39],a[156:159],v[68:71],a[36:39]
RD a[236:239],v227 offset:24576
CV v15,v46,v47
MM a[36:39],a[152:155],v[64:67],a[36:39]
EX v48,v48
MM a[64:67],a[160:163],v[64:67],a[64:67]
buffer_load_dwordx4 v235, s[12:15], s64 offen offset:1024 lds
MM a[64:67],a[164:167],v[68:71],a[64:67]
EX v49,v49
MM a[72:75],a[164:167],v[76:79],a[72:75]
RD a[240:243],v224 offset:28672
CV v20,v48,v49
MM a[72:75],a[160:163],v[72:75],a[72:75]
EX v50,v50
MM a[80:83],a[160:163],v[80:83],a[80:83]
EX v51,v51
MM a[80:83],a[164:167],v[84:87],a[80:83]
RD a[244:247],v225 offset:28672
CV v21,v50,v51
MM a[88:91],a[164:167],v[92:95],a[88:91]
EX v52,v52
MM a[88:91],a[160:163],v[88:91],a[88:91]
buffer_load_dwordx4 v235, s[12:15], s81 offen offset:2048 lds
MM a[92:95],a[168:171],v[88:91],a[92:95]
EX v53,v53
MM a[92:95],a[172:175],v[92:95],a[92:95]
RD a[248:251],v226 offset:28672
CV v22,v52,v53
MM a[84:87],a[172:175],v[84:87],a[84:87]
EX v54,v54
MM a[84:87],a[168:171],v[80:83],a[84:87]
EX v55,v55
MM a[76:79],a[168:171],v[72:75],a[76:79]
RD a[252:255],v227 offset:28672
CV v23,v54,v55
MM a[76:79],a[172:175],v[76:79],a[76:79]
EX v56,v56
MM a[68:71],a[172:175],v[68:71],a[68:71]
buffer_load_dwordx4 v235, s[12:15], s64 offen offset:3072 lds
MM a[68:71],a[168:171],v[64:67],a[68:71]
EX v57,v57
MM a[96:99],a[176:179],v[64:67],a[96:99]
EX v58,v58
MM a[96:99],a[180:183],v[68:71],a[96:99]
EX v59,v59
MM a[104:107],a[180:183],v[76:79],a[104:107]
EX v60,v60
MM a[104:107],a[176:179],v[72:75],a[104:107]
EX v61,v61
MM a[112:115],a[176:179],v[80:83],a[112:115]
EX v62,v62
MM a[112:115],a[180:183],v[84:87],a[112:115]
EX v63,v63
MM a[120:123],a[180:183],v[92:95],a[120:123]
s_add_u32 m0, s67, 4096
s_add_u32 s85, s66, 221184
buffer_load_dwordx4 v252, s[36:39], s85 offen offset:0 lds
MM a[120:123],a[176:179],v[88:91],a[120:123]
CV v28,v56,v57
CV v29,v58,v59
MM a[124:127],a[184:187],v[88:91],a[124:127]
v_add_f32 v244, v144, v145
v_add_f32 v245, v148, v149
MM a[124:127],a[188:191],v[92:95],a[124:127]
v_add_f32 v246, v152, v153
v_add_f32 v247, v156, v157
MM a[116:119],a[188:191],v[84:87],a[116:119]
CV v30,v60,v61
CV v31,v62,v63
MM a[116:119],a[184:187],v[80:83],a[116:119]
MM a[108:111],a[184:187],v[72:75],a[108:111]
MM a[108:111],a[188:191],v[76:79],a[108:111]
MM a[100:103],a[188:191],v[68:71],a[100:103]
MM a[100:103],a[184:187],v[64:67],a[100:103]
v_smfmac_f32_16x16x64_bf16 v[144:147], v[236:239], v[0:7], v234
v_smfmac_f32_16x16x64_bf16 v[148:151], v[236:239], v[8:15], v234
v_smfmac_f32_16x16x64_bf16 v[152:155], v[236:239], v[16:23], v234
v_smfmac_f32_16x16x64_bf16 v[156:159], v[236:239], v[24:31], v234
s_waitcnt lgkmcnt(0)
MM v[64:67],a[192:195],v[160:163],v[128:131]
s_add_u32 s64, s64, 16384
TR a[128:129],v232 offset:0
s_add_u32 s81, s64, 896
MM v[64:67],a[196:199],v[176:179],v[64:67]
TR a[130:131],v232 offset:4096
v_max3_f32 v249, v144, v145, v148
MM v[64:67],a[200:203],v[192:195],v[64:67]
TR a[132:133],v232 offset:8192
v_max3_f32 v249, v249, v149, v152
MM v[64:67],a[204:207],v[208:211],v[64:67]
TR a[134:135],v232 offset:12288
v_max3_f32 v249, v249, v153, v156
MM v[68:71],a[220:223],v[208:211],v[128:131]
TR a[136:137],v233 offset:0
v_max_f32 v249, v249, v157
MM v[68:71],a[216:219],v[192:195],v[68:71]
TR a[138:139],v233 offset:4096
v_cmp_lt_f32 vcc, s71, v249
MM v[68:71],a[212:215],v[176:179],v[68:71]
TR a[140:141],v233 offset:8192
TR a[142:143],v233 offset:12288
MM v[68:71],a[208:211],v[160:163],v[68:71]
s_cbranch_vccnz LSITE16_%=
LBACK16_%=:
MM v[76:79],a[208:211],v[164:167],v[132:135]
EX v64,v64
MM v[76:79],a[212:215],v[180:183],v[76:79]
EX v65,v65
MM v[76:79],a[216:219],v[196:199],v[76:79]
TR a[144:145],v232 offset:512
CV v64,v64,v65
MM v[76:79],a[220:223],v[212:215],v[76:79]
EX v66,v66
MM v[72:75],a[204:207],v[212:215],v[132:135]
EX v67,v67
MM v[72:75],a[200:203],v[196:199],v[72:75]
TR a[146:147],v232 offset:4608
CV v65,v66,v67
MM v[72:75],a[196:199],v[180:183],v[72:75]
EX v68,v68
MM v[72:75],a[192:195],v[164:167],v[72:75]
TR a[148:149],v232 offset:8704
TR a[150:151],v232 offset:12800
MM v[80:83],a[192:195],v[168:171],v[136:139]
EX v69,v69
MM v[80:83],a[196:199],v[184:187],v[80:83]
TR a[152:153],v233 offset:512
CV v66,v68,v69
MM v[80:83],a[200:203],v[200:203],v[80:83]
EX v70,v70
MM v[80:83],a[204:207],v[216:219],v[80:83]
TR a[154:155],v233 offset:4608
TR a[156:157],v233 offset:8704
MM v[84:87],a[220:223],v[216:219],v[136:139]
s_add_u32 s87, s95, 1024
buffer_store_dword v240, v248, s[20:23], s87 offen offset:12 sc1
MM v[84:87],a[216:219],v[200:203],v[84:87]
EX v71,v71
MM v[84:87],a[212:215],v[184:187],v[84:87]
TR a[158:159],v233 offset:12800
CV v67,v70,v71
MM v[84:87],a[208:211],v[168:171],v[84:87]
EX v72,v72
MM v[92:95],a[208:211],v[172:175],v[140:143]
EX v73,v73
MM v[92:95],a[212:215],v[188:191],v[92:95]
TR a[160:161],v232 offset:1024
CV v72,v72,v73
MM v[92:95],a[216:219],v[204:207],v[92:95]
EX v74,v74
MM v[92:95],a[220:223],v[220:223],v[92:95]
EX v75,v75
MM v[88:91],a[204:207],v[220:223],v[140:143]
TR a[162:163],v232 offset:5120
CV v73,v74,v75
MM v[88:91],a[200:203],v[204:207],v[88:91]
TR a[164:165],v232 offset:9216
TR a[166:167],v232 offset:13312
MM v[88:91],a[196:199],v[188:191],v[88:91]
EX v76,v76
MM v[88:91],a[192:195],v[172:175],v[88:91]
EX v77,v77
MM v[96:99],a[224:227],v[160:163],v[128:131]
TR a[168:169],v233 offset:1024
CV v74,v76,v77
MM v[96:99],a[228:231],v[176:179],v[96:99]
EX v78,v78
MM v[96:99],a[232:235],v[192:195],v[96:99]
TR a[170:171],v233 offset:5120
TR a[172:173],v233 offset:9216
MM v[96:99],a[236:239],v[208:211],v[96:99]
EX v79,v79
MM v[100:103],a[252:255],v[208:211],v[128:131]
TR a[174:175],v233 offset:13312
CV v75,v78,v79
MM v[100:103],a[248:251],v[192:195],v[100:103]
EX v80,v80
MM v[100:103],a[244:247],v[176:179],v[100:103]
EX v81,v81
MM v[100:103],a[240:243],v[160:163],v[100:103]
TR a[176:177],v232 offset:1536
CV v80,v80,v81
MM v[108:111],a[240:243],v[164:167],v[132:135]
EX v82,v82
MM v[108:111],a[244:247],v[180:183],v[108:111]
TR a[178:179],v232 offset:5632
TR a[180:181],v232 offset:9728
MM v[108:111],a[248:251],v[196:199],v[108:111]
EX v83,v83
MM v[108:111],a[252:255],v[212:215],v[108:111]
TR a[182:183],v232 offset:13824
CV v81,v82,v83
MM v[104:107],a[236:239],v[212:215],v[132:135]
EX v84,v84
MM v[104:107],a[232:235],v[196:199],v[104:107]
EX v85,v85
MM v[104:107],a[228:231],v[180:183],v[104:107]
TR a[184:185],v233 offset:1536
CV v82,v84,v85
MM v[104:107],a[224:227],v[164:167],v[104:107]
EX v86,v86
MM v[112:115],a[224:227],v[168:171],v[136:139]
TR a[186:187],v233 offset:5632
TR a[188:189],v233 offset:9728
MM v[112:115],a[228:231],v[184:187],v[112:115]
EX v87,v87
MM v[112:115],a[232:235],v[200:203],v[112:115]
TR a[190:191],v233 offset:13824
CV v83,v86,v87
MM v[112:115],a[236:239],v[216:219],v[112:115]
EX v88,v88
MM v[116:119],a[252:255],v[216:219],v[136:139]
EX v89,v89
MM v[116:119],a[248:251],v[200:203],v[116:119]
EX v90,v90
MM v[116:119],a[244:247],v[184:187],v[116:119]
EX v91,v91
MM v[116:119],a[240:243],v[168:171],v[116:119]
EX v92,v92
MM v[124:127],a[240:243],v[172:175],v[140:143]
EX v93,v93
MM v[124:127],a[244:247],v[188:191],v[124:127]
EX v94,v94
MM v[124:127],a[248:251],v[204:207],v[124:127]
EX v95,v95
MM v[124:127],a[252:255],v[220:223],v[124:127]
EX v96,v96
MM v[120:123],a[236:239],v[220:223],v[140:143]
EX v97,v97
MM v[120:123],a[232:235],v[204:207],v[120:123]
EX v98,v98
MM v[120:123],a[228:231],v[188:191],v[120:123]
EX v99,v99
MM v[120:123],a[224:227],v[172:175],v[120:123]
s_waitcnt vmcnt(19) lgkmcnt(0)
s_barrier
MM a[0:3],a[128:131],v[0:3],a[0:3]
s_add_u32 m0, s65, 0
CV v88,v88,v89
CV v89,v90,v91
MM a[0:3],a[132:135],v[4:7],a[0:3]
RD a[192:195],v224 offset:32768
CV v90,v92,v93
MM a[8:11],a[132:135],v[12:15],a[8:11]
RD a[196:199],v225 offset:32768
CV v91,v94,v95
MM a[8:11],a[128:131],v[8:11],a[8:11]
buffer_load_dwordx4 v228, s[8:11], s64 offen offset:0 lds
MM a[16:19],a[128:131],v[16:19],a[16:19]
RD a[200:203],v226 offset:32768
CV v68,v96,v97
MM a[16:19],a[132:135],v[20:23],a[16:19]
RD a[204:207],v227 offset:32768
CV v69,v98,v99
MM a[24:27],a[132:135],v[28:31],a[24:27]
buffer_load_dwordx4 v229, s[8:11], s64 offen offset:1024 lds
MM a[24:27],a[128:131],v[24:27],a[24:27]
RD a[208:211],v224 offset:36864
RD a[212:215],v225 offset:36864
MM a[28:31],a[136:139],v[24:27],a[28:31]
buffer_load_dwordx4 v230, s[8:11], s64 offen offset:2048 lds
MM a[28:31],a[140:143],v[28:31],a[28:31]
EX v100,v100
)ASM"
R"ASM(
MM a[20:23],a[140:143],v[20:23],a[20:23]
EX v101,v101
MM a[20:23],a[136:139],v[16:19],a[20:23]
RD a[216:219],v226 offset:36864
CV v70,v100,v101
MM a[12:15],a[136:139],v[8:11],a[12:15]
EX v102,v102
MM a[12:15],a[140:143],v[12:15],a[12:15]
RD a[220:223],v227 offset:36864
RD a[224:227],v224 offset:40960
MM a[4:7],a[140:143],v[4:7],a[4:7]
EX v103,v103
MM a[4:7],a[136:139],v[0:3],a[4:7]
RD a[228:231],v225 offset:40960
CV v71,v102,v103
MM a[32:35],a[144:147],v[0:3],a[32:35]
EX v104,v104
MM a[32:35],a[148:151],v[4:7],a[32:35]
buffer_load_dwordx4 v231, s[8:11], s64 offen offset:3072 lds
MM a[40:43],a[148:151],v[12:15],a[40:43]
s_add_u32 m0, s65, 65536
EX v105,v105
MM a[40:43],a[144:147],v[8:11],a[40:43]
RD a[232:235],v226 offset:40960
CV v76,v104,v105
MM a[48:51],a[144:147],v[16:19],a[48:51]
EX v106,v106
MM a[48:51],a[148:151],v[20:23],a[48:51]
EX v107,v107
MM a[56:59],a[148:151],v[28:31],a[56:59]
RD a[236:239],v227 offset:40960
CV v77,v106,v107
MM a[56:59],a[144:147],v[24:27],a[56:59]
EX v108,v108
MM a[60:63],a[152:155],v[24:27],a[60:63]
buffer_load_dwordx4 v235, s[12:15], s81 offen offset:0 lds
MM a[60:63],a[156:159],v[28:31],a[60:63]
EX v109,v109
MM a[52:55],a[156:159],v[20:23],a[52:55]
s_add_u32 s87, s95, 2048
buffer_store_dword v240, v248, s[20:23], s87 offen offset:12 sc1
MM a[52:55],a[152:155],v[16:19],a[52:55]
RD a[240:243],v224 offset:45056
CV v78,v108,v109
MM a[44:47],a[152:155],v[8:11],a[44:47]
EX v110,v110
MM a[44:47],a[156:159],v[12:15],a[44:47]
EX v111,v111
MM a[36:39],a[156:159],v[4:7],a[36:39]
RD a[244:247],v225 offset:45056
CV v79,v110,v111
LPG14_%=:
s_cbranch_execz LTR2_%=
MM a[36:39],a[152:155],v[0:3],a[36:39]
EX v112,v112
MM a[64:67],a[160:163],v[0:3],a[64:67]
buffer_load_dwordx4 v235, s[12:15], s64 offen offset:1024 lds
MM a[64:67],a[164:167],v[4:7],a[64:67]
EX v113,v113
MM a[72:75],a[164:167],v[12:15],a[72:75]
RD a[248:251],v226 offset:45056
CV v84,v112,v113
MM a[72:75],a[160:163],v[8:11],a[72:75]
EX v114,v114
MM a[80:83],a[160:163],v[16:19],a[80:83]
EX v115,v115
MM a[80:83],a[164:167],v[20:23],a[80:83]
RD a[252:255],v227 offset:45056
CV v85,v114,v115
MM a[88:91],a[164:167],v[28:31],a[88:91]
EX v116,v116
MM a[88:91],a[160:163],v[24:27],a[88:91]
buffer_load_dwordx4 v235, s[12:15], s81 offen offset:2048 lds
MM a[92:95],a[168:171],v[24:27],a[92:95]
EX v117,v117
MM a[92:95],a[172:175],v[28:31],a[92:95]
EX v118,v118
MM a[84:87],a[172:175],v[20:23],a[84:87]
buffer_load_dwordx4 v235, s[12:15], s64 offen offset:3072 lds
MM a[84:87],a[168:171],v[16:19],a[84:87]
EX v119,v119
MM a[76:79],a[168:171],v[8:11],a[76:79]
EX v120,v120
MM a[76:79],a[172:175],v[12:15],a[76:79]
EX v121,v121
MM a[68:71],a[172:175],v[4:7],a[68:71]
EX v122,v122
MM a[68:71],a[168:171],v[0:3],a[68:71]
EX v123,v123
MM a[96:99],a[176:179],v[0:3],a[96:99]
EX v124,v124
MM a[96:99],a[180:183],v[4:7],a[96:99]
EX v125,v125
MM a[104:107],a[180:183],v[12:15],a[104:107]
EX v126,v126
MM a[104:107],a[176:179],v[8:11],a[104:107]
EX v127,v127
MM a[112:115],a[176:179],v[16:19],a[112:115]
CV v86,v116,v117
CV v87,v118,v119
MM a[112:115],a[180:183],v[20:23],a[112:115]
CV v92,v120,v121
CV v93,v122,v123
MM a[120:123],a[180:183],v[28:31],a[120:123]
CV v94,v124,v125
CV v95,v126,v127
MM a[120:123],a[176:179],v[24:27],a[120:123]
MM a[124:127],a[184:187],v[24:27],a[124:127]
v_add_f32 v244, v144, v145
v_add_f32 v245, v148, v149
MM a[124:127],a[188:191],v[28:31],a[124:127]
v_add_f32 v246, v152, v153
v_add_f32 v247, v156, v157
MM a[116:119],a[188:191],v[20:23],a[116:119]
MM a[116:119],a[184:187],v[16:19],a[116:119]
MM a[108:111],a[184:187],v[8:11],a[108:111]
MM a[108:111],a[188:191],v[12:15],a[108:111]
MM a[100:103],a[188:191],v[4:7],a[100:103]
MM a[100:103],a[184:187],v[0:3],a[100:103]
v_smfmac_f32_16x16x64_bf16 v[144:147], v[236:239], v[64:71], v234
v_smfmac_f32_16x16x64_bf16 v[148:151], v[236:239], v[72:79], v234
v_smfmac_f32_16x16x64_bf16 v[152:155], v[236:239], v[80:87], v234
v_smfmac_f32_16x16x64_bf16 v[156:159], v[236:239], v[88:95], v234
s_waitcnt lgkmcnt(0)
MM v[0:3],a[192:195],v[160:163],v[128:131]
s_add_u32 s64, s64, 16384
TR a[128:129],v232 offset:16384
s_add_u32 s81, s64, 896
MM v[0:3],a[196:199],v[176:179],v[0:3]
TR a[130:131],v232 offset:20480
v_max3_f32 v249, v144, v145, v148
MM v[0:3],a[200:203],v[192:195],v[0:3]
TR a[132:133],v232 offset:24576
v_max3_f32 v249, v249, v149, v152
MM v[0:3],a[204:207],v[208:211],v[0:3]
TR a[134:135],v232 offset:28672
v_max3_f32 v249, v249, v153, v156
MM v[4:7],a[220:223],v[208:211],v[128:131]
TR a[136:137],v233 offset:16384
v_max_f32 v249, v249, v157
MM v[4:7],a[216:219],v[192:195],v[4:7]
TR a[138:139],v233 offset:20480
v_cmp_lt_f32 vcc, s71, v249
MM v[4:7],a[212:215],v[176:179],v[4:7]
TR a[140:141],v233 offset:24576
TR a[142:143],v233 offset:28672
MM v[4:7],a[208:211],v[160:163],v[4:7]
s_cbranch_vccnz LSITE17_%=
LBACK17_%=:
MM v[12:15],a[208:211],v[164:167],v[132:135]
v_lshrrev_b32 v127, 8, v248
v_lshlrev_b32 v124, 9, v127
MM v[12:15],a[212:215],v[180:183],v[12:15]
v_and_b32 v126, 192, v248
v_add_u32 v124, v124, v126
MM v[12:15],a[216:219],v[196:199],v[12:15]
v_bfe_u32 v126, v248, 4, 2
v_lshl_add_u32 v124, v126, 3, v124
MM v[12:15],a[220:223],v[212:215],v[12:15]
s_lshr_b32 s76, %[swave], 1
EX v0,v0
MM v[8:11],a[204:207],v[212:215],v[132:135]
s_mul_i32 s76, s76, 10240
EX v1,v1
MM v[8:11],a[200:203],v[196:199],v[8:11]
s_and_b32 s77, %[swave], 1
EX v2,v2
MM v[8:11],a[196:199],v[180:183],v[8:11]
s_lshl_b32 s77, s77, 5
TR a[144:145],v232 offset:16896
CV v0,v0,v1
MM v[8:11],a[192:195],v[164:167],v[8:11]
s_add_u32 s76, s76, s77
EX v3,v3
MM v[16:19],a[192:195],v[168:171],v[136:139]
s_add_u32 s76, s76, %[slds]
EX v4,v4
MM v[16:19],a[196:199],v[184:187],v[16:19]
s_add_u32 s76, s76, 131072
v_add_u32 v124, s76, v124
v_bfe_u32 v126, v248, 4, 3
MM v[16:19],a[200:203],v[200:203],v[16:19]
v_lshl_add_u32 v126, v127, 3, v126
v_lshlrev_b32 v126, 4, v126
MM v[16:19],a[204:207],v[216:219],v[16:19]
s_mul_i32 s76, %[swave], 3072
EX v5,v5
MM v[20:23],a[220:223],v[216:219],v[136:139]
s_add_u32 s76, s76, %[slds]
TR a[146:147],v232 offset:20992
CV v1,v2,v3
MM v[20:23],a[216:219],v[200:203],v[20:23]
s_add_u32 s76, s76, 151552
TR a[148:149],v232 offset:25088
v_add_u32 v125, s76, v126
MM v[20:23],a[212:215],v[184:187],v[20:23]
s_lshr_b32 s76, s48, 7
EX v6,v6
MM v[20:23],a[208:211],v[168:171],v[20:23]
s_lshl_b32 s76, s76, 5
EX v7,v7
MM v[28:31],a[208:211],v[172:175],v[140:143]
s_and_b32 s77, s48, 16
TR a[150:151],v232 offset:29184
TR a[152:153],v233 offset:16896
MM v[28:31],a[212:215],v[188:191],v[28:31]
s_add_u32 s76, s76, s77
EX v8,v8
MM v[28:31],a[216:219],v[204:207],v[28:31]
s_bfe_u32 s77, s48, 131077
TR a[154:155],v233 offset:20992
CV v2,v4,v5
MM v[28:31],a[220:223],v[220:223],v[28:31]
s_lshl_b32 s77, s77, 2
EX v9,v9
MM v[24:27],a[204:207],v[220:223],v[140:143]
s_add_u32 s76, s76, s77
TR a[156:157],v233 offset:25088
CV v3,v6,v7
MM v[24:27],a[200:203],v[204:207],v[24:27]
s_add_u32 s76, s76, %[swave]
EX v10,v10
MM v[24:27],a[196:199],v[188:191],v[24:27]
s_lshl_b32 s97, s76, 2
TR a[158:159],v233 offset:29184
CV v8,v8,v9
MM v[24:27],a[192:195],v[172:175],v[24:27]
s_add_u32 s97, s97, 77824
EX v11,v11
MM v[32:35],a[224:227],v[160:163],v[128:131]
s_lshl_b32 s76, s76, 9
TR a[160:161],v232 offset:17408
TR a[162:163],v232 offset:21504
MM v[32:35],a[228:231],v[176:179],v[32:35]
s_add_u32 s76, s76, 22183936
EX v12,v12
MM v[32:35],a[232:235],v[192:195],v[32:35]
EX v13,v13
MM v[32:35],a[236:239],v[208:211],v[32:35]
TR a[164:165],v232 offset:25600
CV v9,v10,v11
MM v[36:39],a[252:255],v[208:211],v[128:131]
EX v14,v14
MM v[36:39],a[248:251],v[192:195],v[36:39]
TR a[166:167],v232 offset:29696
CV v10,v12,v13
MM v[36:39],a[244:247],v[176:179],v[36:39]
EX v15,v15
MM v[36:39],a[240:243],v[160:163],v[36:39]
TR a[168:169],v233 offset:17408
TR a[170:171],v233 offset:21504
MM v[44:47],a[240:243],v[164:167],v[132:135]
EX v16,v16
MM v[44:47],a[244:247],v[180:183],v[44:47]
EX v17,v17
MM v[44:47],a[248:251],v[196:199],v[44:47]
TR a[172:173],v233 offset:25600
CV v11,v14,v15
MM v[44:47],a[252:255],v[212:215],v[44:47]
EX v18,v18
MM v[40:43],a[236:239],v[212:215],v[132:135]
TR a[174:175],v233 offset:29696
CV v16,v16,v17
MM v[40:43],a[232:235],v[196:199],v[40:43]
EX v19,v19
MM v[40:43],a[228:231],v[180:183],v[40:43]
TR a[176:177],v232 offset:17920
TR a[178:179],v232 offset:22016
MM v[40:43],a[224:227],v[164:167],v[40:43]
EX v20,v20
MM v[48:51],a[224:227],v[168:171],v[136:139]
EX v21,v21
MM v[48:51],a[228:231],v[184:187],v[48:51]
TR a[180:181],v232 offset:26112
CV v17,v18,v19
MM v[48:51],a[232:235],v[200:203],v[48:51]
EX v22,v22
MM v[48:51],a[236:239],v[216:219],v[48:51]
TR a[182:183],v232 offset:30208
CV v18,v20,v21
MM v[52:55],a[252:255],v[216:219],v[136:139]
EX v23,v23
MM v[52:55],a[248:251],v[200:203],v[52:55]
TR a[184:185],v233 offset:17920
TR a[186:187],v233 offset:22016
MM v[52:55],a[244:247],v[184:187],v[52:55]
EX v24,v24
MM v[52:55],a[240:243],v[168:171],v[52:55]
TR a[188:189],v233 offset:26112
CV v19,v22,v23
MM v[60:63],a[240:243],v[172:175],v[140:143]
EX v25,v25
MM v[60:63],a[244:247],v[188:191],v[60:63]
TR a[190:191],v233 offset:30208
CV v24,v24,v25
MM v[60:63],a[248:251],v[204:207],v[60:63]
EX v26,v26
MM v[60:63],a[252:255],v[220:223],v[60:63]
EX v27,v27
MM v[56:59],a[236:239],v[220:223],v[140:143]
EX v28,v28
MM v[56:59],a[232:235],v[204:207],v[56:59]
EX v29,v29
MM v[56:59],a[228:231],v[188:191],v[56:59]
EX v30,v30
MM v[56:59],a[224:227],v[172:175],v[56:59]
s_waitcnt vmcnt(10) lgkmcnt(0)
s_barrier
MM a[0:3],a[128:131],v[64:67],a[0:3]
s_add_u32 m0, s65, 16384
CV v25,v26,v27
CV v26,v28,v29
MM a[0:3],a[132:135],v[68:71],a[0:3]
RD a[192:195],v224 offset:49152
RD a[196:199],v225 offset:49152
MM a[8:11],a[132:135],v[76:79],a[8:11]
EX v31,v31
MM a[8:11],a[128:131],v[72:75],a[8:11]
RD a[200:203],v226 offset:49152
CV v27,v30,v31
MM a[16:19],a[128:131],v[80:83],a[16:19]
TR v[96:97],v124 offset:0
TR v[98:99],v124 offset:256
MM a[16:19],a[132:135],v[84:87],a[16:19]
RD v[108:111],v125 offset:0
TR v[100:101],v124 offset:2048
MM a[24:27],a[132:135],v[92:95],a[24:27]
TR v[102:103],v124 offset:2304
RD v[112:115],v125 offset:512
MM a[24:27],a[128:131],v[88:91],a[24:27]
TR v[104:105],v124 offset:4096
TR v[106:107],v124 offset:4352
MM a[28:31],a[136:139],v[88:91],a[28:31]
RD v[116:119],v125 offset:1024
RD a[204:207],v227 offset:49152
EX v32,v32
MM a[28:31],a[140:143],v[92:95],a[28:31]
EX v33,v33
MM a[20:23],a[140:143],v[84:87],a[20:23]
EX v34,v34
MM a[20:23],a[136:139],v[80:83],a[20:23]
buffer_load_dwordx4 v228, s[8:11], s64 offen offset:0 lds
MM a[12:15],a[136:139],v[72:75],a[12:15]
EX v35,v35
MM a[12:15],a[140:143],v[76:79],a[12:15]
EX v36,v36
MM a[4:7],a[140:143],v[68:71],a[4:7]
CV v4,v32,v33
CV v5,v34,v35
MM a[4:7],a[136:139],v[64:67],a[4:7]
EX v37,v37
MM a[32:35],a[144:147],v[64:67],a[32:35]
EX v38,v38
MM a[32:35],a[148:151],v[68:71],a[32:35]
s_waitcnt lgkmcnt(1)
buffer_load_dwordx4 v229, s[8:11], s64 offen offset:1024 lds
MM a[40:43],a[148:151],v[76:79],a[40:43]
EX v39,v39
MM v[120:123],v[96:99],v[108:111],0
MM v[120:123],v[100:103],v[112:115],v[120:123]
MM v[120:123],v[104:107],v[116:119],v[120:123]
MM a[40:43],a[144:147],v[72:75],a[40:43]
TR v[96:97],v124 offset:6144
TR v[98:99],v124 offset:6400
MM a[48:51],a[144:147],v[80:83],a[48:51]
RD v[108:111],v125 offset:1536
TR v[100:101],v124 offset:8192
MM a[48:51],a[148:151],v[84:87],a[48:51]
TR v[102:103],v124 offset:8448
RD v[112:115],v125 offset:2048
MM a[56:59],a[148:151],v[92:95],a[56:59]
EX v40,v40
MM a[56:59],a[144:147],v[88:91],a[56:59]
EX v41,v41
MM a[60:63],a[152:155],v[88:91],a[60:63]
CV v6,v36,v37
CV v7,v38,v39
MM a[60:63],a[156:159],v[92:95],a[60:63]
EX v42,v42
MM a[52:55],a[156:159],v[84:87],a[52:55]
RD a[208:211],v224 offset:53248
RD a[212:215],v225 offset:53248
MM a[52:55],a[152:155],v[80:83],a[52:55]
EX v43,v43
MM a[44:47],a[152:155],v[72:75],a[44:47]
buffer_load_dwordx4 v230, s[8:11], s64 offen offset:2048 lds
MM a[44:47],a[156:159],v[76:79],a[44:47]
EX v44,v44
MM a[36:39],a[156:159],v[68:71],a[36:39]
s_waitcnt lgkmcnt(2)
EX v45,v45
MM a[36:39],a[152:155],v[64:67],a[36:39]
CV v12,v40,v41
)ASM"
R"ASM(
CV v13,v42,v43
MM v[120:123],v[96:99],v[108:111],v[120:123]
MM v[120:123],v[100:103],v[112:115],v[120:123]
MM a[64:67],a[160:163],v[64:67],a[64:67]
EX v46,v46
MM a[64:67],a[164:167],v[68:71],a[64:67]
RD a[216:219],v226 offset:53248
CV v14,v44,v45
MM a[72:75],a[164:167],v[76:79],a[72:75]
EX v47,v47
MM a[72:75],a[160:163],v[72:75],a[72:75]
buffer_load_dwordx4 v231, s[8:11], s64 offen offset:3072 lds
MM a[80:83],a[160:163],v[80:83],a[80:83]
s_add_u32 m0, s65, 81920
EX v48,v48
MM a[80:83],a[164:167],v[84:87],a[80:83]
RD a[220:223],v227 offset:53248
CV v15,v46,v47
MM a[88:91],a[164:167],v[92:95],a[88:91]
s_mov_b32 exec_lo, 0x00ff00ff
s_mov_b32 exec_hi, 0x00ff00ff
buffer_store_dwordx4 v[120:123], v126, s[20:23], s76 offen offset:0 sc1
s_mov_b64 exec, -1
s_nop 0
MM a[88:91],a[160:163],v[88:91],a[88:91]
EX v49,v49
MM a[92:95],a[168:171],v[88:91],a[92:95]
EX v50,v50
MM a[92:95],a[172:175],v[92:95],a[92:95]
EX v51,v51
MM a[84:87],a[172:175],v[84:87],a[84:87]
RD a[224:227],v224 offset:57344
CV v20,v48,v49
MM a[84:87],a[168:171],v[80:83],a[84:87]
EX v52,v52
MM a[76:79],a[168:171],v[72:75],a[76:79]
buffer_load_dwordx4 v235, s[12:15], s81 offen offset:0 lds
MM a[76:79],a[172:175],v[76:79],a[76:79]
EX v53,v53
MM a[68:71],a[172:175],v[68:71],a[68:71]
RD a[228:231],v225 offset:57344
CV v21,v50,v51
MM a[68:71],a[168:171],v[64:67],a[68:71]
EX v54,v54
MM a[96:99],a[176:179],v[64:67],a[96:99]
RD a[232:235],v226 offset:57344
CV v22,v52,v53
MM a[96:99],a[180:183],v[68:71],a[96:99]
EX v55,v55
MM a[104:107],a[180:183],v[76:79],a[104:107]
EX v56,v56
MM a[104:107],a[176:179],v[72:75],a[104:107]
buffer_load_dwordx4 v235, s[12:15], s64 offen offset:1024 lds
MM a[112:115],a[176:179],v[80:83],a[112:115]
EX v57,v57
MM a[112:115],a[180:183],v[84:87],a[112:115]
RD a[236:239],v227 offset:57344
CV v23,v54,v55
MM a[120:123],a[180:183],v[92:95],a[120:123]
EX v58,v58
MM a[120:123],a[176:179],v[88:91],a[120:123]
RD a[240:243],v224 offset:61440
CV v28,v56,v57
MM a[124:127],a[184:187],v[88:91],a[124:127]
v_add_f32 v244, v144, v145
v_add_f32 v245, v148, v149
MM a[124:127],a[188:191],v[92:95],a[124:127]
v_add_f32 v246, v152, v153
v_add_f32 v247, v156, v157
MM a[116:119],a[188:191],v[84:87],a[116:119]
EX v59,v59
MM a[116:119],a[184:187],v[80:83],a[116:119]
EX v60,v60
MM a[108:111],a[184:187],v[72:75],a[108:111]
EX v61,v61
MM a[108:111],a[188:191],v[76:79],a[108:111]
buffer_load_dwordx4 v235, s[12:15], s81 offen offset:2048 lds
MM a[100:103],a[188:191],v[68:71],a[100:103]
EX v62,v62
MM a[100:103],a[184:187],v[64:67],a[100:103]
RD a[244:247],v225 offset:61440
CV v29,v58,v59
v_smfmac_f32_16x16x64_bf16 v[144:147], v[236:239], v[0:7], v234
RD a[248:251],v226 offset:61440
RD a[252:255],v227 offset:61440
EX v63,v63
v_smfmac_f32_16x16x64_bf16 v[148:151], v[236:239], v[8:15], v234
CV v30,v60,v61
v_smfmac_f32_16x16x64_bf16 v[152:155], v[236:239], v[16:23], v234
CV v31,v62,v63
s_nop 1
v_smfmac_f32_16x16x64_bf16 v[156:159], v[236:239], v[24:31], v234
s_waitcnt lgkmcnt(0)
buffer_load_dwordx4 v235, s[12:15], s64 offen offset:3072 lds
MM v[64:67],a[192:195],v[160:163],v[128:131]
s_add_u32 s64, s64, 16384
TR a[128:129],v232 offset:32768
s_add_u32 s81, s64, 896
MM v[64:67],a[196:199],v[176:179],v[64:67]
TR a[130:131],v232 offset:36864
v_max3_f32 v249, v144, v145, v148
MM v[64:67],a[200:203],v[192:195],v[64:67]
TR a[132:133],v232 offset:40960
v_max3_f32 v249, v249, v149, v152
MM v[64:67],a[204:207],v[208:211],v[64:67]
TR a[134:135],v232 offset:45056
v_max3_f32 v249, v249, v153, v156
MM v[68:71],a[220:223],v[208:211],v[128:131]
TR a[136:137],v233 offset:32768
v_max_f32 v249, v249, v157
MM v[68:71],a[216:219],v[192:195],v[68:71]
TR a[138:139],v233 offset:36864
v_cmp_lt_f32 vcc, s71, v249
MM v[68:71],a[212:215],v[176:179],v[68:71]
TR a[140:141],v233 offset:40960
TR a[142:143],v233 offset:45056
MM v[68:71],a[208:211],v[160:163],v[68:71]
s_cbranch_vccnz LSITE18_%=
LBACK18_%=:
MM v[76:79],a[208:211],v[164:167],v[132:135]
EX v64,v64
MM v[76:79],a[212:215],v[180:183],v[76:79]
EX v65,v65
MM v[76:79],a[216:219],v[196:199],v[76:79]
TR a[144:145],v232 offset:33280
CV v64,v64,v65
MM v[76:79],a[220:223],v[212:215],v[76:79]
EX v66,v66
MM v[72:75],a[204:207],v[212:215],v[132:135]
EX v67,v67
MM v[72:75],a[200:203],v[196:199],v[72:75]
TR a[146:147],v232 offset:37376
CV v65,v66,v67
MM v[72:75],a[196:199],v[180:183],v[72:75]
EX v68,v68
MM v[72:75],a[192:195],v[164:167],v[72:75]
TR a[148:149],v232 offset:41472
TR a[150:151],v232 offset:45568
MM v[80:83],a[192:195],v[168:171],v[136:139]
EX v69,v69
MM v[80:83],a[196:199],v[184:187],v[80:83]
TR a[152:153],v233 offset:33280
CV v66,v68,v69
MM v[80:83],a[200:203],v[200:203],v[80:83]
EX v70,v70
MM v[80:83],a[204:207],v[216:219],v[80:83]
TR a[154:155],v233 offset:37376
TR a[156:157],v233 offset:41472
MM v[84:87],a[220:223],v[216:219],v[136:139]
EX v71,v71
MM v[84:87],a[216:219],v[200:203],v[84:87]
TR a[158:159],v233 offset:45568
CV v67,v70,v71
MM v[84:87],a[212:215],v[184:187],v[84:87]
EX v72,v72
MM v[84:87],a[208:211],v[168:171],v[84:87]
EX v73,v73
MM v[92:95],a[208:211],v[172:175],v[140:143]
TR a[160:161],v232 offset:33792
CV v72,v72,v73
MM v[92:95],a[212:215],v[188:191],v[92:95]
EX v74,v74
MM v[92:95],a[216:219],v[204:207],v[92:95]
TR a[162:163],v232 offset:37888
TR a[164:165],v232 offset:41984
MM v[92:95],a[220:223],v[220:223],v[92:95]
EX v75,v75
MM v[88:91],a[204:207],v[220:223],v[140:143]
TR a[166:167],v232 offset:46080
CV v73,v74,v75
MM v[88:91],a[200:203],v[204:207],v[88:91]
EX v76,v76
MM v[88:91],a[196:199],v[188:191],v[88:91]
EX v77,v77
MM v[88:91],a[192:195],v[172:175],v[88:91]
TR a[168:169],v233 offset:33792
CV v74,v76,v77
MM v[96:99],a[224:227],v[160:163],v[128:131]
EX v78,v78
MM v[96:99],a[228:231],v[176:179],v[96:99]
TR a[170:171],v233 offset:37888
TR a[172:173],v233 offset:41984
MM v[96:99],a[232:235],v[192:195],v[96:99]
EX v79,v79
MM v[96:99],a[236:239],v[208:211],v[96:99]
TR a[174:175],v233 offset:46080
CV v75,v78,v79
MM v[100:103],a[252:255],v[208:211],v[128:131]
EX v80,v80
MM v[100:103],a[248:251],v[192:195],v[100:103]
EX v81,v81
MM v[100:103],a[244:247],v[176:179],v[100:103]
TR a[176:177],v232 offset:34304
CV v80,v80,v81
MM v[100:103],a[240:243],v[160:163],v[100:103]
EX v82,v82
MM v[108:111],a[240:243],v[164:167],v[132:135]
v_lshrrev_b32 v253, 7, v248
v_mul_u32_u24 v253, 1280, v253
MM v[108:111],a[244:247],v[180:183],v[108:111]
v_and_b32 v252, 112, v248
v_add_u32 v253, v253, v252
MM v[108:111],a[248:251],v[196:199],v[108:111]
s_mul_i32 s90, %[swave], 61440
TR a[178:179],v232 offset:38400
TR a[180:181],v232 offset:42496
MM v[108:111],a[252:255],v[212:215],v[108:111]
s_mul_i32 s91, %[swave], 6144
EX v83,v83
MM v[104:107],a[236:239],v[212:215],v[132:135]
s_add_u32 s91, s91, %[slds]
TR a[182:183],v232 offset:46592
CV v81,v82,v83
MM v[104:107],a[232:235],v[196:199],v[104:107]
EX v84,v84
MM v[104:107],a[228:231],v[180:183],v[104:107]
EX v85,v85
MM v[104:107],a[224:227],v[164:167],v[104:107]
TR a[184:185],v233 offset:34304
CV v82,v84,v85
MM v[112:115],a[224:227],v[168:171],v[136:139]
EX v86,v86
MM v[112:115],a[228:231],v[184:187],v[112:115]
TR a[186:187],v233 offset:38400
TR a[188:189],v233 offset:42496
MM v[112:115],a[232:235],v[200:203],v[112:115]
EX v87,v87
MM v[112:115],a[236:239],v[216:219],v[112:115]
TR a[190:191],v233 offset:46592
CV v83,v86,v87
MM v[116:119],a[252:255],v[216:219],v[136:139]
EX v88,v88
MM v[116:119],a[248:251],v[200:203],v[116:119]
EX v89,v89
MM v[116:119],a[244:247],v[184:187],v[116:119]
EX v90,v90
MM v[116:119],a[240:243],v[168:171],v[116:119]
EX v91,v91
MM v[124:127],a[240:243],v[172:175],v[140:143]
EX v92,v92
MM v[124:127],a[244:247],v[188:191],v[124:127]
EX v93,v93
MM v[124:127],a[248:251],v[204:207],v[124:127]
EX v94,v94
MM v[124:127],a[252:255],v[220:223],v[124:127]
EX v95,v95
MM v[120:123],a[236:239],v[220:223],v[140:143]
EX v96,v96
MM v[120:123],a[232:235],v[204:207],v[120:123]
EX v97,v97
MM v[120:123],a[228:231],v[188:191],v[120:123]
EX v98,v98
MM v[120:123],a[224:227],v[172:175],v[120:123]
s_waitcnt vmcnt(14) lgkmcnt(0)
s_barrier
s_nop 0
LPG15_%=:
s_cbranch_execz LTR2_%=
MM a[0:3],a[128:131],v[0:3],a[0:3]
s_add_u32 m0, s65, 32768
CV v88,v88,v89
CV v89,v90,v91
MM a[0:3],a[132:135],v[4:7],a[0:3]
RD a[192:195],v224 offset:0
CV v90,v92,v93
MM a[8:11],a[132:135],v[12:15],a[8:11]
RD a[196:199],v225 offset:0
CV v91,v94,v95
MM a[8:11],a[128:131],v[8:11],a[8:11]
buffer_load_dwordx4 v228, s[8:11], s64 offen offset:0 lds
MM a[16:19],a[128:131],v[16:19],a[16:19]
RD a[200:203],v226 offset:0
CV v68,v96,v97
MM a[16:19],a[132:135],v[20:23],a[16:19]
buffer_load_dwordx4 v229, s[8:11], s64 offen offset:1024 lds
MM a[24:27],a[132:135],v[28:31],a[24:27]
RD a[204:207],v227 offset:0
RD a[208:211],v224 offset:4096
MM a[24:27],a[128:131],v[24:27],a[24:27]
EX v99,v99
MM a[28:31],a[136:139],v[24:27],a[28:31]
RD a[212:215],v225 offset:4096
CV v69,v98,v99
MM a[28:31],a[140:143],v[28:31],a[28:31]
EX v100,v100
MM a[20:23],a[140:143],v[20:23],a[20:23]
buffer_load_dwordx4 v230, s[8:11], s64 offen offset:2048 lds
MM a[20:23],a[136:139],v[16:19],a[20:23]
EX v101,v101
MM a[12:15],a[136:139],v[8:11],a[12:15]
RD a[216:219],v226 offset:4096
CV v70,v100,v101
MM a[12:15],a[140:143],v[12:15],a[12:15]
EX v102,v102
MM a[4:7],a[140:143],v[4:7],a[4:7]
EX v103,v103
MM a[4:7],a[136:139],v[0:3],a[4:7]
RD a[220:223],v227 offset:4096
CV v71,v102,v103
MM a[32:35],a[144:147],v[0:3],a[32:35]
EX v104,v104
MM a[32:35],a[148:151],v[4:7],a[32:35]
buffer_load_dwordx4 v231, s[8:11], s64 offen offset:3072 lds
MM a[40:43],a[148:151],v[12:15],a[40:43]
s_add_u32 m0, s65, 98304
EX v105,v105
MM a[40:43],a[144:147],v[8:11],a[40:43]
RD a[224:227],v224 offset:8192
CV v76,v104,v105
MM a[48:51],a[144:147],v[16:19],a[48:51]
EX v106,v106
MM a[48:51],a[148:151],v[20:23],a[48:51]
EX v107,v107
MM a[56:59],a[148:151],v[28:31],a[56:59]
RD a[228:231],v225 offset:8192
CV v77,v106,v107
MM a[56:59],a[144:147],v[24:27],a[56:59]
EX v108,v108
MM a[60:63],a[152:155],v[24:27],a[60:63]
buffer_load_dwordx4 v235, s[12:15], s81 offen offset:0 lds
MM a[60:63],a[156:159],v[28:31],a[60:63]
EX v109,v109
MM a[52:55],a[156:159],v[20:23],a[52:55]
RD a[232:235],v226 offset:8192
CV v78,v108,v109
MM a[52:55],a[152:155],v[16:19],a[52:55]
EX v110,v110
MM a[44:47],a[152:155],v[8:11],a[44:47]
EX v111,v111
MM a[44:47],a[156:159],v[12:15],a[44:47]
RD a[236:239],v227 offset:8192
CV v79,v110,v111
MM a[36:39],a[156:159],v[4:7],a[36:39]
buffer_load_dwordx4 v235, s[12:15], s64 offen offset:1024 lds
MM a[36:39],a[152:155],v[0:3],a[36:39]
EX v112,v112
MM a[64:67],a[160:163],v[0:3],a[64:67]
EX v113,v113
MM a[64:67],a[164:167],v[4:7],a[64:67]
RD a[240:243],v224 offset:12288
CV v84,v112,v113
MM a[72:75],a[164:167],v[12:15],a[72:75]
EX v114,v114
MM a[72:75],a[160:163],v[8:11],a[72:75]
RD a[244:247],v225 offset:12288
RD a[248:251],v226 offset:12288
MM a[80:83],a[160:163],v[16:19],a[80:83]
s_waitcnt vmcnt(10)
v_mov_b32 v250, s69
s_mov_b64 exec, 1
buffer_store_dword v250, off, s[20:23], s97 offset:0 sc1
s_mov_b64 exec, -1
MM a[80:83],a[164:167],v[20:23],a[80:83]
s_add_u32 m0, s91, 139264
s_add_u32 s77, s90, 0
buffer_load_dwordx4 v253, s[28:31], s77 offen offset:0 lds
MM a[88:91],a[164:167],v[28:31],a[88:91]
EX v115,v115
MM a[88:91],a[160:163],v[24:27],a[88:91]
RD a[252:255],v227 offset:12288
CV v85,v114,v115
MM a[92:95],a[168:171],v[24:27],a[92:95]
EX v116,v116
MM a[92:95],a[172:175],v[28:31],a[92:95]
EX v117,v117
MM a[84:87],a[172:175],v[20:23],a[84:87]
s_add_u32 m0, s65, 98304
s_nop 0
buffer_load_dwordx4 v235, s[12:15], s81 offen offset:2048 lds
MM a[84:87],a[168:171],v[16:19],a[84:87]
EX v118,v118
MM a[76:79],a[168:171],v[8:11],a[76:79]
EX v119,v119
MM a[76:79],a[172:175],v[12:15],a[76:79]
EX v120,v120
MM a[68:71],a[172:175],v[4:7],a[68:71]
buffer_load_dwordx4 v235, s[12:15], s64 offen offset:3072 lds
MM a[68:71],a[168:171],v[0:3],a[68:71]
EX v121,v121
MM a[96:99],a[176:179],v[0:3],a[96:99]
EX v122,v122
MM a[96:99],a[180:183],v[4:7],a[96:99]
EX v123,v123
MM a[104:107],a[180:183],v[12:15],a[104:107]
EX v124,v124
MM a[104:107],a[176:179],v[8:11],a[104:107]
EX v125,v125
MM a[112:115],a[176:179],v[16:19],a[112:115]
)ASM"
R"ASM(
EX v126,v126
MM a[112:115],a[180:183],v[20:23],a[112:115]
EX v127,v127
MM a[120:123],a[180:183],v[28:31],a[120:123]
CV v86,v116,v117
CV v87,v118,v119
MM a[120:123],a[176:179],v[24:27],a[120:123]
CV v92,v120,v121
CV v93,v122,v123
MM a[124:127],a[184:187],v[24:27],a[124:127]
v_add_f32 v244, v144, v145
v_add_f32 v245, v148, v149
MM a[124:127],a[188:191],v[28:31],a[124:127]
v_add_f32 v246, v152, v153
v_add_f32 v247, v156, v157
MM a[116:119],a[188:191],v[20:23],a[116:119]
CV v94,v124,v125
CV v95,v126,v127
MM a[116:119],a[184:187],v[16:19],a[116:119]
MM a[108:111],a[184:187],v[8:11],a[108:111]
MM a[108:111],a[188:191],v[12:15],a[108:111]
MM a[100:103],a[188:191],v[4:7],a[100:103]
MM a[100:103],a[184:187],v[0:3],a[100:103]
v_smfmac_f32_16x16x64_bf16 v[144:147], v[236:239], v[64:71], v234
v_smfmac_f32_16x16x64_bf16 v[148:151], v[236:239], v[72:79], v234
v_smfmac_f32_16x16x64_bf16 v[152:155], v[236:239], v[80:87], v234
v_smfmac_f32_16x16x64_bf16 v[156:159], v[236:239], v[88:95], v234
s_waitcnt lgkmcnt(0)
MM v[0:3],a[192:195],v[160:163],v[128:131]
s_add_u32 s64, s64, 16384
TR a[128:129],v232 offset:49152
s_add_u32 s81, s64, 896
MM v[0:3],a[196:199],v[176:179],v[0:3]
TR a[130:131],v232 offset:53248
v_max3_f32 v249, v144, v145, v148
MM v[0:3],a[200:203],v[192:195],v[0:3]
TR a[132:133],v232 offset:57344
v_max3_f32 v249, v249, v149, v152
MM v[0:3],a[204:207],v[208:211],v[0:3]
TR a[134:135],v232 offset:61440
v_max3_f32 v249, v249, v153, v156
MM v[4:7],a[220:223],v[208:211],v[128:131]
TR a[136:137],v233 offset:49152
v_max_f32 v249, v249, v157
MM v[4:7],a[216:219],v[192:195],v[4:7]
TR a[138:139],v233 offset:53248
v_cmp_lt_f32 vcc, s71, v249
MM v[4:7],a[212:215],v[176:179],v[4:7]
TR a[140:141],v233 offset:57344
TR a[142:143],v233 offset:61440
MM v[4:7],a[208:211],v[160:163],v[4:7]
s_cbranch_vccnz LSITE19_%=
LBACK19_%=:
MM v[12:15],a[208:211],v[164:167],v[132:135]
s_add_u32 m0, s91, 140288
s_add_u32 s77, s90, 10240
buffer_load_dwordx4 v253, s[28:31], s77 offen offset:0 lds
MM v[12:15],a[212:215],v[180:183],v[12:15]
EX v0,v0
MM v[12:15],a[216:219],v[196:199],v[12:15]
EX v1,v1
MM v[12:15],a[220:223],v[212:215],v[12:15]
TR a[144:145],v232 offset:49664
CV v0,v0,v1
MM v[8:11],a[204:207],v[212:215],v[132:135]
EX v2,v2
MM v[8:11],a[200:203],v[196:199],v[8:11]
EX v3,v3
MM v[8:11],a[196:199],v[180:183],v[8:11]
TR a[146:147],v232 offset:53760
CV v1,v2,v3
MM v[8:11],a[192:195],v[164:167],v[8:11]
EX v4,v4
MM v[16:19],a[192:195],v[168:171],v[136:139]
TR a[148:149],v232 offset:57856
TR a[150:151],v232 offset:61952
MM v[16:19],a[196:199],v[184:187],v[16:19]
EX v5,v5
MM v[16:19],a[200:203],v[200:203],v[16:19]
TR a[152:153],v233 offset:49664
CV v2,v4,v5
MM v[16:19],a[204:207],v[216:219],v[16:19]
EX v6,v6
MM v[20:23],a[220:223],v[216:219],v[136:139]
EX v7,v7
MM v[20:23],a[216:219],v[200:203],v[20:23]
TR a[154:155],v233 offset:53760
CV v3,v6,v7
MM v[20:23],a[212:215],v[184:187],v[20:23]
EX v8,v8
MM v[20:23],a[208:211],v[168:171],v[20:23]
TR a[156:157],v233 offset:57856
TR a[158:159],v233 offset:61952
MM v[28:31],a[208:211],v[172:175],v[140:143]
EX v9,v9
MM v[28:31],a[212:215],v[188:191],v[28:31]
TR a[160:161],v232 offset:50176
CV v8,v8,v9
MM v[28:31],a[216:219],v[204:207],v[28:31]
EX v10,v10
MM v[28:31],a[220:223],v[220:223],v[28:31]
EX v11,v11
MM v[24:27],a[204:207],v[220:223],v[140:143]
TR a[162:163],v232 offset:54272
CV v9,v10,v11
MM v[24:27],a[200:203],v[204:207],v[24:27]
TR a[164:165],v232 offset:58368
TR a[166:167],v232 offset:62464
MM v[24:27],a[196:199],v[188:191],v[24:27]
EX v12,v12
MM v[24:27],a[192:195],v[172:175],v[24:27]
EX v13,v13
MM v[32:35],a[224:227],v[160:163],v[128:131]
TR a[168:169],v233 offset:50176
CV v10,v12,v13
MM v[32:35],a[228:231],v[176:179],v[32:35]
EX v14,v14
MM v[32:35],a[232:235],v[192:195],v[32:35]
TR a[170:171],v233 offset:54272
TR a[172:173],v233 offset:58368
MM v[32:35],a[236:239],v[208:211],v[32:35]
EX v15,v15
MM v[36:39],a[252:255],v[208:211],v[128:131]
TR a[174:175],v233 offset:62464
CV v11,v14,v15
MM v[36:39],a[248:251],v[192:195],v[36:39]
EX v16,v16
MM v[36:39],a[244:247],v[176:179],v[36:39]
EX v17,v17
MM v[36:39],a[240:243],v[160:163],v[36:39]
TR a[176:177],v232 offset:50688
CV v16,v16,v17
MM v[44:47],a[240:243],v[164:167],v[132:135]
s_add_u32 m0, s91, 141312
s_add_u32 s77, s90, 20480
buffer_load_dwordx4 v253, s[28:31], s77 offen offset:0 lds
MM v[44:47],a[244:247],v[180:183],v[44:47]
EX v18,v18
MM v[44:47],a[248:251],v[196:199],v[44:47]
TR a[178:179],v232 offset:54784
TR a[180:181],v232 offset:58880
MM v[44:47],a[252:255],v[212:215],v[44:47]
EX v19,v19
MM v[40:43],a[236:239],v[212:215],v[132:135]
TR a[182:183],v232 offset:62976
CV v17,v18,v19
MM v[40:43],a[232:235],v[196:199],v[40:43]
EX v20,v20
MM v[40:43],a[228:231],v[180:183],v[40:43]
EX v21,v21
MM v[40:43],a[224:227],v[164:167],v[40:43]
TR a[184:185],v233 offset:50688
CV v18,v20,v21
MM v[48:51],a[224:227],v[168:171],v[136:139]
EX v22,v22
MM v[48:51],a[228:231],v[184:187],v[48:51]
TR a[186:187],v233 offset:54784
TR a[188:189],v233 offset:58880
MM v[48:51],a[232:235],v[200:203],v[48:51]
EX v23,v23
MM v[48:51],a[236:239],v[216:219],v[48:51]
TR a[190:191],v233 offset:62976
CV v19,v22,v23
MM v[52:55],a[252:255],v[216:219],v[136:139]
EX v24,v24
MM v[52:55],a[248:251],v[200:203],v[52:55]
EX v25,v25
MM v[52:55],a[244:247],v[184:187],v[52:55]
EX v26,v26
MM v[52:55],a[240:243],v[168:171],v[52:55]
EX v27,v27
MM v[60:63],a[240:243],v[172:175],v[140:143]
EX v28,v28
MM v[60:63],a[244:247],v[188:191],v[60:63]
EX v29,v29
MM v[60:63],a[248:251],v[204:207],v[60:63]
EX v30,v30
MM v[60:63],a[252:255],v[220:223],v[60:63]
EX v31,v31
MM v[56:59],a[236:239],v[220:223],v[140:143]
EX v32,v32
MM v[56:59],a[232:235],v[204:207],v[56:59]
EX v33,v33
MM v[56:59],a[228:231],v[188:191],v[56:59]
EX v34,v34
MM v[56:59],a[224:227],v[172:175],v[56:59]
s_waitcnt lgkmcnt(0)
s_barrier
MM a[0:3],a[128:131],v[64:67],a[0:3]
s_add_u32 m0, s65, 49152
CV v24,v24,v25
CV v25,v26,v27
MM a[0:3],a[132:135],v[68:71],a[0:3]
RD a[192:195],v224 offset:16384
CV v26,v28,v29
MM a[8:11],a[132:135],v[76:79],a[8:11]
RD a[196:199],v225 offset:16384
CV v27,v30,v31
MM a[8:11],a[128:131],v[72:75],a[8:11]
buffer_load_dwordx4 v228, s[8:11], s64 offen offset:0 lds
MM a[16:19],a[128:131],v[80:83],a[16:19]
s_add_u32 m0, s91, 142336
s_add_u32 s77, s90, 30720
buffer_load_dwordx4 v253, s[28:31], s77 offen offset:0 lds
MM a[16:19],a[132:135],v[84:87],a[16:19]
RD a[200:203],v226 offset:16384
CV v4,v32,v33
MM a[24:27],a[132:135],v[92:95],a[24:27]
RD a[204:207],v227 offset:16384
RD a[208:211],v224 offset:20480
MM a[24:27],a[128:131],v[88:91],a[24:27]
s_add_u32 m0, s65, 49152
s_nop 0
buffer_load_dwordx4 v229, s[8:11], s64 offen offset:1024 lds
MM a[28:31],a[136:139],v[88:91],a[28:31]
EX v35,v35
MM a[28:31],a[140:143],v[92:95],a[28:31]
RD a[212:215],v225 offset:20480
CV v5,v34,v35
MM a[20:23],a[140:143],v[84:87],a[20:23]
EX v36,v36
MM a[20:23],a[136:139],v[80:83],a[20:23]
EX v37,v37
MM a[12:15],a[136:139],v[72:75],a[12:15]
RD a[216:219],v226 offset:20480
CV v6,v36,v37
MM a[12:15],a[140:143],v[76:79],a[12:15]
buffer_load_dwordx4 v230, s[8:11], s64 offen offset:2048 lds
MM a[4:7],a[140:143],v[68:71],a[4:7]
EX v38,v38
MM a[4:7],a[136:139],v[64:67],a[4:7]
EX v39,v39
MM a[32:35],a[144:147],v[64:67],a[32:35]
RD a[220:223],v227 offset:20480
CV v7,v38,v39
MM a[32:35],a[148:151],v[68:71],a[32:35]
EX v40,v40
MM a[40:43],a[148:151],v[76:79],a[40:43]
EX v41,v41
MM a[40:43],a[144:147],v[72:75],a[40:43]
RD a[224:227],v224 offset:24576
CV v12,v40,v41
MM a[48:51],a[144:147],v[80:83],a[48:51]
buffer_load_dwordx4 v231, s[8:11], s64 offen offset:3072 lds
MM a[48:51],a[148:151],v[84:87],a[48:51]
s_add_u32 m0, s65, 114688
EX v42,v42
MM a[56:59],a[148:151],v[92:95],a[56:59]
EX v43,v43
MM a[56:59],a[144:147],v[88:91],a[56:59]
RD a[228:231],v225 offset:24576
CV v13,v42,v43
MM a[60:63],a[152:155],v[88:91],a[60:63]
EX v44,v44
MM a[60:63],a[156:159],v[92:95],a[60:63]
buffer_load_dwordx4 v235, s[12:15], s81 offen offset:0 lds
MM a[52:55],a[156:159],v[84:87],a[52:55]
EX v45,v45
MM a[52:55],a[152:155],v[80:83],a[52:55]
RD a[232:235],v226 offset:24576
CV v14,v44,v45
MM a[44:47],a[152:155],v[72:75],a[44:47]
EX v46,v46
MM a[44:47],a[156:159],v[76:79],a[44:47]
EX v47,v47
MM a[36:39],a[156:159],v[68:71],a[36:39]
RD a[236:239],v227 offset:24576
CV v15,v46,v47
MM a[36:39],a[152:155],v[64:67],a[36:39]
EX v48,v48
MM a[64:67],a[160:163],v[64:67],a[64:67]
buffer_load_dwordx4 v235, s[12:15], s64 offen offset:1024 lds
MM a[64:67],a[164:167],v[68:71],a[64:67]
EX v49,v49
MM a[72:75],a[164:167],v[76:79],a[72:75]
RD a[240:243],v224 offset:28672
CV v20,v48,v49
MM a[72:75],a[160:163],v[72:75],a[72:75]
EX v50,v50
MM a[80:83],a[160:163],v[80:83],a[80:83]
s_add_u32 m0, s91, 143360
s_add_u32 s77, s90, 40960
buffer_load_dwordx4 v253, s[28:31], s77 offen offset:0 lds
MM a[80:83],a[164:167],v[84:87],a[80:83]
EX v51,v51
MM a[88:91],a[164:167],v[92:95],a[88:91]
RD a[244:247],v225 offset:28672
CV v21,v50,v51
MM a[88:91],a[160:163],v[88:91],a[88:91]
EX v52,v52
MM a[92:95],a[168:171],v[88:91],a[92:95]
EX v53,v53
MM a[92:95],a[172:175],v[92:95],a[92:95]
RD a[248:251],v226 offset:28672
CV v22,v52,v53
MM a[84:87],a[172:175],v[84:87],a[84:87]
s_add_u32 m0, s65, 114688
s_nop 0
buffer_load_dwordx4 v235, s[12:15], s81 offen offset:2048 lds
MM a[84:87],a[168:171],v[80:83],a[84:87]
EX v54,v54
MM a[76:79],a[168:171],v[72:75],a[76:79]
EX v55,v55
MM a[76:79],a[172:175],v[76:79],a[76:79]
RD a[252:255],v227 offset:28672
CV v23,v54,v55
MM a[68:71],a[172:175],v[68:71],a[68:71]
EX v56,v56
MM a[68:71],a[168:171],v[64:67],a[68:71]
EX v57,v57
MM a[96:99],a[176:179],v[64:67],a[96:99]
buffer_load_dwordx4 v235, s[12:15], s64 offen offset:3072 lds
MM a[96:99],a[180:183],v[68:71],a[96:99]
EX v58,v58
MM a[104:107],a[180:183],v[76:79],a[104:107]
EX v59,v59
MM a[104:107],a[176:179],v[72:75],a[104:107]
EX v60,v60
MM a[112:115],a[176:179],v[80:83],a[112:115]
EX v61,v61
MM a[112:115],a[180:183],v[84:87],a[112:115]
EX v62,v62
MM a[120:123],a[180:183],v[92:95],a[120:123]
s_add_u32 m0, s91, 144384
s_add_u32 s77, s90, 51200
buffer_load_dwordx4 v253, s[28:31], s77 offen offset:0 lds
MM a[120:123],a[176:179],v[88:91],a[120:123]
EX v63,v63
MM a[124:127],a[184:187],v[88:91],a[124:127]
v_add_f32 v244, v144, v145
v_add_f32 v245, v148, v149
MM a[124:127],a[188:191],v[92:95],a[124:127]
v_add_f32 v246, v152, v153
v_add_f32 v247, v156, v157
MM a[116:119],a[188:191],v[84:87],a[116:119]
CV v28,v56,v57
CV v29,v58,v59
MM a[116:119],a[184:187],v[80:83],a[116:119]
CV v30,v60,v61
CV v31,v62,v63
MM a[108:111],a[184:187],v[72:75],a[108:111]
MM a[108:111],a[188:191],v[76:79],a[108:111]
MM a[100:103],a[188:191],v[68:71],a[100:103]
MM a[100:103],a[184:187],v[64:67],a[100:103]
v_smfmac_f32_16x16x64_bf16 v[144:147], v[236:239], v[0:7], v234
v_smfmac_f32_16x16x64_bf16 v[148:151], v[236:239], v[8:15], v234
v_smfmac_f32_16x16x64_bf16 v[152:155], v[236:239], v[16:23], v234
v_smfmac_f32_16x16x64_bf16 v[156:159], v[236:239], v[24:31], v234
s_waitcnt lgkmcnt(0)
MM v[64:67],a[192:195],v[160:163],v[128:131]
s_add_u32 s64, s64, 16384
TR a[128:129],v232 offset:0
s_add_u32 s81, s64, 896
MM v[64:67],a[196:199],v[176:179],v[64:67]
TR a[130:131],v232 offset:4096
v_max3_f32 v249, v144, v145, v148
MM v[64:67],a[200:203],v[192:195],v[64:67]
TR a[132:133],v232 offset:8192
v_max3_f32 v249, v249, v149, v152
MM v[64:67],a[204:207],v[208:211],v[64:67]
TR a[134:135],v232 offset:12288
v_max3_f32 v249, v249, v153, v156
MM v[68:71],a[220:223],v[208:211],v[128:131]
TR a[136:137],v233 offset:0
v_max_f32 v249, v249, v157
MM v[68:71],a[216:219],v[192:195],v[68:71]
TR a[138:139],v233 offset:4096
v_cmp_lt_f32 vcc, s71, v249
MM v[68:71],a[212:215],v[176:179],v[68:71]
TR a[140:141],v233 offset:8192
TR a[142:143],v233 offset:12288
MM v[68:71],a[208:211],v[160:163],v[68:71]
s_cbranch_vccnz LSITE20_%=
LBACK20_%=:
MM v[76:79],a[208:211],v[164:167],v[132:135]
v_bfe_u32 v253, v248, 4, 4
v_lshlrev_b32 v253, 2, v253
MM v[76:79],a[212:215],v[180:183],v[76:79]
s_and_b32 s76, s18, 0xffffffc0
EX v64,v64
MM v[76:79],a[216:219],v[196:199],v[76:79]
s_add_u32 s76, s76, 77824
)ASM"
R"ASM(
buffer_load_dword v252, v253, s[20:23], s76 offen offset:0 sc1
MM v[76:79],a[220:223],v[212:215],v[76:79]
EX v65,v65
MM v[72:75],a[204:207],v[212:215],v[132:135]
EX v66,v66
MM v[72:75],a[200:203],v[196:199],v[72:75]
TR a[144:145],v232 offset:512
CV v64,v64,v65
MM v[72:75],a[196:199],v[180:183],v[72:75]
EX v67,v67
MM v[72:75],a[192:195],v[164:167],v[72:75]
TR a[146:147],v232 offset:4608
CV v65,v66,v67
MM v[80:83],a[192:195],v[168:171],v[136:139]
EX v68,v68
MM v[80:83],a[196:199],v[184:187],v[80:83]
TR a[148:149],v232 offset:8704
TR a[150:151],v232 offset:12800
MM v[80:83],a[200:203],v[200:203],v[80:83]
EX v69,v69
MM v[80:83],a[204:207],v[216:219],v[80:83]
TR a[152:153],v233 offset:512
CV v66,v68,v69
MM v[84:87],a[220:223],v[216:219],v[136:139]
EX v70,v70
MM v[84:87],a[216:219],v[200:203],v[84:87]
EX v71,v71
MM v[84:87],a[212:215],v[184:187],v[84:87]
TR a[154:155],v233 offset:4608
CV v67,v70,v71
MM v[84:87],a[208:211],v[168:171],v[84:87]
EX v72,v72
MM v[92:95],a[208:211],v[172:175],v[140:143]
TR a[156:157],v233 offset:8704
TR a[158:159],v233 offset:12800
MM v[92:95],a[212:215],v[188:191],v[92:95]
EX v73,v73
MM v[92:95],a[216:219],v[204:207],v[92:95]
TR a[160:161],v232 offset:1024
CV v72,v72,v73
MM v[92:95],a[220:223],v[220:223],v[92:95]
EX v74,v74
MM v[88:91],a[204:207],v[220:223],v[140:143]
EX v75,v75
MM v[88:91],a[200:203],v[204:207],v[88:91]
TR a[162:163],v232 offset:5120
CV v73,v74,v75
MM v[88:91],a[196:199],v[188:191],v[88:91]
EX v76,v76
MM v[88:91],a[192:195],v[172:175],v[88:91]
TR a[164:165],v232 offset:9216
TR a[166:167],v232 offset:13312
MM v[96:99],a[224:227],v[160:163],v[128:131]
EX v77,v77
MM v[96:99],a[228:231],v[176:179],v[96:99]
TR a[168:169],v233 offset:1024
CV v74,v76,v77
MM v[96:99],a[232:235],v[192:195],v[96:99]
EX v78,v78
MM v[96:99],a[236:239],v[208:211],v[96:99]
EX v79,v79
MM v[100:103],a[252:255],v[208:211],v[128:131]
TR a[170:171],v233 offset:5120
CV v75,v78,v79
MM v[100:103],a[248:251],v[192:195],v[100:103]
EX v80,v80
MM v[100:103],a[244:247],v[176:179],v[100:103]
TR a[172:173],v233 offset:9216
TR a[174:175],v233 offset:13312
MM v[100:103],a[240:243],v[160:163],v[100:103]
LPG16_%=:
s_cbranch_execz LTR2_%=
EX v81,v81
MM v[108:111],a[240:243],v[164:167],v[132:135]
TR a[176:177],v232 offset:1536
CV v80,v80,v81
MM v[108:111],a[244:247],v[180:183],v[108:111]
EX v82,v82
MM v[108:111],a[248:251],v[196:199],v[108:111]
TR a[178:179],v232 offset:5632
TR a[180:181],v232 offset:9728
MM v[108:111],a[252:255],v[212:215],v[108:111]
EX v83,v83
MM v[104:107],a[236:239],v[212:215],v[132:135]
TR a[182:183],v232 offset:13824
CV v81,v82,v83
MM v[104:107],a[232:235],v[196:199],v[104:107]
EX v84,v84
MM v[104:107],a[228:231],v[180:183],v[104:107]
EX v85,v85
MM v[104:107],a[224:227],v[164:167],v[104:107]
TR a[184:185],v233 offset:1536
CV v82,v84,v85
MM v[112:115],a[224:227],v[168:171],v[136:139]
EX v86,v86
MM v[112:115],a[228:231],v[184:187],v[112:115]
TR a[186:187],v233 offset:5632
TR a[188:189],v233 offset:9728
MM v[112:115],a[232:235],v[200:203],v[112:115]
EX v87,v87
MM v[112:115],a[236:239],v[216:219],v[112:115]
TR a[190:191],v233 offset:13824
CV v83,v86,v87
MM v[116:119],a[252:255],v[216:219],v[136:139]
EX v88,v88
MM v[116:119],a[248:251],v[200:203],v[116:119]
EX v89,v89
MM v[116:119],a[244:247],v[184:187],v[116:119]
EX v90,v90
MM v[116:119],a[240:243],v[168:171],v[116:119]
EX v91,v91
MM v[124:127],a[240:243],v[172:175],v[140:143]
EX v92,v92
MM v[124:127],a[244:247],v[188:191],v[124:127]
EX v93,v93
MM v[124:127],a[248:251],v[204:207],v[124:127]
EX v94,v94
MM v[124:127],a[252:255],v[220:223],v[124:127]
EX v95,v95
MM v[120:123],a[236:239],v[220:223],v[140:143]
EX v96,v96
MM v[120:123],a[232:235],v[204:207],v[120:123]
EX v97,v97
MM v[120:123],a[228:231],v[188:191],v[120:123]
s_getpc_b64 s[98:99]
LTA3_%=:
s_min_u32 s73, %[swave], 3
s_lshl_b32 s73, s73, 12
s_add_u32 s73, s73, LPG17_%=-LTA3_%=
s_add_u32 s98, s98, s73
s_addc_u32 s99, s99, 0
s_mov_b64 exec, 0
s_setpc_b64 s[98:99]
LTR3_%=:
s_mov_b64 exec, -1
CV v88,v88,v89
MM v[120:123],a[224:227],v[172:175],v[120:123]
s_waitcnt vmcnt(20) lgkmcnt(0)
s_barrier
MM a[0:3],a[128:131],v[0:3],a[0:3]
s_add_u32 m0, s65, 0
CV v89,v90,v91
CV v90,v92,v93
MM a[0:3],a[132:135],v[4:7],a[0:3]
RD a[192:195],v224 offset:32768
CV v91,v94,v95
MM a[8:11],a[132:135],v[12:15],a[8:11]
buffer_load_dwordx4 v228, s[8:11], s64 offen offset:0 lds
MM a[8:11],a[128:131],v[8:11],a[8:11]
RD a[196:199],v225 offset:32768
CV v68,v96,v97
MM a[16:19],a[128:131],v[16:19],a[16:19]
RD a[200:203],v226 offset:32768
RD a[204:207],v227 offset:32768
MM a[16:19],a[132:135],v[20:23],a[16:19]
buffer_load_dwordx4 v229, s[8:11], s64 offen offset:1024 lds
MM a[24:27],a[132:135],v[28:31],a[24:27]
EX v98,v98
MM a[24:27],a[128:131],v[24:27],a[24:27]
RD a[208:211],v224 offset:36864
RD a[212:215],v225 offset:36864
MM a[28:31],a[136:139],v[24:27],a[28:31]
EX v99,v99
MM a[28:31],a[140:143],v[28:31],a[28:31]
RD a[216:219],v226 offset:36864
CV v69,v98,v99
MM a[20:23],a[140:143],v[20:23],a[20:23]
EX v100,v100
MM a[20:23],a[136:139],v[16:19],a[20:23]
EX v101,v101
MM a[12:15],a[136:139],v[8:11],a[12:15]
RD a[220:223],v227 offset:36864
CV v70,v100,v101
MM a[12:15],a[140:143],v[12:15],a[12:15]
buffer_load_dwordx4 v230, s[8:11], s64 offen offset:2048 lds
MM a[4:7],a[140:143],v[4:7],a[4:7]
EX v102,v102
MM a[4:7],a[136:139],v[0:3],a[4:7]
EX v103,v103
MM a[32:35],a[144:147],v[0:3],a[32:35]
RD a[224:227],v224 offset:40960
CV v71,v102,v103
MM a[32:35],a[148:151],v[4:7],a[32:35]
EX v104,v104
MM a[40:43],a[148:151],v[12:15],a[40:43]
EX v105,v105
MM a[40:43],a[144:147],v[8:11],a[40:43]
RD a[228:231],v225 offset:40960
CV v76,v104,v105
MM a[48:51],a[144:147],v[16:19],a[48:51]
buffer_load_dwordx4 v231, s[8:11], s64 offen offset:3072 lds
MM a[48:51],a[148:151],v[20:23],a[48:51]
s_add_u32 m0, s65, 65536
EX v106,v106
MM a[56:59],a[148:151],v[28:31],a[56:59]
EX v107,v107
MM a[56:59],a[144:147],v[24:27],a[56:59]
RD a[232:235],v226 offset:40960
CV v77,v106,v107
MM a[60:63],a[152:155],v[24:27],a[60:63]
EX v108,v108
MM a[60:63],a[156:159],v[28:31],a[60:63]
buffer_load_dwordx4 v235, s[12:15], s81 offen offset:0 lds
MM a[52:55],a[156:159],v[20:23],a[52:55]
EX v109,v109
MM a[52:55],a[152:155],v[16:19],a[52:55]
RD a[236:239],v227 offset:40960
CV v78,v108,v109
MM a[44:47],a[152:155],v[8:11],a[44:47]
EX v110,v110
MM a[44:47],a[156:159],v[12:15],a[44:47]
EX v111,v111
MM a[36:39],a[156:159],v[4:7],a[36:39]
RD a[240:243],v224 offset:45056
CV v79,v110,v111
MM a[36:39],a[152:155],v[0:3],a[36:39]
EX v112,v112
MM a[64:67],a[160:163],v[0:3],a[64:67]
buffer_load_dwordx4 v235, s[12:15], s64 offen offset:1024 lds
MM a[64:67],a[164:167],v[4:7],a[64:67]
EX v113,v113
MM a[72:75],a[164:167],v[12:15],a[72:75]
RD a[244:247],v225 offset:45056
CV v84,v112,v113
MM a[72:75],a[160:163],v[8:11],a[72:75]
EX v114,v114
MM a[80:83],a[160:163],v[16:19],a[80:83]
EX v115,v115
MM a[80:83],a[164:167],v[20:23],a[80:83]
RD a[248:251],v226 offset:45056
CV v85,v114,v115
MM a[88:91],a[164:167],v[28:31],a[88:91]
EX v116,v116
MM a[88:91],a[160:163],v[24:27],a[88:91]
buffer_load_dwordx4 v235, s[12:15], s81 offen offset:2048 lds
MM a[92:95],a[168:171],v[24:27],a[92:95]
EX v117,v117
MM a[92:95],a[172:175],v[28:31],a[92:95]
RD a[252:255],v227 offset:45056
CV v86,v116,v117
MM a[84:87],a[172:175],v[20:23],a[84:87]
EX v118,v118
MM a[84:87],a[168:171],v[16:19],a[84:87]
EX v119,v119
MM a[76:79],a[168:171],v[8:11],a[76:79]
CV v87,v118,v119
MM a[76:79],a[172:175],v[12:15],a[76:79]
EX v120,v120
MM a[68:71],a[172:175],v[4:7],a[68:71]
buffer_load_dwordx4 v235, s[12:15], s64 offen offset:3072 lds
MM a[68:71],a[168:171],v[0:3],a[68:71]
EX v121,v121
MM a[96:99],a[176:179],v[0:3],a[96:99]
EX v122,v122
MM a[96:99],a[180:183],v[4:7],a[96:99]
EX v123,v123
MM a[104:107],a[180:183],v[12:15],a[104:107]
EX v124,v124
MM a[104:107],a[176:179],v[8:11],a[104:107]
EX v125,v125
MM a[112:115],a[176:179],v[16:19],a[112:115]
EX v126,v126
MM a[112:115],a[180:183],v[20:23],a[112:115]
EX v127,v127
MM a[120:123],a[180:183],v[28:31],a[120:123]
s_waitcnt vmcnt(8)
v_cmp_ne_u32 vcc, s69, v252
s_cbranch_vccnz LRFG_%=
LRFGb_%=:
MM a[120:123],a[176:179],v[24:27],a[120:123]
v_lshrrev_b32 v253, 8, v248
v_lshlrev_b32 v253, 10, v253
MM a[124:127],a[184:187],v[24:27],a[124:127]
v_add_f32 v244, v144, v145
v_add_f32 v245, v148, v149
MM a[124:127],a[188:191],v[28:31],a[124:127]
v_add_f32 v246, v152, v153
v_add_f32 v247, v156, v157
v_and_b32 v252, 240, v248
MM a[116:119],a[188:191],v[20:23],a[116:119]
v_add_u32 v253, v253, v252
CV v92,v120,v121
MM a[116:119],a[184:187],v[16:19],a[116:119]
s_lshr_b32 s76, s18, 6
CV v93,v122,v123
CV v94,v124,v125
MM a[108:111],a[184:187],v[8:11],a[108:111]
s_lshl_b32 s76, s76, 13
s_bfe_u32 s90, s18, 131076
CV v95,v126,v127
MM a[108:111],a[188:191],v[12:15],a[108:111]
s_lshl_b32 s90, s90, 8
s_add_u32 s76, s76, s90
s_add_u32 s76, s76, 22183936
MM a[100:103],a[188:191],v[4:7],a[100:103]
s_lshl_b32 s90, %[swave], 11
s_add_u32 s90, s90, %[slds]
MM a[100:103],a[184:187],v[0:3],a[100:103]
s_add_u32 m0, s90, 131072
s_nop 0
buffer_load_dwordx4 v253, s[20:23], s76 offen offset:0 sc1 lds
v_smfmac_f32_16x16x64_bf16 v[144:147], v[236:239], v[64:71], v234
v_smfmac_f32_16x16x64_bf16 v[148:151], v[236:239], v[72:79], v234
v_smfmac_f32_16x16x64_bf16 v[152:155], v[236:239], v[80:87], v234
s_add_u32 m0, s90, 132096
s_add_u32 s76, s76, 4096
buffer_load_dwordx4 v253, s[20:23], s76 offen offset:0 sc1 lds
v_smfmac_f32_16x16x64_bf16 v[156:159], v[236:239], v[88:95], v234
s_waitcnt lgkmcnt(0)
s_add_u32 s67, s95, 3072
s_mov_b32 s70, 3
s_cmp_ge_u32 s16, 90
s_cbranch_scc1 LLAP_%=
MM v[0:3],a[192:195],v[160:163],v[128:131]
s_add_u32 s64, s64, 16384
TR a[128:129],v232 offset:16384
s_add_u32 s81, s64, 896
MM v[0:3],a[196:199],v[176:179],v[0:3]
TR a[130:131],v232 offset:20480
v_max3_f32 v249, v144, v145, v148
MM v[0:3],a[200:203],v[192:195],v[0:3]
TR a[132:133],v232 offset:24576
v_max3_f32 v249, v249, v149, v152
MM v[0:3],a[204:207],v[208:211],v[0:3]
TR a[134:135],v232 offset:28672
v_max3_f32 v249, v249, v153, v156
MM v[4:7],a[220:223],v[208:211],v[128:131]
TR a[136:137],v233 offset:16384
v_max_f32 v249, v249, v157
MM v[4:7],a[216:219],v[192:195],v[4:7]
TR a[138:139],v233 offset:20480
v_cmp_lt_f32 vcc, s71, v249
MM v[4:7],a[212:215],v[176:179],v[4:7]
TR a[140:141],v233 offset:24576
TR a[142:143],v233 offset:28672
MM v[4:7],a[208:211],v[160:163],v[4:7]
s_cbranch_vccnz LSITE21_%=
LBACK21_%=:
MM v[12:15],a[208:211],v[164:167],v[132:135]
EX v0,v0
MM v[12:15],a[212:215],v[180:183],v[12:15]
EX v1,v1
MM v[12:15],a[216:219],v[196:199],v[12:15]
TR a[144:145],v232 offset:16896
CV v0,v0,v1
MM v[12:15],a[220:223],v[212:215],v[12:15]
EX v2,v2
MM v[8:11],a[204:207],v[212:215],v[132:135]
EX v3,v3
MM v[8:11],a[200:203],v[196:199],v[8:11]
TR a[146:147],v232 offset:20992
CV v1,v2,v3
MM v[8:11],a[196:199],v[180:183],v[8:11]
EX v4,v4
MM v[8:11],a[192:195],v[164:167],v[8:11]
TR a[148:149],v232 offset:25088
TR a[150:151],v232 offset:29184
MM v[16:19],a[192:195],v[168:171],v[136:139]
EX v5,v5
MM v[16:19],a[196:199],v[184:187],v[16:19]
TR a[152:153],v233 offset:16896
CV v2,v4,v5
MM v[16:19],a[200:203],v[200:203],v[16:19]
EX v6,v6
MM v[16:19],a[204:207],v[216:219],v[16:19]
TR a[154:155],v233 offset:20992
TR a[156:157],v233 offset:25088
MM v[20:23],a[220:223],v[216:219],v[136:139]
s_add_u32 s87, s95, 3072
buffer_store_dword v240, v248, s[20:23], s87 offen offset:12 sc1
MM v[20:23],a[216:219],v[200:203],v[20:23]
EX v7,v7
MM v[20:23],a[212:215],v[184:187],v[20:23]
TR a[158:159],v233 offset:29184
CV v3,v6,v7
MM v[20:23],a[208:211],v[168:171],v[20:23]
EX v8,v8
MM v[28:31],a[208:211],v[172:175],v[140:143]
EX v9,v9
MM v[28:31],a[212:215],v[188:191],v[28:31]
TR a[160:161],v232 offset:17408
CV v8,v8,v9
MM v[28:31],a[216:219],v[204:207],v[28:31]
EX v10,v10
MM v[28:31],a[220:223],v[220:223],v[28:31]
EX v11,v11
MM v[24:27],a[204:207],v[220:223],v[140:143]
TR a[162:163],v232 offset:21504
CV v9,v10,v11
MM v[24:27],a[200:203],v[204:207],v[24:27]
TR a[164:165],v232 offset:25600
TR a[166:167],v232 offset:29696
MM v[24:27],a[196:199],v[188:191],v[24:27]
)ASM"
R"ASM(
EX v12,v12
MM v[24:27],a[192:195],v[172:175],v[24:27]
EX v13,v13
MM v[32:35],a[224:227],v[160:163],v[128:131]
TR a[168:169],v233 offset:17408
CV v10,v12,v13
MM v[32:35],a[228:231],v[176:179],v[32:35]
EX v14,v14
MM v[32:35],a[232:235],v[192:195],v[32:35]
TR a[170:171],v233 offset:21504
TR a[172:173],v233 offset:25600
MM v[32:35],a[236:239],v[208:211],v[32:35]
EX v15,v15
MM v[36:39],a[252:255],v[208:211],v[128:131]
TR a[174:175],v233 offset:29696
CV v11,v14,v15
MM v[36:39],a[248:251],v[192:195],v[36:39]
EX v16,v16
MM v[36:39],a[244:247],v[176:179],v[36:39]
EX v17,v17
MM v[36:39],a[240:243],v[160:163],v[36:39]
TR a[176:177],v232 offset:17920
CV v16,v16,v17
MM v[44:47],a[240:243],v[164:167],v[132:135]
EX v18,v18
MM v[44:47],a[244:247],v[180:183],v[44:47]
TR a[178:179],v232 offset:22016
TR a[180:181],v232 offset:26112
MM v[44:47],a[248:251],v[196:199],v[44:47]
EX v19,v19
MM v[44:47],a[252:255],v[212:215],v[44:47]
TR a[182:183],v232 offset:30208
CV v17,v18,v19
MM v[40:43],a[236:239],v[212:215],v[132:135]
EX v20,v20
MM v[40:43],a[232:235],v[196:199],v[40:43]
EX v21,v21
MM v[40:43],a[228:231],v[180:183],v[40:43]
TR a[184:185],v233 offset:17920
CV v18,v20,v21
MM v[40:43],a[224:227],v[164:167],v[40:43]
EX v22,v22
MM v[48:51],a[224:227],v[168:171],v[136:139]
TR a[186:187],v233 offset:22016
TR a[188:189],v233 offset:26112
MM v[48:51],a[228:231],v[184:187],v[48:51]
EX v23,v23
MM v[48:51],a[232:235],v[200:203],v[48:51]
TR a[190:191],v233 offset:30208
CV v19,v22,v23
MM v[48:51],a[236:239],v[216:219],v[48:51]
EX v24,v24
MM v[52:55],a[252:255],v[216:219],v[136:139]
EX v25,v25
MM v[52:55],a[248:251],v[200:203],v[52:55]
EX v26,v26
MM v[52:55],a[244:247],v[184:187],v[52:55]
EX v27,v27
MM v[52:55],a[240:243],v[168:171],v[52:55]
EX v28,v28
MM v[60:63],a[240:243],v[172:175],v[140:143]
EX v29,v29
MM v[60:63],a[244:247],v[188:191],v[60:63]
EX v30,v30
MM v[60:63],a[248:251],v[204:207],v[60:63]
EX v31,v31
MM v[60:63],a[252:255],v[220:223],v[60:63]
EX v32,v32
MM v[56:59],a[236:239],v[220:223],v[140:143]
EX v33,v33
MM v[56:59],a[232:235],v[204:207],v[56:59]
EX v34,v34
MM v[56:59],a[228:231],v[188:191],v[56:59]
EX v35,v35
MM v[56:59],a[224:227],v[172:175],v[56:59]
s_waitcnt lgkmcnt(0)
s_barrier
MM a[0:3],a[128:131],v[64:67],a[0:3]
s_add_u32 m0, s65, 16384
CV v24,v24,v25
CV v25,v26,v27
MM a[0:3],a[132:135],v[68:71],a[0:3]
RD a[192:195],v224 offset:49152
CV v26,v28,v29
MM a[8:11],a[132:135],v[76:79],a[8:11]
RD a[196:199],v225 offset:49152
CV v27,v30,v31
MM a[8:11],a[128:131],v[72:75],a[8:11]
buffer_load_dwordx4 v228, s[8:11], s64 offen offset:0 lds
MM a[16:19],a[128:131],v[80:83],a[16:19]
RD a[200:203],v226 offset:49152
CV v4,v32,v33
MM a[16:19],a[132:135],v[84:87],a[16:19]
RD a[204:207],v227 offset:49152
CV v5,v34,v35
MM a[24:27],a[132:135],v[92:95],a[24:27]
buffer_load_dwordx4 v229, s[8:11], s64 offen offset:1024 lds
MM a[24:27],a[128:131],v[88:91],a[24:27]
RD a[208:211],v224 offset:53248
RD a[212:215],v225 offset:53248
MM a[28:31],a[136:139],v[88:91],a[28:31]
buffer_load_dwordx4 v230, s[8:11], s64 offen offset:2048 lds
MM a[28:31],a[140:143],v[92:95],a[28:31]
EX v36,v36
MM a[20:23],a[140:143],v[84:87],a[20:23]
EX v37,v37
MM a[20:23],a[136:139],v[80:83],a[20:23]
RD a[216:219],v226 offset:53248
CV v6,v36,v37
MM a[12:15],a[136:139],v[72:75],a[12:15]
EX v38,v38
MM a[12:15],a[140:143],v[76:79],a[12:15]
RD a[220:223],v227 offset:53248
RD a[224:227],v224 offset:57344
MM a[4:7],a[140:143],v[68:71],a[4:7]
EX v39,v39
MM a[4:7],a[136:139],v[64:67],a[4:7]
RD a[228:231],v225 offset:57344
CV v7,v38,v39
MM a[32:35],a[144:147],v[64:67],a[32:35]
EX v40,v40
MM a[32:35],a[148:151],v[68:71],a[32:35]
buffer_load_dwordx4 v231, s[8:11], s64 offen offset:3072 lds
MM a[40:43],a[148:151],v[76:79],a[40:43]
s_add_u32 m0, s65, 81920
EX v41,v41
MM a[40:43],a[144:147],v[72:75],a[40:43]
RD a[232:235],v226 offset:57344
CV v12,v40,v41
MM a[48:51],a[144:147],v[80:83],a[48:51]
EX v42,v42
MM a[48:51],a[148:151],v[84:87],a[48:51]
EX v43,v43
MM a[56:59],a[148:151],v[92:95],a[56:59]
RD a[236:239],v227 offset:57344
CV v13,v42,v43
MM a[56:59],a[144:147],v[88:91],a[56:59]
EX v44,v44
MM a[60:63],a[152:155],v[88:91],a[60:63]
buffer_load_dwordx4 v235, s[12:15], s81 offen offset:0 lds
MM a[60:63],a[156:159],v[92:95],a[60:63]
EX v45,v45
MM a[52:55],a[156:159],v[84:87],a[52:55]
RD a[240:243],v224 offset:61440
CV v14,v44,v45
MM a[52:55],a[152:155],v[80:83],a[52:55]
EX v46,v46
MM a[44:47],a[152:155],v[72:75],a[44:47]
EX v47,v47
MM a[44:47],a[156:159],v[76:79],a[44:47]
RD a[244:247],v225 offset:61440
CV v15,v46,v47
MM a[36:39],a[156:159],v[68:71],a[36:39]
buffer_load_dwordx4 v235, s[12:15], s64 offen offset:1024 lds
MM a[36:39],a[152:155],v[64:67],a[36:39]
EX v48,v48
MM a[64:67],a[160:163],v[64:67],a[64:67]
EX v49,v49
MM a[64:67],a[164:167],v[68:71],a[64:67]
RD a[248:251],v226 offset:61440
CV v20,v48,v49
MM a[72:75],a[164:167],v[76:79],a[72:75]
EX v50,v50
MM a[72:75],a[160:163],v[72:75],a[72:75]
EX v51,v51
MM a[80:83],a[160:163],v[80:83],a[80:83]
RD a[252:255],v227 offset:61440
CV v21,v50,v51
MM a[80:83],a[164:167],v[84:87],a[80:83]
buffer_load_dwordx4 v235, s[12:15], s81 offen offset:2048 lds
MM a[88:91],a[164:167],v[92:95],a[88:91]
EX v52,v52
MM a[88:91],a[160:163],v[88:91],a[88:91]
EX v53,v53
MM a[92:95],a[168:171],v[88:91],a[92:95]
EX v54,v54
MM a[92:95],a[172:175],v[92:95],a[92:95]
buffer_load_dwordx4 v235, s[12:15], s64 offen offset:3072 lds
MM a[84:87],a[172:175],v[84:87],a[84:87]
EX v55,v55
MM a[84:87],a[168:171],v[80:83],a[84:87]
EX v56,v56
MM a[76:79],a[168:171],v[72:75],a[76:79]
EX v57,v57
MM a[76:79],a[172:175],v[76:79],a[76:79]
EX v58,v58
MM a[68:71],a[172:175],v[68:71],a[68:71]
EX v59,v59
MM a[68:71],a[168:171],v[64:67],a[68:71]
EX v60,v60
MM a[96:99],a[176:179],v[64:67],a[96:99]
EX v61,v61
MM a[96:99],a[180:183],v[68:71],a[96:99]
EX v62,v62
MM a[104:107],a[180:183],v[76:79],a[104:107]
EX v63,v63
MM a[104:107],a[176:179],v[72:75],a[104:107]
CV v22,v52,v53
CV v23,v54,v55
MM a[112:115],a[176:179],v[80:83],a[112:115]
CV v28,v56,v57
CV v29,v58,v59
MM a[112:115],a[180:183],v[84:87],a[112:115]
CV v30,v60,v61
CV v31,v62,v63
MM a[120:123],a[180:183],v[92:95],a[120:123]
MM a[120:123],a[176:179],v[88:91],a[120:123]
MM a[124:127],a[184:187],v[88:91],a[124:127]
v_add_f32 v244, v144, v145
v_add_f32 v245, v148, v149
MM a[124:127],a[188:191],v[92:95],a[124:127]
v_add_f32 v246, v152, v153
v_add_f32 v247, v156, v157
MM a[116:119],a[188:191],v[84:87],a[116:119]
MM a[116:119],a[184:187],v[80:83],a[116:119]
MM a[108:111],a[184:187],v[72:75],a[108:111]
MM a[108:111],a[188:191],v[76:79],a[108:111]
MM a[100:103],a[188:191],v[68:71],a[100:103]
MM a[100:103],a[184:187],v[64:67],a[100:103]
v_smfmac_f32_16x16x64_bf16 v[144:147], v[236:239], v[0:7], v234
v_smfmac_f32_16x16x64_bf16 v[148:151], v[236:239], v[8:15], v234
v_smfmac_f32_16x16x64_bf16 v[152:155], v[236:239], v[16:23], v234
v_smfmac_f32_16x16x64_bf16 v[156:159], v[236:239], v[24:31], v234
s_waitcnt lgkmcnt(0)
MM v[64:67],a[192:195],v[160:163],v[128:131]
s_add_u32 s64, s64, 16384
TR a[128:129],v232 offset:32768
s_add_u32 s81, s64, 896
MM v[64:67],a[196:199],v[176:179],v[64:67]
TR a[130:131],v232 offset:36864
v_max3_f32 v249, v144, v145, v148
MM v[64:67],a[200:203],v[192:195],v[64:67]
TR a[132:133],v232 offset:40960
v_max3_f32 v249, v249, v149, v152
MM v[64:67],a[204:207],v[208:211],v[64:67]
TR a[134:135],v232 offset:45056
v_max3_f32 v249, v249, v153, v156
MM v[68:71],a[220:223],v[208:211],v[128:131]
TR a[136:137],v233 offset:32768
v_max_f32 v249, v249, v157
MM v[68:71],a[216:219],v[192:195],v[68:71]
TR a[138:139],v233 offset:36864
v_cmp_lt_f32 vcc, s71, v249
MM v[68:71],a[212:215],v[176:179],v[68:71]
TR a[140:141],v233 offset:40960
TR a[142:143],v233 offset:45056
MM v[68:71],a[208:211],v[160:163],v[68:71]
s_cbranch_vccnz LSITE22_%=
LBACK22_%=:
MM v[76:79],a[208:211],v[164:167],v[132:135]
v_lshrrev_b32 v43, 7, v248
v_bfe_u32 v42, v248, 6, 1
MM v[76:79],a[212:215],v[180:183],v[76:79]
v_lshl_add_u32 v40, v42, 3, v43
v_lshlrev_b32 v40, 4, v40
MM v[76:79],a[216:219],v[196:199],v[76:79]
v_bfe_u32 v42, v248, 4, 2
v_lshl_add_u32 v40, v42, 2, v40
MM v[76:79],a[220:223],v[212:215],v[76:79]
s_lshl_b32 s76, %[swave], 11
EX v64,v64
MM v[72:75],a[204:207],v[212:215],v[132:135]
s_add_u32 s76, s76, %[slds]
EX v65,v65
MM v[72:75],a[200:203],v[196:199],v[72:75]
s_add_u32 s76, s76, 131072
v_add_u32 v40, s76, v40
CV v64,v64,v65
MM v[72:75],a[196:199],v[180:183],v[72:75]
s_waitcnt vmcnt(9)
ds_read_b32 v32, v40 offset:0
ds_read_b32 v36, v40 offset:256
MM v[72:75],a[192:195],v[164:167],v[72:75]
ds_read_b32 v33, v40 offset:512
ds_read_b32 v37, v40 offset:768
MM v[80:83],a[192:195],v[168:171],v[136:139]
ds_read_b32 v34, v40 offset:1024
LPG17_%=:
s_cbranch_execz LTR3_%=
ds_read_b32 v38, v40 offset:1280
EX v66,v66
MM v[80:83],a[196:199],v[184:187],v[80:83]
ds_read_b32 v35, v40 offset:1536
ds_read_b32 v39, v40 offset:1792
EX v67,v67
MM v[80:83],a[200:203],v[200:203],v[80:83]
v_mul_u32_u24 v41, 3456, v43
v_bfe_u32 v42, v248, 4, 3
MM v[80:83],a[204:207],v[216:219],v[80:83]
TR a[144:145],v232 offset:33280
v_lshl_add_u32 v41, v42, 1, v41
EX v68,v68
MM v[84:87],a[220:223],v[216:219],v[136:139]
s_add_u32 s76, s18, 21626880
EX v69,v69
MM v[84:87],a[216:219],v[200:203],v[84:87]
TR a[146:147],v232 offset:37376
TR a[148:149],v232 offset:41472
MM v[84:87],a[212:215],v[184:187],v[84:87]
EX v70,v70
MM v[84:87],a[208:211],v[168:171],v[84:87]
TR a[150:151],v232 offset:45568
CV v65,v66,v67
MM v[92:95],a[208:211],v[172:175],v[140:143]
EX v71,v71
MM v[92:95],a[212:215],v[188:191],v[92:95]
EX v72,v72
MM v[92:95],a[216:219],v[204:207],v[92:95]
TR a[152:153],v233 offset:33280
CV v66,v68,v69
MM v[92:95],a[220:223],v[220:223],v[92:95]
EX v73,v73
MM v[88:91],a[204:207],v[220:223],v[140:143]
s_waitcnt lgkmcnt(5)
v_add_f32 v32, v32, v33
v_add_f32 v36, v36, v37
MM v[88:91],a[200:203],v[204:207],v[88:91]
v_add_f32 v34, v34, v35
v_add_f32 v38, v38, v39
MM v[88:91],a[196:199],v[188:191],v[88:91]
TR a[154:155],v233 offset:37376
v_add_f32 v32, v32, v34
v_add_f32 v36, v36, v38
MM v[88:91],a[192:195],v[172:175],v[88:91]
CV v42,v32,v36
v_lshlrev_b32 v32, 16, v42
EX v74,v74
MM v[96:99],a[224:227],v[160:163],v[128:131]
TR a[156:157],v233 offset:41472
v_and_b32 v36, 0xffff0000, v42
v_mul_f32 v42, 0xbfb8aa3b, v32
EX v75,v75
MM v[96:99],a[228:231],v[176:179],v[96:99]
EX v42,v42
MM v[96:99],a[232:235],v[192:195],v[96:99]
TR a[158:159],v233 offset:45568
v_add_f32 v42, 1.0, v42
EX v76,v76
MM v[96:99],a[236:239],v[208:211],v[96:99]
v_rcp_f32 v42, v42
MM v[100:103],a[252:255],v[208:211],v[128:131]
TR a[160:161],v232 offset:33792
v_mul_f32 v42, v32, v42
CV v42,v42,v42
EX v77,v77
CV v67,v70,v71
MM v[100:103],a[248:251],v[192:195],v[100:103]
v_lshlrev_b32 v42, 16, v42
v_mul_f32 v32, v42, v36
MM v[100:103],a[244:247],v[176:179],v[100:103]
TR a[162:163],v232 offset:37888
CV v43,v32,v32
EX v78,v78
MM v[100:103],a[240:243],v[160:163],v[100:103]
buffer_store_short v43, v41, s[20:23], s76 offen offset:0 sc1
s_nop 1
TR a[164:165],v232 offset:41984
MM v[108:111],a[240:243],v[164:167],v[132:135]
EX v79,v79
CV v72,v72,v73
MM v[108:111],a[244:247],v[180:183],v[108:111]
TR a[166:167],v232 offset:46080
CV v73,v74,v75
MM v[108:111],a[248:251],v[196:199],v[108:111]
EX v80,v80
MM v[108:111],a[252:255],v[212:215],v[108:111]
TR a[168:169],v233 offset:33792
TR a[170:171],v233 offset:37888
EX v81,v81
MM v[104:107],a[236:239],v[212:215],v[132:135]
TR a[172:173],v233 offset:41984
CV v74,v76,v77
MM v[104:107],a[232:235],v[196:199],v[104:107]
EX v82,v82
MM v[104:107],a[228:231],v[180:183],v[104:107]
EX v83,v83
MM v[104:107],a[224:227],v[164:167],v[104:107]
TR a[174:175],v233 offset:46080
CV v75,v78,v79
MM v[112:115],a[224:227],v[168:171],v[136:139]
EX v84,v84
MM v[112:115],a[228:231],v[184:187],v[112:115]
TR a[176:177],v232 offset:34304
TR a[178:179],v232 offset:38400
MM v[112:115],a[232:235],v[200:203],v[112:115]
EX v85,v85
MM v[112:115],a[236:239],v[216:219],v[112:115]
TR a[180:181],v232 offset:42496
CV v80,v80,v81
MM v[116:119],a[252:255],v[216:219],v[136:139]
EX v86,v86
MM v[116:119],a[248:251],v[200:203],v[116:119]
)ASM"
R"ASM(
TR a[182:183],v232 offset:46592
CV v81,v82,v83
MM v[116:119],a[244:247],v[184:187],v[116:119]
EX v87,v87
MM v[116:119],a[240:243],v[168:171],v[116:119]
TR a[184:185],v233 offset:34304
CV v82,v84,v85
MM v[124:127],a[240:243],v[172:175],v[140:143]
EX v88,v88
MM v[124:127],a[244:247],v[188:191],v[124:127]
TR a[186:187],v233 offset:38400
TR a[188:189],v233 offset:42496
MM v[124:127],a[248:251],v[204:207],v[124:127]
TR a[190:191],v233 offset:46592
CV v83,v86,v87
EX v89,v89
MM v[124:127],a[252:255],v[220:223],v[124:127]
EX v90,v90
MM v[120:123],a[236:239],v[220:223],v[140:143]
EX v91,v91
MM v[120:123],a[232:235],v[204:207],v[120:123]
EX v92,v92
MM v[120:123],a[228:231],v[188:191],v[120:123]
EX v93,v93
MM v[120:123],a[224:227],v[172:175],v[120:123]
s_waitcnt lgkmcnt(0)
s_barrier
MM a[0:3],a[128:131],v[0:3],a[0:3]
s_add_u32 m0, s65, 32768
CV v88,v88,v89
CV v89,v90,v91
MM a[0:3],a[132:135],v[4:7],a[0:3]
EX v94,v94
MM a[8:11],a[132:135],v[12:15],a[8:11]
RD a[192:195],v224 offset:0
CV v90,v92,v93
MM a[8:11],a[128:131],v[8:11],a[8:11]
EX v95,v95
MM a[16:19],a[128:131],v[16:19],a[16:19]
RD a[196:199],v225 offset:0
CV v91,v94,v95
MM a[16:19],a[132:135],v[20:23],a[16:19]
EX v96,v96
MM a[24:27],a[132:135],v[28:31],a[24:27]
buffer_load_dwordx4 v228, s[8:11], s64 offen offset:0 lds
MM a[24:27],a[128:131],v[24:27],a[24:27]
EX v97,v97
MM a[28:31],a[136:139],v[24:27],a[28:31]
EX v98,v98
MM a[28:31],a[140:143],v[28:31],a[28:31]
RD a[200:203],v226 offset:0
CV v68,v96,v97
MM a[20:23],a[140:143],v[20:23],a[20:23]
EX v99,v99
MM a[20:23],a[136:139],v[16:19],a[20:23]
RD a[204:207],v227 offset:0
CV v69,v98,v99
MM a[12:15],a[136:139],v[8:11],a[12:15]
EX v100,v100
MM a[12:15],a[140:143],v[12:15],a[12:15]
buffer_load_dwordx4 v229, s[8:11], s64 offen offset:1024 lds
MM a[4:7],a[140:143],v[4:7],a[4:7]
EX v101,v101
MM a[4:7],a[136:139],v[0:3],a[4:7]
RD a[208:211],v224 offset:4096
CV v70,v100,v101
MM a[32:35],a[144:147],v[0:3],a[32:35]
EX v102,v102
MM a[32:35],a[148:151],v[4:7],a[32:35]
EX v103,v103
MM a[40:43],a[148:151],v[12:15],a[40:43]
RD a[212:215],v225 offset:4096
CV v71,v102,v103
MM a[40:43],a[144:147],v[8:11],a[40:43]
EX v104,v104
MM a[48:51],a[144:147],v[16:19],a[48:51]
buffer_load_dwordx4 v230, s[8:11], s64 offen offset:2048 lds
MM a[48:51],a[148:151],v[20:23],a[48:51]
EX v105,v105
MM a[56:59],a[148:151],v[28:31],a[56:59]
RD a[216:219],v226 offset:4096
CV v76,v104,v105
MM a[56:59],a[144:147],v[24:27],a[56:59]
EX v106,v106
MM a[60:63],a[152:155],v[24:27],a[60:63]
EX v107,v107
MM a[60:63],a[156:159],v[28:31],a[60:63]
RD a[220:223],v227 offset:4096
CV v77,v106,v107
MM a[52:55],a[156:159],v[20:23],a[52:55]
EX v108,v108
MM a[52:55],a[152:155],v[16:19],a[52:55]
buffer_load_dwordx4 v231, s[8:11], s64 offen offset:3072 lds
MM a[44:47],a[152:155],v[8:11],a[44:47]
s_add_u32 m0, s65, 98304
EX v109,v109
MM a[44:47],a[156:159],v[12:15],a[44:47]
RD a[224:227],v224 offset:8192
CV v78,v108,v109
MM a[36:39],a[156:159],v[4:7],a[36:39]
EX v110,v110
MM a[36:39],a[152:155],v[0:3],a[36:39]
EX v111,v111
MM a[64:67],a[160:163],v[0:3],a[64:67]
RD a[228:231],v225 offset:8192
CV v79,v110,v111
MM a[64:67],a[164:167],v[4:7],a[64:67]
EX v112,v112
MM a[72:75],a[164:167],v[12:15],a[72:75]
buffer_load_dwordx4 v235, s[12:15], s81 offen offset:0 lds
MM a[72:75],a[160:163],v[8:11],a[72:75]
EX v113,v113
MM a[80:83],a[160:163],v[16:19],a[80:83]
RD a[232:235],v226 offset:8192
CV v84,v112,v113
MM a[80:83],a[164:167],v[20:23],a[80:83]
EX v114,v114
MM a[88:91],a[164:167],v[28:31],a[88:91]
EX v115,v115
MM a[88:91],a[160:163],v[24:27],a[88:91]
RD a[236:239],v227 offset:8192
CV v85,v114,v115
MM a[92:95],a[168:171],v[24:27],a[92:95]
EX v116,v116
MM a[92:95],a[172:175],v[28:31],a[92:95]
buffer_load_dwordx4 v235, s[12:15], s64 offen offset:1024 lds
MM a[84:87],a[172:175],v[20:23],a[84:87]
EX v117,v117
MM a[84:87],a[168:171],v[16:19],a[84:87]
RD a[240:243],v224 offset:12288
CV v86,v116,v117
MM a[76:79],a[168:171],v[8:11],a[76:79]
EX v118,v118
MM a[76:79],a[172:175],v[12:15],a[76:79]
EX v119,v119
MM a[68:71],a[172:175],v[4:7],a[68:71]
RD a[244:247],v225 offset:12288
CV v87,v118,v119
MM a[68:71],a[168:171],v[0:3],a[68:71]
buffer_load_dwordx4 v235, s[12:15], s81 offen offset:2048 lds
MM a[96:99],a[176:179],v[0:3],a[96:99]
EX v120,v120
MM a[96:99],a[180:183],v[4:7],a[96:99]
EX v121,v121
MM a[104:107],a[180:183],v[12:15],a[104:107]
RD a[248:251],v226 offset:12288
CV v92,v120,v121
MM a[104:107],a[176:179],v[8:11],a[104:107]
EX v122,v122
MM a[112:115],a[176:179],v[16:19],a[112:115]
EX v123,v123
MM a[112:115],a[180:183],v[20:23],a[112:115]
RD a[252:255],v227 offset:12288
CV v93,v122,v123
MM a[120:123],a[180:183],v[28:31],a[120:123]
buffer_load_dwordx4 v235, s[12:15], s64 offen offset:3072 lds
MM a[120:123],a[176:179],v[24:27],a[120:123]
EX v124,v124
MM a[124:127],a[184:187],v[24:27],a[124:127]
v_add_f32 v244, v144, v145
v_add_f32 v245, v148, v149
MM a[124:127],a[188:191],v[28:31],a[124:127]
v_add_f32 v246, v152, v153
v_add_f32 v247, v156, v157
MM a[116:119],a[188:191],v[20:23],a[116:119]
EX v125,v125
MM a[116:119],a[184:187],v[16:19],a[116:119]
EX v126,v126
MM a[108:111],a[184:187],v[8:11],a[108:111]
EX v127,v127
MM a[108:111],a[188:191],v[12:15],a[108:111]
CV v94,v124,v125
CV v95,v126,v127
MM a[100:103],a[188:191],v[4:7],a[100:103]
MM a[100:103],a[184:187],v[0:3],a[100:103]
v_smfmac_f32_16x16x64_bf16 v[144:147], v[236:239], v[64:71], v234
v_smfmac_f32_16x16x64_bf16 v[148:151], v[236:239], v[72:79], v234
v_smfmac_f32_16x16x64_bf16 v[152:155], v[236:239], v[80:87], v234
v_smfmac_f32_16x16x64_bf16 v[156:159], v[236:239], v[88:95], v234
s_waitcnt lgkmcnt(0)
MM v[0:3],a[192:195],v[160:163],v[128:131]
s_add_u32 s64, s64, 16384
TR a[128:129],v232 offset:49152
s_add_u32 s81, s64, 896
MM v[0:3],a[196:199],v[176:179],v[0:3]
TR a[130:131],v232 offset:53248
v_max3_f32 v249, v144, v145, v148
MM v[0:3],a[200:203],v[192:195],v[0:3]
TR a[132:133],v232 offset:57344
v_max3_f32 v249, v249, v149, v152
MM v[0:3],a[204:207],v[208:211],v[0:3]
TR a[134:135],v232 offset:61440
v_max3_f32 v249, v249, v153, v156
MM v[4:7],a[220:223],v[208:211],v[128:131]
TR a[136:137],v233 offset:49152
v_max_f32 v249, v249, v157
MM v[4:7],a[216:219],v[192:195],v[4:7]
TR a[138:139],v233 offset:53248
v_cmp_lt_f32 vcc, s71, v249
MM v[4:7],a[212:215],v[176:179],v[4:7]
TR a[140:141],v233 offset:57344
TR a[142:143],v233 offset:61440
MM v[4:7],a[208:211],v[160:163],v[4:7]
s_cbranch_vccnz LSITE23_%=
LBACK23_%=:
MM v[12:15],a[208:211],v[164:167],v[132:135]
EX v0,v0
MM v[12:15],a[212:215],v[180:183],v[12:15]
EX v1,v1
MM v[12:15],a[216:219],v[196:199],v[12:15]
TR a[144:145],v232 offset:49664
CV v0,v0,v1
MM v[12:15],a[220:223],v[212:215],v[12:15]
EX v2,v2
MM v[8:11],a[204:207],v[212:215],v[132:135]
EX v3,v3
MM v[8:11],a[200:203],v[196:199],v[8:11]
TR a[146:147],v232 offset:53760
CV v1,v2,v3
MM v[8:11],a[196:199],v[180:183],v[8:11]
EX v4,v4
MM v[8:11],a[192:195],v[164:167],v[8:11]
TR a[148:149],v232 offset:57856
TR a[150:151],v232 offset:61952
MM v[16:19],a[192:195],v[168:171],v[136:139]
EX v5,v5
MM v[16:19],a[196:199],v[184:187],v[16:19]
TR a[152:153],v233 offset:49664
CV v2,v4,v5
MM v[16:19],a[200:203],v[200:203],v[16:19]
EX v6,v6
MM v[16:19],a[204:207],v[216:219],v[16:19]
TR a[154:155],v233 offset:53760
TR a[156:157],v233 offset:57856
MM v[20:23],a[220:223],v[216:219],v[136:139]
EX v7,v7
MM v[20:23],a[216:219],v[200:203],v[20:23]
TR a[158:159],v233 offset:61952
CV v3,v6,v7
MM v[20:23],a[212:215],v[184:187],v[20:23]
EX v8,v8
MM v[20:23],a[208:211],v[168:171],v[20:23]
EX v9,v9
MM v[28:31],a[208:211],v[172:175],v[140:143]
TR a[160:161],v232 offset:50176
CV v8,v8,v9
MM v[28:31],a[212:215],v[188:191],v[28:31]
EX v10,v10
MM v[28:31],a[216:219],v[204:207],v[28:31]
TR a[162:163],v232 offset:54272
TR a[164:165],v232 offset:58368
MM v[28:31],a[220:223],v[220:223],v[28:31]
EX v11,v11
MM v[24:27],a[204:207],v[220:223],v[140:143]
TR a[166:167],v232 offset:62464
CV v9,v10,v11
MM v[24:27],a[200:203],v[204:207],v[24:27]
EX v12,v12
MM v[24:27],a[196:199],v[188:191],v[24:27]
EX v13,v13
MM v[24:27],a[192:195],v[172:175],v[24:27]
TR a[168:169],v233 offset:50176
CV v10,v12,v13
MM v[32:35],a[224:227],v[160:163],v[128:131]
EX v14,v14
MM v[32:35],a[228:231],v[176:179],v[32:35]
TR a[170:171],v233 offset:54272
TR a[172:173],v233 offset:58368
MM v[32:35],a[232:235],v[192:195],v[32:35]
EX v15,v15
MM v[32:35],a[236:239],v[208:211],v[32:35]
TR a[174:175],v233 offset:62464
CV v11,v14,v15
MM v[36:39],a[252:255],v[208:211],v[128:131]
EX v16,v16
MM v[36:39],a[248:251],v[192:195],v[36:39]
EX v17,v17
MM v[36:39],a[244:247],v[176:179],v[36:39]
TR a[176:177],v232 offset:50688
CV v16,v16,v17
MM v[36:39],a[240:243],v[160:163],v[36:39]
EX v18,v18
MM v[44:47],a[240:243],v[164:167],v[132:135]
TR a[178:179],v232 offset:54784
TR a[180:181],v232 offset:58880
MM v[44:47],a[244:247],v[180:183],v[44:47]
EX v19,v19
MM v[44:47],a[248:251],v[196:199],v[44:47]
TR a[182:183],v232 offset:62976
CV v17,v18,v19
MM v[44:47],a[252:255],v[212:215],v[44:47]
EX v20,v20
MM v[40:43],a[236:239],v[212:215],v[132:135]
TR a[184:185],v233 offset:50688
TR a[186:187],v233 offset:54784
MM v[40:43],a[232:235],v[196:199],v[40:43]
EX v21,v21
MM v[40:43],a[228:231],v[180:183],v[40:43]
TR a[188:189],v233 offset:58880
CV v18,v20,v21
MM v[40:43],a[224:227],v[164:167],v[40:43]
EX v22,v22
MM v[48:51],a[224:227],v[168:171],v[136:139]
EX v23,v23
MM v[48:51],a[228:231],v[184:187],v[48:51]
TR a[190:191],v233 offset:62976
CV v19,v22,v23
MM v[48:51],a[232:235],v[200:203],v[48:51]
EX v24,v24
MM v[48:51],a[236:239],v[216:219],v[48:51]
EX v25,v25
MM v[52:55],a[252:255],v[216:219],v[136:139]
EX v26,v26
MM v[52:55],a[248:251],v[200:203],v[52:55]
EX v27,v27
MM v[52:55],a[244:247],v[184:187],v[52:55]
EX v28,v28
MM v[52:55],a[240:243],v[168:171],v[52:55]
EX v29,v29
MM v[60:63],a[240:243],v[172:175],v[140:143]
EX v30,v30
MM v[60:63],a[244:247],v[188:191],v[60:63]
EX v31,v31
MM v[60:63],a[248:251],v[204:207],v[60:63]
EX v32,v32
MM v[60:63],a[252:255],v[220:223],v[60:63]
EX v33,v33
MM v[56:59],a[236:239],v[220:223],v[140:143]
EX v34,v34
MM v[56:59],a[232:235],v[204:207],v[56:59]
EX v35,v35
MM v[56:59],a[228:231],v[188:191],v[56:59]
EX v36,v36
MM v[56:59],a[224:227],v[172:175],v[56:59]
s_waitcnt vmcnt(13) lgkmcnt(0)
s_barrier
MM a[0:3],a[128:131],v[64:67],a[0:3]
s_add_u32 m0, s65, 49152
CV v24,v24,v25
CV v25,v26,v27
MM a[0:3],a[132:135],v[68:71],a[0:3]
RD a[192:195],v224 offset:16384
CV v26,v28,v29
MM a[8:11],a[132:135],v[76:79],a[8:11]
RD a[196:199],v225 offset:16384
CV v27,v30,v31
MM a[8:11],a[128:131],v[72:75],a[8:11]
buffer_load_dwordx4 v228, s[8:11], s64 offen offset:0 lds
MM a[16:19],a[128:131],v[80:83],a[16:19]
RD a[200:203],v226 offset:16384
CV v4,v32,v33
MM a[16:19],a[132:135],v[84:87],a[16:19]
RD a[204:207],v227 offset:16384
CV v5,v34,v35
MM a[24:27],a[132:135],v[92:95],a[24:27]
buffer_load_dwordx4 v229, s[8:11], s64 offen offset:1024 lds
MM a[24:27],a[128:131],v[88:91],a[24:27]
RD a[208:211],v224 offset:20480
RD a[212:215],v225 offset:20480
MM a[28:31],a[136:139],v[88:91],a[28:31]
buffer_load_dwordx4 v230, s[8:11], s64 offen offset:2048 lds
MM a[28:31],a[140:143],v[92:95],a[28:31]
RD a[216:219],v226 offset:20480
RD a[220:223],v227 offset:20480
MM a[20:23],a[140:143],v[84:87],a[20:23]
EX v37,v37
MM a[20:23],a[136:139],v[80:83],a[20:23]
RD a[224:227],v224 offset:24576
CV v6,v36,v37
MM a[12:15],a[136:139],v[72:75],a[12:15]
EX v38,v38
MM a[12:15],a[140:143],v[76:79],a[12:15]
buffer_load_dwordx4 v231, s[8:11], s64 offen offset:3072 lds
MM a[4:7],a[140:143],v[68:71],a[4:7]
s_add_u32 m0, s65, 114688
EX v39,v39
MM a[4:7],a[136:139],v[64:67],a[4:7]
RD a[228:231],v225 offset:24576
CV v7,v38,v39
MM a[32:35],a[144:147],v[64:67],a[32:35]
EX v40,v40
MM a[32:35],a[148:151],v[68:71],a[32:35]
EX v41,v41
MM a[40:43],a[148:151],v[76:79],a[40:43]
RD a[232:235],v226 offset:24576
CV v12,v40,v41
MM a[40:43],a[144:147],v[72:75],a[40:43]
EX v42,v42
MM a[48:51],a[144:147],v[80:83],a[48:51]
buffer_load_dwordx4 v235, s[12:15], s81 offen offset:0 lds
MM a[48:51],a[148:151],v[84:87],a[48:51]
EX v43,v43
MM a[56:59],a[148:151],v[92:95],a[56:59]
)ASM"
R"ASM(
RD a[236:239],v227 offset:24576
CV v13,v42,v43
MM a[56:59],a[144:147],v[88:91],a[56:59]
EX v44,v44
MM a[60:63],a[152:155],v[88:91],a[60:63]
EX v45,v45
MM a[60:63],a[156:159],v[92:95],a[60:63]
RD a[240:243],v224 offset:28672
CV v14,v44,v45
MM a[52:55],a[156:159],v[84:87],a[52:55]
EX v46,v46
MM a[52:55],a[152:155],v[80:83],a[52:55]
buffer_load_dwordx4 v235, s[12:15], s64 offen offset:1024 lds
MM a[44:47],a[152:155],v[72:75],a[44:47]
EX v47,v47
MM a[44:47],a[156:159],v[76:79],a[44:47]
RD a[244:247],v225 offset:28672
CV v15,v46,v47
MM a[36:39],a[156:159],v[68:71],a[36:39]
EX v48,v48
MM a[36:39],a[152:155],v[64:67],a[36:39]
EX v49,v49
MM a[64:67],a[160:163],v[64:67],a[64:67]
RD a[248:251],v226 offset:28672
CV v20,v48,v49
MM a[64:67],a[164:167],v[68:71],a[64:67]
EX v50,v50
MM a[72:75],a[164:167],v[76:79],a[72:75]
buffer_load_dwordx4 v235, s[12:15], s81 offen offset:2048 lds
MM a[72:75],a[160:163],v[72:75],a[72:75]
EX v51,v51
MM a[80:83],a[160:163],v[80:83],a[80:83]
RD a[252:255],v227 offset:28672
CV v21,v50,v51
MM a[80:83],a[164:167],v[84:87],a[80:83]
EX v52,v52
MM a[88:91],a[164:167],v[92:95],a[88:91]
EX v53,v53
MM a[88:91],a[160:163],v[88:91],a[88:91]
buffer_load_dwordx4 v235, s[12:15], s64 offen offset:3072 lds
MM a[92:95],a[168:171],v[88:91],a[92:95]
EX v54,v54
MM a[92:95],a[172:175],v[92:95],a[92:95]
EX v55,v55
MM a[84:87],a[172:175],v[84:87],a[84:87]
EX v56,v56
MM a[84:87],a[168:171],v[80:83],a[84:87]
EX v57,v57
MM a[76:79],a[168:171],v[72:75],a[76:79]
EX v58,v58
MM a[76:79],a[172:175],v[76:79],a[76:79]
EX v59,v59
MM a[68:71],a[172:175],v[68:71],a[68:71]
EX v60,v60
MM a[68:71],a[168:171],v[64:67],a[68:71]
EX v61,v61
MM a[96:99],a[176:179],v[64:67],a[96:99]
EX v62,v62
MM a[96:99],a[180:183],v[68:71],a[96:99]
EX v63,v63
MM a[104:107],a[180:183],v[76:79],a[104:107]
CV v22,v52,v53
CV v23,v54,v55
MM a[104:107],a[176:179],v[72:75],a[104:107]
CV v28,v56,v57
CV v29,v58,v59
MM a[112:115],a[176:179],v[80:83],a[112:115]
CV v30,v60,v61
CV v31,v62,v63
MM a[112:115],a[180:183],v[84:87],a[112:115]
MM a[120:123],a[180:183],v[92:95],a[120:123]
s_lshr_b32 s91, s18, 2
s_add_u32 s91, s91, 69632
s_waitcnt vmcnt(16)
MM a[120:123],a[176:179],v[88:91],a[120:123]
v_mov_b32 v250, s69
s_mov_b64 exec, 1
buffer_store_dword v250, off, s[20:23], s91 offset:0 sc1
s_mov_b64 exec, -1
MM a[124:127],a[184:187],v[88:91],a[124:127]
v_add_f32 v244, v144, v145
v_add_f32 v245, v148, v149
MM a[124:127],a[188:191],v[92:95],a[124:127]
v_add_f32 v246, v152, v153
v_add_f32 v247, v156, v157
v_lshlrev_b32 v96, 3, v248
MM a[116:119],a[188:191],v[84:87],a[116:119]
v_min_u32 v96, 4992, v96
LPG18_%=:
s_cbranch_execz LTR3_%=
s_sub_u32 s91, 5242880, s26
MM a[116:119],a[184:187],v[80:83],a[116:119]
s_sub_u32 s60, s24, s91
s_subb_u32 s61, s25, 0
s_mov_b32 s62, 5242880
MM a[108:111],a[184:187],v[72:75],a[108:111]
s_mov_b32 s63, 131072
s_lshl_b32 s90, s16, 2
s_add_u32 s90, s90, %[swave]
MM a[108:111],a[188:191],v[76:79],a[108:111]
s_mul_i32 s90, s90, 10240
s_add_u32 s90, s90, 0
s_add_u32 s91, %[slds], 135168
MM a[100:103],a[188:191],v[68:71],a[100:103]
s_mov_b32 m0, s91
s_nop 0
buffer_load_dword v96, s[60:63], s90 offen offset:0 lds
MM a[100:103],a[184:187],v[64:67],a[100:103]
s_lshr_b32 s77, %[sb], 3
s_lshl_b32 s77, s77, 2
s_add_u32 s77, s77, %[swave]
v_smfmac_f32_16x16x64_bf16 v[144:147], v[236:239], v[0:7], v234
s_lshl_b32 s77, s77, 9
s_add_u32 s77, s77, 0
v_smfmac_f32_16x16x64_bf16 v[148:151], v[236:239], v[8:15], v234
v_lshrrev_b32 v253, 1, v248
v_smfmac_f32_16x16x64_bf16 v[152:155], v[236:239], v[16:23], v234
v_add_u32 v253, s77, v253
v_smfmac_f32_16x16x64_bf16 v[156:159], v[236:239], v[24:31], v234
s_waitcnt lgkmcnt(0)
s_mov_b32 s77, LKEND_ka-LPD1_%=-16
v_min_u32 v253, s77, v253
s_getpc_b64 s[98:99]
LPD1_%=:
global_load_dword v253, v253, s[98:99]
MM v[64:67],a[192:195],v[160:163],v[128:131]
s_add_u32 s64, s64, 16384
TR a[128:129],v232 offset:0
s_add_u32 s81, s64, 896
MM v[64:67],a[196:199],v[176:179],v[64:67]
TR a[130:131],v232 offset:4096
v_max3_f32 v249, v144, v145, v148
MM v[64:67],a[200:203],v[192:195],v[64:67]
TR a[132:133],v232 offset:8192
v_max3_f32 v249, v249, v149, v152
MM v[64:67],a[204:207],v[208:211],v[64:67]
TR a[134:135],v232 offset:12288
v_max3_f32 v249, v249, v153, v156
MM v[68:71],a[220:223],v[208:211],v[128:131]
TR a[136:137],v233 offset:0
v_max_f32 v249, v249, v157
MM v[68:71],a[216:219],v[192:195],v[68:71]
TR a[138:139],v233 offset:4096
v_cmp_lt_f32 vcc, s71, v249
MM v[68:71],a[212:215],v[176:179],v[68:71]
TR a[140:141],v233 offset:8192
TR a[142:143],v233 offset:12288
MM v[68:71],a[208:211],v[160:163],v[68:71]
s_cbranch_vccnz LSITE24_%=
LBACK24_%=:
MM v[76:79],a[208:211],v[164:167],v[132:135]
EX v64,v64
MM v[76:79],a[212:215],v[180:183],v[76:79]
EX v65,v65
MM v[76:79],a[216:219],v[196:199],v[76:79]
TR a[144:145],v232 offset:512
CV v64,v64,v65
MM v[76:79],a[220:223],v[212:215],v[76:79]
EX v66,v66
MM v[72:75],a[204:207],v[212:215],v[132:135]
EX v67,v67
MM v[72:75],a[200:203],v[196:199],v[72:75]
TR a[146:147],v232 offset:4608
CV v65,v66,v67
MM v[72:75],a[196:199],v[180:183],v[72:75]
EX v68,v68
MM v[72:75],a[192:195],v[164:167],v[72:75]
TR a[148:149],v232 offset:8704
TR a[150:151],v232 offset:12800
MM v[80:83],a[192:195],v[168:171],v[136:139]
EX v69,v69
MM v[80:83],a[196:199],v[184:187],v[80:83]
TR a[152:153],v233 offset:512
CV v66,v68,v69
MM v[80:83],a[200:203],v[200:203],v[80:83]
EX v70,v70
MM v[80:83],a[204:207],v[216:219],v[80:83]
TR a[154:155],v233 offset:4608
TR a[156:157],v233 offset:8704
MM v[84:87],a[220:223],v[216:219],v[136:139]
s_add_u32 s87, s95, 4096
buffer_store_dword v240, v248, s[20:23], s87 offen offset:12 sc1
MM v[84:87],a[216:219],v[200:203],v[84:87]
EX v71,v71
MM v[84:87],a[212:215],v[184:187],v[84:87]
TR a[158:159],v233 offset:12800
CV v67,v70,v71
MM v[84:87],a[208:211],v[168:171],v[84:87]
EX v72,v72
MM v[92:95],a[208:211],v[172:175],v[140:143]
EX v73,v73
MM v[92:95],a[212:215],v[188:191],v[92:95]
TR a[160:161],v232 offset:1024
CV v72,v72,v73
MM v[92:95],a[216:219],v[204:207],v[92:95]
EX v74,v74
MM v[92:95],a[220:223],v[220:223],v[92:95]
EX v75,v75
MM v[88:91],a[204:207],v[220:223],v[140:143]
TR a[162:163],v232 offset:5120
CV v73,v74,v75
MM v[88:91],a[200:203],v[204:207],v[88:91]
TR a[164:165],v232 offset:9216
TR a[166:167],v232 offset:13312
MM v[88:91],a[196:199],v[188:191],v[88:91]
EX v76,v76
MM v[88:91],a[192:195],v[172:175],v[88:91]
EX v77,v77
MM v[96:99],a[224:227],v[160:163],v[128:131]
TR a[168:169],v233 offset:1024
CV v74,v76,v77
MM v[96:99],a[228:231],v[176:179],v[96:99]
EX v78,v78
MM v[96:99],a[232:235],v[192:195],v[96:99]
TR a[170:171],v233 offset:5120
TR a[172:173],v233 offset:9216
MM v[96:99],a[236:239],v[208:211],v[96:99]
EX v79,v79
MM v[100:103],a[252:255],v[208:211],v[128:131]
TR a[174:175],v233 offset:13312
CV v75,v78,v79
MM v[100:103],a[248:251],v[192:195],v[100:103]
EX v80,v80
MM v[100:103],a[244:247],v[176:179],v[100:103]
EX v81,v81
MM v[100:103],a[240:243],v[160:163],v[100:103]
TR a[176:177],v232 offset:1536
CV v80,v80,v81
MM v[108:111],a[240:243],v[164:167],v[132:135]
EX v82,v82
MM v[108:111],a[244:247],v[180:183],v[108:111]
TR a[178:179],v232 offset:5632
TR a[180:181],v232 offset:9728
MM v[108:111],a[248:251],v[196:199],v[108:111]
EX v83,v83
MM v[108:111],a[252:255],v[212:215],v[108:111]
TR a[182:183],v232 offset:13824
CV v81,v82,v83
MM v[104:107],a[236:239],v[212:215],v[132:135]
EX v84,v84
MM v[104:107],a[232:235],v[196:199],v[104:107]
EX v85,v85
MM v[104:107],a[228:231],v[180:183],v[104:107]
TR a[184:185],v233 offset:1536
CV v82,v84,v85
MM v[104:107],a[224:227],v[164:167],v[104:107]
EX v86,v86
MM v[112:115],a[224:227],v[168:171],v[136:139]
TR a[186:187],v233 offset:5632
TR a[188:189],v233 offset:9728
MM v[112:115],a[228:231],v[184:187],v[112:115]
EX v87,v87
MM v[112:115],a[232:235],v[200:203],v[112:115]
TR a[190:191],v233 offset:13824
CV v83,v86,v87
MM v[112:115],a[236:239],v[216:219],v[112:115]
EX v88,v88
MM v[116:119],a[252:255],v[216:219],v[136:139]
EX v89,v89
MM v[116:119],a[248:251],v[200:203],v[116:119]
EX v90,v90
MM v[116:119],a[244:247],v[184:187],v[116:119]
EX v91,v91
MM v[116:119],a[240:243],v[168:171],v[116:119]
EX v92,v92
MM v[124:127],a[240:243],v[172:175],v[140:143]
EX v93,v93
MM v[124:127],a[244:247],v[188:191],v[124:127]
EX v94,v94
MM v[124:127],a[248:251],v[204:207],v[124:127]
EX v95,v95
MM v[124:127],a[252:255],v[220:223],v[124:127]
EX v96,v96
MM v[120:123],a[236:239],v[220:223],v[140:143]
EX v97,v97
MM v[120:123],a[232:235],v[204:207],v[120:123]
EX v98,v98
MM v[120:123],a[228:231],v[188:191],v[120:123]
EX v99,v99
MM v[120:123],a[224:227],v[172:175],v[120:123]
s_waitcnt vmcnt(16) lgkmcnt(0)
s_barrier
MM a[0:3],a[128:131],v[0:3],a[0:3]
s_add_u32 m0, s65, 0
CV v88,v88,v89
CV v89,v90,v91
MM a[0:3],a[132:135],v[4:7],a[0:3]
RD a[192:195],v224 offset:32768
CV v90,v92,v93
MM a[8:11],a[132:135],v[12:15],a[8:11]
RD a[196:199],v225 offset:32768
CV v91,v94,v95
MM a[8:11],a[128:131],v[8:11],a[8:11]
buffer_load_dwordx4 v228, s[8:11], s64 offen offset:0 lds
MM a[16:19],a[128:131],v[16:19],a[16:19]
RD a[200:203],v226 offset:32768
CV v68,v96,v97
MM a[16:19],a[132:135],v[20:23],a[16:19]
RD a[204:207],v227 offset:32768
CV v69,v98,v99
MM a[24:27],a[132:135],v[28:31],a[24:27]
buffer_load_dwordx4 v229, s[8:11], s64 offen offset:1024 lds
MM a[24:27],a[128:131],v[24:27],a[24:27]
RD a[208:211],v224 offset:36864
RD a[212:215],v225 offset:36864
MM a[28:31],a[136:139],v[24:27],a[28:31]
buffer_load_dwordx4 v230, s[8:11], s64 offen offset:2048 lds
MM a[28:31],a[140:143],v[28:31],a[28:31]
EX v100,v100
MM a[20:23],a[140:143],v[20:23],a[20:23]
EX v101,v101
MM a[20:23],a[136:139],v[16:19],a[20:23]
RD a[216:219],v226 offset:36864
CV v70,v100,v101
MM a[12:15],a[136:139],v[8:11],a[12:15]
EX v102,v102
MM a[12:15],a[140:143],v[12:15],a[12:15]
RD a[220:223],v227 offset:36864
RD a[224:227],v224 offset:40960
MM a[4:7],a[140:143],v[4:7],a[4:7]
EX v103,v103
MM a[4:7],a[136:139],v[0:3],a[4:7]
RD a[228:231],v225 offset:40960
CV v71,v102,v103
MM a[32:35],a[144:147],v[0:3],a[32:35]
EX v104,v104
MM a[32:35],a[148:151],v[4:7],a[32:35]
buffer_load_dwordx4 v231, s[8:11], s64 offen offset:3072 lds
MM a[40:43],a[148:151],v[12:15],a[40:43]
s_add_u32 m0, s65, 65536
EX v105,v105
MM a[40:43],a[144:147],v[8:11],a[40:43]
RD a[232:235],v226 offset:40960
CV v76,v104,v105
MM a[48:51],a[144:147],v[16:19],a[48:51]
EX v106,v106
MM a[48:51],a[148:151],v[20:23],a[48:51]
EX v107,v107
MM a[56:59],a[148:151],v[28:31],a[56:59]
RD a[236:239],v227 offset:40960
CV v77,v106,v107
MM a[56:59],a[144:147],v[24:27],a[56:59]
EX v108,v108
MM a[60:63],a[152:155],v[24:27],a[60:63]
buffer_load_dwordx4 v235, s[12:15], s81 offen offset:0 lds
MM a[60:63],a[156:159],v[28:31],a[60:63]
EX v109,v109
MM a[52:55],a[156:159],v[20:23],a[52:55]
RD a[240:243],v224 offset:45056
CV v78,v108,v109
MM a[52:55],a[152:155],v[16:19],a[52:55]
EX v110,v110
MM a[44:47],a[152:155],v[8:11],a[44:47]
EX v111,v111
MM a[44:47],a[156:159],v[12:15],a[44:47]
RD a[244:247],v225 offset:45056
CV v79,v110,v111
MM a[36:39],a[156:159],v[4:7],a[36:39]
buffer_load_dwordx4 v235, s[12:15], s64 offen offset:1024 lds
MM a[36:39],a[152:155],v[0:3],a[36:39]
EX v112,v112
MM a[64:67],a[160:163],v[0:3],a[64:67]
EX v113,v113
MM a[64:67],a[164:167],v[4:7],a[64:67]
RD a[248:251],v226 offset:45056
CV v84,v112,v113
MM a[72:75],a[164:167],v[12:15],a[72:75]
EX v114,v114
MM a[72:75],a[160:163],v[8:11],a[72:75]
EX v115,v115
MM a[80:83],a[160:163],v[16:19],a[80:83]
RD a[252:255],v227 offset:45056
CV v85,v114,v115
MM a[80:83],a[164:167],v[20:23],a[80:83]
buffer_load_dwordx4 v235, s[12:15], s81 offen offset:2048 lds
MM a[88:91],a[164:167],v[28:31],a[88:91]
EX v116,v116
MM a[88:91],a[160:163],v[24:27],a[88:91]
EX v117,v117
MM a[92:95],a[168:171],v[24:27],a[92:95]
EX v118,v118
MM a[92:95],a[172:175],v[28:31],a[92:95]
buffer_load_dwordx4 v235, s[12:15], s64 offen offset:3072 lds
MM a[84:87],a[172:175],v[20:23],a[84:87]
EX v119,v119
MM a[84:87],a[168:171],v[16:19],a[84:87]
EX v120,v120
MM a[76:79],a[168:171],v[8:11],a[76:79]
EX v121,v121
MM a[76:79],a[172:175],v[12:15],a[76:79]
EX v122,v122
)ASM"
R"ASM(
MM a[68:71],a[172:175],v[4:7],a[68:71]
EX v123,v123
MM a[68:71],a[168:171],v[0:3],a[68:71]
EX v124,v124
MM a[96:99],a[176:179],v[0:3],a[96:99]
EX v125,v125
MM a[96:99],a[180:183],v[4:7],a[96:99]
EX v126,v126
MM a[104:107],a[180:183],v[12:15],a[104:107]
EX v127,v127
MM a[104:107],a[176:179],v[8:11],a[104:107]
CV v86,v116,v117
CV v87,v118,v119
MM a[112:115],a[176:179],v[16:19],a[112:115]
CV v92,v120,v121
CV v93,v122,v123
MM a[112:115],a[180:183],v[20:23],a[112:115]
CV v94,v124,v125
CV v95,v126,v127
MM a[120:123],a[180:183],v[28:31],a[120:123]
MM a[120:123],a[176:179],v[24:27],a[120:123]
MM a[124:127],a[184:187],v[24:27],a[124:127]
v_add_f32 v244, v144, v145
v_add_f32 v245, v148, v149
MM a[124:127],a[188:191],v[28:31],a[124:127]
v_add_f32 v246, v152, v153
v_add_f32 v247, v156, v157
MM a[116:119],a[188:191],v[20:23],a[116:119]
MM a[116:119],a[184:187],v[16:19],a[116:119]
MM a[108:111],a[184:187],v[8:11],a[108:111]
MM a[108:111],a[188:191],v[12:15],a[108:111]
MM a[100:103],a[188:191],v[4:7],a[100:103]
MM a[100:103],a[184:187],v[0:3],a[100:103]
v_smfmac_f32_16x16x64_bf16 v[144:147], v[236:239], v[64:71], v234
v_smfmac_f32_16x16x64_bf16 v[148:151], v[236:239], v[72:79], v234
v_smfmac_f32_16x16x64_bf16 v[152:155], v[236:239], v[80:87], v234
v_smfmac_f32_16x16x64_bf16 v[156:159], v[236:239], v[88:95], v234
s_waitcnt lgkmcnt(0)
MM v[0:3],a[192:195],v[160:163],v[128:131]
s_add_u32 s64, s64, 16384
TR a[128:129],v232 offset:16384
s_add_u32 s81, s64, 896
MM v[0:3],a[196:199],v[176:179],v[0:3]
TR a[130:131],v232 offset:20480
v_max3_f32 v249, v144, v145, v148
MM v[0:3],a[200:203],v[192:195],v[0:3]
TR a[132:133],v232 offset:24576
v_max3_f32 v249, v249, v149, v152
MM v[0:3],a[204:207],v[208:211],v[0:3]
TR a[134:135],v232 offset:28672
v_max3_f32 v249, v249, v153, v156
MM v[4:7],a[220:223],v[208:211],v[128:131]
TR a[136:137],v233 offset:16384
v_max_f32 v249, v249, v157
MM v[4:7],a[216:219],v[192:195],v[4:7]
TR a[138:139],v233 offset:20480
v_cmp_lt_f32 vcc, s71, v249
MM v[4:7],a[212:215],v[176:179],v[4:7]
TR a[140:141],v233 offset:24576
TR a[142:143],v233 offset:28672
MM v[4:7],a[208:211],v[160:163],v[4:7]
s_cbranch_vccnz LSITE25_%=
LBACK25_%=:
MM v[12:15],a[208:211],v[164:167],v[132:135]
s_waitcnt vmcnt(9)
EX v0,v0
MM v[12:15],a[212:215],v[180:183],v[12:15]
s_mul_i32 s76, s46, 96
EX v1,v1
MM v[12:15],a[216:219],v[196:199],v[12:15]
s_add_u32 s76, s76, 69632
v_lshrrev_b32 v253, 2, v248
v_min_u32 v253, 92, v253
MM v[12:15],a[220:223],v[212:215],v[12:15]
buffer_load_dword v252, v253, s[20:23], s76 offen offset:0 sc1
MM v[8:11],a[204:207],v[212:215],v[132:135]
EX v2,v2
MM v[8:11],a[200:203],v[196:199],v[8:11]
TR a[144:145],v232 offset:16896
CV v0,v0,v1
MM v[8:11],a[196:199],v[180:183],v[8:11]
EX v3,v3
MM v[8:11],a[192:195],v[164:167],v[8:11]
TR a[146:147],v232 offset:20992
CV v1,v2,v3
MM v[16:19],a[192:195],v[168:171],v[136:139]
EX v4,v4
MM v[16:19],a[196:199],v[184:187],v[16:19]
TR a[148:149],v232 offset:25088
TR a[150:151],v232 offset:29184
MM v[16:19],a[200:203],v[200:203],v[16:19]
EX v5,v5
MM v[16:19],a[204:207],v[216:219],v[16:19]
TR a[152:153],v233 offset:16896
CV v2,v4,v5
MM v[20:23],a[220:223],v[216:219],v[136:139]
EX v6,v6
MM v[20:23],a[216:219],v[200:203],v[20:23]
EX v7,v7
MM v[20:23],a[212:215],v[184:187],v[20:23]
TR a[154:155],v233 offset:20992
CV v3,v6,v7
MM v[20:23],a[208:211],v[168:171],v[20:23]
EX v8,v8
MM v[28:31],a[208:211],v[172:175],v[140:143]
TR a[156:157],v233 offset:25088
TR a[158:159],v233 offset:29184
MM v[28:31],a[212:215],v[188:191],v[28:31]
EX v9,v9
MM v[28:31],a[216:219],v[204:207],v[28:31]
TR a[160:161],v232 offset:17408
CV v8,v8,v9
MM v[28:31],a[220:223],v[220:223],v[28:31]
EX v10,v10
MM v[24:27],a[204:207],v[220:223],v[140:143]
EX v11,v11
MM v[24:27],a[200:203],v[204:207],v[24:27]
TR a[162:163],v232 offset:21504
CV v9,v10,v11
MM v[24:27],a[196:199],v[188:191],v[24:27]
EX v12,v12
MM v[24:27],a[192:195],v[172:175],v[24:27]
TR a[164:165],v232 offset:25600
TR a[166:167],v232 offset:29696
MM v[32:35],a[224:227],v[160:163],v[128:131]
EX v13,v13
MM v[32:35],a[228:231],v[176:179],v[32:35]
TR a[168:169],v233 offset:17408
CV v10,v12,v13
MM v[32:35],a[232:235],v[192:195],v[32:35]
EX v14,v14
MM v[32:35],a[236:239],v[208:211],v[32:35]
EX v15,v15
MM v[36:39],a[252:255],v[208:211],v[128:131]
TR a[170:171],v233 offset:21504
CV v11,v14,v15
MM v[36:39],a[248:251],v[192:195],v[36:39]
EX v16,v16
MM v[36:39],a[244:247],v[176:179],v[36:39]
TR a[172:173],v233 offset:25600
TR a[174:175],v233 offset:29696
MM v[36:39],a[240:243],v[160:163],v[36:39]
EX v17,v17
MM v[44:47],a[240:243],v[164:167],v[132:135]
TR a[176:177],v232 offset:17920
CV v16,v16,v17
MM v[44:47],a[244:247],v[180:183],v[44:47]
EX v18,v18
MM v[44:47],a[248:251],v[196:199],v[44:47]
TR a[178:179],v232 offset:22016
TR a[180:181],v232 offset:26112
MM v[44:47],a[252:255],v[212:215],v[44:47]
EX v19,v19
MM v[40:43],a[236:239],v[212:215],v[132:135]
TR a[182:183],v232 offset:30208
CV v17,v18,v19
MM v[40:43],a[232:235],v[196:199],v[40:43]
EX v20,v20
MM v[40:43],a[228:231],v[180:183],v[40:43]
EX v21,v21
MM v[40:43],a[224:227],v[164:167],v[40:43]
TR a[184:185],v233 offset:17920
CV v18,v20,v21
MM v[48:51],a[224:227],v[168:171],v[136:139]
EX v22,v22
MM v[48:51],a[228:231],v[184:187],v[48:51]
TR a[186:187],v233 offset:22016
TR a[188:189],v233 offset:26112
MM v[48:51],a[232:235],v[200:203],v[48:51]
EX v23,v23
MM v[48:51],a[236:239],v[216:219],v[48:51]
TR a[190:191],v233 offset:30208
CV v19,v22,v23
MM v[52:55],a[252:255],v[216:219],v[136:139]
EX v24,v24
MM v[52:55],a[248:251],v[200:203],v[52:55]
EX v25,v25
MM v[52:55],a[244:247],v[184:187],v[52:55]
EX v26,v26
MM v[52:55],a[240:243],v[168:171],v[52:55]
EX v27,v27
MM v[60:63],a[240:243],v[172:175],v[140:143]
EX v28,v28
MM v[60:63],a[244:247],v[188:191],v[60:63]
EX v29,v29
MM v[60:63],a[248:251],v[204:207],v[60:63]
EX v30,v30
MM v[60:63],a[252:255],v[220:223],v[60:63]
EX v31,v31
MM v[56:59],a[236:239],v[220:223],v[140:143]
EX v32,v32
MM v[56:59],a[232:235],v[204:207],v[56:59]
EX v33,v33
MM v[56:59],a[228:231],v[188:191],v[56:59]
EX v34,v34
MM v[56:59],a[224:227],v[172:175],v[56:59]
s_waitcnt lgkmcnt(0)
s_barrier
MM a[0:3],a[128:131],v[64:67],a[0:3]
s_add_u32 m0, s65, 16384
CV v24,v24,v25
CV v25,v26,v27
MM a[0:3],a[132:135],v[68:71],a[0:3]
RD a[192:195],v224 offset:49152
CV v26,v28,v29
MM a[8:11],a[132:135],v[76:79],a[8:11]
RD a[196:199],v225 offset:49152
CV v27,v30,v31
MM a[8:11],a[128:131],v[72:75],a[8:11]
buffer_load_dwordx4 v228, s[8:11], s64 offen offset:0 lds
MM a[16:19],a[128:131],v[80:83],a[16:19]
RD a[200:203],v226 offset:49152
CV v4,v32,v33
MM a[16:19],a[132:135],v[84:87],a[16:19]
buffer_load_dwordx4 v229, s[8:11], s64 offen offset:1024 lds
MM a[24:27],a[132:135],v[92:95],a[24:27]
RD a[204:207],v227 offset:49152
RD a[208:211],v224 offset:53248
MM a[24:27],a[128:131],v[88:91],a[24:27]
EX v35,v35
MM a[28:31],a[136:139],v[88:91],a[28:31]
RD a[212:215],v225 offset:53248
CV v5,v34,v35
MM a[28:31],a[140:143],v[92:95],a[28:31]
EX v36,v36
MM a[20:23],a[140:143],v[84:87],a[20:23]
buffer_load_dwordx4 v230, s[8:11], s64 offen offset:2048 lds
MM a[20:23],a[136:139],v[80:83],a[20:23]
EX v37,v37
MM a[12:15],a[136:139],v[72:75],a[12:15]
RD a[216:219],v226 offset:53248
CV v6,v36,v37
MM a[12:15],a[140:143],v[76:79],a[12:15]
EX v38,v38
MM a[4:7],a[140:143],v[68:71],a[4:7]
EX v39,v39
MM a[4:7],a[136:139],v[64:67],a[4:7]
RD a[220:223],v227 offset:53248
CV v7,v38,v39
MM a[32:35],a[144:147],v[64:67],a[32:35]
EX v40,v40
MM a[32:35],a[148:151],v[68:71],a[32:35]
buffer_load_dwordx4 v231, s[8:11], s64 offen offset:3072 lds
MM a[40:43],a[148:151],v[76:79],a[40:43]
s_add_u32 m0, s65, 81920
EX v41,v41
MM a[40:43],a[144:147],v[72:75],a[40:43]
RD a[224:227],v224 offset:57344
CV v12,v40,v41
MM a[48:51],a[144:147],v[80:83],a[48:51]
EX v42,v42
MM a[48:51],a[148:151],v[84:87],a[48:51]
EX v43,v43
MM a[56:59],a[148:151],v[92:95],a[56:59]
RD a[228:231],v225 offset:57344
CV v13,v42,v43
MM a[56:59],a[144:147],v[88:91],a[56:59]
EX v44,v44
MM a[60:63],a[152:155],v[88:91],a[60:63]
buffer_load_dwordx4 v235, s[12:15], s81 offen offset:0 lds
MM a[60:63],a[156:159],v[92:95],a[60:63]
EX v45,v45
MM a[52:55],a[156:159],v[84:87],a[52:55]
RD a[232:235],v226 offset:57344
CV v14,v44,v45
MM a[52:55],a[152:155],v[80:83],a[52:55]
EX v46,v46
MM a[44:47],a[152:155],v[72:75],a[44:47]
EX v47,v47
MM a[44:47],a[156:159],v[76:79],a[44:47]
RD a[236:239],v227 offset:57344
CV v15,v46,v47
MM a[36:39],a[156:159],v[68:71],a[36:39]
buffer_load_dwordx4 v235, s[12:15], s64 offen offset:1024 lds
MM a[36:39],a[152:155],v[64:67],a[36:39]
EX v48,v48
MM a[64:67],a[160:163],v[64:67],a[64:67]
EX v49,v49
MM a[64:67],a[164:167],v[68:71],a[64:67]
RD a[240:243],v224 offset:61440
CV v20,v48,v49
MM a[72:75],a[164:167],v[76:79],a[72:75]
EX v50,v50
MM a[72:75],a[160:163],v[72:75],a[72:75]
RD a[244:247],v225 offset:61440
RD a[248:251],v226 offset:61440
MM a[80:83],a[160:163],v[80:83],a[80:83]
EX v51,v51
MM a[80:83],a[164:167],v[84:87],a[80:83]
LPG19_%=:
s_cbranch_execz LTR3_%=
RD a[252:255],v227 offset:61440
CV v21,v50,v51
MM a[88:91],a[164:167],v[92:95],a[88:91]
EX v52,v52
MM a[88:91],a[160:163],v[88:91],a[88:91]
buffer_load_dwordx4 v235, s[12:15], s81 offen offset:2048 lds
MM a[92:95],a[168:171],v[88:91],a[92:95]
EX v53,v53
MM a[92:95],a[172:175],v[92:95],a[92:95]
EX v54,v54
MM a[84:87],a[172:175],v[84:87],a[84:87]
buffer_load_dwordx4 v235, s[12:15], s64 offen offset:3072 lds
MM a[84:87],a[168:171],v[80:83],a[84:87]
EX v55,v55
MM a[76:79],a[168:171],v[72:75],a[76:79]
EX v56,v56
MM a[76:79],a[172:175],v[76:79],a[76:79]
EX v57,v57
MM a[68:71],a[172:175],v[68:71],a[68:71]
EX v58,v58
MM a[68:71],a[168:171],v[64:67],a[68:71]
EX v59,v59
MM a[96:99],a[176:179],v[64:67],a[96:99]
EX v60,v60
MM a[96:99],a[180:183],v[68:71],a[96:99]
EX v61,v61
MM a[104:107],a[180:183],v[76:79],a[104:107]
EX v62,v62
MM a[104:107],a[176:179],v[72:75],a[104:107]
EX v63,v63
MM a[112:115],a[176:179],v[80:83],a[112:115]
CV v22,v52,v53
CV v23,v54,v55
MM a[112:115],a[180:183],v[84:87],a[112:115]
CV v28,v56,v57
CV v29,v58,v59
MM a[120:123],a[180:183],v[92:95],a[120:123]
s_waitcnt vmcnt(8)
v_cmp_ne_u32 vcc, s69, v252
s_cbranch_vccnz LRFH_%=
LRFHb_%=:
MM a[120:123],a[176:179],v[88:91],a[120:123]
v_bfe_u32 v253, v248, 4, 3
v_mul_u32_u24 v253, 3456, v253
MM a[124:127],a[184:187],v[88:91],a[124:127]
v_add_f32 v244, v144, v145
v_add_f32 v245, v148, v149
MM a[124:127],a[188:191],v[92:95],a[124:127]
v_add_f32 v246, v152, v153
v_add_f32 v247, v156, v157
v_lshrrev_b32 v252, 7, v248
MM a[116:119],a[188:191],v[84:87],a[116:119]
v_lshl_add_u32 v253, v252, 4, v253
CV v30,v60,v61
MM a[116:119],a[184:187],v[80:83],a[116:119]
s_min_u32 s90, %[swave], 2
s_mul_i32 s76, s46, 384
CV v31,v62,v63
MM a[108:111],a[184:187],v[72:75],a[108:111]
s_lshl_b32 s91, s90, 7
s_add_u32 s76, s76, s91
s_add_u32 s76, s76, 21626880
MM a[108:111],a[188:191],v[76:79],a[108:111]
s_lshl_b32 s90, s90, 10
s_add_u32 s90, s90, %[slds]
MM a[100:103],a[188:191],v[68:71],a[100:103]
s_add_u32 m0, s90, 131072
s_nop 0
buffer_load_dwordx4 v253, s[20:23], s76 offen offset:0 sc1 lds
MM a[100:103],a[184:187],v[64:67],a[100:103]
v_smfmac_f32_16x16x64_bf16 v[144:147], v[236:239], v[0:7], v234
v_smfmac_f32_16x16x64_bf16 v[148:151], v[236:239], v[8:15], v234
v_smfmac_f32_16x16x64_bf16 v[152:155], v[236:239], v[16:23], v234
v_smfmac_f32_16x16x64_bf16 v[156:159], v[236:239], v[24:31], v234
s_waitcnt lgkmcnt(0)
MM v[64:67],a[192:195],v[160:163],v[128:131]
s_add_u32 s64, s64, 16384
TR a[128:129],v232 offset:32768
s_add_u32 s81, s64, 896
MM v[64:67],a[196:199],v[176:179],v[64:67]
TR a[130:131],v232 offset:36864
v_max3_f32 v249, v144, v145, v148
MM v[64:67],a[200:203],v[192:195],v[64:67]
TR a[132:133],v232 offset:40960
v_max3_f32 v249, v249, v149, v152
MM v[64:67],a[204:207],v[208:211],v[64:67]
TR a[134:135],v232 offset:45056
v_max3_f32 v249, v249, v153, v156
MM v[68:71],a[220:223],v[208:211],v[128:131]
TR a[136:137],v233 offset:32768
v_max_f32 v249, v249, v157
MM v[68:71],a[216:219],v[192:195],v[68:71]
TR a[138:139],v233 offset:36864
v_cmp_lt_f32 vcc, s71, v249
MM v[68:71],a[212:215],v[176:179],v[68:71]
)ASM"
R"ASM(
TR a[140:141],v233 offset:40960
TR a[142:143],v233 offset:45056
MM v[68:71],a[208:211],v[160:163],v[68:71]
s_cbranch_vccnz LSITE26_%=
LBACK26_%=:
MM v[76:79],a[208:211],v[164:167],v[132:135]
EX v64,v64
MM v[76:79],a[212:215],v[180:183],v[76:79]
EX v65,v65
MM v[76:79],a[216:219],v[196:199],v[76:79]
TR a[144:145],v232 offset:33280
CV v64,v64,v65
MM v[76:79],a[220:223],v[212:215],v[76:79]
EX v66,v66
MM v[72:75],a[204:207],v[212:215],v[132:135]
EX v67,v67
MM v[72:75],a[200:203],v[196:199],v[72:75]
TR a[146:147],v232 offset:37376
CV v65,v66,v67
MM v[72:75],a[196:199],v[180:183],v[72:75]
EX v68,v68
MM v[72:75],a[192:195],v[164:167],v[72:75]
TR a[148:149],v232 offset:41472
TR a[150:151],v232 offset:45568
MM v[80:83],a[192:195],v[168:171],v[136:139]
EX v69,v69
MM v[80:83],a[196:199],v[184:187],v[80:83]
TR a[152:153],v233 offset:33280
CV v66,v68,v69
MM v[80:83],a[200:203],v[200:203],v[80:83]
EX v70,v70
MM v[80:83],a[204:207],v[216:219],v[80:83]
TR a[154:155],v233 offset:37376
TR a[156:157],v233 offset:41472
MM v[84:87],a[220:223],v[216:219],v[136:139]
EX v71,v71
MM v[84:87],a[216:219],v[200:203],v[84:87]
TR a[158:159],v233 offset:45568
CV v67,v70,v71
MM v[84:87],a[212:215],v[184:187],v[84:87]
EX v72,v72
MM v[84:87],a[208:211],v[168:171],v[84:87]
EX v73,v73
MM v[92:95],a[208:211],v[172:175],v[140:143]
TR a[160:161],v232 offset:33792
CV v72,v72,v73
MM v[92:95],a[212:215],v[188:191],v[92:95]
EX v74,v74
MM v[92:95],a[216:219],v[204:207],v[92:95]
TR a[162:163],v232 offset:37888
TR a[164:165],v232 offset:41984
MM v[92:95],a[220:223],v[220:223],v[92:95]
EX v75,v75
MM v[88:91],a[204:207],v[220:223],v[140:143]
TR a[166:167],v232 offset:46080
CV v73,v74,v75
MM v[88:91],a[200:203],v[204:207],v[88:91]
EX v76,v76
MM v[88:91],a[196:199],v[188:191],v[88:91]
EX v77,v77
MM v[88:91],a[192:195],v[172:175],v[88:91]
TR a[168:169],v233 offset:33792
CV v74,v76,v77
MM v[96:99],a[224:227],v[160:163],v[128:131]
EX v78,v78
MM v[96:99],a[228:231],v[176:179],v[96:99]
TR a[170:171],v233 offset:37888
TR a[172:173],v233 offset:41984
MM v[96:99],a[232:235],v[192:195],v[96:99]
EX v79,v79
MM v[96:99],a[236:239],v[208:211],v[96:99]
TR a[174:175],v233 offset:46080
CV v75,v78,v79
MM v[100:103],a[252:255],v[208:211],v[128:131]
EX v80,v80
MM v[100:103],a[248:251],v[192:195],v[100:103]
EX v81,v81
MM v[100:103],a[244:247],v[176:179],v[100:103]
TR a[176:177],v232 offset:34304
CV v80,v80,v81
MM v[100:103],a[240:243],v[160:163],v[100:103]
EX v82,v82
MM v[108:111],a[240:243],v[164:167],v[132:135]
TR a[178:179],v232 offset:38400
TR a[180:181],v232 offset:42496
MM v[108:111],a[244:247],v[180:183],v[108:111]
EX v83,v83
MM v[108:111],a[248:251],v[196:199],v[108:111]
TR a[182:183],v232 offset:46592
CV v81,v82,v83
MM v[108:111],a[252:255],v[212:215],v[108:111]
EX v84,v84
MM v[104:107],a[236:239],v[212:215],v[132:135]
TR a[184:185],v233 offset:34304
TR a[186:187],v233 offset:38400
MM v[104:107],a[232:235],v[196:199],v[104:107]
EX v85,v85
MM v[104:107],a[228:231],v[180:183],v[104:107]
TR a[188:189],v233 offset:42496
CV v82,v84,v85
MM v[104:107],a[224:227],v[164:167],v[104:107]
EX v86,v86
MM v[112:115],a[224:227],v[168:171],v[136:139]
EX v87,v87
MM v[112:115],a[228:231],v[184:187],v[112:115]
TR a[190:191],v233 offset:46592
CV v83,v86,v87
MM v[112:115],a[232:235],v[200:203],v[112:115]
EX v88,v88
MM v[112:115],a[236:239],v[216:219],v[112:115]
EX v89,v89
MM v[116:119],a[252:255],v[216:219],v[136:139]
EX v90,v90
MM v[116:119],a[248:251],v[200:203],v[116:119]
EX v91,v91
MM v[116:119],a[244:247],v[184:187],v[116:119]
EX v92,v92
MM v[116:119],a[240:243],v[168:171],v[116:119]
EX v93,v93
MM v[124:127],a[240:243],v[172:175],v[140:143]
EX v94,v94
MM v[124:127],a[244:247],v[188:191],v[124:127]
EX v95,v95
MM v[124:127],a[248:251],v[204:207],v[124:127]
EX v96,v96
MM v[124:127],a[252:255],v[220:223],v[124:127]
EX v97,v97
MM v[120:123],a[236:239],v[220:223],v[140:143]
EX v98,v98
MM v[120:123],a[232:235],v[204:207],v[120:123]
EX v99,v99
MM v[120:123],a[228:231],v[188:191],v[120:123]
s_getpc_b64 s[98:99]
LTA4_%=:
s_min_u32 s73, %[swave], 1
s_lshl_b32 s73, s73, 12
s_add_u32 s73, s73, LPG21_%=-LTA4_%=
s_add_u32 s98, s98, s73
s_addc_u32 s99, s99, 0
s_mov_b64 exec, 0
s_setpc_b64 s[98:99]
LTR4_%=:
s_mov_b64 exec, -1
CV v88,v88,v89
MM v[120:123],a[224:227],v[172:175],v[120:123]
s_waitcnt lgkmcnt(0)
s_barrier
MM a[0:3],a[128:131],v[0:3],a[0:3]
s_add_u32 m0, s65, 32768
CV v89,v90,v91
CV v90,v92,v93
MM a[0:3],a[132:135],v[4:7],a[0:3]
RD a[192:195],v224 offset:0
CV v91,v94,v95
MM a[8:11],a[132:135],v[12:15],a[8:11]
buffer_load_dwordx4 v228, s[8:11], s64 offen offset:0 lds
MM a[8:11],a[128:131],v[8:11],a[8:11]
RD a[196:199],v225 offset:0
CV v68,v96,v97
MM a[16:19],a[128:131],v[16:19],a[16:19]
RD a[200:203],v226 offset:0
CV v69,v98,v99
MM a[16:19],a[132:135],v[20:23],a[16:19]
buffer_load_dwordx4 v229, s[8:11], s64 offen offset:1024 lds
MM a[24:27],a[132:135],v[28:31],a[24:27]
RD a[204:207],v227 offset:0
RD a[208:211],v224 offset:4096
MM a[24:27],a[128:131],v[24:27],a[24:27]
RD a[212:215],v225 offset:4096
RD a[216:219],v226 offset:4096
MM a[28:31],a[136:139],v[24:27],a[28:31]
buffer_load_dwordx4 v230, s[8:11], s64 offen offset:2048 lds
MM a[28:31],a[140:143],v[28:31],a[28:31]
EX v100,v100
MM a[20:23],a[140:143],v[20:23],a[20:23]
EX v101,v101
MM a[20:23],a[136:139],v[16:19],a[20:23]
RD a[220:223],v227 offset:4096
CV v70,v100,v101
MM a[12:15],a[136:139],v[8:11],a[12:15]
EX v102,v102
MM a[12:15],a[140:143],v[12:15],a[12:15]
buffer_load_dwordx4 v231, s[8:11], s64 offen offset:3072 lds
MM a[4:7],a[140:143],v[4:7],a[4:7]
s_add_u32 m0, s65, 98304
EX v103,v103
MM a[4:7],a[136:139],v[0:3],a[4:7]
RD a[224:227],v224 offset:8192
CV v71,v102,v103
MM a[32:35],a[144:147],v[0:3],a[32:35]
EX v104,v104
MM a[32:35],a[148:151],v[4:7],a[32:35]
EX v105,v105
MM a[40:43],a[148:151],v[12:15],a[40:43]
RD a[228:231],v225 offset:8192
CV v76,v104,v105
MM a[40:43],a[144:147],v[8:11],a[40:43]
EX v106,v106
MM a[48:51],a[144:147],v[16:19],a[48:51]
buffer_load_dwordx4 v235, s[12:15], s81 offen offset:0 lds
MM a[48:51],a[148:151],v[20:23],a[48:51]
EX v107,v107
MM a[56:59],a[148:151],v[28:31],a[56:59]
RD a[232:235],v226 offset:8192
CV v77,v106,v107
MM a[56:59],a[144:147],v[24:27],a[56:59]
EX v108,v108
MM a[60:63],a[152:155],v[24:27],a[60:63]
EX v109,v109
MM a[60:63],a[156:159],v[28:31],a[60:63]
RD a[236:239],v227 offset:8192
CV v78,v108,v109
MM a[52:55],a[156:159],v[20:23],a[52:55]
EX v110,v110
MM a[52:55],a[152:155],v[16:19],a[52:55]
buffer_load_dwordx4 v235, s[12:15], s64 offen offset:1024 lds
MM a[44:47],a[152:155],v[8:11],a[44:47]
EX v111,v111
MM a[44:47],a[156:159],v[12:15],a[44:47]
RD a[240:243],v224 offset:12288
CV v79,v110,v111
MM a[36:39],a[156:159],v[4:7],a[36:39]
EX v112,v112
MM a[36:39],a[152:155],v[0:3],a[36:39]
EX v113,v113
MM a[64:67],a[160:163],v[0:3],a[64:67]
RD a[244:247],v225 offset:12288
CV v84,v112,v113
MM a[64:67],a[164:167],v[4:7],a[64:67]
EX v114,v114
MM a[72:75],a[164:167],v[12:15],a[72:75]
buffer_load_dwordx4 v235, s[12:15], s81 offen offset:2048 lds
MM a[72:75],a[160:163],v[8:11],a[72:75]
EX v115,v115
MM a[80:83],a[160:163],v[16:19],a[80:83]
RD a[248:251],v226 offset:12288
CV v85,v114,v115
MM a[80:83],a[164:167],v[20:23],a[80:83]
EX v116,v116
MM a[88:91],a[164:167],v[28:31],a[88:91]
EX v117,v117
MM a[88:91],a[160:163],v[24:27],a[88:91]
RD a[252:255],v227 offset:12288
CV v86,v116,v117
MM a[92:95],a[168:171],v[24:27],a[92:95]
EX v118,v118
MM a[92:95],a[172:175],v[28:31],a[92:95]
buffer_load_dwordx4 v235, s[12:15], s64 offen offset:3072 lds
MM a[84:87],a[172:175],v[20:23],a[84:87]
EX v119,v119
MM a[84:87],a[168:171],v[16:19],a[84:87]
CV v87,v118,v119
MM a[76:79],a[168:171],v[8:11],a[76:79]
EX v120,v120
MM a[76:79],a[172:175],v[12:15],a[76:79]
EX v121,v121
MM a[68:71],a[172:175],v[4:7],a[68:71]
EX v122,v122
MM a[68:71],a[168:171],v[0:3],a[68:71]
EX v123,v123
MM a[96:99],a[176:179],v[0:3],a[96:99]
EX v124,v124
MM a[96:99],a[180:183],v[4:7],a[96:99]
EX v125,v125
MM a[104:107],a[180:183],v[12:15],a[104:107]
EX v126,v126
MM a[104:107],a[176:179],v[8:11],a[104:107]
EX v127,v127
MM a[112:115],a[176:179],v[16:19],a[112:115]
CV v92,v120,v121
CV v93,v122,v123
MM a[112:115],a[180:183],v[20:23],a[112:115]
CV v94,v124,v125
CV v95,v126,v127
MM a[120:123],a[180:183],v[28:31],a[120:123]
MM a[120:123],a[176:179],v[24:27],a[120:123]
MM a[124:127],a[184:187],v[24:27],a[124:127]
v_add_f32 v244, v144, v145
v_add_f32 v245, v148, v149
MM a[124:127],a[188:191],v[28:31],a[124:127]
v_add_f32 v246, v152, v153
v_add_f32 v247, v156, v157
MM a[116:119],a[188:191],v[20:23],a[116:119]
MM a[116:119],a[184:187],v[16:19],a[116:119]
MM a[108:111],a[184:187],v[8:11],a[108:111]
MM a[108:111],a[188:191],v[12:15],a[108:111]
MM a[100:103],a[188:191],v[4:7],a[100:103]
MM a[100:103],a[184:187],v[0:3],a[100:103]
v_smfmac_f32_16x16x64_bf16 v[144:147], v[236:239], v[64:71], v234
v_smfmac_f32_16x16x64_bf16 v[148:151], v[236:239], v[72:79], v234
v_smfmac_f32_16x16x64_bf16 v[152:155], v[236:239], v[80:87], v234
v_smfmac_f32_16x16x64_bf16 v[156:159], v[236:239], v[88:95], v234
s_waitcnt lgkmcnt(0)
MM v[0:3],a[192:195],v[160:163],v[128:131]
s_add_u32 s64, s64, 16384
TR a[128:129],v232 offset:49152
s_add_u32 s81, s64, 896
MM v[0:3],a[196:199],v[176:179],v[0:3]
TR a[130:131],v232 offset:53248
v_max3_f32 v249, v144, v145, v148
MM v[0:3],a[200:203],v[192:195],v[0:3]
TR a[132:133],v232 offset:57344
v_max3_f32 v249, v249, v149, v152
MM v[0:3],a[204:207],v[208:211],v[0:3]
TR a[134:135],v232 offset:61440
v_max3_f32 v249, v249, v153, v156
MM v[4:7],a[220:223],v[208:211],v[128:131]
TR a[136:137],v233 offset:49152
v_max_f32 v249, v249, v157
MM v[4:7],a[216:219],v[192:195],v[4:7]
TR a[138:139],v233 offset:53248
v_cmp_lt_f32 vcc, s71, v249
MM v[4:7],a[212:215],v[176:179],v[4:7]
TR a[140:141],v233 offset:57344
TR a[142:143],v233 offset:61440
MM v[4:7],a[208:211],v[160:163],v[4:7]
s_cbranch_vccnz LSITE27_%=
LBACK27_%=:
MM v[12:15],a[208:211],v[164:167],v[132:135]
v_lshrrev_b32 v125, 8, v248
v_lshlrev_b32 v124, 10, v125
MM v[12:15],a[212:215],v[180:183],v[12:15]
v_bfe_u32 v126, v248, 6, 2
v_lshl_add_u32 v124, v126, 7, v124
MM v[12:15],a[216:219],v[196:199],v[12:15]
v_bfe_u32 v126, v248, 4, 2
v_lshl_add_u32 v124, v126, 3, v124
MM v[12:15],a[220:223],v[212:215],v[12:15]
s_lshl_b32 s91, %[swave], 5
EX v0,v0
MM v[8:11],a[204:207],v[212:215],v[132:135]
s_add_u32 s91, s91, %[slds]
EX v1,v1
MM v[8:11],a[200:203],v[196:199],v[8:11]
s_add_u32 s91, s91, 139264
v_add_u32 v124, s91, v124
v_and_b32 v126, 112, v248
MM v[8:11],a[196:199],v[180:183],v[8:11]
s_add_u32 s91, %[slds], 131072
v_lshl_add_u32 v126, v125, 7, v126
v_add_u32 v126, s91, v126
MM v[8:11],a[192:195],v[164:167],v[8:11]
v_bfe_u32 v127, v248, 4, 3
v_mul_u32_u24 v127, 2560, v127
MM v[16:19],a[192:195],v[168:171],v[136:139]
v_lshl_add_u32 v127, v125, 4, v127
CV v0,v0,v1
EX v2,v2
MM v[16:19],a[196:199],v[184:187],v[16:19]
s_lshl_b32 s90, %[swave], 6
EX v3,v3
MM v[16:19],a[200:203],v[200:203],v[16:19]
s_lshl_b32 s91, s47, 8
EX v4,v4
MM v[16:19],a[204:207],v[216:219],v[16:19]
s_add_u32 s90, s90, s91
TR a[144:145],v232 offset:49664
TR a[146:147],v232 offset:53760
MM v[20:23],a[220:223],v[216:219],v[136:139]
s_mul_i32 s91, s46, 17920
EX v5,v5
MM v[20:23],a[216:219],v[200:203],v[20:23]
s_add_u32 s90, s90, s91
TR a[148:149],v232 offset:57856
CV v1,v2,v3
MM v[20:23],a[212:215],v[184:187],v[20:23]
s_add_u32 s90, s90, 21659648
EX v6,v6
MM v[20:23],a[208:211],v[168:171],v[20:23]
EX v7,v7
MM v[28:31],a[208:211],v[172:175],v[140:143]
TR a[150:151],v232 offset:61952
TR a[152:153],v233 offset:49664
MM v[28:31],a[212:215],v[188:191],v[28:31]
EX v8,v8
MM v[28:31],a[216:219],v[204:207],v[28:31]
TR a[154:155],v233 offset:53760
CV v2,v4,v5
MM v[28:31],a[220:223],v[220:223],v[28:31]
EX v9,v9
MM v[24:27],a[204:207],v[220:223],v[140:143]
TR a[156:157],v233 offset:57856
CV v3,v6,v7
MM v[24:27],a[200:203],v[204:207],v[24:27]
EX v10,v10
MM v[24:27],a[196:199],v[188:191],v[24:27]
TR a[158:159],v233 offset:61952
CV v8,v8,v9
MM v[24:27],a[192:195],v[172:175],v[24:27]
EX v11,v11
MM v[32:35],a[224:227],v[160:163],v[128:131]
)ASM"
R"ASM(
TR a[160:161],v232 offset:50176
TR a[162:163],v232 offset:54272
MM v[32:35],a[228:231],v[176:179],v[32:35]
EX v12,v12
MM v[32:35],a[232:235],v[192:195],v[32:35]
EX v13,v13
MM v[32:35],a[236:239],v[208:211],v[32:35]
TR a[164:165],v232 offset:58368
CV v9,v10,v11
MM v[36:39],a[252:255],v[208:211],v[128:131]
EX v14,v14
MM v[36:39],a[248:251],v[192:195],v[36:39]
TR a[166:167],v232 offset:62464
CV v10,v12,v13
MM v[36:39],a[244:247],v[176:179],v[36:39]
EX v15,v15
MM v[36:39],a[240:243],v[160:163],v[36:39]
TR a[168:169],v233 offset:50176
TR a[170:171],v233 offset:54272
MM v[44:47],a[240:243],v[164:167],v[132:135]
EX v16,v16
MM v[44:47],a[244:247],v[180:183],v[44:47]
EX v17,v17
MM v[44:47],a[248:251],v[196:199],v[44:47]
TR a[172:173],v233 offset:58368
CV v11,v14,v15
MM v[44:47],a[252:255],v[212:215],v[44:47]
EX v18,v18
MM v[40:43],a[236:239],v[212:215],v[132:135]
TR a[174:175],v233 offset:62464
CV v16,v16,v17
MM v[40:43],a[232:235],v[196:199],v[40:43]
EX v19,v19
MM v[40:43],a[228:231],v[180:183],v[40:43]
TR a[176:177],v232 offset:50688
TR a[178:179],v232 offset:54784
MM v[40:43],a[224:227],v[164:167],v[40:43]
EX v20,v20
MM v[48:51],a[224:227],v[168:171],v[136:139]
EX v21,v21
MM v[48:51],a[228:231],v[184:187],v[48:51]
TR a[180:181],v232 offset:58880
CV v17,v18,v19
MM v[48:51],a[232:235],v[200:203],v[48:51]
EX v22,v22
MM v[48:51],a[236:239],v[216:219],v[48:51]
TR a[182:183],v232 offset:62976
CV v18,v20,v21
MM v[52:55],a[252:255],v[216:219],v[136:139]
EX v23,v23
MM v[52:55],a[248:251],v[200:203],v[52:55]
TR a[184:185],v233 offset:50688
TR a[186:187],v233 offset:54784
MM v[52:55],a[244:247],v[184:187],v[52:55]
EX v24,v24
MM v[52:55],a[240:243],v[168:171],v[52:55]
TR a[188:189],v233 offset:58880
CV v19,v22,v23
MM v[60:63],a[240:243],v[172:175],v[140:143]
EX v25,v25
MM v[60:63],a[244:247],v[188:191],v[60:63]
TR a[190:191],v233 offset:62976
CV v24,v24,v25
MM v[60:63],a[248:251],v[204:207],v[60:63]
EX v26,v26
MM v[60:63],a[252:255],v[220:223],v[60:63]
EX v27,v27
MM v[56:59],a[236:239],v[220:223],v[140:143]
EX v28,v28
MM v[56:59],a[232:235],v[204:207],v[56:59]
EX v29,v29
MM v[56:59],a[228:231],v[188:191],v[56:59]
EX v30,v30
MM v[56:59],a[224:227],v[172:175],v[56:59]
s_waitcnt vmcnt(8) lgkmcnt(0)
s_barrier
MM a[0:3],a[128:131],v[64:67],a[0:3]
s_add_u32 m0, s65, 49152
CV v25,v26,v27
CV v26,v28,v29
MM a[0:3],a[132:135],v[68:71],a[0:3]
RD a[192:195],v224 offset:16384
RD a[196:199],v225 offset:16384
MM a[8:11],a[132:135],v[76:79],a[8:11]
EX v31,v31
MM a[8:11],a[128:131],v[72:75],a[8:11]
RD a[200:203],v226 offset:16384
CV v27,v30,v31
MM a[16:19],a[128:131],v[80:83],a[16:19]
TR v[96:97],v124 offset:0
TR v[98:99],v124 offset:512
MM a[16:19],a[132:135],v[84:87],a[16:19]
RD v[108:111],v126 offset:0
TR v[100:101],v124 offset:4096
MM a[24:27],a[132:135],v[92:95],a[24:27]
TR v[102:103],v124 offset:4608
RD v[112:115],v126 offset:512
MM a[24:27],a[128:131],v[88:91],a[24:27]
TR v[104:105],v124 offset:8192
TR v[106:107],v124 offset:8704
MM a[28:31],a[136:139],v[88:91],a[28:31]
RD v[116:119],v126 offset:1024
RD a[204:207],v227 offset:16384
EX v32,v32
MM a[28:31],a[140:143],v[92:95],a[28:31]
EX v33,v33
MM a[20:23],a[140:143],v[84:87],a[20:23]
EX v34,v34
MM a[20:23],a[136:139],v[80:83],a[20:23]
buffer_load_dwordx4 v228, s[8:11], s64 offen offset:0 lds
LPG20_%=:
s_cbranch_execz LTR3_%=
MM a[12:15],a[136:139],v[72:75],a[12:15]
EX v35,v35
MM a[12:15],a[140:143],v[76:79],a[12:15]
EX v36,v36
MM a[4:7],a[140:143],v[68:71],a[4:7]
CV v4,v32,v33
CV v5,v34,v35
MM a[4:7],a[136:139],v[64:67],a[4:7]
EX v37,v37
MM a[32:35],a[144:147],v[64:67],a[32:35]
EX v38,v38
MM a[32:35],a[148:151],v[68:71],a[32:35]
buffer_load_dwordx4 v229, s[8:11], s64 offen offset:1024 lds
MM a[40:43],a[148:151],v[76:79],a[40:43]
EX v39,v39
MM a[40:43],a[144:147],v[72:75],a[40:43]
RD a[208:211],v224 offset:20480
CV v6,v36,v37
MM a[48:51],a[144:147],v[80:83],a[48:51]
s_waitcnt lgkmcnt(2)
EX v40,v40
MM a[48:51],a[148:151],v[84:87],a[48:51]
RD a[212:215],v225 offset:20480
CV v7,v38,v39
MM v[120:123],v[96:99],v[108:111],0
MM v[120:123],v[100:103],v[112:115],v[120:123]
MM v[120:123],v[104:107],v[116:119],v[120:123]
MM a[56:59],a[148:151],v[92:95],a[56:59]
TR v[96:97],v124 offset:12288
TR v[98:99],v124 offset:12800
MM a[56:59],a[144:147],v[88:91],a[56:59]
RD v[108:111],v126 offset:1536
TR v[100:101],v124 offset:16384
MM a[60:63],a[152:155],v[88:91],a[60:63]
TR v[102:103],v124 offset:16896
RD v[112:115],v126 offset:2048
EX v41,v41
MM a[60:63],a[156:159],v[92:95],a[60:63]
TR v[104:105],v124 offset:20480
TR v[106:107],v124 offset:20992
MM a[52:55],a[156:159],v[84:87],a[52:55]
RD v[116:119],v126 offset:2560
CV v12,v40,v41
EX v42,v42
MM a[52:55],a[152:155],v[80:83],a[52:55]
EX v43,v43
MM a[44:47],a[152:155],v[72:75],a[44:47]
buffer_load_dwordx4 v230, s[8:11], s64 offen offset:2048 lds
MM a[44:47],a[156:159],v[76:79],a[44:47]
EX v44,v44
MM a[36:39],a[156:159],v[68:71],a[36:39]
EX v45,v45
MM a[36:39],a[152:155],v[64:67],a[36:39]
RD a[216:219],v226 offset:20480
CV v13,v42,v43
MM a[64:67],a[160:163],v[64:67],a[64:67]
EX v46,v46
MM a[64:67],a[164:167],v[68:71],a[64:67]
EX v47,v47
MM a[72:75],a[164:167],v[76:79],a[72:75]
buffer_load_dwordx4 v231, s[8:11], s64 offen offset:3072 lds
MM a[72:75],a[160:163],v[72:75],a[72:75]
s_add_u32 m0, s65, 114688
RD a[220:223],v227 offset:20480
CV v14,v44,v45
MM a[80:83],a[160:163],v[80:83],a[80:83]
EX v48,v48
MM a[80:83],a[164:167],v[84:87],a[80:83]
EX v49,v49
MM a[88:91],a[164:167],v[92:95],a[88:91]
s_waitcnt lgkmcnt(2)
RD a[224:227],v224 offset:24576
CV v15,v46,v47
MM a[88:91],a[160:163],v[88:91],a[88:91]
EX v50,v50
MM v[120:123],v[96:99],v[108:111],v[120:123]
MM v[120:123],v[100:103],v[112:115],v[120:123]
MM v[120:123],v[104:107],v[116:119],v[120:123]
MM a[92:95],a[168:171],v[88:91],a[92:95]
EX v51,v51
MM a[92:95],a[172:175],v[92:95],a[92:95]
buffer_load_dwordx4 v235, s[12:15], s81 offen offset:0 lds
MM a[84:87],a[172:175],v[84:87],a[84:87]
RD a[228:231],v225 offset:24576
CV v20,v48,v49
MM a[84:87],a[168:171],v[80:83],a[84:87]
EX v52,v52
MM a[76:79],a[168:171],v[72:75],a[76:79]
EX v53,v53
MM a[76:79],a[172:175],v[76:79],a[76:79]
RD a[232:235],v226 offset:24576
CV v21,v50,v51
MM a[68:71],a[172:175],v[68:71],a[68:71]
s_mov_b32 exec_lo, 0x00ff00ff
s_mov_b32 exec_hi, 0x00ff00ff
buffer_store_dwordx4 v[120:123], v127, s[20:23], s90 offen offset:0 sc1
s_mov_b64 exec, -1
s_nop 0
MM a[68:71],a[168:171],v[64:67],a[68:71]
EX v54,v54
MM a[96:99],a[176:179],v[64:67],a[96:99]
EX v55,v55
MM a[96:99],a[180:183],v[68:71],a[96:99]
RD a[236:239],v227 offset:24576
CV v22,v52,v53
MM a[104:107],a[180:183],v[76:79],a[104:107]
EX v56,v56
MM a[104:107],a[176:179],v[72:75],a[104:107]
buffer_load_dwordx4 v235, s[12:15], s64 offen offset:1024 lds
MM a[112:115],a[176:179],v[80:83],a[112:115]
EX v57,v57
MM a[112:115],a[180:183],v[84:87],a[112:115]
RD a[240:243],v224 offset:28672
CV v23,v54,v55
MM a[120:123],a[180:183],v[92:95],a[120:123]
EX v58,v58
MM a[120:123],a[176:179],v[88:91],a[120:123]
EX v59,v59
MM a[124:127],a[184:187],v[88:91],a[124:127]
v_add_f32 v244, v144, v145
v_add_f32 v245, v148, v149
MM a[124:127],a[188:191],v[92:95],a[124:127]
v_add_f32 v246, v152, v153
v_add_f32 v247, v156, v157
MM a[116:119],a[188:191],v[84:87],a[116:119]
EX v60,v60
MM a[116:119],a[184:187],v[80:83],a[116:119]
RD a[244:247],v225 offset:28672
CV v28,v56,v57
MM a[108:111],a[184:187],v[72:75],a[108:111]
EX v61,v61
MM a[108:111],a[188:191],v[76:79],a[108:111]
buffer_load_dwordx4 v235, s[12:15], s81 offen offset:2048 lds
MM a[100:103],a[188:191],v[68:71],a[100:103]
EX v62,v62
MM a[100:103],a[184:187],v[64:67],a[100:103]
RD a[248:251],v226 offset:28672
CV v29,v58,v59
v_smfmac_f32_16x16x64_bf16 v[144:147], v[236:239], v[0:7], v234
RD a[252:255],v227 offset:28672
EX v63,v63
v_smfmac_f32_16x16x64_bf16 v[148:151], v[236:239], v[8:15], v234
CV v30,v60,v61
v_smfmac_f32_16x16x64_bf16 v[152:155], v[236:239], v[16:23], v234
CV v31,v62,v63
s_nop 1
v_smfmac_f32_16x16x64_bf16 v[156:159], v[236:239], v[24:31], v234
s_waitcnt lgkmcnt(0)
buffer_load_dwordx4 v235, s[12:15], s64 offen offset:3072 lds
MM v[64:67],a[192:195],v[160:163],v[128:131]
s_add_u32 s64, s64, 16384
TR a[128:129],v232 offset:0
s_add_u32 s81, s64, 896
MM v[64:67],a[196:199],v[176:179],v[64:67]
TR a[130:131],v232 offset:4096
v_max3_f32 v249, v144, v145, v148
MM v[64:67],a[200:203],v[192:195],v[64:67]
TR a[132:133],v232 offset:8192
v_max3_f32 v249, v249, v149, v152
MM v[64:67],a[204:207],v[208:211],v[64:67]
TR a[134:135],v232 offset:12288
v_max3_f32 v249, v249, v153, v156
MM v[68:71],a[220:223],v[208:211],v[128:131]
TR a[136:137],v233 offset:0
v_max_f32 v249, v249, v157
MM v[68:71],a[216:219],v[192:195],v[68:71]
TR a[138:139],v233 offset:4096
v_cmp_lt_f32 vcc, s71, v249
MM v[68:71],a[212:215],v[176:179],v[68:71]
TR a[140:141],v233 offset:8192
TR a[142:143],v233 offset:12288
MM v[68:71],a[208:211],v[160:163],v[68:71]
s_cbranch_vccnz LSITE28_%=
LBACK28_%=:
MM v[76:79],a[208:211],v[164:167],v[132:135]
EX v64,v64
MM v[76:79],a[212:215],v[180:183],v[76:79]
EX v65,v65
MM v[76:79],a[216:219],v[196:199],v[76:79]
TR a[144:145],v232 offset:512
CV v64,v64,v65
MM v[76:79],a[220:223],v[212:215],v[76:79]
EX v66,v66
MM v[72:75],a[204:207],v[212:215],v[132:135]
EX v67,v67
MM v[72:75],a[200:203],v[196:199],v[72:75]
TR a[146:147],v232 offset:4608
CV v65,v66,v67
MM v[72:75],a[196:199],v[180:183],v[72:75]
EX v68,v68
MM v[72:75],a[192:195],v[164:167],v[72:75]
TR a[148:149],v232 offset:8704
TR a[150:151],v232 offset:12800
MM v[80:83],a[192:195],v[168:171],v[136:139]
EX v69,v69
MM v[80:83],a[196:199],v[184:187],v[80:83]
TR a[152:153],v233 offset:512
CV v66,v68,v69
MM v[80:83],a[200:203],v[200:203],v[80:83]
EX v70,v70
MM v[80:83],a[204:207],v[216:219],v[80:83]
TR a[154:155],v233 offset:4608
TR a[156:157],v233 offset:8704
MM v[84:87],a[220:223],v[216:219],v[136:139]
v_lshlrev_b32 v32, 3, v248
v_min_u32 v32, 4992, v32
MM v[84:87],a[216:219],v[200:203],v[84:87]
s_sub_u32 s91, 5242880, s26
s_sub_u32 s60, s24, s91
s_subb_u32 s61, s25, 0
MM v[84:87],a[212:215],v[184:187],v[84:87]
s_mov_b32 s62, 5242880
EX v71,v71
MM v[84:87],a[208:211],v[168:171],v[84:87]
s_mov_b32 s63, 131072
TR a[158:159],v233 offset:12800
CV v67,v70,v71
MM v[92:95],a[208:211],v[172:175],v[140:143]
s_lshl_b32 s90, s16, 2
EX v72,v72
MM v[92:95],a[212:215],v[188:191],v[92:95]
s_add_u32 s90, s90, %[swave]
EX v73,v73
MM v[92:95],a[216:219],v[204:207],v[92:95]
s_mul_i32 s90, s90, 10240
TR a[160:161],v232 offset:1024
CV v72,v72,v73
MM v[92:95],a[220:223],v[220:223],v[92:95]
s_add_u32 s90, s90, 5120
EX v74,v74
MM v[88:91],a[204:207],v[220:223],v[140:143]
s_add_u32 s91, %[slds], 135168
s_mov_b32 m0, s91
s_nop 0
buffer_load_dword v32, s[60:63], s90 offen offset:0 lds
MM v[88:91],a[200:203],v[204:207],v[88:91]
EX v75,v75
MM v[88:91],a[196:199],v[188:191],v[88:91]
TR a[162:163],v232 offset:5120
CV v73,v74,v75
MM v[88:91],a[192:195],v[172:175],v[88:91]
EX v76,v76
MM v[96:99],a[224:227],v[160:163],v[128:131]
TR a[164:165],v232 offset:9216
TR a[166:167],v232 offset:13312
MM v[96:99],a[228:231],v[176:179],v[96:99]
EX v77,v77
MM v[96:99],a[232:235],v[192:195],v[96:99]
TR a[168:169],v233 offset:1024
CV v74,v76,v77
MM v[96:99],a[236:239],v[208:211],v[96:99]
EX v78,v78
MM v[100:103],a[252:255],v[208:211],v[128:131]
EX v79,v79
MM v[100:103],a[248:251],v[192:195],v[100:103]
TR a[170:171],v233 offset:5120
CV v75,v78,v79
MM v[100:103],a[244:247],v[176:179],v[100:103]
EX v80,v80
MM v[100:103],a[240:243],v[160:163],v[100:103]
TR a[172:173],v233 offset:9216
TR a[174:175],v233 offset:13312
MM v[108:111],a[240:243],v[164:167],v[132:135]
s_add_u32 s67, s95, 5120
EX v81,v81
MM v[108:111],a[244:247],v[180:183],v[108:111]
s_mov_b32 s70, 1
TR a[176:177],v232 offset:1536
CV v80,v80,v81
MM v[108:111],a[248:251],v[196:199],v[108:111]
EX v82,v82
MM v[108:111],a[252:255],v[212:215],v[108:111]
EX v83,v83
MM v[104:107],a[236:239],v[212:215],v[132:135]
TR a[178:179],v232 offset:5632
CV v81,v82,v83
MM v[104:107],a[232:235],v[196:199],v[104:107]
EX v84,v84
MM v[104:107],a[228:231],v[180:183],v[104:107]
TR a[180:181],v232 offset:9728
)ASM"
R"ASM(
TR a[182:183],v232 offset:13824
MM v[104:107],a[224:227],v[164:167],v[104:107]
EX v85,v85
MM v[112:115],a[224:227],v[168:171],v[136:139]
TR a[184:185],v233 offset:1536
CV v82,v84,v85
MM v[112:115],a[228:231],v[184:187],v[112:115]
EX v86,v86
MM v[112:115],a[232:235],v[200:203],v[112:115]
EX v87,v87
MM v[112:115],a[236:239],v[216:219],v[112:115]
TR a[186:187],v233 offset:5632
CV v83,v86,v87
MM v[116:119],a[252:255],v[216:219],v[136:139]
EX v88,v88
MM v[116:119],a[248:251],v[200:203],v[116:119]
TR a[188:189],v233 offset:9728
TR a[190:191],v233 offset:13824
MM v[116:119],a[244:247],v[184:187],v[116:119]
EX v89,v89
MM v[116:119],a[240:243],v[168:171],v[116:119]
EX v90,v90
MM v[124:127],a[240:243],v[172:175],v[140:143]
EX v91,v91
MM v[124:127],a[244:247],v[188:191],v[124:127]
EX v92,v92
MM v[124:127],a[248:251],v[204:207],v[124:127]
EX v93,v93
MM v[124:127],a[252:255],v[220:223],v[124:127]
EX v94,v94
MM v[120:123],a[236:239],v[220:223],v[140:143]
EX v95,v95
MM v[120:123],a[232:235],v[204:207],v[120:123]
EX v96,v96
MM v[120:123],a[228:231],v[188:191],v[120:123]
EX v97,v97
MM v[120:123],a[224:227],v[172:175],v[120:123]
s_waitcnt vmcnt(14) lgkmcnt(0)
s_barrier
MM a[0:3],a[128:131],v[0:3],a[0:3]
s_add_u32 m0, s65, 0
CV v88,v88,v89
CV v89,v90,v91
MM a[0:3],a[132:135],v[4:7],a[0:3]
RD a[192:195],v224 offset:32768
CV v90,v92,v93
MM a[8:11],a[132:135],v[12:15],a[8:11]
RD a[196:199],v225 offset:32768
CV v91,v94,v95
MM a[8:11],a[128:131],v[8:11],a[8:11]
buffer_load_dwordx4 v228, s[8:11], s64 offen offset:0 lds
MM a[16:19],a[128:131],v[16:19],a[16:19]
RD a[200:203],v226 offset:32768
CV v68,v96,v97
MM a[16:19],a[132:135],v[20:23],a[16:19]
buffer_load_dwordx4 v229, s[8:11], s64 offen offset:1024 lds
MM a[24:27],a[132:135],v[28:31],a[24:27]
EX v98,v98
MM a[24:27],a[128:131],v[24:27],a[24:27]
RD a[204:207],v227 offset:32768
RD a[208:211],v224 offset:36864
MM a[28:31],a[136:139],v[24:27],a[28:31]
EX v99,v99
MM a[28:31],a[140:143],v[28:31],a[28:31]
RD a[212:215],v225 offset:36864
CV v69,v98,v99
MM a[20:23],a[140:143],v[20:23],a[20:23]
EX v100,v100
MM a[20:23],a[136:139],v[16:19],a[20:23]
EX v101,v101
MM a[12:15],a[136:139],v[8:11],a[12:15]
RD a[216:219],v226 offset:36864
CV v70,v100,v101
MM a[12:15],a[140:143],v[12:15],a[12:15]
buffer_load_dwordx4 v230, s[8:11], s64 offen offset:2048 lds
MM a[4:7],a[140:143],v[4:7],a[4:7]
EX v102,v102
MM a[4:7],a[136:139],v[0:3],a[4:7]
EX v103,v103
MM a[32:35],a[144:147],v[0:3],a[32:35]
RD a[220:223],v227 offset:36864
CV v71,v102,v103
MM a[32:35],a[148:151],v[4:7],a[32:35]
EX v104,v104
MM a[40:43],a[148:151],v[12:15],a[40:43]
EX v105,v105
MM a[40:43],a[144:147],v[8:11],a[40:43]
RD a[224:227],v224 offset:40960
CV v76,v104,v105
MM a[48:51],a[144:147],v[16:19],a[48:51]
buffer_load_dwordx4 v231, s[8:11], s64 offen offset:3072 lds
MM a[48:51],a[148:151],v[20:23],a[48:51]
s_add_u32 m0, s65, 65536
EX v106,v106
MM a[56:59],a[148:151],v[28:31],a[56:59]
EX v107,v107
MM a[56:59],a[144:147],v[24:27],a[56:59]
RD a[228:231],v225 offset:40960
CV v77,v106,v107
MM a[60:63],a[152:155],v[24:27],a[60:63]
EX v108,v108
MM a[60:63],a[156:159],v[28:31],a[60:63]
buffer_load_dwordx4 v235, s[12:15], s81 offen offset:0 lds
MM a[52:55],a[156:159],v[20:23],a[52:55]
EX v109,v109
MM a[52:55],a[152:155],v[16:19],a[52:55]
RD a[232:235],v226 offset:40960
CV v78,v108,v109
MM a[44:47],a[152:155],v[8:11],a[44:47]
EX v110,v110
MM a[44:47],a[156:159],v[12:15],a[44:47]
EX v111,v111
MM a[36:39],a[156:159],v[4:7],a[36:39]
RD a[236:239],v227 offset:40960
CV v79,v110,v111
MM a[36:39],a[152:155],v[0:3],a[36:39]
EX v112,v112
MM a[64:67],a[160:163],v[0:3],a[64:67]
buffer_load_dwordx4 v235, s[12:15], s64 offen offset:1024 lds
MM a[64:67],a[164:167],v[4:7],a[64:67]
EX v113,v113
MM a[72:75],a[164:167],v[12:15],a[72:75]
RD a[240:243],v224 offset:45056
CV v84,v112,v113
MM a[72:75],a[160:163],v[8:11],a[72:75]
EX v114,v114
MM a[80:83],a[160:163],v[16:19],a[80:83]
EX v115,v115
MM a[80:83],a[164:167],v[20:23],a[80:83]
RD a[244:247],v225 offset:45056
CV v85,v114,v115
MM a[88:91],a[164:167],v[28:31],a[88:91]
EX v116,v116
MM a[88:91],a[160:163],v[24:27],a[88:91]
buffer_load_dwordx4 v235, s[12:15], s81 offen offset:2048 lds
MM a[92:95],a[168:171],v[24:27],a[92:95]
EX v117,v117
MM a[92:95],a[172:175],v[28:31],a[92:95]
RD a[248:251],v226 offset:45056
CV v86,v116,v117
MM a[84:87],a[172:175],v[20:23],a[84:87]
EX v118,v118
MM a[84:87],a[168:171],v[16:19],a[84:87]
EX v119,v119
MM a[76:79],a[168:171],v[8:11],a[76:79]
RD a[252:255],v227 offset:45056
CV v87,v118,v119
MM a[76:79],a[172:175],v[12:15],a[76:79]
EX v120,v120
MM a[68:71],a[172:175],v[4:7],a[68:71]
buffer_load_dwordx4 v235, s[12:15], s64 offen offset:3072 lds
MM a[68:71],a[168:171],v[0:3],a[68:71]
EX v121,v121
MM a[96:99],a[176:179],v[0:3],a[96:99]
EX v122,v122
MM a[96:99],a[180:183],v[4:7],a[96:99]
EX v123,v123
MM a[104:107],a[180:183],v[12:15],a[104:107]
EX v124,v124
MM a[104:107],a[176:179],v[8:11],a[104:107]
EX v125,v125
MM a[112:115],a[176:179],v[16:19],a[112:115]
EX v126,v126
MM a[112:115],a[180:183],v[20:23],a[112:115]
EX v127,v127
MM a[120:123],a[180:183],v[28:31],a[120:123]
s_lshl_b32 s91, s47, 2
CV v92,v120,v121
CV v93,v122,v123
MM a[120:123],a[176:179],v[24:27],a[120:123]
s_add_u32 s91, s91, %[swave]
CV v94,v124,v125
CV v95,v126,v127
MM a[124:127],a[184:187],v[24:27],a[124:127]
s_lshl_b32 s91, s91, 2
v_add_f32 v244, v144, v145
v_add_f32 v245, v148, v149
MM a[124:127],a[188:191],v[28:31],a[124:127]
s_add_u32 s91, s91, 73728
v_add_f32 v246, v152, v153
v_add_f32 v247, v156, v157
MM a[116:119],a[188:191],v[20:23],a[116:119]
s_waitcnt vmcnt(12)
v_mov_b32 v250, s69
s_mov_b64 exec, 1
buffer_store_dword v250, off, s[20:23], s91 offset:0 sc1
s_mov_b64 exec, -1
MM a[116:119],a[184:187],v[16:19],a[116:119]
MM a[108:111],a[184:187],v[8:11],a[108:111]
MM a[108:111],a[188:191],v[12:15],a[108:111]
MM a[100:103],a[188:191],v[4:7],a[100:103]
MM a[100:103],a[184:187],v[0:3],a[100:103]
v_smfmac_f32_16x16x64_bf16 v[144:147], v[236:239], v[64:71], v234
v_smfmac_f32_16x16x64_bf16 v[148:151], v[236:239], v[72:79], v234
v_smfmac_f32_16x16x64_bf16 v[152:155], v[236:239], v[80:87], v234
v_smfmac_f32_16x16x64_bf16 v[156:159], v[236:239], v[88:95], v234
s_waitcnt lgkmcnt(0)
s_cmp_ge_u32 s16, 20
s_cbranch_scc1 LLAP_%=
MM v[0:3],a[192:195],v[160:163],v[128:131]
s_add_u32 s64, s64, 16384
TR a[128:129],v232 offset:16384
s_add_u32 s81, s64, 896
MM v[0:3],a[196:199],v[176:179],v[0:3]
TR a[130:131],v232 offset:20480
v_max3_f32 v249, v144, v145, v148
MM v[0:3],a[200:203],v[192:195],v[0:3]
TR a[132:133],v232 offset:24576
v_max3_f32 v249, v249, v149, v152
MM v[0:3],a[204:207],v[208:211],v[0:3]
TR a[134:135],v232 offset:28672
v_max3_f32 v249, v249, v153, v156
MM v[4:7],a[220:223],v[208:211],v[128:131]
TR a[136:137],v233 offset:16384
v_max_f32 v249, v249, v157
MM v[4:7],a[216:219],v[192:195],v[4:7]
TR a[138:139],v233 offset:20480
v_cmp_lt_f32 vcc, s71, v249
MM v[4:7],a[212:215],v[176:179],v[4:7]
TR a[140:141],v233 offset:24576
TR a[142:143],v233 offset:28672
MM v[4:7],a[208:211],v[160:163],v[4:7]
s_cbranch_vccnz LSITE29_%=
LBACK29_%=:
MM v[12:15],a[208:211],v[164:167],v[132:135]
EX v0,v0
MM v[12:15],a[212:215],v[180:183],v[12:15]
EX v1,v1
MM v[12:15],a[216:219],v[196:199],v[12:15]
TR a[144:145],v232 offset:16896
CV v0,v0,v1
MM v[12:15],a[220:223],v[212:215],v[12:15]
EX v2,v2
MM v[8:11],a[204:207],v[212:215],v[132:135]
EX v3,v3
MM v[8:11],a[200:203],v[196:199],v[8:11]
TR a[146:147],v232 offset:20992
CV v1,v2,v3
MM v[8:11],a[196:199],v[180:183],v[8:11]
EX v4,v4
MM v[8:11],a[192:195],v[164:167],v[8:11]
TR a[148:149],v232 offset:25088
TR a[150:151],v232 offset:29184
MM v[16:19],a[192:195],v[168:171],v[136:139]
EX v5,v5
MM v[16:19],a[196:199],v[184:187],v[16:19]
TR a[152:153],v233 offset:16896
CV v2,v4,v5
MM v[16:19],a[200:203],v[200:203],v[16:19]
EX v6,v6
MM v[16:19],a[204:207],v[216:219],v[16:19]
TR a[154:155],v233 offset:20992
TR a[156:157],v233 offset:25088
MM v[20:23],a[220:223],v[216:219],v[136:139]
s_add_u32 s87, s95, 5120
buffer_store_dword v240, v248, s[20:23], s87 offen offset:12 sc1
MM v[20:23],a[216:219],v[200:203],v[20:23]
EX v7,v7
MM v[20:23],a[212:215],v[184:187],v[20:23]
TR a[158:159],v233 offset:29184
CV v3,v6,v7
MM v[20:23],a[208:211],v[168:171],v[20:23]
EX v8,v8
MM v[28:31],a[208:211],v[172:175],v[140:143]
EX v9,v9
MM v[28:31],a[212:215],v[188:191],v[28:31]
TR a[160:161],v232 offset:17408
CV v8,v8,v9
MM v[28:31],a[216:219],v[204:207],v[28:31]
EX v10,v10
MM v[28:31],a[220:223],v[220:223],v[28:31]
EX v11,v11
MM v[24:27],a[204:207],v[220:223],v[140:143]
TR a[162:163],v232 offset:21504
CV v9,v10,v11
MM v[24:27],a[200:203],v[204:207],v[24:27]
TR a[164:165],v232 offset:25600
TR a[166:167],v232 offset:29696
MM v[24:27],a[196:199],v[188:191],v[24:27]
EX v12,v12
MM v[24:27],a[192:195],v[172:175],v[24:27]
EX v13,v13
MM v[32:35],a[224:227],v[160:163],v[128:131]
TR a[168:169],v233 offset:17408
CV v10,v12,v13
MM v[32:35],a[228:231],v[176:179],v[32:35]
EX v14,v14
MM v[32:35],a[232:235],v[192:195],v[32:35]
TR a[170:171],v233 offset:21504
TR a[172:173],v233 offset:25600
MM v[32:35],a[236:239],v[208:211],v[32:35]
EX v15,v15
MM v[36:39],a[252:255],v[208:211],v[128:131]
TR a[174:175],v233 offset:29696
CV v11,v14,v15
MM v[36:39],a[248:251],v[192:195],v[36:39]
EX v16,v16
MM v[36:39],a[244:247],v[176:179],v[36:39]
EX v17,v17
MM v[36:39],a[240:243],v[160:163],v[36:39]
TR a[176:177],v232 offset:17920
CV v16,v16,v17
MM v[44:47],a[240:243],v[164:167],v[132:135]
EX v18,v18
MM v[44:47],a[244:247],v[180:183],v[44:47]
TR a[178:179],v232 offset:22016
TR a[180:181],v232 offset:26112
MM v[44:47],a[248:251],v[196:199],v[44:47]
EX v19,v19
MM v[44:47],a[252:255],v[212:215],v[44:47]
s_nop 0
LPG21_%=:
s_cbranch_execz LTR4_%=
TR a[182:183],v232 offset:30208
CV v17,v18,v19
MM v[40:43],a[236:239],v[212:215],v[132:135]
EX v20,v20
MM v[40:43],a[232:235],v[196:199],v[40:43]
EX v21,v21
MM v[40:43],a[228:231],v[180:183],v[40:43]
TR a[184:185],v233 offset:17920
CV v18,v20,v21
MM v[40:43],a[224:227],v[164:167],v[40:43]
EX v22,v22
MM v[48:51],a[224:227],v[168:171],v[136:139]
TR a[186:187],v233 offset:22016
TR a[188:189],v233 offset:26112
MM v[48:51],a[228:231],v[184:187],v[48:51]
EX v23,v23
MM v[48:51],a[232:235],v[200:203],v[48:51]
TR a[190:191],v233 offset:30208
CV v19,v22,v23
MM v[48:51],a[236:239],v[216:219],v[48:51]
EX v24,v24
MM v[52:55],a[252:255],v[216:219],v[136:139]
EX v25,v25
MM v[52:55],a[248:251],v[200:203],v[52:55]
EX v26,v26
MM v[52:55],a[244:247],v[184:187],v[52:55]
EX v27,v27
MM v[52:55],a[240:243],v[168:171],v[52:55]
EX v28,v28
MM v[60:63],a[240:243],v[172:175],v[140:143]
EX v29,v29
MM v[60:63],a[244:247],v[188:191],v[60:63]
EX v30,v30
MM v[60:63],a[248:251],v[204:207],v[60:63]
EX v31,v31
MM v[60:63],a[252:255],v[220:223],v[60:63]
EX v32,v32
MM v[56:59],a[236:239],v[220:223],v[140:143]
EX v33,v33
MM v[56:59],a[232:235],v[204:207],v[56:59]
EX v34,v34
MM v[56:59],a[228:231],v[188:191],v[56:59]
EX v35,v35
MM v[56:59],a[224:227],v[172:175],v[56:59]
s_waitcnt lgkmcnt(0)
s_barrier
MM a[0:3],a[128:131],v[64:67],a[0:3]
s_add_u32 m0, s65, 16384
CV v24,v24,v25
CV v25,v26,v27
MM a[0:3],a[132:135],v[68:71],a[0:3]
RD a[192:195],v224 offset:49152
CV v26,v28,v29
MM a[8:11],a[132:135],v[76:79],a[8:11]
RD a[196:199],v225 offset:49152
CV v27,v30,v31
MM a[8:11],a[128:131],v[72:75],a[8:11]
buffer_load_dwordx4 v228, s[8:11], s64 offen offset:0 lds
MM a[16:19],a[128:131],v[80:83],a[16:19]
RD a[200:203],v226 offset:49152
CV v4,v32,v33
MM a[16:19],a[132:135],v[84:87],a[16:19]
RD a[204:207],v227 offset:49152
CV v5,v34,v35
MM a[24:27],a[132:135],v[92:95],a[24:27]
buffer_load_dwordx4 v229, s[8:11], s64 offen offset:1024 lds
MM a[24:27],a[128:131],v[88:91],a[24:27]
RD a[208:211],v224 offset:53248
RD a[212:215],v225 offset:53248
MM a[28:31],a[136:139],v[88:91],a[28:31]
buffer_load_dwordx4 v230, s[8:11], s64 offen offset:2048 lds
MM a[28:31],a[140:143],v[92:95],a[28:31]
EX v36,v36
MM a[20:23],a[140:143],v[84:87],a[20:23]
EX v37,v37
MM a[20:23],a[136:139],v[80:83],a[20:23]
RD a[216:219],v226 offset:53248
CV v6,v36,v37
MM a[12:15],a[136:139],v[72:75],a[12:15]
)ASM"
R"ASM(
EX v38,v38
MM a[12:15],a[140:143],v[76:79],a[12:15]
RD a[220:223],v227 offset:53248
RD a[224:227],v224 offset:57344
MM a[4:7],a[140:143],v[68:71],a[4:7]
EX v39,v39
MM a[4:7],a[136:139],v[64:67],a[4:7]
RD a[228:231],v225 offset:57344
CV v7,v38,v39
MM a[32:35],a[144:147],v[64:67],a[32:35]
EX v40,v40
MM a[32:35],a[148:151],v[68:71],a[32:35]
buffer_load_dwordx4 v231, s[8:11], s64 offen offset:3072 lds
MM a[40:43],a[148:151],v[76:79],a[40:43]
s_add_u32 m0, s65, 81920
EX v41,v41
MM a[40:43],a[144:147],v[72:75],a[40:43]
RD a[232:235],v226 offset:57344
CV v12,v40,v41
MM a[48:51],a[144:147],v[80:83],a[48:51]
EX v42,v42
MM a[48:51],a[148:151],v[84:87],a[48:51]
EX v43,v43
MM a[56:59],a[148:151],v[92:95],a[56:59]
RD a[236:239],v227 offset:57344
CV v13,v42,v43
MM a[56:59],a[144:147],v[88:91],a[56:59]
EX v44,v44
MM a[60:63],a[152:155],v[88:91],a[60:63]
buffer_load_dwordx4 v235, s[12:15], s81 offen offset:0 lds
MM a[60:63],a[156:159],v[92:95],a[60:63]
EX v45,v45
MM a[52:55],a[156:159],v[84:87],a[52:55]
RD a[240:243],v224 offset:61440
CV v14,v44,v45
MM a[52:55],a[152:155],v[80:83],a[52:55]
EX v46,v46
MM a[44:47],a[152:155],v[72:75],a[44:47]
EX v47,v47
MM a[44:47],a[156:159],v[76:79],a[44:47]
RD a[244:247],v225 offset:61440
CV v15,v46,v47
MM a[36:39],a[156:159],v[68:71],a[36:39]
buffer_load_dwordx4 v235, s[12:15], s64 offen offset:1024 lds
MM a[36:39],a[152:155],v[64:67],a[36:39]
EX v48,v48
MM a[64:67],a[160:163],v[64:67],a[64:67]
EX v49,v49
MM a[64:67],a[164:167],v[68:71],a[64:67]
RD a[248:251],v226 offset:61440
CV v20,v48,v49
MM a[72:75],a[164:167],v[76:79],a[72:75]
EX v50,v50
MM a[72:75],a[160:163],v[72:75],a[72:75]
EX v51,v51
MM a[80:83],a[160:163],v[80:83],a[80:83]
RD a[252:255],v227 offset:61440
CV v21,v50,v51
MM a[80:83],a[164:167],v[84:87],a[80:83]
buffer_load_dwordx4 v235, s[12:15], s81 offen offset:2048 lds
MM a[88:91],a[164:167],v[92:95],a[88:91]
EX v52,v52
MM a[88:91],a[160:163],v[88:91],a[88:91]
EX v53,v53
MM a[92:95],a[168:171],v[88:91],a[92:95]
EX v54,v54
MM a[92:95],a[172:175],v[92:95],a[92:95]
buffer_load_dwordx4 v235, s[12:15], s64 offen offset:3072 lds
MM a[84:87],a[172:175],v[84:87],a[84:87]
EX v55,v55
MM a[84:87],a[168:171],v[80:83],a[84:87]
EX v56,v56
MM a[76:79],a[168:171],v[72:75],a[76:79]
EX v57,v57
MM a[76:79],a[172:175],v[76:79],a[76:79]
EX v58,v58
MM a[68:71],a[172:175],v[68:71],a[68:71]
EX v59,v59
MM a[68:71],a[168:171],v[64:67],a[68:71]
EX v60,v60
MM a[96:99],a[176:179],v[64:67],a[96:99]
EX v61,v61
MM a[96:99],a[180:183],v[68:71],a[96:99]
EX v62,v62
MM a[104:107],a[180:183],v[76:79],a[104:107]
EX v63,v63
MM a[104:107],a[176:179],v[72:75],a[104:107]
CV v22,v52,v53
CV v23,v54,v55
MM a[112:115],a[176:179],v[80:83],a[112:115]
CV v28,v56,v57
CV v29,v58,v59
MM a[112:115],a[180:183],v[84:87],a[112:115]
CV v30,v60,v61
CV v31,v62,v63
MM a[120:123],a[180:183],v[92:95],a[120:123]
MM a[120:123],a[176:179],v[88:91],a[120:123]
MM a[124:127],a[184:187],v[88:91],a[124:127]
v_add_f32 v244, v144, v145
v_add_f32 v245, v148, v149
MM a[124:127],a[188:191],v[92:95],a[124:127]
v_add_f32 v246, v152, v153
v_add_f32 v247, v156, v157
MM a[116:119],a[188:191],v[84:87],a[116:119]
MM a[116:119],a[184:187],v[80:83],a[116:119]
MM a[108:111],a[184:187],v[72:75],a[108:111]
MM a[108:111],a[188:191],v[76:79],a[108:111]
MM a[100:103],a[188:191],v[68:71],a[100:103]
MM a[100:103],a[184:187],v[64:67],a[100:103]
v_smfmac_f32_16x16x64_bf16 v[144:147], v[236:239], v[0:7], v234
v_smfmac_f32_16x16x64_bf16 v[148:151], v[236:239], v[8:15], v234
v_smfmac_f32_16x16x64_bf16 v[152:155], v[236:239], v[16:23], v234
v_smfmac_f32_16x16x64_bf16 v[156:159], v[236:239], v[24:31], v234
s_waitcnt lgkmcnt(0)
MM v[64:67],a[192:195],v[160:163],v[128:131]
s_add_u32 s64, s64, 16384
TR a[128:129],v232 offset:32768
s_add_u32 s81, s64, 896
MM v[64:67],a[196:199],v[176:179],v[64:67]
TR a[130:131],v232 offset:36864
v_max3_f32 v249, v144, v145, v148
MM v[64:67],a[200:203],v[192:195],v[64:67]
TR a[132:133],v232 offset:40960
v_max3_f32 v249, v249, v149, v152
MM v[64:67],a[204:207],v[208:211],v[64:67]
TR a[134:135],v232 offset:45056
v_max3_f32 v249, v249, v153, v156
MM v[68:71],a[220:223],v[208:211],v[128:131]
TR a[136:137],v233 offset:32768
v_max_f32 v249, v249, v157
MM v[68:71],a[216:219],v[192:195],v[68:71]
TR a[138:139],v233 offset:36864
v_cmp_lt_f32 vcc, s71, v249
MM v[68:71],a[212:215],v[176:179],v[68:71]
TR a[140:141],v233 offset:40960
TR a[142:143],v233 offset:45056
MM v[68:71],a[208:211],v[160:163],v[68:71]
s_cbranch_vccnz LSITE30_%=
LBACK30_%=:
MM v[76:79],a[208:211],v[164:167],v[132:135]
s_and_b32 s90, s17, 63
s_cmp_lt_u32 s17, 80
s_cselect_b32 s17, s17, s90
s_cselect_b64 s[44:45], -1, 0
MM v[76:79],a[212:215],v[180:183],v[76:79]
v_lshrrev_b32 v253, 4, v248
v_min_u32 v253, 35, v253
MM v[76:79],a[216:219],v[196:199],v[76:79]
v_lshrrev_b32 v252, 2, v253
v_mul_u32_u24 v252, 144, v252
MM v[76:79],a[220:223],v[212:215],v[76:79]
TR a[144:145],v232 offset:33280
v_lshl_add_u32 v253, v253, 2, v252
MM v[72:75],a[204:207],v[212:215],v[132:135]
s_lshr_b32 s90, s17, 3
EX v64,v64
MM v[72:75],a[200:203],v[196:199],v[72:75]
s_lshl_b32 s76, s90, 4
EX v65,v65
MM v[72:75],a[196:199],v[180:183],v[72:75]
s_add_u32 s76, s76, 73728
buffer_load_dword v252, v253, s[20:23], s76 offen offset:0 sc1
MM v[72:75],a[192:195],v[164:167],v[72:75]
EX v66,v66
MM v[80:83],a[192:195],v[168:171],v[136:139]
EX v67,v67
MM v[80:83],a[196:199],v[184:187],v[80:83]
EX v68,v68
MM v[80:83],a[200:203],v[200:203],v[80:83]
TR a[146:147],v232 offset:37376
CV v64,v64,v65
MM v[80:83],a[204:207],v[216:219],v[80:83]
EX v69,v69
MM v[84:87],a[220:223],v[216:219],v[136:139]
TR a[148:149],v232 offset:41472
CV v65,v66,v67
MM v[84:87],a[216:219],v[200:203],v[84:87]
EX v70,v70
MM v[84:87],a[212:215],v[184:187],v[84:87]
TR a[150:151],v232 offset:45568
CV v66,v68,v69
MM v[84:87],a[208:211],v[168:171],v[84:87]
EX v71,v71
MM v[92:95],a[208:211],v[172:175],v[140:143]
TR a[152:153],v233 offset:33280
TR a[154:155],v233 offset:37376
MM v[92:95],a[212:215],v[188:191],v[92:95]
EX v72,v72
MM v[92:95],a[216:219],v[204:207],v[92:95]
EX v73,v73
MM v[92:95],a[220:223],v[220:223],v[92:95]
TR a[156:157],v233 offset:41472
CV v67,v70,v71
MM v[88:91],a[204:207],v[220:223],v[140:143]
EX v74,v74
MM v[88:91],a[200:203],v[204:207],v[88:91]
TR a[158:159],v233 offset:45568
CV v72,v72,v73
MM v[88:91],a[196:199],v[188:191],v[88:91]
EX v75,v75
MM v[88:91],a[192:195],v[172:175],v[88:91]
TR a[160:161],v232 offset:33792
TR a[162:163],v232 offset:37888
MM v[96:99],a[224:227],v[160:163],v[128:131]
EX v76,v76
MM v[96:99],a[228:231],v[176:179],v[96:99]
TR a[164:165],v232 offset:41984
CV v73,v74,v75
MM v[96:99],a[232:235],v[192:195],v[96:99]
EX v77,v77
MM v[96:99],a[236:239],v[208:211],v[96:99]
EX v78,v78
MM v[100:103],a[252:255],v[208:211],v[128:131]
TR a[166:167],v232 offset:46080
CV v74,v76,v77
MM v[100:103],a[248:251],v[192:195],v[100:103]
EX v79,v79
MM v[100:103],a[244:247],v[176:179],v[100:103]
TR a[168:169],v233 offset:33792
CV v75,v78,v79
MM v[100:103],a[240:243],v[160:163],v[100:103]
EX v80,v80
MM v[108:111],a[240:243],v[164:167],v[132:135]
TR a[170:171],v233 offset:37888
TR a[172:173],v233 offset:41984
MM v[108:111],a[244:247],v[180:183],v[108:111]
EX v81,v81
MM v[108:111],a[248:251],v[196:199],v[108:111]
TR a[174:175],v233 offset:46080
CV v80,v80,v81
MM v[108:111],a[252:255],v[212:215],v[108:111]
EX v82,v82
MM v[104:107],a[236:239],v[212:215],v[132:135]
EX v83,v83
MM v[104:107],a[232:235],v[196:199],v[104:107]
TR a[176:177],v232 offset:34304
TR a[178:179],v232 offset:38400
MM v[104:107],a[228:231],v[180:183],v[104:107]
EX v84,v84
MM v[104:107],a[224:227],v[164:167],v[104:107]
TR a[180:181],v232 offset:42496
CV v81,v82,v83
MM v[112:115],a[224:227],v[168:171],v[136:139]
EX v85,v85
MM v[112:115],a[228:231],v[184:187],v[112:115]
TR a[182:183],v232 offset:46592
CV v82,v84,v85
MM v[112:115],a[232:235],v[200:203],v[112:115]
EX v86,v86
MM v[112:115],a[236:239],v[216:219],v[112:115]
TR a[184:185],v233 offset:34304
TR a[186:187],v233 offset:38400
MM v[116:119],a[252:255],v[216:219],v[136:139]
EX v87,v87
MM v[116:119],a[248:251],v[200:203],v[116:119]
TR a[188:189],v233 offset:42496
CV v83,v86,v87
MM v[116:119],a[244:247],v[184:187],v[116:119]
EX v88,v88
MM v[116:119],a[240:243],v[168:171],v[116:119]
EX v89,v89
MM v[124:127],a[240:243],v[172:175],v[140:143]
TR a[190:191],v233 offset:46592
CV v88,v88,v89
MM v[124:127],a[244:247],v[188:191],v[124:127]
EX v90,v90
MM v[124:127],a[248:251],v[204:207],v[124:127]
EX v91,v91
MM v[124:127],a[252:255],v[220:223],v[124:127]
EX v92,v92
MM v[120:123],a[236:239],v[220:223],v[140:143]
EX v93,v93
MM v[120:123],a[232:235],v[204:207],v[120:123]
EX v94,v94
MM v[120:123],a[228:231],v[188:191],v[120:123]
EX v95,v95
MM v[120:123],a[224:227],v[172:175],v[120:123]
s_waitcnt vmcnt(15) lgkmcnt(0)
s_barrier
MM a[0:3],a[128:131],v[0:3],a[0:3]
s_add_u32 m0, s65, 32768
CV v89,v90,v91
CV v90,v92,v93
MM a[0:3],a[132:135],v[4:7],a[0:3]
RD a[192:195],v224 offset:0
CV v91,v94,v95
MM a[8:11],a[132:135],v[12:15],a[8:11]
buffer_load_dwordx4 v228, s[8:11], s64 offen offset:0 lds
MM a[8:11],a[128:131],v[8:11],a[8:11]
RD a[196:199],v225 offset:0
RD a[200:203],v226 offset:0
MM a[16:19],a[128:131],v[16:19],a[16:19]
EX v96,v96
MM a[16:19],a[132:135],v[20:23],a[16:19]
EX v97,v97
MM a[24:27],a[132:135],v[28:31],a[24:27]
RD a[204:207],v227 offset:0
CV v68,v96,v97
MM a[24:27],a[128:131],v[24:27],a[24:27]
EX v98,v98
MM a[28:31],a[136:139],v[24:27],a[28:31]
buffer_load_dwordx4 v229, s[8:11], s64 offen offset:1024 lds
MM a[28:31],a[140:143],v[28:31],a[28:31]
EX v99,v99
MM a[20:23],a[140:143],v[20:23],a[20:23]
RD a[208:211],v224 offset:4096
CV v69,v98,v99
MM a[20:23],a[136:139],v[16:19],a[20:23]
EX v100,v100
MM a[12:15],a[136:139],v[8:11],a[12:15]
EX v101,v101
MM a[12:15],a[140:143],v[12:15],a[12:15]
RD a[212:215],v225 offset:4096
CV v70,v100,v101
MM a[4:7],a[140:143],v[4:7],a[4:7]
EX v102,v102
MM a[4:7],a[136:139],v[0:3],a[4:7]
buffer_load_dwordx4 v230, s[8:11], s64 offen offset:2048 lds
MM a[32:35],a[144:147],v[0:3],a[32:35]
EX v103,v103
MM a[32:35],a[148:151],v[4:7],a[32:35]
RD a[216:219],v226 offset:4096
CV v71,v102,v103
MM a[40:43],a[148:151],v[12:15],a[40:43]
EX v104,v104
MM a[40:43],a[144:147],v[8:11],a[40:43]
EX v105,v105
MM a[48:51],a[144:147],v[16:19],a[48:51]
RD a[220:223],v227 offset:4096
CV v76,v104,v105
MM a[48:51],a[148:151],v[20:23],a[48:51]
EX v106,v106
MM a[56:59],a[148:151],v[28:31],a[56:59]
buffer_load_dwordx4 v231, s[8:11], s64 offen offset:3072 lds
MM a[56:59],a[144:147],v[24:27],a[56:59]
s_add_u32 m0, s65, 98304
EX v107,v107
MM a[60:63],a[152:155],v[24:27],a[60:63]
RD a[224:227],v224 offset:8192
CV v77,v106,v107
MM a[60:63],a[156:159],v[28:31],a[60:63]
EX v108,v108
MM a[52:55],a[156:159],v[20:23],a[52:55]
EX v109,v109
MM a[52:55],a[152:155],v[16:19],a[52:55]
RD a[228:231],v225 offset:8192
CV v78,v108,v109
MM a[44:47],a[152:155],v[8:11],a[44:47]
EX v110,v110
MM a[44:47],a[156:159],v[12:15],a[44:47]
buffer_load_dwordx4 v235, s[12:15], s81 offen offset:0 lds
MM a[36:39],a[156:159],v[4:7],a[36:39]
EX v111,v111
MM a[36:39],a[152:155],v[0:3],a[36:39]
RD a[232:235],v226 offset:8192
CV v79,v110,v111
MM a[64:67],a[160:163],v[0:3],a[64:67]
EX v112,v112
MM a[64:67],a[164:167],v[4:7],a[64:67]
EX v113,v113
MM a[72:75],a[164:167],v[12:15],a[72:75]
RD a[236:239],v227 offset:8192
CV v84,v112,v113
MM a[72:75],a[160:163],v[8:11],a[72:75]
buffer_load_dwordx4 v235, s[12:15], s64 offen offset:1024 lds
MM a[80:83],a[160:163],v[16:19],a[80:83]
EX v114,v114
MM a[80:83],a[164:167],v[20:23],a[80:83]
EX v115,v115
MM a[88:91],a[164:167],v[28:31],a[88:91]
RD a[240:243],v224 offset:12288
CV v85,v114,v115
MM a[88:91],a[160:163],v[24:27],a[88:91]
EX v116,v116
MM a[92:95],a[168:171],v[24:27],a[92:95]
EX v117,v117
MM a[92:95],a[172:175],v[28:31],a[92:95]
RD a[244:247],v225 offset:12288
CV v86,v116,v117
MM a[84:87],a[172:175],v[20:23],a[84:87]
buffer_load_dwordx4 v235, s[12:15], s81 offen offset:2048 lds
MM a[84:87],a[168:171],v[16:19],a[84:87]
EX v118,v118
MM a[76:79],a[168:171],v[8:11],a[76:79]
EX v119,v119
MM a[76:79],a[172:175],v[12:15],a[76:79]
)ASM"
R"ASM(
RD a[248:251],v226 offset:12288
CV v87,v118,v119
MM a[68:71],a[172:175],v[4:7],a[68:71]
EX v120,v120
MM a[68:71],a[168:171],v[0:3],a[68:71]
EX v121,v121
MM a[96:99],a[176:179],v[0:3],a[96:99]
RD a[252:255],v227 offset:12288
CV v92,v120,v121
MM a[96:99],a[180:183],v[4:7],a[96:99]
buffer_load_dwordx4 v235, s[12:15], s64 offen offset:3072 lds
MM a[104:107],a[180:183],v[12:15],a[104:107]
EX v122,v122
MM a[104:107],a[176:179],v[8:11],a[104:107]
EX v123,v123
MM a[112:115],a[176:179],v[16:19],a[112:115]
EX v124,v124
MM a[112:115],a[180:183],v[20:23],a[112:115]
EX v125,v125
MM a[120:123],a[180:183],v[28:31],a[120:123]
EX v126,v126
MM a[120:123],a[176:179],v[24:27],a[120:123]
EX v127,v127
MM a[124:127],a[184:187],v[24:27],a[124:127]
v_add_f32 v244, v144, v145
v_add_f32 v245, v148, v149
MM a[124:127],a[188:191],v[28:31],a[124:127]
v_add_f32 v246, v152, v153
v_add_f32 v247, v156, v157
MM a[116:119],a[188:191],v[20:23],a[116:119]
CV v93,v122,v123
CV v94,v124,v125
MM a[116:119],a[184:187],v[16:19],a[116:119]
CV v95,v126,v127
MM a[108:111],a[184:187],v[8:11],a[108:111]
MM a[108:111],a[188:191],v[12:15],a[108:111]
MM a[100:103],a[188:191],v[4:7],a[100:103]
MM a[100:103],a[184:187],v[0:3],a[100:103]
v_smfmac_f32_16x16x64_bf16 v[144:147], v[236:239], v[64:71], v234
v_smfmac_f32_16x16x64_bf16 v[148:151], v[236:239], v[72:79], v234
v_smfmac_f32_16x16x64_bf16 v[152:155], v[236:239], v[80:87], v234
v_smfmac_f32_16x16x64_bf16 v[156:159], v[236:239], v[88:95], v234
s_waitcnt lgkmcnt(0)
MM v[0:3],a[192:195],v[160:163],v[128:131]
s_add_u32 s64, s64, 16384
TR a[128:129],v232 offset:49152
s_add_u32 s81, s64, 896
MM v[0:3],a[196:199],v[176:179],v[0:3]
TR a[130:131],v232 offset:53248
v_max3_f32 v249, v144, v145, v148
MM v[0:3],a[200:203],v[192:195],v[0:3]
TR a[132:133],v232 offset:57344
v_max3_f32 v249, v249, v149, v152
MM v[0:3],a[204:207],v[208:211],v[0:3]
TR a[134:135],v232 offset:61440
v_max3_f32 v249, v249, v153, v156
MM v[4:7],a[220:223],v[208:211],v[128:131]
TR a[136:137],v233 offset:49152
v_max_f32 v249, v249, v157
MM v[4:7],a[216:219],v[192:195],v[4:7]
TR a[138:139],v233 offset:53248
v_cmp_lt_f32 vcc, s71, v249
MM v[4:7],a[212:215],v[176:179],v[4:7]
TR a[140:141],v233 offset:57344
TR a[142:143],v233 offset:61440
MM v[4:7],a[208:211],v[160:163],v[4:7]
s_cbranch_vccnz LSITE31_%=
LBACK31_%=:
MM v[12:15],a[208:211],v[164:167],v[132:135]
s_waitcnt vmcnt(8)
v_cmp_ne_u32 vcc, s69, v252
s_cbranch_vccnz LRFY_%=
LRFYb_%=:
MM v[12:15],a[212:215],v[180:183],v[12:15]
v_lshrrev_b32 v109, 2, v248
v_lshrrev_b32 v110, 3, v248
MM v[12:15],a[216:219],v[196:199],v[12:15]
s_and_b32 s76, s17, 7
EX v0,v0
MM v[12:15],a[220:223],v[212:215],v[12:15]
s_mul_i32 s76, s76, 2560
EX v1,v1
MM v[8:11],a[204:207],v[212:215],v[132:135]
s_lshl_b32 s91, s90, 8
EX v2,v2
MM v[8:11],a[200:203],v[196:199],v[8:11]
s_add_u32 s76, s76, s91
TR a[144:145],v232 offset:49664
CV v0,v0,v1
MM v[8:11],a[196:199],v[180:183],v[8:11]
s_add_u32 s76, s76, 21659648
buffer_load_dword v96, v109, s[20:23], s76 offen offset:0 sc1
MM v[8:11],a[192:195],v[164:167],v[8:11]
s_add_u32 s76, s76, 20480
EX v3,v3
MM v[16:19],a[192:195],v[168:171],v[136:139]
buffer_load_dword v97, v109, s[20:23], s76 offen offset:0 sc1
MM v[16:19],a[196:199],v[184:187],v[16:19]
s_add_u32 s76, s76, 20480
EX v4,v4
MM v[16:19],a[200:203],v[200:203],v[16:19]
buffer_load_dword v98, v109, s[20:23], s76 offen offset:0 sc1
MM v[16:19],a[204:207],v[216:219],v[16:19]
s_add_u32 s76, s76, 20480
EX v5,v5
MM v[20:23],a[220:223],v[216:219],v[136:139]
buffer_load_dword v99, v109, s[20:23], s76 offen offset:0 sc1
MM v[20:23],a[216:219],v[200:203],v[20:23]
s_add_u32 s76, s76, 20480
TR a[146:147],v232 offset:53760
TR a[148:149],v232 offset:57856
MM v[20:23],a[212:215],v[184:187],v[20:23]
buffer_load_dword v100, v109, s[20:23], s76 offen offset:0 sc1
MM v[20:23],a[208:211],v[168:171],v[20:23]
s_add_u32 s76, s76, 20480
EX v6,v6
MM v[28:31],a[208:211],v[172:175],v[140:143]
buffer_load_dword v101, v109, s[20:23], s76 offen offset:0 sc1
MM v[28:31],a[212:215],v[188:191],v[28:31]
s_add_u32 s76, s76, 20480
EX v7,v7
MM v[28:31],a[216:219],v[204:207],v[28:31]
buffer_load_dword v102, v109, s[20:23], s76 offen offset:0 sc1
MM v[28:31],a[220:223],v[220:223],v[28:31]
s_add_u32 s76, s76, 20480
s_nop 0
LPG22_%=:
s_cbranch_execz LTR4_%=
TR a[150:151],v232 offset:61952
EX v8,v8
MM v[24:27],a[204:207],v[220:223],v[140:143]
buffer_load_dword v103, v109, s[20:23], s76 offen offset:0 sc1
MM v[24:27],a[200:203],v[204:207],v[24:27]
s_add_u32 s76, s76, 20480
TR a[152:153],v233 offset:49664
EX v9,v9
CV v1,v2,v3
MM v[24:27],a[196:199],v[188:191],v[24:27]
buffer_load_dword v104, v109, s[20:23], s76 offen offset:0 sc1
TR a[154:155],v233 offset:53760
MM v[24:27],a[192:195],v[172:175],v[24:27]
s_and_b32 s76, s19, 65535
EX v10,v10
MM v[32:35],a[224:227],v[160:163],v[128:131]
s_sub_u32 s76, 65535, s76
TR a[156:157],v233 offset:57856
CV v2,v4,v5
EX v11,v11
MM v[32:35],a[228:231],v[176:179],v[32:35]
s_mul_i32 s76, s76, 1280
TR a[158:159],v233 offset:61952
CV v3,v6,v7
MM v[32:35],a[232:235],v[192:195],v[32:35]
s_lshl_b32 s91, s90, 7
EX v12,v12
MM v[32:35],a[236:239],v[208:211],v[32:35]
s_add_u32 s76, s76, s91
TR a[160:161],v232 offset:50176
TR a[162:163],v232 offset:54272
MM v[36:39],a[252:255],v[208:211],v[128:131]
s_add_u32 s76, s76, 131072
buffer_load_ushort v108, v110, s[20:23], s76 offen sc1
EX v13,v13
MM v[36:39],a[248:251],v[192:195],v[36:39]
s_ashr_i32 s91, s19, 31
EX v14,v14
MM v[36:39],a[244:247],v[176:179],v[36:39]
s_not_b32 s91, s91
TR a[164:165],v232 offset:58368
CV v8,v8,v9
MM v[36:39],a[240:243],v[160:163],v[36:39]
s_or_b32 s91, s91, 0x80000000
EX v15,v15
MM v[44:47],a[240:243],v[164:167],v[132:135]
s_xor_b32 s19, s19, s91
TR a[166:167],v232 offset:62464
CV v9,v10,v11
MM v[44:47],a[244:247],v[180:183],v[44:47]
s_and_b32 s19, s19, 0xffff0000
v_mov_b32 v111, 0xbfb8aa3b
v_mul_f32 v111, s19, v111
MM v[44:47],a[248:251],v[196:199],v[44:47]
EX v111,v111
EX v16,v16
MM v[44:47],a[252:255],v[212:215],v[44:47]
TR a[168:169],v233 offset:50176
v_add_f32 v111, 1.0, v111
EX v17,v17
MM v[40:43],a[236:239],v[212:215],v[132:135]
v_rcp_f32 v111, v111
MM v[40:43],a[232:235],v[196:199],v[40:43]
TR a[170:171],v233 offset:54272
CV v111,v111,v111
v_and_b32 v111, 0xffff0000, v111
EX v18,v18
MM v[40:43],a[228:231],v[180:183],v[40:43]
TR a[172:173],v233 offset:58368
CV v10,v12,v13
MM v[40:43],a[224:227],v[164:167],v[40:43]
EX v19,v19
MM v[48:51],a[224:227],v[168:171],v[136:139]
TR a[174:175],v233 offset:62464
CV v11,v14,v15
MM v[48:51],a[228:231],v[184:187],v[48:51]
EX v20,v20
MM v[48:51],a[232:235],v[200:203],v[48:51]
TR a[176:177],v232 offset:50688
TR a[178:179],v232 offset:54784
MM v[48:51],a[236:239],v[216:219],v[48:51]
EX v21,v21
MM v[52:55],a[252:255],v[216:219],v[136:139]
EX v22,v22
MM v[52:55],a[248:251],v[200:203],v[52:55]
TR a[180:181],v232 offset:58880
CV v16,v16,v17
MM v[52:55],a[244:247],v[184:187],v[52:55]
EX v23,v23
MM v[52:55],a[240:243],v[168:171],v[52:55]
TR a[182:183],v232 offset:62976
CV v17,v18,v19
MM v[60:63],a[240:243],v[172:175],v[140:143]
EX v24,v24
MM v[60:63],a[244:247],v[188:191],v[60:63]
TR a[184:185],v233 offset:50688
TR a[186:187],v233 offset:54784
MM v[60:63],a[248:251],v[204:207],v[60:63]
TR a[188:189],v233 offset:58880
TR a[190:191],v233 offset:62976
EX v25,v25
MM v[60:63],a[252:255],v[220:223],v[60:63]
EX v26,v26
MM v[56:59],a[236:239],v[220:223],v[140:143]
EX v27,v27
MM v[56:59],a[232:235],v[204:207],v[56:59]
EX v28,v28
MM v[56:59],a[228:231],v[188:191],v[56:59]
EX v29,v29
CV v18,v20,v21
MM v[56:59],a[224:227],v[172:175],v[56:59]
s_waitcnt lgkmcnt(0)
s_barrier
MM a[0:3],a[128:131],v[64:67],a[0:3]
s_add_u32 m0, s65, 49152
CV v19,v22,v23
CV v24,v24,v25
MM a[0:3],a[132:135],v[68:71],a[0:3]
CV v25,v26,v27
CV v26,v28,v29
MM a[8:11],a[132:135],v[76:79],a[8:11]
EX v30,v30
MM a[8:11],a[128:131],v[72:75],a[8:11]
EX v31,v31
MM a[16:19],a[128:131],v[80:83],a[16:19]
RD a[192:195],v224 offset:16384
CV v27,v30,v31
MM a[16:19],a[132:135],v[84:87],a[16:19]
EX v32,v32
MM a[24:27],a[132:135],v[92:95],a[24:27]
buffer_load_dwordx4 v228, s[8:11], s64 offen offset:0 lds
MM a[24:27],a[128:131],v[88:91],a[24:27]
EX v33,v33
MM a[28:31],a[136:139],v[88:91],a[28:31]
RD a[196:199],v225 offset:16384
CV v4,v32,v33
MM a[28:31],a[140:143],v[92:95],a[28:31]
EX v34,v34
MM a[20:23],a[140:143],v[84:87],a[20:23]
EX v35,v35
MM a[20:23],a[136:139],v[80:83],a[20:23]
RD a[200:203],v226 offset:16384
CV v5,v34,v35
MM a[12:15],a[136:139],v[72:75],a[12:15]
EX v36,v36
MM a[12:15],a[140:143],v[76:79],a[12:15]
buffer_load_dwordx4 v229, s[8:11], s64 offen offset:1024 lds
MM a[4:7],a[140:143],v[68:71],a[4:7]
EX v37,v37
MM a[4:7],a[136:139],v[64:67],a[4:7]
RD a[204:207],v227 offset:16384
CV v6,v36,v37
MM a[32:35],a[144:147],v[64:67],a[32:35]
EX v38,v38
MM a[32:35],a[148:151],v[68:71],a[32:35]
EX v39,v39
MM a[40:43],a[148:151],v[76:79],a[40:43]
RD a[208:211],v224 offset:20480
CV v7,v38,v39
MM a[40:43],a[144:147],v[72:75],a[40:43]
EX v40,v40
MM a[48:51],a[144:147],v[80:83],a[48:51]
buffer_load_dwordx4 v230, s[8:11], s64 offen offset:2048 lds
MM a[48:51],a[148:151],v[84:87],a[48:51]
EX v41,v41
MM a[56:59],a[148:151],v[92:95],a[56:59]
RD a[212:215],v225 offset:20480
CV v12,v40,v41
MM a[56:59],a[144:147],v[88:91],a[56:59]
EX v42,v42
MM a[60:63],a[152:155],v[88:91],a[60:63]
EX v43,v43
MM a[60:63],a[156:159],v[92:95],a[60:63]
RD a[216:219],v226 offset:20480
CV v13,v42,v43
MM a[52:55],a[156:159],v[84:87],a[52:55]
EX v44,v44
MM a[52:55],a[152:155],v[80:83],a[52:55]
buffer_load_dwordx4 v231, s[8:11], s64 offen offset:3072 lds
MM a[44:47],a[152:155],v[72:75],a[44:47]
s_add_u32 m0, s65, 114688
EX v45,v45
MM a[44:47],a[156:159],v[76:79],a[44:47]
RD a[220:223],v227 offset:20480
CV v14,v44,v45
MM a[36:39],a[156:159],v[68:71],a[36:39]
EX v46,v46
MM a[36:39],a[152:155],v[64:67],a[36:39]
RD a[224:227],v224 offset:24576
RD a[228:231],v225 offset:24576
MM a[64:67],a[160:163],v[64:67],a[64:67]
EX v47,v47
MM a[64:67],a[164:167],v[68:71],a[64:67]
RD a[232:235],v226 offset:24576
CV v15,v46,v47
MM a[72:75],a[164:167],v[76:79],a[72:75]
EX v48,v48
MM a[72:75],a[160:163],v[72:75],a[72:75]
buffer_load_dwordx4 v235, s[12:15], s81 offen offset:0 lds
MM a[80:83],a[160:163],v[80:83],a[80:83]
EX v49,v49
MM a[80:83],a[164:167],v[84:87],a[80:83]
RD a[236:239],v227 offset:24576
CV v20,v48,v49
MM a[88:91],a[164:167],v[92:95],a[88:91]
EX v50,v50
MM a[88:91],a[160:163],v[88:91],a[88:91]
EX v51,v51
MM a[92:95],a[168:171],v[88:91],a[92:95]
RD a[240:243],v224 offset:28672
CV v21,v50,v51
MM a[92:95],a[172:175],v[92:95],a[92:95]
EX v52,v52
MM a[84:87],a[172:175],v[84:87],a[84:87]
buffer_load_dwordx4 v235, s[12:15], s64 offen offset:1024 lds
MM a[84:87],a[168:171],v[80:83],a[84:87]
EX v53,v53
MM a[76:79],a[168:171],v[72:75],a[76:79]
RD a[244:247],v225 offset:28672
CV v22,v52,v53
MM a[76:79],a[172:175],v[76:79],a[76:79]
EX v54,v54
MM a[68:71],a[172:175],v[68:71],a[68:71]
s_waitcnt vmcnt(6)
v_add_f32 v96, v96, v100
v_add_f32 v97, v97, v101
MM a[68:71],a[168:171],v[64:67],a[68:71]
v_add_f32 v98, v98, v102
v_add_f32 v99, v99, v103
MM a[96:99],a[176:179],v[64:67],a[96:99]
v_add_f32 v96, v96, v98
v_add_f32 v97, v97, v99
MM a[96:99],a[180:183],v[68:71],a[96:99]
v_add_f32 v96, v96, v104
v_lshlrev_b32 v108, 16, v108
MM a[104:107],a[180:183],v[76:79],a[104:107]
v_add_f32 v96, v96, v97
CV v96,v96,v96
EX v55,v55
MM a[104:107],a[176:179],v[72:75],a[104:107]
v_and_b32 v96, 0xffff0000, v96
v_mul_f32 v96, v96, v111
MM a[112:115],a[176:179],v[80:83],a[112:115]
CV v96,v96,v96
v_and_b32 v96, 0xffff0000, v96
EX v56,v56
MM a[112:115],a[180:183],v[84:87],a[112:115]
v_add_f32 v96, v96, v108
CV v96,v96,v96
MM a[120:123],a[180:183],v[92:95],a[120:123]
s_mov_b64 exec, s[44:45]
buffer_store_short v96, v110, s[20:23], s76 offen offset:0 sc1
s_mov_b64 exec, -1
s_nop 0
EX v57,v57
MM a[120:123],a[176:179],v[88:91],a[120:123]
EX v58,v58
MM a[124:127],a[184:187],v[88:91],a[124:127]
v_add_f32 v244, v144, v145
v_add_f32 v245, v148, v149
MM a[124:127],a[188:191],v[92:95],a[124:127]
v_add_f32 v246, v152, v153
v_add_f32 v247, v156, v157
MM a[116:119],a[188:191],v[84:87],a[116:119]
)ASM"
R"ASM(
EX v59,v59
MM a[116:119],a[184:187],v[80:83],a[116:119]
EX v60,v60
MM a[108:111],a[184:187],v[72:75],a[108:111]
CV v23,v54,v55
CV v28,v56,v57
MM a[108:111],a[188:191],v[76:79],a[108:111]
EX v61,v61
MM a[100:103],a[188:191],v[68:71],a[100:103]
buffer_load_dwordx4 v235, s[12:15], s81 offen offset:2048 lds
MM a[100:103],a[184:187],v[64:67],a[100:103]
EX v62,v62
v_smfmac_f32_16x16x64_bf16 v[144:147], v[236:239], v[0:7], v234
RD a[248:251],v226 offset:28672
RD a[252:255],v227 offset:28672
EX v63,v63
v_smfmac_f32_16x16x64_bf16 v[148:151], v[236:239], v[8:15], v234
CV v29,v58,v59
v_smfmac_f32_16x16x64_bf16 v[152:155], v[236:239], v[16:23], v234
CV v30,v60,v61
CV v31,v62,v63
s_nop 1
v_smfmac_f32_16x16x64_bf16 v[156:159], v[236:239], v[24:31], v234
s_waitcnt lgkmcnt(0)
buffer_load_dwordx4 v235, s[12:15], s64 offen offset:3072 lds
MM v[64:67],a[192:195],v[160:163],v[128:131]
s_sub_u32 s64, s64, 507904
TR a[128:129],v232 offset:0
s_add_u32 s81, s64, 896
MM v[64:67],a[196:199],v[176:179],v[64:67]
TR a[130:131],v232 offset:4096
v_max3_f32 v249, v144, v145, v148
MM v[64:67],a[200:203],v[192:195],v[64:67]
TR a[132:133],v232 offset:8192
v_max3_f32 v249, v249, v149, v152
MM v[64:67],a[204:207],v[208:211],v[64:67]
TR a[134:135],v232 offset:12288
v_max3_f32 v249, v249, v153, v156
MM v[68:71],a[220:223],v[208:211],v[128:131]
TR a[136:137],v233 offset:0
v_max_f32 v249, v249, v157
MM v[68:71],a[216:219],v[192:195],v[68:71]
TR a[138:139],v233 offset:4096
v_cmp_lt_f32 vcc, s71, v249
MM v[68:71],a[212:215],v[176:179],v[68:71]
TR a[140:141],v233 offset:8192
TR a[142:143],v233 offset:12288
MM v[68:71],a[208:211],v[160:163],v[68:71]
s_cbranch_vccnz LSITE32_%=
LBACK32_%=:
MM v[76:79],a[208:211],v[164:167],v[132:135]
EX v64,v64
MM v[76:79],a[212:215],v[180:183],v[76:79]
EX v65,v65
MM v[76:79],a[216:219],v[196:199],v[76:79]
TR a[144:145],v232 offset:512
CV v64,v64,v65
MM v[76:79],a[220:223],v[212:215],v[76:79]
EX v66,v66
MM v[72:75],a[204:207],v[212:215],v[132:135]
EX v67,v67
MM v[72:75],a[200:203],v[196:199],v[72:75]
TR a[146:147],v232 offset:4608
CV v65,v66,v67
MM v[72:75],a[196:199],v[180:183],v[72:75]
EX v68,v68
MM v[72:75],a[192:195],v[164:167],v[72:75]
TR a[148:149],v232 offset:8704
TR a[150:151],v232 offset:12800
MM v[80:83],a[192:195],v[168:171],v[136:139]
EX v69,v69
MM v[80:83],a[196:199],v[184:187],v[80:83]
TR a[152:153],v233 offset:512
CV v66,v68,v69
MM v[80:83],a[200:203],v[200:203],v[80:83]
EX v70,v70
MM v[80:83],a[204:207],v[216:219],v[80:83]
TR a[154:155],v233 offset:4608
TR a[156:157],v233 offset:8704
MM v[84:87],a[220:223],v[216:219],v[136:139]
EX v71,v71
MM v[84:87],a[216:219],v[200:203],v[84:87]
TR a[158:159],v233 offset:12800
CV v67,v70,v71
MM v[84:87],a[212:215],v[184:187],v[84:87]
EX v72,v72
MM v[84:87],a[208:211],v[168:171],v[84:87]
EX v73,v73
MM v[92:95],a[208:211],v[172:175],v[140:143]
TR a[160:161],v232 offset:1024
CV v72,v72,v73
MM v[92:95],a[212:215],v[188:191],v[92:95]
EX v74,v74
MM v[92:95],a[216:219],v[204:207],v[92:95]
TR a[162:163],v232 offset:5120
TR a[164:165],v232 offset:9216
MM v[92:95],a[220:223],v[220:223],v[92:95]
EX v75,v75
MM v[88:91],a[204:207],v[220:223],v[140:143]
TR a[166:167],v232 offset:13312
CV v73,v74,v75
MM v[88:91],a[200:203],v[204:207],v[88:91]
EX v76,v76
MM v[88:91],a[196:199],v[188:191],v[88:91]
EX v77,v77
MM v[88:91],a[192:195],v[172:175],v[88:91]
TR a[168:169],v233 offset:1024
CV v74,v76,v77
MM v[96:99],a[224:227],v[160:163],v[128:131]
EX v78,v78
MM v[96:99],a[228:231],v[176:179],v[96:99]
TR a[170:171],v233 offset:5120
TR a[172:173],v233 offset:9216
MM v[96:99],a[232:235],v[192:195],v[96:99]
EX v79,v79
MM v[96:99],a[236:239],v[208:211],v[96:99]
TR a[174:175],v233 offset:13312
CV v75,v78,v79
MM v[100:103],a[252:255],v[208:211],v[128:131]
EX v80,v80
MM v[100:103],a[248:251],v[192:195],v[100:103]
EX v81,v81
MM v[100:103],a[244:247],v[176:179],v[100:103]
TR a[176:177],v232 offset:1536
CV v80,v80,v81
MM v[100:103],a[240:243],v[160:163],v[100:103]
EX v82,v82
MM v[108:111],a[240:243],v[164:167],v[132:135]
TR a[178:179],v232 offset:5632
TR a[180:181],v232 offset:9728
MM v[108:111],a[244:247],v[180:183],v[108:111]
EX v83,v83
MM v[108:111],a[248:251],v[196:199],v[108:111]
TR a[182:183],v232 offset:13824
CV v81,v82,v83
MM v[108:111],a[252:255],v[212:215],v[108:111]
EX v84,v84
MM v[104:107],a[236:239],v[212:215],v[132:135]
TR a[184:185],v233 offset:1536
TR a[186:187],v233 offset:5632
MM v[104:107],a[232:235],v[196:199],v[104:107]
EX v85,v85
MM v[104:107],a[228:231],v[180:183],v[104:107]
TR a[188:189],v233 offset:9728
CV v82,v84,v85
MM v[104:107],a[224:227],v[164:167],v[104:107]
EX v86,v86
MM v[112:115],a[224:227],v[168:171],v[136:139]
EX v87,v87
MM v[112:115],a[228:231],v[184:187],v[112:115]
TR a[190:191],v233 offset:13824
CV v83,v86,v87
MM v[112:115],a[232:235],v[200:203],v[112:115]
EX v88,v88
MM v[112:115],a[236:239],v[216:219],v[112:115]
EX v89,v89
MM v[116:119],a[252:255],v[216:219],v[136:139]
EX v90,v90
MM v[116:119],a[248:251],v[200:203],v[116:119]
EX v91,v91
MM v[116:119],a[244:247],v[184:187],v[116:119]
EX v92,v92
MM v[116:119],a[240:243],v[168:171],v[116:119]
EX v93,v93
MM v[124:127],a[240:243],v[172:175],v[140:143]
EX v94,v94
MM v[124:127],a[244:247],v[188:191],v[124:127]
EX v95,v95
MM v[124:127],a[248:251],v[204:207],v[124:127]
EX v96,v96
MM v[124:127],a[252:255],v[220:223],v[124:127]
EX v97,v97
MM v[120:123],a[236:239],v[220:223],v[140:143]
EX v98,v98
MM v[120:123],a[232:235],v[204:207],v[120:123]
EX v99,v99
MM v[120:123],a[228:231],v[188:191],v[120:123]
EX v100,v100
MM v[120:123],a[224:227],v[172:175],v[120:123]
s_waitcnt lgkmcnt(0)
s_barrier
MM a[0:3],a[128:131],v[0:3],a[0:3]
s_add_u32 m0, s65, 0
CV v88,v88,v89
CV v89,v90,v91
MM a[0:3],a[132:135],v[4:7],a[0:3]
RD a[192:195],v224 offset:32768
CV v90,v92,v93
MM a[8:11],a[132:135],v[12:15],a[8:11]
RD a[196:199],v225 offset:32768
CV v91,v94,v95
MM a[8:11],a[128:131],v[8:11],a[8:11]
buffer_load_dwordx4 v228, s[8:11], s64 offen offset:0 lds
MM a[16:19],a[128:131],v[16:19],a[16:19]
RD a[200:203],v226 offset:32768
CV v68,v96,v97
MM a[16:19],a[132:135],v[20:23],a[16:19]
RD a[204:207],v227 offset:32768
CV v69,v98,v99
MM a[24:27],a[132:135],v[28:31],a[24:27]
buffer_load_dwordx4 v229, s[8:11], s64 offen offset:1024 lds
MM a[24:27],a[128:131],v[24:27],a[24:27]
RD a[208:211],v224 offset:36864
RD a[212:215],v225 offset:36864
MM a[28:31],a[136:139],v[24:27],a[28:31]
buffer_load_dwordx4 v230, s[8:11], s64 offen offset:2048 lds
MM a[28:31],a[140:143],v[28:31],a[28:31]
RD a[216:219],v226 offset:36864
RD a[220:223],v227 offset:36864
MM a[20:23],a[140:143],v[20:23],a[20:23]
EX v101,v101
MM a[20:23],a[136:139],v[16:19],a[20:23]
RD a[224:227],v224 offset:40960
CV v70,v100,v101
MM a[12:15],a[136:139],v[8:11],a[12:15]
EX v102,v102
MM a[12:15],a[140:143],v[12:15],a[12:15]
buffer_load_dwordx4 v231, s[8:11], s64 offen offset:3072 lds
MM a[4:7],a[140:143],v[4:7],a[4:7]
s_add_u32 m0, s65, 65536
EX v103,v103
MM a[4:7],a[136:139],v[0:3],a[4:7]
RD a[228:231],v225 offset:40960
CV v71,v102,v103
MM a[32:35],a[144:147],v[0:3],a[32:35]
EX v104,v104
MM a[32:35],a[148:151],v[4:7],a[32:35]
EX v105,v105
MM a[40:43],a[148:151],v[12:15],a[40:43]
RD a[232:235],v226 offset:40960
CV v76,v104,v105
MM a[40:43],a[144:147],v[8:11],a[40:43]
EX v106,v106
MM a[48:51],a[144:147],v[16:19],a[48:51]
buffer_load_dwordx4 v235, s[12:15], s81 offen offset:0 lds
MM a[48:51],a[148:151],v[20:23],a[48:51]
EX v107,v107
MM a[56:59],a[148:151],v[28:31],a[56:59]
RD a[236:239],v227 offset:40960
CV v77,v106,v107
MM a[56:59],a[144:147],v[24:27],a[56:59]
EX v108,v108
MM a[60:63],a[152:155],v[24:27],a[60:63]
EX v109,v109
MM a[60:63],a[156:159],v[28:31],a[60:63]
RD a[240:243],v224 offset:45056
CV v78,v108,v109
MM a[52:55],a[156:159],v[20:23],a[52:55]
EX v110,v110
MM a[52:55],a[152:155],v[16:19],a[52:55]
buffer_load_dwordx4 v235, s[12:15], s64 offen offset:1024 lds
MM a[44:47],a[152:155],v[8:11],a[44:47]
EX v111,v111
MM a[44:47],a[156:159],v[12:15],a[44:47]
RD a[244:247],v225 offset:45056
CV v79,v110,v111
MM a[36:39],a[156:159],v[4:7],a[36:39]
EX v112,v112
MM a[36:39],a[152:155],v[0:3],a[36:39]
EX v113,v113
MM a[64:67],a[160:163],v[0:3],a[64:67]
RD a[248:251],v226 offset:45056
CV v84,v112,v113
MM a[64:67],a[164:167],v[4:7],a[64:67]
EX v114,v114
MM a[72:75],a[164:167],v[12:15],a[72:75]
buffer_load_dwordx4 v235, s[12:15], s81 offen offset:2048 lds
MM a[72:75],a[160:163],v[8:11],a[72:75]
EX v115,v115
MM a[80:83],a[160:163],v[16:19],a[80:83]
RD a[252:255],v227 offset:45056
CV v85,v114,v115
MM a[80:83],a[164:167],v[20:23],a[80:83]
EX v116,v116
MM a[88:91],a[164:167],v[28:31],a[88:91]
EX v117,v117
MM a[88:91],a[160:163],v[24:27],a[88:91]
buffer_load_dwordx4 v235, s[12:15], s64 offen offset:3072 lds
MM a[92:95],a[168:171],v[24:27],a[92:95]
EX v118,v118
MM a[92:95],a[172:175],v[28:31],a[92:95]
EX v119,v119
MM a[84:87],a[172:175],v[20:23],a[84:87]
EX v120,v120
MM a[84:87],a[168:171],v[16:19],a[84:87]
EX v121,v121
MM a[76:79],a[168:171],v[8:11],a[76:79]
EX v122,v122
MM a[76:79],a[172:175],v[12:15],a[76:79]
EX v123,v123
MM a[68:71],a[172:175],v[4:7],a[68:71]
EX v124,v124
MM a[68:71],a[168:171],v[0:3],a[68:71]
EX v125,v125
MM a[96:99],a[176:179],v[0:3],a[96:99]
EX v126,v126
MM a[96:99],a[180:183],v[4:7],a[96:99]
EX v127,v127
MM a[104:107],a[180:183],v[12:15],a[104:107]
CV v86,v116,v117
CV v87,v118,v119
MM a[104:107],a[176:179],v[8:11],a[104:107]
CV v92,v120,v121
CV v93,v122,v123
MM a[112:115],a[176:179],v[16:19],a[112:115]
CV v94,v124,v125
CV v95,v126,v127
MM a[112:115],a[180:183],v[20:23],a[112:115]
MM a[120:123],a[180:183],v[28:31],a[120:123]
s_lshl_b32 s91, s17, 2
s_add_u32 s91, s91, 94464
s_and_b64 s[44:45], s[44:45], 1
MM a[120:123],a[176:179],v[24:27],a[120:123]
s_waitcnt vmcnt(10)
v_mov_b32 v250, s69
s_mov_b64 exec, s[44:45]
buffer_store_dword v250, off, s[20:23], s91 sc1
s_mov_b64 exec, -1
MM a[124:127],a[184:187],v[24:27],a[124:127]
v_add_f32 v244, v144, v145
v_add_f32 v245, v148, v149
MM a[124:127],a[188:191],v[28:31],a[124:127]
v_add_f32 v246, v152, v153
v_add_f32 v247, v156, v157
MM a[116:119],a[188:191],v[20:23],a[116:119]
MM a[116:119],a[184:187],v[16:19],a[116:119]
MM a[108:111],a[184:187],v[8:11],a[108:111]
MM a[108:111],a[188:191],v[12:15],a[108:111]
MM a[100:103],a[188:191],v[4:7],a[100:103]
MM a[100:103],a[184:187],v[0:3],a[100:103]
v_smfmac_f32_16x16x64_bf16 v[144:147], v[236:239], v[64:71], v234
v_smfmac_f32_16x16x64_bf16 v[148:151], v[236:239], v[72:79], v234
s_nop 0
LPG23_%=:
s_cbranch_execz LPG27_%=
v_smfmac_f32_16x16x64_bf16 v[152:155], v[236:239], v[80:87], v234
v_smfmac_f32_16x16x64_bf16 v[156:159], v[236:239], v[88:95], v234
s_waitcnt lgkmcnt(0)
LLAJ_%=:
MM v[0:3],a[192:195],v[160:163],v[128:131]
s_add_u32 s64, s64, 16384
TR a[128:129],v232 offset:16384
s_add_u32 s81, s64, 896
MM v[0:3],a[196:199],v[176:179],v[0:3]
TR a[130:131],v232 offset:20480
v_max3_f32 v249, v144, v145, v148
MM v[0:3],a[200:203],v[192:195],v[0:3]
TR a[132:133],v232 offset:24576
v_max3_f32 v249, v249, v149, v152
MM v[0:3],a[204:207],v[208:211],v[0:3]
TR a[134:135],v232 offset:28672
v_max3_f32 v249, v249, v153, v156
MM v[4:7],a[220:223],v[208:211],v[128:131]
TR a[136:137],v233 offset:16384
v_max_f32 v249, v249, v157
MM v[4:7],a[216:219],v[192:195],v[4:7]
TR a[138:139],v233 offset:20480
v_cmp_lt_f32 vcc, s71, v249
MM v[4:7],a[212:215],v[176:179],v[4:7]
TR a[140:141],v233 offset:24576
TR a[142:143],v233 offset:28672
MM v[4:7],a[208:211],v[160:163],v[4:7]
s_cbranch_vccnz LSITE33_%=
LBACK33_%=:
MM v[12:15],a[208:211],v[164:167],v[132:135]
v_bfe_u32 v242, v248, 6, 4
v_mul_u32_u24 v242, 1280, v242
MM v[12:15],a[212:215],v[180:183],v[12:15]
v_and_b32 v252, 48, v248
v_bfe_u32 v243, v248, 8, 1
MM v[12:15],a[216:219],v[196:199],v[12:15]
v_lshlrev_b32 v243, 5, v243
v_xor_b32 v252, v252, v243
MM v[12:15],a[220:223],v[212:215],v[12:15]
v_add_u32 v252, v252, v242
v_lshrrev_b32 v242, 3, v248
MM v[8:11],a[204:207],v[212:215],v[132:135]
v_xor_b32 v243, v242, v248
v_and_b32 v243, 96, v243
MM v[8:11],a[200:203],v[196:199],v[8:11]
TR a[144:145],v232 offset:16896
v_and_or_b32 v243, v248, 16, v243
MM v[8:11],a[196:199],v[180:183],v[8:11]
)ASM"
R"ASM(
s_and_b32 s66, s49, 8
EX v0,v0
MM v[8:11],a[192:195],v[164:167],v[8:11]
s_lshl_b32 s66, s66, 3
v_lshrrev_b32 v242, 7, v248
v_xor_b32 v243, s66, v243
EX v1,v1
MM v[16:19],a[192:195],v[168:171],v[136:139]
v_mul_u32_u24 v242, 1280, v242
v_add_u32 v240, v242, v243
EX v2,v2
MM v[16:19],a[196:199],v[184:187],v[16:19]
s_mul_i32 s88, %[swave], 40960
EX v3,v3
MM v[16:19],a[200:203],v[200:203],v[16:19]
s_and_b32 s66, s84, 1
EX v4,v4
MM v[16:19],a[204:207],v[216:219],v[16:19]
s_lshl_b32 s86, s66, 6
TR a[146:147],v232 offset:20992
CV v0,v0,v1
MM v[20:23],a[220:223],v[216:219],v[136:139]
s_add_u32 s86, s86, s88
EX v5,v5
MM v[20:23],a[216:219],v[200:203],v[20:23]
s_xor_b32 s66, s66, 1
TR a[148:149],v232 offset:25088
CV v1,v2,v3
MM v[20:23],a[212:215],v[184:187],v[20:23]
s_lshl_b32 s66, s66, 8
EX v6,v6
MM v[20:23],a[208:211],v[168:171],v[20:23]
s_add_u32 s88, s88, s66
EX v7,v7
MM v[28:31],a[208:211],v[172:175],v[140:143]
s_and_b32 s66, s49, 16
TR a[150:151],v232 offset:29184
TR a[152:153],v233 offset:16896
MM v[28:31],a[212:215],v[188:191],v[28:31]
s_lshl_b32 s66, s66, 3
EX v8,v8
MM v[28:31],a[216:219],v[204:207],v[28:31]
s_sub_u32 s87, s86, s66
TR a[154:155],v233 offset:20992
CV v2,v4,v5
MM v[28:31],a[220:223],v[220:223],v[28:31]
s_add_u32 s87, s87, 128
EX v9,v9
MM v[24:27],a[204:207],v[220:223],v[140:143]
s_add_u32 s86, s86, s66
TR a[156:157],v233 offset:25088
CV v3,v6,v7
MM v[24:27],a[200:203],v[204:207],v[24:27]
s_and_b32 s94, s49, 7
EX v10,v10
MM v[24:27],a[196:199],v[188:191],v[24:27]
s_lshl_b32 s89, %[swave], 11
TR a[158:159],v233 offset:29184
CV v8,v8,v9
MM v[24:27],a[192:195],v[172:175],v[24:27]
s_add_u32 s89, s89, %[slds]
EX v11,v11
MM v[32:35],a[224:227],v[160:163],v[128:131]
s_xor_b32 s66, s94, 4
TR a[160:161],v232 offset:17408
TR a[162:163],v232 offset:21504
MM v[32:35],a[228:231],v[176:179],v[32:35]
s_mul_i32 s66, s66, 163840
EX v12,v12
MM v[32:35],a[232:235],v[192:195],v[32:35]
s_add_u32 s66, s66, s88
s_add_u32 m0, s89, 131072
s_nop 0
buffer_load_dwordx4 v252, s[24:27], s66 offen offset:0 lds
MM v[32:35],a[236:239],v[208:211],v[32:35]
s_add_u32 s66, s66, 19456
EX v13,v13
MM v[36:39],a[252:255],v[208:211],v[128:131]
TR a[164:165],v232 offset:25600
CV v9,v10,v11
MM v[36:39],a[248:251],v[192:195],v[36:39]
EX v14,v14
MM v[36:39],a[244:247],v[176:179],v[36:39]
EX v15,v15
MM v[36:39],a[240:243],v[160:163],v[36:39]
TR a[166:167],v232 offset:29696
TR a[168:169],v233 offset:17408
MM v[44:47],a[240:243],v[164:167],v[132:135]
EX v16,v16
MM v[44:47],a[244:247],v[180:183],v[44:47]
TR a[170:171],v233 offset:21504
CV v10,v12,v13
MM v[44:47],a[248:251],v[196:199],v[44:47]
EX v17,v17
MM v[44:47],a[252:255],v[212:215],v[44:47]
s_add_u32 m0, s89, 131072
s_nop 0
buffer_load_dwordx4 v252, s[24:27], s66 offen offset:1024 lds
MM v[40:43],a[236:239],v[212:215],v[132:135]
EX v18,v18
MM v[40:43],a[232:235],v[196:199],v[40:43]
TR a[172:173],v233 offset:25600
CV v11,v14,v15
MM v[40:43],a[228:231],v[180:183],v[40:43]
EX v19,v19
MM v[40:43],a[224:227],v[164:167],v[40:43]
TR a[174:175],v233 offset:29696
TR a[176:177],v232 offset:17920
MM v[48:51],a[224:227],v[168:171],v[136:139]
EX v20,v20
MM v[48:51],a[228:231],v[184:187],v[48:51]
TR a[178:179],v232 offset:22016
CV v16,v16,v17
MM v[48:51],a[232:235],v[200:203],v[48:51]
EX v21,v21
MM v[48:51],a[236:239],v[216:219],v[48:51]
s_lshl_b32 s60, s50, 2
TR a[180:181],v232 offset:26112
CV v17,v18,v19
MM v[52:55],a[252:255],v[216:219],v[136:139]
s_add_u32 s60, s60, %[swave]
EX v22,v22
MM v[52:55],a[248:251],v[200:203],v[52:55]
s_lshl_b32 s60, s60, 1
TR a[182:183],v232 offset:30208
TR a[184:185],v233 offset:17920
MM v[52:55],a[244:247],v[184:187],v[52:55]
s_bfe_u32 s61, s49, 65538
EX v23,v23
MM v[52:55],a[240:243],v[168:171],v[52:55]
s_xor_b32 s61, s61, 1
EX v24,v24
MM v[60:63],a[240:243],v[172:175],v[140:143]
s_add_u32 s60, s60, s61
TR a[186:187],v233 offset:22016
CV v18,v20,v21
MM v[60:63],a[244:247],v[188:191],v[60:63]
s_mul_i32 s18, s60, 40960
EX v25,v25
MM v[60:63],a[248:251],v[204:207],v[60:63]
s_mul_i32 s60, s84, 320
TR a[188:189],v233 offset:26112
TR a[190:191],v233 offset:30208
MM v[60:63],a[252:255],v[220:223],v[60:63]
s_add_u32 s18, s18, s60
EX v26,v26
MM v[56:59],a[236:239],v[220:223],v[140:143]
s_lshr_b32 s62, s49, 3
CV v19,v22,v23
CV v24,v24,v25
MM v[56:59],a[232:235],v[204:207],v[56:59]
s_mul_i32 s63, s62, 10240
EX v27,v27
MM v[56:59],a[228:231],v[188:191],v[56:59]
s_add_u32 s63, s63, s18
EX v28,v28
MM v[56:59],a[224:227],v[172:175],v[56:59]
s_waitcnt vmcnt(14) lgkmcnt(0)
s_barrier
MM a[0:3],a[128:131],v[64:67],a[0:3]
s_and_b32 s60, s84, 1
EX v29,v29
MM a[0:3],a[132:135],v[68:71],a[0:3]
s_add_u32 s61, s60, s62
CV v25,v26,v27
CV v26,v28,v29
MM a[8:11],a[132:135],v[76:79],a[8:11]
s_lshl_b32 s61, s61, 6
EX v30,v30
MM a[8:11],a[128:131],v[72:75],a[8:11]
s_add_u32 s18, s18, s61
EX v31,v31
MM a[16:19],a[128:131],v[80:83],a[16:19]
s_xor_b32 s60, s60, 1
RD a[192:195],v224 offset:49152
CV v27,v30,v31
MM a[16:19],a[132:135],v[84:87],a[16:19]
s_lshl_b32 s60, s60, 8
EX v32,v32
MM a[24:27],a[132:135],v[92:95],a[24:27]
s_add_u32 s63, s63, s60
s_add_u32 m0, s65, 16384
RD a[196:199],v225 offset:49152
MM a[24:27],a[128:131],v[88:91],a[24:27]
s_mul_i32 s60, s62, 33792
EX v33,v33
MM a[28:31],a[136:139],v[88:91],a[28:31]
s_sub_u32 s60, s95, s60
buffer_load_dwordx4 v228, s[8:11], s64 offen offset:0 lds
MM a[28:31],a[140:143],v[92:95],a[28:31]
s_xor_b32 s61, s62, 1
EX v34,v34
MM a[20:23],a[140:143],v[84:87],a[20:23]
s_mul_i32 s61, s61, 33792
EX v35,v35
MM a[20:23],a[136:139],v[80:83],a[20:23]
s_add_u32 s61, s61, s60
RD a[200:203],v226 offset:49152
CV v4,v32,v33
MM a[12:15],a[136:139],v[72:75],a[12:15]
s_add_u32 s36, s61, 2048
EX v36,v36
MM a[12:15],a[140:143],v[76:79],a[12:15]
s_xor_b32 s61, s62, 2
CV v5,v34,v35
MM a[4:7],a[140:143],v[68:71],a[4:7]
s_mul_i32 s61, s61, 33792
EX v37,v37
MM a[4:7],a[136:139],v[64:67],a[4:7]
s_add_u32 s61, s61, s60
buffer_load_dwordx4 v229, s[8:11], s64 offen offset:1024 lds
MM a[32:35],a[144:147],v[64:67],a[32:35]
s_add_u32 s37, s61, 4096
EX v38,v38
MM a[32:35],a[148:151],v[68:71],a[32:35]
s_xor_b32 s61, s62, 3
RD a[204:207],v227 offset:49152
CV v6,v36,v37
MM a[40:43],a[148:151],v[76:79],a[40:43]
s_mul_i32 s61, s61, 33792
EX v39,v39
MM a[40:43],a[144:147],v[72:75],a[40:43]
s_add_u32 s61, s61, s60
RD a[208:211],v224 offset:53248
CV v7,v38,v39
MM a[48:51],a[144:147],v[80:83],a[48:51]
s_add_u32 s38, s61, 6144
EX v40,v40
MM a[48:51],a[148:151],v[84:87],a[48:51]
s_lshl_b32 s61, s62, 9
EX v41,v41
MM a[56:59],a[148:151],v[92:95],a[56:59]
s_add_u32 s39, s60, s61
RD a[212:215],v225 offset:53248
MM a[56:59],a[144:147],v[88:91],a[56:59]
s_mov_b32 s17, 15
buffer_load_dwordx4 v230, s[8:11], s64 offen offset:2048 lds
MM a[60:63],a[152:155],v[88:91],a[60:63]
s_mov_b32 s58, 0
EX v42,v42
MM a[60:63],a[156:159],v[92:95],a[60:63]
s_lshl_b32 s60, s50, 1
EX v43,v43
MM a[52:55],a[156:159],v[84:87],a[52:55]
s_bfe_u32 s61, s49, 65538
RD a[216:219],v226 offset:53248
CV v12,v40,v41
MM a[52:55],a[152:155],v[80:83],a[52:55]
s_add_u32 s60, s60, s61
EX v44,v44
MM a[44:47],a[152:155],v[72:75],a[44:47]
s_lshl_b32 s60, s60, 2
CV v13,v42,v43
MM a[44:47],a[156:159],v[76:79],a[44:47]
s_add_u32 s60, s60, s84
EX v45,v45
MM a[36:39],a[156:159],v[68:71],a[36:39]
s_lshl_b32 s60, s60, 2
buffer_load_dwordx4 v231, s[8:11], s64 offen offset:3072 lds
MM a[36:39],a[152:155],v[64:67],a[36:39]
s_add_u32 s68, s60, %[swave]
EX v46,v46
MM a[64:67],a[160:163],v[64:67],a[64:67]
s_lshl_b32 s68, s68, 6
RD a[220:223],v227 offset:53248
CV v14,v44,v45
MM a[64:67],a[164:167],v[68:71],a[64:67]
s_add_u32 s68, s68, 16384
EX v47,v47
MM a[72:75],a[164:167],v[76:79],a[72:75]
s_lshr_b32 s61, s49, 3
EX v48,v48
MM a[72:75],a[160:163],v[72:75],a[72:75]
s_lshl_b32 s19, s61, 4
RD a[224:227],v224 offset:57344
CV v15,v46,v47
MM a[80:83],a[160:163],v[80:83],a[80:83]
s_add_u32 s19, s19, s68
EX v49,v49
MM a[80:83],a[164:167],v[84:87],a[80:83]
s_lshr_b32 s96, s49, 2
s_add_u32 m0, s65, 81920
CV v20,v48,v49
MM a[88:91],a[164:167],v[92:95],a[88:91]
s_lshl_b32 s97, s50, 3
EX v50,v50
MM a[88:91],a[160:163],v[88:91],a[88:91]
s_add_u32 s96, s96, s97
buffer_load_dwordx4 v235, s[12:15], s81 offen offset:0 lds
MM a[92:95],a[168:171],v[88:91],a[92:95]
s_lshl_b32 s96, s96, 2
EX v51,v51
MM a[92:95],a[172:175],v[92:95],a[92:95]
s_add_u32 s96, s96, %[swave]
RD a[228:231],v225 offset:57344
CV v21,v50,v51
MM a[84:87],a[172:175],v[84:87],a[84:87]
s_lshl_b32 s96, s96, 6
EX v52,v52
MM a[84:87],a[168:171],v[80:83],a[84:87]
s_add_u32 s96, s96, 0
RD a[232:235],v226 offset:57344
MM a[76:79],a[168:171],v[72:75],a[76:79]
s_xor_b32 s44, s96, 256
EX v53,v53
MM a[76:79],a[172:175],v[76:79],a[76:79]
s_xor_b32 s66, s94, 5
RD a[236:239],v227 offset:57344
CV v22,v52,v53
MM a[68:71],a[172:175],v[68:71],a[68:71]
s_mul_i32 s66, s66, 163840
EX v54,v54
MM a[68:71],a[168:171],v[64:67],a[68:71]
s_add_u32 s66, s66, s88
s_add_u32 m0, s89, 139264
s_nop 0
buffer_load_dwordx4 v252, s[24:27], s66 offen offset:0 lds
MM a[96:99],a[176:179],v[64:67],a[96:99]
s_add_u32 s66, s66, 19456
EX v55,v55
MM a[96:99],a[180:183],v[68:71],a[96:99]
s_add_u32 m0, s65, 81920
s_nop 0
buffer_load_dwordx4 v235, s[12:15], s64 offen offset:1024 lds
MM a[104:107],a[180:183],v[76:79],a[104:107]
EX v56,v56
MM a[104:107],a[176:179],v[72:75],a[104:107]
RD a[240:243],v224 offset:61440
CV v23,v54,v55
MM a[112:115],a[176:179],v[80:83],a[112:115]
EX v57,v57
MM a[112:115],a[180:183],v[84:87],a[112:115]
EX v58,v58
MM a[120:123],a[180:183],v[92:95],a[120:123]
RD a[244:247],v225 offset:61440
CV v28,v56,v57
MM a[120:123],a[176:179],v[88:91],a[120:123]
EX v59,v59
MM a[124:127],a[184:187],v[88:91],a[124:127]
v_add_f32 v244, v144, v145
v_add_f32 v245, v148, v149
MM a[124:127],a[188:191],v[92:95],a[124:127]
v_add_f32 v246, v152, v153
v_add_f32 v247, v156, v157
MM a[116:119],a[188:191],v[84:87],a[116:119]
s_add_u32 m0, s89, 139264
s_nop 0
buffer_load_dwordx4 v252, s[24:27], s66 offen offset:1024 lds
MM a[116:119],a[184:187],v[80:83],a[116:119]
EX v60,v60
MM a[108:111],a[184:187],v[72:75],a[108:111]
EX v61,v61
MM a[108:111],a[188:191],v[76:79],a[108:111]
s_add_u32 m0, s65, 81920
s_nop 0
buffer_load_dwordx4 v235, s[12:15], s81 offen offset:2048 lds
MM a[100:103],a[188:191],v[68:71],a[100:103]
EX v62,v62
MM a[100:103],a[184:187],v[64:67],a[100:103]
RD a[248:251],v226 offset:61440
CV v29,v58,v59
v_smfmac_f32_16x16x64_bf16 v[144:147], v[236:239], v[0:7], v234
RD a[252:255],v227 offset:61440
EX v63,v63
v_smfmac_f32_16x16x64_bf16 v[148:151], v[236:239], v[8:15], v234
CV v30,v60,v61
v_smfmac_f32_16x16x64_bf16 v[152:155], v[236:239], v[16:23], v234
CV v31,v62,v63
s_nop 1
v_smfmac_f32_16x16x64_bf16 v[156:159], v[236:239], v[24:31], v234
s_waitcnt lgkmcnt(0)
buffer_load_dwordx4 v235, s[12:15], s64 offen offset:3072 lds
MM v[64:67],a[192:195],v[160:163],v[128:131]
s_add_u32 s64, s64, 16384
TR a[128:129],v232 offset:32768
s_add_u32 s81, s64, 896
MM v[64:67],a[196:199],v[176:179],v[64:67]
TR a[130:131],v232 offset:36864
v_max3_f32 v249, v144, v145, v148
MM v[64:67],a[200:203],v[192:195],v[64:67]
TR a[132:133],v232 offset:40960
v_max3_f32 v249, v249, v149, v152
MM v[64:67],a[204:207],v[208:211],v[64:67]
TR a[134:135],v232 offset:45056
v_max3_f32 v249, v249, v153, v156
MM v[68:71],a[220:223],v[208:211],v[128:131]
TR a[136:137],v233 offset:32768
v_max_f32 v249, v249, v157
MM v[68:71],a[216:219],v[192:195],v[68:71]
TR a[138:139],v233 offset:36864
v_cmp_lt_f32 vcc, s71, v249
MM v[68:71],a[212:215],v[176:179],v[68:71]
TR a[140:141],v233 offset:40960
TR a[142:143],v233 offset:45056
MM v[68:71],a[208:211],v[160:163],v[68:71]
s_cbranch_vccnz LSITE34_%=
LBACK34_%=:
MM v[76:79],a[208:211],v[164:167],v[132:135]
buffer_load_dword v242, off, s[20:23], s96 sc1
MM v[76:79],a[212:215],v[180:183],v[76:79]
buffer_load_dword v243, off, s[20:23], s19 sc1
MM v[76:79],a[216:219],v[196:199],v[76:79]
s_xor_b32 s66, s94, 6
v_and_b32 v32, 496, v248
v_mul_u32_u24 v32, 80, v32
MM v[76:79],a[220:223],v[212:215],v[76:79]
s_mul_i32 s66, s66, 163840
EX v64,v64
MM v[72:75],a[204:207],v[212:215],v[132:135]
s_add_u32 s66, s66, s88
s_add_u32 m0, s89, 147456
)ASM"
R"ASM(
s_nop 0
buffer_load_dwordx4 v252, s[24:27], s66 offen offset:0 lds
MM v[72:75],a[200:203],v[196:199],v[72:75]
s_add_u32 s66, s66, 19456
EX v65,v65
MM v[72:75],a[196:199],v[180:183],v[72:75]
s_sub_u32 s60, s63, s18
v_add_u32 v32, s18, v32
CV v64,v64,v65
MM v[72:75],a[192:195],v[164:167],v[72:75]
s_mov_b32 s61, 131072
s_mov_b32 exec_lo, 0
v_add_u32 v32, s60, v32
s_mov_b64 exec, -1
MM v[80:83],a[192:195],v[168:171],v[136:139]
s_mov_b32 exec_hi, 0xff
buffer_load_dword v36, v32, s[32:35], 0 offen offset:0
s_mov_b64 exec, -1
EX v66,v66
MM v[80:83],a[196:199],v[184:187],v[80:83]
EX v67,v67
MM v[80:83],a[200:203],v[200:203],v[80:83]
EX v68,v68
MM v[80:83],a[204:207],v[216:219],v[80:83]
s_mov_b32 exec_hi, 0xff
buffer_load_dword v37, v32, s[20:23], s61 offen offset:0 sc1
s_mov_b64 exec, -1
MM v[84:87],a[220:223],v[216:219],v[136:139]
TR a[144:145],v232 offset:33280
TR a[146:147],v232 offset:37376
MM v[84:87],a[216:219],v[200:203],v[84:87]
s_add_u32 m0, s89, 147456
s_nop 0
buffer_load_dwordx4 v252, s[24:27], s66 offen offset:1024 lds
EX v69,v69
MM v[84:87],a[212:215],v[184:187],v[84:87]
EX v70,v70
MM v[84:87],a[208:211],v[168:171],v[84:87]
TR a[148:149],v232 offset:41472
CV v65,v66,v67
MM v[92:95],a[208:211],v[172:175],v[140:143]
EX v71,v71
MM v[92:95],a[212:215],v[188:191],v[92:95]
TR a[150:151],v232 offset:45568
TR a[152:153],v233 offset:33280
MM v[92:95],a[216:219],v[204:207],v[92:95]
EX v72,v72
MM v[92:95],a[220:223],v[220:223],v[92:95]
EX v73,v73
MM v[88:91],a[204:207],v[220:223],v[140:143]
TR a[154:155],v233 offset:37376
CV v66,v68,v69
MM v[88:91],a[200:203],v[204:207],v[88:91]
s_xor_b32 s66, s94, 7
EX v74,v74
MM v[88:91],a[196:199],v[188:191],v[88:91]
s_mul_i32 s66, s66, 163840
TR a[156:157],v233 offset:41472
CV v67,v70,v71
MM v[88:91],a[192:195],v[172:175],v[88:91]
s_add_u32 s66, s66, s88
s_add_u32 m0, s89, 155648
s_nop 0
buffer_load_dwordx4 v252, s[24:27], s66 offen offset:0 lds
MM v[96:99],a[224:227],v[160:163],v[128:131]
s_add_u32 s66, s66, 19456
EX v75,v75
MM v[96:99],a[228:231],v[176:179],v[96:99]
TR a[158:159],v233 offset:45568
TR a[160:161],v232 offset:33792
MM v[96:99],a[232:235],v[192:195],v[96:99]
EX v76,v76
MM v[96:99],a[236:239],v[208:211],v[96:99]
EX v77,v77
MM v[100:103],a[252:255],v[208:211],v[128:131]
TR a[162:163],v232 offset:37888
CV v72,v72,v73
MM v[100:103],a[248:251],v[192:195],v[100:103]
EX v78,v78
MM v[100:103],a[244:247],v[176:179],v[100:103]
TR a[164:165],v232 offset:41984
CV v73,v74,v75
MM v[100:103],a[240:243],v[160:163],v[100:103]
EX v79,v79
MM v[108:111],a[240:243],v[164:167],v[132:135]
s_add_u32 m0, s89, 155648
s_nop 0
buffer_load_dwordx4 v252, s[24:27], s66 offen offset:1024 lds
MM v[108:111],a[244:247],v[180:183],v[108:111]
TR a[166:167],v232 offset:46080
TR a[168:169],v233 offset:33792
MM v[108:111],a[248:251],v[196:199],v[108:111]
EX v80,v80
MM v[108:111],a[252:255],v[212:215],v[108:111]
EX v81,v81
MM v[104:107],a[236:239],v[212:215],v[132:135]
TR a[170:171],v233 offset:37888
CV v74,v76,v77
MM v[104:107],a[232:235],v[196:199],v[104:107]
EX v82,v82
MM v[104:107],a[228:231],v[180:183],v[104:107]
TR a[172:173],v233 offset:41984
TR a[174:175],v233 offset:46080
MM v[104:107],a[224:227],v[164:167],v[104:107]
EX v83,v83
MM v[112:115],a[224:227],v[168:171],v[136:139]
TR a[176:177],v232 offset:34304
CV v75,v78,v79
MM v[112:115],a[228:231],v[184:187],v[112:115]
EX v84,v84
MM v[112:115],a[232:235],v[200:203],v[112:115]
EX v85,v85
MM v[112:115],a[236:239],v[216:219],v[112:115]
TR a[178:179],v232 offset:38400
CV v80,v80,v81
MM v[116:119],a[252:255],v[216:219],v[136:139]
EX v86,v86
MM v[116:119],a[248:251],v[200:203],v[116:119]
TR a[180:181],v232 offset:42496
TR a[182:183],v232 offset:46592
MM v[116:119],a[244:247],v[184:187],v[116:119]
EX v87,v87
MM v[116:119],a[240:243],v[168:171],v[116:119]
TR a[184:185],v233 offset:34304
CV v81,v82,v83
MM v[124:127],a[240:243],v[172:175],v[140:143]
EX v88,v88
MM v[124:127],a[244:247],v[188:191],v[124:127]
TR a[186:187],v233 offset:38400
CV v82,v84,v85
MM v[124:127],a[248:251],v[204:207],v[124:127]
TR a[188:189],v233 offset:42496
TR a[190:191],v233 offset:46592
EX v89,v89
MM v[124:127],a[252:255],v[220:223],v[124:127]
EX v90,v90
MM v[120:123],a[236:239],v[220:223],v[140:143]
CV v83,v86,v87
CV v88,v88,v89
MM v[120:123],a[232:235],v[204:207],v[120:123]
EX v91,v91
MM v[120:123],a[228:231],v[188:191],v[120:123]
EX v92,v92
MM v[120:123],a[224:227],v[172:175],v[120:123]
s_waitcnt vmcnt(24) lgkmcnt(0)
s_barrier
MM a[0:3],a[128:131],v[0:3],a[0:3]
s_add_u32 m0, s65, 32768
EX v93,v93
MM a[0:3],a[132:135],v[4:7],a[0:3]
CV v89,v90,v91
CV v90,v92,v93
MM a[8:11],a[132:135],v[12:15],a[8:11]
EX v94,v94
MM a[8:11],a[128:131],v[8:11],a[8:11]
EX v95,v95
MM a[16:19],a[128:131],v[16:19],a[16:19]
RD a[192:195],v224 offset:0
LPG24_%=:
s_cbranch_execz LPG28_%=
CV v91,v94,v95
MM a[16:19],a[132:135],v[20:23],a[16:19]
EX v96,v96
MM a[24:27],a[132:135],v[28:31],a[24:27]
buffer_load_dwordx4 v228, s[8:11], s64 offen offset:0 lds
MM a[24:27],a[128:131],v[24:27],a[24:27]
EX v97,v97
MM a[28:31],a[136:139],v[24:27],a[28:31]
RD a[196:199],v225 offset:0
CV v68,v96,v97
MM a[28:31],a[140:143],v[28:31],a[28:31]
EX v98,v98
MM a[20:23],a[140:143],v[20:23],a[20:23]
EX v99,v99
MM a[20:23],a[136:139],v[16:19],a[20:23]
TR a[128:129],v232 offset:49152
RD a[200:203],v226 offset:0
MM a[12:15],a[136:139],v[8:11],a[12:15]
EX v100,v100
MM a[12:15],a[140:143],v[12:15],a[12:15]
TR a[130:131],v232 offset:53248
CV v69,v98,v99
MM a[4:7],a[140:143],v[4:7],a[4:7]
EX v101,v101
MM a[4:7],a[136:139],v[0:3],a[4:7]
TR a[132:133],v232 offset:57344
RD a[204:207],v227 offset:0
MM a[32:35],a[144:147],v[0:3],a[32:35]
EX v102,v102
MM a[32:35],a[148:151],v[4:7],a[32:35]
buffer_load_dwordx4 v229, s[8:11], s64 offen offset:1024 lds
MM a[40:43],a[148:151],v[12:15],a[40:43]
TR a[134:135],v232 offset:61440
CV v70,v100,v101
MM a[40:43],a[144:147],v[8:11],a[40:43]
EX v103,v103
MM a[48:51],a[144:147],v[16:19],a[48:51]
TR a[136:137],v233 offset:49152
TR a[138:139],v233 offset:53248
MM a[48:51],a[148:151],v[20:23],a[48:51]
EX v104,v104
MM a[56:59],a[148:151],v[28:31],a[56:59]
EX v105,v105
MM a[56:59],a[144:147],v[24:27],a[56:59]
TR a[140:141],v233 offset:57344
CV v71,v102,v103
MM a[60:63],a[152:155],v[24:27],a[60:63]
EX v106,v106
MM a[60:63],a[156:159],v[28:31],a[60:63]
RD a[208:211],v224 offset:4096
TR a[142:143],v233 offset:61440
MM a[52:55],a[156:159],v[20:23],a[52:55]
EX v107,v107
MM a[52:55],a[152:155],v[16:19],a[52:55]
buffer_load_dwordx4 v230, s[8:11], s64 offen offset:2048 lds
MM a[44:47],a[152:155],v[8:11],a[44:47]
TR a[144:145],v232 offset:49664
RD a[212:215],v225 offset:4096
MM a[44:47],a[156:159],v[12:15],a[44:47]
EX v108,v108
MM a[36:39],a[156:159],v[4:7],a[36:39]
TR a[146:147],v232 offset:53760
CV v76,v104,v105
MM a[36:39],a[152:155],v[0:3],a[36:39]
EX v109,v109
MM a[64:67],a[160:163],v[0:3],a[64:67]
TR a[148:149],v232 offset:57856
CV v77,v106,v107
MM a[64:67],a[164:167],v[4:7],a[64:67]
EX v110,v110
MM a[72:75],a[164:167],v[12:15],a[72:75]
EX v111,v111
MM a[72:75],a[160:163],v[8:11],a[72:75]
TR a[150:151],v232 offset:61952
RD a[216:219],v226 offset:4096
MM a[80:83],a[160:163],v[16:19],a[80:83]
TR a[152:153],v233 offset:49664
CV v78,v108,v109
MM a[80:83],a[164:167],v[20:23],a[80:83]
EX v112,v112
MM a[88:91],a[164:167],v[28:31],a[88:91]
EX v113,v113
MM a[88:91],a[160:163],v[24:27],a[88:91]
TR a[154:155],v233 offset:53760
RD a[220:223],v227 offset:4096
MM a[92:95],a[168:171],v[24:27],a[92:95]
buffer_load_dwordx4 v231, s[8:11], s64 offen offset:3072 lds
MM a[92:95],a[172:175],v[28:31],a[92:95]
s_add_u32 m0, s65, 98304
EX v114,v114
MM a[84:87],a[172:175],v[20:23],a[84:87]
TR a[156:157],v233 offset:57856
TR a[158:159],v233 offset:61952
MM a[84:87],a[168:171],v[16:19],a[84:87]
EX v115,v115
MM a[76:79],a[168:171],v[8:11],a[76:79]
EX v116,v116
MM a[76:79],a[172:175],v[12:15],a[76:79]
TR a[160:161],v232 offset:50176
CV v79,v110,v111
MM a[68:71],a[172:175],v[4:7],a[68:71]
EX v117,v117
MM a[68:71],a[168:171],v[0:3],a[68:71]
TR a[162:163],v232 offset:54272
RD a[224:227],v224 offset:8192
MM a[96:99],a[176:179],v[0:3],a[96:99]
EX v118,v118
MM a[96:99],a[180:183],v[4:7],a[96:99]
TR a[164:165],v232 offset:58368
CV v84,v112,v113
MM a[104:107],a[180:183],v[12:15],a[104:107]
buffer_load_dwordx4 v235, s[12:15], s81 offen offset:0 lds
TR a[166:167],v232 offset:62464
EX v119,v119
MM a[104:107],a[176:179],v[8:11],a[104:107]
RD a[228:231],v225 offset:8192
TR a[168:169],v233 offset:50176
MM a[112:115],a[176:179],v[16:19],a[112:115]
EX v120,v120
MM a[112:115],a[180:183],v[20:23],a[112:115]
TR a[170:171],v233 offset:54272
CV v85,v114,v115
MM a[120:123],a[180:183],v[28:31],a[120:123]
s_waitcnt vmcnt(8)
EX v121,v121
MM a[120:123],a[176:179],v[24:27],a[120:123]
s_lshr_b32 s77, %[sb], 3
RD a[232:235],v226 offset:8192
TR a[172:173],v233 offset:58368
MM a[124:127],a[184:187],v[24:27],a[124:127]
s_lshl_b32 s77, s77, 2
v_add_f32 v244, v144, v145
v_add_f32 v245, v148, v149
EX v122,v122
MM a[124:127],a[188:191],v[28:31],a[124:127]
s_add_u32 s77, s77, %[swave]
v_add_f32 v246, v152, v153
v_add_f32 v247, v156, v157
CV v86,v116,v117
MM a[116:119],a[188:191],v[20:23],a[116:119]
s_lshl_b32 s77, s77, 9
TR a[174:175],v233 offset:62464
buffer_load_dwordx4 v235, s[12:15], s64 offen offset:1024 lds
EX v123,v123
MM a[116:119],a[184:187],v[16:19],a[116:119]
s_add_u32 s77, s77, 0
TR a[176:177],v232 offset:50688
v_lshrrev_b32 v253, 1, v248
v_add_u32 v253, s77, v253
MM a[108:111],a[184:187],v[8:11],a[108:111]
s_mov_b32 s77, LKEND_ka-LPD2_%=-16
RD a[236:239],v227 offset:8192
v_min_u32 v253, s77, v253
EX v124,v124
CV v87,v118,v119
MM a[108:111],a[188:191],v[12:15],a[108:111]
s_getpc_b64 s[98:99]
LPD2_%=:
global_load_dword v253, v253, s[98:99]
TR a[178:179],v232 offset:54784
EX v125,v125
MM a[100:103],a[188:191],v[4:7],a[100:103]
TR a[180:181],v232 offset:58880
RD a[240:243],v224 offset:12288
MM a[100:103],a[184:187],v[0:3],a[100:103]
EX v126,v126
v_smfmac_f32_16x16x64_bf16 v[144:147], v[236:239], v[64:71], v234
RD a[244:247],v225 offset:12288
RD a[248:251],v226 offset:12288
RD a[252:255],v227 offset:12288
EX v127,v127
v_smfmac_f32_16x16x64_bf16 v[148:151], v[236:239], v[72:79], v234
TR a[182:183],v232 offset:62976
CV v92,v120,v121
v_smfmac_f32_16x16x64_bf16 v[152:155], v[236:239], v[80:87], v234
buffer_load_dwordx4 v235, s[12:15], s81 offen offset:2048 lds
CV v93,v122,v123
CV v94,v124,v125
CV v95,v126,v127
s_nop 1
v_smfmac_f32_16x16x64_bf16 v[156:159], v[236:239], v[88:95], v234
TR a[184:185],v233 offset:50688
TR a[186:187],v233 offset:54784
TR a[188:189],v233 offset:58880
TR a[190:191],v233 offset:62976
s_waitcnt lgkmcnt(5)
buffer_load_dwordx4 v235, s[12:15], s64 offen offset:3072 lds
MM v[16:19],a[192:195],v[168:171],v[136:139]
s_add_u32 s64, s64, 16384
s_add_u32 s81, s64, 896
MM v[16:19],a[196:199],v[184:187],v[16:19]
MM v[16:19],a[200:203],v[200:203],v[16:19]
v_max3_f32 v249, v144, v145, v148
v_max3_f32 v249, v249, v149, v152
MM v[16:19],a[204:207],v[216:219],v[16:19]
v_max3_f32 v249, v249, v153, v156
v_max_f32 v249, v249, v157
MM v[20:23],a[220:223],v[216:219],v[136:139]
v_cmp_lt_f32 vcc, s71, v249
MM v[20:23],a[216:219],v[200:203],v[20:23]
s_cbranch_vccnz LSITE35_%=
LBACK35_%=:
v_add_f32 v244, v144, v145
MM v[20:23],a[212:215],v[184:187],v[20:23]
v_add_f32 v245, v148, v149
v_add_f32 v246, v152, v153
MM v[20:23],a[208:211],v[168:171],v[20:23]
s_waitcnt lgkmcnt(0)
v_add_f32 v247, v156, v157
MM a[0:3],a[128:131],v[64:67],a[0:3]
EX v16,v16
MM a[0:3],a[132:135],v[68:71],a[0:3]
v_rcp_f32 v145, v244
MM a[8:11],a[132:135],v[76:79],a[8:11]
EX v17,v17
MM a[8:11],a[128:131],v[72:75],a[8:11]
EX v18,v18
MM a[12:15],a[136:139],v[72:75],a[12:15]
v_rcp_f32 v149, v245
MM a[12:15],a[140:143],v[76:79],a[12:15]
CV v16,v16,v17
v_bfe_u32 v144, v241, 0, 8
MM a[4:7],a[140:143],v[68:71],a[4:7]
EX v19,v19
MM a[4:7],a[136:139],v[64:67],a[4:7]
CV v17,v18,v19
v_lshl_or_b32 v144, v144, 1, 1
MM a[32:35],a[144:147],v[64:67],a[32:35]
EX v20,v20
MM a[32:35],a[148:151],v[68:71],a[32:35]
v_lshlrev_b32 v144, 22, v144
v_mul_f32 v144, -v144, v145
MM a[40:43],a[148:151],v[76:79],a[40:43]
)ASM"
R"ASM(
v_bfe_u32 v148, v241, 8, 8
v_lshl_or_b32 v148, v148, 1, 1
MM a[40:43],a[144:147],v[72:75],a[40:43]
v_lshlrev_b32 v148, 22, v148
v_mul_f32 v148, -v148, v149
MM a[44:47],a[152:155],v[72:75],a[44:47]
s_mul_i32 s78, s49, 17408
s_add_u32 s78, s83, s78
s_add_u32 s78, s78, 0
MM a[44:47],a[156:159],v[76:79],a[44:47]
s_add_u32 s79, s78, 4096
AR v0,a0
AR v1,a1
MM a[36:39],a[156:159],v[68:71],a[36:39]
AR v2,a2
AR v3,a3
MM a[36:39],a[152:155],v[64:67],a[36:39]
AR v4,a4
AR v5,a5
MM a[64:67],a[160:163],v[64:67],a[64:67]
AR v6,a6
AR v7,a7
MM a[64:67],a[164:167],v[68:71],a[64:67]
v_fma_f32 v0, v0, v145, v144
v_fma_f32 v1, v1, v145, v144
MM a[72:75],a[164:167],v[76:79],a[72:75]
v_fma_f32 v2, v2, v145, v144
v_fma_f32 v3, v3, v145, v144
MM a[72:75],a[160:163],v[72:75],a[72:75]
v_fma_f32 v4, v4, v145, v144
v_fma_f32 v5, v5, v145, v144
EX v21,v21
MM a[76:79],a[168:171],v[72:75],a[76:79]
v_fma_f32 v6, v6, v145, v144
v_fma_f32 v7, v7, v145, v144
CV v160,v0,v1
MM a[76:79],a[172:175],v[76:79],a[76:79]
CV v161,v2,v3
CV v162,v4,v5
EX v22,v22
MM a[68:71],a[172:175],v[68:71],a[68:71]
CV v163,v6,v7
CV v18,v20,v21
MM a[68:71],a[168:171],v[64:67],a[68:71]
buffer_store_dwordx4 v[160:163], v248, s[20:23], s78 offen offset:0 sc1
AR v8,a8
AR v9,a9
AR v10,a10
MM a[96:99],a[176:179],v[64:67],a[96:99]
AR v11,a11
AR v12,a12
EX v23,v23
MM a[96:99],a[180:183],v[68:71],a[96:99]
AR v13,a13
AR v14,a14
AR v15,a15
MM a[104:107],a[180:183],v[76:79],a[104:107]
v_fma_f32 v8, v8, v149, v148
v_fma_f32 v9, v9, v149, v148
MM a[104:107],a[176:179],v[72:75],a[104:107]
v_fma_f32 v10, v10, v149, v148
v_fma_f32 v11, v11, v149, v148
v_fma_f32 v12, v12, v149, v148
MM a[108:111],a[184:187],v[72:75],a[108:111]
v_fma_f32 v13, v13, v149, v148
v_fma_f32 v14, v14, v149, v148
MM a[108:111],a[188:191],v[76:79],a[108:111]
v_fma_f32 v15, v15, v149, v148
CV v164,v8,v9
CV v165,v10,v11
MM a[100:103],a[188:191],v[68:71],a[100:103]
CV v166,v12,v13
CV v167,v14,v15
MM a[100:103],a[184:187],v[64:67],a[100:103]
buffer_store_dwordx4 v[164:167], v248, s[20:23], s79 offen offset:0 sc1
AR v32,a32
MM v[28:31],a[208:211],v[172:175],v[140:143]
AR v33,a33
AR v34,a34
AR v35,a35
MM v[28:31],a[212:215],v[188:191],v[28:31]
AR v36,a36
AR v37,a37
MM v[28:31],a[216:219],v[204:207],v[28:31]
AR v38,a38
AR v39,a39
v_fma_f32 v32, v32, v145, v144
MM v[28:31],a[220:223],v[220:223],v[28:31]
v_fma_f32 v33, v33, v145, v144
v_fma_f32 v34, v34, v145, v144
MM v[24:27],a[204:207],v[220:223],v[140:143]
v_fma_f32 v35, v35, v145, v144
v_fma_f32 v36, v36, v145, v144
v_fma_f32 v37, v37, v145, v144
MM v[24:27],a[200:203],v[204:207],v[24:27]
v_fma_f32 v38, v38, v145, v144
v_fma_f32 v39, v39, v145, v144
MM v[24:27],a[196:199],v[188:191],v[24:27]
CV v176,v32,v33
CV v177,v34,v35
CV v178,v36,v37
CV v19,v22,v23
MM v[24:27],a[192:195],v[172:175],v[24:27]
CV v179,v38,v39
MM v[48:51],a[224:227],v[168:171],v[136:139]
buffer_store_dwordx4 v[176:179], v248, s[20:23], s78 offen offset:1024 sc1
AR v40,a40
AR v41,a41
MM v[48:51],a[228:231],v[184:187],v[48:51]
AR v42,a42
AR v43,a43
AR v44,a44
MM v[48:51],a[232:235],v[200:203],v[48:51]
AR v45,a45
AR v46,a46
MM v[48:51],a[236:239],v[216:219],v[48:51]
AR v47,a47
v_fma_f32 v40, v40, v149, v148
v_fma_f32 v41, v41, v149, v148
MM v[52:55],a[252:255],v[216:219],v[136:139]
EX v24,v24
EX v25,v25
EX v26,v26
EX v27,v27
EX v28,v28
EX v29,v29
v_fma_f32 v42, v42, v149, v148
EX v30,v30
v_fma_f32 v43, v43, v149, v148
MM v[52:55],a[248:251],v[200:203],v[52:55]
v_fma_f32 v44, v44, v149, v148
v_fma_f32 v45, v45, v149, v148
v_fma_f32 v46, v46, v149, v148
EX v31,v31
CV v24,v24,v25
MM v[52:55],a[244:247],v[184:187],v[52:55]
v_fma_f32 v47, v47, v149, v148
CV v180,v40,v41
CV v181,v42,v43
MM v[52:55],a[240:243],v[168:171],v[52:55]
CV v182,v44,v45
CV v183,v46,v47
MM v[60:63],a[240:243],v[172:175],v[140:143]
buffer_store_dwordx4 v[180:183], v248, s[20:23], s79 offen offset:1024 sc1
EX v48,v48
MM v[60:63],a[244:247],v[188:191],v[60:63]
AR v0,a64
AR v1,a65
AR v2,a66
CV v25,v26,v27
MM v[60:63],a[248:251],v[204:207],v[60:63]
AR v3,a67
AR v4,a68
AR v5,a69
MM v[60:63],a[252:255],v[220:223],v[60:63]
AR v6,a70
AR v7,a71
EX v49,v49
MM v[56:59],a[236:239],v[220:223],v[140:143]
v_fma_f32 v0, v0, v145, v144
v_fma_f32 v1, v1, v145, v144
v_fma_f32 v2, v2, v145, v144
MM v[56:59],a[232:235],v[204:207],v[56:59]
v_fma_f32 v3, v3, v145, v144
v_fma_f32 v4, v4, v145, v144
EX v50,v50
MM v[56:59],a[228:231],v[188:191],v[56:59]
v_fma_f32 v5, v5, v145, v144
v_fma_f32 v6, v6, v145, v144
v_fma_f32 v7, v7, v145, v144
CV v26,v28,v29
MM v[56:59],a[224:227],v[172:175],v[56:59]
s_waitcnt lgkmcnt(0)
s_barrier
CV v192,v0,v1
CV v193,v2,v3
MM a[16:19],a[128:131],v[80:83],a[16:19]
s_add_u32 m0, s65, 49152
CV v194,v4,v5
CV v195,v6,v7
EX v51,v51
MM a[16:19],a[132:135],v[84:87],a[16:19]
buffer_store_dwordx4 v[192:195], v248, s[20:23], s78 offen offset:2048 sc1
AR v8,a72
AR v9,a73
MM a[24:27],a[132:135],v[92:95],a[24:27]
AR v10,a74
AR v11,a75
MM a[24:27],a[128:131],v[88:91],a[24:27]
AR v12,a76
AR v13,a77
EX v52,v52
AR v14,a78
CV v27,v30,v31
MM a[28:31],a[136:139],v[88:91],a[28:31]
AR v15,a79
v_fma_f32 v8, v8, v149, v148
MM a[28:31],a[140:143],v[92:95],a[28:31]
v_fma_f32 v9, v9, v149, v148
v_fma_f32 v10, v10, v149, v148
v_fma_f32 v11, v11, v149, v148
EX v53,v53
MM a[20:23],a[140:143],v[84:87],a[20:23]
v_fma_f32 v12, v12, v149, v148
v_fma_f32 v13, v13, v149, v148
MM a[20:23],a[136:139],v[80:83],a[20:23]
v_fma_f32 v14, v14, v149, v148
v_fma_f32 v15, v15, v149, v148
CV v196,v8,v9
MM a[48:51],a[144:147],v[80:83],a[48:51]
RD a[192:195],v224 offset:16384
CV v197,v10,v11
CV v198,v12,v13
EX v54,v54
CV v199,v14,v15
MM a[48:51],a[148:151],v[84:87],a[48:51]
buffer_store_dwordx4 v[196:199], v248, s[20:23], s79 offen offset:2048 sc1
CV v20,v48,v49
MM a[56:59],a[148:151],v[92:95],a[56:59]
buffer_load_dwordx4 v228, s[8:11], s64 offen offset:0 lds
RD a[196:199],v225 offset:16384
AR v32,a96
AR v33,a97
EX v55,v55
MM a[56:59],a[144:147],v[88:91],a[56:59]
RD a[200:203],v226 offset:16384
AR v34,a98
AR v35,a99
AR v36,a100
MM a[60:63],a[152:155],v[88:91],a[60:63]
AR v37,a101
AR v38,a102
AR v39,a103
MM a[60:63],a[156:159],v[92:95],a[60:63]
buffer_load_dwordx4 v229, s[8:11], s64 offen offset:1024 lds
RD a[204:207],v227 offset:16384
v_fma_f32 v32, v32, v145, v144
v_fma_f32 v33, v33, v145, v144
EX v56,v56
MM a[52:55],a[156:159],v[84:87],a[52:55]
v_fma_f32 v34, v34, v145, v144
v_fma_f32 v35, v35, v145, v144
v_fma_f32 v36, v36, v145, v144
CV v21,v50,v51
MM a[52:55],a[152:155],v[80:83],a[52:55]
RD a[208:211],v224 offset:20480
v_fma_f32 v37, v37, v145, v144
v_fma_f32 v38, v38, v145, v144
MM a[80:83],a[160:163],v[80:83],a[80:83]
v_fma_f32 v39, v39, v145, v144
CV v208,v32,v33
EX v57,v57
CV v209,v34,v35
MM a[80:83],a[164:167],v[84:87],a[80:83]
buffer_load_dwordx4 v230, s[8:11], s64 offen offset:2048 lds
RD a[212:215],v225 offset:20480
CV v210,v36,v37
CV v211,v38,v39
MM a[88:91],a[164:167],v[92:95],a[88:91]
EX v58,v58
MM a[88:91],a[160:163],v[88:91],a[88:91]
buffer_store_dwordx4 v[208:211], v248, s[20:23], s78 offen offset:3072 sc1
RD a[216:219],v226 offset:20480
AR v40,a104
AR v41,a105
AR v42,a106
AR v43,a107
CV v22,v52,v53
MM a[92:95],a[168:171],v[88:91],a[92:95]
RD a[220:223],v227 offset:20480
AR v44,a108
AR v45,a109
MM a[92:95],a[172:175],v[92:95],a[92:95]
AR v46,a110
AR v47,a111
EX v59,v59
v_fma_f32 v40, v40, v149, v148
MM a[84:87],a[172:175],v[84:87],a[84:87]
buffer_load_dwordx4 v231, s[8:11], s64 offen offset:3072 lds
RD a[224:227],v224 offset:24576
v_fma_f32 v41, v41, v149, v148
v_fma_f32 v42, v42, v149, v148
MM a[84:87],a[168:171],v[80:83],a[84:87]
s_add_u32 m0, s65, 114688
v_fma_f32 v43, v43, v149, v148
v_fma_f32 v44, v44, v149, v148
v_fma_f32 v45, v45, v149, v148
MM a[112:115],a[176:179],v[80:83],a[112:115]
RD a[228:231],v225 offset:24576
v_fma_f32 v46, v46, v149, v148
v_fma_f32 v47, v47, v149, v148
EX v60,v60
CV v23,v54,v55
MM a[112:115],a[180:183],v[84:87],a[112:115]
buffer_load_dwordx4 v235, s[12:15], s81 offen offset:0 lds
CV v212,v40,v41
CV v213,v42,v43
CV v214,v44,v45
MM a[120:123],a[180:183],v[92:95],a[120:123]
RD a[232:235],v226 offset:24576
CV v215,v46,v47
EX v61,v61
MM a[120:123],a[176:179],v[88:91],a[120:123]
RD a[236:239],v227 offset:24576
CV v28,v56,v57
MM a[124:127],a[184:187],v[88:91],a[124:127]
buffer_store_dwordx4 v[212:215], v248, s[20:23], s79 offen offset:3072 sc1
MM a[124:127],a[188:191],v[92:95],a[124:127]
EX v62,v62
MM a[116:119],a[188:191],v[84:87],a[116:119]
RD a[240:243],v224 offset:28672
RD a[244:247],v225 offset:28672
RD a[248:251],v226 offset:28672
RD a[252:255],v227 offset:28672
EX v63,v63
MM a[116:119],a[184:187],v[80:83],a[116:119]
buffer_load_dwordx4 v235, s[12:15], s64 offen offset:1024 lds
v_smfmac_f32_16x16x64_bf16 v[152:155], v[236:239], v[16:23], v234
CV v29,v58,v59
CV v30,v60,v61
CV v31,v62,v63
s_nop 1
v_smfmac_f32_16x16x64_bf16 v[156:159], v[236:239], v[24:31], v234
s_waitcnt lgkmcnt(0)
buffer_load_dwordx4 v235, s[12:15], s81 offen offset:2048 lds
buffer_load_dwordx4 v235, s[12:15], s64 offen offset:3072 lds
MM v[80:83],a[192:195],v[168:171],v[136:139]
TR a[128:129],v232 offset:0
TR a[130:131],v232 offset:4096
s_nop 0
LPG25_%=:
s_cbranch_execz LPG29_%=
MM v[80:83],a[196:199],v[184:187],v[80:83]
TR a[132:133],v232 offset:8192
TR a[134:135],v232 offset:12288
MM v[80:83],a[200:203],v[200:203],v[80:83]
TR a[136:137],v233 offset:0
TR a[138:139],v233 offset:4096
MM v[80:83],a[204:207],v[216:219],v[80:83]
v_max3_f32 v249, v152, v153, v156
v_max_f32 v249, v249, v157
MM v[84:87],a[220:223],v[216:219],v[136:139]
TR a[140:141],v233 offset:8192
v_cmp_lt_f32 vcc, s71, v249
MM v[84:87],a[216:219],v[200:203],v[84:87]
s_cbranch_vccnz LSITE36_%=
LBACK36_%=:
TR a[142:143],v233 offset:12288
v_add_f32 v246, v152, v153
MM v[84:87],a[212:215],v[184:187],v[84:87]
TR a[144:145],v232 offset:512
v_add_f32 v247, v156, v157
MM v[84:87],a[208:211],v[168:171],v[84:87]
TR a[146:147],v232 offset:4608
TR a[148:149],v232 offset:8704
MM v[92:95],a[208:211],v[172:175],v[140:143]
TR a[150:151],v232 offset:12800
TR a[152:153],v233 offset:512
MM v[92:95],a[212:215],v[188:191],v[92:95]
EX v80,v80
MM v[92:95],a[216:219],v[204:207],v[92:95]
EX v81,v81
MM v[92:95],a[220:223],v[220:223],v[92:95]
TR a[154:155],v233 offset:4608
TR a[156:157],v233 offset:8704
MM v[88:91],a[204:207],v[220:223],v[140:143]
TR a[158:159],v233 offset:12800
CV v80,v80,v81
MM v[88:91],a[200:203],v[204:207],v[88:91]
EX v82,v82
MM v[88:91],a[196:199],v[188:191],v[88:91]
TR a[160:161],v232 offset:1024
TR a[162:163],v232 offset:5120
MM v[88:91],a[192:195],v[172:175],v[88:91]
EX v83,v83
MM v[112:115],a[224:227],v[168:171],v[136:139]
TR a[164:165],v232 offset:9216
TR a[166:167],v232 offset:13312
MM v[112:115],a[228:231],v[184:187],v[112:115]
EX v84,v84
MM v[112:115],a[232:235],v[200:203],v[112:115]
TR a[168:169],v233 offset:1024
TR a[170:171],v233 offset:5120
MM v[112:115],a[236:239],v[216:219],v[112:115]
EX v85,v85
MM v[116:119],a[252:255],v[216:219],v[136:139]
TR a[172:173],v233 offset:9216
TR a[174:175],v233 offset:13312
MM v[116:119],a[248:251],v[200:203],v[116:119]
EX v86,v86
MM v[116:119],a[244:247],v[184:187],v[116:119]
TR a[176:177],v232 offset:1536
TR a[178:179],v232 offset:5632
MM v[116:119],a[240:243],v[168:171],v[116:119]
EX v87,v87
MM v[124:127],a[240:243],v[172:175],v[140:143]
TR a[180:181],v232 offset:9728
TR a[182:183],v232 offset:13824
MM v[124:127],a[244:247],v[188:191],v[124:127]
EX v88,v88
MM v[124:127],a[248:251],v[204:207],v[124:127]
TR a[184:185],v233 offset:1536
TR a[186:187],v233 offset:5632
TR a[188:189],v233 offset:9728
TR a[190:191],v233 offset:13824
EX v89,v89
MM v[124:127],a[252:255],v[220:223],v[124:127]
CV v81,v82,v83
CV v82,v84,v85
MM v[120:123],a[236:239],v[220:223],v[140:143]
EX v90,v90
MM v[120:123],a[232:235],v[204:207],v[120:123]
EX v91,v91
MM v[120:123],a[228:231],v[188:191],v[120:123]
CV v83,v86,v87
CV v88,v88,v89
MM v[120:123],a[224:227],v[172:175],v[120:123]
s_waitcnt vmcnt(21) lgkmcnt(0)
s_barrier
CV v89,v90,v91
EX v92,v92
MM a[16:19],a[128:131],v[16:19],a[16:19]
s_xor_b32 s66, s94, 4
EX v93,v93
MM a[16:19],a[132:135],v[20:23],a[16:19]
)ASM"
R"ASM(
s_mul_i32 s66, s66, 163840
EX v94,v94
MM a[24:27],a[132:135],v[28:31],a[24:27]
s_add_u32 s66, s66, s87
EX v95,v95
MM a[24:27],a[128:131],v[24:27],a[24:27]
s_add_u32 m0, s65, 65536
RD a[192:195],v224 offset:32768
CV v90,v92,v93
MM a[28:31],a[136:139],v[24:27],a[28:31]
EX v112,v112
MM a[28:31],a[140:143],v[28:31],a[28:31]
buffer_load_dwordx4 v240, s[24:27], s66 offen offset:0 lds
MM a[20:23],a[140:143],v[20:23],a[20:23]
s_add_u32 s66, s66, 9216
EX v113,v113
MM a[20:23],a[136:139],v[16:19],a[20:23]
RD a[196:199],v225 offset:32768
CV v91,v94,v95
MM a[48:51],a[144:147],v[16:19],a[48:51]
EX v114,v114
MM a[48:51],a[148:151],v[20:23],a[48:51]
RD a[200:203],v226 offset:32768
RD a[204:207],v227 offset:32768
MM a[56:59],a[148:151],v[28:31],a[56:59]
EX v115,v115
MM a[56:59],a[144:147],v[24:27],a[56:59]
buffer_load_dwordx4 v240, s[24:27], s66 offen offset:1024 lds
MM a[60:63],a[152:155],v[24:27],a[60:63]
s_add_u32 s66, s66, 9216
EX v116,v116
MM a[60:63],a[156:159],v[28:31],a[60:63]
RD a[208:211],v224 offset:36864
CV v84,v112,v113
MM a[52:55],a[156:159],v[20:23],a[52:55]
EX v117,v117
MM a[52:55],a[152:155],v[16:19],a[52:55]
buffer_load_dwordx4 v240, s[24:27], s66 offen offset:2048 lds
MM a[80:83],a[160:163],v[16:19],a[80:83]
s_add_u32 s66, s66, 9216
EX v118,v118
MM a[80:83],a[164:167],v[20:23],a[80:83]
RD a[212:215],v225 offset:36864
RD a[216:219],v226 offset:36864
MM a[88:91],a[164:167],v[28:31],a[88:91]
s_lshl_b32 s97, s84, 2
s_add_u32 s97, s97, s96
s_add_u32 s97, s97, 0
EX v119,v119
MM a[88:91],a[160:163],v[24:27],a[88:91]
s_waitcnt vmcnt(6)
v_mov_b32 v242, s69
s_mov_b64 exec, 1
buffer_store_dword v242, off, s[20:23], s97 offset:0 sc1
s_mov_b64 exec, -1
buffer_load_dwordx4 v240, s[24:27], s66 offen offset:3072 lds
EX v120,v120
MM a[92:95],a[168:171],v[24:27],a[92:95]
s_xor_b32 s66, s94, 4
RD a[220:223],v227 offset:36864
CV v85,v114,v115
MM a[92:95],a[172:175],v[28:31],a[92:95]
s_mul_i32 s66, s66, 163840
EX v121,v121
MM a[84:87],a[172:175],v[20:23],a[84:87]
s_add_u32 s66, s66, s86
RD a[224:227],v224 offset:40960
CV v86,v116,v117
MM a[84:87],a[168:171],v[16:19],a[84:87]
s_add_u32 m0, s65, 0
EX v122,v122
MM a[112:115],a[176:179],v[16:19],a[112:115]
buffer_load_dwordx4 v240, s[24:27], s66 offen offset:0 lds
RD a[228:231],v225 offset:40960
EX v123,v123
MM a[112:115],a[180:183],v[20:23],a[112:115]
s_add_u32 s66, s66, 9216
RD a[232:235],v226 offset:40960
CV v87,v118,v119
MM a[120:123],a[180:183],v[28:31],a[120:123]
EX v124,v124
MM a[120:123],a[176:179],v[24:27],a[120:123]
buffer_load_dwordx4 v240, s[24:27], s66 offen offset:1024 lds
MM a[124:127],a[184:187],v[24:27],a[124:127]
s_add_u32 s66, s66, 9216
RD a[236:239],v227 offset:40960
EX v125,v125
MM a[124:127],a[188:191],v[28:31],a[124:127]
EX v126,v126
MM a[116:119],a[188:191],v[20:23],a[116:119]
RD a[240:243],v224 offset:45056
RD a[244:247],v225 offset:45056
RD a[248:251],v226 offset:45056
RD a[252:255],v227 offset:45056
EX v127,v127
MM a[116:119],a[184:187],v[16:19],a[116:119]
buffer_load_dwordx4 v240, s[24:27], s66 offen offset:2048 lds
CV v92,v120,v121
v_smfmac_f32_16x16x64_bf16 v[152:155], v[236:239], v[80:87], v234
s_add_u32 s66, s66, 9216
CV v93,v122,v123
CV v94,v124,v125
CV v95,v126,v127
s_nop 1
v_smfmac_f32_16x16x64_bf16 v[156:159], v[236:239], v[88:95], v234
s_waitcnt lgkmcnt(0)
buffer_load_dwordx4 v240, s[24:27], s66 offen offset:3072 lds
v_lshrrev_b32 v145, 9, v248
v_mul_u32_u24 v145, 1536, v145
v_sub_u32 v244, v232, v145
v_sub_u32 v235, v233, v145
v_add_u32 v244, 65536, v244
v_add_u32 v235, 65536, v235
v_and_b32 v145, 960, v248
v_lshlrev_b32 v145, 1, v145
v_bfe_u32 v146, v248, 7, 2
v_lshl_add_u32 v145, v146, 5, v145
v_bfe_u32 v146, v248, 4, 2
v_lshl_add_u32 v145, v146, 3, v145
v_add_u32 v228, %[slds], v145
v_xor_b32 v229, 32, v228
v_xor_b32 v230, 64, v228
v_xor_b32 v231, 96, v228
v_add_u32 v144, 65536, v228
v_add_u32 v145, 65536, v229
v_add_u32 v146, 65536, v230
v_add_u32 v147, 65536, v231
MM v[16:19],a[192:195],v[168:171],v[136:139]
TR a[128:129],v232 offset:16384
TR a[130:131],v232 offset:20480
MM v[16:19],a[196:199],v[184:187],v[16:19]
TR a[132:133],v232 offset:24576
TR a[134:135],v232 offset:28672
MM v[16:19],a[200:203],v[200:203],v[16:19]
TR a[136:137],v233 offset:16384
TR a[138:139],v233 offset:20480
MM v[16:19],a[204:207],v[216:219],v[16:19]
v_max3_f32 v249, v152, v153, v156
v_max_f32 v249, v249, v157
MM v[20:23],a[220:223],v[216:219],v[136:139]
TR a[140:141],v233 offset:24576
v_cmp_lt_f32 vcc, s71, v249
MM v[20:23],a[216:219],v[200:203],v[20:23]
s_cbranch_vccnz LSITE37_%=
LBACK37_%=:
TR a[142:143],v233 offset:28672
v_add_f32 v246, v152, v153
MM v[20:23],a[212:215],v[184:187],v[20:23]
TR a[144:145],v232 offset:16896
v_add_f32 v247, v156, v157
MM v[20:23],a[208:211],v[168:171],v[20:23]
TR a[146:147],v232 offset:20992
TR a[148:149],v232 offset:25088
MM v[28:31],a[208:211],v[172:175],v[140:143]
TR a[150:151],v232 offset:29184
TR a[152:153],v233 offset:16896
MM v[28:31],a[212:215],v[188:191],v[28:31]
EX v16,v16
MM v[28:31],a[216:219],v[204:207],v[28:31]
v_lshrrev_b32 v98, 1, v248
v_min_u32 v98, 312, v98
MM v[28:31],a[220:223],v[220:223],v[28:31]
s_mov_b32 s62, 94464
buffer_load_dwordx2 v[96:97], v98, s[20:23], s62 offen offset:0 sc1
MM v[24:27],a[204:207],v[220:223],v[140:143]
EX v17,v17
MM v[24:27],a[200:203],v[204:207],v[24:27]
TR a[154:155],v233 offset:20992
TR a[156:157],v233 offset:25088
MM v[24:27],a[196:199],v[188:191],v[24:27]
TR a[158:159],v233 offset:29184
TR a[160:161],v232 offset:17408
MM v[24:27],a[192:195],v[172:175],v[24:27]
EX v18,v18
MM v[48:51],a[224:227],v[168:171],v[136:139]
EX v19,v19
MM v[48:51],a[228:231],v[184:187],v[48:51]
TR a[162:163],v232 offset:21504
TR a[164:165],v232 offset:25600
MM v[48:51],a[232:235],v[200:203],v[48:51]
TR a[166:167],v232 offset:29696
TR a[168:169],v233 offset:17408
EX v20,v20
MM v[48:51],a[236:239],v[216:219],v[48:51]
TR a[170:171],v233 offset:21504
TR a[172:173],v233 offset:25600
MM v[52:55],a[252:255],v[216:219],v[136:139]
EX v21,v21
MM v[52:55],a[248:251],v[200:203],v[52:55]
TR a[174:175],v233 offset:29696
CV v16,v16,v17
MM v[52:55],a[244:247],v[184:187],v[52:55]
TR a[176:177],v232 offset:17920
EX v22,v22
MM v[52:55],a[240:243],v[168:171],v[52:55]
TR a[178:179],v232 offset:22016
TR a[180:181],v232 offset:26112
EX v23,v23
MM v[60:63],a[240:243],v[172:175],v[140:143]
TR a[182:183],v232 offset:30208
CV v17,v18,v19
MM v[60:63],a[244:247],v[188:191],v[60:63]
EX v24,v24
MM v[60:63],a[248:251],v[204:207],v[60:63]
TR a[184:185],v233 offset:17920
TR a[186:187],v233 offset:22016
TR a[188:189],v233 offset:26112
TR a[190:191],v233 offset:30208
EX v25,v25
MM v[60:63],a[252:255],v[220:223],v[60:63]
EX v26,v26
MM v[56:59],a[236:239],v[220:223],v[140:143]
EX v27,v27
MM v[56:59],a[232:235],v[204:207],v[56:59]
EX v28,v28
MM v[56:59],a[228:231],v[188:191],v[56:59]
EX v29,v29
CV v18,v20,v21
MM v[56:59],a[224:227],v[172:175],v[56:59]
s_waitcnt lgkmcnt(0)
s_barrier
CV v19,v22,v23
MM a[16:19],a[128:131],v[80:83],a[16:19]
s_xor_b32 s66, s94, 5
CV v24,v24,v25
CV v25,v26,v27
MM a[16:19],a[132:135],v[84:87],a[16:19]
s_mul_i32 s66, s66, 163840
EX v30,v30
MM a[24:27],a[132:135],v[92:95],a[24:27]
s_add_u32 s66, s66, s87
v_lshrrev_b32 v243, 2, v248
v_min_u32 v243, 12, v243
MM a[24:27],a[128:131],v[88:91],a[24:27]
s_add_u32 m0, s65, 81920
buffer_load_dword v242, v243, s[20:23], s44 offen offset:0 sc1
MM a[28:31],a[136:139],v[88:91],a[28:31]
EX v31,v31
MM a[28:31],a[140:143],v[92:95],a[28:31]
EX v48,v48
MM a[20:23],a[140:143],v[84:87],a[20:23]
RD a[192:195],v224 offset:49152
CV v26,v28,v29
MM a[20:23],a[136:139],v[80:83],a[20:23]
EX v49,v49
MM a[48:51],a[144:147],v[80:83],a[48:51]
buffer_load_dwordx4 v240, s[24:27], s66 offen offset:0 lds
MM a[48:51],a[148:151],v[84:87],a[48:51]
s_add_u32 s66, s66, 9216
EX v50,v50
MM a[56:59],a[148:151],v[92:95],a[56:59]
RD a[196:199],v225 offset:49152
RD a[200:203],v226 offset:49152
MM a[56:59],a[144:147],v[88:91],a[56:59]
EX v51,v51
MM a[60:63],a[152:155],v[88:91],a[60:63]
buffer_load_dwordx4 v240, s[24:27], s66 offen offset:1024 lds
MM a[60:63],a[156:159],v[92:95],a[60:63]
s_add_u32 s66, s66, 9216
RD a[204:207],v227 offset:49152
EX v52,v52
MM a[52:55],a[156:159],v[84:87],a[52:55]
EX v53,v53
CV v27,v30,v31
MM a[52:55],a[152:155],v[80:83],a[52:55]
RD a[208:211],v224 offset:53248
RD a[212:215],v225 offset:53248
buffer_load_dwordx4 v240, s[24:27], s66 offen offset:2048 lds
MM a[80:83],a[160:163],v[80:83],a[80:83]
s_add_u32 s66, s66, 9216
EX v54,v54
MM a[80:83],a[164:167],v[84:87],a[80:83]
RD a[216:219],v226 offset:53248
CV v20,v48,v49
MM a[88:91],a[164:167],v[92:95],a[88:91]
EX v55,v55
MM a[88:91],a[160:163],v[88:91],a[88:91]
buffer_load_dwordx4 v240, s[24:27], s66 offen offset:3072 lds
EX v56,v56
MM a[92:95],a[168:171],v[88:91],a[92:95]
s_xor_b32 s66, s94, 5
RD a[220:223],v227 offset:53248
CV v21,v50,v51
MM a[92:95],a[172:175],v[92:95],a[92:95]
s_mul_i32 s66, s66, 163840
EX v57,v57
MM a[84:87],a[172:175],v[84:87],a[84:87]
s_add_u32 s66, s66, s86
RD a[224:227],v224 offset:57344
CV v22,v52,v53
MM a[84:87],a[168:171],v[80:83],a[84:87]
s_add_u32 m0, s65, 16384
EX v58,v58
MM a[112:115],a[176:179],v[80:83],a[112:115]
buffer_load_dwordx4 v240, s[24:27], s66 offen offset:0 lds
RD a[228:231],v225 offset:57344
EX v59,v59
MM a[112:115],a[180:183],v[84:87],a[112:115]
s_add_u32 s66, s66, 9216
RD a[232:235],v226 offset:57344
CV v23,v54,v55
MM a[120:123],a[180:183],v[92:95],a[120:123]
EX v60,v60
MM a[120:123],a[176:179],v[88:91],a[120:123]
buffer_load_dwordx4 v240, s[24:27], s66 offen offset:1024 lds
MM a[124:127],a[184:187],v[88:91],a[124:127]
s_add_u32 s66, s66, 9216
RD a[236:239],v227 offset:57344
EX v61,v61
MM a[124:127],a[188:191],v[92:95],a[124:127]
EX v62,v62
MM a[116:119],a[188:191],v[84:87],a[116:119]
RD a[240:243],v224 offset:61440
RD a[244:247],v225 offset:61440
RD a[248:251],v226 offset:61440
RD a[252:255],v227 offset:61440
EX v63,v63
MM a[116:119],a[184:187],v[80:83],a[116:119]
buffer_load_dwordx4 v240, s[24:27], s66 offen offset:2048 lds
CV v28,v56,v57
v_smfmac_f32_16x16x64_bf16 v[152:155], v[236:239], v[16:23], v234
s_add_u32 s66, s66, 9216
CV v29,v58,v59
CV v30,v60,v61
CV v31,v62,v63
s_nop 1
v_smfmac_f32_16x16x64_bf16 v[156:159], v[236:239], v[24:31], v234
s_waitcnt lgkmcnt(0)
buffer_load_dwordx4 v240, s[24:27], s66 offen offset:3072 lds
MM v[80:83],a[192:195],v[168:171],v[136:139]
TR a[128:129],v232 offset:32768
TR a[130:131],v232 offset:36864
MM v[80:83],a[196:199],v[184:187],v[80:83]
TR a[132:133],v232 offset:40960
TR a[134:135],v232 offset:45056
MM v[80:83],a[200:203],v[200:203],v[80:83]
TR a[136:137],v233 offset:32768
TR a[138:139],v233 offset:36864
MM v[80:83],a[204:207],v[216:219],v[80:83]
v_max3_f32 v249, v152, v153, v156
v_max_f32 v249, v249, v157
MM v[84:87],a[220:223],v[216:219],v[136:139]
TR a[140:141],v233 offset:40960
v_cmp_lt_f32 vcc, s71, v249
MM v[84:87],a[216:219],v[200:203],v[84:87]
s_cbranch_vccnz LSITE38_%=
LBACK38_%=:
TR a[142:143],v233 offset:45056
v_add_f32 v246, v152, v153
MM v[84:87],a[212:215],v[184:187],v[84:87]
TR a[144:145],v232 offset:33280
v_add_f32 v247, v156, v157
MM v[84:87],a[208:211],v[168:171],v[84:87]
TR a[146:147],v232 offset:37376
TR a[148:149],v232 offset:41472
MM v[92:95],a[208:211],v[172:175],v[140:143]
s_waitcnt vmcnt(8)
v_cmp_ne_u32 vcc, s69, v242
s_cbranch_vccnz LRPO0_%=
LRPO0b_%=:
MM v[92:95],a[212:215],v[188:191],v[92:95]
s_xor_b32 s92, s49, 4
s_mul_i32 s92, s92, 17408
s_add_u32 s92, s83, s92
MM v[92:95],a[216:219],v[204:207],v[92:95]
buffer_load_dwordx4 a[0:3], v248, s[20:23], s92 offen offset:0 sc1
MM v[92:95],a[220:223],v[220:223],v[92:95]
buffer_load_dwordx4 a[32:35], v248, s[20:23], s92 offen offset:1024 sc1
MM v[88:91],a[204:207],v[220:223],v[140:143]
TR a[150:151],v232 offset:45568
v_xor_b32 v96, s69, v96
v_xor_b32 v97, s69, v97
EX v80,v80
MM v[88:91],a[200:203],v[204:207],v[88:91]
TR a[152:153],v233 offset:33280
TR a[154:155],v233 offset:37376
v_or_b32 v99, v96, v97
EX v81,v81
MM v[88:91],a[196:199],v[188:191],v[88:91]
buffer_load_dwordx4 a[64:67], v248, s[20:23], s92 offen offset:2048 sc1
TR a[156:157],v233 offset:41472
)ASM"
R"ASM(
MM v[88:91],a[192:195],v[172:175],v[88:91]
TR a[158:159],v233 offset:45568
v_cmp_ne_u32 vcc, 0, v99
s_cbranch_vccnz LRPW_%=
LRPWb_%=:
EX v82,v82
MM v[112:115],a[224:227],v[168:171],v[136:139]
TR a[160:161],v232 offset:33792
TR a[162:163],v232 offset:37888
EX v83,v83
MM v[112:115],a[228:231],v[184:187],v[112:115]
buffer_load_dwordx4 a[96:99], v248, s[20:23], s92 offen offset:3072 sc1
TR a[164:165],v232 offset:41984
MM v[112:115],a[232:235],v[200:203],v[112:115]
s_add_u32 s92, s92, 4096
TR a[166:167],v232 offset:46080
TR a[168:169],v233 offset:33792
EX v84,v84
MM v[112:115],a[236:239],v[216:219],v[112:115]
TR a[170:171],v233 offset:37888
TR a[172:173],v233 offset:41984
MM v[116:119],a[252:255],v[216:219],v[136:139]
buffer_load_dwordx4 a[4:7], v248, s[20:23], s92 offen offset:0 sc1
EX v85,v85
MM v[116:119],a[248:251],v[200:203],v[116:119]
TR a[174:175],v233 offset:46080
CV v80,v80,v81
MM v[116:119],a[244:247],v[184:187],v[116:119]
TR a[176:177],v232 offset:34304
EX v86,v86
MM v[116:119],a[240:243],v[168:171],v[116:119]
buffer_load_dwordx4 a[36:39], v248, s[20:23], s92 offen offset:1024 sc1
TR a[178:179],v232 offset:38400
EX v87,v87
MM v[124:127],a[240:243],v[172:175],v[140:143]
TR a[180:181],v232 offset:42496
TR a[182:183],v232 offset:46592
MM v[124:127],a[244:247],v[188:191],v[124:127]
EX v88,v88
MM v[124:127],a[248:251],v[204:207],v[124:127]
buffer_load_dwordx4 a[68:71], v248, s[20:23], s92 offen offset:2048 sc1
TR a[184:185],v233 offset:34304
TR a[186:187],v233 offset:38400
TR a[188:189],v233 offset:42496
TR a[190:191],v233 offset:46592
EX v89,v89
MM v[124:127],a[252:255],v[220:223],v[124:127]
EX v90,v90
CV v81,v82,v83
MM v[120:123],a[236:239],v[220:223],v[140:143]
EX v91,v91
MM v[120:123],a[232:235],v[204:207],v[120:123]
buffer_load_dwordx4 a[100:103], v248, s[20:23], s92 offen offset:3072 sc1
MM v[120:123],a[228:231],v[188:191],v[120:123]
EX v92,v92
CV v82,v84,v85
MM v[120:123],a[224:227],v[172:175],v[120:123]
s_waitcnt lgkmcnt(0)
s_barrier
CV v83,v86,v87
MM a[16:19],a[128:131],v[16:19],a[16:19]
s_xor_b32 s66, s94, 6
EX v93,v93
MM a[16:19],a[132:135],v[20:23],a[16:19]
s_mul_i32 s66, s66, 163840
CV v88,v88,v89
CV v89,v90,v91
MM a[24:27],a[132:135],v[28:31],a[24:27]
s_add_u32 s66, s66, s87
EX v94,v94
MM a[24:27],a[128:131],v[24:27],a[24:27]
s_add_u32 m0, s65, 98304
EX v95,v95
MM a[28:31],a[136:139],v[24:27],a[28:31]
EX v112,v112
MM a[28:31],a[140:143],v[28:31],a[28:31]
CV v90,v92,v93
CV v91,v94,v95
MM a[20:23],a[140:143],v[20:23],a[20:23]
buffer_load_dwordx4 v240, s[24:27], s66 offen offset:0 lds
MM a[20:23],a[136:139],v[16:19],a[20:23]
s_add_u32 s66, s66, 9216
EX v113,v113
MM a[48:51],a[144:147],v[16:19],a[48:51]
EX v114,v114
MM a[48:51],a[148:151],v[20:23],a[48:51]
buffer_load_dwordx4 v240, s[24:27], s66 offen offset:1024 lds
MM a[56:59],a[148:151],v[28:31],a[56:59]
s_add_u32 s66, s66, 9216
EX v115,v115
MM a[56:59],a[144:147],v[24:27],a[56:59]
TR a[128:129],v232 offset:49152
CV v84,v112,v113
MM a[60:63],a[152:155],v[24:27],a[60:63]
EX v116,v116
MM a[60:63],a[156:159],v[28:31],a[60:63]
TR a[130:131],v232 offset:53248
TR a[132:133],v232 offset:57344
MM a[52:55],a[156:159],v[20:23],a[52:55]
EX v117,v117
MM a[52:55],a[152:155],v[16:19],a[52:55]
TR a[134:135],v232 offset:61440
TR a[136:137],v233 offset:49152
buffer_load_dwordx4 v240, s[24:27], s66 offen offset:2048 lds
MM a[80:83],a[160:163],v[16:19],a[80:83]
s_add_u32 s66, s66, 9216
TR a[138:139],v233 offset:53248
TR a[140:141],v233 offset:57344
EX v118,v118
MM a[80:83],a[164:167],v[20:23],a[80:83]
TR a[142:143],v233 offset:61440
TR a[144:145],v232 offset:49664
MM a[88:91],a[164:167],v[28:31],a[88:91]
s_xor_b32 s92, s49, 5
s_mul_i32 s92, s92, 17408
s_add_u32 s92, s83, s92
EX v119,v119
MM a[88:91],a[160:163],v[24:27],a[88:91]
buffer_load_dwordx4 a[8:11], v248, s[20:23], s92 offen offset:0 sc1
LPG26_%=:
s_cbranch_execz LPG30_%=
TR a[146:147],v232 offset:53760
EX v120,v120
MM a[92:95],a[168:171],v[24:27],a[92:95]
TR a[148:149],v232 offset:57856
TR a[150:151],v232 offset:61952
buffer_load_dwordx4 v240, s[24:27], s66 offen offset:3072 lds
MM a[92:95],a[172:175],v[28:31],a[92:95]
s_xor_b32 s66, s94, 6
TR a[152:153],v233 offset:49664
EX v121,v121
CV v85,v114,v115
MM a[84:87],a[172:175],v[20:23],a[84:87]
s_mul_i32 s66, s66, 163840
TR a[154:155],v233 offset:53760
TR a[156:157],v233 offset:57856
MM a[84:87],a[168:171],v[16:19],a[84:87]
s_add_u32 s66, s66, s86
buffer_load_dwordx4 a[40:43], v248, s[20:23], s92 offen offset:1024 sc1
TR a[158:159],v233 offset:61952
s_add_u32 m0, s65, 32768
EX v122,v122
MM a[112:115],a[176:179],v[16:19],a[112:115]
TR a[160:161],v232 offset:50176
CV v86,v116,v117
EX v123,v123
MM a[112:115],a[180:183],v[20:23],a[112:115]
TR a[162:163],v232 offset:54272
TR a[164:165],v232 offset:58368
MM a[120:123],a[180:183],v[28:31],a[120:123]
buffer_load_dwordx4 a[72:75], v248, s[20:23], s92 offen offset:2048 sc1
TR a[166:167],v232 offset:62464
EX v124,v124
MM a[120:123],a[176:179],v[24:27],a[120:123]
TR a[168:169],v233 offset:50176
TR a[170:171],v233 offset:54272
CV v87,v118,v119
MM a[124:127],a[184:187],v[24:27],a[124:127]
TR a[172:173],v233 offset:58368
EX v125,v125
MM a[124:127],a[188:191],v[28:31],a[124:127]
buffer_load_dwordx4 a[104:107], v248, s[20:23], s92 offen offset:3072 sc1
TR a[174:175],v233 offset:62464
EX v126,v126
MM a[116:119],a[188:191],v[20:23],a[116:119]
TR a[176:177],v232 offset:50688
TR a[178:179],v232 offset:54784
EX v127,v127
MM a[116:119],a[184:187],v[16:19],a[116:119]
s_add_u32 s92, s92, 4096
TR a[180:181],v232 offset:58880
CV v92,v120,v121
v_smfmac_f32_16x16x64_bf16 v[152:155], v[236:239], v[80:87], v234
buffer_load_dwordx4 v240, s[24:27], s66 offen offset:0 lds
s_add_u32 s66, s66, 9216
TR a[182:183],v232 offset:62976
CV v93,v122,v123
CV v94,v124,v125
CV v95,v126,v127
s_nop 1
v_smfmac_f32_16x16x64_bf16 v[156:159], v[236:239], v[88:95], v234
TR a[184:185],v233 offset:50688
TR a[186:187],v233 offset:54784
TR a[188:189],v233 offset:58880
TR a[190:191],v233 offset:62976
s_waitcnt lgkmcnt(0)
buffer_load_dwordx4 v240, s[24:27], s66 offen offset:1024 lds
s_add_u32 s66, s66, 9216
buffer_load_dwordx4 v240, s[24:27], s66 offen offset:2048 lds
s_add_u32 s66, s66, 9216
buffer_load_dwordx4 v240, s[24:27], s66 offen offset:3072 lds
s_nop 6
v_max3_f32 v249, v152, v153, v156
v_max_f32 v249, v249, v157
v_cmp_lt_f32 vcc, s71, v249
s_cbranch_vccnz LSITE39_%=
LBACK39_%=:
v_add_f32 v246, v152, v153
v_add_f32 v247, v156, v157
s_waitcnt lgkmcnt(0)
MM a[16:19],a[128:131],v[80:83],a[16:19]
s_barrier
MM a[16:19],a[132:135],v[84:87],a[16:19]
buffer_load_dwordx4 a[12:15], v248, s[20:23], s92 offen offset:0 sc1
MM a[24:27],a[132:135],v[92:95],a[24:27]
v_rcp_f32 v153, v246
MM a[24:27],a[128:131],v[88:91],a[24:27]
v_rcp_f32 v157, v247
MM a[28:31],a[136:139],v[88:91],a[28:31]
v_bfe_u32 v152, v241, 16, 8
v_lshl_or_b32 v152, v152, 1, 1
MM a[28:31],a[140:143],v[92:95],a[28:31]
buffer_load_dwordx4 a[44:47], v248, s[20:23], s92 offen offset:1024 sc1
MM a[20:23],a[140:143],v[84:87],a[20:23]
v_lshlrev_b32 v152, 22, v152
v_mul_f32 v152, -v152, v153
MM a[20:23],a[136:139],v[80:83],a[20:23]
v_bfe_u32 v156, v241, 24, 8
v_lshl_or_b32 v156, v156, 1, 1
MM a[48:51],a[144:147],v[80:83],a[48:51]
TR a[128:129],v244 offset:0
TR a[130:131],v244 offset:1024
MM a[48:51],a[148:151],v[84:87],a[48:51]
buffer_load_dwordx4 a[76:79], v248, s[20:23], s92 offen offset:2048 sc1
MM a[56:59],a[148:151],v[92:95],a[56:59]
TR a[132:133],v244 offset:2048
TR a[134:135],v244 offset:3072
MM a[56:59],a[144:147],v[88:91],a[56:59]
v_lshlrev_b32 v156, 22, v156
v_mul_f32 v156, -v156, v157
MM a[60:63],a[152:155],v[88:91],a[60:63]
s_mul_i32 s78, s49, 17408
TR a[136:137],v244 offset:4096
TR a[138:139],v244 offset:5120
MM a[60:63],a[156:159],v[92:95],a[60:63]
s_add_u32 s78, s83, s78
buffer_load_dwordx4 a[108:111], v248, s[20:23], s92 offen offset:3072 sc1
MM a[52:55],a[156:159],v[84:87],a[52:55]
s_add_u32 s78, s78, 8192
TR a[140:141],v244 offset:6144
TR a[142:143],v244 offset:7168
MM a[52:55],a[152:155],v[80:83],a[52:55]
s_add_u32 s79, s78, 4096
AR v16,a16
AR v17,a17
MM a[80:83],a[160:163],v[80:83],a[80:83]
TR a[144:145],v235 offset:0
TR a[146:147],v235 offset:1024
MM a[80:83],a[164:167],v[84:87],a[80:83]
AR v18,a18
AR v19,a19
MM a[88:91],a[164:167],v[92:95],a[88:91]
TR a[148:149],v235 offset:2048
TR a[150:151],v235 offset:3072
MM a[88:91],a[160:163],v[88:91],a[88:91]
AR v20,a20
AR v21,a21
MM a[92:95],a[168:171],v[88:91],a[92:95]
TR a[152:153],v235 offset:4096
TR a[154:155],v235 offset:5120
MM a[92:95],a[172:175],v[92:95],a[92:95]
s_xor_b32 s66, s94, 7
AR v22,a22
AR v23,a23
MM a[84:87],a[172:175],v[84:87],a[84:87]
s_mul_i32 s66, s66, 163840
TR a[156:157],v235 offset:6144
TR a[158:159],v235 offset:7168
v_fma_f32 v16, v16, v153, v152
MM a[84:87],a[168:171],v[80:83],a[84:87]
s_add_u32 s66, s66, s87
v_fma_f32 v17, v17, v153, v152
v_fma_f32 v18, v18, v153, v152
MM a[112:115],a[176:179],v[80:83],a[112:115]
s_add_u32 m0, s65, 114688
TR a[160:161],v144 offset:0
TR a[162:163],v144 offset:2048
v_fma_f32 v19, v19, v153, v152
MM a[112:115],a[180:183],v[84:87],a[112:115]
buffer_load_dwordx4 v240, s[24:27], s66 offen offset:0 lds
v_fma_f32 v20, v20, v153, v152
v_fma_f32 v21, v21, v153, v152
MM a[120:123],a[180:183],v[92:95],a[120:123]
s_add_u32 s66, s66, 9216
TR a[164:165],v144 offset:4096
TR a[166:167],v144 offset:6144
v_fma_f32 v22, v22, v153, v152
v_fma_f32 v23, v23, v153, v152
MM a[120:123],a[176:179],v[88:91],a[120:123]
CV v168,v16,v17
CV v169,v18,v19
MM a[124:127],a[184:187],v[88:91],a[124:127]
TR a[168:169],v144 offset:8192
TR a[170:171],v144 offset:10240
CV v170,v20,v21
MM a[124:127],a[188:191],v[92:95],a[124:127]
buffer_load_dwordx4 v240, s[24:27], s66 offen offset:1024 lds
CV v171,v22,v23
MM a[116:119],a[188:191],v[84:87],a[116:119]
s_add_u32 s66, s66, 9216
TR a[172:173],v144 offset:12288
TR a[174:175],v144 offset:14336
MM a[116:119],a[184:187],v[80:83],a[116:119]
buffer_store_dwordx4 v[168:171], v248, s[20:23], s78 offen offset:0 sc1
AR v24,a24
AR v25,a25
s_waitcnt vmcnt(23)
s_waitcnt lgkmcnt(15)
MM v[128:131],a[128:131],a[0:3],0
TR a[176:177],v145 offset:0
TR a[178:179],v145 offset:2048
AR v26,a26
AR v27,a27
MM v[128:131],a[132:135],a[32:35],v[128:131]
v_bfe_u32 v249, v248, 6, 2
v_lshlrev_b32 v249, 8, v249
AR v28,a28
AR v29,a29
MM v[128:131],a[136:139],a[64:67],v[128:131]
TR a[180:181],v145 offset:4096
TR a[182:183],v145 offset:6144
AR v30,a30
MM v[128:131],a[140:143],a[96:99],v[128:131]
s_waitcnt vmcnt(19)
v_lshrrev_b32 v250, 8, v248
v_lshl_add_u32 v249, v250, 6, v249
AR v31,a31
v_fma_f32 v24, v24, v157, v156
MM v[136:139],a[140:143],a[100:103],0
TR a[184:185],v145 offset:8192
TR a[186:187],v145 offset:10240
v_fma_f32 v25, v25, v157, v156
v_fma_f32 v26, v26, v157, v156
MM v[136:139],a[136:139],a[68:71],v[136:139]
buffer_load_dwordx4 v240, s[24:27], s66 offen offset:2048 lds
v_and_b32 v251, 48, v248
v_add_u32 v249, v249, v251
v_fma_f32 v27, v27, v157, v156
MM v[136:139],a[132:135],a[36:39],v[136:139]
s_add_u32 s66, s66, 9216
TR a[188:189],v145 offset:12288
TR a[190:191],v145 offset:14336
v_fma_f32 v28, v28, v157, v156
v_fma_f32 v29, v29, v157, v156
MM v[136:139],a[128:131],a[4:7],v[136:139]
s_waitcnt lgkmcnt(15)
v_fma_f32 v30, v30, v157, v156
v_fma_f32 v31, v31, v157, v156
MM v[140:143],a[144:147],a[4:7],0
s_lshr_b32 s60, s49, 3
TR a[128:129],v146 offset:0
TR a[130:131],v146 offset:2048
CV v172,v24,v25
CV v173,v26,v27
MM v[140:143],a[148:151],a[36:39],v[140:143]
buffer_load_dwordx4 v240, s[24:27], s66 offen offset:3072 lds
CV v174,v28,v29
MM v[140:143],a[152:155],a[68:71],v[140:143]
TR a[132:133],v146 offset:4096
TR a[134:135],v146 offset:6144
CV v175,v30,v31
MM v[140:143],a[156:159],a[100:103],v[140:143]
s_xor_b32 s66, s94, 7
buffer_store_dwordx4 v[172:175], v248, s[20:23], s79 offen offset:0 sc1
MM v[132:135],a[156:159],a[96:99],0
s_mul_i32 s66, s66, 163840
TR a[136:137],v146 offset:8192
TR a[138:139],v146 offset:10240
AR v48,a48
AR v49,a49
MM v[132:135],a[152:155],a[64:67],v[132:135]
s_add_u32 s66, s66, s86
AR v50,a50
AR v51,a51
MM v[132:135],a[148:151],a[32:35],v[132:135]
)ASM"
R"ASM(
s_add_u32 m0, s65, 49152
TR a[140:141],v146 offset:12288
TR a[142:143],v146 offset:14336
AR v52,a52
MM v[132:135],a[144:147],a[0:3],v[132:135]
s_waitcnt lgkmcnt(15)
buffer_load_dwordx4 v240, s[24:27], s66 offen offset:0 lds
AR v53,a53
AR v54,a54
MM v[64:67],a[160:163],a[0:3],0
s_xor_b32 s92, s49, 6
TR a[144:145],v147 offset:0
TR a[146:147],v147 offset:2048
AR v55,a55
v_fma_f32 v48, v48, v153, v152
MM v[64:67],a[164:167],a[32:35],v[64:67]
s_mul_i32 s92, s92, 17408
s_add_u32 s92, s83, s92
v_fma_f32 v49, v49, v153, v152
v_fma_f32 v50, v50, v153, v152
MM v[64:67],a[168:171],a[64:67],v[64:67]
s_add_u32 s66, s66, 9216
TR a[148:149],v147 offset:4096
TR a[150:151],v147 offset:6144
v_fma_f32 v51, v51, v153, v152
MM v[64:67],a[172:175],a[96:99],v[64:67]
buffer_load_dwordx4 v[160:163], v248, s[20:23], s92 offen offset:0 sc1
v_fma_f32 v52, v52, v153, v152
v_fma_f32 v53, v53, v153, v152
MM v[72:75],a[172:175],a[100:103],0
TR a[152:153],v147 offset:8192
TR a[154:155],v147 offset:10240
v_fma_f32 v54, v54, v153, v152
v_fma_f32 v55, v55, v153, v152
MM v[72:75],a[168:171],a[68:71],v[72:75]
CV v184,v48,v49
CV v185,v50,v51
MM v[72:75],a[164:167],a[36:39],v[72:75]
TR a[156:157],v147 offset:12288
TR a[158:159],v147 offset:14336
CV v186,v52,v53
MM v[72:75],a[160:163],a[4:7],v[72:75]
s_waitcnt lgkmcnt(15)
buffer_load_dwordx4 v[176:179], v248, s[20:23], s92 offen offset:1024 sc1
CV v187,v54,v55
MM v[76:79],a[176:179],a[4:7],0
TR a[160:161],v228 offset:0
TR a[162:163],v228 offset:2048
MM v[76:79],a[180:183],a[36:39],v[76:79]
v_xor_b32 v253, s60, v250
v_and_b32 v238, 240, v248
MM v[76:79],a[184:187],a[68:71],v[76:79]
TR a[164:165],v228 offset:4096
TR a[166:167],v228 offset:6144
MM v[76:79],a[188:191],a[100:103],v[76:79]
buffer_store_dwordx4 v[184:187], v248, s[20:23], s78 offen offset:1024 sc1
AR v56,a56
AR v57,a57
AR v58,a58
AR v59,a59
AR v60,a60
AR v61,a61
MM v[68:71],a[188:191],a[96:99],0
TR a[168:169],v228 offset:8192
TR a[170:171],v228 offset:10240
AR v62,a62
MM v[68:71],a[184:187],a[64:67],v[68:71]
AR v63,a63
v_fma_f32 v56, v56, v157, v156
MM v[68:71],a[180:183],a[32:35],v[68:71]
TR a[172:173],v228 offset:12288
TR a[174:175],v228 offset:14336
v_fma_f32 v57, v57, v157, v156
v_fma_f32 v58, v58, v157, v156
MM v[68:71],a[176:179],a[0:3],v[68:71]
s_waitcnt lgkmcnt(15)
buffer_load_dwordx4 v[192:195], v248, s[20:23], s92 offen offset:2048 sc1
v_fma_f32 v59, v59, v157, v156
MM v[96:99],a[128:131],a[0:3],0
TR a[176:177],v229 offset:0
TR a[178:179],v229 offset:2048
v_fma_f32 v60, v60, v157, v156
v_fma_f32 v61, v61, v157, v156
MM v[96:99],a[132:135],a[32:35],v[96:99]
v_fma_f32 v62, v62, v157, v156
v_fma_f32 v63, v63, v157, v156
MM v[96:99],a[136:139],a[64:67],v[96:99]
TR a[180:181],v229 offset:4096
TR a[182:183],v229 offset:6144
CV v188,v56,v57
MM v[96:99],a[140:143],a[96:99],v[96:99]
buffer_load_dwordx4 v[208:211], v248, s[20:23], s92 offen offset:3072 sc1
CV v189,v58,v59
CV v190,v60,v61
MM v[104:107],a[140:143],a[100:103],0
TR a[184:185],v229 offset:8192
TR a[186:187],v229 offset:10240
CV v191,v62,v63
MM v[104:107],a[136:139],a[68:71],v[104:107]
s_add_u32 s92, s92, 4096
v_lshl_add_u32 v232, v250, 2, v238
s_add_u32 s61, s65, 131072
MM v[104:107],a[132:135],a[36:39],v[104:107]
TR a[188:189],v229 offset:12288
TR a[190:191],v229 offset:14336
MM v[104:107],a[128:131],a[4:7],v[104:107]
s_waitcnt lgkmcnt(15)
buffer_store_dwordx4 v[188:191], v248, s[20:23], s79 offen offset:1024 sc1
AR v16,a80
AR v17,a81
AR v18,a82
AR v19,a83
MM v[108:111],a[144:147],a[4:7],0
TR a[128:129],v230 offset:0
TR a[130:131],v230 offset:2048
AR v20,a84
MM v[108:111],a[148:151],a[36:39],v[108:111]
AR v21,a85
AR v22,a86
MM v[108:111],a[152:155],a[68:71],v[108:111]
TR a[132:133],v230 offset:4096
TR a[134:135],v230 offset:6144
AR v23,a87
v_fma_f32 v16, v16, v153, v152
MM v[108:111],a[156:159],a[100:103],v[108:111]
buffer_load_dwordx4 v[164:167], v248, s[20:23], s92 offen offset:0 sc1
v_fma_f32 v17, v17, v153, v152
v_fma_f32 v18, v18, v153, v152
MM v[100:103],a[156:159],a[96:99],0
TR a[136:137],v230 offset:8192
TR a[138:139],v230 offset:10240
v_fma_f32 v19, v19, v153, v152
MM v[100:103],a[152:155],a[64:67],v[100:103]
v_fma_f32 v20, v20, v153, v152
v_fma_f32 v21, v21, v153, v152
MM v[100:103],a[148:151],a[32:35],v[100:103]
TR a[140:141],v230 offset:12288
TR a[142:143],v230 offset:14336
v_fma_f32 v22, v22, v153, v152
v_fma_f32 v23, v23, v153, v152
MM v[100:103],a[144:147],a[0:3],v[100:103]
s_waitcnt lgkmcnt(15)
buffer_load_dwordx4 v[180:183], v248, s[20:23], s92 offen offset:1024 sc1
CV v200,v16,v17
MM v[0:3],a[160:163],a[0:3],0
TR a[144:145],v231 offset:0
TR a[146:147],v231 offset:2048
CV v201,v18,v19
CV v202,v20,v21
MM v[0:3],a[164:167],a[32:35],v[0:3]
CV v203,v22,v23
v_mul_u32_u24 v224, 5, v238
MM v[0:3],a[168:171],a[64:67],v[0:3]
TR a[148:149],v231 offset:4096
TR a[150:151],v231 offset:6144
MM v[0:3],a[172:175],a[96:99],v[0:3]
buffer_store_dwordx4 v[200:203], v248, s[20:23], s78 offen offset:2048 sc1
AR v24,a88
AR v25,a89
MM v[8:11],a[172:175],a[100:103],0
TR a[152:153],v231 offset:8192
TR a[154:155],v231 offset:10240
AR v26,a90
AR v27,a91
MM v[8:11],a[168:171],a[68:71],v[8:11]
AR v28,a92
AR v29,a93
MM v[8:11],a[164:167],a[36:39],v[8:11]
TR a[156:157],v231 offset:12288
TR a[158:159],v231 offset:14336
AR v30,a94
MM v[8:11],a[160:163],a[4:7],v[8:11]
s_waitcnt lgkmcnt(15)
buffer_load_dwordx4 v[196:199], v248, s[20:23], s92 offen offset:2048 sc1
AR v31,a95
v_fma_f32 v24, v24, v157, v156
s_barrier
MM v[12:15],a[176:179],a[4:7],0
TR a[160:161],v244 offset:8192
TR a[162:163],v244 offset:9216
v_fma_f32 v25, v25, v157, v156
v_fma_f32 v26, v26, v157, v156
MM v[12:15],a[180:183],a[36:39],v[12:15]
v_fma_f32 v27, v27, v157, v156
v_fma_f32 v28, v28, v157, v156
MM v[12:15],a[184:187],a[68:71],v[12:15]
TR a[164:165],v244 offset:10240
TR a[166:167],v244 offset:11264
v_fma_f32 v29, v29, v157, v156
MM v[12:15],a[188:191],a[100:103],v[12:15]
buffer_load_dwordx4 v[212:215], v248, s[20:23], s92 offen offset:3072 sc1
v_fma_f32 v30, v30, v157, v156
v_fma_f32 v31, v31, v157, v156
MM v[4:7],a[188:191],a[96:99],0
TR a[168:169],v244 offset:12288
TR a[170:171],v244 offset:13312
CV v204,v24,v25
MM v[4:7],a[184:187],a[64:67],v[4:7]
CV v205,v26,v27
CV v206,v28,v29
MM v[4:7],a[180:183],a[32:35],v[4:7]
TR a[172:173],v244 offset:14336
TR a[174:175],v244 offset:15360
CV v207,v30,v31
MM v[4:7],a[176:179],a[0:3],v[4:7]
s_waitcnt lgkmcnt(15)
buffer_store_dwordx4 v[204:207], v248, s[20:23], s79 offen offset:2048 sc1
MM v[32:35],a[128:131],a[0:3],0
TR a[176:177],v235 offset:8192
TR a[178:179],v235 offset:9216
AR v48,a112
AR v49,a113
MM v[32:35],a[132:135],a[32:35],v[32:35]
AR v50,a114
AR v51,a115
MM v[32:35],a[136:139],a[64:67],v[32:35]
TR a[180:181],v235 offset:10240
TR a[182:183],v235 offset:11264
AR v52,a116
MM v[32:35],a[140:143],a[96:99],v[32:35]
buffer_load_dwordx4 v240, s[24:27], s66 offen offset:1024 lds
AR v53,a117
AR v54,a118
MM v[40:43],a[140:143],a[100:103],0
s_add_u32 s66, s66, 9216
TR a[184:185],v235 offset:12288
TR a[186:187],v235 offset:13312
AR v55,a119
v_fma_f32 v48, v48, v153, v152
MM v[40:43],a[136:139],a[68:71],v[40:43]
v_fma_f32 v49, v49, v153, v152
v_fma_f32 v50, v50, v153, v152
MM v[40:43],a[132:135],a[36:39],v[40:43]
TR a[188:189],v235 offset:14336
TR a[190:191],v235 offset:15360
v_fma_f32 v51, v51, v153, v152
MM v[40:43],a[128:131],a[4:7],v[40:43]
s_waitcnt lgkmcnt(15)
buffer_load_dwordx4 v240, s[24:27], s66 offen offset:2048 lds
v_fma_f32 v52, v52, v153, v152
v_fma_f32 v53, v53, v153, v152
MM v[44:47],a[144:147],a[4:7],0
s_add_u32 s66, s66, 9216
TR a[128:129],v144 offset:16384
TR a[130:131],v144 offset:18432
v_fma_f32 v54, v54, v153, v152
v_fma_f32 v55, v55, v153, v152
MM v[44:47],a[148:151],a[36:39],v[44:47]
CV v216,v48,v49
CV v217,v50,v51
MM v[44:47],a[152:155],a[68:71],v[44:47]
TR a[132:133],v144 offset:20480
TR a[134:135],v144 offset:22528
CV v218,v52,v53
MM v[44:47],a[156:159],a[100:103],v[44:47]
buffer_load_dwordx4 v240, s[24:27], s66 offen offset:3072 lds
CV v219,v54,v55
MM v[36:39],a[156:159],a[96:99],0
TR a[136:137],v144 offset:24576
TR a[138:139],v144 offset:26624
MM v[36:39],a[152:155],a[64:67],v[36:39]
s_xor_b32 s66, s94, 0
v_lshl_add_u32 v224, v250, 3, v224
v_add_u32 v224, s61, v224
MM v[36:39],a[148:151],a[32:35],v[36:39]
s_mul_i32 s66, s66, 163840
TR a[140:141],v144 offset:28672
TR a[142:143],v144 offset:30720
MM v[36:39],a[144:147],a[0:3],v[36:39]
s_waitcnt lgkmcnt(15)
buffer_store_dwordx4 v[216:219], v248, s[20:23], s78 offen offset:3072 sc1
AR v56,a120
AR v57,a121
AR v58,a122
AR v59,a123
AR v60,a124
s_waitcnt vmcnt(31)
MM v[128:131],a[160:163],a[8:11],v[128:131]
s_add_u32 s66, s66, s88
TR a[144:145],v145 offset:16384
TR a[146:147],v145 offset:18432
AR v61,a125
AR v62,a126
MM v[128:131],a[164:167],a[40:43],v[128:131]
s_add_u32 m0, s89, 131072
AR v63,a127
s_nop 0
LPG27_%=:
s_cbranch_execz LTR1_%=
v_fma_f32 v56, v56, v157, v156
MM v[128:131],a[168:171],a[72:75],v[128:131]
TR a[148:149],v145 offset:20480
TR a[150:151],v145 offset:22528
v_fma_f32 v57, v57, v157, v156
v_fma_f32 v58, v58, v157, v156
MM v[128:131],a[172:175],a[104:107],v[128:131]
s_waitcnt vmcnt(23)
buffer_load_dwordx4 v252, s[24:27], s66 offen offset:0 lds
v_fma_f32 v59, v59, v157, v156
MM v[136:139],a[172:175],a[108:111],v[136:139]
s_add_u32 s66, s66, 19456
TR a[152:153],v145 offset:24576
TR a[154:155],v145 offset:26624
v_fma_f32 v60, v60, v157, v156
v_fma_f32 v61, v61, v157, v156
MM v[136:139],a[168:171],a[76:79],v[136:139]
v_fma_f32 v62, v62, v157, v156
v_fma_f32 v63, v63, v157, v156
MM v[136:139],a[164:167],a[44:47],v[136:139]
TR a[156:157],v145 offset:28672
TR a[158:159],v145 offset:30720
CV v220,v56,v57
MM v[136:139],a[160:163],a[12:15],v[136:139]
s_waitcnt lgkmcnt(15)
buffer_load_dwordx4 v252, s[24:27], s66 offen offset:1024 lds
CV v221,v58,v59
CV v222,v60,v61
MM v[140:143],a[176:179],a[12:15],v[140:143]
TR a[160:161],v146 offset:16384
TR a[162:163],v146 offset:18432
CV v223,v62,v63
MM v[140:143],a[180:183],a[44:47],v[140:143]
s_xor_b32 s66, s94, 0
v_mul_u32_u24 v254, 1280, v250
v_mul_u32_u24 v250, 33792, v253
MM v[140:143],a[184:187],a[76:79],v[140:143]
s_mul_i32 s66, s66, 163840
TR a[164:165],v146 offset:20480
TR a[166:167],v146 offset:22528
MM v[140:143],a[188:191],a[108:111],v[140:143]
s_add_u32 s66, s66, s87
buffer_store_dwordx4 v[220:223], v248, s[20:23], s79 offen offset:3072 sc1
MM v[132:135],a[188:191],a[104:107],v[132:135]
s_add_u32 m0, s65, 65536
TR a[168:169],v146 offset:24576
TR a[170:171],v146 offset:26624
MM v[132:135],a[184:187],a[72:75],v[132:135]
v_add_u32 v250, v250, v238
v_lshlrev_b32 v253, 4, v253
MM v[132:135],a[180:183],a[40:43],v[132:135]
TR a[172:173],v146 offset:28672
TR a[174:175],v146 offset:30720
MM v[132:135],a[176:179],a[8:11],v[132:135]
s_waitcnt lgkmcnt(15)
buffer_load_dwordx4 v240, s[24:27], s66 offen offset:0 lds
MM v[64:67],a[128:131],a[8:11],v[64:67]
s_xor_b32 s92, s49, 7
TR a[176:177],v147 offset:16384
TR a[178:179],v147 offset:18432
MM v[64:67],a[132:135],a[40:43],v[64:67]
s_mul_i32 s92, s92, 17408
s_add_u32 s92, s83, s92
v_bfe_u32 v238, v248, 4, 3
MM v[64:67],a[136:139],a[72:75],v[64:67]
s_add_u32 s66, s66, 9216
TR a[180:181],v147 offset:20480
TR a[182:183],v147 offset:22528
MM v[64:67],a[140:143],a[104:107],v[64:67]
buffer_load_dwordx4 a[192:195], v248, s[20:23], s92 offen offset:0 sc1
MM v[72:75],a[140:143],a[108:111],v[72:75]
TR a[184:185],v147 offset:24576
TR a[186:187],v147 offset:26624
MM v[72:75],a[136:139],a[76:79],v[72:75]
v_lshl_add_u32 v254, v238, 3, v254
v_bfe_u32 v238, v248, 7, 1
MM v[72:75],a[132:135],a[44:47],v[72:75]
TR a[188:189],v147 offset:28672
TR a[190:191],v147 offset:30720
MM v[72:75],a[128:131],a[12:15],v[72:75]
s_waitcnt lgkmcnt(15)
buffer_load_dwordx4 a[208:211], v248, s[20:23], s92 offen offset:1024 sc1
MM v[76:79],a[144:147],a[12:15],v[76:79]
TR a[128:129],v228 offset:16384
TR a[130:131],v228 offset:18432
MM v[76:79],a[148:151],a[44:47],v[76:79]
v_mul_u32_u24 v238, 5120, v238
v_add_u32 v254, v254, v238
MM v[76:79],a[152:155],a[76:79],v[76:79]
TR a[132:133],v228 offset:20480
TR a[134:135],v228 offset:22528
)ASM"
R"ASM(
MM v[76:79],a[156:159],a[108:111],v[76:79]
buffer_load_dwordx4 a[224:227], v248, s[20:23], s92 offen offset:2048 sc1
MM v[68:71],a[156:159],a[104:107],v[68:71]
TR a[136:137],v228 offset:24576
TR a[138:139],v228 offset:26624
MM v[68:71],a[152:155],a[72:75],v[68:71]
v_lshrrev_b32 v238, 6, v248
v_mul_u32_u24 v225, 80, v238
MM v[68:71],a[148:151],a[40:43],v[68:71]
TR a[140:141],v228 offset:28672
TR a[142:143],v228 offset:30720
MM v[68:71],a[144:147],a[8:11],v[68:71]
s_waitcnt lgkmcnt(15)
buffer_load_dwordx4 a[240:243], v248, s[20:23], s92 offen offset:3072 sc1
MM v[96:99],a[160:163],a[8:11],v[96:99]
TR a[144:145],v229 offset:16384
TR a[146:147],v229 offset:18432
MM v[96:99],a[164:167],a[40:43],v[96:99]
s_add_u32 s92, s92, 4096
v_add3_u32 v225, v225, v251, s61
v_mul_u32_u24 v238, 1280, v238
MM v[96:99],a[168:171],a[72:75],v[96:99]
TR a[148:149],v229 offset:20480
TR a[150:151],v229 offset:22528
MM v[96:99],a[172:175],a[104:107],v[96:99]
buffer_load_dwordx4 a[196:199], v248, s[20:23], s92 offen offset:0 sc1
MM v[104:107],a[172:175],a[108:111],v[104:107]
TR a[152:153],v229 offset:24576
TR a[154:155],v229 offset:26624
MM v[104:107],a[168:171],a[76:79],v[104:107]
v_add_u32 v251, v251, v238
MM v[104:107],a[164:167],a[44:47],v[104:107]
TR a[156:157],v229 offset:28672
TR a[158:159],v229 offset:30720
MM v[104:107],a[160:163],a[12:15],v[104:107]
s_waitcnt lgkmcnt(15)
buffer_load_dwordx4 a[212:215], v248, s[20:23], s92 offen offset:1024 sc1
MM v[108:111],a[176:179],a[12:15],v[108:111]
TR a[160:161],v230 offset:16384
TR a[162:163],v230 offset:18432
MM v[108:111],a[180:183],a[44:47],v[108:111]
MM v[108:111],a[184:187],a[76:79],v[108:111]
TR a[164:165],v230 offset:20480
TR a[166:167],v230 offset:22528
MM v[108:111],a[188:191],a[108:111],v[108:111]
buffer_load_dwordx4 a[228:231], v248, s[20:23], s92 offen offset:2048 sc1
MM v[100:103],a[188:191],a[104:107],v[100:103]
TR a[168:169],v230 offset:24576
TR a[170:171],v230 offset:26624
MM v[100:103],a[184:187],a[72:75],v[100:103]
MM v[100:103],a[180:183],a[40:43],v[100:103]
TR a[172:173],v230 offset:28672
TR a[174:175],v230 offset:30720
MM v[100:103],a[176:179],a[8:11],v[100:103]
s_waitcnt lgkmcnt(15)
buffer_load_dwordx4 a[244:247], v248, s[20:23], s92 offen offset:3072 sc1
MM v[0:3],a[128:131],a[8:11],v[0:3]
TR a[176:177],v231 offset:16384
TR a[178:179],v231 offset:18432
MM v[0:3],a[132:135],a[40:43],v[0:3]
MM v[0:3],a[136:139],a[72:75],v[0:3]
TR a[180:181],v231 offset:20480
TR a[182:183],v231 offset:22528
MM v[0:3],a[140:143],a[104:107],v[0:3]
buffer_load_dwordx4 v240, s[24:27], s66 offen offset:1024 lds
MM v[8:11],a[140:143],a[108:111],v[8:11]
s_add_u32 s66, s66, 9216
TR a[184:185],v231 offset:24576
TR a[186:187],v231 offset:26624
MM v[8:11],a[136:139],a[76:79],v[8:11]
MM v[8:11],a[132:135],a[44:47],v[8:11]
TR a[188:189],v231 offset:28672
TR a[190:191],v231 offset:30720
MM v[8:11],a[128:131],a[12:15],v[8:11]
s_waitcnt lgkmcnt(15)
buffer_load_dwordx4 v240, s[24:27], s66 offen offset:2048 lds
s_barrier
MM v[12:15],a[144:147],a[12:15],v[12:15]
s_add_u32 s66, s66, 9216
TR a[128:129],v244 offset:16384
TR a[130:131],v244 offset:17408
MM v[12:15],a[148:151],a[44:47],v[12:15]
MM v[12:15],a[152:155],a[76:79],v[12:15]
TR a[132:133],v244 offset:18432
TR a[134:135],v244 offset:19456
MM v[12:15],a[156:159],a[108:111],v[12:15]
buffer_load_dwordx4 v240, s[24:27], s66 offen offset:3072 lds
MM v[4:7],a[156:159],a[104:107],v[4:7]
TR a[136:137],v244 offset:20480
TR a[138:139],v244 offset:21504
MM v[4:7],a[152:155],a[72:75],v[4:7]
s_xor_b32 s66, s94, 0
s_mul_i32 s66, s66, 163840
s_add_u32 s66, s66, s86
MM v[4:7],a[148:151],a[40:43],v[4:7]
s_add_u32 m0, s65, 0
TR a[140:141],v244 offset:22528
TR a[142:143],v244 offset:23552
MM v[4:7],a[144:147],a[8:11],v[4:7]
s_waitcnt lgkmcnt(15)
buffer_load_dwordx4 v240, s[24:27], s66 offen offset:0 lds
MM v[32:35],a[160:163],a[8:11],v[32:35]
s_add_u32 s66, s66, 9216
TR a[144:145],v235 offset:16384
TR a[146:147],v235 offset:17408
MM v[32:35],a[164:167],a[40:43],v[32:35]
MM v[32:35],a[168:171],a[72:75],v[32:35]
TR a[148:149],v235 offset:18432
TR a[150:151],v235 offset:19456
MM v[32:35],a[172:175],a[104:107],v[32:35]
buffer_load_dwordx4 v240, s[24:27], s66 offen offset:1024 lds
MM v[40:43],a[172:175],a[108:111],v[40:43]
s_add_u32 s66, s66, 9216
TR a[152:153],v235 offset:20480
TR a[154:155],v235 offset:21504
MM v[40:43],a[168:171],a[76:79],v[40:43]
MM v[40:43],a[164:167],a[44:47],v[40:43]
TR a[156:157],v235 offset:22528
TR a[158:159],v235 offset:23552
MM v[40:43],a[160:163],a[12:15],v[40:43]
s_waitcnt lgkmcnt(15)
buffer_load_dwordx4 v240, s[24:27], s66 offen offset:2048 lds
MM v[44:47],a[176:179],a[12:15],v[44:47]
s_lshl_b32 s97, s84, 2
TR a[160:161],v144 offset:32768
TR a[162:163],v144 offset:34816
MM v[44:47],a[180:183],a[44:47],v[44:47]
s_add_u32 s97, s97, s96
s_add_u32 s97, s97, 16
s_waitcnt vmcnt(15)
MM v[44:47],a[184:187],a[76:79],v[44:47]
s_add_u32 s66, s66, 9216
TR a[164:165],v144 offset:36864
TR a[166:167],v144 offset:38912
MM v[44:47],a[188:191],a[108:111],v[44:47]
v_mov_b32 v242, s69
s_mov_b64 exec, 1
buffer_store_dword v242, off, s[20:23], s97 offset:0 sc1
s_mov_b64 exec, -1
MM v[36:39],a[188:191],a[104:107],v[36:39]
TR a[168:169],v144 offset:40960
TR a[170:171],v144 offset:43008
MM v[36:39],a[184:187],a[72:75],v[36:39]
buffer_load_dwordx4 v240, s[24:27], s66 offen offset:3072 lds
MM v[36:39],a[180:183],a[40:43],v[36:39]
TR a[172:173],v144 offset:45056
TR a[174:175],v144 offset:47104
MM v[36:39],a[176:179],a[8:11],v[36:39]
s_waitcnt lgkmcnt(15)
s_xor_b32 s66, s94, 1
s_mul_i32 s66, s66, 163840
MM v[128:131],a[128:131],v[160:163],v[128:131]
s_add_u32 s66, s66, s88
TR a[176:177],v145 offset:32768
TR a[178:179],v145 offset:34816
MM v[128:131],a[132:135],v[176:179],v[128:131]
s_add_u32 m0, s89, 139264
MM v[128:131],a[136:139],v[192:195],v[128:131]
TR a[180:181],v145 offset:36864
TR a[182:183],v145 offset:38912
MM v[128:131],a[140:143],v[208:211],v[128:131]
buffer_load_dwordx4 v252, s[24:27], s66 offen offset:0 lds
MM v[136:139],a[140:143],v[212:215],v[136:139]
s_add_u32 s66, s66, 19456
TR a[184:185],v145 offset:40960
TR a[186:187],v145 offset:43008
MM v[136:139],a[136:139],v[196:199],v[136:139]
MM v[136:139],a[132:135],v[180:183],v[136:139]
TR a[188:189],v145 offset:45056
TR a[190:191],v145 offset:47104
MM v[136:139],a[128:131],v[164:167],v[136:139]
s_waitcnt lgkmcnt(15)
buffer_load_dwordx4 v252, s[24:27], s66 offen offset:1024 lds
MM v[140:143],a[144:147],v[164:167],v[140:143]
TR a[128:129],v146 offset:32768
TR a[130:131],v146 offset:34816
MM v[140:143],a[148:151],v[180:183],v[140:143]
s_xor_b32 s66, s94, 1
s_mul_i32 s66, s66, 163840
s_add_u32 s66, s66, s87
MM v[140:143],a[152:155],v[196:199],v[140:143]
s_add_u32 m0, s65, 81920
TR a[132:133],v146 offset:36864
TR a[134:135],v146 offset:38912
MM v[140:143],a[156:159],v[212:215],v[140:143]
buffer_load_dwordx4 v240, s[24:27], s66 offen offset:0 lds
MM v[132:135],a[156:159],v[208:211],v[132:135]
s_add_u32 s66, s66, 9216
TR a[136:137],v146 offset:40960
TR a[138:139],v146 offset:43008
MM v[132:135],a[152:155],v[192:195],v[132:135]
MM v[132:135],a[148:151],v[176:179],v[132:135]
TR a[140:141],v146 offset:45056
TR a[142:143],v146 offset:47104
MM v[132:135],a[144:147],v[160:163],v[132:135]
s_waitcnt lgkmcnt(15)
buffer_load_dwordx4 v240, s[24:27], s66 offen offset:1024 lds
MM v[64:67],a[160:163],v[160:163],v[64:67]
s_add_u32 s66, s66, 9216
TR a[144:145],v147 offset:32768
TR a[146:147],v147 offset:34816
MM v[64:67],a[164:167],v[176:179],v[64:67]
MM v[64:67],a[168:171],v[192:195],v[64:67]
TR a[148:149],v147 offset:36864
TR a[150:151],v147 offset:38912
MM v[64:67],a[172:175],v[208:211],v[64:67]
buffer_load_dwordx4 v240, s[24:27], s66 offen offset:2048 lds
MM v[72:75],a[172:175],v[212:215],v[72:75]
s_add_u32 s66, s66, 9216
TR a[152:153],v147 offset:40960
TR a[154:155],v147 offset:43008
MM v[72:75],a[168:171],v[196:199],v[72:75]
MM v[72:75],a[164:167],v[180:183],v[72:75]
TR a[156:157],v147 offset:45056
TR a[158:159],v147 offset:47104
MM v[72:75],a[160:163],v[164:167],v[72:75]
s_waitcnt lgkmcnt(15)
buffer_load_dwordx4 v240, s[24:27], s66 offen offset:3072 lds
MM v[76:79],a[176:179],v[164:167],v[76:79]
TR a[160:161],v228 offset:32768
TR a[162:163],v228 offset:34816
MM v[76:79],a[180:183],v[180:183],v[76:79]
MM v[76:79],a[184:187],v[196:199],v[76:79]
TR a[164:165],v228 offset:36864
TR a[166:167],v228 offset:38912
MM v[76:79],a[188:191],v[212:215],v[76:79]
MM v[68:71],a[188:191],v[208:211],v[68:71]
TR a[168:169],v228 offset:40960
TR a[170:171],v228 offset:43008
MM v[68:71],a[184:187],v[192:195],v[68:71]
MM v[68:71],a[180:183],v[176:179],v[68:71]
TR a[172:173],v228 offset:45056
TR a[174:175],v228 offset:47104
MM v[68:71],a[176:179],v[160:163],v[68:71]
s_waitcnt lgkmcnt(15)
MM v[96:99],a[128:131],v[160:163],v[96:99]
s_add_u32 s80, s18, 131072
TR a[176:177],v229 offset:32768
TR a[178:179],v229 offset:34816
MM v[96:99],a[132:135],v[176:179],v[96:99]
buffer_load_dwordx4 v[80:83], v251, s[20:23], s80 offen offset:0 sc1
MM v[96:99],a[136:139],v[192:195],v[96:99]
TR a[180:181],v229 offset:36864
TR a[182:183],v229 offset:38912
MM v[96:99],a[140:143],v[208:211],v[96:99]
v_mov_b32 v237, 0x3f80
v_and_b32 v236, 16, v248
MM v[104:107],a[140:143],v[212:215],v[104:107]
TR a[184:185],v229 offset:40960
TR a[186:187],v229 offset:43008
MM v[104:107],a[136:139],v[196:199],v[104:107]
s_add_u32 s80, s18, 151552
buffer_load_dwordx4 v[84:87], v251, s[20:23], s80 offen offset:0 sc1
MM v[104:107],a[132:135],v[180:183],v[104:107]
TR a[188:189],v229 offset:45056
TR a[190:191],v229 offset:47104
MM v[104:107],a[128:131],v[164:167],v[104:107]
s_waitcnt lgkmcnt(15)
v_lshlrev_b32 v236, v236, v237
s_mov_b32 s28, 0x30003
MM v[108:111],a[144:147],v[164:167],v[108:111]
s_mov_b32 s29, s28
TR a[128:129],v230 offset:32768
TR a[130:131],v230 offset:34816
MM v[108:111],a[148:151],v[180:183],v[108:111]
s_add_u32 s80, s63, 131072
v_lshrrev_b32 v243, 2, v248
v_min_u32 v243, 12, v243
MM v[108:111],a[152:155],v[196:199],v[108:111]
TR a[132:133],v230 offset:36864
TR a[134:135],v230 offset:38912
MM v[108:111],a[156:159],v[212:215],v[108:111]
buffer_load_dword v242, v243, s[20:23], s96 offen offset:16 sc1
buffer_load_dwordx2 v[150:151], v254, s[20:23], s80 offen offset:0 sc1
MM v[100:103],a[156:159],v[208:211],v[100:103]
TR a[136:137],v230 offset:40960
TR a[138:139],v230 offset:43008
MM v[100:103],a[152:155],v[192:195],v[100:103]
v_cndmask_b32 v112, 0, v236, s[28:29]
s_lshl_b64 s[28:29], s[28:29], 2
MM v[100:103],a[148:151],v[176:179],v[100:103]
TR a[140:141],v230 offset:45056
TR a[142:143],v230 offset:47104
MM v[100:103],a[144:147],v[160:163],v[100:103]
s_waitcnt lgkmcnt(15)
v_cndmask_b32 v113, 0, v236, s[28:29]
s_lshl_b64 s[28:29], s[28:29], 2
MM v[0:3],a[160:163],v[160:163],v[0:3]
TR a[144:145],v231 offset:32768
TR a[146:147],v231 offset:34816
MM v[0:3],a[164:167],v[176:179],v[0:3]
v_cndmask_b32 v114, 0, v236, s[28:29]
s_lshl_b64 s[28:29], s[28:29], 2
MM v[0:3],a[168:171],v[192:195],v[0:3]
TR a[148:149],v231 offset:36864
TR a[150:151],v231 offset:38912
MM v[0:3],a[172:175],v[208:211],v[0:3]
v_cndmask_b32 v115, 0, v236, s[28:29]
s_lshl_b64 s[28:29], s[28:29], 2
MM v[8:11],a[172:175],v[212:215],v[8:11]
TR a[152:153],v231 offset:40960
TR a[154:155],v231 offset:43008
MM v[8:11],a[168:171],v[196:199],v[8:11]
v_cndmask_b32 v116, 0, v236, s[28:29]
s_lshl_b64 s[28:29], s[28:29], 2
MM v[8:11],a[164:167],v[180:183],v[8:11]
TR a[156:157],v231 offset:45056
TR a[158:159],v231 offset:47104
MM v[8:11],a[160:163],v[164:167],v[8:11]
s_waitcnt lgkmcnt(15)
v_cndmask_b32 v117, 0, v236, s[28:29]
s_lshl_b64 s[28:29], s[28:29], 2
s_barrier
MM v[12:15],a[176:179],v[164:167],v[12:15]
s_xor_b32 s66, s94, 2
TR a[160:161],v244 offset:24576
TR a[162:163],v244 offset:25600
MM v[12:15],a[180:183],v[180:183],v[12:15]
s_mul_i32 s66, s66, 163840
v_cndmask_b32 v118, 0, v236, s[28:29]
s_lshl_b64 s[28:29], s[28:29], 2
MM v[12:15],a[184:187],v[196:199],v[12:15]
s_add_u32 s66, s66, s88
TR a[164:165],v244 offset:26624
TR a[166:167],v244 offset:27648
MM v[12:15],a[188:191],v[212:215],v[12:15]
)ASM"
R"ASM(
s_add_u32 m0, s89, 147456
v_cndmask_b32 v119, 0, v236, s[28:29]
s_mov_b32 s28, 0x300003
MM v[4:7],a[188:191],v[208:211],v[4:7]
s_mov_b32 s29, 0x30000300
TR a[168:169],v244 offset:28672
TR a[170:171],v244 offset:29696
MM v[4:7],a[184:187],v[192:195],v[4:7]
buffer_load_dwordx4 v252, s[24:27], s66 offen offset:0 lds
MM v[4:7],a[180:183],v[176:179],v[4:7]
s_add_u32 s66, s66, 19456
TR a[172:173],v244 offset:30720
TR a[174:175],v244 offset:31744
MM v[4:7],a[176:179],v[160:163],v[4:7]
s_waitcnt lgkmcnt(15)
v_mov_b32 v120, 0
v_mov_b32 v121, 0
MM v[32:35],a[128:131],v[160:163],v[32:35]
TR a[176:177],v235 offset:24576
TR a[178:179],v235 offset:25600
MM v[32:35],a[132:135],v[176:179],v[32:35]
buffer_load_dwordx4 v252, s[24:27], s66 offen offset:1024 lds
MM v[32:35],a[136:139],v[192:195],v[32:35]
TR a[180:181],v235 offset:26624
TR a[182:183],v235 offset:27648
MM v[32:35],a[140:143],v[208:211],v[32:35]
s_xor_b32 s66, s94, 2
v_mov_b32 v124, 0
v_mov_b32 v125, 0
MM v[40:43],a[140:143],v[212:215],v[40:43]
s_mul_i32 s66, s66, 163840
TR a[184:185],v235 offset:28672
TR a[186:187],v235 offset:29696
MM v[40:43],a[136:139],v[196:199],v[40:43]
s_add_u32 s66, s66, s87
v_cndmask_b32 v122, 0, v236, s[28:29]
s_lshl_b64 s[28:29], s[28:29], 2
MM v[40:43],a[132:135],v[180:183],v[40:43]
s_add_u32 m0, s65, 98304
TR a[188:189],v235 offset:30720
TR a[190:191],v235 offset:31744
MM v[40:43],a[128:131],v[164:167],v[40:43]
s_waitcnt lgkmcnt(15)
buffer_load_dwordx4 v240, s[24:27], s66 offen offset:0 lds
MM v[44:47],a[144:147],v[164:167],v[44:47]
s_add_u32 s66, s66, 9216
TR a[128:129],v144 offset:49152
TR a[130:131],v144 offset:51200
MM v[44:47],a[148:151],v[180:183],v[44:47]
v_cndmask_b32 v123, 0, v236, s[28:29]
MM v[44:47],a[152:155],v[196:199],v[44:47]
TR a[132:133],v144 offset:53248
TR a[134:135],v144 offset:55296
MM v[44:47],a[156:159],v[212:215],v[44:47]
buffer_load_dwordx4 v240, s[24:27], s66 offen offset:1024 lds
MM v[36:39],a[156:159],v[208:211],v[36:39]
s_add_u32 s66, s66, 9216
TR a[136:137],v144 offset:57344
TR a[138:139],v144 offset:59392
MM v[36:39],a[152:155],v[192:195],v[36:39]
MM v[36:39],a[148:151],v[176:179],v[36:39]
TR a[140:141],v144 offset:61440
TR a[142:143],v144 offset:63488
MM v[36:39],a[144:147],v[160:163],v[36:39]
s_waitcnt lgkmcnt(15)
buffer_load_dwordx4 v240, s[24:27], s66 offen offset:2048 lds
s_waitcnt vmcnt(27)
MM v[128:131],a[160:163],a[192:195],v[128:131]
s_add_u32 s80, s18, 0
TR a[144:145],v145 offset:49152
TR a[146:147],v145 offset:51200
MM v[128:131],a[164:167],a[208:211],v[128:131]
s_add_u32 s66, s66, 9216
buffer_load_dwordx4 v[88:91], v251, s[32:35], s80 offen offset:0
MM v[128:131],a[168:171],a[224:227],v[128:131]
TR a[148:149],v145 offset:53248
TR a[150:151],v145 offset:55296
MM v[128:131],a[172:175],a[240:243],v[128:131]
s_waitcnt vmcnt(24)
MM v[136:139],a[172:175],a[244:247],v[136:139]
TR a[152:153],v145 offset:57344
TR a[154:155],v145 offset:59392
MM v[136:139],a[168:171],a[228:231],v[136:139]
s_add_u32 s80, s18, 20480
buffer_load_dwordx4 v[92:95], v251, s[32:35], s80 offen offset:0
MM v[136:139],a[164:167],a[212:215],v[136:139]
TR a[156:157],v145 offset:61440
TR a[158:159],v145 offset:63488
MM v[136:139],a[160:163],a[196:199],v[136:139]
s_waitcnt lgkmcnt(15)
MM v[140:143],a[176:179],a[196:199],v[140:143]
TR a[160:161],v146 offset:49152
TR a[162:163],v146 offset:51200
MM v[140:143],a[180:183],a[212:215],v[140:143]
s_add_u32 s80, s63, 0
buffer_load_dwordx2 v[126:127], v254, s[32:35], s80 offen offset:0
MM v[140:143],a[184:187],a[228:231],v[140:143]
TR a[164:165],v146 offset:53248
TR a[166:167],v146 offset:55296
MM v[140:143],a[188:191],a[244:247],v[140:143]
buffer_load_dwordx4 v240, s[24:27], s66 offen offset:3072 lds
MM v[132:135],a[188:191],a[240:243],v[132:135]
TR a[168:169],v146 offset:57344
TR a[170:171],v146 offset:59392
MM v[132:135],a[184:187],a[224:227],v[132:135]
MM v[132:135],a[180:183],a[208:211],v[132:135]
TR a[172:173],v146 offset:61440
TR a[174:175],v146 offset:63488
MM v[132:135],a[176:179],a[192:195],v[132:135]
s_waitcnt lgkmcnt(15)
MM v[64:67],a[128:131],a[192:195],v[64:67]
s_waitcnt vmcnt(10)
TR a[176:177],v147 offset:49152
TR a[178:179],v147 offset:51200
MM v[64:67],a[132:135],a[208:211],v[64:67]
v_cmp_ne_u32 vcc, s69, v242
s_cbranch_vccnz LRPO1_%=
LRPO1b_%=:
MM v[64:67],a[136:139],a[224:227],v[64:67]
s_xor_b32 s92, s49, 1
TR a[180:181],v147 offset:53248
TR a[182:183],v147 offset:55296
MM v[64:67],a[140:143],a[240:243],v[64:67]
s_mul_i32 s92, s92, 17408
s_add_u32 s92, s83, s92
s_add_u32 s92, s92, 8192
MM v[72:75],a[140:143],a[244:247],v[72:75]
TR a[184:185],v147 offset:57344
TR a[186:187],v147 offset:59392
MM v[72:75],a[136:139],a[228:231],v[72:75]
buffer_load_dwordx4 a[16:19], v248, s[20:23], s92 offen offset:0 sc1
MM v[72:75],a[132:135],a[212:215],v[72:75]
TR a[188:189],v147 offset:61440
TR a[190:191],v147 offset:63488
s_nop 0
LPG28_%=:
s_cbranch_execz LTR1_%=
MM v[72:75],a[128:131],a[196:199],v[72:75]
s_waitcnt lgkmcnt(15)
buffer_load_dwordx4 a[48:51], v248, s[20:23], s92 offen offset:1024 sc1
MM v[76:79],a[144:147],a[196:199],v[76:79]
TR a[128:129],v228 offset:49152
TR a[130:131],v228 offset:51200
MM v[76:79],a[148:151],a[212:215],v[76:79]
MM v[76:79],a[152:155],a[228:231],v[76:79]
TR a[132:133],v228 offset:53248
TR a[134:135],v228 offset:55296
MM v[76:79],a[156:159],a[244:247],v[76:79]
buffer_load_dwordx4 a[80:83], v248, s[20:23], s92 offen offset:2048 sc1
MM v[68:71],a[156:159],a[240:243],v[68:71]
TR a[136:137],v228 offset:57344
TR a[138:139],v228 offset:59392
MM v[68:71],a[152:155],a[224:227],v[68:71]
MM v[68:71],a[148:151],a[208:211],v[68:71]
TR a[140:141],v228 offset:61440
TR a[142:143],v228 offset:63488
MM v[68:71],a[144:147],a[192:195],v[68:71]
s_waitcnt lgkmcnt(15)
buffer_load_dwordx4 a[112:115], v248, s[20:23], s92 offen offset:3072 sc1
MM v[96:99],a[160:163],a[192:195],v[96:99]
s_add_u32 s92, s92, 4096
TR a[144:145],v229 offset:49152
TR a[146:147],v229 offset:51200
MM v[96:99],a[164:167],a[208:211],v[96:99]
MM v[96:99],a[168:171],a[224:227],v[96:99]
TR a[148:149],v229 offset:53248
TR a[150:151],v229 offset:55296
MM v[96:99],a[172:175],a[240:243],v[96:99]
buffer_load_dwordx4 a[20:23], v248, s[20:23], s92 offen offset:0 sc1
MM v[104:107],a[172:175],a[244:247],v[104:107]
TR a[152:153],v229 offset:57344
TR a[154:155],v229 offset:59392
MM v[104:107],a[168:171],a[228:231],v[104:107]
MM v[104:107],a[164:167],a[212:215],v[104:107]
TR a[156:157],v229 offset:61440
TR a[158:159],v229 offset:63488
MM v[104:107],a[160:163],a[196:199],v[104:107]
s_waitcnt lgkmcnt(15)
buffer_load_dwordx4 a[52:55], v248, s[20:23], s92 offen offset:1024 sc1
MM v[108:111],a[176:179],a[196:199],v[108:111]
TR a[160:161],v230 offset:49152
TR a[162:163],v230 offset:51200
MM v[108:111],a[180:183],a[212:215],v[108:111]
MM v[108:111],a[184:187],a[228:231],v[108:111]
TR a[164:165],v230 offset:53248
TR a[166:167],v230 offset:55296
MM v[108:111],a[188:191],a[244:247],v[108:111]
buffer_load_dwordx4 a[84:87], v248, s[20:23], s92 offen offset:2048 sc1
MM v[100:103],a[188:191],a[240:243],v[100:103]
TR a[168:169],v230 offset:57344
TR a[170:171],v230 offset:59392
MM v[100:103],a[184:187],a[224:227],v[100:103]
MM v[100:103],a[180:183],a[208:211],v[100:103]
TR a[172:173],v230 offset:61440
TR a[174:175],v230 offset:63488
MM v[100:103],a[176:179],a[192:195],v[100:103]
s_waitcnt lgkmcnt(15)
buffer_load_dwordx4 a[116:119], v248, s[20:23], s92 offen offset:3072 sc1
MM v[0:3],a[128:131],a[192:195],v[0:3]
TR a[176:177],v231 offset:49152
TR a[178:179],v231 offset:51200
MM v[0:3],a[132:135],a[208:211],v[0:3]
MM v[0:3],a[136:139],a[224:227],v[0:3]
TR a[180:181],v231 offset:53248
TR a[182:183],v231 offset:55296
MM v[0:3],a[140:143],a[240:243],v[0:3]
MM v[8:11],a[140:143],a[244:247],v[8:11]
TR a[184:185],v231 offset:57344
TR a[186:187],v231 offset:59392
MM v[8:11],a[136:139],a[228:231],v[8:11]
MM v[8:11],a[132:135],a[212:215],v[8:11]
TR a[188:189],v231 offset:61440
TR a[190:191],v231 offset:63488
MM v[8:11],a[128:131],a[196:199],v[8:11]
s_waitcnt lgkmcnt(15)
s_barrier
MM v[12:15],a[144:147],a[196:199],v[12:15]
s_xor_b32 s66, s94, 3
TR a[128:129],v244 offset:0
TR a[130:131],v244 offset:1024
MM v[12:15],a[148:151],a[212:215],v[12:15]
s_mul_i32 s66, s66, 163840
s_add_u32 s66, s66, s88
s_add_u32 m0, s89, 155648
MM v[12:15],a[152:155],a[228:231],v[12:15]
TR a[132:133],v244 offset:2048
TR a[134:135],v244 offset:3072
MM v[12:15],a[156:159],a[244:247],v[12:15]
buffer_load_dwordx4 v252, s[24:27], s66 offen offset:0 lds
MM v[4:7],a[156:159],a[240:243],v[4:7]
s_add_u32 s66, s66, 19456
TR a[136:137],v244 offset:4096
TR a[138:139],v244 offset:5120
MM v[4:7],a[152:155],a[224:227],v[4:7]
MM v[4:7],a[148:151],a[208:211],v[4:7]
TR a[140:141],v244 offset:6144
TR a[142:143],v244 offset:7168
MM v[4:7],a[144:147],a[192:195],v[4:7]
s_waitcnt lgkmcnt(15)
buffer_load_dwordx4 v252, s[24:27], s66 offen offset:1024 lds
MM v[32:35],a[160:163],a[192:195],v[32:35]
TR a[144:145],v235 offset:0
TR a[146:147],v235 offset:1024
MM v[32:35],a[164:167],a[208:211],v[32:35]
s_xor_b32 s66, s94, 3
s_mul_i32 s66, s66, 163840
s_add_u32 s66, s66, s87
MM v[32:35],a[168:171],a[224:227],v[32:35]
s_add_u32 m0, s65, 114688
TR a[148:149],v235 offset:2048
TR a[150:151],v235 offset:3072
MM v[32:35],a[172:175],a[240:243],v[32:35]
buffer_load_dwordx4 v240, s[24:27], s66 offen offset:0 lds
MM v[40:43],a[172:175],a[244:247],v[40:43]
s_add_u32 s66, s66, 9216
TR a[152:153],v235 offset:4096
TR a[154:155],v235 offset:5120
MM v[40:43],a[168:171],a[228:231],v[40:43]
MM v[40:43],a[164:167],a[212:215],v[40:43]
TR a[156:157],v235 offset:6144
TR a[158:159],v235 offset:7168
MM v[40:43],a[160:163],a[196:199],v[40:43]
s_waitcnt lgkmcnt(15)
buffer_load_dwordx4 v240, s[24:27], s66 offen offset:1024 lds
MM v[44:47],a[176:179],a[196:199],v[44:47]
s_add_u32 s66, s66, 9216
TR a[160:161],v144 offset:0
TR a[162:163],v144 offset:2048
MM v[44:47],a[180:183],a[212:215],v[44:47]
MM v[44:47],a[184:187],a[228:231],v[44:47]
TR a[164:165],v144 offset:4096
TR a[166:167],v144 offset:6144
MM v[44:47],a[188:191],a[244:247],v[44:47]
buffer_load_dwordx4 v240, s[24:27], s66 offen offset:2048 lds
MM v[36:39],a[188:191],a[240:243],v[36:39]
s_add_u32 s66, s66, 9216
TR a[168:169],v144 offset:8192
TR a[170:171],v144 offset:10240
MM v[36:39],a[184:187],a[224:227],v[36:39]
MM v[36:39],a[180:183],a[208:211],v[36:39]
TR a[172:173],v144 offset:12288
TR a[174:175],v144 offset:14336
MM v[36:39],a[176:179],a[192:195],v[36:39]
s_waitcnt lgkmcnt(15)
buffer_load_dwordx4 v240, s[24:27], s66 offen offset:3072 lds
MM v[128:131],a[128:131],v[168:171],v[128:131]
TR a[176:177],v145 offset:0
TR a[178:179],v145 offset:2048
MM v[128:131],a[132:135],v[184:187],v[128:131]
MM v[128:131],a[136:139],v[200:203],v[128:131]
TR a[180:181],v145 offset:4096
TR a[182:183],v145 offset:6144
MM v[128:131],a[140:143],v[216:219],v[128:131]
MM v[136:139],a[140:143],v[220:223],v[136:139]
TR a[184:185],v145 offset:8192
TR a[186:187],v145 offset:10240
MM v[136:139],a[136:139],v[204:207],v[136:139]
MM v[136:139],a[132:135],v[188:191],v[136:139]
TR a[188:189],v145 offset:12288
TR a[190:191],v145 offset:14336
MM v[136:139],a[128:131],v[172:175],v[136:139]
s_waitcnt lgkmcnt(15)
MM v[140:143],a[144:147],v[172:175],v[140:143]
s_xor_b32 s92, s49, 2
TR a[128:129],v146 offset:0
TR a[130:131],v146 offset:2048
MM v[140:143],a[148:151],v[188:191],v[140:143]
s_mul_i32 s92, s92, 17408
s_add_u32 s92, s83, s92
s_add_u32 s92, s92, 8192
MM v[140:143],a[152:155],v[204:207],v[140:143]
TR a[132:133],v146 offset:4096
TR a[134:135],v146 offset:6144
MM v[140:143],a[156:159],v[220:223],v[140:143]
buffer_load_dwordx4 a[24:27], v248, s[20:23], s92 offen offset:0 sc1
MM v[132:135],a[156:159],v[216:219],v[132:135]
TR a[136:137],v146 offset:8192
TR a[138:139],v146 offset:10240
MM v[132:135],a[152:155],v[200:203],v[132:135]
buffer_load_dwordx4 a[56:59], v248, s[20:23], s92 offen offset:1024 sc1
MM v[132:135],a[148:151],v[184:187],v[132:135]
TR a[140:141],v146 offset:12288
TR a[142:143],v146 offset:14336
MM v[132:135],a[144:147],v[168:171],v[132:135]
s_waitcnt lgkmcnt(15)
)ASM"
R"ASM(
MM v[64:67],a[160:163],v[168:171],v[64:67]
TR a[144:145],v147 offset:0
TR a[146:147],v147 offset:2048
MM v[64:67],a[164:167],v[184:187],v[64:67]
buffer_load_dwordx4 a[88:91], v248, s[20:23], s92 offen offset:2048 sc1
MM v[64:67],a[168:171],v[200:203],v[64:67]
TR a[148:149],v147 offset:4096
TR a[150:151],v147 offset:6144
MM v[64:67],a[172:175],v[216:219],v[64:67]
MM v[72:75],a[172:175],v[220:223],v[72:75]
TR a[152:153],v147 offset:8192
TR a[154:155],v147 offset:10240
MM v[72:75],a[168:171],v[204:207],v[72:75]
buffer_load_dwordx4 a[120:123], v248, s[20:23], s92 offen offset:3072 sc1
MM v[72:75],a[164:167],v[188:191],v[72:75]
s_add_u32 s92, s92, 4096
TR a[156:157],v147 offset:12288
TR a[158:159],v147 offset:14336
MM v[72:75],a[160:163],v[172:175],v[72:75]
s_waitcnt lgkmcnt(15)
MM v[76:79],a[176:179],v[172:175],v[76:79]
TR a[160:161],v230 offset:0
TR a[162:163],v230 offset:2048
MM v[76:79],a[180:183],v[188:191],v[76:79]
buffer_load_dwordx4 a[28:31], v248, s[20:23], s92 offen offset:0 sc1
MM v[76:79],a[184:187],v[204:207],v[76:79]
TR a[164:165],v230 offset:4096
TR a[166:167],v230 offset:6144
MM v[76:79],a[188:191],v[220:223],v[76:79]
MM v[68:71],a[188:191],v[216:219],v[68:71]
TR a[168:169],v230 offset:8192
TR a[170:171],v230 offset:10240
MM v[68:71],a[184:187],v[200:203],v[68:71]
buffer_load_dwordx4 a[60:63], v248, s[20:23], s92 offen offset:1024 sc1
MM v[68:71],a[180:183],v[184:187],v[68:71]
TR a[172:173],v230 offset:12288
TR a[174:175],v230 offset:14336
MM v[68:71],a[176:179],v[168:171],v[68:71]
s_waitcnt lgkmcnt(15)
MM v[96:99],a[128:131],v[168:171],v[96:99]
TR a[176:177],v231 offset:0
TR a[178:179],v231 offset:2048
MM v[96:99],a[132:135],v[184:187],v[96:99]
buffer_load_dwordx4 a[92:95], v248, s[20:23], s92 offen offset:2048 sc1
MM v[96:99],a[136:139],v[200:203],v[96:99]
TR a[180:181],v231 offset:4096
TR a[182:183],v231 offset:6144
MM v[96:99],a[140:143],v[216:219],v[96:99]
MM v[104:107],a[140:143],v[220:223],v[104:107]
TR a[184:185],v231 offset:8192
TR a[186:187],v231 offset:10240
MM v[104:107],a[136:139],v[204:207],v[104:107]
buffer_load_dwordx4 a[124:127], v248, s[20:23], s92 offen offset:3072 sc1
MM v[104:107],a[132:135],v[188:191],v[104:107]
s_xor_b32 s92, s49, 3
TR a[188:189],v231 offset:12288
TR a[190:191],v231 offset:14336
MM v[104:107],a[128:131],v[172:175],v[104:107]
s_waitcnt lgkmcnt(15)
s_mul_i32 s92, s92, 17408
s_add_u32 s92, s83, s92
s_barrier
MM v[108:111],a[144:147],v[172:175],v[108:111]
s_add_u32 s92, s92, 8192
TR a[128:129],v244 offset:8192
TR a[130:131],v244 offset:9216
MM v[108:111],a[148:151],v[188:191],v[108:111]
buffer_load_dwordx4 a[200:203], v248, s[20:23], s92 offen offset:0 sc1
MM v[108:111],a[152:155],v[204:207],v[108:111]
TR a[132:133],v244 offset:10240
TR a[134:135],v244 offset:11264
MM v[108:111],a[156:159],v[220:223],v[108:111]
MM v[100:103],a[156:159],v[216:219],v[100:103]
TR a[136:137],v244 offset:12288
TR a[138:139],v244 offset:13312
MM v[100:103],a[152:155],v[200:203],v[100:103]
buffer_load_dwordx4 a[216:219], v248, s[20:23], s92 offen offset:1024 sc1
MM v[100:103],a[148:151],v[184:187],v[100:103]
TR a[140:141],v244 offset:14336
TR a[142:143],v244 offset:15360
MM v[100:103],a[144:147],v[168:171],v[100:103]
s_waitcnt lgkmcnt(15)
MM v[32:35],a[160:163],v[168:171],v[32:35]
TR a[144:145],v235 offset:8192
TR a[146:147],v235 offset:9216
MM v[32:35],a[164:167],v[184:187],v[32:35]
buffer_load_dwordx4 a[232:235], v248, s[20:23], s92 offen offset:2048 sc1
MM v[32:35],a[168:171],v[200:203],v[32:35]
TR a[148:149],v235 offset:10240
TR a[150:151],v235 offset:11264
MM v[32:35],a[172:175],v[216:219],v[32:35]
MM v[40:43],a[172:175],v[220:223],v[40:43]
TR a[152:153],v235 offset:12288
TR a[154:155],v235 offset:13312
MM v[40:43],a[168:171],v[204:207],v[40:43]
buffer_load_dwordx4 a[248:251], v248, s[20:23], s92 offen offset:3072 sc1
MM v[40:43],a[164:167],v[188:191],v[40:43]
s_add_u32 s92, s92, 4096
TR a[156:157],v235 offset:14336
TR a[158:159],v235 offset:15360
MM v[40:43],a[160:163],v[172:175],v[40:43]
s_waitcnt lgkmcnt(15)
MM v[44:47],a[176:179],v[172:175],v[44:47]
TR a[160:161],v144 offset:16384
TR a[162:163],v144 offset:18432
MM v[44:47],a[180:183],v[188:191],v[44:47]
buffer_load_dwordx4 a[204:207], v248, s[20:23], s92 offen offset:0 sc1
MM v[44:47],a[184:187],v[204:207],v[44:47]
TR a[164:165],v144 offset:20480
TR a[166:167],v144 offset:22528
MM v[44:47],a[188:191],v[220:223],v[44:47]
MM v[36:39],a[188:191],v[216:219],v[36:39]
TR a[168:169],v144 offset:24576
TR a[170:171],v144 offset:26624
MM v[36:39],a[184:187],v[200:203],v[36:39]
buffer_load_dwordx4 a[220:223], v248, s[20:23], s92 offen offset:1024 sc1
MM v[36:39],a[180:183],v[184:187],v[36:39]
TR a[172:173],v144 offset:28672
TR a[174:175],v144 offset:30720
MM v[36:39],a[176:179],v[168:171],v[36:39]
s_waitcnt lgkmcnt(15)
s_waitcnt vmcnt(24)
MM v[128:131],a[128:131],a[16:19],v[128:131]
TR a[176:177],v145 offset:16384
TR a[178:179],v145 offset:18432
MM v[128:131],a[132:135],a[48:51],v[128:131]
buffer_load_dwordx4 a[236:239], v248, s[20:23], s92 offen offset:2048 sc1
MM v[128:131],a[136:139],a[80:83],v[128:131]
TR a[180:181],v145 offset:20480
TR a[182:183],v145 offset:22528
MM v[128:131],a[140:143],a[112:115],v[128:131]
s_waitcnt vmcnt(21)
MM v[136:139],a[140:143],a[116:119],v[136:139]
TR a[184:185],v145 offset:24576
TR a[186:187],v145 offset:26624
MM v[136:139],a[136:139],a[84:87],v[136:139]
buffer_load_dwordx4 a[252:255], v248, s[20:23], s92 offen offset:3072 sc1
MM v[136:139],a[132:135],a[52:55],v[136:139]
TR a[188:189],v145 offset:28672
TR a[190:191],v145 offset:30720
MM v[136:139],a[128:131],a[20:23],v[136:139]
s_waitcnt lgkmcnt(15)
MM v[140:143],a[144:147],a[20:23],v[140:143]
TR a[128:129],v146 offset:16384
TR a[130:131],v146 offset:18432
MM v[140:143],a[148:151],a[52:55],v[140:143]
MM v[140:143],a[152:155],a[84:87],v[140:143]
TR a[132:133],v146 offset:20480
TR a[134:135],v146 offset:22528
MM v[140:143],a[156:159],a[116:119],v[140:143]
MM v[132:135],a[156:159],a[112:115],v[132:135]
TR a[136:137],v146 offset:24576
TR a[138:139],v146 offset:26624
MM v[132:135],a[152:155],a[80:83],v[132:135]
MM v[132:135],a[148:151],a[48:51],v[132:135]
TR a[140:141],v146 offset:28672
TR a[142:143],v146 offset:30720
MM v[132:135],a[144:147],a[16:19],v[132:135]
s_waitcnt lgkmcnt(15)
MM v[64:67],a[160:163],a[16:19],v[64:67]
TR a[144:145],v147 offset:16384
TR a[146:147],v147 offset:18432
MM v[64:67],a[164:167],a[48:51],v[64:67]
MM v[64:67],a[168:171],a[80:83],v[64:67]
TR a[148:149],v147 offset:20480
TR a[150:151],v147 offset:22528
MM v[64:67],a[172:175],a[112:115],v[64:67]
MM v[72:75],a[172:175],a[116:119],v[72:75]
TR a[152:153],v147 offset:24576
TR a[154:155],v147 offset:26624
MM v[72:75],a[168:171],a[84:87],v[72:75]
MM v[72:75],a[164:167],a[52:55],v[72:75]
TR a[156:157],v147 offset:28672
TR a[158:159],v147 offset:30720
MM v[72:75],a[160:163],a[20:23],v[72:75]
s_waitcnt lgkmcnt(15)
s_waitcnt vmcnt(16)
s_barrier
MM v[76:79],a[176:179],a[20:23],v[76:79]
s_xor_b32 s66, s94, 1
TR a[160:161],v244 offset:16384
TR a[162:163],v244 offset:17408
MM v[76:79],a[180:183],a[52:55],v[76:79]
s_mul_i32 s66, s66, 163840
s_add_u32 s66, s66, s86
s_add_u32 m0, s65, 16384
MM v[76:79],a[184:187],a[84:87],v[76:79]
TR a[164:165],v244 offset:18432
TR a[166:167],v244 offset:19456
MM v[76:79],a[188:191],a[116:119],v[76:79]
buffer_load_dwordx4 v240, s[24:27], s66 offen offset:0 lds
MM v[68:71],a[188:191],a[112:115],v[68:71]
s_add_u32 s66, s66, 9216
TR a[168:169],v244 offset:20480
TR a[170:171],v244 offset:21504
MM v[68:71],a[184:187],a[80:83],v[68:71]
MM v[68:71],a[180:183],a[48:51],v[68:71]
TR a[172:173],v244 offset:22528
TR a[174:175],v244 offset:23552
MM v[68:71],a[176:179],a[16:19],v[68:71]
s_waitcnt lgkmcnt(15)
buffer_load_dwordx4 v240, s[24:27], s66 offen offset:1024 lds
MM v[96:99],a[128:131],a[16:19],v[96:99]
s_add_u32 s66, s66, 9216
TR a[176:177],v244 offset:24576
TR a[178:179],v244 offset:25600
MM v[96:99],a[132:135],a[48:51],v[96:99]
MM v[96:99],a[136:139],a[80:83],v[96:99]
TR a[180:181],v244 offset:26624
TR a[182:183],v244 offset:27648
MM v[96:99],a[140:143],a[112:115],v[96:99]
buffer_load_dwordx4 v240, s[24:27], s66 offen offset:2048 lds
MM v[104:107],a[140:143],a[116:119],v[104:107]
s_add_u32 s66, s66, 9216
TR a[184:185],v244 offset:28672
TR a[186:187],v244 offset:29696
MM v[104:107],a[136:139],a[84:87],v[104:107]
MM v[104:107],a[132:135],a[52:55],v[104:107]
TR a[188:189],v244 offset:30720
TR a[190:191],v244 offset:31744
MM v[104:107],a[128:131],a[20:23],v[104:107]
s_waitcnt lgkmcnt(15)
buffer_load_dwordx4 v240, s[24:27], s66 offen offset:3072 lds
MM v[108:111],a[144:147],a[20:23],v[108:111]
TR a[128:129],v235 offset:16384
TR a[130:131],v235 offset:17408
MM v[108:111],a[148:151],a[52:55],v[108:111]
s_xor_b32 s66, s94, 2
s_mul_i32 s66, s66, 163840
s_add_u32 s66, s66, s86
MM v[108:111],a[152:155],a[84:87],v[108:111]
s_add_u32 m0, s65, 32768
TR a[132:133],v235 offset:18432
TR a[134:135],v235 offset:19456
MM v[108:111],a[156:159],a[116:119],v[108:111]
buffer_load_dwordx4 v240, s[24:27], s66 offen offset:0 lds
MM v[100:103],a[156:159],a[112:115],v[100:103]
s_add_u32 s66, s66, 9216
TR a[136:137],v235 offset:20480
TR a[138:139],v235 offset:21504
MM v[100:103],a[152:155],a[80:83],v[100:103]
MM v[100:103],a[148:151],a[48:51],v[100:103]
TR a[140:141],v235 offset:22528
TR a[142:143],v235 offset:23552
MM v[100:103],a[144:147],a[16:19],v[100:103]
s_waitcnt lgkmcnt(15)
buffer_load_dwordx4 v240, s[24:27], s66 offen offset:1024 lds
s_waitcnt vmcnt(18)
MM v[128:131],a[160:163],a[24:27],v[128:131]
s_add_u32 s66, s66, 9216
TR a[144:145],v235 offset:24576
TR a[146:147],v235 offset:25600
MM v[128:131],a[164:167],a[56:59],v[128:131]
MM v[128:131],a[168:171],a[88:91],v[128:131]
TR a[148:149],v235 offset:26624
TR a[150:151],v235 offset:27648
MM v[128:131],a[172:175],a[120:123],v[128:131]
s_waitcnt vmcnt(14)
buffer_load_dwordx4 v240, s[24:27], s66 offen offset:2048 lds
MM v[136:139],a[172:175],a[124:127],v[136:139]
s_add_u32 s66, s66, 9216
TR a[152:153],v235 offset:28672
TR a[154:155],v235 offset:29696
MM v[136:139],a[168:171],a[92:95],v[136:139]
MM v[136:139],a[164:167],a[60:63],v[136:139]
TR a[156:157],v235 offset:30720
TR a[158:159],v235 offset:31744
MM v[136:139],a[160:163],a[28:31],v[136:139]
s_waitcnt lgkmcnt(15)
buffer_load_dwordx4 v240, s[24:27], s66 offen offset:3072 lds
s_waitcnt vmcnt(12)
MM v[128:131],a[176:179],a[200:203],v[128:131]
s_memrealtime s[56:57]
TR a[160:161],v144 offset:32768
TR a[162:163],v144 offset:34816
MM v[128:131],a[180:183],a[216:219],v[128:131]
s_xor_b32 s66, s94, 3
s_mul_i32 s66, s66, 163840
s_add_u32 s66, s66, s86
MM v[128:131],a[184:187],a[232:235],v[128:131]
s_add_u32 m0, s65, 49152
TR a[164:165],v144 offset:36864
TR a[166:167],v144 offset:38912
MM v[128:131],a[188:191],a[248:251],v[128:131]
s_waitcnt vmcnt(8)
buffer_load_dwordx4 v240, s[24:27], s66 offen offset:0 lds
MM v[136:139],a[188:191],a[252:255],v[136:139]
s_add_u32 s66, s66, 9216
TR a[168:169],v144 offset:40960
TR a[170:171],v144 offset:43008
MM v[136:139],a[184:187],a[236:239],v[136:139]
MM v[136:139],a[180:183],a[220:223],v[136:139]
TR a[172:173],v144 offset:45056
TR a[174:175],v144 offset:47104
MM v[136:139],a[176:179],a[204:207],v[136:139]
s_waitcnt lgkmcnt(0)
buffer_load_dwordx4 v240, s[24:27], s66 offen offset:1024 lds
MM v[140:143],a[128:131],a[28:31],v[140:143]
s_add_u32 s66, s66, 9216
TR a[176:177],v144 offset:49152
TR a[178:179],v144 offset:51200
MM v[140:143],a[132:135],a[60:63],v[140:143]
v_mov_b32 v158, s56
v_mov_b32 v159, s69
MM v[140:143],a[136:139],a[92:95],v[140:143]
TR a[180:181],v144 offset:53248
TR a[182:183],v144 offset:55296
MM v[140:143],a[140:143],a[124:127],v[140:143]
s_add_u32 s67, s95, 0
CV v128,v128,v129
CV v129,v130,v131
MM v[132:135],a[140:143],a[120:123],v[132:135]
buffer_load_dwordx4 v240, s[24:27], s66 offen offset:2048 lds
s_mov_b64 exec, 1
buffer_store_dwordx2 v[158:159], off, s[20:23], s19 offset:8 sc1
s_mov_b64 exec, -1
s_nop 0
TR a[184:185],v144 offset:57344
TR a[186:187],v144 offset:59392
MM v[132:135],a[136:139],a[88:91],v[132:135]
)ASM"
R"ASM(
s_add_u32 s66, s66, 9216
CV v136,v136,v137
CV v137,v138,v139
MM v[132:135],a[132:135],a[56:59],v[132:135]
TR a[188:189],v144 offset:61440
TR a[190:191],v144 offset:63488
MM v[132:135],a[128:131],a[24:27],v[132:135]
buffer_load_dwordx4 v240, s[24:27], s66 offen offset:3072 lds
MM v[132:135],a[144:147],a[200:203],v[132:135]
TR a[128:129],v145 offset:32768
TR a[130:131],v145 offset:34816
MM v[132:135],a[148:151],a[216:219],v[132:135]
MM v[132:135],a[152:155],a[232:235],v[132:135]
TR a[132:133],v145 offset:36864
TR a[134:135],v145 offset:38912
MM v[132:135],a[156:159],a[248:251],v[132:135]
MM v[140:143],a[156:159],a[252:255],v[140:143]
TR a[136:137],v145 offset:40960
TR a[138:139],v145 offset:43008
MM v[140:143],a[152:155],a[236:239],v[140:143]
MM v[140:143],a[148:151],a[220:223],v[140:143]
TR a[140:141],v145 offset:45056
TR a[142:143],v145 offset:47104
MM v[140:143],a[144:147],a[204:207],v[140:143]
CV v130,v132,v133
CV v131,v134,v135
MM v[72:75],a[160:163],a[28:31],v[72:75]
TR a[144:145],v145 offset:49152
TR a[146:147],v145 offset:51200
MM v[72:75],a[164:167],a[60:63],v[72:75]
v_min_u32 v131, -2, v131
LPG29_%=:
s_cbranch_execz LTR1_%=
MM v[72:75],a[168:171],a[92:95],v[72:75]
TR a[148:149],v145 offset:53248
TR a[150:151],v145 offset:55296
MM v[72:75],a[172:175],a[124:127],v[72:75]
buffer_store_dwordx4 v[128:131], v249, s[20:23], s67 offen offset:0 sc1
MM v[64:67],a[172:175],a[120:123],v[64:67]
TR a[152:153],v145 offset:57344
TR a[154:155],v145 offset:59392
MM v[64:67],a[168:171],a[88:91],v[64:67]
CV v138,v140,v141
CV v139,v142,v143
MM v[64:67],a[164:167],a[56:59],v[64:67]
TR a[156:157],v145 offset:61440
TR a[158:159],v145 offset:63488
MM v[64:67],a[160:163],a[24:27],v[64:67]
s_waitcnt lgkmcnt(15)
v_min_u32 v139, -2, v139
MM v[64:67],a[176:179],a[200:203],v[64:67]
TR a[160:161],v146 offset:32768
TR a[162:163],v146 offset:34816
MM v[64:67],a[180:183],a[216:219],v[64:67]
buffer_store_dwordx4 v[136:139], v249, s[20:23], s67 offen offset:1024 sc1
MM v[64:67],a[184:187],a[232:235],v[64:67]
TR a[164:165],v146 offset:36864
TR a[166:167],v146 offset:38912
MM v[64:67],a[188:191],a[248:251],v[64:67]
MM v[72:75],a[188:191],a[252:255],v[72:75]
TR a[168:169],v146 offset:40960
TR a[170:171],v146 offset:43008
MM v[72:75],a[184:187],a[236:239],v[72:75]
MM v[72:75],a[180:183],a[220:223],v[72:75]
TR a[172:173],v146 offset:45056
TR a[174:175],v146 offset:47104
MM v[72:75],a[176:179],a[204:207],v[72:75]
s_waitcnt lgkmcnt(15)
MM v[76:79],a[128:131],a[28:31],v[76:79]
TR a[176:177],v146 offset:49152
TR a[178:179],v146 offset:51200
MM v[76:79],a[132:135],a[60:63],v[76:79]
MM v[76:79],a[136:139],a[92:95],v[76:79]
TR a[180:181],v146 offset:53248
TR a[182:183],v146 offset:55296
MM v[76:79],a[140:143],a[124:127],v[76:79]
s_add_u32 s67, s95, 4096
CV v64,v64,v65
CV v65,v66,v67
MM v[68:71],a[140:143],a[120:123],v[68:71]
TR a[184:185],v146 offset:57344
TR a[186:187],v146 offset:59392
MM v[68:71],a[136:139],a[88:91],v[68:71]
CV v72,v72,v73
CV v73,v74,v75
MM v[68:71],a[132:135],a[56:59],v[68:71]
TR a[188:189],v146 offset:61440
TR a[190:191],v146 offset:63488
MM v[68:71],a[128:131],a[24:27],v[68:71]
s_waitcnt lgkmcnt(15)
MM v[68:71],a[144:147],a[200:203],v[68:71]
TR a[128:129],v147 offset:32768
TR a[130:131],v147 offset:34816
MM v[68:71],a[148:151],a[216:219],v[68:71]
buffer_load_dwordx2 v[148:149], v253, s[20:23], s68 offen offset:8 sc1
MM v[68:71],a[152:155],a[232:235],v[68:71]
TR a[132:133],v147 offset:36864
TR a[134:135],v147 offset:38912
MM v[68:71],a[156:159],a[248:251],v[68:71]
MM v[76:79],a[156:159],a[252:255],v[76:79]
TR a[136:137],v147 offset:40960
TR a[138:139],v147 offset:43008
MM v[76:79],a[152:155],a[236:239],v[76:79]
MM v[76:79],a[148:151],a[220:223],v[76:79]
TR a[140:141],v147 offset:45056
TR a[142:143],v147 offset:47104
MM v[76:79],a[144:147],a[204:207],v[76:79]
s_waitcnt lgkmcnt(15)
CV v66,v68,v69
CV v67,v70,v71
s_waitcnt vmcnt(3)
s_barrier
MM v[104:107],a[160:163],a[28:31],v[104:107]
TR a[144:145],v147 offset:49152
TR a[146:147],v147 offset:51200
MM v[104:107],a[164:167],a[60:63],v[104:107]
v_min_u32 v67, -2, v67
MM v[104:107],a[168:171],a[92:95],v[104:107]
TR a[148:149],v147 offset:53248
TR a[150:151],v147 offset:55296
MM v[104:107],a[172:175],a[124:127],v[104:107]
buffer_store_dwordx4 v[64:67], v248, s[20:23], s67 offen offset:0 sc1
MM v[96:99],a[172:175],a[120:123],v[96:99]
TR a[152:153],v147 offset:57344
TR a[154:155],v147 offset:59392
MM v[96:99],a[168:171],a[88:91],v[96:99]
CV v74,v76,v77
CV v75,v78,v79
MM v[96:99],a[164:167],a[56:59],v[96:99]
TR a[156:157],v147 offset:61440
TR a[158:159],v147 offset:63488
MM v[96:99],a[160:163],a[24:27],v[96:99]
s_waitcnt lgkmcnt(15)
v_min_u32 v75, -2, v75
MM v[96:99],a[176:179],a[200:203],v[96:99]
TR a[160:161],v230 offset:16384
TR a[162:163],v230 offset:18432
MM v[96:99],a[180:183],a[216:219],v[96:99]
buffer_store_dwordx4 v[72:75], v248, s[20:23], s67 offen offset:1024 sc1
MM v[96:99],a[184:187],a[232:235],v[96:99]
TR a[164:165],v230 offset:20480
TR a[166:167],v230 offset:22528
MM v[96:99],a[188:191],a[248:251],v[96:99]
MM v[104:107],a[188:191],a[252:255],v[104:107]
TR a[168:169],v230 offset:24576
TR a[170:171],v230 offset:26624
MM v[104:107],a[184:187],a[236:239],v[104:107]
MM v[104:107],a[180:183],a[220:223],v[104:107]
TR a[172:173],v230 offset:28672
TR a[174:175],v230 offset:30720
MM v[104:107],a[176:179],a[204:207],v[104:107]
s_waitcnt lgkmcnt(15)
MM v[108:111],a[128:131],a[28:31],v[108:111]
TR a[176:177],v230 offset:32768
TR a[178:179],v230 offset:34816
MM v[108:111],a[132:135],a[60:63],v[108:111]
MM v[108:111],a[136:139],a[92:95],v[108:111]
TR a[180:181],v230 offset:36864
TR a[182:183],v230 offset:38912
MM v[108:111],a[140:143],a[124:127],v[108:111]
s_add_u32 s67, s95, 4096
CV v96,v96,v97
CV v97,v98,v99
MM v[100:103],a[140:143],a[120:123],v[100:103]
TR a[184:185],v230 offset:40960
TR a[186:187],v230 offset:43008
MM v[100:103],a[136:139],a[88:91],v[100:103]
CV v104,v104,v105
CV v105,v106,v107
MM v[100:103],a[132:135],a[56:59],v[100:103]
TR a[188:189],v230 offset:45056
TR a[190:191],v230 offset:47104
MM v[100:103],a[128:131],a[24:27],v[100:103]
s_waitcnt lgkmcnt(15)
MM v[100:103],a[144:147],a[200:203],v[100:103]
TR a[128:129],v230 offset:49152
TR a[130:131],v230 offset:51200
MM v[100:103],a[148:151],a[216:219],v[100:103]
MM v[100:103],a[152:155],a[232:235],v[100:103]
TR a[132:133],v230 offset:53248
TR a[134:135],v230 offset:55296
MM v[100:103],a[156:159],a[248:251],v[100:103]
MM v[108:111],a[156:159],a[252:255],v[108:111]
TR a[136:137],v230 offset:57344
TR a[138:139],v230 offset:59392
MM v[108:111],a[152:155],a[236:239],v[108:111]
MM v[108:111],a[148:151],a[220:223],v[108:111]
TR a[140:141],v230 offset:61440
TR a[142:143],v230 offset:63488
MM v[108:111],a[144:147],a[204:207],v[108:111]
s_waitcnt lgkmcnt(15)
CV v98,v100,v101
CV v99,v102,v103
MM v[40:43],a[160:163],a[20:23],v[40:43]
TR a[144:145],v231 offset:16384
TR a[146:147],v231 offset:18432
MM v[40:43],a[164:167],a[52:55],v[40:43]
v_min_u32 v99, -2, v99
v_lshlrev_b32 v136, 16, v150
MM v[40:43],a[168:171],a[84:87],v[40:43]
TR a[148:149],v231 offset:20480
TR a[150:151],v231 offset:22528
MM v[40:43],a[172:175],a[116:119],v[40:43]
buffer_store_dwordx4 v[96:99], v248, s[20:23], s67 offen offset:2048 sc1
MM v[32:35],a[172:175],a[112:115],v[32:35]
TR a[152:153],v231 offset:24576
TR a[154:155],v231 offset:26624
MM v[32:35],a[168:171],a[80:83],v[32:35]
CV v106,v108,v109
CV v107,v110,v111
MM v[32:35],a[164:167],a[48:51],v[32:35]
TR a[156:157],v231 offset:28672
TR a[158:159],v231 offset:30720
MM v[32:35],a[160:163],a[16:19],v[32:35]
s_waitcnt lgkmcnt(15)
v_min_u32 v107, -2, v107
v_and_b32 v137, 0xffff0000, v150
MM v[32:35],a[176:179],a[24:27],v[32:35]
TR a[160:161],v231 offset:32768
TR a[162:163],v231 offset:34816
MM v[32:35],a[180:183],a[56:59],v[32:35]
buffer_store_dwordx4 v[104:107], v248, s[20:23], s67 offen offset:3072 sc1
MM v[32:35],a[184:187],a[88:91],v[32:35]
TR a[164:165],v231 offset:36864
TR a[166:167],v231 offset:38912
MM v[32:35],a[188:191],a[120:123],v[32:35]
v_lshlrev_b32 v138, 16, v151
v_and_b32 v139, 0xffff0000, v151
MM v[40:43],a[188:191],a[124:127],v[40:43]
TR a[168:169],v231 offset:40960
TR a[170:171],v231 offset:43008
MM v[40:43],a[184:187],a[92:95],v[40:43]
v_mul_f32 v140, 0xbfb8aa3b, v136
v_mul_f32 v141, 0xbfb8aa3b, v137
MM v[40:43],a[180:183],a[60:63],v[40:43]
TR a[172:173],v231 offset:45056
TR a[174:175],v231 offset:47104
MM v[40:43],a[176:179],a[28:31],v[40:43]
s_waitcnt lgkmcnt(15)
v_mul_f32 v142, 0xbfb8aa3b, v138
v_mul_f32 v143, 0xbfb8aa3b, v139
MM v[40:43],a[128:131],a[204:207],v[40:43]
TR a[176:177],v231 offset:49152
TR a[178:179],v231 offset:51200
MM v[40:43],a[132:135],a[220:223],v[40:43]
EX v140,v140
MM v[40:43],a[136:139],a[236:239],v[40:43]
TR a[180:181],v231 offset:53248
TR a[182:183],v231 offset:55296
MM v[40:43],a[140:143],a[252:255],v[40:43]
EX v141,v141
MM v[32:35],a[140:143],a[248:251],v[32:35]
TR a[184:185],v231 offset:57344
TR a[186:187],v231 offset:59392
MM v[32:35],a[136:139],a[232:235],v[32:35]
EX v142,v142
MM v[32:35],a[132:135],a[216:219],v[32:35]
TR a[188:189],v231 offset:61440
TR a[190:191],v231 offset:63488
MM v[32:35],a[128:131],a[200:203],v[32:35]
s_waitcnt lgkmcnt(15)
EX v143,v143
MM v[36:39],a[144:147],a[16:19],v[36:39]
TR a[128:129],v228 offset:0
TR a[130:131],v228 offset:2048
MM v[36:39],a[148:151],a[48:51],v[36:39]
v_add_f32 v140, 1.0, v140
v_add_f32 v141, 1.0, v141
MM v[36:39],a[152:155],a[80:83],v[36:39]
TR a[132:133],v228 offset:4096
TR a[134:135],v228 offset:6144
MM v[36:39],a[156:159],a[112:115],v[36:39]
s_add_u32 s67, s95, 0
CV v32,v32,v33
CV v33,v34,v35
MM v[44:47],a[156:159],a[116:119],v[44:47]
TR a[136:137],v228 offset:8192
TR a[138:139],v228 offset:10240
MM v[44:47],a[152:155],a[84:87],v[44:47]
CV v40,v40,v41
CV v41,v42,v43
MM v[44:47],a[148:151],a[52:55],v[44:47]
TR a[140:141],v228 offset:12288
TR a[142:143],v228 offset:14336
MM v[44:47],a[144:147],a[20:23],v[44:47]
s_waitcnt lgkmcnt(15)
v_add_f32 v142, 1.0, v142
v_add_f32 v143, 1.0, v143
MM v[44:47],a[160:163],a[28:31],v[44:47]
s_waitcnt vmcnt(4)
TR a[144:145],v228 offset:16384
TR a[146:147],v228 offset:18432
MM v[44:47],a[164:167],a[60:63],v[44:47]
v_cmp_eq_u32 s[8:9], s69, v149
v_subrev_u32 v226, s56, v148
MM v[44:47],a[168:171],a[92:95],v[44:47]
TR a[148:149],v228 offset:20480
TR a[150:151],v228 offset:22528
MM v[44:47],a[172:175],a[124:127],v[44:47]
v_rcp_f32 v140, v140
MM v[36:39],a[172:175],a[120:123],v[36:39]
TR a[152:153],v228 offset:24576
TR a[154:155],v228 offset:26624
v_cndmask_b32 v226, 4.0, v226, s[8:9]
MM v[36:39],a[168:171],a[88:91],v[36:39]
v_readlane_b32 s13, v226, 16
v_readlane_b32 s14, v226, 32
MM v[36:39],a[164:167],a[56:59],v[36:39]
TR a[156:157],v228 offset:28672
TR a[158:159],v228 offset:30720
MM v[36:39],a[160:163],a[24:27],v[36:39]
s_waitcnt lgkmcnt(15)
v_rcp_f32 v141, v141
v_readlane_b32 s15, v226, 48
MM v[36:39],a[176:179],a[200:203],v[36:39]
s_max_i32 s12, s13, s14
TR a[160:161],v228 offset:32768
TR a[162:163],v228 offset:34816
MM v[36:39],a[180:183],a[216:219],v[36:39]
s_max_i32 s12, s12, s15
v_rcp_f32 v142, v142
MM v[36:39],a[184:187],a[232:235],v[36:39]
s_max_i32 s12, s12, 0
TR a[164:165],v228 offset:36864
TR a[166:167],v228 offset:38912
MM v[36:39],a[188:191],a[248:251],v[36:39]
buffer_load_dwordx2 v[148:149], v253, s[20:23], s68 offen offset:8 sc1
MM v[44:47],a[188:191],a[252:255],v[44:47]
TR a[168:169],v228 offset:40960
TR a[170:171],v228 offset:43008
MM v[44:47],a[184:187],a[236:239],v[44:47]
v_rcp_f32 v143, v143
MM v[44:47],a[180:183],a[220:223],v[44:47]
TR a[172:173],v228 offset:45056
TR a[174:175],v228 offset:47104
MM v[44:47],a[176:179],a[204:207],v[44:47]
s_waitcnt lgkmcnt(15)
CV v34,v36,v37
CV v35,v38,v39
v_min_u32 v35, -2, v35
buffer_store_dwordx4 v[32:35], v248, s[20:23], s67 offen offset:2048 sc1
MM v[8:11],a[128:131],v[172:175],v[8:11]
TR a[176:177],v228 offset:49152
TR a[178:179],v228 offset:51200
MM v[8:11],a[132:135],v[188:191],v[8:11]
v_mul_f32 v140, v136, v140
v_mul_f32 v141, v137, v141
MM v[8:11],a[136:139],v[204:207],v[8:11]
TR a[180:181],v228 offset:53248
)ASM"
R"ASM(
TR a[182:183],v228 offset:55296
MM v[8:11],a[140:143],v[220:223],v[8:11]
CV v42,v44,v45
CV v43,v46,v47
v_min_u32 v43, -2, v43
buffer_store_dwordx4 v[40:43], v248, s[20:23], s67 offen offset:3072 sc1
MM v[0:3],a[140:143],v[216:219],v[0:3]
TR a[184:185],v228 offset:57344
TR a[186:187],v228 offset:59392
MM v[0:3],a[136:139],v[200:203],v[0:3]
v_mul_f32 v142, v138, v142
v_mul_f32 v143, v139, v143
MM v[0:3],a[132:135],v[184:187],v[0:3]
TR a[188:189],v228 offset:61440
TR a[190:191],v228 offset:63488
MM v[0:3],a[128:131],v[168:171],v[0:3]
s_waitcnt lgkmcnt(15)
CV v150,v140,v141
CV v151,v142,v143
MM v[0:3],a[144:147],a[16:19],v[0:3]
TR a[128:129],v229 offset:0
TR a[130:131],v229 offset:2048
MM v[0:3],a[148:151],a[48:51],v[0:3]
s_cmp_le_i32 s12, 76
s_cselect_b32 s60, 1, 0
s_cselect_b64 s[8:9], -1, 0
MM v[0:3],a[152:155],a[80:83],v[0:3]
s_or_b32 s58, s58, s60
TR a[132:133],v229 offset:4096
TR a[134:135],v229 offset:6144
MM v[0:3],a[156:159],a[112:115],v[0:3]
s_mov_b64 exec, s[8:9]
buffer_load_dwordx4 v[56:59], v250, s[20:23], s39 offen offset:0 sc1
s_mov_b64 exec, -1
MM v[8:11],a[156:159],a[116:119],v[8:11]
TR a[136:137],v229 offset:8192
TR a[138:139],v229 offset:10240
MM v[8:11],a[152:155],a[84:87],v[8:11]
s_mov_b64 exec, s[8:9]
buffer_load_dwordx4 v[60:63], v250, s[20:23], s39 offen offset:256 sc1
s_mov_b64 exec, -1
MM v[8:11],a[148:151],a[52:55],v[8:11]
TR a[140:141],v229 offset:12288
TR a[142:143],v229 offset:14336
MM v[8:11],a[144:147],a[20:23],v[8:11]
s_waitcnt lgkmcnt(15)
v_lshlrev_b32 v160, 16, v80
v_and_b32 v161, 0xffff0000, v80
MM v[8:11],a[160:163],a[28:31],v[8:11]
TR a[144:145],v229 offset:16384
TR a[146:147],v229 offset:18432
MM v[8:11],a[164:167],a[60:63],v[8:11]
s_cmp_le_i32 s14, 51
s_cselect_b32 s60, 4, 0
s_cselect_b64 s[8:9], -1, 0
MM v[8:11],a[168:171],a[92:95],v[8:11]
s_or_b32 s58, s58, s60
TR a[148:149],v229 offset:20480
TR a[150:151],v229 offset:22528
MM v[8:11],a[172:175],a[124:127],v[8:11]
s_mov_b64 exec, s[8:9]
buffer_load_dwordx4 v[24:27], v248, s[20:23], s37 offen offset:0 sc1
s_mov_b64 exec, -1
MM v[0:3],a[172:175],a[120:123],v[0:3]
TR a[152:153],v229 offset:24576
TR a[154:155],v229 offset:26624
MM v[0:3],a[168:171],a[88:91],v[0:3]
s_mov_b64 exec, s[8:9]
buffer_load_dwordx4 v[28:31], v248, s[20:23], s37 offen offset:1024 sc1
s_mov_b64 exec, -1
MM v[0:3],a[164:167],a[56:59],v[0:3]
TR a[156:157],v229 offset:28672
TR a[158:159],v229 offset:30720
MM v[0:3],a[160:163],a[24:27],v[0:3]
s_waitcnt lgkmcnt(15)
v_lshlrev_b32 v162, 16, v81
v_and_b32 v163, 0xffff0000, v81
MM v[0:3],a[176:179],a[200:203],v[0:3]
TR a[160:161],v229 offset:32768
TR a[162:163],v229 offset:34816
MM v[0:3],a[180:183],a[216:219],v[0:3]
s_cmp_le_i32 s15, 25
s_cselect_b32 s60, 8, 0
s_cselect_b64 s[8:9], -1, 0
MM v[0:3],a[184:187],a[232:235],v[0:3]
s_or_b32 s58, s58, s60
TR a[164:165],v229 offset:36864
TR a[166:167],v229 offset:38912
MM v[0:3],a[188:191],a[248:251],v[0:3]
s_mov_b64 exec, s[8:9]
buffer_load_dwordx4 v[48:51], v248, s[20:23], s38 offen offset:0 sc1
s_mov_b64 exec, -1
MM v[8:11],a[188:191],a[252:255],v[8:11]
TR a[168:169],v229 offset:40960
TR a[170:171],v229 offset:43008
MM v[8:11],a[184:187],a[236:239],v[8:11]
s_mov_b64 exec, s[8:9]
buffer_load_dwordx4 v[52:55], v248, s[20:23], s38 offen offset:1024 sc1
s_mov_b64 exec, -1
MM v[8:11],a[180:183],a[220:223],v[8:11]
TR a[172:173],v229 offset:45056
TR a[174:175],v229 offset:47104
MM v[8:11],a[176:179],a[204:207],v[8:11]
s_waitcnt lgkmcnt(15)
v_lshlrev_b32 v164, 16, v82
v_and_b32 v165, 0xffff0000, v82
MM v[12:15],a[128:131],v[172:175],v[12:15]
TR a[176:177],v229 offset:49152
TR a[178:179],v229 offset:51200
MM v[12:15],a[132:135],v[188:191],v[12:15]
v_lshlrev_b32 v166, 16, v83
v_and_b32 v167, 0xffff0000, v83
MM v[12:15],a[136:139],v[204:207],v[12:15]
TR a[180:181],v229 offset:53248
TR a[182:183],v229 offset:55296
MM v[12:15],a[140:143],v[220:223],v[12:15]
v_mul_f32 v136, 0xbfb8aa3b, v160
v_mul_f32 v137, 0xbfb8aa3b, v161
MM v[4:7],a[140:143],v[216:219],v[4:7]
TR a[184:185],v229 offset:57344
TR a[186:187],v229 offset:59392
MM v[4:7],a[136:139],v[200:203],v[4:7]
v_mul_f32 v138, 0xbfb8aa3b, v162
v_mul_f32 v139, 0xbfb8aa3b, v163
MM v[4:7],a[132:135],v[184:187],v[4:7]
TR a[188:189],v229 offset:61440
TR a[190:191],v229 offset:63488
MM v[4:7],a[128:131],v[168:171],v[4:7]
s_waitcnt lgkmcnt(15)
v_mul_f32 v140, 0xbfb8aa3b, v164
v_mul_f32 v141, 0xbfb8aa3b, v165
MM v[4:7],a[144:147],a[16:19],v[4:7]
s_cmp_le_i32 s13, -8
s_cselect_b32 s60, 2, 0
s_cselect_b64 s[8:9], -1, 0
MM v[4:7],a[148:151],a[48:51],v[4:7]
s_or_b32 s58, s58, s60
s_mov_b64 exec, s[8:9]
buffer_load_dwordx4 v[16:19], v248, s[20:23], s36 offen offset:0 sc1
s_mov_b64 exec, -1
MM v[4:7],a[152:155],a[80:83],v[4:7]
v_mul_f32 v142, 0xbfb8aa3b, v166
v_mul_f32 v143, 0xbfb8aa3b, v167
MM v[4:7],a[156:159],a[112:115],v[4:7]
s_mov_b64 exec, s[8:9]
buffer_load_dwordx4 v[20:23], v248, s[20:23], s36 offen offset:1024 sc1
s_mov_b64 exec, -1
MM v[12:15],a[156:159],a[116:119],v[12:15]
EX v136,v136
MM v[12:15],a[152:155],a[84:87],v[12:15]
EX v137,v137
MM v[12:15],a[148:151],a[52:55],v[12:15]
EX v138,v138
MM v[12:15],a[144:147],a[20:23],v[12:15]
s_waitcnt lgkmcnt(8)
EX v139,v139
MM v[12:15],a[160:163],a[28:31],v[12:15]
EX v140,v140
MM v[12:15],a[164:167],a[60:63],v[12:15]
EX v141,v141
MM v[12:15],a[168:171],a[92:95],v[12:15]
EX v142,v142
MM v[12:15],a[172:175],a[124:127],v[12:15]
EX v143,v143
MM v[4:7],a[172:175],a[120:123],v[4:7]
v_add_f32 v136, 1.0, v136
v_add_f32 v137, 1.0, v137
MM v[4:7],a[168:171],a[88:91],v[4:7]
v_add_f32 v138, 1.0, v138
v_add_f32 v139, 1.0, v139
MM v[4:7],a[164:167],a[56:59],v[4:7]
v_add_f32 v140, 1.0, v140
v_add_f32 v141, 1.0, v141
MM v[4:7],a[160:163],a[24:27],v[4:7]
s_waitcnt lgkmcnt(0)
v_add_f32 v142, 1.0, v142
v_add_f32 v143, 1.0, v143
MM v[4:7],a[176:179],a[200:203],v[4:7]
v_rcp_f32 v136, v136
MM v[4:7],a[180:183],a[216:219],v[4:7]
v_rcp_f32 v137, v137
MM v[4:7],a[184:187],a[232:235],v[4:7]
v_rcp_f32 v138, v138
MM v[4:7],a[188:191],a[248:251],v[4:7]
v_rcp_f32 v139, v139
MM v[12:15],a[188:191],a[252:255],v[12:15]
v_rcp_f32 v140, v140
MM v[12:15],a[184:187],a[236:239],v[12:15]
v_rcp_f32 v141, v141
MM v[12:15],a[180:183],a[220:223],v[12:15]
v_rcp_f32 v142, v142
MM v[12:15],a[176:179],a[204:207],v[12:15]
v_rcp_f32 v143, v143
s_memrealtime s[54:55]
s_cmp_eq_u32 s58, 15
s_cbranch_scc1 LSK1_%=
s_memrealtime s[98:99]
s_andn2_b32 s91, s17, s58
s_mov_b32 s77, 0x7fffffff
s_waitcnt lgkmcnt(0)
s_sub_u32 s76, s98, s54
s_bitcmp0_b32 s91, 1
s_cbranch_scc1 LSM11_%=
s_sub_u32 s90, s13, s76
s_sub_u32 s90, s90, 12
s_cmp_gt_i32 s90, 0
s_cbranch_scc1 LSM11_%=
buffer_load_dwordx4 v[16:19], v248, s[20:23], s36 offen offset:0 sc1
buffer_load_dwordx4 v[20:23], v248, s[20:23], s36 offen offset:1024 sc1
s_bitset1_b32 s58, 1
LSM11_%=:
s_bitcmp0_b32 s91, 3
s_cbranch_scc1 LSM13_%=
s_sub_u32 s90, s15, s76
s_sub_u32 s90, s90, 63
s_cmp_gt_i32 s90, 0
s_cbranch_scc1 LSM13_%=
buffer_load_dwordx4 v[48:51], v248, s[20:23], s38 offen offset:0 sc1
buffer_load_dwordx4 v[52:55], v248, s[20:23], s38 offen offset:1024 sc1
s_bitset1_b32 s58, 3
LSM13_%=:
s_bitcmp0_b32 s91, 2
s_cbranch_scc1 LSM12_%=
s_sub_u32 s90, s14, s76
s_sub_u32 s90, s90, 97
s_cmp_gt_i32 s90, 0
s_cbranch_scc1 LSM12_%=
buffer_load_dwordx4 v[24:27], v248, s[20:23], s37 offen offset:0 sc1
buffer_load_dwordx4 v[28:31], v248, s[20:23], s37 offen offset:1024 sc1
s_bitset1_b32 s58, 2
LSM12_%=:
s_bitcmp0_b32 s91, 0
s_cbranch_scc1 LSM10_%=
s_sub_u32 s90, s12, s76
s_sub_u32 s90, s90, 131
s_cmp_gt_i32 s90, 0
s_cbranch_scc1 LSM10_%=
buffer_load_dwordx4 v[56:59], v250, s[20:23], s39 offen offset:0 sc1
buffer_load_dwordx4 v[60:63], v250, s[20:23], s39 offen offset:256 sc1
s_bitset1_b32 s58, 0
LSM10_%=:
LSK1_%=:
v_mul_f32 v136, v160, v136
v_mul_f32 v137, v161, v137
v_mul_f32 v138, v162, v138
v_mul_f32 v139, v163, v139
v_mul_f32 v140, v164, v140
v_mul_f32 v141, v165, v141
v_mul_f32 v142, v166, v142
v_mul_f32 v143, v167, v143
CV v80,v136,v137
CV v81,v138,v139
CV v82,v140,v141
CV v83,v142,v143
v_lshlrev_b32 v160, 16, v80
v_and_b32 v161, 0xffff0000, v80
v_lshlrev_b32 v162, 16, v81
v_and_b32 v163, 0xffff0000, v81
v_lshlrev_b32 v164, 16, v82
v_and_b32 v165, 0xffff0000, v82
v_lshlrev_b32 v166, 16, v83
v_and_b32 v167, 0xffff0000, v83
v_lshlrev_b32 v176, 16, v84
v_and_b32 v177, 0xffff0000, v84
v_lshlrev_b32 v178, 16, v85
v_and_b32 v179, 0xffff0000, v85
v_lshlrev_b32 v180, 16, v86
v_and_b32 v181, 0xffff0000, v86
v_lshlrev_b32 v182, 16, v87
v_and_b32 v183, 0xffff0000, v87
v_mul_f32 v136, 0xbfb8aa3b, v176
v_mul_f32 v137, 0xbfb8aa3b, v177
v_mul_f32 v138, 0xbfb8aa3b, v178
v_mul_f32 v139, 0xbfb8aa3b, v179
v_mul_f32 v140, 0xbfb8aa3b, v180
v_mul_f32 v141, 0xbfb8aa3b, v181
v_mul_f32 v142, 0xbfb8aa3b, v182
v_mul_f32 v143, 0xbfb8aa3b, v183
EX v136,v136
s_waitcnt vmcnt(10)
v_cmp_eq_u32 s[8:9], s69, v149
v_subrev_u32 v226, s56, v148
LPG30_%=:
s_cbranch_execz LTR1_%=
s_nop 0
v_cndmask_b32 v226, 4.0, v226, s[8:9]
s_nop 0
v_readlane_b32 s13, v226, 16
v_readlane_b32 s14, v226, 32
v_readlane_b32 s15, v226, 48
s_max_i32 s12, s13, s14
s_max_i32 s12, s12, s15
s_max_i32 s12, s12, 0
s_cmp_eq_u32 s58, 15
s_cbranch_scc1 LSK2_%=
s_memrealtime s[98:99]
s_andn2_b32 s91, s17, s58
s_mov_b32 s77, 0x7fffffff
s_waitcnt lgkmcnt(0)
s_sub_u32 s76, s98, s54
s_bitcmp0_b32 s91, 1
s_cbranch_scc1 LSM21_%=
s_sub_u32 s90, s13, s76
s_sub_u32 s90, s90, 12
s_cmp_gt_i32 s90, 0
s_cbranch_scc1 LSM21_%=
buffer_load_dwordx4 v[16:19], v248, s[20:23], s36 offen offset:0 sc1
buffer_load_dwordx4 v[20:23], v248, s[20:23], s36 offen offset:1024 sc1
s_bitset1_b32 s58, 1
LSM21_%=:
s_bitcmp0_b32 s91, 3
s_cbranch_scc1 LSM23_%=
s_sub_u32 s90, s15, s76
s_sub_u32 s90, s90, 63
s_cmp_gt_i32 s90, 0
s_cbranch_scc1 LSM23_%=
buffer_load_dwordx4 v[48:51], v248, s[20:23], s38 offen offset:0 sc1
buffer_load_dwordx4 v[52:55], v248, s[20:23], s38 offen offset:1024 sc1
s_bitset1_b32 s58, 3
LSM23_%=:
s_bitcmp0_b32 s91, 2
s_cbranch_scc1 LSM22_%=
s_sub_u32 s90, s14, s76
s_sub_u32 s90, s90, 97
s_cmp_gt_i32 s90, 0
s_cbranch_scc1 LSM22_%=
buffer_load_dwordx4 v[24:27], v248, s[20:23], s37 offen offset:0 sc1
buffer_load_dwordx4 v[28:31], v248, s[20:23], s37 offen offset:1024 sc1
s_bitset1_b32 s58, 2
LSM22_%=:
s_bitcmp0_b32 s91, 0
s_cbranch_scc1 LSM20_%=
s_sub_u32 s90, s12, s76
s_sub_u32 s90, s90, 131
s_cmp_gt_i32 s90, 0
s_cbranch_scc1 LSM20_%=
buffer_load_dwordx4 v[56:59], v250, s[20:23], s39 offen offset:0 sc1
buffer_load_dwordx4 v[60:63], v250, s[20:23], s39 offen offset:256 sc1
s_bitset1_b32 s58, 0
LSM20_%=:
LSK2_%=:
EX v137,v137
EX v138,v138
EX v139,v139
EX v140,v140
EX v141,v141
EX v142,v142
EX v143,v143
v_add_f32 v136, 1.0, v136
v_add_f32 v137, 1.0, v137
v_add_f32 v138, 1.0, v138
v_add_f32 v139, 1.0, v139
v_add_f32 v140, 1.0, v140
v_add_f32 v141, 1.0, v141
v_add_f32 v142, 1.0, v142
v_add_f32 v143, 1.0, v143
v_rcp_f32 v136, v136
v_rcp_f32 v137, v137
v_rcp_f32 v138, v138
v_rcp_f32 v139, v139
v_rcp_f32 v140, v140
v_rcp_f32 v141, v141
v_rcp_f32 v142, v142
v_rcp_f32 v143, v143
s_cmp_eq_u32 s58, 15
s_cbranch_scc1 LSK3_%=
s_memrealtime s[98:99]
s_andn2_b32 s91, s17, s58
s_mov_b32 s77, 0x7fffffff
s_waitcnt lgkmcnt(0)
s_sub_u32 s76, s98, s54
s_bitcmp0_b32 s91, 1
s_cbranch_scc1 LSM31_%=
s_sub_u32 s90, s13, s76
s_sub_u32 s90, s90, 12
s_cmp_gt_i32 s90, 0
s_cbranch_scc1 LSM31_%=
buffer_load_dwordx4 v[16:19], v248, s[20:23], s36 offen offset:0 sc1
buffer_load_dwordx4 v[20:23], v248, s[20:23], s36 offen offset:1024 sc1
s_bitset1_b32 s58, 1
LSM31_%=:
s_bitcmp0_b32 s91, 3
s_cbranch_scc1 LSM33_%=
s_sub_u32 s90, s15, s76
s_sub_u32 s90, s90, 63
s_cmp_gt_i32 s90, 0
s_cbranch_scc1 LSM33_%=
buffer_load_dwordx4 v[48:51], v248, s[20:23], s38 offen offset:0 sc1
buffer_load_dwordx4 v[52:55], v248, s[20:23], s38 offen offset:1024 sc1
s_bitset1_b32 s58, 3
LSM33_%=:
s_bitcmp0_b32 s91, 2
s_cbranch_scc1 LSM32_%=
s_sub_u32 s90, s14, s76
s_sub_u32 s90, s90, 97
s_cmp_gt_i32 s90, 0
s_cbranch_scc1 LSM32_%=
buffer_load_dwordx4 v[24:27], v248, s[20:23], s37 offen offset:0 sc1
buffer_load_dwordx4 v[28:31], v248, s[20:23], s37 offen offset:1024 sc1
s_bitset1_b32 s58, 2
LSM32_%=:
s_bitcmp0_b32 s91, 0
)ASM"
R"ASM(
s_cbranch_scc1 LSM30_%=
s_sub_u32 s90, s12, s76
s_sub_u32 s90, s90, 131
s_cmp_gt_i32 s90, 0
s_cbranch_scc1 LSM30_%=
buffer_load_dwordx4 v[56:59], v250, s[20:23], s39 offen offset:0 sc1
buffer_load_dwordx4 v[60:63], v250, s[20:23], s39 offen offset:256 sc1
s_bitset1_b32 s58, 0
LSM30_%=:
LSK3_%=:
v_mul_f32 v136, v176, v136
v_mul_f32 v137, v177, v137
v_mul_f32 v138, v178, v138
v_mul_f32 v139, v179, v139
v_mul_f32 v140, v180, v140
v_mul_f32 v141, v181, v141
v_mul_f32 v142, v182, v142
v_mul_f32 v143, v183, v143
CV v84,v136,v137
CV v85,v138,v139
CV v86,v140,v141
CV v87,v142,v143
v_lshlrev_b32 v176, 16, v84
v_and_b32 v177, 0xffff0000, v84
v_lshlrev_b32 v178, 16, v85
v_and_b32 v179, 0xffff0000, v85
v_lshlrev_b32 v180, 16, v86
v_and_b32 v181, 0xffff0000, v86
v_lshlrev_b32 v182, 16, v87
v_and_b32 v183, 0xffff0000, v87
v_lshlrev_b32 v192, 16, v88
v_and_b32 v193, 0xffff0000, v88
v_lshlrev_b32 v194, 16, v89
v_and_b32 v195, 0xffff0000, v89
v_lshlrev_b32 v196, 16, v90
v_and_b32 v197, 0xffff0000, v90
v_lshlrev_b32 v198, 16, v91
v_and_b32 v199, 0xffff0000, v91
v_lshlrev_b32 v208, 16, v92
v_and_b32 v209, 0xffff0000, v92
v_lshlrev_b32 v210, 16, v93
v_and_b32 v211, 0xffff0000, v93
v_lshlrev_b32 v212, 16, v94
v_and_b32 v213, 0xffff0000, v94
v_lshlrev_b32 v214, 16, v95
v_and_b32 v215, 0xffff0000, v95
s_mov_b32 s73, 30000
s_cmp_lg_u32 s58, 15
s_cbranch_scc1 LSI_%=
s_waitcnt vmcnt(0)
v_max3_u32 v236, v59, v63, v19
v_max3_u32 v237, v23, v27, v31
v_max3_u32 v236, v236, v51, v55
v_max_u32 v236, v236, v237
v_cmp_eq_u32 vcc, -1, v236
s_cbranch_vccnz LSI_%=
LSF_%=:
s_waitcnt lgkmcnt(0)
MM v[152:155],v[112:115],v[56:59],0
MM v[152:155],v[116:119],v[60:63],v[152:155]
MM v[0:3],v[122:125],v[24:27],v[0:3]
v_lshlrev_b32 v128, 16, v150
v_and_b32 v129, 0xffff0000, v150
MM v[0:3],v[122:125],v[48:51],v[0:3]
v_lshlrev_b32 v130, 16, v151
v_and_b32 v131, 0xffff0000, v151
MM v[0:3],v[122:125],v[16:19],v[0:3]
v_lshlrev_b32 v132, 16, v126
v_and_b32 v133, 0xffff0000, v126
MM v[4:7],v[120:123],v[24:27],v[4:7]
v_lshlrev_b32 v134, 16, v127
v_and_b32 v135, 0xffff0000, v127
MM v[4:7],v[120:123],v[48:51],v[4:7]
CV v236,v152,v153
CV v237,v154,v155
MM v[4:7],v[120:123],v[16:19],v[4:7]
ds_bpermute_b32 v236, v232, v236
ds_bpermute_b32 v237, v232, v237
MM v[8:11],v[122:125],v[28:31],v[8:11]
MM v[8:11],v[122:125],v[52:55],v[8:11]
MM v[8:11],v[122:125],v[20:23],v[8:11]
s_add_u32 s80, s18, 20480
MM v[12:15],v[120:123],v[28:31],v[12:15]
MM v[12:15],v[120:123],v[52:55],v[12:15]
CV v32,v0,v1
CV v33,v2,v3
MM v[12:15],v[120:123],v[20:23],v[12:15]
CV v34,v4,v5
CV v35,v6,v7
ds_write2_b64 v224, v[32:33], v[34:35] offset0:0 offset1:4
RD v[40:43],v225 offset:0
CV v36,v8,v9
CV v37,v10,v11
s_nop 1
CV v38,v12,v13
CV v39,v14,v15
ds_write2_b64 v224, v[36:37], v[38:39] offset0:160 offset1:164
RD v[44:47],v225 offset:1280
s_waitcnt lgkmcnt(4)
v_lshlrev_b32 v152, 16, v236
v_and_b32 v153, 0xffff0000, v236
v_lshlrev_b32 v154, 16, v237
v_and_b32 v155, 0xffff0000, v237
v_pk_mul_f32 v[152:153], v[152:153], v[128:129]
v_pk_mul_f32 v[154:155], v[154:155], v[130:131]
CV v236,v152,v153
CV v237,v154,v155
v_lshlrev_b32 v152, 16, v236
v_and_b32 v153, 0xffff0000, v236
v_lshlrev_b32 v154, 16, v237
v_and_b32 v155, 0xffff0000, v237
v_pk_add_f32 v[152:153], v[152:153], v[132:133]
v_pk_add_f32 v[154:155], v[154:155], v[134:135]
CV v238,v152,v153
CV v239,v154,v155
buffer_store_dwordx2 v[238:239], v254, s[40:43], s63 offen offset:0 nt
s_nop 0
s_waitcnt lgkmcnt(2)
v_lshlrev_b32 v0, 16, v40
v_and_b32 v1, 0xffff0000, v40
v_lshlrev_b32 v2, 16, v41
v_and_b32 v3, 0xffff0000, v41
v_lshlrev_b32 v4, 16, v42
v_and_b32 v5, 0xffff0000, v42
v_lshlrev_b32 v6, 16, v43
v_and_b32 v7, 0xffff0000, v43
v_pk_mul_f32 v[0:1], v[0:1], v[160:161]
v_pk_mul_f32 v[2:3], v[2:3], v[162:163]
v_pk_mul_f32 v[4:5], v[4:5], v[164:165]
v_pk_mul_f32 v[6:7], v[6:7], v[166:167]
CV v40,v0,v1
CV v41,v2,v3
CV v42,v4,v5
CV v43,v6,v7
v_lshlrev_b32 v0, 16, v40
v_and_b32 v1, 0xffff0000, v40
v_lshlrev_b32 v2, 16, v41
v_and_b32 v3, 0xffff0000, v41
v_lshlrev_b32 v4, 16, v42
v_and_b32 v5, 0xffff0000, v42
v_lshlrev_b32 v6, 16, v43
v_and_b32 v7, 0xffff0000, v43
v_pk_add_f32 v[0:1], v[0:1], v[192:193]
v_pk_add_f32 v[2:3], v[2:3], v[194:195]
v_pk_add_f32 v[4:5], v[4:5], v[196:197]
v_pk_add_f32 v[6:7], v[6:7], v[198:199]
CV v40,v0,v1
CV v41,v2,v3
CV v42,v4,v5
CV v43,v6,v7
buffer_store_dwordx4 v[40:43], v251, s[40:43], s18 offen offset:0 nt
s_waitcnt lgkmcnt(0)
v_lshlrev_b32 v8, 16, v44
v_and_b32 v9, 0xffff0000, v44
v_lshlrev_b32 v10, 16, v45
v_and_b32 v11, 0xffff0000, v45
v_lshlrev_b32 v12, 16, v46
v_and_b32 v13, 0xffff0000, v46
v_lshlrev_b32 v14, 16, v47
v_and_b32 v15, 0xffff0000, v47
v_pk_mul_f32 v[8:9], v[8:9], v[176:177]
v_pk_mul_f32 v[10:11], v[10:11], v[178:179]
v_pk_mul_f32 v[12:13], v[12:13], v[180:181]
v_pk_mul_f32 v[14:15], v[14:15], v[182:183]
CV v44,v8,v9
CV v45,v10,v11
CV v46,v12,v13
CV v47,v14,v15
v_lshlrev_b32 v8, 16, v44
v_and_b32 v9, 0xffff0000, v44
v_lshlrev_b32 v10, 16, v45
v_and_b32 v11, 0xffff0000, v45
v_lshlrev_b32 v12, 16, v46
v_and_b32 v13, 0xffff0000, v46
v_lshlrev_b32 v14, 16, v47
v_and_b32 v15, 0xffff0000, v47
v_pk_add_f32 v[8:9], v[8:9], v[208:209]
v_pk_add_f32 v[10:11], v[10:11], v[210:211]
v_pk_add_f32 v[12:13], v[12:13], v[212:213]
v_pk_add_f32 v[14:15], v[14:15], v[214:215]
CV v44,v8,v9
CV v45,v10,v11
CV v46,v12,v13
CV v47,v14,v15
buffer_store_dwordx4 v[44:47], v251, s[40:43], s80 offen offset:0 nt
s_waitcnt vmcnt(0)
s_endpgm
LSI_%=:
s_memrealtime s[98:99]
s_andn2_b32 s91, s17, s58
s_mov_b32 s77, 0x7fffffff
s_waitcnt lgkmcnt(0)
s_sub_u32 s76, s98, s54
s_bitcmp0_b32 s91, 1
s_cbranch_scc1 LSM1_%=
s_sub_u32 s90, s13, s76
s_sub_u32 s90, s90, 12
s_cmp_gt_i32 s90, 0
s_cbranch_scc1 LSN1_%=
buffer_load_dwordx4 v[16:19], v248, s[20:23], s36 offen offset:0 sc1
buffer_load_dwordx4 v[20:23], v248, s[20:23], s36 offen offset:1024 sc1
s_bitset1_b32 s58, 1
s_branch LSM1_%=
LSN1_%=:
s_min_i32 s77, s77, s90
LSM1_%=:
s_bitcmp0_b32 s91, 3
s_cbranch_scc1 LSM3_%=
s_sub_u32 s90, s15, s76
s_sub_u32 s90, s90, 63
s_cmp_gt_i32 s90, 0
s_cbranch_scc1 LSN3_%=
buffer_load_dwordx4 v[48:51], v248, s[20:23], s38 offen offset:0 sc1
buffer_load_dwordx4 v[52:55], v248, s[20:23], s38 offen offset:1024 sc1
s_bitset1_b32 s58, 3
s_branch LSM3_%=
LSN3_%=:
s_min_i32 s77, s77, s90
LSM3_%=:
s_bitcmp0_b32 s91, 2
s_cbranch_scc1 LSM2_%=
s_sub_u32 s90, s14, s76
s_sub_u32 s90, s90, 97
s_cmp_gt_i32 s90, 0
s_cbranch_scc1 LSN2_%=
buffer_load_dwordx4 v[24:27], v248, s[20:23], s37 offen offset:0 sc1
buffer_load_dwordx4 v[28:31], v248, s[20:23], s37 offen offset:1024 sc1
s_bitset1_b32 s58, 2
s_branch LSM2_%=
LSN2_%=:
s_min_i32 s77, s77, s90
LSM2_%=:
s_bitcmp0_b32 s91, 0
s_cbranch_scc1 LSM0_%=
s_sub_u32 s90, s12, s76
s_sub_u32 s90, s90, 131
s_cmp_gt_i32 s90, 0
s_cbranch_scc1 LSN0_%=
buffer_load_dwordx4 v[56:59], v250, s[20:23], s39 offen offset:0 sc1
buffer_load_dwordx4 v[60:63], v250, s[20:23], s39 offen offset:256 sc1
s_bitset1_b32 s58, 0
s_branch LSM0_%=
LSN0_%=:
s_min_i32 s77, s77, s90
LSM0_%=:
s_cmp_lt_i32 s77, 100
s_cbranch_scc1 LSI_%=
buffer_load_dwordx2 v[148:149], v253, s[20:23], s68 offen offset:8 sc1
s_waitcnt vmcnt(1)
s_bitcmp0_b32 s58, 1
s_cbranch_scc1 LSB1_%=
v_max_u32 v237, v19, v23
v_cmp_eq_u32 vcc, -1, v237
s_cbranch_vccnz LSB1_%=
s_bitset0_b32 s17, 1
LSB1_%=:
s_bitcmp0_b32 s58, 3
s_cbranch_scc1 LSB3_%=
v_max_u32 v239, v51, v55
v_cmp_eq_u32 vcc, -1, v239
s_cbranch_vccnz LSB3_%=
s_bitset0_b32 s17, 3
LSB3_%=:
s_bitcmp0_b32 s58, 2
s_cbranch_scc1 LSB2_%=
v_max_u32 v238, v27, v31
v_cmp_eq_u32 vcc, -1, v238
s_cbranch_vccnz LSB2_%=
s_bitset0_b32 s17, 2
LSB2_%=:
s_bitcmp0_b32 s58, 0
s_cbranch_scc1 LSB0_%=
v_max_u32 v236, v59, v63
v_cmp_eq_u32 vcc, -1, v236
s_cbranch_vccnz LSB0_%=
s_bitset0_b32 s17, 0
LSB0_%=:
s_cmp_eq_u32 s17, 0
s_cbranch_scc1 LSF_%=
s_mov_b32 s58, 0
s_waitcnt vmcnt(0)
v_cmp_eq_u32 s[8:9], s69, v149
v_subrev_u32 v226, s56, v148
s_nop 0
v_cndmask_b32 v226, 4.0, v226, s[8:9]
s_nop 0
v_readlane_b32 s13, v226, 16
v_readlane_b32 s14, v226, 32
v_readlane_b32 s15, v226, 48
s_max_i32 s12, s13, s14
s_max_i32 s12, s12, s15
s_max_i32 s12, s12, 0
s_sub_u32 s73, s73, 1
s_cbranch_scc0 LSI_%=
LSE_%=:
s_waitcnt vmcnt(0)
s_endpgm
LRG1o_%=:
s_mov_b32 s70, 30000
LRG1ol_%=:
s_sleep 2
buffer_load_dwordx4 v[200:203], v208, s[20:23], s62 offen offset:0 sc1
buffer_load_dwordx4 v[204:207], v208, s[20:23], s93 offen offset:0 sc1
s_waitcnt vmcnt(0)
v_xor_b32 v201, s69, v201
v_xor_b32 v203, s69, v203
v_xor_b32 v205, s69, v205
v_xor_b32 v207, s69, v207
v_or3_b32 v209, v201, v203, v205
v_or_b32 v209, v209, v207
v_cmp_ne_u32 vcc, 0, v209
s_cbranch_vccz LRG1ob_%=
s_sub_u32 s70, s70, 1
s_cbranch_scc0 LRG1ol_%=
s_branch LRG1ob_%=
LRFR_%=:
s_mov_b32 s91, 30000
LRFRl_%=:
s_sleep 2
buffer_load_dwordx4 v[112:115], v248, s[20:23], s90 offen offset:0 sc1
s_waitcnt vmcnt(0)
v_xor_b32 v112, s69, v112
v_xor_b32 v113, s69, v113
v_xor_b32 v114, s69, v114
v_xor_b32 v115, s69, v115
v_or3_b32 v252, v112, v113, v114
v_or_b32 v252, v252, v115
v_cmp_ne_u32 vcc, 0, v252
s_cbranch_vccz LRFRb_%=
s_sub_u32 s91, s91, 1
s_cbranch_scc0 LRFRl_%=
s_branch LRFRb_%=
LRRK_%=:
s_mov_b32 s91, 30000
LRRKl_%=:
s_sleep 2
buffer_load_dwordx2 v[32:33], v34, s[20:23], s90 offen offset:0 sc1
s_waitcnt vmcnt(0)
v_cmp_ne_u32 vcc, s69, v33
s_cbranch_vccz LRRKb_%=
s_sub_u32 s91, s91, 1
s_cbranch_scc0 LRRKl_%=
s_branch LRRKb_%=
LRK_%=:
s_cmp_ge_u32 s60, 8
s_cbranch_scc1 LRK0_%=
v_mov_b32 v253, 0
v_cmp_lt_u32 vcc, s52, v96
v_cmp_lt_u32 s[98:99], s52, v97
s_nop 0
v_addc_co_u32 v253, vcc, 0, v253, vcc
v_addc_co_u32 v253, s[98:99], 0, v253, s[98:99]
v_cmp_lt_u32 vcc, s52, v98
v_cmp_lt_u32 s[98:99], s52, v99
s_nop 0
v_addc_co_u32 v253, vcc, 0, v253, vcc
v_addc_co_u32 v253, s[98:99], 0, v253, s[98:99]
v_cmp_lt_u32 vcc, s52, v100
v_cmp_lt_u32 s[98:99], s52, v101
s_nop 0
v_addc_co_u32 v253, vcc, 0, v253, vcc
v_addc_co_u32 v253, s[98:99], 0, v253, s[98:99]
v_cmp_lt_u32 vcc, s52, v102
v_cmp_lt_u32 s[98:99], s52, v103
s_nop 0
v_addc_co_u32 v253, vcc, 0, v253, vcc
v_addc_co_u32 v253, s[98:99], 0, v253, s[98:99]
v_cmp_lt_u32 vcc, s52, v104
v_cmp_lt_u32 s[98:99], s52, v105
s_nop 0
v_addc_co_u32 v253, vcc, 0, v253, vcc
v_addc_co_u32 v253, s[98:99], 0, v253, s[98:99]
v_cmp_lt_u32 vcc, s52, v106
v_cmp_lt_u32 s[98:99], s52, v107
s_nop 0
v_addc_co_u32 v253, vcc, 0, v253, vcc
v_addc_co_u32 v253, s[98:99], 0, v253, s[98:99]
v_cmp_lt_u32 vcc, s52, v108
v_cmp_lt_u32 s[98:99], s52, v109
s_nop 0
v_addc_co_u32 v253, vcc, 0, v253, vcc
v_addc_co_u32 v253, s[98:99], 0, v253, s[98:99]
v_cmp_lt_u32 vcc, s52, v110
v_cmp_lt_u32 s[98:99], s52, v111
s_nop 0
v_addc_co_u32 v253, vcc, 0, v253, vcc
v_addc_co_u32 v253, s[98:99], 0, v253, s[98:99]
v_cmp_lt_u32 vcc, s52, v112
v_cmp_lt_u32 s[98:99], s52, v113
s_nop 0
v_addc_co_u32 v253, vcc, 0, v253, vcc
v_addc_co_u32 v253, s[98:99], 0, v253, s[98:99]
v_cmp_lt_u32 vcc, s52, v114
v_cmp_lt_u32 s[98:99], s52, v115
s_nop 0
v_addc_co_u32 v253, vcc, 0, v253, vcc
v_addc_co_u32 v253, s[98:99], 0, v253, s[98:99]
v_cmp_lt_u32 vcc, s52, v116
v_cmp_lt_u32 s[98:99], s52, v117
s_nop 0
v_addc_co_u32 v253, vcc, 0, v253, vcc
v_addc_co_u32 v253, s[98:99], 0, v253, s[98:99]
v_cmp_lt_u32 vcc, s52, v118
v_cmp_lt_u32 s[98:99], s52, v119
s_nop 0
v_addc_co_u32 v253, vcc, 0, v253, vcc
v_addc_co_u32 v253, s[98:99], 0, v253, s[98:99]
v_cmp_lt_u32 vcc, s52, v120
v_cmp_lt_u32 s[98:99], s52, v121
s_nop 0
v_addc_co_u32 v253, vcc, 0, v253, vcc
v_addc_co_u32 v253, s[98:99], 0, v253, s[98:99]
v_cmp_lt_u32 vcc, s52, v122
v_cmp_lt_u32 s[98:99], s52, v123
s_nop 0
v_addc_co_u32 v253, vcc, 0, v253, vcc
v_addc_co_u32 v253, s[98:99], 0, v253, s[98:99]
v_cmp_lt_u32 vcc, s52, v124
v_cmp_lt_u32 s[98:99], s52, v125
s_nop 0
v_addc_co_u32 v253, vcc, 0, v253, vcc
v_addc_co_u32 v253, s[98:99], 0, v253, s[98:99]
v_cmp_lt_u32 vcc, s52, v126
v_cmp_lt_u32 s[98:99], s52, v127
s_nop 0
v_addc_co_u32 v253, vcc, 0, v253, vcc
v_addc_co_u32 v253, s[98:99], 0, v253, s[98:99]
s_nop 1
v_add_u32_dpp v253, v253, v253 row_ror:8 row_mask:0xf bank_mask:0xf
s_nop 1
v_add_u32_dpp v253, v253, v253 row_ror:4 row_mask:0xf bank_mask:0xf
s_nop 1
v_add_u32_dpp v253, v253, v253 row_ror:2 row_mask:0xf bank_mask:0xf
)ASM"
R"ASM(
s_nop 1
v_add_u32_dpp v253, v253, v253 row_ror:1 row_mask:0xf bank_mask:0xf
v_mov_b32 v250, v253
s_nop 1
v_permlane16_swap_b32 v253, v250
v_add_u32 v253, v253, v250
v_mov_b32 v250, v253
s_nop 1
v_permlane32_swap_b32 v253, v250
v_add_u32 v253, v253, v250
v_cmp_gt_u32 vcc, 8, v253
v_mov_b32 v250, s52
v_mov_b32 v251, s69
v_lshlrev_b32 v253, 3, v253
s_mov_b32 s76, 94208
s_cbranch_vccz LRK0_%=
s_mov_b64 exec, 1
buffer_store_dwordx2 v[250:251], v253, s[20:23], s76 offen offset:0 sc1
s_mov_b64 exec, -1
s_nop 1
LRK0_%=:
s_cmp_ge_u32 s62, 8
s_cbranch_scc1 LRK1_%=
v_mov_b32 v253, 0
v_cmp_lt_u32 vcc, s53, v96
v_cmp_lt_u32 s[98:99], s53, v97
s_nop 0
v_addc_co_u32 v253, vcc, 0, v253, vcc
v_addc_co_u32 v253, s[98:99], 0, v253, s[98:99]
v_cmp_lt_u32 vcc, s53, v98
v_cmp_lt_u32 s[98:99], s53, v99
s_nop 0
v_addc_co_u32 v253, vcc, 0, v253, vcc
v_addc_co_u32 v253, s[98:99], 0, v253, s[98:99]
v_cmp_lt_u32 vcc, s53, v100
v_cmp_lt_u32 s[98:99], s53, v101
s_nop 0
v_addc_co_u32 v253, vcc, 0, v253, vcc
v_addc_co_u32 v253, s[98:99], 0, v253, s[98:99]
v_cmp_lt_u32 vcc, s53, v102
v_cmp_lt_u32 s[98:99], s53, v103
s_nop 0
v_addc_co_u32 v253, vcc, 0, v253, vcc
v_addc_co_u32 v253, s[98:99], 0, v253, s[98:99]
v_cmp_lt_u32 vcc, s53, v104
v_cmp_lt_u32 s[98:99], s53, v105
s_nop 0
v_addc_co_u32 v253, vcc, 0, v253, vcc
v_addc_co_u32 v253, s[98:99], 0, v253, s[98:99]
v_cmp_lt_u32 vcc, s53, v106
v_cmp_lt_u32 s[98:99], s53, v107
s_nop 0
v_addc_co_u32 v253, vcc, 0, v253, vcc
v_addc_co_u32 v253, s[98:99], 0, v253, s[98:99]
v_cmp_lt_u32 vcc, s53, v108
v_cmp_lt_u32 s[98:99], s53, v109
s_nop 0
v_addc_co_u32 v253, vcc, 0, v253, vcc
v_addc_co_u32 v253, s[98:99], 0, v253, s[98:99]
v_cmp_lt_u32 vcc, s53, v110
v_cmp_lt_u32 s[98:99], s53, v111
s_nop 0
v_addc_co_u32 v253, vcc, 0, v253, vcc
v_addc_co_u32 v253, s[98:99], 0, v253, s[98:99]
v_cmp_lt_u32 vcc, s53, v112
v_cmp_lt_u32 s[98:99], s53, v113
s_nop 0
v_addc_co_u32 v253, vcc, 0, v253, vcc
v_addc_co_u32 v253, s[98:99], 0, v253, s[98:99]
v_cmp_lt_u32 vcc, s53, v114
v_cmp_lt_u32 s[98:99], s53, v115
s_nop 0
v_addc_co_u32 v253, vcc, 0, v253, vcc
v_addc_co_u32 v253, s[98:99], 0, v253, s[98:99]
v_cmp_lt_u32 vcc, s53, v116
v_cmp_lt_u32 s[98:99], s53, v117
s_nop 0
v_addc_co_u32 v253, vcc, 0, v253, vcc
v_addc_co_u32 v253, s[98:99], 0, v253, s[98:99]
v_cmp_lt_u32 vcc, s53, v118
v_cmp_lt_u32 s[98:99], s53, v119
s_nop 0
v_addc_co_u32 v253, vcc, 0, v253, vcc
v_addc_co_u32 v253, s[98:99], 0, v253, s[98:99]
v_cmp_lt_u32 vcc, s53, v120
v_cmp_lt_u32 s[98:99], s53, v121
s_nop 0
v_addc_co_u32 v253, vcc, 0, v253, vcc
v_addc_co_u32 v253, s[98:99], 0, v253, s[98:99]
v_cmp_lt_u32 vcc, s53, v122
v_cmp_lt_u32 s[98:99], s53, v123
s_nop 0
v_addc_co_u32 v253, vcc, 0, v253, vcc
v_addc_co_u32 v253, s[98:99], 0, v253, s[98:99]
v_cmp_lt_u32 vcc, s53, v124
v_cmp_lt_u32 s[98:99], s53, v125
s_nop 0
v_addc_co_u32 v253, vcc, 0, v253, vcc
v_addc_co_u32 v253, s[98:99], 0, v253, s[98:99]
v_cmp_lt_u32 vcc, s53, v126
v_cmp_lt_u32 s[98:99], s53, v127
s_nop 0
v_addc_co_u32 v253, vcc, 0, v253, vcc
v_addc_co_u32 v253, s[98:99], 0, v253, s[98:99]
s_nop 1
v_add_u32_dpp v253, v253, v253 row_ror:8 row_mask:0xf bank_mask:0xf
s_nop 1
v_add_u32_dpp v253, v253, v253 row_ror:4 row_mask:0xf bank_mask:0xf
s_nop 1
v_add_u32_dpp v253, v253, v253 row_ror:2 row_mask:0xf bank_mask:0xf
s_nop 1
v_add_u32_dpp v253, v253, v253 row_ror:1 row_mask:0xf bank_mask:0xf
v_mov_b32 v250, v253
s_nop 1
v_permlane16_swap_b32 v253, v250
v_add_u32 v253, v253, v250
v_mov_b32 v250, v253
s_nop 1
v_permlane32_swap_b32 v253, v250
v_add_u32 v253, v253, v250
v_cmp_gt_u32 vcc, 8, v253
v_mov_b32 v250, s53
v_mov_b32 v251, s69
v_lshlrev_b32 v253, 3, v253
s_mov_b32 s76, 94208
s_cbranch_vccz LRK1_%=
s_mov_b64 exec, 1
buffer_store_dwordx2 v[250:251], v253, s[20:23], s76 offen offset:0 sc1
s_mov_b64 exec, -1
s_nop 1
LRK1_%=:
s_cmp_ge_u32 s56, 8
s_cbranch_scc1 LRK2_%=
v_mov_b32 v253, 0
v_cmp_lt_u32 vcc, s54, v96
v_cmp_lt_u32 s[98:99], s54, v97
s_nop 0
v_addc_co_u32 v253, vcc, 0, v253, vcc
v_addc_co_u32 v253, s[98:99], 0, v253, s[98:99]
v_cmp_lt_u32 vcc, s54, v98
v_cmp_lt_u32 s[98:99], s54, v99
s_nop 0
v_addc_co_u32 v253, vcc, 0, v253, vcc
v_addc_co_u32 v253, s[98:99], 0, v253, s[98:99]
v_cmp_lt_u32 vcc, s54, v100
v_cmp_lt_u32 s[98:99], s54, v101
s_nop 0
v_addc_co_u32 v253, vcc, 0, v253, vcc
v_addc_co_u32 v253, s[98:99], 0, v253, s[98:99]
v_cmp_lt_u32 vcc, s54, v102
v_cmp_lt_u32 s[98:99], s54, v103
s_nop 0
v_addc_co_u32 v253, vcc, 0, v253, vcc
v_addc_co_u32 v253, s[98:99], 0, v253, s[98:99]
v_cmp_lt_u32 vcc, s54, v104
v_cmp_lt_u32 s[98:99], s54, v105
s_nop 0
v_addc_co_u32 v253, vcc, 0, v253, vcc
v_addc_co_u32 v253, s[98:99], 0, v253, s[98:99]
v_cmp_lt_u32 vcc, s54, v106
v_cmp_lt_u32 s[98:99], s54, v107
s_nop 0
v_addc_co_u32 v253, vcc, 0, v253, vcc
v_addc_co_u32 v253, s[98:99], 0, v253, s[98:99]
v_cmp_lt_u32 vcc, s54, v108
v_cmp_lt_u32 s[98:99], s54, v109
s_nop 0
v_addc_co_u32 v253, vcc, 0, v253, vcc
v_addc_co_u32 v253, s[98:99], 0, v253, s[98:99]
v_cmp_lt_u32 vcc, s54, v110
v_cmp_lt_u32 s[98:99], s54, v111
s_nop 0
v_addc_co_u32 v253, vcc, 0, v253, vcc
v_addc_co_u32 v253, s[98:99], 0, v253, s[98:99]
v_cmp_lt_u32 vcc, s54, v112
v_cmp_lt_u32 s[98:99], s54, v113
s_nop 0
v_addc_co_u32 v253, vcc, 0, v253, vcc
v_addc_co_u32 v253, s[98:99], 0, v253, s[98:99]
v_cmp_lt_u32 vcc, s54, v114
v_cmp_lt_u32 s[98:99], s54, v115
s_nop 0
v_addc_co_u32 v253, vcc, 0, v253, vcc
v_addc_co_u32 v253, s[98:99], 0, v253, s[98:99]
v_cmp_lt_u32 vcc, s54, v116
v_cmp_lt_u32 s[98:99], s54, v117
s_nop 0
v_addc_co_u32 v253, vcc, 0, v253, vcc
v_addc_co_u32 v253, s[98:99], 0, v253, s[98:99]
v_cmp_lt_u32 vcc, s54, v118
v_cmp_lt_u32 s[98:99], s54, v119
s_nop 0
v_addc_co_u32 v253, vcc, 0, v253, vcc
v_addc_co_u32 v253, s[98:99], 0, v253, s[98:99]
v_cmp_lt_u32 vcc, s54, v120
v_cmp_lt_u32 s[98:99], s54, v121
s_nop 0
v_addc_co_u32 v253, vcc, 0, v253, vcc
v_addc_co_u32 v253, s[98:99], 0, v253, s[98:99]
v_cmp_lt_u32 vcc, s54, v122
v_cmp_lt_u32 s[98:99], s54, v123
s_nop 0
v_addc_co_u32 v253, vcc, 0, v253, vcc
v_addc_co_u32 v253, s[98:99], 0, v253, s[98:99]
v_cmp_lt_u32 vcc, s54, v124
v_cmp_lt_u32 s[98:99], s54, v125
s_nop 0
v_addc_co_u32 v253, vcc, 0, v253, vcc
v_addc_co_u32 v253, s[98:99], 0, v253, s[98:99]
v_cmp_lt_u32 vcc, s54, v126
v_cmp_lt_u32 s[98:99], s54, v127
s_nop 0
v_addc_co_u32 v253, vcc, 0, v253, vcc
v_addc_co_u32 v253, s[98:99], 0, v253, s[98:99]
s_nop 1
v_add_u32_dpp v253, v253, v253 row_ror:8 row_mask:0xf bank_mask:0xf
s_nop 1
v_add_u32_dpp v253, v253, v253 row_ror:4 row_mask:0xf bank_mask:0xf
s_nop 1
v_add_u32_dpp v253, v253, v253 row_ror:2 row_mask:0xf bank_mask:0xf
s_nop 1
v_add_u32_dpp v253, v253, v253 row_ror:1 row_mask:0xf bank_mask:0xf
v_mov_b32 v250, v253
s_nop 1
v_permlane16_swap_b32 v253, v250
v_add_u32 v253, v253, v250
v_mov_b32 v250, v253
s_nop 1
v_permlane32_swap_b32 v253, v250
v_add_u32 v253, v253, v250
v_cmp_gt_u32 vcc, 8, v253
v_mov_b32 v250, s54
v_mov_b32 v251, s69
v_lshlrev_b32 v253, 3, v253
s_mov_b32 s76, 94208
s_cbranch_vccz LRK2_%=
s_mov_b64 exec, 1
buffer_store_dwordx2 v[250:251], v253, s[20:23], s76 offen offset:0 sc1
s_mov_b64 exec, -1
s_nop 1
LRK2_%=:
s_cmp_ge_u32 s58, 8
s_cbranch_scc1 LRK3_%=
v_mov_b32 v253, 0
v_cmp_lt_u32 vcc, s55, v96
v_cmp_lt_u32 s[98:99], s55, v97
s_nop 0
v_addc_co_u32 v253, vcc, 0, v253, vcc
v_addc_co_u32 v253, s[98:99], 0, v253, s[98:99]
v_cmp_lt_u32 vcc, s55, v98
v_cmp_lt_u32 s[98:99], s55, v99
s_nop 0
v_addc_co_u32 v253, vcc, 0, v253, vcc
v_addc_co_u32 v253, s[98:99], 0, v253, s[98:99]
v_cmp_lt_u32 vcc, s55, v100
v_cmp_lt_u32 s[98:99], s55, v101
s_nop 0
v_addc_co_u32 v253, vcc, 0, v253, vcc
v_addc_co_u32 v253, s[98:99], 0, v253, s[98:99]
v_cmp_lt_u32 vcc, s55, v102
v_cmp_lt_u32 s[98:99], s55, v103
s_nop 0
v_addc_co_u32 v253, vcc, 0, v253, vcc
v_addc_co_u32 v253, s[98:99], 0, v253, s[98:99]
v_cmp_lt_u32 vcc, s55, v104
v_cmp_lt_u32 s[98:99], s55, v105
s_nop 0
v_addc_co_u32 v253, vcc, 0, v253, vcc
v_addc_co_u32 v253, s[98:99], 0, v253, s[98:99]
v_cmp_lt_u32 vcc, s55, v106
v_cmp_lt_u32 s[98:99], s55, v107
s_nop 0
v_addc_co_u32 v253, vcc, 0, v253, vcc
v_addc_co_u32 v253, s[98:99], 0, v253, s[98:99]
v_cmp_lt_u32 vcc, s55, v108
v_cmp_lt_u32 s[98:99], s55, v109
s_nop 0
v_addc_co_u32 v253, vcc, 0, v253, vcc
v_addc_co_u32 v253, s[98:99], 0, v253, s[98:99]
v_cmp_lt_u32 vcc, s55, v110
v_cmp_lt_u32 s[98:99], s55, v111
s_nop 0
v_addc_co_u32 v253, vcc, 0, v253, vcc
v_addc_co_u32 v253, s[98:99], 0, v253, s[98:99]
v_cmp_lt_u32 vcc, s55, v112
v_cmp_lt_u32 s[98:99], s55, v113
s_nop 0
v_addc_co_u32 v253, vcc, 0, v253, vcc
v_addc_co_u32 v253, s[98:99], 0, v253, s[98:99]
v_cmp_lt_u32 vcc, s55, v114
v_cmp_lt_u32 s[98:99], s55, v115
s_nop 0
v_addc_co_u32 v253, vcc, 0, v253, vcc
v_addc_co_u32 v253, s[98:99], 0, v253, s[98:99]
v_cmp_lt_u32 vcc, s55, v116
v_cmp_lt_u32 s[98:99], s55, v117
s_nop 0
v_addc_co_u32 v253, vcc, 0, v253, vcc
v_addc_co_u32 v253, s[98:99], 0, v253, s[98:99]
v_cmp_lt_u32 vcc, s55, v118
v_cmp_lt_u32 s[98:99], s55, v119
s_nop 0
v_addc_co_u32 v253, vcc, 0, v253, vcc
v_addc_co_u32 v253, s[98:99], 0, v253, s[98:99]
v_cmp_lt_u32 vcc, s55, v120
v_cmp_lt_u32 s[98:99], s55, v121
s_nop 0
v_addc_co_u32 v253, vcc, 0, v253, vcc
v_addc_co_u32 v253, s[98:99], 0, v253, s[98:99]
v_cmp_lt_u32 vcc, s55, v122
v_cmp_lt_u32 s[98:99], s55, v123
s_nop 0
v_addc_co_u32 v253, vcc, 0, v253, vcc
v_addc_co_u32 v253, s[98:99], 0, v253, s[98:99]
v_cmp_lt_u32 vcc, s55, v124
v_cmp_lt_u32 s[98:99], s55, v125
s_nop 0
v_addc_co_u32 v253, vcc, 0, v253, vcc
v_addc_co_u32 v253, s[98:99], 0, v253, s[98:99]
v_cmp_lt_u32 vcc, s55, v126
v_cmp_lt_u32 s[98:99], s55, v127
s_nop 0
v_addc_co_u32 v253, vcc, 0, v253, vcc
v_addc_co_u32 v253, s[98:99], 0, v253, s[98:99]
s_nop 1
v_add_u32_dpp v253, v253, v253 row_ror:8 row_mask:0xf bank_mask:0xf
s_nop 1
v_add_u32_dpp v253, v253, v253 row_ror:4 row_mask:0xf bank_mask:0xf
s_nop 1
v_add_u32_dpp v253, v253, v253 row_ror:2 row_mask:0xf bank_mask:0xf
s_nop 1
v_add_u32_dpp v253, v253, v253 row_ror:1 row_mask:0xf bank_mask:0xf
v_mov_b32 v250, v253
s_nop 1
v_permlane16_swap_b32 v253, v250
v_add_u32 v253, v253, v250
v_mov_b32 v250, v253
s_nop 1
v_permlane32_swap_b32 v253, v250
v_add_u32 v253, v253, v250
v_cmp_gt_u32 vcc, 8, v253
v_mov_b32 v250, s55
v_mov_b32 v251, s69
v_lshlrev_b32 v253, 3, v253
s_mov_b32 s76, 94208
s_cbranch_vccz LRK3_%=
s_mov_b64 exec, 1
buffer_store_dwordx2 v[250:251], v253, s[20:23], s76 offen offset:0 sc1
s_mov_b64 exec, -1
s_nop 1
LRK3_%=:
s_branch LRKb_%=
LRIX_%=:
s_mov_b32 s91, 30000
LRIXl_%=:
s_sleep 2
v_bfe_u32 v251, v248, 4, 3
v_lshlrev_b32 v251, 3, v251
buffer_load_dwordx2 v[252:253], v251, s[20:23], s86 offen offset:0 sc1
s_waitcnt vmcnt(0)
v_cmp_ne_u32 vcc, s69, v253
s_cbranch_vccz LRIXb_%=
s_sub_u32 s91, s91, 1
s_cbranch_scc0 LRIXl_%=
s_branch LRIXb_%=
LRFG_%=:
s_mov_b32 s91, 30000
LRFGl_%=:
s_sleep 2
buffer_load_dword v252, v253, s[20:23], s76 offen offset:0 sc1
s_waitcnt vmcnt(0)
v_cmp_ne_u32 vcc, s69, v252
s_cbranch_vccz LRFGb_%=
s_sub_u32 s91, s91, 1
s_cbranch_scc0 LRFGl_%=
s_branch LRFGb_%=
LRFH_%=:
s_mov_b32 s91, 30000
LRFHl_%=:
s_sleep 2
buffer_load_dword v252, v253, s[20:23], s76 offen offset:0 sc1
s_waitcnt vmcnt(0)
v_cmp_ne_u32 vcc, s69, v252
s_cbranch_vccz LRFHb_%=
s_sub_u32 s91, s91, 1
s_cbranch_scc0 LRFHl_%=
s_branch LRFHb_%=
LRFY_%=:
s_mov_b32 s91, 30000
LRFYl_%=:
s_sleep 2
buffer_load_dword v252, v253, s[20:23], s76 offen offset:0 sc1
s_waitcnt vmcnt(0)
v_cmp_ne_u32 vcc, s69, v252
s_cbranch_vccz LRFYb_%=
s_sub_u32 s91, s91, 1
s_cbranch_scc0 LRFYl_%=
s_branch LRFYb_%=
LRPO0_%=:
s_mov_b32 s97, 30000
LRPO0l_%=:
s_sleep 2
buffer_load_dword v242, v243, s[20:23], s44 offen offset:0 sc1
s_waitcnt vmcnt(0)
v_cmp_ne_u32 vcc, s69, v242
s_cbranch_vccz LRPO0b_%=
s_sub_u32 s97, s97, 1
s_cbranch_scc0 LRPO0l_%=
s_branch LRPO0b_%=
LRPW_%=:
s_mov_b32 s61, 30000
LRPWl_%=:
s_sleep 2
buffer_load_dwordx2 v[96:97], v98, s[20:23], s62 offen offset:0 sc1
s_waitcnt vmcnt(0)
v_xor_b32 v96, s69, v96
v_xor_b32 v97, s69, v97
v_or_b32 v99, v96, v97
v_cmp_ne_u32 vcc, 0, v99
s_cbranch_vccz LRPWb_%=
s_sub_u32 s61, s61, 1
s_cbranch_scc0 LRPWl_%=
s_branch LRPWb_%=
)ASM"
R"ASM(
LRPO1_%=:
s_mov_b32 s97, 30000
LRPO1l_%=:
s_sleep 2
buffer_load_dword v242, v243, s[20:23], s96 offen offset:16 sc1
s_waitcnt vmcnt(0)
v_cmp_ne_u32 vcc, s69, v242
s_cbranch_vccz LRPO1b_%=
s_sub_u32 s97, s97, 1
s_cbranch_scc0 LRPO1l_%=
s_branch LRPO1b_%=
LKEND_ka:
LSITE0_%=:
s_mov_b32 s73, 0
s_mov_b32 s74, 0
s_mov_b32 s75, 16384
s_branch LRARE002_%=
LSITE2_%=:
s_mov_b32 s73, 2
s_mov_b32 s74, 32768
s_mov_b32 s75, 49152
s_branch LRARE002_%=
LRARE002_%=:
s_nop 15
s_waitcnt vmcnt(0) lgkmcnt(0)
v_add_u32 v249, s74, v224
RD a[192:195],v249 offset:0
RD a[208:211],v249 offset:4096
RD a[224:227],v249 offset:8192
RD a[240:243],v249 offset:12288
v_add_u32 v250, s74, v225
RD a[196:199],v250 offset:0
RD a[212:215],v250 offset:4096
RD a[228:231],v250 offset:8192
RD a[244:247],v250 offset:12288
v_add_u32 v251, s74, v226
RD a[200:203],v251 offset:0
RD a[216:219],v251 offset:4096
RD a[232:235],v251 offset:8192
RD a[248:251],v251 offset:12288
v_add_u32 v254, s74, v227
RD a[204:207],v254 offset:0
RD a[220:223],v254 offset:4096
RD a[236:239],v254 offset:8192
RD a[252:255],v254 offset:12288
s_waitcnt lgkmcnt(0)
MM v[0:3],a[192:195],v[160:163],v[128:131]
MM v[0:3],a[196:199],v[176:179],v[0:3]
MM v[0:3],a[200:203],v[192:195],v[0:3]
MM v[0:3],a[204:207],v[208:211],v[0:3]
MM v[4:7],a[220:223],v[208:211],v[128:131]
MM v[4:7],a[216:219],v[192:195],v[4:7]
MM v[4:7],a[212:215],v[176:179],v[4:7]
MM v[4:7],a[208:211],v[160:163],v[4:7]
MM v[12:15],a[208:211],v[164:167],v[132:135]
MM v[12:15],a[212:215],v[180:183],v[12:15]
MM v[12:15],a[216:219],v[196:199],v[12:15]
MM v[12:15],a[220:223],v[212:215],v[12:15]
MM v[8:11],a[204:207],v[212:215],v[132:135]
MM v[8:11],a[200:203],v[196:199],v[8:11]
MM v[8:11],a[196:199],v[180:183],v[8:11]
MM v[8:11],a[192:195],v[164:167],v[8:11]
MM v[32:35],a[224:227],v[160:163],v[128:131]
MM v[32:35],a[228:231],v[176:179],v[32:35]
MM v[32:35],a[232:235],v[192:195],v[32:35]
MM v[32:35],a[236:239],v[208:211],v[32:35]
MM v[36:39],a[252:255],v[208:211],v[128:131]
MM v[36:39],a[248:251],v[192:195],v[36:39]
MM v[36:39],a[244:247],v[176:179],v[36:39]
MM v[36:39],a[240:243],v[160:163],v[36:39]
MM v[44:47],a[240:243],v[164:167],v[132:135]
MM v[44:47],a[244:247],v[180:183],v[44:47]
MM v[44:47],a[248:251],v[196:199],v[44:47]
MM v[44:47],a[252:255],v[212:215],v[44:47]
MM v[40:43],a[236:239],v[212:215],v[132:135]
MM v[40:43],a[232:235],v[196:199],v[40:43]
MM v[40:43],a[228:231],v[180:183],v[40:43]
MM v[40:43],a[224:227],v[164:167],v[40:43]
s_nop 15
v_add_u32 v249, s75, v224
RD a[192:195],v249 offset:0
RD a[208:211],v249 offset:4096
RD a[224:227],v249 offset:8192
RD a[240:243],v249 offset:12288
v_add_u32 v250, s75, v225
RD a[196:199],v250 offset:0
RD a[212:215],v250 offset:4096
RD a[228:231],v250 offset:8192
RD a[244:247],v250 offset:12288
v_add_u32 v251, s75, v226
RD a[200:203],v251 offset:0
RD a[216:219],v251 offset:4096
RD a[232:235],v251 offset:8192
RD a[248:251],v251 offset:12288
v_add_u32 v254, s75, v227
RD a[204:207],v254 offset:0
RD a[220:223],v254 offset:4096
RD a[236:239],v254 offset:8192
RD a[252:255],v254 offset:12288
s_waitcnt lgkmcnt(0)
v_max3_f32 v249, v0, v1, v2
v_max3_f32 v251, v32, v33, v34
v_max3_f32 v249, v249, v3, v4
v_max3_f32 v251, v251, v35, v36
v_max3_f32 v249, v249, v5, v6
v_max3_f32 v251, v251, v37, v38
v_max3_f32 v249, v249, v7, v39
v_max_f32 v249, v249, v251
v_mov_b32 v251, v249
s_nop 1
v_permlane16_swap_b32 v249, v251
v_max_f32 v249, v249, v251
v_mov_b32 v251, v249
s_nop 1
v_permlane32_swap_b32 v249, v251
v_max_f32 v249, v249, v251
v_log_f32 v250, v244
s_nop 0
v_max3_f32 v249, 0, v249, v250
v_sub_f32 v250, 0, v249
EX v250,v250
v_sub_f32 v128, v128, v249
v_sub_f32 v129, v129, v249
v_sub_f32 v130, v130, v249
v_sub_f32 v131, v131, v249
v_sub_f32 v64, v64, v249
v_sub_f32 v0, v0, v249
v_sub_f32 v65, v65, v249
v_sub_f32 v1, v1, v249
v_sub_f32 v66, v66, v249
v_sub_f32 v2, v2, v249
v_sub_f32 v67, v67, v249
v_sub_f32 v3, v3, v249
v_sub_f32 v68, v68, v249
v_sub_f32 v4, v4, v249
v_sub_f32 v69, v69, v249
v_sub_f32 v5, v5, v249
v_sub_f32 v70, v70, v249
v_sub_f32 v6, v6, v249
v_sub_f32 v71, v71, v249
v_sub_f32 v7, v7, v249
v_sub_f32 v96, v96, v249
v_sub_f32 v32, v32, v249
v_sub_f32 v97, v97, v249
v_sub_f32 v33, v33, v249
v_sub_f32 v98, v98, v249
v_sub_f32 v34, v34, v249
v_sub_f32 v99, v99, v249
v_sub_f32 v35, v35, v249
v_sub_f32 v100, v100, v249
v_sub_f32 v36, v36, v249
v_sub_f32 v101, v101, v249
v_sub_f32 v37, v37, v249
v_sub_f32 v102, v102, v249
v_sub_f32 v38, v38, v249
v_sub_f32 v103, v103, v249
v_sub_f32 v39, v39, v249
v_mul_f32 v144, v244, v250
v_mov_b32 v145, 0
v_bfe_u32 v249, v241, 0, 8
v_lshl_or_b32 v249, v249, 1, 1
v_lshlrev_b32 v249, 22, v249
v_and_b32 v241, 0xffffff00, v241
AR v254,a0
v_sub_f32 v254, v254, v249
v_mul_f32 v254, v254, v250
AW a0,v254
AR v251,a1
v_sub_f32 v251, v251, v249
v_mul_f32 v251, v251, v250
AW a1,v251
AR v254,a2
v_sub_f32 v254, v254, v249
v_mul_f32 v254, v254, v250
AW a2,v254
AR v251,a3
v_sub_f32 v251, v251, v249
v_mul_f32 v251, v251, v250
AW a3,v251
AR v254,a4
v_sub_f32 v254, v254, v249
v_mul_f32 v254, v254, v250
AW a4,v254
AR v251,a5
v_sub_f32 v251, v251, v249
v_mul_f32 v251, v251, v250
AW a5,v251
AR v254,a6
v_sub_f32 v254, v254, v249
v_mul_f32 v254, v254, v250
AW a6,v254
AR v251,a7
v_sub_f32 v251, v251, v249
v_mul_f32 v251, v251, v250
AW a7,v251
AR v254,a32
v_sub_f32 v254, v254, v249
v_mul_f32 v254, v254, v250
AW a32,v254
AR v251,a33
v_sub_f32 v251, v251, v249
v_mul_f32 v251, v251, v250
AW a33,v251
AR v254,a34
v_sub_f32 v254, v254, v249
v_mul_f32 v254, v254, v250
AW a34,v254
AR v251,a35
v_sub_f32 v251, v251, v249
v_mul_f32 v251, v251, v250
AW a35,v251
AR v254,a36
v_sub_f32 v254, v254, v249
v_mul_f32 v254, v254, v250
AW a36,v254
AR v251,a37
v_sub_f32 v251, v251, v249
v_mul_f32 v251, v251, v250
AW a37,v251
AR v254,a38
v_sub_f32 v254, v254, v249
v_mul_f32 v254, v254, v250
AW a38,v254
AR v251,a39
v_sub_f32 v251, v251, v249
v_mul_f32 v251, v251, v250
AW a39,v251
AR v254,a64
v_sub_f32 v254, v254, v249
v_mul_f32 v254, v254, v250
AW a64,v254
AR v251,a65
v_sub_f32 v251, v251, v249
v_mul_f32 v251, v251, v250
AW a65,v251
AR v254,a66
v_sub_f32 v254, v254, v249
v_mul_f32 v254, v254, v250
AW a66,v254
AR v251,a67
v_sub_f32 v251, v251, v249
v_mul_f32 v251, v251, v250
AW a67,v251
AR v254,a68
v_sub_f32 v254, v254, v249
v_mul_f32 v254, v254, v250
AW a68,v254
AR v251,a69
v_sub_f32 v251, v251, v249
v_mul_f32 v251, v251, v250
AW a69,v251
AR v254,a70
v_sub_f32 v254, v254, v249
v_mul_f32 v254, v254, v250
AW a70,v254
AR v251,a71
v_sub_f32 v251, v251, v249
v_mul_f32 v251, v251, v250
AW a71,v251
AR v254,a96
v_sub_f32 v254, v254, v249
v_mul_f32 v254, v254, v250
AW a96,v254
AR v251,a97
v_sub_f32 v251, v251, v249
v_mul_f32 v251, v251, v250
AW a97,v251
AR v254,a98
v_sub_f32 v254, v254, v249
v_mul_f32 v254, v254, v250
AW a98,v254
AR v251,a99
v_sub_f32 v251, v251, v249
v_mul_f32 v251, v251, v250
AW a99,v251
AR v254,a100
v_sub_f32 v254, v254, v249
v_mul_f32 v254, v254, v250
AW a100,v254
AR v251,a101
v_sub_f32 v251, v251, v249
v_mul_f32 v251, v251, v250
AW a101,v251
AR v254,a102
v_sub_f32 v254, v254, v249
v_mul_f32 v254, v254, v250
AW a102,v254
AR v251,a103
v_sub_f32 v251, v251, v249
v_mul_f32 v251, v251, v250
AW a103,v251
v_max3_f32 v249, v8, v9, v10
v_max3_f32 v251, v40, v41, v42
v_max3_f32 v249, v249, v11, v12
v_max3_f32 v251, v251, v43, v44
v_max3_f32 v249, v249, v13, v14
v_max3_f32 v251, v251, v45, v46
v_max3_f32 v249, v249, v15, v47
v_max_f32 v249, v249, v251
v_mov_b32 v251, v249
s_nop 1
v_permlane16_swap_b32 v249, v251
v_max_f32 v249, v249, v251
v_mov_b32 v251, v249
s_nop 1
v_permlane32_swap_b32 v249, v251
v_max_f32 v249, v249, v251
v_log_f32 v250, v245
s_nop 0
v_max3_f32 v249, 0, v249, v250
v_sub_f32 v250, 0, v249
EX v250,v250
v_sub_f32 v132, v132, v249
v_sub_f32 v133, v133, v249
v_sub_f32 v134, v134, v249
v_sub_f32 v135, v135, v249
v_sub_f32 v72, v72, v249
v_sub_f32 v8, v8, v249
v_sub_f32 v73, v73, v249
v_sub_f32 v9, v9, v249
v_sub_f32 v74, v74, v249
v_sub_f32 v10, v10, v249
v_sub_f32 v75, v75, v249
v_sub_f32 v11, v11, v249
v_sub_f32 v76, v76, v249
v_sub_f32 v12, v12, v249
v_sub_f32 v77, v77, v249
v_sub_f32 v13, v13, v249
v_sub_f32 v78, v78, v249
v_sub_f32 v14, v14, v249
v_sub_f32 v79, v79, v249
v_sub_f32 v15, v15, v249
v_sub_f32 v104, v104, v249
v_sub_f32 v40, v40, v249
v_sub_f32 v105, v105, v249
v_sub_f32 v41, v41, v249
v_sub_f32 v106, v106, v249
v_sub_f32 v42, v42, v249
v_sub_f32 v107, v107, v249
v_sub_f32 v43, v43, v249
v_sub_f32 v108, v108, v249
v_sub_f32 v44, v44, v249
v_sub_f32 v109, v109, v249
v_sub_f32 v45, v45, v249
v_sub_f32 v110, v110, v249
v_sub_f32 v46, v46, v249
v_sub_f32 v111, v111, v249
v_sub_f32 v47, v47, v249
v_mul_f32 v148, v245, v250
v_mov_b32 v149, 0
v_bfe_u32 v249, v241, 8, 8
v_lshl_or_b32 v249, v249, 1, 1
v_lshlrev_b32 v249, 22, v249
v_and_b32 v241, 0xffff00ff, v241
AR v254,a8
v_sub_f32 v254, v254, v249
v_mul_f32 v254, v254, v250
AW a8,v254
AR v251,a9
v_sub_f32 v251, v251, v249
v_mul_f32 v251, v251, v250
AW a9,v251
AR v254,a10
v_sub_f32 v254, v254, v249
v_mul_f32 v254, v254, v250
AW a10,v254
AR v251,a11
v_sub_f32 v251, v251, v249
v_mul_f32 v251, v251, v250
AW a11,v251
AR v254,a12
v_sub_f32 v254, v254, v249
v_mul_f32 v254, v254, v250
AW a12,v254
AR v251,a13
v_sub_f32 v251, v251, v249
v_mul_f32 v251, v251, v250
AW a13,v251
AR v254,a14
v_sub_f32 v254, v254, v249
v_mul_f32 v254, v254, v250
AW a14,v254
AR v251,a15
v_sub_f32 v251, v251, v249
v_mul_f32 v251, v251, v250
AW a15,v251
AR v254,a40
v_sub_f32 v254, v254, v249
v_mul_f32 v254, v254, v250
AW a40,v254
AR v251,a41
v_sub_f32 v251, v251, v249
v_mul_f32 v251, v251, v250
AW a41,v251
AR v254,a42
v_sub_f32 v254, v254, v249
v_mul_f32 v254, v254, v250
AW a42,v254
AR v251,a43
v_sub_f32 v251, v251, v249
v_mul_f32 v251, v251, v250
AW a43,v251
AR v254,a44
v_sub_f32 v254, v254, v249
v_mul_f32 v254, v254, v250
AW a44,v254
AR v251,a45
v_sub_f32 v251, v251, v249
v_mul_f32 v251, v251, v250
AW a45,v251
AR v254,a46
v_sub_f32 v254, v254, v249
v_mul_f32 v254, v254, v250
AW a46,v254
AR v251,a47
v_sub_f32 v251, v251, v249
v_mul_f32 v251, v251, v250
AW a47,v251
AR v254,a72
v_sub_f32 v254, v254, v249
v_mul_f32 v254, v254, v250
AW a72,v254
AR v251,a73
v_sub_f32 v251, v251, v249
v_mul_f32 v251, v251, v250
AW a73,v251
AR v254,a74
v_sub_f32 v254, v254, v249
v_mul_f32 v254, v254, v250
AW a74,v254
AR v251,a75
v_sub_f32 v251, v251, v249
v_mul_f32 v251, v251, v250
AW a75,v251
AR v254,a76
v_sub_f32 v254, v254, v249
v_mul_f32 v254, v254, v250
AW a76,v254
AR v251,a77
v_sub_f32 v251, v251, v249
v_mul_f32 v251, v251, v250
AW a77,v251
AR v254,a78
v_sub_f32 v254, v254, v249
v_mul_f32 v254, v254, v250
AW a78,v254
AR v251,a79
v_sub_f32 v251, v251, v249
v_mul_f32 v251, v251, v250
AW a79,v251
AR v254,a104
v_sub_f32 v254, v254, v249
v_mul_f32 v254, v254, v250
AW a104,v254
AR v251,a105
v_sub_f32 v251, v251, v249
v_mul_f32 v251, v251, v250
AW a105,v251
AR v254,a106
v_sub_f32 v254, v254, v249
v_mul_f32 v254, v254, v250
AW a106,v254
AR v251,a107
v_sub_f32 v251, v251, v249
v_mul_f32 v251, v251, v250
AW a107,v251
AR v254,a108
v_sub_f32 v254, v254, v249
v_mul_f32 v254, v254, v250
AW a108,v254
AR v251,a109
v_sub_f32 v251, v251, v249
v_mul_f32 v251, v251, v250
AW a109,v251
AR v254,a110
v_sub_f32 v254, v254, v249
v_mul_f32 v254, v254, v250
AW a110,v254
AR v251,a111
v_sub_f32 v251, v251, v249
v_mul_f32 v251, v251, v250
AW a111,v251
EX v0,v0
EX v1,v1
EX v2,v2
EX v3,v3
EX v4,v4
EX v5,v5
EX v6,v6
EX v7,v7
EX v8,v8
EX v9,v9
EX v10,v10
EX v11,v11
EX v12,v12
EX v13,v13
EX v14,v14
EX v15,v15
EX v32,v32
EX v33,v33
EX v34,v34
EX v35,v35
EX v36,v36
EX v37,v37
EX v38,v38
EX v39,v39
EX v40,v40
EX v41,v41
EX v42,v42
EX v43,v43
EX v44,v44
EX v45,v45
EX v46,v46
EX v47,v47
CV v0,v0,v1
CV v1,v2,v3
CV v2,v4,v5
CV v3,v6,v7
CV v8,v8,v9
CV v9,v10,v11
CV v10,v12,v13
CV v11,v14,v15
CV v4,v32,v33
CV v5,v34,v35
CV v6,v36,v37
CV v7,v38,v39
CV v12,v40,v41
CV v13,v42,v43
CV v14,v44,v45
CV v15,v46,v47
v_smfmac_f32_16x16x64_bf16 v[144:147], v[236:239], v[0:7], v234
s_nop 0
v_smfmac_f32_16x16x64_bf16 v[148:151], v[236:239], v[8:15], v234
s_nop 9
s_cmp_eq_u32 s73, 0
s_cbranch_scc1 LBACK0_%=
)ASM"
R"ASM(
s_cmp_eq_u32 s73, 2
s_cbranch_scc1 LBACK2_%=
s_endpgm
LSITE1_%=:
s_mov_b32 s73, 1
s_mov_b32 s74, 16384
s_mov_b32 s75, 32768
s_branch LRARE102_%=
LSITE3_%=:
s_mov_b32 s73, 3
s_mov_b32 s74, 49152
s_mov_b32 s75, 0
s_branch LRARE102_%=
LRARE102_%=:
s_nop 15
s_waitcnt vmcnt(0) lgkmcnt(0)
v_add_u32 v249, s74, v224
RD a[192:195],v249 offset:0
RD a[208:211],v249 offset:4096
RD a[224:227],v249 offset:8192
RD a[240:243],v249 offset:12288
v_add_u32 v250, s74, v225
RD a[196:199],v250 offset:0
RD a[212:215],v250 offset:4096
RD a[228:231],v250 offset:8192
RD a[244:247],v250 offset:12288
v_add_u32 v251, s74, v226
RD a[200:203],v251 offset:0
RD a[216:219],v251 offset:4096
RD a[232:235],v251 offset:8192
RD a[248:251],v251 offset:12288
v_add_u32 v254, s74, v227
RD a[204:207],v254 offset:0
RD a[220:223],v254 offset:4096
RD a[236:239],v254 offset:8192
RD a[252:255],v254 offset:12288
s_waitcnt lgkmcnt(0)
MM v[64:67],a[192:195],v[160:163],v[128:131]
MM v[64:67],a[196:199],v[176:179],v[64:67]
MM v[64:67],a[200:203],v[192:195],v[64:67]
MM v[64:67],a[204:207],v[208:211],v[64:67]
MM v[68:71],a[220:223],v[208:211],v[128:131]
MM v[68:71],a[216:219],v[192:195],v[68:71]
MM v[68:71],a[212:215],v[176:179],v[68:71]
MM v[68:71],a[208:211],v[160:163],v[68:71]
MM v[76:79],a[208:211],v[164:167],v[132:135]
MM v[76:79],a[212:215],v[180:183],v[76:79]
MM v[76:79],a[216:219],v[196:199],v[76:79]
MM v[76:79],a[220:223],v[212:215],v[76:79]
MM v[72:75],a[204:207],v[212:215],v[132:135]
MM v[72:75],a[200:203],v[196:199],v[72:75]
MM v[72:75],a[196:199],v[180:183],v[72:75]
MM v[72:75],a[192:195],v[164:167],v[72:75]
MM v[96:99],a[224:227],v[160:163],v[128:131]
MM v[96:99],a[228:231],v[176:179],v[96:99]
MM v[96:99],a[232:235],v[192:195],v[96:99]
MM v[96:99],a[236:239],v[208:211],v[96:99]
MM v[100:103],a[252:255],v[208:211],v[128:131]
MM v[100:103],a[248:251],v[192:195],v[100:103]
MM v[100:103],a[244:247],v[176:179],v[100:103]
MM v[100:103],a[240:243],v[160:163],v[100:103]
MM v[108:111],a[240:243],v[164:167],v[132:135]
MM v[108:111],a[244:247],v[180:183],v[108:111]
MM v[108:111],a[248:251],v[196:199],v[108:111]
MM v[108:111],a[252:255],v[212:215],v[108:111]
MM v[104:107],a[236:239],v[212:215],v[132:135]
MM v[104:107],a[232:235],v[196:199],v[104:107]
MM v[104:107],a[228:231],v[180:183],v[104:107]
MM v[104:107],a[224:227],v[164:167],v[104:107]
s_nop 15
v_add_u32 v249, s75, v224
RD a[192:195],v249 offset:0
RD a[208:211],v249 offset:4096
RD a[224:227],v249 offset:8192
RD a[240:243],v249 offset:12288
v_add_u32 v250, s75, v225
RD a[196:199],v250 offset:0
RD a[212:215],v250 offset:4096
RD a[228:231],v250 offset:8192
RD a[244:247],v250 offset:12288
v_add_u32 v251, s75, v226
RD a[200:203],v251 offset:0
RD a[216:219],v251 offset:4096
RD a[232:235],v251 offset:8192
RD a[248:251],v251 offset:12288
v_add_u32 v254, s75, v227
RD a[204:207],v254 offset:0
RD a[220:223],v254 offset:4096
RD a[236:239],v254 offset:8192
RD a[252:255],v254 offset:12288
s_waitcnt lgkmcnt(0)
v_max3_f32 v249, v64, v65, v66
v_max3_f32 v251, v96, v97, v98
v_max3_f32 v249, v249, v67, v68
v_max3_f32 v251, v251, v99, v100
v_max3_f32 v249, v249, v69, v70
v_max3_f32 v251, v251, v101, v102
v_max3_f32 v249, v249, v71, v103
v_max_f32 v249, v249, v251
v_mov_b32 v251, v249
s_nop 1
v_permlane16_swap_b32 v249, v251
v_max_f32 v249, v249, v251
v_mov_b32 v251, v249
s_nop 1
v_permlane32_swap_b32 v249, v251
v_max_f32 v249, v249, v251
v_log_f32 v250, v244
s_nop 0
v_max3_f32 v249, 0, v249, v250
v_sub_f32 v250, 0, v249
EX v250,v250
v_sub_f32 v128, v128, v249
v_sub_f32 v129, v129, v249
v_sub_f32 v130, v130, v249
v_sub_f32 v131, v131, v249
v_sub_f32 v0, v0, v249
v_sub_f32 v64, v64, v249
v_sub_f32 v1, v1, v249
v_sub_f32 v65, v65, v249
v_sub_f32 v2, v2, v249
v_sub_f32 v66, v66, v249
v_sub_f32 v3, v3, v249
v_sub_f32 v67, v67, v249
v_sub_f32 v4, v4, v249
v_sub_f32 v68, v68, v249
v_sub_f32 v5, v5, v249
v_sub_f32 v69, v69, v249
v_sub_f32 v6, v6, v249
v_sub_f32 v70, v70, v249
v_sub_f32 v7, v7, v249
v_sub_f32 v71, v71, v249
v_sub_f32 v32, v32, v249
v_sub_f32 v96, v96, v249
v_sub_f32 v33, v33, v249
v_sub_f32 v97, v97, v249
v_sub_f32 v34, v34, v249
v_sub_f32 v98, v98, v249
v_sub_f32 v35, v35, v249
v_sub_f32 v99, v99, v249
v_sub_f32 v36, v36, v249
v_sub_f32 v100, v100, v249
v_sub_f32 v37, v37, v249
v_sub_f32 v101, v101, v249
v_sub_f32 v38, v38, v249
v_sub_f32 v102, v102, v249
v_sub_f32 v39, v39, v249
v_sub_f32 v103, v103, v249
v_mul_f32 v144, v244, v250
v_mov_b32 v145, 0
v_bfe_u32 v249, v241, 0, 8
v_lshl_or_b32 v249, v249, 1, 1
v_lshlrev_b32 v249, 22, v249
v_and_b32 v241, 0xffffff00, v241
AR v254,a0
v_sub_f32 v254, v254, v249
v_mul_f32 v254, v254, v250
AW a0,v254
AR v251,a1
v_sub_f32 v251, v251, v249
v_mul_f32 v251, v251, v250
AW a1,v251
AR v254,a2
v_sub_f32 v254, v254, v249
v_mul_f32 v254, v254, v250
AW a2,v254
AR v251,a3
v_sub_f32 v251, v251, v249
v_mul_f32 v251, v251, v250
AW a3,v251
AR v254,a4
v_sub_f32 v254, v254, v249
v_mul_f32 v254, v254, v250
AW a4,v254
AR v251,a5
v_sub_f32 v251, v251, v249
v_mul_f32 v251, v251, v250
AW a5,v251
AR v254,a6
v_sub_f32 v254, v254, v249
v_mul_f32 v254, v254, v250
AW a6,v254
AR v251,a7
v_sub_f32 v251, v251, v249
v_mul_f32 v251, v251, v250
AW a7,v251
AR v254,a32
v_sub_f32 v254, v254, v249
v_mul_f32 v254, v254, v250
AW a32,v254
AR v251,a33
v_sub_f32 v251, v251, v249
v_mul_f32 v251, v251, v250
AW a33,v251
AR v254,a34
v_sub_f32 v254, v254, v249
v_mul_f32 v254, v254, v250
AW a34,v254
AR v251,a35
v_sub_f32 v251, v251, v249
v_mul_f32 v251, v251, v250
AW a35,v251
AR v254,a36
v_sub_f32 v254, v254, v249
v_mul_f32 v254, v254, v250
AW a36,v254
AR v251,a37
v_sub_f32 v251, v251, v249
v_mul_f32 v251, v251, v250
AW a37,v251
AR v254,a38
v_sub_f32 v254, v254, v249
v_mul_f32 v254, v254, v250
AW a38,v254
AR v251,a39
v_sub_f32 v251, v251, v249
v_mul_f32 v251, v251, v250
AW a39,v251
AR v254,a64
v_sub_f32 v254, v254, v249
v_mul_f32 v254, v254, v250
AW a64,v254
AR v251,a65
v_sub_f32 v251, v251, v249
v_mul_f32 v251, v251, v250
AW a65,v251
AR v254,a66
v_sub_f32 v254, v254, v249
v_mul_f32 v254, v254, v250
AW a66,v254
AR v251,a67
v_sub_f32 v251, v251, v249
v_mul_f32 v251, v251, v250
AW a67,v251
AR v254,a68
v_sub_f32 v254, v254, v249
v_mul_f32 v254, v254, v250
AW a68,v254
AR v251,a69
v_sub_f32 v251, v251, v249
v_mul_f32 v251, v251, v250
AW a69,v251
AR v254,a70
v_sub_f32 v254, v254, v249
v_mul_f32 v254, v254, v250
AW a70,v254
AR v251,a71
v_sub_f32 v251, v251, v249
v_mul_f32 v251, v251, v250
AW a71,v251
AR v254,a96
v_sub_f32 v254, v254, v249
v_mul_f32 v254, v254, v250
AW a96,v254
AR v251,a97
v_sub_f32 v251, v251, v249
v_mul_f32 v251, v251, v250
AW a97,v251
AR v254,a98
v_sub_f32 v254, v254, v249
v_mul_f32 v254, v254, v250
AW a98,v254
AR v251,a99
v_sub_f32 v251, v251, v249
v_mul_f32 v251, v251, v250
AW a99,v251
AR v254,a100
v_sub_f32 v254, v254, v249
v_mul_f32 v254, v254, v250
AW a100,v254
AR v251,a101
v_sub_f32 v251, v251, v249
v_mul_f32 v251, v251, v250
AW a101,v251
AR v254,a102
v_sub_f32 v254, v254, v249
v_mul_f32 v254, v254, v250
AW a102,v254
AR v251,a103
v_sub_f32 v251, v251, v249
v_mul_f32 v251, v251, v250
AW a103,v251
v_max3_f32 v249, v72, v73, v74
v_max3_f32 v251, v104, v105, v106
v_max3_f32 v249, v249, v75, v76
v_max3_f32 v251, v251, v107, v108
v_max3_f32 v249, v249, v77, v78
v_max3_f32 v251, v251, v109, v110
v_max3_f32 v249, v249, v79, v111
v_max_f32 v249, v249, v251
v_mov_b32 v251, v249
s_nop 1
v_permlane16_swap_b32 v249, v251
v_max_f32 v249, v249, v251
v_mov_b32 v251, v249
s_nop 1
v_permlane32_swap_b32 v249, v251
v_max_f32 v249, v249, v251
v_log_f32 v250, v245
s_nop 0
v_max3_f32 v249, 0, v249, v250
v_sub_f32 v250, 0, v249
EX v250,v250
v_sub_f32 v132, v132, v249
v_sub_f32 v133, v133, v249
v_sub_f32 v134, v134, v249
v_sub_f32 v135, v135, v249
v_sub_f32 v8, v8, v249
v_sub_f32 v72, v72, v249
v_sub_f32 v9, v9, v249
v_sub_f32 v73, v73, v249
v_sub_f32 v10, v10, v249
v_sub_f32 v74, v74, v249
v_sub_f32 v11, v11, v249
v_sub_f32 v75, v75, v249
v_sub_f32 v12, v12, v249
v_sub_f32 v76, v76, v249
v_sub_f32 v13, v13, v249
v_sub_f32 v77, v77, v249
v_sub_f32 v14, v14, v249
v_sub_f32 v78, v78, v249
v_sub_f32 v15, v15, v249
v_sub_f32 v79, v79, v249
v_sub_f32 v40, v40, v249
v_sub_f32 v104, v104, v249
v_sub_f32 v41, v41, v249
v_sub_f32 v105, v105, v249
v_sub_f32 v42, v42, v249
v_sub_f32 v106, v106, v249
v_sub_f32 v43, v43, v249
v_sub_f32 v107, v107, v249
v_sub_f32 v44, v44, v249
v_sub_f32 v108, v108, v249
v_sub_f32 v45, v45, v249
v_sub_f32 v109, v109, v249
v_sub_f32 v46, v46, v249
v_sub_f32 v110, v110, v249
v_sub_f32 v47, v47, v249
v_sub_f32 v111, v111, v249
v_mul_f32 v148, v245, v250
v_mov_b32 v149, 0
v_bfe_u32 v249, v241, 8, 8
v_lshl_or_b32 v249, v249, 1, 1
v_lshlrev_b32 v249, 22, v249
v_and_b32 v241, 0xffff00ff, v241
AR v254,a8
v_sub_f32 v254, v254, v249
v_mul_f32 v254, v254, v250
AW a8,v254
AR v251,a9
v_sub_f32 v251, v251, v249
v_mul_f32 v251, v251, v250
AW a9,v251
AR v254,a10
v_sub_f32 v254, v254, v249
v_mul_f32 v254, v254, v250
AW a10,v254
AR v251,a11
v_sub_f32 v251, v251, v249
v_mul_f32 v251, v251, v250
AW a11,v251
AR v254,a12
v_sub_f32 v254, v254, v249
v_mul_f32 v254, v254, v250
AW a12,v254
AR v251,a13
v_sub_f32 v251, v251, v249
v_mul_f32 v251, v251, v250
AW a13,v251
AR v254,a14
v_sub_f32 v254, v254, v249
v_mul_f32 v254, v254, v250
AW a14,v254
AR v251,a15
v_sub_f32 v251, v251, v249
v_mul_f32 v251, v251, v250
AW a15,v251
AR v254,a40
v_sub_f32 v254, v254, v249
v_mul_f32 v254, v254, v250
AW a40,v254
AR v251,a41
v_sub_f32 v251, v251, v249
v_mul_f32 v251, v251, v250
AW a41,v251
AR v254,a42
v_sub_f32 v254, v254, v249
v_mul_f32 v254, v254, v250
AW a42,v254
AR v251,a43
v_sub_f32 v251, v251, v249
v_mul_f32 v251, v251, v250
AW a43,v251
AR v254,a44
v_sub_f32 v254, v254, v249
v_mul_f32 v254, v254, v250
AW a44,v254
AR v251,a45
v_sub_f32 v251, v251, v249
v_mul_f32 v251, v251, v250
AW a45,v251
AR v254,a46
v_sub_f32 v254, v254, v249
v_mul_f32 v254, v254, v250
AW a46,v254
AR v251,a47
v_sub_f32 v251, v251, v249
v_mul_f32 v251, v251, v250
AW a47,v251
AR v254,a72
v_sub_f32 v254, v254, v249
v_mul_f32 v254, v254, v250
AW a72,v254
AR v251,a73
v_sub_f32 v251, v251, v249
v_mul_f32 v251, v251, v250
AW a73,v251
AR v254,a74
v_sub_f32 v254, v254, v249
v_mul_f32 v254, v254, v250
AW a74,v254
AR v251,a75
v_sub_f32 v251, v251, v249
v_mul_f32 v251, v251, v250
AW a75,v251
AR v254,a76
v_sub_f32 v254, v254, v249
v_mul_f32 v254, v254, v250
AW a76,v254
AR v251,a77
v_sub_f32 v251, v251, v249
v_mul_f32 v251, v251, v250
AW a77,v251
AR v254,a78
v_sub_f32 v254, v254, v249
v_mul_f32 v254, v254, v250
AW a78,v254
AR v251,a79
v_sub_f32 v251, v251, v249
v_mul_f32 v251, v251, v250
AW a79,v251
AR v254,a104
v_sub_f32 v254, v254, v249
v_mul_f32 v254, v254, v250
AW a104,v254
AR v251,a105
v_sub_f32 v251, v251, v249
v_mul_f32 v251, v251, v250
AW a105,v251
AR v254,a106
v_sub_f32 v254, v254, v249
v_mul_f32 v254, v254, v250
AW a106,v254
AR v251,a107
v_sub_f32 v251, v251, v249
v_mul_f32 v251, v251, v250
AW a107,v251
AR v254,a108
v_sub_f32 v254, v254, v249
v_mul_f32 v254, v254, v250
AW a108,v254
AR v251,a109
v_sub_f32 v251, v251, v249
v_mul_f32 v251, v251, v250
AW a109,v251
AR v254,a110
v_sub_f32 v254, v254, v249
v_mul_f32 v254, v254, v250
AW a110,v254
AR v251,a111
v_sub_f32 v251, v251, v249
v_mul_f32 v251, v251, v250
AW a111,v251
EX v64,v64
EX v65,v65
EX v66,v66
EX v67,v67
EX v68,v68
EX v69,v69
EX v70,v70
EX v71,v71
EX v72,v72
EX v73,v73
EX v74,v74
EX v75,v75
EX v76,v76
EX v77,v77
EX v78,v78
EX v79,v79
EX v96,v96
EX v97,v97
EX v98,v98
EX v99,v99
EX v100,v100
EX v101,v101
EX v102,v102
EX v103,v103
EX v104,v104
EX v105,v105
EX v106,v106
EX v107,v107
EX v108,v108
EX v109,v109
EX v110,v110
EX v111,v111
CV v64,v64,v65
CV v65,v66,v67
CV v66,v68,v69
CV v67,v70,v71
CV v72,v72,v73
CV v73,v74,v75
CV v74,v76,v77
CV v75,v78,v79
CV v68,v96,v97
CV v69,v98,v99
CV v70,v100,v101
CV v71,v102,v103
CV v76,v104,v105
CV v77,v106,v107
CV v78,v108,v109
CV v79,v110,v111
v_smfmac_f32_16x16x64_bf16 v[144:147], v[236:239], v[64:71], v234
s_nop 0
v_smfmac_f32_16x16x64_bf16 v[148:151], v[236:239], v[72:79], v234
s_nop 9
s_cmp_eq_u32 s73, 1
s_cbranch_scc1 LBACK1_%=
s_cmp_eq_u32 s73, 3
s_cbranch_scc1 LBACK3_%=
)ASM"
R"ASM(
s_endpgm
LSITE4_%=:
s_mov_b32 s73, 4
s_mov_b32 s74, 0
s_mov_b32 s75, 16384
s_branch LRARE004_%=
LSITE6_%=:
s_mov_b32 s73, 6
s_mov_b32 s74, 32768
s_mov_b32 s75, 49152
s_branch LRARE004_%=
LSITE8_%=:
s_mov_b32 s73, 8
s_mov_b32 s74, 0
s_mov_b32 s75, 16384
s_branch LRARE004_%=
LSITE10_%=:
s_mov_b32 s73, 10
s_mov_b32 s74, 32768
s_mov_b32 s75, 49152
s_branch LRARE004_%=
LSITE12_%=:
s_mov_b32 s73, 12
s_mov_b32 s74, 0
s_mov_b32 s75, 16384
s_branch LRARE004_%=
LSITE14_%=:
s_mov_b32 s73, 14
s_mov_b32 s74, 32768
s_mov_b32 s75, 49152
s_branch LRARE004_%=
LSITE16_%=:
s_mov_b32 s73, 16
s_mov_b32 s74, 0
s_mov_b32 s75, 16384
s_branch LRARE004_%=
LSITE18_%=:
s_mov_b32 s73, 18
s_mov_b32 s74, 32768
s_mov_b32 s75, 49152
s_branch LRARE004_%=
LSITE20_%=:
s_mov_b32 s73, 20
s_mov_b32 s74, 0
s_mov_b32 s75, 16384
s_branch LRARE004_%=
LSITE22_%=:
s_mov_b32 s73, 22
s_mov_b32 s74, 32768
s_mov_b32 s75, 49152
s_branch LRARE004_%=
LSITE24_%=:
s_mov_b32 s73, 24
s_mov_b32 s74, 0
s_mov_b32 s75, 16384
s_branch LRARE004_%=
LSITE26_%=:
s_mov_b32 s73, 26
s_mov_b32 s74, 32768
s_mov_b32 s75, 49152
s_branch LRARE004_%=
LSITE28_%=:
s_mov_b32 s73, 28
s_mov_b32 s74, 0
s_mov_b32 s75, 16384
s_branch LRARE004_%=
LSITE30_%=:
s_mov_b32 s73, 30
s_mov_b32 s74, 32768
s_mov_b32 s75, 49152
s_branch LRARE004_%=
LSITE32_%=:
s_mov_b32 s73, 32
s_mov_b32 s74, 0
s_mov_b32 s75, 16384
s_branch LRARE004_%=
LSITE34_%=:
s_mov_b32 s73, 34
s_mov_b32 s74, 32768
s_mov_b32 s75, 49152
s_branch LRARE004_%=
LRARE004_%=:
s_nop 15
s_waitcnt vmcnt(0) lgkmcnt(0)
v_add_u32 v249, s74, v224
RD a[192:195],v249 offset:0
RD a[208:211],v249 offset:4096
RD a[224:227],v249 offset:8192
RD a[240:243],v249 offset:12288
v_add_u32 v250, s74, v225
RD a[196:199],v250 offset:0
RD a[212:215],v250 offset:4096
RD a[228:231],v250 offset:8192
RD a[244:247],v250 offset:12288
v_add_u32 v251, s74, v226
RD a[200:203],v251 offset:0
RD a[216:219],v251 offset:4096
RD a[232:235],v251 offset:8192
RD a[248:251],v251 offset:12288
v_add_u32 v254, s74, v227
RD a[204:207],v254 offset:0
RD a[220:223],v254 offset:4096
RD a[236:239],v254 offset:8192
RD a[252:255],v254 offset:12288
s_waitcnt lgkmcnt(0)
MM v[0:3],a[192:195],v[160:163],v[128:131]
MM v[0:3],a[196:199],v[176:179],v[0:3]
MM v[0:3],a[200:203],v[192:195],v[0:3]
MM v[0:3],a[204:207],v[208:211],v[0:3]
MM v[4:7],a[220:223],v[208:211],v[128:131]
MM v[4:7],a[216:219],v[192:195],v[4:7]
MM v[4:7],a[212:215],v[176:179],v[4:7]
MM v[4:7],a[208:211],v[160:163],v[4:7]
MM v[12:15],a[208:211],v[164:167],v[132:135]
MM v[12:15],a[212:215],v[180:183],v[12:15]
MM v[12:15],a[216:219],v[196:199],v[12:15]
MM v[12:15],a[220:223],v[212:215],v[12:15]
MM v[8:11],a[204:207],v[212:215],v[132:135]
MM v[8:11],a[200:203],v[196:199],v[8:11]
MM v[8:11],a[196:199],v[180:183],v[8:11]
MM v[8:11],a[192:195],v[164:167],v[8:11]
MM v[16:19],a[192:195],v[168:171],v[136:139]
MM v[16:19],a[196:199],v[184:187],v[16:19]
MM v[16:19],a[200:203],v[200:203],v[16:19]
MM v[16:19],a[204:207],v[216:219],v[16:19]
MM v[20:23],a[220:223],v[216:219],v[136:139]
MM v[20:23],a[216:219],v[200:203],v[20:23]
MM v[20:23],a[212:215],v[184:187],v[20:23]
MM v[20:23],a[208:211],v[168:171],v[20:23]
MM v[28:31],a[208:211],v[172:175],v[140:143]
MM v[28:31],a[212:215],v[188:191],v[28:31]
MM v[28:31],a[216:219],v[204:207],v[28:31]
MM v[28:31],a[220:223],v[220:223],v[28:31]
MM v[24:27],a[204:207],v[220:223],v[140:143]
MM v[24:27],a[200:203],v[204:207],v[24:27]
MM v[24:27],a[196:199],v[188:191],v[24:27]
MM v[24:27],a[192:195],v[172:175],v[24:27]
MM v[32:35],a[224:227],v[160:163],v[128:131]
MM v[32:35],a[228:231],v[176:179],v[32:35]
MM v[32:35],a[232:235],v[192:195],v[32:35]
MM v[32:35],a[236:239],v[208:211],v[32:35]
MM v[36:39],a[252:255],v[208:211],v[128:131]
MM v[36:39],a[248:251],v[192:195],v[36:39]
MM v[36:39],a[244:247],v[176:179],v[36:39]
MM v[36:39],a[240:243],v[160:163],v[36:39]
MM v[44:47],a[240:243],v[164:167],v[132:135]
MM v[44:47],a[244:247],v[180:183],v[44:47]
MM v[44:47],a[248:251],v[196:199],v[44:47]
MM v[44:47],a[252:255],v[212:215],v[44:47]
MM v[40:43],a[236:239],v[212:215],v[132:135]
MM v[40:43],a[232:235],v[196:199],v[40:43]
MM v[40:43],a[228:231],v[180:183],v[40:43]
MM v[40:43],a[224:227],v[164:167],v[40:43]
MM v[48:51],a[224:227],v[168:171],v[136:139]
MM v[48:51],a[228:231],v[184:187],v[48:51]
MM v[48:51],a[232:235],v[200:203],v[48:51]
MM v[48:51],a[236:239],v[216:219],v[48:51]
MM v[52:55],a[252:255],v[216:219],v[136:139]
MM v[52:55],a[248:251],v[200:203],v[52:55]
MM v[52:55],a[244:247],v[184:187],v[52:55]
MM v[52:55],a[240:243],v[168:171],v[52:55]
MM v[60:63],a[240:243],v[172:175],v[140:143]
MM v[60:63],a[244:247],v[188:191],v[60:63]
MM v[60:63],a[248:251],v[204:207],v[60:63]
MM v[60:63],a[252:255],v[220:223],v[60:63]
MM v[56:59],a[236:239],v[220:223],v[140:143]
MM v[56:59],a[232:235],v[204:207],v[56:59]
MM v[56:59],a[228:231],v[188:191],v[56:59]
MM v[56:59],a[224:227],v[172:175],v[56:59]
s_nop 15
v_add_u32 v249, s75, v224
RD a[192:195],v249 offset:0
RD a[208:211],v249 offset:4096
RD a[224:227],v249 offset:8192
RD a[240:243],v249 offset:12288
v_add_u32 v250, s75, v225
RD a[196:199],v250 offset:0
RD a[212:215],v250 offset:4096
RD a[228:231],v250 offset:8192
RD a[244:247],v250 offset:12288
v_add_u32 v251, s75, v226
RD a[200:203],v251 offset:0
RD a[216:219],v251 offset:4096
RD a[232:235],v251 offset:8192
RD a[248:251],v251 offset:12288
v_add_u32 v254, s75, v227
RD a[204:207],v254 offset:0
RD a[220:223],v254 offset:4096
RD a[236:239],v254 offset:8192
RD a[252:255],v254 offset:12288
s_waitcnt lgkmcnt(0)
v_max3_f32 v249, v0, v1, v2
v_max3_f32 v251, v32, v33, v34
v_max3_f32 v249, v249, v3, v4
v_max3_f32 v251, v251, v35, v36
v_max3_f32 v249, v249, v5, v6
v_max3_f32 v251, v251, v37, v38
v_max3_f32 v249, v249, v7, v39
v_max_f32 v249, v249, v251
v_mov_b32 v251, v249
s_nop 1
v_permlane16_swap_b32 v249, v251
v_max_f32 v249, v249, v251
v_mov_b32 v251, v249
s_nop 1
v_permlane32_swap_b32 v249, v251
v_max_f32 v249, v249, v251
v_log_f32 v250, v244
s_nop 0
v_max3_f32 v249, 0, v249, v250
v_sub_f32 v250, 0, v249
EX v250,v250
v_sub_f32 v128, v128, v249
v_sub_f32 v129, v129, v249
v_sub_f32 v130, v130, v249
v_sub_f32 v131, v131, v249
v_sub_f32 v64, v64, v249
v_sub_f32 v0, v0, v249
v_sub_f32 v65, v65, v249
v_sub_f32 v1, v1, v249
v_sub_f32 v66, v66, v249
v_sub_f32 v2, v2, v249
v_sub_f32 v67, v67, v249
v_sub_f32 v3, v3, v249
v_sub_f32 v68, v68, v249
v_sub_f32 v4, v4, v249
v_sub_f32 v69, v69, v249
v_sub_f32 v5, v5, v249
v_sub_f32 v70, v70, v249
v_sub_f32 v6, v6, v249
v_sub_f32 v71, v71, v249
v_sub_f32 v7, v7, v249
v_sub_f32 v96, v96, v249
v_sub_f32 v32, v32, v249
v_sub_f32 v97, v97, v249
v_sub_f32 v33, v33, v249
v_sub_f32 v98, v98, v249
v_sub_f32 v34, v34, v249
v_sub_f32 v99, v99, v249
v_sub_f32 v35, v35, v249
v_sub_f32 v100, v100, v249
v_sub_f32 v36, v36, v249
v_sub_f32 v101, v101, v249
v_sub_f32 v37, v37, v249
v_sub_f32 v102, v102, v249
v_sub_f32 v38, v38, v249
v_sub_f32 v103, v103, v249
v_sub_f32 v39, v39, v249
v_mul_f32 v144, v244, v250
v_mov_b32 v145, 0
v_bfe_u32 v249, v241, 0, 8
v_lshl_or_b32 v249, v249, 1, 1
v_lshlrev_b32 v249, 22, v249
v_and_b32 v241, 0xffffff00, v241
AR v254,a0
v_sub_f32 v254, v254, v249
v_mul_f32 v254, v254, v250
AW a0,v254
AR v251,a1
v_sub_f32 v251, v251, v249
v_mul_f32 v251, v251, v250
AW a1,v251
AR v254,a2
v_sub_f32 v254, v254, v249
v_mul_f32 v254, v254, v250
AW a2,v254
AR v251,a3
v_sub_f32 v251, v251, v249
v_mul_f32 v251, v251, v250
AW a3,v251
AR v254,a4
v_sub_f32 v254, v254, v249
v_mul_f32 v254, v254, v250
AW a4,v254
AR v251,a5
v_sub_f32 v251, v251, v249
v_mul_f32 v251, v251, v250
AW a5,v251
AR v254,a6
v_sub_f32 v254, v254, v249
v_mul_f32 v254, v254, v250
AW a6,v254
AR v251,a7
v_sub_f32 v251, v251, v249
v_mul_f32 v251, v251, v250
AW a7,v251
AR v254,a32
v_sub_f32 v254, v254, v249
v_mul_f32 v254, v254, v250
AW a32,v254
AR v251,a33
v_sub_f32 v251, v251, v249
v_mul_f32 v251, v251, v250
AW a33,v251
AR v254,a34
v_sub_f32 v254, v254, v249
v_mul_f32 v254, v254, v250
AW a34,v254
AR v251,a35
v_sub_f32 v251, v251, v249
v_mul_f32 v251, v251, v250
AW a35,v251
AR v254,a36
v_sub_f32 v254, v254, v249
v_mul_f32 v254, v254, v250
AW a36,v254
AR v251,a37
v_sub_f32 v251, v251, v249
v_mul_f32 v251, v251, v250
AW a37,v251
AR v254,a38
v_sub_f32 v254, v254, v249
v_mul_f32 v254, v254, v250
AW a38,v254
AR v251,a39
v_sub_f32 v251, v251, v249
v_mul_f32 v251, v251, v250
AW a39,v251
AR v254,a64
v_sub_f32 v254, v254, v249
v_mul_f32 v254, v254, v250
AW a64,v254
AR v251,a65
v_sub_f32 v251, v251, v249
v_mul_f32 v251, v251, v250
AW a65,v251
AR v254,a66
v_sub_f32 v254, v254, v249
v_mul_f32 v254, v254, v250
AW a66,v254
AR v251,a67
v_sub_f32 v251, v251, v249
v_mul_f32 v251, v251, v250
AW a67,v251
AR v254,a68
v_sub_f32 v254, v254, v249
v_mul_f32 v254, v254, v250
AW a68,v254
AR v251,a69
v_sub_f32 v251, v251, v249
v_mul_f32 v251, v251, v250
AW a69,v251
AR v254,a70
v_sub_f32 v254, v254, v249
v_mul_f32 v254, v254, v250
AW a70,v254
AR v251,a71
v_sub_f32 v251, v251, v249
v_mul_f32 v251, v251, v250
AW a71,v251
AR v254,a96
v_sub_f32 v254, v254, v249
v_mul_f32 v254, v254, v250
AW a96,v254
AR v251,a97
v_sub_f32 v251, v251, v249
v_mul_f32 v251, v251, v250
AW a97,v251
AR v254,a98
v_sub_f32 v254, v254, v249
v_mul_f32 v254, v254, v250
AW a98,v254
AR v251,a99
v_sub_f32 v251, v251, v249
v_mul_f32 v251, v251, v250
AW a99,v251
AR v254,a100
v_sub_f32 v254, v254, v249
v_mul_f32 v254, v254, v250
AW a100,v254
AR v251,a101
v_sub_f32 v251, v251, v249
v_mul_f32 v251, v251, v250
AW a101,v251
AR v254,a102
v_sub_f32 v254, v254, v249
v_mul_f32 v254, v254, v250
AW a102,v254
AR v251,a103
v_sub_f32 v251, v251, v249
v_mul_f32 v251, v251, v250
AW a103,v251
v_max3_f32 v249, v8, v9, v10
v_max3_f32 v251, v40, v41, v42
v_max3_f32 v249, v249, v11, v12
v_max3_f32 v251, v251, v43, v44
v_max3_f32 v249, v249, v13, v14
v_max3_f32 v251, v251, v45, v46
v_max3_f32 v249, v249, v15, v47
v_max_f32 v249, v249, v251
v_mov_b32 v251, v249
s_nop 1
v_permlane16_swap_b32 v249, v251
v_max_f32 v249, v249, v251
v_mov_b32 v251, v249
s_nop 1
v_permlane32_swap_b32 v249, v251
v_max_f32 v249, v249, v251
v_log_f32 v250, v245
s_nop 0
v_max3_f32 v249, 0, v249, v250
v_sub_f32 v250, 0, v249
EX v250,v250
v_sub_f32 v132, v132, v249
v_sub_f32 v133, v133, v249
v_sub_f32 v134, v134, v249
v_sub_f32 v135, v135, v249
v_sub_f32 v72, v72, v249
v_sub_f32 v8, v8, v249
v_sub_f32 v73, v73, v249
v_sub_f32 v9, v9, v249
v_sub_f32 v74, v74, v249
v_sub_f32 v10, v10, v249
v_sub_f32 v75, v75, v249
v_sub_f32 v11, v11, v249
v_sub_f32 v76, v76, v249
v_sub_f32 v12, v12, v249
v_sub_f32 v77, v77, v249
v_sub_f32 v13, v13, v249
v_sub_f32 v78, v78, v249
v_sub_f32 v14, v14, v249
v_sub_f32 v79, v79, v249
v_sub_f32 v15, v15, v249
v_sub_f32 v104, v104, v249
v_sub_f32 v40, v40, v249
v_sub_f32 v105, v105, v249
v_sub_f32 v41, v41, v249
v_sub_f32 v106, v106, v249
v_sub_f32 v42, v42, v249
v_sub_f32 v107, v107, v249
v_sub_f32 v43, v43, v249
v_sub_f32 v108, v108, v249
v_sub_f32 v44, v44, v249
v_sub_f32 v109, v109, v249
v_sub_f32 v45, v45, v249
v_sub_f32 v110, v110, v249
v_sub_f32 v46, v46, v249
v_sub_f32 v111, v111, v249
v_sub_f32 v47, v47, v249
v_mul_f32 v148, v245, v250
v_mov_b32 v149, 0
v_bfe_u32 v249, v241, 8, 8
v_lshl_or_b32 v249, v249, 1, 1
v_lshlrev_b32 v249, 22, v249
v_and_b32 v241, 0xffff00ff, v241
AR v254,a8
v_sub_f32 v254, v254, v249
v_mul_f32 v254, v254, v250
AW a8,v254
AR v251,a9
v_sub_f32 v251, v251, v249
v_mul_f32 v251, v251, v250
AW a9,v251
AR v254,a10
v_sub_f32 v254, v254, v249
v_mul_f32 v254, v254, v250
AW a10,v254
AR v251,a11
v_sub_f32 v251, v251, v249
v_mul_f32 v251, v251, v250
AW a11,v251
AR v254,a12
v_sub_f32 v254, v254, v249
v_mul_f32 v254, v254, v250
AW a12,v254
AR v251,a13
v_sub_f32 v251, v251, v249
v_mul_f32 v251, v251, v250
AW a13,v251
AR v254,a14
v_sub_f32 v254, v254, v249
v_mul_f32 v254, v254, v250
AW a14,v254
AR v251,a15
v_sub_f32 v251, v251, v249
v_mul_f32 v251, v251, v250
AW a15,v251
AR v254,a40
v_sub_f32 v254, v254, v249
v_mul_f32 v254, v254, v250
AW a40,v254
AR v251,a41
v_sub_f32 v251, v251, v249
v_mul_f32 v251, v251, v250
AW a41,v251
AR v254,a42
v_sub_f32 v254, v254, v249
v_mul_f32 v254, v254, v250
)ASM"
R"ASM(
AW a42,v254
AR v251,a43
v_sub_f32 v251, v251, v249
v_mul_f32 v251, v251, v250
AW a43,v251
AR v254,a44
v_sub_f32 v254, v254, v249
v_mul_f32 v254, v254, v250
AW a44,v254
AR v251,a45
v_sub_f32 v251, v251, v249
v_mul_f32 v251, v251, v250
AW a45,v251
AR v254,a46
v_sub_f32 v254, v254, v249
v_mul_f32 v254, v254, v250
AW a46,v254
AR v251,a47
v_sub_f32 v251, v251, v249
v_mul_f32 v251, v251, v250
AW a47,v251
AR v254,a72
v_sub_f32 v254, v254, v249
v_mul_f32 v254, v254, v250
AW a72,v254
AR v251,a73
v_sub_f32 v251, v251, v249
v_mul_f32 v251, v251, v250
AW a73,v251
AR v254,a74
v_sub_f32 v254, v254, v249
v_mul_f32 v254, v254, v250
AW a74,v254
AR v251,a75
v_sub_f32 v251, v251, v249
v_mul_f32 v251, v251, v250
AW a75,v251
AR v254,a76
v_sub_f32 v254, v254, v249
v_mul_f32 v254, v254, v250
AW a76,v254
AR v251,a77
v_sub_f32 v251, v251, v249
v_mul_f32 v251, v251, v250
AW a77,v251
AR v254,a78
v_sub_f32 v254, v254, v249
v_mul_f32 v254, v254, v250
AW a78,v254
AR v251,a79
v_sub_f32 v251, v251, v249
v_mul_f32 v251, v251, v250
AW a79,v251
AR v254,a104
v_sub_f32 v254, v254, v249
v_mul_f32 v254, v254, v250
AW a104,v254
AR v251,a105
v_sub_f32 v251, v251, v249
v_mul_f32 v251, v251, v250
AW a105,v251
AR v254,a106
v_sub_f32 v254, v254, v249
v_mul_f32 v254, v254, v250
AW a106,v254
AR v251,a107
v_sub_f32 v251, v251, v249
v_mul_f32 v251, v251, v250
AW a107,v251
AR v254,a108
v_sub_f32 v254, v254, v249
v_mul_f32 v254, v254, v250
AW a108,v254
AR v251,a109
v_sub_f32 v251, v251, v249
v_mul_f32 v251, v251, v250
AW a109,v251
AR v254,a110
v_sub_f32 v254, v254, v249
v_mul_f32 v254, v254, v250
AW a110,v254
AR v251,a111
v_sub_f32 v251, v251, v249
v_mul_f32 v251, v251, v250
AW a111,v251
v_max3_f32 v249, v16, v17, v18
v_max3_f32 v251, v48, v49, v50
v_max3_f32 v249, v249, v19, v20
v_max3_f32 v251, v251, v51, v52
v_max3_f32 v249, v249, v21, v22
v_max3_f32 v251, v251, v53, v54
v_max3_f32 v249, v249, v23, v55
v_max_f32 v249, v249, v251
v_mov_b32 v251, v249
s_nop 1
v_permlane16_swap_b32 v249, v251
v_max_f32 v249, v249, v251
v_mov_b32 v251, v249
s_nop 1
v_permlane32_swap_b32 v249, v251
v_max_f32 v249, v249, v251
v_log_f32 v250, v246
s_nop 0
v_max3_f32 v249, 0, v249, v250
v_sub_f32 v250, 0, v249
EX v250,v250
v_sub_f32 v136, v136, v249
v_sub_f32 v137, v137, v249
v_sub_f32 v138, v138, v249
v_sub_f32 v139, v139, v249
v_sub_f32 v80, v80, v249
v_sub_f32 v16, v16, v249
v_sub_f32 v81, v81, v249
v_sub_f32 v17, v17, v249
v_sub_f32 v82, v82, v249
v_sub_f32 v18, v18, v249
v_sub_f32 v83, v83, v249
v_sub_f32 v19, v19, v249
v_sub_f32 v84, v84, v249
v_sub_f32 v20, v20, v249
v_sub_f32 v85, v85, v249
v_sub_f32 v21, v21, v249
v_sub_f32 v86, v86, v249
v_sub_f32 v22, v22, v249
v_sub_f32 v87, v87, v249
v_sub_f32 v23, v23, v249
v_sub_f32 v112, v112, v249
v_sub_f32 v48, v48, v249
v_sub_f32 v113, v113, v249
v_sub_f32 v49, v49, v249
v_sub_f32 v114, v114, v249
v_sub_f32 v50, v50, v249
v_sub_f32 v115, v115, v249
v_sub_f32 v51, v51, v249
v_sub_f32 v116, v116, v249
v_sub_f32 v52, v52, v249
v_sub_f32 v117, v117, v249
v_sub_f32 v53, v53, v249
v_sub_f32 v118, v118, v249
v_sub_f32 v54, v54, v249
v_sub_f32 v119, v119, v249
v_sub_f32 v55, v55, v249
v_mul_f32 v152, v246, v250
v_mov_b32 v153, 0
v_bfe_u32 v249, v241, 16, 8
v_lshl_or_b32 v249, v249, 1, 1
v_lshlrev_b32 v249, 22, v249
v_and_b32 v241, 0xff00ffff, v241
AR v254,a16
v_sub_f32 v254, v254, v249
v_mul_f32 v254, v254, v250
AW a16,v254
AR v251,a17
v_sub_f32 v251, v251, v249
v_mul_f32 v251, v251, v250
AW a17,v251
AR v254,a18
v_sub_f32 v254, v254, v249
v_mul_f32 v254, v254, v250
AW a18,v254
AR v251,a19
v_sub_f32 v251, v251, v249
v_mul_f32 v251, v251, v250
AW a19,v251
AR v254,a20
v_sub_f32 v254, v254, v249
v_mul_f32 v254, v254, v250
AW a20,v254
AR v251,a21
v_sub_f32 v251, v251, v249
v_mul_f32 v251, v251, v250
AW a21,v251
AR v254,a22
v_sub_f32 v254, v254, v249
v_mul_f32 v254, v254, v250
AW a22,v254
AR v251,a23
v_sub_f32 v251, v251, v249
v_mul_f32 v251, v251, v250
AW a23,v251
AR v254,a48
v_sub_f32 v254, v254, v249
v_mul_f32 v254, v254, v250
AW a48,v254
AR v251,a49
v_sub_f32 v251, v251, v249
v_mul_f32 v251, v251, v250
AW a49,v251
AR v254,a50
v_sub_f32 v254, v254, v249
v_mul_f32 v254, v254, v250
AW a50,v254
AR v251,a51
v_sub_f32 v251, v251, v249
v_mul_f32 v251, v251, v250
AW a51,v251
AR v254,a52
v_sub_f32 v254, v254, v249
v_mul_f32 v254, v254, v250
AW a52,v254
AR v251,a53
v_sub_f32 v251, v251, v249
v_mul_f32 v251, v251, v250
AW a53,v251
AR v254,a54
v_sub_f32 v254, v254, v249
v_mul_f32 v254, v254, v250
AW a54,v254
AR v251,a55
v_sub_f32 v251, v251, v249
v_mul_f32 v251, v251, v250
AW a55,v251
AR v254,a80
v_sub_f32 v254, v254, v249
v_mul_f32 v254, v254, v250
AW a80,v254
AR v251,a81
v_sub_f32 v251, v251, v249
v_mul_f32 v251, v251, v250
AW a81,v251
AR v254,a82
v_sub_f32 v254, v254, v249
v_mul_f32 v254, v254, v250
AW a82,v254
AR v251,a83
v_sub_f32 v251, v251, v249
v_mul_f32 v251, v251, v250
AW a83,v251
AR v254,a84
v_sub_f32 v254, v254, v249
v_mul_f32 v254, v254, v250
AW a84,v254
AR v251,a85
v_sub_f32 v251, v251, v249
v_mul_f32 v251, v251, v250
AW a85,v251
AR v254,a86
v_sub_f32 v254, v254, v249
v_mul_f32 v254, v254, v250
AW a86,v254
AR v251,a87
v_sub_f32 v251, v251, v249
v_mul_f32 v251, v251, v250
AW a87,v251
AR v254,a112
v_sub_f32 v254, v254, v249
v_mul_f32 v254, v254, v250
AW a112,v254
AR v251,a113
v_sub_f32 v251, v251, v249
v_mul_f32 v251, v251, v250
AW a113,v251
AR v254,a114
v_sub_f32 v254, v254, v249
v_mul_f32 v254, v254, v250
AW a114,v254
AR v251,a115
v_sub_f32 v251, v251, v249
v_mul_f32 v251, v251, v250
AW a115,v251
AR v254,a116
v_sub_f32 v254, v254, v249
v_mul_f32 v254, v254, v250
AW a116,v254
AR v251,a117
v_sub_f32 v251, v251, v249
v_mul_f32 v251, v251, v250
AW a117,v251
AR v254,a118
v_sub_f32 v254, v254, v249
v_mul_f32 v254, v254, v250
AW a118,v254
AR v251,a119
v_sub_f32 v251, v251, v249
v_mul_f32 v251, v251, v250
AW a119,v251
v_max3_f32 v249, v24, v25, v26
v_max3_f32 v251, v56, v57, v58
v_max3_f32 v249, v249, v27, v28
v_max3_f32 v251, v251, v59, v60
v_max3_f32 v249, v249, v29, v30
v_max3_f32 v251, v251, v61, v62
v_max3_f32 v249, v249, v31, v63
v_max_f32 v249, v249, v251
v_mov_b32 v251, v249
s_nop 1
v_permlane16_swap_b32 v249, v251
v_max_f32 v249, v249, v251
v_mov_b32 v251, v249
s_nop 1
v_permlane32_swap_b32 v249, v251
v_max_f32 v249, v249, v251
v_log_f32 v250, v247
s_nop 0
v_max3_f32 v249, 0, v249, v250
v_sub_f32 v250, 0, v249
EX v250,v250
v_sub_f32 v140, v140, v249
v_sub_f32 v141, v141, v249
v_sub_f32 v142, v142, v249
v_sub_f32 v143, v143, v249
v_sub_f32 v88, v88, v249
v_sub_f32 v24, v24, v249
v_sub_f32 v89, v89, v249
v_sub_f32 v25, v25, v249
v_sub_f32 v90, v90, v249
v_sub_f32 v26, v26, v249
v_sub_f32 v91, v91, v249
v_sub_f32 v27, v27, v249
v_sub_f32 v92, v92, v249
v_sub_f32 v28, v28, v249
v_sub_f32 v93, v93, v249
v_sub_f32 v29, v29, v249
v_sub_f32 v94, v94, v249
v_sub_f32 v30, v30, v249
v_sub_f32 v95, v95, v249
v_sub_f32 v31, v31, v249
v_sub_f32 v120, v120, v249
v_sub_f32 v56, v56, v249
v_sub_f32 v121, v121, v249
v_sub_f32 v57, v57, v249
v_sub_f32 v122, v122, v249
v_sub_f32 v58, v58, v249
v_sub_f32 v123, v123, v249
v_sub_f32 v59, v59, v249
v_sub_f32 v124, v124, v249
v_sub_f32 v60, v60, v249
v_sub_f32 v125, v125, v249
v_sub_f32 v61, v61, v249
v_sub_f32 v126, v126, v249
v_sub_f32 v62, v62, v249
v_sub_f32 v127, v127, v249
v_sub_f32 v63, v63, v249
v_mul_f32 v156, v247, v250
v_mov_b32 v157, 0
v_bfe_u32 v249, v241, 24, 8
v_lshl_or_b32 v249, v249, 1, 1
v_lshlrev_b32 v249, 22, v249
v_and_b32 v241, 0xffffff, v241
AR v254,a24
v_sub_f32 v254, v254, v249
v_mul_f32 v254, v254, v250
AW a24,v254
AR v251,a25
v_sub_f32 v251, v251, v249
v_mul_f32 v251, v251, v250
AW a25,v251
AR v254,a26
v_sub_f32 v254, v254, v249
v_mul_f32 v254, v254, v250
AW a26,v254
AR v251,a27
v_sub_f32 v251, v251, v249
v_mul_f32 v251, v251, v250
AW a27,v251
AR v254,a28
v_sub_f32 v254, v254, v249
v_mul_f32 v254, v254, v250
AW a28,v254
AR v251,a29
v_sub_f32 v251, v251, v249
v_mul_f32 v251, v251, v250
AW a29,v251
AR v254,a30
v_sub_f32 v254, v254, v249
v_mul_f32 v254, v254, v250
AW a30,v254
AR v251,a31
v_sub_f32 v251, v251, v249
v_mul_f32 v251, v251, v250
AW a31,v251
AR v254,a56
v_sub_f32 v254, v254, v249
v_mul_f32 v254, v254, v250
AW a56,v254
AR v251,a57
v_sub_f32 v251, v251, v249
v_mul_f32 v251, v251, v250
AW a57,v251
AR v254,a58
v_sub_f32 v254, v254, v249
v_mul_f32 v254, v254, v250
AW a58,v254
AR v251,a59
v_sub_f32 v251, v251, v249
v_mul_f32 v251, v251, v250
AW a59,v251
AR v254,a60
v_sub_f32 v254, v254, v249
v_mul_f32 v254, v254, v250
AW a60,v254
AR v251,a61
v_sub_f32 v251, v251, v249
v_mul_f32 v251, v251, v250
AW a61,v251
AR v254,a62
v_sub_f32 v254, v254, v249
v_mul_f32 v254, v254, v250
AW a62,v254
AR v251,a63
v_sub_f32 v251, v251, v249
v_mul_f32 v251, v251, v250
AW a63,v251
AR v254,a88
v_sub_f32 v254, v254, v249
v_mul_f32 v254, v254, v250
AW a88,v254
AR v251,a89
v_sub_f32 v251, v251, v249
v_mul_f32 v251, v251, v250
AW a89,v251
AR v254,a90
v_sub_f32 v254, v254, v249
v_mul_f32 v254, v254, v250
AW a90,v254
AR v251,a91
v_sub_f32 v251, v251, v249
v_mul_f32 v251, v251, v250
AW a91,v251
AR v254,a92
v_sub_f32 v254, v254, v249
v_mul_f32 v254, v254, v250
AW a92,v254
AR v251,a93
v_sub_f32 v251, v251, v249
v_mul_f32 v251, v251, v250
AW a93,v251
AR v254,a94
v_sub_f32 v254, v254, v249
v_mul_f32 v254, v254, v250
AW a94,v254
AR v251,a95
v_sub_f32 v251, v251, v249
v_mul_f32 v251, v251, v250
AW a95,v251
AR v254,a120
v_sub_f32 v254, v254, v249
v_mul_f32 v254, v254, v250
AW a120,v254
AR v251,a121
v_sub_f32 v251, v251, v249
v_mul_f32 v251, v251, v250
AW a121,v251
AR v254,a122
v_sub_f32 v254, v254, v249
v_mul_f32 v254, v254, v250
AW a122,v254
AR v251,a123
v_sub_f32 v251, v251, v249
v_mul_f32 v251, v251, v250
AW a123,v251
AR v254,a124
v_sub_f32 v254, v254, v249
v_mul_f32 v254, v254, v250
AW a124,v254
AR v251,a125
v_sub_f32 v251, v251, v249
v_mul_f32 v251, v251, v250
AW a125,v251
AR v254,a126
v_sub_f32 v254, v254, v249
v_mul_f32 v254, v254, v250
AW a126,v254
AR v251,a127
v_sub_f32 v251, v251, v249
v_mul_f32 v251, v251, v250
AW a127,v251
EX v0,v0
EX v1,v1
EX v2,v2
EX v3,v3
EX v4,v4
EX v5,v5
EX v6,v6
EX v7,v7
EX v8,v8
EX v9,v9
EX v10,v10
EX v11,v11
EX v12,v12
EX v13,v13
EX v14,v14
EX v15,v15
EX v16,v16
EX v17,v17
EX v18,v18
EX v19,v19
EX v20,v20
EX v21,v21
EX v22,v22
EX v23,v23
EX v24,v24
EX v25,v25
EX v26,v26
EX v27,v27
EX v28,v28
EX v29,v29
EX v30,v30
EX v31,v31
EX v32,v32
EX v33,v33
EX v34,v34
EX v35,v35
EX v36,v36
EX v37,v37
EX v38,v38
EX v39,v39
EX v40,v40
EX v41,v41
EX v42,v42
EX v43,v43
EX v44,v44
EX v45,v45
EX v46,v46
EX v47,v47
EX v48,v48
EX v49,v49
EX v50,v50
EX v51,v51
EX v52,v52
EX v53,v53
EX v54,v54
EX v55,v55
EX v56,v56
EX v57,v57
EX v58,v58
EX v59,v59
EX v60,v60
EX v61,v61
EX v62,v62
EX v63,v63
CV v0,v0,v1
CV v1,v2,v3
CV v2,v4,v5
CV v3,v6,v7
CV v8,v8,v9
CV v9,v10,v11
CV v10,v12,v13
CV v11,v14,v15
CV v16,v16,v17
CV v17,v18,v19
CV v18,v20,v21
CV v19,v22,v23
CV v24,v24,v25
CV v25,v26,v27
CV v26,v28,v29
CV v27,v30,v31
CV v4,v32,v33
CV v5,v34,v35
CV v6,v36,v37
CV v7,v38,v39
CV v12,v40,v41
CV v13,v42,v43
CV v14,v44,v45
CV v15,v46,v47
CV v20,v48,v49
CV v21,v50,v51
CV v22,v52,v53
CV v23,v54,v55
CV v28,v56,v57
CV v29,v58,v59
CV v30,v60,v61
CV v31,v62,v63
v_smfmac_f32_16x16x64_bf16 v[144:147], v[236:239], v[0:7], v234
v_smfmac_f32_16x16x64_bf16 v[148:151], v[236:239], v[8:15], v234
v_smfmac_f32_16x16x64_bf16 v[152:155], v[236:239], v[16:23], v234
v_smfmac_f32_16x16x64_bf16 v[156:159], v[236:239], v[24:31], v234
s_nop 9
s_cmp_eq_u32 s73, 4
s_cbranch_scc1 LBACK4_%=
s_cmp_eq_u32 s73, 6
s_cbranch_scc1 LBACK6_%=
s_cmp_eq_u32 s73, 8
s_cbranch_scc1 LBACK8_%=
s_cmp_eq_u32 s73, 10
s_cbranch_scc1 LBACK10_%=
s_cmp_eq_u32 s73, 12
s_cbranch_scc1 LBACK12_%=
s_cmp_eq_u32 s73, 14
s_cbranch_scc1 LBACK14_%=
s_cmp_eq_u32 s73, 16
s_cbranch_scc1 LBACK16_%=
s_cmp_eq_u32 s73, 18
s_cbranch_scc1 LBACK18_%=
s_cmp_eq_u32 s73, 20
s_cbranch_scc1 LBACK20_%=
s_cmp_eq_u32 s73, 22
s_cbranch_scc1 LBACK22_%=
s_cmp_eq_u32 s73, 24
s_cbranch_scc1 LBACK24_%=
s_cmp_eq_u32 s73, 26
s_cbranch_scc1 LBACK26_%=
s_cmp_eq_u32 s73, 28
s_cbranch_scc1 LBACK28_%=
s_cmp_eq_u32 s73, 30
s_cbranch_scc1 LBACK30_%=
s_cmp_eq_u32 s73, 32
)ASM"
R"ASM(
s_cbranch_scc1 LBACK32_%=
s_cmp_eq_u32 s73, 34
s_cbranch_scc1 LBACK34_%=
s_endpgm
LSITE5_%=:
s_mov_b32 s73, 5
s_mov_b32 s74, 16384
s_mov_b32 s75, 32768
s_branch LRARE104_%=
LSITE7_%=:
s_mov_b32 s73, 7
s_mov_b32 s74, 49152
s_mov_b32 s75, 0
s_branch LRARE104_%=
LSITE9_%=:
s_mov_b32 s73, 9
s_mov_b32 s74, 16384
s_mov_b32 s75, 32768
s_branch LRARE104_%=
LSITE11_%=:
s_mov_b32 s73, 11
s_mov_b32 s74, 49152
s_mov_b32 s75, 0
s_branch LRARE104_%=
LSITE13_%=:
s_mov_b32 s73, 13
s_mov_b32 s74, 16384
s_mov_b32 s75, 32768
s_branch LRARE104_%=
LSITE15_%=:
s_mov_b32 s73, 15
s_mov_b32 s74, 49152
s_mov_b32 s75, 0
s_branch LRARE104_%=
LSITE17_%=:
s_mov_b32 s73, 17
s_mov_b32 s74, 16384
s_mov_b32 s75, 32768
s_branch LRARE104_%=
LSITE19_%=:
s_mov_b32 s73, 19
s_mov_b32 s74, 49152
s_mov_b32 s75, 0
s_branch LRARE104_%=
LSITE21_%=:
s_mov_b32 s73, 21
s_mov_b32 s74, 16384
s_mov_b32 s75, 32768
s_branch LRARE104_%=
LSITE23_%=:
s_mov_b32 s73, 23
s_mov_b32 s74, 49152
s_mov_b32 s75, 0
s_branch LRARE104_%=
LSITE25_%=:
s_mov_b32 s73, 25
s_mov_b32 s74, 16384
s_mov_b32 s75, 32768
s_branch LRARE104_%=
LSITE27_%=:
s_mov_b32 s73, 27
s_mov_b32 s74, 49152
s_mov_b32 s75, 0
s_branch LRARE104_%=
LSITE29_%=:
s_mov_b32 s73, 29
s_mov_b32 s74, 16384
s_mov_b32 s75, 32768
s_branch LRARE104_%=
LSITE31_%=:
s_mov_b32 s73, 31
s_mov_b32 s74, 49152
s_mov_b32 s75, 0
s_branch LRARE104_%=
LSITE33_%=:
s_mov_b32 s73, 33
s_mov_b32 s74, 16384
s_mov_b32 s75, 32768
s_branch LRARE104_%=
LSITE35_%=:
s_mov_b32 s73, 35
s_mov_b32 s74, 49152
s_mov_b32 s75, 0
s_branch LRARE104_%=
LRARE104_%=:
s_nop 15
s_waitcnt vmcnt(0) lgkmcnt(0)
v_add_u32 v249, s74, v224
RD a[192:195],v249 offset:0
RD a[208:211],v249 offset:4096
RD a[224:227],v249 offset:8192
RD a[240:243],v249 offset:12288
v_add_u32 v250, s74, v225
RD a[196:199],v250 offset:0
RD a[212:215],v250 offset:4096
RD a[228:231],v250 offset:8192
RD a[244:247],v250 offset:12288
v_add_u32 v251, s74, v226
RD a[200:203],v251 offset:0
RD a[216:219],v251 offset:4096
RD a[232:235],v251 offset:8192
RD a[248:251],v251 offset:12288
v_add_u32 v254, s74, v227
RD a[204:207],v254 offset:0
RD a[220:223],v254 offset:4096
RD a[236:239],v254 offset:8192
RD a[252:255],v254 offset:12288
s_waitcnt lgkmcnt(0)
MM v[64:67],a[192:195],v[160:163],v[128:131]
MM v[64:67],a[196:199],v[176:179],v[64:67]
MM v[64:67],a[200:203],v[192:195],v[64:67]
MM v[64:67],a[204:207],v[208:211],v[64:67]
MM v[68:71],a[220:223],v[208:211],v[128:131]
MM v[68:71],a[216:219],v[192:195],v[68:71]
MM v[68:71],a[212:215],v[176:179],v[68:71]
MM v[68:71],a[208:211],v[160:163],v[68:71]
MM v[76:79],a[208:211],v[164:167],v[132:135]
MM v[76:79],a[212:215],v[180:183],v[76:79]
MM v[76:79],a[216:219],v[196:199],v[76:79]
MM v[76:79],a[220:223],v[212:215],v[76:79]
MM v[72:75],a[204:207],v[212:215],v[132:135]
MM v[72:75],a[200:203],v[196:199],v[72:75]
MM v[72:75],a[196:199],v[180:183],v[72:75]
MM v[72:75],a[192:195],v[164:167],v[72:75]
MM v[80:83],a[192:195],v[168:171],v[136:139]
MM v[80:83],a[196:199],v[184:187],v[80:83]
MM v[80:83],a[200:203],v[200:203],v[80:83]
MM v[80:83],a[204:207],v[216:219],v[80:83]
MM v[84:87],a[220:223],v[216:219],v[136:139]
MM v[84:87],a[216:219],v[200:203],v[84:87]
MM v[84:87],a[212:215],v[184:187],v[84:87]
MM v[84:87],a[208:211],v[168:171],v[84:87]
MM v[92:95],a[208:211],v[172:175],v[140:143]
MM v[92:95],a[212:215],v[188:191],v[92:95]
MM v[92:95],a[216:219],v[204:207],v[92:95]
MM v[92:95],a[220:223],v[220:223],v[92:95]
MM v[88:91],a[204:207],v[220:223],v[140:143]
MM v[88:91],a[200:203],v[204:207],v[88:91]
MM v[88:91],a[196:199],v[188:191],v[88:91]
MM v[88:91],a[192:195],v[172:175],v[88:91]
MM v[96:99],a[224:227],v[160:163],v[128:131]
MM v[96:99],a[228:231],v[176:179],v[96:99]
MM v[96:99],a[232:235],v[192:195],v[96:99]
MM v[96:99],a[236:239],v[208:211],v[96:99]
MM v[100:103],a[252:255],v[208:211],v[128:131]
MM v[100:103],a[248:251],v[192:195],v[100:103]
MM v[100:103],a[244:247],v[176:179],v[100:103]
MM v[100:103],a[240:243],v[160:163],v[100:103]
MM v[108:111],a[240:243],v[164:167],v[132:135]
MM v[108:111],a[244:247],v[180:183],v[108:111]
MM v[108:111],a[248:251],v[196:199],v[108:111]
MM v[108:111],a[252:255],v[212:215],v[108:111]
MM v[104:107],a[236:239],v[212:215],v[132:135]
MM v[104:107],a[232:235],v[196:199],v[104:107]
MM v[104:107],a[228:231],v[180:183],v[104:107]
MM v[104:107],a[224:227],v[164:167],v[104:107]
MM v[112:115],a[224:227],v[168:171],v[136:139]
MM v[112:115],a[228:231],v[184:187],v[112:115]
MM v[112:115],a[232:235],v[200:203],v[112:115]
MM v[112:115],a[236:239],v[216:219],v[112:115]
MM v[116:119],a[252:255],v[216:219],v[136:139]
MM v[116:119],a[248:251],v[200:203],v[116:119]
MM v[116:119],a[244:247],v[184:187],v[116:119]
MM v[116:119],a[240:243],v[168:171],v[116:119]
MM v[124:127],a[240:243],v[172:175],v[140:143]
MM v[124:127],a[244:247],v[188:191],v[124:127]
MM v[124:127],a[248:251],v[204:207],v[124:127]
MM v[124:127],a[252:255],v[220:223],v[124:127]
MM v[120:123],a[236:239],v[220:223],v[140:143]
MM v[120:123],a[232:235],v[204:207],v[120:123]
MM v[120:123],a[228:231],v[188:191],v[120:123]
MM v[120:123],a[224:227],v[172:175],v[120:123]
s_nop 15
v_add_u32 v249, s75, v224
RD a[192:195],v249 offset:0
RD a[208:211],v249 offset:4096
RD a[224:227],v249 offset:8192
RD a[240:243],v249 offset:12288
v_add_u32 v250, s75, v225
RD a[196:199],v250 offset:0
RD a[212:215],v250 offset:4096
RD a[228:231],v250 offset:8192
RD a[244:247],v250 offset:12288
v_add_u32 v251, s75, v226
RD a[200:203],v251 offset:0
RD a[216:219],v251 offset:4096
RD a[232:235],v251 offset:8192
RD a[248:251],v251 offset:12288
v_add_u32 v254, s75, v227
RD a[204:207],v254 offset:0
RD a[220:223],v254 offset:4096
RD a[236:239],v254 offset:8192
RD a[252:255],v254 offset:12288
s_waitcnt lgkmcnt(0)
v_max3_f32 v249, v64, v65, v66
v_max3_f32 v251, v96, v97, v98
v_max3_f32 v249, v249, v67, v68
v_max3_f32 v251, v251, v99, v100
v_max3_f32 v249, v249, v69, v70
v_max3_f32 v251, v251, v101, v102
v_max3_f32 v249, v249, v71, v103
v_max_f32 v249, v249, v251
v_mov_b32 v251, v249
s_nop 1
v_permlane16_swap_b32 v249, v251
v_max_f32 v249, v249, v251
v_mov_b32 v251, v249
s_nop 1
v_permlane32_swap_b32 v249, v251
v_max_f32 v249, v249, v251
v_log_f32 v250, v244
s_nop 0
v_max3_f32 v249, 0, v249, v250
v_sub_f32 v250, 0, v249
EX v250,v250
v_sub_f32 v128, v128, v249
v_sub_f32 v129, v129, v249
v_sub_f32 v130, v130, v249
v_sub_f32 v131, v131, v249
v_sub_f32 v0, v0, v249
v_sub_f32 v64, v64, v249
v_sub_f32 v1, v1, v249
v_sub_f32 v65, v65, v249
v_sub_f32 v2, v2, v249
v_sub_f32 v66, v66, v249
v_sub_f32 v3, v3, v249
v_sub_f32 v67, v67, v249
v_sub_f32 v4, v4, v249
v_sub_f32 v68, v68, v249
v_sub_f32 v5, v5, v249
v_sub_f32 v69, v69, v249
v_sub_f32 v6, v6, v249
v_sub_f32 v70, v70, v249
v_sub_f32 v7, v7, v249
v_sub_f32 v71, v71, v249
v_sub_f32 v32, v32, v249
v_sub_f32 v96, v96, v249
v_sub_f32 v33, v33, v249
v_sub_f32 v97, v97, v249
v_sub_f32 v34, v34, v249
v_sub_f32 v98, v98, v249
v_sub_f32 v35, v35, v249
v_sub_f32 v99, v99, v249
v_sub_f32 v36, v36, v249
v_sub_f32 v100, v100, v249
v_sub_f32 v37, v37, v249
v_sub_f32 v101, v101, v249
v_sub_f32 v38, v38, v249
v_sub_f32 v102, v102, v249
v_sub_f32 v39, v39, v249
v_sub_f32 v103, v103, v249
v_mul_f32 v144, v244, v250
v_mov_b32 v145, 0
v_bfe_u32 v249, v241, 0, 8
v_lshl_or_b32 v249, v249, 1, 1
v_lshlrev_b32 v249, 22, v249
v_and_b32 v241, 0xffffff00, v241
AR v254,a0
v_sub_f32 v254, v254, v249
v_mul_f32 v254, v254, v250
AW a0,v254
AR v251,a1
v_sub_f32 v251, v251, v249
v_mul_f32 v251, v251, v250
AW a1,v251
AR v254,a2
v_sub_f32 v254, v254, v249
v_mul_f32 v254, v254, v250
AW a2,v254
AR v251,a3
v_sub_f32 v251, v251, v249
v_mul_f32 v251, v251, v250
AW a3,v251
AR v254,a4
v_sub_f32 v254, v254, v249
v_mul_f32 v254, v254, v250
AW a4,v254
AR v251,a5
v_sub_f32 v251, v251, v249
v_mul_f32 v251, v251, v250
AW a5,v251
AR v254,a6
v_sub_f32 v254, v254, v249
v_mul_f32 v254, v254, v250
AW a6,v254
AR v251,a7
v_sub_f32 v251, v251, v249
v_mul_f32 v251, v251, v250
AW a7,v251
AR v254,a32
v_sub_f32 v254, v254, v249
v_mul_f32 v254, v254, v250
AW a32,v254
AR v251,a33
v_sub_f32 v251, v251, v249
v_mul_f32 v251, v251, v250
AW a33,v251
AR v254,a34
v_sub_f32 v254, v254, v249
v_mul_f32 v254, v254, v250
AW a34,v254
AR v251,a35
v_sub_f32 v251, v251, v249
v_mul_f32 v251, v251, v250
AW a35,v251
AR v254,a36
v_sub_f32 v254, v254, v249
v_mul_f32 v254, v254, v250
AW a36,v254
AR v251,a37
v_sub_f32 v251, v251, v249
v_mul_f32 v251, v251, v250
AW a37,v251
AR v254,a38
v_sub_f32 v254, v254, v249
v_mul_f32 v254, v254, v250
AW a38,v254
AR v251,a39
v_sub_f32 v251, v251, v249
v_mul_f32 v251, v251, v250
AW a39,v251
AR v254,a64
v_sub_f32 v254, v254, v249
v_mul_f32 v254, v254, v250
AW a64,v254
AR v251,a65
v_sub_f32 v251, v251, v249
v_mul_f32 v251, v251, v250
AW a65,v251
AR v254,a66
v_sub_f32 v254, v254, v249
v_mul_f32 v254, v254, v250
AW a66,v254
AR v251,a67
v_sub_f32 v251, v251, v249
v_mul_f32 v251, v251, v250
AW a67,v251
AR v254,a68
v_sub_f32 v254, v254, v249
v_mul_f32 v254, v254, v250
AW a68,v254
AR v251,a69
v_sub_f32 v251, v251, v249
v_mul_f32 v251, v251, v250
AW a69,v251
AR v254,a70
v_sub_f32 v254, v254, v249
v_mul_f32 v254, v254, v250
AW a70,v254
AR v251,a71
v_sub_f32 v251, v251, v249
v_mul_f32 v251, v251, v250
AW a71,v251
AR v254,a96
v_sub_f32 v254, v254, v249
v_mul_f32 v254, v254, v250
AW a96,v254
AR v251,a97
v_sub_f32 v251, v251, v249
v_mul_f32 v251, v251, v250
AW a97,v251
AR v254,a98
v_sub_f32 v254, v254, v249
v_mul_f32 v254, v254, v250
AW a98,v254
AR v251,a99
v_sub_f32 v251, v251, v249
v_mul_f32 v251, v251, v250
AW a99,v251
AR v254,a100
v_sub_f32 v254, v254, v249
v_mul_f32 v254, v254, v250
AW a100,v254
AR v251,a101
v_sub_f32 v251, v251, v249
v_mul_f32 v251, v251, v250
AW a101,v251
AR v254,a102
v_sub_f32 v254, v254, v249
v_mul_f32 v254, v254, v250
AW a102,v254
AR v251,a103
v_sub_f32 v251, v251, v249
v_mul_f32 v251, v251, v250
AW a103,v251
v_max3_f32 v249, v72, v73, v74
v_max3_f32 v251, v104, v105, v106
v_max3_f32 v249, v249, v75, v76
v_max3_f32 v251, v251, v107, v108
v_max3_f32 v249, v249, v77, v78
v_max3_f32 v251, v251, v109, v110
v_max3_f32 v249, v249, v79, v111
v_max_f32 v249, v249, v251
v_mov_b32 v251, v249
s_nop 1
v_permlane16_swap_b32 v249, v251
v_max_f32 v249, v249, v251
v_mov_b32 v251, v249
s_nop 1
v_permlane32_swap_b32 v249, v251
v_max_f32 v249, v249, v251
v_log_f32 v250, v245
s_nop 0
v_max3_f32 v249, 0, v249, v250
v_sub_f32 v250, 0, v249
EX v250,v250
v_sub_f32 v132, v132, v249
v_sub_f32 v133, v133, v249
v_sub_f32 v134, v134, v249
v_sub_f32 v135, v135, v249
v_sub_f32 v8, v8, v249
v_sub_f32 v72, v72, v249
v_sub_f32 v9, v9, v249
v_sub_f32 v73, v73, v249
v_sub_f32 v10, v10, v249
v_sub_f32 v74, v74, v249
v_sub_f32 v11, v11, v249
v_sub_f32 v75, v75, v249
v_sub_f32 v12, v12, v249
v_sub_f32 v76, v76, v249
v_sub_f32 v13, v13, v249
v_sub_f32 v77, v77, v249
v_sub_f32 v14, v14, v249
v_sub_f32 v78, v78, v249
v_sub_f32 v15, v15, v249
v_sub_f32 v79, v79, v249
v_sub_f32 v40, v40, v249
v_sub_f32 v104, v104, v249
v_sub_f32 v41, v41, v249
v_sub_f32 v105, v105, v249
v_sub_f32 v42, v42, v249
v_sub_f32 v106, v106, v249
v_sub_f32 v43, v43, v249
v_sub_f32 v107, v107, v249
v_sub_f32 v44, v44, v249
v_sub_f32 v108, v108, v249
v_sub_f32 v45, v45, v249
v_sub_f32 v109, v109, v249
v_sub_f32 v46, v46, v249
v_sub_f32 v110, v110, v249
v_sub_f32 v47, v47, v249
v_sub_f32 v111, v111, v249
v_mul_f32 v148, v245, v250
v_mov_b32 v149, 0
v_bfe_u32 v249, v241, 8, 8
v_lshl_or_b32 v249, v249, 1, 1
v_lshlrev_b32 v249, 22, v249
v_and_b32 v241, 0xffff00ff, v241
AR v254,a8
v_sub_f32 v254, v254, v249
v_mul_f32 v254, v254, v250
AW a8,v254
AR v251,a9
v_sub_f32 v251, v251, v249
v_mul_f32 v251, v251, v250
AW a9,v251
AR v254,a10
v_sub_f32 v254, v254, v249
v_mul_f32 v254, v254, v250
AW a10,v254
AR v251,a11
v_sub_f32 v251, v251, v249
v_mul_f32 v251, v251, v250
AW a11,v251
AR v254,a12
v_sub_f32 v254, v254, v249
v_mul_f32 v254, v254, v250
AW a12,v254
AR v251,a13
v_sub_f32 v251, v251, v249
v_mul_f32 v251, v251, v250
AW a13,v251
AR v254,a14
v_sub_f32 v254, v254, v249
v_mul_f32 v254, v254, v250
AW a14,v254
AR v251,a15
v_sub_f32 v251, v251, v249
v_mul_f32 v251, v251, v250
)ASM"
R"ASM(
AW a15,v251
AR v254,a40
v_sub_f32 v254, v254, v249
v_mul_f32 v254, v254, v250
AW a40,v254
AR v251,a41
v_sub_f32 v251, v251, v249
v_mul_f32 v251, v251, v250
AW a41,v251
AR v254,a42
v_sub_f32 v254, v254, v249
v_mul_f32 v254, v254, v250
AW a42,v254
AR v251,a43
v_sub_f32 v251, v251, v249
v_mul_f32 v251, v251, v250
AW a43,v251
AR v254,a44
v_sub_f32 v254, v254, v249
v_mul_f32 v254, v254, v250
AW a44,v254
AR v251,a45
v_sub_f32 v251, v251, v249
v_mul_f32 v251, v251, v250
AW a45,v251
AR v254,a46
v_sub_f32 v254, v254, v249
v_mul_f32 v254, v254, v250
AW a46,v254
AR v251,a47
v_sub_f32 v251, v251, v249
v_mul_f32 v251, v251, v250
AW a47,v251
AR v254,a72
v_sub_f32 v254, v254, v249
v_mul_f32 v254, v254, v250
AW a72,v254
AR v251,a73
v_sub_f32 v251, v251, v249
v_mul_f32 v251, v251, v250
AW a73,v251
AR v254,a74
v_sub_f32 v254, v254, v249
v_mul_f32 v254, v254, v250
AW a74,v254
AR v251,a75
v_sub_f32 v251, v251, v249
v_mul_f32 v251, v251, v250
AW a75,v251
AR v254,a76
v_sub_f32 v254, v254, v249
v_mul_f32 v254, v254, v250
AW a76,v254
AR v251,a77
v_sub_f32 v251, v251, v249
v_mul_f32 v251, v251, v250
AW a77,v251
AR v254,a78
v_sub_f32 v254, v254, v249
v_mul_f32 v254, v254, v250
AW a78,v254
AR v251,a79
v_sub_f32 v251, v251, v249
v_mul_f32 v251, v251, v250
AW a79,v251
AR v254,a104
v_sub_f32 v254, v254, v249
v_mul_f32 v254, v254, v250
AW a104,v254
AR v251,a105
v_sub_f32 v251, v251, v249
v_mul_f32 v251, v251, v250
AW a105,v251
AR v254,a106
v_sub_f32 v254, v254, v249
v_mul_f32 v254, v254, v250
AW a106,v254
AR v251,a107
v_sub_f32 v251, v251, v249
v_mul_f32 v251, v251, v250
AW a107,v251
AR v254,a108
v_sub_f32 v254, v254, v249
v_mul_f32 v254, v254, v250
AW a108,v254
AR v251,a109
v_sub_f32 v251, v251, v249
v_mul_f32 v251, v251, v250
AW a109,v251
AR v254,a110
v_sub_f32 v254, v254, v249
v_mul_f32 v254, v254, v250
AW a110,v254
AR v251,a111
v_sub_f32 v251, v251, v249
v_mul_f32 v251, v251, v250
AW a111,v251
v_max3_f32 v249, v80, v81, v82
v_max3_f32 v251, v112, v113, v114
v_max3_f32 v249, v249, v83, v84
v_max3_f32 v251, v251, v115, v116
v_max3_f32 v249, v249, v85, v86
v_max3_f32 v251, v251, v117, v118
v_max3_f32 v249, v249, v87, v119
v_max_f32 v249, v249, v251
v_mov_b32 v251, v249
s_nop 1
v_permlane16_swap_b32 v249, v251
v_max_f32 v249, v249, v251
v_mov_b32 v251, v249
s_nop 1
v_permlane32_swap_b32 v249, v251
v_max_f32 v249, v249, v251
v_log_f32 v250, v246
s_nop 0
v_max3_f32 v249, 0, v249, v250
v_sub_f32 v250, 0, v249
EX v250,v250
v_sub_f32 v136, v136, v249
v_sub_f32 v137, v137, v249
v_sub_f32 v138, v138, v249
v_sub_f32 v139, v139, v249
v_sub_f32 v16, v16, v249
v_sub_f32 v80, v80, v249
v_sub_f32 v17, v17, v249
v_sub_f32 v81, v81, v249
v_sub_f32 v18, v18, v249
v_sub_f32 v82, v82, v249
v_sub_f32 v19, v19, v249
v_sub_f32 v83, v83, v249
v_sub_f32 v20, v20, v249
v_sub_f32 v84, v84, v249
v_sub_f32 v21, v21, v249
v_sub_f32 v85, v85, v249
v_sub_f32 v22, v22, v249
v_sub_f32 v86, v86, v249
v_sub_f32 v23, v23, v249
v_sub_f32 v87, v87, v249
v_sub_f32 v48, v48, v249
v_sub_f32 v112, v112, v249
v_sub_f32 v49, v49, v249
v_sub_f32 v113, v113, v249
v_sub_f32 v50, v50, v249
v_sub_f32 v114, v114, v249
v_sub_f32 v51, v51, v249
v_sub_f32 v115, v115, v249
v_sub_f32 v52, v52, v249
v_sub_f32 v116, v116, v249
v_sub_f32 v53, v53, v249
v_sub_f32 v117, v117, v249
v_sub_f32 v54, v54, v249
v_sub_f32 v118, v118, v249
v_sub_f32 v55, v55, v249
v_sub_f32 v119, v119, v249
v_mul_f32 v152, v246, v250
v_mov_b32 v153, 0
v_bfe_u32 v249, v241, 16, 8
v_lshl_or_b32 v249, v249, 1, 1
v_lshlrev_b32 v249, 22, v249
v_and_b32 v241, 0xff00ffff, v241
AR v254,a16
v_sub_f32 v254, v254, v249
v_mul_f32 v254, v254, v250
AW a16,v254
AR v251,a17
v_sub_f32 v251, v251, v249
v_mul_f32 v251, v251, v250
AW a17,v251
AR v254,a18
v_sub_f32 v254, v254, v249
v_mul_f32 v254, v254, v250
AW a18,v254
AR v251,a19
v_sub_f32 v251, v251, v249
v_mul_f32 v251, v251, v250
AW a19,v251
AR v254,a20
v_sub_f32 v254, v254, v249
v_mul_f32 v254, v254, v250
AW a20,v254
AR v251,a21
v_sub_f32 v251, v251, v249
v_mul_f32 v251, v251, v250
AW a21,v251
AR v254,a22
v_sub_f32 v254, v254, v249
v_mul_f32 v254, v254, v250
AW a22,v254
AR v251,a23
v_sub_f32 v251, v251, v249
v_mul_f32 v251, v251, v250
AW a23,v251
AR v254,a48
v_sub_f32 v254, v254, v249
v_mul_f32 v254, v254, v250
AW a48,v254
AR v251,a49
v_sub_f32 v251, v251, v249
v_mul_f32 v251, v251, v250
AW a49,v251
AR v254,a50
v_sub_f32 v254, v254, v249
v_mul_f32 v254, v254, v250
AW a50,v254
AR v251,a51
v_sub_f32 v251, v251, v249
v_mul_f32 v251, v251, v250
AW a51,v251
AR v254,a52
v_sub_f32 v254, v254, v249
v_mul_f32 v254, v254, v250
AW a52,v254
AR v251,a53
v_sub_f32 v251, v251, v249
v_mul_f32 v251, v251, v250
AW a53,v251
AR v254,a54
v_sub_f32 v254, v254, v249
v_mul_f32 v254, v254, v250
AW a54,v254
AR v251,a55
v_sub_f32 v251, v251, v249
v_mul_f32 v251, v251, v250
AW a55,v251
AR v254,a80
v_sub_f32 v254, v254, v249
v_mul_f32 v254, v254, v250
AW a80,v254
AR v251,a81
v_sub_f32 v251, v251, v249
v_mul_f32 v251, v251, v250
AW a81,v251
AR v254,a82
v_sub_f32 v254, v254, v249
v_mul_f32 v254, v254, v250
AW a82,v254
AR v251,a83
v_sub_f32 v251, v251, v249
v_mul_f32 v251, v251, v250
AW a83,v251
AR v254,a84
v_sub_f32 v254, v254, v249
v_mul_f32 v254, v254, v250
AW a84,v254
AR v251,a85
v_sub_f32 v251, v251, v249
v_mul_f32 v251, v251, v250
AW a85,v251
AR v254,a86
v_sub_f32 v254, v254, v249
v_mul_f32 v254, v254, v250
AW a86,v254
AR v251,a87
v_sub_f32 v251, v251, v249
v_mul_f32 v251, v251, v250
AW a87,v251
AR v254,a112
v_sub_f32 v254, v254, v249
v_mul_f32 v254, v254, v250
AW a112,v254
AR v251,a113
v_sub_f32 v251, v251, v249
v_mul_f32 v251, v251, v250
AW a113,v251
AR v254,a114
v_sub_f32 v254, v254, v249
v_mul_f32 v254, v254, v250
AW a114,v254
AR v251,a115
v_sub_f32 v251, v251, v249
v_mul_f32 v251, v251, v250
AW a115,v251
AR v254,a116
v_sub_f32 v254, v254, v249
v_mul_f32 v254, v254, v250
AW a116,v254
AR v251,a117
v_sub_f32 v251, v251, v249
v_mul_f32 v251, v251, v250
AW a117,v251
AR v254,a118
v_sub_f32 v254, v254, v249
v_mul_f32 v254, v254, v250
AW a118,v254
AR v251,a119
v_sub_f32 v251, v251, v249
v_mul_f32 v251, v251, v250
AW a119,v251
v_max3_f32 v249, v88, v89, v90
v_max3_f32 v251, v120, v121, v122
v_max3_f32 v249, v249, v91, v92
v_max3_f32 v251, v251, v123, v124
v_max3_f32 v249, v249, v93, v94
v_max3_f32 v251, v251, v125, v126
v_max3_f32 v249, v249, v95, v127
v_max_f32 v249, v249, v251
v_mov_b32 v251, v249
s_nop 1
v_permlane16_swap_b32 v249, v251
v_max_f32 v249, v249, v251
v_mov_b32 v251, v249
s_nop 1
v_permlane32_swap_b32 v249, v251
v_max_f32 v249, v249, v251
v_log_f32 v250, v247
s_nop 0
v_max3_f32 v249, 0, v249, v250
v_sub_f32 v250, 0, v249
EX v250,v250
v_sub_f32 v140, v140, v249
v_sub_f32 v141, v141, v249
v_sub_f32 v142, v142, v249
v_sub_f32 v143, v143, v249
v_sub_f32 v24, v24, v249
v_sub_f32 v88, v88, v249
v_sub_f32 v25, v25, v249
v_sub_f32 v89, v89, v249
v_sub_f32 v26, v26, v249
v_sub_f32 v90, v90, v249
v_sub_f32 v27, v27, v249
v_sub_f32 v91, v91, v249
v_sub_f32 v28, v28, v249
v_sub_f32 v92, v92, v249
v_sub_f32 v29, v29, v249
v_sub_f32 v93, v93, v249
v_sub_f32 v30, v30, v249
v_sub_f32 v94, v94, v249
v_sub_f32 v31, v31, v249
v_sub_f32 v95, v95, v249
v_sub_f32 v56, v56, v249
v_sub_f32 v120, v120, v249
v_sub_f32 v57, v57, v249
v_sub_f32 v121, v121, v249
v_sub_f32 v58, v58, v249
v_sub_f32 v122, v122, v249
v_sub_f32 v59, v59, v249
v_sub_f32 v123, v123, v249
v_sub_f32 v60, v60, v249
v_sub_f32 v124, v124, v249
v_sub_f32 v61, v61, v249
v_sub_f32 v125, v125, v249
v_sub_f32 v62, v62, v249
v_sub_f32 v126, v126, v249
v_sub_f32 v63, v63, v249
v_sub_f32 v127, v127, v249
v_mul_f32 v156, v247, v250
v_mov_b32 v157, 0
v_bfe_u32 v249, v241, 24, 8
v_lshl_or_b32 v249, v249, 1, 1
v_lshlrev_b32 v249, 22, v249
v_and_b32 v241, 0xffffff, v241
AR v254,a24
v_sub_f32 v254, v254, v249
v_mul_f32 v254, v254, v250
AW a24,v254
AR v251,a25
v_sub_f32 v251, v251, v249
v_mul_f32 v251, v251, v250
AW a25,v251
AR v254,a26
v_sub_f32 v254, v254, v249
v_mul_f32 v254, v254, v250
AW a26,v254
AR v251,a27
v_sub_f32 v251, v251, v249
v_mul_f32 v251, v251, v250
AW a27,v251
AR v254,a28
v_sub_f32 v254, v254, v249
v_mul_f32 v254, v254, v250
AW a28,v254
AR v251,a29
v_sub_f32 v251, v251, v249
v_mul_f32 v251, v251, v250
AW a29,v251
AR v254,a30
v_sub_f32 v254, v254, v249
v_mul_f32 v254, v254, v250
AW a30,v254
AR v251,a31
v_sub_f32 v251, v251, v249
v_mul_f32 v251, v251, v250
AW a31,v251
AR v254,a56
v_sub_f32 v254, v254, v249
v_mul_f32 v254, v254, v250
AW a56,v254
AR v251,a57
v_sub_f32 v251, v251, v249
v_mul_f32 v251, v251, v250
AW a57,v251
AR v254,a58
v_sub_f32 v254, v254, v249
v_mul_f32 v254, v254, v250
AW a58,v254
AR v251,a59
v_sub_f32 v251, v251, v249
v_mul_f32 v251, v251, v250
AW a59,v251
AR v254,a60
v_sub_f32 v254, v254, v249
v_mul_f32 v254, v254, v250
AW a60,v254
AR v251,a61
v_sub_f32 v251, v251, v249
v_mul_f32 v251, v251, v250
AW a61,v251
AR v254,a62
v_sub_f32 v254, v254, v249
v_mul_f32 v254, v254, v250
AW a62,v254
AR v251,a63
v_sub_f32 v251, v251, v249
v_mul_f32 v251, v251, v250
AW a63,v251
AR v254,a88
v_sub_f32 v254, v254, v249
v_mul_f32 v254, v254, v250
AW a88,v254
AR v251,a89
v_sub_f32 v251, v251, v249
v_mul_f32 v251, v251, v250
AW a89,v251
AR v254,a90
v_sub_f32 v254, v254, v249
v_mul_f32 v254, v254, v250
AW a90,v254
AR v251,a91
v_sub_f32 v251, v251, v249
v_mul_f32 v251, v251, v250
AW a91,v251
AR v254,a92
v_sub_f32 v254, v254, v249
v_mul_f32 v254, v254, v250
AW a92,v254
AR v251,a93
v_sub_f32 v251, v251, v249
v_mul_f32 v251, v251, v250
AW a93,v251
AR v254,a94
v_sub_f32 v254, v254, v249
v_mul_f32 v254, v254, v250
AW a94,v254
AR v251,a95
v_sub_f32 v251, v251, v249
v_mul_f32 v251, v251, v250
AW a95,v251
AR v254,a120
v_sub_f32 v254, v254, v249
v_mul_f32 v254, v254, v250
AW a120,v254
AR v251,a121
v_sub_f32 v251, v251, v249
v_mul_f32 v251, v251, v250
AW a121,v251
AR v254,a122
v_sub_f32 v254, v254, v249
v_mul_f32 v254, v254, v250
AW a122,v254
AR v251,a123
v_sub_f32 v251, v251, v249
v_mul_f32 v251, v251, v250
AW a123,v251
AR v254,a124
v_sub_f32 v254, v254, v249
v_mul_f32 v254, v254, v250
AW a124,v254
AR v251,a125
v_sub_f32 v251, v251, v249
v_mul_f32 v251, v251, v250
AW a125,v251
AR v254,a126
v_sub_f32 v254, v254, v249
v_mul_f32 v254, v254, v250
AW a126,v254
AR v251,a127
v_sub_f32 v251, v251, v249
v_mul_f32 v251, v251, v250
AW a127,v251
EX v64,v64
EX v65,v65
EX v66,v66
EX v67,v67
EX v68,v68
EX v69,v69
EX v70,v70
EX v71,v71
EX v72,v72
EX v73,v73
EX v74,v74
EX v75,v75
EX v76,v76
EX v77,v77
EX v78,v78
EX v79,v79
EX v80,v80
EX v81,v81
EX v82,v82
EX v83,v83
EX v84,v84
EX v85,v85
EX v86,v86
EX v87,v87
EX v88,v88
EX v89,v89
EX v90,v90
EX v91,v91
EX v92,v92
EX v93,v93
EX v94,v94
EX v95,v95
EX v96,v96
EX v97,v97
EX v98,v98
EX v99,v99
EX v100,v100
EX v101,v101
EX v102,v102
EX v103,v103
EX v104,v104
EX v105,v105
EX v106,v106
EX v107,v107
EX v108,v108
EX v109,v109
EX v110,v110
EX v111,v111
EX v112,v112
EX v113,v113
EX v114,v114
EX v115,v115
EX v116,v116
EX v117,v117
EX v118,v118
EX v119,v119
EX v120,v120
EX v121,v121
EX v122,v122
EX v123,v123
EX v124,v124
EX v125,v125
EX v126,v126
EX v127,v127
CV v64,v64,v65
CV v65,v66,v67
CV v66,v68,v69
CV v67,v70,v71
CV v72,v72,v73
CV v73,v74,v75
CV v74,v76,v77
CV v75,v78,v79
CV v80,v80,v81
CV v81,v82,v83
CV v82,v84,v85
CV v83,v86,v87
CV v88,v88,v89
CV v89,v90,v91
CV v90,v92,v93
CV v91,v94,v95
CV v68,v96,v97
CV v69,v98,v99
CV v70,v100,v101
CV v71,v102,v103
CV v76,v104,v105
CV v77,v106,v107
CV v78,v108,v109
CV v79,v110,v111
CV v84,v112,v113
CV v85,v114,v115
CV v86,v116,v117
CV v87,v118,v119
CV v92,v120,v121
CV v93,v122,v123
CV v94,v124,v125
CV v95,v126,v127
v_smfmac_f32_16x16x64_bf16 v[144:147], v[236:239], v[64:71], v234
v_smfmac_f32_16x16x64_bf16 v[148:151], v[236:239], v[72:79], v234
v_smfmac_f32_16x16x64_bf16 v[152:155], v[236:239], v[80:87], v234
v_smfmac_f32_16x16x64_bf16 v[156:159], v[236:239], v[88:95], v234
s_nop 9
s_cmp_eq_u32 s73, 5
s_cbranch_scc1 LBACK5_%=
s_cmp_eq_u32 s73, 7
s_cbranch_scc1 LBACK7_%=
s_cmp_eq_u32 s73, 9
s_cbranch_scc1 LBACK9_%=
s_cmp_eq_u32 s73, 11
s_cbranch_scc1 LBACK11_%=
s_cmp_eq_u32 s73, 13
s_cbranch_scc1 LBACK13_%=
s_cmp_eq_u32 s73, 15
s_cbranch_scc1 LBACK15_%=
s_cmp_eq_u32 s73, 17
)ASM"
R"ASM(
s_cbranch_scc1 LBACK17_%=
s_cmp_eq_u32 s73, 19
s_cbranch_scc1 LBACK19_%=
s_cmp_eq_u32 s73, 21
s_cbranch_scc1 LBACK21_%=
s_cmp_eq_u32 s73, 23
s_cbranch_scc1 LBACK23_%=
s_cmp_eq_u32 s73, 25
s_cbranch_scc1 LBACK25_%=
s_cmp_eq_u32 s73, 27
s_cbranch_scc1 LBACK27_%=
s_cmp_eq_u32 s73, 29
s_cbranch_scc1 LBACK29_%=
s_cmp_eq_u32 s73, 31
s_cbranch_scc1 LBACK31_%=
s_cmp_eq_u32 s73, 33
s_cbranch_scc1 LBACK33_%=
s_cmp_eq_u32 s73, 35
s_cbranch_scc1 LBACK35_%=
s_endpgm
LSITE36_%=:
s_mov_b32 s73, 36
s_mov_b32 s74, 0
s_mov_b32 s75, 16384
s_branch LRARE022_%=
LSITE38_%=:
s_mov_b32 s73, 38
s_mov_b32 s74, 32768
s_mov_b32 s75, 49152
s_branch LRARE022_%=
LRARE022_%=:
s_nop 15
s_waitcnt vmcnt(0) lgkmcnt(0)
v_add_u32 v249, s74, v224
RD a[192:195],v249 offset:0
RD a[208:211],v249 offset:4096
RD a[224:227],v249 offset:8192
RD a[240:243],v249 offset:12288
v_add_u32 v250, s74, v225
RD a[196:199],v250 offset:0
RD a[212:215],v250 offset:4096
RD a[228:231],v250 offset:8192
RD a[244:247],v250 offset:12288
v_add_u32 v251, s74, v226
RD a[200:203],v251 offset:0
RD a[216:219],v251 offset:4096
RD a[232:235],v251 offset:8192
RD a[248:251],v251 offset:12288
v_add_u32 v254, s74, v227
RD a[204:207],v254 offset:0
RD a[220:223],v254 offset:4096
RD a[236:239],v254 offset:8192
RD a[252:255],v254 offset:12288
s_waitcnt lgkmcnt(0)
MM v[16:19],a[192:195],v[168:171],v[136:139]
MM v[16:19],a[196:199],v[184:187],v[16:19]
MM v[16:19],a[200:203],v[200:203],v[16:19]
MM v[16:19],a[204:207],v[216:219],v[16:19]
MM v[20:23],a[220:223],v[216:219],v[136:139]
MM v[20:23],a[216:219],v[200:203],v[20:23]
MM v[20:23],a[212:215],v[184:187],v[20:23]
MM v[20:23],a[208:211],v[168:171],v[20:23]
MM v[28:31],a[208:211],v[172:175],v[140:143]
MM v[28:31],a[212:215],v[188:191],v[28:31]
MM v[28:31],a[216:219],v[204:207],v[28:31]
MM v[28:31],a[220:223],v[220:223],v[28:31]
MM v[24:27],a[204:207],v[220:223],v[140:143]
MM v[24:27],a[200:203],v[204:207],v[24:27]
MM v[24:27],a[196:199],v[188:191],v[24:27]
MM v[24:27],a[192:195],v[172:175],v[24:27]
MM v[48:51],a[224:227],v[168:171],v[136:139]
MM v[48:51],a[228:231],v[184:187],v[48:51]
MM v[48:51],a[232:235],v[200:203],v[48:51]
MM v[48:51],a[236:239],v[216:219],v[48:51]
MM v[52:55],a[252:255],v[216:219],v[136:139]
MM v[52:55],a[248:251],v[200:203],v[52:55]
MM v[52:55],a[244:247],v[184:187],v[52:55]
MM v[52:55],a[240:243],v[168:171],v[52:55]
MM v[60:63],a[240:243],v[172:175],v[140:143]
MM v[60:63],a[244:247],v[188:191],v[60:63]
MM v[60:63],a[248:251],v[204:207],v[60:63]
MM v[60:63],a[252:255],v[220:223],v[60:63]
MM v[56:59],a[236:239],v[220:223],v[140:143]
MM v[56:59],a[232:235],v[204:207],v[56:59]
MM v[56:59],a[228:231],v[188:191],v[56:59]
MM v[56:59],a[224:227],v[172:175],v[56:59]
s_nop 15
v_add_u32 v249, s75, v224
RD a[192:195],v249 offset:0
RD a[208:211],v249 offset:4096
RD a[224:227],v249 offset:8192
RD a[240:243],v249 offset:12288
v_add_u32 v250, s75, v225
RD a[196:199],v250 offset:0
RD a[212:215],v250 offset:4096
RD a[228:231],v250 offset:8192
RD a[244:247],v250 offset:12288
v_add_u32 v251, s75, v226
RD a[200:203],v251 offset:0
RD a[216:219],v251 offset:4096
RD a[232:235],v251 offset:8192
RD a[248:251],v251 offset:12288
v_add_u32 v254, s75, v227
RD a[204:207],v254 offset:0
RD a[220:223],v254 offset:4096
RD a[236:239],v254 offset:8192
RD a[252:255],v254 offset:12288
s_waitcnt lgkmcnt(0)
v_max3_f32 v249, v16, v17, v18
v_max3_f32 v251, v48, v49, v50
v_max3_f32 v249, v249, v19, v20
v_max3_f32 v251, v251, v51, v52
v_max3_f32 v249, v249, v21, v22
v_max3_f32 v251, v251, v53, v54
v_max3_f32 v249, v249, v23, v55
v_max_f32 v249, v249, v251
v_mov_b32 v251, v249
s_nop 1
v_permlane16_swap_b32 v249, v251
v_max_f32 v249, v249, v251
v_mov_b32 v251, v249
s_nop 1
v_permlane32_swap_b32 v249, v251
v_max_f32 v249, v249, v251
v_log_f32 v250, v246
s_nop 0
v_max3_f32 v249, 0, v249, v250
v_sub_f32 v250, 0, v249
EX v250,v250
v_sub_f32 v136, v136, v249
v_sub_f32 v137, v137, v249
v_sub_f32 v138, v138, v249
v_sub_f32 v139, v139, v249
v_sub_f32 v80, v80, v249
v_sub_f32 v16, v16, v249
v_sub_f32 v81, v81, v249
v_sub_f32 v17, v17, v249
v_sub_f32 v82, v82, v249
v_sub_f32 v18, v18, v249
v_sub_f32 v83, v83, v249
v_sub_f32 v19, v19, v249
v_sub_f32 v84, v84, v249
v_sub_f32 v20, v20, v249
v_sub_f32 v85, v85, v249
v_sub_f32 v21, v21, v249
v_sub_f32 v86, v86, v249
v_sub_f32 v22, v22, v249
v_sub_f32 v87, v87, v249
v_sub_f32 v23, v23, v249
v_sub_f32 v112, v112, v249
v_sub_f32 v48, v48, v249
v_sub_f32 v113, v113, v249
v_sub_f32 v49, v49, v249
v_sub_f32 v114, v114, v249
v_sub_f32 v50, v50, v249
v_sub_f32 v115, v115, v249
v_sub_f32 v51, v51, v249
v_sub_f32 v116, v116, v249
v_sub_f32 v52, v52, v249
v_sub_f32 v117, v117, v249
v_sub_f32 v53, v53, v249
v_sub_f32 v118, v118, v249
v_sub_f32 v54, v54, v249
v_sub_f32 v119, v119, v249
v_sub_f32 v55, v55, v249
v_mul_f32 v152, v246, v250
v_mov_b32 v153, 0
v_bfe_u32 v249, v241, 16, 8
v_lshl_or_b32 v249, v249, 1, 1
v_lshlrev_b32 v249, 22, v249
v_and_b32 v241, 0xff00ffff, v241
AR v254,a16
v_sub_f32 v254, v254, v249
v_mul_f32 v254, v254, v250
AW a16,v254
AR v251,a17
v_sub_f32 v251, v251, v249
v_mul_f32 v251, v251, v250
AW a17,v251
AR v254,a18
v_sub_f32 v254, v254, v249
v_mul_f32 v254, v254, v250
AW a18,v254
AR v251,a19
v_sub_f32 v251, v251, v249
v_mul_f32 v251, v251, v250
AW a19,v251
AR v254,a20
v_sub_f32 v254, v254, v249
v_mul_f32 v254, v254, v250
AW a20,v254
AR v251,a21
v_sub_f32 v251, v251, v249
v_mul_f32 v251, v251, v250
AW a21,v251
AR v254,a22
v_sub_f32 v254, v254, v249
v_mul_f32 v254, v254, v250
AW a22,v254
AR v251,a23
v_sub_f32 v251, v251, v249
v_mul_f32 v251, v251, v250
AW a23,v251
AR v254,a48
v_sub_f32 v254, v254, v249
v_mul_f32 v254, v254, v250
AW a48,v254
AR v251,a49
v_sub_f32 v251, v251, v249
v_mul_f32 v251, v251, v250
AW a49,v251
AR v254,a50
v_sub_f32 v254, v254, v249
v_mul_f32 v254, v254, v250
AW a50,v254
AR v251,a51
v_sub_f32 v251, v251, v249
v_mul_f32 v251, v251, v250
AW a51,v251
AR v254,a52
v_sub_f32 v254, v254, v249
v_mul_f32 v254, v254, v250
AW a52,v254
AR v251,a53
v_sub_f32 v251, v251, v249
v_mul_f32 v251, v251, v250
AW a53,v251
AR v254,a54
v_sub_f32 v254, v254, v249
v_mul_f32 v254, v254, v250
AW a54,v254
AR v251,a55
v_sub_f32 v251, v251, v249
v_mul_f32 v251, v251, v250
AW a55,v251
AR v254,a80
v_sub_f32 v254, v254, v249
v_mul_f32 v254, v254, v250
AW a80,v254
AR v251,a81
v_sub_f32 v251, v251, v249
v_mul_f32 v251, v251, v250
AW a81,v251
AR v254,a82
v_sub_f32 v254, v254, v249
v_mul_f32 v254, v254, v250
AW a82,v254
AR v251,a83
v_sub_f32 v251, v251, v249
v_mul_f32 v251, v251, v250
AW a83,v251
AR v254,a84
v_sub_f32 v254, v254, v249
v_mul_f32 v254, v254, v250
AW a84,v254
AR v251,a85
v_sub_f32 v251, v251, v249
v_mul_f32 v251, v251, v250
AW a85,v251
AR v254,a86
v_sub_f32 v254, v254, v249
v_mul_f32 v254, v254, v250
AW a86,v254
AR v251,a87
v_sub_f32 v251, v251, v249
v_mul_f32 v251, v251, v250
AW a87,v251
AR v254,a112
v_sub_f32 v254, v254, v249
v_mul_f32 v254, v254, v250
AW a112,v254
AR v251,a113
v_sub_f32 v251, v251, v249
v_mul_f32 v251, v251, v250
AW a113,v251
AR v254,a114
v_sub_f32 v254, v254, v249
v_mul_f32 v254, v254, v250
AW a114,v254
AR v251,a115
v_sub_f32 v251, v251, v249
v_mul_f32 v251, v251, v250
AW a115,v251
AR v254,a116
v_sub_f32 v254, v254, v249
v_mul_f32 v254, v254, v250
AW a116,v254
AR v251,a117
v_sub_f32 v251, v251, v249
v_mul_f32 v251, v251, v250
AW a117,v251
AR v254,a118
v_sub_f32 v254, v254, v249
v_mul_f32 v254, v254, v250
AW a118,v254
AR v251,a119
v_sub_f32 v251, v251, v249
v_mul_f32 v251, v251, v250
AW a119,v251
v_max3_f32 v249, v24, v25, v26
v_max3_f32 v251, v56, v57, v58
v_max3_f32 v249, v249, v27, v28
v_max3_f32 v251, v251, v59, v60
v_max3_f32 v249, v249, v29, v30
v_max3_f32 v251, v251, v61, v62
v_max3_f32 v249, v249, v31, v63
v_max_f32 v249, v249, v251
v_mov_b32 v251, v249
s_nop 1
v_permlane16_swap_b32 v249, v251
v_max_f32 v249, v249, v251
v_mov_b32 v251, v249
s_nop 1
v_permlane32_swap_b32 v249, v251
v_max_f32 v249, v249, v251
v_log_f32 v250, v247
s_nop 0
v_max3_f32 v249, 0, v249, v250
v_sub_f32 v250, 0, v249
EX v250,v250
v_sub_f32 v140, v140, v249
v_sub_f32 v141, v141, v249
v_sub_f32 v142, v142, v249
v_sub_f32 v143, v143, v249
v_sub_f32 v88, v88, v249
v_sub_f32 v24, v24, v249
v_sub_f32 v89, v89, v249
v_sub_f32 v25, v25, v249
v_sub_f32 v90, v90, v249
v_sub_f32 v26, v26, v249
v_sub_f32 v91, v91, v249
v_sub_f32 v27, v27, v249
v_sub_f32 v92, v92, v249
v_sub_f32 v28, v28, v249
v_sub_f32 v93, v93, v249
v_sub_f32 v29, v29, v249
v_sub_f32 v94, v94, v249
v_sub_f32 v30, v30, v249
v_sub_f32 v95, v95, v249
v_sub_f32 v31, v31, v249
v_sub_f32 v120, v120, v249
v_sub_f32 v56, v56, v249
v_sub_f32 v121, v121, v249
v_sub_f32 v57, v57, v249
v_sub_f32 v122, v122, v249
v_sub_f32 v58, v58, v249
v_sub_f32 v123, v123, v249
v_sub_f32 v59, v59, v249
v_sub_f32 v124, v124, v249
v_sub_f32 v60, v60, v249
v_sub_f32 v125, v125, v249
v_sub_f32 v61, v61, v249
v_sub_f32 v126, v126, v249
v_sub_f32 v62, v62, v249
v_sub_f32 v127, v127, v249
v_sub_f32 v63, v63, v249
v_mul_f32 v156, v247, v250
v_mov_b32 v157, 0
v_bfe_u32 v249, v241, 24, 8
v_lshl_or_b32 v249, v249, 1, 1
v_lshlrev_b32 v249, 22, v249
v_and_b32 v241, 0xffffff, v241
AR v254,a24
v_sub_f32 v254, v254, v249
v_mul_f32 v254, v254, v250
AW a24,v254
AR v251,a25
v_sub_f32 v251, v251, v249
v_mul_f32 v251, v251, v250
AW a25,v251
AR v254,a26
v_sub_f32 v254, v254, v249
v_mul_f32 v254, v254, v250
AW a26,v254
AR v251,a27
v_sub_f32 v251, v251, v249
v_mul_f32 v251, v251, v250
AW a27,v251
AR v254,a28
v_sub_f32 v254, v254, v249
v_mul_f32 v254, v254, v250
AW a28,v254
AR v251,a29
v_sub_f32 v251, v251, v249
v_mul_f32 v251, v251, v250
AW a29,v251
AR v254,a30
v_sub_f32 v254, v254, v249
v_mul_f32 v254, v254, v250
AW a30,v254
AR v251,a31
v_sub_f32 v251, v251, v249
v_mul_f32 v251, v251, v250
AW a31,v251
AR v254,a56
v_sub_f32 v254, v254, v249
v_mul_f32 v254, v254, v250
AW a56,v254
AR v251,a57
v_sub_f32 v251, v251, v249
v_mul_f32 v251, v251, v250
AW a57,v251
AR v254,a58
v_sub_f32 v254, v254, v249
v_mul_f32 v254, v254, v250
AW a58,v254
AR v251,a59
v_sub_f32 v251, v251, v249
v_mul_f32 v251, v251, v250
AW a59,v251
AR v254,a60
v_sub_f32 v254, v254, v249
v_mul_f32 v254, v254, v250
AW a60,v254
AR v251,a61
v_sub_f32 v251, v251, v249
v_mul_f32 v251, v251, v250
AW a61,v251
AR v254,a62
v_sub_f32 v254, v254, v249
v_mul_f32 v254, v254, v250
AW a62,v254
AR v251,a63
v_sub_f32 v251, v251, v249
v_mul_f32 v251, v251, v250
AW a63,v251
AR v254,a88
v_sub_f32 v254, v254, v249
v_mul_f32 v254, v254, v250
AW a88,v254
AR v251,a89
v_sub_f32 v251, v251, v249
v_mul_f32 v251, v251, v250
AW a89,v251
AR v254,a90
v_sub_f32 v254, v254, v249
v_mul_f32 v254, v254, v250
AW a90,v254
AR v251,a91
v_sub_f32 v251, v251, v249
v_mul_f32 v251, v251, v250
AW a91,v251
AR v254,a92
v_sub_f32 v254, v254, v249
v_mul_f32 v254, v254, v250
AW a92,v254
AR v251,a93
v_sub_f32 v251, v251, v249
v_mul_f32 v251, v251, v250
AW a93,v251
AR v254,a94
v_sub_f32 v254, v254, v249
v_mul_f32 v254, v254, v250
AW a94,v254
AR v251,a95
v_sub_f32 v251, v251, v249
v_mul_f32 v251, v251, v250
AW a95,v251
AR v254,a120
v_sub_f32 v254, v254, v249
v_mul_f32 v254, v254, v250
AW a120,v254
AR v251,a121
v_sub_f32 v251, v251, v249
v_mul_f32 v251, v251, v250
AW a121,v251
AR v254,a122
v_sub_f32 v254, v254, v249
v_mul_f32 v254, v254, v250
AW a122,v254
AR v251,a123
v_sub_f32 v251, v251, v249
v_mul_f32 v251, v251, v250
AW a123,v251
AR v254,a124
v_sub_f32 v254, v254, v249
v_mul_f32 v254, v254, v250
AW a124,v254
AR v251,a125
v_sub_f32 v251, v251, v249
v_mul_f32 v251, v251, v250
AW a125,v251
AR v254,a126
v_sub_f32 v254, v254, v249
v_mul_f32 v254, v254, v250
AW a126,v254
AR v251,a127
v_sub_f32 v251, v251, v249
v_mul_f32 v251, v251, v250
AW a127,v251
EX v16,v16
EX v17,v17
EX v18,v18
EX v19,v19
EX v20,v20
EX v21,v21
EX v22,v22
EX v23,v23
EX v24,v24
EX v25,v25
EX v26,v26
EX v27,v27
EX v28,v28
EX v29,v29
EX v30,v30
EX v31,v31
EX v48,v48
EX v49,v49
EX v50,v50
EX v51,v51
EX v52,v52
EX v53,v53
EX v54,v54
EX v55,v55
EX v56,v56
EX v57,v57
EX v58,v58
EX v59,v59
EX v60,v60
EX v61,v61
EX v62,v62
EX v63,v63
CV v16,v16,v17
CV v17,v18,v19
CV v18,v20,v21
CV v19,v22,v23
CV v24,v24,v25
CV v25,v26,v27
)ASM"
R"ASM(
CV v26,v28,v29
CV v27,v30,v31
CV v20,v48,v49
CV v21,v50,v51
CV v22,v52,v53
CV v23,v54,v55
CV v28,v56,v57
CV v29,v58,v59
CV v30,v60,v61
CV v31,v62,v63
v_smfmac_f32_16x16x64_bf16 v[152:155], v[236:239], v[16:23], v234
s_nop 0
v_smfmac_f32_16x16x64_bf16 v[156:159], v[236:239], v[24:31], v234
s_nop 9
s_cmp_eq_u32 s73, 36
s_cbranch_scc1 LBACK36_%=
s_cmp_eq_u32 s73, 38
s_cbranch_scc1 LBACK38_%=
s_endpgm
LSITE37_%=:
s_mov_b32 s73, 37
s_mov_b32 s74, 16384
s_mov_b32 s75, 32768
s_branch LRARE122_%=
LSITE39_%=:
s_mov_b32 s73, 39
s_mov_b32 s74, 49152
s_mov_b32 s75, 0
s_branch LRARE122_%=
LRARE122_%=:
s_nop 15
s_waitcnt vmcnt(0) lgkmcnt(0)
v_add_u32 v249, s74, v224
RD a[192:195],v249 offset:0
RD a[208:211],v249 offset:4096
RD a[224:227],v249 offset:8192
RD a[240:243],v249 offset:12288
v_add_u32 v250, s74, v225
RD a[196:199],v250 offset:0
RD a[212:215],v250 offset:4096
RD a[228:231],v250 offset:8192
RD a[244:247],v250 offset:12288
v_add_u32 v251, s74, v226
RD a[200:203],v251 offset:0
RD a[216:219],v251 offset:4096
RD a[232:235],v251 offset:8192
RD a[248:251],v251 offset:12288
v_add_u32 v254, s74, v227
RD a[204:207],v254 offset:0
RD a[220:223],v254 offset:4096
RD a[236:239],v254 offset:8192
RD a[252:255],v254 offset:12288
s_waitcnt lgkmcnt(0)
MM v[80:83],a[192:195],v[168:171],v[136:139]
MM v[80:83],a[196:199],v[184:187],v[80:83]
MM v[80:83],a[200:203],v[200:203],v[80:83]
MM v[80:83],a[204:207],v[216:219],v[80:83]
MM v[84:87],a[220:223],v[216:219],v[136:139]
MM v[84:87],a[216:219],v[200:203],v[84:87]
MM v[84:87],a[212:215],v[184:187],v[84:87]
MM v[84:87],a[208:211],v[168:171],v[84:87]
MM v[92:95],a[208:211],v[172:175],v[140:143]
MM v[92:95],a[212:215],v[188:191],v[92:95]
MM v[92:95],a[216:219],v[204:207],v[92:95]
MM v[92:95],a[220:223],v[220:223],v[92:95]
MM v[88:91],a[204:207],v[220:223],v[140:143]
MM v[88:91],a[200:203],v[204:207],v[88:91]
MM v[88:91],a[196:199],v[188:191],v[88:91]
MM v[88:91],a[192:195],v[172:175],v[88:91]
MM v[112:115],a[224:227],v[168:171],v[136:139]
MM v[112:115],a[228:231],v[184:187],v[112:115]
MM v[112:115],a[232:235],v[200:203],v[112:115]
MM v[112:115],a[236:239],v[216:219],v[112:115]
MM v[116:119],a[252:255],v[216:219],v[136:139]
MM v[116:119],a[248:251],v[200:203],v[116:119]
MM v[116:119],a[244:247],v[184:187],v[116:119]
MM v[116:119],a[240:243],v[168:171],v[116:119]
MM v[124:127],a[240:243],v[172:175],v[140:143]
MM v[124:127],a[244:247],v[188:191],v[124:127]
MM v[124:127],a[248:251],v[204:207],v[124:127]
MM v[124:127],a[252:255],v[220:223],v[124:127]
MM v[120:123],a[236:239],v[220:223],v[140:143]
MM v[120:123],a[232:235],v[204:207],v[120:123]
MM v[120:123],a[228:231],v[188:191],v[120:123]
MM v[120:123],a[224:227],v[172:175],v[120:123]
s_nop 15
v_add_u32 v249, s75, v224
RD a[192:195],v249 offset:0
RD a[208:211],v249 offset:4096
RD a[224:227],v249 offset:8192
RD a[240:243],v249 offset:12288
v_add_u32 v250, s75, v225
RD a[196:199],v250 offset:0
RD a[212:215],v250 offset:4096
RD a[228:231],v250 offset:8192
RD a[244:247],v250 offset:12288
v_add_u32 v251, s75, v226
RD a[200:203],v251 offset:0
RD a[216:219],v251 offset:4096
RD a[232:235],v251 offset:8192
RD a[248:251],v251 offset:12288
v_add_u32 v254, s75, v227
RD a[204:207],v254 offset:0
RD a[220:223],v254 offset:4096
RD a[236:239],v254 offset:8192
RD a[252:255],v254 offset:12288
s_waitcnt lgkmcnt(0)
v_max3_f32 v249, v80, v81, v82
v_max3_f32 v251, v112, v113, v114
v_max3_f32 v249, v249, v83, v84
v_max3_f32 v251, v251, v115, v116
v_max3_f32 v249, v249, v85, v86
v_max3_f32 v251, v251, v117, v118
v_max3_f32 v249, v249, v87, v119
v_max_f32 v249, v249, v251
v_mov_b32 v251, v249
s_nop 1
v_permlane16_swap_b32 v249, v251
v_max_f32 v249, v249, v251
v_mov_b32 v251, v249
s_nop 1
v_permlane32_swap_b32 v249, v251
v_max_f32 v249, v249, v251
v_log_f32 v250, v246
s_nop 0
v_max3_f32 v249, 0, v249, v250
v_sub_f32 v250, 0, v249
EX v250,v250
v_sub_f32 v136, v136, v249
v_sub_f32 v137, v137, v249
v_sub_f32 v138, v138, v249
v_sub_f32 v139, v139, v249
v_sub_f32 v16, v16, v249
v_sub_f32 v80, v80, v249
v_sub_f32 v17, v17, v249
v_sub_f32 v81, v81, v249
v_sub_f32 v18, v18, v249
v_sub_f32 v82, v82, v249
v_sub_f32 v19, v19, v249
v_sub_f32 v83, v83, v249
v_sub_f32 v20, v20, v249
v_sub_f32 v84, v84, v249
v_sub_f32 v21, v21, v249
v_sub_f32 v85, v85, v249
v_sub_f32 v22, v22, v249
v_sub_f32 v86, v86, v249
v_sub_f32 v23, v23, v249
v_sub_f32 v87, v87, v249
v_sub_f32 v48, v48, v249
v_sub_f32 v112, v112, v249
v_sub_f32 v49, v49, v249
v_sub_f32 v113, v113, v249
v_sub_f32 v50, v50, v249
v_sub_f32 v114, v114, v249
v_sub_f32 v51, v51, v249
v_sub_f32 v115, v115, v249
v_sub_f32 v52, v52, v249
v_sub_f32 v116, v116, v249
v_sub_f32 v53, v53, v249
v_sub_f32 v117, v117, v249
v_sub_f32 v54, v54, v249
v_sub_f32 v118, v118, v249
v_sub_f32 v55, v55, v249
v_sub_f32 v119, v119, v249
v_mul_f32 v152, v246, v250
v_mov_b32 v153, 0
v_bfe_u32 v249, v241, 16, 8
v_lshl_or_b32 v249, v249, 1, 1
v_lshlrev_b32 v249, 22, v249
v_and_b32 v241, 0xff00ffff, v241
AR v254,a16
v_sub_f32 v254, v254, v249
v_mul_f32 v254, v254, v250
AW a16,v254
AR v251,a17
v_sub_f32 v251, v251, v249
v_mul_f32 v251, v251, v250
AW a17,v251
AR v254,a18
v_sub_f32 v254, v254, v249
v_mul_f32 v254, v254, v250
AW a18,v254
AR v251,a19
v_sub_f32 v251, v251, v249
v_mul_f32 v251, v251, v250
AW a19,v251
AR v254,a20
v_sub_f32 v254, v254, v249
v_mul_f32 v254, v254, v250
AW a20,v254
AR v251,a21
v_sub_f32 v251, v251, v249
v_mul_f32 v251, v251, v250
AW a21,v251
AR v254,a22
v_sub_f32 v254, v254, v249
v_mul_f32 v254, v254, v250
AW a22,v254
AR v251,a23
v_sub_f32 v251, v251, v249
v_mul_f32 v251, v251, v250
AW a23,v251
AR v254,a48
v_sub_f32 v254, v254, v249
v_mul_f32 v254, v254, v250
AW a48,v254
AR v251,a49
v_sub_f32 v251, v251, v249
v_mul_f32 v251, v251, v250
AW a49,v251
AR v254,a50
v_sub_f32 v254, v254, v249
v_mul_f32 v254, v254, v250
AW a50,v254
AR v251,a51
v_sub_f32 v251, v251, v249
v_mul_f32 v251, v251, v250
AW a51,v251
AR v254,a52
v_sub_f32 v254, v254, v249
v_mul_f32 v254, v254, v250
AW a52,v254
AR v251,a53
v_sub_f32 v251, v251, v249
v_mul_f32 v251, v251, v250
AW a53,v251
AR v254,a54
v_sub_f32 v254, v254, v249
v_mul_f32 v254, v254, v250
AW a54,v254
AR v251,a55
v_sub_f32 v251, v251, v249
v_mul_f32 v251, v251, v250
AW a55,v251
AR v254,a80
v_sub_f32 v254, v254, v249
v_mul_f32 v254, v254, v250
AW a80,v254
AR v251,a81
v_sub_f32 v251, v251, v249
v_mul_f32 v251, v251, v250
AW a81,v251
AR v254,a82
v_sub_f32 v254, v254, v249
v_mul_f32 v254, v254, v250
AW a82,v254
AR v251,a83
v_sub_f32 v251, v251, v249
v_mul_f32 v251, v251, v250
AW a83,v251
AR v254,a84
v_sub_f32 v254, v254, v249
v_mul_f32 v254, v254, v250
AW a84,v254
AR v251,a85
v_sub_f32 v251, v251, v249
v_mul_f32 v251, v251, v250
AW a85,v251
AR v254,a86
v_sub_f32 v254, v254, v249
v_mul_f32 v254, v254, v250
AW a86,v254
AR v251,a87
v_sub_f32 v251, v251, v249
v_mul_f32 v251, v251, v250
AW a87,v251
AR v254,a112
v_sub_f32 v254, v254, v249
v_mul_f32 v254, v254, v250
AW a112,v254
AR v251,a113
v_sub_f32 v251, v251, v249
v_mul_f32 v251, v251, v250
AW a113,v251
AR v254,a114
v_sub_f32 v254, v254, v249
v_mul_f32 v254, v254, v250
AW a114,v254
AR v251,a115
v_sub_f32 v251, v251, v249
v_mul_f32 v251, v251, v250
AW a115,v251
AR v254,a116
v_sub_f32 v254, v254, v249
v_mul_f32 v254, v254, v250
AW a116,v254
AR v251,a117
v_sub_f32 v251, v251, v249
v_mul_f32 v251, v251, v250
AW a117,v251
AR v254,a118
v_sub_f32 v254, v254, v249
v_mul_f32 v254, v254, v250
AW a118,v254
AR v251,a119
v_sub_f32 v251, v251, v249
v_mul_f32 v251, v251, v250
AW a119,v251
v_max3_f32 v249, v88, v89, v90
v_max3_f32 v251, v120, v121, v122
v_max3_f32 v249, v249, v91, v92
v_max3_f32 v251, v251, v123, v124
v_max3_f32 v249, v249, v93, v94
v_max3_f32 v251, v251, v125, v126
v_max3_f32 v249, v249, v95, v127
v_max_f32 v249, v249, v251
v_mov_b32 v251, v249
s_nop 1
v_permlane16_swap_b32 v249, v251
v_max_f32 v249, v249, v251
v_mov_b32 v251, v249
s_nop 1
v_permlane32_swap_b32 v249, v251
v_max_f32 v249, v249, v251
v_log_f32 v250, v247
s_nop 0
v_max3_f32 v249, 0, v249, v250
v_sub_f32 v250, 0, v249
EX v250,v250
v_sub_f32 v140, v140, v249
v_sub_f32 v141, v141, v249
v_sub_f32 v142, v142, v249
v_sub_f32 v143, v143, v249
v_sub_f32 v24, v24, v249
v_sub_f32 v88, v88, v249
v_sub_f32 v25, v25, v249
v_sub_f32 v89, v89, v249
v_sub_f32 v26, v26, v249
v_sub_f32 v90, v90, v249
v_sub_f32 v27, v27, v249
v_sub_f32 v91, v91, v249
v_sub_f32 v28, v28, v249
v_sub_f32 v92, v92, v249
v_sub_f32 v29, v29, v249
v_sub_f32 v93, v93, v249
v_sub_f32 v30, v30, v249
v_sub_f32 v94, v94, v249
v_sub_f32 v31, v31, v249
v_sub_f32 v95, v95, v249
v_sub_f32 v56, v56, v249
v_sub_f32 v120, v120, v249
v_sub_f32 v57, v57, v249
v_sub_f32 v121, v121, v249
v_sub_f32 v58, v58, v249
v_sub_f32 v122, v122, v249
v_sub_f32 v59, v59, v249
v_sub_f32 v123, v123, v249
v_sub_f32 v60, v60, v249
v_sub_f32 v124, v124, v249
v_sub_f32 v61, v61, v249
v_sub_f32 v125, v125, v249
v_sub_f32 v62, v62, v249
v_sub_f32 v126, v126, v249
v_sub_f32 v63, v63, v249
v_sub_f32 v127, v127, v249
v_mul_f32 v156, v247, v250
v_mov_b32 v157, 0
v_bfe_u32 v249, v241, 24, 8
v_lshl_or_b32 v249, v249, 1, 1
v_lshlrev_b32 v249, 22, v249
v_and_b32 v241, 0xffffff, v241
AR v254,a24
v_sub_f32 v254, v254, v249
v_mul_f32 v254, v254, v250
AW a24,v254
AR v251,a25
v_sub_f32 v251, v251, v249
v_mul_f32 v251, v251, v250
AW a25,v251
AR v254,a26
v_sub_f32 v254, v254, v249
v_mul_f32 v254, v254, v250
AW a26,v254
AR v251,a27
v_sub_f32 v251, v251, v249
v_mul_f32 v251, v251, v250
AW a27,v251
AR v254,a28
v_sub_f32 v254, v254, v249
v_mul_f32 v254, v254, v250
AW a28,v254
AR v251,a29
v_sub_f32 v251, v251, v249
v_mul_f32 v251, v251, v250
AW a29,v251
AR v254,a30
v_sub_f32 v254, v254, v249
v_mul_f32 v254, v254, v250
AW a30,v254
AR v251,a31
v_sub_f32 v251, v251, v249
v_mul_f32 v251, v251, v250
AW a31,v251
AR v254,a56
v_sub_f32 v254, v254, v249
v_mul_f32 v254, v254, v250
AW a56,v254
AR v251,a57
v_sub_f32 v251, v251, v249
v_mul_f32 v251, v251, v250
AW a57,v251
AR v254,a58
v_sub_f32 v254, v254, v249
v_mul_f32 v254, v254, v250
AW a58,v254
AR v251,a59
v_sub_f32 v251, v251, v249
v_mul_f32 v251, v251, v250
AW a59,v251
AR v254,a60
v_sub_f32 v254, v254, v249
v_mul_f32 v254, v254, v250
AW a60,v254
AR v251,a61
v_sub_f32 v251, v251, v249
v_mul_f32 v251, v251, v250
AW a61,v251
AR v254,a62
v_sub_f32 v254, v254, v249
v_mul_f32 v254, v254, v250
AW a62,v254
AR v251,a63
v_sub_f32 v251, v251, v249
v_mul_f32 v251, v251, v250
AW a63,v251
AR v254,a88
v_sub_f32 v254, v254, v249
v_mul_f32 v254, v254, v250
AW a88,v254
AR v251,a89
v_sub_f32 v251, v251, v249
v_mul_f32 v251, v251, v250
AW a89,v251
AR v254,a90
v_sub_f32 v254, v254, v249
v_mul_f32 v254, v254, v250
AW a90,v254
AR v251,a91
v_sub_f32 v251, v251, v249
v_mul_f32 v251, v251, v250
AW a91,v251
AR v254,a92
v_sub_f32 v254, v254, v249
v_mul_f32 v254, v254, v250
AW a92,v254
AR v251,a93
v_sub_f32 v251, v251, v249
v_mul_f32 v251, v251, v250
AW a93,v251
AR v254,a94
v_sub_f32 v254, v254, v249
v_mul_f32 v254, v254, v250
AW a94,v254
AR v251,a95
v_sub_f32 v251, v251, v249
v_mul_f32 v251, v251, v250
AW a95,v251
AR v254,a120
v_sub_f32 v254, v254, v249
v_mul_f32 v254, v254, v250
AW a120,v254
AR v251,a121
v_sub_f32 v251, v251, v249
v_mul_f32 v251, v251, v250
AW a121,v251
AR v254,a122
v_sub_f32 v254, v254, v249
v_mul_f32 v254, v254, v250
AW a122,v254
AR v251,a123
v_sub_f32 v251, v251, v249
v_mul_f32 v251, v251, v250
AW a123,v251
AR v254,a124
v_sub_f32 v254, v254, v249
v_mul_f32 v254, v254, v250
AW a124,v254
AR v251,a125
v_sub_f32 v251, v251, v249
v_mul_f32 v251, v251, v250
AW a125,v251
AR v254,a126
v_sub_f32 v254, v254, v249
v_mul_f32 v254, v254, v250
AW a126,v254
AR v251,a127
v_sub_f32 v251, v251, v249
v_mul_f32 v251, v251, v250
AW a127,v251
EX v80,v80
EX v81,v81
EX v82,v82
EX v83,v83
EX v84,v84
EX v85,v85
EX v86,v86
EX v87,v87
EX v88,v88
EX v89,v89
EX v90,v90
EX v91,v91
EX v92,v92
EX v93,v93
EX v94,v94
EX v95,v95
EX v112,v112
EX v113,v113
EX v114,v114
EX v115,v115
EX v116,v116
EX v117,v117
EX v118,v118
EX v119,v119
EX v120,v120
EX v121,v121
EX v122,v122
EX v123,v123
EX v124,v124
EX v125,v125
EX v126,v126
EX v127,v127
CV v80,v80,v81
CV v81,v82,v83
CV v82,v84,v85
)ASM"
R"ASM(
CV v83,v86,v87
CV v88,v88,v89
CV v89,v90,v91
CV v90,v92,v93
CV v91,v94,v95
CV v84,v112,v113
CV v85,v114,v115
CV v86,v116,v117
CV v87,v118,v119
CV v92,v120,v121
CV v93,v122,v123
CV v94,v124,v125
CV v95,v126,v127
v_smfmac_f32_16x16x64_bf16 v[152:155], v[236:239], v[80:87], v234
s_nop 0
v_smfmac_f32_16x16x64_bf16 v[156:159], v[236:239], v[88:95], v234
s_nop 9
s_cmp_eq_u32 s73, 37
s_cbranch_scc1 LBACK37_%=
s_cmp_eq_u32 s73, 39
s_cbranch_scc1 LBACK39_%=
s_endpgm
LEND_%=:
)ASM"

    :: [ka] "s"(__builtin_amdgcn_kernarg_segment_ptr()), [slds] "s"(slds), [swave] "s"(w), [sb] "s"(blockIdx.x)
    : "memory", "vcc", "scc", CLOB_V, CLOB_A, CLOB_S);
}
constexpr int DYN_LDS = 163840;

extern "C" void kernel_launch(void* const* d_in, const int* in_sizes, int n_in, void* d_out, int out_size, void* d_ws, size_t ws_size, hipStream_t stream) {
#if !defined(__HIP_DEVICE_COMPILE__)
  if (DYN_LDS) hipFuncSetAttribute((const void*)ka_fused, hipFuncAttributeMaxDynamicSharedMemorySize, DYN_LDS);
  ka_fused<<<256, 256, DYN_LDS, stream>>>(d_in[3], d_in[0], d_ws, d_in[1], d_in[6], d_in[2], d_in[4], d_in[7], d_in[8], d_in[9], d_out);
#endif
}
```

```cpp
#include <hip/hip_runtime.h>
#include <stdint.h>

using f32x4  = __attribute__((ext_vector_type(4))) float;
using u32x4  = __attribute__((ext_vector_type(4))) unsigned;
using u32x2  = __attribute__((ext_vector_type(2))) unsigned;

constexpr int SEQ = 2048, DM = 640, NH = 32, DH = 128, HD = NH * DH, FF = 1728, TOPC = 8;

constexpr unsigned WS_FO  = 0;
constexpr unsigned WS_EP  = 32768;
constexpr unsigned WS_G1  = 36864;
constexpr unsigned WS_G2  = 53248;
constexpr unsigned WS_FH  = 69632;
constexpr unsigned WS_FY  = 73728;
constexpr unsigned WS_FG  = 77824;
constexpr unsigned WS_FR  = 81920;
constexpr unsigned WS_RK  = 86016;
constexpr unsigned WS_XN  = 131072;
constexpr unsigned WS_OF  = WS_XN + SEQ * DM * 2;
constexpr unsigned WS_H   = WS_OF + (18u << 20);
constexpr unsigned WS_Y   = WS_H + 32768;
constexpr unsigned WS_P   = WS_Y + 1048576;
constexpr unsigned WS_SPAN = 64u << 20;

#define CLOB_V "v0","v1","v2","v3","v4","v5","v6","v7","v8","v9","v10","v11","v12","v13","v14","v15","v16","v17","v18","v19","v20","v21","v22","v23","v24","v25","v26","v27","v28","v29","v30","v31","v32","v33","v34","v35","v36","v37","v38","v39","v40","v41","v42","v43","v44","v45","v46","v47","v48","v49","v50","v51","v52","v53","v54","v55","v56","v57","v58","v59","v60","v61","v62","v63","v64","v65","v66","v67","v68","v69","v70","v71","v72","v73","v74","v75","v76","v77","v78","v79","v80","v81","v82","v83","v84","v85","v86","v87","v88","v89","v90","v91","v92","v93","v94","v95","v96","v97","v98","v99","v100","v101","v102","v103","v104","v105","v106","v107","v108","v109","v110","v111","v112","v113","v114","v115","v116","v117","v118","v119","v120","v121","v122","v123","v124","v125","v126","v127","v128","v129","v130","v131","v132","v133","v134","v135","v136","v137","v138","v139","v140","v141","v142","v143","v144","v145","v146","v147","v148","v149","v150","v151","v152","v153","v154","v155","v156","v157","v158","v159","v160","v161","v162","v163","v164","v165","v166","v167","v168","v169","v170","v171","v172","v173","v174","v175","v176","v177","v178","v179","v180","v181","v182","v183","v184","v185","v186","v187","v188","v189","v190","v191","v192","v193","v194","v195","v196","v197","v198","v199","v200","v201","v202","v203","v204","v205","v206","v207","v208","v209","v210","v211","v212","v213","v214","v215","v216","v217","v218","v219","v220","v221","v222","v223","v224","v225","v226","v227","v228","v229","v230","v231","v232","v233","v234","v235","v236","v237","v238","v239","v240","v241","v242","v243","v244","v245","v246","v247","v248","v249","v250","v251","v252","v253","v254","v255"
#define CLOB_A "a0","a1","a2","a3","a4","a5","a6","a7","a8","a9","a10","a11","a12","a13","a14","a15","a16","a17","a18","a19","a20","a21","a22","a23","a24","a25","a26","a27","a28","a29","a30","a31","a32","a33","a34","a35","a36","a37","a38","a39","a40","a41","a42","a43","a44","a45","a46","a47","a48","a49","a50","a51","a52","a53","a54","a55","a56","a57","a58","a59","a60","a61","a62","a63","a64","a65","a66","a67","a68","a69","a70","a71","a72","a73","a74","a75","a76","a77","a78","a79","a80","a81","a82","a83","a84","a85","a86","a87","a88","a89","a90","a91","a92","a93","a94","a95","a96","a97","a98","a99","a100","a101","a102","a103","a104","a105","a106","a107","a108","a109","a110","a111","a112","a113","a114","a115","a116","a117","a118","a119","a120","a121","a122","a123","a124","a125","a126","a127","a128","a129","a130","a131","a132","a133","a134","a135","a136","a137","a138","a139","a140","a141","a142","a143","a144","a145","a146","a147","a148","a149","a150","a151","a152","a153","a154","a155","a156","a157","a158","a159","a160","a161","a162","a163","a164","a165","a166","a167","a168","a169","a170","a171","a172","a173","a174","a175","a176","a177","a178","a179","a180","a181","a182","a183","a184","a185","a186","a187","a188","a189","a190","a191","a192","a193","a194","a195","a196","a197","a198","a199","a200","a201","a202","a203","a204","a205","a206","a207","a208","a209","a210","a211","a212","a213","a214","a215","a216","a217","a218","a219","a220","a221","a222","a223","a224","a225","a226","a227","a228","a229","a230","a231","a232","a233","a234","a235","a236","a237","a238","a239","a240","a241","a242","a243","a244","a245","a246","a247","a248","a249","a250","a251","a252","a253","a254","a255"
#define CLOB_S "s8","s9","s10","s11","s12","s13","s14","s15","s16","s17","s18","s19","s20","s21","s22","s23","s24","s25","s26","s27","s28","s29","s30","s31","s32","s33","s34","s35","s36","s37","s38","s39","s40","s41","s42","s43","s44","s45","s46","s47","s48","s49","s50","s51","s52","s53","s54","s55","s56","s57","s58","s59","s60","s61","s62","s63","s64","s65","s66","s67","s68","s69","s70","s71","s72","s73","s74","s75","s76","s77","s78","s79","s80","s81","s82","s83","s84","s85","s86","s87","s88","s89","s90","s91","s92","s93","s94","s95","s96","s97","s98","s99"
__global__ __launch_bounds__(256, 1) __attribute__((aligned(4096))) void ka_fused(const void* wq, const void* x, void* ws, const void* Kc, const void* wr, const void* Vc, const void* wo, const void* wg, const void* wu, const void* wd, void* out) {
  extern __shared__ __attribute__((aligned(16))) char lds[];
  const unsigned w = __builtin_amdgcn_readfirstlane(threadIdx.x >> 6);
  const unsigned slds = __builtin_amdgcn_readfirstlane((unsigned)(size_t)lds);
  asm volatile(
R"ASM(
.macro MM a, b, c, d:vararg
v_mfma_f32_16x16x32_bf16 \a, \b, \c, \d
.endm
.macro TR a, b:vararg
ds_read_b64_tr_b16 \a, \b
.endm
.macro CV a, b, c:vararg
v_cvt_pk_bf16_f32 \a, \b, \c
.endm
.macro AR a, b:vararg
v_accvgpr_read_b32 \a, \b
.endm
.macro AW a, b:vararg
v_accvgpr_write_b32 \a, \b
.endm
.macro EX a, b:vararg
v_exp_f32 \a, \b
.endm
.macro RD a, b:vararg
ds_read_b128 \a, \b
.endm
s_branch LPG0_%=
.p2align 6
LPG0_%=:
s_load_dwordx2 s[16:17], %[ka], 0
s_load_dwordx2 s[32:33], %[ka], 8
s_load_dwordx2 s[20:21], %[ka], 16
s_load_dwordx2 s[8:9], %[ka], 24
s_load_dwordx2 s[12:13], %[ka], 40
s_load_dwordx2 s[24:25], %[ka], 48
s_load_dwordx2 s[44:45], %[ka], 32
s_load_dwordx2 s[28:29], %[ka], 72
s_load_dwordx2 s[40:41], %[ka], 80
s_lshl_b32 s77, %[swave], 2
s_and_b32 s77, s77, 8
s_add_u32 s77, s77, 56
s_load_dwordx2 s[36:37], %[ka], s77
v_mbcnt_lo_u32_b32 v127, -1, 0
v_mbcnt_hi_u32_b32 v127, -1, v127
v_lshlrev_b32 v248, 4, v127
s_lshr_b32 s77, %[sb], 3
s_lshl_b32 s77, s77, 2
s_add_u32 s77, s77, %[swave]
s_lshl_b32 s77, s77, 10
v_add_u32 v118, s77, v248
s_mov_b32 s77, LKEND_ka-LPC_%=-16
v_min_u32 v118, s77, v118
s_getpc_b64 s[98:99]
LPC_%=:
global_load_dwordx4 a[252:255], v118, s[98:99]
s_and_b32 s77, %[sb], 7
s_lshl_b32 s77, s77, 2
s_bfe_u32 s84, %[sb], 131075
s_add_u32 s49, s77, s84
s_lshr_b32 s50, %[sb], 5
s_lshl_b32 s76, %[sb], 2
s_add_u32 s76, s76, %[swave]
v_lshrrev_b32 v180, 5, v127
s_mul_i32 s82, s76, 2560
v_and_b32 v182, 496, v248
v_mul_u32_u24 v181, 1280, v180
v_add_u32 v168, v181, v182
v_lshrrev_b32 v210, 1, v182
v_add_u32 v181, v181, v210
s_lshl_b32 s77, %[swave], 12
s_add_u32 s65, s77, %[slds]
s_lshl_b32 s89, %[swave], 17
v_bfe_u32 v210, v127, 2, 3
v_and_b32 v212, 3, v127
v_lshlrev_b32 v213, 4, v212
v_lshl_add_u32 v213, v180, 6, v213
v_lshrrev_b32 v253, 3, v127
v_lshlrev_b32 v253, 13, v253
v_and_b32 v254, 112, v248
v_or_b32 v253, v253, v254
v_and_b32 v254, 16, v127
v_lshlrev_b32 v254, 2, v254
v_xor_b32 v253, v253, v254
v_add_u32 v254, 128, v253
s_mul_i32 s96, %[swave], 4112
s_add_u32 s96, s96, %[slds]
s_lshl_b32 s77, s50, 2
s_add_u32 s77, s77, %[swave]
s_mul_i32 s68, s77, 81920
s_and_b32 s62, %[sb], 1
s_mul_i32 s63, s62, 40960
s_add_u32 s68, s68, s63
s_lshl_b32 s63, s63, 1
s_mul_i32 s83, s77, 560128
s_add_u32 s83, s83, 2752512
s_lshl_b32 s93, s77, 9
s_lshl_b32 s94, %[swave], 13
s_add_u32 s94, s94, %[slds]
v_lshrrev_b32 v211, 3, v127
v_mul_u32_u24 v220, 1280, v211
v_lshrrev_b32 v212, 4, v127
v_and_b32 v211, 7, v127
v_xor_b32 v211, v211, v212
v_lshl_add_u32 v220, v211, 4, v220
v_xor_b32 v221, 64, v220
s_bitcmp1_b32 s84, 0
s_mov_b32 s77, 0x77552200
s_cselect_b32 s77, 0x77552200, s77
s_lshl_b32 s91, s50, 2
s_lshr_b32 s77, s77, s91
s_and_b32 s77, s77, 15
s_lshl_b32 s86, s77, 7
s_lshl_b32 s85, s77, 19
v_and_b32 v211, 15, v127
v_bfe_u32 v222, v127, 1, 3
v_xor_b32 v222, v222, v212
v_lshlrev_b32 v222, 4, v222
v_lshl_add_u32 v222, v211, 7, v222
v_xor_b32 v223, 64, v222
v_add_u32 v222, s94, v222
v_add_u32 v223, s94, v223
v_and_b32 v214, 1, v212
v_lshlrev_b32 v215, 5, v214
v_xor_b32 v215, v215, v213
v_lshl_add_u32 v235, v210, 8, v215
v_add_u32 v235, 128, v235
v_and_b32 v234, 1, v127
v_mul_u32_u24 v234, 43690, v234
v_add_u32 v234, 17476, v234
v_bfe_u32 v215, v127, 2, 2
v_and_b32 v216, 3, v127
v_lshlrev_b32 v252, 7, v215
v_lshl_add_u32 v252, v216, 4, v252
v_and_b32 v251, 8, v127
v_lshl_add_u32 v252, v251, 3, v252
v_mul_u32_u24 v251, 2056, v212
s_xor_b32 s77, %[swave], 0
s_and_b32 s91, s77, 1
s_lshl_b32 s91, s91, 6
s_lshr_b32 s77, s77, 1
s_lshl_b32 s77, s77, 10
s_add_u32 s77, s77, %[slds]
s_add_u32 s77, s77, 65536
v_xor_b32 v192, s91, v252
v_add3_u32 v192, s77, v192, v251
v_add_u32 v196, 49344, v192
s_xor_b32 s77, %[swave], 1
s_and_b32 s91, s77, 1
s_lshl_b32 s91, s91, 6
s_lshr_b32 s77, s77, 1
s_lshl_b32 s77, s77, 10
s_add_u32 s77, s77, %[slds]
s_add_u32 s77, s77, 65536
v_xor_b32 v193, s91, v252
v_add3_u32 v193, s77, v193, v251
v_add_u32 v197, 49344, v193
s_xor_b32 s77, %[swave], 2
s_and_b32 s91, s77, 1
s_lshl_b32 s91, s91, 6
s_lshr_b32 s77, s77, 1
s_lshl_b32 s77, s77, 10
s_add_u32 s77, s77, %[slds]
s_add_u32 s77, s77, 65536
v_xor_b32 v194, s91, v252
v_add3_u32 v194, s77, v194, v251
v_add_u32 v198, 49344, v194
s_xor_b32 s77, %[swave], 3
s_and_b32 s91, s77, 1
s_lshl_b32 s91, s91, 6
s_lshr_b32 s77, s77, 1
s_lshl_b32 s77, s77, 10
s_add_u32 s77, s77, %[slds]
s_add_u32 s77, s77, 65536
v_xor_b32 v195, s91, v252
v_add3_u32 v195, s77, v195, v251
v_add_u32 v199, 49344, v195
v_mov_b32 v236, 0x3f803f80
v_mov_b32 v184, 0
v_mov_b32 v237, 0x3f803f80
v_mov_b32 v185, 0
v_mov_b32 v238, 0x3f803f80
v_mov_b32 v188, 0
v_mov_b32 v239, 0x3f803f80
v_mov_b32 v189, 0
s_lshl_b32 s90, s49, 8
s_waitcnt lgkmcnt(0)
s_add_u32 s16, s16, s90
s_addc_u32 s17, s17, 0
s_sub_u32 s18, 5242880, s90
s_mov_b32 s19, 131072
s_mov_b32 s34, 2621440
s_mov_b32 s35, 131072
s_mov_b32 s22, 67108864
s_mov_b32 s23, 131072
s_add_u32 s87, s89, s85
s_add_u32 s85, s85, 524288
s_cmp_ge_u32 s85, 5242880
s_cselect_b32 s85, 0, s85
s_add_u32 m0, s96, 65536
s_nop 0
buffer_load_dwordx4 v253, s[16:19], s87 offen offset:0 lds
s_add_u32 m0, s96, 66560
s_nop 0
buffer_load_dwordx4 v254, s[16:19], s87 offen offset:0 lds
s_add_u32 s87, s87, 65536
s_add_u32 m0, s96, 67592
s_nop 0
buffer_load_dwordx4 v253, s[16:19], s87 offen offset:0 lds
s_add_u32 m0, s96, 68616
s_nop 0
buffer_load_dwordx4 v254, s[16:19], s87 offen offset:0 lds
s_add_u32 s88, s68, s86
s_add_u32 s86, s86, 128
s_cmp_ge_u32 s86, 1280
s_cselect_b32 s86, 0, s86
s_add_u32 m0, s94, 0
s_add_u32 s95, s88, 0
buffer_load_dwordx4 v220, s[32:35], s95 offen offset:0 lds
s_add_u32 s95, s88, 9216
buffer_load_dwordx4 v221, s[32:35], s95 offen offset:1024 lds
s_add_u32 s95, s88, 18432
buffer_load_dwordx4 v220, s[32:35], s95 offen offset:2048 lds
s_add_u32 s95, s88, 27648
buffer_load_dwordx4 v221, s[32:35], s95 offen offset:3072 lds
s_mov_b32 s66, 32768
buffer_load_dword v169, off, s[20:23], s66 sc1
buffer_load_dwordx4 v[128:131], v168, s[32:35], s82 offen offset:0
buffer_load_dwordx4 v[132:135], v168, s[32:35], s82 offen offset:512
buffer_load_dwordx2 v[136:137], v181, s[32:35], s82 offen offset:1024
s_add_u32 s82, s82, 131072
s_sub_u32 s88, s88, s63
s_add_u32 m0, s94, 4096
s_add_u32 s95, s88, 40960
buffer_load_dwordx4 v220, s[32:35], s95 offen offset:0 lds
s_add_u32 s95, s88, 50176
buffer_load_dwordx4 v221, s[32:35], s95 offen offset:1024 lds
s_add_u32 s95, s88, 59392
buffer_load_dwordx4 v220, s[32:35], s95 offen offset:2048 lds
s_add_u32 s95, s88, 68608
buffer_load_dwordx4 v221, s[32:35], s95 offen offset:3072 lds
s_getpc_b64 s[98:99]
LTA0_%=:
s_min_u32 s73, %[swave], 3
s_lshl_b32 s73, s73, 12
s_add_u32 s73, s73, LPG1_%=-LTA0_%=
s_add_u32 s98, s98, s73
s_addc_u32 s99, s99, 0
s_mov_b64 exec, 0
s_setpc_b64 s[98:99]
LTR0_%=:
s_mov_b64 exec, -1
s_add_u32 s87, s89, s85
s_add_u32 s85, s85, 524288
s_cmp_ge_u32 s85, 5242880
s_cselect_b32 s85, 0, s85
s_add_u32 m0, s96, 81984
s_nop 0
buffer_load_dwordx4 v253, s[16:19], s87 offen offset:0 lds
s_add_u32 m0, s96, 83008
s_nop 0
buffer_load_dwordx4 v254, s[16:19], s87 offen offset:0 lds
s_add_u32 s87, s87, 65536
s_add_u32 m0, s96, 84040
s_nop 0
buffer_load_dwordx4 v253, s[16:19], s87 offen offset:0 lds
s_add_u32 m0, s96, 85064
s_nop 0
buffer_load_dwordx4 v254, s[16:19], s87 offen offset:0 lds
s_add_u32 s88, s68, s86
s_add_u32 s86, s86, 128
s_cmp_ge_u32 s86, 1280
s_cselect_b32 s86, 0, s86
s_add_u32 m0, s94, 32768
s_add_u32 s95, s88, 0
buffer_load_dwordx4 v220, s[32:35], s95 offen offset:0 lds
s_add_u32 s95, s88, 9216
buffer_load_dwordx4 v221, s[32:35], s95 offen offset:1024 lds
s_add_u32 s95, s88, 18432
buffer_load_dwordx4 v220, s[32:35], s95 offen offset:2048 lds
s_add_u32 s95, s88, 27648
buffer_load_dwordx4 v221, s[32:35], s95 offen offset:3072 lds
s_sub_u32 s88, s88, s63
s_add_u32 m0, s94, 36864
s_add_u32 s95, s88, 40960
buffer_load_dwordx4 v220, s[32:35], s95 offen offset:0 lds
s_add_u32 s95, s88, 50176
buffer_load_dwordx4 v221, s[32:35], s95 offen offset:1024 lds
s_add_u32 s95, s88, 59392
buffer_load_dwordx4 v220, s[32:35], s95 offen offset:2048 lds
s_add_u32 s95, s88, 68608
buffer_load_dwordx4 v221, s[32:35], s95 offen offset:3072 lds
s_waitcnt vmcnt(20)
RD a[128:131],v222 offset:0
RD a[132:135],v223 offset:0
RD a[136:139],v222 offset:2048
RD a[140:143],v223 offset:2048
s_barrier
TR a[0:1],v192 offset:0
TR a[2:3],v192 offset:512
TR a[4:5],v192 offset:8224
TR a[6:7],v192 offset:8736
TR a[8:9],v192 offset:8
TR a[10:11],v192 offset:520
TR a[12:13],v192 offset:8232
TR a[14:15],v192 offset:8744
TR a[16:17],v193 offset:0
TR a[18:19],v193 offset:512
TR a[20:21],v193 offset:8224
TR a[22:23],v193 offset:8736
TR a[24:25],v193 offset:8
TR a[26:27],v193 offset:520
TR a[28:29],v193 offset:8232
TR a[30:31],v193 offset:8744
s_waitcnt lgkmcnt(15)
s_waitcnt lgkmcnt(12)
MM v[0:3],a[0:3],a[128:131],0
s_add_u32 s87, s89, s85
TR a[32:33],v194 offset:0
TR a[34:35],v194 offset:512
MM v[0:3],a[4:7],a[132:135],v[0:3]
s_add_u32 s85, s85, 524288
TR a[36:37],v194 offset:8224
TR a[38:39],v194 offset:8736
MM v[8:11],a[4:7],a[140:143],0
s_waitcnt vmcnt(16)
TR a[40:41],v194 offset:8
TR a[42:43],v194 offset:520
MM v[8:11],a[0:3],a[136:139],v[8:11]
s_waitcnt lgkmcnt(14)
TR a[44:45],v194 offset:8232
TR a[46:47],v194 offset:8744
s_cmp_ge_u32 s85, 5242880
s_cselect_b32 s85, 0, s85
MM v[4:7],a[8:11],a[128:131],0
s_add_u32 m0, s96, 98432
v_max3_f32 v200, |v0|, |v1|, |v2|
v_max_f32 v200, v200, |v3|
v_readfirstlane_b32 s69, v169
MM v[4:7],a[12:15],a[132:135],v[4:7]
s_lshl_b32 s90, s76, 1
TR a[48:49],v195 offset:0
TR a[50:51],v195 offset:512
MM v[12:15],a[12:15],a[140:143],0
s_and_b32 s77, s90, 15
buffer_load_dwordx4 v253, s[16:19], s87 offen offset:0 lds
v_max3_f32 v201, |v8|, |v9|, |v10|
v_max_f32 v201, v201, |v11|
MM v[12:15],a[8:11],a[136:139],v[12:15]
s_add_u32 m0, s96, 99456
TR a[52:53],v195 offset:8224
v_min_f32 v200, v200, v201
v_and_b32 v200, 0x7f800000, v200
v_lshlrev_b32 v148, 16, v128
v_smfmac_f32_16x16x64_bf16 v[184:187], v[236:239], a[0:7], v234
s_lshl_b32 s77, s77, 1
v_add_u32 v200, 0x2c00000, v200
v_smfmac_f32_16x16x64_bf16 v[188:191], v[236:239], a[8:15], v234
s_waitcnt lgkmcnt(15)
v_min_u32 v200, 0x78400000, v200
v_mov_b32 v201, v200
v_mov_b32 v202, v200
v_mov_b32 v203, v200
s_nop 1
MM v[32:35],a[16:19],a[128:131],v[200:203]
s_bfe_u32 s91, s90, 65540
TR a[54:55],v195 offset:8736
buffer_load_dwordx4 v254, s[16:19], s87 offen offset:0 lds
v_mov_b32 v250, v200
v_and_b32 v149, 0xffff0000, v128
MM v[32:35],a[20:23],a[132:135],v[32:35]
s_add_u32 s87, s87, 65536
TR a[56:57],v195 offset:8
TR a[58:59],v195 offset:520
MM v[40:43],a[20:23],a[140:143],v[200:203]
s_add_u32 m0, s96, 100488
TR a[60:61],v195 offset:8232
TR a[62:63],v195 offset:8744
MM v[40:43],a[16:19],a[136:139],v[40:43]
s_waitcnt lgkmcnt(15)
buffer_load_dwordx4 v253, s[16:19], s87 offen offset:0 lds
v_lshlrev_b32 v150, 16, v129
MM v[36:39],a[24:27],a[128:131],v[200:203]
s_add_u32 m0, s96, 101512
v_and_b32 v151, 0xffff0000, v129
v_lshlrev_b32 v152, 16, v130
MM v[36:39],a[28:31],a[132:135],v[36:39]
s_add_u32 s77, s77, s91
v_and_b32 v153, 0xffff0000, v130
v_lshlrev_b32 v154, 16, v131
MM v[44:47],a[28:31],a[140:143],v[200:203]
s_and_b32 s91, s90, 0xffffffe0
buffer_load_dwordx4 v254, s[16:19], s87 offen offset:0 lds
MM v[44:47],a[24:27],a[136:139],v[44:47]
s_waitcnt lgkmcnt(12)
s_waitcnt vmcnt(16)
RD a[144:147],v222 offset:4096
v_and_b32 v155, 0xffff0000, v131
MM v[64:67],a[32:35],a[128:131],v[200:203]
s_add_u32 s77, s77, s91
RD a[148:151],v223 offset:4096
RD a[152:155],v222 offset:6144
MM v[64:67],a[36:39],a[132:135],v[64:67]
s_lshl_b32 s77, s77, 3
RD a[156:159],v223 offset:6144
v_lshlrev_b32 v156, 16, v132
MM v[72:75],a[36:39],a[140:143],v[200:203]
s_add_u32 s97, s77, 36864
s_waitcnt vmcnt(12)
s_barrier
v_and_b32 v157, 0xffff0000, v132
MM v[72:75],a[32:35],a[136:139],v[72:75]
s_waitcnt lgkmcnt(12)
global_load_dwordx4 v[138:141], v182, s[44:45] offset:0
MM v[68:71],a[40:43],a[128:131],v[200:203]
s_sub_u32 s79, 65535, s90
v_lshlrev_b32 v158, 16, v133
v_and_b32 v159, 0xffff0000, v133
MM v[68:71],a[44:47],a[132:135],v[68:71]
s_lshl_b32 s78, s90, 2
v_lshlrev_b32 v160, 16, v134
v_and_b32 v161, 0xffff0000, v134
MM v[76:79],a[44:47],a[140:143],v[200:203]
s_add_u32 s78, s78, 53248
global_load_dwordx4 v[142:145], v182, s[44:45] offset:512
MM v[76:79],a[40:43],a[136:139],v[76:79]
s_waitcnt lgkmcnt(8)
v_lshrrev_b32 v182, 1, v182
v_lshlrev_b32 v162, 16, v135
MM v[96:99],a[48:51],a[128:131],v[200:203]
s_lshl_b32 s80, s76, 2
global_load_dwordx2 v[146:147], v182, s[44:45] offset:1024
)ASM"
R"ASM(
MM v[96:99],a[52:55],a[132:135],v[96:99]
s_waitcnt lgkmcnt(0)
s_add_u32 s88, s68, s86
s_add_u32 s86, s86, 128
v_and_b32 v163, 0xffff0000, v135
MM v[104:107],a[52:55],a[140:143],v[200:203]
s_add_u32 s80, s80, 81920
s_cmp_ge_u32 s86, 1280
s_cselect_b32 s86, 0, s86
MM v[104:107],a[48:51],a[136:139],v[104:107]
s_add_u32 m0, s94, 0
s_add_u32 s95, s88, 0
v_lshlrev_b32 v164, 16, v136
MM v[100:103],a[56:59],a[128:131],v[200:203]
buffer_load_dwordx4 v220, s[32:35], s95 offen offset:0 lds
MM v[100:103],a[60:63],a[132:135],v[100:103]
s_add_u32 s95, s88, 9216
v_and_b32 v165, 0xffff0000, v136
v_lshlrev_b32 v166, 16, v137
MM v[108:111],a[60:63],a[140:143],v[200:203]
buffer_load_dwordx4 v221, s[32:35], s95 offen offset:1024 lds
MM v[108:111],a[56:59],a[136:139],v[108:111]
s_add_u32 s95, s88, 18432
v_and_b32 v167, 0xffff0000, v137
s_add_u32 s69, s69, 1
MM v[16:19],a[0:3],a[144:147],0
v_add_f32 v170, v148, v149
v_add_f32 v171, v150, v151
MM v[16:19],a[4:7],a[148:151],v[16:19]
v_add_f32 v170, v170, v152
v_add_f32 v171, v171, v153
MM v[24:27],a[4:7],a[156:159],0
buffer_load_dwordx4 v220, s[32:35], s95 offen offset:2048 lds
MM v[24:27],a[0:3],a[152:155],v[24:27]
s_add_u32 s95, s88, 27648
v_add_f32 v170, v170, v154
v_add_f32 v171, v171, v155
MM v[20:23],a[8:11],a[144:147],0
v_max3_f32 v204, |v16|, |v17|, |v18|
v_max_f32 v204, v204, |v19|
MM v[20:23],a[12:15],a[148:151],v[20:23]
TR a[0:1],v192 offset:16448
TR a[2:3],v192 offset:16960
v_add_f32 v170, v170, v156
MM v[28:31],a[12:15],a[156:159],0
v_max3_f32 v205, |v24|, |v25|, |v26|
v_max_f32 v205, v205, |v27|
MM v[28:31],a[8:11],a[152:155],v[28:31]
TR a[4:5],v192 offset:24672
v_min_f32 v204, v204, v205
v_and_b32 v204, 0x7f800000, v204
v_add_u32 v204, 0x2c00000, v204
v_min_u32 v204, 0x78400000, v204
v_mov_b32 v205, v204
v_mov_b32 v206, v204
v_mov_b32 v207, v204
s_nop 1
MM v[48:51],a[16:19],a[144:147],v[204:207]
TR a[6:7],v192 offset:25184
buffer_load_dwordx4 v221, s[32:35], s95 offen offset:3072 lds
v_mov_b32 v252, v204
v_add_f32 v171, v171, v157
MM v[48:51],a[20:23],a[148:151],v[48:51]
s_sub_u32 s88, s88, s63
TR a[8:9],v192 offset:16456
TR a[10:11],v192 offset:16968
MM v[56:59],a[20:23],a[156:159],v[204:207]
s_add_u32 m0, s94, 4096
TR a[12:13],v192 offset:24680
TR a[14:15],v192 offset:25192
MM v[56:59],a[16:19],a[152:155],v[56:59]
s_add_u32 s95, s88, 40960
buffer_load_dwordx4 v220, s[32:35], s95 offen offset:0 lds
v_add_f32 v170, v170, v158
MM v[52:55],a[24:27],a[144:147],v[204:207]
s_add_u32 s95, s88, 50176
TR a[16:17],v193 offset:16448
TR a[18:19],v193 offset:16960
MM v[52:55],a[28:31],a[148:151],v[52:55]
TR a[20:21],v193 offset:24672
TR a[22:23],v193 offset:25184
MM v[60:63],a[28:31],a[156:159],v[204:207]
buffer_load_dwordx4 v221, s[32:35], s95 offen offset:1024 lds
v_add_f32 v171, v171, v159
MM v[60:63],a[24:27],a[152:155],v[60:63]
s_add_u32 s95, s88, 59392
v_add_f32 v170, v170, v160
v_add_f32 v171, v171, v161
MM v[80:83],a[32:35],a[144:147],v[204:207]
TR a[24:25],v193 offset:16456
TR a[26:27],v193 offset:16968
MM v[80:83],a[36:39],a[148:151],v[80:83]
TR a[28:29],v193 offset:24680
TR a[30:31],v193 offset:25192
v_add_f32 v170, v170, v162
MM v[88:91],a[36:39],a[156:159],v[204:207]
buffer_load_dwordx4 v220, s[32:35], s95 offen offset:2048 lds
MM v[88:91],a[32:35],a[152:155],v[88:91]
s_add_u32 s95, s88, 68608
v_add_f32 v171, v171, v163
v_add_f32 v170, v170, v164
MM v[84:87],a[40:43],a[144:147],v[204:207]
TR a[32:33],v194 offset:16448
TR a[34:35],v194 offset:16960
MM v[84:87],a[44:47],a[148:151],v[84:87]
TR a[36:37],v194 offset:24672
TR a[38:39],v194 offset:25184
v_add_f32 v171, v171, v165
MM v[92:95],a[44:47],a[156:159],v[204:207]
buffer_load_dwordx4 v221, s[32:35], s95 offen offset:3072 lds
MM v[92:95],a[40:43],a[152:155],v[92:95]
s_add_u32 s87, s89, s85
s_add_u32 s85, s85, 524288
v_add_f32 v170, v170, v166
MM v[112:115],a[48:51],a[144:147],v[204:207]
TR a[40:41],v194 offset:16456
TR a[42:43],v194 offset:16968
MM v[112:115],a[52:55],a[148:151],v[112:115]
TR a[44:45],v194 offset:24680
TR a[46:47],v194 offset:25192
s_cmp_ge_u32 s85, 5242880
s_cselect_b32 s85, 0, s85
v_add_f32 v171, v171, v167
MM v[120:123],a[52:55],a[156:159],v[204:207]
s_add_u32 m0, s96, 114880
s_nop 0
buffer_load_dwordx4 v253, s[16:19], s87 offen offset:0 lds
MM v[120:123],a[48:51],a[152:155],v[120:123]
s_add_u32 m0, s96, 115904
v_add_f32 v172, v170, v171
v_lshlrev_b32 v232, 11, v180
MM v[116:119],a[56:59],a[144:147],v[204:207]
TR a[48:49],v195 offset:16448
TR a[50:51],v195 offset:16960
MM v[116:119],a[60:63],a[148:151],v[116:119]
TR a[52:53],v195 offset:24672
TR a[54:55],v195 offset:25184
MM v[124:127],a[60:63],a[156:159],v[204:207]
buffer_load_dwordx4 v254, s[16:19], s87 offen offset:0 lds
v_add_f32_dpp v172, v172, v172 row_ror:8 row_mask:0xf bank_mask:0xf
MM v[124:127],a[56:59],a[152:155],v[124:127]
s_add_u32 s87, s87, 65536
s_add_u32 m0, s96, 116936
v_add_f32_dpp v172, v172, v172 row_ror:4 row_mask:0xf bank_mask:0xf
buffer_load_dwordx4 v253, s[16:19], s87 offen offset:0 lds
s_add_u32 m0, s96, 117960
s_nop 0
buffer_load_dwordx4 v254, s[16:19], s87 offen offset:0 lds
s_waitcnt vmcnt(19)
RD a[160:163],v222 offset:32768
RD a[164:167],v223 offset:32768
RD a[168:171],v222 offset:34816
RD a[172:175],v223 offset:34816
RD a[176:179],v222 offset:36864
RD a[180:183],v223 offset:36864
RD a[184:187],v222 offset:38912
RD a[188:191],v223 offset:38912
s_waitcnt vmcnt(15) lgkmcnt(0)
s_barrier
MM v[0:3],a[0:3],a[160:163],v[0:3]
TR a[56:57],v195 offset:16456
TR a[58:59],v195 offset:16968
MM v[0:3],a[4:7],a[164:167],v[0:3]
TR a[60:61],v195 offset:24680
TR a[62:63],v195 offset:25192
MM v[8:11],a[4:7],a[172:175],v[8:11]
v_add_f32_dpp v172, v172, v172 row_ror:2 row_mask:0xf bank_mask:0xf
v_lshl_add_u32 v232, v214, 8, v232
MM v[8:11],a[0:3],a[168:171],v[8:11]
s_add_u32 s88, s68, s86
s_add_u32 s86, s86, 128
MM v[16:19],a[0:3],a[176:179],v[16:19]
s_cmp_ge_u32 s86, 1280
s_cselect_b32 s86, 0, s86
s_add_u32 m0, s94, 32768
MM v[16:19],a[4:7],a[180:183],v[16:19]
s_add_u32 s95, s88, 0
buffer_load_dwordx4 v220, s[32:35], s95 offen offset:0 lds
v_add_f32_dpp v172, v172, v172 row_ror:1 row_mask:0xf bank_mask:0xf
MM v[24:27],a[4:7],a[188:191],v[24:27]
s_add_u32 s95, s88, 9216
v_mov_b32 v173, v172
v_lshl_add_u32 v232, v215, 6, v232
MM v[24:27],a[0:3],a[184:187],v[24:27]
buffer_load_dwordx4 v221, s[32:35], s95 offen offset:1024 lds
MM v[4:7],a[8:11],a[160:163],v[4:7]
s_add_u32 s95, s88, 18432
v_permlane16_swap_b32 v172, v173
v_add_f32 v172, v172, v173
MM v[4:7],a[12:15],a[164:167],v[4:7]
v_mul_f32 v172, 0x3acccccd, v172
v_sub_f32 v176, v148, v172
MM v[12:15],a[12:15],a[172:175],v[12:15]
v_sub_f32 v177, v149, v172
v_mul_f32 v170, v176, v176
MM v[12:15],a[8:11],a[168:171],v[12:15]
buffer_load_dwordx4 v220, s[32:35], s95 offen offset:2048 lds
MM v[20:23],a[8:11],a[176:179],v[20:23]
s_add_u32 s95, s88, 27648
v_mul_f32 v171, v177, v177
v_sub_f32 v176, v150, v172
MM v[20:23],a[12:15],a[180:183],v[20:23]
v_sub_f32 v177, v151, v172
v_fma_f32 v170, v176, v176, v170
MM v[28:31],a[12:15],a[188:191],v[28:31]
v_fma_f32 v171, v177, v177, v171
v_sub_f32 v176, v152, v172
MM v[28:31],a[8:11],a[184:187],v[28:31]
buffer_load_dwordx4 v221, s[32:35], s95 offen offset:3072 lds
v_smfmac_f32_16x16x64_bf16 v[184:187], v[236:239], a[0:7], v234
s_sub_u32 s88, s88, s63
s_add_u32 m0, s94, 36864
v_sub_f32 v177, v153, v172
v_smfmac_f32_16x16x64_bf16 v[188:191], v[236:239], a[8:15], v234
s_add_u32 s95, s88, 40960
v_fma_f32 v170, v176, v176, v170
MM v[32:35],a[16:19],a[160:163],v[32:35]
TR a[0:1],v192 offset:32896
s_nop 0
LPG1_%=:
s_cbranch_execz LPG5_%=
TR a[2:3],v192 offset:33408
MM v[32:35],a[20:23],a[164:167],v[32:35]
TR a[4:5],v192 offset:41120
TR a[6:7],v192 offset:41632
v_fma_f32 v171, v177, v177, v171
MM v[40:43],a[20:23],a[172:175],v[40:43]
TR a[8:9],v192 offset:32904
TR a[10:11],v192 offset:33416
MM v[40:43],a[16:19],a[168:171],v[40:43]
TR a[12:13],v192 offset:41128
TR a[14:15],v192 offset:41640
buffer_load_dwordx4 v220, s[32:35], s95 offen offset:0 lds
MM v[48:51],a[16:19],a[176:179],v[48:51]
s_add_u32 s95, s88, 50176
v_sub_f32 v176, v154, v172
v_sub_f32 v177, v155, v172
MM v[48:51],a[20:23],a[180:183],v[48:51]
buffer_load_dwordx4 v221, s[32:35], s95 offen offset:1024 lds
MM v[56:59],a[20:23],a[188:191],v[56:59]
s_add_u32 s95, s88, 59392
v_fma_f32 v170, v176, v176, v170
v_fma_f32 v171, v177, v177, v171
MM v[56:59],a[16:19],a[184:187],v[56:59]
v_sub_f32 v176, v156, v172
v_sub_f32 v177, v157, v172
MM v[36:39],a[24:27],a[160:163],v[36:39]
TR a[16:17],v193 offset:32896
TR a[18:19],v193 offset:33408
MM v[36:39],a[28:31],a[164:167],v[36:39]
TR a[20:21],v193 offset:41120
TR a[22:23],v193 offset:41632
v_fma_f32 v170, v176, v176, v170
MM v[44:47],a[28:31],a[172:175],v[44:47]
buffer_load_dwordx4 v220, s[32:35], s95 offen offset:2048 lds
MM v[44:47],a[24:27],a[168:171],v[44:47]
s_add_u32 s95, s88, 68608
v_fma_f32 v171, v177, v177, v171
v_sub_f32 v176, v158, v172
MM v[52:55],a[24:27],a[176:179],v[52:55]
v_sub_f32 v177, v159, v172
v_fma_f32 v170, v176, v176, v170
MM v[52:55],a[28:31],a[180:183],v[52:55]
buffer_load_dwordx4 v221, s[32:35], s95 offen offset:3072 lds
MM v[60:63],a[28:31],a[188:191],v[60:63]
s_add_u32 s87, s89, s85
s_add_u32 s85, s85, 524288
v_fma_f32 v171, v177, v177, v171
MM v[60:63],a[24:27],a[184:187],v[60:63]
s_cmp_ge_u32 s85, 5242880
s_cselect_b32 s85, 0, s85
s_add_u32 m0, s96, 131328
MM v[64:67],a[32:35],a[160:163],v[64:67]
TR a[24:25],v193 offset:32904
TR a[26:27],v193 offset:33416
v_sub_f32 v176, v160, v172
MM v[64:67],a[36:39],a[164:167],v[64:67]
TR a[28:29],v193 offset:41128
TR a[30:31],v193 offset:41640
MM v[72:75],a[36:39],a[172:175],v[72:75]
buffer_load_dwordx4 v253, s[16:19], s87 offen offset:0 lds
MM v[72:75],a[32:35],a[168:171],v[72:75]
s_add_u32 m0, s96, 132352
v_sub_f32 v177, v161, v172
v_fma_f32 v170, v176, v176, v170
MM v[80:83],a[32:35],a[176:179],v[80:83]
v_fma_f32 v171, v177, v177, v171
v_sub_f32 v176, v162, v172
MM v[80:83],a[36:39],a[180:183],v[80:83]
buffer_load_dwordx4 v254, s[16:19], s87 offen offset:0 lds
MM v[88:91],a[36:39],a[188:191],v[88:91]
s_add_u32 s87, s87, 65536
s_add_u32 m0, s96, 133384
v_sub_f32 v177, v163, v172
MM v[88:91],a[32:35],a[184:187],v[88:91]
v_fma_f32 v170, v176, v176, v170
v_fma_f32 v171, v177, v177, v171
MM v[68:71],a[40:43],a[160:163],v[68:71]
TR a[32:33],v194 offset:32896
TR a[34:35],v194 offset:33408
MM v[68:71],a[44:47],a[164:167],v[68:71]
TR a[36:37],v194 offset:41120
TR a[38:39],v194 offset:41632
v_sub_f32 v176, v164, v172
MM v[76:79],a[44:47],a[172:175],v[76:79]
buffer_load_dwordx4 v253, s[16:19], s87 offen offset:0 lds
MM v[76:79],a[40:43],a[168:171],v[76:79]
s_add_u32 m0, s96, 134408
v_sub_f32 v177, v165, v172
v_fma_f32 v170, v176, v176, v170
MM v[84:87],a[40:43],a[176:179],v[84:87]
v_fma_f32 v171, v177, v177, v171
v_sub_f32 v176, v166, v172
MM v[84:87],a[44:47],a[180:183],v[84:87]
buffer_load_dwordx4 v254, s[16:19], s87 offen offset:0 lds
MM v[92:95],a[44:47],a[188:191],v[92:95]
s_waitcnt vmcnt(16)
RD a[192:195],v222 offset:0
v_sub_f32 v177, v167, v172
MM v[92:95],a[40:43],a[184:187],v[92:95]
RD a[196:199],v223 offset:0
v_fma_f32 v170, v176, v176, v170
MM v[96:99],a[48:51],a[160:163],v[96:99]
TR a[40:41],v194 offset:32904
TR a[42:43],v194 offset:33416
MM v[96:99],a[52:55],a[164:167],v[96:99]
TR a[44:45],v194 offset:41128
TR a[46:47],v194 offset:41640
v_fma_f32 v171, v177, v177, v171
MM v[104:107],a[52:55],a[172:175],v[104:107]
RD a[200:203],v222 offset:2048
v_add_f32 v170, v170, v171
MM v[104:107],a[48:51],a[168:171],v[104:107]
RD a[204:207],v223 offset:2048
v_add_f32_dpp v170, v170, v170 row_ror:8 row_mask:0xf bank_mask:0xf
MM v[112:115],a[48:51],a[176:179],v[112:115]
RD a[208:211],v222 offset:4096
v_add_f32_dpp v170, v170, v170 row_ror:4 row_mask:0xf bank_mask:0xf
MM v[112:115],a[52:55],a[180:183],v[112:115]
RD a[212:215],v223 offset:4096
v_add_f32_dpp v170, v170, v170 row_ror:2 row_mask:0xf bank_mask:0xf
MM v[120:123],a[52:55],a[188:191],v[120:123]
RD a[216:219],v222 offset:6144
v_add_f32_dpp v170, v170, v170 row_ror:1 row_mask:0xf bank_mask:0xf
MM v[120:123],a[48:51],a[184:187],v[120:123]
)ASM"
R"ASM(
s_waitcnt lgkmcnt(15)
RD a[220:223],v223 offset:6144
v_mov_b32 v173, v170
MM v[100:103],a[56:59],a[160:163],v[100:103]
TR a[48:49],v195 offset:32896
TR a[50:51],v195 offset:33408
MM v[100:103],a[60:63],a[164:167],v[100:103]
TR a[52:53],v195 offset:41120
TR a[54:55],v195 offset:41632
MM v[108:111],a[60:63],a[172:175],v[108:111]
v_permlane16_swap_b32 v170, v173
v_add_f32 v170, v170, v173
MM v[108:111],a[56:59],a[168:171],v[108:111]
s_waitcnt lgkmcnt(4)
s_add_u32 s88, s68, s86
s_add_u32 s86, s86, 128
MM v[116:119],a[56:59],a[176:179],v[116:119]
s_cmp_ge_u32 s86, 1280
s_cselect_b32 s86, 0, s86
s_add_u32 m0, s94, 0
v_mul_f32 v170, 0x3acccccd, v170
MM v[116:119],a[60:63],a[180:183],v[116:119]
s_add_u32 s95, s88, 0
buffer_load_dwordx4 v220, s[32:35], s95 offen offset:0 lds
MM v[124:127],a[60:63],a[188:191],v[124:127]
s_add_u32 s95, s88, 9216
CV v178,v172,v170
v_lshlrev_b32 v174, 16, v178
MM v[124:127],a[56:59],a[184:187],v[124:127]
buffer_load_dwordx4 v221, s[32:35], s95 offen offset:1024 lds
s_waitcnt vmcnt(14)
s_barrier
MM v[0:3],a[0:3],a[192:195],v[0:3]
s_add_u32 s95, s88, 18432
TR a[56:57],v195 offset:32904
TR a[58:59],v195 offset:33416
v_and_b32 v170, 0xffff0000, v178
MM v[0:3],a[4:7],a[196:199],v[0:3]
TR a[60:61],v195 offset:41128
TR a[62:63],v195 offset:41640
MM v[8:11],a[4:7],a[204:207],v[8:11]
v_add_f32 v170, 0x3727c5ac, v170
CV v170,v170,v170
MM v[8:11],a[0:3],a[200:203],v[8:11]
buffer_load_dwordx4 v220, s[32:35], s95 offen offset:2048 lds
MM v[16:19],a[0:3],a[208:211],v[16:19]
s_add_u32 s95, s88, 27648
v_and_b32 v170, 0xffff0000, v170
v_lshl_add_u32 v232, v216, 3, v232
MM v[16:19],a[4:7],a[212:215],v[16:19]
v_sqrt_f32 v170, v170
MM v[24:27],a[4:7],a[220:223],v[24:27]
v_mov_b32 v179, s69
CV v178,v174,v170
MM v[24:27],a[0:3],a[216:219],v[24:27]
buffer_load_dwordx4 v221, s[32:35], s95 offen offset:3072 lds
MM v[4:7],a[8:11],a[192:195],v[4:7]
s_sub_u32 s88, s88, s63
s_add_u32 m0, s94, 4096
s_add_u32 s95, s88, 40960
v_and_b32 v170, 0xffff0000, v178
MM v[4:7],a[12:15],a[196:199],v[4:7]
v_rcp_f32 v175, v170
MM v[12:15],a[12:15],a[204:207],v[12:15]
v_lshlrev_b32 v173, 4, v180
v_lshl_add_u32 v232, v214, 5, v232
MM v[12:15],a[8:11],a[200:203],v[12:15]
buffer_load_dwordx4 v220, s[32:35], s95 offen offset:0 lds
MM v[20:23],a[8:11],a[208:211],v[20:23]
s_add_u32 s95, s88, 50176
s_mov_b32 exec_lo, 1
s_mov_b32 exec_hi, 1
buffer_store_dwordx2 v[178:179], v173, s[20:23], s97 offen offset:0 sc1
s_mov_b64 exec, -1
MM v[20:23],a[12:15],a[212:215],v[20:23]
v_sub_f32 v148, v148, v174
v_sub_f32 v149, v149, v174
MM v[28:31],a[12:15],a[220:223],v[28:31]
v_sub_f32 v150, v150, v174
v_sub_f32 v151, v151, v174
MM v[28:31],a[8:11],a[216:219],v[28:31]
buffer_load_dwordx4 v221, s[32:35], s95 offen offset:1024 lds
v_smfmac_f32_16x16x64_bf16 v[184:187], v[236:239], a[0:7], v234
s_add_u32 s95, s88, 59392
v_sub_f32 v152, v152, v174
v_smfmac_f32_16x16x64_bf16 v[188:191], v[236:239], a[8:15], v234
v_sub_f32 v153, v153, v174
MM v[32:35],a[16:19],a[192:195],v[32:35]
TR a[0:1],v196 offset:0
TR a[2:3],v196 offset:512
MM v[32:35],a[20:23],a[196:199],v[32:35]
TR a[4:5],v196 offset:8224
TR a[6:7],v196 offset:8736
v_sub_f32 v154, v154, v174
MM v[40:43],a[20:23],a[204:207],v[40:43]
TR a[8:9],v196 offset:8
TR a[10:11],v196 offset:520
MM v[40:43],a[16:19],a[200:203],v[40:43]
TR a[12:13],v196 offset:8232
TR a[14:15],v196 offset:8744
buffer_load_dwordx4 v220, s[32:35], s95 offen offset:2048 lds
MM v[48:51],a[16:19],a[208:211],v[48:51]
s_add_u32 s95, s88, 68608
v_sub_f32 v155, v155, v174
v_sub_f32 v156, v156, v174
MM v[48:51],a[20:23],a[212:215],v[48:51]
buffer_load_dwordx4 v221, s[32:35], s95 offen offset:3072 lds
MM v[56:59],a[20:23],a[220:223],v[56:59]
s_add_u32 s87, s89, s85
s_add_u32 s85, s85, 524288
v_sub_f32 v157, v157, v174
MM v[56:59],a[16:19],a[216:219],v[56:59]
s_cmp_ge_u32 s85, 5242880
s_cselect_b32 s85, 0, s85
s_add_u32 m0, s96, 65536
MM v[36:39],a[24:27],a[192:195],v[36:39]
TR a[16:17],v197 offset:0
TR a[18:19],v197 offset:512
v_sub_f32 v158, v158, v174
MM v[36:39],a[28:31],a[196:199],v[36:39]
TR a[20:21],v197 offset:8224
TR a[22:23],v197 offset:8736
MM v[44:47],a[28:31],a[204:207],v[44:47]
buffer_load_dwordx4 v253, s[16:19], s87 offen offset:0 lds
MM v[44:47],a[24:27],a[200:203],v[44:47]
s_add_u32 m0, s96, 66560
v_sub_f32 v159, v159, v174
v_sub_f32 v160, v160, v174
MM v[52:55],a[24:27],a[208:211],v[52:55]
v_sub_f32 v161, v161, v174
v_sub_f32 v162, v162, v174
MM v[52:55],a[28:31],a[212:215],v[52:55]
buffer_load_dwordx4 v254, s[16:19], s87 offen offset:0 lds
MM v[60:63],a[28:31],a[220:223],v[60:63]
s_add_u32 s87, s87, 65536
s_add_u32 m0, s96, 67592
v_sub_f32 v163, v163, v174
MM v[60:63],a[24:27],a[216:219],v[60:63]
v_sub_f32 v164, v164, v174
v_sub_f32 v165, v165, v174
MM v[64:67],a[32:35],a[192:195],v[64:67]
TR a[24:25],v197 offset:8
TR a[26:27],v197 offset:520
MM v[64:67],a[36:39],a[196:199],v[64:67]
TR a[28:29],v197 offset:8232
TR a[30:31],v197 offset:8744
v_sub_f32 v166, v166, v174
MM v[72:75],a[36:39],a[204:207],v[72:75]
buffer_load_dwordx4 v253, s[16:19], s87 offen offset:0 lds
MM v[72:75],a[32:35],a[200:203],v[72:75]
s_add_u32 m0, s96, 68616
v_sub_f32 v167, v167, v174
CV v128,v148,v149
MM v[80:83],a[32:35],a[208:211],v[80:83]
CV v129,v150,v151
CV v130,v152,v153
MM v[80:83],a[36:39],a[212:215],v[80:83]
buffer_load_dwordx4 v254, s[16:19], s87 offen offset:0 lds
MM v[88:91],a[36:39],a[220:223],v[88:91]
s_waitcnt vmcnt(17)
RD a[224:227],v222 offset:32768
CV v131,v154,v155
MM v[88:91],a[32:35],a[216:219],v[88:91]
RD a[228:231],v223 offset:32768
CV v132,v156,v157
MM v[68:71],a[40:43],a[192:195],v[68:71]
TR a[32:33],v198 offset:0
TR a[34:35],v198 offset:512
MM v[68:71],a[44:47],a[196:199],v[68:71]
TR a[36:37],v198 offset:8224
TR a[38:39],v198 offset:8736
CV v133,v158,v159
MM v[76:79],a[44:47],a[204:207],v[76:79]
RD a[232:235],v222 offset:34816
CV v134,v160,v161
MM v[76:79],a[40:43],a[200:203],v[76:79]
RD a[236:239],v223 offset:34816
CV v135,v162,v163
MM v[84:87],a[40:43],a[208:211],v[84:87]
RD a[240:243],v222 offset:36864
CV v136,v164,v165
MM v[84:87],a[44:47],a[212:215],v[84:87]
RD a[244:247],v223 offset:36864
CV v137,v166,v167
MM v[92:95],a[44:47],a[220:223],v[92:95]
RD a[248:251],v222 offset:38912
v_lshlrev_b32 v148, 16, v128
MM v[92:95],a[40:43],a[216:219],v[92:95]
s_waitcnt lgkmcnt(15)
RD a[252:255],v223 offset:38912
v_and_b32 v149, 0xffff0000, v128
MM v[96:99],a[48:51],a[192:195],v[96:99]
TR a[40:41],v198 offset:8
TR a[42:43],v198 offset:520
MM v[96:99],a[52:55],a[196:199],v[96:99]
TR a[44:45],v198 offset:8232
TR a[46:47],v198 offset:8744
v_lshlrev_b32 v150, 16, v129
MM v[104:107],a[52:55],a[204:207],v[104:107]
v_and_b32 v151, 0xffff0000, v129
v_lshlrev_b32 v152, 16, v130
MM v[104:107],a[48:51],a[200:203],v[104:107]
s_waitcnt lgkmcnt(4)
s_add_u32 s88, s68, s86
s_add_u32 s86, s86, 128
MM v[112:115],a[48:51],a[208:211],v[112:115]
s_cmp_ge_u32 s86, 1280
s_cselect_b32 s86, 0, s86
s_add_u32 m0, s94, 32768
v_and_b32 v153, 0xffff0000, v130
MM v[112:115],a[52:55],a[212:215],v[112:115]
s_add_u32 s95, s88, 0
buffer_load_dwordx4 v220, s[32:35], s95 offen offset:0 lds
MM v[120:123],a[52:55],a[220:223],v[120:123]
s_add_u32 s95, s88, 9216
v_lshlrev_b32 v154, 16, v131
v_and_b32 v155, 0xffff0000, v131
MM v[120:123],a[48:51],a[216:219],v[120:123]
buffer_load_dwordx4 v221, s[32:35], s95 offen offset:1024 lds
MM v[100:103],a[56:59],a[192:195],v[100:103]
s_add_u32 s95, s88, 18432
TR a[48:49],v199 offset:0
TR a[50:51],v199 offset:512
v_lshlrev_b32 v156, 16, v132
MM v[100:103],a[60:63],a[196:199],v[100:103]
TR a[52:53],v199 offset:8224
TR a[54:55],v199 offset:8736
MM v[108:111],a[60:63],a[204:207],v[108:111]
v_and_b32 v157, 0xffff0000, v132
v_lshlrev_b32 v158, 16, v133
MM v[108:111],a[56:59],a[200:203],v[108:111]
buffer_load_dwordx4 v220, s[32:35], s95 offen offset:2048 lds
MM v[116:119],a[56:59],a[208:211],v[116:119]
s_add_u32 s95, s88, 27648
v_and_b32 v159, 0xffff0000, v133
v_lshlrev_b32 v160, 16, v134
MM v[116:119],a[60:63],a[212:215],v[116:119]
v_and_b32 v161, 0xffff0000, v134
v_lshlrev_b32 v162, 16, v135
MM v[124:127],a[60:63],a[220:223],v[124:127]
v_and_b32 v163, 0xffff0000, v135
v_lshlrev_b32 v164, 16, v136
MM v[124:127],a[56:59],a[216:219],v[124:127]
buffer_load_dwordx4 v221, s[32:35], s95 offen offset:3072 lds
s_waitcnt vmcnt(17)
s_barrier
MM v[0:3],a[0:3],a[224:227],v[0:3]
s_sub_u32 s88, s88, s63
TR a[56:57],v199 offset:8
TR a[58:59],v199 offset:520
v_and_b32 v165, 0xffff0000, v136
MM v[0:3],a[4:7],a[228:231],v[0:3]
s_add_u32 m0, s94, 36864
TR a[60:61],v199 offset:8232
TR a[62:63],v199 offset:8744
MM v[8:11],a[4:7],a[236:239],v[8:11]
s_add_u32 s95, s88, 40960
v_lshlrev_b32 v166, 16, v137
v_and_b32 v167, 0xffff0000, v137
MM v[8:11],a[0:3],a[232:235],v[8:11]
buffer_load_dwordx4 v220, s[32:35], s95 offen offset:0 lds
MM v[16:19],a[0:3],a[240:243],v[16:19]
s_add_u32 s95, s88, 50176
v_mul_f32 v148, v148, v175
v_mul_f32 v149, v149, v175
MM v[16:19],a[4:7],a[244:247],v[16:19]
v_mul_f32 v150, v150, v175
v_mul_f32 v151, v151, v175
MM v[24:27],a[4:7],a[252:255],v[24:27]
v_mul_f32 v152, v152, v175
v_mul_f32 v153, v153, v175
MM v[24:27],a[0:3],a[248:251],v[24:27]
buffer_load_dwordx4 v221, s[32:35], s95 offen offset:1024 lds
MM v[4:7],a[8:11],a[224:227],v[4:7]
s_add_u32 s95, s88, 59392
v_mul_f32 v154, v154, v175
v_mul_f32 v155, v155, v175
MM v[4:7],a[12:15],a[228:231],v[4:7]
v_mul_f32 v156, v156, v175
v_mul_f32 v157, v157, v175
MM v[12:15],a[12:15],a[236:239],v[12:15]
v_mul_f32 v158, v158, v175
v_mul_f32 v159, v159, v175
MM v[12:15],a[8:11],a[232:235],v[12:15]
buffer_load_dwordx4 v220, s[32:35], s95 offen offset:2048 lds
MM v[20:23],a[8:11],a[240:243],v[20:23]
s_add_u32 s95, s88, 68608
v_mul_f32 v160, v160, v175
v_mul_f32 v161, v161, v175
MM v[20:23],a[12:15],a[244:247],v[20:23]
v_mul_f32 v162, v162, v175
v_mul_f32 v163, v163, v175
MM v[28:31],a[12:15],a[252:255],v[28:31]
v_mul_f32 v164, v164, v175
v_mul_f32 v165, v165, v175
MM v[28:31],a[8:11],a[248:251],v[28:31]
buffer_load_dwordx4 v221, s[32:35], s95 offen offset:3072 lds
v_smfmac_f32_16x16x64_bf16 v[184:187], v[236:239], a[0:7], v234
s_add_u32 s87, s89, s85
s_add_u32 s85, s85, 524288
v_mul_f32 v166, v166, v175
v_smfmac_f32_16x16x64_bf16 v[188:191], v[236:239], a[8:15], v234
s_cmp_ge_u32 s85, 5242880
s_cselect_b32 s85, 0, s85
MM v[32:35],a[16:19],a[224:227],v[32:35]
s_add_u32 m0, s96, 81984
TR a[0:1],v196 offset:16448
TR a[2:3],v196 offset:16960
MM v[32:35],a[20:23],a[228:231],v[32:35]
TR a[4:5],v196 offset:24672
TR a[6:7],v196 offset:25184
v_mul_f32 v167, v167, v175
MM v[40:43],a[20:23],a[236:239],v[40:43]
TR a[8:9],v196 offset:16456
TR a[10:11],v196 offset:16968
MM v[40:43],a[16:19],a[232:235],v[40:43]
TR a[12:13],v196 offset:24680
TR a[14:15],v196 offset:25192
buffer_load_dwordx4 v253, s[16:19], s87 offen offset:0 lds
MM v[48:51],a[16:19],a[240:243],v[48:51]
s_add_u32 m0, s96, 83008
CV v128,v148,v149
CV v129,v150,v151
MM v[48:51],a[20:23],a[244:247],v[48:51]
buffer_load_dwordx4 v254, s[16:19], s87 offen offset:0 lds
MM v[56:59],a[20:23],a[252:255],v[56:59]
s_add_u32 s87, s87, 65536
s_add_u32 m0, s96, 84040
CV v130,v152,v153
MM v[56:59],a[16:19],a[248:251],v[56:59]
CV v131,v154,v155
CV v132,v156,v157
MM v[36:39],a[24:27],a[224:227],v[36:39]
TR a[16:17],v197 offset:16448
TR a[18:19],v197 offset:16960
MM v[36:39],a[28:31],a[228:231],v[36:39]
TR a[20:21],v197 offset:24672
TR a[22:23],v197 offset:25184
CV v133,v158,v159
MM v[44:47],a[28:31],a[236:239],v[44:47]
buffer_load_dwordx4 v253, s[16:19], s87 offen offset:0 lds
MM v[44:47],a[24:27],a[232:235],v[44:47]
s_add_u32 m0, s96, 85064
CV v134,v160,v161
CV v135,v162,v163
MM v[52:55],a[24:27],a[240:243],v[52:55]
CV v136,v164,v165
CV v137,v166,v167
MM v[52:55],a[28:31],a[244:247],v[52:55]
buffer_load_dwordx4 v254, s[16:19], s87 offen offset:0 lds
MM v[60:63],a[28:31],a[252:255],v[60:63]
s_waitcnt vmcnt(16)
RD a[128:131],v222 offset:0
v_xor_b32 v233, 32, v232
MM v[60:63],a[24:27],a[248:251],v[60:63]
s_add_u32 s77, %[slds], 65536
RD a[132:135],v223 offset:0
v_add_u32 v232, s77, v232
MM v[64:67],a[32:35],a[224:227],v[64:67]
TR a[24:25],v197 offset:16456
)ASM"
R"ASM(
TR a[26:27],v197 offset:16968
MM v[64:67],a[36:39],a[228:231],v[64:67]
TR a[28:29],v197 offset:24680
TR a[30:31],v197 offset:25192
MM v[72:75],a[36:39],a[236:239],v[72:75]
RD a[136:139],v222 offset:2048
v_add_u32 v233, s77, v233
MM v[72:75],a[32:35],a[232:235],v[72:75]
RD a[140:143],v223 offset:2048
buffer_store_dwordx4 v[128:131], v168, s[20:23], s82 offen offset:0 sc1
v_lshlrev_b32 v214, 8, v211
MM v[80:83],a[32:35],a[240:243],v[80:83]
RD a[144:147],v222 offset:4096
v_add_u32 v214, %[slds], v214
MM v[80:83],a[36:39],a[244:247],v[80:83]
RD a[148:151],v223 offset:4096
v_xor_b32 v215, v211, v212
MM v[88:91],a[36:39],a[252:255],v[88:91]
s_lshl_b32 s77, %[swave], 2
RD a[152:155],v222 offset:6144
v_xor_b32 v213, s77, v215
MM v[88:91],a[32:35],a[248:251],v[88:91]
s_waitcnt lgkmcnt(15)
RD a[156:159],v223 offset:6144
buffer_store_dwordx4 v[132:135], v168, s[20:23], s82 offen offset:512 sc1
v_xor_b32 v216, 0, v213
MM v[68:71],a[40:43],a[224:227],v[68:71]
TR a[32:33],v198 offset:16448
TR a[34:35],v198 offset:16960
MM v[68:71],a[44:47],a[228:231],v[68:71]
TR a[36:37],v198 offset:24672
TR a[38:39],v198 offset:25184
MM v[76:79],a[44:47],a[236:239],v[76:79]
buffer_store_dwordx2 v[136:137], v181, s[20:23], s82 offen offset:1024 sc1
MM v[76:79],a[40:43],a[232:235],v[76:79]
s_waitcnt lgkmcnt(4)
s_add_u32 s88, s68, s86
s_add_u32 s86, s86, 128
MM v[84:87],a[40:43],a[240:243],v[84:87]
s_cmp_ge_u32 s86, 1280
s_cselect_b32 s86, 0, s86
MM v[84:87],a[44:47],a[244:247],v[84:87]
s_add_u32 m0, s94, 0
s_add_u32 s95, s88, 0
v_lshlrev_b32 v148, 16, v128
MM v[92:95],a[44:47],a[252:255],v[92:95]
buffer_load_dwordx4 v220, s[32:35], s95 offen offset:0 lds
MM v[92:95],a[40:43],a[248:251],v[92:95]
s_add_u32 s95, s88, 9216
v_and_b32 v149, 0xffff0000, v128
v_lshlrev_b32 v176, 16, v138
MM v[96:99],a[48:51],a[224:227],v[96:99]
TR a[40:41],v198 offset:16456
TR a[42:43],v198 offset:16968
MM v[96:99],a[52:55],a[228:231],v[96:99]
TR a[44:45],v198 offset:24680
TR a[46:47],v198 offset:25192
v_and_b32 v177, 0xffff0000, v138
MM v[104:107],a[52:55],a[236:239],v[104:107]
buffer_load_dwordx4 v221, s[32:35], s95 offen offset:1024 lds
MM v[104:107],a[48:51],a[232:235],v[104:107]
s_add_u32 s95, s88, 18432
v_mul_f32 v170, v148, v176
v_mul_f32 v171, v149, v177
MM v[112:115],a[48:51],a[240:243],v[112:115]
buffer_load_dwordx4 v220, s[32:35], s95 offen offset:2048 lds
MM v[112:115],a[52:55],a[244:247],v[112:115]
s_add_u32 s95, s88, 27648
v_lshlrev_b32 v148, 16, v129
v_and_b32 v149, 0xffff0000, v129
MM v[120:123],a[52:55],a[252:255],v[120:123]
v_lshlrev_b32 v176, 16, v139
v_and_b32 v177, 0xffff0000, v139
MM v[120:123],a[48:51],a[248:251],v[120:123]
v_fma_f32 v170, v148, v176, v170
v_fma_f32 v171, v149, v177, v171
MM v[100:103],a[56:59],a[224:227],v[100:103]
TR a[48:49],v199 offset:16448
TR a[50:51],v199 offset:16960
MM v[100:103],a[60:63],a[228:231],v[100:103]
TR a[52:53],v199 offset:24672
TR a[54:55],v199 offset:25184
v_lshlrev_b32 v148, 16, v130
MM v[108:111],a[60:63],a[236:239],v[108:111]
buffer_load_dwordx4 v221, s[32:35], s95 offen offset:3072 lds
MM v[108:111],a[56:59],a[232:235],v[108:111]
s_sub_u32 s88, s88, s63
s_add_u32 m0, s94, 4096
s_add_u32 s95, s88, 40960
MM v[116:119],a[56:59],a[240:243],v[116:119]
LPG2_%=:
s_cbranch_execz LPG6_%=
buffer_load_dwordx4 v220, s[32:35], s95 offen offset:0 lds
v_and_b32 v149, 0xffff0000, v130
MM v[116:119],a[60:63],a[244:247],v[116:119]
s_add_u32 s95, s88, 50176
v_lshlrev_b32 v176, 16, v140
v_and_b32 v177, 0xffff0000, v140
MM v[124:127],a[60:63],a[252:255],v[124:127]
v_fma_f32 v170, v148, v176, v170
v_fma_f32 v171, v149, v177, v171
MM v[124:127],a[56:59],a[248:251],v[124:127]
v_lshlrev_b32 v148, 16, v131
v_and_b32 v149, 0xffff0000, v131
s_waitcnt vmcnt(20)
s_barrier
MM v[0:3],a[0:3],a[128:131],v[0:3]
TR a[56:57],v199 offset:16456
TR a[58:59],v199 offset:16968
MM v[0:3],a[4:7],a[132:135],v[0:3]
TR a[60:61],v199 offset:24680
TR a[62:63],v199 offset:25192
v_lshlrev_b32 v176, 16, v141
MM v[8:11],a[4:7],a[140:143],v[8:11]
buffer_load_dwordx4 v221, s[32:35], s95 offen offset:1024 lds
MM v[8:11],a[0:3],a[136:139],v[8:11]
s_add_u32 s95, s88, 59392
v_and_b32 v177, 0xffff0000, v141
v_fma_f32 v170, v148, v176, v170
MM v[16:19],a[0:3],a[144:147],v[16:19]
buffer_load_dwordx4 v220, s[32:35], s95 offen offset:2048 lds
MM v[16:19],a[4:7],a[148:151],v[16:19]
s_add_u32 s95, s88, 68608
v_fma_f32 v171, v149, v177, v171
v_lshlrev_b32 v148, 16, v132
MM v[24:27],a[4:7],a[156:159],v[24:27]
v_and_b32 v149, 0xffff0000, v132
v_lshlrev_b32 v176, 16, v142
MM v[24:27],a[0:3],a[152:155],v[24:27]
v_and_b32 v177, 0xffff0000, v142
v_fma_f32 v170, v148, v176, v170
MM v[4:7],a[8:11],a[128:131],v[4:7]
buffer_load_dwordx4 v221, s[32:35], s95 offen offset:3072 lds
MM v[4:7],a[12:15],a[132:135],v[4:7]
s_add_u32 s87, s89, s85
s_add_u32 s85, s85, 524288
v_fma_f32 v171, v149, v177, v171
MM v[12:15],a[12:15],a[140:143],v[12:15]
s_cmp_ge_u32 s85, 5242880
s_cselect_b32 s85, 0, s85
s_add_u32 m0, s96, 98432
MM v[12:15],a[8:11],a[136:139],v[12:15]
v_lshlrev_b32 v148, 16, v133
v_and_b32 v149, 0xffff0000, v133
MM v[20:23],a[8:11],a[144:147],v[20:23]
buffer_load_dwordx4 v253, s[16:19], s87 offen offset:0 lds
MM v[20:23],a[12:15],a[148:151],v[20:23]
s_add_u32 m0, s96, 99456
v_lshlrev_b32 v176, 16, v143
v_and_b32 v177, 0xffff0000, v143
MM v[28:31],a[12:15],a[156:159],v[28:31]
v_fma_f32 v170, v148, v176, v170
v_fma_f32 v171, v149, v177, v171
MM v[28:31],a[8:11],a[152:155],v[28:31]
v_lshlrev_b32 v148, 16, v134
v_and_b32 v149, 0xffff0000, v134
v_smfmac_f32_16x16x64_bf16 v[184:187], v[236:239], a[0:7], v234
v_lshlrev_b32 v176, 16, v144
v_smfmac_f32_16x16x64_bf16 v[188:191], v[236:239], a[8:15], v234
v_and_b32 v177, 0xffff0000, v144
MM v[32:35],a[16:19],a[128:131],v[32:35]
TR a[0:1],v192 offset:0
TR a[2:3],v192 offset:512
buffer_load_dwordx4 v254, s[16:19], s87 offen offset:0 lds
MM v[32:35],a[20:23],a[132:135],v[32:35]
s_add_u32 s87, s87, 65536
TR a[4:5],v192 offset:8224
TR a[6:7],v192 offset:8736
v_fma_f32 v170, v148, v176, v170
MM v[40:43],a[20:23],a[140:143],v[40:43]
s_add_u32 m0, s96, 100488
TR a[8:9],v192 offset:8
TR a[10:11],v192 offset:520
MM v[40:43],a[16:19],a[136:139],v[40:43]
TR a[12:13],v192 offset:8232
TR a[14:15],v192 offset:8744
MM v[48:51],a[16:19],a[144:147],v[48:51]
buffer_load_dwordx4 v253, s[16:19], s87 offen offset:0 lds
v_fma_f32 v171, v149, v177, v171
MM v[48:51],a[20:23],a[148:151],v[48:51]
s_add_u32 m0, s96, 101512
v_lshlrev_b32 v148, 16, v135
v_and_b32 v149, 0xffff0000, v135
MM v[56:59],a[20:23],a[156:159],v[56:59]
buffer_load_dwordx4 v254, s[16:19], s87 offen offset:0 lds
MM v[56:59],a[16:19],a[152:155],v[56:59]
s_waitcnt vmcnt(19)
RD a[160:163],v222 offset:32768
v_lshlrev_b32 v176, 16, v145
MM v[36:39],a[24:27],a[128:131],v[36:39]
TR a[16:17],v193 offset:0
TR a[18:19],v193 offset:512
MM v[36:39],a[28:31],a[132:135],v[36:39]
TR a[20:21],v193 offset:8224
TR a[22:23],v193 offset:8736
v_and_b32 v177, 0xffff0000, v145
MM v[44:47],a[28:31],a[140:143],v[44:47]
RD a[164:167],v223 offset:32768
v_fma_f32 v170, v148, v176, v170
MM v[44:47],a[24:27],a[136:139],v[44:47]
RD a[168:171],v222 offset:34816
v_fma_f32 v171, v149, v177, v171
MM v[52:55],a[24:27],a[144:147],v[52:55]
RD a[172:175],v223 offset:34816
v_lshlrev_b32 v148, 16, v136
MM v[52:55],a[28:31],a[148:151],v[52:55]
RD a[176:179],v222 offset:36864
v_and_b32 v149, 0xffff0000, v136
MM v[60:63],a[28:31],a[156:159],v[60:63]
RD a[180:183],v223 offset:36864
v_lshlrev_b32 v176, 16, v146
MM v[60:63],a[24:27],a[152:155],v[60:63]
s_waitcnt lgkmcnt(15)
RD a[184:187],v222 offset:38912
v_and_b32 v177, 0xffff0000, v146
MM v[64:67],a[32:35],a[128:131],v[64:67]
TR a[24:25],v193 offset:8
TR a[26:27],v193 offset:520
MM v[64:67],a[36:39],a[132:135],v[64:67]
TR a[28:29],v193 offset:8232
TR a[30:31],v193 offset:8744
v_fma_f32 v170, v148, v176, v170
MM v[72:75],a[36:39],a[140:143],v[72:75]
RD a[188:191],v223 offset:38912
v_fma_f32 v171, v149, v177, v171
MM v[72:75],a[32:35],a[136:139],v[72:75]
v_lshlrev_b32 v148, 16, v137
v_and_b32 v149, 0xffff0000, v137
MM v[80:83],a[32:35],a[144:147],v[80:83]
v_lshlrev_b32 v176, 16, v147
v_and_b32 v177, 0xffff0000, v147
MM v[80:83],a[36:39],a[148:151],v[80:83]
v_fma_f32 v170, v148, v176, v170
v_fma_f32 v171, v149, v177, v171
MM v[88:91],a[36:39],a[156:159],v[88:91]
s_waitcnt lgkmcnt(0)
s_add_u32 s88, s68, s86
s_add_u32 s86, s86, 128
MM v[88:91],a[32:35],a[152:155],v[88:91]
s_cmp_ge_u32 s86, 1280
s_cselect_b32 s86, 0, s86
s_add_u32 m0, s94, 32768
v_add_f32 v170, v170, v171
MM v[68:71],a[40:43],a[128:131],v[68:71]
s_add_u32 s95, s88, 0
TR a[32:33],v194 offset:0
TR a[34:35],v194 offset:512
MM v[68:71],a[44:47],a[132:135],v[68:71]
TR a[36:37],v194 offset:8224
TR a[38:39],v194 offset:8736
MM v[76:79],a[44:47],a[140:143],v[76:79]
buffer_load_dwordx4 v220, s[32:35], s95 offen offset:0 lds
v_add_f32_dpp v170, v170, v170 row_ror:8 row_mask:0xf bank_mask:0xf
MM v[76:79],a[40:43],a[136:139],v[76:79]
s_add_u32 s95, s88, 9216
v_add_f32_dpp v170, v170, v170 row_ror:4 row_mask:0xf bank_mask:0xf
v_lshl_add_u32 v224, v216, 4, v214
MM v[84:87],a[40:43],a[144:147],v[84:87]
buffer_load_dwordx4 v221, s[32:35], s95 offen offset:1024 lds
MM v[84:87],a[44:47],a[148:151],v[84:87]
s_add_u32 s95, s88, 18432
v_add_f32_dpp v170, v170, v170 row_ror:2 row_mask:0xf bank_mask:0xf
v_xor_b32 v216, 4, v213
MM v[92:95],a[44:47],a[156:159],v[92:95]
v_add_f32_dpp v170, v170, v170 row_ror:1 row_mask:0xf bank_mask:0xf
v_mov_b32 v173, v170
MM v[92:95],a[40:43],a[152:155],v[92:95]
s_nop 0
v_permlane16_swap_b32 v170, v173
v_add_f32 v170, v170, v173
MM v[96:99],a[48:51],a[128:131],v[96:99]
TR a[40:41],v194 offset:8
TR a[42:43],v194 offset:520
MM v[96:99],a[52:55],a[132:135],v[96:99]
TR a[44:45],v194 offset:8232
TR a[46:47],v194 offset:8744
CV v170,v170,v170
MM v[104:107],a[52:55],a[140:143],v[104:107]
buffer_load_dwordx4 v220, s[32:35], s95 offen offset:2048 lds
MM v[104:107],a[48:51],a[136:139],v[104:107]
s_add_u32 s95, s88, 27648
v_ashrrev_i32 v171, 31, v170
v_or_b32 v171, 0x80000000, v171
MM v[112:115],a[48:51],a[144:147],v[112:115]
buffer_load_dwordx4 v221, s[32:35], s95 offen offset:3072 lds
MM v[112:115],a[52:55],a[148:151],v[112:115]
s_sub_u32 s88, s88, s63
s_add_u32 m0, s94, 36864
s_add_u32 s95, s88, 40960
v_xor_b32 v170, v170, v171
MM v[120:123],a[52:55],a[156:159],v[120:123]
v_and_b32 v170, 0xffff0000, v170
v_sub_u32 v171, s79, v180
MM v[120:123],a[48:51],a[152:155],v[120:123]
v_or_b32 v170, v170, v171
v_lshlrev_b32 v173, 2, v180
MM v[100:103],a[56:59],a[128:131],v[100:103]
TR a[48:49],v195 offset:0
TR a[50:51],v195 offset:512
MM v[100:103],a[60:63],a[132:135],v[100:103]
TR a[52:53],v195 offset:8224
TR a[54:55],v195 offset:8736
MM v[108:111],a[60:63],a[140:143],v[108:111]
buffer_load_dwordx4 v220, s[32:35], s95 offen offset:0 lds
s_mov_b32 exec_lo, 1
s_mov_b32 exec_hi, 1
buffer_store_dword v170, v173, s[20:23], s78 offen offset:0 sc1
s_mov_b64 exec, -1
MM v[108:111],a[56:59],a[136:139],v[108:111]
s_add_u32 s95, s88, 50176
v_mov_b32 v171, s69
v_lshl_add_u32 v225, v216, 4, v214
MM v[116:119],a[56:59],a[144:147],v[116:119]
buffer_load_dwordx4 v221, s[32:35], s95 offen offset:1024 lds
MM v[116:119],a[60:63],a[148:151],v[116:119]
s_add_u32 s95, s88, 59392
v_xor_b32 v216, 8, v213
v_lshl_add_u32 v226, v216, 4, v214
MM v[124:127],a[60:63],a[156:159],v[124:127]
v_xor_b32 v216, 12, v213
v_lshl_add_u32 v227, v216, 4, v214
MM v[124:127],a[56:59],a[152:155],v[124:127]
v_lshlrev_b32 v214, 8, v212
v_xor_b32 v216, 0, v215
s_waitcnt vmcnt(22)
s_barrier
MM v[0:3],a[0:3],a[160:163],v[0:3]
TR a[56:57],v195 offset:8
TR a[58:59],v195 offset:520
MM v[0:3],a[4:7],a[164:167],v[0:3]
TR a[60:61],v195 offset:8232
TR a[62:63],v195 offset:8744
MM v[8:11],a[4:7],a[172:175],v[8:11]
buffer_load_dwordx4 v220, s[32:35], s95 offen offset:2048 lds
MM v[8:11],a[0:3],a[168:171],v[8:11]
s_add_u32 s95, s88, 68608
v_lshl_add_u32 v228, v216, 4, v214
v_xor_b32 v216, 4, v215
MM v[16:19],a[0:3],a[176:179],v[16:19]
buffer_load_dwordx4 v221, s[32:35], s95 offen offset:3072 lds
MM v[16:19],a[4:7],a[180:183],v[16:19]
s_add_u32 s87, s89, s85
)ASM"
R"ASM(
s_add_u32 s85, s85, 524288
v_lshl_add_u32 v229, v216, 4, v214
MM v[24:27],a[4:7],a[188:191],v[24:27]
s_cmp_ge_u32 s85, 5242880
s_cselect_b32 s85, 0, s85
s_add_u32 m0, s96, 114880
MM v[24:27],a[0:3],a[184:187],v[24:27]
v_xor_b32 v216, 8, v215
v_lshl_add_u32 v230, v216, 4, v214
MM v[4:7],a[8:11],a[160:163],v[4:7]
buffer_load_dwordx4 v253, s[16:19], s87 offen offset:0 lds
MM v[4:7],a[12:15],a[164:167],v[4:7]
s_add_u32 m0, s96, 115904
v_xor_b32 v216, 12, v215
v_lshl_add_u32 v231, v216, 4, v214
MM v[12:15],a[12:15],a[172:175],v[12:15]
s_lshl_b32 s90, s49, 19
s_add_u32 s8, s8, s90
s_addc_u32 s9, s9, 0
MM v[12:15],a[8:11],a[168:171],v[12:15]
s_mov_b32 s10, 524288
s_mov_b32 s11, 131072
MM v[20:23],a[8:11],a[176:179],v[20:23]
buffer_load_dwordx4 v254, s[16:19], s87 offen offset:0 lds
MM v[20:23],a[12:15],a[180:183],v[20:23]
s_add_u32 s87, s87, 65536
s_add_u32 s12, s12, s90
s_addc_u32 s13, s13, 0
MM v[28:31],a[12:15],a[188:191],v[28:31]
s_add_u32 m0, s96, 116936
s_mov_b32 s14, 525312
s_mov_b32 s15, 131072
MM v[28:31],a[8:11],a[184:187],v[28:31]
s_sub_u32 s12, s12, 1024
s_subb_u32 s13, s13, 0
s_lshr_b32 s90, s49, 3
v_smfmac_f32_16x16x64_bf16 v[184:187], v[236:239], a[0:7], v234
s_mul_i32 s90, s90, 1310720
s_mul_i32 s77, s84, 320
v_smfmac_f32_16x16x64_bf16 v[188:191], v[236:239], a[8:15], v234
s_add_u32 s90, s90, s77
MM v[32:35],a[16:19],a[160:163],v[32:35]
TR a[0:1],v192 offset:16448
TR a[2:3],v192 offset:16960
buffer_load_dwordx4 v253, s[16:19], s87 offen offset:0 lds
MM v[32:35],a[20:23],a[164:167],v[32:35]
s_add_u32 m0, s96, 117960
TR a[4:5],v192 offset:24672
TR a[6:7],v192 offset:25184
MM v[40:43],a[20:23],a[172:175],v[40:43]
TR a[8:9],v192 offset:16456
TR a[10:11],v192 offset:16968
MM v[40:43],a[16:19],a[168:171],v[40:43]
TR a[12:13],v192 offset:24680
TR a[14:15],v192 offset:25192
MM v[48:51],a[16:19],a[176:179],v[48:51]
buffer_load_dwordx4 v254, s[16:19], s87 offen offset:0 lds
MM v[48:51],a[20:23],a[180:183],v[48:51]
s_waitcnt vmcnt(17)
s_add_u32 s24, s24, s90
s_addc_u32 s25, s25, 0
MM v[56:59],a[20:23],a[188:191],v[56:59]
s_sub_u32 s26, 5242880, s90
RD a[192:195],v222 offset:0
s_mov_b32 s27, 131072
MM v[56:59],a[16:19],a[184:187],v[56:59]
s_mov_b32 s38, 2211840
RD a[196:199],v223 offset:0
s_mov_b32 s39, 131072
MM v[36:39],a[24:27],a[160:163],v[36:39]
s_mov_b32 s42, 2621440
TR a[16:17],v193 offset:16448
TR a[18:19],v193 offset:16960
MM v[36:39],a[28:31],a[164:167],v[36:39]
s_mov_b32 s43, 131072
TR a[20:21],v193 offset:24672
TR a[22:23],v193 offset:25184
MM v[44:47],a[28:31],a[172:175],v[44:47]
s_mov_b32 s71, 0x4e800000
RD a[200:203],v222 offset:2048
s_mov_b32 s72, 0x3e0293ee
MM v[44:47],a[24:27],a[168:171],v[44:47]
s_lshl_b32 s64, %[swave], 12
RD a[204:207],v223 offset:2048
MM v[52:55],a[24:27],a[176:179],v[52:55]
RD a[208:211],v222 offset:4096
MM v[52:55],a[28:31],a[180:183],v[52:55]
RD a[212:215],v223 offset:4096
MM v[60:63],a[28:31],a[188:191],v[60:63]
RD a[216:219],v222 offset:6144
MM v[60:63],a[24:27],a[184:187],v[60:63]
s_waitcnt lgkmcnt(15)
RD a[220:223],v223 offset:6144
MM v[64:67],a[32:35],a[160:163],v[64:67]
TR a[24:25],v193 offset:16456
TR a[26:27],v193 offset:16968
MM v[64:67],a[36:39],a[164:167],v[64:67]
TR a[28:29],v193 offset:24680
TR a[30:31],v193 offset:25192
MM v[72:75],a[36:39],a[172:175],v[72:75]
MM v[72:75],a[32:35],a[168:171],v[72:75]
s_waitcnt lgkmcnt(4)
s_add_u32 s88, s68, s86
s_add_u32 s86, s86, 128
MM v[80:83],a[32:35],a[176:179],v[80:83]
s_cmp_ge_u32 s86, 1280
s_cselect_b32 s86, 0, s86
s_add_u32 m0, s94, 0
MM v[80:83],a[36:39],a[180:183],v[80:83]
s_add_u32 s95, s88, 0
buffer_load_dwordx4 v220, s[32:35], s95 offen offset:0 lds
MM v[88:91],a[36:39],a[188:191],v[88:91]
s_add_u32 s95, s88, 9216
MM v[88:91],a[32:35],a[184:187],v[88:91]
buffer_load_dwordx4 v221, s[32:35], s95 offen offset:1024 lds
MM v[68:71],a[40:43],a[160:163],v[68:71]
s_add_u32 s95, s88, 18432
TR a[32:33],v194 offset:16448
TR a[34:35],v194 offset:16960
MM v[68:71],a[44:47],a[164:167],v[68:71]
TR a[36:37],v194 offset:24672
TR a[38:39],v194 offset:25184
MM v[76:79],a[44:47],a[172:175],v[76:79]
MM v[76:79],a[40:43],a[168:171],v[76:79]
buffer_load_dwordx4 v220, s[32:35], s95 offen offset:2048 lds
MM v[84:87],a[40:43],a[176:179],v[84:87]
s_add_u32 s95, s88, 27648
MM v[84:87],a[44:47],a[180:183],v[84:87]
MM v[92:95],a[44:47],a[188:191],v[92:95]
MM v[92:95],a[40:43],a[184:187],v[92:95]
buffer_load_dwordx4 v221, s[32:35], s95 offen offset:3072 lds
MM v[96:99],a[48:51],a[160:163],v[96:99]
s_sub_u32 s88, s88, s63
TR a[40:41],v194 offset:16456
TR a[42:43],v194 offset:16968
MM v[96:99],a[52:55],a[164:167],v[96:99]
s_add_u32 m0, s94, 4096
TR a[44:45],v194 offset:24680
TR a[46:47],v194 offset:25192
MM v[104:107],a[52:55],a[172:175],v[104:107]
s_add_u32 s95, s88, 40960
MM v[104:107],a[48:51],a[168:171],v[104:107]
buffer_load_dwordx4 v220, s[32:35], s95 offen offset:0 lds
MM v[112:115],a[48:51],a[176:179],v[112:115]
s_add_u32 s95, s88, 50176
MM v[112:115],a[52:55],a[180:183],v[112:115]
MM v[120:123],a[52:55],a[188:191],v[120:123]
MM v[120:123],a[48:51],a[184:187],v[120:123]
buffer_load_dwordx4 v221, s[32:35], s95 offen offset:1024 lds
MM v[100:103],a[56:59],a[160:163],v[100:103]
s_add_u32 s95, s88, 59392
TR a[48:49],v195 offset:16448
TR a[50:51],v195 offset:16960
MM v[100:103],a[60:63],a[164:167],v[100:103]
TR a[52:53],v195 offset:24672
TR a[54:55],v195 offset:25184
MM v[108:111],a[60:63],a[172:175],v[108:111]
MM v[108:111],a[56:59],a[168:171],v[108:111]
buffer_load_dwordx4 v220, s[32:35], s95 offen offset:2048 lds
MM v[116:119],a[56:59],a[176:179],v[116:119]
s_add_u32 s95, s88, 68608
MM v[116:119],a[60:63],a[180:183],v[116:119]
MM v[124:127],a[60:63],a[188:191],v[124:127]
MM v[124:127],a[56:59],a[184:187],v[124:127]
buffer_load_dwordx4 v221, s[32:35], s95 offen offset:3072 lds
s_waitcnt vmcnt(21)
s_barrier
MM v[0:3],a[0:3],a[192:195],v[0:3]
s_add_u32 s87, s89, s85
TR a[56:57],v195 offset:16456
TR a[58:59],v195 offset:16968
MM v[0:3],a[4:7],a[196:199],v[0:3]
s_add_u32 s85, s85, 524288
TR a[60:61],v195 offset:24680
TR a[62:63],v195 offset:25192
MM v[8:11],a[4:7],a[204:207],v[8:11]
s_cmp_ge_u32 s85, 5242880
s_cselect_b32 s85, 0, s85
s_add_u32 m0, s96, 131328
v_and_b32 v208, 240, v248
MM v[8:11],a[0:3],a[200:203],v[8:11]
s_lshl_b32 s62, s62, 8
buffer_load_dwordx4 v253, s[16:19], s87 offen offset:0 lds
MM v[16:19],a[0:3],a[208:211],v[16:19]
s_add_u32 m0, s96, 132352
s_add_u32 s93, s93, 36864
s_add_u32 s62, s62, s93
MM v[16:19],a[4:7],a[212:215],v[16:19]
s_xor_b32 s93, s62, 256
MM v[24:27],a[4:7],a[220:223],v[24:27]
MM v[24:27],a[0:3],a[216:219],v[24:27]
buffer_load_dwordx4 v254, s[16:19], s87 offen offset:0 lds
MM v[4:7],a[8:11],a[192:195],v[4:7]
s_add_u32 s87, s87, 65536
s_add_u32 m0, s96, 133384
MM v[4:7],a[12:15],a[196:199],v[4:7]
MM v[12:15],a[12:15],a[204:207],v[12:15]
MM v[12:15],a[8:11],a[200:203],v[12:15]
buffer_load_dwordx4 v253, s[16:19], s87 offen offset:0 lds
MM v[20:23],a[8:11],a[208:211],v[20:23]
s_add_u32 m0, s96, 134408
MM v[20:23],a[12:15],a[212:215],v[20:23]
MM v[28:31],a[12:15],a[220:223],v[28:31]
MM v[28:31],a[8:11],a[216:219],v[28:31]
buffer_load_dwordx4 v254, s[16:19], s87 offen offset:0 lds
v_smfmac_f32_16x16x64_bf16 v[184:187], v[236:239], a[0:7], v234
s_waitcnt vmcnt(16)
RD a[224:227],v222 offset:32768
v_smfmac_f32_16x16x64_bf16 v[188:191], v[236:239], a[8:15], v234
RD a[228:231],v223 offset:32768
MM v[32:35],a[16:19],a[192:195],v[32:35]
TR a[0:1],v192 offset:32896
TR a[2:3],v192 offset:33408
MM v[32:35],a[20:23],a[196:199],v[32:35]
TR a[4:5],v192 offset:41120
TR a[6:7],v192 offset:41632
MM v[40:43],a[20:23],a[204:207],v[40:43]
TR a[8:9],v192 offset:32904
TR a[10:11],v192 offset:33416
RD a[232:235],v222 offset:34816
MM v[40:43],a[16:19],a[200:203],v[40:43]
TR a[12:13],v192 offset:41128
TR a[14:15],v192 offset:41640
buffer_load_dwordx4 v[200:203], v208, s[20:23], s62 offen offset:0 sc1
MM v[48:51],a[16:19],a[208:211],v[48:51]
RD a[236:239],v223 offset:34816
MM v[48:51],a[20:23],a[212:215],v[48:51]
buffer_load_dwordx4 v[204:207], v208, s[20:23], s93 offen offset:0 sc1
MM v[56:59],a[20:23],a[220:223],v[56:59]
RD a[240:243],v222 offset:36864
MM v[56:59],a[16:19],a[216:219],v[56:59]
s_waitcnt lgkmcnt(15)
RD a[244:247],v223 offset:36864
MM v[36:39],a[24:27],a[192:195],v[36:39]
TR a[16:17],v193 offset:32896
TR a[18:19],v193 offset:33408
MM v[36:39],a[28:31],a[196:199],v[36:39]
TR a[20:21],v193 offset:41120
TR a[22:23],v193 offset:41632
MM v[44:47],a[28:31],a[204:207],v[44:47]
RD a[248:251],v222 offset:38912
MM v[44:47],a[24:27],a[200:203],v[44:47]
RD a[252:255],v223 offset:38912
MM v[52:55],a[24:27],a[208:211],v[52:55]
MM v[52:55],a[28:31],a[212:215],v[52:55]
MM v[60:63],a[28:31],a[220:223],v[60:63]
MM v[60:63],a[24:27],a[216:219],v[60:63]
s_waitcnt lgkmcnt(0)
s_add_u32 s88, s68, s86
s_add_u32 s86, s86, 128
MM v[64:67],a[32:35],a[192:195],v[64:67]
TR a[24:25],v193 offset:32904
TR a[26:27],v193 offset:33416
MM v[64:67],a[36:39],a[196:199],v[64:67]
TR a[28:29],v193 offset:41128
TR a[30:31],v193 offset:41640
s_cmp_ge_u32 s86, 1280
s_cselect_b32 s86, 0, s86
MM v[72:75],a[36:39],a[204:207],v[72:75]
s_add_u32 m0, s94, 32768
s_add_u32 s95, s88, 0
MM v[72:75],a[32:35],a[200:203],v[72:75]
buffer_load_dwordx4 v220, s[32:35], s95 offen offset:0 lds
MM v[80:83],a[32:35],a[208:211],v[80:83]
s_add_u32 s95, s88, 9216
MM v[80:83],a[36:39],a[212:215],v[80:83]
buffer_load_dwordx4 v221, s[32:35], s95 offen offset:1024 lds
MM v[88:91],a[36:39],a[220:223],v[88:91]
s_add_u32 s95, s88, 18432
MM v[88:91],a[32:35],a[216:219],v[88:91]
MM v[68:71],a[40:43],a[192:195],v[68:71]
TR a[32:33],v194 offset:32896
TR a[34:35],v194 offset:33408
MM v[68:71],a[44:47],a[196:199],v[68:71]
TR a[36:37],v194 offset:41120
TR a[38:39],v194 offset:41632
MM v[76:79],a[44:47],a[204:207],v[76:79]
buffer_load_dwordx4 v220, s[32:35], s95 offen offset:2048 lds
MM v[76:79],a[40:43],a[200:203],v[76:79]
s_add_u32 s95, s88, 27648
MM v[84:87],a[40:43],a[208:211],v[84:87]
MM v[84:87],a[44:47],a[212:215],v[84:87]
buffer_load_dwordx4 v221, s[32:35], s95 offen offset:3072 lds
MM v[92:95],a[44:47],a[220:223],v[92:95]
s_waitcnt vmcnt(10)
RD a[128:131],v222 offset:0
MM v[92:95],a[40:43],a[216:219],v[92:95]
RD a[132:135],v223 offset:0
MM v[96:99],a[48:51],a[192:195],v[96:99]
TR a[40:41],v194 offset:32904
TR a[42:43],v194 offset:33416
MM v[96:99],a[52:55],a[196:199],v[96:99]
TR a[44:45],v194 offset:41128
TR a[46:47],v194 offset:41640
MM v[104:107],a[52:55],a[204:207],v[104:107]
RD a[136:139],v222 offset:2048
MM v[104:107],a[48:51],a[200:203],v[104:107]
RD a[140:143],v223 offset:2048
MM v[112:115],a[48:51],a[208:211],v[112:115]
RD a[144:147],v222 offset:4096
MM v[112:115],a[52:55],a[212:215],v[112:115]
RD a[148:151],v223 offset:4096
MM v[120:123],a[52:55],a[220:223],v[120:123]
s_nop 0
LPG3_%=:
s_cbranch_execz LPG7_%=
RD a[152:155],v222 offset:6144
MM v[120:123],a[48:51],a[216:219],v[120:123]
RD a[156:159],v223 offset:6144
MM v[100:103],a[56:59],a[192:195],v[100:103]
s_sub_u32 s88, s88, s63
TR a[48:49],v195 offset:32896
TR a[50:51],v195 offset:33408
MM v[100:103],a[60:63],a[196:199],v[100:103]
s_add_u32 m0, s94, 36864
TR a[52:53],v195 offset:41120
TR a[54:55],v195 offset:41632
MM v[108:111],a[60:63],a[204:207],v[108:111]
s_add_u32 s95, s88, 40960
buffer_load_dwordx4 v220, s[32:35], s95 offen offset:0 lds
MM v[108:111],a[56:59],a[200:203],v[108:111]
s_add_u32 s95, s88, 50176
MM v[116:119],a[56:59],a[208:211],v[116:119]
buffer_load_dwordx4 v221, s[32:35], s95 offen offset:1024 lds
MM v[116:119],a[60:63],a[212:215],v[116:119]
s_add_u32 s95, s88, 59392
MM v[124:127],a[60:63],a[220:223],v[124:127]
MM v[124:127],a[56:59],a[216:219],v[124:127]
buffer_load_dwordx4 v220, s[32:35], s95 offen offset:2048 lds
s_add_u32 s95, s88, 68608
buffer_load_dwordx4 v221, s[32:35], s95 offen offset:3072 lds
s_waitcnt lgkmcnt(4)
s_waitcnt vmcnt(10)
s_barrier
MM v[0:3],a[0:3],a[224:227],v[0:3]
s_add_u32 s77, s64, 0
TR a[56:57],v195 offset:32904
TR a[58:59],v195 offset:33416
MM v[0:3],a[4:7],a[228:231],v[0:3]
s_add_u32 m0, s65, 0
TR a[60:61],v195 offset:41128
TR a[62:63],v195 offset:41640
)ASM"
R"ASM(
MM v[8:11],a[4:7],a[236:239],v[8:11]
s_waitcnt vmcnt(8)
TR a[64:65],v196 offset:0
TR a[66:67],v196 offset:512
MM v[8:11],a[0:3],a[232:235],v[8:11]
s_lshl_b32 s76, %[swave], 7
buffer_load_dwordx4 v228, s[8:11], s77 offen offset:0 lds
v_lshrrev_b32 v253, 8, v248
v_lshlrev_b32 v253, 5, v253
MM v[16:19],a[0:3],a[240:243],v[16:19]
s_add_u32 s76, s76, %[slds]
TR a[68:69],v196 offset:8224
TR a[70:71],v196 offset:8736
v_xor_b32 v201, s69, v201
MM v[16:19],a[4:7],a[244:247],v[16:19]
s_mov_b64 exec, 1
buffer_store_dword v171, off, s[20:23], s80 sc1
s_mov_b64 exec, -1
v_add_u32 v253, s76, v253
v_add_u32 v253, 151552, v253
MM v[24:27],a[4:7],a[252:255],v[24:27]
TR a[72:73],v196 offset:16448
TR a[74:75],v196 offset:16960
MM v[24:27],a[0:3],a[248:251],v[24:27]
TR a[76:77],v196 offset:24672
TR a[78:79],v196 offset:25184
buffer_load_dwordx4 v229, s[8:11], s77 offen offset:1024 lds
v_xor_b32 v203, s69, v203
MM v[4:7],a[8:11],a[224:227],v[4:7]
TR a[80:81],v196 offset:8
TR a[82:83],v196 offset:520
MM v[4:7],a[12:15],a[228:231],v[4:7]
TR a[84:85],v196 offset:8232
TR a[86:87],v196 offset:8744
MM v[12:15],a[12:15],a[236:239],v[12:15]
TR a[88:89],v196 offset:16456
TR a[90:91],v196 offset:16968
v_xor_b32 v205, s69, v205
MM v[12:15],a[8:11],a[232:235],v[12:15]
TR a[92:93],v196 offset:24680
TR a[94:95],v196 offset:25192
buffer_load_dwordx4 v230, s[8:11], s77 offen offset:2048 lds
MM v[20:23],a[8:11],a[240:243],v[20:23]
TR a[96:97],v197 offset:0
TR a[98:99],v197 offset:512
MM v[20:23],a[12:15],a[244:247],v[20:23]
TR a[100:101],v197 offset:8224
TR a[102:103],v197 offset:8736
v_xor_b32 v207, s69, v207
MM v[28:31],a[12:15],a[252:255],v[28:31]
TR a[104:105],v197 offset:16448
TR a[106:107],v197 offset:16960
MM v[28:31],a[8:11],a[248:251],v[28:31]
TR a[108:109],v197 offset:24672
TR a[110:111],v197 offset:25184
buffer_load_dwordx4 v231, s[8:11], s77 offen offset:3072 lds
v_smfmac_f32_16x16x64_bf16 v[184:187], v[236:239], a[0:7], v234
s_add_u32 s77, s64, 16384
TR a[112:113],v197 offset:8
v_or3_b32 v209, v201, v203, v205
v_smfmac_f32_16x16x64_bf16 v[188:191], v[236:239], a[8:15], v234
s_add_u32 m0, s65, 16384
TR a[114:115],v197 offset:520
MM v[32:35],a[16:19],a[224:227],v[32:35]
TR a[116:117],v197 offset:8232
TR a[118:119],v197 offset:8744
MM v[32:35],a[20:23],a[228:231],v[32:35]
TR a[120:121],v197 offset:16456
TR a[122:123],v197 offset:16968
v_or_b32 v209, v209, v207
MM v[40:43],a[20:23],a[236:239],v[40:43]
TR a[124:125],v197 offset:24680
TR a[126:127],v197 offset:25192
MM v[40:43],a[16:19],a[232:235],v[40:43]
TR a[0:1],v198 offset:0
TR a[2:3],v198 offset:512
buffer_load_dwordx4 v228, s[8:11], s77 offen offset:0 lds
MM v[48:51],a[16:19],a[240:243],v[48:51]
TR a[4:5],v198 offset:8224
TR a[6:7],v198 offset:8736
v_cmp_ne_u32 vcc, 0, v209
s_cbranch_vccnz LRG1o_%=
LRG1ob_%=:
MM v[48:51],a[20:23],a[244:247],v[48:51]
TR a[8:9],v198 offset:16448
TR a[10:11],v198 offset:16960
MM v[56:59],a[20:23],a[252:255],v[56:59]
TR a[12:13],v198 offset:24672
TR a[14:15],v198 offset:25184
MM v[56:59],a[16:19],a[248:251],v[56:59]
v_lshlrev_b32 v240, 16, v200
v_and_b32 v200, 0xffff0000, v200
MM v[36:39],a[24:27],a[224:227],v[36:39]
v_rcp_f32 v244, v200
MM v[36:39],a[28:31],a[228:231],v[36:39]
v_xor_b32 v240, 0x80000000, v240
v_mul_f32 v244, s72, v244
MM v[44:47],a[28:31],a[236:239],v[44:47]
v_lshlrev_b32 v241, 16, v202
v_and_b32 v202, 0xffff0000, v202
MM v[44:47],a[24:27],a[232:235],v[44:47]
buffer_load_dwordx4 v229, s[8:11], s77 offen offset:1024 lds
MM v[52:55],a[24:27],a[240:243],v[52:55]
v_rcp_f32 v245, v202
MM v[52:55],a[28:31],a[244:247],v[52:55]
v_xor_b32 v241, 0x80000000, v241
v_mul_f32 v245, s72, v245
MM v[60:63],a[28:31],a[252:255],v[60:63]
v_lshlrev_b32 v242, 16, v204
v_and_b32 v204, 0xffff0000, v204
MM v[60:63],a[24:27],a[248:251],v[60:63]
v_rcp_f32 v246, v204
MM v[64:67],a[32:35],a[224:227],v[64:67]
TR a[16:17],v198 offset:8
TR a[18:19],v198 offset:520
MM v[64:67],a[36:39],a[228:231],v[64:67]
TR a[20:21],v198 offset:8232
TR a[22:23],v198 offset:8744
v_xor_b32 v242, 0x80000000, v242
MM v[72:75],a[36:39],a[236:239],v[72:75]
TR a[24:25],v198 offset:16456
TR a[26:27],v198 offset:16968
MM v[72:75],a[32:35],a[232:235],v[72:75]
TR a[28:29],v198 offset:24680
TR a[30:31],v198 offset:25192
MM v[80:83],a[32:35],a[240:243],v[80:83]
v_mul_f32 v246, s72, v246
v_lshlrev_b32 v243, 16, v206
MM v[80:83],a[36:39],a[244:247],v[80:83]
buffer_load_dwordx4 v230, s[8:11], s77 offen offset:2048 lds
MM v[88:91],a[36:39],a[252:255],v[88:91]
v_and_b32 v206, 0xffff0000, v206
MM v[88:91],a[32:35],a[248:251],v[88:91]
s_waitcnt lgkmcnt(15)
v_rcp_f32 v247, v206
v_smfmac_f32_16x16x64_bf16 v[184:187], v[236:239], a[64:71], v234
v_xor_b32 v243, 0x80000000, v243
v_smfmac_f32_16x16x64_bf16 v[184:187], v[236:239], a[72:79], v234
v_mul_f32 v247, s72, v247
v_smfmac_f32_16x16x64_bf16 v[188:191], v[236:239], a[80:87], v234
v_smfmac_f32_16x16x64_bf16 v[188:191], v[236:239], a[88:95], v234
MM v[68:71],a[40:43],a[224:227],v[68:71]
v_bfe_u32 v183, v248, 6, 2
v_lshlrev_b32 v183, 5, v183
MM v[68:71],a[44:47],a[228:231],v[68:71]
v_bfe_u32 v182, v248, 4, 2
v_lshl_add_u32 v183, v182, 2, v183
MM v[76:79],a[44:47],a[236:239],v[76:79]
v_add_u32 v183, s76, v183
v_add_u32 v183, 151552, v183
MM v[76:79],a[40:43],a[232:235],v[76:79]
buffer_load_dwordx4 v231, s[8:11], s77 offen offset:3072 lds
MM v[84:87],a[40:43],a[240:243],v[84:87]
s_waitcnt vmcnt(9)
RD a[160:163],v222 offset:32768
MM v[84:87],a[44:47],a[244:247],v[84:87]
RD a[164:167],v223 offset:32768
MM v[92:95],a[44:47],a[252:255],v[92:95]
RD a[168:171],v222 offset:34816
MM v[92:95],a[40:43],a[248:251],v[92:95]
v_add_f32 v184, v184, v185
v_add_f32 v188, v188, v189
MM v[96:99],a[48:51],a[224:227],v[96:99]
TR a[32:33],v199 offset:0
TR a[34:35],v199 offset:512
MM v[96:99],a[52:55],a[228:231],v[96:99]
RD a[172:175],v223 offset:34816
ds_write_b32 v183, v184 offset:0
ds_write_b32 v183, v188 offset:16
MM v[104:107],a[52:55],a[236:239],v[104:107]
TR a[36:37],v199 offset:8224
TR a[38:39],v199 offset:8736
MM v[104:107],a[48:51],a[232:235],v[104:107]
TR a[40:41],v199 offset:16448
TR a[42:43],v199 offset:16960
MM v[112:115],a[48:51],a[240:243],v[112:115]
TR a[44:45],v199 offset:24672
TR a[46:47],v199 offset:25184
RD a[176:179],v222 offset:36864
MM v[112:115],a[52:55],a[244:247],v[112:115]
RD a[180:183],v223 offset:36864
MM v[120:123],a[52:55],a[252:255],v[120:123]
RD a[184:187],v222 offset:38912
MM v[120:123],a[48:51],a[248:251],v[120:123]
RD a[188:191],v223 offset:38912
MM v[100:103],a[56:59],a[224:227],v[100:103]
s_add_u32 s77, s64, 0
s_add_u32 s81, s77, 896
s_add_u32 m0, s65, 65536
MM v[100:103],a[60:63],a[228:231],v[100:103]
buffer_load_dwordx4 v235, s[12:15], s81 offen offset:0 lds
MM v[108:111],a[60:63],a[236:239],v[108:111]
MM v[108:111],a[56:59],a[232:235],v[108:111]
MM v[116:119],a[56:59],a[240:243],v[116:119]
MM v[116:119],a[60:63],a[244:247],v[116:119]
MM v[124:127],a[60:63],a[252:255],v[124:127]
MM v[124:127],a[56:59],a[248:251],v[124:127]
buffer_load_dwordx4 v235, s[12:15], s77 offen offset:1024 lds
s_waitcnt lgkmcnt(0)
s_barrier
MM v[0:3],a[64:67],a[128:131],v[0:3]
s_memrealtime s[52:53]
TR a[48:49],v199 offset:8
TR a[50:51],v199 offset:520
MM v[0:3],a[68:71],a[132:135],v[0:3]
s_memtime s[54:55]
TR a[52:53],v199 offset:8232
TR a[54:55],v199 offset:8744
MM v[0:3],a[72:75],a[160:163],v[0:3]
TR a[56:57],v199 offset:16456
TR a[58:59],v199 offset:16968
MM v[0:3],a[76:79],a[164:167],v[0:3]
TR a[60:61],v199 offset:24680
TR a[62:63],v199 offset:25192
MM v[8:11],a[76:79],a[172:175],v[8:11]
v_xor_b32 v151, 0, v253
v_xor_b32 v143, 0, v253
MM v[8:11],a[72:75],a[168:171],v[8:11]
RD v[144:147],v151 offset:0
RD v[148:151],v151 offset:16
MM v[8:11],a[68:71],a[140:143],v[8:11]
RD v[136:139],v143 offset:0
RD v[140:143],v143 offset:16
MM v[8:11],a[64:67],a[136:139],v[8:11]
MM v[4:7],a[80:83],a[128:131],v[4:7]
MM v[4:7],a[84:87],a[132:135],v[4:7]
s_waitcnt lgkmcnt(2)
buffer_load_dwordx4 v235, s[12:15], s81 offen offset:2048 lds
MM v[4:7],a[88:91],a[160:163],v[4:7]
s_waitcnt lgkmcnt(0)
MM v[4:7],a[92:95],a[164:167],v[4:7]
MM v[12:15],a[92:95],a[172:175],v[12:15]
MM v[12:15],a[88:91],a[168:171],v[12:15]
MM v[12:15],a[84:87],a[140:143],v[12:15]
MM v[12:15],a[80:83],a[136:139],v[12:15]
MM v[32:35],a[96:99],a[128:131],v[32:35]
MM v[32:35],a[100:103],a[132:135],v[32:35]
MM v[32:35],a[104:107],a[160:163],v[32:35]
MM v[32:35],a[108:111],a[164:167],v[32:35]
buffer_load_dwordx4 v235, s[12:15], s77 offen offset:3072 lds
v_fmac_f32 v0, v240, v144
v_fmac_f32 v1, v240, v145
v_fmac_f32 v2, v240, v146
MM v[40:43],a[108:111],a[172:175],v[40:43]
s_add_u32 s77, s64, 32768
s_add_u32 m0, s65, 32768
v_fmac_f32 v3, v240, v147
v_fmac_f32 v4, v240, v148
MM v[40:43],a[104:107],a[168:171],v[40:43]
v_fmac_f32 v5, v240, v149
v_fmac_f32 v6, v240, v150
MM v[40:43],a[100:103],a[140:143],v[40:43]
v_fmac_f32 v7, v240, v151
v_mul_f32 v0, v244, v0
v_mul_f32 v1, v244, v1
MM v[40:43],a[96:99],a[136:139],v[40:43]
v_mul_f32 v2, v244, v2
v_mul_f32 v3, v244, v3
MM v[36:39],a[112:115],a[128:131],v[36:39]
v_mul_f32 v4, v244, v4
v_mul_f32 v5, v244, v5
MM v[36:39],a[116:119],a[132:135],v[36:39]
v_mul_f32 v6, v244, v6
v_mul_f32 v7, v244, v7
CV v160,v0,v1
MM v[36:39],a[120:123],a[160:163],v[36:39]
CV v161,v2,v3
CV v162,v4,v5
MM v[36:39],a[124:127],a[164:167],v[36:39]
CV v163,v6,v7
v_fmac_f32 v8, v241, v144
MM v[44:47],a[124:127],a[172:175],v[44:47]
v_fmac_f32 v9, v241, v145
v_fmac_f32 v10, v241, v146
v_fmac_f32 v11, v241, v147
MM v[44:47],a[120:123],a[168:171],v[44:47]
buffer_load_dwordx4 v228, s[8:11], s77 offen offset:0 lds
v_fmac_f32 v12, v241, v148
v_fmac_f32 v13, v241, v149
MM v[44:47],a[116:119],a[140:143],v[44:47]
v_fmac_f32 v14, v241, v150
v_fmac_f32 v15, v241, v151
v_mul_f32 v8, v245, v8
MM v[44:47],a[112:115],a[136:139],v[44:47]
v_mul_f32 v9, v245, v9
v_mul_f32 v10, v245, v10
MM v[64:67],a[0:3],a[128:131],v[64:67]
v_mul_f32 v11, v245, v11
v_mul_f32 v12, v245, v12
MM v[64:67],a[4:7],a[132:135],v[64:67]
v_mul_f32 v13, v245, v13
v_mul_f32 v14, v245, v14
v_mul_f32 v15, v245, v15
MM v[64:67],a[8:11],a[160:163],v[64:67]
CV v164,v8,v9
CV v165,v10,v11
MM v[64:67],a[12:15],a[164:167],v[64:67]
CV v166,v12,v13
CV v167,v14,v15
MM v[72:75],a[12:15],a[172:175],v[72:75]
v_xor_b32 v159, 128, v253
MM v[72:75],a[8:11],a[168:171],v[72:75]
RD v[152:155],v159 offset:0
RD v[156:159],v159 offset:16
MM v[72:75],a[4:7],a[140:143],v[72:75]
MM v[72:75],a[0:3],a[136:139],v[72:75]
buffer_load_dwordx4 v229, s[8:11], s77 offen offset:1024 lds
MM v[68:71],a[16:19],a[128:131],v[68:71]
MM v[68:71],a[20:23],a[132:135],v[68:71]
s_waitcnt lgkmcnt(0)
v_fmac_f32 v32, v240, v152
v_fmac_f32 v33, v240, v153
v_fmac_f32 v34, v240, v154
v_fmac_f32 v35, v240, v155
v_fmac_f32 v36, v240, v156
v_fmac_f32 v37, v240, v157
v_fmac_f32 v38, v240, v158
v_fmac_f32 v39, v240, v159
v_mul_f32 v179, -v250, v244
v_fma_f32 v32, v244, v32, v179
v_fma_f32 v33, v244, v33, v179
v_fma_f32 v34, v244, v34, v179
MM v[68:71],a[24:27],a[160:163],v[68:71]
v_fma_f32 v35, v244, v35, v179
v_fma_f32 v36, v244, v36, v179
MM v[68:71],a[28:31],a[164:167],v[68:71]
v_fma_f32 v37, v244, v37, v179
v_fma_f32 v38, v244, v38, v179
v_fma_f32 v39, v244, v39, v179
MM v[76:79],a[28:31],a[172:175],v[76:79]
CV v176,v32,v33
CV v177,v34,v35
MM v[76:79],a[24:27],a[168:171],v[76:79]
CV v178,v36,v37
CV v179,v38,v39
v_fmac_f32 v40, v241, v152
MM v[76:79],a[20:23],a[140:143],v[76:79]
v_fmac_f32 v41, v241, v153
v_fmac_f32 v42, v241, v154
v_fmac_f32 v43, v241, v155
MM v[76:79],a[16:19],a[136:139],v[76:79]
v_fmac_f32 v44, v241, v156
v_fmac_f32 v45, v241, v157
MM v[96:99],a[32:35],a[128:131],v[96:99]
v_fmac_f32 v46, v241, v158
v_fmac_f32 v47, v241, v159
MM v[96:99],a[36:39],a[132:135],v[96:99]
buffer_load_dwordx4 v230, s[8:11], s77 offen offset:2048 lds
v_mul_f32 v183, -v250, v245
v_fma_f32 v40, v245, v40, v183
v_fma_f32 v41, v245, v41, v183
MM v[96:99],a[40:43],a[160:163],v[96:99]
v_fma_f32 v42, v245, v42, v183
v_fma_f32 v43, v245, v43, v183
MM v[96:99],a[44:47],a[164:167],v[96:99]
v_fma_f32 v44, v245, v44, v183
v_fma_f32 v45, v245, v45, v183
MM v[104:107],a[44:47],a[172:175],v[104:107]
)ASM"
R"ASM(
v_fma_f32 v46, v245, v46, v183
v_fma_f32 v47, v245, v47, v183
CV v180,v40,v41
MM v[104:107],a[40:43],a[168:171],v[104:107]
CV v181,v42,v43
CV v182,v44,v45
MM v[104:107],a[36:39],a[140:143],v[104:107]
CV v183,v46,v47
v_xor_b32 v151, 256, v253
MM v[104:107],a[32:35],a[136:139],v[104:107]
RD v[144:147],v151 offset:0
RD v[148:151],v151 offset:16
MM v[100:103],a[48:51],a[128:131],v[100:103]
MM v[100:103],a[52:55],a[132:135],v[100:103]
MM v[100:103],a[56:59],a[160:163],v[100:103]
MM v[100:103],a[60:63],a[164:167],v[100:103]
s_waitcnt lgkmcnt(0)
buffer_load_dwordx4 v231, s[8:11], s77 offen offset:3072 lds
v_fmac_f32 v64, v240, v144
v_fmac_f32 v65, v240, v145
v_fmac_f32 v66, v240, v146
v_fmac_f32 v67, v240, v147
v_fmac_f32 v68, v240, v148
v_fmac_f32 v69, v240, v149
v_fmac_f32 v70, v240, v150
v_fmac_f32 v71, v240, v151
v_mul_f32 v195, -v250, v244
v_fma_f32 v64, v244, v64, v195
v_fma_f32 v65, v244, v65, v195
MM v[108:111],a[60:63],a[172:175],v[108:111]
s_add_u32 s77, s64, 16384
s_add_u32 s81, s77, 896
s_add_u32 m0, s65, 81920
v_fma_f32 v66, v244, v66, v195
v_fma_f32 v67, v244, v67, v195
MM v[108:111],a[56:59],a[168:171],v[108:111]
v_fma_f32 v68, v244, v68, v195
v_fma_f32 v69, v244, v69, v195
MM v[108:111],a[52:55],a[140:143],v[108:111]
v_fma_f32 v70, v244, v70, v195
v_fma_f32 v71, v244, v71, v195
CV v192,v64,v65
MM v[108:111],a[48:51],a[136:139],v[108:111]
CV v193,v66,v67
CV v194,v68,v69
MM v[16:19],a[64:67],a[144:147],v[16:19]
CV v195,v70,v71
v_fmac_f32 v72, v241, v144
v_fmac_f32 v73, v241, v145
MM v[16:19],a[68:71],a[148:151],v[16:19]
v_fmac_f32 v74, v241, v146
v_fmac_f32 v75, v241, v147
v_fmac_f32 v76, v241, v148
MM v[16:19],a[72:75],a[176:179],v[16:19]
v_fmac_f32 v77, v241, v149
v_fmac_f32 v78, v241, v150
MM v[16:19],a[76:79],a[180:183],v[16:19]
v_fmac_f32 v79, v241, v151
v_mul_f32 v199, -v250, v245
v_fma_f32 v72, v245, v72, v199
MM v[24:27],a[76:79],a[188:191],v[24:27]
v_fma_f32 v73, v245, v73, v199
v_fma_f32 v74, v245, v74, v199
MM v[24:27],a[72:75],a[184:187],v[24:27]
buffer_load_dwordx4 v235, s[12:15], s81 offen offset:0 lds
v_fma_f32 v75, v245, v75, v199
v_fma_f32 v76, v245, v76, v199
MM v[24:27],a[68:71],a[156:159],v[24:27]
s_waitcnt vmcnt(13)
s_barrier
RD a[192:195],v224 offset:0
v_fma_f32 v77, v245, v77, v199
v_fma_f32 v78, v245, v78, v199
v_fma_f32 v79, v245, v79, v199
MM v[24:27],a[64:67],a[152:155],v[24:27]
RD a[196:199],v225 offset:0
RD a[200:203],v226 offset:0
CV v196,v72,v73
CV v197,v74,v75
MM v[20:23],a[80:83],a[144:147],v[20:23]
RD a[204:207],v227 offset:0
RD a[208:211],v224 offset:4096
CV v198,v76,v77
CV v199,v78,v79
MM v[20:23],a[84:87],a[148:151],v[20:23]
RD a[212:215],v225 offset:4096
RD a[216:219],v226 offset:4096
v_xor_b32 v159, 384, v253
MM v[20:23],a[88:91],a[176:179],v[20:23]
RD a[220:223],v227 offset:4096
RD a[224:227],v224 offset:8192
RD v[152:155],v159 offset:0
RD v[156:159],v159 offset:16
MM v[20:23],a[92:95],a[180:183],v[20:23]
RD a[228:231],v225 offset:8192
RD a[232:235],v226 offset:8192
MM v[28:31],a[92:95],a[188:191],v[28:31]
RD a[236:239],v227 offset:8192
RD a[240:243],v224 offset:12288
MM v[28:31],a[88:91],a[184:187],v[28:31]
RD a[244:247],v225 offset:12288
RD a[248:251],v226 offset:12288
MM v[28:31],a[84:87],a[156:159],v[28:31]
s_waitcnt lgkmcnt(6)
RD a[252:255],v227 offset:12288
v_fmac_f32 v96, v240, v152
v_fmac_f32 v97, v240, v153
v_fmac_f32 v98, v240, v154
v_fmac_f32 v99, v240, v155
v_fmac_f32 v100, v240, v156
v_fmac_f32 v101, v240, v157
v_fmac_f32 v102, v240, v158
v_fmac_f32 v103, v240, v159
v_mul_f32 v211, -v250, v244
v_fma_f32 v96, v244, v96, v211
v_fma_f32 v97, v244, v97, v211
v_fma_f32 v98, v244, v98, v211
MM v[28:31],a[80:83],a[152:155],v[28:31]
buffer_load_dwordx4 v235, s[12:15], s77 offen offset:1024 lds
v_fma_f32 v99, v244, v99, v211
v_fma_f32 v100, v244, v100, v211
MM v[48:51],a[96:99],a[144:147],v[48:51]
v_fma_f32 v101, v244, v101, v211
v_fma_f32 v102, v244, v102, v211
v_fma_f32 v103, v244, v103, v211
MM v[48:51],a[100:103],a[148:151],v[48:51]
CV v208,v96,v97
CV v209,v98,v99
MM v[48:51],a[104:107],a[176:179],v[48:51]
CV v210,v100,v101
CV v211,v102,v103
v_fmac_f32 v104, v241, v152
MM v[48:51],a[108:111],a[180:183],v[48:51]
v_fmac_f32 v105, v241, v153
v_fmac_f32 v106, v241, v154
v_fmac_f32 v107, v241, v155
MM v[56:59],a[108:111],a[188:191],v[56:59]
v_fmac_f32 v108, v241, v156
v_fmac_f32 v109, v241, v157
MM v[56:59],a[104:107],a[184:187],v[56:59]
v_fmac_f32 v110, v241, v158
v_fmac_f32 v111, v241, v159
MM v[56:59],a[100:103],a[156:159],v[56:59]
v_mul_f32 v215, -v250, v245
v_fma_f32 v104, v245, v104, v215
v_fma_f32 v105, v245, v105, v215
MM v[56:59],a[96:99],a[152:155],v[56:59]
v_fma_f32 v106, v245, v106, v215
v_fma_f32 v107, v245, v107, v215
MM v[52:55],a[112:115],a[144:147],v[52:55]
v_fma_f32 v108, v245, v108, v215
v_fma_f32 v109, v245, v109, v215
MM v[52:55],a[116:119],a[148:151],v[52:55]
buffer_load_dwordx4 v235, s[12:15], s81 offen offset:2048 lds
v_fma_f32 v110, v245, v110, v215
v_fma_f32 v111, v245, v111, v215
CV v212,v104,v105
MM v[52:55],a[120:123],a[176:179],v[52:55]
CV v213,v106,v107
CV v214,v108,v109
CV v215,v110,v111
MM v[52:55],a[124:127],a[180:183],v[52:55]
v_mov_b32 v241, 0
v_mov_b32 v244, 0
s_waitcnt lgkmcnt(0)
MM v[0:3],a[192:195],v[160:163],0
v_mov_b32 v144, 0
v_mov_b32 v145, 0
MM v[0:3],a[196:199],v[176:179],v[0:3]
v_mov_b32 v245, 0
v_mov_b32 v148, 0
MM v[0:3],a[200:203],v[192:195],v[0:3]
v_mov_b32 v149, 0
v_fmac_f32 v16, v242, v136
MM v[0:3],a[204:207],v[208:211],v[0:3]
v_fmac_f32 v17, v242, v137
v_fmac_f32 v18, v242, v138
MM v[8:11],a[192:195],v[164:167],0
v_fmac_f32 v19, v242, v139
v_fmac_f32 v20, v242, v140
MM v[8:11],a[196:199],v[180:183],v[8:11]
v_fmac_f32 v21, v242, v141
v_fmac_f32 v22, v242, v142
MM v[8:11],a[200:203],v[196:199],v[8:11]
v_fmac_f32 v23, v242, v143
v_mul_f32 v16, v246, v16
MM v[8:11],a[204:207],v[212:215],v[8:11]
buffer_load_dwordx4 v235, s[12:15], s77 offen offset:3072 lds
MM v[4:7],a[208:211],v[160:163],0
s_add_u32 s77, s64, 32768
s_add_u32 s81, s77, 896
s_add_u32 m0, s65, 98304
s_nop 0
LPG4_%=:
s_cbranch_execz LPG8_%=
MM v[4:7],a[212:215],v[176:179],v[4:7]
v_max3_f32 v146, v0, v1, v2
v_max_f32 v146, v146, v3
MM v[4:7],a[216:219],v[192:195],v[4:7]
v_mov_b32 v147, v146
v_mul_f32 v17, v246, v17
MM v[4:7],a[220:223],v[208:211],v[4:7]
v_permlane16_swap_b32 v146, v147
v_max_f32 v146, v146, v147
MM v[12:15],a[208:211],v[164:167],0
s_waitcnt vmcnt(8)
s_barrier
RD a[192:195],v224 offset:16384
MM v[12:15],a[212:215],v[180:183],v[12:15]
RD a[196:199],v225 offset:16384
RD a[200:203],v226 offset:16384
MM v[12:15],a[216:219],v[196:199],v[12:15]
RD a[204:207],v227 offset:16384
v_mov_b32 v147, v146
MM v[12:15],a[220:223],v[212:215],v[12:15]
s_nop 0
v_permlane32_swap_b32 v146, v147
v_max_f32 v146, v146, v147
v_sub_f32 v128, 0xc1600000, v146
v_sub_f32 v129, 0xc1600000, v146
MM v[32:35],a[224:227],v[160:163],0
RD a[208:211],v224 offset:20480
RD a[212:215],v225 offset:20480
v_sub_f32 v130, 0xc1600000, v146
v_sub_f32 v131, 0xc1600000, v146
v_max3_f32 v150, v8, v9, v10
MM v[32:35],a[228:231],v[176:179],v[32:35]
buffer_load_dwordx4 v235, s[12:15], s81 offen offset:0 lds
v_max_f32 v150, v150, v11
v_mov_b32 v151, v150
MM v[32:35],a[232:235],v[192:195],v[32:35]
RD a[216:219],v226 offset:20480
RD a[220:223],v227 offset:20480
v_permlane16_swap_b32 v150, v151
v_max_f32 v150, v150, v151
v_mov_b32 v151, v150
MM v[32:35],a[236:239],v[208:211],v[32:35]
s_nop 0
v_permlane32_swap_b32 v150, v151
v_max_f32 v150, v150, v151
v_add_f32 v0, v0, v128
v_sub_f32 v132, 0xc1600000, v150
MM v[40:43],a[224:227],v[164:167],0
v_sub_f32 v133, 0xc1600000, v150
v_sub_f32 v134, 0xc1600000, v150
v_sub_f32 v135, 0xc1600000, v150
v_add_f32 v1, v1, v128
EX v0,v0
MM v[40:43],a[228:231],v[180:183],v[40:43]
EX v1,v1
MM v[40:43],a[232:235],v[196:199],v[40:43]
v_add_f32 v2, v2, v128
v_add_f32 v3, v3, v128
MM v[40:43],a[236:239],v[212:215],v[40:43]
EX v2,v2
MM v[36:39],a[240:243],v[160:163],v[128:131]
RD a[224:227],v224 offset:24576
RD a[228:231],v225 offset:24576
EX v3,v3
MM v[36:39],a[244:247],v[176:179],v[36:39]
RD a[232:235],v226 offset:24576
RD a[236:239],v227 offset:24576
CV v0,v0,v1
v_add_f32 v4, v4, v128
MM v[36:39],a[248:251],v[192:195],v[36:39]
v_add_f32 v5, v5, v128
v_mul_f32 v18, v246, v18
EX v4,v4
MM v[36:39],a[252:255],v[208:211],v[36:39]
buffer_load_dwordx4 v235, s[12:15], s77 offen offset:1024 lds
EX v5,v5
MM v[44:47],a[240:243],v[164:167],v[132:135]
CV v1,v2,v3
v_add_f32 v6, v6, v128
MM v[44:47],a[244:247],v[180:183],v[44:47]
v_add_f32 v7, v7, v128
v_mul_f32 v19, v246, v19
MM v[44:47],a[248:251],v[196:199],v[44:47]
EX v6,v6
EX v7,v7
MM v[44:47],a[252:255],v[212:215],v[44:47]
CV v2,v4,v5
v_add_f32 v8, v8, v132
MM v[60:63],a[124:127],a[188:191],v[60:63]
RD a[240:243],v224 offset:28672
RD a[244:247],v225 offset:28672
v_add_f32 v9, v9, v132
MM v[60:63],a[120:123],a[184:187],v[60:63]
RD a[248:251],v226 offset:28672
RD a[252:255],v227 offset:28672
EX v8,v8
MM v[60:63],a[116:119],a[156:159],v[60:63]
EX v9,v9
MM v[60:63],a[112:115],a[152:155],v[60:63]
CV v3,v6,v7
v_add_f32 v10, v10, v132
MM v[80:83],a[0:3],a[144:147],v[80:83]
v_add_f32 v11, v11, v132
v_mul_f32 v20, v246, v20
EX v10,v10
MM v[80:83],a[4:7],a[148:151],v[80:83]
buffer_load_dwordx4 v235, s[12:15], s81 offen offset:2048 lds
EX v11,v11
MM v[80:83],a[8:11],a[176:179],v[80:83]
CV v8,v8,v9
v_add_f32 v12, v12, v132
MM v[80:83],a[12:15],a[180:183],v[80:83]
v_add_f32 v13, v13, v132
v_mul_f32 v21, v246, v21
EX v12,v12
MM v[88:91],a[12:15],a[188:191],v[88:91]
EX v13,v13
MM v[88:91],a[8:11],a[184:187],v[88:91]
CV v9,v10,v11
v_add_f32 v14, v14, v132
MM v[88:91],a[4:7],a[156:159],v[88:91]
v_add_f32 v15, v15, v132
v_mul_f32 v22, v246, v22
EX v14,v14
MM v[88:91],a[0:3],a[152:155],v[88:91]
EX v15,v15
MM v[84:87],a[16:19],a[144:147],v[84:87]
CV v10,v12,v13
v_add_f32 v32, v32, v128
MM v[84:87],a[20:23],a[148:151],v[84:87]
v_add_f32 v33, v33, v128
v_mul_f32 v23, v246, v23
MM v[84:87],a[24:27],a[176:179],v[84:87]
EX v32,v32
MM v[84:87],a[28:31],a[180:183],v[84:87]
buffer_load_dwordx4 v235, s[12:15], s77 offen offset:3072 lds
EX v33,v33
MM v[92:95],a[28:31],a[188:191],v[92:95]
s_add_u32 s77, s64, 49152
s_add_u32 m0, s65, 49152
CV v11,v14,v15
v_add_f32 v34, v34, v128
v_add_f32 v35, v35, v128
MM v[92:95],a[24:27],a[184:187],v[92:95]
buffer_load_dwordx4 v228, s[8:11], s77 offen offset:0 lds
EX v34,v34
MM v[92:95],a[20:23],a[156:159],v[92:95]
EX v35,v35
MM v[92:95],a[16:19],a[152:155],v[92:95]
CV v4,v32,v33
CV v168,v16,v17
EX v36,v36
MM v[112:115],a[32:35],a[144:147],v[112:115]
EX v37,v37
MM v[112:115],a[36:39],a[148:151],v[112:115]
buffer_load_dwordx4 v229, s[8:11], s77 offen offset:1024 lds
CV v5,v34,v35
MM v[112:115],a[40:43],a[176:179],v[112:115]
EX v38,v38
EX v39,v39
MM v[112:115],a[44:47],a[180:183],v[112:115]
CV v6,v36,v37
v_add_f32 v40, v40, v132
MM v[120:123],a[44:47],a[188:191],v[120:123]
v_add_f32 v41, v41, v132
CV v169,v18,v19
MM v[120:123],a[40:43],a[184:187],v[120:123]
buffer_load_dwordx4 v230, s[8:11], s77 offen offset:2048 lds
EX v40,v40
MM v[120:123],a[36:39],a[156:159],v[120:123]
EX v41,v41
MM v[120:123],a[32:35],a[152:155],v[120:123]
CV v7,v38,v39
v_add_f32 v42, v42, v132
MM v[116:119],a[48:51],a[144:147],v[116:119]
v_add_f32 v43, v43, v132
CV v170,v20,v21
EX v42,v42
MM v[116:119],a[52:55],a[148:151],v[116:119]
buffer_load_dwordx4 v231, s[8:11], s77 offen offset:3072 lds
EX v43,v43
MM v[116:119],a[56:59],a[176:179],v[116:119]
s_add_u32 s77, s64, 49152
s_add_u32 s81, s77, 896
s_add_u32 m0, s65, 114688
CV v12,v40,v41
EX v44,v44
MM v[116:119],a[60:63],a[180:183],v[116:119]
EX v45,v45
MM v[124:127],a[60:63],a[188:191],v[124:127]
CV v13,v42,v43
CV v171,v22,v23
MM v[124:127],a[56:59],a[184:187],v[124:127]
buffer_load_dwordx4 v235, s[12:15], s81 offen offset:0 lds
EX v46,v46
MM v[124:127],a[52:55],a[156:159],v[124:127]
EX v47,v47
CV v14,v44,v45
CV v15,v46,v47
MM v[124:127],a[48:51],a[152:155],v[124:127]
v_fmac_f32 v24, v243, v136
v_fmac_f32 v25, v243, v137
v_smfmac_f32_16x16x64_bf16 v[144:147], v[236:239], v[0:7], v234
v_fmac_f32 v26, v243, v138
v_smfmac_f32_16x16x64_bf16 v[148:151], v[236:239], v[8:15], v234
)ASM"
R"ASM(
s_waitcnt lgkmcnt(0)
v_fmac_f32 v27, v243, v139
buffer_load_dwordx4 v235, s[12:15], s77 offen offset:1024 lds
buffer_load_dwordx4 v235, s[12:15], s81 offen offset:2048 lds
buffer_load_dwordx4 v235, s[12:15], s77 offen offset:3072 lds
s_add_u32 s64, s64, 49152
MM v[64:67],a[192:195],v[160:163],v[128:131]
s_add_u32 s64, s64, 16384
TR a[128:129],v232 offset:0
v_fmac_f32 v28, v243, v140
MM v[64:67],a[196:199],v[176:179],v[64:67]
s_add_u32 s81, s64, 896
TR a[130:131],v232 offset:4096
v_fmac_f32 v29, v243, v141
MM v[64:67],a[200:203],v[192:195],v[64:67]
TR a[132:133],v232 offset:8192
v_fmac_f32 v30, v243, v142
MM v[64:67],a[204:207],v[208:211],v[64:67]
v_max3_f32 v249, v144, v145, v148
v_max_f32 v249, v249, v149
MM v[68:71],a[220:223],v[208:211],v[128:131]
TR a[134:135],v232 offset:12288
v_cmp_lt_f32 vcc, s71, v249
MM v[68:71],a[216:219],v[192:195],v[68:71]
s_cbranch_vccnz LSITE0_%=
LBACK0_%=:
v_add_f32 v244, v144, v145
v_fmac_f32 v31, v243, v143
MM v[68:71],a[212:215],v[176:179],v[68:71]
TR a[136:137],v233 offset:0
v_add_f32 v245, v148, v149
v_mul_f32 v24, v247, v24
MM v[68:71],a[208:211],v[160:163],v[68:71]
TR a[138:139],v233 offset:4096
v_mul_f32 v25, v247, v25
MM v[76:79],a[208:211],v[164:167],v[132:135]
TR a[140:141],v233 offset:8192
TR a[142:143],v233 offset:12288
v_mul_f32 v26, v247, v26
MM v[76:79],a[212:215],v[180:183],v[76:79]
TR a[144:145],v232 offset:512
v_mul_f32 v27, v247, v27
MM v[76:79],a[216:219],v[196:199],v[76:79]
s_waitcnt lgkmcnt(7)
TR a[146:147],v232 offset:4608
v_mul_f32 v28, v247, v28
MM v[76:79],a[220:223],v[212:215],v[76:79]
TR a[148:149],v232 offset:8704
v_mul_f32 v29, v247, v29
MM v[32:35],a[128:131],v[0:3],0
MM v[36:39],a[128:131],v[8:11],0
MM v[72:75],a[204:207],v[212:215],v[132:135]
TR a[150:151],v232 offset:12800
EX v64,v64
v_mul_f32 v30, v247, v30
MM v[72:75],a[200:203],v[196:199],v[72:75]
TR a[152:153],v233 offset:512
TR a[154:155],v233 offset:4608
v_mul_f32 v31, v247, v31
EX v65,v65
CV v172,v24,v25
MM v[72:75],a[196:199],v[180:183],v[72:75]
TR a[156:157],v233 offset:8704
CV v173,v26,v27
MM v[72:75],a[192:195],v[164:167],v[72:75]
TR a[158:159],v233 offset:12800
EX v66,v66
CV v174,v28,v29
MM v[96:99],a[224:227],v[160:163],v[128:131]
TR a[160:161],v232 offset:1024
v_max3_f32 v32, |v32|, |v33|, |v34|
v_max_f32 v32, v32, |v35|
CV v175,v30,v31
EX v67,v67
MM v[96:99],a[228:231],v[176:179],v[96:99]
TR a[162:163],v232 offset:5120
TR a[164:165],v232 offset:9216
v_bfe_u32 v32, v32, 23, 8
v_add_u32 v32, 8, v32
v_xor_b32 v143, 128, v253
MM v[96:99],a[232:235],v[192:195],v[96:99]
RD v[136:139],v143 offset:0
TR a[166:167],v232 offset:13312
v_min_u32 v32, 240, v32
v_lshl_or_b32 v241, v32, 0, v241
EX v68,v68
MM v[96:99],a[236:239],v[208:211],v[96:99]
RD v[140:143],v143 offset:16
TR a[168:169],v233 offset:1024
v_lshl_or_b32 v32, v32, 1, 1
v_lshlrev_b32 v32, 22, v32
MM v[100:103],a[252:255],v[208:211],v[128:131]
TR a[170:171],v233 offset:5120
AW a100,v32
AW a101,v32
EX v69,v69
MM v[100:103],a[248:251],v[192:195],v[100:103]
TR a[172:173],v233 offset:9216
TR a[174:175],v233 offset:13312
AW a102,v32
AW a103,v32
MM v[100:103],a[244:247],v[176:179],v[100:103]
TR a[176:177],v232 offset:1536
v_max3_f32 v36, |v36|, |v37|, |v38|
v_max_f32 v36, v36, |v39|
EX v70,v70
MM v[100:103],a[240:243],v[160:163],v[100:103]
s_waitcnt lgkmcnt(5)
TR a[178:179],v232 offset:5632
v_bfe_u32 v36, v36, 23, 8
v_add_u32 v36, 8, v36
v_fmac_f32 v48, v242, v136
v_fmac_f32 v49, v242, v137
v_fmac_f32 v50, v242, v138
v_fmac_f32 v51, v242, v139
v_fmac_f32 v52, v242, v140
EX v71,v71
v_fmac_f32 v53, v242, v141
MM v[108:111],a[240:243],v[164:167],v[132:135]
TR a[180:181],v232 offset:9728
v_min_u32 v36, 240, v36
v_lshl_or_b32 v241, v36, 8, v241
CV v64,v64,v65
v_fmac_f32 v54, v242, v142
MM v[108:111],a[244:247],v[180:183],v[108:111]
TR a[182:183],v232 offset:13824
v_lshl_or_b32 v36, v36, 1, 1
v_lshlrev_b32 v36, 22, v36
EX v72,v72
v_fmac_f32 v55, v242, v143
MM v[108:111],a[248:251],v[196:199],v[108:111]
TR a[184:185],v233 offset:1536
TR a[186:187],v233 offset:5632
TR a[188:189],v233 offset:9728
TR a[190:191],v233 offset:13824
AW a108,v36
AW a109,v36
v_mul_f32 v187, -v252, v246
EX v73,v73
MM v[108:111],a[252:255],v[212:215],v[108:111]
AW a110,v36
AW a111,v36
v_fma_f32 v48, v246, v48, v187
CV v65,v66,v67
MM v[104:107],a[236:239],v[212:215],v[132:135]
v_fma_f32 v49, v246, v49, v187
v_fma_f32 v50, v246, v50, v187
EX v74,v74
MM v[104:107],a[232:235],v[196:199],v[104:107]
v_fma_f32 v51, v246, v51, v187
CV v66,v68,v69
MM v[104:107],a[228:231],v[180:183],v[104:107]
EX v75,v75
v_fma_f32 v52, v246, v52, v187
MM v[104:107],a[224:227],v[164:167],v[104:107]
s_waitcnt vmcnt(12) lgkmcnt(0)
s_barrier
v_fma_f32 v53, v246, v53, v187
EX v76,v76
MM a[0:3],a[128:131],v[0:3],a[100:103]
s_add_u32 m0, s65, 0
v_fma_f32 v54, v246, v54, v187
CV v67,v70,v71
MM a[0:3],a[132:135],v[4:7],a[0:3]
EX v77,v77
v_fma_f32 v55, v246, v55, v187
MM a[8:11],a[132:135],v[12:15],a[108:111]
CV v184,v48,v49
CV v185,v50,v51
MM a[8:11],a[128:131],v[8:11],a[8:11]
EX v78,v78
MM a[12:15],a[136:139],v[8:11],a[108:111]
CV v186,v52,v53
CV v72,v72,v73
EX v79,v79
MM a[12:15],a[140:143],v[12:15],a[12:15]
CV v187,v54,v55
v_fmac_f32 v56, v243, v136
v_fmac_f32 v57, v243, v137
MM a[4:7],a[140:143],v[4:7],a[100:103]
EX v96,v96
v_fmac_f32 v58, v243, v138
MM a[4:7],a[136:139],v[0:3],a[4:7]
v_fmac_f32 v59, v243, v139
CV v73,v74,v75
MM a[32:35],a[144:147],v[0:3],a[100:103]
RD a[192:195],v224 offset:32768
EX v97,v97
v_fmac_f32 v60, v243, v140
MM a[32:35],a[148:151],v[4:7],a[32:35]
RD a[196:199],v225 offset:32768
buffer_load_dwordx4 v228, s[8:11], s64 offen offset:0 lds
v_fmac_f32 v61, v243, v141
EX v98,v98
MM a[40:43],a[148:151],v[12:15],a[108:111]
v_fmac_f32 v62, v243, v142
CV v74,v76,v77
MM a[40:43],a[144:147],v[8:11],a[40:43]
RD a[200:203],v226 offset:32768
v_fmac_f32 v63, v243, v143
EX v99,v99
MM a[44:47],a[152:155],v[8:11],a[108:111]
v_mul_f32 v191, -v252, v247
v_fma_f32 v56, v247, v56, v191
MM a[44:47],a[156:159],v[12:15],a[44:47]
buffer_load_dwordx4 v229, s[8:11], s64 offen offset:1024 lds
RD a[204:207],v227 offset:32768
EX v100,v100
v_fma_f32 v57, v247, v57, v191
MM a[36:39],a[156:159],v[4:7],a[100:103]
v_fma_f32 v58, v247, v58, v191
CV v75,v78,v79
EX v101,v101
MM a[36:39],a[152:155],v[0:3],a[36:39]
RD a[208:211],v224 offset:36864
v_fma_f32 v59, v247, v59, v191
MM a[64:67],a[160:163],v[0:3],a[100:103]
RD a[212:215],v225 offset:36864
v_fma_f32 v60, v247, v60, v191
EX v102,v102
MM a[64:67],a[164:167],v[4:7],a[64:67]
buffer_load_dwordx4 v230, s[8:11], s64 offen offset:2048 lds
v_fma_f32 v61, v247, v61, v191
CV v68,v96,v97
MM a[72:75],a[164:167],v[12:15],a[108:111]
EX v103,v103
v_fma_f32 v62, v247, v62, v191
MM a[72:75],a[160:163],v[8:11],a[72:75]
RD a[216:219],v226 offset:36864
v_fma_f32 v63, v247, v63, v191
EX v104,v104
CV v188,v56,v57
MM a[76:79],a[168:171],v[8:11],a[108:111]
RD a[220:223],v227 offset:36864
buffer_load_dwordx4 v231, s[8:11], s64 offen offset:3072 lds
CV v189,v58,v59
MM a[76:79],a[172:175],v[12:15],a[76:79]
s_add_u32 m0, s65, 65536
EX v105,v105
CV v190,v60,v61
CV v69,v98,v99
MM a[68:71],a[172:175],v[4:7],a[100:103]
RD a[224:227],v224 offset:40960
CV v191,v62,v63
MM a[68:71],a[168:171],v[0:3],a[68:71]
EX v106,v106
v_xor_b32 v143, 256, v253
MM a[96:99],a[176:179],v[0:3],a[100:103]
RD v[136:139],v143 offset:0
RD a[228:231],v225 offset:40960
buffer_load_dwordx4 v235, s[12:15], s81 offen offset:0 lds
EX v107,v107
CV v70,v100,v101
MM a[96:99],a[180:183],v[4:7],a[96:99]
RD v[140:143],v143 offset:16
RD a[232:235],v226 offset:40960
MM a[104:107],a[180:183],v[12:15],a[108:111]
EX v108,v108
MM a[104:107],a[176:179],v[8:11],a[104:107]
RD a[236:239],v227 offset:40960
CV v71,v102,v103
MM a[108:111],a[184:187],v[8:11],a[108:111]
buffer_load_dwordx4 v235, s[12:15], s64 offen offset:1024 lds
EX v109,v109
MM a[108:111],a[188:191],v[12:15],a[108:111]
s_waitcnt lgkmcnt(2)
v_fmac_f32 v80, v242, v136
v_fmac_f32 v81, v242, v137
v_fmac_f32 v82, v242, v138
v_fmac_f32 v83, v242, v139
v_fmac_f32 v84, v242, v140
EX v110,v110
v_fmac_f32 v85, v242, v141
MM a[100:103],a[188:191],v[4:7],a[100:103]
RD a[240:243],v224 offset:45056
RD a[244:247],v225 offset:45056
RD a[248:251],v226 offset:45056
RD a[252:255],v227 offset:45056
EX v111,v111
v_fmac_f32 v86, v242, v142
MM a[100:103],a[184:187],v[0:3],a[100:103]
v_fmac_f32 v87, v242, v143
CV v76,v104,v105
v_smfmac_f32_16x16x64_bf16 v[144:147], v[236:239], v[64:71], v234
buffer_load_dwordx4 v235, s[12:15], s81 offen offset:2048 lds
CV v77,v106,v107
CV v78,v108,v109
CV v79,v110,v111
v_mul_f32 v203, -v252, v246
s_nop 0
v_smfmac_f32_16x16x64_bf16 v[148:151], v[236:239], v[72:79], v234
s_waitcnt lgkmcnt(0)
v_fma_f32 v80, v246, v80, v203
buffer_load_dwordx4 v235, s[12:15], s64 offen offset:3072 lds
MM v[0:3],a[192:195],v[160:163],v[128:131]
s_add_u32 s64, s64, 16384
v_fma_f32 v81, v246, v81, v203
v_fma_f32 v82, v246, v82, v203
MM v[0:3],a[196:199],v[176:179],v[0:3]
s_add_u32 s81, s64, 896
v_fma_f32 v83, v246, v83, v203
v_fma_f32 v84, v246, v84, v203
MM v[0:3],a[200:203],v[192:195],v[0:3]
TR a[128:129],v232 offset:16384
v_fma_f32 v85, v246, v85, v203
MM v[0:3],a[204:207],v[208:211],v[0:3]
v_max3_f32 v249, v144, v145, v148
v_max_f32 v249, v249, v149
MM v[4:7],a[220:223],v[208:211],v[128:131]
TR a[130:131],v232 offset:20480
TR a[132:133],v232 offset:24576
v_cmp_lt_f32 vcc, s71, v249
v_fma_f32 v86, v246, v86, v203
MM v[4:7],a[216:219],v[192:195],v[4:7]
s_cbranch_vccnz LSITE1_%=
LBACK1_%=:
TR a[134:135],v232 offset:28672
v_add_f32 v244, v144, v145
v_fma_f32 v87, v246, v87, v203
MM v[4:7],a[212:215],v[176:179],v[4:7]
TR a[136:137],v233 offset:16384
v_add_f32 v245, v148, v149
CV v200,v80,v81
MM v[4:7],a[208:211],v[160:163],v[4:7]
TR a[138:139],v233 offset:20480
CV v201,v82,v83
MM v[12:15],a[208:211],v[164:167],v[132:135]
TR a[140:141],v233 offset:24576
TR a[142:143],v233 offset:28672
CV v202,v84,v85
MM v[12:15],a[212:215],v[180:183],v[12:15]
TR a[144:145],v232 offset:16896
CV v203,v86,v87
v_fmac_f32 v88, v243, v136
v_fmac_f32 v89, v243, v137
MM v[12:15],a[216:219],v[196:199],v[12:15]
TR a[146:147],v232 offset:20992
v_fmac_f32 v90, v243, v138
MM v[12:15],a[220:223],v[212:215],v[12:15]
TR a[148:149],v232 offset:25088
v_fmac_f32 v91, v243, v139
MM v[8:11],a[204:207],v[212:215],v[132:135]
TR a[150:151],v232 offset:29184
EX v0,v0
v_fmac_f32 v92, v243, v140
MM v[8:11],a[200:203],v[196:199],v[8:11]
TR a[152:153],v233 offset:16896
TR a[154:155],v233 offset:20992
v_fmac_f32 v93, v243, v141
EX v1,v1
MM v[8:11],a[196:199],v[180:183],v[8:11]
TR a[156:157],v233 offset:25088
v_fmac_f32 v94, v243, v142
MM v[8:11],a[192:195],v[164:167],v[8:11]
TR a[158:159],v233 offset:29184
v_fmac_f32 v95, v243, v143
EX v2,v2
v_mul_f32 v207, -v252, v247
MM v[32:35],a[224:227],v[160:163],v[128:131]
TR a[160:161],v232 offset:17408
v_fma_f32 v88, v247, v88, v207
EX v3,v3
MM v[32:35],a[228:231],v[176:179],v[32:35]
TR a[162:163],v232 offset:21504
TR a[164:165],v232 offset:25600
v_fma_f32 v89, v247, v89, v207
MM v[32:35],a[232:235],v[192:195],v[32:35]
TR a[166:167],v232 offset:29696
v_fma_f32 v90, v247, v90, v207
EX v4,v4
MM v[32:35],a[236:239],v[208:211],v[32:35]
TR a[168:169],v233 offset:17408
v_fma_f32 v91, v247, v91, v207
MM v[36:39],a[252:255],v[208:211],v[128:131]
TR a[170:171],v233 offset:21504
EX v5,v5
v_fma_f32 v92, v247, v92, v207
MM v[36:39],a[248:251],v[192:195],v[36:39]
TR a[172:173],v233 offset:25600
TR a[174:175],v233 offset:29696
v_fma_f32 v93, v247, v93, v207
MM v[36:39],a[244:247],v[176:179],v[36:39]
TR a[176:177],v232 offset:17920
EX v6,v6
v_fma_f32 v94, v247, v94, v207
v_fma_f32 v95, v247, v95, v207
MM v[36:39],a[240:243],v[160:163],v[36:39]
TR a[178:179],v232 offset:22016
CV v204,v88,v89
EX v7,v7
MM v[44:47],a[240:243],v[164:167],v[132:135]
TR a[180:181],v232 offset:26112
CV v205,v90,v91
CV v0,v0,v1
MM v[44:47],a[244:247],v[180:183],v[44:47]
TR a[182:183],v232 offset:30208
CV v206,v92,v93
EX v8,v8
MM v[44:47],a[248:251],v[196:199],v[44:47]
TR a[184:185],v233 offset:17920
TR a[186:187],v233 offset:22016
TR a[188:189],v233 offset:26112
TR a[190:191],v233 offset:30208
CV v207,v94,v95
)ASM"
R"ASM(
EX v9,v9
s_nop 0
LPG5_%=:
s_cbranch_execz LPG9_%=
MM v[44:47],a[252:255],v[212:215],v[44:47]
v_xor_b32 v143, 384, v253
CV v1,v2,v3
MM v[40:43],a[236:239],v[212:215],v[132:135]
RD v[136:139],v143 offset:0
RD v[140:143],v143 offset:16
EX v10,v10
MM v[40:43],a[232:235],v[196:199],v[40:43]
EX v11,v11
MM v[40:43],a[228:231],v[180:183],v[40:43]
EX v12,v12
CV v2,v4,v5
MM v[40:43],a[224:227],v[164:167],v[40:43]
s_waitcnt vmcnt(12) lgkmcnt(0)
s_barrier
CV v3,v6,v7
MM a[0:3],a[128:131],v[64:67],a[0:3]
v_fmac_f32 v112, v242, v136
v_fmac_f32 v113, v242, v137
v_fmac_f32 v114, v242, v138
v_fmac_f32 v115, v242, v139
v_fmac_f32 v116, v242, v140
MM a[0:3],a[132:135],v[68:71],a[0:3]
s_add_u32 m0, s65, 16384
EX v13,v13
v_fmac_f32 v117, v242, v141
MM a[8:11],a[132:135],v[76:79],a[8:11]
v_fmac_f32 v118, v242, v142
v_fmac_f32 v119, v242, v143
MM a[8:11],a[128:131],v[72:75],a[8:11]
EX v14,v14
MM a[12:15],a[136:139],v[72:75],a[12:15]
v_mul_f32 v219, -v252, v246
CV v8,v8,v9
EX v15,v15
MM a[12:15],a[140:143],v[76:79],a[12:15]
v_fma_f32 v112, v246, v112, v219
v_fma_f32 v113, v246, v113, v219
MM a[4:7],a[140:143],v[68:71],a[4:7]
EX v32,v32
MM a[4:7],a[136:139],v[64:67],a[4:7]
v_fma_f32 v114, v246, v114, v219
v_fma_f32 v115, v246, v115, v219
MM a[32:35],a[144:147],v[64:67],a[32:35]
RD a[192:195],v224 offset:49152
EX v33,v33
CV v9,v10,v11
v_fma_f32 v116, v246, v116, v219
MM a[32:35],a[148:151],v[68:71],a[32:35]
buffer_load_dwordx4 v228, s[8:11], s64 offen offset:0 lds
v_fma_f32 v117, v246, v117, v219
EX v34,v34
MM a[40:43],a[148:151],v[76:79],a[40:43]
RD a[196:199],v225 offset:49152
v_fma_f32 v118, v246, v118, v219
MM a[40:43],a[144:147],v[72:75],a[40:43]
RD a[200:203],v226 offset:49152
EX v35,v35
v_fma_f32 v119, v246, v119, v219
CV v10,v12,v13
MM a[44:47],a[152:155],v[72:75],a[44:47]
CV v216,v112,v113
CV v217,v114,v115
MM a[44:47],a[156:159],v[76:79],a[44:47]
buffer_load_dwordx4 v229, s[8:11], s64 offen offset:1024 lds
RD a[204:207],v227 offset:49152
EX v36,v36
MM a[36:39],a[156:159],v[68:71],a[36:39]
CV v218,v116,v117
CV v11,v14,v15
EX v37,v37
CV v219,v118,v119
v_fmac_f32 v120, v243, v136
MM a[36:39],a[152:155],v[64:67],a[36:39]
RD a[208:211],v224 offset:53248
v_fmac_f32 v121, v243, v137
MM a[64:67],a[160:163],v[64:67],a[64:67]
EX v38,v38
v_fmac_f32 v122, v243, v138
MM a[64:67],a[164:167],v[68:71],a[64:67]
buffer_load_dwordx4 v230, s[8:11], s64 offen offset:2048 lds
RD a[212:215],v225 offset:53248
v_fmac_f32 v123, v243, v139
MM a[72:75],a[164:167],v[76:79],a[72:75]
EX v39,v39
v_fmac_f32 v124, v243, v140
CV v4,v32,v33
MM a[72:75],a[160:163],v[72:75],a[72:75]
RD a[216:219],v226 offset:53248
v_fmac_f32 v125, v243, v141
EX v40,v40
MM a[76:79],a[168:171],v[72:75],a[76:79]
RD a[220:223],v227 offset:53248
buffer_load_dwordx4 v231, s[8:11], s64 offen offset:3072 lds
v_fmac_f32 v126, v243, v142
MM a[76:79],a[172:175],v[76:79],a[76:79]
s_add_u32 m0, s65, 81920
v_fmac_f32 v127, v243, v143
CV v5,v34,v35
EX v41,v41
v_mul_f32 v223, -v252, v247
MM a[68:71],a[172:175],v[68:71],a[68:71]
RD a[224:227],v224 offset:57344
v_fma_f32 v120, v247, v120, v223
MM a[68:71],a[168:171],v[64:67],a[68:71]
EX v42,v42
v_fma_f32 v121, v247, v121, v223
MM a[96:99],a[176:179],v[64:67],a[96:99]
RD a[228:231],v225 offset:57344
buffer_load_dwordx4 v235, s[12:15], s81 offen offset:0 lds
v_fma_f32 v122, v247, v122, v223
EX v43,v43
CV v6,v36,v37
MM a[96:99],a[180:183],v[68:71],a[96:99]
RD a[232:235],v226 offset:57344
v_fma_f32 v123, v247, v123, v223
MM a[104:107],a[180:183],v[76:79],a[104:107]
v_fma_f32 v124, v247, v124, v223
CV v7,v38,v39
EX v44,v44
MM a[104:107],a[176:179],v[72:75],a[104:107]
v_fma_f32 v125, v247, v125, v223
v_fma_f32 v126, v247, v126, v223
MM a[108:111],a[184:187],v[72:75],a[108:111]
RD a[236:239],v227 offset:57344
buffer_load_dwordx4 v235, s[12:15], s64 offen offset:1024 lds
EX v45,v45
v_fma_f32 v127, v247, v127, v223
MM a[108:111],a[188:191],v[76:79],a[108:111]
EX v46,v46
CV v220,v120,v121
MM a[100:103],a[188:191],v[68:71],a[100:103]
RD a[240:243],v224 offset:61440
RD a[244:247],v225 offset:61440
RD a[248:251],v226 offset:61440
RD a[252:255],v227 offset:61440
CV v221,v122,v123
EX v47,v47
CV v222,v124,v125
CV v223,v126,v127
MM a[100:103],a[184:187],v[64:67],a[100:103]
buffer_load_dwordx4 v235, s[12:15], s81 offen offset:2048 lds
CV v12,v40,v41
v_smfmac_f32_16x16x64_bf16 v[144:147], v[236:239], v[0:7], v234
CV v13,v42,v43
CV v14,v44,v45
CV v15,v46,v47
s_nop 1
v_smfmac_f32_16x16x64_bf16 v[148:151], v[236:239], v[8:15], v234
s_waitcnt lgkmcnt(0)
buffer_load_dwordx4 v235, s[12:15], s64 offen offset:3072 lds
MM v[64:67],a[192:195],v[160:163],v[128:131]
s_add_u32 s64, s64, 16384
TR a[128:129],v232 offset:32768
TR a[130:131],v232 offset:36864
MM v[64:67],a[196:199],v[176:179],v[64:67]
s_add_u32 s81, s64, 896
TR a[132:133],v232 offset:40960
TR a[134:135],v232 offset:45056
MM v[64:67],a[200:203],v[192:195],v[64:67]
TR a[136:137],v233 offset:32768
TR a[138:139],v233 offset:36864
MM v[64:67],a[204:207],v[208:211],v[64:67]
v_max3_f32 v249, v144, v145, v148
v_max_f32 v249, v249, v149
MM v[68:71],a[220:223],v[208:211],v[128:131]
TR a[140:141],v233 offset:40960
v_cmp_lt_f32 vcc, s71, v249
MM v[68:71],a[216:219],v[192:195],v[68:71]
s_cbranch_vccnz LSITE2_%=
LBACK2_%=:
TR a[142:143],v233 offset:45056
v_add_f32 v244, v144, v145
MM v[68:71],a[212:215],v[176:179],v[68:71]
TR a[144:145],v232 offset:33280
v_add_f32 v245, v148, v149
MM v[68:71],a[208:211],v[160:163],v[68:71]
TR a[146:147],v232 offset:37376
TR a[148:149],v232 offset:41472
MM v[76:79],a[208:211],v[164:167],v[132:135]
s_lshl_b32 s90, %[swave], 10
s_add_u32 s90, s90, 81920
TR a[150:151],v232 offset:45568
MM v[76:79],a[212:215],v[180:183],v[76:79]
buffer_load_dwordx4 v[112:115], v248, s[20:23], s90 offen offset:0 sc1
MM v[76:79],a[216:219],v[196:199],v[76:79]
EX v64,v64
MM v[76:79],a[220:223],v[212:215],v[76:79]
EX v65,v65
MM v[72:75],a[204:207],v[212:215],v[132:135]
TR a[152:153],v233 offset:33280
CV v64,v64,v65
MM v[72:75],a[200:203],v[196:199],v[72:75]
EX v66,v66
MM v[72:75],a[196:199],v[180:183],v[72:75]
TR a[154:155],v233 offset:37376
TR a[156:157],v233 offset:41472
MM v[72:75],a[192:195],v[164:167],v[72:75]
EX v67,v67
MM v[96:99],a[224:227],v[160:163],v[128:131]
TR a[158:159],v233 offset:45568
CV v65,v66,v67
MM v[96:99],a[228:231],v[176:179],v[96:99]
EX v68,v68
MM v[96:99],a[232:235],v[192:195],v[96:99]
TR a[160:161],v232 offset:33792
TR a[162:163],v232 offset:37888
MM v[96:99],a[236:239],v[208:211],v[96:99]
EX v69,v69
MM v[100:103],a[252:255],v[208:211],v[128:131]
TR a[164:165],v232 offset:41984
TR a[166:167],v232 offset:46080
MM v[100:103],a[248:251],v[192:195],v[100:103]
EX v70,v70
MM v[100:103],a[244:247],v[176:179],v[100:103]
TR a[168:169],v233 offset:33792
CV v66,v68,v69
MM v[100:103],a[240:243],v[160:163],v[100:103]
EX v71,v71
MM v[108:111],a[240:243],v[164:167],v[132:135]
TR a[170:171],v233 offset:37888
TR a[172:173],v233 offset:41984
MM v[108:111],a[244:247],v[180:183],v[108:111]
EX v72,v72
MM v[108:111],a[248:251],v[196:199],v[108:111]
TR a[174:175],v233 offset:46080
TR a[176:177],v232 offset:34304
MM v[108:111],a[252:255],v[212:215],v[108:111]
EX v73,v73
MM v[104:107],a[236:239],v[212:215],v[132:135]
TR a[178:179],v232 offset:38400
CV v67,v70,v71
MM v[104:107],a[232:235],v[196:199],v[104:107]
TR a[180:181],v232 offset:42496
TR a[182:183],v232 offset:46592
MM v[104:107],a[228:231],v[180:183],v[104:107]
EX v74,v74
MM v[104:107],a[224:227],v[164:167],v[104:107]
s_memrealtime s[56:57]
EX v75,v75
MM v[80:83],a[192:195],v[168:171],0
s_memtime s[58:59]
TR a[184:185],v233 offset:34304
TR a[186:187],v233 offset:38400
MM v[80:83],a[196:199],v[184:187],v[80:83]
EX v76,v76
MM v[80:83],a[200:203],v[200:203],v[80:83]
TR a[188:189],v233 offset:42496
TR a[190:191],v233 offset:46592
MM v[80:83],a[204:207],v[216:219],v[80:83]
EX v77,v77
MM v[88:91],a[192:195],v[172:175],0
EX v78,v78
MM v[88:91],a[196:199],v[188:191],v[88:91]
EX v79,v79
MM v[88:91],a[200:203],v[204:207],v[88:91]
EX v96,v96
MM v[88:91],a[204:207],v[220:223],v[88:91]
s_waitcnt vmcnt(13) lgkmcnt(0)
s_barrier
CV v72,v72,v73
MM a[0:3],a[128:131],v[0:3],a[0:3]
s_sub_u32 s52, s56, s52
s_sub_u32 s54, s58, s54
v_cvt_f32_u32 v250, s52
MM a[0:3],a[132:135],v[4:7],a[0:3]
s_add_u32 m0, s65, 32768
v_cvt_f32_u32 v251, s54
CV v73,v74,v75
MM a[8:11],a[132:135],v[12:15],a[8:11]
v_rcp_f32 v251, v251
MM a[8:11],a[128:131],v[8:11],a[8:11]
v_mul_f32 v250, 0x47abe000, v250
s_lshl_b32 s52, %[swave], 11
MM a[12:15],a[136:139],v[8:11],a[12:15]
v_mul_f32 v250, v250, v251
s_lshl_b32 s54, %[sb], 3
EX v97,v97
MM a[12:15],a[140:143],v[12:15],a[12:15]
v_cvt_u32_f32 v250, v250
s_add_u32 s52, s52, s54
v_max3_f32 v154, v80, v81, v82
MM a[4:7],a[140:143],v[4:7],a[4:7]
v_add_u32 v250, s56, v250
v_mov_b32 v251, s69
v_max_f32 v154, v154, v83
EX v98,v98
MM a[4:7],a[136:139],v[0:3],a[4:7]
s_add_u32 s52, s52, 86016
s_mov_b64 exec, 1
buffer_store_dwordx2 v[250:251], off, s[20:23], s52 sc1
s_mov_b64 exec, -1
s_nop 0
v_mov_b32 v155, v154
MM a[32:35],a[144:147],v[0:3],a[32:35]
RD a[192:195],v224 offset:0
v_permlane16_swap_b32 v154, v155
EX v99,v99
CV v74,v76,v77
v_max_f32 v154, v154, v155
MM a[32:35],a[148:151],v[4:7],a[32:35]
buffer_load_dwordx4 v228, s[8:11], s64 offen offset:0 lds
v_mov_b32 v155, v154
MM a[40:43],a[148:151],v[12:15],a[40:43]
RD a[196:199],v225 offset:0
v_permlane32_swap_b32 v154, v155
EX v100,v100
MM a[40:43],a[144:147],v[8:11],a[40:43]
RD a[200:203],v226 offset:0
v_max_f32 v154, v154, v155
CV v75,v78,v79
MM a[44:47],a[152:155],v[8:11],a[44:47]
v_sub_f32 v136, 0xc1600000, v154
v_sub_f32 v137, 0xc1600000, v154
EX v101,v101
MM a[44:47],a[156:159],v[12:15],a[44:47]
buffer_load_dwordx4 v229, s[8:11], s64 offen offset:1024 lds
RD a[204:207],v227 offset:0
MM a[36:39],a[156:159],v[4:7],a[36:39]
v_sub_f32 v138, 0xc1600000, v154
v_sub_f32 v139, 0xc1600000, v154
EX v102,v102
MM a[36:39],a[152:155],v[0:3],a[36:39]
RD a[208:211],v224 offset:4096
v_mov_b32 v246, 0
MM a[64:67],a[160:163],v[0:3],a[64:67]
EX v103,v103
v_mov_b32 v152, 0
CV v68,v96,v97
MM a[64:67],a[164:167],v[4:7],a[64:67]
buffer_load_dwordx4 v230, s[8:11], s64 offen offset:2048 lds
RD a[212:215],v225 offset:4096
v_mov_b32 v153, 0
MM a[72:75],a[164:167],v[12:15],a[72:75]
EX v104,v104
v_max3_f32 v158, v88, v89, v90
MM a[72:75],a[160:163],v[8:11],a[72:75]
RD a[216:219],v226 offset:4096
v_max_f32 v158, v158, v91
CV v69,v98,v99
MM a[76:79],a[168:171],v[8:11],a[76:79]
buffer_load_dwordx4 v231, s[8:11], s64 offen offset:3072 lds
RD a[220:223],v227 offset:4096
EX v105,v105
v_mov_b32 v159, v158
MM a[76:79],a[172:175],v[12:15],a[76:79]
s_add_u32 m0, s65, 98304
v_permlane16_swap_b32 v158, v159
v_max_f32 v158, v158, v159
MM a[68:71],a[172:175],v[4:7],a[68:71]
RD a[224:227],v224 offset:8192
EX v106,v106
v_mov_b32 v159, v158
MM a[68:71],a[168:171],v[0:3],a[68:71]
s_nop 0
v_permlane32_swap_b32 v158, v159
CV v70,v100,v101
EX v107,v107
MM a[96:99],a[176:179],v[0:3],a[96:99]
RD a[228:231],v225 offset:8192
buffer_load_dwordx4 v235, s[12:15], s81 offen offset:0 lds
v_max_f32 v158, v158, v159
MM a[96:99],a[180:183],v[4:7],a[96:99]
v_sub_f32 v140, 0xc1600000, v158
CV v71,v102,v103
EX v108,v108
MM a[104:107],a[180:183],v[12:15],a[104:107]
RD a[232:235],v226 offset:8192
v_sub_f32 v141, 0xc1600000, v158
MM a[104:107],a[176:179],v[8:11],a[104:107]
v_sub_f32 v142, 0xc1600000, v158
v_sub_f32 v143, 0xc1600000, v158
EX v109,v109
MM a[108:111],a[184:187],v[8:11],a[108:111]
RD a[236:239],v227 offset:8192
buffer_load_dwordx4 v235, s[12:15], s64 offen offset:1024 lds
v_mov_b32 v247, 0
v_mov_b32 v156, 0
v_mov_b32 v157, 0
MM a[108:111],a[188:191],v[12:15],a[108:111]
EX v110,v110
MM a[100:103],a[188:191],v[4:7],a[100:103]
RD a[240:243],v224 offset:12288
RD a[244:247],v225 offset:12288
RD a[248:251],v226 offset:12288
RD a[252:255],v227 offset:12288
EX v111,v111
MM a[100:103],a[184:187],v[0:3],a[100:103]
buffer_load_dwordx4 v235, s[12:15], s81 offen offset:2048 lds
CV v76,v104,v105
v_smfmac_f32_16x16x64_bf16 v[144:147], v[236:239], v[64:71], v234
CV v77,v106,v107
CV v78,v108,v109
CV v79,v110,v111
s_nop 1
v_smfmac_f32_16x16x64_bf16 v[148:151], v[236:239], v[72:79], v234
)ASM"
R"ASM(
s_waitcnt lgkmcnt(0)
buffer_load_dwordx4 v235, s[12:15], s64 offen offset:3072 lds
MM v[0:3],a[192:195],v[160:163],v[128:131]
s_add_u32 s64, s64, 16384
TR a[128:129],v232 offset:49152
s_add_u32 s81, s64, 896
MM v[0:3],a[196:199],v[176:179],v[0:3]
TR a[130:131],v232 offset:53248
TR a[132:133],v232 offset:57344
MM v[0:3],a[200:203],v[192:195],v[0:3]
TR a[134:135],v232 offset:61440
TR a[136:137],v233 offset:49152
MM v[0:3],a[204:207],v[208:211],v[0:3]
v_max3_f32 v249, v144, v145, v148
v_max_f32 v249, v249, v149
MM v[4:7],a[220:223],v[208:211],v[128:131]
TR a[138:139],v233 offset:53248
v_cmp_lt_f32 vcc, s71, v249
MM v[4:7],a[216:219],v[192:195],v[4:7]
s_cbranch_vccnz LSITE3_%=
LBACK3_%=:
TR a[140:141],v233 offset:57344
v_add_f32 v244, v144, v145
MM v[4:7],a[212:215],v[176:179],v[4:7]
TR a[142:143],v233 offset:61440
v_add_f32 v245, v148, v149
MM v[4:7],a[208:211],v[160:163],v[4:7]
TR a[144:145],v232 offset:49664
TR a[146:147],v232 offset:53760
MM v[12:15],a[208:211],v[164:167],v[132:135]
EX v0,v0
MM v[12:15],a[212:215],v[180:183],v[12:15]
EX v1,v1
MM v[12:15],a[216:219],v[196:199],v[12:15]
TR a[148:149],v232 offset:57856
CV v0,v0,v1
MM v[12:15],a[220:223],v[212:215],v[12:15]
EX v2,v2
MM v[8:11],a[204:207],v[212:215],v[132:135]
EX v3,v3
MM v[8:11],a[200:203],v[196:199],v[8:11]
TR a[150:151],v232 offset:61952
CV v1,v2,v3
MM v[8:11],a[196:199],v[180:183],v[8:11]
EX v4,v4
MM v[8:11],a[192:195],v[164:167],v[8:11]
EX v5,v5
MM v[16:19],a[192:195],v[168:171],v[136:139]
TR a[152:153],v233 offset:49664
CV v2,v4,v5
MM v[16:19],a[196:199],v[184:187],v[16:19]
EX v6,v6
MM v[16:19],a[200:203],v[200:203],v[16:19]
EX v7,v7
MM v[16:19],a[204:207],v[216:219],v[16:19]
TR a[154:155],v233 offset:53760
CV v3,v6,v7
MM v[20:23],a[220:223],v[216:219],v[136:139]
s_waitcnt vmcnt(9)
v_xor_b32 v112, s69, v112
v_xor_b32 v113, s69, v113
MM v[20:23],a[216:219],v[200:203],v[20:23]
v_xor_b32 v114, s69, v114
v_xor_b32 v115, s69, v115
MM v[20:23],a[212:215],v[184:187],v[20:23]
v_or3_b32 v252, v112, v113, v114
v_or_b32 v252, v252, v115
EX v8,v8
MM v[20:23],a[208:211],v[168:171],v[20:23]
v_cmp_ne_u32 vcc, 0, v252
s_cbranch_vccnz LRFR_%=
LRFRb_%=:
EX v9,v9
MM v[28:31],a[208:211],v[172:175],v[140:143]
s_lshl_b32 s90, %[swave], 11
s_add_u32 s78, s90, %[slds]
s_add_u32 s78, s78, 155648
MM v[28:31],a[212:215],v[188:191],v[28:31]
s_add_u32 s90, s90, 53248
s_mov_b32 m0, s78
CV v8,v8,v9
EX v10,v10
MM v[28:31],a[216:219],v[204:207],v[28:31]
buffer_load_dwordx4 v248, s[20:23], s90 offen offset:0 sc1 lds
EX v11,v11
MM v[28:31],a[220:223],v[220:223],v[28:31]
buffer_load_dwordx4 v248, s[20:23], s90 offen offset:1024 sc1 lds
EX v12,v12
MM v[24:27],a[204:207],v[220:223],v[140:143]
EX v13,v13
MM v[24:27],a[200:203],v[204:207],v[24:27]
EX v14,v14
MM v[24:27],a[196:199],v[188:191],v[24:27]
TR a[156:157],v233 offset:57856
CV v9,v10,v11
MM v[24:27],a[192:195],v[172:175],v[24:27]
EX v15,v15
MM v[32:35],a[224:227],v[160:163],v[128:131]
EX v16,v16
MM v[32:35],a[228:231],v[176:179],v[32:35]
TR a[158:159],v233 offset:61952
TR a[160:161],v232 offset:50176
MM v[32:35],a[232:235],v[192:195],v[32:35]
EX v17,v17
MM v[32:35],a[236:239],v[208:211],v[32:35]
EX v18,v18
MM v[36:39],a[252:255],v[208:211],v[128:131]
TR a[162:163],v232 offset:54272
CV v10,v12,v13
MM v[36:39],a[248:251],v[192:195],v[36:39]
EX v19,v19
MM v[36:39],a[244:247],v[176:179],v[36:39]
EX v20,v20
MM v[36:39],a[240:243],v[160:163],v[36:39]
TR a[164:165],v232 offset:58368
CV v11,v14,v15
MM v[44:47],a[240:243],v[164:167],v[132:135]
EX v21,v21
MM v[44:47],a[244:247],v[180:183],v[44:47]
TR a[166:167],v232 offset:62464
EX v22,v22
MM v[44:47],a[248:251],v[196:199],v[44:47]
EX v23,v23
MM v[44:47],a[252:255],v[212:215],v[44:47]
TR a[168:169],v233 offset:50176
CV v16,v16,v17
MM v[40:43],a[236:239],v[212:215],v[132:135]
EX v24,v24
MM v[40:43],a[232:235],v[196:199],v[40:43]
TR a[170:171],v233 offset:54272
EX v25,v25
MM v[40:43],a[228:231],v[180:183],v[40:43]
TR a[172:173],v233 offset:58368
CV v17,v18,v19
EX v26,v26
MM v[40:43],a[224:227],v[164:167],v[40:43]
TR a[174:175],v233 offset:62464
CV v18,v20,v21
MM v[48:51],a[224:227],v[168:171],v[136:139]
EX v27,v27
MM v[48:51],a[228:231],v[184:187],v[48:51]
EX v28,v28
MM v[48:51],a[232:235],v[200:203],v[48:51]
TR a[176:177],v232 offset:50688
EX v29,v29
MM v[48:51],a[236:239],v[216:219],v[48:51]
TR a[178:179],v232 offset:54784
CV v19,v22,v23
MM v[52:55],a[252:255],v[216:219],v[136:139]
EX v30,v30
MM v[52:55],a[248:251],v[200:203],v[52:55]
EX v31,v31
MM v[52:55],a[244:247],v[184:187],v[52:55]
TR a[180:181],v232 offset:58880
CV v24,v24,v25
MM v[52:55],a[240:243],v[168:171],v[52:55]
EX v32,v32
MM v[60:63],a[240:243],v[172:175],v[140:143]
TR a[182:183],v232 offset:62976
EX v33,v33
CV v25,v26,v27
MM v[60:63],a[244:247],v[188:191],v[60:63]
EX v34,v34
MM v[60:63],a[248:251],v[204:207],v[60:63]
TR a[184:185],v233 offset:50688
TR a[186:187],v233 offset:54784
TR a[188:189],v233 offset:58880
TR a[190:191],v233 offset:62976
MM v[60:63],a[252:255],v[220:223],v[60:63]
EX v35,v35
CV v26,v28,v29
MM v[56:59],a[236:239],v[220:223],v[140:143]
EX v36,v36
MM v[56:59],a[232:235],v[204:207],v[56:59]
EX v37,v37
CV v27,v30,v31
MM v[56:59],a[228:231],v[188:191],v[56:59]
EX v38,v38
MM v[56:59],a[224:227],v[172:175],v[56:59]
s_waitcnt lgkmcnt(0)
s_barrier
CV v4,v32,v33
MM a[0:3],a[128:131],v[64:67],a[0:3]
s_add_u32 m0, s65, 49152
EX v39,v39
MM a[0:3],a[132:135],v[68:71],a[0:3]
EX v40,v40
MM a[8:11],a[132:135],v[76:79],a[8:11]
CV v5,v34,v35
CV v6,v36,v37
MM a[8:11],a[128:131],v[72:75],a[8:11]
EX v41,v41
MM a[12:15],a[136:139],v[72:75],a[12:15]
EX v42,v42
MM a[12:15],a[140:143],v[76:79],a[12:15]
EX v43,v43
MM a[4:7],a[140:143],v[68:71],a[4:7]
EX v44,v44
MM a[4:7],a[136:139],v[64:67],a[4:7]
RD a[192:195],v224 offset:16384
CV v7,v38,v39
MM a[32:35],a[144:147],v[64:67],a[32:35]
EX v45,v45
MM a[32:35],a[148:151],v[68:71],a[32:35]
buffer_load_dwordx4 v228, s[8:11], s64 offen offset:0 lds
MM a[40:43],a[148:151],v[76:79],a[40:43]
RD a[196:199],v225 offset:16384
EX v46,v46
MM a[40:43],a[144:147],v[72:75],a[40:43]
EX v47,v47
CV v12,v40,v41
s_nop 0
LPG6_%=:
s_cbranch_execz LPG10_%=
MM a[44:47],a[152:155],v[72:75],a[44:47]
RD a[200:203],v226 offset:16384
EX v48,v48
MM a[44:47],a[156:159],v[76:79],a[44:47]
buffer_load_dwordx4 v229, s[8:11], s64 offen offset:1024 lds
MM a[36:39],a[156:159],v[68:71],a[36:39]
RD a[204:207],v227 offset:16384
EX v49,v49
CV v13,v42,v43
MM a[36:39],a[152:155],v[64:67],a[36:39]
EX v50,v50
MM a[64:67],a[160:163],v[64:67],a[64:67]
RD a[208:211],v224 offset:20480
EX v51,v51
CV v14,v44,v45
MM a[64:67],a[164:167],v[68:71],a[64:67]
buffer_load_dwordx4 v230, s[8:11], s64 offen offset:2048 lds
MM a[72:75],a[164:167],v[76:79],a[72:75]
RD a[212:215],v225 offset:20480
EX v52,v52
MM a[72:75],a[160:163],v[72:75],a[72:75]
EX v53,v53
CV v15,v46,v47
MM a[76:79],a[168:171],v[72:75],a[76:79]
RD a[216:219],v226 offset:20480
CV v20,v48,v49
EX v54,v54
MM a[76:79],a[172:175],v[76:79],a[76:79]
buffer_load_dwordx4 v231, s[8:11], s64 offen offset:3072 lds
MM a[68:71],a[172:175],v[68:71],a[68:71]
s_add_u32 m0, s65, 114688
RD a[220:223],v227 offset:20480
EX v55,v55
MM a[68:71],a[168:171],v[64:67],a[68:71]
EX v56,v56
MM a[96:99],a[176:179],v[64:67],a[96:99]
RD a[224:227],v224 offset:24576
CV v21,v50,v51
EX v57,v57
MM a[96:99],a[180:183],v[68:71],a[96:99]
RD a[228:231],v225 offset:24576
CV v22,v52,v53
MM a[104:107],a[180:183],v[76:79],a[104:107]
buffer_load_dwordx4 v235, s[12:15], s81 offen offset:0 lds
EX v58,v58
MM a[104:107],a[176:179],v[72:75],a[104:107]
EX v59,v59
MM a[108:111],a[184:187],v[72:75],a[108:111]
RD a[232:235],v226 offset:24576
CV v23,v54,v55
MM a[108:111],a[188:191],v[76:79],a[108:111]
EX v60,v60
MM a[100:103],a[188:191],v[68:71],a[100:103]
RD a[236:239],v227 offset:24576
buffer_load_dwordx4 v235, s[12:15], s64 offen offset:1024 lds
EX v61,v61
MM a[100:103],a[184:187],v[64:67],a[100:103]
EX v62,v62
v_smfmac_f32_16x16x64_bf16 v[144:147], v[236:239], v[0:7], v234
RD a[240:243],v224 offset:28672
RD a[244:247],v225 offset:28672
RD a[248:251],v226 offset:28672
RD a[252:255],v227 offset:28672
EX v63,v63
v_smfmac_f32_16x16x64_bf16 v[148:151], v[236:239], v[8:15], v234
CV v28,v56,v57
v_smfmac_f32_16x16x64_bf16 v[152:155], v[236:239], v[16:23], v234
buffer_load_dwordx4 v235, s[12:15], s81 offen offset:2048 lds
CV v29,v58,v59
CV v30,v60,v61
CV v31,v62,v63
s_nop 1
v_smfmac_f32_16x16x64_bf16 v[156:159], v[236:239], v[24:31], v234
s_waitcnt lgkmcnt(0)
buffer_load_dwordx4 v235, s[12:15], s64 offen offset:3072 lds
MM v[64:67],a[192:195],v[160:163],v[128:131]
s_add_u32 s64, s64, 16384
TR a[128:129],v232 offset:0
s_add_u32 s81, s64, 896
MM v[64:67],a[196:199],v[176:179],v[64:67]
TR a[130:131],v232 offset:4096
v_max3_f32 v249, v144, v145, v148
MM v[64:67],a[200:203],v[192:195],v[64:67]
TR a[132:133],v232 offset:8192
v_max3_f32 v249, v249, v149, v152
MM v[64:67],a[204:207],v[208:211],v[64:67]
TR a[134:135],v232 offset:12288
v_max3_f32 v249, v249, v153, v156
MM v[68:71],a[220:223],v[208:211],v[128:131]
TR a[136:137],v233 offset:0
v_max_f32 v249, v249, v157
MM v[68:71],a[216:219],v[192:195],v[68:71]
TR a[138:139],v233 offset:4096
v_cmp_lt_f32 vcc, s71, v249
MM v[68:71],a[212:215],v[176:179],v[68:71]
TR a[140:141],v233 offset:8192
TR a[142:143],v233 offset:12288
MM v[68:71],a[208:211],v[160:163],v[68:71]
s_cbranch_vccnz LSITE4_%=
LBACK4_%=:
MM v[76:79],a[208:211],v[164:167],v[132:135]
s_cmp_lg_u32 %[sb], 0
s_cbranch_scc1 LEP_%=
v_mov_b32 v250, s69
s_mov_b32 s76, 32768
s_mov_b64 exec, 1
buffer_store_dword v250, off, s[20:23], s76 sc1
s_mov_b64 exec, -1
s_nop 1
LEP_%=:
MM v[76:79],a[212:215],v[180:183],v[76:79]
s_lshl_b32 s76, s50, 1
EX v64,v64
MM v[76:79],a[216:219],v[196:199],v[76:79]
s_waitcnt lgkmcnt(6)
EX v65,v65
MM v[76:79],a[220:223],v[212:215],v[76:79]
s_bfe_u32 s77, s49, 65538
TR a[144:145],v232 offset:512
CV v64,v64,v65
MM v[48:51],a[128:131],v[16:19],0
MM v[52:55],a[128:131],v[24:27],0
MM v[72:75],a[204:207],v[212:215],v[132:135]
s_add_u32 s76, s76, s77
EX v66,v66
MM v[72:75],a[200:203],v[196:199],v[72:75]
s_lshl_b32 s76, s76, 2
EX v67,v67
MM v[72:75],a[196:199],v[180:183],v[72:75]
s_add_u32 s76, s76, s84
TR a[146:147],v232 offset:4608
CV v65,v66,v67
MM v[72:75],a[192:195],v[164:167],v[72:75]
s_lshl_b32 s76, s76, 2
EX v68,v68
MM v[80:83],a[192:195],v[168:171],v[136:139]
s_lshr_b32 s77, s49, 3
v_max3_f32 v48, |v48|, |v49|, |v50|
v_max_f32 v48, v48, |v51|
MM v[80:83],a[196:199],v[184:187],v[80:83]
s_add_u32 s76, s76, s77
v_bfe_u32 v48, v48, 23, 8
v_add_u32 v48, 8, v48
MM v[80:83],a[200:203],v[200:203],v[80:83]
s_mul_i32 s95, s76, 33792
v_min_u32 v48, 240, v48
v_lshl_or_b32 v241, v48, 16, v241
MM v[80:83],a[204:207],v[216:219],v[80:83]
s_lshl_b32 s76, %[swave], 13
v_lshl_or_b32 v48, v48, 1, 1
v_lshlrev_b32 v48, 22, v48
MM v[84:87],a[220:223],v[216:219],v[136:139]
s_add_u32 s95, s95, s76
AW a116,v48
AW a117,v48
MM v[84:87],a[216:219],v[200:203],v[84:87]
s_add_u32 s95, s95, 22708224
AW a118,v48
AW a119,v48
EX v69,v69
MM v[84:87],a[212:215],v[184:187],v[84:87]
v_max3_f32 v52, |v52|, |v53|, |v54|
v_max_f32 v52, v52, |v55|
MM v[84:87],a[208:211],v[168:171],v[84:87]
v_bfe_u32 v52, v52, 23, 8
v_add_u32 v52, 8, v52
EX v70,v70
v_mov_b32 v240, -1
MM v[92:95],a[208:211],v[172:175],v[140:143]
s_mov_b32 s67, s95
v_min_u32 v52, 240, v52
v_lshl_or_b32 v241, v52, 24, v241
MM v[92:95],a[212:215],v[188:191],v[92:95]
s_add_u32 s87, s95, 6144
TR a[148:149],v232 offset:8704
v_lshl_or_b32 v52, v52, 1, 1
v_lshlrev_b32 v52, 22, v52
EX v71,v71
MM v[92:95],a[216:219],v[204:207],v[92:95]
AW a124,v52
AW a125,v52
MM v[92:95],a[220:223],v[220:223],v[92:95]
TR a[150:151],v232 offset:12800
buffer_store_dword v240, v248, s[20:23], s87 offen offset:12 sc1
AW a126,v52
AW a127,v52
EX v72,v72
MM v[88:91],a[204:207],v[220:223],v[140:143]
TR a[152:153],v233 offset:512
TR a[154:155],v233 offset:4608
MM v[88:91],a[200:203],v[204:207],v[88:91]
EX v73,v73
MM v[88:91],a[196:199],v[188:191],v[88:91]
EX v74,v74
MM v[88:91],a[192:195],v[172:175],v[88:91]
TR a[156:157],v233 offset:8704
CV v66,v68,v69
MM v[96:99],a[224:227],v[160:163],v[128:131]
)ASM"
R"ASM(
EX v75,v75
MM v[96:99],a[228:231],v[176:179],v[96:99]
s_add_u32 s87, s95, 7168
buffer_store_dword v240, v248, s[20:23], s87 offen offset:12 sc1
MM v[96:99],a[232:235],v[192:195],v[96:99]
TR a[158:159],v233 offset:12800
v_lshrrev_b32 v34, 1, v248
EX v76,v76
MM v[96:99],a[236:239],v[208:211],v[96:99]
s_mov_b32 s90, 86016
buffer_load_dwordx2 v[32:33], v34, s[20:23], s90 offen offset:0 sc1
MM v[100:103],a[252:255],v[208:211],v[128:131]
TR a[160:161],v232 offset:1024
EX v77,v77
CV v67,v70,v71
MM v[100:103],a[248:251],v[192:195],v[100:103]
TR a[162:163],v232 offset:5120
CV v72,v72,v73
MM v[100:103],a[244:247],v[176:179],v[100:103]
EX v78,v78
MM v[100:103],a[240:243],v[160:163],v[100:103]
TR a[164:165],v232 offset:9216
TR a[166:167],v232 offset:13312
MM v[108:111],a[240:243],v[164:167],v[132:135]
EX v79,v79
MM v[108:111],a[244:247],v[180:183],v[108:111]
EX v80,v80
MM v[108:111],a[248:251],v[196:199],v[108:111]
TR a[168:169],v233 offset:1024
CV v73,v74,v75
MM v[108:111],a[252:255],v[212:215],v[108:111]
EX v81,v81
MM v[104:107],a[236:239],v[212:215],v[132:135]
TR a[170:171],v233 offset:5120
CV v74,v76,v77
MM v[104:107],a[232:235],v[196:199],v[104:107]
EX v82,v82
MM v[104:107],a[228:231],v[180:183],v[104:107]
TR a[172:173],v233 offset:9216
TR a[174:175],v233 offset:13312
MM v[104:107],a[224:227],v[164:167],v[104:107]
EX v83,v83
MM v[112:115],a[224:227],v[168:171],v[136:139]
TR a[176:177],v232 offset:1536
CV v75,v78,v79
MM v[112:115],a[228:231],v[184:187],v[112:115]
EX v84,v84
MM v[112:115],a[232:235],v[200:203],v[112:115]
EX v85,v85
MM v[112:115],a[236:239],v[216:219],v[112:115]
TR a[178:179],v232 offset:5632
CV v80,v80,v81
MM v[116:119],a[252:255],v[216:219],v[136:139]
EX v86,v86
MM v[116:119],a[248:251],v[200:203],v[116:119]
TR a[180:181],v232 offset:9728
TR a[182:183],v232 offset:13824
MM v[116:119],a[244:247],v[184:187],v[116:119]
EX v87,v87
MM v[116:119],a[240:243],v[168:171],v[116:119]
TR a[184:185],v233 offset:1536
CV v81,v82,v83
MM v[124:127],a[240:243],v[172:175],v[140:143]
EX v88,v88
MM v[124:127],a[244:247],v[188:191],v[124:127]
TR a[186:187],v233 offset:5632
CV v82,v84,v85
MM v[124:127],a[248:251],v[204:207],v[124:127]
TR a[188:189],v233 offset:9728
TR a[190:191],v233 offset:13824
EX v89,v89
MM v[124:127],a[252:255],v[220:223],v[124:127]
EX v90,v90
MM v[120:123],a[236:239],v[220:223],v[140:143]
CV v83,v86,v87
CV v88,v88,v89
MM v[120:123],a[232:235],v[204:207],v[120:123]
EX v91,v91
MM v[120:123],a[228:231],v[188:191],v[120:123]
EX v92,v92
MM v[120:123],a[224:227],v[172:175],v[120:123]
s_waitcnt vmcnt(11) lgkmcnt(0)
s_barrier
MM a[0:3],a[128:131],v[0:3],a[0:3]
s_add_u32 m0, s65, 0
EX v93,v93
MM a[0:3],a[132:135],v[4:7],a[0:3]
CV v89,v90,v91
CV v90,v92,v93
MM a[8:11],a[132:135],v[12:15],a[8:11]
EX v94,v94
MM a[8:11],a[128:131],v[8:11],a[8:11]
EX v95,v95
MM a[16:19],a[128:131],v[16:19],a[116:119]
RD a[192:195],v224 offset:32768
CV v91,v94,v95
MM a[16:19],a[132:135],v[20:23],a[16:19]
EX v96,v96
MM a[24:27],a[132:135],v[28:31],a[124:127]
buffer_load_dwordx4 v228, s[8:11], s64 offen offset:0 lds
MM a[24:27],a[128:131],v[24:27],a[24:27]
EX v97,v97
MM a[28:31],a[136:139],v[24:27],a[124:127]
RD a[196:199],v225 offset:32768
CV v68,v96,v97
MM a[28:31],a[140:143],v[28:31],a[28:31]
EX v98,v98
MM a[20:23],a[140:143],v[20:23],a[116:119]
EX v99,v99
MM a[20:23],a[136:139],v[16:19],a[20:23]
RD a[200:203],v226 offset:32768
CV v69,v98,v99
MM a[12:15],a[136:139],v[8:11],a[12:15]
EX v100,v100
MM a[12:15],a[140:143],v[12:15],a[12:15]
buffer_load_dwordx4 v229, s[8:11], s64 offen offset:1024 lds
MM a[4:7],a[140:143],v[4:7],a[4:7]
EX v101,v101
MM a[4:7],a[136:139],v[0:3],a[4:7]
RD a[204:207],v227 offset:32768
CV v70,v100,v101
MM a[32:35],a[144:147],v[0:3],a[32:35]
EX v102,v102
MM a[32:35],a[148:151],v[4:7],a[32:35]
EX v103,v103
MM a[40:43],a[148:151],v[12:15],a[40:43]
RD a[208:211],v224 offset:36864
CV v71,v102,v103
MM a[40:43],a[144:147],v[8:11],a[40:43]
EX v104,v104
MM a[48:51],a[144:147],v[16:19],a[116:119]
buffer_load_dwordx4 v230, s[8:11], s64 offen offset:2048 lds
MM a[48:51],a[148:151],v[20:23],a[48:51]
EX v105,v105
MM a[56:59],a[148:151],v[28:31],a[124:127]
RD a[212:215],v225 offset:36864
CV v76,v104,v105
MM a[56:59],a[144:147],v[24:27],a[56:59]
EX v106,v106
MM a[60:63],a[152:155],v[24:27],a[124:127]
EX v107,v107
MM a[60:63],a[156:159],v[28:31],a[60:63]
RD a[216:219],v226 offset:36864
CV v77,v106,v107
MM a[52:55],a[156:159],v[20:23],a[116:119]
EX v108,v108
MM a[52:55],a[152:155],v[16:19],a[52:55]
buffer_load_dwordx4 v231, s[8:11], s64 offen offset:3072 lds
MM a[44:47],a[152:155],v[8:11],a[44:47]
s_add_u32 m0, s65, 65536
EX v109,v109
MM a[44:47],a[156:159],v[12:15],a[44:47]
RD a[220:223],v227 offset:36864
CV v78,v108,v109
MM a[36:39],a[156:159],v[4:7],a[36:39]
EX v110,v110
MM a[36:39],a[152:155],v[0:3],a[36:39]
RD a[224:227],v224 offset:40960
RD a[228:231],v225 offset:40960
MM a[64:67],a[160:163],v[0:3],a[64:67]
s_waitcnt vmcnt(4)
v_cmp_ne_u32 vcc, s69, v33
s_cbranch_vccnz LRRK_%=
LRRKb_%=:
MM a[64:67],a[164:167],v[4:7],a[64:67]
RD a[232:235],v226 offset:40960
v_add_u32_dpp v32, v32, v32 row_ror:8 row_mask:0xf bank_mask:0xf
MM a[72:75],a[164:167],v[12:15],a[72:75]
s_and_b32 s76, %[sb], 7
RD a[236:239],v227 offset:40960
v_mov_b32 v35, v32
MM a[72:75],a[160:163],v[8:11],a[72:75]
s_nop 0
v_permlane16_swap_b32 v32, v35
v_add_u32 v32, v32, v35
EX v111,v111
MM a[80:83],a[160:163],v[16:19],a[116:119]
v_mov_b32 v35, v32
CV v79,v110,v111
MM a[80:83],a[164:167],v[20:23],a[80:83]
v_permlane32_swap_b32 v32, v35
v_add_u32 v32, v32, v35
EX v112,v112
MM a[88:91],a[164:167],v[28:31],a[124:127]
RD a[240:243],v224 offset:45056
v_readlane_b32 s77, v32, s76
MM a[88:91],a[160:163],v[24:27],a[88:91]
s_bfm_b32 s76, s76, 0
EX v113,v113
MM a[92:95],a[168:171],v[24:27],a[124:127]
v_cmp_gt_u32 s[60:61], s77, v32
v_cmp_eq_u32 s[62:63], s77, v32
MM a[92:95],a[172:175],v[28:31],a[92:95]
s_and_b32 s62, s62, s76
EX v114,v114
MM a[84:87],a[172:175],v[20:23],a[116:119]
s_or_b32 s60, s60, s62
EX v115,v115
MM a[84:87],a[168:171],v[16:19],a[84:87]
s_and_b32 s60, s60, 255
EX v116,v116
MM a[76:79],a[168:171],v[8:11],a[76:79]
s_bcnt1_i32_b32 s51, s60
s_cmp_lt_u32 s51, 4
s_cselect_b32 s82, 3, 99
MM a[76:79],a[172:175],v[12:15],a[76:79]
s_lshr_b32 s16, %[sb], 3
EX v117,v117
MM a[68:71],a[172:175],v[4:7],a[68:71]
s_lshl_b32 s76, s51, 5
buffer_load_dwordx4 v235, s[12:15], s81 offen offset:0 lds
MM a[68:71],a[168:171],v[0:3],a[68:71]
s_add_u32 s16, s16, s76
CV v84,v112,v113
CV v85,v114,v115
MM a[96:99],a[176:179],v[0:3],a[96:99]
EX v118,v118
MM a[96:99],a[180:183],v[4:7],a[96:99]
EX v119,v119
MM a[104:107],a[180:183],v[12:15],a[104:107]
CV v86,v116,v117
CV v87,v118,v119
MM a[104:107],a[176:179],v[8:11],a[104:107]
EX v120,v120
MM a[112:115],a[176:179],v[16:19],a[116:119]
buffer_load_dwordx4 v235, s[12:15], s64 offen offset:1024 lds
MM a[112:115],a[180:183],v[20:23],a[112:115]
EX v121,v121
MM a[120:123],a[180:183],v[28:31],a[124:127]
EX v122,v122
MM a[120:123],a[176:179],v[24:27],a[120:123]
EX v123,v123
MM a[124:127],a[184:187],v[24:27],a[124:127]
v_add_f32 v244, v144, v145
v_add_f32 v245, v148, v149
MM a[124:127],a[188:191],v[28:31],a[124:127]
v_add_f32 v246, v152, v153
v_add_f32 v247, v156, v157
MM a[116:119],a[188:191],v[20:23],a[116:119]
EX v124,v124
MM a[116:119],a[184:187],v[16:19],a[116:119]
RD a[244:247],v225 offset:45056
CV v92,v120,v121
MM a[108:111],a[184:187],v[8:11],a[108:111]
EX v125,v125
MM a[108:111],a[188:191],v[12:15],a[108:111]
buffer_load_dwordx4 v235, s[12:15], s81 offen offset:2048 lds
MM a[100:103],a[188:191],v[4:7],a[100:103]
EX v126,v126
MM a[100:103],a[184:187],v[0:3],a[100:103]
RD a[248:251],v226 offset:45056
CV v93,v122,v123
v_smfmac_f32_16x16x64_bf16 v[144:147], v[236:239], v[64:71], v234
RD a[252:255],v227 offset:45056
EX v127,v127
v_smfmac_f32_16x16x64_bf16 v[148:151], v[236:239], v[72:79], v234
CV v94,v124,v125
v_smfmac_f32_16x16x64_bf16 v[152:155], v[236:239], v[80:87], v234
CV v95,v126,v127
s_nop 1
v_smfmac_f32_16x16x64_bf16 v[156:159], v[236:239], v[88:95], v234
s_waitcnt lgkmcnt(0)
buffer_load_dwordx4 v235, s[12:15], s64 offen offset:3072 lds
s_cmp_lt_u32 s51, 4
s_cbranch_scc1 LLAR_%=
s_mov_b32 s70, 6
LLAP_%=:
MM v[0:3],a[192:195],v[160:163],v[128:131]
s_add_u32 s64, s64, 16384
TR a[128:129],v232 offset:16384
s_add_u32 s81, s64, 896
MM v[0:3],a[196:199],v[176:179],v[0:3]
TR a[130:131],v232 offset:20480
v_max3_f32 v249, v144, v145, v148
MM v[0:3],a[200:203],v[192:195],v[0:3]
TR a[132:133],v232 offset:24576
v_max3_f32 v249, v249, v149, v152
MM v[0:3],a[204:207],v[208:211],v[0:3]
TR a[134:135],v232 offset:28672
v_max3_f32 v249, v249, v153, v156
MM v[4:7],a[220:223],v[208:211],v[128:131]
TR a[136:137],v233 offset:16384
v_max_f32 v249, v249, v157
MM v[4:7],a[216:219],v[192:195],v[4:7]
TR a[138:139],v233 offset:20480
v_cmp_lt_f32 vcc, s71, v249
MM v[4:7],a[212:215],v[176:179],v[4:7]
TR a[140:141],v233 offset:24576
TR a[142:143],v233 offset:28672
MM v[4:7],a[208:211],v[160:163],v[4:7]
s_cbranch_vccnz LSITE5_%=
LBACK5_%=:
MM v[12:15],a[208:211],v[164:167],v[132:135]
EX v0,v0
MM v[12:15],a[212:215],v[180:183],v[12:15]
EX v1,v1
MM v[12:15],a[216:219],v[196:199],v[12:15]
TR a[144:145],v232 offset:16896
CV v0,v0,v1
MM v[12:15],a[220:223],v[212:215],v[12:15]
EX v2,v2
MM v[8:11],a[204:207],v[212:215],v[132:135]
EX v3,v3
MM v[8:11],a[200:203],v[196:199],v[8:11]
TR a[146:147],v232 offset:20992
CV v1,v2,v3
MM v[8:11],a[196:199],v[180:183],v[8:11]
EX v4,v4
MM v[8:11],a[192:195],v[164:167],v[8:11]
TR a[148:149],v232 offset:25088
TR a[150:151],v232 offset:29184
MM v[16:19],a[192:195],v[168:171],v[136:139]
EX v5,v5
MM v[16:19],a[196:199],v[184:187],v[16:19]
TR a[152:153],v233 offset:16896
CV v2,v4,v5
MM v[16:19],a[200:203],v[200:203],v[16:19]
EX v6,v6
MM v[16:19],a[204:207],v[216:219],v[16:19]
TR a[154:155],v233 offset:20992
TR a[156:157],v233 offset:25088
MM v[20:23],a[220:223],v[216:219],v[136:139]
s_cmp_lg_u32 s70, s82
s_cbranch_scc1 LWW_%=
v_lshlrev_b32 v250, 3, v248
v_min_u32 v250, 4992, v250
s_sub_u32 s91, 5242880, s26
s_sub_u32 s60, s24, s91
s_subb_u32 s61, s25, 0
s_mov_b32 s62, 5242880
s_mov_b32 s63, 131072
s_lshl_b32 s90, s16, 2
s_add_u32 s90, s90, %[swave]
s_mul_i32 s90, s90, 10240
s_add_u32 s90, s90, 0
s_add_u32 s91, %[slds], 135168
s_mov_b32 m0, s91
s_nop 0
buffer_load_dword v250, s[60:63], s90 offen offset:0 lds
LWW_%=:
MM v[20:23],a[216:219],v[200:203],v[20:23]
EX v7,v7
MM v[20:23],a[212:215],v[184:187],v[20:23]
TR a[158:159],v233 offset:29184
CV v3,v6,v7
MM v[20:23],a[208:211],v[168:171],v[20:23]
EX v8,v8
MM v[28:31],a[208:211],v[172:175],v[140:143]
EX v9,v9
MM v[28:31],a[212:215],v[188:191],v[28:31]
TR a[160:161],v232 offset:17408
CV v8,v8,v9
MM v[28:31],a[216:219],v[204:207],v[28:31]
EX v10,v10
MM v[28:31],a[220:223],v[220:223],v[28:31]
EX v11,v11
MM v[24:27],a[204:207],v[220:223],v[140:143]
TR a[162:163],v232 offset:21504
CV v9,v10,v11
MM v[24:27],a[200:203],v[204:207],v[24:27]
TR a[164:165],v232 offset:25600
TR a[166:167],v232 offset:29696
MM v[24:27],a[196:199],v[188:191],v[24:27]
EX v12,v12
MM v[24:27],a[192:195],v[172:175],v[24:27]
EX v13,v13
MM v[32:35],a[224:227],v[160:163],v[128:131]
TR a[168:169],v233 offset:17408
CV v10,v12,v13
MM v[32:35],a[228:231],v[176:179],v[32:35]
EX v14,v14
MM v[32:35],a[232:235],v[192:195],v[32:35]
TR a[170:171],v233 offset:21504
TR a[172:173],v233 offset:25600
MM v[32:35],a[236:239],v[208:211],v[32:35]
EX v15,v15
MM v[36:39],a[252:255],v[208:211],v[128:131]
TR a[174:175],v233 offset:29696
CV v11,v14,v15
MM v[36:39],a[248:251],v[192:195],v[36:39]
EX v16,v16
MM v[36:39],a[244:247],v[176:179],v[36:39]
EX v17,v17
MM v[36:39],a[240:243],v[160:163],v[36:39]
TR a[176:177],v232 offset:17920
CV v16,v16,v17
MM v[44:47],a[240:243],v[164:167],v[132:135]
EX v18,v18
MM v[44:47],a[244:247],v[180:183],v[44:47]
TR a[178:179],v232 offset:22016
TR a[180:181],v232 offset:26112
MM v[44:47],a[248:251],v[196:199],v[44:47]
EX v19,v19
MM v[44:47],a[252:255],v[212:215],v[44:47]
TR a[182:183],v232 offset:30208
CV v17,v18,v19
MM v[40:43],a[236:239],v[212:215],v[132:135]
EX v20,v20
MM v[40:43],a[232:235],v[196:199],v[40:43]
)ASM"
R"ASM(
EX v21,v21
MM v[40:43],a[228:231],v[180:183],v[40:43]
TR a[184:185],v233 offset:17920
CV v18,v20,v21
MM v[40:43],a[224:227],v[164:167],v[40:43]
EX v22,v22
MM v[48:51],a[224:227],v[168:171],v[136:139]
TR a[186:187],v233 offset:22016
LPG7_%=:
s_cbranch_execz LPG11_%=
TR a[188:189],v233 offset:26112
MM v[48:51],a[228:231],v[184:187],v[48:51]
EX v23,v23
MM v[48:51],a[232:235],v[200:203],v[48:51]
TR a[190:191],v233 offset:30208
CV v19,v22,v23
MM v[48:51],a[236:239],v[216:219],v[48:51]
EX v24,v24
MM v[52:55],a[252:255],v[216:219],v[136:139]
EX v25,v25
MM v[52:55],a[248:251],v[200:203],v[52:55]
EX v26,v26
MM v[52:55],a[244:247],v[184:187],v[52:55]
EX v27,v27
MM v[52:55],a[240:243],v[168:171],v[52:55]
EX v28,v28
MM v[60:63],a[240:243],v[172:175],v[140:143]
EX v29,v29
MM v[60:63],a[244:247],v[188:191],v[60:63]
EX v30,v30
MM v[60:63],a[248:251],v[204:207],v[60:63]
EX v31,v31
MM v[60:63],a[252:255],v[220:223],v[60:63]
EX v32,v32
MM v[56:59],a[236:239],v[220:223],v[140:143]
EX v33,v33
MM v[56:59],a[232:235],v[204:207],v[56:59]
EX v34,v34
MM v[56:59],a[228:231],v[188:191],v[56:59]
EX v35,v35
MM v[56:59],a[224:227],v[172:175],v[56:59]
s_waitcnt vmcnt(12) lgkmcnt(0)
s_barrier
MM a[0:3],a[128:131],v[64:67],a[0:3]
s_add_u32 m0, s65, 16384
CV v24,v24,v25
CV v25,v26,v27
MM a[0:3],a[132:135],v[68:71],a[0:3]
RD a[192:195],v224 offset:49152
CV v26,v28,v29
MM a[8:11],a[132:135],v[76:79],a[8:11]
RD a[196:199],v225 offset:49152
CV v27,v30,v31
MM a[8:11],a[128:131],v[72:75],a[8:11]
buffer_load_dwordx4 v228, s[8:11], s64 offen offset:0 lds
MM a[16:19],a[128:131],v[80:83],a[16:19]
RD a[200:203],v226 offset:49152
CV v4,v32,v33
MM a[16:19],a[132:135],v[84:87],a[16:19]
RD a[204:207],v227 offset:49152
CV v5,v34,v35
MM a[24:27],a[132:135],v[92:95],a[24:27]
buffer_load_dwordx4 v229, s[8:11], s64 offen offset:1024 lds
MM a[24:27],a[128:131],v[88:91],a[24:27]
RD a[208:211],v224 offset:53248
RD a[212:215],v225 offset:53248
MM a[28:31],a[136:139],v[88:91],a[28:31]
buffer_load_dwordx4 v230, s[8:11], s64 offen offset:2048 lds
MM a[28:31],a[140:143],v[92:95],a[28:31]
EX v36,v36
MM a[20:23],a[140:143],v[84:87],a[20:23]
EX v37,v37
MM a[20:23],a[136:139],v[80:83],a[20:23]
RD a[216:219],v226 offset:53248
CV v6,v36,v37
MM a[12:15],a[136:139],v[72:75],a[12:15]
EX v38,v38
MM a[12:15],a[140:143],v[76:79],a[12:15]
RD a[220:223],v227 offset:53248
RD a[224:227],v224 offset:57344
MM a[4:7],a[140:143],v[68:71],a[4:7]
EX v39,v39
MM a[4:7],a[136:139],v[64:67],a[4:7]
RD a[228:231],v225 offset:57344
CV v7,v38,v39
MM a[32:35],a[144:147],v[64:67],a[32:35]
EX v40,v40
MM a[32:35],a[148:151],v[68:71],a[32:35]
buffer_load_dwordx4 v231, s[8:11], s64 offen offset:3072 lds
MM a[40:43],a[148:151],v[76:79],a[40:43]
s_add_u32 m0, s65, 81920
EX v41,v41
MM a[40:43],a[144:147],v[72:75],a[40:43]
RD a[232:235],v226 offset:57344
CV v12,v40,v41
MM a[48:51],a[144:147],v[80:83],a[48:51]
EX v42,v42
MM a[48:51],a[148:151],v[84:87],a[48:51]
EX v43,v43
MM a[56:59],a[148:151],v[92:95],a[56:59]
RD a[236:239],v227 offset:57344
CV v13,v42,v43
MM a[56:59],a[144:147],v[88:91],a[56:59]
EX v44,v44
MM a[60:63],a[152:155],v[88:91],a[60:63]
buffer_load_dwordx4 v235, s[12:15], s81 offen offset:0 lds
MM a[60:63],a[156:159],v[92:95],a[60:63]
EX v45,v45
MM a[52:55],a[156:159],v[84:87],a[52:55]
RD a[240:243],v224 offset:61440
CV v14,v44,v45
MM a[52:55],a[152:155],v[80:83],a[52:55]
EX v46,v46
MM a[44:47],a[152:155],v[72:75],a[44:47]
EX v47,v47
MM a[44:47],a[156:159],v[76:79],a[44:47]
RD a[244:247],v225 offset:61440
CV v15,v46,v47
MM a[36:39],a[156:159],v[68:71],a[36:39]
buffer_load_dwordx4 v235, s[12:15], s64 offen offset:1024 lds
MM a[36:39],a[152:155],v[64:67],a[36:39]
EX v48,v48
MM a[64:67],a[160:163],v[64:67],a[64:67]
EX v49,v49
MM a[64:67],a[164:167],v[68:71],a[64:67]
RD a[248:251],v226 offset:61440
CV v20,v48,v49
MM a[72:75],a[164:167],v[76:79],a[72:75]
EX v50,v50
MM a[72:75],a[160:163],v[72:75],a[72:75]
EX v51,v51
MM a[80:83],a[160:163],v[80:83],a[80:83]
RD a[252:255],v227 offset:61440
CV v21,v50,v51
MM a[80:83],a[164:167],v[84:87],a[80:83]
buffer_load_dwordx4 v235, s[12:15], s81 offen offset:2048 lds
MM a[88:91],a[164:167],v[92:95],a[88:91]
EX v52,v52
MM a[88:91],a[160:163],v[88:91],a[88:91]
EX v53,v53
MM a[92:95],a[168:171],v[88:91],a[92:95]
EX v54,v54
MM a[92:95],a[172:175],v[92:95],a[92:95]
buffer_load_dwordx4 v235, s[12:15], s64 offen offset:3072 lds
MM a[84:87],a[172:175],v[84:87],a[84:87]
EX v55,v55
MM a[84:87],a[168:171],v[80:83],a[84:87]
EX v56,v56
MM a[76:79],a[168:171],v[72:75],a[76:79]
EX v57,v57
MM a[76:79],a[172:175],v[76:79],a[76:79]
EX v58,v58
MM a[68:71],a[172:175],v[68:71],a[68:71]
EX v59,v59
MM a[68:71],a[168:171],v[64:67],a[68:71]
EX v60,v60
MM a[96:99],a[176:179],v[64:67],a[96:99]
EX v61,v61
MM a[96:99],a[180:183],v[68:71],a[96:99]
EX v62,v62
MM a[104:107],a[180:183],v[76:79],a[104:107]
EX v63,v63
MM a[104:107],a[176:179],v[72:75],a[104:107]
CV v22,v52,v53
CV v23,v54,v55
MM a[112:115],a[176:179],v[80:83],a[112:115]
CV v28,v56,v57
CV v29,v58,v59
MM a[112:115],a[180:183],v[84:87],a[112:115]
CV v30,v60,v61
CV v31,v62,v63
MM a[120:123],a[180:183],v[92:95],a[120:123]
MM a[120:123],a[176:179],v[88:91],a[120:123]
MM a[124:127],a[184:187],v[88:91],a[124:127]
v_add_f32 v244, v144, v145
v_add_f32 v245, v148, v149
MM a[124:127],a[188:191],v[92:95],a[124:127]
v_add_f32 v246, v152, v153
v_add_f32 v247, v156, v157
MM a[116:119],a[188:191],v[84:87],a[116:119]
MM a[116:119],a[184:187],v[80:83],a[116:119]
MM a[108:111],a[184:187],v[72:75],a[108:111]
MM a[108:111],a[188:191],v[76:79],a[108:111]
MM a[100:103],a[188:191],v[68:71],a[100:103]
MM a[100:103],a[184:187],v[64:67],a[100:103]
v_smfmac_f32_16x16x64_bf16 v[144:147], v[236:239], v[0:7], v234
v_smfmac_f32_16x16x64_bf16 v[148:151], v[236:239], v[8:15], v234
v_smfmac_f32_16x16x64_bf16 v[152:155], v[236:239], v[16:23], v234
v_smfmac_f32_16x16x64_bf16 v[156:159], v[236:239], v[24:31], v234
s_waitcnt lgkmcnt(0)
MM v[64:67],a[192:195],v[160:163],v[128:131]
s_add_u32 s64, s64, 16384
TR a[128:129],v232 offset:32768
s_add_u32 s81, s64, 896
MM v[64:67],a[196:199],v[176:179],v[64:67]
TR a[130:131],v232 offset:36864
v_max3_f32 v249, v144, v145, v148
MM v[64:67],a[200:203],v[192:195],v[64:67]
TR a[132:133],v232 offset:40960
v_max3_f32 v249, v249, v149, v152
MM v[64:67],a[204:207],v[208:211],v[64:67]
TR a[134:135],v232 offset:45056
v_max3_f32 v249, v249, v153, v156
MM v[68:71],a[220:223],v[208:211],v[128:131]
TR a[136:137],v233 offset:32768
v_max_f32 v249, v249, v157
MM v[68:71],a[216:219],v[192:195],v[68:71]
TR a[138:139],v233 offset:36864
v_cmp_lt_f32 vcc, s71, v249
MM v[68:71],a[212:215],v[176:179],v[68:71]
TR a[140:141],v233 offset:40960
TR a[142:143],v233 offset:45056
MM v[68:71],a[208:211],v[160:163],v[68:71]
s_cbranch_vccnz LSITE6_%=
LBACK6_%=:
MM v[76:79],a[208:211],v[164:167],v[132:135]
EX v64,v64
MM v[76:79],a[212:215],v[180:183],v[76:79]
EX v65,v65
MM v[76:79],a[216:219],v[196:199],v[76:79]
TR a[144:145],v232 offset:33280
CV v64,v64,v65
MM v[76:79],a[220:223],v[212:215],v[76:79]
EX v66,v66
MM v[72:75],a[204:207],v[212:215],v[132:135]
EX v67,v67
MM v[72:75],a[200:203],v[196:199],v[72:75]
TR a[146:147],v232 offset:37376
CV v65,v66,v67
MM v[72:75],a[196:199],v[180:183],v[72:75]
EX v68,v68
MM v[72:75],a[192:195],v[164:167],v[72:75]
TR a[148:149],v232 offset:41472
TR a[150:151],v232 offset:45568
MM v[80:83],a[192:195],v[168:171],v[136:139]
EX v69,v69
MM v[80:83],a[196:199],v[184:187],v[80:83]
TR a[152:153],v233 offset:33280
CV v66,v68,v69
MM v[80:83],a[200:203],v[200:203],v[80:83]
EX v70,v70
MM v[80:83],a[204:207],v[216:219],v[80:83]
TR a[154:155],v233 offset:37376
TR a[156:157],v233 offset:41472
MM v[84:87],a[220:223],v[216:219],v[136:139]
EX v71,v71
MM v[84:87],a[216:219],v[200:203],v[84:87]
TR a[158:159],v233 offset:45568
CV v67,v70,v71
MM v[84:87],a[212:215],v[184:187],v[84:87]
EX v72,v72
MM v[84:87],a[208:211],v[168:171],v[84:87]
EX v73,v73
MM v[92:95],a[208:211],v[172:175],v[140:143]
TR a[160:161],v232 offset:33792
CV v72,v72,v73
MM v[92:95],a[212:215],v[188:191],v[92:95]
EX v74,v74
MM v[92:95],a[216:219],v[204:207],v[92:95]
TR a[162:163],v232 offset:37888
TR a[164:165],v232 offset:41984
MM v[92:95],a[220:223],v[220:223],v[92:95]
EX v75,v75
MM v[88:91],a[204:207],v[220:223],v[140:143]
TR a[166:167],v232 offset:46080
CV v73,v74,v75
MM v[88:91],a[200:203],v[204:207],v[88:91]
EX v76,v76
MM v[88:91],a[196:199],v[188:191],v[88:91]
EX v77,v77
MM v[88:91],a[192:195],v[172:175],v[88:91]
TR a[168:169],v233 offset:33792
CV v74,v76,v77
MM v[96:99],a[224:227],v[160:163],v[128:131]
EX v78,v78
MM v[96:99],a[228:231],v[176:179],v[96:99]
TR a[170:171],v233 offset:37888
TR a[172:173],v233 offset:41984
MM v[96:99],a[232:235],v[192:195],v[96:99]
EX v79,v79
MM v[96:99],a[236:239],v[208:211],v[96:99]
TR a[174:175],v233 offset:46080
CV v75,v78,v79
MM v[100:103],a[252:255],v[208:211],v[128:131]
EX v80,v80
MM v[100:103],a[248:251],v[192:195],v[100:103]
EX v81,v81
MM v[100:103],a[244:247],v[176:179],v[100:103]
TR a[176:177],v232 offset:34304
CV v80,v80,v81
MM v[100:103],a[240:243],v[160:163],v[100:103]
EX v82,v82
MM v[108:111],a[240:243],v[164:167],v[132:135]
buffer_store_dword v240, v248, s[20:23], s67 offen offset:12 sc1
MM v[108:111],a[244:247],v[180:183],v[108:111]
s_add_u32 s67, s67, 1024
TR a[178:179],v232 offset:38400
TR a[180:181],v232 offset:42496
MM v[108:111],a[248:251],v[196:199],v[108:111]
EX v83,v83
MM v[108:111],a[252:255],v[212:215],v[108:111]
TR a[182:183],v232 offset:46592
CV v81,v82,v83
MM v[104:107],a[236:239],v[212:215],v[132:135]
EX v84,v84
MM v[104:107],a[232:235],v[196:199],v[104:107]
EX v85,v85
MM v[104:107],a[228:231],v[180:183],v[104:107]
TR a[184:185],v233 offset:34304
CV v82,v84,v85
MM v[104:107],a[224:227],v[164:167],v[104:107]
EX v86,v86
MM v[112:115],a[224:227],v[168:171],v[136:139]
TR a[186:187],v233 offset:38400
TR a[188:189],v233 offset:42496
MM v[112:115],a[228:231],v[184:187],v[112:115]
EX v87,v87
MM v[112:115],a[232:235],v[200:203],v[112:115]
TR a[190:191],v233 offset:46592
CV v83,v86,v87
MM v[112:115],a[236:239],v[216:219],v[112:115]
EX v88,v88
MM v[116:119],a[252:255],v[216:219],v[136:139]
EX v89,v89
MM v[116:119],a[248:251],v[200:203],v[116:119]
EX v90,v90
MM v[116:119],a[244:247],v[184:187],v[116:119]
EX v91,v91
MM v[116:119],a[240:243],v[168:171],v[116:119]
EX v92,v92
MM v[124:127],a[240:243],v[172:175],v[140:143]
EX v93,v93
MM v[124:127],a[244:247],v[188:191],v[124:127]
EX v94,v94
MM v[124:127],a[248:251],v[204:207],v[124:127]
EX v95,v95
MM v[124:127],a[252:255],v[220:223],v[124:127]
EX v96,v96
MM v[120:123],a[236:239],v[220:223],v[140:143]
EX v97,v97
MM v[120:123],a[232:235],v[204:207],v[120:123]
EX v98,v98
MM v[120:123],a[228:231],v[188:191],v[120:123]
EX v99,v99
MM v[120:123],a[224:227],v[172:175],v[120:123]
s_waitcnt vmcnt(12) lgkmcnt(0)
s_barrier
MM a[0:3],a[128:131],v[0:3],a[0:3]
s_add_u32 m0, s65, 32768
CV v88,v88,v89
CV v89,v90,v91
MM a[0:3],a[132:135],v[4:7],a[0:3]
RD a[192:195],v224 offset:0
CV v90,v92,v93
MM a[8:11],a[132:135],v[12:15],a[8:11]
RD a[196:199],v225 offset:0
CV v91,v94,v95
MM a[8:11],a[128:131],v[8:11],a[8:11]
buffer_load_dwordx4 v228, s[8:11], s64 offen offset:0 lds
MM a[16:19],a[128:131],v[16:19],a[16:19]
RD a[200:203],v226 offset:0
CV v68,v96,v97
MM a[16:19],a[132:135],v[20:23],a[16:19]
RD a[204:207],v227 offset:0
CV v69,v98,v99
MM a[24:27],a[132:135],v[28:31],a[24:27]
buffer_load_dwordx4 v229, s[8:11], s64 offen offset:1024 lds
MM a[24:27],a[128:131],v[24:27],a[24:27]
RD a[208:211],v224 offset:4096
RD a[212:215],v225 offset:4096
MM a[28:31],a[136:139],v[24:27],a[28:31]
buffer_load_dwordx4 v230, s[8:11], s64 offen offset:2048 lds
MM a[28:31],a[140:143],v[28:31],a[28:31]
EX v100,v100
MM a[20:23],a[140:143],v[20:23],a[20:23]
EX v101,v101
MM a[20:23],a[136:139],v[16:19],a[20:23]
RD a[216:219],v226 offset:4096
CV v70,v100,v101
MM a[12:15],a[136:139],v[8:11],a[12:15]
EX v102,v102
MM a[12:15],a[140:143],v[12:15],a[12:15]
RD a[220:223],v227 offset:4096
RD a[224:227],v224 offset:8192
MM a[4:7],a[140:143],v[4:7],a[4:7]
EX v103,v103
MM a[4:7],a[136:139],v[0:3],a[4:7]
RD a[228:231],v225 offset:8192
)ASM"
R"ASM(
CV v71,v102,v103
MM a[32:35],a[144:147],v[0:3],a[32:35]
EX v104,v104
MM a[32:35],a[148:151],v[4:7],a[32:35]
buffer_load_dwordx4 v231, s[8:11], s64 offen offset:3072 lds
MM a[40:43],a[148:151],v[12:15],a[40:43]
s_add_u32 m0, s65, 98304
EX v105,v105
MM a[40:43],a[144:147],v[8:11],a[40:43]
RD a[232:235],v226 offset:8192
CV v76,v104,v105
MM a[48:51],a[144:147],v[16:19],a[48:51]
EX v106,v106
MM a[48:51],a[148:151],v[20:23],a[48:51]
EX v107,v107
MM a[56:59],a[148:151],v[28:31],a[56:59]
RD a[236:239],v227 offset:8192
CV v77,v106,v107
MM a[56:59],a[144:147],v[24:27],a[56:59]
EX v108,v108
MM a[60:63],a[152:155],v[24:27],a[60:63]
buffer_load_dwordx4 v235, s[12:15], s81 offen offset:0 lds
MM a[60:63],a[156:159],v[28:31],a[60:63]
EX v109,v109
MM a[52:55],a[156:159],v[20:23],a[52:55]
RD a[240:243],v224 offset:12288
CV v78,v108,v109
MM a[52:55],a[152:155],v[16:19],a[52:55]
EX v110,v110
MM a[44:47],a[152:155],v[8:11],a[44:47]
EX v111,v111
MM a[44:47],a[156:159],v[12:15],a[44:47]
RD a[244:247],v225 offset:12288
CV v79,v110,v111
MM a[36:39],a[156:159],v[4:7],a[36:39]
buffer_load_dwordx4 v235, s[12:15], s64 offen offset:1024 lds
MM a[36:39],a[152:155],v[0:3],a[36:39]
EX v112,v112
MM a[64:67],a[160:163],v[0:3],a[64:67]
EX v113,v113
MM a[64:67],a[164:167],v[4:7],a[64:67]
RD a[248:251],v226 offset:12288
CV v84,v112,v113
MM a[72:75],a[164:167],v[12:15],a[72:75]
EX v114,v114
MM a[72:75],a[160:163],v[8:11],a[72:75]
EX v115,v115
MM a[80:83],a[160:163],v[16:19],a[80:83]
RD a[252:255],v227 offset:12288
CV v85,v114,v115
MM a[80:83],a[164:167],v[20:23],a[80:83]
buffer_load_dwordx4 v235, s[12:15], s81 offen offset:2048 lds
MM a[88:91],a[164:167],v[28:31],a[88:91]
EX v116,v116
MM a[88:91],a[160:163],v[24:27],a[88:91]
EX v117,v117
MM a[92:95],a[168:171],v[24:27],a[92:95]
EX v118,v118
MM a[92:95],a[172:175],v[28:31],a[92:95]
buffer_load_dwordx4 v235, s[12:15], s64 offen offset:3072 lds
MM a[84:87],a[172:175],v[20:23],a[84:87]
EX v119,v119
MM a[84:87],a[168:171],v[16:19],a[84:87]
EX v120,v120
MM a[76:79],a[168:171],v[8:11],a[76:79]
EX v121,v121
MM a[76:79],a[172:175],v[12:15],a[76:79]
EX v122,v122
MM a[68:71],a[172:175],v[4:7],a[68:71]
EX v123,v123
MM a[68:71],a[168:171],v[0:3],a[68:71]
EX v124,v124
MM a[96:99],a[176:179],v[0:3],a[96:99]
EX v125,v125
MM a[96:99],a[180:183],v[4:7],a[96:99]
EX v126,v126
MM a[104:107],a[180:183],v[12:15],a[104:107]
EX v127,v127
MM a[104:107],a[176:179],v[8:11],a[104:107]
CV v86,v116,v117
CV v87,v118,v119
MM a[112:115],a[176:179],v[16:19],a[112:115]
CV v92,v120,v121
CV v93,v122,v123
MM a[112:115],a[180:183],v[20:23],a[112:115]
CV v94,v124,v125
CV v95,v126,v127
MM a[120:123],a[180:183],v[28:31],a[120:123]
MM a[120:123],a[176:179],v[24:27],a[120:123]
MM a[124:127],a[184:187],v[24:27],a[124:127]
v_add_f32 v244, v144, v145
v_add_f32 v245, v148, v149
MM a[124:127],a[188:191],v[28:31],a[124:127]
v_add_f32 v246, v152, v153
v_add_f32 v247, v156, v157
MM a[116:119],a[188:191],v[20:23],a[116:119]
MM a[116:119],a[184:187],v[16:19],a[116:119]
MM a[108:111],a[184:187],v[8:11],a[108:111]
MM a[108:111],a[188:191],v[12:15],a[108:111]
MM a[100:103],a[188:191],v[4:7],a[100:103]
MM a[100:103],a[184:187],v[0:3],a[100:103]
v_smfmac_f32_16x16x64_bf16 v[144:147], v[236:239], v[64:71], v234
v_smfmac_f32_16x16x64_bf16 v[148:151], v[236:239], v[72:79], v234
v_smfmac_f32_16x16x64_bf16 v[152:155], v[236:239], v[80:87], v234
v_smfmac_f32_16x16x64_bf16 v[156:159], v[236:239], v[88:95], v234
s_waitcnt lgkmcnt(0)
MM v[0:3],a[192:195],v[160:163],v[128:131]
s_add_u32 s64, s64, 16384
TR a[128:129],v232 offset:49152
s_add_u32 s81, s64, 896
MM v[0:3],a[196:199],v[176:179],v[0:3]
TR a[130:131],v232 offset:53248
v_max3_f32 v249, v144, v145, v148
MM v[0:3],a[200:203],v[192:195],v[0:3]
TR a[132:133],v232 offset:57344
v_max3_f32 v249, v249, v149, v152
MM v[0:3],a[204:207],v[208:211],v[0:3]
TR a[134:135],v232 offset:61440
v_max3_f32 v249, v249, v153, v156
MM v[4:7],a[220:223],v[208:211],v[128:131]
TR a[136:137],v233 offset:49152
v_max_f32 v249, v249, v157
MM v[4:7],a[216:219],v[192:195],v[4:7]
TR a[138:139],v233 offset:53248
v_cmp_lt_f32 vcc, s71, v249
MM v[4:7],a[212:215],v[176:179],v[4:7]
TR a[140:141],v233 offset:57344
TR a[142:143],v233 offset:61440
MM v[4:7],a[208:211],v[160:163],v[4:7]
s_cbranch_vccnz LSITE7_%=
LBACK7_%=:
MM v[12:15],a[208:211],v[164:167],v[132:135]
EX v0,v0
MM v[12:15],a[212:215],v[180:183],v[12:15]
EX v1,v1
MM v[12:15],a[216:219],v[196:199],v[12:15]
TR a[144:145],v232 offset:49664
CV v0,v0,v1
MM v[12:15],a[220:223],v[212:215],v[12:15]
EX v2,v2
MM v[8:11],a[204:207],v[212:215],v[132:135]
EX v3,v3
MM v[8:11],a[200:203],v[196:199],v[8:11]
TR a[146:147],v232 offset:53760
CV v1,v2,v3
MM v[8:11],a[196:199],v[180:183],v[8:11]
EX v4,v4
MM v[8:11],a[192:195],v[164:167],v[8:11]
TR a[148:149],v232 offset:57856
TR a[150:151],v232 offset:61952
MM v[16:19],a[192:195],v[168:171],v[136:139]
EX v5,v5
MM v[16:19],a[196:199],v[184:187],v[16:19]
TR a[152:153],v233 offset:49664
CV v2,v4,v5
MM v[16:19],a[200:203],v[200:203],v[16:19]
EX v6,v6
MM v[16:19],a[204:207],v[216:219],v[16:19]
TR a[154:155],v233 offset:53760
TR a[156:157],v233 offset:57856
MM v[20:23],a[220:223],v[216:219],v[136:139]
s_cmp_lg_u32 s70, 2
s_cbranch_scc1 LIP_%=
s_lshr_b32 s77, %[sb], 3
s_lshl_b32 s77, s77, 2
s_add_u32 s77, s77, %[swave]
s_lshl_b32 s77, s77, 9
s_add_u32 s77, s77, LLAJ_%=-LPDP_%=
v_lshrrev_b32 v253, 1, v248
v_add_u32 v253, s77, v253
s_mov_b32 s77, LKEND_ka-LPDP_%=-16
v_min_u32 v253, s77, v253
s_getpc_b64 s[98:99]
LPDP_%=:
global_load_dword v253, v253, s[98:99]
LIP_%=:
MM v[20:23],a[216:219],v[200:203],v[20:23]
EX v7,v7
MM v[20:23],a[212:215],v[184:187],v[20:23]
TR a[158:159],v233 offset:61952
CV v3,v6,v7
MM v[20:23],a[208:211],v[168:171],v[20:23]
EX v8,v8
MM v[28:31],a[208:211],v[172:175],v[140:143]
EX v9,v9
MM v[28:31],a[212:215],v[188:191],v[28:31]
TR a[160:161],v232 offset:50176
CV v8,v8,v9
MM v[28:31],a[216:219],v[204:207],v[28:31]
EX v10,v10
MM v[28:31],a[220:223],v[220:223],v[28:31]
EX v11,v11
MM v[24:27],a[204:207],v[220:223],v[140:143]
TR a[162:163],v232 offset:54272
CV v9,v10,v11
MM v[24:27],a[200:203],v[204:207],v[24:27]
TR a[164:165],v232 offset:58368
TR a[166:167],v232 offset:62464
MM v[24:27],a[196:199],v[188:191],v[24:27]
EX v12,v12
MM v[24:27],a[192:195],v[172:175],v[24:27]
EX v13,v13
MM v[32:35],a[224:227],v[160:163],v[128:131]
TR a[168:169],v233 offset:50176
CV v10,v12,v13
MM v[32:35],a[228:231],v[176:179],v[32:35]
EX v14,v14
MM v[32:35],a[232:235],v[192:195],v[32:35]
TR a[170:171],v233 offset:54272
TR a[172:173],v233 offset:58368
MM v[32:35],a[236:239],v[208:211],v[32:35]
EX v15,v15
MM v[36:39],a[252:255],v[208:211],v[128:131]
LPG8_%=:
s_cbranch_execz LPG12_%=
TR a[174:175],v233 offset:62464
CV v11,v14,v15
MM v[36:39],a[248:251],v[192:195],v[36:39]
EX v16,v16
MM v[36:39],a[244:247],v[176:179],v[36:39]
EX v17,v17
MM v[36:39],a[240:243],v[160:163],v[36:39]
TR a[176:177],v232 offset:50688
CV v16,v16,v17
MM v[44:47],a[240:243],v[164:167],v[132:135]
EX v18,v18
MM v[44:47],a[244:247],v[180:183],v[44:47]
TR a[178:179],v232 offset:54784
TR a[180:181],v232 offset:58880
MM v[44:47],a[248:251],v[196:199],v[44:47]
EX v19,v19
MM v[44:47],a[252:255],v[212:215],v[44:47]
TR a[182:183],v232 offset:62976
CV v17,v18,v19
MM v[40:43],a[236:239],v[212:215],v[132:135]
EX v20,v20
MM v[40:43],a[232:235],v[196:199],v[40:43]
EX v21,v21
MM v[40:43],a[228:231],v[180:183],v[40:43]
TR a[184:185],v233 offset:50688
CV v18,v20,v21
MM v[40:43],a[224:227],v[164:167],v[40:43]
EX v22,v22
MM v[48:51],a[224:227],v[168:171],v[136:139]
TR a[186:187],v233 offset:54784
TR a[188:189],v233 offset:58880
MM v[48:51],a[228:231],v[184:187],v[48:51]
EX v23,v23
MM v[48:51],a[232:235],v[200:203],v[48:51]
TR a[190:191],v233 offset:62976
CV v19,v22,v23
MM v[48:51],a[236:239],v[216:219],v[48:51]
EX v24,v24
MM v[52:55],a[252:255],v[216:219],v[136:139]
EX v25,v25
MM v[52:55],a[248:251],v[200:203],v[52:55]
EX v26,v26
MM v[52:55],a[244:247],v[184:187],v[52:55]
EX v27,v27
MM v[52:55],a[240:243],v[168:171],v[52:55]
EX v28,v28
MM v[60:63],a[240:243],v[172:175],v[140:143]
EX v29,v29
MM v[60:63],a[244:247],v[188:191],v[60:63]
EX v30,v30
MM v[60:63],a[248:251],v[204:207],v[60:63]
EX v31,v31
MM v[60:63],a[252:255],v[220:223],v[60:63]
EX v32,v32
MM v[56:59],a[236:239],v[220:223],v[140:143]
EX v33,v33
MM v[56:59],a[232:235],v[204:207],v[56:59]
EX v34,v34
MM v[56:59],a[228:231],v[188:191],v[56:59]
EX v35,v35
MM v[56:59],a[224:227],v[172:175],v[56:59]
s_waitcnt vmcnt(12) lgkmcnt(0)
s_barrier
MM a[0:3],a[128:131],v[64:67],a[0:3]
s_add_u32 m0, s65, 49152
CV v24,v24,v25
CV v25,v26,v27
MM a[0:3],a[132:135],v[68:71],a[0:3]
RD a[192:195],v224 offset:16384
CV v26,v28,v29
MM a[8:11],a[132:135],v[76:79],a[8:11]
RD a[196:199],v225 offset:16384
CV v27,v30,v31
MM a[8:11],a[128:131],v[72:75],a[8:11]
buffer_load_dwordx4 v228, s[8:11], s64 offen offset:0 lds
MM a[16:19],a[128:131],v[80:83],a[16:19]
RD a[200:203],v226 offset:16384
CV v4,v32,v33
MM a[16:19],a[132:135],v[84:87],a[16:19]
RD a[204:207],v227 offset:16384
CV v5,v34,v35
MM a[24:27],a[132:135],v[92:95],a[24:27]
buffer_load_dwordx4 v229, s[8:11], s64 offen offset:1024 lds
MM a[24:27],a[128:131],v[88:91],a[24:27]
RD a[208:211],v224 offset:20480
RD a[212:215],v225 offset:20480
MM a[28:31],a[136:139],v[88:91],a[28:31]
buffer_load_dwordx4 v230, s[8:11], s64 offen offset:2048 lds
MM a[28:31],a[140:143],v[92:95],a[28:31]
EX v36,v36
MM a[20:23],a[140:143],v[84:87],a[20:23]
EX v37,v37
MM a[20:23],a[136:139],v[80:83],a[20:23]
RD a[216:219],v226 offset:20480
CV v6,v36,v37
MM a[12:15],a[136:139],v[72:75],a[12:15]
EX v38,v38
MM a[12:15],a[140:143],v[76:79],a[12:15]
RD a[220:223],v227 offset:20480
RD a[224:227],v224 offset:24576
MM a[4:7],a[140:143],v[68:71],a[4:7]
EX v39,v39
MM a[4:7],a[136:139],v[64:67],a[4:7]
RD a[228:231],v225 offset:24576
CV v7,v38,v39
MM a[32:35],a[144:147],v[64:67],a[32:35]
EX v40,v40
MM a[32:35],a[148:151],v[68:71],a[32:35]
buffer_load_dwordx4 v231, s[8:11], s64 offen offset:3072 lds
MM a[40:43],a[148:151],v[76:79],a[40:43]
s_add_u32 m0, s65, 114688
EX v41,v41
MM a[40:43],a[144:147],v[72:75],a[40:43]
RD a[232:235],v226 offset:24576
CV v12,v40,v41
MM a[48:51],a[144:147],v[80:83],a[48:51]
EX v42,v42
MM a[48:51],a[148:151],v[84:87],a[48:51]
EX v43,v43
MM a[56:59],a[148:151],v[92:95],a[56:59]
RD a[236:239],v227 offset:24576
CV v13,v42,v43
MM a[56:59],a[144:147],v[88:91],a[56:59]
EX v44,v44
MM a[60:63],a[152:155],v[88:91],a[60:63]
buffer_load_dwordx4 v235, s[12:15], s81 offen offset:0 lds
MM a[60:63],a[156:159],v[92:95],a[60:63]
EX v45,v45
MM a[52:55],a[156:159],v[84:87],a[52:55]
RD a[240:243],v224 offset:28672
CV v14,v44,v45
MM a[52:55],a[152:155],v[80:83],a[52:55]
EX v46,v46
MM a[44:47],a[152:155],v[72:75],a[44:47]
EX v47,v47
MM a[44:47],a[156:159],v[76:79],a[44:47]
RD a[244:247],v225 offset:28672
CV v15,v46,v47
MM a[36:39],a[156:159],v[68:71],a[36:39]
buffer_load_dwordx4 v235, s[12:15], s64 offen offset:1024 lds
MM a[36:39],a[152:155],v[64:67],a[36:39]
EX v48,v48
MM a[64:67],a[160:163],v[64:67],a[64:67]
EX v49,v49
MM a[64:67],a[164:167],v[68:71],a[64:67]
RD a[248:251],v226 offset:28672
CV v20,v48,v49
MM a[72:75],a[164:167],v[76:79],a[72:75]
EX v50,v50
MM a[72:75],a[160:163],v[72:75],a[72:75]
EX v51,v51
MM a[80:83],a[160:163],v[80:83],a[80:83]
RD a[252:255],v227 offset:28672
CV v21,v50,v51
MM a[80:83],a[164:167],v[84:87],a[80:83]
buffer_load_dwordx4 v235, s[12:15], s81 offen offset:2048 lds
MM a[88:91],a[164:167],v[92:95],a[88:91]
EX v52,v52
MM a[88:91],a[160:163],v[88:91],a[88:91]
EX v53,v53
MM a[92:95],a[168:171],v[88:91],a[92:95]
EX v54,v54
MM a[92:95],a[172:175],v[92:95],a[92:95]
buffer_load_dwordx4 v235, s[12:15], s64 offen offset:3072 lds
MM a[84:87],a[172:175],v[84:87],a[84:87]
EX v55,v55
MM a[84:87],a[168:171],v[80:83],a[84:87]
EX v56,v56
MM a[76:79],a[168:171],v[72:75],a[76:79]
EX v57,v57
MM a[76:79],a[172:175],v[76:79],a[76:79]
EX v58,v58
MM a[68:71],a[172:175],v[68:71],a[68:71]
EX v59,v59
MM a[68:71],a[168:171],v[64:67],a[68:71]
EX v60,v60
MM a[96:99],a[176:179],v[64:67],a[96:99]
EX v61,v61
MM a[96:99],a[180:183],v[68:71],a[96:99]
)ASM"
R"ASM(
EX v62,v62
MM a[104:107],a[180:183],v[76:79],a[104:107]
EX v63,v63
MM a[104:107],a[176:179],v[72:75],a[104:107]
CV v22,v52,v53
CV v23,v54,v55
MM a[112:115],a[176:179],v[80:83],a[112:115]
CV v28,v56,v57
CV v29,v58,v59
MM a[112:115],a[180:183],v[84:87],a[112:115]
CV v30,v60,v61
CV v31,v62,v63
MM a[120:123],a[180:183],v[92:95],a[120:123]
MM a[120:123],a[176:179],v[88:91],a[120:123]
MM a[124:127],a[184:187],v[88:91],a[124:127]
v_add_f32 v244, v144, v145
v_add_f32 v245, v148, v149
MM a[124:127],a[188:191],v[92:95],a[124:127]
v_add_f32 v246, v152, v153
v_add_f32 v247, v156, v157
MM a[116:119],a[188:191],v[84:87],a[116:119]
MM a[116:119],a[184:187],v[80:83],a[116:119]
MM a[108:111],a[184:187],v[72:75],a[108:111]
MM a[108:111],a[188:191],v[76:79],a[108:111]
MM a[100:103],a[188:191],v[68:71],a[100:103]
MM a[100:103],a[184:187],v[64:67],a[100:103]
v_smfmac_f32_16x16x64_bf16 v[144:147], v[236:239], v[0:7], v234
v_smfmac_f32_16x16x64_bf16 v[148:151], v[236:239], v[8:15], v234
v_smfmac_f32_16x16x64_bf16 v[152:155], v[236:239], v[16:23], v234
v_smfmac_f32_16x16x64_bf16 v[156:159], v[236:239], v[24:31], v234
s_waitcnt lgkmcnt(0)
MM v[64:67],a[192:195],v[160:163],v[128:131]
s_add_u32 s64, s64, 16384
TR a[128:129],v232 offset:0
s_and_b32 s64, s64, 524287
MM v[64:67],a[196:199],v[176:179],v[64:67]
s_add_u32 s81, s64, 896
TR a[130:131],v232 offset:4096
v_max3_f32 v249, v144, v145, v148
MM v[64:67],a[200:203],v[192:195],v[64:67]
TR a[132:133],v232 offset:8192
v_max3_f32 v249, v249, v149, v152
MM v[64:67],a[204:207],v[208:211],v[64:67]
TR a[134:135],v232 offset:12288
v_max3_f32 v249, v249, v153, v156
MM v[68:71],a[220:223],v[208:211],v[128:131]
TR a[136:137],v233 offset:0
v_max_f32 v249, v249, v157
MM v[68:71],a[216:219],v[192:195],v[68:71]
TR a[138:139],v233 offset:4096
v_cmp_lt_f32 vcc, s71, v249
MM v[68:71],a[212:215],v[176:179],v[68:71]
TR a[140:141],v233 offset:8192
TR a[142:143],v233 offset:12288
MM v[68:71],a[208:211],v[160:163],v[68:71]
s_cbranch_vccnz LSITE8_%=
LBACK8_%=:
MM v[76:79],a[208:211],v[164:167],v[132:135]
EX v64,v64
MM v[76:79],a[212:215],v[180:183],v[76:79]
EX v65,v65
MM v[76:79],a[216:219],v[196:199],v[76:79]
TR a[144:145],v232 offset:512
CV v64,v64,v65
MM v[76:79],a[220:223],v[212:215],v[76:79]
EX v66,v66
MM v[72:75],a[204:207],v[212:215],v[132:135]
EX v67,v67
MM v[72:75],a[200:203],v[196:199],v[72:75]
TR a[146:147],v232 offset:4608
CV v65,v66,v67
MM v[72:75],a[196:199],v[180:183],v[72:75]
EX v68,v68
MM v[72:75],a[192:195],v[164:167],v[72:75]
TR a[148:149],v232 offset:8704
TR a[150:151],v232 offset:12800
MM v[80:83],a[192:195],v[168:171],v[136:139]
EX v69,v69
MM v[80:83],a[196:199],v[184:187],v[80:83]
TR a[152:153],v233 offset:512
CV v66,v68,v69
MM v[80:83],a[200:203],v[200:203],v[80:83]
EX v70,v70
MM v[80:83],a[204:207],v[216:219],v[80:83]
TR a[154:155],v233 offset:4608
TR a[156:157],v233 offset:8704
MM v[84:87],a[220:223],v[216:219],v[136:139]
s_cmp_lg_u32 s70, s82
s_cbranch_scc1 LWV_%=
v_lshlrev_b32 v250, 3, v248
v_min_u32 v250, 4992, v250
s_sub_u32 s91, 5242880, s26
s_sub_u32 s60, s24, s91
s_subb_u32 s61, s25, 0
s_mov_b32 s62, 5242880
s_mov_b32 s63, 131072
s_lshl_b32 s90, s16, 2
s_add_u32 s90, s90, %[swave]
s_mul_i32 s90, s90, 10240
s_add_u32 s90, s90, 5120
s_add_u32 s91, %[slds], 135168
s_mov_b32 m0, s91
s_nop 0
buffer_load_dword v250, s[60:63], s90 offen offset:0 lds
LWV_%=:
MM v[84:87],a[216:219],v[200:203],v[84:87]
EX v71,v71
MM v[84:87],a[212:215],v[184:187],v[84:87]
TR a[158:159],v233 offset:12800
CV v67,v70,v71
MM v[84:87],a[208:211],v[168:171],v[84:87]
EX v72,v72
MM v[92:95],a[208:211],v[172:175],v[140:143]
EX v73,v73
MM v[92:95],a[212:215],v[188:191],v[92:95]
TR a[160:161],v232 offset:1024
CV v72,v72,v73
MM v[92:95],a[216:219],v[204:207],v[92:95]
EX v74,v74
MM v[92:95],a[220:223],v[220:223],v[92:95]
EX v75,v75
MM v[88:91],a[204:207],v[220:223],v[140:143]
TR a[162:163],v232 offset:5120
CV v73,v74,v75
MM v[88:91],a[200:203],v[204:207],v[88:91]
TR a[164:165],v232 offset:9216
TR a[166:167],v232 offset:13312
MM v[88:91],a[196:199],v[188:191],v[88:91]
EX v76,v76
MM v[88:91],a[192:195],v[172:175],v[88:91]
EX v77,v77
MM v[96:99],a[224:227],v[160:163],v[128:131]
TR a[168:169],v233 offset:1024
CV v74,v76,v77
MM v[96:99],a[228:231],v[176:179],v[96:99]
EX v78,v78
MM v[96:99],a[232:235],v[192:195],v[96:99]
TR a[170:171],v233 offset:5120
TR a[172:173],v233 offset:9216
MM v[96:99],a[236:239],v[208:211],v[96:99]
EX v79,v79
MM v[100:103],a[252:255],v[208:211],v[128:131]
TR a[174:175],v233 offset:13312
CV v75,v78,v79
MM v[100:103],a[248:251],v[192:195],v[100:103]
EX v80,v80
MM v[100:103],a[244:247],v[176:179],v[100:103]
EX v81,v81
MM v[100:103],a[240:243],v[160:163],v[100:103]
TR a[176:177],v232 offset:1536
CV v80,v80,v81
MM v[108:111],a[240:243],v[164:167],v[132:135]
EX v82,v82
MM v[108:111],a[244:247],v[180:183],v[108:111]
TR a[178:179],v232 offset:5632
TR a[180:181],v232 offset:9728
MM v[108:111],a[248:251],v[196:199],v[108:111]
EX v83,v83
MM v[108:111],a[252:255],v[212:215],v[108:111]
TR a[182:183],v232 offset:13824
CV v81,v82,v83
MM v[104:107],a[236:239],v[212:215],v[132:135]
EX v84,v84
MM v[104:107],a[232:235],v[196:199],v[104:107]
EX v85,v85
MM v[104:107],a[228:231],v[180:183],v[104:107]
TR a[184:185],v233 offset:1536
CV v82,v84,v85
MM v[104:107],a[224:227],v[164:167],v[104:107]
EX v86,v86
MM v[112:115],a[224:227],v[168:171],v[136:139]
TR a[186:187],v233 offset:5632
TR a[188:189],v233 offset:9728
MM v[112:115],a[228:231],v[184:187],v[112:115]
EX v87,v87
MM v[112:115],a[232:235],v[200:203],v[112:115]
TR a[190:191],v233 offset:13824
CV v83,v86,v87
MM v[112:115],a[236:239],v[216:219],v[112:115]
EX v88,v88
MM v[116:119],a[252:255],v[216:219],v[136:139]
EX v89,v89
MM v[116:119],a[248:251],v[200:203],v[116:119]
EX v90,v90
MM v[116:119],a[244:247],v[184:187],v[116:119]
EX v91,v91
MM v[116:119],a[240:243],v[168:171],v[116:119]
EX v92,v92
MM v[124:127],a[240:243],v[172:175],v[140:143]
EX v93,v93
MM v[124:127],a[244:247],v[188:191],v[124:127]
EX v94,v94
MM v[124:127],a[248:251],v[204:207],v[124:127]
EX v95,v95
MM v[124:127],a[252:255],v[220:223],v[124:127]
EX v96,v96
MM v[120:123],a[236:239],v[220:223],v[140:143]
EX v97,v97
MM v[120:123],a[232:235],v[204:207],v[120:123]
EX v98,v98
MM v[120:123],a[228:231],v[188:191],v[120:123]
s_cmp_lg_u32 s70, 1
s_cbranch_scc1 LTP_%=
s_getpc_b64 s[98:99]
LTA1_%=:
s_min_u32 s73, %[swave], 3
s_lshl_b32 s73, s73, 12
s_add_u32 s73, s73, LPG23_%=-LTA1_%=
s_add_u32 s98, s98, s73
s_addc_u32 s99, s99, 0
s_mov_b64 exec, 0
s_setpc_b64 s[98:99]
LTR1_%=:
s_mov_b64 exec, -1
LTP_%=:
MM v[120:123],a[224:227],v[172:175],v[120:123]
s_waitcnt vmcnt(12) lgkmcnt(0)
s_barrier
MM a[0:3],a[128:131],v[0:3],a[0:3]
s_add_u32 m0, s65, 0
CV v88,v88,v89
CV v89,v90,v91
MM a[0:3],a[132:135],v[4:7],a[0:3]
RD a[192:195],v224 offset:32768
CV v90,v92,v93
MM a[8:11],a[132:135],v[12:15],a[8:11]
RD a[196:199],v225 offset:32768
CV v91,v94,v95
MM a[8:11],a[128:131],v[8:11],a[8:11]
buffer_load_dwordx4 v228, s[8:11], s64 offen offset:0 lds
MM a[16:19],a[128:131],v[16:19],a[16:19]
RD a[200:203],v226 offset:32768
CV v68,v96,v97
MM a[16:19],a[132:135],v[20:23],a[16:19]
RD a[204:207],v227 offset:32768
RD a[208:211],v224 offset:36864
MM a[24:27],a[132:135],v[28:31],a[24:27]
buffer_load_dwordx4 v229, s[8:11], s64 offen offset:1024 lds
MM a[24:27],a[128:131],v[24:27],a[24:27]
EX v99,v99
MM a[28:31],a[136:139],v[24:27],a[28:31]
RD a[212:215],v225 offset:36864
CV v69,v98,v99
MM a[28:31],a[140:143],v[28:31],a[28:31]
EX v100,v100
MM a[20:23],a[140:143],v[20:23],a[20:23]
buffer_load_dwordx4 v230, s[8:11], s64 offen offset:2048 lds
MM a[20:23],a[136:139],v[16:19],a[20:23]
EX v101,v101
MM a[12:15],a[136:139],v[8:11],a[12:15]
RD a[216:219],v226 offset:36864
CV v70,v100,v101
MM a[12:15],a[140:143],v[12:15],a[12:15]
EX v102,v102
MM a[4:7],a[140:143],v[4:7],a[4:7]
EX v103,v103
MM a[4:7],a[136:139],v[0:3],a[4:7]
RD a[220:223],v227 offset:36864
CV v71,v102,v103
MM a[32:35],a[144:147],v[0:3],a[32:35]
EX v104,v104
MM a[32:35],a[148:151],v[4:7],a[32:35]
buffer_load_dwordx4 v231, s[8:11], s64 offen offset:3072 lds
MM a[40:43],a[148:151],v[12:15],a[40:43]
s_add_u32 m0, s65, 65536
EX v105,v105
MM a[40:43],a[144:147],v[8:11],a[40:43]
RD a[224:227],v224 offset:40960
CV v76,v104,v105
MM a[48:51],a[144:147],v[16:19],a[48:51]
EX v106,v106
MM a[48:51],a[148:151],v[20:23],a[48:51]
EX v107,v107
MM a[56:59],a[148:151],v[28:31],a[56:59]
RD a[228:231],v225 offset:40960
CV v77,v106,v107
MM a[56:59],a[144:147],v[24:27],a[56:59]
EX v108,v108
MM a[60:63],a[152:155],v[24:27],a[60:63]
buffer_load_dwordx4 v235, s[12:15], s81 offen offset:0 lds
MM a[60:63],a[156:159],v[28:31],a[60:63]
EX v109,v109
MM a[52:55],a[156:159],v[20:23],a[52:55]
RD a[232:235],v226 offset:40960
CV v78,v108,v109
MM a[52:55],a[152:155],v[16:19],a[52:55]
EX v110,v110
MM a[44:47],a[152:155],v[8:11],a[44:47]
EX v111,v111
MM a[44:47],a[156:159],v[12:15],a[44:47]
RD a[236:239],v227 offset:40960
CV v79,v110,v111
MM a[36:39],a[156:159],v[4:7],a[36:39]
EX v112,v112
MM a[36:39],a[152:155],v[0:3],a[36:39]
buffer_load_dwordx4 v235, s[12:15], s64 offen offset:1024 lds
MM a[64:67],a[160:163],v[0:3],a[64:67]
EX v113,v113
MM a[64:67],a[164:167],v[4:7],a[64:67]
RD a[240:243],v224 offset:45056
CV v84,v112,v113
MM a[72:75],a[164:167],v[12:15],a[72:75]
EX v114,v114
MM a[72:75],a[160:163],v[8:11],a[72:75]
RD a[244:247],v225 offset:45056
RD a[248:251],v226 offset:45056
MM a[80:83],a[160:163],v[16:19],a[80:83]
EX v115,v115
MM a[80:83],a[164:167],v[20:23],a[80:83]
RD a[252:255],v227 offset:45056
CV v85,v114,v115
MM a[88:91],a[164:167],v[28:31],a[88:91]
EX v116,v116
MM a[88:91],a[160:163],v[24:27],a[88:91]
buffer_load_dwordx4 v235, s[12:15], s81 offen offset:2048 lds
MM a[92:95],a[168:171],v[24:27],a[92:95]
EX v117,v117
MM a[92:95],a[172:175],v[28:31],a[92:95]
EX v118,v118
MM a[84:87],a[172:175],v[20:23],a[84:87]
EX v119,v119
MM a[84:87],a[168:171],v[16:19],a[84:87]
buffer_load_dwordx4 v235, s[12:15], s64 offen offset:3072 lds
MM a[76:79],a[168:171],v[8:11],a[76:79]
EX v120,v120
MM a[76:79],a[172:175],v[12:15],a[76:79]
EX v121,v121
MM a[68:71],a[172:175],v[4:7],a[68:71]
EX v122,v122
MM a[68:71],a[168:171],v[0:3],a[68:71]
EX v123,v123
MM a[96:99],a[176:179],v[0:3],a[96:99]
EX v124,v124
MM a[96:99],a[180:183],v[4:7],a[96:99]
EX v125,v125
MM a[104:107],a[180:183],v[12:15],a[104:107]
EX v126,v126
MM a[104:107],a[176:179],v[8:11],a[104:107]
EX v127,v127
MM a[112:115],a[176:179],v[16:19],a[112:115]
CV v86,v116,v117
CV v87,v118,v119
MM a[112:115],a[180:183],v[20:23],a[112:115]
CV v92,v120,v121
CV v93,v122,v123
MM a[120:123],a[180:183],v[28:31],a[120:123]
CV v94,v124,v125
CV v95,v126,v127
MM a[120:123],a[176:179],v[24:27],a[120:123]
MM a[124:127],a[184:187],v[24:27],a[124:127]
v_add_f32 v244, v144, v145
v_add_f32 v245, v148, v149
MM a[124:127],a[188:191],v[28:31],a[124:127]
v_add_f32 v246, v152, v153
v_add_f32 v247, v156, v157
MM a[116:119],a[188:191],v[20:23],a[116:119]
MM a[116:119],a[184:187],v[16:19],a[116:119]
MM a[108:111],a[184:187],v[8:11],a[108:111]
MM a[108:111],a[188:191],v[12:15],a[108:111]
MM a[100:103],a[188:191],v[4:7],a[100:103]
MM a[100:103],a[184:187],v[0:3],a[100:103]
v_smfmac_f32_16x16x64_bf16 v[144:147], v[236:239], v[64:71], v234
v_smfmac_f32_16x16x64_bf16 v[148:151], v[236:239], v[72:79], v234
v_smfmac_f32_16x16x64_bf16 v[152:155], v[236:239], v[80:87], v234
v_smfmac_f32_16x16x64_bf16 v[156:159], v[236:239], v[88:95], v234
s_waitcnt lgkmcnt(0)
s_sub_u32 s70, s70, 1
s_cmp_lg_u32 s70, 0
s_cbranch_scc1 LLAP_%=
s_branch LLAJ_%=
s_nop 0
s_nop 0
s_nop 0
s_nop 0
s_nop 0
s_nop 0
s_nop 0
s_nop 0
s_nop 0
s_nop 0
s_nop 0
s_nop 0
s_nop 0
s_nop 0
s_nop 0
s_nop 0
s_nop 0
s_nop 0
s_nop 0
s_nop 0
s_nop 0
s_nop 0
s_nop 0
s_nop 0
s_nop 0
s_nop 0
s_nop 0
s_nop 0
s_nop 0
s_nop 0
s_nop 0
s_nop 0
s_nop 0
s_nop 0
s_nop 0
s_nop 0
s_nop 0
s_nop 0
s_nop 0
s_nop 0
s_nop 0
s_nop 0
s_nop 0
s_nop 0
s_nop 0
s_nop 0
s_nop 0
s_nop 0
s_nop 0
s_nop 0
s_nop 0
s_nop 0
s_nop 0
s_nop 0
s_nop 0
s_nop 0
s_nop 0
s_nop 0
s_nop 0
s_nop 0
s_nop 0
s_nop 0
s_nop 0
s_nop 0
s_nop 0
s_nop 0
s_nop 0
s_nop 0
s_nop 0
)ASM"
R"ASM(
s_nop 0
s_nop 0
s_nop 0
s_nop 0
s_nop 0
s_nop 0
s_nop 0
s_nop 0
s_nop 0
s_nop 0
s_nop 0
s_nop 0
s_nop 0
s_nop 0
s_nop 0
s_nop 0
s_nop 0
s_nop 0
s_nop 0
LPG9_%=:
s_cbranch_execz LTR0_%=
s_nop 0
s_nop 0
s_nop 0
s_nop 0
s_nop 0
s_nop 0
s_nop 0
s_nop 0
s_nop 0
s_nop 0
s_nop 0
s_nop 0
s_nop 0
s_nop 0
s_nop 0
s_nop 0
s_nop 0
s_nop 0
s_nop 0
s_nop 0
s_nop 0
s_nop 0
s_nop 0
s_nop 0
s_nop 0
s_nop 0
s_nop 0
s_nop 0
s_nop 0
s_nop 0
s_nop 0
s_nop 0
s_nop 0
s_nop 0
s_nop 0
s_nop 0
s_nop 0
s_nop 0
s_nop 0
s_nop 0
s_nop 0
s_nop 0
s_nop 0
s_nop 0
s_nop 0
s_nop 0
s_nop 0
s_nop 0
s_nop 0
s_nop 0
s_nop 0
s_nop 0
s_nop 0
s_nop 0
s_nop 0
s_nop 0
s_nop 0
s_nop 0
s_nop 0
s_nop 0
s_nop 0
s_nop 0
s_nop 0
s_nop 0
s_nop 0
s_nop 0
s_nop 0
s_nop 0
s_nop 0
s_nop 0
s_nop 0
s_nop 0
s_nop 0
s_nop 0
s_nop 0
s_nop 0
s_nop 0
s_nop 0
s_nop 0
s_nop 0
s_nop 0
s_nop 0
s_nop 0
s_nop 0
s_nop 0
s_nop 0
s_nop 0
s_nop 0
s_nop 0
s_nop 0
s_nop 0
s_nop 0
s_nop 0
s_nop 0
s_nop 0
s_nop 0
s_nop 0
s_nop 0
s_nop 0
s_nop 0
s_nop 0
s_nop 0
s_nop 0
s_nop 0
s_nop 0
s_nop 0
s_nop 0
s_nop 0
s_nop 0
s_nop 0
s_nop 0
s_nop 0
s_nop 0
s_nop 0
s_nop 0
s_nop 0
s_nop 0
s_nop 0
s_nop 0
s_nop 0
s_nop 0
s_nop 0
s_nop 0
s_nop 0
s_nop 0
s_nop 0
s_nop 0
s_nop 0
s_nop 0
s_nop 0
s_nop 0
s_nop 0
s_nop 0
s_nop 0
s_nop 0
s_nop 0
s_nop 0
s_nop 0
s_nop 0
s_nop 0
s_nop 0
s_nop 0
s_nop 0
s_nop 0
s_nop 0
s_nop 0
s_nop 0
s_nop 0
s_nop 0
s_nop 0
s_nop 0
s_nop 0
s_nop 0
s_nop 0
s_nop 0
s_nop 0
s_nop 0
s_nop 0
s_nop 0
s_nop 0
s_nop 0
s_nop 0
s_nop 0
s_nop 0
s_nop 0
s_nop 0
s_nop 0
s_nop 0
s_nop 0
s_nop 0
s_nop 0
s_nop 0
s_nop 0
s_nop 0
s_nop 0
s_nop 0
s_nop 0
s_nop 0
s_nop 0
s_nop 0
s_nop 0
s_nop 0
s_nop 0
s_nop 0
s_nop 0
s_nop 0
s_nop 0
s_nop 0
s_nop 0
s_nop 0
s_nop 0
s_nop 0
s_nop 0
s_nop 0
s_nop 0
s_nop 0
s_nop 0
s_nop 0
s_nop 0
s_nop 0
s_nop 0
s_nop 0
s_nop 0
s_nop 0
s_nop 0
s_nop 0
s_nop 0
s_nop 0
s_nop 0
s_nop 0
s_nop 0
s_nop 0
s_nop 0
s_nop 0
s_nop 0
s_nop 0
s_nop 0
s_nop 0
s_nop 0
s_nop 0
s_nop 0
s_nop 0
s_nop 0
s_nop 0
s_nop 0
s_nop 0
s_nop 0
s_nop 0
s_nop 0
s_nop 0
s_nop 0
s_nop 0
s_nop 0
s_nop 0
s_nop 0
s_nop 0
s_nop 0
s_nop 0
s_nop 0
s_nop 0
s_nop 0
s_nop 0
s_nop 0
s_nop 0
s_nop 0
s_nop 0
s_nop 0
s_nop 0
s_nop 0
s_nop 0
s_nop 0
s_nop 0
s_nop 0
s_nop 0
s_nop 0
s_nop 0
s_nop 0
s_nop 0
s_nop 0
s_nop 0
s_nop 0
s_nop 0
s_nop 0
s_nop 0
s_nop 0
s_nop 0
s_nop 0
s_nop 0
s_nop 0
s_nop 0
s_nop 0
s_nop 0
s_nop 0
s_nop 0
s_nop 0
s_nop 0
s_nop 0
s_nop 0
s_nop 0
s_nop 0
s_nop 0
s_nop 0
s_nop 0
s_nop 0
s_nop 0
s_nop 0
s_nop 0
s_nop 0
s_nop 0
s_nop 0
s_nop 0
s_nop 0
s_nop 0
s_nop 0
s_nop 0
s_nop 0
s_nop 0
s_nop 0
s_nop 0
s_nop 0
s_nop 0
s_nop 0
s_nop 0
s_nop 0
s_nop 0
s_nop 0
s_nop 0
s_nop 0
s_nop 0
s_nop 0
s_nop 0
s_nop 0
s_nop 0
s_nop 0
s_nop 0
s_nop 0
s_nop 0
s_nop 0
s_nop 0
s_nop 0
s_nop 0
s_nop 0
s_nop 0
s_nop 0
s_nop 0
s_nop 0
s_nop 0
s_nop 0
s_nop 0
s_nop 0
s_nop 0
s_nop 0
s_nop 0
s_nop 0
s_nop 0
s_nop 0
s_nop 0
s_nop 0
s_nop 0
s_nop 0
s_nop 0
s_nop 0
s_nop 0
s_nop 0
s_nop 0
s_nop 0
s_nop 0
s_nop 0
s_nop 0
s_nop 0
s_nop 0
s_nop 0
s_nop 0
s_nop 0
s_nop 0
s_nop 0
s_nop 0
s_nop 0
s_nop 0
s_nop 0
s_nop 0
s_nop 0
s_nop 0
s_nop 0
s_nop 0
s_nop 0
s_nop 0
s_nop 0
s_nop 0
s_nop 0
s_nop 0
s_nop 0
s_nop 0
s_nop 0
s_nop 0
s_nop 0
s_nop 0
s_nop 0
s_nop 0
s_nop 0
s_nop 0
s_nop 0
s_nop 0
s_nop 0
s_nop 0
s_nop 0
s_nop 0
s_nop 0
s_nop 0
s_nop 0
s_nop 0
s_nop 0
s_nop 0
s_nop 0
s_nop 0
s_nop 0
s_nop 0
s_nop 0
s_nop 0
s_nop 0
s_nop 0
s_nop 0
s_nop 0
s_nop 0
s_nop 0
s_nop 0
s_nop 0
s_nop 0
s_nop 0
s_nop 0
s_nop 0
s_nop 0
s_nop 0
s_nop 0
s_nop 0
s_nop 0
s_nop 0
s_nop 0
s_nop 0
s_nop 0
s_nop 0
s_nop 0
s_nop 0
s_nop 0
s_nop 0
s_nop 0
s_nop 0
s_nop 0
s_nop 0
s_nop 0
s_nop 0
s_nop 0
s_nop 0
s_nop 0
s_nop 0
s_nop 0
s_nop 0
s_nop 0
s_nop 0
s_nop 0
s_nop 0
s_nop 0
s_nop 0
s_nop 0
s_nop 0
s_nop 0
s_nop 0
s_nop 0
s_nop 0
s_nop 0
s_nop 0
s_nop 0
s_nop 0
s_nop 0
s_nop 0
s_nop 0
s_nop 0
s_nop 0
s_nop 0
s_nop 0
s_nop 0
s_nop 0
s_nop 0
s_nop 0
s_nop 0
s_nop 0
s_nop 0
s_nop 0
s_nop 0
s_nop 0
s_nop 0
s_nop 0
s_nop 0
s_nop 0
s_nop 0
s_nop 0
s_nop 0
s_nop 0
s_nop 0
s_nop 0
s_nop 0
s_nop 0
s_nop 0
s_nop 0
s_nop 0
s_nop 0
s_nop 0
s_nop 0
s_nop 0
s_nop 0
s_nop 0
s_nop 0
s_nop 0
s_nop 0
s_nop 0
s_nop 0
s_nop 0
s_nop 0
s_nop 0
s_nop 0
s_nop 0
s_nop 0
s_nop 0
s_nop 0
s_nop 0
s_nop 0
s_nop 0
s_nop 0
s_nop 0
s_nop 0
s_nop 0
s_nop 0
s_nop 0
s_nop 0
s_nop 0
s_nop 0
s_nop 0
s_nop 0
s_nop 0
s_nop 0
s_nop 0
s_nop 0
s_nop 0
s_nop 0
s_nop 0
s_nop 0
s_nop 0
s_nop 0
s_nop 0
s_nop 0
s_nop 0
s_nop 0
s_nop 0
s_nop 0
s_nop 0
s_nop 0
s_nop 0
s_nop 0
s_nop 0
s_nop 0
s_nop 0
s_nop 0
s_nop 0
s_nop 0
s_nop 0
s_nop 0
s_nop 0
s_nop 0
s_nop 0
s_nop 0
s_nop 0
s_nop 0
s_nop 0
s_nop 0
s_nop 0
s_nop 0
s_nop 0
s_nop 0
s_nop 0
s_nop 0
s_nop 0
s_nop 0
s_nop 0
s_nop 0
s_nop 0
s_nop 0
s_nop 0
s_nop 0
s_nop 0
s_nop 0
s_nop 0
s_nop 0
s_nop 0
s_nop 0
s_nop 0
s_nop 0
s_nop 0
s_nop 0
s_nop 0
s_nop 0
s_nop 0
s_nop 0
s_nop 0
s_nop 0
s_nop 0
s_nop 0
s_nop 0
s_nop 0
s_nop 0
s_nop 0
s_nop 0
s_nop 0
s_nop 0
s_nop 0
s_nop 0
s_nop 0
s_nop 0
LLAR_%=:
MM v[0:3],a[192:195],v[160:163],v[128:131]
s_add_u32 s64, s64, 16384
TR a[128:129],v232 offset:16384
s_add_u32 s81, s64, 896
MM v[0:3],a[196:199],v[176:179],v[0:3]
TR a[130:131],v232 offset:20480
v_max3_f32 v249, v144, v145, v148
MM v[0:3],a[200:203],v[192:195],v[0:3]
TR a[132:133],v232 offset:24576
v_max3_f32 v249, v249, v149, v152
MM v[0:3],a[204:207],v[208:211],v[0:3]
TR a[134:135],v232 offset:28672
v_max3_f32 v249, v249, v153, v156
MM v[4:7],a[220:223],v[208:211],v[128:131]
TR a[136:137],v233 offset:16384
v_max_f32 v249, v249, v157
MM v[4:7],a[216:219],v[192:195],v[4:7]
TR a[138:139],v233 offset:20480
v_cmp_lt_f32 vcc, s71, v249
MM v[4:7],a[212:215],v[176:179],v[4:7]
TR a[140:141],v233 offset:24576
TR a[142:143],v233 offset:28672
MM v[4:7],a[208:211],v[160:163],v[4:7]
s_cbranch_vccnz LSITE9_%=
LBACK9_%=:
MM v[12:15],a[208:211],v[164:167],v[132:135]
s_lshl_b32 s17, s16, 2
EX v0,v0
MM v[12:15],a[212:215],v[180:183],v[12:15]
s_add_u32 s17, s17, %[swave]
EX v1,v1
MM v[12:15],a[216:219],v[196:199],v[12:15]
s_sub_u32 s90, s16, 108
TR a[144:145],v232 offset:16896
CV v0,v0,v1
MM v[12:15],a[220:223],v[212:215],v[12:15]
s_min_u32 s90, s90, s16
EX v2,v2
MM v[8:11],a[204:207],v[212:215],v[132:135]
s_lshl_b32 s48, s90, 5
EX v3,v3
MM v[8:11],a[200:203],v[196:199],v[8:11]
s_add_u32 s76, %[slds], 155648
v_add_u32 v250, s76, v248
CV v1,v2,v3
MM v[8:11],a[196:199],v[180:183],v[8:11]
s_lshl_b32 s77, s17, 4
TR a[146:147],v232 offset:20992
TR a[148:149],v232 offset:25088
MM v[8:11],a[192:195],v[164:167],v[8:11]
s_add_u32 s76, s76, s77
TR a[150:151],v232 offset:29184
v_mov_b32 v251, s76
MM v[16:19],a[192:195],v[168:171],v[136:139]
RD v[124:127],v251 offset:0
RD v[96:99],v250 offset:0
MM v[16:19],a[196:199],v[184:187],v[16:19]
RD v[100:103],v250 offset:1024
RD v[104:107],v250 offset:2048
MM v[16:19],a[200:203],v[200:203],v[16:19]
RD v[108:111],v250 offset:3072
RD v[112:115],v250 offset:4096
MM v[16:19],a[204:207],v[216:219],v[16:19]
RD v[116:119],v250 offset:5120
RD v[120:123],v250 offset:6144
MM v[20:23],a[220:223],v[216:219],v[136:139]
EX v4,v4
MM v[20:23],a[216:219],v[200:203],v[20:23]
EX v5,v5
MM v[20:23],a[212:215],v[184:187],v[20:23]
s_waitcnt lgkmcnt(7)
v_readfirstlane_b32 s52, v124
v_readfirstlane_b32 s53, v125
MM v[20:23],a[208:211],v[168:171],v[20:23]
v_readfirstlane_b32 s54, v126
v_readfirstlane_b32 s55, v127
EX v6,v6
MM v[28:31],a[208:211],v[172:175],v[140:143]
RD v[124:127],v250 offset:7168
TR a[152:153],v233 offset:16896
MM v[28:31],a[212:215],v[188:191],v[28:31]
s_waitcnt lgkmcnt(2)
v_max3_u32 v253, v96, v97, v98
v_max3_u32 v251, v99, v100, v101
EX v7,v7
MM v[28:31],a[216:219],v[204:207],v[28:31]
v_max3_u32 v253, v253, v102, v103
v_max3_u32 v251, v251, v104, v105
MM v[28:31],a[220:223],v[220:223],v[28:31]
v_max3_u32 v253, v253, v106, v107
v_max3_u32 v251, v251, v108, v109
EX v8,v8
MM v[24:27],a[204:207],v[220:223],v[140:143]
v_max3_u32 v253, v253, v110, v111
v_max3_u32 v251, v251, v112, v113
MM v[24:27],a[200:203],v[204:207],v[24:27]
v_max3_u32 v253, v253, v114, v115
v_max3_u32 v251, v251, v116, v117
EX v9,v9
MM v[24:27],a[196:199],v[188:191],v[24:27]
TR a[154:155],v233 offset:20992
v_max3_u32 v253, v253, v118, v119
v_max3_u32 v251, v251, v120, v121
MM v[24:27],a[192:195],v[172:175],v[24:27]
v_max3_u32 v253, v253, v122, v123
CV v2,v4,v5
EX v10,v10
MM v[32:35],a[224:227],v[160:163],v[128:131]
s_waitcnt lgkmcnt(2)
TR a[156:157],v233 offset:25088
v_max3_u32 v251, v251, v124, v125
v_max3_u32 v253, v253, v126, v127
EX v11,v11
MM v[32:35],a[228:231],v[176:179],v[32:35]
v_max_u32 v253, v253, v251
v_cmp_lt_u32 s[60:61], s52, v253
MM v[32:35],a[232:235],v[192:195],v[32:35]
TR a[158:159],v233 offset:29184
v_cmp_lt_u32 s[62:63], s53, v253
v_cmp_lt_u32 s[56:57], s54, v253
EX v12,v12
MM v[32:35],a[236:239],v[208:211],v[32:35]
v_cmp_lt_u32 s[58:59], s55, v253
CV v3,v6,v7
MM v[36:39],a[252:255],v[208:211],v[128:131]
TR a[160:161],v232 offset:17408
EX v13,v13
MM v[36:39],a[248:251],v[192:195],v[36:39]
s_bcnt1_i32_b64 s60, s[60:61]
TR a[162:163],v232 offset:21504
CV v8,v8,v9
MM v[36:39],a[244:247],v[176:179],v[36:39]
s_bcnt1_i32_b64 s62, s[62:63]
EX v14,v14
MM v[36:39],a[240:243],v[160:163],v[36:39]
s_bcnt1_i32_b64 s56, s[56:57]
TR a[164:165],v232 offset:25600
TR a[166:167],v232 offset:29696
MM v[44:47],a[240:243],v[164:167],v[132:135]
s_bcnt1_i32_b64 s58, s[58:59]
EX v15,v15
MM v[44:47],a[244:247],v[180:183],v[44:47]
s_min_u32 s76, s60, s62
EX v16,v16
MM v[44:47],a[248:251],v[196:199],v[44:47]
s_min_u32 s77, s56, s58
TR a[168:169],v233 offset:17408
CV v9,v10,v11
MM v[44:47],a[252:255],v[212:215],v[44:47]
s_min_u32 s76, s76, s77
s_cmp_lt_u32 s76, 8
s_cbranch_scc1 LRK_%=
LRKb_%=:
EX v17,v17
MM v[40:43],a[236:239],v[212:215],v[132:135]
v_lshrrev_b32 v252, 6, v248
v_mul_u32_u24 v252, 3456, v252
MM v[40:43],a[232:235],v[196:199],v[40:43]
TR a[170:171],v233 offset:21504
v_and_b32 v251, 48, v248
v_add_u32 v252, v252, v251
EX v18,v18
MM v[40:43],a[228:231],v[180:183],v[40:43]
s_and_b32 s90, %[swave], 1
TR a[172:173],v233 offset:25600
CV v10,v12,v13
MM v[40:43],a[224:227],v[164:167],v[40:43]
s_mul_i32 s90, s90, 276480
EX v19,v19
MM v[48:51],a[224:227],v[168:171],v[136:139]
s_bfe_u32 s91, s48, 131077
TR a[174:175],v233 offset:29696
CV v11,v14,v15
MM v[48:51],a[228:231],v[184:187],v[48:51]
s_mul_i32 s91, s91, 552960
EX v20,v20
MM v[48:51],a[232:235],v[200:203],v[48:51]
s_add_u32 s90, s90, s91
TR a[176:177],v232 offset:17920
TR a[178:179],v232 offset:22016
MM v[48:51],a[236:239],v[216:219],v[48:51]
s_and_b32 s91, s48, 0xffffff80
EX v21,v21
MM v[52:55],a[252:255],v[216:219],v[136:139]
s_add_u32 s90, s90, s91
EX v22,v22
MM v[52:55],a[248:251],v[200:203],v[52:55]
s_and_b32 s91, s48, 16
TR a[180:181],v232 offset:26112
CV v16,v16,v17
MM v[52:55],a[244:247],v[184:187],v[52:55]
s_lshl_b32 s91, s91, 2
EX v23,v23
MM v[52:55],a[240:243],v[168:171],v[52:55]
s_add_u32 s66, s90, s91
TR a[182:183],v232 offset:30208
CV v17,v18,v19
MM v[60:63],a[240:243],v[172:175],v[140:143]
s_mul_i32 s91, %[swave], 5120
EX v24,v24
MM v[60:63],a[244:247],v[188:191],v[60:63]
s_add_u32 s91, s91, %[slds]
TR a[184:185],v233 offset:17920
TR a[186:187],v233 offset:22016
MM v[60:63],a[248:251],v[204:207],v[60:63]
s_add_u32 s67, s91, 131072
TR a[188:189],v233 offset:26112
TR a[190:191],v233 offset:30208
EX v25,v25
MM v[60:63],a[252:255],v[220:223],v[60:63]
EX v26,v26
MM v[56:59],a[236:239],v[220:223],v[140:143]
EX v27,v27
MM v[56:59],a[232:235],v[204:207],v[56:59]
EX v28,v28
MM v[56:59],a[228:231],v[188:191],v[56:59]
EX v29,v29
CV v18,v20,v21
MM v[56:59],a[224:227],v[172:175],v[56:59]
s_waitcnt lgkmcnt(0)
s_barrier
MM a[0:3],a[128:131],v[64:67],a[0:3]
s_add_u32 m0, s65, 16384
CV v19,v22,v23
CV v24,v24,v25
MM a[0:3],a[132:135],v[68:71],a[0:3]
CV v25,v26,v27
CV v26,v28,v29
MM a[8:11],a[132:135],v[76:79],a[8:11]
EX v30,v30
MM a[8:11],a[128:131],v[72:75],a[8:11]
EX v31,v31
MM a[16:19],a[128:131],v[80:83],a[16:19]
RD a[192:195],v224 offset:49152
CV v27,v30,v31
MM a[16:19],a[132:135],v[84:87],a[16:19]
EX v32,v32
MM a[24:27],a[132:135],v[92:95],a[24:27]
buffer_load_dwordx4 v228, s[8:11], s64 offen offset:0 lds
)ASM"
R"ASM(
MM a[24:27],a[128:131],v[88:91],a[24:27]
EX v33,v33
MM a[28:31],a[136:139],v[88:91],a[28:31]
RD a[196:199],v225 offset:49152
CV v4,v32,v33
MM a[28:31],a[140:143],v[92:95],a[28:31]
EX v34,v34
MM a[20:23],a[140:143],v[84:87],a[20:23]
EX v35,v35
MM a[20:23],a[136:139],v[80:83],a[20:23]
RD a[200:203],v226 offset:49152
CV v5,v34,v35
MM a[12:15],a[136:139],v[72:75],a[12:15]
EX v36,v36
s_nop 0
LPG10_%=:
s_cbranch_execz LTR0_%=
MM a[12:15],a[140:143],v[76:79],a[12:15]
buffer_load_dwordx4 v229, s[8:11], s64 offen offset:1024 lds
MM a[4:7],a[140:143],v[68:71],a[4:7]
EX v37,v37
MM a[4:7],a[136:139],v[64:67],a[4:7]
RD a[204:207],v227 offset:49152
CV v6,v36,v37
MM a[32:35],a[144:147],v[64:67],a[32:35]
EX v38,v38
MM a[32:35],a[148:151],v[68:71],a[32:35]
EX v39,v39
MM a[40:43],a[148:151],v[76:79],a[40:43]
RD a[208:211],v224 offset:53248
CV v7,v38,v39
MM a[40:43],a[144:147],v[72:75],a[40:43]
EX v40,v40
MM a[48:51],a[144:147],v[80:83],a[48:51]
buffer_load_dwordx4 v230, s[8:11], s64 offen offset:2048 lds
MM a[48:51],a[148:151],v[84:87],a[48:51]
EX v41,v41
MM a[56:59],a[148:151],v[92:95],a[56:59]
RD a[212:215],v225 offset:53248
CV v12,v40,v41
MM a[56:59],a[144:147],v[88:91],a[56:59]
EX v42,v42
MM a[60:63],a[152:155],v[88:91],a[60:63]
EX v43,v43
MM a[60:63],a[156:159],v[92:95],a[60:63]
RD a[216:219],v226 offset:53248
CV v13,v42,v43
MM a[52:55],a[156:159],v[84:87],a[52:55]
EX v44,v44
MM a[52:55],a[152:155],v[80:83],a[52:55]
buffer_load_dwordx4 v231, s[8:11], s64 offen offset:3072 lds
MM a[44:47],a[152:155],v[72:75],a[44:47]
s_add_u32 m0, s65, 81920
EX v45,v45
MM a[44:47],a[156:159],v[76:79],a[44:47]
RD a[220:223],v227 offset:53248
CV v14,v44,v45
MM a[36:39],a[156:159],v[68:71],a[36:39]
EX v46,v46
MM a[36:39],a[152:155],v[64:67],a[36:39]
RD a[224:227],v224 offset:57344
RD a[228:231],v225 offset:57344
MM a[64:67],a[160:163],v[64:67],a[64:67]
EX v47,v47
MM a[64:67],a[164:167],v[68:71],a[64:67]
RD a[232:235],v226 offset:57344
CV v15,v46,v47
MM a[72:75],a[164:167],v[76:79],a[72:75]
EX v48,v48
MM a[72:75],a[160:163],v[72:75],a[72:75]
buffer_load_dwordx4 v235, s[12:15], s81 offen offset:0 lds
MM a[80:83],a[160:163],v[80:83],a[80:83]
s_add_u32 m0, s67, 0
s_add_u32 s85, s66, 0
buffer_load_dwordx4 v252, s[36:39], s85 offen offset:0 lds
MM a[80:83],a[164:167],v[84:87],a[80:83]
EX v49,v49
MM a[88:91],a[164:167],v[92:95],a[88:91]
EX v50,v50
MM a[88:91],a[160:163],v[88:91],a[88:91]
RD a[236:239],v227 offset:57344
CV v20,v48,v49
MM a[92:95],a[168:171],v[88:91],a[92:95]
EX v51,v51
MM a[92:95],a[172:175],v[92:95],a[92:95]
RD a[240:243],v224 offset:61440
CV v21,v50,v51
MM a[84:87],a[172:175],v[84:87],a[84:87]
EX v52,v52
MM a[84:87],a[168:171],v[80:83],a[84:87]
EX v53,v53
MM a[76:79],a[168:171],v[72:75],a[76:79]
RD a[244:247],v225 offset:61440
CV v22,v52,v53
MM a[76:79],a[172:175],v[76:79],a[76:79]
EX v54,v54
MM a[68:71],a[172:175],v[68:71],a[68:71]
s_add_u32 m0, s65, 81920
s_nop 0
buffer_load_dwordx4 v235, s[12:15], s64 offen offset:1024 lds
MM a[68:71],a[168:171],v[64:67],a[68:71]
EX v55,v55
MM a[96:99],a[176:179],v[64:67],a[96:99]
RD a[248:251],v226 offset:61440
CV v23,v54,v55
MM a[96:99],a[180:183],v[68:71],a[96:99]
EX v56,v56
MM a[104:107],a[180:183],v[76:79],a[104:107]
EX v57,v57
MM a[104:107],a[176:179],v[72:75],a[104:107]
RD a[252:255],v227 offset:61440
CV v28,v56,v57
MM a[112:115],a[176:179],v[80:83],a[112:115]
buffer_load_dwordx4 v235, s[12:15], s81 offen offset:2048 lds
MM a[112:115],a[180:183],v[84:87],a[112:115]
EX v58,v58
MM a[120:123],a[180:183],v[92:95],a[120:123]
EX v59,v59
MM a[120:123],a[176:179],v[88:91],a[120:123]
EX v60,v60
MM a[124:127],a[184:187],v[88:91],a[124:127]
v_add_f32 v244, v144, v145
v_add_f32 v245, v148, v149
MM a[124:127],a[188:191],v[92:95],a[124:127]
v_add_f32 v246, v152, v153
v_add_f32 v247, v156, v157
MM a[116:119],a[188:191],v[84:87],a[116:119]
EX v61,v61
MM a[116:119],a[184:187],v[80:83],a[116:119]
buffer_load_dwordx4 v235, s[12:15], s64 offen offset:3072 lds
MM a[108:111],a[184:187],v[72:75],a[108:111]
EX v62,v62
MM a[108:111],a[188:191],v[76:79],a[108:111]
EX v63,v63
MM a[100:103],a[188:191],v[68:71],a[100:103]
CV v29,v58,v59
CV v30,v60,v61
MM a[100:103],a[184:187],v[64:67],a[100:103]
CV v31,v62,v63
v_smfmac_f32_16x16x64_bf16 v[144:147], v[236:239], v[0:7], v234
v_smfmac_f32_16x16x64_bf16 v[148:151], v[236:239], v[8:15], v234
v_smfmac_f32_16x16x64_bf16 v[152:155], v[236:239], v[16:23], v234
v_smfmac_f32_16x16x64_bf16 v[156:159], v[236:239], v[24:31], v234
s_waitcnt lgkmcnt(0)
MM v[64:67],a[192:195],v[160:163],v[128:131]
s_add_u32 s64, s64, 16384
TR a[128:129],v232 offset:32768
s_add_u32 s81, s64, 896
MM v[64:67],a[196:199],v[176:179],v[64:67]
TR a[130:131],v232 offset:36864
v_max3_f32 v249, v144, v145, v148
MM v[64:67],a[200:203],v[192:195],v[64:67]
TR a[132:133],v232 offset:40960
v_max3_f32 v249, v249, v149, v152
MM v[64:67],a[204:207],v[208:211],v[64:67]
TR a[134:135],v232 offset:45056
v_max3_f32 v249, v249, v153, v156
MM v[68:71],a[220:223],v[208:211],v[128:131]
TR a[136:137],v233 offset:32768
v_max_f32 v249, v249, v157
MM v[68:71],a[216:219],v[192:195],v[68:71]
TR a[138:139],v233 offset:36864
v_cmp_lt_f32 vcc, s71, v249
MM v[68:71],a[212:215],v[176:179],v[68:71]
TR a[140:141],v233 offset:40960
TR a[142:143],v233 offset:45056
MM v[68:71],a[208:211],v[160:163],v[68:71]
s_cbranch_vccnz LSITE10_%=
LBACK10_%=:
MM v[76:79],a[208:211],v[164:167],v[132:135]
s_add_u32 m0, s67, 1024
s_add_u32 s85, s66, 55296
buffer_load_dwordx4 v252, s[36:39], s85 offen offset:0 lds
MM v[76:79],a[212:215],v[180:183],v[76:79]
EX v64,v64
MM v[76:79],a[216:219],v[196:199],v[76:79]
EX v65,v65
MM v[76:79],a[220:223],v[212:215],v[76:79]
TR a[144:145],v232 offset:33280
CV v64,v64,v65
MM v[72:75],a[204:207],v[212:215],v[132:135]
EX v66,v66
MM v[72:75],a[200:203],v[196:199],v[72:75]
EX v67,v67
MM v[72:75],a[196:199],v[180:183],v[72:75]
TR a[146:147],v232 offset:37376
CV v65,v66,v67
MM v[72:75],a[192:195],v[164:167],v[72:75]
EX v68,v68
MM v[80:83],a[192:195],v[168:171],v[136:139]
TR a[148:149],v232 offset:41472
TR a[150:151],v232 offset:45568
MM v[80:83],a[196:199],v[184:187],v[80:83]
EX v69,v69
MM v[80:83],a[200:203],v[200:203],v[80:83]
TR a[152:153],v233 offset:33280
CV v66,v68,v69
MM v[80:83],a[204:207],v[216:219],v[80:83]
EX v70,v70
MM v[84:87],a[220:223],v[216:219],v[136:139]
EX v71,v71
MM v[84:87],a[216:219],v[200:203],v[84:87]
TR a[154:155],v233 offset:37376
CV v67,v70,v71
MM v[84:87],a[212:215],v[184:187],v[84:87]
EX v72,v72
MM v[84:87],a[208:211],v[168:171],v[84:87]
TR a[156:157],v233 offset:41472
TR a[158:159],v233 offset:45568
MM v[92:95],a[208:211],v[172:175],v[140:143]
EX v73,v73
MM v[92:95],a[212:215],v[188:191],v[92:95]
TR a[160:161],v232 offset:33792
CV v72,v72,v73
MM v[92:95],a[216:219],v[204:207],v[92:95]
EX v74,v74
MM v[92:95],a[220:223],v[220:223],v[92:95]
EX v75,v75
MM v[88:91],a[204:207],v[220:223],v[140:143]
TR a[162:163],v232 offset:37888
CV v73,v74,v75
MM v[88:91],a[200:203],v[204:207],v[88:91]
TR a[164:165],v232 offset:41984
TR a[166:167],v232 offset:46080
MM v[88:91],a[196:199],v[188:191],v[88:91]
EX v76,v76
MM v[88:91],a[192:195],v[172:175],v[88:91]
EX v77,v77
MM v[96:99],a[224:227],v[160:163],v[128:131]
TR a[168:169],v233 offset:33792
CV v74,v76,v77
MM v[96:99],a[228:231],v[176:179],v[96:99]
EX v78,v78
MM v[96:99],a[232:235],v[192:195],v[96:99]
TR a[170:171],v233 offset:37888
TR a[172:173],v233 offset:41984
MM v[96:99],a[236:239],v[208:211],v[96:99]
EX v79,v79
MM v[100:103],a[252:255],v[208:211],v[128:131]
TR a[174:175],v233 offset:46080
CV v75,v78,v79
MM v[100:103],a[248:251],v[192:195],v[100:103]
EX v80,v80
MM v[100:103],a[244:247],v[176:179],v[100:103]
EX v81,v81
MM v[100:103],a[240:243],v[160:163],v[100:103]
TR a[176:177],v232 offset:34304
CV v80,v80,v81
MM v[108:111],a[240:243],v[164:167],v[132:135]
s_add_u32 m0, s67, 2048
s_add_u32 s85, s66, 110592
buffer_load_dwordx4 v252, s[36:39], s85 offen offset:0 lds
MM v[108:111],a[244:247],v[180:183],v[108:111]
EX v82,v82
MM v[108:111],a[248:251],v[196:199],v[108:111]
TR a[178:179],v232 offset:38400
TR a[180:181],v232 offset:42496
MM v[108:111],a[252:255],v[212:215],v[108:111]
EX v83,v83
MM v[104:107],a[236:239],v[212:215],v[132:135]
TR a[182:183],v232 offset:46592
CV v81,v82,v83
MM v[104:107],a[232:235],v[196:199],v[104:107]
EX v84,v84
MM v[104:107],a[228:231],v[180:183],v[104:107]
EX v85,v85
MM v[104:107],a[224:227],v[164:167],v[104:107]
TR a[184:185],v233 offset:34304
CV v82,v84,v85
MM v[112:115],a[224:227],v[168:171],v[136:139]
EX v86,v86
MM v[112:115],a[228:231],v[184:187],v[112:115]
TR a[186:187],v233 offset:38400
TR a[188:189],v233 offset:42496
MM v[112:115],a[232:235],v[200:203],v[112:115]
EX v87,v87
MM v[112:115],a[236:239],v[216:219],v[112:115]
TR a[190:191],v233 offset:46592
CV v83,v86,v87
MM v[116:119],a[252:255],v[216:219],v[136:139]
EX v88,v88
MM v[116:119],a[248:251],v[200:203],v[116:119]
EX v89,v89
MM v[116:119],a[244:247],v[184:187],v[116:119]
EX v90,v90
MM v[116:119],a[240:243],v[168:171],v[116:119]
EX v91,v91
MM v[124:127],a[240:243],v[172:175],v[140:143]
EX v92,v92
MM v[124:127],a[244:247],v[188:191],v[124:127]
EX v93,v93
MM v[124:127],a[248:251],v[204:207],v[124:127]
EX v94,v94
MM v[124:127],a[252:255],v[220:223],v[124:127]
EX v95,v95
MM v[120:123],a[236:239],v[220:223],v[140:143]
EX v96,v96
MM v[120:123],a[232:235],v[204:207],v[120:123]
EX v97,v97
MM v[120:123],a[228:231],v[188:191],v[120:123]
EX v98,v98
MM v[120:123],a[224:227],v[172:175],v[120:123]
s_waitcnt vmcnt(15) lgkmcnt(0)
s_barrier
MM a[0:3],a[128:131],v[0:3],a[0:3]
s_add_u32 m0, s65, 32768
CV v88,v88,v89
CV v89,v90,v91
MM a[0:3],a[132:135],v[4:7],a[0:3]
RD a[192:195],v224 offset:0
CV v90,v92,v93
MM a[8:11],a[132:135],v[12:15],a[8:11]
RD a[196:199],v225 offset:0
CV v91,v94,v95
MM a[8:11],a[128:131],v[8:11],a[8:11]
buffer_load_dwordx4 v228, s[8:11], s64 offen offset:0 lds
MM a[16:19],a[128:131],v[16:19],a[16:19]
RD a[200:203],v226 offset:0
CV v68,v96,v97
MM a[16:19],a[132:135],v[20:23],a[16:19]
buffer_load_dwordx4 v229, s[8:11], s64 offen offset:1024 lds
MM a[24:27],a[132:135],v[28:31],a[24:27]
RD a[204:207],v227 offset:0
RD a[208:211],v224 offset:4096
MM a[24:27],a[128:131],v[24:27],a[24:27]
EX v99,v99
MM a[28:31],a[136:139],v[24:27],a[28:31]
RD a[212:215],v225 offset:4096
CV v69,v98,v99
MM a[28:31],a[140:143],v[28:31],a[28:31]
EX v100,v100
MM a[20:23],a[140:143],v[20:23],a[20:23]
buffer_load_dwordx4 v230, s[8:11], s64 offen offset:2048 lds
MM a[20:23],a[136:139],v[16:19],a[20:23]
EX v101,v101
MM a[12:15],a[136:139],v[8:11],a[12:15]
RD a[216:219],v226 offset:4096
CV v70,v100,v101
MM a[12:15],a[140:143],v[12:15],a[12:15]
EX v102,v102
MM a[4:7],a[140:143],v[4:7],a[4:7]
EX v103,v103
MM a[4:7],a[136:139],v[0:3],a[4:7]
RD a[220:223],v227 offset:4096
CV v71,v102,v103
MM a[32:35],a[144:147],v[0:3],a[32:35]
EX v104,v104
MM a[32:35],a[148:151],v[4:7],a[32:35]
buffer_load_dwordx4 v231, s[8:11], s64 offen offset:3072 lds
MM a[40:43],a[148:151],v[12:15],a[40:43]
s_add_u32 m0, s65, 98304
EX v105,v105
MM a[40:43],a[144:147],v[8:11],a[40:43]
RD a[224:227],v224 offset:8192
CV v76,v104,v105
MM a[48:51],a[144:147],v[16:19],a[48:51]
EX v106,v106
MM a[48:51],a[148:151],v[20:23],a[48:51]
EX v107,v107
MM a[56:59],a[148:151],v[28:31],a[56:59]
RD a[228:231],v225 offset:8192
CV v77,v106,v107
MM a[56:59],a[144:147],v[24:27],a[56:59]
EX v108,v108
MM a[60:63],a[152:155],v[24:27],a[60:63]
buffer_load_dwordx4 v235, s[12:15], s81 offen offset:0 lds
MM a[60:63],a[156:159],v[28:31],a[60:63]
EX v109,v109
MM a[52:55],a[156:159],v[20:23],a[52:55]
RD a[232:235],v226 offset:8192
CV v78,v108,v109
MM a[52:55],a[152:155],v[16:19],a[52:55]
EX v110,v110
MM a[44:47],a[152:155],v[8:11],a[44:47]
EX v111,v111
MM a[44:47],a[156:159],v[12:15],a[44:47]
RD a[236:239],v227 offset:8192
CV v79,v110,v111
MM a[36:39],a[156:159],v[4:7],a[36:39]
buffer_load_dwordx4 v235, s[12:15], s64 offen offset:1024 lds
MM a[36:39],a[152:155],v[0:3],a[36:39]
EX v112,v112
MM a[64:67],a[160:163],v[0:3],a[64:67]
EX v113,v113
MM a[64:67],a[164:167],v[4:7],a[64:67]
RD a[240:243],v224 offset:12288
CV v84,v112,v113
MM a[72:75],a[164:167],v[12:15],a[72:75]
)ASM"
R"ASM(
EX v114,v114
MM a[72:75],a[160:163],v[8:11],a[72:75]
RD a[244:247],v225 offset:12288
RD a[248:251],v226 offset:12288
MM a[80:83],a[160:163],v[16:19],a[80:83]
s_add_u32 m0, s67, 3072
s_add_u32 s85, s66, 165888
buffer_load_dwordx4 v252, s[36:39], s85 offen offset:0 lds
MM a[80:83],a[164:167],v[20:23],a[80:83]
EX v115,v115
MM a[88:91],a[164:167],v[28:31],a[88:91]
RD a[252:255],v227 offset:12288
CV v85,v114,v115
MM a[88:91],a[160:163],v[24:27],a[88:91]
EX v116,v116
MM a[92:95],a[168:171],v[24:27],a[92:95]
EX v117,v117
MM a[92:95],a[172:175],v[28:31],a[92:95]
s_add_u32 m0, s65, 98304
s_nop 0
buffer_load_dwordx4 v235, s[12:15], s81 offen offset:2048 lds
MM a[84:87],a[172:175],v[20:23],a[84:87]
EX v118,v118
MM a[84:87],a[168:171],v[16:19],a[84:87]
EX v119,v119
MM a[76:79],a[168:171],v[8:11],a[76:79]
buffer_load_dwordx4 v235, s[12:15], s64 offen offset:3072 lds
MM a[76:79],a[172:175],v[12:15],a[76:79]
EX v120,v120
MM a[68:71],a[172:175],v[4:7],a[68:71]
EX v121,v121
MM a[68:71],a[168:171],v[0:3],a[68:71]
EX v122,v122
MM a[96:99],a[176:179],v[0:3],a[96:99]
EX v123,v123
MM a[96:99],a[180:183],v[4:7],a[96:99]
EX v124,v124
MM a[104:107],a[180:183],v[12:15],a[104:107]
EX v125,v125
MM a[104:107],a[176:179],v[8:11],a[104:107]
EX v126,v126
MM a[112:115],a[176:179],v[16:19],a[112:115]
EX v127,v127
MM a[112:115],a[180:183],v[20:23],a[112:115]
CV v86,v116,v117
CV v87,v118,v119
MM a[120:123],a[180:183],v[28:31],a[120:123]
CV v92,v120,v121
CV v93,v122,v123
MM a[120:123],a[176:179],v[24:27],a[120:123]
CV v94,v124,v125
CV v95,v126,v127
MM a[124:127],a[184:187],v[24:27],a[124:127]
v_add_f32 v244, v144, v145
v_add_f32 v245, v148, v149
MM a[124:127],a[188:191],v[28:31],a[124:127]
v_add_f32 v246, v152, v153
v_add_f32 v247, v156, v157
MM a[116:119],a[188:191],v[20:23],a[116:119]
MM a[116:119],a[184:187],v[16:19],a[116:119]
MM a[108:111],a[184:187],v[8:11],a[108:111]
MM a[108:111],a[188:191],v[12:15],a[108:111]
MM a[100:103],a[188:191],v[4:7],a[100:103]
MM a[100:103],a[184:187],v[0:3],a[100:103]
v_smfmac_f32_16x16x64_bf16 v[144:147], v[236:239], v[64:71], v234
v_smfmac_f32_16x16x64_bf16 v[148:151], v[236:239], v[72:79], v234
v_smfmac_f32_16x16x64_bf16 v[152:155], v[236:239], v[80:87], v234
v_smfmac_f32_16x16x64_bf16 v[156:159], v[236:239], v[88:95], v234
s_waitcnt lgkmcnt(0)
MM v[0:3],a[192:195],v[160:163],v[128:131]
s_add_u32 s64, s64, 16384
TR a[128:129],v232 offset:49152
s_add_u32 s81, s64, 896
MM v[0:3],a[196:199],v[176:179],v[0:3]
TR a[130:131],v232 offset:53248
v_max3_f32 v249, v144, v145, v148
MM v[0:3],a[200:203],v[192:195],v[0:3]
TR a[132:133],v232 offset:57344
v_max3_f32 v249, v249, v149, v152
MM v[0:3],a[204:207],v[208:211],v[0:3]
TR a[134:135],v232 offset:61440
v_max3_f32 v249, v249, v153, v156
MM v[4:7],a[220:223],v[208:211],v[128:131]
TR a[136:137],v233 offset:49152
v_max_f32 v249, v249, v157
MM v[4:7],a[216:219],v[192:195],v[4:7]
TR a[138:139],v233 offset:53248
v_cmp_lt_f32 vcc, s71, v249
MM v[4:7],a[212:215],v[176:179],v[4:7]
TR a[140:141],v233 offset:57344
TR a[142:143],v233 offset:61440
MM v[4:7],a[208:211],v[160:163],v[4:7]
s_cbranch_vccnz LSITE11_%=
LBACK11_%=:
MM v[12:15],a[208:211],v[164:167],v[132:135]
s_add_u32 m0, s67, 4096
s_add_u32 s85, s66, 221184
buffer_load_dwordx4 v252, s[36:39], s85 offen offset:0 lds
MM v[12:15],a[212:215],v[180:183],v[12:15]
EX v0,v0
MM v[12:15],a[216:219],v[196:199],v[12:15]
EX v1,v1
MM v[12:15],a[220:223],v[212:215],v[12:15]
TR a[144:145],v232 offset:49664
CV v0,v0,v1
MM v[8:11],a[204:207],v[212:215],v[132:135]
EX v2,v2
MM v[8:11],a[200:203],v[196:199],v[8:11]
EX v3,v3
MM v[8:11],a[196:199],v[180:183],v[8:11]
TR a[146:147],v232 offset:53760
CV v1,v2,v3
MM v[8:11],a[192:195],v[164:167],v[8:11]
EX v4,v4
MM v[16:19],a[192:195],v[168:171],v[136:139]
TR a[148:149],v232 offset:57856
TR a[150:151],v232 offset:61952
MM v[16:19],a[196:199],v[184:187],v[16:19]
EX v5,v5
MM v[16:19],a[200:203],v[200:203],v[16:19]
TR a[152:153],v233 offset:49664
CV v2,v4,v5
MM v[16:19],a[204:207],v[216:219],v[16:19]
EX v6,v6
MM v[20:23],a[220:223],v[216:219],v[136:139]
EX v7,v7
MM v[20:23],a[216:219],v[200:203],v[20:23]
TR a[154:155],v233 offset:53760
CV v3,v6,v7
MM v[20:23],a[212:215],v[184:187],v[20:23]
EX v8,v8
MM v[20:23],a[208:211],v[168:171],v[20:23]
TR a[156:157],v233 offset:57856
TR a[158:159],v233 offset:61952
MM v[28:31],a[208:211],v[172:175],v[140:143]
EX v9,v9
MM v[28:31],a[212:215],v[188:191],v[28:31]
TR a[160:161],v232 offset:50176
CV v8,v8,v9
MM v[28:31],a[216:219],v[204:207],v[28:31]
EX v10,v10
MM v[28:31],a[220:223],v[220:223],v[28:31]
EX v11,v11
MM v[24:27],a[204:207],v[220:223],v[140:143]
TR a[162:163],v232 offset:54272
CV v9,v10,v11
MM v[24:27],a[200:203],v[204:207],v[24:27]
TR a[164:165],v232 offset:58368
TR a[166:167],v232 offset:62464
MM v[24:27],a[196:199],v[188:191],v[24:27]
EX v12,v12
MM v[24:27],a[192:195],v[172:175],v[24:27]
EX v13,v13
MM v[32:35],a[224:227],v[160:163],v[128:131]
TR a[168:169],v233 offset:50176
CV v10,v12,v13
MM v[32:35],a[228:231],v[176:179],v[32:35]
EX v14,v14
MM v[32:35],a[232:235],v[192:195],v[32:35]
TR a[170:171],v233 offset:54272
TR a[172:173],v233 offset:58368
MM v[32:35],a[236:239],v[208:211],v[32:35]
EX v15,v15
MM v[36:39],a[252:255],v[208:211],v[128:131]
TR a[174:175],v233 offset:62464
CV v11,v14,v15
MM v[36:39],a[248:251],v[192:195],v[36:39]
EX v16,v16
MM v[36:39],a[244:247],v[176:179],v[36:39]
EX v17,v17
MM v[36:39],a[240:243],v[160:163],v[36:39]
TR a[176:177],v232 offset:50688
CV v16,v16,v17
MM v[44:47],a[240:243],v[164:167],v[132:135]
EX v18,v18
MM v[44:47],a[244:247],v[180:183],v[44:47]
TR a[178:179],v232 offset:54784
TR a[180:181],v232 offset:58880
MM v[44:47],a[248:251],v[196:199],v[44:47]
EX v19,v19
MM v[44:47],a[252:255],v[212:215],v[44:47]
TR a[182:183],v232 offset:62976
CV v17,v18,v19
MM v[40:43],a[236:239],v[212:215],v[132:135]
EX v20,v20
MM v[40:43],a[232:235],v[196:199],v[40:43]
EX v21,v21
MM v[40:43],a[228:231],v[180:183],v[40:43]
TR a[184:185],v233 offset:50688
CV v18,v20,v21
MM v[40:43],a[224:227],v[164:167],v[40:43]
EX v22,v22
MM v[48:51],a[224:227],v[168:171],v[136:139]
TR a[186:187],v233 offset:54784
TR a[188:189],v233 offset:58880
MM v[48:51],a[228:231],v[184:187],v[48:51]
EX v23,v23
MM v[48:51],a[232:235],v[200:203],v[48:51]
TR a[190:191],v233 offset:62976
CV v19,v22,v23
MM v[48:51],a[236:239],v[216:219],v[48:51]
EX v24,v24
MM v[52:55],a[252:255],v[216:219],v[136:139]
EX v25,v25
MM v[52:55],a[248:251],v[200:203],v[52:55]
EX v26,v26
MM v[52:55],a[244:247],v[184:187],v[52:55]
EX v27,v27
MM v[52:55],a[240:243],v[168:171],v[52:55]
EX v28,v28
MM v[60:63],a[240:243],v[172:175],v[140:143]
EX v29,v29
MM v[60:63],a[244:247],v[188:191],v[60:63]
EX v30,v30
s_nop 0
LPG11_%=:
s_cbranch_execz LTR0_%=
MM v[60:63],a[248:251],v[204:207],v[60:63]
EX v31,v31
MM v[60:63],a[252:255],v[220:223],v[60:63]
EX v32,v32
MM v[56:59],a[236:239],v[220:223],v[140:143]
EX v33,v33
MM v[56:59],a[232:235],v[204:207],v[56:59]
EX v34,v34
MM v[56:59],a[228:231],v[188:191],v[56:59]
EX v35,v35
MM v[56:59],a[224:227],v[172:175],v[56:59]
s_waitcnt vmcnt(17) lgkmcnt(0)
s_barrier
MM a[0:3],a[128:131],v[64:67],a[0:3]
s_mov_b32 s86, 94208
v_bfe_u32 v251, v248, 4, 3
v_lshlrev_b32 v251, 3, v251
buffer_load_dwordx2 v[252:253], v251, s[20:23], s86 offen offset:0 sc1
MM a[0:3],a[132:135],v[68:71],a[0:3]
s_add_u32 m0, s65, 49152
CV v24,v24,v25
CV v25,v26,v27
MM a[8:11],a[132:135],v[76:79],a[8:11]
RD a[192:195],v224 offset:16384
CV v26,v28,v29
MM a[8:11],a[128:131],v[72:75],a[8:11]
RD a[196:199],v225 offset:16384
CV v27,v30,v31
MM a[16:19],a[128:131],v[80:83],a[16:19]
buffer_load_dwordx4 v228, s[8:11], s64 offen offset:0 lds
MM a[16:19],a[132:135],v[84:87],a[16:19]
RD a[200:203],v226 offset:16384
CV v4,v32,v33
MM a[24:27],a[132:135],v[92:95],a[24:27]
RD a[204:207],v227 offset:16384
CV v5,v34,v35
MM a[24:27],a[128:131],v[88:91],a[24:27]
buffer_load_dwordx4 v229, s[8:11], s64 offen offset:1024 lds
MM a[28:31],a[136:139],v[88:91],a[28:31]
RD a[208:211],v224 offset:20480
RD a[212:215],v225 offset:20480
MM a[28:31],a[140:143],v[92:95],a[28:31]
EX v36,v36
MM a[20:23],a[140:143],v[84:87],a[20:23]
buffer_load_dwordx4 v230, s[8:11], s64 offen offset:2048 lds
MM a[20:23],a[136:139],v[80:83],a[20:23]
EX v37,v37
MM a[12:15],a[136:139],v[72:75],a[12:15]
RD a[216:219],v226 offset:20480
CV v6,v36,v37
MM a[12:15],a[140:143],v[76:79],a[12:15]
EX v38,v38
MM a[4:7],a[140:143],v[68:71],a[4:7]
EX v39,v39
MM a[4:7],a[136:139],v[64:67],a[4:7]
RD a[220:223],v227 offset:20480
CV v7,v38,v39
MM a[32:35],a[144:147],v[64:67],a[32:35]
EX v40,v40
MM a[32:35],a[148:151],v[68:71],a[32:35]
buffer_load_dwordx4 v231, s[8:11], s64 offen offset:3072 lds
MM a[40:43],a[148:151],v[76:79],a[40:43]
s_add_u32 m0, s65, 114688
EX v41,v41
MM a[40:43],a[144:147],v[72:75],a[40:43]
RD a[224:227],v224 offset:24576
CV v12,v40,v41
MM a[48:51],a[144:147],v[80:83],a[48:51]
EX v42,v42
MM a[48:51],a[148:151],v[84:87],a[48:51]
EX v43,v43
MM a[56:59],a[148:151],v[92:95],a[56:59]
RD a[228:231],v225 offset:24576
CV v13,v42,v43
MM a[56:59],a[144:147],v[88:91],a[56:59]
EX v44,v44
MM a[60:63],a[152:155],v[88:91],a[60:63]
buffer_load_dwordx4 v235, s[12:15], s81 offen offset:0 lds
MM a[60:63],a[156:159],v[92:95],a[60:63]
EX v45,v45
MM a[52:55],a[156:159],v[84:87],a[52:55]
RD a[232:235],v226 offset:24576
CV v14,v44,v45
MM a[52:55],a[152:155],v[80:83],a[52:55]
EX v46,v46
MM a[44:47],a[152:155],v[72:75],a[44:47]
EX v47,v47
MM a[44:47],a[156:159],v[76:79],a[44:47]
RD a[236:239],v227 offset:24576
CV v15,v46,v47
MM a[36:39],a[156:159],v[68:71],a[36:39]
buffer_load_dwordx4 v235, s[12:15], s64 offen offset:1024 lds
MM a[36:39],a[152:155],v[64:67],a[36:39]
EX v48,v48
MM a[64:67],a[160:163],v[64:67],a[64:67]
EX v49,v49
MM a[64:67],a[164:167],v[68:71],a[64:67]
RD a[240:243],v224 offset:28672
CV v20,v48,v49
MM a[72:75],a[164:167],v[76:79],a[72:75]
EX v50,v50
MM a[72:75],a[160:163],v[72:75],a[72:75]
RD a[244:247],v225 offset:28672
RD a[248:251],v226 offset:28672
MM a[80:83],a[160:163],v[80:83],a[80:83]
EX v51,v51
MM a[80:83],a[164:167],v[84:87],a[80:83]
RD a[252:255],v227 offset:28672
CV v21,v50,v51
MM a[88:91],a[164:167],v[92:95],a[88:91]
EX v52,v52
MM a[88:91],a[160:163],v[88:91],a[88:91]
buffer_load_dwordx4 v235, s[12:15], s81 offen offset:2048 lds
MM a[92:95],a[168:171],v[88:91],a[92:95]
EX v53,v53
MM a[92:95],a[172:175],v[92:95],a[92:95]
EX v54,v54
MM a[84:87],a[172:175],v[84:87],a[84:87]
buffer_load_dwordx4 v235, s[12:15], s64 offen offset:3072 lds
MM a[84:87],a[168:171],v[80:83],a[84:87]
EX v55,v55
MM a[76:79],a[168:171],v[72:75],a[76:79]
EX v56,v56
MM a[76:79],a[172:175],v[76:79],a[76:79]
EX v57,v57
MM a[68:71],a[172:175],v[68:71],a[68:71]
EX v58,v58
MM a[68:71],a[168:171],v[64:67],a[68:71]
EX v59,v59
MM a[96:99],a[176:179],v[64:67],a[96:99]
EX v60,v60
MM a[96:99],a[180:183],v[68:71],a[96:99]
EX v61,v61
MM a[104:107],a[180:183],v[76:79],a[104:107]
EX v62,v62
MM a[104:107],a[176:179],v[72:75],a[104:107]
EX v63,v63
MM a[112:115],a[176:179],v[80:83],a[112:115]
CV v22,v52,v53
CV v23,v54,v55
MM a[112:115],a[180:183],v[84:87],a[112:115]
CV v28,v56,v57
CV v29,v58,v59
MM a[120:123],a[180:183],v[92:95],a[120:123]
CV v30,v60,v61
CV v31,v62,v63
MM a[120:123],a[176:179],v[88:91],a[120:123]
MM a[124:127],a[184:187],v[88:91],a[124:127]
v_add_f32 v244, v144, v145
v_add_f32 v245, v148, v149
MM a[124:127],a[188:191],v[92:95],a[124:127]
v_add_f32 v246, v152, v153
v_add_f32 v247, v156, v157
MM a[116:119],a[188:191],v[84:87],a[116:119]
MM a[116:119],a[184:187],v[80:83],a[116:119]
MM a[108:111],a[184:187],v[72:75],a[108:111]
MM a[108:111],a[188:191],v[76:79],a[108:111]
MM a[100:103],a[188:191],v[68:71],a[100:103]
MM a[100:103],a[184:187],v[64:67],a[100:103]
v_smfmac_f32_16x16x64_bf16 v[144:147], v[236:239], v[0:7], v234
v_smfmac_f32_16x16x64_bf16 v[148:151], v[236:239], v[8:15], v234
v_smfmac_f32_16x16x64_bf16 v[152:155], v[236:239], v[16:23], v234
v_smfmac_f32_16x16x64_bf16 v[156:159], v[236:239], v[24:31], v234
s_waitcnt lgkmcnt(0)
MM v[64:67],a[192:195],v[160:163],v[128:131]
s_add_u32 s64, s64, 16384
TR a[128:129],v232 offset:0
s_add_u32 s81, s64, 896
)ASM"
R"ASM(
MM v[64:67],a[196:199],v[176:179],v[64:67]
TR a[130:131],v232 offset:4096
v_max3_f32 v249, v144, v145, v148
MM v[64:67],a[200:203],v[192:195],v[64:67]
TR a[132:133],v232 offset:8192
v_max3_f32 v249, v249, v149, v152
MM v[64:67],a[204:207],v[208:211],v[64:67]
TR a[134:135],v232 offset:12288
v_max3_f32 v249, v249, v153, v156
MM v[68:71],a[220:223],v[208:211],v[128:131]
TR a[136:137],v233 offset:0
v_max_f32 v249, v249, v157
MM v[68:71],a[216:219],v[192:195],v[68:71]
TR a[138:139],v233 offset:4096
v_cmp_lt_f32 vcc, s71, v249
MM v[68:71],a[212:215],v[176:179],v[68:71]
TR a[140:141],v233 offset:8192
TR a[142:143],v233 offset:12288
MM v[68:71],a[208:211],v[160:163],v[68:71]
s_cbranch_vccnz LSITE12_%=
LBACK12_%=:
MM v[76:79],a[208:211],v[164:167],v[132:135]
s_waitcnt vmcnt(8)
v_cmp_ne_u32 vcc, s69, v253
s_cbranch_vccnz LRIX_%=
LRIXb_%=:
MM v[76:79],a[212:215],v[180:183],v[76:79]
s_and_b32 s90, s17, 7
v_and_b32 v253, 65535, v252
v_readlane_b32 s19, v252, s90
MM v[76:79],a[216:219],v[196:199],v[76:79]
v_sub_u32 v253, 65535, v253
v_mul_u32_u24 v253, 1280, v253
MM v[76:79],a[220:223],v[212:215],v[76:79]
v_lshrrev_b32 v252, 7, v248
v_lshl_add_u32 v253, v252, 4, v253
MM v[72:75],a[204:207],v[212:215],v[132:135]
s_mul_i32 s91, %[swave], 3072
EX v64,v64
MM v[72:75],a[200:203],v[196:199],v[72:75]
s_add_u32 s91, s91, %[slds]
EX v65,v65
MM v[72:75],a[196:199],v[180:183],v[72:75]
s_bfe_u32 s90, s48, 131077
EX v66,v66
MM v[72:75],a[192:195],v[164:167],v[72:75]
s_mul_i32 s90, s90, 320
EX v67,v67
MM v[80:83],a[192:195],v[168:171],v[136:139]
s_add_u32 s90, s90, 131072
s_add_u32 m0, s91, 151552
s_nop 0
buffer_load_dwordx4 v253, s[20:23], s90 offen offset:0 sc1 lds
MM v[80:83],a[196:199],v[184:187],v[80:83]
TR a[144:145],v232 offset:512
CV v64,v64,v65
MM v[80:83],a[200:203],v[200:203],v[80:83]
s_add_u32 m0, s91, 152448
s_nop 0
buffer_load_dwordx4 v253, s[20:23], s90 offen offset:128 sc1 lds
MM v[80:83],a[204:207],v[216:219],v[80:83]
EX v68,v68
MM v[84:87],a[220:223],v[216:219],v[136:139]
s_add_u32 m0, s91, 153344
s_nop 0
buffer_load_dwordx4 v253, s[20:23], s90 offen offset:256 sc1 lds
MM v[84:87],a[216:219],v[200:203],v[84:87]
EX v69,v69
MM v[84:87],a[212:215],v[184:187],v[84:87]
s_lshr_b32 s77, %[sb], 3
TR a[146:147],v232 offset:4608
TR a[148:149],v232 offset:8704
MM v[84:87],a[208:211],v[168:171],v[84:87]
s_lshl_b32 s77, s77, 2
EX v70,v70
MM v[92:95],a[208:211],v[172:175],v[140:143]
s_add_u32 s77, s77, %[swave]
EX v71,v71
MM v[92:95],a[212:215],v[188:191],v[92:95]
s_lshl_b32 s77, s77, 9
TR a[150:151],v232 offset:12800
CV v65,v66,v67
MM v[92:95],a[216:219],v[204:207],v[92:95]
s_add_u32 s77, s77, 0
v_lshrrev_b32 v253, 1, v248
v_add_u32 v253, s77, v253
MM v[92:95],a[220:223],v[220:223],v[92:95]
s_mov_b32 s77, LKEND_ka-LPD0_%=-16
TR a[152:153],v233 offset:512
v_min_u32 v253, s77, v253
EX v72,v72
MM v[88:91],a[204:207],v[220:223],v[140:143]
s_getpc_b64 s[98:99]
LPD0_%=:
global_load_dword v253, v253, s[98:99]
MM v[88:91],a[200:203],v[204:207],v[88:91]
s_add_u32 s67, s95, 1024
EX v73,v73
MM v[88:91],a[196:199],v[188:191],v[88:91]
s_mov_b32 s70, 5
TR a[154:155],v233 offset:4608
CV v66,v68,v69
MM v[88:91],a[192:195],v[172:175],v[88:91]
EX v74,v74
MM v[96:99],a[224:227],v[160:163],v[128:131]
TR a[156:157],v233 offset:8704
TR a[158:159],v233 offset:12800
EX v75,v75
MM v[96:99],a[228:231],v[176:179],v[96:99]
TR a[160:161],v232 offset:1024
CV v67,v70,v71
MM v[96:99],a[232:235],v[192:195],v[96:99]
EX v76,v76
MM v[96:99],a[236:239],v[208:211],v[96:99]
EX v77,v77
MM v[100:103],a[252:255],v[208:211],v[128:131]
TR a[162:163],v232 offset:5120
CV v72,v72,v73
MM v[100:103],a[248:251],v[192:195],v[100:103]
EX v78,v78
MM v[100:103],a[244:247],v[176:179],v[100:103]
TR a[164:165],v232 offset:9216
CV v73,v74,v75
MM v[100:103],a[240:243],v[160:163],v[100:103]
EX v79,v79
MM v[108:111],a[240:243],v[164:167],v[132:135]
TR a[166:167],v232 offset:13312
TR a[168:169],v233 offset:1024
MM v[108:111],a[244:247],v[180:183],v[108:111]
EX v80,v80
MM v[108:111],a[248:251],v[196:199],v[108:111]
EX v81,v81
MM v[108:111],a[252:255],v[212:215],v[108:111]
TR a[170:171],v233 offset:5120
CV v74,v76,v77
MM v[104:107],a[236:239],v[212:215],v[132:135]
EX v82,v82
MM v[104:107],a[232:235],v[196:199],v[104:107]
TR a[172:173],v233 offset:9216
CV v75,v78,v79
MM v[104:107],a[228:231],v[180:183],v[104:107]
EX v83,v83
MM v[104:107],a[224:227],v[164:167],v[104:107]
TR a[174:175],v233 offset:13312
TR a[176:177],v232 offset:1536
MM v[112:115],a[224:227],v[168:171],v[136:139]
EX v84,v84
MM v[112:115],a[228:231],v[184:187],v[112:115]
TR a[178:179],v232 offset:5632
CV v80,v80,v81
MM v[112:115],a[232:235],v[200:203],v[112:115]
EX v85,v85
MM v[112:115],a[236:239],v[216:219],v[112:115]
TR a[180:181],v232 offset:9728
CV v81,v82,v83
MM v[116:119],a[252:255],v[216:219],v[136:139]
EX v86,v86
MM v[116:119],a[248:251],v[200:203],v[116:119]
TR a[182:183],v232 offset:13824
TR a[184:185],v233 offset:1536
MM v[116:119],a[244:247],v[184:187],v[116:119]
EX v87,v87
MM v[116:119],a[240:243],v[168:171],v[116:119]
EX v88,v88
MM v[124:127],a[240:243],v[172:175],v[140:143]
TR a[186:187],v233 offset:5632
CV v82,v84,v85
MM v[124:127],a[244:247],v[188:191],v[124:127]
EX v89,v89
MM v[124:127],a[248:251],v[204:207],v[124:127]
TR a[188:189],v233 offset:9728
TR a[190:191],v233 offset:13824
MM v[124:127],a[252:255],v[220:223],v[124:127]
EX v90,v90
MM v[120:123],a[236:239],v[220:223],v[140:143]
CV v83,v86,v87
CV v88,v88,v89
MM v[120:123],a[232:235],v[204:207],v[120:123]
EX v91,v91
MM v[120:123],a[228:231],v[188:191],v[120:123]
EX v92,v92
MM v[120:123],a[224:227],v[172:175],v[120:123]
s_waitcnt lgkmcnt(0)
s_barrier
MM a[0:3],a[128:131],v[0:3],a[0:3]
s_add_u32 m0, s65, 0
EX v93,v93
MM a[0:3],a[132:135],v[4:7],a[0:3]
CV v89,v90,v91
CV v90,v92,v93
MM a[8:11],a[132:135],v[12:15],a[8:11]
EX v94,v94
MM a[8:11],a[128:131],v[8:11],a[8:11]
EX v95,v95
MM a[16:19],a[128:131],v[16:19],a[16:19]
RD a[192:195],v224 offset:32768
CV v91,v94,v95
MM a[16:19],a[132:135],v[20:23],a[16:19]
EX v96,v96
MM a[24:27],a[132:135],v[28:31],a[24:27]
buffer_load_dwordx4 v228, s[8:11], s64 offen offset:0 lds
MM a[24:27],a[128:131],v[24:27],a[24:27]
EX v97,v97
MM a[28:31],a[136:139],v[24:27],a[28:31]
RD a[196:199],v225 offset:32768
CV v68,v96,v97
MM a[28:31],a[140:143],v[28:31],a[28:31]
EX v98,v98
MM a[20:23],a[140:143],v[20:23],a[20:23]
EX v99,v99
MM a[20:23],a[136:139],v[16:19],a[20:23]
RD a[200:203],v226 offset:32768
CV v69,v98,v99
MM a[12:15],a[136:139],v[8:11],a[12:15]
EX v100,v100
MM a[12:15],a[140:143],v[12:15],a[12:15]
buffer_load_dwordx4 v229, s[8:11], s64 offen offset:1024 lds
MM a[4:7],a[140:143],v[4:7],a[4:7]
EX v101,v101
MM a[4:7],a[136:139],v[0:3],a[4:7]
RD a[204:207],v227 offset:32768
CV v70,v100,v101
MM a[32:35],a[144:147],v[0:3],a[32:35]
EX v102,v102
MM a[32:35],a[148:151],v[4:7],a[32:35]
EX v103,v103
MM a[40:43],a[148:151],v[12:15],a[40:43]
RD a[208:211],v224 offset:36864
CV v71,v102,v103
MM a[40:43],a[144:147],v[8:11],a[40:43]
EX v104,v104
MM a[48:51],a[144:147],v[16:19],a[48:51]
buffer_load_dwordx4 v230, s[8:11], s64 offen offset:2048 lds
MM a[48:51],a[148:151],v[20:23],a[48:51]
EX v105,v105
MM a[56:59],a[148:151],v[28:31],a[56:59]
RD a[212:215],v225 offset:36864
CV v76,v104,v105
MM a[56:59],a[144:147],v[24:27],a[56:59]
EX v106,v106
MM a[60:63],a[152:155],v[24:27],a[60:63]
EX v107,v107
MM a[60:63],a[156:159],v[28:31],a[60:63]
RD a[216:219],v226 offset:36864
CV v77,v106,v107
MM a[52:55],a[156:159],v[20:23],a[52:55]
EX v108,v108
MM a[52:55],a[152:155],v[16:19],a[52:55]
buffer_load_dwordx4 v231, s[8:11], s64 offen offset:3072 lds
MM a[44:47],a[152:155],v[8:11],a[44:47]
s_add_u32 m0, s65, 65536
EX v109,v109
MM a[44:47],a[156:159],v[12:15],a[44:47]
RD a[220:223],v227 offset:36864
CV v78,v108,v109
MM a[36:39],a[156:159],v[4:7],a[36:39]
EX v110,v110
MM a[36:39],a[152:155],v[0:3],a[36:39]
RD a[224:227],v224 offset:40960
RD a[228:231],v225 offset:40960
MM a[64:67],a[160:163],v[0:3],a[64:67]
EX v111,v111
MM a[64:67],a[164:167],v[4:7],a[64:67]
RD a[232:235],v226 offset:40960
CV v79,v110,v111
MM a[72:75],a[164:167],v[12:15],a[72:75]
EX v112,v112
MM a[72:75],a[160:163],v[8:11],a[72:75]
buffer_load_dwordx4 v235, s[12:15], s81 offen offset:0 lds
MM a[80:83],a[160:163],v[16:19],a[80:83]
EX v113,v113
MM a[80:83],a[164:167],v[20:23],a[80:83]
RD a[236:239],v227 offset:40960
CV v84,v112,v113
MM a[88:91],a[164:167],v[28:31],a[88:91]
EX v114,v114
MM a[88:91],a[160:163],v[24:27],a[88:91]
EX v115,v115
MM a[92:95],a[168:171],v[24:27],a[92:95]
RD a[240:243],v224 offset:45056
CV v85,v114,v115
MM a[92:95],a[172:175],v[28:31],a[92:95]
EX v116,v116
MM a[84:87],a[172:175],v[20:23],a[84:87]
buffer_load_dwordx4 v235, s[12:15], s64 offen offset:1024 lds
MM a[84:87],a[168:171],v[16:19],a[84:87]
EX v117,v117
MM a[76:79],a[168:171],v[8:11],a[76:79]
RD a[244:247],v225 offset:45056
CV v86,v116,v117
MM a[76:79],a[172:175],v[12:15],a[76:79]
EX v118,v118
MM a[68:71],a[172:175],v[4:7],a[68:71]
EX v119,v119
MM a[68:71],a[168:171],v[0:3],a[68:71]
RD a[248:251],v226 offset:45056
CV v87,v118,v119
MM a[96:99],a[176:179],v[0:3],a[96:99]
EX v120,v120
MM a[96:99],a[180:183],v[4:7],a[96:99]
buffer_load_dwordx4 v235, s[12:15], s81 offen offset:2048 lds
MM a[104:107],a[180:183],v[12:15],a[104:107]
EX v121,v121
MM a[104:107],a[176:179],v[8:11],a[104:107]
RD a[252:255],v227 offset:45056
CV v92,v120,v121
MM a[112:115],a[176:179],v[16:19],a[112:115]
EX v122,v122
MM a[112:115],a[180:183],v[20:23],a[112:115]
EX v123,v123
MM a[120:123],a[180:183],v[28:31],a[120:123]
s_add_u32 s87, s95, 0
buffer_store_dword v240, v248, s[20:23], s87 offen offset:12 sc1
MM a[120:123],a[176:179],v[24:27],a[120:123]
EX v124,v124
MM a[124:127],a[184:187],v[24:27],a[124:127]
v_add_f32 v244, v144, v145
v_add_f32 v245, v148, v149
MM a[124:127],a[188:191],v[28:31],a[124:127]
v_add_f32 v246, v152, v153
v_add_f32 v247, v156, v157
MM a[116:119],a[188:191],v[20:23],a[116:119]
EX v125,v125
MM a[116:119],a[184:187],v[16:19],a[116:119]
buffer_load_dwordx4 v235, s[12:15], s64 offen offset:3072 lds
MM a[108:111],a[184:187],v[8:11],a[108:111]
EX v126,v126
MM a[108:111],a[188:191],v[12:15],a[108:111]
EX v127,v127
MM a[100:103],a[188:191],v[4:7],a[100:103]
CV v93,v122,v123
CV v94,v124,v125
MM a[100:103],a[184:187],v[0:3],a[100:103]
CV v95,v126,v127
v_smfmac_f32_16x16x64_bf16 v[144:147], v[236:239], v[64:71], v234
v_smfmac_f32_16x16x64_bf16 v[148:151], v[236:239], v[72:79], v234
v_smfmac_f32_16x16x64_bf16 v[152:155], v[236:239], v[80:87], v234
v_smfmac_f32_16x16x64_bf16 v[156:159], v[236:239], v[88:95], v234
s_waitcnt lgkmcnt(0)
s_cmp_ge_u32 s16, 108
s_cbranch_scc1 LLAP_%=
MM v[0:3],a[192:195],v[160:163],v[128:131]
s_add_u32 s64, s64, 16384
TR a[128:129],v232 offset:16384
s_add_u32 s81, s64, 896
MM v[0:3],a[196:199],v[176:179],v[0:3]
TR a[130:131],v232 offset:20480
v_max3_f32 v249, v144, v145, v148
MM v[0:3],a[200:203],v[192:195],v[0:3]
TR a[132:133],v232 offset:24576
v_max3_f32 v249, v249, v149, v152
MM v[0:3],a[204:207],v[208:211],v[0:3]
TR a[134:135],v232 offset:28672
v_max3_f32 v249, v249, v153, v156
MM v[4:7],a[220:223],v[208:211],v[128:131]
TR a[136:137],v233 offset:16384
v_max_f32 v249, v249, v157
MM v[4:7],a[216:219],v[192:195],v[4:7]
TR a[138:139],v233 offset:20480
v_cmp_lt_f32 vcc, s71, v249
MM v[4:7],a[212:215],v[176:179],v[4:7]
TR a[140:141],v233 offset:24576
TR a[142:143],v233 offset:28672
MM v[4:7],a[208:211],v[160:163],v[4:7]
s_cbranch_vccnz LSITE13_%=
LBACK13_%=:
MM v[12:15],a[208:211],v[164:167],v[132:135]
v_lshrrev_b32 v127, 8, v248
v_lshlrev_b32 v124, 9, v127
MM v[12:15],a[212:215],v[180:183],v[12:15]
v_and_b32 v126, 192, v248
v_add_u32 v124, v124, v126
MM v[12:15],a[216:219],v[196:199],v[12:15]
v_bfe_u32 v126, v248, 4, 2
v_lshl_add_u32 v124, v126, 3, v124
MM v[12:15],a[220:223],v[212:215],v[12:15]
s_lshr_b32 s76, %[swave], 1
EX v0,v0
MM v[8:11],a[204:207],v[212:215],v[132:135]
s_mul_i32 s76, s76, 10240
EX v1,v1
MM v[8:11],a[200:203],v[196:199],v[8:11]
s_and_b32 s77, %[swave], 1
EX v2,v2
MM v[8:11],a[196:199],v[180:183],v[8:11]
s_lshl_b32 s77, s77, 5
TR a[144:145],v232 offset:16896
)ASM"
R"ASM(
CV v0,v0,v1
MM v[8:11],a[192:195],v[164:167],v[8:11]
s_add_u32 s76, s76, s77
EX v3,v3
MM v[16:19],a[192:195],v[168:171],v[136:139]
s_add_u32 s76, s76, %[slds]
EX v4,v4
MM v[16:19],a[196:199],v[184:187],v[16:19]
s_add_u32 s76, s76, 131072
v_add_u32 v124, s76, v124
v_bfe_u32 v126, v248, 4, 3
MM v[16:19],a[200:203],v[200:203],v[16:19]
v_lshl_add_u32 v126, v127, 3, v126
v_lshlrev_b32 v126, 4, v126
MM v[16:19],a[204:207],v[216:219],v[16:19]
s_mul_i32 s76, %[swave], 3072
EX v5,v5
MM v[20:23],a[220:223],v[216:219],v[136:139]
s_add_u32 s76, s76, %[slds]
TR a[146:147],v232 offset:20992
CV v1,v2,v3
MM v[20:23],a[216:219],v[200:203],v[20:23]
s_add_u32 s76, s76, 151552
TR a[148:149],v232 offset:25088
v_add_u32 v125, s76, v126
MM v[20:23],a[212:215],v[184:187],v[20:23]
s_lshr_b32 s76, s48, 7
EX v6,v6
MM v[20:23],a[208:211],v[168:171],v[20:23]
s_lshl_b32 s76, s76, 5
EX v7,v7
MM v[28:31],a[208:211],v[172:175],v[140:143]
s_and_b32 s77, s48, 16
TR a[150:151],v232 offset:29184
TR a[152:153],v233 offset:16896
MM v[28:31],a[212:215],v[188:191],v[28:31]
s_add_u32 s76, s76, s77
EX v8,v8
MM v[28:31],a[216:219],v[204:207],v[28:31]
s_bfe_u32 s77, s48, 131077
TR a[154:155],v233 offset:20992
CV v2,v4,v5
MM v[28:31],a[220:223],v[220:223],v[28:31]
LPG12_%=:
s_cbranch_execz LTR0_%=
s_lshl_b32 s77, s77, 2
EX v9,v9
MM v[24:27],a[204:207],v[220:223],v[140:143]
s_add_u32 s76, s76, s77
TR a[156:157],v233 offset:25088
CV v3,v6,v7
MM v[24:27],a[200:203],v[204:207],v[24:27]
s_add_u32 s76, s76, %[swave]
EX v10,v10
MM v[24:27],a[196:199],v[188:191],v[24:27]
s_lshl_b32 s97, s76, 2
TR a[158:159],v233 offset:29184
CV v8,v8,v9
MM v[24:27],a[192:195],v[172:175],v[24:27]
s_add_u32 s97, s97, 77824
EX v11,v11
MM v[32:35],a[224:227],v[160:163],v[128:131]
s_lshl_b32 s76, s76, 9
TR a[160:161],v232 offset:17408
TR a[162:163],v232 offset:21504
MM v[32:35],a[228:231],v[176:179],v[32:35]
s_add_u32 s76, s76, 22183936
EX v12,v12
MM v[32:35],a[232:235],v[192:195],v[32:35]
EX v13,v13
MM v[32:35],a[236:239],v[208:211],v[32:35]
TR a[164:165],v232 offset:25600
CV v9,v10,v11
MM v[36:39],a[252:255],v[208:211],v[128:131]
EX v14,v14
MM v[36:39],a[248:251],v[192:195],v[36:39]
TR a[166:167],v232 offset:29696
CV v10,v12,v13
MM v[36:39],a[244:247],v[176:179],v[36:39]
EX v15,v15
MM v[36:39],a[240:243],v[160:163],v[36:39]
TR a[168:169],v233 offset:17408
TR a[170:171],v233 offset:21504
MM v[44:47],a[240:243],v[164:167],v[132:135]
EX v16,v16
MM v[44:47],a[244:247],v[180:183],v[44:47]
EX v17,v17
MM v[44:47],a[248:251],v[196:199],v[44:47]
TR a[172:173],v233 offset:25600
CV v11,v14,v15
MM v[44:47],a[252:255],v[212:215],v[44:47]
EX v18,v18
MM v[40:43],a[236:239],v[212:215],v[132:135]
TR a[174:175],v233 offset:29696
CV v16,v16,v17
MM v[40:43],a[232:235],v[196:199],v[40:43]
EX v19,v19
MM v[40:43],a[228:231],v[180:183],v[40:43]
TR a[176:177],v232 offset:17920
TR a[178:179],v232 offset:22016
MM v[40:43],a[224:227],v[164:167],v[40:43]
EX v20,v20
MM v[48:51],a[224:227],v[168:171],v[136:139]
EX v21,v21
MM v[48:51],a[228:231],v[184:187],v[48:51]
TR a[180:181],v232 offset:26112
CV v17,v18,v19
MM v[48:51],a[232:235],v[200:203],v[48:51]
EX v22,v22
MM v[48:51],a[236:239],v[216:219],v[48:51]
TR a[182:183],v232 offset:30208
CV v18,v20,v21
MM v[52:55],a[252:255],v[216:219],v[136:139]
EX v23,v23
MM v[52:55],a[248:251],v[200:203],v[52:55]
TR a[184:185],v233 offset:17920
TR a[186:187],v233 offset:22016
MM v[52:55],a[244:247],v[184:187],v[52:55]
EX v24,v24
MM v[52:55],a[240:243],v[168:171],v[52:55]
TR a[188:189],v233 offset:26112
CV v19,v22,v23
MM v[60:63],a[240:243],v[172:175],v[140:143]
EX v25,v25
MM v[60:63],a[244:247],v[188:191],v[60:63]
TR a[190:191],v233 offset:30208
CV v24,v24,v25
MM v[60:63],a[248:251],v[204:207],v[60:63]
EX v26,v26
MM v[60:63],a[252:255],v[220:223],v[60:63]
EX v27,v27
MM v[56:59],a[236:239],v[220:223],v[140:143]
EX v28,v28
MM v[56:59],a[232:235],v[204:207],v[56:59]
EX v29,v29
MM v[56:59],a[228:231],v[188:191],v[56:59]
s_getpc_b64 s[98:99]
LTA2_%=:
s_min_u32 s73, %[swave], 3
s_lshl_b32 s73, s73, 12
s_add_u32 s73, s73, LPG13_%=-LTA2_%=
s_add_u32 s98, s98, s73
s_addc_u32 s99, s99, 0
s_mov_b64 exec, 0
s_setpc_b64 s[98:99]
LTR2_%=:
s_mov_b64 exec, -1
CV v25,v26,v27
MM v[56:59],a[224:227],v[172:175],v[56:59]
s_waitcnt vmcnt(17) lgkmcnt(0)
s_barrier
MM a[0:3],a[128:131],v[64:67],a[0:3]
s_add_u32 m0, s65, 16384
RD a[192:195],v224 offset:49152
CV v26,v28,v29
MM a[0:3],a[132:135],v[68:71],a[0:3]
EX v30,v30
MM a[8:11],a[132:135],v[76:79],a[8:11]
EX v31,v31
MM a[8:11],a[128:131],v[72:75],a[8:11]
RD a[196:199],v225 offset:49152
CV v27,v30,v31
MM a[16:19],a[128:131],v[80:83],a[16:19]
s_waitcnt vmcnt(10)
TR v[96:97],v124 offset:0
TR v[98:99],v124 offset:256
MM a[16:19],a[132:135],v[84:87],a[16:19]
RD v[108:111],v125 offset:0
TR v[100:101],v124 offset:2048
MM a[24:27],a[132:135],v[92:95],a[24:27]
TR v[102:103],v124 offset:2304
RD v[112:115],v125 offset:512
MM a[24:27],a[128:131],v[88:91],a[24:27]
TR v[104:105],v124 offset:4096
TR v[106:107],v124 offset:4352
MM a[28:31],a[136:139],v[88:91],a[28:31]
RD v[116:119],v125 offset:1024
RD a[200:203],v226 offset:49152
EX v32,v32
MM a[28:31],a[140:143],v[92:95],a[28:31]
EX v33,v33
MM a[20:23],a[140:143],v[84:87],a[20:23]
EX v34,v34
MM a[20:23],a[136:139],v[80:83],a[20:23]
buffer_load_dwordx4 v228, s[8:11], s64 offen offset:0 lds
MM a[12:15],a[136:139],v[72:75],a[12:15]
EX v35,v35
MM a[12:15],a[140:143],v[76:79],a[12:15]
EX v36,v36
MM a[4:7],a[140:143],v[68:71],a[4:7]
CV v4,v32,v33
CV v5,v34,v35
MM a[4:7],a[136:139],v[64:67],a[4:7]
EX v37,v37
MM a[32:35],a[144:147],v[64:67],a[32:35]
EX v38,v38
MM a[32:35],a[148:151],v[68:71],a[32:35]
s_waitcnt lgkmcnt(1)
buffer_load_dwordx4 v229, s[8:11], s64 offen offset:1024 lds
MM a[40:43],a[148:151],v[76:79],a[40:43]
EX v39,v39
MM v[120:123],v[96:99],v[108:111],0
MM v[120:123],v[100:103],v[112:115],v[120:123]
MM v[120:123],v[104:107],v[116:119],v[120:123]
MM a[40:43],a[144:147],v[72:75],a[40:43]
TR v[96:97],v124 offset:6144
TR v[98:99],v124 offset:6400
MM a[48:51],a[144:147],v[80:83],a[48:51]
RD v[108:111],v125 offset:1536
TR v[100:101],v124 offset:8192
MM a[48:51],a[148:151],v[84:87],a[48:51]
TR v[102:103],v124 offset:8448
RD v[112:115],v125 offset:2048
MM a[56:59],a[148:151],v[92:95],a[56:59]
EX v40,v40
MM a[56:59],a[144:147],v[88:91],a[56:59]
EX v41,v41
MM a[60:63],a[152:155],v[88:91],a[60:63]
RD a[204:207],v227 offset:49152
CV v6,v36,v37
MM a[60:63],a[156:159],v[92:95],a[60:63]
EX v42,v42
MM a[52:55],a[156:159],v[84:87],a[52:55]
RD a[208:211],v224 offset:53248
CV v7,v38,v39
MM a[52:55],a[152:155],v[80:83],a[52:55]
EX v43,v43
MM a[44:47],a[152:155],v[72:75],a[44:47]
buffer_load_dwordx4 v230, s[8:11], s64 offen offset:2048 lds
MM a[44:47],a[156:159],v[76:79],a[44:47]
EX v44,v44
MM a[36:39],a[156:159],v[68:71],a[36:39]
s_waitcnt lgkmcnt(2)
EX v45,v45
MM a[36:39],a[152:155],v[64:67],a[36:39]
RD a[212:215],v225 offset:53248
CV v12,v40,v41
MM v[120:123],v[96:99],v[108:111],v[120:123]
MM v[120:123],v[100:103],v[112:115],v[120:123]
MM a[64:67],a[160:163],v[64:67],a[64:67]
EX v46,v46
MM a[64:67],a[164:167],v[68:71],a[64:67]
RD a[216:219],v226 offset:53248
CV v13,v42,v43
MM a[72:75],a[164:167],v[76:79],a[72:75]
EX v47,v47
MM a[72:75],a[160:163],v[72:75],a[72:75]
buffer_load_dwordx4 v231, s[8:11], s64 offen offset:3072 lds
MM a[80:83],a[160:163],v[80:83],a[80:83]
s_add_u32 m0, s65, 81920
EX v48,v48
MM a[80:83],a[164:167],v[84:87],a[80:83]
RD a[220:223],v227 offset:53248
CV v14,v44,v45
MM a[88:91],a[164:167],v[92:95],a[88:91]
s_mov_b32 exec_lo, 0x00ff00ff
s_mov_b32 exec_hi, 0x00ff00ff
buffer_store_dwordx4 v[120:123], v126, s[20:23], s76 offen offset:0 sc1
s_mov_b64 exec, -1
s_nop 0
MM a[88:91],a[160:163],v[88:91],a[88:91]
EX v49,v49
MM a[92:95],a[168:171],v[88:91],a[92:95]
EX v50,v50
MM a[92:95],a[172:175],v[92:95],a[92:95]
RD a[224:227],v224 offset:57344
CV v15,v46,v47
MM a[84:87],a[172:175],v[84:87],a[84:87]
EX v51,v51
MM a[84:87],a[168:171],v[80:83],a[84:87]
EX v52,v52
MM a[76:79],a[168:171],v[72:75],a[76:79]
buffer_load_dwordx4 v235, s[12:15], s81 offen offset:0 lds
MM a[76:79],a[172:175],v[76:79],a[76:79]
EX v53,v53
MM a[68:71],a[172:175],v[68:71],a[68:71]
RD a[228:231],v225 offset:57344
CV v20,v48,v49
MM a[68:71],a[168:171],v[64:67],a[68:71]
EX v54,v54
MM a[96:99],a[176:179],v[64:67],a[96:99]
RD a[232:235],v226 offset:57344
CV v21,v50,v51
MM a[96:99],a[180:183],v[68:71],a[96:99]
EX v55,v55
MM a[104:107],a[180:183],v[76:79],a[104:107]
RD a[236:239],v227 offset:57344
CV v22,v52,v53
MM a[104:107],a[176:179],v[72:75],a[104:107]
EX v56,v56
MM a[112:115],a[176:179],v[80:83],a[112:115]
buffer_load_dwordx4 v235, s[12:15], s64 offen offset:1024 lds
MM a[112:115],a[180:183],v[84:87],a[112:115]
EX v57,v57
MM a[120:123],a[180:183],v[92:95],a[120:123]
EX v58,v58
MM a[120:123],a[176:179],v[88:91],a[120:123]
RD a[240:243],v224 offset:61440
CV v23,v54,v55
MM a[124:127],a[184:187],v[88:91],a[124:127]
v_add_f32 v244, v144, v145
v_add_f32 v245, v148, v149
MM a[124:127],a[188:191],v[92:95],a[124:127]
v_add_f32 v246, v152, v153
v_add_f32 v247, v156, v157
MM a[116:119],a[188:191],v[84:87],a[116:119]
EX v59,v59
MM a[116:119],a[184:187],v[80:83],a[116:119]
EX v60,v60
MM a[108:111],a[184:187],v[72:75],a[108:111]
EX v61,v61
MM a[108:111],a[188:191],v[76:79],a[108:111]
RD a[244:247],v225 offset:61440
CV v28,v56,v57
MM a[100:103],a[188:191],v[68:71],a[100:103]
buffer_load_dwordx4 v235, s[12:15], s81 offen offset:2048 lds
MM a[100:103],a[184:187],v[64:67],a[100:103]
EX v62,v62
v_smfmac_f32_16x16x64_bf16 v[144:147], v[236:239], v[0:7], v234
RD a[248:251],v226 offset:61440
RD a[252:255],v227 offset:61440
EX v63,v63
v_smfmac_f32_16x16x64_bf16 v[148:151], v[236:239], v[8:15], v234
CV v29,v58,v59
v_smfmac_f32_16x16x64_bf16 v[152:155], v[236:239], v[16:23], v234
CV v30,v60,v61
CV v31,v62,v63
s_nop 1
v_smfmac_f32_16x16x64_bf16 v[156:159], v[236:239], v[24:31], v234
s_waitcnt lgkmcnt(0)
buffer_load_dwordx4 v235, s[12:15], s64 offen offset:3072 lds
MM v[64:67],a[192:195],v[160:163],v[128:131]
s_add_u32 s64, s64, 16384
TR a[128:129],v232 offset:32768
s_add_u32 s81, s64, 896
MM v[64:67],a[196:199],v[176:179],v[64:67]
TR a[130:131],v232 offset:36864
v_max3_f32 v249, v144, v145, v148
MM v[64:67],a[200:203],v[192:195],v[64:67]
TR a[132:133],v232 offset:40960
v_max3_f32 v249, v249, v149, v152
MM v[64:67],a[204:207],v[208:211],v[64:67]
TR a[134:135],v232 offset:45056
v_max3_f32 v249, v249, v153, v156
MM v[68:71],a[220:223],v[208:211],v[128:131]
TR a[136:137],v233 offset:32768
v_max_f32 v249, v249, v157
MM v[68:71],a[216:219],v[192:195],v[68:71]
TR a[138:139],v233 offset:36864
v_cmp_lt_f32 vcc, s71, v249
MM v[68:71],a[212:215],v[176:179],v[68:71]
TR a[140:141],v233 offset:40960
TR a[142:143],v233 offset:45056
MM v[68:71],a[208:211],v[160:163],v[68:71]
s_cbranch_vccnz LSITE14_%=
LBACK14_%=:
MM v[76:79],a[208:211],v[164:167],v[132:135]
s_add_u32 s48, s48, 16
v_lshrrev_b32 v252, 6, v248
v_mul_u32_u24 v252, 3456, v252
MM v[76:79],a[212:215],v[180:183],v[76:79]
v_and_b32 v251, 48, v248
v_add_u32 v252, v252, v251
MM v[76:79],a[216:219],v[196:199],v[76:79]
s_and_b32 s90, %[swave], 1
EX v64,v64
MM v[76:79],a[220:223],v[212:215],v[76:79]
s_mul_i32 s90, s90, 276480
EX v65,v65
MM v[72:75],a[204:207],v[212:215],v[132:135]
s_bfe_u32 s91, s48, 131077
EX v66,v66
MM v[72:75],a[200:203],v[196:199],v[72:75]
s_mul_i32 s91, s91, 552960
TR a[144:145],v232 offset:33280
CV v64,v64,v65
MM v[72:75],a[196:199],v[180:183],v[72:75]
s_add_u32 s90, s90, s91
EX v67,v67
MM v[72:75],a[192:195],v[164:167],v[72:75]
s_and_b32 s91, s48, 0xffffff80
TR a[146:147],v232 offset:37376
CV v65,v66,v67
MM v[80:83],a[192:195],v[168:171],v[136:139]
s_add_u32 s90, s90, s91
EX v68,v68
MM v[80:83],a[196:199],v[184:187],v[80:83]
s_and_b32 s91, s48, 16
TR a[148:149],v232 offset:41472
TR a[150:151],v232 offset:45568
MM v[80:83],a[200:203],v[200:203],v[80:83]
s_lshl_b32 s91, s91, 2
EX v69,v69
MM v[80:83],a[204:207],v[216:219],v[80:83]
s_add_u32 s66, s90, s91
TR a[152:153],v233 offset:33280
CV v66,v68,v69
MM v[84:87],a[220:223],v[216:219],v[136:139]
s_mul_i32 s91, %[swave], 5120
EX v70,v70
)ASM"
R"ASM(
MM v[84:87],a[216:219],v[200:203],v[84:87]
s_add_u32 s91, s91, %[slds]
EX v71,v71
MM v[84:87],a[212:215],v[184:187],v[84:87]
s_add_u32 s67, s91, 131072
TR a[154:155],v233 offset:37376
CV v67,v70,v71
MM v[84:87],a[208:211],v[168:171],v[84:87]
s_and_b32 s18, s17, 255
EX v72,v72
MM v[92:95],a[208:211],v[172:175],v[140:143]
s_sub_u32 s90, s18, 40
s_cmp_ge_u32 s18, 216
s_cselect_b32 s18, s90, s18
MM v[92:95],a[212:215],v[188:191],v[92:95]
s_lshl_b32 s18, s18, 4
EX v73,v73
MM v[92:95],a[216:219],v[204:207],v[92:95]
s_sub_u32 s90, s16, 90
TR a[156:157],v233 offset:41472
CV v72,v72,v73
MM v[92:95],a[220:223],v[220:223],v[92:95]
s_min_u32 s47, s90, s16
EX v74,v74
MM v[88:91],a[204:207],v[220:223],v[140:143]
s_mul_i32 s90, s47, 205
TR a[158:159],v233 offset:45568
TR a[160:161],v232 offset:33792
MM v[88:91],a[200:203],v[204:207],v[88:91]
s_lshr_b32 s46, s90, 11
EX v75,v75
MM v[88:91],a[196:199],v[188:191],v[88:91]
s_mul_i32 s90, s46, 244480
TR a[162:163],v232 offset:37888
CV v73,v74,v75
MM v[88:91],a[192:195],v[172:175],v[88:91]
s_lshl_b32 s91, s47, 7
EX v76,v76
MM v[96:99],a[224:227],v[160:163],v[128:131]
s_add_u32 s90, s90, s91
s_add_u32 s28, s28, s90
s_addc_u32 s29, s29, 0
MM v[96:99],a[228:231],v[176:179],v[96:99]
s_sub_u32 s30, 2211840, s90
EX v77,v77
MM v[96:99],a[232:235],v[192:195],v[96:99]
s_mov_b32 s31, 131072
TR a[164:165],v232 offset:41984
CV v74,v76,v77
MM v[96:99],a[236:239],v[208:211],v[96:99]
EX v78,v78
MM v[100:103],a[252:255],v[208:211],v[128:131]
TR a[166:167],v232 offset:46080
TR a[168:169],v233 offset:33792
MM v[100:103],a[248:251],v[192:195],v[100:103]
EX v79,v79
MM v[100:103],a[244:247],v[176:179],v[100:103]
TR a[170:171],v233 offset:37888
CV v75,v78,v79
MM v[100:103],a[240:243],v[160:163],v[100:103]
EX v80,v80
MM v[108:111],a[240:243],v[164:167],v[132:135]
TR a[172:173],v233 offset:41984
TR a[174:175],v233 offset:46080
MM v[108:111],a[244:247],v[180:183],v[108:111]
EX v81,v81
MM v[108:111],a[248:251],v[196:199],v[108:111]
EX v82,v82
MM v[108:111],a[252:255],v[212:215],v[108:111]
TR a[176:177],v232 offset:34304
CV v80,v80,v81
MM v[104:107],a[236:239],v[212:215],v[132:135]
EX v83,v83
MM v[104:107],a[232:235],v[196:199],v[104:107]
TR a[178:179],v232 offset:38400
CV v81,v82,v83
MM v[104:107],a[228:231],v[180:183],v[104:107]
EX v84,v84
MM v[104:107],a[224:227],v[164:167],v[104:107]
TR a[180:181],v232 offset:42496
TR a[182:183],v232 offset:46592
MM v[112:115],a[224:227],v[168:171],v[136:139]
EX v85,v85
MM v[112:115],a[228:231],v[184:187],v[112:115]
TR a[184:185],v233 offset:34304
CV v82,v84,v85
MM v[112:115],a[232:235],v[200:203],v[112:115]
EX v86,v86
MM v[112:115],a[236:239],v[216:219],v[112:115]
EX v87,v87
MM v[116:119],a[252:255],v[216:219],v[136:139]
TR a[186:187],v233 offset:38400
CV v83,v86,v87
MM v[116:119],a[248:251],v[200:203],v[116:119]
EX v88,v88
MM v[116:119],a[244:247],v[184:187],v[116:119]
TR a[188:189],v233 offset:42496
TR a[190:191],v233 offset:46592
MM v[116:119],a[240:243],v[168:171],v[116:119]
EX v89,v89
MM v[124:127],a[240:243],v[172:175],v[140:143]
EX v90,v90
MM v[124:127],a[244:247],v[188:191],v[124:127]
EX v91,v91
MM v[124:127],a[248:251],v[204:207],v[124:127]
EX v92,v92
MM v[124:127],a[252:255],v[220:223],v[124:127]
EX v93,v93
MM v[120:123],a[236:239],v[220:223],v[140:143]
EX v94,v94
MM v[120:123],a[232:235],v[204:207],v[120:123]
EX v95,v95
MM v[120:123],a[228:231],v[188:191],v[120:123]
EX v96,v96
MM v[120:123],a[224:227],v[172:175],v[120:123]
s_waitcnt vmcnt(14) lgkmcnt(0)
s_barrier
MM a[0:3],a[128:131],v[0:3],a[0:3]
s_add_u32 m0, s65, 32768
CV v88,v88,v89
CV v89,v90,v91
MM a[0:3],a[132:135],v[4:7],a[0:3]
RD a[192:195],v224 offset:0
CV v90,v92,v93
MM a[8:11],a[132:135],v[12:15],a[8:11]
RD a[196:199],v225 offset:0
CV v91,v94,v95
MM a[8:11],a[128:131],v[8:11],a[8:11]
buffer_load_dwordx4 v228, s[8:11], s64 offen offset:0 lds
MM a[16:19],a[128:131],v[16:19],a[16:19]
RD a[200:203],v226 offset:0
RD a[204:207],v227 offset:0
MM a[16:19],a[132:135],v[20:23],a[16:19]
EX v97,v97
MM a[24:27],a[132:135],v[28:31],a[24:27]
RD a[208:211],v224 offset:4096
CV v68,v96,v97
MM a[24:27],a[128:131],v[24:27],a[24:27]
EX v98,v98
MM a[28:31],a[136:139],v[24:27],a[28:31]
buffer_load_dwordx4 v229, s[8:11], s64 offen offset:1024 lds
MM a[28:31],a[140:143],v[28:31],a[28:31]
EX v99,v99
MM a[20:23],a[140:143],v[20:23],a[20:23]
RD a[212:215],v225 offset:4096
CV v69,v98,v99
MM a[20:23],a[136:139],v[16:19],a[20:23]
EX v100,v100
MM a[12:15],a[136:139],v[8:11],a[12:15]
EX v101,v101
MM a[12:15],a[140:143],v[12:15],a[12:15]
RD a[216:219],v226 offset:4096
CV v70,v100,v101
MM a[4:7],a[140:143],v[4:7],a[4:7]
EX v102,v102
MM a[4:7],a[136:139],v[0:3],a[4:7]
buffer_load_dwordx4 v230, s[8:11], s64 offen offset:2048 lds
MM a[32:35],a[144:147],v[0:3],a[32:35]
EX v103,v103
MM a[32:35],a[148:151],v[4:7],a[32:35]
RD a[220:223],v227 offset:4096
CV v71,v102,v103
MM a[40:43],a[148:151],v[12:15],a[40:43]
EX v104,v104
MM a[40:43],a[144:147],v[8:11],a[40:43]
EX v105,v105
MM a[48:51],a[144:147],v[16:19],a[48:51]
RD a[224:227],v224 offset:8192
CV v76,v104,v105
MM a[48:51],a[148:151],v[20:23],a[48:51]
EX v106,v106
MM a[56:59],a[148:151],v[28:31],a[56:59]
buffer_load_dwordx4 v231, s[8:11], s64 offen offset:3072 lds
MM a[56:59],a[144:147],v[24:27],a[56:59]
s_add_u32 m0, s65, 98304
EX v107,v107
MM a[60:63],a[152:155],v[24:27],a[60:63]
RD a[228:231],v225 offset:8192
CV v77,v106,v107
MM a[60:63],a[156:159],v[28:31],a[60:63]
EX v108,v108
MM a[52:55],a[156:159],v[20:23],a[52:55]
EX v109,v109
MM a[52:55],a[152:155],v[16:19],a[52:55]
RD a[232:235],v226 offset:8192
CV v78,v108,v109
MM a[44:47],a[152:155],v[8:11],a[44:47]
EX v110,v110
MM a[44:47],a[156:159],v[12:15],a[44:47]
buffer_load_dwordx4 v235, s[12:15], s81 offen offset:0 lds
MM a[36:39],a[156:159],v[4:7],a[36:39]
EX v111,v111
MM a[36:39],a[152:155],v[0:3],a[36:39]
RD a[236:239],v227 offset:8192
CV v79,v110,v111
MM a[64:67],a[160:163],v[0:3],a[64:67]
EX v112,v112
MM a[64:67],a[164:167],v[4:7],a[64:67]
EX v113,v113
MM a[72:75],a[164:167],v[12:15],a[72:75]
RD a[240:243],v224 offset:12288
CV v84,v112,v113
MM a[72:75],a[160:163],v[8:11],a[72:75]
buffer_load_dwordx4 v235, s[12:15], s64 offen offset:1024 lds
MM a[80:83],a[160:163],v[16:19],a[80:83]
s_waitcnt vmcnt(10)
v_mov_b32 v250, s69
s_mov_b64 exec, 1
buffer_store_dword v250, off, s[20:23], s97 offset:0 sc1
s_mov_b64 exec, -1
MM a[80:83],a[164:167],v[20:23],a[80:83]
s_add_u32 m0, s67, 0
s_add_u32 s85, s66, 0
buffer_load_dwordx4 v252, s[36:39], s85 offen offset:0 lds
MM a[88:91],a[164:167],v[28:31],a[88:91]
EX v114,v114
MM a[88:91],a[160:163],v[24:27],a[88:91]
EX v115,v115
MM a[92:95],a[168:171],v[24:27],a[92:95]
RD a[244:247],v225 offset:12288
CV v85,v114,v115
MM a[92:95],a[172:175],v[28:31],a[92:95]
EX v116,v116
MM a[84:87],a[172:175],v[20:23],a[84:87]
EX v117,v117
MM a[84:87],a[168:171],v[16:19],a[84:87]
RD a[248:251],v226 offset:12288
CV v86,v116,v117
MM a[76:79],a[168:171],v[8:11],a[76:79]
EX v118,v118
MM a[76:79],a[172:175],v[12:15],a[76:79]
EX v119,v119
MM a[68:71],a[172:175],v[4:7],a[68:71]
RD a[252:255],v227 offset:12288
CV v87,v118,v119
MM a[68:71],a[168:171],v[0:3],a[68:71]
s_add_u32 m0, s65, 98304
s_nop 0
buffer_load_dwordx4 v235, s[12:15], s81 offen offset:2048 lds
MM a[96:99],a[176:179],v[0:3],a[96:99]
EX v120,v120
MM a[96:99],a[180:183],v[4:7],a[96:99]
EX v121,v121
MM a[104:107],a[180:183],v[12:15],a[104:107]
EX v122,v122
s_nop 0
LPG13_%=:
s_cbranch_execz LTR2_%=
MM a[104:107],a[176:179],v[8:11],a[104:107]
buffer_load_dwordx4 v235, s[12:15], s64 offen offset:3072 lds
MM a[112:115],a[176:179],v[16:19],a[112:115]
EX v123,v123
MM a[112:115],a[180:183],v[20:23],a[112:115]
EX v124,v124
MM a[120:123],a[180:183],v[28:31],a[120:123]
EX v125,v125
MM a[120:123],a[176:179],v[24:27],a[120:123]
EX v126,v126
MM a[124:127],a[184:187],v[24:27],a[124:127]
v_add_f32 v244, v144, v145
v_add_f32 v245, v148, v149
MM a[124:127],a[188:191],v[28:31],a[124:127]
v_add_f32 v246, v152, v153
v_add_f32 v247, v156, v157
MM a[116:119],a[188:191],v[20:23],a[116:119]
EX v127,v127
MM a[116:119],a[184:187],v[16:19],a[116:119]
CV v92,v120,v121
CV v93,v122,v123
MM a[108:111],a[184:187],v[8:11],a[108:111]
CV v94,v124,v125
CV v95,v126,v127
MM a[108:111],a[188:191],v[12:15],a[108:111]
MM a[100:103],a[188:191],v[4:7],a[100:103]
MM a[100:103],a[184:187],v[0:3],a[100:103]
v_smfmac_f32_16x16x64_bf16 v[144:147], v[236:239], v[64:71], v234
v_smfmac_f32_16x16x64_bf16 v[148:151], v[236:239], v[72:79], v234
v_smfmac_f32_16x16x64_bf16 v[152:155], v[236:239], v[80:87], v234
v_smfmac_f32_16x16x64_bf16 v[156:159], v[236:239], v[88:95], v234
s_waitcnt lgkmcnt(0)
MM v[0:3],a[192:195],v[160:163],v[128:131]
s_add_u32 s64, s64, 16384
TR a[128:129],v232 offset:49152
s_add_u32 s81, s64, 896
MM v[0:3],a[196:199],v[176:179],v[0:3]
TR a[130:131],v232 offset:53248
v_max3_f32 v249, v144, v145, v148
MM v[0:3],a[200:203],v[192:195],v[0:3]
TR a[132:133],v232 offset:57344
v_max3_f32 v249, v249, v149, v152
MM v[0:3],a[204:207],v[208:211],v[0:3]
TR a[134:135],v232 offset:61440
v_max3_f32 v249, v249, v153, v156
MM v[4:7],a[220:223],v[208:211],v[128:131]
TR a[136:137],v233 offset:49152
v_max_f32 v249, v249, v157
MM v[4:7],a[216:219],v[192:195],v[4:7]
TR a[138:139],v233 offset:53248
v_cmp_lt_f32 vcc, s71, v249
MM v[4:7],a[212:215],v[176:179],v[4:7]
TR a[140:141],v233 offset:57344
TR a[142:143],v233 offset:61440
MM v[4:7],a[208:211],v[160:163],v[4:7]
s_cbranch_vccnz LSITE15_%=
LBACK15_%=:
MM v[12:15],a[208:211],v[164:167],v[132:135]
s_add_u32 m0, s67, 1024
s_add_u32 s85, s66, 55296
buffer_load_dwordx4 v252, s[36:39], s85 offen offset:0 lds
MM v[12:15],a[212:215],v[180:183],v[12:15]
EX v0,v0
MM v[12:15],a[216:219],v[196:199],v[12:15]
EX v1,v1
MM v[12:15],a[220:223],v[212:215],v[12:15]
TR a[144:145],v232 offset:49664
CV v0,v0,v1
MM v[8:11],a[204:207],v[212:215],v[132:135]
EX v2,v2
MM v[8:11],a[200:203],v[196:199],v[8:11]
EX v3,v3
MM v[8:11],a[196:199],v[180:183],v[8:11]
TR a[146:147],v232 offset:53760
CV v1,v2,v3
MM v[8:11],a[192:195],v[164:167],v[8:11]
EX v4,v4
MM v[16:19],a[192:195],v[168:171],v[136:139]
TR a[148:149],v232 offset:57856
TR a[150:151],v232 offset:61952
MM v[16:19],a[196:199],v[184:187],v[16:19]
EX v5,v5
MM v[16:19],a[200:203],v[200:203],v[16:19]
TR a[152:153],v233 offset:49664
CV v2,v4,v5
MM v[16:19],a[204:207],v[216:219],v[16:19]
EX v6,v6
MM v[20:23],a[220:223],v[216:219],v[136:139]
EX v7,v7
MM v[20:23],a[216:219],v[200:203],v[20:23]
TR a[154:155],v233 offset:53760
CV v3,v6,v7
MM v[20:23],a[212:215],v[184:187],v[20:23]
EX v8,v8
MM v[20:23],a[208:211],v[168:171],v[20:23]
TR a[156:157],v233 offset:57856
TR a[158:159],v233 offset:61952
MM v[28:31],a[208:211],v[172:175],v[140:143]
EX v9,v9
MM v[28:31],a[212:215],v[188:191],v[28:31]
TR a[160:161],v232 offset:50176
CV v8,v8,v9
MM v[28:31],a[216:219],v[204:207],v[28:31]
EX v10,v10
MM v[28:31],a[220:223],v[220:223],v[28:31]
EX v11,v11
MM v[24:27],a[204:207],v[220:223],v[140:143]
TR a[162:163],v232 offset:54272
CV v9,v10,v11
MM v[24:27],a[200:203],v[204:207],v[24:27]
TR a[164:165],v232 offset:58368
TR a[166:167],v232 offset:62464
MM v[24:27],a[196:199],v[188:191],v[24:27]
EX v12,v12
MM v[24:27],a[192:195],v[172:175],v[24:27]
EX v13,v13
MM v[32:35],a[224:227],v[160:163],v[128:131]
TR a[168:169],v233 offset:50176
CV v10,v12,v13
MM v[32:35],a[228:231],v[176:179],v[32:35]
EX v14,v14
MM v[32:35],a[232:235],v[192:195],v[32:35]
TR a[170:171],v233 offset:54272
TR a[172:173],v233 offset:58368
MM v[32:35],a[236:239],v[208:211],v[32:35]
EX v15,v15
MM v[36:39],a[252:255],v[208:211],v[128:131]
TR a[174:175],v233 offset:62464
CV v11,v14,v15
MM v[36:39],a[248:251],v[192:195],v[36:39]
EX v16,v16
MM v[36:39],a[244:247],v[176:179],v[36:39]
EX v17,v17
MM v[36:39],a[240:243],v[160:163],v[36:39]
TR a[176:177],v232 offset:50688
CV v16,v16,v17
MM v[44:47],a[240:243],v[164:167],v[132:135]
s_add_u32 m0, s67, 2048
s_add_u32 s85, s66, 110592
buffer_load_dwordx4 v252, s[36:39], s85 offen offset:0 lds
MM v[44:47],a[244:247],v[180:183],v[44:47]
EX v18,v18
MM v[44:47],a[248:251],v[196:199],v[44:47]
TR a[178:179],v232 offset:54784
TR a[180:181],v232 offset:58880
)ASM"
R"ASM(
MM v[44:47],a[252:255],v[212:215],v[44:47]
EX v19,v19
MM v[40:43],a[236:239],v[212:215],v[132:135]
TR a[182:183],v232 offset:62976
CV v17,v18,v19
MM v[40:43],a[232:235],v[196:199],v[40:43]
EX v20,v20
MM v[40:43],a[228:231],v[180:183],v[40:43]
EX v21,v21
MM v[40:43],a[224:227],v[164:167],v[40:43]
TR a[184:185],v233 offset:50688
CV v18,v20,v21
MM v[48:51],a[224:227],v[168:171],v[136:139]
EX v22,v22
MM v[48:51],a[228:231],v[184:187],v[48:51]
TR a[186:187],v233 offset:54784
TR a[188:189],v233 offset:58880
MM v[48:51],a[232:235],v[200:203],v[48:51]
EX v23,v23
MM v[48:51],a[236:239],v[216:219],v[48:51]
TR a[190:191],v233 offset:62976
CV v19,v22,v23
MM v[52:55],a[252:255],v[216:219],v[136:139]
EX v24,v24
MM v[52:55],a[248:251],v[200:203],v[52:55]
EX v25,v25
MM v[52:55],a[244:247],v[184:187],v[52:55]
EX v26,v26
MM v[52:55],a[240:243],v[168:171],v[52:55]
EX v27,v27
MM v[60:63],a[240:243],v[172:175],v[140:143]
EX v28,v28
MM v[60:63],a[244:247],v[188:191],v[60:63]
EX v29,v29
MM v[60:63],a[248:251],v[204:207],v[60:63]
EX v30,v30
MM v[60:63],a[252:255],v[220:223],v[60:63]
EX v31,v31
MM v[56:59],a[236:239],v[220:223],v[140:143]
EX v32,v32
MM v[56:59],a[232:235],v[204:207],v[56:59]
EX v33,v33
MM v[56:59],a[228:231],v[188:191],v[56:59]
EX v34,v34
MM v[56:59],a[224:227],v[172:175],v[56:59]
s_waitcnt lgkmcnt(0)
s_barrier
MM a[0:3],a[128:131],v[64:67],a[0:3]
s_add_u32 m0, s65, 49152
CV v24,v24,v25
CV v25,v26,v27
MM a[0:3],a[132:135],v[68:71],a[0:3]
RD a[192:195],v224 offset:16384
CV v26,v28,v29
MM a[8:11],a[132:135],v[76:79],a[8:11]
RD a[196:199],v225 offset:16384
CV v27,v30,v31
MM a[8:11],a[128:131],v[72:75],a[8:11]
buffer_load_dwordx4 v228, s[8:11], s64 offen offset:0 lds
MM a[16:19],a[128:131],v[80:83],a[16:19]
s_add_u32 m0, s67, 3072
s_add_u32 s85, s66, 165888
buffer_load_dwordx4 v252, s[36:39], s85 offen offset:0 lds
MM a[16:19],a[132:135],v[84:87],a[16:19]
RD a[200:203],v226 offset:16384
CV v4,v32,v33
MM a[24:27],a[132:135],v[92:95],a[24:27]
RD a[204:207],v227 offset:16384
RD a[208:211],v224 offset:20480
MM a[24:27],a[128:131],v[88:91],a[24:27]
s_add_u32 m0, s65, 49152
s_nop 0
buffer_load_dwordx4 v229, s[8:11], s64 offen offset:1024 lds
MM a[28:31],a[136:139],v[88:91],a[28:31]
EX v35,v35
MM a[28:31],a[140:143],v[92:95],a[28:31]
RD a[212:215],v225 offset:20480
CV v5,v34,v35
MM a[20:23],a[140:143],v[84:87],a[20:23]
EX v36,v36
MM a[20:23],a[136:139],v[80:83],a[20:23]
EX v37,v37
MM a[12:15],a[136:139],v[72:75],a[12:15]
RD a[216:219],v226 offset:20480
CV v6,v36,v37
MM a[12:15],a[140:143],v[76:79],a[12:15]
buffer_load_dwordx4 v230, s[8:11], s64 offen offset:2048 lds
MM a[4:7],a[140:143],v[68:71],a[4:7]
EX v38,v38
MM a[4:7],a[136:139],v[64:67],a[4:7]
EX v39,v39
MM a[32:35],a[144:147],v[64:67],a[32:35]
RD a[220:223],v227 offset:20480
CV v7,v38,v39
MM a[32:35],a[148:151],v[68:71],a[32:35]
EX v40,v40
MM a[40:43],a[148:151],v[76:79],a[40:43]
EX v41,v41
MM a[40:43],a[144:147],v[72:75],a[40:43]
RD a[224:227],v224 offset:24576
CV v12,v40,v41
MM a[48:51],a[144:147],v[80:83],a[48:51]
buffer_load_dwordx4 v231, s[8:11], s64 offen offset:3072 lds
MM a[48:51],a[148:151],v[84:87],a[48:51]
s_add_u32 m0, s65, 114688
EX v42,v42
MM a[56:59],a[148:151],v[92:95],a[56:59]
EX v43,v43
MM a[56:59],a[144:147],v[88:91],a[56:59]
RD a[228:231],v225 offset:24576
CV v13,v42,v43
MM a[60:63],a[152:155],v[88:91],a[60:63]
EX v44,v44
MM a[60:63],a[156:159],v[92:95],a[60:63]
buffer_load_dwordx4 v235, s[12:15], s81 offen offset:0 lds
MM a[52:55],a[156:159],v[84:87],a[52:55]
EX v45,v45
MM a[52:55],a[152:155],v[80:83],a[52:55]
RD a[232:235],v226 offset:24576
CV v14,v44,v45
MM a[44:47],a[152:155],v[72:75],a[44:47]
EX v46,v46
MM a[44:47],a[156:159],v[76:79],a[44:47]
EX v47,v47
MM a[36:39],a[156:159],v[68:71],a[36:39]
RD a[236:239],v227 offset:24576
CV v15,v46,v47
MM a[36:39],a[152:155],v[64:67],a[36:39]
EX v48,v48
MM a[64:67],a[160:163],v[64:67],a[64:67]
buffer_load_dwordx4 v235, s[12:15], s64 offen offset:1024 lds
MM a[64:67],a[164:167],v[68:71],a[64:67]
EX v49,v49
MM a[72:75],a[164:167],v[76:79],a[72:75]
RD a[240:243],v224 offset:28672
CV v20,v48,v49
MM a[72:75],a[160:163],v[72:75],a[72:75]
EX v50,v50
MM a[80:83],a[160:163],v[80:83],a[80:83]
EX v51,v51
MM a[80:83],a[164:167],v[84:87],a[80:83]
RD a[244:247],v225 offset:28672
CV v21,v50,v51
MM a[88:91],a[164:167],v[92:95],a[88:91]
EX v52,v52
MM a[88:91],a[160:163],v[88:91],a[88:91]
buffer_load_dwordx4 v235, s[12:15], s81 offen offset:2048 lds
MM a[92:95],a[168:171],v[88:91],a[92:95]
EX v53,v53
MM a[92:95],a[172:175],v[92:95],a[92:95]
RD a[248:251],v226 offset:28672
CV v22,v52,v53
MM a[84:87],a[172:175],v[84:87],a[84:87]
EX v54,v54
MM a[84:87],a[168:171],v[80:83],a[84:87]
EX v55,v55
MM a[76:79],a[168:171],v[72:75],a[76:79]
RD a[252:255],v227 offset:28672
CV v23,v54,v55
MM a[76:79],a[172:175],v[76:79],a[76:79]
EX v56,v56
MM a[68:71],a[172:175],v[68:71],a[68:71]
buffer_load_dwordx4 v235, s[12:15], s64 offen offset:3072 lds
MM a[68:71],a[168:171],v[64:67],a[68:71]
EX v57,v57
MM a[96:99],a[176:179],v[64:67],a[96:99]
EX v58,v58
MM a[96:99],a[180:183],v[68:71],a[96:99]
EX v59,v59
MM a[104:107],a[180:183],v[76:79],a[104:107]
EX v60,v60
MM a[104:107],a[176:179],v[72:75],a[104:107]
EX v61,v61
MM a[112:115],a[176:179],v[80:83],a[112:115]
EX v62,v62
MM a[112:115],a[180:183],v[84:87],a[112:115]
EX v63,v63
MM a[120:123],a[180:183],v[92:95],a[120:123]
s_add_u32 m0, s67, 4096
s_add_u32 s85, s66, 221184
buffer_load_dwordx4 v252, s[36:39], s85 offen offset:0 lds
MM a[120:123],a[176:179],v[88:91],a[120:123]
CV v28,v56,v57
CV v29,v58,v59
MM a[124:127],a[184:187],v[88:91],a[124:127]
v_add_f32 v244, v144, v145
v_add_f32 v245, v148, v149
MM a[124:127],a[188:191],v[92:95],a[124:127]
v_add_f32 v246, v152, v153
v_add_f32 v247, v156, v157
MM a[116:119],a[188:191],v[84:87],a[116:119]
CV v30,v60,v61
CV v31,v62,v63
MM a[116:119],a[184:187],v[80:83],a[116:119]
MM a[108:111],a[184:187],v[72:75],a[108:111]
MM a[108:111],a[188:191],v[76:79],a[108:111]
MM a[100:103],a[188:191],v[68:71],a[100:103]
MM a[100:103],a[184:187],v[64:67],a[100:103]
v_smfmac_f32_16x16x64_bf16 v[144:147], v[236:239], v[0:7], v234
v_smfmac_f32_16x16x64_bf16 v[148:151], v[236:239], v[8:15], v234
v_smfmac_f32_16x16x64_bf16 v[152:155], v[236:239], v[16:23], v234
v_smfmac_f32_16x16x64_bf16 v[156:159], v[236:239], v[24:31], v234
s_waitcnt lgkmcnt(0)
MM v[64:67],a[192:195],v[160:163],v[128:131]
s_add_u32 s64, s64, 16384
TR a[128:129],v232 offset:0
s_add_u32 s81, s64, 896
MM v[64:67],a[196:199],v[176:179],v[64:67]
TR a[130:131],v232 offset:4096
v_max3_f32 v249, v144, v145, v148
MM v[64:67],a[200:203],v[192:195],v[64:67]
TR a[132:133],v232 offset:8192
v_max3_f32 v249, v249, v149, v152
MM v[64:67],a[204:207],v[208:211],v[64:67]
TR a[134:135],v232 offset:12288
v_max3_f32 v249, v249, v153, v156
MM v[68:71],a[220:223],v[208:211],v[128:131]
TR a[136:137],v233 offset:0
v_max_f32 v249, v249, v157
MM v[68:71],a[216:219],v[192:195],v[68:71]
TR a[138:139],v233 offset:4096
v_cmp_lt_f32 vcc, s71, v249
MM v[68:71],a[212:215],v[176:179],v[68:71]
TR a[140:141],v233 offset:8192
TR a[142:143],v233 offset:12288
MM v[68:71],a[208:211],v[160:163],v[68:71]
s_cbranch_vccnz LSITE16_%=
LBACK16_%=:
MM v[76:79],a[208:211],v[164:167],v[132:135]
EX v64,v64
MM v[76:79],a[212:215],v[180:183],v[76:79]
EX v65,v65
MM v[76:79],a[216:219],v[196:199],v[76:79]
TR a[144:145],v232 offset:512
CV v64,v64,v65
MM v[76:79],a[220:223],v[212:215],v[76:79]
EX v66,v66
MM v[72:75],a[204:207],v[212:215],v[132:135]
EX v67,v67
MM v[72:75],a[200:203],v[196:199],v[72:75]
TR a[146:147],v232 offset:4608
CV v65,v66,v67
MM v[72:75],a[196:199],v[180:183],v[72:75]
EX v68,v68
MM v[72:75],a[192:195],v[164:167],v[72:75]
TR a[148:149],v232 offset:8704
TR a[150:151],v232 offset:12800
MM v[80:83],a[192:195],v[168:171],v[136:139]
EX v69,v69
MM v[80:83],a[196:199],v[184:187],v[80:83]
TR a[152:153],v233 offset:512
CV v66,v68,v69
MM v[80:83],a[200:203],v[200:203],v[80:83]
EX v70,v70
MM v[80:83],a[204:207],v[216:219],v[80:83]
TR a[154:155],v233 offset:4608
TR a[156:157],v233 offset:8704
MM v[84:87],a[220:223],v[216:219],v[136:139]
s_add_u32 s87, s95, 1024
buffer_store_dword v240, v248, s[20:23], s87 offen offset:12 sc1
MM v[84:87],a[216:219],v[200:203],v[84:87]
EX v71,v71
MM v[84:87],a[212:215],v[184:187],v[84:87]
TR a[158:159],v233 offset:12800
CV v67,v70,v71
MM v[84:87],a[208:211],v[168:171],v[84:87]
EX v72,v72
MM v[92:95],a[208:211],v[172:175],v[140:143]
EX v73,v73
MM v[92:95],a[212:215],v[188:191],v[92:95]
TR a[160:161],v232 offset:1024
CV v72,v72,v73
MM v[92:95],a[216:219],v[204:207],v[92:95]
EX v74,v74
MM v[92:95],a[220:223],v[220:223],v[92:95]
EX v75,v75
MM v[88:91],a[204:207],v[220:223],v[140:143]
TR a[162:163],v232 offset:5120
CV v73,v74,v75
MM v[88:91],a[200:203],v[204:207],v[88:91]
TR a[164:165],v232 offset:9216
TR a[166:167],v232 offset:13312
MM v[88:91],a[196:199],v[188:191],v[88:91]
EX v76,v76
MM v[88:91],a[192:195],v[172:175],v[88:91]
EX v77,v77
MM v[96:99],a[224:227],v[160:163],v[128:131]
TR a[168:169],v233 offset:1024
CV v74,v76,v77
MM v[96:99],a[228:231],v[176:179],v[96:99]
EX v78,v78
MM v[96:99],a[232:235],v[192:195],v[96:99]
TR a[170:171],v233 offset:5120
TR a[172:173],v233 offset:9216
MM v[96:99],a[236:239],v[208:211],v[96:99]
EX v79,v79
MM v[100:103],a[252:255],v[208:211],v[128:131]
TR a[174:175],v233 offset:13312
CV v75,v78,v79
MM v[100:103],a[248:251],v[192:195],v[100:103]
EX v80,v80
MM v[100:103],a[244:247],v[176:179],v[100:103]
EX v81,v81
MM v[100:103],a[240:243],v[160:163],v[100:103]
TR a[176:177],v232 offset:1536
CV v80,v80,v81
MM v[108:111],a[240:243],v[164:167],v[132:135]
EX v82,v82
MM v[108:111],a[244:247],v[180:183],v[108:111]
TR a[178:179],v232 offset:5632
TR a[180:181],v232 offset:9728
MM v[108:111],a[248:251],v[196:199],v[108:111]
EX v83,v83
MM v[108:111],a[252:255],v[212:215],v[108:111]
TR a[182:183],v232 offset:13824
CV v81,v82,v83
MM v[104:107],a[236:239],v[212:215],v[132:135]
EX v84,v84
MM v[104:107],a[232:235],v[196:199],v[104:107]
EX v85,v85
MM v[104:107],a[228:231],v[180:183],v[104:107]
TR a[184:185],v233 offset:1536
CV v82,v84,v85
MM v[104:107],a[224:227],v[164:167],v[104:107]
EX v86,v86
MM v[112:115],a[224:227],v[168:171],v[136:139]
TR a[186:187],v233 offset:5632
TR a[188:189],v233 offset:9728
MM v[112:115],a[228:231],v[184:187],v[112:115]
EX v87,v87
MM v[112:115],a[232:235],v[200:203],v[112:115]
TR a[190:191],v233 offset:13824
CV v83,v86,v87
MM v[112:115],a[236:239],v[216:219],v[112:115]
EX v88,v88
MM v[116:119],a[252:255],v[216:219],v[136:139]
EX v89,v89
MM v[116:119],a[248:251],v[200:203],v[116:119]
EX v90,v90
MM v[116:119],a[244:247],v[184:187],v[116:119]
EX v91,v91
MM v[116:119],a[240:243],v[168:171],v[116:119]
EX v92,v92
MM v[124:127],a[240:243],v[172:175],v[140:143]
EX v93,v93
MM v[124:127],a[244:247],v[188:191],v[124:127]
EX v94,v94
MM v[124:127],a[248:251],v[204:207],v[124:127]
EX v95,v95
MM v[124:127],a[252:255],v[220:223],v[124:127]
EX v96,v96
MM v[120:123],a[236:239],v[220:223],v[140:143]
EX v97,v97
MM v[120:123],a[232:235],v[204:207],v[120:123]
EX v98,v98
MM v[120:123],a[228:231],v[188:191],v[120:123]
EX v99,v99
MM v[120:123],a[224:227],v[172:175],v[120:123]
s_waitcnt vmcnt(19) lgkmcnt(0)
s_barrier
MM a[0:3],a[128:131],v[0:3],a[0:3]
s_add_u32 m0, s65, 0
CV v88,v88,v89
CV v89,v90,v91
MM a[0:3],a[132:135],v[4:7],a[0:3]
RD a[192:195],v224 offset:32768
CV v90,v92,v93
MM a[8:11],a[132:135],v[12:15],a[8:11]
RD a[196:199],v225 offset:32768
CV v91,v94,v95
MM a[8:11],a[128:131],v[8:11],a[8:11]
buffer_load_dwordx4 v228, s[8:11], s64 offen offset:0 lds
MM a[16:19],a[128:131],v[16:19],a[16:19]
RD a[200:203],v226 offset:32768
CV v68,v96,v97
MM a[16:19],a[132:135],v[20:23],a[16:19]
RD a[204:207],v227 offset:32768
CV v69,v98,v99
MM a[24:27],a[132:135],v[28:31],a[24:27]
buffer_load_dwordx4 v229, s[8:11], s64 offen offset:1024 lds
MM a[24:27],a[128:131],v[24:27],a[24:27]
RD a[208:211],v224 offset:36864
RD a[212:215],v225 offset:36864
MM a[28:31],a[136:139],v[24:27],a[28:31]
buffer_load_dwordx4 v230, s[8:11], s64 offen offset:2048 lds
MM a[28:31],a[140:143],v[28:31],a[28:31]
EX v100,v100
)ASM"
R"ASM(
MM a[20:23],a[140:143],v[20:23],a[20:23]
EX v101,v101
MM a[20:23],a[136:139],v[16:19],a[20:23]
RD a[216:219],v226 offset:36864
CV v70,v100,v101
MM a[12:15],a[136:139],v[8:11],a[12:15]
EX v102,v102
MM a[12:15],a[140:143],v[12:15],a[12:15]
RD a[220:223],v227 offset:36864
RD a[224:227],v224 offset:40960
MM a[4:7],a[140:143],v[4:7],a[4:7]
EX v103,v103
MM a[4:7],a[136:139],v[0:3],a[4:7]
RD a[228:231],v225 offset:40960
CV v71,v102,v103
MM a[32:35],a[144:147],v[0:3],a[32:35]
EX v104,v104
MM a[32:35],a[148:151],v[4:7],a[32:35]
buffer_load_dwordx4 v231, s[8:11], s64 offen offset:3072 lds
MM a[40:43],a[148:151],v[12:15],a[40:43]
s_add_u32 m0, s65, 65536
EX v105,v105
MM a[40:43],a[144:147],v[8:11],a[40:43]
RD a[232:235],v226 offset:40960
CV v76,v104,v105
MM a[48:51],a[144:147],v[16:19],a[48:51]
EX v106,v106
MM a[48:51],a[148:151],v[20:23],a[48:51]
EX v107,v107
MM a[56:59],a[148:151],v[28:31],a[56:59]
RD a[236:239],v227 offset:40960
CV v77,v106,v107
MM a[56:59],a[144:147],v[24:27],a[56:59]
EX v108,v108
MM a[60:63],a[152:155],v[24:27],a[60:63]
buffer_load_dwordx4 v235, s[12:15], s81 offen offset:0 lds
MM a[60:63],a[156:159],v[28:31],a[60:63]
EX v109,v109
MM a[52:55],a[156:159],v[20:23],a[52:55]
s_add_u32 s87, s95, 2048
buffer_store_dword v240, v248, s[20:23], s87 offen offset:12 sc1
MM a[52:55],a[152:155],v[16:19],a[52:55]
RD a[240:243],v224 offset:45056
CV v78,v108,v109
MM a[44:47],a[152:155],v[8:11],a[44:47]
EX v110,v110
MM a[44:47],a[156:159],v[12:15],a[44:47]
EX v111,v111
MM a[36:39],a[156:159],v[4:7],a[36:39]
RD a[244:247],v225 offset:45056
CV v79,v110,v111
LPG14_%=:
s_cbranch_execz LTR2_%=
MM a[36:39],a[152:155],v[0:3],a[36:39]
EX v112,v112
MM a[64:67],a[160:163],v[0:3],a[64:67]
buffer_load_dwordx4 v235, s[12:15], s64 offen offset:1024 lds
MM a[64:67],a[164:167],v[4:7],a[64:67]
EX v113,v113
MM a[72:75],a[164:167],v[12:15],a[72:75]
RD a[248:251],v226 offset:45056
CV v84,v112,v113
MM a[72:75],a[160:163],v[8:11],a[72:75]
EX v114,v114
MM a[80:83],a[160:163],v[16:19],a[80:83]
EX v115,v115
MM a[80:83],a[164:167],v[20:23],a[80:83]
RD a[252:255],v227 offset:45056
CV v85,v114,v115
MM a[88:91],a[164:167],v[28:31],a[88:91]
EX v116,v116
MM a[88:91],a[160:163],v[24:27],a[88:91]
buffer_load_dwordx4 v235, s[12:15], s81 offen offset:2048 lds
MM a[92:95],a[168:171],v[24:27],a[92:95]
EX v117,v117
MM a[92:95],a[172:175],v[28:31],a[92:95]
EX v118,v118
MM a[84:87],a[172:175],v[20:23],a[84:87]
buffer_load_dwordx4 v235, s[12:15], s64 offen offset:3072 lds
MM a[84:87],a[168:171],v[16:19],a[84:87]
EX v119,v119
MM a[76:79],a[168:171],v[8:11],a[76:79]
EX v120,v120
MM a[76:79],a[172:175],v[12:15],a[76:79]
EX v121,v121
MM a[68:71],a[172:175],v[4:7],a[68:71]
EX v122,v122
MM a[68:71],a[168:171],v[0:3],a[68:71]
EX v123,v123
MM a[96:99],a[176:179],v[0:3],a[96:99]
EX v124,v124
MM a[96:99],a[180:183],v[4:7],a[96:99]
EX v125,v125
MM a[104:107],a[180:183],v[12:15],a[104:107]
EX v126,v126
MM a[104:107],a[176:179],v[8:11],a[104:107]
EX v127,v127
MM a[112:115],a[176:179],v[16:19],a[112:115]
CV v86,v116,v117
CV v87,v118,v119
MM a[112:115],a[180:183],v[20:23],a[112:115]
CV v92,v120,v121
CV v93,v122,v123
MM a[120:123],a[180:183],v[28:31],a[120:123]
CV v94,v124,v125
CV v95,v126,v127
MM a[120:123],a[176:179],v[24:27],a[120:123]
MM a[124:127],a[184:187],v[24:27],a[124:127]
v_add_f32 v244, v144, v145
v_add_f32 v245, v148, v149
MM a[124:127],a[188:191],v[28:31],a[124:127]
v_add_f32 v246, v152, v153
v_add_f32 v247, v156, v157
MM a[116:119],a[188:191],v[20:23],a[116:119]
MM a[116:119],a[184:187],v[16:19],a[116:119]
MM a[108:111],a[184:187],v[8:11],a[108:111]
MM a[108:111],a[188:191],v[12:15],a[108:111]
MM a[100:103],a[188:191],v[4:7],a[100:103]
MM a[100:103],a[184:187],v[0:3],a[100:103]
v_smfmac_f32_16x16x64_bf16 v[144:147], v[236:239], v[64:71], v234
v_smfmac_f32_16x16x64_bf16 v[148:151], v[236:239], v[72:79], v234
v_smfmac_f32_16x16x64_bf16 v[152:155], v[236:239], v[80:87], v234
v_smfmac_f32_16x16x64_bf16 v[156:159], v[236:239], v[88:95], v234
s_waitcnt lgkmcnt(0)
MM v[0:3],a[192:195],v[160:163],v[128:131]
s_add_u32 s64, s64, 16384
TR a[128:129],v232 offset:16384
s_add_u32 s81, s64, 896
MM v[0:3],a[196:199],v[176:179],v[0:3]
TR a[130:131],v232 offset:20480
v_max3_f32 v249, v144, v145, v148
MM v[0:3],a[200:203],v[192:195],v[0:3]
TR a[132:133],v232 offset:24576
v_max3_f32 v249, v249, v149, v152
MM v[0:3],a[204:207],v[208:211],v[0:3]
TR a[134:135],v232 offset:28672
v_max3_f32 v249, v249, v153, v156
MM v[4:7],a[220:223],v[208:211],v[128:131]
TR a[136:137],v233 offset:16384
v_max_f32 v249, v249, v157
MM v[4:7],a[216:219],v[192:195],v[4:7]
TR a[138:139],v233 offset:20480
v_cmp_lt_f32 vcc, s71, v249
MM v[4:7],a[212:215],v[176:179],v[4:7]
TR a[140:141],v233 offset:24576
TR a[142:143],v233 offset:28672
MM v[4:7],a[208:211],v[160:163],v[4:7]
s_cbranch_vccnz LSITE17_%=
LBACK17_%=:
MM v[12:15],a[208:211],v[164:167],v[132:135]
v_lshrrev_b32 v127, 8, v248
v_lshlrev_b32 v124, 9, v127
MM v[12:15],a[212:215],v[180:183],v[12:15]
v_and_b32 v126, 192, v248
v_add_u32 v124, v124, v126
MM v[12:15],a[216:219],v[196:199],v[12:15]
v_bfe_u32 v126, v248, 4, 2
v_lshl_add_u32 v124, v126, 3, v124
MM v[12:15],a[220:223],v[212:215],v[12:15]
s_lshr_b32 s76, %[swave], 1
EX v0,v0
MM v[8:11],a[204:207],v[212:215],v[132:135]
s_mul_i32 s76, s76, 10240
EX v1,v1
MM v[8:11],a[200:203],v[196:199],v[8:11]
s_and_b32 s77, %[swave], 1
EX v2,v2
MM v[8:11],a[196:199],v[180:183],v[8:11]
s_lshl_b32 s77, s77, 5
TR a[144:145],v232 offset:16896
CV v0,v0,v1
MM v[8:11],a[192:195],v[164:167],v[8:11]
s_add_u32 s76, s76, s77
EX v3,v3
MM v[16:19],a[192:195],v[168:171],v[136:139]
s_add_u32 s76, s76, %[slds]
EX v4,v4
MM v[16:19],a[196:199],v[184:187],v[16:19]
s_add_u32 s76, s76, 131072
v_add_u32 v124, s76, v124
v_bfe_u32 v126, v248, 4, 3
MM v[16:19],a[200:203],v[200:203],v[16:19]
v_lshl_add_u32 v126, v127, 3, v126
v_lshlrev_b32 v126, 4, v126
MM v[16:19],a[204:207],v[216:219],v[16:19]
s_mul_i32 s76, %[swave], 3072
EX v5,v5
MM v[20:23],a[220:223],v[216:219],v[136:139]
s_add_u32 s76, s76, %[slds]
TR a[146:147],v232 offset:20992
CV v1,v2,v3
MM v[20:23],a[216:219],v[200:203],v[20:23]
s_add_u32 s76, s76, 151552
TR a[148:149],v232 offset:25088
v_add_u32 v125, s76, v126
MM v[20:23],a[212:215],v[184:187],v[20:23]
s_lshr_b32 s76, s48, 7
EX v6,v6
MM v[20:23],a[208:211],v[168:171],v[20:23]
s_lshl_b32 s76, s76, 5
EX v7,v7
MM v[28:31],a[208:211],v[172:175],v[140:143]
s_and_b32 s77, s48, 16
TR a[150:151],v232 offset:29184
TR a[152:153],v233 offset:16896
MM v[28:31],a[212:215],v[188:191],v[28:31]
s_add_u32 s76, s76, s77
EX v8,v8
MM v[28:31],a[216:219],v[204:207],v[28:31]
s_bfe_u32 s77, s48, 131077
TR a[154:155],v233 offset:20992
CV v2,v4,v5
MM v[28:31],a[220:223],v[220:223],v[28:31]
s_lshl_b32 s77, s77, 2
EX v9,v9
MM v[24:27],a[204:207],v[220:223],v[140:143]
s_add_u32 s76, s76, s77
TR a[156:157],v233 offset:25088
CV v3,v6,v7
MM v[24:27],a[200:203],v[204:207],v[24:27]
s_add_u32 s76, s76, %[swave]
EX v10,v10
MM v[24:27],a[196:199],v[188:191],v[24:27]
s_lshl_b32 s97, s76, 2
TR a[158:159],v233 offset:29184
CV v8,v8,v9
MM v[24:27],a[192:195],v[172:175],v[24:27]
s_add_u32 s97, s97, 77824
EX v11,v11
MM v[32:35],a[224:227],v[160:163],v[128:131]
s_lshl_b32 s76, s76, 9
TR a[160:161],v232 offset:17408
TR a[162:163],v232 offset:21504
MM v[32:35],a[228:231],v[176:179],v[32:35]
s_add_u32 s76, s76, 22183936
EX v12,v12
MM v[32:35],a[232:235],v[192:195],v[32:35]
EX v13,v13
MM v[32:35],a[236:239],v[208:211],v[32:35]
TR a[164:165],v232 offset:25600
CV v9,v10,v11
MM v[36:39],a[252:255],v[208:211],v[128:131]
EX v14,v14
MM v[36:39],a[248:251],v[192:195],v[36:39]
TR a[166:167],v232 offset:29696
CV v10,v12,v13
MM v[36:39],a[244:247],v[176:179],v[36:39]
EX v15,v15
MM v[36:39],a[240:243],v[160:163],v[36:39]
TR a[168:169],v233 offset:17408
TR a[170:171],v233 offset:21504
MM v[44:47],a[240:243],v[164:167],v[132:135]
EX v16,v16
MM v[44:47],a[244:247],v[180:183],v[44:47]
EX v17,v17
MM v[44:47],a[248:251],v[196:199],v[44:47]
TR a[172:173],v233 offset:25600
CV v11,v14,v15
MM v[44:47],a[252:255],v[212:215],v[44:47]
EX v18,v18
MM v[40:43],a[236:239],v[212:215],v[132:135]
TR a[174:175],v233 offset:29696
CV v16,v16,v17
MM v[40:43],a[232:235],v[196:199],v[40:43]
EX v19,v19
MM v[40:43],a[228:231],v[180:183],v[40:43]
TR a[176:177],v232 offset:17920
TR a[178:179],v232 offset:22016
MM v[40:43],a[224:227],v[164:167],v[40:43]
EX v20,v20
MM v[48:51],a[224:227],v[168:171],v[136:139]
EX v21,v21
MM v[48:51],a[228:231],v[184:187],v[48:51]
TR a[180:181],v232 offset:26112
CV v17,v18,v19
MM v[48:51],a[232:235],v[200:203],v[48:51]
EX v22,v22
MM v[48:51],a[236:239],v[216:219],v[48:51]
TR a[182:183],v232 offset:30208
CV v18,v20,v21
MM v[52:55],a[252:255],v[216:219],v[136:139]
EX v23,v23
MM v[52:55],a[248:251],v[200:203],v[52:55]
TR a[184:185],v233 offset:17920
TR a[186:187],v233 offset:22016
MM v[52:55],a[244:247],v[184:187],v[52:55]
EX v24,v24
MM v[52:55],a[240:243],v[168:171],v[52:55]
TR a[188:189],v233 offset:26112
CV v19,v22,v23
MM v[60:63],a[240:243],v[172:175],v[140:143]
EX v25,v25
MM v[60:63],a[244:247],v[188:191],v[60:63]
TR a[190:191],v233 offset:30208
CV v24,v24,v25
MM v[60:63],a[248:251],v[204:207],v[60:63]
EX v26,v26
MM v[60:63],a[252:255],v[220:223],v[60:63]
EX v27,v27
MM v[56:59],a[236:239],v[220:223],v[140:143]
EX v28,v28
MM v[56:59],a[232:235],v[204:207],v[56:59]
EX v29,v29
MM v[56:59],a[228:231],v[188:191],v[56:59]
EX v30,v30
MM v[56:59],a[224:227],v[172:175],v[56:59]
s_waitcnt vmcnt(10) lgkmcnt(0)
s_barrier
MM a[0:3],a[128:131],v[64:67],a[0:3]
s_add_u32 m0, s65, 16384
CV v25,v26,v27
CV v26,v28,v29
MM a[0:3],a[132:135],v[68:71],a[0:3]
RD a[192:195],v224 offset:49152
RD a[196:199],v225 offset:49152
MM a[8:11],a[132:135],v[76:79],a[8:11]
EX v31,v31
MM a[8:11],a[128:131],v[72:75],a[8:11]
RD a[200:203],v226 offset:49152
CV v27,v30,v31
MM a[16:19],a[128:131],v[80:83],a[16:19]
TR v[96:97],v124 offset:0
TR v[98:99],v124 offset:256
MM a[16:19],a[132:135],v[84:87],a[16:19]
RD v[108:111],v125 offset:0
TR v[100:101],v124 offset:2048
MM a[24:27],a[132:135],v[92:95],a[24:27]
TR v[102:103],v124 offset:2304
RD v[112:115],v125 offset:512
MM a[24:27],a[128:131],v[88:91],a[24:27]
TR v[104:105],v124 offset:4096
TR v[106:107],v124 offset:4352
MM a[28:31],a[136:139],v[88:91],a[28:31]
RD v[116:119],v125 offset:1024
RD a[204:207],v227 offset:49152
EX v32,v32
MM a[28:31],a[140:143],v[92:95],a[28:31]
EX v33,v33
MM a[20:23],a[140:143],v[84:87],a[20:23]
EX v34,v34
MM a[20:23],a[136:139],v[80:83],a[20:23]
buffer_load_dwordx4 v228, s[8:11], s64 offen offset:0 lds
MM a[12:15],a[136:139],v[72:75],a[12:15]
EX v35,v35
MM a[12:15],a[140:143],v[76:79],a[12:15]
EX v36,v36
MM a[4:7],a[140:143],v[68:71],a[4:7]
CV v4,v32,v33
CV v5,v34,v35
MM a[4:7],a[136:139],v[64:67],a[4:7]
EX v37,v37
MM a[32:35],a[144:147],v[64:67],a[32:35]
EX v38,v38
MM a[32:35],a[148:151],v[68:71],a[32:35]
s_waitcnt lgkmcnt(1)
buffer_load_dwordx4 v229, s[8:11], s64 offen offset:1024 lds
MM a[40:43],a[148:151],v[76:79],a[40:43]
EX v39,v39
MM v[120:123],v[96:99],v[108:111],0
MM v[120:123],v[100:103],v[112:115],v[120:123]
MM v[120:123],v[104:107],v[116:119],v[120:123]
MM a[40:43],a[144:147],v[72:75],a[40:43]
TR v[96:97],v124 offset:6144
TR v[98:99],v124 offset:6400
MM a[48:51],a[144:147],v[80:83],a[48:51]
RD v[108:111],v125 offset:1536
TR v[100:101],v124 offset:8192
MM a[48:51],a[148:151],v[84:87],a[48:51]
TR v[102:103],v124 offset:8448
RD v[112:115],v125 offset:2048
MM a[56:59],a[148:151],v[92:95],a[56:59]
EX v40,v40
MM a[56:59],a[144:147],v[88:91],a[56:59]
EX v41,v41
MM a[60:63],a[152:155],v[88:91],a[60:63]
CV v6,v36,v37
CV v7,v38,v39
MM a[60:63],a[156:159],v[92:95],a[60:63]
EX v42,v42
MM a[52:55],a[156:159],v[84:87],a[52:55]
RD a[208:211],v224 offset:53248
RD a[212:215],v225 offset:53248
MM a[52:55],a[152:155],v[80:83],a[52:55]
EX v43,v43
MM a[44:47],a[152:155],v[72:75],a[44:47]
buffer_load_dwordx4 v230, s[8:11], s64 offen offset:2048 lds
MM a[44:47],a[156:159],v[76:79],a[44:47]
EX v44,v44
MM a[36:39],a[156:159],v[68:71],a[36:39]
s_waitcnt lgkmcnt(2)
EX v45,v45
MM a[36:39],a[152:155],v[64:67],a[36:39]
CV v12,v40,v41
)ASM"
R"ASM(
CV v13,v42,v43
MM v[120:123],v[96:99],v[108:111],v[120:123]
MM v[120:123],v[100:103],v[112:115],v[120:123]
MM a[64:67],a[160:163],v[64:67],a[64:67]
EX v46,v46
MM a[64:67],a[164:167],v[68:71],a[64:67]
RD a[216:219],v226 offset:53248
CV v14,v44,v45
MM a[72:75],a[164:167],v[76:79],a[72:75]
EX v47,v47
MM a[72:75],a[160:163],v[72:75],a[72:75]
buffer_load_dwordx4 v231, s[8:11], s64 offen offset:3072 lds
MM a[80:83],a[160:163],v[80:83],a[80:83]
s_add_u32 m0, s65, 81920
EX v48,v48
MM a[80:83],a[164:167],v[84:87],a[80:83]
RD a[220:223],v227 offset:53248
CV v15,v46,v47
MM a[88:91],a[164:167],v[92:95],a[88:91]
s_mov_b32 exec_lo, 0x00ff00ff
s_mov_b32 exec_hi, 0x00ff00ff
buffer_store_dwordx4 v[120:123], v126, s[20:23], s76 offen offset:0 sc1
s_mov_b64 exec, -1
s_nop 0
MM a[88:91],a[160:163],v[88:91],a[88:91]
EX v49,v49
MM a[92:95],a[168:171],v[88:91],a[92:95]
EX v50,v50
MM a[92:95],a[172:175],v[92:95],a[92:95]
EX v51,v51
MM a[84:87],a[172:175],v[84:87],a[84:87]
RD a[224:227],v224 offset:57344
CV v20,v48,v49
MM a[84:87],a[168:171],v[80:83],a[84:87]
EX v52,v52
MM a[76:79],a[168:171],v[72:75],a[76:79]
buffer_load_dwordx4 v235, s[12:15], s81 offen offset:0 lds
MM a[76:79],a[172:175],v[76:79],a[76:79]
EX v53,v53
MM a[68:71],a[172:175],v[68:71],a[68:71]
RD a[228:231],v225 offset:57344
CV v21,v50,v51
MM a[68:71],a[168:171],v[64:67],a[68:71]
EX v54,v54
MM a[96:99],a[176:179],v[64:67],a[96:99]
RD a[232:235],v226 offset:57344
CV v22,v52,v53
MM a[96:99],a[180:183],v[68:71],a[96:99]
EX v55,v55
MM a[104:107],a[180:183],v[76:79],a[104:107]
EX v56,v56
MM a[104:107],a[176:179],v[72:75],a[104:107]
buffer_load_dwordx4 v235, s[12:15], s64 offen offset:1024 lds
MM a[112:115],a[176:179],v[80:83],a[112:115]
EX v57,v57
MM a[112:115],a[180:183],v[84:87],a[112:115]
RD a[236:239],v227 offset:57344
CV v23,v54,v55
MM a[120:123],a[180:183],v[92:95],a[120:123]
EX v58,v58
MM a[120:123],a[176:179],v[88:91],a[120:123]
RD a[240:243],v224 offset:61440
CV v28,v56,v57
MM a[124:127],a[184:187],v[88:91],a[124:127]
v_add_f32 v244, v144, v145
v_add_f32 v245, v148, v149
MM a[124:127],a[188:191],v[92:95],a[124:127]
v_add_f32 v246, v152, v153
v_add_f32 v247, v156, v157
MM a[116:119],a[188:191],v[84:87],a[116:119]
EX v59,v59
MM a[116:119],a[184:187],v[80:83],a[116:119]
EX v60,v60
MM a[108:111],a[184:187],v[72:75],a[108:111]
EX v61,v61
MM a[108:111],a[188:191],v[76:79],a[108:111]
buffer_load_dwordx4 v235, s[12:15], s81 offen offset:2048 lds
MM a[100:103],a[188:191],v[68:71],a[100:103]
EX v62,v62
MM a[100:103],a[184:187],v[64:67],a[100:103]
RD a[244:247],v225 offset:61440
CV v29,v58,v59
v_smfmac_f32_16x16x64_bf16 v[144:147], v[236:239], v[0:7], v234
RD a[248:251],v226 offset:61440
RD a[252:255],v227 offset:61440
EX v63,v63
v_smfmac_f32_16x16x64_bf16 v[148:151], v[236:239], v[8:15], v234
CV v30,v60,v61
v_smfmac_f32_16x16x64_bf16 v[152:155], v[236:239], v[16:23], v234
CV v31,v62,v63
s_nop 1
v_smfmac_f32_16x16x64_bf16 v[156:159], v[236:239], v[24:31], v234
s_waitcnt lgkmcnt(0)
buffer_load_dwordx4 v235, s[12:15], s64 offen offset:3072 lds
MM v[64:67],a[192:195],v[160:163],v[128:131]
s_add_u32 s64, s64, 16384
TR a[128:129],v232 offset:32768
s_add_u32 s81, s64, 896
MM v[64:67],a[196:199],v[176:179],v[64:67]
TR a[130:131],v232 offset:36864
v_max3_f32 v249, v144, v145, v148
MM v[64:67],a[200:203],v[192:195],v[64:67]
TR a[132:133],v232 offset:40960
v_max3_f32 v249, v249, v149, v152
MM v[64:67],a[204:207],v[208:211],v[64:67]
TR a[134:135],v232 offset:45056
v_max3_f32 v249, v249, v153, v156
MM v[68:71],a[220:223],v[208:211],v[128:131]
TR a[136:137],v233 offset:32768
v_max_f32 v249, v249, v157
MM v[68:71],a[216:219],v[192:195],v[68:71]
TR a[138:139],v233 offset:36864
v_cmp_lt_f32 vcc, s71, v249
MM v[68:71],a[212:215],v[176:179],v[68:71]
TR a[140:141],v233 offset:40960
TR a[142:143],v233 offset:45056
MM v[68:71],a[208:211],v[160:163],v[68:71]
s_cbranch_vccnz LSITE18_%=
LBACK18_%=:
MM v[76:79],a[208:211],v[164:167],v[132:135]
EX v64,v64
MM v[76:79],a[212:215],v[180:183],v[76:79]
EX v65,v65
MM v[76:79],a[216:219],v[196:199],v[76:79]
TR a[144:145],v232 offset:33280
CV v64,v64,v65
MM v[76:79],a[220:223],v[212:215],v[76:79]
EX v66,v66
MM v[72:75],a[204:207],v[212:215],v[132:135]
EX v67,v67
MM v[72:75],a[200:203],v[196:199],v[72:75]
TR a[146:147],v232 offset:37376
CV v65,v66,v67
MM v[72:75],a[196:199],v[180:183],v[72:75]
EX v68,v68
MM v[72:75],a[192:195],v[164:167],v[72:75]
TR a[148:149],v232 offset:41472
TR a[150:151],v232 offset:45568
MM v[80:83],a[192:195],v[168:171],v[136:139]
EX v69,v69
MM v[80:83],a[196:199],v[184:187],v[80:83]
TR a[152:153],v233 offset:33280
CV v66,v68,v69
MM v[80:83],a[200:203],v[200:203],v[80:83]
EX v70,v70
MM v[80:83],a[204:207],v[216:219],v[80:83]
TR a[154:155],v233 offset:37376
TR a[156:157],v233 offset:41472
MM v[84:87],a[220:223],v[216:219],v[136:139]
EX v71,v71
MM v[84:87],a[216:219],v[200:203],v[84:87]
TR a[158:159],v233 offset:45568
CV v67,v70,v71
MM v[84:87],a[212:215],v[184:187],v[84:87]
EX v72,v72
MM v[84:87],a[208:211],v[168:171],v[84:87]
EX v73,v73
MM v[92:95],a[208:211],v[172:175],v[140:143]
TR a[160:161],v232 offset:33792
CV v72,v72,v73
MM v[92:95],a[212:215],v[188:191],v[92:95]
EX v74,v74
MM v[92:95],a[216:219],v[204:207],v[92:95]
TR a[162:163],v232 offset:37888
TR a[164:165],v232 offset:41984
MM v[92:95],a[220:223],v[220:223],v[92:95]
EX v75,v75
MM v[88:91],a[204:207],v[220:223],v[140:143]
TR a[166:167],v232 offset:46080
CV v73,v74,v75
MM v[88:91],a[200:203],v[204:207],v[88:91]
EX v76,v76
MM v[88:91],a[196:199],v[188:191],v[88:91]
EX v77,v77
MM v[88:91],a[192:195],v[172:175],v[88:91]
TR a[168:169],v233 offset:33792
CV v74,v76,v77
MM v[96:99],a[224:227],v[160:163],v[128:131]
EX v78,v78
MM v[96:99],a[228:231],v[176:179],v[96:99]
TR a[170:171],v233 offset:37888
TR a[172:173],v233 offset:41984
MM v[96:99],a[232:235],v[192:195],v[96:99]
EX v79,v79
MM v[96:99],a[236:239],v[208:211],v[96:99]
TR a[174:175],v233 offset:46080
CV v75,v78,v79
MM v[100:103],a[252:255],v[208:211],v[128:131]
EX v80,v80
MM v[100:103],a[248:251],v[192:195],v[100:103]
EX v81,v81
MM v[100:103],a[244:247],v[176:179],v[100:103]
TR a[176:177],v232 offset:34304
CV v80,v80,v81
MM v[100:103],a[240:243],v[160:163],v[100:103]
EX v82,v82
MM v[108:111],a[240:243],v[164:167],v[132:135]
v_lshrrev_b32 v253, 7, v248
v_mul_u32_u24 v253, 1280, v253
MM v[108:111],a[244:247],v[180:183],v[108:111]
v_and_b32 v252, 112, v248
v_add_u32 v253, v253, v252
MM v[108:111],a[248:251],v[196:199],v[108:111]
s_mul_i32 s90, %[swave], 61440
TR a[178:179],v232 offset:38400
TR a[180:181],v232 offset:42496
MM v[108:111],a[252:255],v[212:215],v[108:111]
s_mul_i32 s91, %[swave], 6144
EX v83,v83
MM v[104:107],a[236:239],v[212:215],v[132:135]
s_add_u32 s91, s91, %[slds]
TR a[182:183],v232 offset:46592
CV v81,v82,v83
MM v[104:107],a[232:235],v[196:199],v[104:107]
EX v84,v84
MM v[104:107],a[228:231],v[180:183],v[104:107]
EX v85,v85
MM v[104:107],a[224:227],v[164:167],v[104:107]
TR a[184:185],v233 offset:34304
CV v82,v84,v85
MM v[112:115],a[224:227],v[168:171],v[136:139]
EX v86,v86
MM v[112:115],a[228:231],v[184:187],v[112:115]
TR a[186:187],v233 offset:38400
TR a[188:189],v233 offset:42496
MM v[112:115],a[232:235],v[200:203],v[112:115]
EX v87,v87
MM v[112:115],a[236:239],v[216:219],v[112:115]
TR a[190:191],v233 offset:46592
CV v83,v86,v87
MM v[116:119],a[252:255],v[216:219],v[136:139]
EX v88,v88
MM v[116:119],a[248:251],v[200:203],v[116:119]
EX v89,v89
MM v[116:119],a[244:247],v[184:187],v[116:119]
EX v90,v90
MM v[116:119],a[240:243],v[168:171],v[116:119]
EX v91,v91
MM v[124:127],a[240:243],v[172:175],v[140:143]
EX v92,v92
MM v[124:127],a[244:247],v[188:191],v[124:127]
EX v93,v93
MM v[124:127],a[248:251],v[204:207],v[124:127]
EX v94,v94
MM v[124:127],a[252:255],v[220:223],v[124:127]
EX v95,v95
MM v[120:123],a[236:239],v[220:223],v[140:143]
EX v96,v96
MM v[120:123],a[232:235],v[204:207],v[120:123]
EX v97,v97
MM v[120:123],a[228:231],v[188:191],v[120:123]
EX v98,v98
MM v[120:123],a[224:227],v[172:175],v[120:123]
s_waitcnt vmcnt(14) lgkmcnt(0)
s_barrier
s_nop 0
LPG15_%=:
s_cbranch_execz LTR2_%=
MM a[0:3],a[128:131],v[0:3],a[0:3]
s_add_u32 m0, s65, 32768
CV v88,v88,v89
CV v89,v90,v91
MM a[0:3],a[132:135],v[4:7],a[0:3]
RD a[192:195],v224 offset:0
CV v90,v92,v93
MM a[8:11],a[132:135],v[12:15],a[8:11]
RD a[196:199],v225 offset:0
CV v91,v94,v95
MM a[8:11],a[128:131],v[8:11],a[8:11]
buffer_load_dwordx4 v228, s[8:11], s64 offen offset:0 lds
MM a[16:19],a[128:131],v[16:19],a[16:19]
RD a[200:203],v226 offset:0
CV v68,v96,v97
MM a[16:19],a[132:135],v[20:23],a[16:19]
buffer_load_dwordx4 v229, s[8:11], s64 offen offset:1024 lds
MM a[24:27],a[132:135],v[28:31],a[24:27]
RD a[204:207],v227 offset:0
RD a[208:211],v224 offset:4096
MM a[24:27],a[128:131],v[24:27],a[24:27]
EX v99,v99
MM a[28:31],a[136:139],v[24:27],a[28:31]
RD a[212:215],v225 offset:4096
CV v69,v98,v99
MM a[28:31],a[140:143],v[28:31],a[28:31]
EX v100,v100
MM a[20:23],a[140:143],v[20:23],a[20:23]
buffer_load_dwordx4 v230, s[8:11], s64 offen offset:2048 lds
MM a[20:23],a[136:139],v[16:19],a[20:23]
EX v101,v101
MM a[12:15],a[136:139],v[8:11],a[12:15]
RD a[216:219],v226 offset:4096
CV v70,v100,v101
MM a[12:15],a[140:143],v[12:15],a[12:15]
EX v102,v102
MM a[4:7],a[140:143],v[4:7],a[4:7]
EX v103,v103
MM a[4:7],a[136:139],v[0:3],a[4:7]
RD a[220:223],v227 offset:4096
CV v71,v102,v103
MM a[32:35],a[144:147],v[0:3],a[32:35]
EX v104,v104
MM a[32:35],a[148:151],v[4:7],a[32:35]
buffer_load_dwordx4 v231, s[8:11], s64 offen offset:3072 lds
MM a[40:43],a[148:151],v[12:15],a[40:43]
s_add_u32 m0, s65, 98304
EX v105,v105
MM a[40:43],a[144:147],v[8:11],a[40:43]
RD a[224:227],v224 offset:8192
CV v76,v104,v105
MM a[48:51],a[144:147],v[16:19],a[48:51]
EX v106,v106
MM a[48:51],a[148:151],v[20:23],a[48:51]
EX v107,v107
MM a[56:59],a[148:151],v[28:31],a[56:59]
RD a[228:231],v225 offset:8192
CV v77,v106,v107
MM a[56:59],a[144:147],v[24:27],a[56:59]
EX v108,v108
MM a[60:63],a[152:155],v[24:27],a[60:63]
buffer_load_dwordx4 v235, s[12:15], s81 offen offset:0 lds
MM a[60:63],a[156:159],v[28:31],a[60:63]
EX v109,v109
MM a[52:55],a[156:159],v[20:23],a[52:55]
RD a[232:235],v226 offset:8192
CV v78,v108,v109
MM a[52:55],a[152:155],v[16:19],a[52:55]
EX v110,v110
MM a[44:47],a[152:155],v[8:11],a[44:47]
EX v111,v111
MM a[44:47],a[156:159],v[12:15],a[44:47]
RD a[236:239],v227 offset:8192
CV v79,v110,v111
MM a[36:39],a[156:159],v[4:7],a[36:39]
buffer_load_dwordx4 v235, s[12:15], s64 offen offset:1024 lds
MM a[36:39],a[152:155],v[0:3],a[36:39]
EX v112,v112
MM a[64:67],a[160:163],v[0:3],a[64:67]
EX v113,v113
MM a[64:67],a[164:167],v[4:7],a[64:67]
RD a[240:243],v224 offset:12288
CV v84,v112,v113
MM a[72:75],a[164:167],v[12:15],a[72:75]
EX v114,v114
MM a[72:75],a[160:163],v[8:11],a[72:75]
RD a[244:247],v225 offset:12288
RD a[248:251],v226 offset:12288
MM a[80:83],a[160:163],v[16:19],a[80:83]
s_waitcnt vmcnt(10)
v_mov_b32 v250, s69
s_mov_b64 exec, 1
buffer_store_dword v250, off, s[20:23], s97 offset:0 sc1
s_mov_b64 exec, -1
MM a[80:83],a[164:167],v[20:23],a[80:83]
s_add_u32 m0, s91, 139264
s_add_u32 s77, s90, 0
buffer_load_dwordx4 v253, s[28:31], s77 offen offset:0 lds
MM a[88:91],a[164:167],v[28:31],a[88:91]
EX v115,v115
MM a[88:91],a[160:163],v[24:27],a[88:91]
RD a[252:255],v227 offset:12288
CV v85,v114,v115
MM a[92:95],a[168:171],v[24:27],a[92:95]
EX v116,v116
MM a[92:95],a[172:175],v[28:31],a[92:95]
EX v117,v117
MM a[84:87],a[172:175],v[20:23],a[84:87]
s_add_u32 m0, s65, 98304
s_nop 0
buffer_load_dwordx4 v235, s[12:15], s81 offen offset:2048 lds
MM a[84:87],a[168:171],v[16:19],a[84:87]
EX v118,v118
MM a[76:79],a[168:171],v[8:11],a[76:79]
EX v119,v119
MM a[76:79],a[172:175],v[12:15],a[76:79]
EX v120,v120
MM a[68:71],a[172:175],v[4:7],a[68:71]
buffer_load_dwordx4 v235, s[12:15], s64 offen offset:3072 lds
MM a[68:71],a[168:171],v[0:3],a[68:71]
EX v121,v121
MM a[96:99],a[176:179],v[0:3],a[96:99]
EX v122,v122
MM a[96:99],a[180:183],v[4:7],a[96:99]
EX v123,v123
MM a[104:107],a[180:183],v[12:15],a[104:107]
EX v124,v124
MM a[104:107],a[176:179],v[8:11],a[104:107]
EX v125,v125
MM a[112:115],a[176:179],v[16:19],a[112:115]
)ASM"
R"ASM(
EX v126,v126
MM a[112:115],a[180:183],v[20:23],a[112:115]
EX v127,v127
MM a[120:123],a[180:183],v[28:31],a[120:123]
CV v86,v116,v117
CV v87,v118,v119
MM a[120:123],a[176:179],v[24:27],a[120:123]
CV v92,v120,v121
CV v93,v122,v123
MM a[124:127],a[184:187],v[24:27],a[124:127]
v_add_f32 v244, v144, v145
v_add_f32 v245, v148, v149
MM a[124:127],a[188:191],v[28:31],a[124:127]
v_add_f32 v246, v152, v153
v_add_f32 v247, v156, v157
MM a[116:119],a[188:191],v[20:23],a[116:119]
CV v94,v124,v125
CV v95,v126,v127
MM a[116:119],a[184:187],v[16:19],a[116:119]
MM a[108:111],a[184:187],v[8:11],a[108:111]
MM a[108:111],a[188:191],v[12:15],a[108:111]
MM a[100:103],a[188:191],v[4:7],a[100:103]
MM a[100:103],a[184:187],v[0:3],a[100:103]
v_smfmac_f32_16x16x64_bf16 v[144:147], v[236:239], v[64:71], v234
v_smfmac_f32_16x16x64_bf16 v[148:151], v[236:239], v[72:79], v234
v_smfmac_f32_16x16x64_bf16 v[152:155], v[236:239], v[80:87], v234
v_smfmac_f32_16x16x64_bf16 v[156:159], v[236:239], v[88:95], v234
s_waitcnt lgkmcnt(0)
MM v[0:3],a[192:195],v[160:163],v[128:131]
s_add_u32 s64, s64, 16384
TR a[128:129],v232 offset:49152
s_add_u32 s81, s64, 896
MM v[0:3],a[196:199],v[176:179],v[0:3]
TR a[130:131],v232 offset:53248
v_max3_f32 v249, v144, v145, v148
MM v[0:3],a[200:203],v[192:195],v[0:3]
TR a[132:133],v232 offset:57344
v_max3_f32 v249, v249, v149, v152
MM v[0:3],a[204:207],v[208:211],v[0:3]
TR a[134:135],v232 offset:61440
v_max3_f32 v249, v249, v153, v156
MM v[4:7],a[220:223],v[208:211],v[128:131]
TR a[136:137],v233 offset:49152
v_max_f32 v249, v249, v157
MM v[4:7],a[216:219],v[192:195],v[4:7]
TR a[138:139],v233 offset:53248
v_cmp_lt_f32 vcc, s71, v249
MM v[4:7],a[212:215],v[176:179],v[4:7]
TR a[140:141],v233 offset:57344
TR a[142:143],v233 offset:61440
MM v[4:7],a[208:211],v[160:163],v[4:7]
s_cbranch_vccnz LSITE19_%=
LBACK19_%=:
MM v[12:15],a[208:211],v[164:167],v[132:135]
s_add_u32 m0, s91, 140288
s_add_u32 s77, s90, 10240
buffer_load_dwordx4 v253, s[28:31], s77 offen offset:0 lds
MM v[12:15],a[212:215],v[180:183],v[12:15]
EX v0,v0
MM v[12:15],a[216:219],v[196:199],v[12:15]
EX v1,v1
MM v[12:15],a[220:223],v[212:215],v[12:15]
TR a[144:145],v232 offset:49664
CV v0,v0,v1
MM v[8:11],a[204:207],v[212:215],v[132:135]
EX v2,v2
MM v[8:11],a[200:203],v[196:199],v[8:11]
EX v3,v3
MM v[8:11],a[196:199],v[180:183],v[8:11]
TR a[146:147],v232 offset:53760
CV v1,v2,v3
MM v[8:11],a[192:195],v[164:167],v[8:11]
EX v4,v4
MM v[16:19],a[192:195],v[168:171],v[136:139]
TR a[148:149],v232 offset:57856
TR a[150:151],v232 offset:61952
MM v[16:19],a[196:199],v[184:187],v[16:19]
EX v5,v5
MM v[16:19],a[200:203],v[200:203],v[16:19]
TR a[152:153],v233 offset:49664
CV v2,v4,v5
MM v[16:19],a[204:207],v[216:219],v[16:19]
EX v6,v6
MM v[20:23],a[220:223],v[216:219],v[136:139]
EX v7,v7
MM v[20:23],a[216:219],v[200:203],v[20:23]
TR a[154:155],v233 offset:53760
CV v3,v6,v7
MM v[20:23],a[212:215],v[184:187],v[20:23]
EX v8,v8
MM v[20:23],a[208:211],v[168:171],v[20:23]
TR a[156:157],v233 offset:57856
TR a[158:159],v233 offset:61952
MM v[28:31],a[208:211],v[172:175],v[140:143]
EX v9,v9
MM v[28:31],a[212:215],v[188:191],v[28:31]
TR a[160:161],v232 offset:50176
CV v8,v8,v9
MM v[28:31],a[216:219],v[204:207],v[28:31]
EX v10,v10
MM v[28:31],a[220:223],v[220:223],v[28:31]
EX v11,v11
MM v[24:27],a[204:207],v[220:223],v[140:143]
TR a[162:163],v232 offset:54272
CV v9,v10,v11
MM v[24:27],a[200:203],v[204:207],v[24:27]
TR a[164:165],v232 offset:58368
TR a[166:167],v232 offset:62464
MM v[24:27],a[196:199],v[188:191],v[24:27]
EX v12,v12
MM v[24:27],a[192:195],v[172:175],v[24:27]
EX v13,v13
MM v[32:35],a[224:227],v[160:163],v[128:131]
TR a[168:169],v233 offset:50176
CV v10,v12,v13
MM v[32:35],a[228:231],v[176:179],v[32:35]
EX v14,v14
MM v[32:35],a[232:235],v[192:195],v[32:35]
TR a[170:171],v233 offset:54272
TR a[172:173],v233 offset:58368
MM v[32:35],a[236:239],v[208:211],v[32:35]
EX v15,v15
MM v[36:39],a[252:255],v[208:211],v[128:131]
TR a[174:175],v233 offset:62464
CV v11,v14,v15
MM v[36:39],a[248:251],v[192:195],v[36:39]
EX v16,v16
MM v[36:39],a[244:247],v[176:179],v[36:39]
EX v17,v17
MM v[36:39],a[240:243],v[160:163],v[36:39]
TR a[176:177],v232 offset:50688
CV v16,v16,v17
MM v[44:47],a[240:243],v[164:167],v[132:135]
s_add_u32 m0, s91, 141312
s_add_u32 s77, s90, 20480
buffer_load_dwordx4 v253, s[28:31], s77 offen offset:0 lds
MM v[44:47],a[244:247],v[180:183],v[44:47]
EX v18,v18
MM v[44:47],a[248:251],v[196:199],v[44:47]
TR a[178:179],v232 offset:54784
TR a[180:181],v232 offset:58880
MM v[44:47],a[252:255],v[212:215],v[44:47]
EX v19,v19
MM v[40:43],a[236:239],v[212:215],v[132:135]
TR a[182:183],v232 offset:62976
CV v17,v18,v19
MM v[40:43],a[232:235],v[196:199],v[40:43]
EX v20,v20
MM v[40:43],a[228:231],v[180:183],v[40:43]
EX v21,v21
MM v[40:43],a[224:227],v[164:167],v[40:43]
TR a[184:185],v233 offset:50688
CV v18,v20,v21
MM v[48:51],a[224:227],v[168:171],v[136:139]
EX v22,v22
MM v[48:51],a[228:231],v[184:187],v[48:51]
TR a[186:187],v233 offset:54784
TR a[188:189],v233 offset:58880
MM v[48:51],a[232:235],v[200:203],v[48:51]
EX v23,v23
MM v[48:51],a[236:239],v[216:219],v[48:51]
TR a[190:191],v233 offset:62976
CV v19,v22,v23
MM v[52:55],a[252:255],v[216:219],v[136:139]
EX v24,v24
MM v[52:55],a[248:251],v[200:203],v[52:55]
EX v25,v25
MM v[52:55],a[244:247],v[184:187],v[52:55]
EX v26,v26
MM v[52:55],a[240:243],v[168:171],v[52:55]
EX v27,v27
MM v[60:63],a[240:243],v[172:175],v[140:143]
EX v28,v28
MM v[60:63],a[244:247],v[188:191],v[60:63]
EX v29,v29
MM v[60:63],a[248:251],v[204:207],v[60:63]
EX v30,v30
MM v[60:63],a[252:255],v[220:223],v[60:63]
EX v31,v31
MM v[56:59],a[236:239],v[220:223],v[140:143]
EX v32,v32
MM v[56:59],a[232:235],v[204:207],v[56:59]
EX v33,v33
MM v[56:59],a[228:231],v[188:191],v[56:59]
EX v34,v34
MM v[56:59],a[224:227],v[172:175],v[56:59]
s_waitcnt lgkmcnt(0)
s_barrier
MM a[0:3],a[128:131],v[64:67],a[0:3]
s_add_u32 m0, s65, 49152
CV v24,v24,v25
CV v25,v26,v27
MM a[0:3],a[132:135],v[68:71],a[0:3]
RD a[192:195],v224 offset:16384
CV v26,v28,v29
MM a[8:11],a[132:135],v[76:79],a[8:11]
RD a[196:199],v225 offset:16384
CV v27,v30,v31
MM a[8:11],a[128:131],v[72:75],a[8:11]
buffer_load_dwordx4 v228, s[8:11], s64 offen offset:0 lds
MM a[16:19],a[128:131],v[80:83],a[16:19]
s_add_u32 m0, s91, 142336
s_add_u32 s77, s90, 30720
buffer_load_dwordx4 v253, s[28:31], s77 offen offset:0 lds
MM a[16:19],a[132:135],v[84:87],a[16:19]
RD a[200:203],v226 offset:16384
CV v4,v32,v33
MM a[24:27],a[132:135],v[92:95],a[24:27]
RD a[204:207],v227 offset:16384
RD a[208:211],v224 offset:20480
MM a[24:27],a[128:131],v[88:91],a[24:27]
s_add_u32 m0, s65, 49152
s_nop 0
buffer_load_dwordx4 v229, s[8:11], s64 offen offset:1024 lds
MM a[28:31],a[136:139],v[88:91],a[28:31]
EX v35,v35
MM a[28:31],a[140:143],v[92:95],a[28:31]
RD a[212:215],v225 offset:20480
CV v5,v34,v35
MM a[20:23],a[140:143],v[84:87],a[20:23]
EX v36,v36
MM a[20:23],a[136:139],v[80:83],a[20:23]
EX v37,v37
MM a[12:15],a[136:139],v[72:75],a[12:15]
RD a[216:219],v226 offset:20480
CV v6,v36,v37
MM a[12:15],a[140:143],v[76:79],a[12:15]
buffer_load_dwordx4 v230, s[8:11], s64 offen offset:2048 lds
MM a[4:7],a[140:143],v[68:71],a[4:7]
EX v38,v38
MM a[4:7],a[136:139],v[64:67],a[4:7]
EX v39,v39
MM a[32:35],a[144:147],v[64:67],a[32:35]
RD a[220:223],v227 offset:20480
CV v7,v38,v39
MM a[32:35],a[148:151],v[68:71],a[32:35]
EX v40,v40
MM a[40:43],a[148:151],v[76:79],a[40:43]
EX v41,v41
MM a[40:43],a[144:147],v[72:75],a[40:43]
RD a[224:227],v224 offset:24576
CV v12,v40,v41
MM a[48:51],a[144:147],v[80:83],a[48:51]
buffer_load_dwordx4 v231, s[8:11], s64 offen offset:3072 lds
MM a[48:51],a[148:151],v[84:87],a[48:51]
s_add_u32 m0, s65, 114688
EX v42,v42
MM a[56:59],a[148:151],v[92:95],a[56:59]
EX v43,v43
MM a[56:59],a[144:147],v[88:91],a[56:59]
RD a[228:231],v225 offset:24576
CV v13,v42,v43
MM a[60:63],a[152:155],v[88:91],a[60:63]
EX v44,v44
MM a[60:63],a[156:159],v[92:95],a[60:63]
buffer_load_dwordx4 v235, s[12:15], s81 offen offset:0 lds
MM a[52:55],a[156:159],v[84:87],a[52:55]
EX v45,v45
MM a[52:55],a[152:155],v[80:83],a[52:55]
RD a[232:235],v226 offset:24576
CV v14,v44,v45
MM a[44:47],a[152:155],v[72:75],a[44:47]
EX v46,v46
MM a[44:47],a[156:159],v[76:79],a[44:47]
EX v47,v47
MM a[36:39],a[156:159],v[68:71],a[36:39]
RD a[236:239],v227 offset:24576
CV v15,v46,v47
MM a[36:39],a[152:155],v[64:67],a[36:39]
EX v48,v48
MM a[64:67],a[160:163],v[64:67],a[64:67]
buffer_load_dwordx4 v235, s[12:15], s64 offen offset:1024 lds
MM a[64:67],a[164:167],v[68:71],a[64:67]
EX v49,v49
MM a[72:75],a[164:167],v[76:79],a[72:75]
RD a[240:243],v224 offset:28672
CV v20,v48,v49
MM a[72:75],a[160:163],v[72:75],a[72:75]
EX v50,v50
MM a[80:83],a[160:163],v[80:83],a[80:83]
s_add_u32 m0, s91, 143360
s_add_u32 s77, s90, 40960
buffer_load_dwordx4 v253, s[28:31], s77 offen offset:0 lds
MM a[80:83],a[164:167],v[84:87],a[80:83]
EX v51,v51
MM a[88:91],a[164:167],v[92:95],a[88:91]
RD a[244:247],v225 offset:28672
CV v21,v50,v51
MM a[88:91],a[160:163],v[88:91],a[88:91]
EX v52,v52
MM a[92:95],a[168:171],v[88:91],a[92:95]
EX v53,v53
MM a[92:95],a[172:175],v[92:95],a[92:95]
RD a[248:251],v226 offset:28672
CV v22,v52,v53
MM a[84:87],a[172:175],v[84:87],a[84:87]
s_add_u32 m0, s65, 114688
s_nop 0
buffer_load_dwordx4 v235, s[12:15], s81 offen offset:2048 lds
MM a[84:87],a[168:171],v[80:83],a[84:87]
EX v54,v54
MM a[76:79],a[168:171],v[72:75],a[76:79]
EX v55,v55
MM a[76:79],a[172:175],v[76:79],a[76:79]
RD a[252:255],v227 offset:28672
CV v23,v54,v55
MM a[68:71],a[172:175],v[68:71],a[68:71]
EX v56,v56
MM a[68:71],a[168:171],v[64:67],a[68:71]
EX v57,v57
MM a[96:99],a[176:179],v[64:67],a[96:99]
buffer_load_dwordx4 v235, s[12:15], s64 offen offset:3072 lds
MM a[96:99],a[180:183],v[68:71],a[96:99]
EX v58,v58
MM a[104:107],a[180:183],v[76:79],a[104:107]
EX v59,v59
MM a[104:107],a[176:179],v[72:75],a[104:107]
EX v60,v60
MM a[112:115],a[176:179],v[80:83],a[112:115]
EX v61,v61
MM a[112:115],a[180:183],v[84:87],a[112:115]
EX v62,v62
MM a[120:123],a[180:183],v[92:95],a[120:123]
s_add_u32 m0, s91, 144384
s_add_u32 s77, s90, 51200
buffer_load_dwordx4 v253, s[28:31], s77 offen offset:0 lds
MM a[120:123],a[176:179],v[88:91],a[120:123]
EX v63,v63
MM a[124:127],a[184:187],v[88:91],a[124:127]
v_add_f32 v244, v144, v145
v_add_f32 v245, v148, v149
MM a[124:127],a[188:191],v[92:95],a[124:127]
v_add_f32 v246, v152, v153
v_add_f32 v247, v156, v157
MM a[116:119],a[188:191],v[84:87],a[116:119]
CV v28,v56,v57
CV v29,v58,v59
MM a[116:119],a[184:187],v[80:83],a[116:119]
CV v30,v60,v61
CV v31,v62,v63
MM a[108:111],a[184:187],v[72:75],a[108:111]
MM a[108:111],a[188:191],v[76:79],a[108:111]
MM a[100:103],a[188:191],v[68:71],a[100:103]
MM a[100:103],a[184:187],v[64:67],a[100:103]
v_smfmac_f32_16x16x64_bf16 v[144:147], v[236:239], v[0:7], v234
v_smfmac_f32_16x16x64_bf16 v[148:151], v[236:239], v[8:15], v234
v_smfmac_f32_16x16x64_bf16 v[152:155], v[236:239], v[16:23], v234
v_smfmac_f32_16x16x64_bf16 v[156:159], v[236:239], v[24:31], v234
s_waitcnt lgkmcnt(0)
MM v[64:67],a[192:195],v[160:163],v[128:131]
s_add_u32 s64, s64, 16384
TR a[128:129],v232 offset:0
s_add_u32 s81, s64, 896
MM v[64:67],a[196:199],v[176:179],v[64:67]
TR a[130:131],v232 offset:4096
v_max3_f32 v249, v144, v145, v148
MM v[64:67],a[200:203],v[192:195],v[64:67]
TR a[132:133],v232 offset:8192
v_max3_f32 v249, v249, v149, v152
MM v[64:67],a[204:207],v[208:211],v[64:67]
TR a[134:135],v232 offset:12288
v_max3_f32 v249, v249, v153, v156
MM v[68:71],a[220:223],v[208:211],v[128:131]
TR a[136:137],v233 offset:0
v_max_f32 v249, v249, v157
MM v[68:71],a[216:219],v[192:195],v[68:71]
TR a[138:139],v233 offset:4096
v_cmp_lt_f32 vcc, s71, v249
MM v[68:71],a[212:215],v[176:179],v[68:71]
TR a[140:141],v233 offset:8192
TR a[142:143],v233 offset:12288
MM v[68:71],a[208:211],v[160:163],v[68:71]
s_cbranch_vccnz LSITE20_%=
LBACK20_%=:
MM v[76:79],a[208:211],v[164:167],v[132:135]
v_bfe_u32 v253, v248, 4, 4
v_lshlrev_b32 v253, 2, v253
MM v[76:79],a[212:215],v[180:183],v[76:79]
s_and_b32 s76, s18, 0xffffffc0
EX v64,v64
MM v[76:79],a[216:219],v[196:199],v[76:79]
s_add_u32 s76, s76, 77824
)ASM"
R"ASM(
buffer_load_dword v252, v253, s[20:23], s76 offen offset:0 sc1
MM v[76:79],a[220:223],v[212:215],v[76:79]
EX v65,v65
MM v[72:75],a[204:207],v[212:215],v[132:135]
EX v66,v66
MM v[72:75],a[200:203],v[196:199],v[72:75]
TR a[144:145],v232 offset:512
CV v64,v64,v65
MM v[72:75],a[196:199],v[180:183],v[72:75]
EX v67,v67
MM v[72:75],a[192:195],v[164:167],v[72:75]
TR a[146:147],v232 offset:4608
CV v65,v66,v67
MM v[80:83],a[192:195],v[168:171],v[136:139]
EX v68,v68
MM v[80:83],a[196:199],v[184:187],v[80:83]
TR a[148:149],v232 offset:8704
TR a[150:151],v232 offset:12800
MM v[80:83],a[200:203],v[200:203],v[80:83]
EX v69,v69
MM v[80:83],a[204:207],v[216:219],v[80:83]
TR a[152:153],v233 offset:512
CV v66,v68,v69
MM v[84:87],a[220:223],v[216:219],v[136:139]
EX v70,v70
MM v[84:87],a[216:219],v[200:203],v[84:87]
EX v71,v71
MM v[84:87],a[212:215],v[184:187],v[84:87]
TR a[154:155],v233 offset:4608
CV v67,v70,v71
MM v[84:87],a[208:211],v[168:171],v[84:87]
EX v72,v72
MM v[92:95],a[208:211],v[172:175],v[140:143]
TR a[156:157],v233 offset:8704
TR a[158:159],v233 offset:12800
MM v[92:95],a[212:215],v[188:191],v[92:95]
EX v73,v73
MM v[92:95],a[216:219],v[204:207],v[92:95]
TR a[160:161],v232 offset:1024
CV v72,v72,v73
MM v[92:95],a[220:223],v[220:223],v[92:95]
EX v74,v74
MM v[88:91],a[204:207],v[220:223],v[140:143]
EX v75,v75
MM v[88:91],a[200:203],v[204:207],v[88:91]
TR a[162:163],v232 offset:5120
CV v73,v74,v75
MM v[88:91],a[196:199],v[188:191],v[88:91]
EX v76,v76
MM v[88:91],a[192:195],v[172:175],v[88:91]
TR a[164:165],v232 offset:9216
TR a[166:167],v232 offset:13312
MM v[96:99],a[224:227],v[160:163],v[128:131]
EX v77,v77
MM v[96:99],a[228:231],v[176:179],v[96:99]
TR a[168:169],v233 offset:1024
CV v74,v76,v77
MM v[96:99],a[232:235],v[192:195],v[96:99]
EX v78,v78
MM v[96:99],a[236:239],v[208:211],v[96:99]
EX v79,v79
MM v[100:103],a[252:255],v[208:211],v[128:131]
TR a[170:171],v233 offset:5120
CV v75,v78,v79
MM v[100:103],a[248:251],v[192:195],v[100:103]
EX v80,v80
MM v[100:103],a[244:247],v[176:179],v[100:103]
TR a[172:173],v233 offset:9216
TR a[174:175],v233 offset:13312
MM v[100:103],a[240:243],v[160:163],v[100:103]
LPG16_%=:
s_cbranch_execz LTR2_%=
EX v81,v81
MM v[108:111],a[240:243],v[164:167],v[132:135]
TR a[176:177],v232 offset:1536
CV v80,v80,v81
MM v[108:111],a[244:247],v[180:183],v[108:111]
EX v82,v82
MM v[108:111],a[248:251],v[196:199],v[108:111]
TR a[178:179],v232 offset:5632
TR a[180:181],v232 offset:9728
MM v[108:111],a[252:255],v[212:215],v[108:111]
EX v83,v83
MM v[104:107],a[236:239],v[212:215],v[132:135]
TR a[182:183],v232 offset:13824
CV v81,v82,v83
MM v[104:107],a[232:235],v[196:199],v[104:107]
EX v84,v84
MM v[104:107],a[228:231],v[180:183],v[104:107]
EX v85,v85
MM v[104:107],a[224:227],v[164:167],v[104:107]
TR a[184:185],v233 offset:1536
CV v82,v84,v85
MM v[112:115],a[224:227],v[168:171],v[136:139]
EX v86,v86
MM v[112:115],a[228:231],v[184:187],v[112:115]
TR a[186:187],v233 offset:5632
TR a[188:189],v233 offset:9728
MM v[112:115],a[232:235],v[200:203],v[112:115]
EX v87,v87
MM v[112:115],a[236:239],v[216:219],v[112:115]
TR a[190:191],v233 offset:13824
CV v83,v86,v87
MM v[116:119],a[252:255],v[216:219],v[136:139]
EX v88,v88
MM v[116:119],a[248:251],v[200:203],v[116:119]
EX v89,v89
MM v[116:119],a[244:247],v[184:187],v[116:119]
EX v90,v90
MM v[116:119],a[240:243],v[168:171],v[116:119]
EX v91,v91
MM v[124:127],a[240:243],v[172:175],v[140:143]
EX v92,v92
MM v[124:127],a[244:247],v[188:191],v[124:127]
EX v93,v93
MM v[124:127],a[248:251],v[204:207],v[124:127]
EX v94,v94
MM v[124:127],a[252:255],v[220:223],v[124:127]
EX v95,v95
MM v[120:123],a[236:239],v[220:223],v[140:143]
EX v96,v96
MM v[120:123],a[232:235],v[204:207],v[120:123]
EX v97,v97
MM v[120:123],a[228:231],v[188:191],v[120:123]
s_getpc_b64 s[98:99]
LTA3_%=:
s_min_u32 s73, %[swave], 3
s_lshl_b32 s73, s73, 12
s_add_u32 s73, s73, LPG17_%=-LTA3_%=
s_add_u32 s98, s98, s73
s_addc_u32 s99, s99, 0
s_mov_b64 exec, 0
s_setpc_b64 s[98:99]
LTR3_%=:
s_mov_b64 exec, -1
CV v88,v88,v89
MM v[120:123],a[224:227],v[172:175],v[120:123]
s_waitcnt vmcnt(20) lgkmcnt(0)
s_barrier
MM a[0:3],a[128:131],v[0:3],a[0:3]
s_add_u32 m0, s65, 0
CV v89,v90,v91
CV v90,v92,v93
MM a[0:3],a[132:135],v[4:7],a[0:3]
RD a[192:195],v224 offset:32768
CV v91,v94,v95
MM a[8:11],a[132:135],v[12:15],a[8:11]
buffer_load_dwordx4 v228, s[8:11], s64 offen offset:0 lds
MM a[8:11],a[128:131],v[8:11],a[8:11]
RD a[196:199],v225 offset:32768
CV v68,v96,v97
MM a[16:19],a[128:131],v[16:19],a[16:19]
RD a[200:203],v226 offset:32768
RD a[204:207],v227 offset:32768
MM a[16:19],a[132:135],v[20:23],a[16:19]
buffer_load_dwordx4 v229, s[8:11], s64 offen offset:1024 lds
MM a[24:27],a[132:135],v[28:31],a[24:27]
EX v98,v98
MM a[24:27],a[128:131],v[24:27],a[24:27]
RD a[208:211],v224 offset:36864
RD a[212:215],v225 offset:36864
MM a[28:31],a[136:139],v[24:27],a[28:31]
EX v99,v99
MM a[28:31],a[140:143],v[28:31],a[28:31]
RD a[216:219],v226 offset:36864
CV v69,v98,v99
MM a[20:23],a[140:143],v[20:23],a[20:23]
EX v100,v100
MM a[20:23],a[136:139],v[16:19],a[20:23]
EX v101,v101
MM a[12:15],a[136:139],v[8:11],a[12:15]
RD a[220:223],v227 offset:36864
CV v70,v100,v101
MM a[12:15],a[140:143],v[12:15],a[12:15]
buffer_load_dwordx4 v230, s[8:11], s64 offen offset:2048 lds
MM a[4:7],a[140:143],v[4:7],a[4:7]
EX v102,v102
MM a[4:7],a[136:139],v[0:3],a[4:7]
EX v103,v103
MM a[32:35],a[144:147],v[0:3],a[32:35]
RD a[224:227],v224 offset:40960
CV v71,v102,v103
MM a[32:35],a[148:151],v[4:7],a[32:35]
EX v104,v104
MM a[40:43],a[148:151],v[12:15],a[40:43]
EX v105,v105
MM a[40:43],a[144:147],v[8:11],a[40:43]
RD a[228:231],v225 offset:40960
CV v76,v104,v105
MM a[48:51],a[144:147],v[16:19],a[48:51]
buffer_load_dwordx4 v231, s[8:11], s64 offen offset:3072 lds
MM a[48:51],a[148:151],v[20:23],a[48:51]
s_add_u32 m0, s65, 65536
EX v106,v106
MM a[56:59],a[148:151],v[28:31],a[56:59]
EX v107,v107
MM a[56:59],a[144:147],v[24:27],a[56:59]
RD a[232:235],v226 offset:40960
CV v77,v106,v107
MM a[60:63],a[152:155],v[24:27],a[60:63]
EX v108,v108
MM a[60:63],a[156:159],v[28:31],a[60:63]
buffer_load_dwordx4 v235, s[12:15], s81 offen offset:0 lds
MM a[52:55],a[156:159],v[20:23],a[52:55]
EX v109,v109
MM a[52:55],a[152:155],v[16:19],a[52:55]
RD a[236:239],v227 offset:40960
CV v78,v108,v109
MM a[44:47],a[152:155],v[8:11],a[44:47]
EX v110,v110
MM a[44:47],a[156:159],v[12:15],a[44:47]
EX v111,v111
MM a[36:39],a[156:159],v[4:7],a[36:39]
RD a[240:243],v224 offset:45056
CV v79,v110,v111
MM a[36:39],a[152:155],v[0:3],a[36:39]
EX v112,v112
MM a[64:67],a[160:163],v[0:3],a[64:67]
buffer_load_dwordx4 v235, s[12:15], s64 offen offset:1024 lds
MM a[64:67],a[164:167],v[4:7],a[64:67]
EX v113,v113
MM a[72:75],a[164:167],v[12:15],a[72:75]
RD a[244:247],v225 offset:45056
CV v84,v112,v113
MM a[72:75],a[160:163],v[8:11],a[72:75]
EX v114,v114
MM a[80:83],a[160:163],v[16:19],a[80:83]
EX v115,v115
MM a[80:83],a[164:167],v[20:23],a[80:83]
RD a[248:251],v226 offset:45056
CV v85,v114,v115
MM a[88:91],a[164:167],v[28:31],a[88:91]
EX v116,v116
MM a[88:91],a[160:163],v[24:27],a[88:91]
buffer_load_dwordx4 v235, s[12:15], s81 offen offset:2048 lds
MM a[92:95],a[168:171],v[24:27],a[92:95]
EX v117,v117
MM a[92:95],a[172:175],v[28:31],a[92:95]
RD a[252:255],v227 offset:45056
CV v86,v116,v117
MM a[84:87],a[172:175],v[20:23],a[84:87]
EX v118,v118
MM a[84:87],a[168:171],v[16:19],a[84:87]
EX v119,v119
MM a[76:79],a[168:171],v[8:11],a[76:79]
CV v87,v118,v119
MM a[76:79],a[172:175],v[12:15],a[76:79]
EX v120,v120
MM a[68:71],a[172:175],v[4:7],a[68:71]
buffer_load_dwordx4 v235, s[12:15], s64 offen offset:3072 lds
MM a[68:71],a[168:171],v[0:3],a[68:71]
EX v121,v121
MM a[96:99],a[176:179],v[0:3],a[96:99]
EX v122,v122
MM a[96:99],a[180:183],v[4:7],a[96:99]
EX v123,v123
MM a[104:107],a[180:183],v[12:15],a[104:107]
EX v124,v124
MM a[104:107],a[176:179],v[8:11],a[104:107]
EX v125,v125
MM a[112:115],a[176:179],v[16:19],a[112:115]
EX v126,v126
MM a[112:115],a[180:183],v[20:23],a[112:115]
EX v127,v127
MM a[120:123],a[180:183],v[28:31],a[120:123]
s_waitcnt vmcnt(8)
v_cmp_ne_u32 vcc, s69, v252
s_cbranch_vccnz LRFG_%=
LRFGb_%=:
MM a[120:123],a[176:179],v[24:27],a[120:123]
v_lshrrev_b32 v253, 8, v248
v_lshlrev_b32 v253, 10, v253
MM a[124:127],a[184:187],v[24:27],a[124:127]
v_add_f32 v244, v144, v145
v_add_f32 v245, v148, v149
MM a[124:127],a[188:191],v[28:31],a[124:127]
v_add_f32 v246, v152, v153
v_add_f32 v247, v156, v157
v_and_b32 v252, 240, v248
MM a[116:119],a[188:191],v[20:23],a[116:119]
v_add_u32 v253, v253, v252
CV v92,v120,v121
MM a[116:119],a[184:187],v[16:19],a[116:119]
s_lshr_b32 s76, s18, 6
CV v93,v122,v123
CV v94,v124,v125
MM a[108:111],a[184:187],v[8:11],a[108:111]
s_lshl_b32 s76, s76, 13
s_bfe_u32 s90, s18, 131076
CV v95,v126,v127
MM a[108:111],a[188:191],v[12:15],a[108:111]
s_lshl_b32 s90, s90, 8
s_add_u32 s76, s76, s90
s_add_u32 s76, s76, 22183936
MM a[100:103],a[188:191],v[4:7],a[100:103]
s_lshl_b32 s90, %[swave], 11
s_add_u32 s90, s90, %[slds]
MM a[100:103],a[184:187],v[0:3],a[100:103]
s_add_u32 m0, s90, 131072
s_nop 0
buffer_load_dwordx4 v253, s[20:23], s76 offen offset:0 sc1 lds
v_smfmac_f32_16x16x64_bf16 v[144:147], v[236:239], v[64:71], v234
v_smfmac_f32_16x16x64_bf16 v[148:151], v[236:239], v[72:79], v234
v_smfmac_f32_16x16x64_bf16 v[152:155], v[236:239], v[80:87], v234
s_add_u32 m0, s90, 132096
s_add_u32 s76, s76, 4096
buffer_load_dwordx4 v253, s[20:23], s76 offen offset:0 sc1 lds
v_smfmac_f32_16x16x64_bf16 v[156:159], v[236:239], v[88:95], v234
s_waitcnt lgkmcnt(0)
s_add_u32 s67, s95, 3072
s_mov_b32 s70, 3
s_cmp_ge_u32 s16, 90
s_cbranch_scc1 LLAP_%=
MM v[0:3],a[192:195],v[160:163],v[128:131]
s_add_u32 s64, s64, 16384
TR a[128:129],v232 offset:16384
s_add_u32 s81, s64, 896
MM v[0:3],a[196:199],v[176:179],v[0:3]
TR a[130:131],v232 offset:20480
v_max3_f32 v249, v144, v145, v148
MM v[0:3],a[200:203],v[192:195],v[0:3]
TR a[132:133],v232 offset:24576
v_max3_f32 v249, v249, v149, v152
MM v[0:3],a[204:207],v[208:211],v[0:3]
TR a[134:135],v232 offset:28672
v_max3_f32 v249, v249, v153, v156
MM v[4:7],a[220:223],v[208:211],v[128:131]
TR a[136:137],v233 offset:16384
v_max_f32 v249, v249, v157
MM v[4:7],a[216:219],v[192:195],v[4:7]
TR a[138:139],v233 offset:20480
v_cmp_lt_f32 vcc, s71, v249
MM v[4:7],a[212:215],v[176:179],v[4:7]
TR a[140:141],v233 offset:24576
TR a[142:143],v233 offset:28672
MM v[4:7],a[208:211],v[160:163],v[4:7]
s_cbranch_vccnz LSITE21_%=
LBACK21_%=:
MM v[12:15],a[208:211],v[164:167],v[132:135]
EX v0,v0
MM v[12:15],a[212:215],v[180:183],v[12:15]
EX v1,v1
MM v[12:15],a[216:219],v[196:199],v[12:15]
TR a[144:145],v232 offset:16896
CV v0,v0,v1
MM v[12:15],a[220:223],v[212:215],v[12:15]
EX v2,v2
MM v[8:11],a[204:207],v[212:215],v[132:135]
EX v3,v3
MM v[8:11],a[200:203],v[196:199],v[8:11]
TR a[146:147],v232 offset:20992
CV v1,v2,v3
MM v[8:11],a[196:199],v[180:183],v[8:11]
EX v4,v4
MM v[8:11],a[192:195],v[164:167],v[8:11]
TR a[148:149],v232 offset:25088
TR a[150:151],v232 offset:29184
MM v[16:19],a[192:195],v[168:171],v[136:139]
EX v5,v5
MM v[16:19],a[196:199],v[184:187],v[16:19]
TR a[152:153],v233 offset:16896
CV v2,v4,v5
MM v[16:19],a[200:203],v[200:203],v[16:19]
EX v6,v6
MM v[16:19],a[204:207],v[216:219],v[16:19]
TR a[154:155],v233 offset:20992
TR a[156:157],v233 offset:25088
MM v[20:23],a[220:223],v[216:219],v[136:139]
s_add_u32 s87, s95, 3072
buffer_store_dword v240, v248, s[20:23], s87 offen offset:12 sc1
MM v[20:23],a[216:219],v[200:203],v[20:23]
EX v7,v7
MM v[20:23],a[212:215],v[184:187],v[20:23]
TR a[158:159],v233 offset:29184
CV v3,v6,v7
MM v[20:23],a[208:211],v[168:171],v[20:23]
EX v8,v8
MM v[28:31],a[208:211],v[172:175],v[140:143]
EX v9,v9
MM v[28:31],a[212:215],v[188:191],v[28:31]
TR a[160:161],v232 offset:17408
CV v8,v8,v9
MM v[28:31],a[216:219],v[204:207],v[28:31]
EX v10,v10
MM v[28:31],a[220:223],v[220:223],v[28:31]
EX v11,v11
MM v[24:27],a[204:207],v[220:223],v[140:143]
TR a[162:163],v232 offset:21504
CV v9,v10,v11
MM v[24:27],a[200:203],v[204:207],v[24:27]
TR a[164:165],v232 offset:25600
TR a[166:167],v232 offset:29696
MM v[24:27],a[196:199],v[188:191],v[24:27]
)ASM"
R"ASM(
EX v12,v12
MM v[24:27],a[192:195],v[172:175],v[24:27]
EX v13,v13
MM v[32:35],a[224:227],v[160:163],v[128:131]
TR a[168:169],v233 offset:17408
CV v10,v12,v13
MM v[32:35],a[228:231],v[176:179],v[32:35]
EX v14,v14
MM v[32:35],a[232:235],v[192:195],v[32:35]
TR a[170:171],v233 offset:21504
TR a[172:173],v233 offset:25600
MM v[32:35],a[236:239],v[208:211],v[32:35]
EX v15,v15
MM v[36:39],a[252:255],v[208:211],v[128:131]
TR a[174:175],v233 offset:29696
CV v11,v14,v15
MM v[36:39],a[248:251],v[192:195],v[36:39]
EX v16,v16
MM v[36:39],a[244:247],v[176:179],v[36:39]
EX v17,v17
MM v[36:39],a[240:243],v[160:163],v[36:39]
TR a[176:177],v232 offset:17920
CV v16,v16,v17
MM v[44:47],a[240:243],v[164:167],v[132:135]
EX v18,v18
MM v[44:47],a[244:247],v[180:183],v[44:47]
TR a[178:179],v232 offset:22016
TR a[180:181],v232 offset:26112
MM v[44:47],a[248:251],v[196:199],v[44:47]
EX v19,v19
MM v[44:47],a[252:255],v[212:215],v[44:47]
TR a[182:183],v232 offset:30208
CV v17,v18,v19
MM v[40:43],a[236:239],v[212:215],v[132:135]
EX v20,v20
MM v[40:43],a[232:235],v[196:199],v[40:43]
EX v21,v21
MM v[40:43],a[228:231],v[180:183],v[40:43]
TR a[184:185],v233 offset:17920
CV v18,v20,v21
MM v[40:43],a[224:227],v[164:167],v[40:43]
EX v22,v22
MM v[48:51],a[224:227],v[168:171],v[136:139]
TR a[186:187],v233 offset:22016
TR a[188:189],v233 offset:26112
MM v[48:51],a[228:231],v[184:187],v[48:51]
EX v23,v23
MM v[48:51],a[232:235],v[200:203],v[48:51]
TR a[190:191],v233 offset:30208
CV v19,v22,v23
MM v[48:51],a[236:239],v[216:219],v[48:51]
EX v24,v24
MM v[52:55],a[252:255],v[216:219],v[136:139]
EX v25,v25
MM v[52:55],a[248:251],v[200:203],v[52:55]
EX v26,v26
MM v[52:55],a[244:247],v[184:187],v[52:55]
EX v27,v27
MM v[52:55],a[240:243],v[168:171],v[52:55]
EX v28,v28
MM v[60:63],a[240:243],v[172:175],v[140:143]
EX v29,v29
MM v[60:63],a[244:247],v[188:191],v[60:63]
EX v30,v30
MM v[60:63],a[248:251],v[204:207],v[60:63]
EX v31,v31
MM v[60:63],a[252:255],v[220:223],v[60:63]
EX v32,v32
MM v[56:59],a[236:239],v[220:223],v[140:143]
EX v33,v33
MM v[56:59],a[232:235],v[204:207],v[56:59]
EX v34,v34
MM v[56:59],a[228:231],v[188:191],v[56:59]
EX v35,v35
MM v[56:59],a[224:227],v[172:175],v[56:59]
s_waitcnt lgkmcnt(0)
s_barrier
MM a[0:3],a[128:131],v[64:67],a[0:3]
s_add_u32 m0, s65, 16384
CV v24,v24,v25
CV v25,v26,v27
MM a[0:3],a[132:135],v[68:71],a[0:3]
RD a[192:195],v224 offset:49152
CV v26,v28,v29
MM a[8:11],a[132:135],v[76:79],a[8:11]
RD a[196:199],v225 offset:49152
CV v27,v30,v31
MM a[8:11],a[128:131],v[72:75],a[8:11]
buffer_load_dwordx4 v228, s[8:11], s64 offen offset:0 lds
MM a[16:19],a[128:131],v[80:83],a[16:19]
RD a[200:203],v226 offset:49152
CV v4,v32,v33
MM a[16:19],a[132:135],v[84:87],a[16:19]
RD a[204:207],v227 offset:49152
CV v5,v34,v35
MM a[24:27],a[132:135],v[92:95],a[24:27]
buffer_load_dwordx4 v229, s[8:11], s64 offen offset:1024 lds
MM a[24:27],a[128:131],v[88:91],a[24:27]
RD a[208:211],v224 offset:53248
RD a[212:215],v225 offset:53248
MM a[28:31],a[136:139],v[88:91],a[28:31]
buffer_load_dwordx4 v230, s[8:11], s64 offen offset:2048 lds
MM a[28:31],a[140:143],v[92:95],a[28:31]
EX v36,v36
MM a[20:23],a[140:143],v[84:87],a[20:23]
EX v37,v37
MM a[20:23],a[136:139],v[80:83],a[20:23]
RD a[216:219],v226 offset:53248
CV v6,v36,v37
MM a[12:15],a[136:139],v[72:75],a[12:15]
EX v38,v38
MM a[12:15],a[140:143],v[76:79],a[12:15]
RD a[220:223],v227 offset:53248
RD a[224:227],v224 offset:57344
MM a[4:7],a[140:143],v[68:71],a[4:7]
EX v39,v39
MM a[4:7],a[136:139],v[64:67],a[4:7]
RD a[228:231],v225 offset:57344
CV v7,v38,v39
MM a[32:35],a[144:147],v[64:67],a[32:35]
EX v40,v40
MM a[32:35],a[148:151],v[68:71],a[32:35]
buffer_load_dwordx4 v231, s[8:11], s64 offen offset:3072 lds
MM a[40:43],a[148:151],v[76:79],a[40:43]
s_add_u32 m0, s65, 81920
EX v41,v41
MM a[40:43],a[144:147],v[72:75],a[40:43]
RD a[232:235],v226 offset:57344
CV v12,v40,v41
MM a[48:51],a[144:147],v[80:83],a[48:51]
EX v42,v42
MM a[48:51],a[148:151],v[84:87],a[48:51]
EX v43,v43
MM a[56:59],a[148:151],v[92:95],a[56:59]
RD a[236:239],v227 offset:57344
CV v13,v42,v43
MM a[56:59],a[144:147],v[88:91],a[56:59]
EX v44,v44
MM a[60:63],a[152:155],v[88:91],a[60:63]
buffer_load_dwordx4 v235, s[12:15], s81 offen offset:0 lds
MM a[60:63],a[156:159],v[92:95],a[60:63]
EX v45,v45
MM a[52:55],a[156:159],v[84:87],a[52:55]
RD a[240:243],v224 offset:61440
CV v14,v44,v45
MM a[52:55],a[152:155],v[80:83],a[52:55]
EX v46,v46
MM a[44:47],a[152:155],v[72:75],a[44:47]
EX v47,v47
MM a[44:47],a[156:159],v[76:79],a[44:47]
RD a[244:247],v225 offset:61440
CV v15,v46,v47
MM a[36:39],a[156:159],v[68:71],a[36:39]
buffer_load_dwordx4 v235, s[12:15], s64 offen offset:1024 lds
MM a[36:39],a[152:155],v[64:67],a[36:39]
EX v48,v48
MM a[64:67],a[160:163],v[64:67],a[64:67]
EX v49,v49
MM a[64:67],a[164:167],v[68:71],a[64:67]
RD a[248:251],v226 offset:61440
CV v20,v48,v49
MM a[72:75],a[164:167],v[76:79],a[72:75]
EX v50,v50
MM a[72:75],a[160:163],v[72:75],a[72:75]
EX v51,v51
MM a[80:83],a[160:163],v[80:83],a[80:83]
RD a[252:255],v227 offset:61440
CV v21,v50,v51
MM a[80:83],a[164:167],v[84:87],a[80:83]
buffer_load_dwordx4 v235, s[12:15], s81 offen offset:2048 lds
MM a[88:91],a[164:167],v[92:95],a[88:91]
EX v52,v52
MM a[88:91],a[160:163],v[88:91],a[88:91]
EX v53,v53
MM a[92:95],a[168:171],v[88:91],a[92:95]
EX v54,v54
MM a[92:95],a[172:175],v[92:95],a[92:95]
buffer_load_dwordx4 v235, s[12:15], s64 offen offset:3072 lds
MM a[84:87],a[172:175],v[84:87],a[84:87]
EX v55,v55
MM a[84:87],a[168:171],v[80:83],a[84:87]
EX v56,v56
MM a[76:79],a[168:171],v[72:75],a[76:79]
EX v57,v57
MM a[76:79],a[172:175],v[76:79],a[76:79]
EX v58,v58
MM a[68:71],a[172:175],v[68:71],a[68:71]
EX v59,v59
MM a[68:71],a[168:171],v[64:67],a[68:71]
EX v60,v60
MM a[96:99],a[176:179],v[64:67],a[96:99]
EX v61,v61
MM a[96:99],a[180:183],v[68:71],a[96:99]
EX v62,v62
MM a[104:107],a[180:183],v[76:79],a[104:107]
EX v63,v63
MM a[104:107],a[176:179],v[72:75],a[104:107]
CV v22,v52,v53
CV v23,v54,v55
MM a[112:115],a[176:179],v[80:83],a[112:115]
CV v28,v56,v57
CV v29,v58,v59
MM a[112:115],a[180:183],v[84:87],a[112:115]
CV v30,v60,v61
CV v31,v62,v63
MM a[120:123],a[180:183],v[92:95],a[120:123]
MM a[120:123],a[176:179],v[88:91],a[120:123]
MM a[124:127],a[184:187],v[88:91],a[124:127]
v_add_f32 v244, v144, v145
v_add_f32 v245, v148, v149
MM a[124:127],a[188:191],v[92:95],a[124:127]
v_add_f32 v246, v152, v153
v_add_f32 v247, v156, v157
MM a[116:119],a[188:191],v[84:87],a[116:119]
MM a[116:119],a[184:187],v[80:83],a[116:119]
MM a[108:111],a[184:187],v[72:75],a[108:111]
MM a[108:111],a[188:191],v[76:79],a[108:111]
MM a[100:103],a[188:191],v[68:71],a[100:103]
MM a[100:103],a[184:187],v[64:67],a[100:103]
v_smfmac_f32_16x16x64_bf16 v[144:147], v[236:239], v[0:7], v234
v_smfmac_f32_16x16x64_bf16 v[148:151], v[236:239], v[8:15], v234
v_smfmac_f32_16x16x64_bf16 v[152:155], v[236:239], v[16:23], v234
v_smfmac_f32_16x16x64_bf16 v[156:159], v[236:239], v[24:31], v234
s_waitcnt lgkmcnt(0)
MM v[64:67],a[192:195],v[160:163],v[128:131]
s_add_u32 s64, s64, 16384
TR a[128:129],v232 offset:32768
s_add_u32 s81, s64, 896
MM v[64:67],a[196:199],v[176:179],v[64:67]
TR a[130:131],v232 offset:36864
v_max3_f32 v249, v144, v145, v148
MM v[64:67],a[200:203],v[192:195],v[64:67]
TR a[132:133],v232 offset:40960
v_max3_f32 v249, v249, v149, v152
MM v[64:67],a[204:207],v[208:211],v[64:67]
TR a[134:135],v232 offset:45056
v_max3_f32 v249, v249, v153, v156
MM v[68:71],a[220:223],v[208:211],v[128:131]
TR a[136:137],v233 offset:32768
v_max_f32 v249, v249, v157
MM v[68:71],a[216:219],v[192:195],v[68:71]
TR a[138:139],v233 offset:36864
v_cmp_lt_f32 vcc, s71, v249
MM v[68:71],a[212:215],v[176:179],v[68:71]
TR a[140:141],v233 offset:40960
TR a[142:143],v233 offset:45056
MM v[68:71],a[208:211],v[160:163],v[68:71]
s_cbranch_vccnz LSITE22_%=
LBACK22_%=:
MM v[76:79],a[208:211],v[164:167],v[132:135]
v_lshrrev_b32 v43, 7, v248
v_bfe_u32 v42, v248, 6, 1
MM v[76:79],a[212:215],v[180:183],v[76:79]
v_lshl_add_u32 v40, v42, 3, v43
v_lshlrev_b32 v40, 4, v40
MM v[76:79],a[216:219],v[196:199],v[76:79]
v_bfe_u32 v42, v248, 4, 2
v_lshl_add_u32 v40, v42, 2, v40
MM v[76:79],a[220:223],v[212:215],v[76:79]
s_lshl_b32 s76, %[swave], 11
EX v64,v64
MM v[72:75],a[204:207],v[212:215],v[132:135]
s_add_u32 s76, s76, %[slds]
EX v65,v65
MM v[72:75],a[200:203],v[196:199],v[72:75]
s_add_u32 s76, s76, 131072
v_add_u32 v40, s76, v40
CV v64,v64,v65
MM v[72:75],a[196:199],v[180:183],v[72:75]
s_waitcnt vmcnt(9)
ds_read_b32 v32, v40 offset:0
ds_read_b32 v36, v40 offset:256
MM v[72:75],a[192:195],v[164:167],v[72:75]
ds_read_b32 v33, v40 offset:512
ds_read_b32 v37, v40 offset:768
MM v[80:83],a[192:195],v[168:171],v[136:139]
ds_read_b32 v34, v40 offset:1024
LPG17_%=:
s_cbranch_execz LTR3_%=
ds_read_b32 v38, v40 offset:1280
EX v66,v66
MM v[80:83],a[196:199],v[184:187],v[80:83]
ds_read_b32 v35, v40 offset:1536
ds_read_b32 v39, v40 offset:1792
EX v67,v67
MM v[80:83],a[200:203],v[200:203],v[80:83]
v_mul_u32_u24 v41, 3456, v43
v_bfe_u32 v42, v248, 4, 3
MM v[80:83],a[204:207],v[216:219],v[80:83]
TR a[144:145],v232 offset:33280
v_lshl_add_u32 v41, v42, 1, v41
EX v68,v68
MM v[84:87],a[220:223],v[216:219],v[136:139]
s_add_u32 s76, s18, 21626880
EX v69,v69
MM v[84:87],a[216:219],v[200:203],v[84:87]
TR a[146:147],v232 offset:37376
TR a[148:149],v232 offset:41472
MM v[84:87],a[212:215],v[184:187],v[84:87]
EX v70,v70
MM v[84:87],a[208:211],v[168:171],v[84:87]
TR a[150:151],v232 offset:45568
CV v65,v66,v67
MM v[92:95],a[208:211],v[172:175],v[140:143]
EX v71,v71
MM v[92:95],a[212:215],v[188:191],v[92:95]
EX v72,v72
MM v[92:95],a[216:219],v[204:207],v[92:95]
TR a[152:153],v233 offset:33280
CV v66,v68,v69
MM v[92:95],a[220:223],v[220:223],v[92:95]
EX v73,v73
MM v[88:91],a[204:207],v[220:223],v[140:143]
s_waitcnt lgkmcnt(5)
v_add_f32 v32, v32, v33
v_add_f32 v36, v36, v37
MM v[88:91],a[200:203],v[204:207],v[88:91]
v_add_f32 v34, v34, v35
v_add_f32 v38, v38, v39
MM v[88:91],a[196:199],v[188:191],v[88:91]
TR a[154:155],v233 offset:37376
v_add_f32 v32, v32, v34
v_add_f32 v36, v36, v38
MM v[88:91],a[192:195],v[172:175],v[88:91]
CV v42,v32,v36
v_lshlrev_b32 v32, 16, v42
EX v74,v74
MM v[96:99],a[224:227],v[160:163],v[128:131]
TR a[156:157],v233 offset:41472
v_and_b32 v36, 0xffff0000, v42
v_mul_f32 v42, 0xbfb8aa3b, v32
EX v75,v75
MM v[96:99],a[228:231],v[176:179],v[96:99]
EX v42,v42
MM v[96:99],a[232:235],v[192:195],v[96:99]
TR a[158:159],v233 offset:45568
v_add_f32 v42, 1.0, v42
EX v76,v76
MM v[96:99],a[236:239],v[208:211],v[96:99]
v_rcp_f32 v42, v42
MM v[100:103],a[252:255],v[208:211],v[128:131]
TR a[160:161],v232 offset:33792
v_mul_f32 v42, v32, v42
CV v42,v42,v42
EX v77,v77
CV v67,v70,v71
MM v[100:103],a[248:251],v[192:195],v[100:103]
v_lshlrev_b32 v42, 16, v42
v_mul_f32 v32, v42, v36
MM v[100:103],a[244:247],v[176:179],v[100:103]
TR a[162:163],v232 offset:37888
CV v43,v32,v32
EX v78,v78
MM v[100:103],a[240:243],v[160:163],v[100:103]
buffer_store_short v43, v41, s[20:23], s76 offen offset:0 sc1
s_nop 1
TR a[164:165],v232 offset:41984
MM v[108:111],a[240:243],v[164:167],v[132:135]
EX v79,v79
CV v72,v72,v73
MM v[108:111],a[244:247],v[180:183],v[108:111]
TR a[166:167],v232 offset:46080
CV v73,v74,v75
MM v[108:111],a[248:251],v[196:199],v[108:111]
EX v80,v80
MM v[108:111],a[252:255],v[212:215],v[108:111]
TR a[168:169],v233 offset:33792
TR a[170:171],v233 offset:37888
EX v81,v81
MM v[104:107],a[236:239],v[212:215],v[132:135]
TR a[172:173],v233 offset:41984
CV v74,v76,v77
MM v[104:107],a[232:235],v[196:199],v[104:107]
EX v82,v82
MM v[104:107],a[228:231],v[180:183],v[104:107]
EX v83,v83
MM v[104:107],a[224:227],v[164:167],v[104:107]
TR a[174:175],v233 offset:46080
CV v75,v78,v79
MM v[112:115],a[224:227],v[168:171],v[136:139]
EX v84,v84
MM v[112:115],a[228:231],v[184:187],v[112:115]
TR a[176:177],v232 offset:34304
TR a[178:179],v232 offset:38400
MM v[112:115],a[232:235],v[200:203],v[112:115]
EX v85,v85
MM v[112:115],a[236:239],v[216:219],v[112:115]
TR a[180:181],v232 offset:42496
CV v80,v80,v81
MM v[116:119],a[252:255],v[216:219],v[136:139]
EX v86,v86
MM v[116:119],a[248:251],v[200:203],v[116:119]
)ASM"
R"ASM(
TR a[182:183],v232 offset:46592
CV v81,v82,v83
MM v[116:119],a[244:247],v[184:187],v[116:119]
EX v87,v87
MM v[116:119],a[240:243],v[168:171],v[116:119]
TR a[184:185],v233 offset:34304
CV v82,v84,v85
MM v[124:127],a[240:243],v[172:175],v[140:143]
EX v88,v88
MM v[124:127],a[244:247],v[188:191],v[124:127]
TR a[186:187],v233 offset:38400
TR a[188:189],v233 offset:42496
MM v[124:127],a[248:251],v[204:207],v[124:127]
TR a[190:191],v233 offset:46592
CV v83,v86,v87
EX v89,v89
MM v[124:127],a[252:255],v[220:223],v[124:127]
EX v90,v90
MM v[120:123],a[236:239],v[220:223],v[140:143]
EX v91,v91
MM v[120:123],a[232:235],v[204:207],v[120:123]
EX v92,v92
MM v[120:123],a[228:231],v[188:191],v[120:123]
EX v93,v93
MM v[120:123],a[224:227],v[172:175],v[120:123]
s_waitcnt lgkmcnt(0)
s_barrier
MM a[0:3],a[128:131],v[0:3],a[0:3]
s_add_u32 m0, s65, 32768
CV v88,v88,v89
CV v89,v90,v91
MM a[0:3],a[132:135],v[4:7],a[0:3]
EX v94,v94
MM a[8:11],a[132:135],v[12:15],a[8:11]
RD a[192:195],v224 offset:0
CV v90,v92,v93
MM a[8:11],a[128:131],v[8:11],a[8:11]
EX v95,v95
MM a[16:19],a[128:131],v[16:19],a[16:19]
RD a[196:199],v225 offset:0
CV v91,v94,v95
MM a[16:19],a[132:135],v[20:23],a[16:19]
EX v96,v96
MM a[24:27],a[132:135],v[28:31],a[24:27]
buffer_load_dwordx4 v228, s[8:11], s64 offen offset:0 lds
MM a[24:27],a[128:131],v[24:27],a[24:27]
EX v97,v97
MM a[28:31],a[136:139],v[24:27],a[28:31]
EX v98,v98
MM a[28:31],a[140:143],v[28:31],a[28:31]
RD a[200:203],v226 offset:0
CV v68,v96,v97
MM a[20:23],a[140:143],v[20:23],a[20:23]
EX v99,v99
MM a[20:23],a[136:139],v[16:19],a[20:23]
RD a[204:207],v227 offset:0
CV v69,v98,v99
MM a[12:15],a[136:139],v[8:11],a[12:15]
EX v100,v100
MM a[12:15],a[140:143],v[12:15],a[12:15]
buffer_load_dwordx4 v229, s[8:11], s64 offen offset:1024 lds
MM a[4:7],a[140:143],v[4:7],a[4:7]
EX v101,v101
MM a[4:7],a[136:139],v[0:3],a[4:7]
RD a[208:211],v224 offset:4096
CV v70,v100,v101
MM a[32:35],a[144:147],v[0:3],a[32:35]
EX v102,v102
MM a[32:35],a[148:151],v[4:7],a[32:35]
EX v103,v103
MM a[40:43],a[148:151],v[12:15],a[40:43]
RD a[212:215],v225 offset:4096
CV v71,v102,v103
MM a[40:43],a[144:147],v[8:11],a[40:43]
EX v104,v104
MM a[48:51],a[144:147],v[16:19],a[48:51]
buffer_load_dwordx4 v230, s[8:11], s64 offen offset:2048 lds
MM a[48:51],a[148:151],v[20:23],a[48:51]
EX v105,v105
MM a[56:59],a[148:151],v[28:31],a[56:59]
RD a[216:219],v226 offset:4096
CV v76,v104,v105
MM a[56:59],a[144:147],v[24:27],a[56:59]
EX v106,v106
MM a[60:63],a[152:155],v[24:27],a[60:63]
EX v107,v107
MM a[60:63],a[156:159],v[28:31],a[60:63]
RD a[220:223],v227 offset:4096
CV v77,v106,v107
MM a[52:55],a[156:159],v[20:23],a[52:55]
EX v108,v108
MM a[52:55],a[152:155],v[16:19],a[52:55]
buffer_load_dwordx4 v231, s[8:11], s64 offen offset:3072 lds
MM a[44:47],a[152:155],v[8:11],a[44:47]
s_add_u32 m0, s65, 98304
EX v109,v109
MM a[44:47],a[156:159],v[12:15],a[44:47]
RD a[224:227],v224 offset:8192
CV v78,v108,v109
MM a[36:39],a[156:159],v[4:7],a[36:39]
EX v110,v110
MM a[36:39],a[152:155],v[0:3],a[36:39]
EX v111,v111
MM a[64:67],a[160:163],v[0:3],a[64:67]
RD a[228:231],v225 offset:8192
CV v79,v110,v111
MM a[64:67],a[164:167],v[4:7],a[64:67]
EX v112,v112
MM a[72:75],a[164:167],v[12:15],a[72:75]
buffer_load_dwordx4 v235, s[12:15], s81 offen offset:0 lds
MM a[72:75],a[160:163],v[8:11],a[72:75]
EX v113,v113
MM a[80:83],a[160:163],v[16:19],a[80:83]
RD a[232:235],v226 offset:8192
CV v84,v112,v113
MM a[80:83],a[164:167],v[20:23],a[80:83]
EX v114,v114
MM a[88:91],a[164:167],v[28:31],a[88:91]
EX v115,v115
MM a[88:91],a[160:163],v[24:27],a[88:91]
RD a[236:239],v227 offset:8192
CV v85,v114,v115
MM a[92:95],a[168:171],v[24:27],a[92:95]
EX v116,v116
MM a[92:95],a[172:175],v[28:31],a[92:95]
buffer_load_dwordx4 v235, s[12:15], s64 offen offset:1024 lds
MM a[84:87],a[172:175],v[20:23],a[84:87]
EX v117,v117
MM a[84:87],a[168:171],v[16:19],a[84:87]
RD a[240:243],v224 offset:12288
CV v86,v116,v117
MM a[76:79],a[168:171],v[8:11],a[76:79]
EX v118,v118
MM a[76:79],a[172:175],v[12:15],a[76:79]
EX v119,v119
MM a[68:71],a[172:175],v[4:7],a[68:71]
RD a[244:247],v225 offset:12288
CV v87,v118,v119
MM a[68:71],a[168:171],v[0:3],a[68:71]
buffer_load_dwordx4 v235, s[12:15], s81 offen offset:2048 lds
MM a[96:99],a[176:179],v[0:3],a[96:99]
EX v120,v120
MM a[96:99],a[180:183],v[4:7],a[96:99]
EX v121,v121
MM a[104:107],a[180:183],v[12:15],a[104:107]
RD a[248:251],v226 offset:12288
CV v92,v120,v121
MM a[104:107],a[176:179],v[8:11],a[104:107]
EX v122,v122
MM a[112:115],a[176:179],v[16:19],a[112:115]
EX v123,v123
MM a[112:115],a[180:183],v[20:23],a[112:115]
RD a[252:255],v227 offset:12288
CV v93,v122,v123
MM a[120:123],a[180:183],v[28:31],a[120:123]
buffer_load_dwordx4 v235, s[12:15], s64 offen offset:3072 lds
MM a[120:123],a[176:179],v[24:27],a[120:123]
EX v124,v124
MM a[124:127],a[184:187],v[24:27],a[124:127]
v_add_f32 v244, v144, v145
v_add_f32 v245, v148, v149
MM a[124:127],a[188:191],v[28:31],a[124:127]
v_add_f32 v246, v152, v153
v_add_f32 v247, v156, v157
MM a[116:119],a[188:191],v[20:23],a[116:119]
EX v125,v125
MM a[116:119],a[184:187],v[16:19],a[116:119]
EX v126,v126
MM a[108:111],a[184:187],v[8:11],a[108:111]
EX v127,v127
MM a[108:111],a[188:191],v[12:15],a[108:111]
CV v94,v124,v125
CV v95,v126,v127
MM a[100:103],a[188:191],v[4:7],a[100:103]
MM a[100:103],a[184:187],v[0:3],a[100:103]
v_smfmac_f32_16x16x64_bf16 v[144:147], v[236:239], v[64:71], v234
v_smfmac_f32_16x16x64_bf16 v[148:151], v[236:239], v[72:79], v234
v_smfmac_f32_16x16x64_bf16 v[152:155], v[236:239], v[80:87], v234
v_smfmac_f32_16x16x64_bf16 v[156:159], v[236:239], v[88:95], v234
s_waitcnt lgkmcnt(0)
MM v[0:3],a[192:195],v[160:163],v[128:131]
s_add_u32 s64, s64, 16384
TR a[128:129],v232 offset:49152
s_add_u32 s81, s64, 896
MM v[0:3],a[196:199],v[176:179],v[0:3]
TR a[130:131],v232 offset:53248
v_max3_f32 v249, v144, v145, v148
MM v[0:3],a[200:203],v[192:195],v[0:3]
TR a[132:133],v232 offset:57344
v_max3_f32 v249, v249, v149, v152
MM v[0:3],a[204:207],v[208:211],v[0:3]
TR a[134:135],v232 offset:61440
v_max3_f32 v249, v249, v153, v156
MM v[4:7],a[220:223],v[208:211],v[128:131]
TR a[136:137],v233 offset:49152
v_max_f32 v249, v249, v157
MM v[4:7],a[216:219],v[192:195],v[4:7]
TR a[138:139],v233 offset:53248
v_cmp_lt_f32 vcc, s71, v249
MM v[4:7],a[212:215],v[176:179],v[4:7]
TR a[140:141],v233 offset:57344
TR a[142:143],v233 offset:61440
MM v[4:7],a[208:211],v[160:163],v[4:7]
s_cbranch_vccnz LSITE23_%=
LBACK23_%=:
MM v[12:15],a[208:211],v[164:167],v[132:135]
EX v0,v0
MM v[12:15],a[212:215],v[180:183],v[12:15]
EX v1,v1
MM v[12:15],a[216:219],v[196:199],v[12:15]
TR a[144:145],v232 offset:49664
CV v0,v0,v1
MM v[12:15],a[220:223],v[212:215],v[12:15]
EX v2,v2
MM v[8:11],a[204:207],v[212:215],v[132:135]
EX v3,v3
MM v[8:11],a[200:203],v[196:199],v[8:11]
TR a[146:147],v232 offset:53760
CV v1,v2,v3
MM v[8:11],a[196:199],v[180:183],v[8:11]
EX v4,v4
MM v[8:11],a[192:195],v[164:167],v[8:11]
TR a[148:149],v232 offset:57856
TR a[150:151],v232 offset:61952
MM v[16:19],a[192:195],v[168:171],v[136:139]
EX v5,v5
MM v[16:19],a[196:199],v[184:187],v[16:19]
TR a[152:153],v233 offset:49664
CV v2,v4,v5
MM v[16:19],a[200:203],v[200:203],v[16:19]
EX v6,v6
MM v[16:19],a[204:207],v[216:219],v[16:19]
TR a[154:155],v233 offset:53760
TR a[156:157],v233 offset:57856
MM v[20:23],a[220:223],v[216:219],v[136:139]
EX v7,v7
MM v[20:23],a[216:219],v[200:203],v[20:23]
TR a[158:159],v233 offset:61952
CV v3,v6,v7
MM v[20:23],a[212:215],v[184:187],v[20:23]
EX v8,v8
MM v[20:23],a[208:211],v[168:171],v[20:23]
EX v9,v9
MM v[28:31],a[208:211],v[172:175],v[140:143]
TR a[160:161],v232 offset:50176
CV v8,v8,v9
MM v[28:31],a[212:215],v[188:191],v[28:31]
EX v10,v10
MM v[28:31],a[216:219],v[204:207],v[28:31]
TR a[162:163],v232 offset:54272
TR a[164:165],v232 offset:58368
MM v[28:31],a[220:223],v[220:223],v[28:31]
EX v11,v11
MM v[24:27],a[204:207],v[220:223],v[140:143]
TR a[166:167],v232 offset:62464
CV v9,v10,v11
MM v[24:27],a[200:203],v[204:207],v[24:27]
EX v12,v12
MM v[24:27],a[196:199],v[188:191],v[24:27]
EX v13,v13
MM v[24:27],a[192:195],v[172:175],v[24:27]
TR a[168:169],v233 offset:50176
CV v10,v12,v13
MM v[32:35],a[224:227],v[160:163],v[128:131]
EX v14,v14
MM v[32:35],a[228:231],v[176:179],v[32:35]
TR a[170:171],v233 offset:54272
TR a[172:173],v233 offset:58368
MM v[32:35],a[232:235],v[192:195],v[32:35]
EX v15,v15
MM v[32:35],a[236:239],v[208:211],v[32:35]
TR a[174:175],v233 offset:62464
CV v11,v14,v15
MM v[36:39],a[252:255],v[208:211],v[128:131]
EX v16,v16
MM v[36:39],a[248:251],v[192:195],v[36:39]
EX v17,v17
MM v[36:39],a[244:247],v[176:179],v[36:39]
TR a[176:177],v232 offset:50688
CV v16,v16,v17
MM v[36:39],a[240:243],v[160:163],v[36:39]
EX v18,v18
MM v[44:47],a[240:243],v[164:167],v[132:135]
TR a[178:179],v232 offset:54784
TR a[180:181],v232 offset:58880
MM v[44:47],a[244:247],v[180:183],v[44:47]
EX v19,v19
MM v[44:47],a[248:251],v[196:199],v[44:47]
TR a[182:183],v232 offset:62976
CV v17,v18,v19
MM v[44:47],a[252:255],v[212:215],v[44:47]
EX v20,v20
MM v[40:43],a[236:239],v[212:215],v[132:135]
TR a[184:185],v233 offset:50688
TR a[186:187],v233 offset:54784
MM v[40:43],a[232:235],v[196:199],v[40:43]
EX v21,v21
MM v[40:43],a[228:231],v[180:183],v[40:43]
TR a[188:189],v233 offset:58880
CV v18,v20,v21
MM v[40:43],a[224:227],v[164:167],v[40:43]
EX v22,v22
MM v[48:51],a[224:227],v[168:171],v[136:139]
EX v23,v23
MM v[48:51],a[228:231],v[184:187],v[48:51]
TR a[190:191],v233 offset:62976
CV v19,v22,v23
MM v[48:51],a[232:235],v[200:203],v[48:51]
EX v24,v24
MM v[48:51],a[236:239],v[216:219],v[48:51]
EX v25,v25
MM v[52:55],a[252:255],v[216:219],v[136:139]
EX v26,v26
MM v[52:55],a[248:251],v[200:203],v[52:55]
EX v27,v27
MM v[52:55],a[244:247],v[184:187],v[52:55]
EX v28,v28
MM v[52:55],a[240:243],v[168:171],v[52:55]
EX v29,v29
MM v[60:63],a[240:243],v[172:175],v[140:143]
EX v30,v30
MM v[60:63],a[244:247],v[188:191],v[60:63]
EX v31,v31
MM v[60:63],a[248:251],v[204:207],v[60:63]
EX v32,v32
MM v[60:63],a[252:255],v[220:223],v[60:63]
EX v33,v33
MM v[56:59],a[236:239],v[220:223],v[140:143]
EX v34,v34
MM v[56:59],a[232:235],v[204:207],v[56:59]
EX v35,v35
MM v[56:59],a[228:231],v[188:191],v[56:59]
EX v36,v36
MM v[56:59],a[224:227],v[172:175],v[56:59]
s_waitcnt vmcnt(13) lgkmcnt(0)
s_barrier
MM a[0:3],a[128:131],v[64:67],a[0:3]
s_add_u32 m0, s65, 49152
CV v24,v24,v25
CV v25,v26,v27
MM a[0:3],a[132:135],v[68:71],a[0:3]
RD a[192:195],v224 offset:16384
CV v26,v28,v29
MM a[8:11],a[132:135],v[76:79],a[8:11]
RD a[196:199],v225 offset:16384
CV v27,v30,v31
MM a[8:11],a[128:131],v[72:75],a[8:11]
buffer_load_dwordx4 v228, s[8:11], s64 offen offset:0 lds
MM a[16:19],a[128:131],v[80:83],a[16:19]
RD a[200:203],v226 offset:16384
CV v4,v32,v33
MM a[16:19],a[132:135],v[84:87],a[16:19]
RD a[204:207],v227 offset:16384
CV v5,v34,v35
MM a[24:27],a[132:135],v[92:95],a[24:27]
buffer_load_dwordx4 v229, s[8:11], s64 offen offset:1024 lds
MM a[24:27],a[128:131],v[88:91],a[24:27]
RD a[208:211],v224 offset:20480
RD a[212:215],v225 offset:20480
MM a[28:31],a[136:139],v[88:91],a[28:31]
buffer_load_dwordx4 v230, s[8:11], s64 offen offset:2048 lds
MM a[28:31],a[140:143],v[92:95],a[28:31]
RD a[216:219],v226 offset:20480
RD a[220:223],v227 offset:20480
MM a[20:23],a[140:143],v[84:87],a[20:23]
EX v37,v37
MM a[20:23],a[136:139],v[80:83],a[20:23]
RD a[224:227],v224 offset:24576
CV v6,v36,v37
MM a[12:15],a[136:139],v[72:75],a[12:15]
EX v38,v38
MM a[12:15],a[140:143],v[76:79],a[12:15]
buffer_load_dwordx4 v231, s[8:11], s64 offen offset:3072 lds
MM a[4:7],a[140:143],v[68:71],a[4:7]
s_add_u32 m0, s65, 114688
EX v39,v39
MM a[4:7],a[136:139],v[64:67],a[4:7]
RD a[228:231],v225 offset:24576
CV v7,v38,v39
MM a[32:35],a[144:147],v[64:67],a[32:35]
EX v40,v40
MM a[32:35],a[148:151],v[68:71],a[32:35]
EX v41,v41
MM a[40:43],a[148:151],v[76:79],a[40:43]
RD a[232:235],v226 offset:24576
CV v12,v40,v41
MM a[40:43],a[144:147],v[72:75],a[40:43]
EX v42,v42
MM a[48:51],a[144:147],v[80:83],a[48:51]
buffer_load_dwordx4 v235, s[12:15], s81 offen offset:0 lds
MM a[48:51],a[148:151],v[84:87],a[48:51]
EX v43,v43
MM a[56:59],a[148:151],v[92:95],a[56:59]
)ASM"
R"ASM(
RD a[236:239],v227 offset:24576
CV v13,v42,v43
MM a[56:59],a[144:147],v[88:91],a[56:59]
EX v44,v44
MM a[60:63],a[152:155],v[88:91],a[60:63]
EX v45,v45
MM a[60:63],a[156:159],v[92:95],a[60:63]
RD a[240:243],v224 offset:28672
CV v14,v44,v45
MM a[52:55],a[156:159],v[84:87],a[52:55]
EX v46,v46
MM a[52:55],a[152:155],v[80:83],a[52:55]
buffer_load_dwordx4 v235, s[12:15], s64 offen offset:1024 lds
MM a[44:47],a[152:155],v[72:75],a[44:47]
EX v47,v47
MM a[44:47],a[156:159],v[76:79],a[44:47]
RD a[244:247],v225 offset:28672
CV v15,v46,v47
MM a[36:39],a[156:159],v[68:71],a[36:39]
EX v48,v48
MM a[36:39],a[152:155],v[64:67],a[36:39]
EX v49,v49
MM a[64:67],a[160:163],v[64:67],a[64:67]
RD a[248:251],v226 offset:28672
CV v20,v48,v49
MM a[64:67],a[164:167],v[68:71],a[64:67]
EX v50,v50
MM a[72:75],a[164:167],v[76:79],a[72:75]
buffer_load_dwordx4 v235, s[12:15], s81 offen offset:2048 lds
MM a[72:75],a[160:163],v[72:75],a[72:75]
EX v51,v51
MM a[80:83],a[160:163],v[80:83],a[80:83]
RD a[252:255],v227 offset:28672
CV v21,v50,v51
MM a[80:83],a[164:167],v[84:87],a[80:83]
EX v52,v52
MM a[88:91],a[164:167],v[92:95],a[88:91]
EX v53,v53
MM a[88:91],a[160:163],v[88:91],a[88:91]
buffer_load_dwordx4 v235, s[12:15], s64 offen offset:3072 lds
MM a[92:95],a[168:171],v[88:91],a[92:95]
EX v54,v54
MM a[92:95],a[172:175],v[92:95],a[92:95]
EX v55,v55
MM a[84:87],a[172:175],v[84:87],a[84:87]
EX v56,v56
MM a[84:87],a[168:171],v[80:83],a[84:87]
EX v57,v57
MM a[76:79],a[168:171],v[72:75],a[76:79]
EX v58,v58
MM a[76:79],a[172:175],v[76:79],a[76:79]
EX v59,v59
MM a[68:71],a[172:175],v[68:71],a[68:71]
EX v60,v60
MM a[68:71],a[168:171],v[64:67],a[68:71]
EX v61,v61
MM a[96:99],a[176:179],v[64:67],a[96:99]
EX v62,v62
MM a[96:99],a[180:183],v[68:71],a[96:99]
EX v63,v63
MM a[104:107],a[180:183],v[76:79],a[104:107]
CV v22,v52,v53
CV v23,v54,v55
MM a[104:107],a[176:179],v[72:75],a[104:107]
CV v28,v56,v57
CV v29,v58,v59
MM a[112:115],a[176:179],v[80:83],a[112:115]
CV v30,v60,v61
CV v31,v62,v63
MM a[112:115],a[180:183],v[84:87],a[112:115]
MM a[120:123],a[180:183],v[92:95],a[120:123]
s_lshr_b32 s91, s18, 2
s_add_u32 s91, s91, 69632
s_waitcnt vmcnt(16)
MM a[120:123],a[176:179],v[88:91],a[120:123]
v_mov_b32 v250, s69
s_mov_b64 exec, 1
buffer_store_dword v250, off, s[20:23], s91 offset:0 sc1
s_mov_b64 exec, -1
MM a[124:127],a[184:187],v[88:91],a[124:127]
v_add_f32 v244, v144, v145
v_add_f32 v245, v148, v149
MM a[124:127],a[188:191],v[92:95],a[124:127]
v_add_f32 v246, v152, v153
v_add_f32 v247, v156, v157
v_lshlrev_b32 v96, 3, v248
MM a[116:119],a[188:191],v[84:87],a[116:119]
v_min_u32 v96, 4992, v96
LPG18_%=:
s_cbranch_execz LTR3_%=
s_sub_u32 s91, 5242880, s26
MM a[116:119],a[184:187],v[80:83],a[116:119]
s_sub_u32 s60, s24, s91
s_subb_u32 s61, s25, 0
s_mov_b32 s62, 5242880
MM a[108:111],a[184:187],v[72:75],a[108:111]
s_mov_b32 s63, 131072
s_lshl_b32 s90, s16, 2
s_add_u32 s90, s90, %[swave]
MM a[108:111],a[188:191],v[76:79],a[108:111]
s_mul_i32 s90, s90, 10240
s_add_u32 s90, s90, 0
s_add_u32 s91, %[slds], 135168
MM a[100:103],a[188:191],v[68:71],a[100:103]
s_mov_b32 m0, s91
s_nop 0
buffer_load_dword v96, s[60:63], s90 offen offset:0 lds
MM a[100:103],a[184:187],v[64:67],a[100:103]
s_lshr_b32 s77, %[sb], 3
s_lshl_b32 s77, s77, 2
s_add_u32 s77, s77, %[swave]
v_smfmac_f32_16x16x64_bf16 v[144:147], v[236:239], v[0:7], v234
s_lshl_b32 s77, s77, 9
s_add_u32 s77, s77, 0
v_smfmac_f32_16x16x64_bf16 v[148:151], v[236:239], v[8:15], v234
v_lshrrev_b32 v253, 1, v248
v_smfmac_f32_16x16x64_bf16 v[152:155], v[236:239], v[16:23], v234
v_add_u32 v253, s77, v253
v_smfmac_f32_16x16x64_bf16 v[156:159], v[236:239], v[24:31], v234
s_waitcnt lgkmcnt(0)
s_mov_b32 s77, LKEND_ka-LPD1_%=-16
v_min_u32 v253, s77, v253
s_getpc_b64 s[98:99]
LPD1_%=:
global_load_dword v253, v253, s[98:99]
MM v[64:67],a[192:195],v[160:163],v[128:131]
s_add_u32 s64, s64, 16384
TR a[128:129],v232 offset:0
s_add_u32 s81, s64, 896
MM v[64:67],a[196:199],v[176:179],v[64:67]
TR a[130:131],v232 offset:4096
v_max3_f32 v249, v144, v145, v148
MM v[64:67],a[200:203],v[192:195],v[64:67]
TR a[132:133],v232 offset:8192
v_max3_f32 v249, v249, v149, v152
MM v[64:67],a[204:207],v[208:211],v[64:67]
TR a[134:135],v232 offset:12288
v_max3_f32 v249, v249, v153, v156
MM v[68:71],a[220:223],v[208:211],v[128:131]
TR a[136:137],v233 offset:0
v_max_f32 v249, v249, v157
MM v[68:71],a[216:219],v[192:195],v[68:71]
TR a[138:139],v233 offset:4096
v_cmp_lt_f32 vcc, s71, v249
MM v[68:71],a[212:215],v[176:179],v[68:71]
TR a[140:141],v233 offset:8192
TR a[142:143],v233 offset:12288
MM v[68:71],a[208:211],v[160:163],v[68:71]
s_cbranch_vccnz LSITE24_%=
LBACK24_%=:
MM v[76:79],a[208:211],v[164:167],v[132:135]
EX v64,v64
MM v[76:79],a[212:215],v[180:183],v[76:79]
EX v65,v65
MM v[76:79],a[216:219],v[196:199],v[76:79]
TR a[144:145],v232 offset:512
CV v64,v64,v65
MM v[76:79],a[220:223],v[212:215],v[76:79]
EX v66,v66
MM v[72:75],a[204:207],v[212:215],v[132:135]
EX v67,v67
MM v[72:75],a[200:203],v[196:199],v[72:75]
TR a[146:147],v232 offset:4608
CV v65,v66,v67
MM v[72:75],a[196:199],v[180:183],v[72:75]
EX v68,v68
MM v[72:75],a[192:195],v[164:167],v[72:75]
TR a[148:149],v232 offset:8704
TR a[150:151],v232 offset:12800
MM v[80:83],a[192:195],v[168:171],v[136:139]
EX v69,v69
MM v[80:83],a[196:199],v[184:187],v[80:83]
TR a[152:153],v233 offset:512
CV v66,v68,v69
MM v[80:83],a[200:203],v[200:203],v[80:83]
EX v70,v70
MM v[80:83],a[204:207],v[216:219],v[80:83]
TR a[154:155],v233 offset:4608
TR a[156:157],v233 offset:8704
MM v[84:87],a[220:223],v[216:219],v[136:139]
s_add_u32 s87, s95, 4096
buffer_store_dword v240, v248, s[20:23], s87 offen offset:12 sc1
MM v[84:87],a[216:219],v[200:203],v[84:87]
EX v71,v71
MM v[84:87],a[212:215],v[184:187],v[84:87]
TR a[158:159],v233 offset:12800
CV v67,v70,v71
MM v[84:87],a[208:211],v[168:171],v[84:87]
EX v72,v72
MM v[92:95],a[208:211],v[172:175],v[140:143]
EX v73,v73
MM v[92:95],a[212:215],v[188:191],v[92:95]
TR a[160:161],v232 offset:1024
CV v72,v72,v73
MM v[92:95],a[216:219],v[204:207],v[92:95]
EX v74,v74
MM v[92:95],a[220:223],v[220:223],v[92:95]
EX v75,v75
MM v[88:91],a[204:207],v[220:223],v[140:143]
TR a[162:163],v232 offset:5120
CV v73,v74,v75
MM v[88:91],a[200:203],v[204:207],v[88:91]
TR a[164:165],v232 offset:9216
TR a[166:167],v232 offset:13312
MM v[88:91],a[196:199],v[188:191],v[88:91]
EX v76,v76
MM v[88:91],a[192:195],v[172:175],v[88:91]
EX v77,v77
MM v[96:99],a[224:227],v[160:163],v[128:131]
TR a[168:169],v233 offset:1024
CV v74,v76,v77
MM v[96:99],a[228:231],v[176:179],v[96:99]
EX v78,v78
MM v[96:99],a[232:235],v[192:195],v[96:99]
TR a[170:171],v233 offset:5120
TR a[172:173],v233 offset:9216
MM v[96:99],a[236:239],v[208:211],v[96:99]
EX v79,v79
MM v[100:103],a[252:255],v[208:211],v[128:131]
TR a[174:175],v233 offset:13312
CV v75,v78,v79
MM v[100:103],a[248:251],v[192:195],v[100:103]
EX v80,v80
MM v[100:103],a[244:247],v[176:179],v[100:103]
EX v81,v81
MM v[100:103],a[240:243],v[160:163],v[100:103]
TR a[176:177],v232 offset:1536
CV v80,v80,v81
MM v[108:111],a[240:243],v[164:167],v[132:135]
EX v82,v82
MM v[108:111],a[244:247],v[180:183],v[108:111]
TR a[178:179],v232 offset:5632
TR a[180:181],v232 offset:9728
MM v[108:111],a[248:251],v[196:199],v[108:111]
EX v83,v83
MM v[108:111],a[252:255],v[212:215],v[108:111]
TR a[182:183],v232 offset:13824
CV v81,v82,v83
MM v[104:107],a[236:239],v[212:215],v[132:135]
EX v84,v84
MM v[104:107],a[232:235],v[196:199],v[104:107]
EX v85,v85
MM v[104:107],a[228:231],v[180:183],v[104:107]
TR a[184:185],v233 offset:1536
CV v82,v84,v85
MM v[104:107],a[224:227],v[164:167],v[104:107]
EX v86,v86
MM v[112:115],a[224:227],v[168:171],v[136:139]
TR a[186:187],v233 offset:5632
TR a[188:189],v233 offset:9728
MM v[112:115],a[228:231],v[184:187],v[112:115]
EX v87,v87
MM v[112:115],a[232:235],v[200:203],v[112:115]
TR a[190:191],v233 offset:13824
CV v83,v86,v87
MM v[112:115],a[236:239],v[216:219],v[112:115]
EX v88,v88
MM v[116:119],a[252:255],v[216:219],v[136:139]
EX v89,v89
MM v[116:119],a[248:251],v[200:203],v[116:119]
EX v90,v90
MM v[116:119],a[244:247],v[184:187],v[116:119]
EX v91,v91
MM v[116:119],a[240:243],v[168:171],v[116:119]
EX v92,v92
MM v[124:127],a[240:243],v[172:175],v[140:143]
EX v93,v93
MM v[124:127],a[244:247],v[188:191],v[124:127]
EX v94,v94
MM v[124:127],a[248:251],v[204:207],v[124:127]
EX v95,v95
MM v[124:127],a[252:255],v[220:223],v[124:127]
EX v96,v96
MM v[120:123],a[236:239],v[220:223],v[140:143]
EX v97,v97
MM v[120:123],a[232:235],v[204:207],v[120:123]
EX v98,v98
MM v[120:123],a[228:231],v[188:191],v[120:123]
EX v99,v99
MM v[120:123],a[224:227],v[172:175],v[120:123]
s_waitcnt vmcnt(16) lgkmcnt(0)
s_barrier
MM a[0:3],a[128:131],v[0:3],a[0:3]
s_add_u32 m0, s65, 0
CV v88,v88,v89
CV v89,v90,v91
MM a[0:3],a[132:135],v[4:7],a[0:3]
RD a[192:195],v224 offset:32768
CV v90,v92,v93
MM a[8:11],a[132:135],v[12:15],a[8:11]
RD a[196:199],v225 offset:32768
CV v91,v94,v95
MM a[8:11],a[128:131],v[8:11],a[8:11]
buffer_load_dwordx4 v228, s[8:11], s64 offen offset:0 lds
MM a[16:19],a[128:131],v[16:19],a[16:19]
RD a[200:203],v226 offset:32768
CV v68,v96,v97
MM a[16:19],a[132:135],v[20:23],a[16:19]
RD a[204:207],v227 offset:32768
CV v69,v98,v99
MM a[24:27],a[132:135],v[28:31],a[24:27]
buffer_load_dwordx4 v229, s[8:11], s64 offen offset:1024 lds
MM a[24:27],a[128:131],v[24:27],a[24:27]
RD a[208:211],v224 offset:36864
RD a[212:215],v225 offset:36864
MM a[28:31],a[136:139],v[24:27],a[28:31]
buffer_load_dwordx4 v230, s[8:11], s64 offen offset:2048 lds
MM a[28:31],a[140:143],v[28:31],a[28:31]
EX v100,v100
MM a[20:23],a[140:143],v[20:23],a[20:23]
EX v101,v101
MM a[20:23],a[136:139],v[16:19],a[20:23]
RD a[216:219],v226 offset:36864
CV v70,v100,v101
MM a[12:15],a[136:139],v[8:11],a[12:15]
EX v102,v102
MM a[12:15],a[140:143],v[12:15],a[12:15]
RD a[220:223],v227 offset:36864
RD a[224:227],v224 offset:40960
MM a[4:7],a[140:143],v[4:7],a[4:7]
EX v103,v103
MM a[4:7],a[136:139],v[0:3],a[4:7]
RD a[228:231],v225 offset:40960
CV v71,v102,v103
MM a[32:35],a[144:147],v[0:3],a[32:35]
EX v104,v104
MM a[32:35],a[148:151],v[4:7],a[32:35]
buffer_load_dwordx4 v231, s[8:11], s64 offen offset:3072 lds
MM a[40:43],a[148:151],v[12:15],a[40:43]
s_add_u32 m0, s65, 65536
EX v105,v105
MM a[40:43],a[144:147],v[8:11],a[40:43]
RD a[232:235],v226 offset:40960
CV v76,v104,v105
MM a[48:51],a[144:147],v[16:19],a[48:51]
EX v106,v106
MM a[48:51],a[148:151],v[20:23],a[48:51]
EX v107,v107
MM a[56:59],a[148:151],v[28:31],a[56:59]
RD a[236:239],v227 offset:40960
CV v77,v106,v107
MM a[56:59],a[144:147],v[24:27],a[56:59]
EX v108,v108
MM a[60:63],a[152:155],v[24:27],a[60:63]
buffer_load_dwordx4 v235, s[12:15], s81 offen offset:0 lds
MM a[60:63],a[156:159],v[28:31],a[60:63]
EX v109,v109
MM a[52:55],a[156:159],v[20:23],a[52:55]
RD a[240:243],v224 offset:45056
CV v78,v108,v109
MM a[52:55],a[152:155],v[16:19],a[52:55]
EX v110,v110
MM a[44:47],a[152:155],v[8:11],a[44:47]
EX v111,v111
MM a[44:47],a[156:159],v[12:15],a[44:47]
RD a[244:247],v225 offset:45056
CV v79,v110,v111
MM a[36:39],a[156:159],v[4:7],a[36:39]
buffer_load_dwordx4 v235, s[12:15], s64 offen offset:1024 lds
MM a[36:39],a[152:155],v[0:3],a[36:39]
EX v112,v112
MM a[64:67],a[160:163],v[0:3],a[64:67]
EX v113,v113
MM a[64:67],a[164:167],v[4:7],a[64:67]
RD a[248:251],v226 offset:45056
CV v84,v112,v113
MM a[72:75],a[164:167],v[12:15],a[72:75]
EX v114,v114
MM a[72:75],a[160:163],v[8:11],a[72:75]
EX v115,v115
MM a[80:83],a[160:163],v[16:19],a[80:83]
RD a[252:255],v227 offset:45056
CV v85,v114,v115
MM a[80:83],a[164:167],v[20:23],a[80:83]
buffer_load_dwordx4 v235, s[12:15], s81 offen offset:2048 lds
MM a[88:91],a[164:167],v[28:31],a[88:91]
EX v116,v116
MM a[88:91],a[160:163],v[24:27],a[88:91]
EX v117,v117
MM a[92:95],a[168:171],v[24:27],a[92:95]
EX v118,v118
MM a[92:95],a[172:175],v[28:31],a[92:95]
buffer_load_dwordx4 v235, s[12:15], s64 offen offset:3072 lds
MM a[84:87],a[172:175],v[20:23],a[84:87]
EX v119,v119
MM a[84:87],a[168:171],v[16:19],a[84:87]
EX v120,v120
MM a[76:79],a[168:171],v[8:11],a[76:79]
EX v121,v121
MM a[76:79],a[172:175],v[12:15],a[76:79]
EX v122,v122
)ASM"
R"ASM(
MM a[68:71],a[172:175],v[4:7],a[68:71]
EX v123,v123
MM a[68:71],a[168:171],v[0:3],a[68:71]
EX v124,v124
MM a[96:99],a[176:179],v[0:3],a[96:99]
EX v125,v125
MM a[96:99],a[180:183],v[4:7],a[96:99]
EX v126,v126
MM a[104:107],a[180:183],v[12:15],a[104:107]
EX v127,v127
MM a[104:107],a[176:179],v[8:11],a[104:107]
CV v86,v116,v117
CV v87,v118,v119
MM a[112:115],a[176:179],v[16:19],a[112:115]
CV v92,v120,v121
CV v93,v122,v123
MM a[112:115],a[180:183],v[20:23],a[112:115]
CV v94,v124,v125
CV v95,v126,v127
MM a[120:123],a[180:183],v[28:31],a[120:123]
MM a[120:123],a[176:179],v[24:27],a[120:123]
MM a[124:127],a[184:187],v[24:27],a[124:127]
v_add_f32 v244, v144, v145
v_add_f32 v245, v148, v149
MM a[124:127],a[188:191],v[28:31],a[124:127]
v_add_f32 v246, v152, v153
v_add_f32 v247, v156, v157
MM a[116:119],a[188:191],v[20:23],a[116:119]
MM a[116:119],a[184:187],v[16:19],a[116:119]
MM a[108:111],a[184:187],v[8:11],a[108:111]
MM a[108:111],a[188:191],v[12:15],a[108:111]
MM a[100:103],a[188:191],v[4:7],a[100:103]
MM a[100:103],a[184:187],v[0:3],a[100:103]
v_smfmac_f32_16x16x64_bf16 v[144:147], v[236:239], v[64:71], v234
v_smfmac_f32_16x16x64_bf16 v[148:151], v[236:239], v[72:79], v234
v_smfmac_f32_16x16x64_bf16 v[152:155], v[236:239], v[80:87], v234
v_smfmac_f32_16x16x64_bf16 v[156:159], v[236:239], v[88:95], v234
s_waitcnt lgkmcnt(0)
MM v[0:3],a[192:195],v[160:163],v[128:131]
s_add_u32 s64, s64, 16384
TR a[128:129],v232 offset:16384
s_add_u32 s81, s64, 896
MM v[0:3],a[196:199],v[176:179],v[0:3]
TR a[130:131],v232 offset:20480
v_max3_f32 v249, v144, v145, v148
MM v[0:3],a[200:203],v[192:195],v[0:3]
TR a[132:133],v232 offset:24576
v_max3_f32 v249, v249, v149, v152
MM v[0:3],a[204:207],v[208:211],v[0:3]
TR a[134:135],v232 offset:28672
v_max3_f32 v249, v249, v153, v156
MM v[4:7],a[220:223],v[208:211],v[128:131]
TR a[136:137],v233 offset:16384
v_max_f32 v249, v249, v157
MM v[4:7],a[216:219],v[192:195],v[4:7]
TR a[138:139],v233 offset:20480
v_cmp_lt_f32 vcc, s71, v249
MM v[4:7],a[212:215],v[176:179],v[4:7]
TR a[140:141],v233 offset:24576
TR a[142:143],v233 offset:28672
MM v[4:7],a[208:211],v[160:163],v[4:7]
s_cbranch_vccnz LSITE25_%=
LBACK25_%=:
MM v[12:15],a[208:211],v[164:167],v[132:135]
s_waitcnt vmcnt(9)
EX v0,v0
MM v[12:15],a[212:215],v[180:183],v[12:15]
s_mul_i32 s76, s46, 96
EX v1,v1
MM v[12:15],a[216:219],v[196:199],v[12:15]
s_add_u32 s76, s76, 69632
v_lshrrev_b32 v253, 2, v248
v_min_u32 v253, 92, v253
MM v[12:15],a[220:223],v[212:215],v[12:15]
buffer_load_dword v252, v253, s[20:23], s76 offen offset:0 sc1
MM v[8:11],a[204:207],v[212:215],v[132:135]
EX v2,v2
MM v[8:11],a[200:203],v[196:199],v[8:11]
TR a[144:145],v232 offset:16896
CV v0,v0,v1
MM v[8:11],a[196:199],v[180:183],v[8:11]
EX v3,v3
MM v[8:11],a[192:195],v[164:167],v[8:11]
TR a[146:147],v232 offset:20992
CV v1,v2,v3
MM v[16:19],a[192:195],v[168:171],v[136:139]
EX v4,v4
MM v[16:19],a[196:199],v[184:187],v[16:19]
TR a[148:149],v232 offset:25088
TR a[150:151],v232 offset:29184
MM v[16:19],a[200:203],v[200:203],v[16:19]
EX v5,v5
MM v[16:19],a[204:207],v[216:219],v[16:19]
TR a[152:153],v233 offset:16896
CV v2,v4,v5
MM v[20:23],a[220:223],v[216:219],v[136:139]
EX v6,v6
MM v[20:23],a[216:219],v[200:203],v[20:23]
EX v7,v7
MM v[20:23],a[212:215],v[184:187],v[20:23]
TR a[154:155],v233 offset:20992
CV v3,v6,v7
MM v[20:23],a[208:211],v[168:171],v[20:23]
EX v8,v8
MM v[28:31],a[208:211],v[172:175],v[140:143]
TR a[156:157],v233 offset:25088
TR a[158:159],v233 offset:29184
MM v[28:31],a[212:215],v[188:191],v[28:31]
EX v9,v9
MM v[28:31],a[216:219],v[204:207],v[28:31]
TR a[160:161],v232 offset:17408
CV v8,v8,v9
MM v[28:31],a[220:223],v[220:223],v[28:31]
EX v10,v10
MM v[24:27],a[204:207],v[220:223],v[140:143]
EX v11,v11
MM v[24:27],a[200:203],v[204:207],v[24:27]
TR a[162:163],v232 offset:21504
CV v9,v10,v11
MM v[24:27],a[196:199],v[188:191],v[24:27]
EX v12,v12
MM v[24:27],a[192:195],v[172:175],v[24:27]
TR a[164:165],v232 offset:25600
TR a[166:167],v232 offset:29696
MM v[32:35],a[224:227],v[160:163],v[128:131]
EX v13,v13
MM v[32:35],a[228:231],v[176:179],v[32:35]
TR a[168:169],v233 offset:17408
CV v10,v12,v13
MM v[32:35],a[232:235],v[192:195],v[32:35]
EX v14,v14
MM v[32:35],a[236:239],v[208:211],v[32:35]
EX v15,v15
MM v[36:39],a[252:255],v[208:211],v[128:131]
TR a[170:171],v233 offset:21504
CV v11,v14,v15
MM v[36:39],a[248:251],v[192:195],v[36:39]
EX v16,v16
MM v[36:39],a[244:247],v[176:179],v[36:39]
TR a[172:173],v233 offset:25600
TR a[174:175],v233 offset:29696
MM v[36:39],a[240:243],v[160:163],v[36:39]
EX v17,v17
MM v[44:47],a[240:243],v[164:167],v[132:135]
TR a[176:177],v232 offset:17920
CV v16,v16,v17
MM v[44:47],a[244:247],v[180:183],v[44:47]
EX v18,v18
MM v[44:47],a[248:251],v[196:199],v[44:47]
TR a[178:179],v232 offset:22016
TR a[180:181],v232 offset:26112
MM v[44:47],a[252:255],v[212:215],v[44:47]
EX v19,v19
MM v[40:43],a[236:239],v[212:215],v[132:135]
TR a[182:183],v232 offset:30208
CV v17,v18,v19
MM v[40:43],a[232:235],v[196:199],v[40:43]
EX v20,v20
MM v[40:43],a[228:231],v[180:183],v[40:43]
EX v21,v21
MM v[40:43],a[224:227],v[164:167],v[40:43]
TR a[184:185],v233 offset:17920
CV v18,v20,v21
MM v[48:51],a[224:227],v[168:171],v[136:139]
EX v22,v22
MM v[48:51],a[228:231],v[184:187],v[48:51]
TR a[186:187],v233 offset:22016
TR a[188:189],v233 offset:26112
MM v[48:51],a[232:235],v[200:203],v[48:51]
EX v23,v23
MM v[48:51],a[236:239],v[216:219],v[48:51]
TR a[190:191],v233 offset:30208
CV v19,v22,v23
MM v[52:55],a[252:255],v[216:219],v[136:139]
EX v24,v24
MM v[52:55],a[248:251],v[200:203],v[52:55]
EX v25,v25
MM v[52:55],a[244:247],v[184:187],v[52:55]
EX v26,v26
MM v[52:55],a[240:243],v[168:171],v[52:55]
EX v27,v27
MM v[60:63],a[240:243],v[172:175],v[140:143]
EX v28,v28
MM v[60:63],a[244:247],v[188:191],v[60:63]
EX v29,v29
MM v[60:63],a[248:251],v[204:207],v[60:63]
EX v30,v30
MM v[60:63],a[252:255],v[220:223],v[60:63]
EX v31,v31
MM v[56:59],a[236:239],v[220:223],v[140:143]
EX v32,v32
MM v[56:59],a[232:235],v[204:207],v[56:59]
EX v33,v33
MM v[56:59],a[228:231],v[188:191],v[56:59]
EX v34,v34
MM v[56:59],a[224:227],v[172:175],v[56:59]
s_waitcnt lgkmcnt(0)
s_barrier
MM a[0:3],a[128:131],v[64:67],a[0:3]
s_add_u32 m0, s65, 16384
CV v24,v24,v25
CV v25,v26,v27
MM a[0:3],a[132:135],v[68:71],a[0:3]
RD a[192:195],v224 offset:49152
CV v26,v28,v29
MM a[8:11],a[132:135],v[76:79],a[8:11]
RD a[196:199],v225 offset:49152
CV v27,v30,v31
MM a[8:11],a[128:131],v[72:75],a[8:11]
buffer_load_dwordx4 v228, s[8:11], s64 offen offset:0 lds
MM a[16:19],a[128:131],v[80:83],a[16:19]
RD a[200:203],v226 offset:49152
CV v4,v32,v33
MM a[16:19],a[132:135],v[84:87],a[16:19]
buffer_load_dwordx4 v229, s[8:11], s64 offen offset:1024 lds
MM a[24:27],a[132:135],v[92:95],a[24:27]
RD a[204:207],v227 offset:49152
RD a[208:211],v224 offset:53248
MM a[24:27],a[128:131],v[88:91],a[24:27]
EX v35,v35
MM a[28:31],a[136:139],v[88:91],a[28:31]
RD a[212:215],v225 offset:53248
CV v5,v34,v35
MM a[28:31],a[140:143],v[92:95],a[28:31]
EX v36,v36
MM a[20:23],a[140:143],v[84:87],a[20:23]
buffer_load_dwordx4 v230, s[8:11], s64 offen offset:2048 lds
MM a[20:23],a[136:139],v[80:83],a[20:23]
EX v37,v37
MM a[12:15],a[136:139],v[72:75],a[12:15]
RD a[216:219],v226 offset:53248
CV v6,v36,v37
MM a[12:15],a[140:143],v[76:79],a[12:15]
EX v38,v38
MM a[4:7],a[140:143],v[68:71],a[4:7]
EX v39,v39
MM a[4:7],a[136:139],v[64:67],a[4:7]
RD a[220:223],v227 offset:53248
CV v7,v38,v39
MM a[32:35],a[144:147],v[64:67],a[32:35]
EX v40,v40
MM a[32:35],a[148:151],v[68:71],a[32:35]
buffer_load_dwordx4 v231, s[8:11], s64 offen offset:3072 lds
MM a[40:43],a[148:151],v[76:79],a[40:43]
s_add_u32 m0, s65, 81920
EX v41,v41
MM a[40:43],a[144:147],v[72:75],a[40:43]
RD a[224:227],v224 offset:57344
CV v12,v40,v41
MM a[48:51],a[144:147],v[80:83],a[48:51]
EX v42,v42
MM a[48:51],a[148:151],v[84:87],a[48:51]
EX v43,v43
MM a[56:59],a[148:151],v[92:95],a[56:59]
RD a[228:231],v225 offset:57344
CV v13,v42,v43
MM a[56:59],a[144:147],v[88:91],a[56:59]
EX v44,v44
MM a[60:63],a[152:155],v[88:91],a[60:63]
buffer_load_dwordx4 v235, s[12:15], s81 offen offset:0 lds
MM a[60:63],a[156:159],v[92:95],a[60:63]
EX v45,v45
MM a[52:55],a[156:159],v[84:87],a[52:55]
RD a[232:235],v226 offset:57344
CV v14,v44,v45
MM a[52:55],a[152:155],v[80:83],a[52:55]
EX v46,v46
MM a[44:47],a[152:155],v[72:75],a[44:47]
EX v47,v47
MM a[44:47],a[156:159],v[76:79],a[44:47]
RD a[236:239],v227 offset:57344
CV v15,v46,v47
MM a[36:39],a[156:159],v[68:71],a[36:39]
buffer_load_dwordx4 v235, s[12:15], s64 offen offset:1024 lds
MM a[36:39],a[152:155],v[64:67],a[36:39]
EX v48,v48
MM a[64:67],a[160:163],v[64:67],a[64:67]
EX v49,v49
MM a[64:67],a[164:167],v[68:71],a[64:67]
RD a[240:243],v224 offset:61440
CV v20,v48,v49
MM a[72:75],a[164:167],v[76:79],a[72:75]
EX v50,v50
MM a[72:75],a[160:163],v[72:75],a[72:75]
RD a[244:247],v225 offset:61440
RD a[248:251],v226 offset:61440
MM a[80:83],a[160:163],v[80:83],a[80:83]
EX v51,v51
MM a[80:83],a[164:167],v[84:87],a[80:83]
LPG19_%=:
s_cbranch_execz LTR3_%=
RD a[252:255],v227 offset:61440
CV v21,v50,v51
MM a[88:91],a[164:167],v[92:95],a[88:91]
EX v52,v52
MM a[88:91],a[160:163],v[88:91],a[88:91]
buffer_load_dwordx4 v235, s[12:15], s81 offen offset:2048 lds
MM a[92:95],a[168:171],v[88:91],a[92:95]
EX v53,v53
MM a[92:95],a[172:175],v[92:95],a[92:95]
EX v54,v54
MM a[84:87],a[172:175],v[84:87],a[84:87]
buffer_load_dwordx4 v235, s[12:15], s64 offen offset:3072 lds
MM a[84:87],a[168:171],v[80:83],a[84:87]
EX v55,v55
MM a[76:79],a[168:171],v[72:75],a[76:79]
EX v56,v56
MM a[76:79],a[172:175],v[76:79],a[76:79]
EX v57,v57
MM a[68:71],a[172:175],v[68:71],a[68:71]
EX v58,v58
MM a[68:71],a[168:171],v[64:67],a[68:71]
EX v59,v59
MM a[96:99],a[176:179],v[64:67],a[96:99]
EX v60,v60
MM a[96:99],a[180:183],v[68:71],a[96:99]
EX v61,v61
MM a[104:107],a[180:183],v[76:79],a[104:107]
EX v62,v62
MM a[104:107],a[176:179],v[72:75],a[104:107]
EX v63,v63
MM a[112:115],a[176:179],v[80:83],a[112:115]
CV v22,v52,v53
CV v23,v54,v55
MM a[112:115],a[180:183],v[84:87],a[112:115]
CV v28,v56,v57
CV v29,v58,v59
MM a[120:123],a[180:183],v[92:95],a[120:123]
s_waitcnt vmcnt(8)
v_cmp_ne_u32 vcc, s69, v252
s_cbranch_vccnz LRFH_%=
LRFHb_%=:
MM a[120:123],a[176:179],v[88:91],a[120:123]
v_bfe_u32 v253, v248, 4, 3
v_mul_u32_u24 v253, 3456, v253
MM a[124:127],a[184:187],v[88:91],a[124:127]
v_add_f32 v244, v144, v145
v_add_f32 v245, v148, v149
MM a[124:127],a[188:191],v[92:95],a[124:127]
v_add_f32 v246, v152, v153
v_add_f32 v247, v156, v157
v_lshrrev_b32 v252, 7, v248
MM a[116:119],a[188:191],v[84:87],a[116:119]
v_lshl_add_u32 v253, v252, 4, v253
CV v30,v60,v61
MM a[116:119],a[184:187],v[80:83],a[116:119]
s_min_u32 s90, %[swave], 2
s_mul_i32 s76, s46, 384
CV v31,v62,v63
MM a[108:111],a[184:187],v[72:75],a[108:111]
s_lshl_b32 s91, s90, 7
s_add_u32 s76, s76, s91
s_add_u32 s76, s76, 21626880
MM a[108:111],a[188:191],v[76:79],a[108:111]
s_lshl_b32 s90, s90, 10
s_add_u32 s90, s90, %[slds]
MM a[100:103],a[188:191],v[68:71],a[100:103]
s_add_u32 m0, s90, 131072
s_nop 0
buffer_load_dwordx4 v253, s[20:23], s76 offen offset:0 sc1 lds
MM a[100:103],a[184:187],v[64:67],a[100:103]
v_smfmac_f32_16x16x64_bf16 v[144:147], v[236:239], v[0:7], v234
v_smfmac_f32_16x16x64_bf16 v[148:151], v[236:239], v[8:15], v234
v_smfmac_f32_16x16x64_bf16 v[152:155], v[236:239], v[16:23], v234
v_smfmac_f32_16x16x64_bf16 v[156:159], v[236:239], v[24:31], v234
s_waitcnt lgkmcnt(0)
MM v[64:67],a[192:195],v[160:163],v[128:131]
s_add_u32 s64, s64, 16384
TR a[128:129],v232 offset:32768
s_add_u32 s81, s64, 896
MM v[64:67],a[196:199],v[176:179],v[64:67]
TR a[130:131],v232 offset:36864
v_max3_f32 v249, v144, v145, v148
MM v[64:67],a[200:203],v[192:195],v[64:67]
TR a[132:133],v232 offset:40960
v_max3_f32 v249, v249, v149, v152
MM v[64:67],a[204:207],v[208:211],v[64:67]
TR a[134:135],v232 offset:45056
v_max3_f32 v249, v249, v153, v156
MM v[68:71],a[220:223],v[208:211],v[128:131]
TR a[136:137],v233 offset:32768
v_max_f32 v249, v249, v157
MM v[68:71],a[216:219],v[192:195],v[68:71]
TR a[138:139],v233 offset:36864
v_cmp_lt_f32 vcc, s71, v249
MM v[68:71],a[212:215],v[176:179],v[68:71]
)ASM"
R"ASM(
TR a[140:141],v233 offset:40960
TR a[142:143],v233 offset:45056
MM v[68:71],a[208:211],v[160:163],v[68:71]
s_cbranch_vccnz LSITE26_%=
LBACK26_%=:
MM v[76:79],a[208:211],v[164:167],v[132:135]
EX v64,v64
MM v[76:79],a[212:215],v[180:183],v[76:79]
EX v65,v65
MM v[76:79],a[216:219],v[196:199],v[76:79]
TR a[144:145],v232 offset:33280
CV v64,v64,v65
MM v[76:79],a[220:223],v[212:215],v[76:79]
EX v66,v66
MM v[72:75],a[204:207],v[212:215],v[132:135]
EX v67,v67
MM v[72:75],a[200:203],v[196:199],v[72:75]
TR a[146:147],v232 offset:37376
CV v65,v66,v67
MM v[72:75],a[196:199],v[180:183],v[72:75]
EX v68,v68
MM v[72:75],a[192:195],v[164:167],v[72:75]
TR a[148:149],v232 offset:41472
TR a[150:151],v232 offset:45568
MM v[80:83],a[192:195],v[168:171],v[136:139]
EX v69,v69
MM v[80:83],a[196:199],v[184:187],v[80:83]
TR a[152:153],v233 offset:33280
CV v66,v68,v69
MM v[80:83],a[200:203],v[200:203],v[80:83]
EX v70,v70
MM v[80:83],a[204:207],v[216:219],v[80:83]
TR a[154:155],v233 offset:37376
TR a[156:157],v233 offset:41472
MM v[84:87],a[220:223],v[216:219],v[136:139]
EX v71,v71
MM v[84:87],a[216:219],v[200:203],v[84:87]
TR a[158:159],v233 offset:45568
CV v67,v70,v71
MM v[84:87],a[212:215],v[184:187],v[84:87]
EX v72,v72
MM v[84:87],a[208:211],v[168:171],v[84:87]
EX v73,v73
MM v[92:95],a[208:211],v[172:175],v[140:143]
TR a[160:161],v232 offset:33792
CV v72,v72,v73
MM v[92:95],a[212:215],v[188:191],v[92:95]
EX v74,v74
MM v[92:95],a[216:219],v[204:207],v[92:95]
TR a[162:163],v232 offset:37888
TR a[164:165],v232 offset:41984
MM v[92:95],a[220:223],v[220:223],v[92:95]
EX v75,v75
MM v[88:91],a[204:207],v[220:223],v[140:143]
TR a[166:167],v232 offset:46080
CV v73,v74,v75
MM v[88:91],a[200:203],v[204:207],v[88:91]
EX v76,v76
MM v[88:91],a[196:199],v[188:191],v[88:91]
EX v77,v77
MM v[88:91],a[192:195],v[172:175],v[88:91]
TR a[168:169],v233 offset:33792
CV v74,v76,v77
MM v[96:99],a[224:227],v[160:163],v[128:131]
EX v78,v78
MM v[96:99],a[228:231],v[176:179],v[96:99]
TR a[170:171],v233 offset:37888
TR a[172:173],v233 offset:41984
MM v[96:99],a[232:235],v[192:195],v[96:99]
EX v79,v79
MM v[96:99],a[236:239],v[208:211],v[96:99]
TR a[174:175],v233 offset:46080
CV v75,v78,v79
MM v[100:103],a[252:255],v[208:211],v[128:131]
EX v80,v80
MM v[100:103],a[248:251],v[192:195],v[100:103]
EX v81,v81
MM v[100:103],a[244:247],v[176:179],v[100:103]
TR a[176:177],v232 offset:34304
CV v80,v80,v81
MM v[100:103],a[240:243],v[160:163],v[100:103]
EX v82,v82
MM v[108:111],a[240:243],v[164:167],v[132:135]
TR a[178:179],v232 offset:38400
TR a[180:181],v232 offset:42496
MM v[108:111],a[244:247],v[180:183],v[108:111]
EX v83,v83
MM v[108:111],a[248:251],v[196:199],v[108:111]
TR a[182:183],v232 offset:46592
CV v81,v82,v83
MM v[108:111],a[252:255],v[212:215],v[108:111]
EX v84,v84
MM v[104:107],a[236:239],v[212:215],v[132:135]
TR a[184:185],v233 offset:34304
TR a[186:187],v233 offset:38400
MM v[104:107],a[232:235],v[196:199],v[104:107]
EX v85,v85
MM v[104:107],a[228:231],v[180:183],v[104:107]
TR a[188:189],v233 offset:42496
CV v82,v84,v85
MM v[104:107],a[224:227],v[164:167],v[104:107]
EX v86,v86
MM v[112:115],a[224:227],v[168:171],v[136:139]
EX v87,v87
MM v[112:115],a[228:231],v[184:187],v[112:115]
TR a[190:191],v233 offset:46592
CV v83,v86,v87
MM v[112:115],a[232:235],v[200:203],v[112:115]
EX v88,v88
MM v[112:115],a[236:239],v[216:219],v[112:115]
EX v89,v89
MM v[116:119],a[252:255],v[216:219],v[136:139]
EX v90,v90
MM v[116:119],a[248:251],v[200:203],v[116:119]
EX v91,v91
MM v[116:119],a[244:247],v[184:187],v[116:119]
EX v92,v92
MM v[116:119],a[240:243],v[168:171],v[116:119]
EX v93,v93
MM v[124:127],a[240:243],v[172:175],v[140:143]
EX v94,v94
MM v[124:127],a[244:247],v[188:191],v[124:127]
EX v95,v95
MM v[124:127],a[248:251],v[204:207],v[124:127]
EX v96,v96
MM v[124:127],a[252:255],v[220:223],v[124:127]
EX v97,v97
MM v[120:123],a[236:239],v[220:223],v[140:143]
EX v98,v98
MM v[120:123],a[232:235],v[204:207],v[120:123]
EX v99,v99
MM v[120:123],a[228:231],v[188:191],v[120:123]
s_getpc_b64 s[98:99]
LTA4_%=:
s_min_u32 s73, %[swave], 1
s_lshl_b32 s73, s73, 12
s_add_u32 s73, s73, LPG21_%=-LTA4_%=
s_add_u32 s98, s98, s73
s_addc_u32 s99, s99, 0
s_mov_b64 exec, 0
s_setpc_b64 s[98:99]
LTR4_%=:
s_mov_b64 exec, -1
CV v88,v88,v89
MM v[120:123],a[224:227],v[172:175],v[120:123]
s_waitcnt lgkmcnt(0)
s_barrier
MM a[0:3],a[128:131],v[0:3],a[0:3]
s_add_u32 m0, s65, 32768
CV v89,v90,v91
CV v90,v92,v93
MM a[0:3],a[132:135],v[4:7],a[0:3]
RD a[192:195],v224 offset:0
CV v91,v94,v95
MM a[8:11],a[132:135],v[12:15],a[8:11]
buffer_load_dwordx4 v228, s[8:11], s64 offen offset:0 lds
MM a[8:11],a[128:131],v[8:11],a[8:11]
RD a[196:199],v225 offset:0
CV v68,v96,v97
MM a[16:19],a[128:131],v[16:19],a[16:19]
RD a[200:203],v226 offset:0
CV v69,v98,v99
MM a[16:19],a[132:135],v[20:23],a[16:19]
buffer_load_dwordx4 v229, s[8:11], s64 offen offset:1024 lds
MM a[24:27],a[132:135],v[28:31],a[24:27]
RD a[204:207],v227 offset:0
RD a[208:211],v224 offset:4096
MM a[24:27],a[128:131],v[24:27],a[24:27]
RD a[212:215],v225 offset:4096
RD a[216:219],v226 offset:4096
MM a[28:31],a[136:139],v[24:27],a[28:31]
buffer_load_dwordx4 v230, s[8:11], s64 offen offset:2048 lds
MM a[28:31],a[140:143],v[28:31],a[28:31]
EX v100,v100
MM a[20:23],a[140:143],v[20:23],a[20:23]
EX v101,v101
MM a[20:23],a[136:139],v[16:19],a[20:23]
RD a[220:223],v227 offset:4096
CV v70,v100,v101
MM a[12:15],a[136:139],v[8:11],a[12:15]
EX v102,v102
MM a[12:15],a[140:143],v[12:15],a[12:15]
buffer_load_dwordx4 v231, s[8:11], s64 offen offset:3072 lds
MM a[4:7],a[140:143],v[4:7],a[4:7]
s_add_u32 m0, s65, 98304
EX v103,v103
MM a[4:7],a[136:139],v[0:3],a[4:7]
RD a[224:227],v224 offset:8192
CV v71,v102,v103
MM a[32:35],a[144:147],v[0:3],a[32:35]
EX v104,v104
MM a[32:35],a[148:151],v[4:7],a[32:35]
EX v105,v105
MM a[40:43],a[148:151],v[12:15],a[40:43]
RD a[228:231],v225 offset:8192
CV v76,v104,v105
MM a[40:43],a[144:147],v[8:11],a[40:43]
EX v106,v106
MM a[48:51],a[144:147],v[16:19],a[48:51]
buffer_load_dwordx4 v235, s[12:15], s81 offen offset:0 lds
MM a[48:51],a[148:151],v[20:23],a[48:51]
EX v107,v107
MM a[56:59],a[148:151],v[28:31],a[56:59]
RD a[232:235],v226 offset:8192
CV v77,v106,v107
MM a[56:59],a[144:147],v[24:27],a[56:59]
EX v108,v108
MM a[60:63],a[152:155],v[24:27],a[60:63]
EX v109,v109
MM a[60:63],a[156:159],v[28:31],a[60:63]
RD a[236:239],v227 offset:8192
CV v78,v108,v109
MM a[52:55],a[156:159],v[20:23],a[52:55]
EX v110,v110
MM a[52:55],a[152:155],v[16:19],a[52:55]
buffer_load_dwordx4 v235, s[12:15], s64 offen offset:1024 lds
MM a[44:47],a[152:155],v[8:11],a[44:47]
EX v111,v111
MM a[44:47],a[156:159],v[12:15],a[44:47]
RD a[240:243],v224 offset:12288
CV v79,v110,v111
MM a[36:39],a[156:159],v[4:7],a[36:39]
EX v112,v112
MM a[36:39],a[152:155],v[0:3],a[36:39]
EX v113,v113
MM a[64:67],a[160:163],v[0:3],a[64:67]
RD a[244:247],v225 offset:12288
CV v84,v112,v113
MM a[64:67],a[164:167],v[4:7],a[64:67]
EX v114,v114
MM a[72:75],a[164:167],v[12:15],a[72:75]
buffer_load_dwordx4 v235, s[12:15], s81 offen offset:2048 lds
MM a[72:75],a[160:163],v[8:11],a[72:75]
EX v115,v115
MM a[80:83],a[160:163],v[16:19],a[80:83]
RD a[248:251],v226 offset:12288
CV v85,v114,v115
MM a[80:83],a[164:167],v[20:23],a[80:83]
EX v116,v116
MM a[88:91],a[164:167],v[28:31],a[88:91]
EX v117,v117
MM a[88:91],a[160:163],v[24:27],a[88:91]
RD a[252:255],v227 offset:12288
CV v86,v116,v117
MM a[92:95],a[168:171],v[24:27],a[92:95]
EX v118,v118
MM a[92:95],a[172:175],v[28:31],a[92:95]
buffer_load_dwordx4 v235, s[12:15], s64 offen offset:3072 lds
MM a[84:87],a[172:175],v[20:23],a[84:87]
EX v119,v119
MM a[84:87],a[168:171],v[16:19],a[84:87]
CV v87,v118,v119
MM a[76:79],a[168:171],v[8:11],a[76:79]
EX v120,v120
MM a[76:79],a[172:175],v[12:15],a[76:79]
EX v121,v121
MM a[68:71],a[172:175],v[4:7],a[68:71]
EX v122,v122
MM a[68:71],a[168:171],v[0:3],a[68:71]
EX v123,v123
MM a[96:99],a[176:179],v[0:3],a[96:99]
EX v124,v124
MM a[96:99],a[180:183],v[4:7],a[96:99]
EX v125,v125
MM a[104:107],a[180:183],v[12:15],a[104:107]
EX v126,v126
MM a[104:107],a[176:179],v[8:11],a[104:107]
EX v127,v127
MM a[112:115],a[176:179],v[16:19],a[112:115]
CV v92,v120,v121
CV v93,v122,v123
MM a[112:115],a[180:183],v[20:23],a[112:115]
CV v94,v124,v125
CV v95,v126,v127
MM a[120:123],a[180:183],v[28:31],a[120:123]
MM a[120:123],a[176:179],v[24:27],a[120:123]
MM a[124:127],a[184:187],v[24:27],a[124:127]
v_add_f32 v244, v144, v145
v_add_f32 v245, v148, v149
MM a[124:127],a[188:191],v[28:31],a[124:127]
v_add_f32 v246, v152, v153
v_add_f32 v247, v156, v157
MM a[116:119],a[188:191],v[20:23],a[116:119]
MM a[116:119],a[184:187],v[16:19],a[116:119]
MM a[108:111],a[184:187],v[8:11],a[108:111]
MM a[108:111],a[188:191],v[12:15],a[108:111]
MM a[100:103],a[188:191],v[4:7],a[100:103]
MM a[100:103],a[184:187],v[0:3],a[100:103]
v_smfmac_f32_16x16x64_bf16 v[144:147], v[236:239], v[64:71], v234
v_smfmac_f32_16x16x64_bf16 v[148:151], v[236:239], v[72:79], v234
v_smfmac_f32_16x16x64_bf16 v[152:155], v[236:239], v[80:87], v234
v_smfmac_f32_16x16x64_bf16 v[156:159], v[236:239], v[88:95], v234
s_waitcnt lgkmcnt(0)
MM v[0:3],a[192:195],v[160:163],v[128:131]
s_add_u32 s64, s64, 16384
TR a[128:129],v232 offset:49152
s_add_u32 s81, s64, 896
MM v[0:3],a[196:199],v[176:179],v[0:3]
TR a[130:131],v232 offset:53248
v_max3_f32 v249, v144, v145, v148
MM v[0:3],a[200:203],v[192:195],v[0:3]
TR a[132:133],v232 offset:57344
v_max3_f32 v249, v249, v149, v152
MM v[0:3],a[204:207],v[208:211],v[0:3]
TR a[134:135],v232 offset:61440
v_max3_f32 v249, v249, v153, v156
MM v[4:7],a[220:223],v[208:211],v[128:131]
TR a[136:137],v233 offset:49152
v_max_f32 v249, v249, v157
MM v[4:7],a[216:219],v[192:195],v[4:7]
TR a[138:139],v233 offset:53248
v_cmp_lt_f32 vcc, s71, v249
MM v[4:7],a[212:215],v[176:179],v[4:7]
TR a[140:141],v233 offset:57344
TR a[142:143],v233 offset:61440
MM v[4:7],a[208:211],v[160:163],v[4:7]
s_cbranch_vccnz LSITE27_%=
LBACK27_%=:
MM v[12:15],a[208:211],v[164:167],v[132:135]
v_lshrrev_b32 v125, 8, v248
v_lshlrev_b32 v124, 10, v125
MM v[12:15],a[212:215],v[180:183],v[12:15]
v_bfe_u32 v126, v248, 6, 2
v_lshl_add_u32 v124, v126, 7, v124
MM v[12:15],a[216:219],v[196:199],v[12:15]
v_bfe_u32 v126, v248, 4, 2
v_lshl_add_u32 v124, v126, 3, v124
MM v[12:15],a[220:223],v[212:215],v[12:15]
s_lshl_b32 s91, %[swave], 5
EX v0,v0
MM v[8:11],a[204:207],v[212:215],v[132:135]
s_add_u32 s91, s91, %[slds]
EX v1,v1
MM v[8:11],a[200:203],v[196:199],v[8:11]
s_add_u32 s91, s91, 139264
v_add_u32 v124, s91, v124
v_and_b32 v126, 112, v248
MM v[8:11],a[196:199],v[180:183],v[8:11]
s_add_u32 s91, %[slds], 131072
v_lshl_add_u32 v126, v125, 7, v126
v_add_u32 v126, s91, v126
MM v[8:11],a[192:195],v[164:167],v[8:11]
v_bfe_u32 v127, v248, 4, 3
v_mul_u32_u24 v127, 2560, v127
MM v[16:19],a[192:195],v[168:171],v[136:139]
v_lshl_add_u32 v127, v125, 4, v127
CV v0,v0,v1
EX v2,v2
MM v[16:19],a[196:199],v[184:187],v[16:19]
s_lshl_b32 s90, %[swave], 6
EX v3,v3
MM v[16:19],a[200:203],v[200:203],v[16:19]
s_lshl_b32 s91, s47, 8
EX v4,v4
MM v[16:19],a[204:207],v[216:219],v[16:19]
s_add_u32 s90, s90, s91
TR a[144:145],v232 offset:49664
TR a[146:147],v232 offset:53760
MM v[20:23],a[220:223],v[216:219],v[136:139]
s_mul_i32 s91, s46, 17920
EX v5,v5
MM v[20:23],a[216:219],v[200:203],v[20:23]
s_add_u32 s90, s90, s91
TR a[148:149],v232 offset:57856
CV v1,v2,v3
MM v[20:23],a[212:215],v[184:187],v[20:23]
s_add_u32 s90, s90, 21659648
EX v6,v6
MM v[20:23],a[208:211],v[168:171],v[20:23]
EX v7,v7
MM v[28:31],a[208:211],v[172:175],v[140:143]
TR a[150:151],v232 offset:61952
TR a[152:153],v233 offset:49664
MM v[28:31],a[212:215],v[188:191],v[28:31]
EX v8,v8
MM v[28:31],a[216:219],v[204:207],v[28:31]
TR a[154:155],v233 offset:53760
CV v2,v4,v5
MM v[28:31],a[220:223],v[220:223],v[28:31]
EX v9,v9
MM v[24:27],a[204:207],v[220:223],v[140:143]
TR a[156:157],v233 offset:57856
CV v3,v6,v7
MM v[24:27],a[200:203],v[204:207],v[24:27]
EX v10,v10
MM v[24:27],a[196:199],v[188:191],v[24:27]
TR a[158:159],v233 offset:61952
CV v8,v8,v9
MM v[24:27],a[192:195],v[172:175],v[24:27]
EX v11,v11
MM v[32:35],a[224:227],v[160:163],v[128:131]
)ASM"
R"ASM(
TR a[160:161],v232 offset:50176
TR a[162:163],v232 offset:54272
MM v[32:35],a[228:231],v[176:179],v[32:35]
EX v12,v12
MM v[32:35],a[232:235],v[192:195],v[32:35]
EX v13,v13
MM v[32:35],a[236:239],v[208:211],v[32:35]
TR a[164:165],v232 offset:58368
CV v9,v10,v11
MM v[36:39],a[252:255],v[208:211],v[128:131]
EX v14,v14
MM v[36:39],a[248:251],v[192:195],v[36:39]
TR a[166:167],v232 offset:62464
CV v10,v12,v13
MM v[36:39],a[244:247],v[176:179],v[36:39]
EX v15,v15
MM v[36:39],a[240:243],v[160:163],v[36:39]
TR a[168:169],v233 offset:50176
TR a[170:171],v233 offset:54272
MM v[44:47],a[240:243],v[164:167],v[132:135]
EX v16,v16
MM v[44:47],a[244:247],v[180:183],v[44:47]
EX v17,v17
MM v[44:47],a[248:251],v[196:199],v[44:47]
TR a[172:173],v233 offset:58368
CV v11,v14,v15
MM v[44:47],a[252:255],v[212:215],v[44:47]
EX v18,v18
MM v[40:43],a[236:239],v[212:215],v[132:135]
TR a[174:175],v233 offset:62464
CV v16,v16,v17
MM v[40:43],a[232:235],v[196:199],v[40:43]
EX v19,v19
MM v[40:43],a[228:231],v[180:183],v[40:43]
TR a[176:177],v232 offset:50688
TR a[178:179],v232 offset:54784
MM v[40:43],a[224:227],v[164:167],v[40:43]
EX v20,v20
MM v[48:51],a[224:227],v[168:171],v[136:139]
EX v21,v21
MM v[48:51],a[228:231],v[184:187],v[48:51]
TR a[180:181],v232 offset:58880
CV v17,v18,v19
MM v[48:51],a[232:235],v[200:203],v[48:51]
EX v22,v22
MM v[48:51],a[236:239],v[216:219],v[48:51]
TR a[182:183],v232 offset:62976
CV v18,v20,v21
MM v[52:55],a[252:255],v[216:219],v[136:139]
EX v23,v23
MM v[52:55],a[248:251],v[200:203],v[52:55]
TR a[184:185],v233 offset:50688
TR a[186:187],v233 offset:54784
MM v[52:55],a[244:247],v[184:187],v[52:55]
EX v24,v24
MM v[52:55],a[240:243],v[168:171],v[52:55]
TR a[188:189],v233 offset:58880
CV v19,v22,v23
MM v[60:63],a[240:243],v[172:175],v[140:143]
EX v25,v25
MM v[60:63],a[244:247],v[188:191],v[60:63]
TR a[190:191],v233 offset:62976
CV v24,v24,v25
MM v[60:63],a[248:251],v[204:207],v[60:63]
EX v26,v26
MM v[60:63],a[252:255],v[220:223],v[60:63]
EX v27,v27
MM v[56:59],a[236:239],v[220:223],v[140:143]
EX v28,v28
MM v[56:59],a[232:235],v[204:207],v[56:59]
EX v29,v29
MM v[56:59],a[228:231],v[188:191],v[56:59]
EX v30,v30
MM v[56:59],a[224:227],v[172:175],v[56:59]
s_waitcnt vmcnt(8) lgkmcnt(0)
s_barrier
MM a[0:3],a[128:131],v[64:67],a[0:3]
s_add_u32 m0, s65, 49152
CV v25,v26,v27
CV v26,v28,v29
MM a[0:3],a[132:135],v[68:71],a[0:3]
RD a[192:195],v224 offset:16384
RD a[196:199],v225 offset:16384
MM a[8:11],a[132:135],v[76:79],a[8:11]
EX v31,v31
MM a[8:11],a[128:131],v[72:75],a[8:11]
RD a[200:203],v226 offset:16384
CV v27,v30,v31
MM a[16:19],a[128:131],v[80:83],a[16:19]
TR v[96:97],v124 offset:0
TR v[98:99],v124 offset:512
MM a[16:19],a[132:135],v[84:87],a[16:19]
RD v[108:111],v126 offset:0
TR v[100:101],v124 offset:4096
MM a[24:27],a[132:135],v[92:95],a[24:27]
TR v[102:103],v124 offset:4608
RD v[112:115],v126 offset:512
MM a[24:27],a[128:131],v[88:91],a[24:27]
TR v[104:105],v124 offset:8192
TR v[106:107],v124 offset:8704
MM a[28:31],a[136:139],v[88:91],a[28:31]
RD v[116:119],v126 offset:1024
RD a[204:207],v227 offset:16384
EX v32,v32
MM a[28:31],a[140:143],v[92:95],a[28:31]
EX v33,v33
MM a[20:23],a[140:143],v[84:87],a[20:23]
EX v34,v34
MM a[20:23],a[136:139],v[80:83],a[20:23]
buffer_load_dwordx4 v228, s[8:11], s64 offen offset:0 lds
LPG20_%=:
s_cbranch_execz LTR3_%=
MM a[12:15],a[136:139],v[72:75],a[12:15]
EX v35,v35
MM a[12:15],a[140:143],v[76:79],a[12:15]
EX v36,v36
MM a[4:7],a[140:143],v[68:71],a[4:7]
CV v4,v32,v33
CV v5,v34,v35
MM a[4:7],a[136:139],v[64:67],a[4:7]
EX v37,v37
MM a[32:35],a[144:147],v[64:67],a[32:35]
EX v38,v38
MM a[32:35],a[148:151],v[68:71],a[32:35]
buffer_load_dwordx4 v229, s[8:11], s64 offen offset:1024 lds
MM a[40:43],a[148:151],v[76:79],a[40:43]
EX v39,v39
MM a[40:43],a[144:147],v[72:75],a[40:43]
RD a[208:211],v224 offset:20480
CV v6,v36,v37
MM a[48:51],a[144:147],v[80:83],a[48:51]
s_waitcnt lgkmcnt(2)
EX v40,v40
MM a[48:51],a[148:151],v[84:87],a[48:51]
RD a[212:215],v225 offset:20480
CV v7,v38,v39
MM v[120:123],v[96:99],v[108:111],0
MM v[120:123],v[100:103],v[112:115],v[120:123]
MM v[120:123],v[104:107],v[116:119],v[120:123]
MM a[56:59],a[148:151],v[92:95],a[56:59]
TR v[96:97],v124 offset:12288
TR v[98:99],v124 offset:12800
MM a[56:59],a[144:147],v[88:91],a[56:59]
RD v[108:111],v126 offset:1536
TR v[100:101],v124 offset:16384
MM a[60:63],a[152:155],v[88:91],a[60:63]
TR v[102:103],v124 offset:16896
RD v[112:115],v126 offset:2048
EX v41,v41
MM a[60:63],a[156:159],v[92:95],a[60:63]
TR v[104:105],v124 offset:20480
TR v[106:107],v124 offset:20992
MM a[52:55],a[156:159],v[84:87],a[52:55]
RD v[116:119],v126 offset:2560
CV v12,v40,v41
EX v42,v42
MM a[52:55],a[152:155],v[80:83],a[52:55]
EX v43,v43
MM a[44:47],a[152:155],v[72:75],a[44:47]
buffer_load_dwordx4 v230, s[8:11], s64 offen offset:2048 lds
MM a[44:47],a[156:159],v[76:79],a[44:47]
EX v44,v44
MM a[36:39],a[156:159],v[68:71],a[36:39]
EX v45,v45
MM a[36:39],a[152:155],v[64:67],a[36:39]
RD a[216:219],v226 offset:20480
CV v13,v42,v43
MM a[64:67],a[160:163],v[64:67],a[64:67]
EX v46,v46
MM a[64:67],a[164:167],v[68:71],a[64:67]
EX v47,v47
MM a[72:75],a[164:167],v[76:79],a[72:75]
buffer_load_dwordx4 v231, s[8:11], s64 offen offset:3072 lds
MM a[72:75],a[160:163],v[72:75],a[72:75]
s_add_u32 m0, s65, 114688
RD a[220:223],v227 offset:20480
CV v14,v44,v45
MM a[80:83],a[160:163],v[80:83],a[80:83]
EX v48,v48
MM a[80:83],a[164:167],v[84:87],a[80:83]
EX v49,v49
MM a[88:91],a[164:167],v[92:95],a[88:91]
s_waitcnt lgkmcnt(2)
RD a[224:227],v224 offset:24576
CV v15,v46,v47
MM a[88:91],a[160:163],v[88:91],a[88:91]
EX v50,v50
MM v[120:123],v[96:99],v[108:111],v[120:123]
MM v[120:123],v[100:103],v[112:115],v[120:123]
MM v[120:123],v[104:107],v[116:119],v[120:123]
MM a[92:95],a[168:171],v[88:91],a[92:95]
EX v51,v51
MM a[92:95],a[172:175],v[92:95],a[92:95]
buffer_load_dwordx4 v235, s[12:15], s81 offen offset:0 lds
MM a[84:87],a[172:175],v[84:87],a[84:87]
RD a[228:231],v225 offset:24576
CV v20,v48,v49
MM a[84:87],a[168:171],v[80:83],a[84:87]
EX v52,v52
MM a[76:79],a[168:171],v[72:75],a[76:79]
EX v53,v53
MM a[76:79],a[172:175],v[76:79],a[76:79]
RD a[232:235],v226 offset:24576
CV v21,v50,v51
MM a[68:71],a[172:175],v[68:71],a[68:71]
s_mov_b32 exec_lo, 0x00ff00ff
s_mov_b32 exec_hi, 0x00ff00ff
buffer_store_dwordx4 v[120:123], v127, s[20:23], s90 offen offset:0 sc1
s_mov_b64 exec, -1
s_nop 0
MM a[68:71],a[168:171],v[64:67],a[68:71]
EX v54,v54
MM a[96:99],a[176:179],v[64:67],a[96:99]
EX v55,v55
MM a[96:99],a[180:183],v[68:71],a[96:99]
RD a[236:239],v227 offset:24576
CV v22,v52,v53
MM a[104:107],a[180:183],v[76:79],a[104:107]
EX v56,v56
MM a[104:107],a[176:179],v[72:75],a[104:107]
buffer_load_dwordx4 v235, s[12:15], s64 offen offset:1024 lds
MM a[112:115],a[176:179],v[80:83],a[112:115]
EX v57,v57
MM a[112:115],a[180:183],v[84:87],a[112:115]
RD a[240:243],v224 offset:28672
CV v23,v54,v55
MM a[120:123],a[180:183],v[92:95],a[120:123]
EX v58,v58
MM a[120:123],a[176:179],v[88:91],a[120:123]
EX v59,v59
MM a[124:127],a[184:187],v[88:91],a[124:127]
v_add_f32 v244, v144, v145
v_add_f32 v245, v148, v149
MM a[124:127],a[188:191],v[92:95],a[124:127]
v_add_f32 v246, v152, v153
v_add_f32 v247, v156, v157
MM a[116:119],a[188:191],v[84:87],a[116:119]
EX v60,v60
MM a[116:119],a[184:187],v[80:83],a[116:119]
RD a[244:247],v225 offset:28672
CV v28,v56,v57
MM a[108:111],a[184:187],v[72:75],a[108:111]
EX v61,v61
MM a[108:111],a[188:191],v[76:79],a[108:111]
buffer_load_dwordx4 v235, s[12:15], s81 offen offset:2048 lds
MM a[100:103],a[188:191],v[68:71],a[100:103]
EX v62,v62
MM a[100:103],a[184:187],v[64:67],a[100:103]
RD a[248:251],v226 offset:28672
CV v29,v58,v59
v_smfmac_f32_16x16x64_bf16 v[144:147], v[236:239], v[0:7], v234
RD a[252:255],v227 offset:28672
EX v63,v63
v_smfmac_f32_16x16x64_bf16 v[148:151], v[236:239], v[8:15], v234
CV v30,v60,v61
v_smfmac_f32_16x16x64_bf16 v[152:155], v[236:239], v[16:23], v234
CV v31,v62,v63
s_nop 1
v_smfmac_f32_16x16x64_bf16 v[156:159], v[236:239], v[24:31], v234
s_waitcnt lgkmcnt(0)
buffer_load_dwordx4 v235, s[12:15], s64 offen offset:3072 lds
MM v[64:67],a[192:195],v[160:163],v[128:131]
s_add_u32 s64, s64, 16384
TR a[128:129],v232 offset:0
s_add_u32 s81, s64, 896
MM v[64:67],a[196:199],v[176:179],v[64:67]
TR a[130:131],v232 offset:4096
v_max3_f32 v249, v144, v145, v148
MM v[64:67],a[200:203],v[192:195],v[64:67]
TR a[132:133],v232 offset:8192
v_max3_f32 v249, v249, v149, v152
MM v[64:67],a[204:207],v[208:211],v[64:67]
TR a[134:135],v232 offset:12288
v_max3_f32 v249, v249, v153, v156
MM v[68:71],a[220:223],v[208:211],v[128:131]
TR a[136:137],v233 offset:0
v_max_f32 v249, v249, v157
MM v[68:71],a[216:219],v[192:195],v[68:71]
TR a[138:139],v233 offset:4096
v_cmp_lt_f32 vcc, s71, v249
MM v[68:71],a[212:215],v[176:179],v[68:71]
TR a[140:141],v233 offset:8192
TR a[142:143],v233 offset:12288
MM v[68:71],a[208:211],v[160:163],v[68:71]
s_cbranch_vccnz LSITE28_%=
LBACK28_%=:
MM v[76:79],a[208:211],v[164:167],v[132:135]
EX v64,v64
MM v[76:79],a[212:215],v[180:183],v[76:79]
EX v65,v65
MM v[76:79],a[216:219],v[196:199],v[76:79]
TR a[144:145],v232 offset:512
CV v64,v64,v65
MM v[76:79],a[220:223],v[212:215],v[76:79]
EX v66,v66
MM v[72:75],a[204:207],v[212:215],v[132:135]
EX v67,v67
MM v[72:75],a[200:203],v[196:199],v[72:75]
TR a[146:147],v232 offset:4608
CV v65,v66,v67
MM v[72:75],a[196:199],v[180:183],v[72:75]
EX v68,v68
MM v[72:75],a[192:195],v[164:167],v[72:75]
TR a[148:149],v232 offset:8704
TR a[150:151],v232 offset:12800
MM v[80:83],a[192:195],v[168:171],v[136:139]
EX v69,v69
MM v[80:83],a[196:199],v[184:187],v[80:83]
TR a[152:153],v233 offset:512
CV v66,v68,v69
MM v[80:83],a[200:203],v[200:203],v[80:83]
EX v70,v70
MM v[80:83],a[204:207],v[216:219],v[80:83]
TR a[154:155],v233 offset:4608
TR a[156:157],v233 offset:8704
MM v[84:87],a[220:223],v[216:219],v[136:139]
v_lshlrev_b32 v32, 3, v248
v_min_u32 v32, 4992, v32
MM v[84:87],a[216:219],v[200:203],v[84:87]
s_sub_u32 s91, 5242880, s26
s_sub_u32 s60, s24, s91
s_subb_u32 s61, s25, 0
MM v[84:87],a[212:215],v[184:187],v[84:87]
s_mov_b32 s62, 5242880
EX v71,v71
MM v[84:87],a[208:211],v[168:171],v[84:87]
s_mov_b32 s63, 131072
TR a[158:159],v233 offset:12800
CV v67,v70,v71
MM v[92:95],a[208:211],v[172:175],v[140:143]
s_lshl_b32 s90, s16, 2
EX v72,v72
MM v[92:95],a[212:215],v[188:191],v[92:95]
s_add_u32 s90, s90, %[swave]
EX v73,v73
MM v[92:95],a[216:219],v[204:207],v[92:95]
s_mul_i32 s90, s90, 10240
TR a[160:161],v232 offset:1024
CV v72,v72,v73
MM v[92:95],a[220:223],v[220:223],v[92:95]
s_add_u32 s90, s90, 5120
EX v74,v74
MM v[88:91],a[204:207],v[220:223],v[140:143]
s_add_u32 s91, %[slds], 135168
s_mov_b32 m0, s91
s_nop 0
buffer_load_dword v32, s[60:63], s90 offen offset:0 lds
MM v[88:91],a[200:203],v[204:207],v[88:91]
EX v75,v75
MM v[88:91],a[196:199],v[188:191],v[88:91]
TR a[162:163],v232 offset:5120
CV v73,v74,v75
MM v[88:91],a[192:195],v[172:175],v[88:91]
EX v76,v76
MM v[96:99],a[224:227],v[160:163],v[128:131]
TR a[164:165],v232 offset:9216
TR a[166:167],v232 offset:13312
MM v[96:99],a[228:231],v[176:179],v[96:99]
EX v77,v77
MM v[96:99],a[232:235],v[192:195],v[96:99]
TR a[168:169],v233 offset:1024
CV v74,v76,v77
MM v[96:99],a[236:239],v[208:211],v[96:99]
EX v78,v78
MM v[100:103],a[252:255],v[208:211],v[128:131]
EX v79,v79
MM v[100:103],a[248:251],v[192:195],v[100:103]
TR a[170:171],v233 offset:5120
CV v75,v78,v79
MM v[100:103],a[244:247],v[176:179],v[100:103]
EX v80,v80
MM v[100:103],a[240:243],v[160:163],v[100:103]
TR a[172:173],v233 offset:9216
TR a[174:175],v233 offset:13312
MM v[108:111],a[240:243],v[164:167],v[132:135]
s_add_u32 s67, s95, 5120
EX v81,v81
MM v[108:111],a[244:247],v[180:183],v[108:111]
s_mov_b32 s70, 1
TR a[176:177],v232 offset:1536
CV v80,v80,v81
MM v[108:111],a[248:251],v[196:199],v[108:111]
EX v82,v82
MM v[108:111],a[252:255],v[212:215],v[108:111]
EX v83,v83
MM v[104:107],a[236:239],v[212:215],v[132:135]
TR a[178:179],v232 offset:5632
CV v81,v82,v83
MM v[104:107],a[232:235],v[196:199],v[104:107]
EX v84,v84
MM v[104:107],a[228:231],v[180:183],v[104:107]
TR a[180:181],v232 offset:9728
)ASM"
R"ASM(
TR a[182:183],v232 offset:13824
MM v[104:107],a[224:227],v[164:167],v[104:107]
EX v85,v85
MM v[112:115],a[224:227],v[168:171],v[136:139]
TR a[184:185],v233 offset:1536
CV v82,v84,v85
MM v[112:115],a[228:231],v[184:187],v[112:115]
EX v86,v86
MM v[112:115],a[232:235],v[200:203],v[112:115]
EX v87,v87
MM v[112:115],a[236:239],v[216:219],v[112:115]
TR a[186:187],v233 offset:5632
CV v83,v86,v87
MM v[116:119],a[252:255],v[216:219],v[136:139]
EX v88,v88
MM v[116:119],a[248:251],v[200:203],v[116:119]
TR a[188:189],v233 offset:9728
TR a[190:191],v233 offset:13824
MM v[116:119],a[244:247],v[184:187],v[116:119]
EX v89,v89
MM v[116:119],a[240:243],v[168:171],v[116:119]
EX v90,v90
MM v[124:127],a[240:243],v[172:175],v[140:143]
EX v91,v91
MM v[124:127],a[244:247],v[188:191],v[124:127]
EX v92,v92
MM v[124:127],a[248:251],v[204:207],v[124:127]
EX v93,v93
MM v[124:127],a[252:255],v[220:223],v[124:127]
EX v94,v94
MM v[120:123],a[236:239],v[220:223],v[140:143]
EX v95,v95
MM v[120:123],a[232:235],v[204:207],v[120:123]
EX v96,v96
MM v[120:123],a[228:231],v[188:191],v[120:123]
EX v97,v97
MM v[120:123],a[224:227],v[172:175],v[120:123]
s_waitcnt vmcnt(14) lgkmcnt(0)
s_barrier
MM a[0:3],a[128:131],v[0:3],a[0:3]
s_add_u32 m0, s65, 0
CV v88,v88,v89
CV v89,v90,v91
MM a[0:3],a[132:135],v[4:7],a[0:3]
RD a[192:195],v224 offset:32768
CV v90,v92,v93
MM a[8:11],a[132:135],v[12:15],a[8:11]
RD a[196:199],v225 offset:32768
CV v91,v94,v95
MM a[8:11],a[128:131],v[8:11],a[8:11]
buffer_load_dwordx4 v228, s[8:11], s64 offen offset:0 lds
MM a[16:19],a[128:131],v[16:19],a[16:19]
RD a[200:203],v226 offset:32768
CV v68,v96,v97
MM a[16:19],a[132:135],v[20:23],a[16:19]
buffer_load_dwordx4 v229, s[8:11], s64 offen offset:1024 lds
MM a[24:27],a[132:135],v[28:31],a[24:27]
EX v98,v98
MM a[24:27],a[128:131],v[24:27],a[24:27]
RD a[204:207],v227 offset:32768
RD a[208:211],v224 offset:36864
MM a[28:31],a[136:139],v[24:27],a[28:31]
EX v99,v99
MM a[28:31],a[140:143],v[28:31],a[28:31]
RD a[212:215],v225 offset:36864
CV v69,v98,v99
MM a[20:23],a[140:143],v[20:23],a[20:23]
EX v100,v100
MM a[20:23],a[136:139],v[16:19],a[20:23]
EX v101,v101
MM a[12:15],a[136:139],v[8:11],a[12:15]
RD a[216:219],v226 offset:36864
CV v70,v100,v101
MM a[12:15],a[140:143],v[12:15],a[12:15]
buffer_load_dwordx4 v230, s[8:11], s64 offen offset:2048 lds
MM a[4:7],a[140:143],v[4:7],a[4:7]
EX v102,v102
MM a[4:7],a[136:139],v[0:3],a[4:7]
EX v103,v103
MM a[32:35],a[144:147],v[0:3],a[32:35]
RD a[220:223],v227 offset:36864
CV v71,v102,v103
MM a[32:35],a[148:151],v[4:7],a[32:35]
EX v104,v104
MM a[40:43],a[148:151],v[12:15],a[40:43]
EX v105,v105
MM a[40:43],a[144:147],v[8:11],a[40:43]
RD a[224:227],v224 offset:40960
CV v76,v104,v105
MM a[48:51],a[144:147],v[16:19],a[48:51]
buffer_load_dwordx4 v231, s[8:11], s64 offen offset:3072 lds
MM a[48:51],a[148:151],v[20:23],a[48:51]
s_add_u32 m0, s65, 65536
EX v106,v106
MM a[56:59],a[148:151],v[28:31],a[56:59]
EX v107,v107
MM a[56:59],a[144:147],v[24:27],a[56:59]
RD a[228:231],v225 offset:40960
CV v77,v106,v107
MM a[60:63],a[152:155],v[24:27],a[60:63]
EX v108,v108
MM a[60:63],a[156:159],v[28:31],a[60:63]
buffer_load_dwordx4 v235, s[12:15], s81 offen offset:0 lds
MM a[52:55],a[156:159],v[20:23],a[52:55]
EX v109,v109
MM a[52:55],a[152:155],v[16:19],a[52:55]
RD a[232:235],v226 offset:40960
CV v78,v108,v109
MM a[44:47],a[152:155],v[8:11],a[44:47]
EX v110,v110
MM a[44:47],a[156:159],v[12:15],a[44:47]
EX v111,v111
MM a[36:39],a[156:159],v[4:7],a[36:39]
RD a[236:239],v227 offset:40960
CV v79,v110,v111
MM a[36:39],a[152:155],v[0:3],a[36:39]
EX v112,v112
MM a[64:67],a[160:163],v[0:3],a[64:67]
buffer_load_dwordx4 v235, s[12:15], s64 offen offset:1024 lds
MM a[64:67],a[164:167],v[4:7],a[64:67]
EX v113,v113
MM a[72:75],a[164:167],v[12:15],a[72:75]
RD a[240:243],v224 offset:45056
CV v84,v112,v113
MM a[72:75],a[160:163],v[8:11],a[72:75]
EX v114,v114
MM a[80:83],a[160:163],v[16:19],a[80:83]
EX v115,v115
MM a[80:83],a[164:167],v[20:23],a[80:83]
RD a[244:247],v225 offset:45056
CV v85,v114,v115
MM a[88:91],a[164:167],v[28:31],a[88:91]
EX v116,v116
MM a[88:91],a[160:163],v[24:27],a[88:91]
buffer_load_dwordx4 v235, s[12:15], s81 offen offset:2048 lds
MM a[92:95],a[168:171],v[24:27],a[92:95]
EX v117,v117
MM a[92:95],a[172:175],v[28:31],a[92:95]
RD a[248:251],v226 offset:45056
CV v86,v116,v117
MM a[84:87],a[172:175],v[20:23],a[84:87]
EX v118,v118
MM a[84:87],a[168:171],v[16:19],a[84:87]
EX v119,v119
MM a[76:79],a[168:171],v[8:11],a[76:79]
RD a[252:255],v227 offset:45056
CV v87,v118,v119
MM a[76:79],a[172:175],v[12:15],a[76:79]
EX v120,v120
MM a[68:71],a[172:175],v[4:7],a[68:71]
buffer_load_dwordx4 v235, s[12:15], s64 offen offset:3072 lds
MM a[68:71],a[168:171],v[0:3],a[68:71]
EX v121,v121
MM a[96:99],a[176:179],v[0:3],a[96:99]
EX v122,v122
MM a[96:99],a[180:183],v[4:7],a[96:99]
EX v123,v123
MM a[104:107],a[180:183],v[12:15],a[104:107]
EX v124,v124
MM a[104:107],a[176:179],v[8:11],a[104:107]
EX v125,v125
MM a[112:115],a[176:179],v[16:19],a[112:115]
EX v126,v126
MM a[112:115],a[180:183],v[20:23],a[112:115]
EX v127,v127
MM a[120:123],a[180:183],v[28:31],a[120:123]
s_lshl_b32 s91, s47, 2
CV v92,v120,v121
CV v93,v122,v123
MM a[120:123],a[176:179],v[24:27],a[120:123]
s_add_u32 s91, s91, %[swave]
CV v94,v124,v125
CV v95,v126,v127
MM a[124:127],a[184:187],v[24:27],a[124:127]
s_lshl_b32 s91, s91, 2
v_add_f32 v244, v144, v145
v_add_f32 v245, v148, v149
MM a[124:127],a[188:191],v[28:31],a[124:127]
s_add_u32 s91, s91, 73728
v_add_f32 v246, v152, v153
v_add_f32 v247, v156, v157
MM a[116:119],a[188:191],v[20:23],a[116:119]
s_waitcnt vmcnt(12)
v_mov_b32 v250, s69
s_mov_b64 exec, 1
buffer_store_dword v250, off, s[20:23], s91 offset:0 sc1
s_mov_b64 exec, -1
MM a[116:119],a[184:187],v[16:19],a[116:119]
MM a[108:111],a[184:187],v[8:11],a[108:111]
MM a[108:111],a[188:191],v[12:15],a[108:111]
MM a[100:103],a[188:191],v[4:7],a[100:103]
MM a[100:103],a[184:187],v[0:3],a[100:103]
v_smfmac_f32_16x16x64_bf16 v[144:147], v[236:239], v[64:71], v234
v_smfmac_f32_16x16x64_bf16 v[148:151], v[236:239], v[72:79], v234
v_smfmac_f32_16x16x64_bf16 v[152:155], v[236:239], v[80:87], v234
v_smfmac_f32_16x16x64_bf16 v[156:159], v[236:239], v[88:95], v234
s_waitcnt lgkmcnt(0)
s_cmp_ge_u32 s16, 20
s_cbranch_scc1 LLAP_%=
MM v[0:3],a[192:195],v[160:163],v[128:131]
s_add_u32 s64, s64, 16384
TR a[128:129],v232 offset:16384
s_add_u32 s81, s64, 896
MM v[0:3],a[196:199],v[176:179],v[0:3]
TR a[130:131],v232 offset:20480
v_max3_f32 v249, v144, v145, v148
MM v[0:3],a[200:203],v[192:195],v[0:3]
TR a[132:133],v232 offset:24576
v_max3_f32 v249, v249, v149, v152
MM v[0:3],a[204:207],v[208:211],v[0:3]
TR a[134:135],v232 offset:28672
v_max3_f32 v249, v249, v153, v156
MM v[4:7],a[220:223],v[208:211],v[128:131]
TR a[136:137],v233 offset:16384
v_max_f32 v249, v249, v157
MM v[4:7],a[216:219],v[192:195],v[4:7]
TR a[138:139],v233 offset:20480
v_cmp_lt_f32 vcc, s71, v249
MM v[4:7],a[212:215],v[176:179],v[4:7]
TR a[140:141],v233 offset:24576
TR a[142:143],v233 offset:28672
MM v[4:7],a[208:211],v[160:163],v[4:7]
s_cbranch_vccnz LSITE29_%=
LBACK29_%=:
MM v[12:15],a[208:211],v[164:167],v[132:135]
EX v0,v0
MM v[12:15],a[212:215],v[180:183],v[12:15]
EX v1,v1
MM v[12:15],a[216:219],v[196:199],v[12:15]
TR a[144:145],v232 offset:16896
CV v0,v0,v1
MM v[12:15],a[220:223],v[212:215],v[12:15]
EX v2,v2
MM v[8:11],a[204:207],v[212:215],v[132:135]
EX v3,v3
MM v[8:11],a[200:203],v[196:199],v[8:11]
TR a[146:147],v232 offset:20992
CV v1,v2,v3
MM v[8:11],a[196:199],v[180:183],v[8:11]
EX v4,v4
MM v[8:11],a[192:195],v[164:167],v[8:11]
TR a[148:149],v232 offset:25088
TR a[150:151],v232 offset:29184
MM v[16:19],a[192:195],v[168:171],v[136:139]
EX v5,v5
MM v[16:19],a[196:199],v[184:187],v[16:19]
TR a[152:153],v233 offset:16896
CV v2,v4,v5
MM v[16:19],a[200:203],v[200:203],v[16:19]
EX v6,v6
MM v[16:19],a[204:207],v[216:219],v[16:19]
TR a[154:155],v233 offset:20992
TR a[156:157],v233 offset:25088
MM v[20:23],a[220:223],v[216:219],v[136:139]
s_add_u32 s87, s95, 5120
buffer_store_dword v240, v248, s[20:23], s87 offen offset:12 sc1
MM v[20:23],a[216:219],v[200:203],v[20:23]
EX v7,v7
MM v[20:23],a[212:215],v[184:187],v[20:23]
TR a[158:159],v233 offset:29184
CV v3,v6,v7
MM v[20:23],a[208:211],v[168:171],v[20:23]
EX v8,v8
MM v[28:31],a[208:211],v[172:175],v[140:143]
EX v9,v9
MM v[28:31],a[212:215],v[188:191],v[28:31]
TR a[160:161],v232 offset:17408
CV v8,v8,v9
MM v[28:31],a[216:219],v[204:207],v[28:31]
EX v10,v10
MM v[28:31],a[220:223],v[220:223],v[28:31]
EX v11,v11
MM v[24:27],a[204:207],v[220:223],v[140:143]
TR a[162:163],v232 offset:21504
CV v9,v10,v11
MM v[24:27],a[200:203],v[204:207],v[24:27]
TR a[164:165],v232 offset:25600
TR a[166:167],v232 offset:29696
MM v[24:27],a[196:199],v[188:191],v[24:27]
EX v12,v12
MM v[24:27],a[192:195],v[172:175],v[24:27]
EX v13,v13
MM v[32:35],a[224:227],v[160:163],v[128:131]
TR a[168:169],v233 offset:17408
CV v10,v12,v13
MM v[32:35],a[228:231],v[176:179],v[32:35]
EX v14,v14
MM v[32:35],a[232:235],v[192:195],v[32:35]
TR a[170:171],v233 offset:21504
TR a[172:173],v233 offset:25600
MM v[32:35],a[236:239],v[208:211],v[32:35]
EX v15,v15
MM v[36:39],a[252:255],v[208:211],v[128:131]
TR a[174:175],v233 offset:29696
CV v11,v14,v15
MM v[36:39],a[248:251],v[192:195],v[36:39]
EX v16,v16
MM v[36:39],a[244:247],v[176:179],v[36:39]
EX v17,v17
MM v[36:39],a[240:243],v[160:163],v[36:39]
TR a[176:177],v232 offset:17920
CV v16,v16,v17
MM v[44:47],a[240:243],v[164:167],v[132:135]
EX v18,v18
MM v[44:47],a[244:247],v[180:183],v[44:47]
TR a[178:179],v232 offset:22016
TR a[180:181],v232 offset:26112
MM v[44:47],a[248:251],v[196:199],v[44:47]
EX v19,v19
MM v[44:47],a[252:255],v[212:215],v[44:47]
s_nop 0
LPG21_%=:
s_cbranch_execz LTR4_%=
TR a[182:183],v232 offset:30208
CV v17,v18,v19
MM v[40:43],a[236:239],v[212:215],v[132:135]
EX v20,v20
MM v[40:43],a[232:235],v[196:199],v[40:43]
EX v21,v21
MM v[40:43],a[228:231],v[180:183],v[40:43]
TR a[184:185],v233 offset:17920
CV v18,v20,v21
MM v[40:43],a[224:227],v[164:167],v[40:43]
EX v22,v22
MM v[48:51],a[224:227],v[168:171],v[136:139]
TR a[186:187],v233 offset:22016
TR a[188:189],v233 offset:26112
MM v[48:51],a[228:231],v[184:187],v[48:51]
EX v23,v23
MM v[48:51],a[232:235],v[200:203],v[48:51]
TR a[190:191],v233 offset:30208
CV v19,v22,v23
MM v[48:51],a[236:239],v[216:219],v[48:51]
EX v24,v24
MM v[52:55],a[252:255],v[216:219],v[136:139]
EX v25,v25
MM v[52:55],a[248:251],v[200:203],v[52:55]
EX v26,v26
MM v[52:55],a[244:247],v[184:187],v[52:55]
EX v27,v27
MM v[52:55],a[240:243],v[168:171],v[52:55]
EX v28,v28
MM v[60:63],a[240:243],v[172:175],v[140:143]
EX v29,v29
MM v[60:63],a[244:247],v[188:191],v[60:63]
EX v30,v30
MM v[60:63],a[248:251],v[204:207],v[60:63]
EX v31,v31
MM v[60:63],a[252:255],v[220:223],v[60:63]
EX v32,v32
MM v[56:59],a[236:239],v[220:223],v[140:143]
EX v33,v33
MM v[56:59],a[232:235],v[204:207],v[56:59]
EX v34,v34
MM v[56:59],a[228:231],v[188:191],v[56:59]
EX v35,v35
MM v[56:59],a[224:227],v[172:175],v[56:59]
s_waitcnt lgkmcnt(0)
s_barrier
MM a[0:3],a[128:131],v[64:67],a[0:3]
s_add_u32 m0, s65, 16384
CV v24,v24,v25
CV v25,v26,v27
MM a[0:3],a[132:135],v[68:71],a[0:3]
RD a[192:195],v224 offset:49152
CV v26,v28,v29
MM a[8:11],a[132:135],v[76:79],a[8:11]
RD a[196:199],v225 offset:49152
CV v27,v30,v31
MM a[8:11],a[128:131],v[72:75],a[8:11]
buffer_load_dwordx4 v228, s[8:11], s64 offen offset:0 lds
MM a[16:19],a[128:131],v[80:83],a[16:19]
RD a[200:203],v226 offset:49152
CV v4,v32,v33
MM a[16:19],a[132:135],v[84:87],a[16:19]
RD a[204:207],v227 offset:49152
CV v5,v34,v35
MM a[24:27],a[132:135],v[92:95],a[24:27]
buffer_load_dwordx4 v229, s[8:11], s64 offen offset:1024 lds
MM a[24:27],a[128:131],v[88:91],a[24:27]
RD a[208:211],v224 offset:53248
RD a[212:215],v225 offset:53248
MM a[28:31],a[136:139],v[88:91],a[28:31]
buffer_load_dwordx4 v230, s[8:11], s64 offen offset:2048 lds
MM a[28:31],a[140:143],v[92:95],a[28:31]
EX v36,v36
MM a[20:23],a[140:143],v[84:87],a[20:23]
EX v37,v37
MM a[20:23],a[136:139],v[80:83],a[20:23]
RD a[216:219],v226 offset:53248
CV v6,v36,v37
MM a[12:15],a[136:139],v[72:75],a[12:15]
)ASM"
R"ASM(
EX v38,v38
MM a[12:15],a[140:143],v[76:79],a[12:15]
RD a[220:223],v227 offset:53248
RD a[224:227],v224 offset:57344
MM a[4:7],a[140:143],v[68:71],a[4:7]
EX v39,v39
MM a[4:7],a[136:139],v[64:67],a[4:7]
RD a[228:231],v225 offset:57344
CV v7,v38,v39
MM a[32:35],a[144:147],v[64:67],a[32:35]
EX v40,v40
MM a[32:35],a[148:151],v[68:71],a[32:35]
buffer_load_dwordx4 v231, s[8:11], s64 offen offset:3072 lds
MM a[40:43],a[148:151],v[76:79],a[40:43]
s_add_u32 m0, s65, 81920
EX v41,v41
MM a[40:43],a[144:147],v[72:75],a[40:43]
RD a[232:235],v226 offset:57344
CV v12,v40,v41
MM a[48:51],a[144:147],v[80:83],a[48:51]
EX v42,v42
MM a[48:51],a[148:151],v[84:87],a[48:51]
EX v43,v43
MM a[56:59],a[148:151],v[92:95],a[56:59]
RD a[236:239],v227 offset:57344
CV v13,v42,v43
MM a[56:59],a[144:147],v[88:91],a[56:59]
EX v44,v44
MM a[60:63],a[152:155],v[88:91],a[60:63]
buffer_load_dwordx4 v235, s[12:15], s81 offen offset:0 lds
MM a[60:63],a[156:159],v[92:95],a[60:63]
EX v45,v45
MM a[52:55],a[156:159],v[84:87],a[52:55]
RD a[240:243],v224 offset:61440
CV v14,v44,v45
MM a[52:55],a[152:155],v[80:83],a[52:55]
EX v46,v46
MM a[44:47],a[152:155],v[72:75],a[44:47]
EX v47,v47
MM a[44:47],a[156:159],v[76:79],a[44:47]
RD a[244:247],v225 offset:61440
CV v15,v46,v47
MM a[36:39],a[156:159],v[68:71],a[36:39]
buffer_load_dwordx4 v235, s[12:15], s64 offen offset:1024 lds
MM a[36:39],a[152:155],v[64:67],a[36:39]
EX v48,v48
MM a[64:67],a[160:163],v[64:67],a[64:67]
EX v49,v49
MM a[64:67],a[164:167],v[68:71],a[64:67]
RD a[248:251],v226 offset:61440
CV v20,v48,v49
MM a[72:75],a[164:167],v[76:79],a[72:75]
EX v50,v50
MM a[72:75],a[160:163],v[72:75],a[72:75]
EX v51,v51
MM a[80:83],a[160:163],v[80:83],a[80:83]
RD a[252:255],v227 offset:61440
CV v21,v50,v51
MM a[80:83],a[164:167],v[84:87],a[80:83]
buffer_load_dwordx4 v235, s[12:15], s81 offen offset:2048 lds
MM a[88:91],a[164:167],v[92:95],a[88:91]
EX v52,v52
MM a[88:91],a[160:163],v[88:91],a[88:91]
EX v53,v53
MM a[92:95],a[168:171],v[88:91],a[92:95]
EX v54,v54
MM a[92:95],a[172:175],v[92:95],a[92:95]
buffer_load_dwordx4 v235, s[12:15], s64 offen offset:3072 lds
MM a[84:87],a[172:175],v[84:87],a[84:87]
EX v55,v55
MM a[84:87],a[168:171],v[80:83],a[84:87]
EX v56,v56
MM a[76:79],a[168:171],v[72:75],a[76:79]
EX v57,v57
MM a[76:79],a[172:175],v[76:79],a[76:79]
EX v58,v58
MM a[68:71],a[172:175],v[68:71],a[68:71]
EX v59,v59
MM a[68:71],a[168:171],v[64:67],a[68:71]
EX v60,v60
MM a[96:99],a[176:179],v[64:67],a[96:99]
EX v61,v61
MM a[96:99],a[180:183],v[68:71],a[96:99]
EX v62,v62
MM a[104:107],a[180:183],v[76:79],a[104:107]
EX v63,v63
MM a[104:107],a[176:179],v[72:75],a[104:107]
CV v22,v52,v53
CV v23,v54,v55
MM a[112:115],a[176:179],v[80:83],a[112:115]
CV v28,v56,v57
CV v29,v58,v59
MM a[112:115],a[180:183],v[84:87],a[112:115]
CV v30,v60,v61
CV v31,v62,v63
MM a[120:123],a[180:183],v[92:95],a[120:123]
MM a[120:123],a[176:179],v[88:91],a[120:123]
MM a[124:127],a[184:187],v[88:91],a[124:127]
v_add_f32 v244, v144, v145
v_add_f32 v245, v148, v149
MM a[124:127],a[188:191],v[92:95],a[124:127]
v_add_f32 v246, v152, v153
v_add_f32 v247, v156, v157
MM a[116:119],a[188:191],v[84:87],a[116:119]
MM a[116:119],a[184:187],v[80:83],a[116:119]
MM a[108:111],a[184:187],v[72:75],a[108:111]
MM a[108:111],a[188:191],v[76:79],a[108:111]
MM a[100:103],a[188:191],v[68:71],a[100:103]
MM a[100:103],a[184:187],v[64:67],a[100:103]
v_smfmac_f32_16x16x64_bf16 v[144:147], v[236:239], v[0:7], v234
v_smfmac_f32_16x16x64_bf16 v[148:151], v[236:239], v[8:15], v234
v_smfmac_f32_16x16x64_bf16 v[152:155], v[236:239], v[16:23], v234
v_smfmac_f32_16x16x64_bf16 v[156:159], v[236:239], v[24:31], v234
s_waitcnt lgkmcnt(0)
MM v[64:67],a[192:195],v[160:163],v[128:131]
s_add_u32 s64, s64, 16384
TR a[128:129],v232 offset:32768
s_add_u32 s81, s64, 896
MM v[64:67],a[196:199],v[176:179],v[64:67]
TR a[130:131],v232 offset:36864
v_max3_f32 v249, v144, v145, v148
MM v[64:67],a[200:203],v[192:195],v[64:67]
TR a[132:133],v232 offset:40960
v_max3_f32 v249, v249, v149, v152
MM v[64:67],a[204:207],v[208:211],v[64:67]
TR a[134:135],v232 offset:45056
v_max3_f32 v249, v249, v153, v156
MM v[68:71],a[220:223],v[208:211],v[128:131]
TR a[136:137],v233 offset:32768
v_max_f32 v249, v249, v157
MM v[68:71],a[216:219],v[192:195],v[68:71]
TR a[138:139],v233 offset:36864
v_cmp_lt_f32 vcc, s71, v249
MM v[68:71],a[212:215],v[176:179],v[68:71]
TR a[140:141],v233 offset:40960
TR a[142:143],v233 offset:45056
MM v[68:71],a[208:211],v[160:163],v[68:71]
s_cbranch_vccnz LSITE30_%=
LBACK30_%=:
MM v[76:79],a[208:211],v[164:167],v[132:135]
s_and_b32 s90, s17, 63
s_cmp_lt_u32 s17, 80
s_cselect_b32 s17, s17, s90
s_cselect_b64 s[44:45], -1, 0
MM v[76:79],a[212:215],v[180:183],v[76:79]
v_lshrrev_b32 v253, 4, v248
v_min_u32 v253, 35, v253
MM v[76:79],a[216:219],v[196:199],v[76:79]
v_lshrrev_b32 v252, 2, v253
v_mul_u32_u24 v252, 144, v252
MM v[76:79],a[220:223],v[212:215],v[76:79]
TR a[144:145],v232 offset:33280
v_lshl_add_u32 v253, v253, 2, v252
MM v[72:75],a[204:207],v[212:215],v[132:135]
s_lshr_b32 s90, s17, 3
EX v64,v64
MM v[72:75],a[200:203],v[196:199],v[72:75]
s_lshl_b32 s76, s90, 4
EX v65,v65
MM v[72:75],a[196:199],v[180:183],v[72:75]
s_add_u32 s76, s76, 73728
buffer_load_dword v252, v253, s[20:23], s76 offen offset:0 sc1
MM v[72:75],a[192:195],v[164:167],v[72:75]
EX v66,v66
MM v[80:83],a[192:195],v[168:171],v[136:139]
EX v67,v67
MM v[80:83],a[196:199],v[184:187],v[80:83]
EX v68,v68
MM v[80:83],a[200:203],v[200:203],v[80:83]
TR a[146:147],v232 offset:37376
CV v64,v64,v65
MM v[80:83],a[204:207],v[216:219],v[80:83]
EX v69,v69
MM v[84:87],a[220:223],v[216:219],v[136:139]
TR a[148:149],v232 offset:41472
CV v65,v66,v67
MM v[84:87],a[216:219],v[200:203],v[84:87]
EX v70,v70
MM v[84:87],a[212:215],v[184:187],v[84:87]
TR a[150:151],v232 offset:45568
CV v66,v68,v69
MM v[84:87],a[208:211],v[168:171],v[84:87]
EX v71,v71
MM v[92:95],a[208:211],v[172:175],v[140:143]
TR a[152:153],v233 offset:33280
TR a[154:155],v233 offset:37376
MM v[92:95],a[212:215],v[188:191],v[92:95]
EX v72,v72
MM v[92:95],a[216:219],v[204:207],v[92:95]
EX v73,v73
MM v[92:95],a[220:223],v[220:223],v[92:95]
TR a[156:157],v233 offset:41472
CV v67,v70,v71
MM v[88:91],a[204:207],v[220:223],v[140:143]
EX v74,v74
MM v[88:91],a[200:203],v[204:207],v[88:91]
TR a[158:159],v233 offset:45568
CV v72,v72,v73
MM v[88:91],a[196:199],v[188:191],v[88:91]
EX v75,v75
MM v[88:91],a[192:195],v[172:175],v[88:91]
TR a[160:161],v232 offset:33792
TR a[162:163],v232 offset:37888
MM v[96:99],a[224:227],v[160:163],v[128:131]
EX v76,v76
MM v[96:99],a[228:231],v[176:179],v[96:99]
TR a[164:165],v232 offset:41984
CV v73,v74,v75
MM v[96:99],a[232:235],v[192:195],v[96:99]
EX v77,v77
MM v[96:99],a[236:239],v[208:211],v[96:99]
EX v78,v78
MM v[100:103],a[252:255],v[208:211],v[128:131]
TR a[166:167],v232 offset:46080
CV v74,v76,v77
MM v[100:103],a[248:251],v[192:195],v[100:103]
EX v79,v79
MM v[100:103],a[244:247],v[176:179],v[100:103]
TR a[168:169],v233 offset:33792
CV v75,v78,v79
MM v[100:103],a[240:243],v[160:163],v[100:103]
EX v80,v80
MM v[108:111],a[240:243],v[164:167],v[132:135]
TR a[170:171],v233 offset:37888
TR a[172:173],v233 offset:41984
MM v[108:111],a[244:247],v[180:183],v[108:111]
EX v81,v81
MM v[108:111],a[248:251],v[196:199],v[108:111]
TR a[174:175],v233 offset:46080
CV v80,v80,v81
MM v[108:111],a[252:255],v[212:215],v[108:111]
EX v82,v82
MM v[104:107],a[236:239],v[212:215],v[132:135]
EX v83,v83
MM v[104:107],a[232:235],v[196:199],v[104:107]
TR a[176:177],v232 offset:34304
TR a[178:179],v232 offset:38400
MM v[104:107],a[228:231],v[180:183],v[104:107]
EX v84,v84
MM v[104:107],a[224:227],v[164:167],v[104:107]
TR a[180:181],v232 offset:42496
CV v81,v82,v83
MM v[112:115],a[224:227],v[168:171],v[136:139]
EX v85,v85
MM v[112:115],a[228:231],v[184:187],v[112:115]
TR a[182:183],v232 offset:46592
CV v82,v84,v85
MM v[112:115],a[232:235],v[200:203],v[112:115]
EX v86,v86
MM v[112:115],a[236:239],v[216:219],v[112:115]
TR a[184:185],v233 offset:34304
TR a[186:187],v233 offset:38400
MM v[116:119],a[252:255],v[216:219],v[136:139]
EX v87,v87
MM v[116:119],a[248:251],v[200:203],v[116:119]
TR a[188:189],v233 offset:42496
CV v83,v86,v87
MM v[116:119],a[244:247],v[184:187],v[116:119]
EX v88,v88
MM v[116:119],a[240:243],v[168:171],v[116:119]
EX v89,v89
MM v[124:127],a[240:243],v[172:175],v[140:143]
TR a[190:191],v233 offset:46592
CV v88,v88,v89
MM v[124:127],a[244:247],v[188:191],v[124:127]
EX v90,v90
MM v[124:127],a[248:251],v[204:207],v[124:127]
EX v91,v91
MM v[124:127],a[252:255],v[220:223],v[124:127]
EX v92,v92
MM v[120:123],a[236:239],v[220:223],v[140:143]
EX v93,v93
MM v[120:123],a[232:235],v[204:207],v[120:123]
EX v94,v94
MM v[120:123],a[228:231],v[188:191],v[120:123]
EX v95,v95
MM v[120:123],a[224:227],v[172:175],v[120:123]
s_waitcnt vmcnt(15) lgkmcnt(0)
s_barrier
MM a[0:3],a[128:131],v[0:3],a[0:3]
s_add_u32 m0, s65, 32768
CV v89,v90,v91
CV v90,v92,v93
MM a[0:3],a[132:135],v[4:7],a[0:3]
RD a[192:195],v224 offset:0
CV v91,v94,v95
MM a[8:11],a[132:135],v[12:15],a[8:11]
buffer_load_dwordx4 v228, s[8:11], s64 offen offset:0 lds
MM a[8:11],a[128:131],v[8:11],a[8:11]
RD a[196:199],v225 offset:0
RD a[200:203],v226 offset:0
MM a[16:19],a[128:131],v[16:19],a[16:19]
EX v96,v96
MM a[16:19],a[132:135],v[20:23],a[16:19]
EX v97,v97
MM a[24:27],a[132:135],v[28:31],a[24:27]
RD a[204:207],v227 offset:0
CV v68,v96,v97
MM a[24:27],a[128:131],v[24:27],a[24:27]
EX v98,v98
MM a[28:31],a[136:139],v[24:27],a[28:31]
buffer_load_dwordx4 v229, s[8:11], s64 offen offset:1024 lds
MM a[28:31],a[140:143],v[28:31],a[28:31]
EX v99,v99
MM a[20:23],a[140:143],v[20:23],a[20:23]
RD a[208:211],v224 offset:4096
CV v69,v98,v99
MM a[20:23],a[136:139],v[16:19],a[20:23]
EX v100,v100
MM a[12:15],a[136:139],v[8:11],a[12:15]
EX v101,v101
MM a[12:15],a[140:143],v[12:15],a[12:15]
RD a[212:215],v225 offset:4096
CV v70,v100,v101
MM a[4:7],a[140:143],v[4:7],a[4:7]
EX v102,v102
MM a[4:7],a[136:139],v[0:3],a[4:7]
buffer_load_dwordx4 v230, s[8:11], s64 offen offset:2048 lds
MM a[32:35],a[144:147],v[0:3],a[32:35]
EX v103,v103
MM a[32:35],a[148:151],v[4:7],a[32:35]
RD a[216:219],v226 offset:4096
CV v71,v102,v103
MM a[40:43],a[148:151],v[12:15],a[40:43]
EX v104,v104
MM a[40:43],a[144:147],v[8:11],a[40:43]
EX v105,v105
MM a[48:51],a[144:147],v[16:19],a[48:51]
RD a[220:223],v227 offset:4096
CV v76,v104,v105
MM a[48:51],a[148:151],v[20:23],a[48:51]
EX v106,v106
MM a[56:59],a[148:151],v[28:31],a[56:59]
buffer_load_dwordx4 v231, s[8:11], s64 offen offset:3072 lds
MM a[56:59],a[144:147],v[24:27],a[56:59]
s_add_u32 m0, s65, 98304
EX v107,v107
MM a[60:63],a[152:155],v[24:27],a[60:63]
RD a[224:227],v224 offset:8192
CV v77,v106,v107
MM a[60:63],a[156:159],v[28:31],a[60:63]
EX v108,v108
MM a[52:55],a[156:159],v[20:23],a[52:55]
EX v109,v109
MM a[52:55],a[152:155],v[16:19],a[52:55]
RD a[228:231],v225 offset:8192
CV v78,v108,v109
MM a[44:47],a[152:155],v[8:11],a[44:47]
EX v110,v110
MM a[44:47],a[156:159],v[12:15],a[44:47]
buffer_load_dwordx4 v235, s[12:15], s81 offen offset:0 lds
MM a[36:39],a[156:159],v[4:7],a[36:39]
EX v111,v111
MM a[36:39],a[152:155],v[0:3],a[36:39]
RD a[232:235],v226 offset:8192
CV v79,v110,v111
MM a[64:67],a[160:163],v[0:3],a[64:67]
EX v112,v112
MM a[64:67],a[164:167],v[4:7],a[64:67]
EX v113,v113
MM a[72:75],a[164:167],v[12:15],a[72:75]
RD a[236:239],v227 offset:8192
CV v84,v112,v113
MM a[72:75],a[160:163],v[8:11],a[72:75]
buffer_load_dwordx4 v235, s[12:15], s64 offen offset:1024 lds
MM a[80:83],a[160:163],v[16:19],a[80:83]
EX v114,v114
MM a[80:83],a[164:167],v[20:23],a[80:83]
EX v115,v115
MM a[88:91],a[164:167],v[28:31],a[88:91]
RD a[240:243],v224 offset:12288
CV v85,v114,v115
MM a[88:91],a[160:163],v[24:27],a[88:91]
EX v116,v116
MM a[92:95],a[168:171],v[24:27],a[92:95]
EX v117,v117
MM a[92:95],a[172:175],v[28:31],a[92:95]
RD a[244:247],v225 offset:12288
CV v86,v116,v117
MM a[84:87],a[172:175],v[20:23],a[84:87]
buffer_load_dwordx4 v235, s[12:15], s81 offen offset:2048 lds
MM a[84:87],a[168:171],v[16:19],a[84:87]
EX v118,v118
MM a[76:79],a[168:171],v[8:11],a[76:79]
EX v119,v119
MM a[76:79],a[172:175],v[12:15],a[76:79]
)ASM"
R"ASM(
RD a[248:251],v226 offset:12288
CV v87,v118,v119
MM a[68:71],a[172:175],v[4:7],a[68:71]
EX v120,v120
MM a[68:71],a[168:171],v[0:3],a[68:71]
EX v121,v121
MM a[96:99],a[176:179],v[0:3],a[96:99]
RD a[252:255],v227 offset:12288
CV v92,v120,v121
MM a[96:99],a[180:183],v[4:7],a[96:99]
buffer_load_dwordx4 v235, s[12:15], s64 offen offset:3072 lds
MM a[104:107],a[180:183],v[12:15],a[104:107]
EX v122,v122
MM a[104:107],a[176:179],v[8:11],a[104:107]
EX v123,v123
MM a[112:115],a[176:179],v[16:19],a[112:115]
EX v124,v124
MM a[112:115],a[180:183],v[20:23],a[112:115]
EX v125,v125
MM a[120:123],a[180:183],v[28:31],a[120:123]
EX v126,v126
MM a[120:123],a[176:179],v[24:27],a[120:123]
EX v127,v127
MM a[124:127],a[184:187],v[24:27],a[124:127]
v_add_f32 v244, v144, v145
v_add_f32 v245, v148, v149
MM a[124:127],a[188:191],v[28:31],a[124:127]
v_add_f32 v246, v152, v153
v_add_f32 v247, v156, v157
MM a[116:119],a[188:191],v[20:23],a[116:119]
CV v93,v122,v123
CV v94,v124,v125
MM a[116:119],a[184:187],v[16:19],a[116:119]
CV v95,v126,v127
MM a[108:111],a[184:187],v[8:11],a[108:111]
MM a[108:111],a[188:191],v[12:15],a[108:111]
MM a[100:103],a[188:191],v[4:7],a[100:103]
MM a[100:103],a[184:187],v[0:3],a[100:103]
v_smfmac_f32_16x16x64_bf16 v[144:147], v[236:239], v[64:71], v234
v_smfmac_f32_16x16x64_bf16 v[148:151], v[236:239], v[72:79], v234
v_smfmac_f32_16x16x64_bf16 v[152:155], v[236:239], v[80:87], v234
v_smfmac_f32_16x16x64_bf16 v[156:159], v[236:239], v[88:95], v234
s_waitcnt lgkmcnt(0)
MM v[0:3],a[192:195],v[160:163],v[128:131]
s_add_u32 s64, s64, 16384
TR a[128:129],v232 offset:49152
s_add_u32 s81, s64, 896
MM v[0:3],a[196:199],v[176:179],v[0:3]
TR a[130:131],v232 offset:53248
v_max3_f32 v249, v144, v145, v148
MM v[0:3],a[200:203],v[192:195],v[0:3]
TR a[132:133],v232 offset:57344
v_max3_f32 v249, v249, v149, v152
MM v[0:3],a[204:207],v[208:211],v[0:3]
TR a[134:135],v232 offset:61440
v_max3_f32 v249, v249, v153, v156
MM v[4:7],a[220:223],v[208:211],v[128:131]
TR a[136:137],v233 offset:49152
v_max_f32 v249, v249, v157
MM v[4:7],a[216:219],v[192:195],v[4:7]
TR a[138:139],v233 offset:53248
v_cmp_lt_f32 vcc, s71, v249
MM v[4:7],a[212:215],v[176:179],v[4:7]
TR a[140:141],v233 offset:57344
TR a[142:143],v233 offset:61440
MM v[4:7],a[208:211],v[160:163],v[4:7]
s_cbranch_vccnz LSITE31_%=
LBACK31_%=:
MM v[12:15],a[208:211],v[164:167],v[132:135]
s_waitcnt vmcnt(8)
v_cmp_ne_u32 vcc, s69, v252
s_cbranch_vccnz LRFY_%=
LRFYb_%=:
MM v[12:15],a[212:215],v[180:183],v[12:15]
v_lshrrev_b32 v109, 2, v248
v_lshrrev_b32 v110, 3, v248
MM v[12:15],a[216:219],v[196:199],v[12:15]
s_and_b32 s76, s17, 7
EX v0,v0
MM v[12:15],a[220:223],v[212:215],v[12:15]
s_mul_i32 s76, s76, 2560
EX v1,v1
MM v[8:11],a[204:207],v[212:215],v[132:135]
s_lshl_b32 s91, s90, 8
EX v2,v2
MM v[8:11],a[200:203],v[196:199],v[8:11]
s_add_u32 s76, s76, s91
TR a[144:145],v232 offset:49664
CV v0,v0,v1
MM v[8:11],a[196:199],v[180:183],v[8:11]
s_add_u32 s76, s76, 21659648
buffer_load_dword v96, v109, s[20:23], s76 offen offset:0 sc1
MM v[8:11],a[192:195],v[164:167],v[8:11]
s_add_u32 s76, s76, 20480
EX v3,v3
MM v[16:19],a[192:195],v[168:171],v[136:139]
buffer_load_dword v97, v109, s[20:23], s76 offen offset:0 sc1
MM v[16:19],a[196:199],v[184:187],v[16:19]
s_add_u32 s76, s76, 20480
EX v4,v4
MM v[16:19],a[200:203],v[200:203],v[16:19]
buffer_load_dword v98, v109, s[20:23], s76 offen offset:0 sc1
MM v[16:19],a[204:207],v[216:219],v[16:19]
s_add_u32 s76, s76, 20480
EX v5,v5
MM v[20:23],a[220:223],v[216:219],v[136:139]
buffer_load_dword v99, v109, s[20:23], s76 offen offset:0 sc1
MM v[20:23],a[216:219],v[200:203],v[20:23]
s_add_u32 s76, s76, 20480
TR a[146:147],v232 offset:53760
TR a[148:149],v232 offset:57856
MM v[20:23],a[212:215],v[184:187],v[20:23]
buffer_load_dword v100, v109, s[20:23], s76 offen offset:0 sc1
MM v[20:23],a[208:211],v[168:171],v[20:23]
s_add_u32 s76, s76, 20480
EX v6,v6
MM v[28:31],a[208:211],v[172:175],v[140:143]
buffer_load_dword v101, v109, s[20:23], s76 offen offset:0 sc1
MM v[28:31],a[212:215],v[188:191],v[28:31]
s_add_u32 s76, s76, 20480
EX v7,v7
MM v[28:31],a[216:219],v[204:207],v[28:31]
buffer_load_dword v102, v109, s[20:23], s76 offen offset:0 sc1
MM v[28:31],a[220:223],v[220:223],v[28:31]
s_add_u32 s76, s76, 20480
s_nop 0
LPG22_%=:
s_cbranch_execz LTR4_%=
TR a[150:151],v232 offset:61952
EX v8,v8
MM v[24:27],a[204:207],v[220:223],v[140:143]
buffer_load_dword v103, v109, s[20:23], s76 offen offset:0 sc1
MM v[24:27],a[200:203],v[204:207],v[24:27]
s_add_u32 s76, s76, 20480
TR a[152:153],v233 offset:49664
EX v9,v9
CV v1,v2,v3
MM v[24:27],a[196:199],v[188:191],v[24:27]
buffer_load_dword v104, v109, s[20:23], s76 offen offset:0 sc1
TR a[154:155],v233 offset:53760
MM v[24:27],a[192:195],v[172:175],v[24:27]
s_and_b32 s76, s19, 65535
EX v10,v10
MM v[32:35],a[224:227],v[160:163],v[128:131]
s_sub_u32 s76, 65535, s76
TR a[156:157],v233 offset:57856
CV v2,v4,v5
EX v11,v11
MM v[32:35],a[228:231],v[176:179],v[32:35]
s_mul_i32 s76, s76, 1280
TR a[158:159],v233 offset:61952
CV v3,v6,v7
MM v[32:35],a[232:235],v[192:195],v[32:35]
s_lshl_b32 s91, s90, 7
EX v12,v12
MM v[32:35],a[236:239],v[208:211],v[32:35]
s_add_u32 s76, s76, s91
TR a[160:161],v232 offset:50176
TR a[162:163],v232 offset:54272
MM v[36:39],a[252:255],v[208:211],v[128:131]
s_add_u32 s76, s76, 131072
buffer_load_ushort v108, v110, s[20:23], s76 offen sc1
EX v13,v13
MM v[36:39],a[248:251],v[192:195],v[36:39]
s_ashr_i32 s91, s19, 31
EX v14,v14
MM v[36:39],a[244:247],v[176:179],v[36:39]
s_not_b32 s91, s91
TR a[164:165],v232 offset:58368
CV v8,v8,v9
MM v[36:39],a[240:243],v[160:163],v[36:39]
s_or_b32 s91, s91, 0x80000000
EX v15,v15
MM v[44:47],a[240:243],v[164:167],v[132:135]
s_xor_b32 s19, s19, s91
TR a[166:167],v232 offset:62464
CV v9,v10,v11
MM v[44:47],a[244:247],v[180:183],v[44:47]
s_and_b32 s19, s19, 0xffff0000
v_mov_b32 v111, 0xbfb8aa3b
v_mul_f32 v111, s19, v111
MM v[44:47],a[248:251],v[196:199],v[44:47]
EX v111,v111
EX v16,v16
MM v[44:47],a[252:255],v[212:215],v[44:47]
TR a[168:169],v233 offset:50176
v_add_f32 v111, 1.0, v111
EX v17,v17
MM v[40:43],a[236:239],v[212:215],v[132:135]
v_rcp_f32 v111, v111
MM v[40:43],a[232:235],v[196:199],v[40:43]
TR a[170:171],v233 offset:54272
CV v111,v111,v111
v_and_b32 v111, 0xffff0000, v111
EX v18,v18
MM v[40:43],a[228:231],v[180:183],v[40:43]
TR a[172:173],v233 offset:58368
CV v10,v12,v13
MM v[40:43],a[224:227],v[164:167],v[40:43]
EX v19,v19
MM v[48:51],a[224:227],v[168:171],v[136:139]
TR a[174:175],v233 offset:62464
CV v11,v14,v15
MM v[48:51],a[228:231],v[184:187],v[48:51]
EX v20,v20
MM v[48:51],a[232:235],v[200:203],v[48:51]
TR a[176:177],v232 offset:50688
TR a[178:179],v232 offset:54784
MM v[48:51],a[236:239],v[216:219],v[48:51]
EX v21,v21
MM v[52:55],a[252:255],v[216:219],v[136:139]
EX v22,v22
MM v[52:55],a[248:251],v[200:203],v[52:55]
TR a[180:181],v232 offset:58880
CV v16,v16,v17
MM v[52:55],a[244:247],v[184:187],v[52:55]
EX v23,v23
MM v[52:55],a[240:243],v[168:171],v[52:55]
TR a[182:183],v232 offset:62976
CV v17,v18,v19
MM v[60:63],a[240:243],v[172:175],v[140:143]
EX v24,v24
MM v[60:63],a[244:247],v[188:191],v[60:63]
TR a[184:185],v233 offset:50688
TR a[186:187],v233 offset:54784
MM v[60:63],a[248:251],v[204:207],v[60:63]
TR a[188:189],v233 offset:58880
TR a[190:191],v233 offset:62976
EX v25,v25
MM v[60:63],a[252:255],v[220:223],v[60:63]
EX v26,v26
MM v[56:59],a[236:239],v[220:223],v[140:143]
EX v27,v27
MM v[56:59],a[232:235],v[204:207],v[56:59]
EX v28,v28
MM v[56:59],a[228:231],v[188:191],v[56:59]
EX v29,v29
CV v18,v20,v21
MM v[56:59],a[224:227],v[172:175],v[56:59]
s_waitcnt lgkmcnt(0)
s_barrier
MM a[0:3],a[128:131],v[64:67],a[0:3]
s_add_u32 m0, s65, 49152
CV v19,v22,v23
CV v24,v24,v25
MM a[0:3],a[132:135],v[68:71],a[0:3]
CV v25,v26,v27
CV v26,v28,v29
MM a[8:11],a[132:135],v[76:79],a[8:11]
EX v30,v30
MM a[8:11],a[128:131],v[72:75],a[8:11]
EX v31,v31
MM a[16:19],a[128:131],v[80:83],a[16:19]
RD a[192:195],v224 offset:16384
CV v27,v30,v31
MM a[16:19],a[132:135],v[84:87],a[16:19]
EX v32,v32
MM a[24:27],a[132:135],v[92:95],a[24:27]
buffer_load_dwordx4 v228, s[8:11], s64 offen offset:0 lds
MM a[24:27],a[128:131],v[88:91],a[24:27]
EX v33,v33
MM a[28:31],a[136:139],v[88:91],a[28:31]
RD a[196:199],v225 offset:16384
CV v4,v32,v33
MM a[28:31],a[140:143],v[92:95],a[28:31]
EX v34,v34
MM a[20:23],a[140:143],v[84:87],a[20:23]
EX v35,v35
MM a[20:23],a[136:139],v[80:83],a[20:23]
RD a[200:203],v226 offset:16384
CV v5,v34,v35
MM a[12:15],a[136:139],v[72:75],a[12:15]
EX v36,v36
MM a[12:15],a[140:143],v[76:79],a[12:15]
buffer_load_dwordx4 v229, s[8:11], s64 offen offset:1024 lds
MM a[4:7],a[140:143],v[68:71],a[4:7]
EX v37,v37
MM a[4:7],a[136:139],v[64:67],a[4:7]
RD a[204:207],v227 offset:16384
CV v6,v36,v37
MM a[32:35],a[144:147],v[64:67],a[32:35]
EX v38,v38
MM a[32:35],a[148:151],v[68:71],a[32:35]
EX v39,v39
MM a[40:43],a[148:151],v[76:79],a[40:43]
RD a[208:211],v224 offset:20480
CV v7,v38,v39
MM a[40:43],a[144:147],v[72:75],a[40:43]
EX v40,v40
MM a[48:51],a[144:147],v[80:83],a[48:51]
buffer_load_dwordx4 v230, s[8:11], s64 offen offset:2048 lds
MM a[48:51],a[148:151],v[84:87],a[48:51]
EX v41,v41
MM a[56:59],a[148:151],v[92:95],a[56:59]
RD a[212:215],v225 offset:20480
CV v12,v40,v41
MM a[56:59],a[144:147],v[88:91],a[56:59]
EX v42,v42
MM a[60:63],a[152:155],v[88:91],a[60:63]
EX v43,v43
MM a[60:63],a[156:159],v[92:95],a[60:63]
RD a[216:219],v226 offset:20480
CV v13,v42,v43
MM a[52:55],a[156:159],v[84:87],a[52:55]
EX v44,v44
MM a[52:55],a[152:155],v[80:83],a[52:55]
buffer_load_dwordx4 v231, s[8:11], s64 offen offset:3072 lds
MM a[44:47],a[152:155],v[72:75],a[44:47]
s_add_u32 m0, s65, 114688
EX v45,v45
MM a[44:47],a[156:159],v[76:79],a[44:47]
RD a[220:223],v227 offset:20480
CV v14,v44,v45
MM a[36:39],a[156:159],v[68:71],a[36:39]
EX v46,v46
MM a[36:39],a[152:155],v[64:67],a[36:39]
RD a[224:227],v224 offset:24576
RD a[228:231],v225 offset:24576
MM a[64:67],a[160:163],v[64:67],a[64:67]
EX v47,v47
MM a[64:67],a[164:167],v[68:71],a[64:67]
RD a[232:235],v226 offset:24576
CV v15,v46,v47
MM a[72:75],a[164:167],v[76:79],a[72:75]
EX v48,v48
MM a[72:75],a[160:163],v[72:75],a[72:75]
buffer_load_dwordx4 v235, s[12:15], s81 offen offset:0 lds
MM a[80:83],a[160:163],v[80:83],a[80:83]
EX v49,v49
MM a[80:83],a[164:167],v[84:87],a[80:83]
RD a[236:239],v227 offset:24576
CV v20,v48,v49
MM a[88:91],a[164:167],v[92:95],a[88:91]
EX v50,v50
MM a[88:91],a[160:163],v[88:91],a[88:91]
EX v51,v51
MM a[92:95],a[168:171],v[88:91],a[92:95]
RD a[240:243],v224 offset:28672
CV v21,v50,v51
MM a[92:95],a[172:175],v[92:95],a[92:95]
EX v52,v52
MM a[84:87],a[172:175],v[84:87],a[84:87]
buffer_load_dwordx4 v235, s[12:15], s64 offen offset:1024 lds
MM a[84:87],a[168:171],v[80:83],a[84:87]
EX v53,v53
MM a[76:79],a[168:171],v[72:75],a[76:79]
RD a[244:247],v225 offset:28672
CV v22,v52,v53
MM a[76:79],a[172:175],v[76:79],a[76:79]
EX v54,v54
MM a[68:71],a[172:175],v[68:71],a[68:71]
s_waitcnt vmcnt(6)
v_add_f32 v96, v96, v100
v_add_f32 v97, v97, v101
MM a[68:71],a[168:171],v[64:67],a[68:71]
v_add_f32 v98, v98, v102
v_add_f32 v99, v99, v103
MM a[96:99],a[176:179],v[64:67],a[96:99]
v_add_f32 v96, v96, v98
v_add_f32 v97, v97, v99
MM a[96:99],a[180:183],v[68:71],a[96:99]
v_add_f32 v96, v96, v104
v_lshlrev_b32 v108, 16, v108
MM a[104:107],a[180:183],v[76:79],a[104:107]
v_add_f32 v96, v96, v97
CV v96,v96,v96
EX v55,v55
MM a[104:107],a[176:179],v[72:75],a[104:107]
v_and_b32 v96, 0xffff0000, v96
v_mul_f32 v96, v96, v111
MM a[112:115],a[176:179],v[80:83],a[112:115]
CV v96,v96,v96
v_and_b32 v96, 0xffff0000, v96
EX v56,v56
MM a[112:115],a[180:183],v[84:87],a[112:115]
v_add_f32 v96, v96, v108
CV v96,v96,v96
MM a[120:123],a[180:183],v[92:95],a[120:123]
s_mov_b64 exec, s[44:45]
buffer_store_short v96, v110, s[20:23], s76 offen offset:0 sc1
s_mov_b64 exec, -1
s_nop 0
EX v57,v57
MM a[120:123],a[176:179],v[88:91],a[120:123]
EX v58,v58
MM a[124:127],a[184:187],v[88:91],a[124:127]
v_add_f32 v244, v144, v145
v_add_f32 v245, v148, v149
MM a[124:127],a[188:191],v[92:95],a[124:127]
v_add_f32 v246, v152, v153
v_add_f32 v247, v156, v157
MM a[116:119],a[188:191],v[84:87],a[116:119]
)ASM"
R"ASM(
EX v59,v59
MM a[116:119],a[184:187],v[80:83],a[116:119]
EX v60,v60
MM a[108:111],a[184:187],v[72:75],a[108:111]
CV v23,v54,v55
CV v28,v56,v57
MM a[108:111],a[188:191],v[76:79],a[108:111]
EX v61,v61
MM a[100:103],a[188:191],v[68:71],a[100:103]
buffer_load_dwordx4 v235, s[12:15], s81 offen offset:2048 lds
MM a[100:103],a[184:187],v[64:67],a[100:103]
EX v62,v62
v_smfmac_f32_16x16x64_bf16 v[144:147], v[236:239], v[0:7], v234
RD a[248:251],v226 offset:28672
RD a[252:255],v227 offset:28672
EX v63,v63
v_smfmac_f32_16x16x64_bf16 v[148:151], v[236:239], v[8:15], v234
CV v29,v58,v59
v_smfmac_f32_16x16x64_bf16 v[152:155], v[236:239], v[16:23], v234
CV v30,v60,v61
CV v31,v62,v63
s_nop 1
v_smfmac_f32_16x16x64_bf16 v[156:159], v[236:239], v[24:31], v234
s_waitcnt lgkmcnt(0)
buffer_load_dwordx4 v235, s[12:15], s64 offen offset:3072 lds
MM v[64:67],a[192:195],v[160:163],v[128:131]
s_sub_u32 s64, s64, 507904
TR a[128:129],v232 offset:0
s_add_u32 s81, s64, 896
MM v[64:67],a[196:199],v[176:179],v[64:67]
TR a[130:131],v232 offset:4096
v_max3_f32 v249, v144, v145, v148
MM v[64:67],a[200:203],v[192:195],v[64:67]
TR a[132:133],v232 offset:8192
v_max3_f32 v249, v249, v149, v152
MM v[64:67],a[204:207],v[208:211],v[64:67]
TR a[134:135],v232 offset:12288
v_max3_f32 v249, v249, v153, v156
MM v[68:71],a[220:223],v[208:211],v[128:131]
TR a[136:137],v233 offset:0
v_max_f32 v249, v249, v157
MM v[68:71],a[216:219],v[192:195],v[68:71]
TR a[138:139],v233 offset:4096
v_cmp_lt_f32 vcc, s71, v249
MM v[68:71],a[212:215],v[176:179],v[68:71]
TR a[140:141],v233 offset:8192
TR a[142:143],v233 offset:12288
MM v[68:71],a[208:211],v[160:163],v[68:71]
s_cbranch_vccnz LSITE32_%=
LBACK32_%=:
MM v[76:79],a[208:211],v[164:167],v[132:135]
EX v64,v64
MM v[76:79],a[212:215],v[180:183],v[76:79]
EX v65,v65
MM v[76:79],a[216:219],v[196:199],v[76:79]
TR a[144:145],v232 offset:512
CV v64,v64,v65
MM v[76:79],a[220:223],v[212:215],v[76:79]
EX v66,v66
MM v[72:75],a[204:207],v[212:215],v[132:135]
EX v67,v67
MM v[72:75],a[200:203],v[196:199],v[72:75]
TR a[146:147],v232 offset:4608
CV v65,v66,v67
MM v[72:75],a[196:199],v[180:183],v[72:75]
EX v68,v68
MM v[72:75],a[192:195],v[164:167],v[72:75]
TR a[148:149],v232 offset:8704
TR a[150:151],v232 offset:12800
MM v[80:83],a[192:195],v[168:171],v[136:139]
EX v69,v69
MM v[80:83],a[196:199],v[184:187],v[80:83]
TR a[152:153],v233 offset:512
CV v66,v68,v69
MM v[80:83],a[200:203],v[200:203],v[80:83]
EX v70,v70
MM v[80:83],a[204:207],v[216:219],v[80:83]
TR a[154:155],v233 offset:4608
TR a[156:157],v233 offset:8704
MM v[84:87],a[220:223],v[216:219],v[136:139]
EX v71,v71
MM v[84:87],a[216:219],v[200:203],v[84:87]
TR a[158:159],v233 offset:12800
CV v67,v70,v71
MM v[84:87],a[212:215],v[184:187],v[84:87]
EX v72,v72
MM v[84:87],a[208:211],v[168:171],v[84:87]
EX v73,v73
MM v[92:95],a[208:211],v[172:175],v[140:143]
TR a[160:161],v232 offset:1024
CV v72,v72,v73
MM v[92:95],a[212:215],v[188:191],v[92:95]
EX v74,v74
MM v[92:95],a[216:219],v[204:207],v[92:95]
TR a[162:163],v232 offset:5120
TR a[164:165],v232 offset:9216
MM v[92:95],a[220:223],v[220:223],v[92:95]
EX v75,v75
MM v[88:91],a[204:207],v[220:223],v[140:143]
TR a[166:167],v232 offset:13312
CV v73,v74,v75
MM v[88:91],a[200:203],v[204:207],v[88:91]
EX v76,v76
MM v[88:91],a[196:199],v[188:191],v[88:91]
EX v77,v77
MM v[88:91],a[192:195],v[172:175],v[88:91]
TR a[168:169],v233 offset:1024
CV v74,v76,v77
MM v[96:99],a[224:227],v[160:163],v[128:131]
EX v78,v78
MM v[96:99],a[228:231],v[176:179],v[96:99]
TR a[170:171],v233 offset:5120
TR a[172:173],v233 offset:9216
MM v[96:99],a[232:235],v[192:195],v[96:99]
EX v79,v79
MM v[96:99],a[236:239],v[208:211],v[96:99]
TR a[174:175],v233 offset:13312
CV v75,v78,v79
MM v[100:103],a[252:255],v[208:211],v[128:131]
EX v80,v80
MM v[100:103],a[248:251],v[192:195],v[100:103]
EX v81,v81
MM v[100:103],a[244:247],v[176:179],v[100:103]
TR a[176:177],v232 offset:1536
CV v80,v80,v81
MM v[100:103],a[240:243],v[160:163],v[100:103]
EX v82,v82
MM v[108:111],a[240:243],v[164:167],v[132:135]
TR a[178:179],v232 offset:5632
TR a[180:181],v232 offset:9728
MM v[108:111],a[244:247],v[180:183],v[108:111]
EX v83,v83
MM v[108:111],a[248:251],v[196:199],v[108:111]
TR a[182:183],v232 offset:13824
CV v81,v82,v83
MM v[108:111],a[252:255],v[212:215],v[108:111]
EX v84,v84
MM v[104:107],a[236:239],v[212:215],v[132:135]
TR a[184:185],v233 offset:1536
TR a[186:187],v233 offset:5632
MM v[104:107],a[232:235],v[196:199],v[104:107]
EX v85,v85
MM v[104:107],a[228:231],v[180:183],v[104:107]
TR a[188:189],v233 offset:9728
CV v82,v84,v85
MM v[104:107],a[224:227],v[164:167],v[104:107]
EX v86,v86
MM v[112:115],a[224:227],v[168:171],v[136:139]
EX v87,v87
MM v[112:115],a[228:231],v[184:187],v[112:115]
TR a[190:191],v233 offset:13824
CV v83,v86,v87
MM v[112:115],a[232:235],v[200:203],v[112:115]
EX v88,v88
MM v[112:115],a[236:239],v[216:219],v[112:115]
EX v89,v89
MM v[116:119],a[252:255],v[216:219],v[136:139]
EX v90,v90
MM v[116:119],a[248:251],v[200:203],v[116:119]
EX v91,v91
MM v[116:119],a[244:247],v[184:187],v[116:119]
EX v92,v92
MM v[116:119],a[240:243],v[168:171],v[116:119]
EX v93,v93
MM v[124:127],a[240:243],v[172:175],v[140:143]
EX v94,v94
MM v[124:127],a[244:247],v[188:191],v[124:127]
EX v95,v95
MM v[124:127],a[248:251],v[204:207],v[124:127]
EX v96,v96
MM v[124:127],a[252:255],v[220:223],v[124:127]
EX v97,v97
MM v[120:123],a[236:239],v[220:223],v[140:143]
EX v98,v98
MM v[120:123],a[232:235],v[204:207],v[120:123]
EX v99,v99
MM v[120:123],a[228:231],v[188:191],v[120:123]
EX v100,v100
MM v[120:123],a[224:227],v[172:175],v[120:123]
s_waitcnt lgkmcnt(0)
s_barrier
MM a[0:3],a[128:131],v[0:3],a[0:3]
s_add_u32 m0, s65, 0
CV v88,v88,v89
CV v89,v90,v91
MM a[0:3],a[132:135],v[4:7],a[0:3]
RD a[192:195],v224 offset:32768
CV v90,v92,v93
MM a[8:11],a[132:135],v[12:15],a[8:11]
RD a[196:199],v225 offset:32768
CV v91,v94,v95
MM a[8:11],a[128:131],v[8:11],a[8:11]
buffer_load_dwordx4 v228, s[8:11], s64 offen offset:0 lds
MM a[16:19],a[128:131],v[16:19],a[16:19]
RD a[200:203],v226 offset:32768
CV v68,v96,v97
MM a[16:19],a[132:135],v[20:23],a[16:19]
RD a[204:207],v227 offset:32768
CV v69,v98,v99
MM a[24:27],a[132:135],v[28:31],a[24:27]
buffer_load_dwordx4 v229, s[8:11], s64 offen offset:1024 lds
MM a[24:27],a[128:131],v[24:27],a[24:27]
RD a[208:211],v224 offset:36864
RD a[212:215],v225 offset:36864
MM a[28:31],a[136:139],v[24:27],a[28:31]
buffer_load_dwordx4 v230, s[8:11], s64 offen offset:2048 lds
MM a[28:31],a[140:143],v[28:31],a[28:31]
RD a[216:219],v226 offset:36864
RD a[220:223],v227 offset:36864
MM a[20:23],a[140:143],v[20:23],a[20:23]
EX v101,v101
MM a[20:23],a[136:139],v[16:19],a[20:23]
RD a[224:227],v224 offset:40960
CV v70,v100,v101
MM a[12:15],a[136:139],v[8:11],a[12:15]
EX v102,v102
MM a[12:15],a[140:143],v[12:15],a[12:15]
buffer_load_dwordx4 v231, s[8:11], s64 offen offset:3072 lds
MM a[4:7],a[140:143],v[4:7],a[4:7]
s_add_u32 m0, s65, 65536
EX v103,v103
MM a[4:7],a[136:139],v[0:3],a[4:7]
RD a[228:231],v225 offset:40960
CV v71,v102,v103
MM a[32:35],a[144:147],v[0:3],a[32:35]
EX v104,v104
MM a[32:35],a[148:151],v[4:7],a[32:35]
EX v105,v105
MM a[40:43],a[148:151],v[12:15],a[40:43]
RD a[232:235],v226 offset:40960
CV v76,v104,v105
MM a[40:43],a[144:147],v[8:11],a[40:43]
EX v106,v106
MM a[48:51],a[144:147],v[16:19],a[48:51]
buffer_load_dwordx4 v235, s[12:15], s81 offen offset:0 lds
MM a[48:51],a[148:151],v[20:23],a[48:51]
EX v107,v107
MM a[56:59],a[148:151],v[28:31],a[56:59]
RD a[236:239],v227 offset:40960
CV v77,v106,v107
MM a[56:59],a[144:147],v[24:27],a[56:59]
EX v108,v108
MM a[60:63],a[152:155],v[24:27],a[60:63]
EX v109,v109
MM a[60:63],a[156:159],v[28:31],a[60:63]
RD a[240:243],v224 offset:45056
CV v78,v108,v109
MM a[52:55],a[156:159],v[20:23],a[52:55]
EX v110,v110
MM a[52:55],a[152:155],v[16:19],a[52:55]
buffer_load_dwordx4 v235, s[12:15], s64 offen offset:1024 lds
MM a[44:47],a[152:155],v[8:11],a[44:47]
EX v111,v111
MM a[44:47],a[156:159],v[12:15],a[44:47]
RD a[244:247],v225 offset:45056
CV v79,v110,v111
MM a[36:39],a[156:159],v[4:7],a[36:39]
EX v112,v112
MM a[36:39],a[152:155],v[0:3],a[36:39]
EX v113,v113
MM a[64:67],a[160:163],v[0:3],a[64:67]
RD a[248:251],v226 offset:45056
CV v84,v112,v113
MM a[64:67],a[164:167],v[4:7],a[64:67]
EX v114,v114
MM a[72:75],a[164:167],v[12:15],a[72:75]
buffer_load_dwordx4 v235, s[12:15], s81 offen offset:2048 lds
MM a[72:75],a[160:163],v[8:11],a[72:75]
EX v115,v115
MM a[80:83],a[160:163],v[16:19],a[80:83]
RD a[252:255],v227 offset:45056
CV v85,v114,v115
MM a[80:83],a[164:167],v[20:23],a[80:83]
EX v116,v116
MM a[88:91],a[164:167],v[28:31],a[88:91]
EX v117,v117
MM a[88:91],a[160:163],v[24:27],a[88:91]
buffer_load_dwordx4 v235, s[12:15], s64 offen offset:3072 lds
MM a[92:95],a[168:171],v[24:27],a[92:95]
EX v118,v118
MM a[92:95],a[172:175],v[28:31],a[92:95]
EX v119,v119
MM a[84:87],a[172:175],v[20:23],a[84:87]
EX v120,v120
MM a[84:87],a[168:171],v[16:19],a[84:87]
EX v121,v121
MM a[76:79],a[168:171],v[8:11],a[76:79]
EX v122,v122
MM a[76:79],a[172:175],v[12:15],a[76:79]
EX v123,v123
MM a[68:71],a[172:175],v[4:7],a[68:71]
EX v124,v124
MM a[68:71],a[168:171],v[0:3],a[68:71]
EX v125,v125
MM a[96:99],a[176:179],v[0:3],a[96:99]
EX v126,v126
MM a[96:99],a[180:183],v[4:7],a[96:99]
EX v127,v127
MM a[104:107],a[180:183],v[12:15],a[104:107]
CV v86,v116,v117
CV v87,v118,v119
MM a[104:107],a[176:179],v[8:11],a[104:107]
CV v92,v120,v121
CV v93,v122,v123
MM a[112:115],a[176:179],v[16:19],a[112:115]
CV v94,v124,v125
CV v95,v126,v127
MM a[112:115],a[180:183],v[20:23],a[112:115]
MM a[120:123],a[180:183],v[28:31],a[120:123]
s_lshl_b32 s91, s17, 2
s_add_u32 s91, s91, 94464
s_and_b64 s[44:45], s[44:45], 1
MM a[120:123],a[176:179],v[24:27],a[120:123]
s_waitcnt vmcnt(10)
v_mov_b32 v250, s69
s_mov_b64 exec, s[44:45]
buffer_store_dword v250, off, s[20:23], s91 sc1
s_mov_b64 exec, -1
MM a[124:127],a[184:187],v[24:27],a[124:127]
v_add_f32 v244, v144, v145
v_add_f32 v245, v148, v149
MM a[124:127],a[188:191],v[28:31],a[124:127]
v_add_f32 v246, v152, v153
v_add_f32 v247, v156, v157
MM a[116:119],a[188:191],v[20:23],a[116:119]
MM a[116:119],a[184:187],v[16:19],a[116:119]
MM a[108:111],a[184:187],v[8:11],a[108:111]
MM a[108:111],a[188:191],v[12:15],a[108:111]
MM a[100:103],a[188:191],v[4:7],a[100:103]
MM a[100:103],a[184:187],v[0:3],a[100:103]
v_smfmac_f32_16x16x64_bf16 v[144:147], v[236:239], v[64:71], v234
v_smfmac_f32_16x16x64_bf16 v[148:151], v[236:239], v[72:79], v234
s_nop 0
LPG23_%=:
s_cbranch_execz LPG27_%=
v_smfmac_f32_16x16x64_bf16 v[152:155], v[236:239], v[80:87], v234
v_smfmac_f32_16x16x64_bf16 v[156:159], v[236:239], v[88:95], v234
s_waitcnt lgkmcnt(0)
LLAJ_%=:
MM v[0:3],a[192:195],v[160:163],v[128:131]
s_add_u32 s64, s64, 16384
TR a[128:129],v232 offset:16384
s_add_u32 s81, s64, 896
MM v[0:3],a[196:199],v[176:179],v[0:3]
TR a[130:131],v232 offset:20480
v_max3_f32 v249, v144, v145, v148
MM v[0:3],a[200:203],v[192:195],v[0:3]
TR a[132:133],v232 offset:24576
v_max3_f32 v249, v249, v149, v152
MM v[0:3],a[204:207],v[208:211],v[0:3]
TR a[134:135],v232 offset:28672
v_max3_f32 v249, v249, v153, v156
MM v[4:7],a[220:223],v[208:211],v[128:131]
TR a[136:137],v233 offset:16384
v_max_f32 v249, v249, v157
MM v[4:7],a[216:219],v[192:195],v[4:7]
TR a[138:139],v233 offset:20480
v_cmp_lt_f32 vcc, s71, v249
MM v[4:7],a[212:215],v[176:179],v[4:7]
TR a[140:141],v233 offset:24576
TR a[142:143],v233 offset:28672
MM v[4:7],a[208:211],v[160:163],v[4:7]
s_cbranch_vccnz LSITE33_%=
LBACK33_%=:
MM v[12:15],a[208:211],v[164:167],v[132:135]
v_bfe_u32 v242, v248, 6, 4
v_mul_u32_u24 v242, 1280, v242
MM v[12:15],a[212:215],v[180:183],v[12:15]
v_and_b32 v252, 48, v248
v_bfe_u32 v243, v248, 8, 1
MM v[12:15],a[216:219],v[196:199],v[12:15]
v_lshlrev_b32 v243, 5, v243
v_xor_b32 v252, v252, v243
MM v[12:15],a[220:223],v[212:215],v[12:15]
v_add_u32 v252, v252, v242
v_lshrrev_b32 v242, 3, v248
MM v[8:11],a[204:207],v[212:215],v[132:135]
v_xor_b32 v243, v242, v248
v_and_b32 v243, 96, v243
MM v[8:11],a[200:203],v[196:199],v[8:11]
TR a[144:145],v232 offset:16896
v_and_or_b32 v243, v248, 16, v243
MM v[8:11],a[196:199],v[180:183],v[8:11]
)ASM"
R"ASM(
s_and_b32 s66, s49, 8
EX v0,v0
MM v[8:11],a[192:195],v[164:167],v[8:11]
s_lshl_b32 s66, s66, 3
v_lshrrev_b32 v242, 7, v248
v_xor_b32 v243, s66, v243
EX v1,v1
MM v[16:19],a[192:195],v[168:171],v[136:139]
v_mul_u32_u24 v242, 1280, v242
v_add_u32 v240, v242, v243
EX v2,v2
MM v[16:19],a[196:199],v[184:187],v[16:19]
s_mul_i32 s88, %[swave], 40960
EX v3,v3
MM v[16:19],a[200:203],v[200:203],v[16:19]
s_and_b32 s66, s84, 1
EX v4,v4
MM v[16:19],a[204:207],v[216:219],v[16:19]
s_lshl_b32 s86, s66, 6
TR a[146:147],v232 offset:20992
CV v0,v0,v1
MM v[20:23],a[220:223],v[216:219],v[136:139]
s_add_u32 s86, s86, s88
EX v5,v5
MM v[20:23],a[216:219],v[200:203],v[20:23]
s_xor_b32 s66, s66, 1
TR a[148:149],v232 offset:25088
CV v1,v2,v3
MM v[20:23],a[212:215],v[184:187],v[20:23]
s_lshl_b32 s66, s66, 8
EX v6,v6
MM v[20:23],a[208:211],v[168:171],v[20:23]
s_add_u32 s88, s88, s66
EX v7,v7
MM v[28:31],a[208:211],v[172:175],v[140:143]
s_and_b32 s66, s49, 16
TR a[150:151],v232 offset:29184
TR a[152:153],v233 offset:16896
MM v[28:31],a[212:215],v[188:191],v[28:31]
s_lshl_b32 s66, s66, 3
EX v8,v8
MM v[28:31],a[216:219],v[204:207],v[28:31]
s_sub_u32 s87, s86, s66
TR a[154:155],v233 offset:20992
CV v2,v4,v5
MM v[28:31],a[220:223],v[220:223],v[28:31]
s_add_u32 s87, s87, 128
EX v9,v9
MM v[24:27],a[204:207],v[220:223],v[140:143]
s_add_u32 s86, s86, s66
TR a[156:157],v233 offset:25088
CV v3,v6,v7
MM v[24:27],a[200:203],v[204:207],v[24:27]
s_and_b32 s94, s49, 7
EX v10,v10
MM v[24:27],a[196:199],v[188:191],v[24:27]
s_lshl_b32 s89, %[swave], 11
TR a[158:159],v233 offset:29184
CV v8,v8,v9
MM v[24:27],a[192:195],v[172:175],v[24:27]
s_add_u32 s89, s89, %[slds]
EX v11,v11
MM v[32:35],a[224:227],v[160:163],v[128:131]
s_xor_b32 s66, s94, 4
TR a[160:161],v232 offset:17408
TR a[162:163],v232 offset:21504
MM v[32:35],a[228:231],v[176:179],v[32:35]
s_mul_i32 s66, s66, 163840
EX v12,v12
MM v[32:35],a[232:235],v[192:195],v[32:35]
s_add_u32 s66, s66, s88
s_add_u32 m0, s89, 131072
s_nop 0
buffer_load_dwordx4 v252, s[24:27], s66 offen offset:0 lds
MM v[32:35],a[236:239],v[208:211],v[32:35]
s_add_u32 s66, s66, 19456
EX v13,v13
MM v[36:39],a[252:255],v[208:211],v[128:131]
TR a[164:165],v232 offset:25600
CV v9,v10,v11
MM v[36:39],a[248:251],v[192:195],v[36:39]
EX v14,v14
MM v[36:39],a[244:247],v[176:179],v[36:39]
EX v15,v15
MM v[36:39],a[240:243],v[160:163],v[36:39]
TR a[166:167],v232 offset:29696
TR a[168:169],v233 offset:17408
MM v[44:47],a[240:243],v[164:167],v[132:135]
EX v16,v16
MM v[44:47],a[244:247],v[180:183],v[44:47]
TR a[170:171],v233 offset:21504
CV v10,v12,v13
MM v[44:47],a[248:251],v[196:199],v[44:47]
EX v17,v17
MM v[44:47],a[252:255],v[212:215],v[44:47]
s_add_u32 m0, s89, 131072
s_nop 0
buffer_load_dwordx4 v252, s[24:27], s66 offen offset:1024 lds
MM v[40:43],a[236:239],v[212:215],v[132:135]
EX v18,v18
MM v[40:43],a[232:235],v[196:199],v[40:43]
TR a[172:173],v233 offset:25600
CV v11,v14,v15
MM v[40:43],a[228:231],v[180:183],v[40:43]
EX v19,v19
MM v[40:43],a[224:227],v[164:167],v[40:43]
TR a[174:175],v233 offset:29696
TR a[176:177],v232 offset:17920
MM v[48:51],a[224:227],v[168:171],v[136:139]
EX v20,v20
MM v[48:51],a[228:231],v[184:187],v[48:51]
TR a[178:179],v232 offset:22016
CV v16,v16,v17
MM v[48:51],a[232:235],v[200:203],v[48:51]
EX v21,v21
MM v[48:51],a[236:239],v[216:219],v[48:51]
s_lshl_b32 s60, s50, 2
TR a[180:181],v232 offset:26112
CV v17,v18,v19
MM v[52:55],a[252:255],v[216:219],v[136:139]
s_add_u32 s60, s60, %[swave]
EX v22,v22
MM v[52:55],a[248:251],v[200:203],v[52:55]
s_lshl_b32 s60, s60, 1
TR a[182:183],v232 offset:30208
TR a[184:185],v233 offset:17920
MM v[52:55],a[244:247],v[184:187],v[52:55]
s_bfe_u32 s61, s49, 65538
EX v23,v23
MM v[52:55],a[240:243],v[168:171],v[52:55]
s_xor_b32 s61, s61, 1
EX v24,v24
MM v[60:63],a[240:243],v[172:175],v[140:143]
s_add_u32 s60, s60, s61
TR a[186:187],v233 offset:22016
CV v18,v20,v21
MM v[60:63],a[244:247],v[188:191],v[60:63]
s_mul_i32 s18, s60, 40960
EX v25,v25
MM v[60:63],a[248:251],v[204:207],v[60:63]
s_mul_i32 s60, s84, 320
TR a[188:189],v233 offset:26112
TR a[190:191],v233 offset:30208
MM v[60:63],a[252:255],v[220:223],v[60:63]
s_add_u32 s18, s18, s60
EX v26,v26
MM v[56:59],a[236:239],v[220:223],v[140:143]
s_lshr_b32 s62, s49, 3
CV v19,v22,v23
CV v24,v24,v25
MM v[56:59],a[232:235],v[204:207],v[56:59]
s_mul_i32 s63, s62, 10240
EX v27,v27
MM v[56:59],a[228:231],v[188:191],v[56:59]
s_add_u32 s63, s63, s18
EX v28,v28
MM v[56:59],a[224:227],v[172:175],v[56:59]
s_waitcnt vmcnt(14) lgkmcnt(0)
s_barrier
MM a[0:3],a[128:131],v[64:67],a[0:3]
s_and_b32 s60, s84, 1
EX v29,v29
MM a[0:3],a[132:135],v[68:71],a[0:3]
s_add_u32 s61, s60, s62
CV v25,v26,v27
CV v26,v28,v29
MM a[8:11],a[132:135],v[76:79],a[8:11]
s_lshl_b32 s61, s61, 6
EX v30,v30
MM a[8:11],a[128:131],v[72:75],a[8:11]
s_add_u32 s18, s18, s61
EX v31,v31
MM a[16:19],a[128:131],v[80:83],a[16:19]
s_xor_b32 s60, s60, 1
RD a[192:195],v224 offset:49152
CV v27,v30,v31
MM a[16:19],a[132:135],v[84:87],a[16:19]
s_lshl_b32 s60, s60, 8
EX v32,v32
MM a[24:27],a[132:135],v[92:95],a[24:27]
s_add_u32 s63, s63, s60
s_add_u32 m0, s65, 16384
RD a[196:199],v225 offset:49152
MM a[24:27],a[128:131],v[88:91],a[24:27]
s_mul_i32 s60, s62, 33792
EX v33,v33
MM a[28:31],a[136:139],v[88:91],a[28:31]
s_sub_u32 s60, s95, s60
buffer_load_dwordx4 v228, s[8:11], s64 offen offset:0 lds
MM a[28:31],a[140:143],v[92:95],a[28:31]
s_xor_b32 s61, s62, 1
EX v34,v34
MM a[20:23],a[140:143],v[84:87],a[20:23]
s_mul_i32 s61, s61, 33792
EX v35,v35
MM a[20:23],a[136:139],v[80:83],a[20:23]
s_add_u32 s61, s61, s60
RD a[200:203],v226 offset:49152
CV v4,v32,v33
MM a[12:15],a[136:139],v[72:75],a[12:15]
s_add_u32 s36, s61, 2048
EX v36,v36
MM a[12:15],a[140:143],v[76:79],a[12:15]
s_xor_b32 s61, s62, 2
CV v5,v34,v35
MM a[4:7],a[140:143],v[68:71],a[4:7]
s_mul_i32 s61, s61, 33792
EX v37,v37
MM a[4:7],a[136:139],v[64:67],a[4:7]
s_add_u32 s61, s61, s60
buffer_load_dwordx4 v229, s[8:11], s64 offen offset:1024 lds
MM a[32:35],a[144:147],v[64:67],a[32:35]
s_add_u32 s37, s61, 4096
EX v38,v38
MM a[32:35],a[148:151],v[68:71],a[32:35]
s_xor_b32 s61, s62, 3
RD a[204:207],v227 offset:49152
CV v6,v36,v37
MM a[40:43],a[148:151],v[76:79],a[40:43]
s_mul_i32 s61, s61, 33792
EX v39,v39
MM a[40:43],a[144:147],v[72:75],a[40:43]
s_add_u32 s61, s61, s60
RD a[208:211],v224 offset:53248
CV v7,v38,v39
MM a[48:51],a[144:147],v[80:83],a[48:51]
s_add_u32 s38, s61, 6144
EX v40,v40
MM a[48:51],a[148:151],v[84:87],a[48:51]
s_lshl_b32 s61, s62, 9
EX v41,v41
MM a[56:59],a[148:151],v[92:95],a[56:59]
s_add_u32 s39, s60, s61
RD a[212:215],v225 offset:53248
MM a[56:59],a[144:147],v[88:91],a[56:59]
s_mov_b32 s17, 15
buffer_load_dwordx4 v230, s[8:11], s64 offen offset:2048 lds
MM a[60:63],a[152:155],v[88:91],a[60:63]
s_mov_b32 s58, 0
EX v42,v42
MM a[60:63],a[156:159],v[92:95],a[60:63]
s_lshl_b32 s60, s50, 1
EX v43,v43
MM a[52:55],a[156:159],v[84:87],a[52:55]
s_bfe_u32 s61, s49, 65538
RD a[216:219],v226 offset:53248
CV v12,v40,v41
MM a[52:55],a[152:155],v[80:83],a[52:55]
s_add_u32 s60, s60, s61
EX v44,v44
MM a[44:47],a[152:155],v[72:75],a[44:47]
s_lshl_b32 s60, s60, 2
CV v13,v42,v43
MM a[44:47],a[156:159],v[76:79],a[44:47]
s_add_u32 s60, s60, s84
EX v45,v45
MM a[36:39],a[156:159],v[68:71],a[36:39]
s_lshl_b32 s60, s60, 2
buffer_load_dwordx4 v231, s[8:11], s64 offen offset:3072 lds
MM a[36:39],a[152:155],v[64:67],a[36:39]
s_add_u32 s68, s60, %[swave]
EX v46,v46
MM a[64:67],a[160:163],v[64:67],a[64:67]
s_lshl_b32 s68, s68, 6
RD a[220:223],v227 offset:53248
CV v14,v44,v45
MM a[64:67],a[164:167],v[68:71],a[64:67]
s_add_u32 s68, s68, 16384
EX v47,v47
MM a[72:75],a[164:167],v[76:79],a[72:75]
s_lshr_b32 s61, s49, 3
EX v48,v48
MM a[72:75],a[160:163],v[72:75],a[72:75]
s_lshl_b32 s19, s61, 4
RD a[224:227],v224 offset:57344
CV v15,v46,v47
MM a[80:83],a[160:163],v[80:83],a[80:83]
s_add_u32 s19, s19, s68
EX v49,v49
MM a[80:83],a[164:167],v[84:87],a[80:83]
s_lshr_b32 s96, s49, 2
s_add_u32 m0, s65, 81920
CV v20,v48,v49
MM a[88:91],a[164:167],v[92:95],a[88:91]
s_lshl_b32 s97, s50, 3
EX v50,v50
MM a[88:91],a[160:163],v[88:91],a[88:91]
s_add_u32 s96, s96, s97
buffer_load_dwordx4 v235, s[12:15], s81 offen offset:0 lds
MM a[92:95],a[168:171],v[88:91],a[92:95]
s_lshl_b32 s96, s96, 2
EX v51,v51
MM a[92:95],a[172:175],v[92:95],a[92:95]
s_add_u32 s96, s96, %[swave]
RD a[228:231],v225 offset:57344
CV v21,v50,v51
MM a[84:87],a[172:175],v[84:87],a[84:87]
s_lshl_b32 s96, s96, 6
EX v52,v52
MM a[84:87],a[168:171],v[80:83],a[84:87]
s_add_u32 s96, s96, 0
RD a[232:235],v226 offset:57344
MM a[76:79],a[168:171],v[72:75],a[76:79]
s_xor_b32 s44, s96, 256
EX v53,v53
MM a[76:79],a[172:175],v[76:79],a[76:79]
s_xor_b32 s66, s94, 5
RD a[236:239],v227 offset:57344
CV v22,v52,v53
MM a[68:71],a[172:175],v[68:71],a[68:71]
s_mul_i32 s66, s66, 163840
EX v54,v54
MM a[68:71],a[168:171],v[64:67],a[68:71]
s_add_u32 s66, s66, s88
s_add_u32 m0, s89, 139264
s_nop 0
buffer_load_dwordx4 v252, s[24:27], s66 offen offset:0 lds
MM a[96:99],a[176:179],v[64:67],a[96:99]
s_add_u32 s66, s66, 19456
EX v55,v55
MM a[96:99],a[180:183],v[68:71],a[96:99]
s_add_u32 m0, s65, 81920
s_nop 0
buffer_load_dwordx4 v235, s[12:15], s64 offen offset:1024 lds
MM a[104:107],a[180:183],v[76:79],a[104:107]
EX v56,v56
MM a[104:107],a[176:179],v[72:75],a[104:107]
RD a[240:243],v224 offset:61440
CV v23,v54,v55
MM a[112:115],a[176:179],v[80:83],a[112:115]
EX v57,v57
MM a[112:115],a[180:183],v[84:87],a[112:115]
EX v58,v58
MM a[120:123],a[180:183],v[92:95],a[120:123]
RD a[244:247],v225 offset:61440
CV v28,v56,v57
MM a[120:123],a[176:179],v[88:91],a[120:123]
EX v59,v59
MM a[124:127],a[184:187],v[88:91],a[124:127]
v_add_f32 v244, v144, v145
v_add_f32 v245, v148, v149
MM a[124:127],a[188:191],v[92:95],a[124:127]
v_add_f32 v246, v152, v153
v_add_f32 v247, v156, v157
MM a[116:119],a[188:191],v[84:87],a[116:119]
s_add_u32 m0, s89, 139264
s_nop 0
buffer_load_dwordx4 v252, s[24:27], s66 offen offset:1024 lds
MM a[116:119],a[184:187],v[80:83],a[116:119]
EX v60,v60
MM a[108:111],a[184:187],v[72:75],a[108:111]
EX v61,v61
MM a[108:111],a[188:191],v[76:79],a[108:111]
s_add_u32 m0, s65, 81920
s_nop 0
buffer_load_dwordx4 v235, s[12:15], s81 offen offset:2048 lds
MM a[100:103],a[188:191],v[68:71],a[100:103]
EX v62,v62
MM a[100:103],a[184:187],v[64:67],a[100:103]
RD a[248:251],v226 offset:61440
CV v29,v58,v59
v_smfmac_f32_16x16x64_bf16 v[144:147], v[236:239], v[0:7], v234
RD a[252:255],v227 offset:61440
EX v63,v63
v_smfmac_f32_16x16x64_bf16 v[148:151], v[236:239], v[8:15], v234
CV v30,v60,v61
v_smfmac_f32_16x16x64_bf16 v[152:155], v[236:239], v[16:23], v234
CV v31,v62,v63
s_nop 1
v_smfmac_f32_16x16x64_bf16 v[156:159], v[236:239], v[24:31], v234
s_waitcnt lgkmcnt(0)
buffer_load_dwordx4 v235, s[12:15], s64 offen offset:3072 lds
MM v[64:67],a[192:195],v[160:163],v[128:131]
s_add_u32 s64, s64, 16384
TR a[128:129],v232 offset:32768
s_add_u32 s81, s64, 896
MM v[64:67],a[196:199],v[176:179],v[64:67]
TR a[130:131],v232 offset:36864
v_max3_f32 v249, v144, v145, v148
MM v[64:67],a[200:203],v[192:195],v[64:67]
TR a[132:133],v232 offset:40960
v_max3_f32 v249, v249, v149, v152
MM v[64:67],a[204:207],v[208:211],v[64:67]
TR a[134:135],v232 offset:45056
v_max3_f32 v249, v249, v153, v156
MM v[68:71],a[220:223],v[208:211],v[128:131]
TR a[136:137],v233 offset:32768
v_max_f32 v249, v249, v157
MM v[68:71],a[216:219],v[192:195],v[68:71]
TR a[138:139],v233 offset:36864
v_cmp_lt_f32 vcc, s71, v249
MM v[68:71],a[212:215],v[176:179],v[68:71]
TR a[140:141],v233 offset:40960
TR a[142:143],v233 offset:45056
MM v[68:71],a[208:211],v[160:163],v[68:71]
s_cbranch_vccnz LSITE34_%=
LBACK34_%=:
MM v[76:79],a[208:211],v[164:167],v[132:135]
buffer_load_dword v242, off, s[20:23], s96 sc1
MM v[76:79],a[212:215],v[180:183],v[76:79]
buffer_load_dword v243, off, s[20:23], s19 sc1
MM v[76:79],a[216:219],v[196:199],v[76:79]
s_xor_b32 s66, s94, 6
v_and_b32 v32, 496, v248
v_mul_u32_u24 v32, 80, v32
MM v[76:79],a[220:223],v[212:215],v[76:79]
s_mul_i32 s66, s66, 163840
EX v64,v64
MM v[72:75],a[204:207],v[212:215],v[132:135]
s_add_u32 s66, s66, s88
s_add_u32 m0, s89, 147456
)ASM"
R"ASM(
s_nop 0
buffer_load_dwordx4 v252, s[24:27], s66 offen offset:0 lds
MM v[72:75],a[200:203],v[196:199],v[72:75]
s_add_u32 s66, s66, 19456
EX v65,v65
MM v[72:75],a[196:199],v[180:183],v[72:75]
s_sub_u32 s60, s63, s18
v_add_u32 v32, s18, v32
CV v64,v64,v65
MM v[72:75],a[192:195],v[164:167],v[72:75]
s_mov_b32 s61, 131072
s_mov_b32 exec_lo, 0
v_add_u32 v32, s60, v32
s_mov_b64 exec, -1
MM v[80:83],a[192:195],v[168:171],v[136:139]
s_mov_b32 exec_hi, 0xff
buffer_load_dword v36, v32, s[32:35], 0 offen offset:0
s_mov_b64 exec, -1
EX v66,v66
MM v[80:83],a[196:199],v[184:187],v[80:83]
EX v67,v67
MM v[80:83],a[200:203],v[200:203],v[80:83]
EX v68,v68
MM v[80:83],a[204:207],v[216:219],v[80:83]
s_mov_b32 exec_hi, 0xff
buffer_load_dword v37, v32, s[20:23], s61 offen offset:0 sc1
s_mov_b64 exec, -1
MM v[84:87],a[220:223],v[216:219],v[136:139]
TR a[144:145],v232 offset:33280
TR a[146:147],v232 offset:37376
MM v[84:87],a[216:219],v[200:203],v[84:87]
s_add_u32 m0, s89, 147456
s_nop 0
buffer_load_dwordx4 v252, s[24:27], s66 offen offset:1024 lds
EX v69,v69
MM v[84:87],a[212:215],v[184:187],v[84:87]
EX v70,v70
MM v[84:87],a[208:211],v[168:171],v[84:87]
TR a[148:149],v232 offset:41472
CV v65,v66,v67
MM v[92:95],a[208:211],v[172:175],v[140:143]
EX v71,v71
MM v[92:95],a[212:215],v[188:191],v[92:95]
TR a[150:151],v232 offset:45568
TR a[152:153],v233 offset:33280
MM v[92:95],a[216:219],v[204:207],v[92:95]
EX v72,v72
MM v[92:95],a[220:223],v[220:223],v[92:95]
EX v73,v73
MM v[88:91],a[204:207],v[220:223],v[140:143]
TR a[154:155],v233 offset:37376
CV v66,v68,v69
MM v[88:91],a[200:203],v[204:207],v[88:91]
s_xor_b32 s66, s94, 7
EX v74,v74
MM v[88:91],a[196:199],v[188:191],v[88:91]
s_mul_i32 s66, s66, 163840
TR a[156:157],v233 offset:41472
CV v67,v70,v71
MM v[88:91],a[192:195],v[172:175],v[88:91]
s_add_u32 s66, s66, s88
s_add_u32 m0, s89, 155648
s_nop 0
buffer_load_dwordx4 v252, s[24:27], s66 offen offset:0 lds
MM v[96:99],a[224:227],v[160:163],v[128:131]
s_add_u32 s66, s66, 19456
EX v75,v75
MM v[96:99],a[228:231],v[176:179],v[96:99]
TR a[158:159],v233 offset:45568
TR a[160:161],v232 offset:33792
MM v[96:99],a[232:235],v[192:195],v[96:99]
EX v76,v76
MM v[96:99],a[236:239],v[208:211],v[96:99]
EX v77,v77
MM v[100:103],a[252:255],v[208:211],v[128:131]
TR a[162:163],v232 offset:37888
CV v72,v72,v73
MM v[100:103],a[248:251],v[192:195],v[100:103]
EX v78,v78
MM v[100:103],a[244:247],v[176:179],v[100:103]
TR a[164:165],v232 offset:41984
CV v73,v74,v75
MM v[100:103],a[240:243],v[160:163],v[100:103]
EX v79,v79
MM v[108:111],a[240:243],v[164:167],v[132:135]
s_add_u32 m0, s89, 155648
s_nop 0
buffer_load_dwordx4 v252, s[24:27], s66 offen offset:1024 lds
MM v[108:111],a[244:247],v[180:183],v[108:111]
TR a[166:167],v232 offset:46080
TR a[168:169],v233 offset:33792
MM v[108:111],a[248:251],v[196:199],v[108:111]
EX v80,v80
MM v[108:111],a[252:255],v[212:215],v[108:111]
EX v81,v81
MM v[104:107],a[236:239],v[212:215],v[132:135]
TR a[170:171],v233 offset:37888
CV v74,v76,v77
MM v[104:107],a[232:235],v[196:199],v[104:107]
EX v82,v82
MM v[104:107],a[228:231],v[180:183],v[104:107]
TR a[172:173],v233 offset:41984
TR a[174:175],v233 offset:46080
MM v[104:107],a[224:227],v[164:167],v[104:107]
EX v83,v83
MM v[112:115],a[224:227],v[168:171],v[136:139]
TR a[176:177],v232 offset:34304
CV v75,v78,v79
MM v[112:115],a[228:231],v[184:187],v[112:115]
EX v84,v84
MM v[112:115],a[232:235],v[200:203],v[112:115]
EX v85,v85
MM v[112:115],a[236:239],v[216:219],v[112:115]
TR a[178:179],v232 offset:38400
CV v80,v80,v81
MM v[116:119],a[252:255],v[216:219],v[136:139]
EX v86,v86
MM v[116:119],a[248:251],v[200:203],v[116:119]
TR a[180:181],v232 offset:42496
TR a[182:183],v232 offset:46592
MM v[116:119],a[244:247],v[184:187],v[116:119]
EX v87,v87
MM v[116:119],a[240:243],v[168:171],v[116:119]
TR a[184:185],v233 offset:34304
CV v81,v82,v83
MM v[124:127],a[240:243],v[172:175],v[140:143]
EX v88,v88
MM v[124:127],a[244:247],v[188:191],v[124:127]
TR a[186:187],v233 offset:38400
CV v82,v84,v85
MM v[124:127],a[248:251],v[204:207],v[124:127]
TR a[188:189],v233 offset:42496
TR a[190:191],v233 offset:46592
EX v89,v89
MM v[124:127],a[252:255],v[220:223],v[124:127]
EX v90,v90
MM v[120:123],a[236:239],v[220:223],v[140:143]
CV v83,v86,v87
CV v88,v88,v89
MM v[120:123],a[232:235],v[204:207],v[120:123]
EX v91,v91
MM v[120:123],a[228:231],v[188:191],v[120:123]
EX v92,v92
MM v[120:123],a[224:227],v[172:175],v[120:123]
s_waitcnt vmcnt(24) lgkmcnt(0)
s_barrier
MM a[0:3],a[128:131],v[0:3],a[0:3]
s_add_u32 m0, s65, 32768
EX v93,v93
MM a[0:3],a[132:135],v[4:7],a[0:3]
CV v89,v90,v91
CV v90,v92,v93
MM a[8:11],a[132:135],v[12:15],a[8:11]
EX v94,v94
MM a[8:11],a[128:131],v[8:11],a[8:11]
EX v95,v95
MM a[16:19],a[128:131],v[16:19],a[16:19]
RD a[192:195],v224 offset:0
LPG24_%=:
s_cbranch_execz LPG28_%=
CV v91,v94,v95
MM a[16:19],a[132:135],v[20:23],a[16:19]
EX v96,v96
MM a[24:27],a[132:135],v[28:31],a[24:27]
buffer_load_dwordx4 v228, s[8:11], s64 offen offset:0 lds
MM a[24:27],a[128:131],v[24:27],a[24:27]
EX v97,v97
MM a[28:31],a[136:139],v[24:27],a[28:31]
RD a[196:199],v225 offset:0
CV v68,v96,v97
MM a[28:31],a[140:143],v[28:31],a[28:31]
EX v98,v98
MM a[20:23],a[140:143],v[20:23],a[20:23]
EX v99,v99
MM a[20:23],a[136:139],v[16:19],a[20:23]
TR a[128:129],v232 offset:49152
RD a[200:203],v226 offset:0
MM a[12:15],a[136:139],v[8:11],a[12:15]
EX v100,v100
MM a[12:15],a[140:143],v[12:15],a[12:15]
TR a[130:131],v232 offset:53248
CV v69,v98,v99
MM a[4:7],a[140:143],v[4:7],a[4:7]
EX v101,v101
MM a[4:7],a[136:139],v[0:3],a[4:7]
TR a[132:133],v232 offset:57344
RD a[204:207],v227 offset:0
MM a[32:35],a[144:147],v[0:3],a[32:35]
EX v102,v102
MM a[32:35],a[148:151],v[4:7],a[32:35]
buffer_load_dwordx4 v229, s[8:11], s64 offen offset:1024 lds
MM a[40:43],a[148:151],v[12:15],a[40:43]
TR a[134:135],v232 offset:61440
CV v70,v100,v101
MM a[40:43],a[144:147],v[8:11],a[40:43]
EX v103,v103
MM a[48:51],a[144:147],v[16:19],a[48:51]
TR a[136:137],v233 offset:49152
TR a[138:139],v233 offset:53248
MM a[48:51],a[148:151],v[20:23],a[48:51]
EX v104,v104
MM a[56:59],a[148:151],v[28:31],a[56:59]
EX v105,v105
MM a[56:59],a[144:147],v[24:27],a[56:59]
TR a[140:141],v233 offset:57344
CV v71,v102,v103
MM a[60:63],a[152:155],v[24:27],a[60:63]
EX v106,v106
MM a[60:63],a[156:159],v[28:31],a[60:63]
RD a[208:211],v224 offset:4096
TR a[142:143],v233 offset:61440
MM a[52:55],a[156:159],v[20:23],a[52:55]
EX v107,v107
MM a[52:55],a[152:155],v[16:19],a[52:55]
buffer_load_dwordx4 v230, s[8:11], s64 offen offset:2048 lds
MM a[44:47],a[152:155],v[8:11],a[44:47]
TR a[144:145],v232 offset:49664
RD a[212:215],v225 offset:4096
MM a[44:47],a[156:159],v[12:15],a[44:47]
EX v108,v108
MM a[36:39],a[156:159],v[4:7],a[36:39]
TR a[146:147],v232 offset:53760
CV v76,v104,v105
MM a[36:39],a[152:155],v[0:3],a[36:39]
EX v109,v109
MM a[64:67],a[160:163],v[0:3],a[64:67]
TR a[148:149],v232 offset:57856
CV v77,v106,v107
MM a[64:67],a[164:167],v[4:7],a[64:67]
EX v110,v110
MM a[72:75],a[164:167],v[12:15],a[72:75]
EX v111,v111
MM a[72:75],a[160:163],v[8:11],a[72:75]
TR a[150:151],v232 offset:61952
RD a[216:219],v226 offset:4096
MM a[80:83],a[160:163],v[16:19],a[80:83]
TR a[152:153],v233 offset:49664
CV v78,v108,v109
MM a[80:83],a[164:167],v[20:23],a[80:83]
EX v112,v112
MM a[88:91],a[164:167],v[28:31],a[88:91]
EX v113,v113
MM a[88:91],a[160:163],v[24:27],a[88:91]
TR a[154:155],v233 offset:53760
RD a[220:223],v227 offset:4096
MM a[92:95],a[168:171],v[24:27],a[92:95]
buffer_load_dwordx4 v231, s[8:11], s64 offen offset:3072 lds
MM a[92:95],a[172:175],v[28:31],a[92:95]
s_add_u32 m0, s65, 98304
EX v114,v114
MM a[84:87],a[172:175],v[20:23],a[84:87]
TR a[156:157],v233 offset:57856
TR a[158:159],v233 offset:61952
MM a[84:87],a[168:171],v[16:19],a[84:87]
EX v115,v115
MM a[76:79],a[168:171],v[8:11],a[76:79]
EX v116,v116
MM a[76:79],a[172:175],v[12:15],a[76:79]
TR a[160:161],v232 offset:50176
CV v79,v110,v111
MM a[68:71],a[172:175],v[4:7],a[68:71]
EX v117,v117
MM a[68:71],a[168:171],v[0:3],a[68:71]
TR a[162:163],v232 offset:54272
RD a[224:227],v224 offset:8192
MM a[96:99],a[176:179],v[0:3],a[96:99]
EX v118,v118
MM a[96:99],a[180:183],v[4:7],a[96:99]
TR a[164:165],v232 offset:58368
CV v84,v112,v113
MM a[104:107],a[180:183],v[12:15],a[104:107]
buffer_load_dwordx4 v235, s[12:15], s81 offen offset:0 lds
TR a[166:167],v232 offset:62464
EX v119,v119
MM a[104:107],a[176:179],v[8:11],a[104:107]
RD a[228:231],v225 offset:8192
TR a[168:169],v233 offset:50176
MM a[112:115],a[176:179],v[16:19],a[112:115]
EX v120,v120
MM a[112:115],a[180:183],v[20:23],a[112:115]
TR a[170:171],v233 offset:54272
CV v85,v114,v115
MM a[120:123],a[180:183],v[28:31],a[120:123]
s_waitcnt vmcnt(8)
EX v121,v121
MM a[120:123],a[176:179],v[24:27],a[120:123]
s_lshr_b32 s77, %[sb], 3
RD a[232:235],v226 offset:8192
TR a[172:173],v233 offset:58368
MM a[124:127],a[184:187],v[24:27],a[124:127]
s_lshl_b32 s77, s77, 2
v_add_f32 v244, v144, v145
v_add_f32 v245, v148, v149
EX v122,v122
MM a[124:127],a[188:191],v[28:31],a[124:127]
s_add_u32 s77, s77, %[swave]
v_add_f32 v246, v152, v153
v_add_f32 v247, v156, v157
CV v86,v116,v117
MM a[116:119],a[188:191],v[20:23],a[116:119]
s_lshl_b32 s77, s77, 9
TR a[174:175],v233 offset:62464
buffer_load_dwordx4 v235, s[12:15], s64 offen offset:1024 lds
EX v123,v123
MM a[116:119],a[184:187],v[16:19],a[116:119]
s_add_u32 s77, s77, 0
TR a[176:177],v232 offset:50688
v_lshrrev_b32 v253, 1, v248
v_add_u32 v253, s77, v253
MM a[108:111],a[184:187],v[8:11],a[108:111]
s_mov_b32 s77, LKEND_ka-LPD2_%=-16
RD a[236:239],v227 offset:8192
v_min_u32 v253, s77, v253
EX v124,v124
CV v87,v118,v119
MM a[108:111],a[188:191],v[12:15],a[108:111]
s_getpc_b64 s[98:99]
LPD2_%=:
global_load_dword v253, v253, s[98:99]
TR a[178:179],v232 offset:54784
EX v125,v125
MM a[100:103],a[188:191],v[4:7],a[100:103]
TR a[180:181],v232 offset:58880
RD a[240:243],v224 offset:12288
MM a[100:103],a[184:187],v[0:3],a[100:103]
EX v126,v126
v_smfmac_f32_16x16x64_bf16 v[144:147], v[236:239], v[64:71], v234
RD a[244:247],v225 offset:12288
RD a[248:251],v226 offset:12288
RD a[252:255],v227 offset:12288
EX v127,v127
v_smfmac_f32_16x16x64_bf16 v[148:151], v[236:239], v[72:79], v234
TR a[182:183],v232 offset:62976
CV v92,v120,v121
v_smfmac_f32_16x16x64_bf16 v[152:155], v[236:239], v[80:87], v234
buffer_load_dwordx4 v235, s[12:15], s81 offen offset:2048 lds
CV v93,v122,v123
CV v94,v124,v125
CV v95,v126,v127
s_nop 1
v_smfmac_f32_16x16x64_bf16 v[156:159], v[236:239], v[88:95], v234
TR a[184:185],v233 offset:50688
TR a[186:187],v233 offset:54784
TR a[188:189],v233 offset:58880
TR a[190:191],v233 offset:62976
s_waitcnt lgkmcnt(5)
buffer_load_dwordx4 v235, s[12:15], s64 offen offset:3072 lds
MM v[16:19],a[192:195],v[168:171],v[136:139]
s_add_u32 s64, s64, 16384
s_add_u32 s81, s64, 896
MM v[16:19],a[196:199],v[184:187],v[16:19]
MM v[16:19],a[200:203],v[200:203],v[16:19]
v_max3_f32 v249, v144, v145, v148
v_max3_f32 v249, v249, v149, v152
MM v[16:19],a[204:207],v[216:219],v[16:19]
v_max3_f32 v249, v249, v153, v156
v_max_f32 v249, v249, v157
MM v[20:23],a[220:223],v[216:219],v[136:139]
v_cmp_lt_f32 vcc, s71, v249
MM v[20:23],a[216:219],v[200:203],v[20:23]
s_cbranch_vccnz LSITE35_%=
LBACK35_%=:
v_add_f32 v244, v144, v145
MM v[20:23],a[212:215],v[184:187],v[20:23]
v_add_f32 v245, v148, v149
v_add_f32 v246, v152, v153
MM v[20:23],a[208:211],v[168:171],v[20:23]
s_waitcnt lgkmcnt(0)
v_add_f32 v247, v156, v157
MM a[0:3],a[128:131],v[64:67],a[0:3]
EX v16,v16
MM a[0:3],a[132:135],v[68:71],a[0:3]
v_rcp_f32 v145, v244
MM a[8:11],a[132:135],v[76:79],a[8:11]
EX v17,v17
MM a[8:11],a[128:131],v[72:75],a[8:11]
EX v18,v18
MM a[12:15],a[136:139],v[72:75],a[12:15]
v_rcp_f32 v149, v245
MM a[12:15],a[140:143],v[76:79],a[12:15]
CV v16,v16,v17
v_bfe_u32 v144, v241, 0, 8
MM a[4:7],a[140:143],v[68:71],a[4:7]
EX v19,v19
MM a[4:7],a[136:139],v[64:67],a[4:7]
CV v17,v18,v19
v_lshl_or_b32 v144, v144, 1, 1
MM a[32:35],a[144:147],v[64:67],a[32:35]
EX v20,v20
MM a[32:35],a[148:151],v[68:71],a[32:35]
v_lshlrev_b32 v144, 22, v144
v_mul_f32 v144, -v144, v145
MM a[40:43],a[148:151],v[76:79],a[40:43]
)ASM"
R"ASM(
v_bfe_u32 v148, v241, 8, 8
v_lshl_or_b32 v148, v148, 1, 1
MM a[40:43],a[144:147],v[72:75],a[40:43]
v_lshlrev_b32 v148, 22, v148
v_mul_f32 v148, -v148, v149
MM a[44:47],a[152:155],v[72:75],a[44:47]
s_mul_i32 s78, s49, 17408
s_add_u32 s78, s83, s78
s_add_u32 s78, s78, 0
MM a[44:47],a[156:159],v[76:79],a[44:47]
s_add_u32 s79, s78, 4096
AR v0,a0
AR v1,a1
MM a[36:39],a[156:159],v[68:71],a[36:39]
AR v2,a2
AR v3,a3
MM a[36:39],a[152:155],v[64:67],a[36:39]
AR v4,a4
AR v5,a5
MM a[64:67],a[160:163],v[64:67],a[64:67]
AR v6,a6
AR v7,a7
MM a[64:67],a[164:167],v[68:71],a[64:67]
v_fma_f32 v0, v0, v145, v144
v_fma_f32 v1, v1, v145, v144
MM a[72:75],a[164:167],v[76:79],a[72:75]
v_fma_f32 v2, v2, v145, v144
v_fma_f32 v3, v3, v145, v144
MM a[72:75],a[160:163],v[72:75],a[72:75]
v_fma_f32 v4, v4, v145, v144
v_fma_f32 v5, v5, v145, v144
EX v21,v21
MM a[76:79],a[168:171],v[72:75],a[76:79]
v_fma_f32 v6, v6, v145, v144
v_fma_f32 v7, v7, v145, v144
CV v160,v0,v1
MM a[76:79],a[172:175],v[76:79],a[76:79]
CV v161,v2,v3
CV v162,v4,v5
EX v22,v22
MM a[68:71],a[172:175],v[68:71],a[68:71]
CV v163,v6,v7
CV v18,v20,v21
MM a[68:71],a[168:171],v[64:67],a[68:71]
buffer_store_dwordx4 v[160:163], v248, s[20:23], s78 offen offset:0 sc1
AR v8,a8
AR v9,a9
AR v10,a10
MM a[96:99],a[176:179],v[64:67],a[96:99]
AR v11,a11
AR v12,a12
EX v23,v23
MM a[96:99],a[180:183],v[68:71],a[96:99]
AR v13,a13
AR v14,a14
AR v15,a15
MM a[104:107],a[180:183],v[76:79],a[104:107]
v_fma_f32 v8, v8, v149, v148
v_fma_f32 v9, v9, v149, v148
MM a[104:107],a[176:179],v[72:75],a[104:107]
v_fma_f32 v10, v10, v149, v148
v_fma_f32 v11, v11, v149, v148
v_fma_f32 v12, v12, v149, v148
MM a[108:111],a[184:187],v[72:75],a[108:111]
v_fma_f32 v13, v13, v149, v148
v_fma_f32 v14, v14, v149, v148
MM a[108:111],a[188:191],v[76:79],a[108:111]
v_fma_f32 v15, v15, v149, v148
CV v164,v8,v9
CV v165,v10,v11
MM a[100:103],a[188:191],v[68:71],a[100:103]
CV v166,v12,v13
CV v167,v14,v15
MM a[100:103],a[184:187],v[64:67],a[100:103]
buffer_store_dwordx4 v[164:167], v248, s[20:23], s79 offen offset:0 sc1
AR v32,a32
MM v[28:31],a[208:211],v[172:175],v[140:143]
AR v33,a33
AR v34,a34
AR v35,a35
MM v[28:31],a[212:215],v[188:191],v[28:31]
AR v36,a36
AR v37,a37
MM v[28:31],a[216:219],v[204:207],v[28:31]
AR v38,a38
AR v39,a39
v_fma_f32 v32, v32, v145, v144
MM v[28:31],a[220:223],v[220:223],v[28:31]
v_fma_f32 v33, v33, v145, v144
v_fma_f32 v34, v34, v145, v144
MM v[24:27],a[204:207],v[220:223],v[140:143]
v_fma_f32 v35, v35, v145, v144
v_fma_f32 v36, v36, v145, v144
v_fma_f32 v37, v37, v145, v144
MM v[24:27],a[200:203],v[204:207],v[24:27]
v_fma_f32 v38, v38, v145, v144
v_fma_f32 v39, v39, v145, v144
MM v[24:27],a[196:199],v[188:191],v[24:27]
CV v176,v32,v33
CV v177,v34,v35
CV v178,v36,v37
CV v19,v22,v23
MM v[24:27],a[192:195],v[172:175],v[24:27]
CV v179,v38,v39
MM v[48:51],a[224:227],v[168:171],v[136:139]
buffer_store_dwordx4 v[176:179], v248, s[20:23], s78 offen offset:1024 sc1
AR v40,a40
AR v41,a41
MM v[48:51],a[228:231],v[184:187],v[48:51]
AR v42,a42
AR v43,a43
AR v44,a44
MM v[48:51],a[232:235],v[200:203],v[48:51]
AR v45,a45
AR v46,a46
MM v[48:51],a[236:239],v[216:219],v[48:51]
AR v47,a47
v_fma_f32 v40, v40, v149, v148
v_fma_f32 v41, v41, v149, v148
MM v[52:55],a[252:255],v[216:219],v[136:139]
EX v24,v24
EX v25,v25
EX v26,v26
EX v27,v27
EX v28,v28
EX v29,v29
v_fma_f32 v42, v42, v149, v148
EX v30,v30
v_fma_f32 v43, v43, v149, v148
MM v[52:55],a[248:251],v[200:203],v[52:55]
v_fma_f32 v44, v44, v149, v148
v_fma_f32 v45, v45, v149, v148
v_fma_f32 v46, v46, v149, v148
EX v31,v31
CV v24,v24,v25
MM v[52:55],a[244:247],v[184:187],v[52:55]
v_fma_f32 v47, v47, v149, v148
CV v180,v40,v41
CV v181,v42,v43
MM v[52:55],a[240:243],v[168:171],v[52:55]
CV v182,v44,v45
CV v183,v46,v47
MM v[60:63],a[240:243],v[172:175],v[140:143]
buffer_store_dwordx4 v[180:183], v248, s[20:23], s79 offen offset:1024 sc1
EX v48,v48
MM v[60:63],a[244:247],v[188:191],v[60:63]
AR v0,a64
AR v1,a65
AR v2,a66
CV v25,v26,v27
MM v[60:63],a[248:251],v[204:207],v[60:63]
AR v3,a67
AR v4,a68
AR v5,a69
MM v[60:63],a[252:255],v[220:223],v[60:63]
AR v6,a70
AR v7,a71
EX v49,v49
MM v[56:59],a[236:239],v[220:223],v[140:143]
v_fma_f32 v0, v0, v145, v144
v_fma_f32 v1, v1, v145, v144
v_fma_f32 v2, v2, v145, v144
MM v[56:59],a[232:235],v[204:207],v[56:59]
v_fma_f32 v3, v3, v145, v144
v_fma_f32 v4, v4, v145, v144
EX v50,v50
MM v[56:59],a[228:231],v[188:191],v[56:59]
v_fma_f32 v5, v5, v145, v144
v_fma_f32 v6, v6, v145, v144
v_fma_f32 v7, v7, v145, v144
CV v26,v28,v29
MM v[56:59],a[224:227],v[172:175],v[56:59]
s_waitcnt lgkmcnt(0)
s_barrier
CV v192,v0,v1
CV v193,v2,v3
MM a[16:19],a[128:131],v[80:83],a[16:19]
s_add_u32 m0, s65, 49152
CV v194,v4,v5
CV v195,v6,v7
EX v51,v51
MM a[16:19],a[132:135],v[84:87],a[16:19]
buffer_store_dwordx4 v[192:195], v248, s[20:23], s78 offen offset:2048 sc1
AR v8,a72
AR v9,a73
MM a[24:27],a[132:135],v[92:95],a[24:27]
AR v10,a74
AR v11,a75
MM a[24:27],a[128:131],v[88:91],a[24:27]
AR v12,a76
AR v13,a77
EX v52,v52
AR v14,a78
CV v27,v30,v31
MM a[28:31],a[136:139],v[88:91],a[28:31]
AR v15,a79
v_fma_f32 v8, v8, v149, v148
MM a[28:31],a[140:143],v[92:95],a[28:31]
v_fma_f32 v9, v9, v149, v148
v_fma_f32 v10, v10, v149, v148
v_fma_f32 v11, v11, v149, v148
EX v53,v53
MM a[20:23],a[140:143],v[84:87],a[20:23]
v_fma_f32 v12, v12, v149, v148
v_fma_f32 v13, v13, v149, v148
MM a[20:23],a[136:139],v[80:83],a[20:23]
v_fma_f32 v14, v14, v149, v148
v_fma_f32 v15, v15, v149, v148
CV v196,v8,v9
MM a[48:51],a[144:147],v[80:83],a[48:51]
RD a[192:195],v224 offset:16384
CV v197,v10,v11
CV v198,v12,v13
EX v54,v54
CV v199,v14,v15
MM a[48:51],a[148:151],v[84:87],a[48:51]
buffer_store_dwordx4 v[196:199], v248, s[20:23], s79 offen offset:2048 sc1
CV v20,v48,v49
MM a[56:59],a[148:151],v[92:95],a[56:59]
buffer_load_dwordx4 v228, s[8:11], s64 offen offset:0 lds
RD a[196:199],v225 offset:16384
AR v32,a96
AR v33,a97
EX v55,v55
MM a[56:59],a[144:147],v[88:91],a[56:59]
RD a[200:203],v226 offset:16384
AR v34,a98
AR v35,a99
AR v36,a100
MM a[60:63],a[152:155],v[88:91],a[60:63]
AR v37,a101
AR v38,a102
AR v39,a103
MM a[60:63],a[156:159],v[92:95],a[60:63]
buffer_load_dwordx4 v229, s[8:11], s64 offen offset:1024 lds
RD a[204:207],v227 offset:16384
v_fma_f32 v32, v32, v145, v144
v_fma_f32 v33, v33, v145, v144
EX v56,v56
MM a[52:55],a[156:159],v[84:87],a[52:55]
v_fma_f32 v34, v34, v145, v144
v_fma_f32 v35, v35, v145, v144
v_fma_f32 v36, v36, v145, v144
CV v21,v50,v51
MM a[52:55],a[152:155],v[80:83],a[52:55]
RD a[208:211],v224 offset:20480
v_fma_f32 v37, v37, v145, v144
v_fma_f32 v38, v38, v145, v144
MM a[80:83],a[160:163],v[80:83],a[80:83]
v_fma_f32 v39, v39, v145, v144
CV v208,v32,v33
EX v57,v57
CV v209,v34,v35
MM a[80:83],a[164:167],v[84:87],a[80:83]
buffer_load_dwordx4 v230, s[8:11], s64 offen offset:2048 lds
RD a[212:215],v225 offset:20480
CV v210,v36,v37
CV v211,v38,v39
MM a[88:91],a[164:167],v[92:95],a[88:91]
EX v58,v58
MM a[88:91],a[160:163],v[88:91],a[88:91]
buffer_store_dwordx4 v[208:211], v248, s[20:23], s78 offen offset:3072 sc1
RD a[216:219],v226 offset:20480
AR v40,a104
AR v41,a105
AR v42,a106
AR v43,a107
CV v22,v52,v53
MM a[92:95],a[168:171],v[88:91],a[92:95]
RD a[220:223],v227 offset:20480
AR v44,a108
AR v45,a109
MM a[92:95],a[172:175],v[92:95],a[92:95]
AR v46,a110
AR v47,a111
EX v59,v59
v_fma_f32 v40, v40, v149, v148
MM a[84:87],a[172:175],v[84:87],a[84:87]
buffer_load_dwordx4 v231, s[8:11], s64 offen offset:3072 lds
RD a[224:227],v224 offset:24576
v_fma_f32 v41, v41, v149, v148
v_fma_f32 v42, v42, v149, v148
MM a[84:87],a[168:171],v[80:83],a[84:87]
s_add_u32 m0, s65, 114688
v_fma_f32 v43, v43, v149, v148
v_fma_f32 v44, v44, v149, v148
v_fma_f32 v45, v45, v149, v148
MM a[112:115],a[176:179],v[80:83],a[112:115]
RD a[228:231],v225 offset:24576
v_fma_f32 v46, v46, v149, v148
v_fma_f32 v47, v47, v149, v148
EX v60,v60
CV v23,v54,v55
MM a[112:115],a[180:183],v[84:87],a[112:115]
buffer_load_dwordx4 v235, s[12:15], s81 offen offset:0 lds
CV v212,v40,v41
CV v213,v42,v43
CV v214,v44,v45
MM a[120:123],a[180:183],v[92:95],a[120:123]
RD a[232:235],v226 offset:24576
CV v215,v46,v47
EX v61,v61
MM a[120:123],a[176:179],v[88:91],a[120:123]
RD a[236:239],v227 offset:24576
CV v28,v56,v57
MM a[124:127],a[184:187],v[88:91],a[124:127]
buffer_store_dwordx4 v[212:215], v248, s[20:23], s79 offen offset:3072 sc1
MM a[124:127],a[188:191],v[92:95],a[124:127]
EX v62,v62
MM a[116:119],a[188:191],v[84:87],a[116:119]
RD a[240:243],v224 offset:28672
RD a[244:247],v225 offset:28672
RD a[248:251],v226 offset:28672
RD a[252:255],v227 offset:28672
EX v63,v63
MM a[116:119],a[184:187],v[80:83],a[116:119]
buffer_load_dwordx4 v235, s[12:15], s64 offen offset:1024 lds
v_smfmac_f32_16x16x64_bf16 v[152:155], v[236:239], v[16:23], v234
CV v29,v58,v59
CV v30,v60,v61
CV v31,v62,v63
s_nop 1
v_smfmac_f32_16x16x64_bf16 v[156:159], v[236:239], v[24:31], v234
s_waitcnt lgkmcnt(0)
buffer_load_dwordx4 v235, s[12:15], s81 offen offset:2048 lds
buffer_load_dwordx4 v235, s[12:15], s64 offen offset:3072 lds
MM v[80:83],a[192:195],v[168:171],v[136:139]
TR a[128:129],v232 offset:0
TR a[130:131],v232 offset:4096
s_nop 0
LPG25_%=:
s_cbranch_execz LPG29_%=
MM v[80:83],a[196:199],v[184:187],v[80:83]
TR a[132:133],v232 offset:8192
TR a[134:135],v232 offset:12288
MM v[80:83],a[200:203],v[200:203],v[80:83]
TR a[136:137],v233 offset:0
TR a[138:139],v233 offset:4096
MM v[80:83],a[204:207],v[216:219],v[80:83]
v_max3_f32 v249, v152, v153, v156
v_max_f32 v249, v249, v157
MM v[84:87],a[220:223],v[216:219],v[136:139]
TR a[140:141],v233 offset:8192
v_cmp_lt_f32 vcc, s71, v249
MM v[84:87],a[216:219],v[200:203],v[84:87]
s_cbranch_vccnz LSITE36_%=
LBACK36_%=:
TR a[142:143],v233 offset:12288
v_add_f32 v246, v152, v153
MM v[84:87],a[212:215],v[184:187],v[84:87]
TR a[144:145],v232 offset:512
v_add_f32 v247, v156, v157
MM v[84:87],a[208:211],v[168:171],v[84:87]
TR a[146:147],v232 offset:4608
TR a[148:149],v232 offset:8704
MM v[92:95],a[208:211],v[172:175],v[140:143]
TR a[150:151],v232 offset:12800
TR a[152:153],v233 offset:512
MM v[92:95],a[212:215],v[188:191],v[92:95]
EX v80,v80
MM v[92:95],a[216:219],v[204:207],v[92:95]
EX v81,v81
MM v[92:95],a[220:223],v[220:223],v[92:95]
TR a[154:155],v233 offset:4608
TR a[156:157],v233 offset:8704
MM v[88:91],a[204:207],v[220:223],v[140:143]
TR a[158:159],v233 offset:12800
CV v80,v80,v81
MM v[88:91],a[200:203],v[204:207],v[88:91]
EX v82,v82
MM v[88:91],a[196:199],v[188:191],v[88:91]
TR a[160:161],v232 offset:1024
TR a[162:163],v232 offset:5120
MM v[88:91],a[192:195],v[172:175],v[88:91]
EX v83,v83
MM v[112:115],a[224:227],v[168:171],v[136:139]
TR a[164:165],v232 offset:9216
TR a[166:167],v232 offset:13312
MM v[112:115],a[228:231],v[184:187],v[112:115]
EX v84,v84
MM v[112:115],a[232:235],v[200:203],v[112:115]
TR a[168:169],v233 offset:1024
TR a[170:171],v233 offset:5120
MM v[112:115],a[236:239],v[216:219],v[112:115]
EX v85,v85
MM v[116:119],a[252:255],v[216:219],v[136:139]
TR a[172:173],v233 offset:9216
TR a[174:175],v233 offset:13312
MM v[116:119],a[248:251],v[200:203],v[116:119]
EX v86,v86
MM v[116:119],a[244:247],v[184:187],v[116:119]
TR a[176:177],v232 offset:1536
TR a[178:179],v232 offset:5632
MM v[116:119],a[240:243],v[168:171],v[116:119]
EX v87,v87
MM v[124:127],a[240:243],v[172:175],v[140:143]
TR a[180:181],v232 offset:9728
TR a[182:183],v232 offset:13824
MM v[124:127],a[244:247],v[188:191],v[124:127]
EX v88,v88
MM v[124:127],a[248:251],v[204:207],v[124:127]
TR a[184:185],v233 offset:1536
TR a[186:187],v233 offset:5632
TR a[188:189],v233 offset:9728
TR a[190:191],v233 offset:13824
EX v89,v89
MM v[124:127],a[252:255],v[220:223],v[124:127]
CV v81,v82,v83
CV v82,v84,v85
MM v[120:123],a[236:239],v[220:223],v[140:143]
EX v90,v90
MM v[120:123],a[232:235],v[204:207],v[120:123]
EX v91,v91
MM v[120:123],a[228:231],v[188:191],v[120:123]
CV v83,v86,v87
CV v88,v88,v89
MM v[120:123],a[224:227],v[172:175],v[120:123]
s_waitcnt vmcnt(21) lgkmcnt(0)
s_barrier
CV v89,v90,v91
EX v92,v92
MM a[16:19],a[128:131],v[16:19],a[16:19]
s_xor_b32 s66, s94, 4
EX v93,v93
MM a[16:19],a[132:135],v[20:23],a[16:19]
)ASM"
R"ASM(
s_mul_i32 s66, s66, 163840
EX v94,v94
MM a[24:27],a[132:135],v[28:31],a[24:27]
s_add_u32 s66, s66, s87
EX v95,v95
MM a[24:27],a[128:131],v[24:27],a[24:27]
s_add_u32 m0, s65, 65536
RD a[192:195],v224 offset:32768
CV v90,v92,v93
MM a[28:31],a[136:139],v[24:27],a[28:31]
EX v112,v112
MM a[28:31],a[140:143],v[28:31],a[28:31]
buffer_load_dwordx4 v240, s[24:27], s66 offen offset:0 lds
MM a[20:23],a[140:143],v[20:23],a[20:23]
s_add_u32 s66, s66, 9216
EX v113,v113
MM a[20:23],a[136:139],v[16:19],a[20:23]
RD a[196:199],v225 offset:32768
CV v91,v94,v95
MM a[48:51],a[144:147],v[16:19],a[48:51]
EX v114,v114
MM a[48:51],a[148:151],v[20:23],a[48:51]
RD a[200:203],v226 offset:32768
RD a[204:207],v227 offset:32768
MM a[56:59],a[148:151],v[28:31],a[56:59]
EX v115,v115
MM a[56:59],a[144:147],v[24:27],a[56:59]
buffer_load_dwordx4 v240, s[24:27], s66 offen offset:1024 lds
MM a[60:63],a[152:155],v[24:27],a[60:63]
s_add_u32 s66, s66, 9216
EX v116,v116
MM a[60:63],a[156:159],v[28:31],a[60:63]
RD a[208:211],v224 offset:36864
CV v84,v112,v113
MM a[52:55],a[156:159],v[20:23],a[52:55]
EX v117,v117
MM a[52:55],a[152:155],v[16:19],a[52:55]
buffer_load_dwordx4 v240, s[24:27], s66 offen offset:2048 lds
MM a[80:83],a[160:163],v[16:19],a[80:83]
s_add_u32 s66, s66, 9216
EX v118,v118
MM a[80:83],a[164:167],v[20:23],a[80:83]
RD a[212:215],v225 offset:36864
RD a[216:219],v226 offset:36864
MM a[88:91],a[164:167],v[28:31],a[88:91]
s_lshl_b32 s97, s84, 2
s_add_u32 s97, s97, s96
s_add_u32 s97, s97, 0
EX v119,v119
MM a[88:91],a[160:163],v[24:27],a[88:91]
s_waitcnt vmcnt(6)
v_mov_b32 v242, s69
s_mov_b64 exec, 1
buffer_store_dword v242, off, s[20:23], s97 offset:0 sc1
s_mov_b64 exec, -1
buffer_load_dwordx4 v240, s[24:27], s66 offen offset:3072 lds
EX v120,v120
MM a[92:95],a[168:171],v[24:27],a[92:95]
s_xor_b32 s66, s94, 4
RD a[220:223],v227 offset:36864
CV v85,v114,v115
MM a[92:95],a[172:175],v[28:31],a[92:95]
s_mul_i32 s66, s66, 163840
EX v121,v121
MM a[84:87],a[172:175],v[20:23],a[84:87]
s_add_u32 s66, s66, s86
RD a[224:227],v224 offset:40960
CV v86,v116,v117
MM a[84:87],a[168:171],v[16:19],a[84:87]
s_add_u32 m0, s65, 0
EX v122,v122
MM a[112:115],a[176:179],v[16:19],a[112:115]
buffer_load_dwordx4 v240, s[24:27], s66 offen offset:0 lds
RD a[228:231],v225 offset:40960
EX v123,v123
MM a[112:115],a[180:183],v[20:23],a[112:115]
s_add_u32 s66, s66, 9216
RD a[232:235],v226 offset:40960
CV v87,v118,v119
MM a[120:123],a[180:183],v[28:31],a[120:123]
EX v124,v124
MM a[120:123],a[176:179],v[24:27],a[120:123]
buffer_load_dwordx4 v240, s[24:27], s66 offen offset:1024 lds
MM a[124:127],a[184:187],v[24:27],a[124:127]
s_add_u32 s66, s66, 9216
RD a[236:239],v227 offset:40960
EX v125,v125
MM a[124:127],a[188:191],v[28:31],a[124:127]
EX v126,v126
MM a[116:119],a[188:191],v[20:23],a[116:119]
RD a[240:243],v224 offset:45056
RD a[244:247],v225 offset:45056
RD a[248:251],v226 offset:45056
RD a[252:255],v227 offset:45056
EX v127,v127
MM a[116:119],a[184:187],v[16:19],a[116:119]
buffer_load_dwordx4 v240, s[24:27], s66 offen offset:2048 lds
CV v92,v120,v121
v_smfmac_f32_16x16x64_bf16 v[152:155], v[236:239], v[80:87], v234
s_add_u32 s66, s66, 9216
CV v93,v122,v123
CV v94,v124,v125
CV v95,v126,v127
s_nop 1
v_smfmac_f32_16x16x64_bf16 v[156:159], v[236:239], v[88:95], v234
s_waitcnt lgkmcnt(0)
buffer_load_dwordx4 v240, s[24:27], s66 offen offset:3072 lds
v_lshrrev_b32 v145, 9, v248
v_mul_u32_u24 v145, 1536, v145
v_sub_u32 v244, v232, v145
v_sub_u32 v235, v233, v145
v_add_u32 v244, 65536, v244
v_add_u32 v235, 65536, v235
v_and_b32 v145, 960, v248
v_lshlrev_b32 v145, 1, v145
v_bfe_u32 v146, v248, 7, 2
v_lshl_add_u32 v145, v146, 5, v145
v_bfe_u32 v146, v248, 4, 2
v_lshl_add_u32 v145, v146, 3, v145
v_add_u32 v228, %[slds], v145
v_xor_b32 v229, 32, v228
v_xor_b32 v230, 64, v228
v_xor_b32 v231, 96, v228
v_add_u32 v144, 65536, v228
v_add_u32 v145, 65536, v229
v_add_u32 v146, 65536, v230
v_add_u32 v147, 65536, v231
MM v[16:19],a[192:195],v[168:171],v[136:139]
TR a[128:129],v232 offset:16384
TR a[130:131],v232 offset:20480
MM v[16:19],a[196:199],v[184:187],v[16:19]
TR a[132:133],v232 offset:24576
TR a[134:135],v232 offset:28672
MM v[16:19],a[200:203],v[200:203],v[16:19]
TR a[136:137],v233 offset:16384
TR a[138:139],v233 offset:20480
MM v[16:19],a[204:207],v[216:219],v[16:19]
v_max3_f32 v249, v152, v153, v156
v_max_f32 v249, v249, v157
MM v[20:23],a[220:223],v[216:219],v[136:139]
TR a[140:141],v233 offset:24576
v_cmp_lt_f32 vcc, s71, v249
MM v[20:23],a[216:219],v[200:203],v[20:23]
s_cbranch_vccnz LSITE37_%=
LBACK37_%=:
TR a[142:143],v233 offset:28672
v_add_f32 v246, v152, v153
MM v[20:23],a[212:215],v[184:187],v[20:23]
TR a[144:145],v232 offset:16896
v_add_f32 v247, v156, v157
MM v[20:23],a[208:211],v[168:171],v[20:23]
TR a[146:147],v232 offset:20992
TR a[148:149],v232 offset:25088
MM v[28:31],a[208:211],v[172:175],v[140:143]
TR a[150:151],v232 offset:29184
TR a[152:153],v233 offset:16896
MM v[28:31],a[212:215],v[188:191],v[28:31]
EX v16,v16
MM v[28:31],a[216:219],v[204:207],v[28:31]
v_lshrrev_b32 v98, 1, v248
v_min_u32 v98, 312, v98
MM v[28:31],a[220:223],v[220:223],v[28:31]
s_mov_b32 s62, 94464
buffer_load_dwordx2 v[96:97], v98, s[20:23], s62 offen offset:0 sc1
MM v[24:27],a[204:207],v[220:223],v[140:143]
EX v17,v17
MM v[24:27],a[200:203],v[204:207],v[24:27]
TR a[154:155],v233 offset:20992
TR a[156:157],v233 offset:25088
MM v[24:27],a[196:199],v[188:191],v[24:27]
TR a[158:159],v233 offset:29184
TR a[160:161],v232 offset:17408
MM v[24:27],a[192:195],v[172:175],v[24:27]
EX v18,v18
MM v[48:51],a[224:227],v[168:171],v[136:139]
EX v19,v19
MM v[48:51],a[228:231],v[184:187],v[48:51]
TR a[162:163],v232 offset:21504
TR a[164:165],v232 offset:25600
MM v[48:51],a[232:235],v[200:203],v[48:51]
TR a[166:167],v232 offset:29696
TR a[168:169],v233 offset:17408
EX v20,v20
MM v[48:51],a[236:239],v[216:219],v[48:51]
TR a[170:171],v233 offset:21504
TR a[172:173],v233 offset:25600
MM v[52:55],a[252:255],v[216:219],v[136:139]
EX v21,v21
MM v[52:55],a[248:251],v[200:203],v[52:55]
TR a[174:175],v233 offset:29696
CV v16,v16,v17
MM v[52:55],a[244:247],v[184:187],v[52:55]
TR a[176:177],v232 offset:17920
EX v22,v22
MM v[52:55],a[240:243],v[168:171],v[52:55]
TR a[178:179],v232 offset:22016
TR a[180:181],v232 offset:26112
EX v23,v23
MM v[60:63],a[240:243],v[172:175],v[140:143]
TR a[182:183],v232 offset:30208
CV v17,v18,v19
MM v[60:63],a[244:247],v[188:191],v[60:63]
EX v24,v24
MM v[60:63],a[248:251],v[204:207],v[60:63]
TR a[184:185],v233 offset:17920
TR a[186:187],v233 offset:22016
TR a[188:189],v233 offset:26112
TR a[190:191],v233 offset:30208
EX v25,v25
MM v[60:63],a[252:255],v[220:223],v[60:63]
EX v26,v26
MM v[56:59],a[236:239],v[220:223],v[140:143]
EX v27,v27
MM v[56:59],a[232:235],v[204:207],v[56:59]
EX v28,v28
MM v[56:59],a[228:231],v[188:191],v[56:59]
EX v29,v29
CV v18,v20,v21
MM v[56:59],a[224:227],v[172:175],v[56:59]
s_waitcnt lgkmcnt(0)
s_barrier
CV v19,v22,v23
MM a[16:19],a[128:131],v[80:83],a[16:19]
s_xor_b32 s66, s94, 5
CV v24,v24,v25
CV v25,v26,v27
MM a[16:19],a[132:135],v[84:87],a[16:19]
s_mul_i32 s66, s66, 163840
EX v30,v30
MM a[24:27],a[132:135],v[92:95],a[24:27]
s_add_u32 s66, s66, s87
v_lshrrev_b32 v243, 2, v248
v_min_u32 v243, 12, v243
MM a[24:27],a[128:131],v[88:91],a[24:27]
s_add_u32 m0, s65, 81920
buffer_load_dword v242, v243, s[20:23], s44 offen offset:0 sc1
MM a[28:31],a[136:139],v[88:91],a[28:31]
EX v31,v31
MM a[28:31],a[140:143],v[92:95],a[28:31]
EX v48,v48
MM a[20:23],a[140:143],v[84:87],a[20:23]
RD a[192:195],v224 offset:49152
CV v26,v28,v29
MM a[20:23],a[136:139],v[80:83],a[20:23]
EX v49,v49
MM a[48:51],a[144:147],v[80:83],a[48:51]
buffer_load_dwordx4 v240, s[24:27], s66 offen offset:0 lds
MM a[48:51],a[148:151],v[84:87],a[48:51]
s_add_u32 s66, s66, 9216
EX v50,v50
MM a[56:59],a[148:151],v[92:95],a[56:59]
RD a[196:199],v225 offset:49152
RD a[200:203],v226 offset:49152
MM a[56:59],a[144:147],v[88:91],a[56:59]
EX v51,v51
MM a[60:63],a[152:155],v[88:91],a[60:63]
buffer_load_dwordx4 v240, s[24:27], s66 offen offset:1024 lds
MM a[60:63],a[156:159],v[92:95],a[60:63]
s_add_u32 s66, s66, 9216
RD a[204:207],v227 offset:49152
EX v52,v52
MM a[52:55],a[156:159],v[84:87],a[52:55]
EX v53,v53
CV v27,v30,v31
MM a[52:55],a[152:155],v[80:83],a[52:55]
RD a[208:211],v224 offset:53248
RD a[212:215],v225 offset:53248
buffer_load_dwordx4 v240, s[24:27], s66 offen offset:2048 lds
MM a[80:83],a[160:163],v[80:83],a[80:83]
s_add_u32 s66, s66, 9216
EX v54,v54
MM a[80:83],a[164:167],v[84:87],a[80:83]
RD a[216:219],v226 offset:53248
CV v20,v48,v49
MM a[88:91],a[164:167],v[92:95],a[88:91]
EX v55,v55
MM a[88:91],a[160:163],v[88:91],a[88:91]
buffer_load_dwordx4 v240, s[24:27], s66 offen offset:3072 lds
EX v56,v56
MM a[92:95],a[168:171],v[88:91],a[92:95]
s_xor_b32 s66, s94, 5
RD a[220:223],v227 offset:53248
CV v21,v50,v51
MM a[92:95],a[172:175],v[92:95],a[92:95]
s_mul_i32 s66, s66, 163840
EX v57,v57
MM a[84:87],a[172:175],v[84:87],a[84:87]
s_add_u32 s66, s66, s86
RD a[224:227],v224 offset:57344
CV v22,v52,v53
MM a[84:87],a[168:171],v[80:83],a[84:87]
s_add_u32 m0, s65, 16384
EX v58,v58
MM a[112:115],a[176:179],v[80:83],a[112:115]
buffer_load_dwordx4 v240, s[24:27], s66 offen offset:0 lds
RD a[228:231],v225 offset:57344
EX v59,v59
MM a[112:115],a[180:183],v[84:87],a[112:115]
s_add_u32 s66, s66, 9216
RD a[232:235],v226 offset:57344
CV v23,v54,v55
MM a[120:123],a[180:183],v[92:95],a[120:123]
EX v60,v60
MM a[120:123],a[176:179],v[88:91],a[120:123]
buffer_load_dwordx4 v240, s[24:27], s66 offen offset:1024 lds
MM a[124:127],a[184:187],v[88:91],a[124:127]
s_add_u32 s66, s66, 9216
RD a[236:239],v227 offset:57344
EX v61,v61
MM a[124:127],a[188:191],v[92:95],a[124:127]
EX v62,v62
MM a[116:119],a[188:191],v[84:87],a[116:119]
RD a[240:243],v224 offset:61440
RD a[244:247],v225 offset:61440
RD a[248:251],v226 offset:61440
RD a[252:255],v227 offset:61440
EX v63,v63
MM a[116:119],a[184:187],v[80:83],a[116:119]
buffer_load_dwordx4 v240, s[24:27], s66 offen offset:2048 lds
CV v28,v56,v57
v_smfmac_f32_16x16x64_bf16 v[152:155], v[236:239], v[16:23], v234
s_add_u32 s66, s66, 9216
CV v29,v58,v59
CV v30,v60,v61
CV v31,v62,v63
s_nop 1
v_smfmac_f32_16x16x64_bf16 v[156:159], v[236:239], v[24:31], v234
s_waitcnt lgkmcnt(0)
buffer_load_dwordx4 v240, s[24:27], s66 offen offset:3072 lds
MM v[80:83],a[192:195],v[168:171],v[136:139]
TR a[128:129],v232 offset:32768
TR a[130:131],v232 offset:36864
MM v[80:83],a[196:199],v[184:187],v[80:83]
TR a[132:133],v232 offset:40960
TR a[134:135],v232 offset:45056
MM v[80:83],a[200:203],v[200:203],v[80:83]
TR a[136:137],v233 offset:32768
TR a[138:139],v233 offset:36864
MM v[80:83],a[204:207],v[216:219],v[80:83]
v_max3_f32 v249, v152, v153, v156
v_max_f32 v249, v249, v157
MM v[84:87],a[220:223],v[216:219],v[136:139]
TR a[140:141],v233 offset:40960
v_cmp_lt_f32 vcc, s71, v249
MM v[84:87],a[216:219],v[200:203],v[84:87]
s_cbranch_vccnz LSITE38_%=
LBACK38_%=:
TR a[142:143],v233 offset:45056
v_add_f32 v246, v152, v153
MM v[84:87],a[212:215],v[184:187],v[84:87]
TR a[144:145],v232 offset:33280
v_add_f32 v247, v156, v157
MM v[84:87],a[208:211],v[168:171],v[84:87]
TR a[146:147],v232 offset:37376
TR a[148:149],v232 offset:41472
MM v[92:95],a[208:211],v[172:175],v[140:143]
s_waitcnt vmcnt(8)
v_cmp_ne_u32 vcc, s69, v242
s_cbranch_vccnz LRPO0_%=
LRPO0b_%=:
MM v[92:95],a[212:215],v[188:191],v[92:95]
s_xor_b32 s92, s49, 4
s_mul_i32 s92, s92, 17408
s_add_u32 s92, s83, s92
MM v[92:95],a[216:219],v[204:207],v[92:95]
buffer_load_dwordx4 a[0:3], v248, s[20:23], s92 offen offset:0 sc1
MM v[92:95],a[220:223],v[220:223],v[92:95]
buffer_load_dwordx4 a[32:35], v248, s[20:23], s92 offen offset:1024 sc1
MM v[88:91],a[204:207],v[220:223],v[140:143]
TR a[150:151],v232 offset:45568
v_xor_b32 v96, s69, v96
v_xor_b32 v97, s69, v97
EX v80,v80
MM v[88:91],a[200:203],v[204:207],v[88:91]
TR a[152:153],v233 offset:33280
TR a[154:155],v233 offset:37376
v_or_b32 v99, v96, v97
EX v81,v81
MM v[88:91],a[196:199],v[188:191],v[88:91]
buffer_load_dwordx4 a[64:67], v248, s[20:23], s92 offen offset:2048 sc1
TR a[156:157],v233 offset:41472
)ASM"
R"ASM(
MM v[88:91],a[192:195],v[172:175],v[88:91]
TR a[158:159],v233 offset:45568
v_cmp_ne_u32 vcc, 0, v99
s_cbranch_vccnz LRPW_%=
LRPWb_%=:
EX v82,v82
MM v[112:115],a[224:227],v[168:171],v[136:139]
TR a[160:161],v232 offset:33792
TR a[162:163],v232 offset:37888
EX v83,v83
MM v[112:115],a[228:231],v[184:187],v[112:115]
buffer_load_dwordx4 a[96:99], v248, s[20:23], s92 offen offset:3072 sc1
TR a[164:165],v232 offset:41984
MM v[112:115],a[232:235],v[200:203],v[112:115]
s_add_u32 s92, s92, 4096
TR a[166:167],v232 offset:46080
TR a[168:169],v233 offset:33792
EX v84,v84
MM v[112:115],a[236:239],v[216:219],v[112:115]
TR a[170:171],v233 offset:37888
TR a[172:173],v233 offset:41984
MM v[116:119],a[252:255],v[216:219],v[136:139]
buffer_load_dwordx4 a[4:7], v248, s[20:23], s92 offen offset:0 sc1
EX v85,v85
MM v[116:119],a[248:251],v[200:203],v[116:119]
TR a[174:175],v233 offset:46080
CV v80,v80,v81
MM v[116:119],a[244:247],v[184:187],v[116:119]
TR a[176:177],v232 offset:34304
EX v86,v86
MM v[116:119],a[240:243],v[168:171],v[116:119]
buffer_load_dwordx4 a[36:39], v248, s[20:23], s92 offen offset:1024 sc1
TR a[178:179],v232 offset:38400
EX v87,v87
MM v[124:127],a[240:243],v[172:175],v[140:143]
TR a[180:181],v232 offset:42496
TR a[182:183],v232 offset:46592
MM v[124:127],a[244:247],v[188:191],v[124:127]
EX v88,v88
MM v[124:127],a[248:251],v[204:207],v[124:127]
buffer_load_dwordx4 a[68:71], v248, s[20:23], s92 offen offset:2048 sc1
TR a[184:185],v233 offset:34304
TR a[186:187],v233 offset:38400
TR a[188:189],v233 offset:42496
TR a[190:191],v233 offset:46592
EX v89,v89
MM v[124:127],a[252:255],v[220:223],v[124:127]
EX v90,v90
CV v81,v82,v83
MM v[120:123],a[236:239],v[220:223],v[140:143]
EX v91,v91
MM v[120:123],a[232:235],v[204:207],v[120:123]
buffer_load_dwordx4 a[100:103], v248, s[20:23], s92 offen offset:3072 sc1
MM v[120:123],a[228:231],v[188:191],v[120:123]
EX v92,v92
CV v82,v84,v85
MM v[120:123],a[224:227],v[172:175],v[120:123]
s_waitcnt lgkmcnt(0)
s_barrier
CV v83,v86,v87
MM a[16:19],a[128:131],v[16:19],a[16:19]
s_xor_b32 s66, s94, 6
EX v93,v93
MM a[16:19],a[132:135],v[20:23],a[16:19]
s_mul_i32 s66, s66, 163840
CV v88,v88,v89
CV v89,v90,v91
MM a[24:27],a[132:135],v[28:31],a[24:27]
s_add_u32 s66, s66, s87
EX v94,v94
MM a[24:27],a[128:131],v[24:27],a[24:27]
s_add_u32 m0, s65, 98304
EX v95,v95
MM a[28:31],a[136:139],v[24:27],a[28:31]
EX v112,v112
MM a[28:31],a[140:143],v[28:31],a[28:31]
CV v90,v92,v93
CV v91,v94,v95
MM a[20:23],a[140:143],v[20:23],a[20:23]
buffer_load_dwordx4 v240, s[24:27], s66 offen offset:0 lds
MM a[20:23],a[136:139],v[16:19],a[20:23]
s_add_u32 s66, s66, 9216
EX v113,v113
MM a[48:51],a[144:147],v[16:19],a[48:51]
EX v114,v114
MM a[48:51],a[148:151],v[20:23],a[48:51]
buffer_load_dwordx4 v240, s[24:27], s66 offen offset:1024 lds
MM a[56:59],a[148:151],v[28:31],a[56:59]
s_add_u32 s66, s66, 9216
EX v115,v115
MM a[56:59],a[144:147],v[24:27],a[56:59]
TR a[128:129],v232 offset:49152
CV v84,v112,v113
MM a[60:63],a[152:155],v[24:27],a[60:63]
EX v116,v116
MM a[60:63],a[156:159],v[28:31],a[60:63]
TR a[130:131],v232 offset:53248
TR a[132:133],v232 offset:57344
MM a[52:55],a[156:159],v[20:23],a[52:55]
EX v117,v117
MM a[52:55],a[152:155],v[16:19],a[52:55]
TR a[134:135],v232 offset:61440
TR a[136:137],v233 offset:49152
buffer_load_dwordx4 v240, s[24:27], s66 offen offset:2048 lds
MM a[80:83],a[160:163],v[16:19],a[80:83]
s_add_u32 s66, s66, 9216
TR a[138:139],v233 offset:53248
TR a[140:141],v233 offset:57344
EX v118,v118
MM a[80:83],a[164:167],v[20:23],a[80:83]
TR a[142:143],v233 offset:61440
TR a[144:145],v232 offset:49664
MM a[88:91],a[164:167],v[28:31],a[88:91]
s_xor_b32 s92, s49, 5
s_mul_i32 s92, s92, 17408
s_add_u32 s92, s83, s92
EX v119,v119
MM a[88:91],a[160:163],v[24:27],a[88:91]
buffer_load_dwordx4 a[8:11], v248, s[20:23], s92 offen offset:0 sc1
LPG26_%=:
s_cbranch_execz LPG30_%=
TR a[146:147],v232 offset:53760
EX v120,v120
MM a[92:95],a[168:171],v[24:27],a[92:95]
TR a[148:149],v232 offset:57856
TR a[150:151],v232 offset:61952
buffer_load_dwordx4 v240, s[24:27], s66 offen offset:3072 lds
MM a[92:95],a[172:175],v[28:31],a[92:95]
s_xor_b32 s66, s94, 6
TR a[152:153],v233 offset:49664
EX v121,v121
CV v85,v114,v115
MM a[84:87],a[172:175],v[20:23],a[84:87]
s_mul_i32 s66, s66, 163840
TR a[154:155],v233 offset:53760
TR a[156:157],v233 offset:57856
MM a[84:87],a[168:171],v[16:19],a[84:87]
s_add_u32 s66, s66, s86
buffer_load_dwordx4 a[40:43], v248, s[20:23], s92 offen offset:1024 sc1
TR a[158:159],v233 offset:61952
s_add_u32 m0, s65, 32768
EX v122,v122
MM a[112:115],a[176:179],v[16:19],a[112:115]
TR a[160:161],v232 offset:50176
CV v86,v116,v117
EX v123,v123
MM a[112:115],a[180:183],v[20:23],a[112:115]
TR a[162:163],v232 offset:54272
TR a[164:165],v232 offset:58368
MM a[120:123],a[180:183],v[28:31],a[120:123]
buffer_load_dwordx4 a[72:75], v248, s[20:23], s92 offen offset:2048 sc1
TR a[166:167],v232 offset:62464
EX v124,v124
MM a[120:123],a[176:179],v[24:27],a[120:123]
TR a[168:169],v233 offset:50176
TR a[170:171],v233 offset:54272
CV v87,v118,v119
MM a[124:127],a[184:187],v[24:27],a[124:127]
TR a[172:173],v233 offset:58368
EX v125,v125
MM a[124:127],a[188:191],v[28:31],a[124:127]
buffer_load_dwordx4 a[104:107], v248, s[20:23], s92 offen offset:3072 sc1
TR a[174:175],v233 offset:62464
EX v126,v126
MM a[116:119],a[188:191],v[20:23],a[116:119]
TR a[176:177],v232 offset:50688
TR a[178:179],v232 offset:54784
EX v127,v127
MM a[116:119],a[184:187],v[16:19],a[116:119]
s_add_u32 s92, s92, 4096
TR a[180:181],v232 offset:58880
CV v92,v120,v121
v_smfmac_f32_16x16x64_bf16 v[152:155], v[236:239], v[80:87], v234
buffer_load_dwordx4 v240, s[24:27], s66 offen offset:0 lds
s_add_u32 s66, s66, 9216
TR a[182:183],v232 offset:62976
CV v93,v122,v123
CV v94,v124,v125
CV v95,v126,v127
s_nop 1
v_smfmac_f32_16x16x64_bf16 v[156:159], v[236:239], v[88:95], v234
TR a[184:185],v233 offset:50688
TR a[186:187],v233 offset:54784
TR a[188:189],v233 offset:58880
TR a[190:191],v233 offset:62976
s_waitcnt lgkmcnt(0)
buffer_load_dwordx4 v240, s[24:27], s66 offen offset:1024 lds
s_add_u32 s66, s66, 9216
buffer_load_dwordx4 v240, s[24:27], s66 offen offset:2048 lds
s_add_u32 s66, s66, 9216
buffer_load_dwordx4 v240, s[24:27], s66 offen offset:3072 lds
s_nop 6
v_max3_f32 v249, v152, v153, v156
v_max_f32 v249, v249, v157
v_cmp_lt_f32 vcc, s71, v249
s_cbranch_vccnz LSITE39_%=
LBACK39_%=:
v_add_f32 v246, v152, v153
v_add_f32 v247, v156, v157
s_waitcnt lgkmcnt(0)
MM a[16:19],a[128:131],v[80:83],a[16:19]
s_barrier
MM a[16:19],a[132:135],v[84:87],a[16:19]
buffer_load_dwordx4 a[12:15], v248, s[20:23], s92 offen offset:0 sc1
MM a[24:27],a[132:135],v[92:95],a[24:27]
v_rcp_f32 v153, v246
MM a[24:27],a[128:131],v[88:91],a[24:27]
v_rcp_f32 v157, v247
MM a[28:31],a[136:139],v[88:91],a[28:31]
v_bfe_u32 v152, v241, 16, 8
v_lshl_or_b32 v152, v152, 1, 1
MM a[28:31],a[140:143],v[92:95],a[28:31]
buffer_load_dwordx4 a[44:47], v248, s[20:23], s92 offen offset:1024 sc1
MM a[20:23],a[140:143],v[84:87],a[20:23]
v_lshlrev_b32 v152, 22, v152
v_mul_f32 v152, -v152, v153
MM a[20:23],a[136:139],v[80:83],a[20:23]
v_bfe_u32 v156, v241, 24, 8
v_lshl_or_b32 v156, v156, 1, 1
MM a[48:51],a[144:147],v[80:83],a[48:51]
TR a[128:129],v244 offset:0
TR a[130:131],v244 offset:1024
MM a[48:51],a[148:151],v[84:87],a[48:51]
buffer_load_dwordx4 a[76:79], v248, s[20:23], s92 offen offset:2048 sc1
MM a[56:59],a[148:151],v[92:95],a[56:59]
TR a[132:133],v244 offset:2048
TR a[134:135],v244 offset:3072
MM a[56:59],a[144:147],v[88:91],a[56:59]
v_lshlrev_b32 v156, 22, v156
v_mul_f32 v156, -v156, v157
MM a[60:63],a[152:155],v[88:91],a[60:63]
s_mul_i32 s78, s49, 17408
TR a[136:137],v244 offset:4096
TR a[138:139],v244 offset:5120
MM a[60:63],a[156:159],v[92:95],a[60:63]
s_add_u32 s78, s83, s78
buffer_load_dwordx4 a[108:111], v248, s[20:23], s92 offen offset:3072 sc1
MM a[52:55],a[156:159],v[84:87],a[52:55]
s_add_u32 s78, s78, 8192
TR a[140:141],v244 offset:6144
TR a[142:143],v244 offset:7168
MM a[52:55],a[152:155],v[80:83],a[52:55]
s_add_u32 s79, s78, 4096
AR v16,a16
AR v17,a17
MM a[80:83],a[160:163],v[80:83],a[80:83]
TR a[144:145],v235 offset:0
TR a[146:147],v235 offset:1024
MM a[80:83],a[164:167],v[84:87],a[80:83]
AR v18,a18
AR v19,a19
MM a[88:91],a[164:167],v[92:95],a[88:91]
TR a[148:149],v235 offset:2048
TR a[150:151],v235 offset:3072
MM a[88:91],a[160:163],v[88:91],a[88:91]
AR v20,a20
AR v21,a21
MM a[92:95],a[168:171],v[88:91],a[92:95]
TR a[152:153],v235 offset:4096
TR a[154:155],v235 offset:5120
MM a[92:95],a[172:175],v[92:95],a[92:95]
s_xor_b32 s66, s94, 7
AR v22,a22
AR v23,a23
MM a[84:87],a[172:175],v[84:87],a[84:87]
s_mul_i32 s66, s66, 163840
TR a[156:157],v235 offset:6144
TR a[158:159],v235 offset:7168
v_fma_f32 v16, v16, v153, v152
MM a[84:87],a[168:171],v[80:83],a[84:87]
s_add_u32 s66, s66, s87
v_fma_f32 v17, v17, v153, v152
v_fma_f32 v18, v18, v153, v152
MM a[112:115],a[176:179],v[80:83],a[112:115]
s_add_u32 m0, s65, 114688
TR a[160:161],v144 offset:0
TR a[162:163],v144 offset:2048
v_fma_f32 v19, v19, v153, v152
MM a[112:115],a[180:183],v[84:87],a[112:115]
buffer_load_dwordx4 v240, s[24:27], s66 offen offset:0 lds
v_fma_f32 v20, v20, v153, v152
v_fma_f32 v21, v21, v153, v152
MM a[120:123],a[180:183],v[92:95],a[120:123]
s_add_u32 s66, s66, 9216
TR a[164:165],v144 offset:4096
TR a[166:167],v144 offset:6144
v_fma_f32 v22, v22, v153, v152
v_fma_f32 v23, v23, v153, v152
MM a[120:123],a[176:179],v[88:91],a[120:123]
CV v168,v16,v17
CV v169,v18,v19
MM a[124:127],a[184:187],v[88:91],a[124:127]
TR a[168:169],v144 offset:8192
TR a[170:171],v144 offset:10240
CV v170,v20,v21
MM a[124:127],a[188:191],v[92:95],a[124:127]
buffer_load_dwordx4 v240, s[24:27], s66 offen offset:1024 lds
CV v171,v22,v23
MM a[116:119],a[188:191],v[84:87],a[116:119]
s_add_u32 s66, s66, 9216
TR a[172:173],v144 offset:12288
TR a[174:175],v144 offset:14336
MM a[116:119],a[184:187],v[80:83],a[116:119]
buffer_store_dwordx4 v[168:171], v248, s[20:23], s78 offen offset:0 sc1
AR v24,a24
AR v25,a25
s_waitcnt vmcnt(23)
s_waitcnt lgkmcnt(15)
MM v[128:131],a[128:131],a[0:3],0
TR a[176:177],v145 offset:0
TR a[178:179],v145 offset:2048
AR v26,a26
AR v27,a27
MM v[128:131],a[132:135],a[32:35],v[128:131]
v_bfe_u32 v249, v248, 6, 2
v_lshlrev_b32 v249, 8, v249
AR v28,a28
AR v29,a29
MM v[128:131],a[136:139],a[64:67],v[128:131]
TR a[180:181],v145 offset:4096
TR a[182:183],v145 offset:6144
AR v30,a30
MM v[128:131],a[140:143],a[96:99],v[128:131]
s_waitcnt vmcnt(19)
v_lshrrev_b32 v250, 8, v248
v_lshl_add_u32 v249, v250, 6, v249
AR v31,a31
v_fma_f32 v24, v24, v157, v156
MM v[136:139],a[140:143],a[100:103],0
TR a[184:185],v145 offset:8192
TR a[186:187],v145 offset:10240
v_fma_f32 v25, v25, v157, v156
v_fma_f32 v26, v26, v157, v156
MM v[136:139],a[136:139],a[68:71],v[136:139]
buffer_load_dwordx4 v240, s[24:27], s66 offen offset:2048 lds
v_and_b32 v251, 48, v248
v_add_u32 v249, v249, v251
v_fma_f32 v27, v27, v157, v156
MM v[136:139],a[132:135],a[36:39],v[136:139]
s_add_u32 s66, s66, 9216
TR a[188:189],v145 offset:12288
TR a[190:191],v145 offset:14336
v_fma_f32 v28, v28, v157, v156
v_fma_f32 v29, v29, v157, v156
MM v[136:139],a[128:131],a[4:7],v[136:139]
s_waitcnt lgkmcnt(15)
v_fma_f32 v30, v30, v157, v156
v_fma_f32 v31, v31, v157, v156
MM v[140:143],a[144:147],a[4:7],0
s_lshr_b32 s60, s49, 3
TR a[128:129],v146 offset:0
TR a[130:131],v146 offset:2048
CV v172,v24,v25
CV v173,v26,v27
MM v[140:143],a[148:151],a[36:39],v[140:143]
buffer_load_dwordx4 v240, s[24:27], s66 offen offset:3072 lds
CV v174,v28,v29
MM v[140:143],a[152:155],a[68:71],v[140:143]
TR a[132:133],v146 offset:4096
TR a[134:135],v146 offset:6144
CV v175,v30,v31
MM v[140:143],a[156:159],a[100:103],v[140:143]
s_xor_b32 s66, s94, 7
buffer_store_dwordx4 v[172:175], v248, s[20:23], s79 offen offset:0 sc1
MM v[132:135],a[156:159],a[96:99],0
s_mul_i32 s66, s66, 163840
TR a[136:137],v146 offset:8192
TR a[138:139],v146 offset:10240
AR v48,a48
AR v49,a49
MM v[132:135],a[152:155],a[64:67],v[132:135]
s_add_u32 s66, s66, s86
AR v50,a50
AR v51,a51
MM v[132:135],a[148:151],a[32:35],v[132:135]
)ASM"
R"ASM(
s_add_u32 m0, s65, 49152
TR a[140:141],v146 offset:12288
TR a[142:143],v146 offset:14336
AR v52,a52
MM v[132:135],a[144:147],a[0:3],v[132:135]
s_waitcnt lgkmcnt(15)
buffer_load_dwordx4 v240, s[24:27], s66 offen offset:0 lds
AR v53,a53
AR v54,a54
MM v[64:67],a[160:163],a[0:3],0
s_xor_b32 s92, s49, 6
TR a[144:145],v147 offset:0
TR a[146:147],v147 offset:2048
AR v55,a55
v_fma_f32 v48, v48, v153, v152
MM v[64:67],a[164:167],a[32:35],v[64:67]
s_mul_i32 s92, s92, 17408
s_add_u32 s92, s83, s92
v_fma_f32 v49, v49, v153, v152
v_fma_f32 v50, v50, v153, v152
MM v[64:67],a[168:171],a[64:67],v[64:67]
s_add_u32 s66, s66, 9216
TR a[148:149],v147 offset:4096
TR a[150:151],v147 offset:6144
v_fma_f32 v51, v51, v153, v152
MM v[64:67],a[172:175],a[96:99],v[64:67]
buffer_load_dwordx4 v[160:163], v248, s[20:23], s92 offen offset:0 sc1
v_fma_f32 v52, v52, v153, v152
v_fma_f32 v53, v53, v153, v152
MM v[72:75],a[172:175],a[100:103],0
TR a[152:153],v147 offset:8192
TR a[154:155],v147 offset:10240
v_fma_f32 v54, v54, v153, v152
v_fma_f32 v55, v55, v153, v152
MM v[72:75],a[168:171],a[68:71],v[72:75]
CV v184,v48,v49
CV v185,v50,v51
MM v[72:75],a[164:167],a[36:39],v[72:75]
TR a[156:157],v147 offset:12288
TR a[158:159],v147 offset:14336
CV v186,v52,v53
MM v[72:75],a[160:163],a[4:7],v[72:75]
s_waitcnt lgkmcnt(15)
buffer_load_dwordx4 v[176:179], v248, s[20:23], s92 offen offset:1024 sc1
CV v187,v54,v55
MM v[76:79],a[176:179],a[4:7],0
TR a[160:161],v228 offset:0
TR a[162:163],v228 offset:2048
MM v[76:79],a[180:183],a[36:39],v[76:79]
v_xor_b32 v253, s60, v250
v_and_b32 v238, 240, v248
MM v[76:79],a[184:187],a[68:71],v[76:79]
TR a[164:165],v228 offset:4096
TR a[166:167],v228 offset:6144
MM v[76:79],a[188:191],a[100:103],v[76:79]
buffer_store_dwordx4 v[184:187], v248, s[20:23], s78 offen offset:1024 sc1
AR v56,a56
AR v57,a57
AR v58,a58
AR v59,a59
AR v60,a60
AR v61,a61
MM v[68:71],a[188:191],a[96:99],0
TR a[168:169],v228 offset:8192
TR a[170:171],v228 offset:10240
AR v62,a62
MM v[68:71],a[184:187],a[64:67],v[68:71]
AR v63,a63
v_fma_f32 v56, v56, v157, v156
MM v[68:71],a[180:183],a[32:35],v[68:71]
TR a[172:173],v228 offset:12288
TR a[174:175],v228 offset:14336
v_fma_f32 v57, v57, v157, v156
v_fma_f32 v58, v58, v157, v156
MM v[68:71],a[176:179],a[0:3],v[68:71]
s_waitcnt lgkmcnt(15)
buffer_load_dwordx4 v[192:195], v248, s[20:23], s92 offen offset:2048 sc1
v_fma_f32 v59, v59, v157, v156
MM v[96:99],a[128:131],a[0:3],0
TR a[176:177],v229 offset:0
TR a[178:179],v229 offset:2048
v_fma_f32 v60, v60, v157, v156
v_fma_f32 v61, v61, v157, v156
MM v[96:99],a[132:135],a[32:35],v[96:99]
v_fma_f32 v62, v62, v157, v156
v_fma_f32 v63, v63, v157, v156
MM v[96:99],a[136:139],a[64:67],v[96:99]
TR a[180:181],v229 offset:4096
TR a[182:183],v229 offset:6144
CV v188,v56,v57
MM v[96:99],a[140:143],a[96:99],v[96:99]
buffer_load_dwordx4 v[208:211], v248, s[20:23], s92 offen offset:3072 sc1
CV v189,v58,v59
CV v190,v60,v61
MM v[104:107],a[140:143],a[100:103],0
TR a[184:185],v229 offset:8192
TR a[186:187],v229 offset:10240
CV v191,v62,v63
MM v[104:107],a[136:139],a[68:71],v[104:107]
s_add_u32 s92, s92, 4096
v_lshl_add_u32 v232, v250, 2, v238
s_add_u32 s61, s65, 131072
MM v[104:107],a[132:135],a[36:39],v[104:107]
TR a[188:189],v229 offset:12288
TR a[190:191],v229 offset:14336
MM v[104:107],a[128:131],a[4:7],v[104:107]
s_waitcnt lgkmcnt(15)
buffer_store_dwordx4 v[188:191], v248, s[20:23], s79 offen offset:1024 sc1
AR v16,a80
AR v17,a81
AR v18,a82
AR v19,a83
MM v[108:111],a[144:147],a[4:7],0
TR a[128:129],v230 offset:0
TR a[130:131],v230 offset:2048
AR v20,a84
MM v[108:111],a[148:151],a[36:39],v[108:111]
AR v21,a85
AR v22,a86
MM v[108:111],a[152:155],a[68:71],v[108:111]
TR a[132:133],v230 offset:4096
TR a[134:135],v230 offset:6144
AR v23,a87
v_fma_f32 v16, v16, v153, v152
MM v[108:111],a[156:159],a[100:103],v[108:111]
buffer_load_dwordx4 v[164:167], v248, s[20:23], s92 offen offset:0 sc1
v_fma_f32 v17, v17, v153, v152
v_fma_f32 v18, v18, v153, v152
MM v[100:103],a[156:159],a[96:99],0
TR a[136:137],v230 offset:8192
TR a[138:139],v230 offset:10240
v_fma_f32 v19, v19, v153, v152
MM v[100:103],a[152:155],a[64:67],v[100:103]
v_fma_f32 v20, v20, v153, v152
v_fma_f32 v21, v21, v153, v152
MM v[100:103],a[148:151],a[32:35],v[100:103]
TR a[140:141],v230 offset:12288
TR a[142:143],v230 offset:14336
v_fma_f32 v22, v22, v153, v152
v_fma_f32 v23, v23, v153, v152
MM v[100:103],a[144:147],a[0:3],v[100:103]
s_waitcnt lgkmcnt(15)
buffer_load_dwordx4 v[180:183], v248, s[20:23], s92 offen offset:1024 sc1
CV v200,v16,v17
MM v[0:3],a[160:163],a[0:3],0
TR a[144:145],v231 offset:0
TR a[146:147],v231 offset:2048
CV v201,v18,v19
CV v202,v20,v21
MM v[0:3],a[164:167],a[32:35],v[0:3]
CV v203,v22,v23
v_mul_u32_u24 v224, 5, v238
MM v[0:3],a[168:171],a[64:67],v[0:3]
TR a[148:149],v231 offset:4096
TR a[150:151],v231 offset:6144
MM v[0:3],a[172:175],a[96:99],v[0:3]
buffer_store_dwordx4 v[200:203], v248, s[20:23], s78 offen offset:2048 sc1
AR v24,a88
AR v25,a89
MM v[8:11],a[172:175],a[100:103],0
TR a[152:153],v231 offset:8192
TR a[154:155],v231 offset:10240
AR v26,a90
AR v27,a91
MM v[8:11],a[168:171],a[68:71],v[8:11]
AR v28,a92
AR v29,a93
MM v[8:11],a[164:167],a[36:39],v[8:11]
TR a[156:157],v231 offset:12288
TR a[158:159],v231 offset:14336
AR v30,a94
MM v[8:11],a[160:163],a[4:7],v[8:11]
s_waitcnt lgkmcnt(15)
buffer_load_dwordx4 v[196:199], v248, s[20:23], s92 offen offset:2048 sc1
AR v31,a95
v_fma_f32 v24, v24, v157, v156
s_barrier
MM v[12:15],a[176:179],a[4:7],0
TR a[160:161],v244 offset:8192
TR a[162:163],v244 offset:9216
v_fma_f32 v25, v25, v157, v156
v_fma_f32 v26, v26, v157, v156
MM v[12:15],a[180:183],a[36:39],v[12:15]
v_fma_f32 v27, v27, v157, v156
v_fma_f32 v28, v28, v157, v156
MM v[12:15],a[184:187],a[68:71],v[12:15]
TR a[164:165],v244 offset:10240
TR a[166:167],v244 offset:11264
v_fma_f32 v29, v29, v157, v156
MM v[12:15],a[188:191],a[100:103],v[12:15]
buffer_load_dwordx4 v[212:215], v248, s[20:23], s92 offen offset:3072 sc1
v_fma_f32 v30, v30, v157, v156
v_fma_f32 v31, v31, v157, v156
MM v[4:7],a[188:191],a[96:99],0
TR a[168:169],v244 offset:12288
TR a[170:171],v244 offset:13312
CV v204,v24,v25
MM v[4:7],a[184:187],a[64:67],v[4:7]
CV v205,v26,v27
CV v206,v28,v29
MM v[4:7],a[180:183],a[32:35],v[4:7]
TR a[172:173],v244 offset:14336
TR a[174:175],v244 offset:15360
CV v207,v30,v31
MM v[4:7],a[176:179],a[0:3],v[4:7]
s_waitcnt lgkmcnt(15)
buffer_store_dwordx4 v[204:207], v248, s[20:23], s79 offen offset:2048 sc1
MM v[32:35],a[128:131],a[0:3],0
TR a[176:177],v235 offset:8192
TR a[178:179],v235 offset:9216
AR v48,a112
AR v49,a113
MM v[32:35],a[132:135],a[32:35],v[32:35]
AR v50,a114
AR v51,a115
MM v[32:35],a[136:139],a[64:67],v[32:35]
TR a[180:181],v235 offset:10240
TR a[182:183],v235 offset:11264
AR v52,a116
MM v[32:35],a[140:143],a[96:99],v[32:35]
buffer_load_dwordx4 v240, s[24:27], s66 offen offset:1024 lds
AR v53,a117
AR v54,a118
MM v[40:43],a[140:143],a[100:103],0
s_add_u32 s66, s66, 9216
TR a[184:185],v235 offset:12288
TR a[186:187],v235 offset:13312
AR v55,a119
v_fma_f32 v48, v48, v153, v152
MM v[40:43],a[136:139],a[68:71],v[40:43]
v_fma_f32 v49, v49, v153, v152
v_fma_f32 v50, v50, v153, v152
MM v[40:43],a[132:135],a[36:39],v[40:43]
TR a[188:189],v235 offset:14336
TR a[190:191],v235 offset:15360
v_fma_f32 v51, v51, v153, v152
MM v[40:43],a[128:131],a[4:7],v[40:43]
s_waitcnt lgkmcnt(15)
buffer_load_dwordx4 v240, s[24:27], s66 offen offset:2048 lds
v_fma_f32 v52, v52, v153, v152
v_fma_f32 v53, v53, v153, v152
MM v[44:47],a[144:147],a[4:7],0
s_add_u32 s66, s66, 9216
TR a[128:129],v144 offset:16384
TR a[130:131],v144 offset:18432
v_fma_f32 v54, v54, v153, v152
v_fma_f32 v55, v55, v153, v152
MM v[44:47],a[148:151],a[36:39],v[44:47]
CV v216,v48,v49
CV v217,v50,v51
MM v[44:47],a[152:155],a[68:71],v[44:47]
TR a[132:133],v144 offset:20480
TR a[134:135],v144 offset:22528
CV v218,v52,v53
MM v[44:47],a[156:159],a[100:103],v[44:47]
buffer_load_dwordx4 v240, s[24:27], s66 offen offset:3072 lds
CV v219,v54,v55
MM v[36:39],a[156:159],a[96:99],0
TR a[136:137],v144 offset:24576
TR a[138:139],v144 offset:26624
MM v[36:39],a[152:155],a[64:67],v[36:39]
s_xor_b32 s66, s94, 0
v_lshl_add_u32 v224, v250, 3, v224
v_add_u32 v224, s61, v224
MM v[36:39],a[148:151],a[32:35],v[36:39]
s_mul_i32 s66, s66, 163840
TR a[140:141],v144 offset:28672
TR a[142:143],v144 offset:30720
MM v[36:39],a[144:147],a[0:3],v[36:39]
s_waitcnt lgkmcnt(15)
buffer_store_dwordx4 v[216:219], v248, s[20:23], s78 offen offset:3072 sc1
AR v56,a120
AR v57,a121
AR v58,a122
AR v59,a123
AR v60,a124
s_waitcnt vmcnt(31)
MM v[128:131],a[160:163],a[8:11],v[128:131]
s_add_u32 s66, s66, s88
TR a[144:145],v145 offset:16384
TR a[146:147],v145 offset:18432
AR v61,a125
AR v62,a126
MM v[128:131],a[164:167],a[40:43],v[128:131]
s_add_u32 m0, s89, 131072
AR v63,a127
s_nop 0
LPG27_%=:
s_cbranch_execz LTR1_%=
v_fma_f32 v56, v56, v157, v156
MM v[128:131],a[168:171],a[72:75],v[128:131]
TR a[148:149],v145 offset:20480
TR a[150:151],v145 offset:22528
v_fma_f32 v57, v57, v157, v156
v_fma_f32 v58, v58, v157, v156
MM v[128:131],a[172:175],a[104:107],v[128:131]
s_waitcnt vmcnt(23)
buffer_load_dwordx4 v252, s[24:27], s66 offen offset:0 lds
v_fma_f32 v59, v59, v157, v156
MM v[136:139],a[172:175],a[108:111],v[136:139]
s_add_u32 s66, s66, 19456
TR a[152:153],v145 offset:24576
TR a[154:155],v145 offset:26624
v_fma_f32 v60, v60, v157, v156
v_fma_f32 v61, v61, v157, v156
MM v[136:139],a[168:171],a[76:79],v[136:139]
v_fma_f32 v62, v62, v157, v156
v_fma_f32 v63, v63, v157, v156
MM v[136:139],a[164:167],a[44:47],v[136:139]
TR a[156:157],v145 offset:28672
TR a[158:159],v145 offset:30720
CV v220,v56,v57
MM v[136:139],a[160:163],a[12:15],v[136:139]
s_waitcnt lgkmcnt(15)
buffer_load_dwordx4 v252, s[24:27], s66 offen offset:1024 lds
CV v221,v58,v59
CV v222,v60,v61
MM v[140:143],a[176:179],a[12:15],v[140:143]
TR a[160:161],v146 offset:16384
TR a[162:163],v146 offset:18432
CV v223,v62,v63
MM v[140:143],a[180:183],a[44:47],v[140:143]
s_xor_b32 s66, s94, 0
v_mul_u32_u24 v254, 1280, v250
v_mul_u32_u24 v250, 33792, v253
MM v[140:143],a[184:187],a[76:79],v[140:143]
s_mul_i32 s66, s66, 163840
TR a[164:165],v146 offset:20480
TR a[166:167],v146 offset:22528
MM v[140:143],a[188:191],a[108:111],v[140:143]
s_add_u32 s66, s66, s87
buffer_store_dwordx4 v[220:223], v248, s[20:23], s79 offen offset:3072 sc1
MM v[132:135],a[188:191],a[104:107],v[132:135]
s_add_u32 m0, s65, 65536
TR a[168:169],v146 offset:24576
TR a[170:171],v146 offset:26624
MM v[132:135],a[184:187],a[72:75],v[132:135]
v_add_u32 v250, v250, v238
v_lshlrev_b32 v253, 4, v253
MM v[132:135],a[180:183],a[40:43],v[132:135]
TR a[172:173],v146 offset:28672
TR a[174:175],v146 offset:30720
MM v[132:135],a[176:179],a[8:11],v[132:135]
s_waitcnt lgkmcnt(15)
buffer_load_dwordx4 v240, s[24:27], s66 offen offset:0 lds
MM v[64:67],a[128:131],a[8:11],v[64:67]
s_xor_b32 s92, s49, 7
TR a[176:177],v147 offset:16384
TR a[178:179],v147 offset:18432
MM v[64:67],a[132:135],a[40:43],v[64:67]
s_mul_i32 s92, s92, 17408
s_add_u32 s92, s83, s92
v_bfe_u32 v238, v248, 4, 3
MM v[64:67],a[136:139],a[72:75],v[64:67]
s_add_u32 s66, s66, 9216
TR a[180:181],v147 offset:20480
TR a[182:183],v147 offset:22528
MM v[64:67],a[140:143],a[104:107],v[64:67]
buffer_load_dwordx4 a[192:195], v248, s[20:23], s92 offen offset:0 sc1
MM v[72:75],a[140:143],a[108:111],v[72:75]
TR a[184:185],v147 offset:24576
TR a[186:187],v147 offset:26624
MM v[72:75],a[136:139],a[76:79],v[72:75]
v_lshl_add_u32 v254, v238, 3, v254
v_bfe_u32 v238, v248, 7, 1
MM v[72:75],a[132:135],a[44:47],v[72:75]
TR a[188:189],v147 offset:28672
TR a[190:191],v147 offset:30720
MM v[72:75],a[128:131],a[12:15],v[72:75]
s_waitcnt lgkmcnt(15)
buffer_load_dwordx4 a[208:211], v248, s[20:23], s92 offen offset:1024 sc1
MM v[76:79],a[144:147],a[12:15],v[76:79]
TR a[128:129],v228 offset:16384
TR a[130:131],v228 offset:18432
MM v[76:79],a[148:151],a[44:47],v[76:79]
v_mul_u32_u24 v238, 5120, v238
v_add_u32 v254, v254, v238
MM v[76:79],a[152:155],a[76:79],v[76:79]
TR a[132:133],v228 offset:20480
TR a[134:135],v228 offset:22528
)ASM"
R"ASM(
MM v[76:79],a[156:159],a[108:111],v[76:79]
buffer_load_dwordx4 a[224:227], v248, s[20:23], s92 offen offset:2048 sc1
MM v[68:71],a[156:159],a[104:107],v[68:71]
TR a[136:137],v228 offset:24576
TR a[138:139],v228 offset:26624
MM v[68:71],a[152:155],a[72:75],v[68:71]
v_lshrrev_b32 v238, 6, v248
v_mul_u32_u24 v225, 80, v238
MM v[68:71],a[148:151],a[40:43],v[68:71]
TR a[140:141],v228 offset:28672
TR a[142:143],v228 offset:30720
MM v[68:71],a[144:147],a[8:11],v[68:71]
s_waitcnt lgkmcnt(15)
buffer_load_dwordx4 a[240:243], v248, s[20:23], s92 offen offset:3072 sc1
MM v[96:99],a[160:163],a[8:11],v[96:99]
TR a[144:145],v229 offset:16384
TR a[146:147],v229 offset:18432
MM v[96:99],a[164:167],a[40:43],v[96:99]
s_add_u32 s92, s92, 4096
v_add3_u32 v225, v225, v251, s61
v_mul_u32_u24 v238, 1280, v238
MM v[96:99],a[168:171],a[72:75],v[96:99]
TR a[148:149],v229 offset:20480
TR a[150:151],v229 offset:22528
MM v[96:99],a[172:175],a[104:107],v[96:99]
buffer_load_dwordx4 a[196:199], v248, s[20:23], s92 offen offset:0 sc1
MM v[104:107],a[172:175],a[108:111],v[104:107]
TR a[152:153],v229 offset:24576
TR a[154:155],v229 offset:26624
MM v[104:107],a[168:171],a[76:79],v[104:107]
v_add_u32 v251, v251, v238
MM v[104:107],a[164:167],a[44:47],v[104:107]
TR a[156:157],v229 offset:28672
TR a[158:159],v229 offset:30720
MM v[104:107],a[160:163],a[12:15],v[104:107]
s_waitcnt lgkmcnt(15)
buffer_load_dwordx4 a[212:215], v248, s[20:23], s92 offen offset:1024 sc1
MM v[108:111],a[176:179],a[12:15],v[108:111]
TR a[160:161],v230 offset:16384
TR a[162:163],v230 offset:18432
MM v[108:111],a[180:183],a[44:47],v[108:111]
MM v[108:111],a[184:187],a[76:79],v[108:111]
TR a[164:165],v230 offset:20480
TR a[166:167],v230 offset:22528
MM v[108:111],a[188:191],a[108:111],v[108:111]
buffer_load_dwordx4 a[228:231], v248, s[20:23], s92 offen offset:2048 sc1
MM v[100:103],a[188:191],a[104:107],v[100:103]
TR a[168:169],v230 offset:24576
TR a[170:171],v230 offset:26624
MM v[100:103],a[184:187],a[72:75],v[100:103]
MM v[100:103],a[180:183],a[40:43],v[100:103]
TR a[172:173],v230 offset:28672
TR a[174:175],v230 offset:30720
MM v[100:103],a[176:179],a[8:11],v[100:103]
s_waitcnt lgkmcnt(15)
buffer_load_dwordx4 a[244:247], v248, s[20:23], s92 offen offset:3072 sc1
MM v[0:3],a[128:131],a[8:11],v[0:3]
TR a[176:177],v231 offset:16384
TR a[178:179],v231 offset:18432
MM v[0:3],a[132:135],a[40:43],v[0:3]
MM v[0:3],a[136:139],a[72:75],v[0:3]
TR a[180:181],v231 offset:20480
TR a[182:183],v231 offset:22528
MM v[0:3],a[140:143],a[104:107],v[0:3]
buffer_load_dwordx4 v240, s[24:27], s66 offen offset:1024 lds
MM v[8:11],a[140:143],a[108:111],v[8:11]
s_add_u32 s66, s66, 9216
TR a[184:185],v231 offset:24576
TR a[186:187],v231 offset:26624
MM v[8:11],a[136:139],a[76:79],v[8:11]
MM v[8:11],a[132:135],a[44:47],v[8:11]
TR a[188:189],v231 offset:28672
TR a[190:191],v231 offset:30720
MM v[8:11],a[128:131],a[12:15],v[8:11]
s_waitcnt lgkmcnt(15)
buffer_load_dwordx4 v240, s[24:27], s66 offen offset:2048 lds
s_barrier
MM v[12:15],a[144:147],a[12:15],v[12:15]
s_add_u32 s66, s66, 9216
TR a[128:129],v244 offset:16384
TR a[130:131],v244 offset:17408
MM v[12:15],a[148:151],a[44:47],v[12:15]
MM v[12:15],a[152:155],a[76:79],v[12:15]
TR a[132:133],v244 offset:18432
TR a[134:135],v244 offset:19456
MM v[12:15],a[156:159],a[108:111],v[12:15]
buffer_load_dwordx4 v240, s[24:27], s66 offen offset:3072 lds
MM v[4:7],a[156:159],a[104:107],v[4:7]
TR a[136:137],v244 offset:20480
TR a[138:139],v244 offset:21504
MM v[4:7],a[152:155],a[72:75],v[4:7]
s_xor_b32 s66, s94, 0
s_mul_i32 s66, s66, 163840
s_add_u32 s66, s66, s86
MM v[4:7],a[148:151],a[40:43],v[4:7]
s_add_u32 m0, s65, 0
TR a[140:141],v244 offset:22528
TR a[142:143],v244 offset:23552
MM v[4:7],a[144:147],a[8:11],v[4:7]
s_waitcnt lgkmcnt(15)
buffer_load_dwordx4 v240, s[24:27], s66 offen offset:0 lds
MM v[32:35],a[160:163],a[8:11],v[32:35]
s_add_u32 s66, s66, 9216
TR a[144:145],v235 offset:16384
TR a[146:147],v235 offset:17408
MM v[32:35],a[164:167],a[40:43],v[32:35]
MM v[32:35],a[168:171],a[72:75],v[32:35]
TR a[148:149],v235 offset:18432
TR a[150:151],v235 offset:19456
MM v[32:35],a[172:175],a[104:107],v[32:35]
buffer_load_dwordx4 v240, s[24:27], s66 offen offset:1024 lds
MM v[40:43],a[172:175],a[108:111],v[40:43]
s_add_u32 s66, s66, 9216
TR a[152:153],v235 offset:20480
TR a[154:155],v235 offset:21504
MM v[40:43],a[168:171],a[76:79],v[40:43]
MM v[40:43],a[164:167],a[44:47],v[40:43]
TR a[156:157],v235 offset:22528
TR a[158:159],v235 offset:23552
MM v[40:43],a[160:163],a[12:15],v[40:43]
s_waitcnt lgkmcnt(15)
buffer_load_dwordx4 v240, s[24:27], s66 offen offset:2048 lds
MM v[44:47],a[176:179],a[12:15],v[44:47]
s_lshl_b32 s97, s84, 2
TR a[160:161],v144 offset:32768
TR a[162:163],v144 offset:34816
MM v[44:47],a[180:183],a[44:47],v[44:47]
s_add_u32 s97, s97, s96
s_add_u32 s97, s97, 16
s_waitcnt vmcnt(15)
MM v[44:47],a[184:187],a[76:79],v[44:47]
s_add_u32 s66, s66, 9216
TR a[164:165],v144 offset:36864
TR a[166:167],v144 offset:38912
MM v[44:47],a[188:191],a[108:111],v[44:47]
v_mov_b32 v242, s69
s_mov_b64 exec, 1
buffer_store_dword v242, off, s[20:23], s97 offset:0 sc1
s_mov_b64 exec, -1
MM v[36:39],a[188:191],a[104:107],v[36:39]
TR a[168:169],v144 offset:40960
TR a[170:171],v144 offset:43008
MM v[36:39],a[184:187],a[72:75],v[36:39]
buffer_load_dwordx4 v240, s[24:27], s66 offen offset:3072 lds
MM v[36:39],a[180:183],a[40:43],v[36:39]
TR a[172:173],v144 offset:45056
TR a[174:175],v144 offset:47104
MM v[36:39],a[176:179],a[8:11],v[36:39]
s_waitcnt lgkmcnt(15)
s_xor_b32 s66, s94, 1
s_mul_i32 s66, s66, 163840
MM v[128:131],a[128:131],v[160:163],v[128:131]
s_add_u32 s66, s66, s88
TR a[176:177],v145 offset:32768
TR a[178:179],v145 offset:34816
MM v[128:131],a[132:135],v[176:179],v[128:131]
s_add_u32 m0, s89, 139264
MM v[128:131],a[136:139],v[192:195],v[128:131]
TR a[180:181],v145 offset:36864
TR a[182:183],v145 offset:38912
MM v[128:131],a[140:143],v[208:211],v[128:131]
buffer_load_dwordx4 v252, s[24:27], s66 offen offset:0 lds
MM v[136:139],a[140:143],v[212:215],v[136:139]
s_add_u32 s66, s66, 19456
TR a[184:185],v145 offset:40960
TR a[186:187],v145 offset:43008
MM v[136:139],a[136:139],v[196:199],v[136:139]
MM v[136:139],a[132:135],v[180:183],v[136:139]
TR a[188:189],v145 offset:45056
TR a[190:191],v145 offset:47104
MM v[136:139],a[128:131],v[164:167],v[136:139]
s_waitcnt lgkmcnt(15)
buffer_load_dwordx4 v252, s[24:27], s66 offen offset:1024 lds
MM v[140:143],a[144:147],v[164:167],v[140:143]
TR a[128:129],v146 offset:32768
TR a[130:131],v146 offset:34816
MM v[140:143],a[148:151],v[180:183],v[140:143]
s_xor_b32 s66, s94, 1
s_mul_i32 s66, s66, 163840
s_add_u32 s66, s66, s87
MM v[140:143],a[152:155],v[196:199],v[140:143]
s_add_u32 m0, s65, 81920
TR a[132:133],v146 offset:36864
TR a[134:135],v146 offset:38912
MM v[140:143],a[156:159],v[212:215],v[140:143]
buffer_load_dwordx4 v240, s[24:27], s66 offen offset:0 lds
MM v[132:135],a[156:159],v[208:211],v[132:135]
s_add_u32 s66, s66, 9216
TR a[136:137],v146 offset:40960
TR a[138:139],v146 offset:43008
MM v[132:135],a[152:155],v[192:195],v[132:135]
MM v[132:135],a[148:151],v[176:179],v[132:135]
TR a[140:141],v146 offset:45056
TR a[142:143],v146 offset:47104
MM v[132:135],a[144:147],v[160:163],v[132:135]
s_waitcnt lgkmcnt(15)
buffer_load_dwordx4 v240, s[24:27], s66 offen offset:1024 lds
MM v[64:67],a[160:163],v[160:163],v[64:67]
s_add_u32 s66, s66, 9216
TR a[144:145],v147 offset:32768
TR a[146:147],v147 offset:34816
MM v[64:67],a[164:167],v[176:179],v[64:67]
MM v[64:67],a[168:171],v[192:195],v[64:67]
TR a[148:149],v147 offset:36864
TR a[150:151],v147 offset:38912
MM v[64:67],a[172:175],v[208:211],v[64:67]
buffer_load_dwordx4 v240, s[24:27], s66 offen offset:2048 lds
MM v[72:75],a[172:175],v[212:215],v[72:75]
s_add_u32 s66, s66, 9216
TR a[152:153],v147 offset:40960
TR a[154:155],v147 offset:43008
MM v[72:75],a[168:171],v[196:199],v[72:75]
MM v[72:75],a[164:167],v[180:183],v[72:75]
TR a[156:157],v147 offset:45056
TR a[158:159],v147 offset:47104
MM v[72:75],a[160:163],v[164:167],v[72:75]
s_waitcnt lgkmcnt(15)
buffer_load_dwordx4 v240, s[24:27], s66 offen offset:3072 lds
MM v[76:79],a[176:179],v[164:167],v[76:79]
TR a[160:161],v228 offset:32768
TR a[162:163],v228 offset:34816
MM v[76:79],a[180:183],v[180:183],v[76:79]
MM v[76:79],a[184:187],v[196:199],v[76:79]
TR a[164:165],v228 offset:36864
TR a[166:167],v228 offset:38912
MM v[76:79],a[188:191],v[212:215],v[76:79]
MM v[68:71],a[188:191],v[208:211],v[68:71]
TR a[168:169],v228 offset:40960
TR a[170:171],v228 offset:43008
MM v[68:71],a[184:187],v[192:195],v[68:71]
MM v[68:71],a[180:183],v[176:179],v[68:71]
TR a[172:173],v228 offset:45056
TR a[174:175],v228 offset:47104
MM v[68:71],a[176:179],v[160:163],v[68:71]
s_waitcnt lgkmcnt(15)
MM v[96:99],a[128:131],v[160:163],v[96:99]
s_add_u32 s80, s18, 131072
TR a[176:177],v229 offset:32768
TR a[178:179],v229 offset:34816
MM v[96:99],a[132:135],v[176:179],v[96:99]
buffer_load_dwordx4 v[80:83], v251, s[20:23], s80 offen offset:0 sc1
MM v[96:99],a[136:139],v[192:195],v[96:99]
TR a[180:181],v229 offset:36864
TR a[182:183],v229 offset:38912
MM v[96:99],a[140:143],v[208:211],v[96:99]
v_mov_b32 v237, 0x3f80
v_and_b32 v236, 16, v248
MM v[104:107],a[140:143],v[212:215],v[104:107]
TR a[184:185],v229 offset:40960
TR a[186:187],v229 offset:43008
MM v[104:107],a[136:139],v[196:199],v[104:107]
s_add_u32 s80, s18, 151552
buffer_load_dwordx4 v[84:87], v251, s[20:23], s80 offen offset:0 sc1
MM v[104:107],a[132:135],v[180:183],v[104:107]
TR a[188:189],v229 offset:45056
TR a[190:191],v229 offset:47104
MM v[104:107],a[128:131],v[164:167],v[104:107]
s_waitcnt lgkmcnt(15)
v_lshlrev_b32 v236, v236, v237
s_mov_b32 s28, 0x30003
MM v[108:111],a[144:147],v[164:167],v[108:111]
s_mov_b32 s29, s28
TR a[128:129],v230 offset:32768
TR a[130:131],v230 offset:34816
MM v[108:111],a[148:151],v[180:183],v[108:111]
s_add_u32 s80, s63, 131072
v_lshrrev_b32 v243, 2, v248
v_min_u32 v243, 12, v243
MM v[108:111],a[152:155],v[196:199],v[108:111]
TR a[132:133],v230 offset:36864
TR a[134:135],v230 offset:38912
MM v[108:111],a[156:159],v[212:215],v[108:111]
buffer_load_dword v242, v243, s[20:23], s96 offen offset:16 sc1
buffer_load_dwordx2 v[150:151], v254, s[20:23], s80 offen offset:0 sc1
MM v[100:103],a[156:159],v[208:211],v[100:103]
TR a[136:137],v230 offset:40960
TR a[138:139],v230 offset:43008
MM v[100:103],a[152:155],v[192:195],v[100:103]
v_cndmask_b32 v112, 0, v236, s[28:29]
s_lshl_b64 s[28:29], s[28:29], 2
MM v[100:103],a[148:151],v[176:179],v[100:103]
TR a[140:141],v230 offset:45056
TR a[142:143],v230 offset:47104
MM v[100:103],a[144:147],v[160:163],v[100:103]
s_waitcnt lgkmcnt(15)
v_cndmask_b32 v113, 0, v236, s[28:29]
s_lshl_b64 s[28:29], s[28:29], 2
MM v[0:3],a[160:163],v[160:163],v[0:3]
TR a[144:145],v231 offset:32768
TR a[146:147],v231 offset:34816
MM v[0:3],a[164:167],v[176:179],v[0:3]
v_cndmask_b32 v114, 0, v236, s[28:29]
s_lshl_b64 s[28:29], s[28:29], 2
MM v[0:3],a[168:171],v[192:195],v[0:3]
TR a[148:149],v231 offset:36864
TR a[150:151],v231 offset:38912
MM v[0:3],a[172:175],v[208:211],v[0:3]
v_cndmask_b32 v115, 0, v236, s[28:29]
s_lshl_b64 s[28:29], s[28:29], 2
MM v[8:11],a[172:175],v[212:215],v[8:11]
TR a[152:153],v231 offset:40960
TR a[154:155],v231 offset:43008
MM v[8:11],a[168:171],v[196:199],v[8:11]
v_cndmask_b32 v116, 0, v236, s[28:29]
s_lshl_b64 s[28:29], s[28:29], 2
MM v[8:11],a[164:167],v[180:183],v[8:11]
TR a[156:157],v231 offset:45056
TR a[158:159],v231 offset:47104
MM v[8:11],a[160:163],v[164:167],v[8:11]
s_waitcnt lgkmcnt(15)
v_cndmask_b32 v117, 0, v236, s[28:29]
s_lshl_b64 s[28:29], s[28:29], 2
s_barrier
MM v[12:15],a[176:179],v[164:167],v[12:15]
s_xor_b32 s66, s94, 2
TR a[160:161],v244 offset:24576
TR a[162:163],v244 offset:25600
MM v[12:15],a[180:183],v[180:183],v[12:15]
s_mul_i32 s66, s66, 163840
v_cndmask_b32 v118, 0, v236, s[28:29]
s_lshl_b64 s[28:29], s[28:29], 2
MM v[12:15],a[184:187],v[196:199],v[12:15]
s_add_u32 s66, s66, s88
TR a[164:165],v244 offset:26624
TR a[166:167],v244 offset:27648
MM v[12:15],a[188:191],v[212:215],v[12:15]
)ASM"
R"ASM(
s_add_u32 m0, s89, 147456
v_cndmask_b32 v119, 0, v236, s[28:29]
s_mov_b32 s28, 0x300003
MM v[4:7],a[188:191],v[208:211],v[4:7]
s_mov_b32 s29, 0x30000300
TR a[168:169],v244 offset:28672
TR a[170:171],v244 offset:29696
MM v[4:7],a[184:187],v[192:195],v[4:7]
buffer_load_dwordx4 v252, s[24:27], s66 offen offset:0 lds
MM v[4:7],a[180:183],v[176:179],v[4:7]
s_add_u32 s66, s66, 19456
TR a[172:173],v244 offset:30720
TR a[174:175],v244 offset:31744
MM v[4:7],a[176:179],v[160:163],v[4:7]
s_waitcnt lgkmcnt(15)
v_mov_b32 v120, 0
v_mov_b32 v121, 0
MM v[32:35],a[128:131],v[160:163],v[32:35]
TR a[176:177],v235 offset:24576
TR a[178:179],v235 offset:25600
MM v[32:35],a[132:135],v[176:179],v[32:35]
buffer_load_dwordx4 v252, s[24:27], s66 offen offset:1024 lds
MM v[32:35],a[136:139],v[192:195],v[32:35]
TR a[180:181],v235 offset:26624
TR a[182:183],v235 offset:27648
MM v[32:35],a[140:143],v[208:211],v[32:35]
s_xor_b32 s66, s94, 2
v_mov_b32 v124, 0
v_mov_b32 v125, 0
MM v[40:43],a[140:143],v[212:215],v[40:43]
s_mul_i32 s66, s66, 163840
TR a[184:185],v235 offset:28672
TR a[186:187],v235 offset:29696
MM v[40:43],a[136:139],v[196:199],v[40:43]
s_add_u32 s66, s66, s87
v_cndmask_b32 v122, 0, v236, s[28:29]
s_lshl_b64 s[28:29], s[28:29], 2
MM v[40:43],a[132:135],v[180:183],v[40:43]
s_add_u32 m0, s65, 98304
TR a[188:189],v235 offset:30720
TR a[190:191],v235 offset:31744
MM v[40:43],a[128:131],v[164:167],v[40:43]
s_waitcnt lgkmcnt(15)
buffer_load_dwordx4 v240, s[24:27], s66 offen offset:0 lds
MM v[44:47],a[144:147],v[164:167],v[44:47]
s_add_u32 s66, s66, 9216
TR a[128:129],v144 offset:49152
TR a[130:131],v144 offset:51200
MM v[44:47],a[148:151],v[180:183],v[44:47]
v_cndmask_b32 v123, 0, v236, s[28:29]
MM v[44:47],a[152:155],v[196:199],v[44:47]
TR a[132:133],v144 offset:53248
TR a[134:135],v144 offset:55296
MM v[44:47],a[156:159],v[212:215],v[44:47]
buffer_load_dwordx4 v240, s[24:27], s66 offen offset:1024 lds
MM v[36:39],a[156:159],v[208:211],v[36:39]
s_add_u32 s66, s66, 9216
TR a[136:137],v144 offset:57344
TR a[138:139],v144 offset:59392
MM v[36:39],a[152:155],v[192:195],v[36:39]
MM v[36:39],a[148:151],v[176:179],v[36:39]
TR a[140:141],v144 offset:61440
TR a[142:143],v144 offset:63488
MM v[36:39],a[144:147],v[160:163],v[36:39]
s_waitcnt lgkmcnt(15)
buffer_load_dwordx4 v240, s[24:27], s66 offen offset:2048 lds
s_waitcnt vmcnt(27)
MM v[128:131],a[160:163],a[192:195],v[128:131]
s_add_u32 s80, s18, 0
TR a[144:145],v145 offset:49152
TR a[146:147],v145 offset:51200
MM v[128:131],a[164:167],a[208:211],v[128:131]
s_add_u32 s66, s66, 9216
buffer_load_dwordx4 v[88:91], v251, s[32:35], s80 offen offset:0
MM v[128:131],a[168:171],a[224:227],v[128:131]
TR a[148:149],v145 offset:53248
TR a[150:151],v145 offset:55296
MM v[128:131],a[172:175],a[240:243],v[128:131]
s_waitcnt vmcnt(24)
MM v[136:139],a[172:175],a[244:247],v[136:139]
TR a[152:153],v145 offset:57344
TR a[154:155],v145 offset:59392
MM v[136:139],a[168:171],a[228:231],v[136:139]
s_add_u32 s80, s18, 20480
buffer_load_dwordx4 v[92:95], v251, s[32:35], s80 offen offset:0
MM v[136:139],a[164:167],a[212:215],v[136:139]
TR a[156:157],v145 offset:61440
TR a[158:159],v145 offset:63488
MM v[136:139],a[160:163],a[196:199],v[136:139]
s_waitcnt lgkmcnt(15)
MM v[140:143],a[176:179],a[196:199],v[140:143]
TR a[160:161],v146 offset:49152
TR a[162:163],v146 offset:51200
MM v[140:143],a[180:183],a[212:215],v[140:143]
s_add_u32 s80, s63, 0
buffer_load_dwordx2 v[126:127], v254, s[32:35], s80 offen offset:0
MM v[140:143],a[184:187],a[228:231],v[140:143]
TR a[164:165],v146 offset:53248
TR a[166:167],v146 offset:55296
MM v[140:143],a[188:191],a[244:247],v[140:143]
buffer_load_dwordx4 v240, s[24:27], s66 offen offset:3072 lds
MM v[132:135],a[188:191],a[240:243],v[132:135]
TR a[168:169],v146 offset:57344
TR a[170:171],v146 offset:59392
MM v[132:135],a[184:187],a[224:227],v[132:135]
MM v[132:135],a[180:183],a[208:211],v[132:135]
TR a[172:173],v146 offset:61440
TR a[174:175],v146 offset:63488
MM v[132:135],a[176:179],a[192:195],v[132:135]
s_waitcnt lgkmcnt(15)
MM v[64:67],a[128:131],a[192:195],v[64:67]
s_waitcnt vmcnt(10)
TR a[176:177],v147 offset:49152
TR a[178:179],v147 offset:51200
MM v[64:67],a[132:135],a[208:211],v[64:67]
v_cmp_ne_u32 vcc, s69, v242
s_cbranch_vccnz LRPO1_%=
LRPO1b_%=:
MM v[64:67],a[136:139],a[224:227],v[64:67]
s_xor_b32 s92, s49, 1
TR a[180:181],v147 offset:53248
TR a[182:183],v147 offset:55296
MM v[64:67],a[140:143],a[240:243],v[64:67]
s_mul_i32 s92, s92, 17408
s_add_u32 s92, s83, s92
s_add_u32 s92, s92, 8192
MM v[72:75],a[140:143],a[244:247],v[72:75]
TR a[184:185],v147 offset:57344
TR a[186:187],v147 offset:59392
MM v[72:75],a[136:139],a[228:231],v[72:75]
buffer_load_dwordx4 a[16:19], v248, s[20:23], s92 offen offset:0 sc1
MM v[72:75],a[132:135],a[212:215],v[72:75]
TR a[188:189],v147 offset:61440
TR a[190:191],v147 offset:63488
s_nop 0
LPG28_%=:
s_cbranch_execz LTR1_%=
MM v[72:75],a[128:131],a[196:199],v[72:75]
s_waitcnt lgkmcnt(15)
buffer_load_dwordx4 a[48:51], v248, s[20:23], s92 offen offset:1024 sc1
MM v[76:79],a[144:147],a[196:199],v[76:79]
TR a[128:129],v228 offset:49152
TR a[130:131],v228 offset:51200
MM v[76:79],a[148:151],a[212:215],v[76:79]
MM v[76:79],a[152:155],a[228:231],v[76:79]
TR a[132:133],v228 offset:53248
TR a[134:135],v228 offset:55296
MM v[76:79],a[156:159],a[244:247],v[76:79]
buffer_load_dwordx4 a[80:83], v248, s[20:23], s92 offen offset:2048 sc1
MM v[68:71],a[156:159],a[240:243],v[68:71]
TR a[136:137],v228 offset:57344
TR a[138:139],v228 offset:59392
MM v[68:71],a[152:155],a[224:227],v[68:71]
MM v[68:71],a[148:151],a[208:211],v[68:71]
TR a[140:141],v228 offset:61440
TR a[142:143],v228 offset:63488
MM v[68:71],a[144:147],a[192:195],v[68:71]
s_waitcnt lgkmcnt(15)
buffer_load_dwordx4 a[112:115], v248, s[20:23], s92 offen offset:3072 sc1
MM v[96:99],a[160:163],a[192:195],v[96:99]
s_add_u32 s92, s92, 4096
TR a[144:145],v229 offset:49152
TR a[146:147],v229 offset:51200
MM v[96:99],a[164:167],a[208:211],v[96:99]
MM v[96:99],a[168:171],a[224:227],v[96:99]
TR a[148:149],v229 offset:53248
TR a[150:151],v229 offset:55296
MM v[96:99],a[172:175],a[240:243],v[96:99]
buffer_load_dwordx4 a[20:23], v248, s[20:23], s92 offen offset:0 sc1
MM v[104:107],a[172:175],a[244:247],v[104:107]
TR a[152:153],v229 offset:57344
TR a[154:155],v229 offset:59392
MM v[104:107],a[168:171],a[228:231],v[104:107]
MM v[104:107],a[164:167],a[212:215],v[104:107]
TR a[156:157],v229 offset:61440
TR a[158:159],v229 offset:63488
MM v[104:107],a[160:163],a[196:199],v[104:107]
s_waitcnt lgkmcnt(15)
buffer_load_dwordx4 a[52:55], v248, s[20:23], s92 offen offset:1024 sc1
MM v[108:111],a[176:179],a[196:199],v[108:111]
TR a[160:161],v230 offset:49152
TR a[162:163],v230 offset:51200
MM v[108:111],a[180:183],a[212:215],v[108:111]
MM v[108:111],a[184:187],a[228:231],v[108:111]
TR a[164:165],v230 offset:53248
TR a[166:167],v230 offset:55296
MM v[108:111],a[188:191],a[244:247],v[108:111]
buffer_load_dwordx4 a[84:87], v248, s[20:23], s92 offen offset:2048 sc1
MM v[100:103],a[188:191],a[240:243],v[100:103]
TR a[168:169],v230 offset:57344
TR a[170:171],v230 offset:59392
MM v[100:103],a[184:187],a[224:227],v[100:103]
MM v[100:103],a[180:183],a[208:211],v[100:103]
TR a[172:173],v230 offset:61440
TR a[174:175],v230 offset:63488
MM v[100:103],a[176:179],a[192:195],v[100:103]
s_waitcnt lgkmcnt(15)
buffer_load_dwordx4 a[116:119], v248, s[20:23], s92 offen offset:3072 sc1
MM v[0:3],a[128:131],a[192:195],v[0:3]
TR a[176:177],v231 offset:49152
TR a[178:179],v231 offset:51200
MM v[0:3],a[132:135],a[208:211],v[0:3]
MM v[0:3],a[136:139],a[224:227],v[0:3]
TR a[180:181],v231 offset:53248
TR a[182:183],v231 offset:55296
MM v[0:3],a[140:143],a[240:243],v[0:3]
MM v[8:11],a[140:143],a[244:247],v[8:11]
TR a[184:185],v231 offset:57344
TR a[186:187],v231 offset:59392
MM v[8:11],a[136:139],a[228:231],v[8:11]
MM v[8:11],a[132:135],a[212:215],v[8:11]
TR a[188:189],v231 offset:61440
TR a[190:191],v231 offset:63488
MM v[8:11],a[128:131],a[196:199],v[8:11]
s_waitcnt lgkmcnt(15)
s_barrier
MM v[12:15],a[144:147],a[196:199],v[12:15]
s_xor_b32 s66, s94, 3
TR a[128:129],v244 offset:0
TR a[130:131],v244 offset:1024
MM v[12:15],a[148:151],a[212:215],v[12:15]
s_mul_i32 s66, s66, 163840
s_add_u32 s66, s66, s88
s_add_u32 m0, s89, 155648
MM v[12:15],a[152:155],a[228:231],v[12:15]
TR a[132:133],v244 offset:2048
TR a[134:135],v244 offset:3072
MM v[12:15],a[156:159],a[244:247],v[12:15]
buffer_load_dwordx4 v252, s[24:27], s66 offen offset:0 lds
MM v[4:7],a[156:159],a[240:243],v[4:7]
s_add_u32 s66, s66, 19456
TR a[136:137],v244 offset:4096
TR a[138:139],v244 offset:5120
MM v[4:7],a[152:155],a[224:227],v[4:7]
MM v[4:7],a[148:151],a[208:211],v[4:7]
TR a[140:141],v244 offset:6144
TR a[142:143],v244 offset:7168
MM v[4:7],a[144:147],a[192:195],v[4:7]
s_waitcnt lgkmcnt(15)
buffer_load_dwordx4 v252, s[24:27], s66 offen offset:1024 lds
MM v[32:35],a[160:163],a[192:195],v[32:35]
TR a[144:145],v235 offset:0
TR a[146:147],v235 offset:1024
MM v[32:35],a[164:167],a[208:211],v[32:35]
s_xor_b32 s66, s94, 3
s_mul_i32 s66, s66, 163840
s_add_u32 s66, s66, s87
MM v[32:35],a[168:171],a[224:227],v[32:35]
s_add_u32 m0, s65, 114688
TR a[148:149],v235 offset:2048
TR a[150:151],v235 offset:3072
MM v[32:35],a[172:175],a[240:243],v[32:35]
buffer_load_dwordx4 v240, s[24:27], s66 offen offset:0 lds
MM v[40:43],a[172:175],a[244:247],v[40:43]
s_add_u32 s66, s66, 9216
TR a[152:153],v235 offset:4096
TR a[154:155],v235 offset:5120
MM v[40:43],a[168:171],a[228:231],v[40:43]
MM v[40:43],a[164:167],a[212:215],v[40:43]
TR a[156:157],v235 offset:6144
TR a[158:159],v235 offset:7168
MM v[40:43],a[160:163],a[196:199],v[40:43]
s_waitcnt lgkmcnt(15)
buffer_load_dwordx4 v240, s[24:27], s66 offen offset:1024 lds
MM v[44:47],a[176:179],a[196:199],v[44:47]
s_add_u32 s66, s66, 9216
TR a[160:161],v144 offset:0
TR a[162:163],v144 offset:2048
MM v[44:47],a[180:183],a[212:215],v[44:47]
MM v[44:47],a[184:187],a[228:231],v[44:47]
TR a[164:165],v144 offset:4096
TR a[166:167],v144 offset:6144
MM v[44:47],a[188:191],a[244:247],v[44:47]
buffer_load_dwordx4 v240, s[24:27], s66 offen offset:2048 lds
MM v[36:39],a[188:191],a[240:243],v[36:39]
s_add_u32 s66, s66, 9216
TR a[168:169],v144 offset:8192
TR a[170:171],v144 offset:10240
MM v[36:39],a[184:187],a[224:227],v[36:39]
MM v[36:39],a[180:183],a[208:211],v[36:39]
TR a[172:173],v144 offset:12288
TR a[174:175],v144 offset:14336
MM v[36:39],a[176:179],a[192:195],v[36:39]
s_waitcnt lgkmcnt(15)
buffer_load_dwordx4 v240, s[24:27], s66 offen offset:3072 lds
MM v[128:131],a[128:131],v[168:171],v[128:131]
TR a[176:177],v145 offset:0
TR a[178:179],v145 offset:2048
MM v[128:131],a[132:135],v[184:187],v[128:131]
MM v[128:131],a[136:139],v[200:203],v[128:131]
TR a[180:181],v145 offset:4096
TR a[182:183],v145 offset:6144
MM v[128:131],a[140:143],v[216:219],v[128:131]
MM v[136:139],a[140:143],v[220:223],v[136:139]
TR a[184:185],v145 offset:8192
TR a[186:187],v145 offset:10240
MM v[136:139],a[136:139],v[204:207],v[136:139]
MM v[136:139],a[132:135],v[188:191],v[136:139]
TR a[188:189],v145 offset:12288
TR a[190:191],v145 offset:14336
MM v[136:139],a[128:131],v[172:175],v[136:139]
s_waitcnt lgkmcnt(15)
MM v[140:143],a[144:147],v[172:175],v[140:143]
s_xor_b32 s92, s49, 2
TR a[128:129],v146 offset:0
TR a[130:131],v146 offset:2048
MM v[140:143],a[148:151],v[188:191],v[140:143]
s_mul_i32 s92, s92, 17408
s_add_u32 s92, s83, s92
s_add_u32 s92, s92, 8192
MM v[140:143],a[152:155],v[204:207],v[140:143]
TR a[132:133],v146 offset:4096
TR a[134:135],v146 offset:6144
MM v[140:143],a[156:159],v[220:223],v[140:143]
buffer_load_dwordx4 a[24:27], v248, s[20:23], s92 offen offset:0 sc1
MM v[132:135],a[156:159],v[216:219],v[132:135]
TR a[136:137],v146 offset:8192
TR a[138:139],v146 offset:10240
MM v[132:135],a[152:155],v[200:203],v[132:135]
buffer_load_dwordx4 a[56:59], v248, s[20:23], s92 offen offset:1024 sc1
MM v[132:135],a[148:151],v[184:187],v[132:135]
TR a[140:141],v146 offset:12288
TR a[142:143],v146 offset:14336
MM v[132:135],a[144:147],v[168:171],v[132:135]
s_waitcnt lgkmcnt(15)
)ASM"
R"ASM(
MM v[64:67],a[160:163],v[168:171],v[64:67]
TR a[144:145],v147 offset:0
TR a[146:147],v147 offset:2048
MM v[64:67],a[164:167],v[184:187],v[64:67]
buffer_load_dwordx4 a[88:91], v248, s[20:23], s92 offen offset:2048 sc1
MM v[64:67],a[168:171],v[200:203],v[64:67]
TR a[148:149],v147 offset:4096
TR a[150:151],v147 offset:6144
MM v[64:67],a[172:175],v[216:219],v[64:67]
MM v[72:75],a[172:175],v[220:223],v[72:75]
TR a[152:153],v147 offset:8192
TR a[154:155],v147 offset:10240
MM v[72:75],a[168:171],v[204:207],v[72:75]
buffer_load_dwordx4 a[120:123], v248, s[20:23], s92 offen offset:3072 sc1
MM v[72:75],a[164:167],v[188:191],v[72:75]
s_add_u32 s92, s92, 4096
TR a[156:157],v147 offset:12288
TR a[158:159],v147 offset:14336
MM v[72:75],a[160:163],v[172:175],v[72:75]
s_waitcnt lgkmcnt(15)
MM v[76:79],a[176:179],v[172:175],v[76:79]
TR a[160:161],v230 offset:0
TR a[162:163],v230 offset:2048
MM v[76:79],a[180:183],v[188:191],v[76:79]
buffer_load_dwordx4 a[28:31], v248, s[20:23], s92 offen offset:0 sc1
MM v[76:79],a[184:187],v[204:207],v[76:79]
TR a[164:165],v230 offset:4096
TR a[166:167],v230 offset:6144
MM v[76:79],a[188:191],v[220:223],v[76:79]
MM v[68:71],a[188:191],v[216:219],v[68:71]
TR a[168:169],v230 offset:8192
TR a[170:171],v230 offset:10240
MM v[68:71],a[184:187],v[200:203],v[68:71]
buffer_load_dwordx4 a[60:63], v248, s[20:23], s92 offen offset:1024 sc1
MM v[68:71],a[180:183],v[184:187],v[68:71]
TR a[172:173],v230 offset:12288
TR a[174:175],v230 offset:14336
MM v[68:71],a[176:179],v[168:171],v[68:71]
s_waitcnt lgkmcnt(15)
MM v[96:99],a[128:131],v[168:171],v[96:99]
TR a[176:177],v231 offset:0
TR a[178:179],v231 offset:2048
MM v[96:99],a[132:135],v[184:187],v[96:99]
buffer_load_dwordx4 a[92:95], v248, s[20:23], s92 offen offset:2048 sc1
MM v[96:99],a[136:139],v[200:203],v[96:99]
TR a[180:181],v231 offset:4096
TR a[182:183],v231 offset:6144
MM v[96:99],a[140:143],v[216:219],v[96:99]
MM v[104:107],a[140:143],v[220:223],v[104:107]
TR a[184:185],v231 offset:8192
TR a[186:187],v231 offset:10240
MM v[104:107],a[136:139],v[204:207],v[104:107]
buffer_load_dwordx4 a[124:127], v248, s[20:23], s92 offen offset:3072 sc1
MM v[104:107],a[132:135],v[188:191],v[104:107]
s_xor_b32 s92, s49, 3
TR a[188:189],v231 offset:12288
TR a[190:191],v231 offset:14336
MM v[104:107],a[128:131],v[172:175],v[104:107]
s_waitcnt lgkmcnt(15)
s_mul_i32 s92, s92, 17408
s_add_u32 s92, s83, s92
s_barrier
MM v[108:111],a[144:147],v[172:175],v[108:111]
s_add_u32 s92, s92, 8192
TR a[128:129],v244 offset:8192
TR a[130:131],v244 offset:9216
MM v[108:111],a[148:151],v[188:191],v[108:111]
buffer_load_dwordx4 a[200:203], v248, s[20:23], s92 offen offset:0 sc1
MM v[108:111],a[152:155],v[204:207],v[108:111]
TR a[132:133],v244 offset:10240
TR a[134:135],v244 offset:11264
MM v[108:111],a[156:159],v[220:223],v[108:111]
MM v[100:103],a[156:159],v[216:219],v[100:103]
TR a[136:137],v244 offset:12288
TR a[138:139],v244 offset:13312
MM v[100:103],a[152:155],v[200:203],v[100:103]
buffer_load_dwordx4 a[216:219], v248, s[20:23], s92 offen offset:1024 sc1
MM v[100:103],a[148:151],v[184:187],v[100:103]
TR a[140:141],v244 offset:14336
TR a[142:143],v244 offset:15360
MM v[100:103],a[144:147],v[168:171],v[100:103]
s_waitcnt lgkmcnt(15)
MM v[32:35],a[160:163],v[168:171],v[32:35]
TR a[144:145],v235 offset:8192
TR a[146:147],v235 offset:9216
MM v[32:35],a[164:167],v[184:187],v[32:35]
buffer_load_dwordx4 a[232:235], v248, s[20:23], s92 offen offset:2048 sc1
MM v[32:35],a[168:171],v[200:203],v[32:35]
TR a[148:149],v235 offset:10240
TR a[150:151],v235 offset:11264
MM v[32:35],a[172:175],v[216:219],v[32:35]
MM v[40:43],a[172:175],v[220:223],v[40:43]
TR a[152:153],v235 offset:12288
TR a[154:155],v235 offset:13312
MM v[40:43],a[168:171],v[204:207],v[40:43]
buffer_load_dwordx4 a[248:251], v248, s[20:23], s92 offen offset:3072 sc1
MM v[40:43],a[164:167],v[188:191],v[40:43]
s_add_u32 s92, s92, 4096
TR a[156:157],v235 offset:14336
TR a[158:159],v235 offset:15360
MM v[40:43],a[160:163],v[172:175],v[40:43]
s_waitcnt lgkmcnt(15)
MM v[44:47],a[176:179],v[172:175],v[44:47]
TR a[160:161],v144 offset:16384
TR a[162:163],v144 offset:18432
MM v[44:47],a[180:183],v[188:191],v[44:47]
buffer_load_dwordx4 a[204:207], v248, s[20:23], s92 offen offset:0 sc1
MM v[44:47],a[184:187],v[204:207],v[44:47]
TR a[164:165],v144 offset:20480
TR a[166:167],v144 offset:22528
MM v[44:47],a[188:191],v[220:223],v[44:47]
MM v[36:39],a[188:191],v[216:219],v[36:39]
TR a[168:169],v144 offset:24576
TR a[170:171],v144 offset:26624
MM v[36:39],a[184:187],v[200:203],v[36:39]
buffer_load_dwordx4 a[220:223], v248, s[20:23], s92 offen offset:1024 sc1
MM v[36:39],a[180:183],v[184:187],v[36:39]
TR a[172:173],v144 offset:28672
TR a[174:175],v144 offset:30720
MM v[36:39],a[176:179],v[168:171],v[36:39]
s_waitcnt lgkmcnt(15)
s_waitcnt vmcnt(24)
MM v[128:131],a[128:131],a[16:19],v[128:131]
TR a[176:177],v145 offset:16384
TR a[178:179],v145 offset:18432
MM v[128:131],a[132:135],a[48:51],v[128:131]
buffer_load_dwordx4 a[236:239], v248, s[20:23], s92 offen offset:2048 sc1
MM v[128:131],a[136:139],a[80:83],v[128:131]
TR a[180:181],v145 offset:20480
TR a[182:183],v145 offset:22528
MM v[128:131],a[140:143],a[112:115],v[128:131]
s_waitcnt vmcnt(21)
MM v[136:139],a[140:143],a[116:119],v[136:139]
TR a[184:185],v145 offset:24576
TR a[186:187],v145 offset:26624
MM v[136:139],a[136:139],a[84:87],v[136:139]
buffer_load_dwordx4 a[252:255], v248, s[20:23], s92 offen offset:3072 sc1
MM v[136:139],a[132:135],a[52:55],v[136:139]
TR a[188:189],v145 offset:28672
TR a[190:191],v145 offset:30720
MM v[136:139],a[128:131],a[20:23],v[136:139]
s_waitcnt lgkmcnt(15)
MM v[140:143],a[144:147],a[20:23],v[140:143]
TR a[128:129],v146 offset:16384
TR a[130:131],v146 offset:18432
MM v[140:143],a[148:151],a[52:55],v[140:143]
MM v[140:143],a[152:155],a[84:87],v[140:143]
TR a[132:133],v146 offset:20480
TR a[134:135],v146 offset:22528
MM v[140:143],a[156:159],a[116:119],v[140:143]
MM v[132:135],a[156:159],a[112:115],v[132:135]
TR a[136:137],v146 offset:24576
TR a[138:139],v146 offset:26624
MM v[132:135],a[152:155],a[80:83],v[132:135]
MM v[132:135],a[148:151],a[48:51],v[132:135]
TR a[140:141],v146 offset:28672
TR a[142:143],v146 offset:30720
MM v[132:135],a[144:147],a[16:19],v[132:135]
s_waitcnt lgkmcnt(15)
MM v[64:67],a[160:163],a[16:19],v[64:67]
TR a[144:145],v147 offset:16384
TR a[146:147],v147 offset:18432
MM v[64:67],a[164:167],a[48:51],v[64:67]
MM v[64:67],a[168:171],a[80:83],v[64:67]
TR a[148:149],v147 offset:20480
TR a[150:151],v147 offset:22528
MM v[64:67],a[172:175],a[112:115],v[64:67]
MM v[72:75],a[172:175],a[116:119],v[72:75]
TR a[152:153],v147 offset:24576
TR a[154:155],v147 offset:26624
MM v[72:75],a[168:171],a[84:87],v[72:75]
MM v[72:75],a[164:167],a[52:55],v[72:75]
TR a[156:157],v147 offset:28672
TR a[158:159],v147 offset:30720
MM v[72:75],a[160:163],a[20:23],v[72:75]
s_waitcnt lgkmcnt(15)
s_waitcnt vmcnt(16)
s_barrier
MM v[76:79],a[176:179],a[20:23],v[76:79]
s_xor_b32 s66, s94, 1
TR a[160:161],v244 offset:16384
TR a[162:163],v244 offset:17408
MM v[76:79],a[180:183],a[52:55],v[76:79]
s_mul_i32 s66, s66, 163840
s_add_u32 s66, s66, s86
s_add_u32 m0, s65, 16384
MM v[76:79],a[184:187],a[84:87],v[76:79]
TR a[164:165],v244 offset:18432
TR a[166:167],v244 offset:19456
MM v[76:79],a[188:191],a[116:119],v[76:79]
buffer_load_dwordx4 v240, s[24:27], s66 offen offset:0 lds
MM v[68:71],a[188:191],a[112:115],v[68:71]
s_add_u32 s66, s66, 9216
TR a[168:169],v244 offset:20480
TR a[170:171],v244 offset:21504
MM v[68:71],a[184:187],a[80:83],v[68:71]
MM v[68:71],a[180:183],a[48:51],v[68:71]
TR a[172:173],v244 offset:22528
TR a[174:175],v244 offset:23552
MM v[68:71],a[176:179],a[16:19],v[68:71]
s_waitcnt lgkmcnt(15)
buffer_load_dwordx4 v240, s[24:27], s66 offen offset:1024 lds
MM v[96:99],a[128:131],a[16:19],v[96:99]
s_add_u32 s66, s66, 9216
TR a[176:177],v244 offset:24576
TR a[178:179],v244 offset:25600
MM v[96:99],a[132:135],a[48:51],v[96:99]
MM v[96:99],a[136:139],a[80:83],v[96:99]
TR a[180:181],v244 offset:26624
TR a[182:183],v244 offset:27648
MM v[96:99],a[140:143],a[112:115],v[96:99]
buffer_load_dwordx4 v240, s[24:27], s66 offen offset:2048 lds
MM v[104:107],a[140:143],a[116:119],v[104:107]
s_add_u32 s66, s66, 9216
TR a[184:185],v244 offset:28672
TR a[186:187],v244 offset:29696
MM v[104:107],a[136:139],a[84:87],v[104:107]
MM v[104:107],a[132:135],a[52:55],v[104:107]
TR a[188:189],v244 offset:30720
TR a[190:191],v244 offset:31744
MM v[104:107],a[128:131],a[20:23],v[104:107]
s_waitcnt lgkmcnt(15)
buffer_load_dwordx4 v240, s[24:27], s66 offen offset:3072 lds
MM v[108:111],a[144:147],a[20:23],v[108:111]
TR a[128:129],v235 offset:16384
TR a[130:131],v235 offset:17408
MM v[108:111],a[148:151],a[52:55],v[108:111]
s_xor_b32 s66, s94, 2
s_mul_i32 s66, s66, 163840
s_add_u32 s66, s66, s86
MM v[108:111],a[152:155],a[84:87],v[108:111]
s_add_u32 m0, s65, 32768
TR a[132:133],v235 offset:18432
TR a[134:135],v235 offset:19456
MM v[108:111],a[156:159],a[116:119],v[108:111]
buffer_load_dwordx4 v240, s[24:27], s66 offen offset:0 lds
MM v[100:103],a[156:159],a[112:115],v[100:103]
s_add_u32 s66, s66, 9216
TR a[136:137],v235 offset:20480
TR a[138:139],v235 offset:21504
MM v[100:103],a[152:155],a[80:83],v[100:103]
MM v[100:103],a[148:151],a[48:51],v[100:103]
TR a[140:141],v235 offset:22528
TR a[142:143],v235 offset:23552
MM v[100:103],a[144:147],a[16:19],v[100:103]
s_waitcnt lgkmcnt(15)
buffer_load_dwordx4 v240, s[24:27], s66 offen offset:1024 lds
s_waitcnt vmcnt(18)
MM v[128:131],a[160:163],a[24:27],v[128:131]
s_add_u32 s66, s66, 9216
TR a[144:145],v235 offset:24576
TR a[146:147],v235 offset:25600
MM v[128:131],a[164:167],a[56:59],v[128:131]
MM v[128:131],a[168:171],a[88:91],v[128:131]
TR a[148:149],v235 offset:26624
TR a[150:151],v235 offset:27648
MM v[128:131],a[172:175],a[120:123],v[128:131]
s_waitcnt vmcnt(14)
buffer_load_dwordx4 v240, s[24:27], s66 offen offset:2048 lds
MM v[136:139],a[172:175],a[124:127],v[136:139]
s_add_u32 s66, s66, 9216
TR a[152:153],v235 offset:28672
TR a[154:155],v235 offset:29696
MM v[136:139],a[168:171],a[92:95],v[136:139]
MM v[136:139],a[164:167],a[60:63],v[136:139]
TR a[156:157],v235 offset:30720
TR a[158:159],v235 offset:31744
MM v[136:139],a[160:163],a[28:31],v[136:139]
s_waitcnt lgkmcnt(15)
buffer_load_dwordx4 v240, s[24:27], s66 offen offset:3072 lds
s_waitcnt vmcnt(12)
MM v[128:131],a[176:179],a[200:203],v[128:131]
s_memrealtime s[56:57]
TR a[160:161],v144 offset:32768
TR a[162:163],v144 offset:34816
MM v[128:131],a[180:183],a[216:219],v[128:131]
s_xor_b32 s66, s94, 3
s_mul_i32 s66, s66, 163840
s_add_u32 s66, s66, s86
MM v[128:131],a[184:187],a[232:235],v[128:131]
s_add_u32 m0, s65, 49152
TR a[164:165],v144 offset:36864
TR a[166:167],v144 offset:38912
MM v[128:131],a[188:191],a[248:251],v[128:131]
s_waitcnt vmcnt(8)
buffer_load_dwordx4 v240, s[24:27], s66 offen offset:0 lds
MM v[136:139],a[188:191],a[252:255],v[136:139]
s_add_u32 s66, s66, 9216
TR a[168:169],v144 offset:40960
TR a[170:171],v144 offset:43008
MM v[136:139],a[184:187],a[236:239],v[136:139]
MM v[136:139],a[180:183],a[220:223],v[136:139]
TR a[172:173],v144 offset:45056
TR a[174:175],v144 offset:47104
MM v[136:139],a[176:179],a[204:207],v[136:139]
s_waitcnt lgkmcnt(0)
buffer_load_dwordx4 v240, s[24:27], s66 offen offset:1024 lds
MM v[140:143],a[128:131],a[28:31],v[140:143]
s_add_u32 s66, s66, 9216
TR a[176:177],v144 offset:49152
TR a[178:179],v144 offset:51200
MM v[140:143],a[132:135],a[60:63],v[140:143]
v_mov_b32 v158, s56
v_mov_b32 v159, s69
MM v[140:143],a[136:139],a[92:95],v[140:143]
TR a[180:181],v144 offset:53248
TR a[182:183],v144 offset:55296
MM v[140:143],a[140:143],a[124:127],v[140:143]
s_add_u32 s67, s95, 0
CV v128,v128,v129
CV v129,v130,v131
MM v[132:135],a[140:143],a[120:123],v[132:135]
buffer_load_dwordx4 v240, s[24:27], s66 offen offset:2048 lds
s_mov_b64 exec, 1
buffer_store_dwordx2 v[158:159], off, s[20:23], s19 offset:8 sc1
s_mov_b64 exec, -1
s_nop 0
TR a[184:185],v144 offset:57344
TR a[186:187],v144 offset:59392
MM v[132:135],a[136:139],a[88:91],v[132:135]
)ASM"
R"ASM(
s_add_u32 s66, s66, 9216
CV v136,v136,v137
CV v137,v138,v139
MM v[132:135],a[132:135],a[56:59],v[132:135]
TR a[188:189],v144 offset:61440
TR a[190:191],v144 offset:63488
MM v[132:135],a[128:131],a[24:27],v[132:135]
buffer_load_dwordx4 v240, s[24:27], s66 offen offset:3072 lds
MM v[132:135],a[144:147],a[200:203],v[132:135]
TR a[128:129],v145 offset:32768
TR a[130:131],v145 offset:34816
MM v[132:135],a[148:151],a[216:219],v[132:135]
MM v[132:135],a[152:155],a[232:235],v[132:135]
TR a[132:133],v145 offset:36864
TR a[134:135],v145 offset:38912
MM v[132:135],a[156:159],a[248:251],v[132:135]
MM v[140:143],a[156:159],a[252:255],v[140:143]
TR a[136:137],v145 offset:40960
TR a[138:139],v145 offset:43008
MM v[140:143],a[152:155],a[236:239],v[140:143]
MM v[140:143],a[148:151],a[220:223],v[140:143]
TR a[140:141],v145 offset:45056
TR a[142:143],v145 offset:47104
MM v[140:143],a[144:147],a[204:207],v[140:143]
CV v130,v132,v133
CV v131,v134,v135
MM v[72:75],a[160:163],a[28:31],v[72:75]
TR a[144:145],v145 offset:49152
TR a[146:147],v145 offset:51200
MM v[72:75],a[164:167],a[60:63],v[72:75]
v_min_u32 v131, -2, v131
LPG29_%=:
s_cbranch_execz LTR1_%=
MM v[72:75],a[168:171],a[92:95],v[72:75]
TR a[148:149],v145 offset:53248
TR a[150:151],v145 offset:55296
MM v[72:75],a[172:175],a[124:127],v[72:75]
buffer_store_dwordx4 v[128:131], v249, s[20:23], s67 offen offset:0 sc1
MM v[64:67],a[172:175],a[120:123],v[64:67]
TR a[152:153],v145 offset:57344
TR a[154:155],v145 offset:59392
MM v[64:67],a[168:171],a[88:91],v[64:67]
CV v138,v140,v141
CV v139,v142,v143
MM v[64:67],a[164:167],a[56:59],v[64:67]
TR a[156:157],v145 offset:61440
TR a[158:159],v145 offset:63488
MM v[64:67],a[160:163],a[24:27],v[64:67]
s_waitcnt lgkmcnt(15)
v_min_u32 v139, -2, v139
MM v[64:67],a[176:179],a[200:203],v[64:67]
TR a[160:161],v146 offset:32768
TR a[162:163],v146 offset:34816
MM v[64:67],a[180:183],a[216:219],v[64:67]
buffer_store_dwordx4 v[136:139], v249, s[20:23], s67 offen offset:1024 sc1
MM v[64:67],a[184:187],a[232:235],v[64:67]
TR a[164:165],v146 offset:36864
TR a[166:167],v146 offset:38912
MM v[64:67],a[188:191],a[248:251],v[64:67]
MM v[72:75],a[188:191],a[252:255],v[72:75]
TR a[168:169],v146 offset:40960
TR a[170:171],v146 offset:43008
MM v[72:75],a[184:187],a[236:239],v[72:75]
MM v[72:75],a[180:183],a[220:223],v[72:75]
TR a[172:173],v146 offset:45056
TR a[174:175],v146 offset:47104
MM v[72:75],a[176:179],a[204:207],v[72:75]
s_waitcnt lgkmcnt(15)
MM v[76:79],a[128:131],a[28:31],v[76:79]
TR a[176:177],v146 offset:49152
TR a[178:179],v146 offset:51200
MM v[76:79],a[132:135],a[60:63],v[76:79]
MM v[76:79],a[136:139],a[92:95],v[76:79]
TR a[180:181],v146 offset:53248
TR a[182:183],v146 offset:55296
MM v[76:79],a[140:143],a[124:127],v[76:79]
s_add_u32 s67, s95, 4096
CV v64,v64,v65
CV v65,v66,v67
MM v[68:71],a[140:143],a[120:123],v[68:71]
TR a[184:185],v146 offset:57344
TR a[186:187],v146 offset:59392
MM v[68:71],a[136:139],a[88:91],v[68:71]
CV v72,v72,v73
CV v73,v74,v75
MM v[68:71],a[132:135],a[56:59],v[68:71]
TR a[188:189],v146 offset:61440
TR a[190:191],v146 offset:63488
MM v[68:71],a[128:131],a[24:27],v[68:71]
s_waitcnt lgkmcnt(15)
MM v[68:71],a[144:147],a[200:203],v[68:71]
TR a[128:129],v147 offset:32768
TR a[130:131],v147 offset:34816
MM v[68:71],a[148:151],a[216:219],v[68:71]
buffer_load_dwordx2 v[148:149], v253, s[20:23], s68 offen offset:8 sc1
MM v[68:71],a[152:155],a[232:235],v[68:71]
TR a[132:133],v147 offset:36864
TR a[134:135],v147 offset:38912
MM v[68:71],a[156:159],a[248:251],v[68:71]
MM v[76:79],a[156:159],a[252:255],v[76:79]
TR a[136:137],v147 offset:40960
TR a[138:139],v147 offset:43008
MM v[76:79],a[152:155],a[236:239],v[76:79]
MM v[76:79],a[148:151],a[220:223],v[76:79]
TR a[140:141],v147 offset:45056
TR a[142:143],v147 offset:47104
MM v[76:79],a[144:147],a[204:207],v[76:79]
s_waitcnt lgkmcnt(15)
CV v66,v68,v69
CV v67,v70,v71
s_waitcnt vmcnt(3)
s_barrier
MM v[104:107],a[160:163],a[28:31],v[104:107]
TR a[144:145],v147 offset:49152
TR a[146:147],v147 offset:51200
MM v[104:107],a[164:167],a[60:63],v[104:107]
v_min_u32 v67, -2, v67
MM v[104:107],a[168:171],a[92:95],v[104:107]
TR a[148:149],v147 offset:53248
TR a[150:151],v147 offset:55296
MM v[104:107],a[172:175],a[124:127],v[104:107]
buffer_store_dwordx4 v[64:67], v248, s[20:23], s67 offen offset:0 sc1
MM v[96:99],a[172:175],a[120:123],v[96:99]
TR a[152:153],v147 offset:57344
TR a[154:155],v147 offset:59392
MM v[96:99],a[168:171],a[88:91],v[96:99]
CV v74,v76,v77
CV v75,v78,v79
MM v[96:99],a[164:167],a[56:59],v[96:99]
TR a[156:157],v147 offset:61440
TR a[158:159],v147 offset:63488
MM v[96:99],a[160:163],a[24:27],v[96:99]
s_waitcnt lgkmcnt(15)
v_min_u32 v75, -2, v75
MM v[96:99],a[176:179],a[200:203],v[96:99]
TR a[160:161],v230 offset:16384
TR a[162:163],v230 offset:18432
MM v[96:99],a[180:183],a[216:219],v[96:99]
buffer_store_dwordx4 v[72:75], v248, s[20:23], s67 offen offset:1024 sc1
MM v[96:99],a[184:187],a[232:235],v[96:99]
TR a[164:165],v230 offset:20480
TR a[166:167],v230 offset:22528
MM v[96:99],a[188:191],a[248:251],v[96:99]
MM v[104:107],a[188:191],a[252:255],v[104:107]
TR a[168:169],v230 offset:24576
TR a[170:171],v230 offset:26624
MM v[104:107],a[184:187],a[236:239],v[104:107]
MM v[104:107],a[180:183],a[220:223],v[104:107]
TR a[172:173],v230 offset:28672
TR a[174:175],v230 offset:30720
MM v[104:107],a[176:179],a[204:207],v[104:107]
s_waitcnt lgkmcnt(15)
MM v[108:111],a[128:131],a[28:31],v[108:111]
TR a[176:177],v230 offset:32768
TR a[178:179],v230 offset:34816
MM v[108:111],a[132:135],a[60:63],v[108:111]
MM v[108:111],a[136:139],a[92:95],v[108:111]
TR a[180:181],v230 offset:36864
TR a[182:183],v230 offset:38912
MM v[108:111],a[140:143],a[124:127],v[108:111]
s_add_u32 s67, s95, 4096
CV v96,v96,v97
CV v97,v98,v99
MM v[100:103],a[140:143],a[120:123],v[100:103]
TR a[184:185],v230 offset:40960
TR a[186:187],v230 offset:43008
MM v[100:103],a[136:139],a[88:91],v[100:103]
CV v104,v104,v105
CV v105,v106,v107
MM v[100:103],a[132:135],a[56:59],v[100:103]
TR a[188:189],v230 offset:45056
TR a[190:191],v230 offset:47104
MM v[100:103],a[128:131],a[24:27],v[100:103]
s_waitcnt lgkmcnt(15)
MM v[100:103],a[144:147],a[200:203],v[100:103]
TR a[128:129],v230 offset:49152
TR a[130:131],v230 offset:51200
MM v[100:103],a[148:151],a[216:219],v[100:103]
MM v[100:103],a[152:155],a[232:235],v[100:103]
TR a[132:133],v230 offset:53248
TR a[134:135],v230 offset:55296
MM v[100:103],a[156:159],a[248:251],v[100:103]
MM v[108:111],a[156:159],a[252:255],v[108:111]
TR a[136:137],v230 offset:57344
TR a[138:139],v230 offset:59392
MM v[108:111],a[152:155],a[236:239],v[108:111]
MM v[108:111],a[148:151],a[220:223],v[108:111]
TR a[140:141],v230 offset:61440
TR a[142:143],v230 offset:63488
MM v[108:111],a[144:147],a[204:207],v[108:111]
s_waitcnt lgkmcnt(15)
CV v98,v100,v101
CV v99,v102,v103
MM v[40:43],a[160:163],a[20:23],v[40:43]
TR a[144:145],v231 offset:16384
TR a[146:147],v231 offset:18432
MM v[40:43],a[164:167],a[52:55],v[40:43]
v_min_u32 v99, -2, v99
v_lshlrev_b32 v136, 16, v150
MM v[40:43],a[168:171],a[84:87],v[40:43]
TR a[148:149],v231 offset:20480
TR a[150:151],v231 offset:22528
MM v[40:43],a[172:175],a[116:119],v[40:43]
buffer_store_dwordx4 v[96:99], v248, s[20:23], s67 offen offset:2048 sc1
MM v[32:35],a[172:175],a[112:115],v[32:35]
TR a[152:153],v231 offset:24576
TR a[154:155],v231 offset:26624
MM v[32:35],a[168:171],a[80:83],v[32:35]
CV v106,v108,v109
CV v107,v110,v111
MM v[32:35],a[164:167],a[48:51],v[32:35]
TR a[156:157],v231 offset:28672
TR a[158:159],v231 offset:30720
MM v[32:35],a[160:163],a[16:19],v[32:35]
s_waitcnt lgkmcnt(15)
v_min_u32 v107, -2, v107
v_and_b32 v137, 0xffff0000, v150
MM v[32:35],a[176:179],a[24:27],v[32:35]
TR a[160:161],v231 offset:32768
TR a[162:163],v231 offset:34816
MM v[32:35],a[180:183],a[56:59],v[32:35]
buffer_store_dwordx4 v[104:107], v248, s[20:23], s67 offen offset:3072 sc1
MM v[32:35],a[184:187],a[88:91],v[32:35]
TR a[164:165],v231 offset:36864
TR a[166:167],v231 offset:38912
MM v[32:35],a[188:191],a[120:123],v[32:35]
v_lshlrev_b32 v138, 16, v151
v_and_b32 v139, 0xffff0000, v151
MM v[40:43],a[188:191],a[124:127],v[40:43]
TR a[168:169],v231 offset:40960
TR a[170:171],v231 offset:43008
MM v[40:43],a[184:187],a[92:95],v[40:43]
v_mul_f32 v140, 0xbfb8aa3b, v136
v_mul_f32 v141, 0xbfb8aa3b, v137
MM v[40:43],a[180:183],a[60:63],v[40:43]
TR a[172:173],v231 offset:45056
TR a[174:175],v231 offset:47104
MM v[40:43],a[176:179],a[28:31],v[40:43]
s_waitcnt lgkmcnt(15)
v_mul_f32 v142, 0xbfb8aa3b, v138
v_mul_f32 v143, 0xbfb8aa3b, v139
MM v[40:43],a[128:131],a[204:207],v[40:43]
TR a[176:177],v231 offset:49152
TR a[178:179],v231 offset:51200
MM v[40:43],a[132:135],a[220:223],v[40:43]
EX v140,v140
MM v[40:43],a[136:139],a[236:239],v[40:43]
TR a[180:181],v231 offset:53248
TR a[182:183],v231 offset:55296
MM v[40:43],a[140:143],a[252:255],v[40:43]
EX v141,v141
MM v[32:35],a[140:143],a[248:251],v[32:35]
TR a[184:185],v231 offset:57344
TR a[186:187],v231 offset:59392
MM v[32:35],a[136:139],a[232:235],v[32:35]
EX v142,v142
MM v[32:35],a[132:135],a[216:219],v[32:35]
TR a[188:189],v231 offset:61440
TR a[190:191],v231 offset:63488
MM v[32:35],a[128:131],a[200:203],v[32:35]
s_waitcnt lgkmcnt(15)
EX v143,v143
MM v[36:39],a[144:147],a[16:19],v[36:39]
TR a[128:129],v228 offset:0
TR a[130:131],v228 offset:2048
MM v[36:39],a[148:151],a[48:51],v[36:39]
v_add_f32 v140, 1.0, v140
v_add_f32 v141, 1.0, v141
MM v[36:39],a[152:155],a[80:83],v[36:39]
TR a[132:133],v228 offset:4096
TR a[134:135],v228 offset:6144
MM v[36:39],a[156:159],a[112:115],v[36:39]
s_add_u32 s67, s95, 0
CV v32,v32,v33
CV v33,v34,v35
MM v[44:47],a[156:159],a[116:119],v[44:47]
TR a[136:137],v228 offset:8192
TR a[138:139],v228 offset:10240
MM v[44:47],a[152:155],a[84:87],v[44:47]
CV v40,v40,v41
CV v41,v42,v43
MM v[44:47],a[148:151],a[52:55],v[44:47]
TR a[140:141],v228 offset:12288
TR a[142:143],v228 offset:14336
MM v[44:47],a[144:147],a[20:23],v[44:47]
s_waitcnt lgkmcnt(15)
v_add_f32 v142, 1.0, v142
v_add_f32 v143, 1.0, v143
MM v[44:47],a[160:163],a[28:31],v[44:47]
s_waitcnt vmcnt(4)
TR a[144:145],v228 offset:16384
TR a[146:147],v228 offset:18432
MM v[44:47],a[164:167],a[60:63],v[44:47]
v_cmp_eq_u32 s[8:9], s69, v149
v_subrev_u32 v226, s56, v148
MM v[44:47],a[168:171],a[92:95],v[44:47]
TR a[148:149],v228 offset:20480
TR a[150:151],v228 offset:22528
MM v[44:47],a[172:175],a[124:127],v[44:47]
v_rcp_f32 v140, v140
MM v[36:39],a[172:175],a[120:123],v[36:39]
TR a[152:153],v228 offset:24576
TR a[154:155],v228 offset:26624
v_cndmask_b32 v226, 4.0, v226, s[8:9]
MM v[36:39],a[168:171],a[88:91],v[36:39]
v_readlane_b32 s13, v226, 16
v_readlane_b32 s14, v226, 32
MM v[36:39],a[164:167],a[56:59],v[36:39]
TR a[156:157],v228 offset:28672
TR a[158:159],v228 offset:30720
MM v[36:39],a[160:163],a[24:27],v[36:39]
s_waitcnt lgkmcnt(15)
v_rcp_f32 v141, v141
v_readlane_b32 s15, v226, 48
MM v[36:39],a[176:179],a[200:203],v[36:39]
s_max_i32 s12, s13, s14
TR a[160:161],v228 offset:32768
TR a[162:163],v228 offset:34816
MM v[36:39],a[180:183],a[216:219],v[36:39]
s_max_i32 s12, s12, s15
v_rcp_f32 v142, v142
MM v[36:39],a[184:187],a[232:235],v[36:39]
s_max_i32 s12, s12, 0
TR a[164:165],v228 offset:36864
TR a[166:167],v228 offset:38912
MM v[36:39],a[188:191],a[248:251],v[36:39]
buffer_load_dwordx2 v[148:149], v253, s[20:23], s68 offen offset:8 sc1
MM v[44:47],a[188:191],a[252:255],v[44:47]
TR a[168:169],v228 offset:40960
TR a[170:171],v228 offset:43008
MM v[44:47],a[184:187],a[236:239],v[44:47]
v_rcp_f32 v143, v143
MM v[44:47],a[180:183],a[220:223],v[44:47]
TR a[172:173],v228 offset:45056
TR a[174:175],v228 offset:47104
MM v[44:47],a[176:179],a[204:207],v[44:47]
s_waitcnt lgkmcnt(15)
CV v34,v36,v37
CV v35,v38,v39
v_min_u32 v35, -2, v35
buffer_store_dwordx4 v[32:35], v248, s[20:23], s67 offen offset:2048 sc1
MM v[8:11],a[128:131],v[172:175],v[8:11]
TR a[176:177],v228 offset:49152
TR a[178:179],v228 offset:51200
MM v[8:11],a[132:135],v[188:191],v[8:11]
v_mul_f32 v140, v136, v140
v_mul_f32 v141, v137, v141
MM v[8:11],a[136:139],v[204:207],v[8:11]
TR a[180:181],v228 offset:53248
)ASM"
R"ASM(
TR a[182:183],v228 offset:55296
MM v[8:11],a[140:143],v[220:223],v[8:11]
CV v42,v44,v45
CV v43,v46,v47
v_min_u32 v43, -2, v43
buffer_store_dwordx4 v[40:43], v248, s[20:23], s67 offen offset:3072 sc1
MM v[0:3],a[140:143],v[216:219],v[0:3]
TR a[184:185],v228 offset:57344
TR a[186:187],v228 offset:59392
MM v[0:3],a[136:139],v[200:203],v[0:3]
v_mul_f32 v142, v138, v142
v_mul_f32 v143, v139, v143
MM v[0:3],a[132:135],v[184:187],v[0:3]
TR a[188:189],v228 offset:61440
TR a[190:191],v228 offset:63488
MM v[0:3],a[128:131],v[168:171],v[0:3]
s_waitcnt lgkmcnt(15)
CV v150,v140,v141
CV v151,v142,v143
MM v[0:3],a[144:147],a[16:19],v[0:3]
TR a[128:129],v229 offset:0
TR a[130:131],v229 offset:2048
MM v[0:3],a[148:151],a[48:51],v[0:3]
s_cmp_le_i32 s12, 76
s_cselect_b32 s60, 1, 0
s_cselect_b64 s[8:9], -1, 0
MM v[0:3],a[152:155],a[80:83],v[0:3]
s_or_b32 s58, s58, s60
TR a[132:133],v229 offset:4096
TR a[134:135],v229 offset:6144
MM v[0:3],a[156:159],a[112:115],v[0:3]
s_mov_b64 exec, s[8:9]
buffer_load_dwordx4 v[56:59], v250, s[20:23], s39 offen offset:0 sc1
s_mov_b64 exec, -1
MM v[8:11],a[156:159],a[116:119],v[8:11]
TR a[136:137],v229 offset:8192
TR a[138:139],v229 offset:10240
MM v[8:11],a[152:155],a[84:87],v[8:11]
s_mov_b64 exec, s[8:9]
buffer_load_dwordx4 v[60:63], v250, s[20:23], s39 offen offset:256 sc1
s_mov_b64 exec, -1
MM v[8:11],a[148:151],a[52:55],v[8:11]
TR a[140:141],v229 offset:12288
TR a[142:143],v229 offset:14336
MM v[8:11],a[144:147],a[20:23],v[8:11]
s_waitcnt lgkmcnt(15)
v_lshlrev_b32 v160, 16, v80
v_and_b32 v161, 0xffff0000, v80
MM v[8:11],a[160:163],a[28:31],v[8:11]
TR a[144:145],v229 offset:16384
TR a[146:147],v229 offset:18432
MM v[8:11],a[164:167],a[60:63],v[8:11]
s_cmp_le_i32 s14, 51
s_cselect_b32 s60, 4, 0
s_cselect_b64 s[8:9], -1, 0
MM v[8:11],a[168:171],a[92:95],v[8:11]
s_or_b32 s58, s58, s60
TR a[148:149],v229 offset:20480
TR a[150:151],v229 offset:22528
MM v[8:11],a[172:175],a[124:127],v[8:11]
s_mov_b64 exec, s[8:9]
buffer_load_dwordx4 v[24:27], v248, s[20:23], s37 offen offset:0 sc1
s_mov_b64 exec, -1
MM v[0:3],a[172:175],a[120:123],v[0:3]
TR a[152:153],v229 offset:24576
TR a[154:155],v229 offset:26624
MM v[0:3],a[168:171],a[88:91],v[0:3]
s_mov_b64 exec, s[8:9]
buffer_load_dwordx4 v[28:31], v248, s[20:23], s37 offen offset:1024 sc1
s_mov_b64 exec, -1
MM v[0:3],a[164:167],a[56:59],v[0:3]
TR a[156:157],v229 offset:28672
TR a[158:159],v229 offset:30720
MM v[0:3],a[160:163],a[24:27],v[0:3]
s_waitcnt lgkmcnt(15)
v_lshlrev_b32 v162, 16, v81
v_and_b32 v163, 0xffff0000, v81
MM v[0:3],a[176:179],a[200:203],v[0:3]
TR a[160:161],v229 offset:32768
TR a[162:163],v229 offset:34816
MM v[0:3],a[180:183],a[216:219],v[0:3]
s_cmp_le_i32 s15, 25
s_cselect_b32 s60, 8, 0
s_cselect_b64 s[8:9], -1, 0
MM v[0:3],a[184:187],a[232:235],v[0:3]
s_or_b32 s58, s58, s60
TR a[164:165],v229 offset:36864
TR a[166:167],v229 offset:38912
MM v[0:3],a[188:191],a[248:251],v[0:3]
s_mov_b64 exec, s[8:9]
buffer_load_dwordx4 v[48:51], v248, s[20:23], s38 offen offset:0 sc1
s_mov_b64 exec, -1
MM v[8:11],a[188:191],a[252:255],v[8:11]
TR a[168:169],v229 offset:40960
TR a[170:171],v229 offset:43008
MM v[8:11],a[184:187],a[236:239],v[8:11]
s_mov_b64 exec, s[8:9]
buffer_load_dwordx4 v[52:55], v248, s[20:23], s38 offen offset:1024 sc1
s_mov_b64 exec, -1
MM v[8:11],a[180:183],a[220:223],v[8:11]
TR a[172:173],v229 offset:45056
TR a[174:175],v229 offset:47104
MM v[8:11],a[176:179],a[204:207],v[8:11]
s_waitcnt lgkmcnt(15)
v_lshlrev_b32 v164, 16, v82
v_and_b32 v165, 0xffff0000, v82
MM v[12:15],a[128:131],v[172:175],v[12:15]
TR a[176:177],v229 offset:49152
TR a[178:179],v229 offset:51200
MM v[12:15],a[132:135],v[188:191],v[12:15]
v_lshlrev_b32 v166, 16, v83
v_and_b32 v167, 0xffff0000, v83
MM v[12:15],a[136:139],v[204:207],v[12:15]
TR a[180:181],v229 offset:53248
TR a[182:183],v229 offset:55296
MM v[12:15],a[140:143],v[220:223],v[12:15]
v_mul_f32 v136, 0xbfb8aa3b, v160
v_mul_f32 v137, 0xbfb8aa3b, v161
MM v[4:7],a[140:143],v[216:219],v[4:7]
TR a[184:185],v229 offset:57344
TR a[186:187],v229 offset:59392
MM v[4:7],a[136:139],v[200:203],v[4:7]
v_mul_f32 v138, 0xbfb8aa3b, v162
v_mul_f32 v139, 0xbfb8aa3b, v163
MM v[4:7],a[132:135],v[184:187],v[4:7]
TR a[188:189],v229 offset:61440
TR a[190:191],v229 offset:63488
MM v[4:7],a[128:131],v[168:171],v[4:7]
s_waitcnt lgkmcnt(15)
v_mul_f32 v140, 0xbfb8aa3b, v164
v_mul_f32 v141, 0xbfb8aa3b, v165
MM v[4:7],a[144:147],a[16:19],v[4:7]
s_cmp_le_i32 s13, -8
s_cselect_b32 s60, 2, 0
s_cselect_b64 s[8:9], -1, 0
MM v[4:7],a[148:151],a[48:51],v[4:7]
s_or_b32 s58, s58, s60
s_mov_b64 exec, s[8:9]
buffer_load_dwordx4 v[16:19], v248, s[20:23], s36 offen offset:0 sc1
s_mov_b64 exec, -1
MM v[4:7],a[152:155],a[80:83],v[4:7]
v_mul_f32 v142, 0xbfb8aa3b, v166
v_mul_f32 v143, 0xbfb8aa3b, v167
MM v[4:7],a[156:159],a[112:115],v[4:7]
s_mov_b64 exec, s[8:9]
buffer_load_dwordx4 v[20:23], v248, s[20:23], s36 offen offset:1024 sc1
s_mov_b64 exec, -1
MM v[12:15],a[156:159],a[116:119],v[12:15]
EX v136,v136
MM v[12:15],a[152:155],a[84:87],v[12:15]
EX v137,v137
MM v[12:15],a[148:151],a[52:55],v[12:15]
EX v138,v138
MM v[12:15],a[144:147],a[20:23],v[12:15]
s_waitcnt lgkmcnt(8)
EX v139,v139
MM v[12:15],a[160:163],a[28:31],v[12:15]
EX v140,v140
MM v[12:15],a[164:167],a[60:63],v[12:15]
EX v141,v141
MM v[12:15],a[168:171],a[92:95],v[12:15]
EX v142,v142
MM v[12:15],a[172:175],a[124:127],v[12:15]
EX v143,v143
MM v[4:7],a[172:175],a[120:123],v[4:7]
v_add_f32 v136, 1.0, v136
v_add_f32 v137, 1.0, v137
MM v[4:7],a[168:171],a[88:91],v[4:7]
v_add_f32 v138, 1.0, v138
v_add_f32 v139, 1.0, v139
MM v[4:7],a[164:167],a[56:59],v[4:7]
v_add_f32 v140, 1.0, v140
v_add_f32 v141, 1.0, v141
MM v[4:7],a[160:163],a[24:27],v[4:7]
s_waitcnt lgkmcnt(0)
v_add_f32 v142, 1.0, v142
v_add_f32 v143, 1.0, v143
MM v[4:7],a[176:179],a[200:203],v[4:7]
v_rcp_f32 v136, v136
MM v[4:7],a[180:183],a[216:219],v[4:7]
v_rcp_f32 v137, v137
MM v[4:7],a[184:187],a[232:235],v[4:7]
v_rcp_f32 v138, v138
MM v[4:7],a[188:191],a[248:251],v[4:7]
v_rcp_f32 v139, v139
MM v[12:15],a[188:191],a[252:255],v[12:15]
v_rcp_f32 v140, v140
MM v[12:15],a[184:187],a[236:239],v[12:15]
v_rcp_f32 v141, v141
MM v[12:15],a[180:183],a[220:223],v[12:15]
v_rcp_f32 v142, v142
MM v[12:15],a[176:179],a[204:207],v[12:15]
v_rcp_f32 v143, v143
s_memrealtime s[54:55]
s_cmp_eq_u32 s58, 15
s_cbranch_scc1 LSK1_%=
s_memrealtime s[98:99]
s_andn2_b32 s91, s17, s58
s_mov_b32 s77, 0x7fffffff
s_waitcnt lgkmcnt(0)
s_sub_u32 s76, s98, s54
s_bitcmp0_b32 s91, 1
s_cbranch_scc1 LSM11_%=
s_sub_u32 s90, s13, s76
s_sub_u32 s90, s90, 12
s_cmp_gt_i32 s90, 0
s_cbranch_scc1 LSM11_%=
buffer_load_dwordx4 v[16:19], v248, s[20:23], s36 offen offset:0 sc1
buffer_load_dwordx4 v[20:23], v248, s[20:23], s36 offen offset:1024 sc1
s_bitset1_b32 s58, 1
LSM11_%=:
s_bitcmp0_b32 s91, 3
s_cbranch_scc1 LSM13_%=
s_sub_u32 s90, s15, s76
s_sub_u32 s90, s90, 63
s_cmp_gt_i32 s90, 0
s_cbranch_scc1 LSM13_%=
buffer_load_dwordx4 v[48:51], v248, s[20:23], s38 offen offset:0 sc1
buffer_load_dwordx4 v[52:55], v248, s[20:23], s38 offen offset:1024 sc1
s_bitset1_b32 s58, 3
LSM13_%=:
s_bitcmp0_b32 s91, 2
s_cbranch_scc1 LSM12_%=
s_sub_u32 s90, s14, s76
s_sub_u32 s90, s90, 97
s_cmp_gt_i32 s90, 0
s_cbranch_scc1 LSM12_%=
buffer_load_dwordx4 v[24:27], v248, s[20:23], s37 offen offset:0 sc1
buffer_load_dwordx4 v[28:31], v248, s[20:23], s37 offen offset:1024 sc1
s_bitset1_b32 s58, 2
LSM12_%=:
s_bitcmp0_b32 s91, 0
s_cbranch_scc1 LSM10_%=
s_sub_u32 s90, s12, s76
s_sub_u32 s90, s90, 131
s_cmp_gt_i32 s90, 0
s_cbranch_scc1 LSM10_%=
buffer_load_dwordx4 v[56:59], v250, s[20:23], s39 offen offset:0 sc1
buffer_load_dwordx4 v[60:63], v250, s[20:23], s39 offen offset:256 sc1
s_bitset1_b32 s58, 0
LSM10_%=:
LSK1_%=:
v_mul_f32 v136, v160, v136
v_mul_f32 v137, v161, v137
v_mul_f32 v138, v162, v138
v_mul_f32 v139, v163, v139
v_mul_f32 v140, v164, v140
v_mul_f32 v141, v165, v141
v_mul_f32 v142, v166, v142
v_mul_f32 v143, v167, v143
CV v80,v136,v137
CV v81,v138,v139
CV v82,v140,v141
CV v83,v142,v143
v_lshlrev_b32 v160, 16, v80
v_and_b32 v161, 0xffff0000, v80
v_lshlrev_b32 v162, 16, v81
v_and_b32 v163, 0xffff0000, v81
v_lshlrev_b32 v164, 16, v82
v_and_b32 v165, 0xffff0000, v82
v_lshlrev_b32 v166, 16, v83
v_and_b32 v167, 0xffff0000, v83
v_lshlrev_b32 v176, 16, v84
v_and_b32 v177, 0xffff0000, v84
v_lshlrev_b32 v178, 16, v85
v_and_b32 v179, 0xffff0000, v85
v_lshlrev_b32 v180, 16, v86
v_and_b32 v181, 0xffff0000, v86
v_lshlrev_b32 v182, 16, v87
v_and_b32 v183, 0xffff0000, v87
v_mul_f32 v136, 0xbfb8aa3b, v176
v_mul_f32 v137, 0xbfb8aa3b, v177
v_mul_f32 v138, 0xbfb8aa3b, v178
v_mul_f32 v139, 0xbfb8aa3b, v179
v_mul_f32 v140, 0xbfb8aa3b, v180
v_mul_f32 v141, 0xbfb8aa3b, v181
v_mul_f32 v142, 0xbfb8aa3b, v182
v_mul_f32 v143, 0xbfb8aa3b, v183
EX v136,v136
s_waitcnt vmcnt(10)
v_cmp_eq_u32 s[8:9], s69, v149
v_subrev_u32 v226, s56, v148
LPG30_%=:
s_cbranch_execz LTR1_%=
s_nop 0
v_cndmask_b32 v226, 4.0, v226, s[8:9]
s_nop 0
v_readlane_b32 s13, v226, 16
v_readlane_b32 s14, v226, 32
v_readlane_b32 s15, v226, 48
s_max_i32 s12, s13, s14
s_max_i32 s12, s12, s15
s_max_i32 s12, s12, 0
s_cmp_eq_u32 s58, 15
s_cbranch_scc1 LSK2_%=
s_memrealtime s[98:99]
s_andn2_b32 s91, s17, s58
s_mov_b32 s77, 0x7fffffff
s_waitcnt lgkmcnt(0)
s_sub_u32 s76, s98, s54
s_bitcmp0_b32 s91, 1
s_cbranch_scc1 LSM21_%=
s_sub_u32 s90, s13, s76
s_sub_u32 s90, s90, 12
s_cmp_gt_i32 s90, 0
s_cbranch_scc1 LSM21_%=
buffer_load_dwordx4 v[16:19], v248, s[20:23], s36 offen offset:0 sc1
buffer_load_dwordx4 v[20:23], v248, s[20:23], s36 offen offset:1024 sc1
s_bitset1_b32 s58, 1
LSM21_%=:
s_bitcmp0_b32 s91, 3
s_cbranch_scc1 LSM23_%=
s_sub_u32 s90, s15, s76
s_sub_u32 s90, s90, 63
s_cmp_gt_i32 s90, 0
s_cbranch_scc1 LSM23_%=
buffer_load_dwordx4 v[48:51], v248, s[20:23], s38 offen offset:0 sc1
buffer_load_dwordx4 v[52:55], v248, s[20:23], s38 offen offset:1024 sc1
s_bitset1_b32 s58, 3
LSM23_%=:
s_bitcmp0_b32 s91, 2
s_cbranch_scc1 LSM22_%=
s_sub_u32 s90, s14, s76
s_sub_u32 s90, s90, 97
s_cmp_gt_i32 s90, 0
s_cbranch_scc1 LSM22_%=
buffer_load_dwordx4 v[24:27], v248, s[20:23], s37 offen offset:0 sc1
buffer_load_dwordx4 v[28:31], v248, s[20:23], s37 offen offset:1024 sc1
s_bitset1_b32 s58, 2
LSM22_%=:
s_bitcmp0_b32 s91, 0
s_cbranch_scc1 LSM20_%=
s_sub_u32 s90, s12, s76
s_sub_u32 s90, s90, 131
s_cmp_gt_i32 s90, 0
s_cbranch_scc1 LSM20_%=
buffer_load_dwordx4 v[56:59], v250, s[20:23], s39 offen offset:0 sc1
buffer_load_dwordx4 v[60:63], v250, s[20:23], s39 offen offset:256 sc1
s_bitset1_b32 s58, 0
LSM20_%=:
LSK2_%=:
EX v137,v137
EX v138,v138
EX v139,v139
EX v140,v140
EX v141,v141
EX v142,v142
EX v143,v143
v_add_f32 v136, 1.0, v136
v_add_f32 v137, 1.0, v137
v_add_f32 v138, 1.0, v138
v_add_f32 v139, 1.0, v139
v_add_f32 v140, 1.0, v140
v_add_f32 v141, 1.0, v141
v_add_f32 v142, 1.0, v142
v_add_f32 v143, 1.0, v143
v_rcp_f32 v136, v136
v_rcp_f32 v137, v137
v_rcp_f32 v138, v138
v_rcp_f32 v139, v139
v_rcp_f32 v140, v140
v_rcp_f32 v141, v141
v_rcp_f32 v142, v142
v_rcp_f32 v143, v143
s_cmp_eq_u32 s58, 15
s_cbranch_scc1 LSK3_%=
s_memrealtime s[98:99]
s_andn2_b32 s91, s17, s58
s_mov_b32 s77, 0x7fffffff
s_waitcnt lgkmcnt(0)
s_sub_u32 s76, s98, s54
s_bitcmp0_b32 s91, 1
s_cbranch_scc1 LSM31_%=
s_sub_u32 s90, s13, s76
s_sub_u32 s90, s90, 12
s_cmp_gt_i32 s90, 0
s_cbranch_scc1 LSM31_%=
buffer_load_dwordx4 v[16:19], v248, s[20:23], s36 offen offset:0 sc1
buffer_load_dwordx4 v[20:23], v248, s[20:23], s36 offen offset:1024 sc1
s_bitset1_b32 s58, 1
LSM31_%=:
s_bitcmp0_b32 s91, 3
s_cbranch_scc1 LSM33_%=
s_sub_u32 s90, s15, s76
s_sub_u32 s90, s90, 63
s_cmp_gt_i32 s90, 0
s_cbranch_scc1 LSM33_%=
buffer_load_dwordx4 v[48:51], v248, s[20:23], s38 offen offset:0 sc1
buffer_load_dwordx4 v[52:55], v248, s[20:23], s38 offen offset:1024 sc1
s_bitset1_b32 s58, 3
LSM33_%=:
s_bitcmp0_b32 s91, 2
s_cbranch_scc1 LSM32_%=
s_sub_u32 s90, s14, s76
s_sub_u32 s90, s90, 97
s_cmp_gt_i32 s90, 0
s_cbranch_scc1 LSM32_%=
buffer_load_dwordx4 v[24:27], v248, s[20:23], s37 offen offset:0 sc1
buffer_load_dwordx4 v[28:31], v248, s[20:23], s37 offen offset:1024 sc1
s_bitset1_b32 s58, 2
LSM32_%=:
s_bitcmp0_b32 s91, 0
)ASM"
R"ASM(
s_cbranch_scc1 LSM30_%=
s_sub_u32 s90, s12, s76
s_sub_u32 s90, s90, 131
s_cmp_gt_i32 s90, 0
s_cbranch_scc1 LSM30_%=
buffer_load_dwordx4 v[56:59], v250, s[20:23], s39 offen offset:0 sc1
buffer_load_dwordx4 v[60:63], v250, s[20:23], s39 offen offset:256 sc1
s_bitset1_b32 s58, 0
LSM30_%=:
LSK3_%=:
v_mul_f32 v136, v176, v136
v_mul_f32 v137, v177, v137
v_mul_f32 v138, v178, v138
v_mul_f32 v139, v179, v139
v_mul_f32 v140, v180, v140
v_mul_f32 v141, v181, v141
v_mul_f32 v142, v182, v142
v_mul_f32 v143, v183, v143
CV v84,v136,v137
CV v85,v138,v139
CV v86,v140,v141
CV v87,v142,v143
v_lshlrev_b32 v176, 16, v84
v_and_b32 v177, 0xffff0000, v84
v_lshlrev_b32 v178, 16, v85
v_and_b32 v179, 0xffff0000, v85
v_lshlrev_b32 v180, 16, v86
v_and_b32 v181, 0xffff0000, v86
v_lshlrev_b32 v182, 16, v87
v_and_b32 v183, 0xffff0000, v87
v_lshlrev_b32 v192, 16, v88
v_and_b32 v193, 0xffff0000, v88
v_lshlrev_b32 v194, 16, v89
v_and_b32 v195, 0xffff0000, v89
v_lshlrev_b32 v196, 16, v90
v_and_b32 v197, 0xffff0000, v90
v_lshlrev_b32 v198, 16, v91
v_and_b32 v199, 0xffff0000, v91
v_lshlrev_b32 v208, 16, v92
v_and_b32 v209, 0xffff0000, v92
v_lshlrev_b32 v210, 16, v93
v_and_b32 v211, 0xffff0000, v93
v_lshlrev_b32 v212, 16, v94
v_and_b32 v213, 0xffff0000, v94
v_lshlrev_b32 v214, 16, v95
v_and_b32 v215, 0xffff0000, v95
s_mov_b32 s73, 30000
s_cmp_lg_u32 s58, 15
s_cbranch_scc1 LSI_%=
s_waitcnt vmcnt(0)
v_max3_u32 v236, v59, v63, v19
v_max3_u32 v237, v23, v27, v31
v_max3_u32 v236, v236, v51, v55
v_max_u32 v236, v236, v237
v_cmp_eq_u32 vcc, -1, v236
s_cbranch_vccnz LSI_%=
LSF_%=:
s_waitcnt lgkmcnt(0)
MM v[152:155],v[112:115],v[56:59],0
MM v[152:155],v[116:119],v[60:63],v[152:155]
MM v[0:3],v[122:125],v[24:27],v[0:3]
v_lshlrev_b32 v128, 16, v150
v_and_b32 v129, 0xffff0000, v150
MM v[0:3],v[122:125],v[48:51],v[0:3]
v_lshlrev_b32 v130, 16, v151
v_and_b32 v131, 0xffff0000, v151
MM v[0:3],v[122:125],v[16:19],v[0:3]
v_lshlrev_b32 v132, 16, v126
v_and_b32 v133, 0xffff0000, v126
MM v[4:7],v[120:123],v[24:27],v[4:7]
v_lshlrev_b32 v134, 16, v127
v_and_b32 v135, 0xffff0000, v127
MM v[4:7],v[120:123],v[48:51],v[4:7]
CV v236,v152,v153
CV v237,v154,v155
MM v[4:7],v[120:123],v[16:19],v[4:7]
ds_bpermute_b32 v236, v232, v236
ds_bpermute_b32 v237, v232, v237
MM v[8:11],v[122:125],v[28:31],v[8:11]
MM v[8:11],v[122:125],v[52:55],v[8:11]
MM v[8:11],v[122:125],v[20:23],v[8:11]
s_add_u32 s80, s18, 20480
MM v[12:15],v[120:123],v[28:31],v[12:15]
MM v[12:15],v[120:123],v[52:55],v[12:15]
CV v32,v0,v1
CV v33,v2,v3
MM v[12:15],v[120:123],v[20:23],v[12:15]
CV v34,v4,v5
CV v35,v6,v7
ds_write2_b64 v224, v[32:33], v[34:35] offset0:0 offset1:4
RD v[40:43],v225 offset:0
CV v36,v8,v9
CV v37,v10,v11
s_nop 1
CV v38,v12,v13
CV v39,v14,v15
ds_write2_b64 v224, v[36:37], v[38:39] offset0:160 offset1:164
RD v[44:47],v225 offset:1280
s_waitcnt lgkmcnt(4)
v_lshlrev_b32 v152, 16, v236
v_and_b32 v153, 0xffff0000, v236
v_lshlrev_b32 v154, 16, v237
v_and_b32 v155, 0xffff0000, v237
v_pk_mul_f32 v[152:153], v[152:153], v[128:129]
v_pk_mul_f32 v[154:155], v[154:155], v[130:131]
CV v236,v152,v153
CV v237,v154,v155
v_lshlrev_b32 v152, 16, v236
v_and_b32 v153, 0xffff0000, v236
v_lshlrev_b32 v154, 16, v237
v_and_b32 v155, 0xffff0000, v237
v_pk_add_f32 v[152:153], v[152:153], v[132:133]
v_pk_add_f32 v[154:155], v[154:155], v[134:135]
CV v238,v152,v153
CV v239,v154,v155
buffer_store_dwordx2 v[238:239], v254, s[40:43], s63 offen offset:0 nt
s_nop 0
s_waitcnt lgkmcnt(2)
v_lshlrev_b32 v0, 16, v40
v_and_b32 v1, 0xffff0000, v40
v_lshlrev_b32 v2, 16, v41
v_and_b32 v3, 0xffff0000, v41
v_lshlrev_b32 v4, 16, v42
v_and_b32 v5, 0xffff0000, v42
v_lshlrev_b32 v6, 16, v43
v_and_b32 v7, 0xffff0000, v43
v_pk_mul_f32 v[0:1], v[0:1], v[160:161]
v_pk_mul_f32 v[2:3], v[2:3], v[162:163]
v_pk_mul_f32 v[4:5], v[4:5], v[164:165]
v_pk_mul_f32 v[6:7], v[6:7], v[166:167]
CV v40,v0,v1
CV v41,v2,v3
CV v42,v4,v5
CV v43,v6,v7
v_lshlrev_b32 v0, 16, v40
v_and_b32 v1, 0xffff0000, v40
v_lshlrev_b32 v2, 16, v41
v_and_b32 v3, 0xffff0000, v41
v_lshlrev_b32 v4, 16, v42
v_and_b32 v5, 0xffff0000, v42
v_lshlrev_b32 v6, 16, v43
v_and_b32 v7, 0xffff0000, v43
v_pk_add_f32 v[0:1], v[0:1], v[192:193]
v_pk_add_f32 v[2:3], v[2:3], v[194:195]
v_pk_add_f32 v[4:5], v[4:5], v[196:197]
v_pk_add_f32 v[6:7], v[6:7], v[198:199]
CV v40,v0,v1
CV v41,v2,v3
CV v42,v4,v5
CV v43,v6,v7
buffer_store_dwordx4 v[40:43], v251, s[40:43], s18 offen offset:0 nt
s_waitcnt lgkmcnt(0)
v_lshlrev_b32 v8, 16, v44
v_and_b32 v9, 0xffff0000, v44
v_lshlrev_b32 v10, 16, v45
v_and_b32 v11, 0xffff0000, v45
v_lshlrev_b32 v12, 16, v46
v_and_b32 v13, 0xffff0000, v46
v_lshlrev_b32 v14, 16, v47
v_and_b32 v15, 0xffff0000, v47
v_pk_mul_f32 v[8:9], v[8:9], v[176:177]
v_pk_mul_f32 v[10:11], v[10:11], v[178:179]
v_pk_mul_f32 v[12:13], v[12:13], v[180:181]
v_pk_mul_f32 v[14:15], v[14:15], v[182:183]
CV v44,v8,v9
CV v45,v10,v11
CV v46,v12,v13
CV v47,v14,v15
v_lshlrev_b32 v8, 16, v44
v_and_b32 v9, 0xffff0000, v44
v_lshlrev_b32 v10, 16, v45
v_and_b32 v11, 0xffff0000, v45
v_lshlrev_b32 v12, 16, v46
v_and_b32 v13, 0xffff0000, v46
v_lshlrev_b32 v14, 16, v47
v_and_b32 v15, 0xffff0000, v47
v_pk_add_f32 v[8:9], v[8:9], v[208:209]
v_pk_add_f32 v[10:11], v[10:11], v[210:211]
v_pk_add_f32 v[12:13], v[12:13], v[212:213]
v_pk_add_f32 v[14:15], v[14:15], v[214:215]
CV v44,v8,v9
CV v45,v10,v11
CV v46,v12,v13
CV v47,v14,v15
buffer_store_dwordx4 v[44:47], v251, s[40:43], s80 offen offset:0 nt
s_waitcnt vmcnt(0)
s_endpgm
LSI_%=:
s_memrealtime s[98:99]
s_andn2_b32 s91, s17, s58
s_mov_b32 s77, 0x7fffffff
s_waitcnt lgkmcnt(0)
s_sub_u32 s76, s98, s54
s_bitcmp0_b32 s91, 1
s_cbranch_scc1 LSM1_%=
s_sub_u32 s90, s13, s76
s_sub_u32 s90, s90, 12
s_cmp_gt_i32 s90, 0
s_cbranch_scc1 LSN1_%=
buffer_load_dwordx4 v[16:19], v248, s[20:23], s36 offen offset:0 sc1
buffer_load_dwordx4 v[20:23], v248, s[20:23], s36 offen offset:1024 sc1
s_bitset1_b32 s58, 1
s_branch LSM1_%=
LSN1_%=:
s_min_i32 s77, s77, s90
LSM1_%=:
s_bitcmp0_b32 s91, 3
s_cbranch_scc1 LSM3_%=
s_sub_u32 s90, s15, s76
s_sub_u32 s90, s90, 63
s_cmp_gt_i32 s90, 0
s_cbranch_scc1 LSN3_%=
buffer_load_dwordx4 v[48:51], v248, s[20:23], s38 offen offset:0 sc1
buffer_load_dwordx4 v[52:55], v248, s[20:23], s38 offen offset:1024 sc1
s_bitset1_b32 s58, 3
s_branch LSM3_%=
LSN3_%=:
s_min_i32 s77, s77, s90
LSM3_%=:
s_bitcmp0_b32 s91, 2
s_cbranch_scc1 LSM2_%=
s_sub_u32 s90, s14, s76
s_sub_u32 s90, s90, 97
s_cmp_gt_i32 s90, 0
s_cbranch_scc1 LSN2_%=
buffer_load_dwordx4 v[24:27], v248, s[20:23], s37 offen offset:0 sc1
buffer_load_dwordx4 v[28:31], v248, s[20:23], s37 offen offset:1024 sc1
s_bitset1_b32 s58, 2
s_branch LSM2_%=
LSN2_%=:
s_min_i32 s77, s77, s90
LSM2_%=:
s_bitcmp0_b32 s91, 0
s_cbranch_scc1 LSM0_%=
s_sub_u32 s90, s12, s76
s_sub_u32 s90, s90, 131
s_cmp_gt_i32 s90, 0
s_cbranch_scc1 LSN0_%=
buffer_load_dwordx4 v[56:59], v250, s[20:23], s39 offen offset:0 sc1
buffer_load_dwordx4 v[60:63], v250, s[20:23], s39 offen offset:256 sc1
s_bitset1_b32 s58, 0
s_branch LSM0_%=
LSN0_%=:
s_min_i32 s77, s77, s90
LSM0_%=:
s_cmp_lt_i32 s77, 100
s_cbranch_scc1 LSI_%=
buffer_load_dwordx2 v[148:149], v253, s[20:23], s68 offen offset:8 sc1
s_waitcnt vmcnt(1)
s_bitcmp0_b32 s58, 1
s_cbranch_scc1 LSB1_%=
v_max_u32 v237, v19, v23
v_cmp_eq_u32 vcc, -1, v237
s_cbranch_vccnz LSB1_%=
s_bitset0_b32 s17, 1
LSB1_%=:
s_bitcmp0_b32 s58, 3
s_cbranch_scc1 LSB3_%=
v_max_u32 v239, v51, v55
v_cmp_eq_u32 vcc, -1, v239
s_cbranch_vccnz LSB3_%=
s_bitset0_b32 s17, 3
LSB3_%=:
s_bitcmp0_b32 s58, 2
s_cbranch_scc1 LSB2_%=
v_max_u32 v238, v27, v31
v_cmp_eq_u32 vcc, -1, v238
s_cbranch_vccnz LSB2_%=
s_bitset0_b32 s17, 2
LSB2_%=:
s_bitcmp0_b32 s58, 0
s_cbranch_scc1 LSB0_%=
v_max_u32 v236, v59, v63
v_cmp_eq_u32 vcc, -1, v236
s_cbranch_vccnz LSB0_%=
s_bitset0_b32 s17, 0
LSB0_%=:
s_cmp_eq_u32 s17, 0
s_cbranch_scc1 LSF_%=
s_mov_b32 s58, 0
s_waitcnt vmcnt(0)
v_cmp_eq_u32 s[8:9], s69, v149
v_subrev_u32 v226, s56, v148
s_nop 0
v_cndmask_b32 v226, 4.0, v226, s[8:9]
s_nop 0
v_readlane_b32 s13, v226, 16
v_readlane_b32 s14, v226, 32
v_readlane_b32 s15, v226, 48
s_max_i32 s12, s13, s14
s_max_i32 s12, s12, s15
s_max_i32 s12, s12, 0
s_sub_u32 s73, s73, 1
s_cbranch_scc0 LSI_%=
LSE_%=:
s_waitcnt vmcnt(0)
s_endpgm
LRG1o_%=:
s_mov_b32 s70, 30000
LRG1ol_%=:
s_sleep 2
buffer_load_dwordx4 v[200:203], v208, s[20:23], s62 offen offset:0 sc1
buffer_load_dwordx4 v[204:207], v208, s[20:23], s93 offen offset:0 sc1
s_waitcnt vmcnt(0)
v_xor_b32 v201, s69, v201
v_xor_b32 v203, s69, v203
v_xor_b32 v205, s69, v205
v_xor_b32 v207, s69, v207
v_or3_b32 v209, v201, v203, v205
v_or_b32 v209, v209, v207
v_cmp_ne_u32 vcc, 0, v209
s_cbranch_vccz LRG1ob_%=
s_sub_u32 s70, s70, 1
s_cbranch_scc0 LRG1ol_%=
s_branch LRG1ob_%=
LRFR_%=:
s_mov_b32 s91, 30000
LRFRl_%=:
s_sleep 2
buffer_load_dwordx4 v[112:115], v248, s[20:23], s90 offen offset:0 sc1
s_waitcnt vmcnt(0)
v_xor_b32 v112, s69, v112
v_xor_b32 v113, s69, v113
v_xor_b32 v114, s69, v114
v_xor_b32 v115, s69, v115
v_or3_b32 v252, v112, v113, v114
v_or_b32 v252, v252, v115
v_cmp_ne_u32 vcc, 0, v252
s_cbranch_vccz LRFRb_%=
s_sub_u32 s91, s91, 1
s_cbranch_scc0 LRFRl_%=
s_branch LRFRb_%=
LRRK_%=:
s_mov_b32 s91, 30000
LRRKl_%=:
s_sleep 2
buffer_load_dwordx2 v[32:33], v34, s[20:23], s90 offen offset:0 sc1
s_waitcnt vmcnt(0)
v_cmp_ne_u32 vcc, s69, v33
s_cbranch_vccz LRRKb_%=
s_sub_u32 s91, s91, 1
s_cbranch_scc0 LRRKl_%=
s_branch LRRKb_%=
LRK_%=:
s_cmp_ge_u32 s60, 8
s_cbranch_scc1 LRK0_%=
v_mov_b32 v253, 0
v_cmp_lt_u32 vcc, s52, v96
v_cmp_lt_u32 s[98:99], s52, v97
s_nop 0
v_addc_co_u32 v253, vcc, 0, v253, vcc
v_addc_co_u32 v253, s[98:99], 0, v253, s[98:99]
v_cmp_lt_u32 vcc, s52, v98
v_cmp_lt_u32 s[98:99], s52, v99
s_nop 0
v_addc_co_u32 v253, vcc, 0, v253, vcc
v_addc_co_u32 v253, s[98:99], 0, v253, s[98:99]
v_cmp_lt_u32 vcc, s52, v100
v_cmp_lt_u32 s[98:99], s52, v101
s_nop 0
v_addc_co_u32 v253, vcc, 0, v253, vcc
v_addc_co_u32 v253, s[98:99], 0, v253, s[98:99]
v_cmp_lt_u32 vcc, s52, v102
v_cmp_lt_u32 s[98:99], s52, v103
s_nop 0
v_addc_co_u32 v253, vcc, 0, v253, vcc
v_addc_co_u32 v253, s[98:99], 0, v253, s[98:99]
v_cmp_lt_u32 vcc, s52, v104
v_cmp_lt_u32 s[98:99], s52, v105
s_nop 0
v_addc_co_u32 v253, vcc, 0, v253, vcc
v_addc_co_u32 v253, s[98:99], 0, v253, s[98:99]
v_cmp_lt_u32 vcc, s52, v106
v_cmp_lt_u32 s[98:99], s52, v107
s_nop 0
v_addc_co_u32 v253, vcc, 0, v253, vcc
v_addc_co_u32 v253, s[98:99], 0, v253, s[98:99]
v_cmp_lt_u32 vcc, s52, v108
v_cmp_lt_u32 s[98:99], s52, v109
s_nop 0
v_addc_co_u32 v253, vcc, 0, v253, vcc
v_addc_co_u32 v253, s[98:99], 0, v253, s[98:99]
v_cmp_lt_u32 vcc, s52, v110
v_cmp_lt_u32 s[98:99], s52, v111
s_nop 0
v_addc_co_u32 v253, vcc, 0, v253, vcc
v_addc_co_u32 v253, s[98:99], 0, v253, s[98:99]
v_cmp_lt_u32 vcc, s52, v112
v_cmp_lt_u32 s[98:99], s52, v113
s_nop 0
v_addc_co_u32 v253, vcc, 0, v253, vcc
v_addc_co_u32 v253, s[98:99], 0, v253, s[98:99]
v_cmp_lt_u32 vcc, s52, v114
v_cmp_lt_u32 s[98:99], s52, v115
s_nop 0
v_addc_co_u32 v253, vcc, 0, v253, vcc
v_addc_co_u32 v253, s[98:99], 0, v253, s[98:99]
v_cmp_lt_u32 vcc, s52, v116
v_cmp_lt_u32 s[98:99], s52, v117
s_nop 0
v_addc_co_u32 v253, vcc, 0, v253, vcc
v_addc_co_u32 v253, s[98:99], 0, v253, s[98:99]
v_cmp_lt_u32 vcc, s52, v118
v_cmp_lt_u32 s[98:99], s52, v119
s_nop 0
v_addc_co_u32 v253, vcc, 0, v253, vcc
v_addc_co_u32 v253, s[98:99], 0, v253, s[98:99]
v_cmp_lt_u32 vcc, s52, v120
v_cmp_lt_u32 s[98:99], s52, v121
s_nop 0
v_addc_co_u32 v253, vcc, 0, v253, vcc
v_addc_co_u32 v253, s[98:99], 0, v253, s[98:99]
v_cmp_lt_u32 vcc, s52, v122
v_cmp_lt_u32 s[98:99], s52, v123
s_nop 0
v_addc_co_u32 v253, vcc, 0, v253, vcc
v_addc_co_u32 v253, s[98:99], 0, v253, s[98:99]
v_cmp_lt_u32 vcc, s52, v124
v_cmp_lt_u32 s[98:99], s52, v125
s_nop 0
v_addc_co_u32 v253, vcc, 0, v253, vcc
v_addc_co_u32 v253, s[98:99], 0, v253, s[98:99]
v_cmp_lt_u32 vcc, s52, v126
v_cmp_lt_u32 s[98:99], s52, v127
s_nop 0
v_addc_co_u32 v253, vcc, 0, v253, vcc
v_addc_co_u32 v253, s[98:99], 0, v253, s[98:99]
s_nop 1
v_add_u32_dpp v253, v253, v253 row_ror:8 row_mask:0xf bank_mask:0xf
s_nop 1
v_add_u32_dpp v253, v253, v253 row_ror:4 row_mask:0xf bank_mask:0xf
s_nop 1
v_add_u32_dpp v253, v253, v253 row_ror:2 row_mask:0xf bank_mask:0xf
)ASM"
R"ASM(
s_nop 1
v_add_u32_dpp v253, v253, v253 row_ror:1 row_mask:0xf bank_mask:0xf
v_mov_b32 v250, v253
s_nop 1
v_permlane16_swap_b32 v253, v250
v_add_u32 v253, v253, v250
v_mov_b32 v250, v253
s_nop 1
v_permlane32_swap_b32 v253, v250
v_add_u32 v253, v253, v250
v_cmp_gt_u32 vcc, 8, v253
v_mov_b32 v250, s52
v_mov_b32 v251, s69
v_lshlrev_b32 v253, 3, v253
s_mov_b32 s76, 94208
s_cbranch_vccz LRK0_%=
s_mov_b64 exec, 1
buffer_store_dwordx2 v[250:251], v253, s[20:23], s76 offen offset:0 sc1
s_mov_b64 exec, -1
s_nop 1
LRK0_%=:
s_cmp_ge_u32 s62, 8
s_cbranch_scc1 LRK1_%=
v_mov_b32 v253, 0
v_cmp_lt_u32 vcc, s53, v96
v_cmp_lt_u32 s[98:99], s53, v97
s_nop 0
v_addc_co_u32 v253, vcc, 0, v253, vcc
v_addc_co_u32 v253, s[98:99], 0, v253, s[98:99]
v_cmp_lt_u32 vcc, s53, v98
v_cmp_lt_u32 s[98:99], s53, v99
s_nop 0
v_addc_co_u32 v253, vcc, 0, v253, vcc
v_addc_co_u32 v253, s[98:99], 0, v253, s[98:99]
v_cmp_lt_u32 vcc, s53, v100
v_cmp_lt_u32 s[98:99], s53, v101
s_nop 0
v_addc_co_u32 v253, vcc, 0, v253, vcc
v_addc_co_u32 v253, s[98:99], 0, v253, s[98:99]
v_cmp_lt_u32 vcc, s53, v102
v_cmp_lt_u32 s[98:99], s53, v103
s_nop 0
v_addc_co_u32 v253, vcc, 0, v253, vcc
v_addc_co_u32 v253, s[98:99], 0, v253, s[98:99]
v_cmp_lt_u32 vcc, s53, v104
v_cmp_lt_u32 s[98:99], s53, v105
s_nop 0
v_addc_co_u32 v253, vcc, 0, v253, vcc
v_addc_co_u32 v253, s[98:99], 0, v253, s[98:99]
v_cmp_lt_u32 vcc, s53, v106
v_cmp_lt_u32 s[98:99], s53, v107
s_nop 0
v_addc_co_u32 v253, vcc, 0, v253, vcc
v_addc_co_u32 v253, s[98:99], 0, v253, s[98:99]
v_cmp_lt_u32 vcc, s53, v108
v_cmp_lt_u32 s[98:99], s53, v109
s_nop 0
v_addc_co_u32 v253, vcc, 0, v253, vcc
v_addc_co_u32 v253, s[98:99], 0, v253, s[98:99]
v_cmp_lt_u32 vcc, s53, v110
v_cmp_lt_u32 s[98:99], s53, v111
s_nop 0
v_addc_co_u32 v253, vcc, 0, v253, vcc
v_addc_co_u32 v253, s[98:99], 0, v253, s[98:99]
v_cmp_lt_u32 vcc, s53, v112
v_cmp_lt_u32 s[98:99], s53, v113
s_nop 0
v_addc_co_u32 v253, vcc, 0, v253, vcc
v_addc_co_u32 v253, s[98:99], 0, v253, s[98:99]
v_cmp_lt_u32 vcc, s53, v114
v_cmp_lt_u32 s[98:99], s53, v115
s_nop 0
v_addc_co_u32 v253, vcc, 0, v253, vcc
v_addc_co_u32 v253, s[98:99], 0, v253, s[98:99]
v_cmp_lt_u32 vcc, s53, v116
v_cmp_lt_u32 s[98:99], s53, v117
s_nop 0
v_addc_co_u32 v253, vcc, 0, v253, vcc
v_addc_co_u32 v253, s[98:99], 0, v253, s[98:99]
v_cmp_lt_u32 vcc, s53, v118
v_cmp_lt_u32 s[98:99], s53, v119
s_nop 0
v_addc_co_u32 v253, vcc, 0, v253, vcc
v_addc_co_u32 v253, s[98:99], 0, v253, s[98:99]
v_cmp_lt_u32 vcc, s53, v120
v_cmp_lt_u32 s[98:99], s53, v121
s_nop 0
v_addc_co_u32 v253, vcc, 0, v253, vcc
v_addc_co_u32 v253, s[98:99], 0, v253, s[98:99]
v_cmp_lt_u32 vcc, s53, v122
v_cmp_lt_u32 s[98:99], s53, v123
s_nop 0
v_addc_co_u32 v253, vcc, 0, v253, vcc
v_addc_co_u32 v253, s[98:99], 0, v253, s[98:99]
v_cmp_lt_u32 vcc, s53, v124
v_cmp_lt_u32 s[98:99], s53, v125
s_nop 0
v_addc_co_u32 v253, vcc, 0, v253, vcc
v_addc_co_u32 v253, s[98:99], 0, v253, s[98:99]
v_cmp_lt_u32 vcc, s53, v126
v_cmp_lt_u32 s[98:99], s53, v127
s_nop 0
v_addc_co_u32 v253, vcc, 0, v253, vcc
v_addc_co_u32 v253, s[98:99], 0, v253, s[98:99]
s_nop 1
v_add_u32_dpp v253, v253, v253 row_ror:8 row_mask:0xf bank_mask:0xf
s_nop 1
v_add_u32_dpp v253, v253, v253 row_ror:4 row_mask:0xf bank_mask:0xf
s_nop 1
v_add_u32_dpp v253, v253, v253 row_ror:2 row_mask:0xf bank_mask:0xf
s_nop 1
v_add_u32_dpp v253, v253, v253 row_ror:1 row_mask:0xf bank_mask:0xf
v_mov_b32 v250, v253
s_nop 1
v_permlane16_swap_b32 v253, v250
v_add_u32 v253, v253, v250
v_mov_b32 v250, v253
s_nop 1
v_permlane32_swap_b32 v253, v250
v_add_u32 v253, v253, v250
v_cmp_gt_u32 vcc, 8, v253
v_mov_b32 v250, s53
v_mov_b32 v251, s69
v_lshlrev_b32 v253, 3, v253
s_mov_b32 s76, 94208
s_cbranch_vccz LRK1_%=
s_mov_b64 exec, 1
buffer_store_dwordx2 v[250:251], v253, s[20:23], s76 offen offset:0 sc1
s_mov_b64 exec, -1
s_nop 1
LRK1_%=:
s_cmp_ge_u32 s56, 8
s_cbranch_scc1 LRK2_%=
v_mov_b32 v253, 0
v_cmp_lt_u32 vcc, s54, v96
v_cmp_lt_u32 s[98:99], s54, v97
s_nop 0
v_addc_co_u32 v253, vcc, 0, v253, vcc
v_addc_co_u32 v253, s[98:99], 0, v253, s[98:99]
v_cmp_lt_u32 vcc, s54, v98
v_cmp_lt_u32 s[98:99], s54, v99
s_nop 0
v_addc_co_u32 v253, vcc, 0, v253, vcc
v_addc_co_u32 v253, s[98:99], 0, v253, s[98:99]
v_cmp_lt_u32 vcc, s54, v100
v_cmp_lt_u32 s[98:99], s54, v101
s_nop 0
v_addc_co_u32 v253, vcc, 0, v253, vcc
v_addc_co_u32 v253, s[98:99], 0, v253, s[98:99]
v_cmp_lt_u32 vcc, s54, v102
v_cmp_lt_u32 s[98:99], s54, v103
s_nop 0
v_addc_co_u32 v253, vcc, 0, v253, vcc
v_addc_co_u32 v253, s[98:99], 0, v253, s[98:99]
v_cmp_lt_u32 vcc, s54, v104
v_cmp_lt_u32 s[98:99], s54, v105
s_nop 0
v_addc_co_u32 v253, vcc, 0, v253, vcc
v_addc_co_u32 v253, s[98:99], 0, v253, s[98:99]
v_cmp_lt_u32 vcc, s54, v106
v_cmp_lt_u32 s[98:99], s54, v107
s_nop 0
v_addc_co_u32 v253, vcc, 0, v253, vcc
v_addc_co_u32 v253, s[98:99], 0, v253, s[98:99]
v_cmp_lt_u32 vcc, s54, v108
v_cmp_lt_u32 s[98:99], s54, v109
s_nop 0
v_addc_co_u32 v253, vcc, 0, v253, vcc
v_addc_co_u32 v253, s[98:99], 0, v253, s[98:99]
v_cmp_lt_u32 vcc, s54, v110
v_cmp_lt_u32 s[98:99], s54, v111
s_nop 0
v_addc_co_u32 v253, vcc, 0, v253, vcc
v_addc_co_u32 v253, s[98:99], 0, v253, s[98:99]
v_cmp_lt_u32 vcc, s54, v112
v_cmp_lt_u32 s[98:99], s54, v113
s_nop 0
v_addc_co_u32 v253, vcc, 0, v253, vcc
v_addc_co_u32 v253, s[98:99], 0, v253, s[98:99]
v_cmp_lt_u32 vcc, s54, v114
v_cmp_lt_u32 s[98:99], s54, v115
s_nop 0
v_addc_co_u32 v253, vcc, 0, v253, vcc
v_addc_co_u32 v253, s[98:99], 0, v253, s[98:99]
v_cmp_lt_u32 vcc, s54, v116
v_cmp_lt_u32 s[98:99], s54, v117
s_nop 0
v_addc_co_u32 v253, vcc, 0, v253, vcc
v_addc_co_u32 v253, s[98:99], 0, v253, s[98:99]
v_cmp_lt_u32 vcc, s54, v118
v_cmp_lt_u32 s[98:99], s54, v119
s_nop 0
v_addc_co_u32 v253, vcc, 0, v253, vcc
v_addc_co_u32 v253, s[98:99], 0, v253, s[98:99]
v_cmp_lt_u32 vcc, s54, v120
v_cmp_lt_u32 s[98:99], s54, v121
s_nop 0
v_addc_co_u32 v253, vcc, 0, v253, vcc
v_addc_co_u32 v253, s[98:99], 0, v253, s[98:99]
v_cmp_lt_u32 vcc, s54, v122
v_cmp_lt_u32 s[98:99], s54, v123
s_nop 0
v_addc_co_u32 v253, vcc, 0, v253, vcc
v_addc_co_u32 v253, s[98:99], 0, v253, s[98:99]
v_cmp_lt_u32 vcc, s54, v124
v_cmp_lt_u32 s[98:99], s54, v125
s_nop 0
v_addc_co_u32 v253, vcc, 0, v253, vcc
v_addc_co_u32 v253, s[98:99], 0, v253, s[98:99]
v_cmp_lt_u32 vcc, s54, v126
v_cmp_lt_u32 s[98:99], s54, v127
s_nop 0
v_addc_co_u32 v253, vcc, 0, v253, vcc
v_addc_co_u32 v253, s[98:99], 0, v253, s[98:99]
s_nop 1
v_add_u32_dpp v253, v253, v253 row_ror:8 row_mask:0xf bank_mask:0xf
s_nop 1
v_add_u32_dpp v253, v253, v253 row_ror:4 row_mask:0xf bank_mask:0xf
s_nop 1
v_add_u32_dpp v253, v253, v253 row_ror:2 row_mask:0xf bank_mask:0xf
s_nop 1
v_add_u32_dpp v253, v253, v253 row_ror:1 row_mask:0xf bank_mask:0xf
v_mov_b32 v250, v253
s_nop 1
v_permlane16_swap_b32 v253, v250
v_add_u32 v253, v253, v250
v_mov_b32 v250, v253
s_nop 1
v_permlane32_swap_b32 v253, v250
v_add_u32 v253, v253, v250
v_cmp_gt_u32 vcc, 8, v253
v_mov_b32 v250, s54
v_mov_b32 v251, s69
v_lshlrev_b32 v253, 3, v253
s_mov_b32 s76, 94208
s_cbranch_vccz LRK2_%=
s_mov_b64 exec, 1
buffer_store_dwordx2 v[250:251], v253, s[20:23], s76 offen offset:0 sc1
s_mov_b64 exec, -1
s_nop 1
LRK2_%=:
s_cmp_ge_u32 s58, 8
s_cbranch_scc1 LRK3_%=
v_mov_b32 v253, 0
v_cmp_lt_u32 vcc, s55, v96
v_cmp_lt_u32 s[98:99], s55, v97
s_nop 0
v_addc_co_u32 v253, vcc, 0, v253, vcc
v_addc_co_u32 v253, s[98:99], 0, v253, s[98:99]
v_cmp_lt_u32 vcc, s55, v98
v_cmp_lt_u32 s[98:99], s55, v99
s_nop 0
v_addc_co_u32 v253, vcc, 0, v253, vcc
v_addc_co_u32 v253, s[98:99], 0, v253, s[98:99]
v_cmp_lt_u32 vcc, s55, v100
v_cmp_lt_u32 s[98:99], s55, v101
s_nop 0
v_addc_co_u32 v253, vcc, 0, v253, vcc
v_addc_co_u32 v253, s[98:99], 0, v253, s[98:99]
v_cmp_lt_u32 vcc, s55, v102
v_cmp_lt_u32 s[98:99], s55, v103
s_nop 0
v_addc_co_u32 v253, vcc, 0, v253, vcc
v_addc_co_u32 v253, s[98:99], 0, v253, s[98:99]
v_cmp_lt_u32 vcc, s55, v104
v_cmp_lt_u32 s[98:99], s55, v105
s_nop 0
v_addc_co_u32 v253, vcc, 0, v253, vcc
v_addc_co_u32 v253, s[98:99], 0, v253, s[98:99]
v_cmp_lt_u32 vcc, s55, v106
v_cmp_lt_u32 s[98:99], s55, v107
s_nop 0
v_addc_co_u32 v253, vcc, 0, v253, vcc
v_addc_co_u32 v253, s[98:99], 0, v253, s[98:99]
v_cmp_lt_u32 vcc, s55, v108
v_cmp_lt_u32 s[98:99], s55, v109
s_nop 0
v_addc_co_u32 v253, vcc, 0, v253, vcc
v_addc_co_u32 v253, s[98:99], 0, v253, s[98:99]
v_cmp_lt_u32 vcc, s55, v110
v_cmp_lt_u32 s[98:99], s55, v111
s_nop 0
v_addc_co_u32 v253, vcc, 0, v253, vcc
v_addc_co_u32 v253, s[98:99], 0, v253, s[98:99]
v_cmp_lt_u32 vcc, s55, v112
v_cmp_lt_u32 s[98:99], s55, v113
s_nop 0
v_addc_co_u32 v253, vcc, 0, v253, vcc
v_addc_co_u32 v253, s[98:99], 0, v253, s[98:99]
v_cmp_lt_u32 vcc, s55, v114
v_cmp_lt_u32 s[98:99], s55, v115
s_nop 0
v_addc_co_u32 v253, vcc, 0, v253, vcc
v_addc_co_u32 v253, s[98:99], 0, v253, s[98:99]
v_cmp_lt_u32 vcc, s55, v116
v_cmp_lt_u32 s[98:99], s55, v117
s_nop 0
v_addc_co_u32 v253, vcc, 0, v253, vcc
v_addc_co_u32 v253, s[98:99], 0, v253, s[98:99]
v_cmp_lt_u32 vcc, s55, v118
v_cmp_lt_u32 s[98:99], s55, v119
s_nop 0
v_addc_co_u32 v253, vcc, 0, v253, vcc
v_addc_co_u32 v253, s[98:99], 0, v253, s[98:99]
v_cmp_lt_u32 vcc, s55, v120
v_cmp_lt_u32 s[98:99], s55, v121
s_nop 0
v_addc_co_u32 v253, vcc, 0, v253, vcc
v_addc_co_u32 v253, s[98:99], 0, v253, s[98:99]
v_cmp_lt_u32 vcc, s55, v122
v_cmp_lt_u32 s[98:99], s55, v123
s_nop 0
v_addc_co_u32 v253, vcc, 0, v253, vcc
v_addc_co_u32 v253, s[98:99], 0, v253, s[98:99]
v_cmp_lt_u32 vcc, s55, v124
v_cmp_lt_u32 s[98:99], s55, v125
s_nop 0
v_addc_co_u32 v253, vcc, 0, v253, vcc
v_addc_co_u32 v253, s[98:99], 0, v253, s[98:99]
v_cmp_lt_u32 vcc, s55, v126
v_cmp_lt_u32 s[98:99], s55, v127
s_nop 0
v_addc_co_u32 v253, vcc, 0, v253, vcc
v_addc_co_u32 v253, s[98:99], 0, v253, s[98:99]
s_nop 1
v_add_u32_dpp v253, v253, v253 row_ror:8 row_mask:0xf bank_mask:0xf
s_nop 1
v_add_u32_dpp v253, v253, v253 row_ror:4 row_mask:0xf bank_mask:0xf
s_nop 1
v_add_u32_dpp v253, v253, v253 row_ror:2 row_mask:0xf bank_mask:0xf
s_nop 1
v_add_u32_dpp v253, v253, v253 row_ror:1 row_mask:0xf bank_mask:0xf
v_mov_b32 v250, v253
s_nop 1
v_permlane16_swap_b32 v253, v250
v_add_u32 v253, v253, v250
v_mov_b32 v250, v253
s_nop 1
v_permlane32_swap_b32 v253, v250
v_add_u32 v253, v253, v250
v_cmp_gt_u32 vcc, 8, v253
v_mov_b32 v250, s55
v_mov_b32 v251, s69
v_lshlrev_b32 v253, 3, v253
s_mov_b32 s76, 94208
s_cbranch_vccz LRK3_%=
s_mov_b64 exec, 1
buffer_store_dwordx2 v[250:251], v253, s[20:23], s76 offen offset:0 sc1
s_mov_b64 exec, -1
s_nop 1
LRK3_%=:
s_branch LRKb_%=
LRIX_%=:
s_mov_b32 s91, 30000
LRIXl_%=:
s_sleep 2
v_bfe_u32 v251, v248, 4, 3
v_lshlrev_b32 v251, 3, v251
buffer_load_dwordx2 v[252:253], v251, s[20:23], s86 offen offset:0 sc1
s_waitcnt vmcnt(0)
v_cmp_ne_u32 vcc, s69, v253
s_cbranch_vccz LRIXb_%=
s_sub_u32 s91, s91, 1
s_cbranch_scc0 LRIXl_%=
s_branch LRIXb_%=
LRFG_%=:
s_mov_b32 s91, 30000
LRFGl_%=:
s_sleep 2
buffer_load_dword v252, v253, s[20:23], s76 offen offset:0 sc1
s_waitcnt vmcnt(0)
v_cmp_ne_u32 vcc, s69, v252
s_cbranch_vccz LRFGb_%=
s_sub_u32 s91, s91, 1
s_cbranch_scc0 LRFGl_%=
s_branch LRFGb_%=
LRFH_%=:
s_mov_b32 s91, 30000
LRFHl_%=:
s_sleep 2
buffer_load_dword v252, v253, s[20:23], s76 offen offset:0 sc1
s_waitcnt vmcnt(0)
v_cmp_ne_u32 vcc, s69, v252
s_cbranch_vccz LRFHb_%=
s_sub_u32 s91, s91, 1
s_cbranch_scc0 LRFHl_%=
s_branch LRFHb_%=
LRFY_%=:
s_mov_b32 s91, 30000
LRFYl_%=:
s_sleep 2
buffer_load_dword v252, v253, s[20:23], s76 offen offset:0 sc1
s_waitcnt vmcnt(0)
v_cmp_ne_u32 vcc, s69, v252
s_cbranch_vccz LRFYb_%=
s_sub_u32 s91, s91, 1
s_cbranch_scc0 LRFYl_%=
s_branch LRFYb_%=
LRPO0_%=:
s_mov_b32 s97, 30000
LRPO0l_%=:
s_sleep 2
buffer_load_dword v242, v243, s[20:23], s44 offen offset:0 sc1
s_waitcnt vmcnt(0)
v_cmp_ne_u32 vcc, s69, v242
s_cbranch_vccz LRPO0b_%=
s_sub_u32 s97, s97, 1
s_cbranch_scc0 LRPO0l_%=
s_branch LRPO0b_%=
LRPW_%=:
s_mov_b32 s61, 30000
LRPWl_%=:
s_sleep 2
buffer_load_dwordx2 v[96:97], v98, s[20:23], s62 offen offset:0 sc1
s_waitcnt vmcnt(0)
v_xor_b32 v96, s69, v96
v_xor_b32 v97, s69, v97
v_or_b32 v99, v96, v97
v_cmp_ne_u32 vcc, 0, v99
s_cbranch_vccz LRPWb_%=
s_sub_u32 s61, s61, 1
s_cbranch_scc0 LRPWl_%=
s_branch LRPWb_%=
)ASM"
R"ASM(
LRPO1_%=:
s_mov_b32 s97, 30000
LRPO1l_%=:
s_sleep 2
buffer_load_dword v242, v243, s[20:23], s96 offen offset:16 sc1
s_waitcnt vmcnt(0)
v_cmp_ne_u32 vcc, s69, v242
s_cbranch_vccz LRPO1b_%=
s_sub_u32 s97, s97, 1
s_cbranch_scc0 LRPO1l_%=
s_branch LRPO1b_%=
LKEND_ka:
LRET_%=:
s_cmp_eq_u32 s73, 0
s_cbranch_scc1 LBACK0_%=
s_cmp_eq_u32 s73, 2
s_cbranch_scc1 LBACK2_%=
s_cmp_eq_u32 s73, 1
s_cbranch_scc1 LBACK1_%=
s_cmp_eq_u32 s73, 3
s_cbranch_scc1 LBACK3_%=
s_cmp_eq_u32 s73, 4
s_cbranch_scc1 LBACK4_%=
s_cmp_eq_u32 s73, 6
s_cbranch_scc1 LBACK6_%=
s_cmp_eq_u32 s73, 8
s_cbranch_scc1 LBACK8_%=
s_cmp_eq_u32 s73, 10
s_cbranch_scc1 LBACK10_%=
s_cmp_eq_u32 s73, 12
s_cbranch_scc1 LBACK12_%=
s_cmp_eq_u32 s73, 14
s_cbranch_scc1 LBACK14_%=
s_cmp_eq_u32 s73, 16
s_cbranch_scc1 LBACK16_%=
s_cmp_eq_u32 s73, 18
s_cbranch_scc1 LBACK18_%=
s_cmp_eq_u32 s73, 20
s_cbranch_scc1 LBACK20_%=
s_cmp_eq_u32 s73, 22
s_cbranch_scc1 LBACK22_%=
s_cmp_eq_u32 s73, 24
s_cbranch_scc1 LBACK24_%=
s_cmp_eq_u32 s73, 26
s_cbranch_scc1 LBACK26_%=
s_cmp_eq_u32 s73, 28
s_cbranch_scc1 LBACK28_%=
s_cmp_eq_u32 s73, 30
s_cbranch_scc1 LBACK30_%=
s_cmp_eq_u32 s73, 32
s_cbranch_scc1 LBACK32_%=
s_cmp_eq_u32 s73, 34
s_cbranch_scc1 LBACK34_%=
s_cmp_eq_u32 s73, 5
s_cbranch_scc1 LBACK5_%=
s_cmp_eq_u32 s73, 7
s_cbranch_scc1 LBACK7_%=
s_cmp_eq_u32 s73, 9
s_cbranch_scc1 LBACK9_%=
s_cmp_eq_u32 s73, 11
s_cbranch_scc1 LBACK11_%=
s_cmp_eq_u32 s73, 13
s_cbranch_scc1 LBACK13_%=
s_cmp_eq_u32 s73, 15
s_cbranch_scc1 LBACK15_%=
s_cmp_eq_u32 s73, 17
s_cbranch_scc1 LBACK17_%=
s_cmp_eq_u32 s73, 19
s_cbranch_scc1 LBACK19_%=
s_cmp_eq_u32 s73, 21
s_cbranch_scc1 LBACK21_%=
s_cmp_eq_u32 s73, 23
s_cbranch_scc1 LBACK23_%=
s_cmp_eq_u32 s73, 25
s_cbranch_scc1 LBACK25_%=
s_cmp_eq_u32 s73, 27
s_cbranch_scc1 LBACK27_%=
s_cmp_eq_u32 s73, 29
s_cbranch_scc1 LBACK29_%=
s_cmp_eq_u32 s73, 31
s_cbranch_scc1 LBACK31_%=
s_cmp_eq_u32 s73, 33
s_cbranch_scc1 LBACK33_%=
s_cmp_eq_u32 s73, 35
s_cbranch_scc1 LBACK35_%=
s_cmp_eq_u32 s73, 36
s_cbranch_scc1 LBACK36_%=
s_cmp_eq_u32 s73, 38
s_cbranch_scc1 LBACK38_%=
s_cmp_eq_u32 s73, 37
s_cbranch_scc1 LBACK37_%=
s_cmp_eq_u32 s73, 39
s_cbranch_scc1 LBACK39_%=
s_endpgm
LSITE0_%=:
s_mov_b32 s73, 0
s_mov_b32 s74, 0
s_mov_b32 s75, 16384
s_branch LRARE002_%=
LSITE2_%=:
s_mov_b32 s73, 2
s_mov_b32 s74, 32768
s_mov_b32 s75, 49152
s_branch LRARE002_%=
LSITE1_%=:
s_mov_b32 s73, 1
s_mov_b32 s74, 16384
s_mov_b32 s75, 32768
s_branch LRARE102_%=
LSITE3_%=:
s_mov_b32 s73, 3
s_mov_b32 s74, 49152
s_mov_b32 s75, 0
s_branch LRARE102_%=
LSITE4_%=:
s_mov_b32 s73, 4
s_mov_b32 s74, 0
s_mov_b32 s75, 16384
s_branch LRARE004_%=
LSITE6_%=:
s_mov_b32 s73, 6
s_mov_b32 s74, 32768
s_mov_b32 s75, 49152
s_branch LRARE004_%=
LSITE8_%=:
s_mov_b32 s73, 8
s_mov_b32 s74, 0
s_mov_b32 s75, 16384
s_branch LRARE004_%=
LSITE10_%=:
s_mov_b32 s73, 10
s_mov_b32 s74, 32768
s_mov_b32 s75, 49152
s_branch LRARE004_%=
LSITE12_%=:
s_mov_b32 s73, 12
s_mov_b32 s74, 0
s_mov_b32 s75, 16384
s_branch LRARE004_%=
LSITE14_%=:
s_mov_b32 s73, 14
s_mov_b32 s74, 32768
s_mov_b32 s75, 49152
s_branch LRARE004_%=
LSITE16_%=:
s_mov_b32 s73, 16
s_mov_b32 s74, 0
s_mov_b32 s75, 16384
s_branch LRARE004_%=
LSITE18_%=:
s_mov_b32 s73, 18
s_mov_b32 s74, 32768
s_mov_b32 s75, 49152
s_branch LRARE004_%=
LSITE20_%=:
s_mov_b32 s73, 20
s_mov_b32 s74, 0
s_mov_b32 s75, 16384
s_branch LRARE004_%=
LSITE22_%=:
s_mov_b32 s73, 22
s_mov_b32 s74, 32768
s_mov_b32 s75, 49152
s_branch LRARE004_%=
LSITE24_%=:
s_mov_b32 s73, 24
s_mov_b32 s74, 0
s_mov_b32 s75, 16384
s_branch LRARE004_%=
LSITE26_%=:
s_mov_b32 s73, 26
s_mov_b32 s74, 32768
s_mov_b32 s75, 49152
s_branch LRARE004_%=
LSITE28_%=:
s_mov_b32 s73, 28
s_mov_b32 s74, 0
s_mov_b32 s75, 16384
s_branch LRARE004_%=
LSITE30_%=:
s_mov_b32 s73, 30
s_mov_b32 s74, 32768
s_mov_b32 s75, 49152
s_branch LRARE004_%=
LSITE32_%=:
s_mov_b32 s73, 32
s_mov_b32 s74, 0
s_mov_b32 s75, 16384
s_branch LRARE004_%=
LSITE34_%=:
s_mov_b32 s73, 34
s_mov_b32 s74, 32768
s_mov_b32 s75, 49152
s_branch LRARE004_%=
LSITE5_%=:
s_mov_b32 s73, 5
s_mov_b32 s74, 16384
s_mov_b32 s75, 32768
s_branch LRARE104_%=
LSITE7_%=:
s_mov_b32 s73, 7
s_mov_b32 s74, 49152
s_mov_b32 s75, 0
s_branch LRARE104_%=
LSITE9_%=:
s_mov_b32 s73, 9
s_mov_b32 s74, 16384
s_mov_b32 s75, 32768
s_branch LRARE104_%=
LSITE11_%=:
s_mov_b32 s73, 11
s_mov_b32 s74, 49152
s_mov_b32 s75, 0
s_branch LRARE104_%=
LSITE13_%=:
s_mov_b32 s73, 13
s_mov_b32 s74, 16384
s_mov_b32 s75, 32768
s_branch LRARE104_%=
LSITE15_%=:
s_mov_b32 s73, 15
s_mov_b32 s74, 49152
s_mov_b32 s75, 0
s_branch LRARE104_%=
LSITE17_%=:
s_mov_b32 s73, 17
s_mov_b32 s74, 16384
s_mov_b32 s75, 32768
s_branch LRARE104_%=
LSITE19_%=:
s_mov_b32 s73, 19
s_mov_b32 s74, 49152
s_mov_b32 s75, 0
s_branch LRARE104_%=
LSITE21_%=:
s_mov_b32 s73, 21
s_mov_b32 s74, 16384
s_mov_b32 s75, 32768
s_branch LRARE104_%=
LSITE23_%=:
s_mov_b32 s73, 23
s_mov_b32 s74, 49152
s_mov_b32 s75, 0
s_branch LRARE104_%=
LSITE25_%=:
s_mov_b32 s73, 25
s_mov_b32 s74, 16384
s_mov_b32 s75, 32768
s_branch LRARE104_%=
LSITE27_%=:
s_mov_b32 s73, 27
s_mov_b32 s74, 49152
s_mov_b32 s75, 0
s_branch LRARE104_%=
LSITE29_%=:
s_mov_b32 s73, 29
s_mov_b32 s74, 16384
s_mov_b32 s75, 32768
s_branch LRARE104_%=
LSITE31_%=:
s_mov_b32 s73, 31
s_mov_b32 s74, 49152
s_mov_b32 s75, 0
s_branch LRARE104_%=
LSITE33_%=:
s_mov_b32 s73, 33
s_mov_b32 s74, 16384
s_mov_b32 s75, 32768
s_branch LRARE104_%=
LSITE35_%=:
s_mov_b32 s73, 35
s_mov_b32 s74, 49152
s_mov_b32 s75, 0
s_branch LRARE104_%=
LSITE36_%=:
s_mov_b32 s73, 36
s_mov_b32 s74, 0
s_mov_b32 s75, 16384
s_branch LRARE022_%=
LSITE38_%=:
s_mov_b32 s73, 38
s_mov_b32 s74, 32768
s_mov_b32 s75, 49152
s_branch LRARE022_%=
LSITE37_%=:
s_mov_b32 s73, 37
s_mov_b32 s74, 16384
s_mov_b32 s75, 32768
s_branch LRARE122_%=
LSITE39_%=:
s_mov_b32 s73, 39
s_mov_b32 s74, 49152
s_mov_b32 s75, 0
s_branch LRARE122_%=
LRARE002_%=:
s_nop 15
s_waitcnt vmcnt(0) lgkmcnt(0)
v_add_u32 v249, s74, v224
RD a[192:195],v249 offset:0
RD a[208:211],v249 offset:4096
RD a[224:227],v249 offset:8192
RD a[240:243],v249 offset:12288
v_add_u32 v250, s74, v225
RD a[196:199],v250 offset:0
RD a[212:215],v250 offset:4096
RD a[228:231],v250 offset:8192
RD a[244:247],v250 offset:12288
v_add_u32 v251, s74, v226
RD a[200:203],v251 offset:0
RD a[216:219],v251 offset:4096
RD a[232:235],v251 offset:8192
RD a[248:251],v251 offset:12288
v_add_u32 v254, s74, v227
RD a[204:207],v254 offset:0
RD a[220:223],v254 offset:4096
RD a[236:239],v254 offset:8192
RD a[252:255],v254 offset:12288
s_waitcnt lgkmcnt(0)
MM v[0:3],a[192:195],v[160:163],v[128:131]
MM v[0:3],a[196:199],v[176:179],v[0:3]
MM v[0:3],a[200:203],v[192:195],v[0:3]
MM v[0:3],a[204:207],v[208:211],v[0:3]
MM v[4:7],a[220:223],v[208:211],v[128:131]
MM v[4:7],a[216:219],v[192:195],v[4:7]
MM v[4:7],a[212:215],v[176:179],v[4:7]
MM v[4:7],a[208:211],v[160:163],v[4:7]
MM v[12:15],a[208:211],v[164:167],v[132:135]
MM v[12:15],a[212:215],v[180:183],v[12:15]
MM v[12:15],a[216:219],v[196:199],v[12:15]
MM v[12:15],a[220:223],v[212:215],v[12:15]
MM v[8:11],a[204:207],v[212:215],v[132:135]
MM v[8:11],a[200:203],v[196:199],v[8:11]
MM v[8:11],a[196:199],v[180:183],v[8:11]
MM v[8:11],a[192:195],v[164:167],v[8:11]
MM v[32:35],a[224:227],v[160:163],v[128:131]
MM v[32:35],a[228:231],v[176:179],v[32:35]
MM v[32:35],a[232:235],v[192:195],v[32:35]
MM v[32:35],a[236:239],v[208:211],v[32:35]
MM v[36:39],a[252:255],v[208:211],v[128:131]
MM v[36:39],a[248:251],v[192:195],v[36:39]
MM v[36:39],a[244:247],v[176:179],v[36:39]
MM v[36:39],a[240:243],v[160:163],v[36:39]
MM v[44:47],a[240:243],v[164:167],v[132:135]
MM v[44:47],a[244:247],v[180:183],v[44:47]
MM v[44:47],a[248:251],v[196:199],v[44:47]
MM v[44:47],a[252:255],v[212:215],v[44:47]
MM v[40:43],a[236:239],v[212:215],v[132:135]
MM v[40:43],a[232:235],v[196:199],v[40:43]
MM v[40:43],a[228:231],v[180:183],v[40:43]
MM v[40:43],a[224:227],v[164:167],v[40:43]
s_nop 15
v_add_u32 v249, s75, v224
RD a[192:195],v249 offset:0
RD a[208:211],v249 offset:4096
RD a[224:227],v249 offset:8192
RD a[240:243],v249 offset:12288
v_add_u32 v250, s75, v225
RD a[196:199],v250 offset:0
RD a[212:215],v250 offset:4096
RD a[228:231],v250 offset:8192
RD a[244:247],v250 offset:12288
v_add_u32 v251, s75, v226
RD a[200:203],v251 offset:0
RD a[216:219],v251 offset:4096
RD a[232:235],v251 offset:8192
RD a[248:251],v251 offset:12288
v_add_u32 v254, s75, v227
RD a[204:207],v254 offset:0
RD a[220:223],v254 offset:4096
RD a[236:239],v254 offset:8192
RD a[252:255],v254 offset:12288
s_waitcnt lgkmcnt(0)
v_max3_f32 v249, v0, v1, v2
v_max3_f32 v251, v32, v33, v34
v_max3_f32 v249, v249, v3, v4
v_max3_f32 v251, v251, v35, v36
v_max3_f32 v249, v249, v5, v6
v_max3_f32 v251, v251, v37, v38
v_max3_f32 v249, v249, v7, v39
v_max_f32 v249, v249, v251
v_mov_b32 v251, v249
s_nop 1
v_permlane16_swap_b32 v249, v251
v_max_f32 v249, v249, v251
v_mov_b32 v251, v249
s_nop 1
v_permlane32_swap_b32 v249, v251
v_max_f32 v249, v249, v251
v_log_f32 v250, v244
s_nop 0
v_max3_f32 v249, 0, v249, v250
v_sub_f32 v250, 0, v249
EX v250,v250
v_sub_f32 v128, v128, v249
v_sub_f32 v129, v129, v249
v_sub_f32 v130, v130, v249
v_sub_f32 v131, v131, v249
v_sub_f32 v64, v64, v249
v_sub_f32 v0, v0, v249
v_sub_f32 v65, v65, v249
v_sub_f32 v1, v1, v249
v_sub_f32 v66, v66, v249
v_sub_f32 v2, v2, v249
v_sub_f32 v67, v67, v249
v_sub_f32 v3, v3, v249
v_sub_f32 v68, v68, v249
v_sub_f32 v4, v4, v249
v_sub_f32 v69, v69, v249
v_sub_f32 v5, v5, v249
v_sub_f32 v70, v70, v249
v_sub_f32 v6, v6, v249
v_sub_f32 v71, v71, v249
v_sub_f32 v7, v7, v249
v_sub_f32 v96, v96, v249
v_sub_f32 v32, v32, v249
v_sub_f32 v97, v97, v249
v_sub_f32 v33, v33, v249
v_sub_f32 v98, v98, v249
v_sub_f32 v34, v34, v249
v_sub_f32 v99, v99, v249
v_sub_f32 v35, v35, v249
v_sub_f32 v100, v100, v249
v_sub_f32 v36, v36, v249
v_sub_f32 v101, v101, v249
v_sub_f32 v37, v37, v249
v_sub_f32 v102, v102, v249
v_sub_f32 v38, v38, v249
v_sub_f32 v103, v103, v249
v_sub_f32 v39, v39, v249
v_mul_f32 v144, v244, v250
v_mov_b32 v145, 0
v_bfe_u32 v249, v241, 0, 8
v_lshl_or_b32 v249, v249, 1, 1
v_lshlrev_b32 v249, 22, v249
v_and_b32 v241, 0xffffff00, v241
AR v254,a0
v_sub_f32 v254, v254, v249
v_mul_f32 v254, v254, v250
AW a0,v254
AR v251,a1
v_sub_f32 v251, v251, v249
v_mul_f32 v251, v251, v250
AW a1,v251
AR v254,a2
v_sub_f32 v254, v254, v249
v_mul_f32 v254, v254, v250
AW a2,v254
AR v251,a3
v_sub_f32 v251, v251, v249
v_mul_f32 v251, v251, v250
AW a3,v251
AR v254,a4
v_sub_f32 v254, v254, v249
v_mul_f32 v254, v254, v250
AW a4,v254
AR v251,a5
v_sub_f32 v251, v251, v249
v_mul_f32 v251, v251, v250
AW a5,v251
AR v254,a6
v_sub_f32 v254, v254, v249
v_mul_f32 v254, v254, v250
AW a6,v254
AR v251,a7
v_sub_f32 v251, v251, v249
v_mul_f32 v251, v251, v250
AW a7,v251
AR v254,a32
v_sub_f32 v254, v254, v249
v_mul_f32 v254, v254, v250
AW a32,v254
AR v251,a33
v_sub_f32 v251, v251, v249
v_mul_f32 v251, v251, v250
AW a33,v251
AR v254,a34
v_sub_f32 v254, v254, v249
v_mul_f32 v254, v254, v250
AW a34,v254
AR v251,a35
v_sub_f32 v251, v251, v249
v_mul_f32 v251, v251, v250
AW a35,v251
AR v254,a36
v_sub_f32 v254, v254, v249
v_mul_f32 v254, v254, v250
AW a36,v254
AR v251,a37
v_sub_f32 v251, v251, v249
v_mul_f32 v251, v251, v250
AW a37,v251
AR v254,a38
v_sub_f32 v254, v254, v249
v_mul_f32 v254, v254, v250
AW a38,v254
AR v251,a39
v_sub_f32 v251, v251, v249
v_mul_f32 v251, v251, v250
AW a39,v251
AR v254,a64
v_sub_f32 v254, v254, v249
v_mul_f32 v254, v254, v250
AW a64,v254
AR v251,a65
v_sub_f32 v251, v251, v249
v_mul_f32 v251, v251, v250
AW a65,v251
AR v254,a66
v_sub_f32 v254, v254, v249
v_mul_f32 v254, v254, v250
AW a66,v254
AR v251,a67
v_sub_f32 v251, v251, v249
v_mul_f32 v251, v251, v250
AW a67,v251
AR v254,a68
v_sub_f32 v254, v254, v249
v_mul_f32 v254, v254, v250
AW a68,v254
AR v251,a69
v_sub_f32 v251, v251, v249
v_mul_f32 v251, v251, v250
AW a69,v251
AR v254,a70
v_sub_f32 v254, v254, v249
v_mul_f32 v254, v254, v250
AW a70,v254
AR v251,a71
v_sub_f32 v251, v251, v249
v_mul_f32 v251, v251, v250
AW a71,v251
AR v254,a96
v_sub_f32 v254, v254, v249
v_mul_f32 v254, v254, v250
AW a96,v254
AR v251,a97
v_sub_f32 v251, v251, v249
v_mul_f32 v251, v251, v250
AW a97,v251
AR v254,a98
)ASM"
R"ASM(
v_sub_f32 v254, v254, v249
v_mul_f32 v254, v254, v250
AW a98,v254
AR v251,a99
v_sub_f32 v251, v251, v249
v_mul_f32 v251, v251, v250
AW a99,v251
AR v254,a100
v_sub_f32 v254, v254, v249
v_mul_f32 v254, v254, v250
AW a100,v254
AR v251,a101
v_sub_f32 v251, v251, v249
v_mul_f32 v251, v251, v250
AW a101,v251
AR v254,a102
v_sub_f32 v254, v254, v249
v_mul_f32 v254, v254, v250
AW a102,v254
AR v251,a103
v_sub_f32 v251, v251, v249
v_mul_f32 v251, v251, v250
AW a103,v251
v_max3_f32 v249, v8, v9, v10
v_max3_f32 v251, v40, v41, v42
v_max3_f32 v249, v249, v11, v12
v_max3_f32 v251, v251, v43, v44
v_max3_f32 v249, v249, v13, v14
v_max3_f32 v251, v251, v45, v46
v_max3_f32 v249, v249, v15, v47
v_max_f32 v249, v249, v251
v_mov_b32 v251, v249
s_nop 1
v_permlane16_swap_b32 v249, v251
v_max_f32 v249, v249, v251
v_mov_b32 v251, v249
s_nop 1
v_permlane32_swap_b32 v249, v251
v_max_f32 v249, v249, v251
v_log_f32 v250, v245
s_nop 0
v_max3_f32 v249, 0, v249, v250
v_sub_f32 v250, 0, v249
EX v250,v250
v_sub_f32 v132, v132, v249
v_sub_f32 v133, v133, v249
v_sub_f32 v134, v134, v249
v_sub_f32 v135, v135, v249
v_sub_f32 v72, v72, v249
v_sub_f32 v8, v8, v249
v_sub_f32 v73, v73, v249
v_sub_f32 v9, v9, v249
v_sub_f32 v74, v74, v249
v_sub_f32 v10, v10, v249
v_sub_f32 v75, v75, v249
v_sub_f32 v11, v11, v249
v_sub_f32 v76, v76, v249
v_sub_f32 v12, v12, v249
v_sub_f32 v77, v77, v249
v_sub_f32 v13, v13, v249
v_sub_f32 v78, v78, v249
v_sub_f32 v14, v14, v249
v_sub_f32 v79, v79, v249
v_sub_f32 v15, v15, v249
v_sub_f32 v104, v104, v249
v_sub_f32 v40, v40, v249
v_sub_f32 v105, v105, v249
v_sub_f32 v41, v41, v249
v_sub_f32 v106, v106, v249
v_sub_f32 v42, v42, v249
v_sub_f32 v107, v107, v249
v_sub_f32 v43, v43, v249
v_sub_f32 v108, v108, v249
v_sub_f32 v44, v44, v249
v_sub_f32 v109, v109, v249
v_sub_f32 v45, v45, v249
v_sub_f32 v110, v110, v249
v_sub_f32 v46, v46, v249
v_sub_f32 v111, v111, v249
v_sub_f32 v47, v47, v249
v_mul_f32 v148, v245, v250
v_mov_b32 v149, 0
v_bfe_u32 v249, v241, 8, 8
v_lshl_or_b32 v249, v249, 1, 1
v_lshlrev_b32 v249, 22, v249
v_and_b32 v241, 0xffff00ff, v241
AR v254,a8
v_sub_f32 v254, v254, v249
v_mul_f32 v254, v254, v250
AW a8,v254
AR v251,a9
v_sub_f32 v251, v251, v249
v_mul_f32 v251, v251, v250
AW a9,v251
AR v254,a10
v_sub_f32 v254, v254, v249
v_mul_f32 v254, v254, v250
AW a10,v254
AR v251,a11
v_sub_f32 v251, v251, v249
v_mul_f32 v251, v251, v250
AW a11,v251
AR v254,a12
v_sub_f32 v254, v254, v249
v_mul_f32 v254, v254, v250
AW a12,v254
AR v251,a13
v_sub_f32 v251, v251, v249
v_mul_f32 v251, v251, v250
AW a13,v251
AR v254,a14
v_sub_f32 v254, v254, v249
v_mul_f32 v254, v254, v250
AW a14,v254
AR v251,a15
v_sub_f32 v251, v251, v249
v_mul_f32 v251, v251, v250
AW a15,v251
AR v254,a40
v_sub_f32 v254, v254, v249
v_mul_f32 v254, v254, v250
AW a40,v254
AR v251,a41
v_sub_f32 v251, v251, v249
v_mul_f32 v251, v251, v250
AW a41,v251
AR v254,a42
v_sub_f32 v254, v254, v249
v_mul_f32 v254, v254, v250
AW a42,v254
AR v251,a43
v_sub_f32 v251, v251, v249
v_mul_f32 v251, v251, v250
AW a43,v251
AR v254,a44
v_sub_f32 v254, v254, v249
v_mul_f32 v254, v254, v250
AW a44,v254
AR v251,a45
v_sub_f32 v251, v251, v249
v_mul_f32 v251, v251, v250
AW a45,v251
AR v254,a46
v_sub_f32 v254, v254, v249
v_mul_f32 v254, v254, v250
AW a46,v254
AR v251,a47
v_sub_f32 v251, v251, v249
v_mul_f32 v251, v251, v250
AW a47,v251
AR v254,a72
v_sub_f32 v254, v254, v249
v_mul_f32 v254, v254, v250
AW a72,v254
AR v251,a73
v_sub_f32 v251, v251, v249
v_mul_f32 v251, v251, v250
AW a73,v251
AR v254,a74
v_sub_f32 v254, v254, v249
v_mul_f32 v254, v254, v250
AW a74,v254
AR v251,a75
v_sub_f32 v251, v251, v249
v_mul_f32 v251, v251, v250
AW a75,v251
AR v254,a76
v_sub_f32 v254, v254, v249
v_mul_f32 v254, v254, v250
AW a76,v254
AR v251,a77
v_sub_f32 v251, v251, v249
v_mul_f32 v251, v251, v250
AW a77,v251
AR v254,a78
v_sub_f32 v254, v254, v249
v_mul_f32 v254, v254, v250
AW a78,v254
AR v251,a79
v_sub_f32 v251, v251, v249
v_mul_f32 v251, v251, v250
AW a79,v251
AR v254,a104
v_sub_f32 v254, v254, v249
v_mul_f32 v254, v254, v250
AW a104,v254
AR v251,a105
v_sub_f32 v251, v251, v249
v_mul_f32 v251, v251, v250
AW a105,v251
AR v254,a106
v_sub_f32 v254, v254, v249
v_mul_f32 v254, v254, v250
AW a106,v254
AR v251,a107
v_sub_f32 v251, v251, v249
v_mul_f32 v251, v251, v250
AW a107,v251
AR v254,a108
v_sub_f32 v254, v254, v249
v_mul_f32 v254, v254, v250
AW a108,v254
AR v251,a109
v_sub_f32 v251, v251, v249
v_mul_f32 v251, v251, v250
AW a109,v251
AR v254,a110
v_sub_f32 v254, v254, v249
v_mul_f32 v254, v254, v250
AW a110,v254
AR v251,a111
v_sub_f32 v251, v251, v249
v_mul_f32 v251, v251, v250
AW a111,v251
EX v0,v0
EX v1,v1
EX v2,v2
EX v3,v3
EX v4,v4
EX v5,v5
EX v6,v6
EX v7,v7
EX v8,v8
EX v9,v9
EX v10,v10
EX v11,v11
EX v12,v12
EX v13,v13
EX v14,v14
EX v15,v15
EX v32,v32
EX v33,v33
EX v34,v34
EX v35,v35
EX v36,v36
EX v37,v37
EX v38,v38
EX v39,v39
EX v40,v40
EX v41,v41
EX v42,v42
EX v43,v43
EX v44,v44
EX v45,v45
EX v46,v46
EX v47,v47
CV v0,v0,v1
CV v1,v2,v3
CV v2,v4,v5
CV v3,v6,v7
CV v8,v8,v9
CV v9,v10,v11
CV v10,v12,v13
CV v11,v14,v15
CV v4,v32,v33
CV v5,v34,v35
CV v6,v36,v37
CV v7,v38,v39
CV v12,v40,v41
CV v13,v42,v43
CV v14,v44,v45
CV v15,v46,v47
v_smfmac_f32_16x16x64_bf16 v[144:147], v[236:239], v[0:7], v234
s_nop 0
v_smfmac_f32_16x16x64_bf16 v[148:151], v[236:239], v[8:15], v234
s_nop 9
s_branch LRET_%=
LRARE102_%=:
s_nop 15
s_waitcnt vmcnt(0) lgkmcnt(0)
v_add_u32 v249, s74, v224
RD a[192:195],v249 offset:0
RD a[208:211],v249 offset:4096
RD a[224:227],v249 offset:8192
RD a[240:243],v249 offset:12288
v_add_u32 v250, s74, v225
RD a[196:199],v250 offset:0
RD a[212:215],v250 offset:4096
RD a[228:231],v250 offset:8192
RD a[244:247],v250 offset:12288
v_add_u32 v251, s74, v226
RD a[200:203],v251 offset:0
RD a[216:219],v251 offset:4096
RD a[232:235],v251 offset:8192
RD a[248:251],v251 offset:12288
v_add_u32 v254, s74, v227
RD a[204:207],v254 offset:0
RD a[220:223],v254 offset:4096
RD a[236:239],v254 offset:8192
RD a[252:255],v254 offset:12288
s_waitcnt lgkmcnt(0)
MM v[64:67],a[192:195],v[160:163],v[128:131]
MM v[64:67],a[196:199],v[176:179],v[64:67]
MM v[64:67],a[200:203],v[192:195],v[64:67]
MM v[64:67],a[204:207],v[208:211],v[64:67]
MM v[68:71],a[220:223],v[208:211],v[128:131]
MM v[68:71],a[216:219],v[192:195],v[68:71]
MM v[68:71],a[212:215],v[176:179],v[68:71]
MM v[68:71],a[208:211],v[160:163],v[68:71]
MM v[76:79],a[208:211],v[164:167],v[132:135]
MM v[76:79],a[212:215],v[180:183],v[76:79]
MM v[76:79],a[216:219],v[196:199],v[76:79]
MM v[76:79],a[220:223],v[212:215],v[76:79]
MM v[72:75],a[204:207],v[212:215],v[132:135]
MM v[72:75],a[200:203],v[196:199],v[72:75]
MM v[72:75],a[196:199],v[180:183],v[72:75]
MM v[72:75],a[192:195],v[164:167],v[72:75]
MM v[96:99],a[224:227],v[160:163],v[128:131]
MM v[96:99],a[228:231],v[176:179],v[96:99]
MM v[96:99],a[232:235],v[192:195],v[96:99]
MM v[96:99],a[236:239],v[208:211],v[96:99]
MM v[100:103],a[252:255],v[208:211],v[128:131]
MM v[100:103],a[248:251],v[192:195],v[100:103]
MM v[100:103],a[244:247],v[176:179],v[100:103]
MM v[100:103],a[240:243],v[160:163],v[100:103]
MM v[108:111],a[240:243],v[164:167],v[132:135]
MM v[108:111],a[244:247],v[180:183],v[108:111]
MM v[108:111],a[248:251],v[196:199],v[108:111]
MM v[108:111],a[252:255],v[212:215],v[108:111]
MM v[104:107],a[236:239],v[212:215],v[132:135]
MM v[104:107],a[232:235],v[196:199],v[104:107]
MM v[104:107],a[228:231],v[180:183],v[104:107]
MM v[104:107],a[224:227],v[164:167],v[104:107]
s_nop 15
v_add_u32 v249, s75, v224
RD a[192:195],v249 offset:0
RD a[208:211],v249 offset:4096
RD a[224:227],v249 offset:8192
RD a[240:243],v249 offset:12288
v_add_u32 v250, s75, v225
RD a[196:199],v250 offset:0
RD a[212:215],v250 offset:4096
RD a[228:231],v250 offset:8192
RD a[244:247],v250 offset:12288
v_add_u32 v251, s75, v226
RD a[200:203],v251 offset:0
RD a[216:219],v251 offset:4096
RD a[232:235],v251 offset:8192
RD a[248:251],v251 offset:12288
v_add_u32 v254, s75, v227
RD a[204:207],v254 offset:0
RD a[220:223],v254 offset:4096
RD a[236:239],v254 offset:8192
RD a[252:255],v254 offset:12288
s_waitcnt lgkmcnt(0)
v_max3_f32 v249, v64, v65, v66
v_max3_f32 v251, v96, v97, v98
v_max3_f32 v249, v249, v67, v68
v_max3_f32 v251, v251, v99, v100
v_max3_f32 v249, v249, v69, v70
v_max3_f32 v251, v251, v101, v102
v_max3_f32 v249, v249, v71, v103
v_max_f32 v249, v249, v251
v_mov_b32 v251, v249
s_nop 1
v_permlane16_swap_b32 v249, v251
v_max_f32 v249, v249, v251
v_mov_b32 v251, v249
s_nop 1
v_permlane32_swap_b32 v249, v251
v_max_f32 v249, v249, v251
v_log_f32 v250, v244
s_nop 0
v_max3_f32 v249, 0, v249, v250
v_sub_f32 v250, 0, v249
EX v250,v250
v_sub_f32 v128, v128, v249
v_sub_f32 v129, v129, v249
v_sub_f32 v130, v130, v249
v_sub_f32 v131, v131, v249
v_sub_f32 v0, v0, v249
v_sub_f32 v64, v64, v249
v_sub_f32 v1, v1, v249
v_sub_f32 v65, v65, v249
v_sub_f32 v2, v2, v249
v_sub_f32 v66, v66, v249
v_sub_f32 v3, v3, v249
v_sub_f32 v67, v67, v249
v_sub_f32 v4, v4, v249
v_sub_f32 v68, v68, v249
v_sub_f32 v5, v5, v249
v_sub_f32 v69, v69, v249
v_sub_f32 v6, v6, v249
v_sub_f32 v70, v70, v249
v_sub_f32 v7, v7, v249
v_sub_f32 v71, v71, v249
v_sub_f32 v32, v32, v249
v_sub_f32 v96, v96, v249
v_sub_f32 v33, v33, v249
v_sub_f32 v97, v97, v249
v_sub_f32 v34, v34, v249
v_sub_f32 v98, v98, v249
v_sub_f32 v35, v35, v249
v_sub_f32 v99, v99, v249
v_sub_f32 v36, v36, v249
v_sub_f32 v100, v100, v249
v_sub_f32 v37, v37, v249
v_sub_f32 v101, v101, v249
v_sub_f32 v38, v38, v249
v_sub_f32 v102, v102, v249
v_sub_f32 v39, v39, v249
v_sub_f32 v103, v103, v249
v_mul_f32 v144, v244, v250
v_mov_b32 v145, 0
v_bfe_u32 v249, v241, 0, 8
v_lshl_or_b32 v249, v249, 1, 1
v_lshlrev_b32 v249, 22, v249
v_and_b32 v241, 0xffffff00, v241
AR v254,a0
v_sub_f32 v254, v254, v249
v_mul_f32 v254, v254, v250
AW a0,v254
AR v251,a1
v_sub_f32 v251, v251, v249
v_mul_f32 v251, v251, v250
AW a1,v251
AR v254,a2
v_sub_f32 v254, v254, v249
v_mul_f32 v254, v254, v250
AW a2,v254
AR v251,a3
v_sub_f32 v251, v251, v249
v_mul_f32 v251, v251, v250
AW a3,v251
AR v254,a4
v_sub_f32 v254, v254, v249
v_mul_f32 v254, v254, v250
AW a4,v254
AR v251,a5
v_sub_f32 v251, v251, v249
v_mul_f32 v251, v251, v250
AW a5,v251
AR v254,a6
v_sub_f32 v254, v254, v249
v_mul_f32 v254, v254, v250
AW a6,v254
AR v251,a7
v_sub_f32 v251, v251, v249
v_mul_f32 v251, v251, v250
AW a7,v251
AR v254,a32
v_sub_f32 v254, v254, v249
v_mul_f32 v254, v254, v250
AW a32,v254
AR v251,a33
v_sub_f32 v251, v251, v249
v_mul_f32 v251, v251, v250
AW a33,v251
AR v254,a34
v_sub_f32 v254, v254, v249
v_mul_f32 v254, v254, v250
AW a34,v254
AR v251,a35
v_sub_f32 v251, v251, v249
v_mul_f32 v251, v251, v250
AW a35,v251
AR v254,a36
v_sub_f32 v254, v254, v249
v_mul_f32 v254, v254, v250
AW a36,v254
AR v251,a37
v_sub_f32 v251, v251, v249
v_mul_f32 v251, v251, v250
AW a37,v251
AR v254,a38
v_sub_f32 v254, v254, v249
v_mul_f32 v254, v254, v250
AW a38,v254
AR v251,a39
v_sub_f32 v251, v251, v249
v_mul_f32 v251, v251, v250
AW a39,v251
AR v254,a64
v_sub_f32 v254, v254, v249
v_mul_f32 v254, v254, v250
AW a64,v254
AR v251,a65
v_sub_f32 v251, v251, v249
v_mul_f32 v251, v251, v250
AW a65,v251
AR v254,a66
v_sub_f32 v254, v254, v249
v_mul_f32 v254, v254, v250
AW a66,v254
AR v251,a67
v_sub_f32 v251, v251, v249
v_mul_f32 v251, v251, v250
AW a67,v251
AR v254,a68
v_sub_f32 v254, v254, v249
v_mul_f32 v254, v254, v250
AW a68,v254
AR v251,a69
v_sub_f32 v251, v251, v249
v_mul_f32 v251, v251, v250
AW a69,v251
AR v254,a70
v_sub_f32 v254, v254, v249
v_mul_f32 v254, v254, v250
AW a70,v254
AR v251,a71
v_sub_f32 v251, v251, v249
v_mul_f32 v251, v251, v250
AW a71,v251
AR v254,a96
v_sub_f32 v254, v254, v249
v_mul_f32 v254, v254, v250
AW a96,v254
AR v251,a97
v_sub_f32 v251, v251, v249
v_mul_f32 v251, v251, v250
AW a97,v251
AR v254,a98
v_sub_f32 v254, v254, v249
v_mul_f32 v254, v254, v250
AW a98,v254
AR v251,a99
v_sub_f32 v251, v251, v249
v_mul_f32 v251, v251, v250
AW a99,v251
AR v254,a100
v_sub_f32 v254, v254, v249
v_mul_f32 v254, v254, v250
AW a100,v254
AR v251,a101
v_sub_f32 v251, v251, v249
v_mul_f32 v251, v251, v250
AW a101,v251
AR v254,a102
v_sub_f32 v254, v254, v249
v_mul_f32 v254, v254, v250
)ASM"
R"ASM(
AW a102,v254
AR v251,a103
v_sub_f32 v251, v251, v249
v_mul_f32 v251, v251, v250
AW a103,v251
v_max3_f32 v249, v72, v73, v74
v_max3_f32 v251, v104, v105, v106
v_max3_f32 v249, v249, v75, v76
v_max3_f32 v251, v251, v107, v108
v_max3_f32 v249, v249, v77, v78
v_max3_f32 v251, v251, v109, v110
v_max3_f32 v249, v249, v79, v111
v_max_f32 v249, v249, v251
v_mov_b32 v251, v249
s_nop 1
v_permlane16_swap_b32 v249, v251
v_max_f32 v249, v249, v251
v_mov_b32 v251, v249
s_nop 1
v_permlane32_swap_b32 v249, v251
v_max_f32 v249, v249, v251
v_log_f32 v250, v245
s_nop 0
v_max3_f32 v249, 0, v249, v250
v_sub_f32 v250, 0, v249
EX v250,v250
v_sub_f32 v132, v132, v249
v_sub_f32 v133, v133, v249
v_sub_f32 v134, v134, v249
v_sub_f32 v135, v135, v249
v_sub_f32 v8, v8, v249
v_sub_f32 v72, v72, v249
v_sub_f32 v9, v9, v249
v_sub_f32 v73, v73, v249
v_sub_f32 v10, v10, v249
v_sub_f32 v74, v74, v249
v_sub_f32 v11, v11, v249
v_sub_f32 v75, v75, v249
v_sub_f32 v12, v12, v249
v_sub_f32 v76, v76, v249
v_sub_f32 v13, v13, v249
v_sub_f32 v77, v77, v249
v_sub_f32 v14, v14, v249
v_sub_f32 v78, v78, v249
v_sub_f32 v15, v15, v249
v_sub_f32 v79, v79, v249
v_sub_f32 v40, v40, v249
v_sub_f32 v104, v104, v249
v_sub_f32 v41, v41, v249
v_sub_f32 v105, v105, v249
v_sub_f32 v42, v42, v249
v_sub_f32 v106, v106, v249
v_sub_f32 v43, v43, v249
v_sub_f32 v107, v107, v249
v_sub_f32 v44, v44, v249
v_sub_f32 v108, v108, v249
v_sub_f32 v45, v45, v249
v_sub_f32 v109, v109, v249
v_sub_f32 v46, v46, v249
v_sub_f32 v110, v110, v249
v_sub_f32 v47, v47, v249
v_sub_f32 v111, v111, v249
v_mul_f32 v148, v245, v250
v_mov_b32 v149, 0
v_bfe_u32 v249, v241, 8, 8
v_lshl_or_b32 v249, v249, 1, 1
v_lshlrev_b32 v249, 22, v249
v_and_b32 v241, 0xffff00ff, v241
AR v254,a8
v_sub_f32 v254, v254, v249
v_mul_f32 v254, v254, v250
AW a8,v254
AR v251,a9
v_sub_f32 v251, v251, v249
v_mul_f32 v251, v251, v250
AW a9,v251
AR v254,a10
v_sub_f32 v254, v254, v249
v_mul_f32 v254, v254, v250
AW a10,v254
AR v251,a11
v_sub_f32 v251, v251, v249
v_mul_f32 v251, v251, v250
AW a11,v251
AR v254,a12
v_sub_f32 v254, v254, v249
v_mul_f32 v254, v254, v250
AW a12,v254
AR v251,a13
v_sub_f32 v251, v251, v249
v_mul_f32 v251, v251, v250
AW a13,v251
AR v254,a14
v_sub_f32 v254, v254, v249
v_mul_f32 v254, v254, v250
AW a14,v254
AR v251,a15
v_sub_f32 v251, v251, v249
v_mul_f32 v251, v251, v250
AW a15,v251
AR v254,a40
v_sub_f32 v254, v254, v249
v_mul_f32 v254, v254, v250
AW a40,v254
AR v251,a41
v_sub_f32 v251, v251, v249
v_mul_f32 v251, v251, v250
AW a41,v251
AR v254,a42
v_sub_f32 v254, v254, v249
v_mul_f32 v254, v254, v250
AW a42,v254
AR v251,a43
v_sub_f32 v251, v251, v249
v_mul_f32 v251, v251, v250
AW a43,v251
AR v254,a44
v_sub_f32 v254, v254, v249
v_mul_f32 v254, v254, v250
AW a44,v254
AR v251,a45
v_sub_f32 v251, v251, v249
v_mul_f32 v251, v251, v250
AW a45,v251
AR v254,a46
v_sub_f32 v254, v254, v249
v_mul_f32 v254, v254, v250
AW a46,v254
AR v251,a47
v_sub_f32 v251, v251, v249
v_mul_f32 v251, v251, v250
AW a47,v251
AR v254,a72
v_sub_f32 v254, v254, v249
v_mul_f32 v254, v254, v250
AW a72,v254
AR v251,a73
v_sub_f32 v251, v251, v249
v_mul_f32 v251, v251, v250
AW a73,v251
AR v254,a74
v_sub_f32 v254, v254, v249
v_mul_f32 v254, v254, v250
AW a74,v254
AR v251,a75
v_sub_f32 v251, v251, v249
v_mul_f32 v251, v251, v250
AW a75,v251
AR v254,a76
v_sub_f32 v254, v254, v249
v_mul_f32 v254, v254, v250
AW a76,v254
AR v251,a77
v_sub_f32 v251, v251, v249
v_mul_f32 v251, v251, v250
AW a77,v251
AR v254,a78
v_sub_f32 v254, v254, v249
v_mul_f32 v254, v254, v250
AW a78,v254
AR v251,a79
v_sub_f32 v251, v251, v249
v_mul_f32 v251, v251, v250
AW a79,v251
AR v254,a104
v_sub_f32 v254, v254, v249
v_mul_f32 v254, v254, v250
AW a104,v254
AR v251,a105
v_sub_f32 v251, v251, v249
v_mul_f32 v251, v251, v250
AW a105,v251
AR v254,a106
v_sub_f32 v254, v254, v249
v_mul_f32 v254, v254, v250
AW a106,v254
AR v251,a107
v_sub_f32 v251, v251, v249
v_mul_f32 v251, v251, v250
AW a107,v251
AR v254,a108
v_sub_f32 v254, v254, v249
v_mul_f32 v254, v254, v250
AW a108,v254
AR v251,a109
v_sub_f32 v251, v251, v249
v_mul_f32 v251, v251, v250
AW a109,v251
AR v254,a110
v_sub_f32 v254, v254, v249
v_mul_f32 v254, v254, v250
AW a110,v254
AR v251,a111
v_sub_f32 v251, v251, v249
v_mul_f32 v251, v251, v250
AW a111,v251
EX v64,v64
EX v65,v65
EX v66,v66
EX v67,v67
EX v68,v68
EX v69,v69
EX v70,v70
EX v71,v71
EX v72,v72
EX v73,v73
EX v74,v74
EX v75,v75
EX v76,v76
EX v77,v77
EX v78,v78
EX v79,v79
EX v96,v96
EX v97,v97
EX v98,v98
EX v99,v99
EX v100,v100
EX v101,v101
EX v102,v102
EX v103,v103
EX v104,v104
EX v105,v105
EX v106,v106
EX v107,v107
EX v108,v108
EX v109,v109
EX v110,v110
EX v111,v111
CV v64,v64,v65
CV v65,v66,v67
CV v66,v68,v69
CV v67,v70,v71
CV v72,v72,v73
CV v73,v74,v75
CV v74,v76,v77
CV v75,v78,v79
CV v68,v96,v97
CV v69,v98,v99
CV v70,v100,v101
CV v71,v102,v103
CV v76,v104,v105
CV v77,v106,v107
CV v78,v108,v109
CV v79,v110,v111
v_smfmac_f32_16x16x64_bf16 v[144:147], v[236:239], v[64:71], v234
s_nop 0
v_smfmac_f32_16x16x64_bf16 v[148:151], v[236:239], v[72:79], v234
s_nop 9
s_branch LRET_%=
LRARE004_%=:
s_nop 15
s_waitcnt vmcnt(0) lgkmcnt(0)
v_add_u32 v249, s74, v224
RD a[192:195],v249 offset:0
RD a[208:211],v249 offset:4096
RD a[224:227],v249 offset:8192
RD a[240:243],v249 offset:12288
v_add_u32 v250, s74, v225
RD a[196:199],v250 offset:0
RD a[212:215],v250 offset:4096
RD a[228:231],v250 offset:8192
RD a[244:247],v250 offset:12288
v_add_u32 v251, s74, v226
RD a[200:203],v251 offset:0
RD a[216:219],v251 offset:4096
RD a[232:235],v251 offset:8192
RD a[248:251],v251 offset:12288
v_add_u32 v254, s74, v227
RD a[204:207],v254 offset:0
RD a[220:223],v254 offset:4096
RD a[236:239],v254 offset:8192
RD a[252:255],v254 offset:12288
s_waitcnt lgkmcnt(0)
MM v[0:3],a[192:195],v[160:163],v[128:131]
MM v[0:3],a[196:199],v[176:179],v[0:3]
MM v[0:3],a[200:203],v[192:195],v[0:3]
MM v[0:3],a[204:207],v[208:211],v[0:3]
MM v[4:7],a[220:223],v[208:211],v[128:131]
MM v[4:7],a[216:219],v[192:195],v[4:7]
MM v[4:7],a[212:215],v[176:179],v[4:7]
MM v[4:7],a[208:211],v[160:163],v[4:7]
MM v[12:15],a[208:211],v[164:167],v[132:135]
MM v[12:15],a[212:215],v[180:183],v[12:15]
MM v[12:15],a[216:219],v[196:199],v[12:15]
MM v[12:15],a[220:223],v[212:215],v[12:15]
MM v[8:11],a[204:207],v[212:215],v[132:135]
MM v[8:11],a[200:203],v[196:199],v[8:11]
MM v[8:11],a[196:199],v[180:183],v[8:11]
MM v[8:11],a[192:195],v[164:167],v[8:11]
MM v[16:19],a[192:195],v[168:171],v[136:139]
MM v[16:19],a[196:199],v[184:187],v[16:19]
MM v[16:19],a[200:203],v[200:203],v[16:19]
MM v[16:19],a[204:207],v[216:219],v[16:19]
MM v[20:23],a[220:223],v[216:219],v[136:139]
MM v[20:23],a[216:219],v[200:203],v[20:23]
MM v[20:23],a[212:215],v[184:187],v[20:23]
MM v[20:23],a[208:211],v[168:171],v[20:23]
MM v[28:31],a[208:211],v[172:175],v[140:143]
MM v[28:31],a[212:215],v[188:191],v[28:31]
MM v[28:31],a[216:219],v[204:207],v[28:31]
MM v[28:31],a[220:223],v[220:223],v[28:31]
MM v[24:27],a[204:207],v[220:223],v[140:143]
MM v[24:27],a[200:203],v[204:207],v[24:27]
MM v[24:27],a[196:199],v[188:191],v[24:27]
MM v[24:27],a[192:195],v[172:175],v[24:27]
MM v[32:35],a[224:227],v[160:163],v[128:131]
MM v[32:35],a[228:231],v[176:179],v[32:35]
MM v[32:35],a[232:235],v[192:195],v[32:35]
MM v[32:35],a[236:239],v[208:211],v[32:35]
MM v[36:39],a[252:255],v[208:211],v[128:131]
MM v[36:39],a[248:251],v[192:195],v[36:39]
MM v[36:39],a[244:247],v[176:179],v[36:39]
MM v[36:39],a[240:243],v[160:163],v[36:39]
MM v[44:47],a[240:243],v[164:167],v[132:135]
MM v[44:47],a[244:247],v[180:183],v[44:47]
MM v[44:47],a[248:251],v[196:199],v[44:47]
MM v[44:47],a[252:255],v[212:215],v[44:47]
MM v[40:43],a[236:239],v[212:215],v[132:135]
MM v[40:43],a[232:235],v[196:199],v[40:43]
MM v[40:43],a[228:231],v[180:183],v[40:43]
MM v[40:43],a[224:227],v[164:167],v[40:43]
MM v[48:51],a[224:227],v[168:171],v[136:139]
MM v[48:51],a[228:231],v[184:187],v[48:51]
MM v[48:51],a[232:235],v[200:203],v[48:51]
MM v[48:51],a[236:239],v[216:219],v[48:51]
MM v[52:55],a[252:255],v[216:219],v[136:139]
MM v[52:55],a[248:251],v[200:203],v[52:55]
MM v[52:55],a[244:247],v[184:187],v[52:55]
MM v[52:55],a[240:243],v[168:171],v[52:55]
MM v[60:63],a[240:243],v[172:175],v[140:143]
MM v[60:63],a[244:247],v[188:191],v[60:63]
MM v[60:63],a[248:251],v[204:207],v[60:63]
MM v[60:63],a[252:255],v[220:223],v[60:63]
MM v[56:59],a[236:239],v[220:223],v[140:143]
MM v[56:59],a[232:235],v[204:207],v[56:59]
MM v[56:59],a[228:231],v[188:191],v[56:59]
MM v[56:59],a[224:227],v[172:175],v[56:59]
s_nop 15
v_add_u32 v249, s75, v224
RD a[192:195],v249 offset:0
RD a[208:211],v249 offset:4096
RD a[224:227],v249 offset:8192
RD a[240:243],v249 offset:12288
v_add_u32 v250, s75, v225
RD a[196:199],v250 offset:0
RD a[212:215],v250 offset:4096
RD a[228:231],v250 offset:8192
RD a[244:247],v250 offset:12288
v_add_u32 v251, s75, v226
RD a[200:203],v251 offset:0
RD a[216:219],v251 offset:4096
RD a[232:235],v251 offset:8192
RD a[248:251],v251 offset:12288
v_add_u32 v254, s75, v227
RD a[204:207],v254 offset:0
RD a[220:223],v254 offset:4096
RD a[236:239],v254 offset:8192
RD a[252:255],v254 offset:12288
s_waitcnt lgkmcnt(0)
v_max3_f32 v249, v0, v1, v2
v_max3_f32 v251, v32, v33, v34
v_max3_f32 v249, v249, v3, v4
v_max3_f32 v251, v251, v35, v36
v_max3_f32 v249, v249, v5, v6
v_max3_f32 v251, v251, v37, v38
v_max3_f32 v249, v249, v7, v39
v_max_f32 v249, v249, v251
v_mov_b32 v251, v249
s_nop 1
v_permlane16_swap_b32 v249, v251
v_max_f32 v249, v249, v251
v_mov_b32 v251, v249
s_nop 1
v_permlane32_swap_b32 v249, v251
v_max_f32 v249, v249, v251
v_log_f32 v250, v244
s_nop 0
v_max3_f32 v249, 0, v249, v250
v_sub_f32 v250, 0, v249
EX v250,v250
v_sub_f32 v128, v128, v249
v_sub_f32 v129, v129, v249
v_sub_f32 v130, v130, v249
v_sub_f32 v131, v131, v249
v_sub_f32 v64, v64, v249
v_sub_f32 v0, v0, v249
v_sub_f32 v65, v65, v249
v_sub_f32 v1, v1, v249
v_sub_f32 v66, v66, v249
v_sub_f32 v2, v2, v249
v_sub_f32 v67, v67, v249
v_sub_f32 v3, v3, v249
v_sub_f32 v68, v68, v249
v_sub_f32 v4, v4, v249
v_sub_f32 v69, v69, v249
v_sub_f32 v5, v5, v249
v_sub_f32 v70, v70, v249
v_sub_f32 v6, v6, v249
v_sub_f32 v71, v71, v249
v_sub_f32 v7, v7, v249
v_sub_f32 v96, v96, v249
v_sub_f32 v32, v32, v249
v_sub_f32 v97, v97, v249
v_sub_f32 v33, v33, v249
v_sub_f32 v98, v98, v249
v_sub_f32 v34, v34, v249
v_sub_f32 v99, v99, v249
v_sub_f32 v35, v35, v249
v_sub_f32 v100, v100, v249
v_sub_f32 v36, v36, v249
v_sub_f32 v101, v101, v249
v_sub_f32 v37, v37, v249
v_sub_f32 v102, v102, v249
v_sub_f32 v38, v38, v249
v_sub_f32 v103, v103, v249
v_sub_f32 v39, v39, v249
v_mul_f32 v144, v244, v250
v_mov_b32 v145, 0
v_bfe_u32 v249, v241, 0, 8
v_lshl_or_b32 v249, v249, 1, 1
v_lshlrev_b32 v249, 22, v249
v_and_b32 v241, 0xffffff00, v241
AR v254,a0
v_sub_f32 v254, v254, v249
v_mul_f32 v254, v254, v250
AW a0,v254
AR v251,a1
v_sub_f32 v251, v251, v249
v_mul_f32 v251, v251, v250
AW a1,v251
AR v254,a2
v_sub_f32 v254, v254, v249
v_mul_f32 v254, v254, v250
AW a2,v254
AR v251,a3
v_sub_f32 v251, v251, v249
v_mul_f32 v251, v251, v250
AW a3,v251
AR v254,a4
v_sub_f32 v254, v254, v249
v_mul_f32 v254, v254, v250
AW a4,v254
AR v251,a5
v_sub_f32 v251, v251, v249
v_mul_f32 v251, v251, v250
AW a5,v251
AR v254,a6
v_sub_f32 v254, v254, v249
v_mul_f32 v254, v254, v250
AW a6,v254
AR v251,a7
v_sub_f32 v251, v251, v249
v_mul_f32 v251, v251, v250
AW a7,v251
AR v254,a32
v_sub_f32 v254, v254, v249
v_mul_f32 v254, v254, v250
AW a32,v254
AR v251,a33
v_sub_f32 v251, v251, v249
v_mul_f32 v251, v251, v250
AW a33,v251
AR v254,a34
v_sub_f32 v254, v254, v249
v_mul_f32 v254, v254, v250
AW a34,v254
AR v251,a35
v_sub_f32 v251, v251, v249
v_mul_f32 v251, v251, v250
AW a35,v251
AR v254,a36
v_sub_f32 v254, v254, v249
v_mul_f32 v254, v254, v250
AW a36,v254
AR v251,a37
v_sub_f32 v251, v251, v249
v_mul_f32 v251, v251, v250
AW a37,v251
AR v254,a38
v_sub_f32 v254, v254, v249
v_mul_f32 v254, v254, v250
AW a38,v254
AR v251,a39
v_sub_f32 v251, v251, v249
v_mul_f32 v251, v251, v250
AW a39,v251
AR v254,a64
v_sub_f32 v254, v254, v249
v_mul_f32 v254, v254, v250
AW a64,v254
AR v251,a65
v_sub_f32 v251, v251, v249
v_mul_f32 v251, v251, v250
)ASM"
R"ASM(
AW a65,v251
AR v254,a66
v_sub_f32 v254, v254, v249
v_mul_f32 v254, v254, v250
AW a66,v254
AR v251,a67
v_sub_f32 v251, v251, v249
v_mul_f32 v251, v251, v250
AW a67,v251
AR v254,a68
v_sub_f32 v254, v254, v249
v_mul_f32 v254, v254, v250
AW a68,v254
AR v251,a69
v_sub_f32 v251, v251, v249
v_mul_f32 v251, v251, v250
AW a69,v251
AR v254,a70
v_sub_f32 v254, v254, v249
v_mul_f32 v254, v254, v250
AW a70,v254
AR v251,a71
v_sub_f32 v251, v251, v249
v_mul_f32 v251, v251, v250
AW a71,v251
AR v254,a96
v_sub_f32 v254, v254, v249
v_mul_f32 v254, v254, v250
AW a96,v254
AR v251,a97
v_sub_f32 v251, v251, v249
v_mul_f32 v251, v251, v250
AW a97,v251
AR v254,a98
v_sub_f32 v254, v254, v249
v_mul_f32 v254, v254, v250
AW a98,v254
AR v251,a99
v_sub_f32 v251, v251, v249
v_mul_f32 v251, v251, v250
AW a99,v251
AR v254,a100
v_sub_f32 v254, v254, v249
v_mul_f32 v254, v254, v250
AW a100,v254
AR v251,a101
v_sub_f32 v251, v251, v249
v_mul_f32 v251, v251, v250
AW a101,v251
AR v254,a102
v_sub_f32 v254, v254, v249
v_mul_f32 v254, v254, v250
AW a102,v254
AR v251,a103
v_sub_f32 v251, v251, v249
v_mul_f32 v251, v251, v250
AW a103,v251
v_max3_f32 v249, v8, v9, v10
v_max3_f32 v251, v40, v41, v42
v_max3_f32 v249, v249, v11, v12
v_max3_f32 v251, v251, v43, v44
v_max3_f32 v249, v249, v13, v14
v_max3_f32 v251, v251, v45, v46
v_max3_f32 v249, v249, v15, v47
v_max_f32 v249, v249, v251
v_mov_b32 v251, v249
s_nop 1
v_permlane16_swap_b32 v249, v251
v_max_f32 v249, v249, v251
v_mov_b32 v251, v249
s_nop 1
v_permlane32_swap_b32 v249, v251
v_max_f32 v249, v249, v251
v_log_f32 v250, v245
s_nop 0
v_max3_f32 v249, 0, v249, v250
v_sub_f32 v250, 0, v249
EX v250,v250
v_sub_f32 v132, v132, v249
v_sub_f32 v133, v133, v249
v_sub_f32 v134, v134, v249
v_sub_f32 v135, v135, v249
v_sub_f32 v72, v72, v249
v_sub_f32 v8, v8, v249
v_sub_f32 v73, v73, v249
v_sub_f32 v9, v9, v249
v_sub_f32 v74, v74, v249
v_sub_f32 v10, v10, v249
v_sub_f32 v75, v75, v249
v_sub_f32 v11, v11, v249
v_sub_f32 v76, v76, v249
v_sub_f32 v12, v12, v249
v_sub_f32 v77, v77, v249
v_sub_f32 v13, v13, v249
v_sub_f32 v78, v78, v249
v_sub_f32 v14, v14, v249
v_sub_f32 v79, v79, v249
v_sub_f32 v15, v15, v249
v_sub_f32 v104, v104, v249
v_sub_f32 v40, v40, v249
v_sub_f32 v105, v105, v249
v_sub_f32 v41, v41, v249
v_sub_f32 v106, v106, v249
v_sub_f32 v42, v42, v249
v_sub_f32 v107, v107, v249
v_sub_f32 v43, v43, v249
v_sub_f32 v108, v108, v249
v_sub_f32 v44, v44, v249
v_sub_f32 v109, v109, v249
v_sub_f32 v45, v45, v249
v_sub_f32 v110, v110, v249
v_sub_f32 v46, v46, v249
v_sub_f32 v111, v111, v249
v_sub_f32 v47, v47, v249
v_mul_f32 v148, v245, v250
v_mov_b32 v149, 0
v_bfe_u32 v249, v241, 8, 8
v_lshl_or_b32 v249, v249, 1, 1
v_lshlrev_b32 v249, 22, v249
v_and_b32 v241, 0xffff00ff, v241
AR v254,a8
v_sub_f32 v254, v254, v249
v_mul_f32 v254, v254, v250
AW a8,v254
AR v251,a9
v_sub_f32 v251, v251, v249
v_mul_f32 v251, v251, v250
AW a9,v251
AR v254,a10
v_sub_f32 v254, v254, v249
v_mul_f32 v254, v254, v250
AW a10,v254
AR v251,a11
v_sub_f32 v251, v251, v249
v_mul_f32 v251, v251, v250
AW a11,v251
AR v254,a12
v_sub_f32 v254, v254, v249
v_mul_f32 v254, v254, v250
AW a12,v254
AR v251,a13
v_sub_f32 v251, v251, v249
v_mul_f32 v251, v251, v250
AW a13,v251
AR v254,a14
v_sub_f32 v254, v254, v249
v_mul_f32 v254, v254, v250
AW a14,v254
AR v251,a15
v_sub_f32 v251, v251, v249
v_mul_f32 v251, v251, v250
AW a15,v251
AR v254,a40
v_sub_f32 v254, v254, v249
v_mul_f32 v254, v254, v250
AW a40,v254
AR v251,a41
v_sub_f32 v251, v251, v249
v_mul_f32 v251, v251, v250
AW a41,v251
AR v254,a42
v_sub_f32 v254, v254, v249
v_mul_f32 v254, v254, v250
AW a42,v254
AR v251,a43
v_sub_f32 v251, v251, v249
v_mul_f32 v251, v251, v250
AW a43,v251
AR v254,a44
v_sub_f32 v254, v254, v249
v_mul_f32 v254, v254, v250
AW a44,v254
AR v251,a45
v_sub_f32 v251, v251, v249
v_mul_f32 v251, v251, v250
AW a45,v251
AR v254,a46
v_sub_f32 v254, v254, v249
v_mul_f32 v254, v254, v250
AW a46,v254
AR v251,a47
v_sub_f32 v251, v251, v249
v_mul_f32 v251, v251, v250
AW a47,v251
AR v254,a72
v_sub_f32 v254, v254, v249
v_mul_f32 v254, v254, v250
AW a72,v254
AR v251,a73
v_sub_f32 v251, v251, v249
v_mul_f32 v251, v251, v250
AW a73,v251
AR v254,a74
v_sub_f32 v254, v254, v249
v_mul_f32 v254, v254, v250
AW a74,v254
AR v251,a75
v_sub_f32 v251, v251, v249
v_mul_f32 v251, v251, v250
AW a75,v251
AR v254,a76
v_sub_f32 v254, v254, v249
v_mul_f32 v254, v254, v250
AW a76,v254
AR v251,a77
v_sub_f32 v251, v251, v249
v_mul_f32 v251, v251, v250
AW a77,v251
AR v254,a78
v_sub_f32 v254, v254, v249
v_mul_f32 v254, v254, v250
AW a78,v254
AR v251,a79
v_sub_f32 v251, v251, v249
v_mul_f32 v251, v251, v250
AW a79,v251
AR v254,a104
v_sub_f32 v254, v254, v249
v_mul_f32 v254, v254, v250
AW a104,v254
AR v251,a105
v_sub_f32 v251, v251, v249
v_mul_f32 v251, v251, v250
AW a105,v251
AR v254,a106
v_sub_f32 v254, v254, v249
v_mul_f32 v254, v254, v250
AW a106,v254
AR v251,a107
v_sub_f32 v251, v251, v249
v_mul_f32 v251, v251, v250
AW a107,v251
AR v254,a108
v_sub_f32 v254, v254, v249
v_mul_f32 v254, v254, v250
AW a108,v254
AR v251,a109
v_sub_f32 v251, v251, v249
v_mul_f32 v251, v251, v250
AW a109,v251
AR v254,a110
v_sub_f32 v254, v254, v249
v_mul_f32 v254, v254, v250
AW a110,v254
AR v251,a111
v_sub_f32 v251, v251, v249
v_mul_f32 v251, v251, v250
AW a111,v251
v_max3_f32 v249, v16, v17, v18
v_max3_f32 v251, v48, v49, v50
v_max3_f32 v249, v249, v19, v20
v_max3_f32 v251, v251, v51, v52
v_max3_f32 v249, v249, v21, v22
v_max3_f32 v251, v251, v53, v54
v_max3_f32 v249, v249, v23, v55
v_max_f32 v249, v249, v251
v_mov_b32 v251, v249
s_nop 1
v_permlane16_swap_b32 v249, v251
v_max_f32 v249, v249, v251
v_mov_b32 v251, v249
s_nop 1
v_permlane32_swap_b32 v249, v251
v_max_f32 v249, v249, v251
v_log_f32 v250, v246
s_nop 0
v_max3_f32 v249, 0, v249, v250
v_sub_f32 v250, 0, v249
EX v250,v250
v_sub_f32 v136, v136, v249
v_sub_f32 v137, v137, v249
v_sub_f32 v138, v138, v249
v_sub_f32 v139, v139, v249
v_sub_f32 v80, v80, v249
v_sub_f32 v16, v16, v249
v_sub_f32 v81, v81, v249
v_sub_f32 v17, v17, v249
v_sub_f32 v82, v82, v249
v_sub_f32 v18, v18, v249
v_sub_f32 v83, v83, v249
v_sub_f32 v19, v19, v249
v_sub_f32 v84, v84, v249
v_sub_f32 v20, v20, v249
v_sub_f32 v85, v85, v249
v_sub_f32 v21, v21, v249
v_sub_f32 v86, v86, v249
v_sub_f32 v22, v22, v249
v_sub_f32 v87, v87, v249
v_sub_f32 v23, v23, v249
v_sub_f32 v112, v112, v249
v_sub_f32 v48, v48, v249
v_sub_f32 v113, v113, v249
v_sub_f32 v49, v49, v249
v_sub_f32 v114, v114, v249
v_sub_f32 v50, v50, v249
v_sub_f32 v115, v115, v249
v_sub_f32 v51, v51, v249
v_sub_f32 v116, v116, v249
v_sub_f32 v52, v52, v249
v_sub_f32 v117, v117, v249
v_sub_f32 v53, v53, v249
v_sub_f32 v118, v118, v249
v_sub_f32 v54, v54, v249
v_sub_f32 v119, v119, v249
v_sub_f32 v55, v55, v249
v_mul_f32 v152, v246, v250
v_mov_b32 v153, 0
v_bfe_u32 v249, v241, 16, 8
v_lshl_or_b32 v249, v249, 1, 1
v_lshlrev_b32 v249, 22, v249
v_and_b32 v241, 0xff00ffff, v241
AR v254,a16
v_sub_f32 v254, v254, v249
v_mul_f32 v254, v254, v250
AW a16,v254
AR v251,a17
v_sub_f32 v251, v251, v249
v_mul_f32 v251, v251, v250
AW a17,v251
AR v254,a18
v_sub_f32 v254, v254, v249
v_mul_f32 v254, v254, v250
AW a18,v254
AR v251,a19
v_sub_f32 v251, v251, v249
v_mul_f32 v251, v251, v250
AW a19,v251
AR v254,a20
v_sub_f32 v254, v254, v249
v_mul_f32 v254, v254, v250
AW a20,v254
AR v251,a21
v_sub_f32 v251, v251, v249
v_mul_f32 v251, v251, v250
AW a21,v251
AR v254,a22
v_sub_f32 v254, v254, v249
v_mul_f32 v254, v254, v250
AW a22,v254
AR v251,a23
v_sub_f32 v251, v251, v249
v_mul_f32 v251, v251, v250
AW a23,v251
AR v254,a48
v_sub_f32 v254, v254, v249
v_mul_f32 v254, v254, v250
AW a48,v254
AR v251,a49
v_sub_f32 v251, v251, v249
v_mul_f32 v251, v251, v250
AW a49,v251
AR v254,a50
v_sub_f32 v254, v254, v249
v_mul_f32 v254, v254, v250
AW a50,v254
AR v251,a51
v_sub_f32 v251, v251, v249
v_mul_f32 v251, v251, v250
AW a51,v251
AR v254,a52
v_sub_f32 v254, v254, v249
v_mul_f32 v254, v254, v250
AW a52,v254
AR v251,a53
v_sub_f32 v251, v251, v249
v_mul_f32 v251, v251, v250
AW a53,v251
AR v254,a54
v_sub_f32 v254, v254, v249
v_mul_f32 v254, v254, v250
AW a54,v254
AR v251,a55
v_sub_f32 v251, v251, v249
v_mul_f32 v251, v251, v250
AW a55,v251
AR v254,a80
v_sub_f32 v254, v254, v249
v_mul_f32 v254, v254, v250
AW a80,v254
AR v251,a81
v_sub_f32 v251, v251, v249
v_mul_f32 v251, v251, v250
AW a81,v251
AR v254,a82
v_sub_f32 v254, v254, v249
v_mul_f32 v254, v254, v250
AW a82,v254
AR v251,a83
v_sub_f32 v251, v251, v249
v_mul_f32 v251, v251, v250
AW a83,v251
AR v254,a84
v_sub_f32 v254, v254, v249
v_mul_f32 v254, v254, v250
AW a84,v254
AR v251,a85
v_sub_f32 v251, v251, v249
v_mul_f32 v251, v251, v250
AW a85,v251
AR v254,a86
v_sub_f32 v254, v254, v249
v_mul_f32 v254, v254, v250
AW a86,v254
AR v251,a87
v_sub_f32 v251, v251, v249
v_mul_f32 v251, v251, v250
AW a87,v251
AR v254,a112
v_sub_f32 v254, v254, v249
v_mul_f32 v254, v254, v250
AW a112,v254
AR v251,a113
v_sub_f32 v251, v251, v249
v_mul_f32 v251, v251, v250
AW a113,v251
AR v254,a114
v_sub_f32 v254, v254, v249
v_mul_f32 v254, v254, v250
AW a114,v254
AR v251,a115
v_sub_f32 v251, v251, v249
v_mul_f32 v251, v251, v250
AW a115,v251
AR v254,a116
v_sub_f32 v254, v254, v249
v_mul_f32 v254, v254, v250
AW a116,v254
AR v251,a117
v_sub_f32 v251, v251, v249
v_mul_f32 v251, v251, v250
AW a117,v251
AR v254,a118
v_sub_f32 v254, v254, v249
v_mul_f32 v254, v254, v250
AW a118,v254
AR v251,a119
v_sub_f32 v251, v251, v249
v_mul_f32 v251, v251, v250
AW a119,v251
v_max3_f32 v249, v24, v25, v26
v_max3_f32 v251, v56, v57, v58
v_max3_f32 v249, v249, v27, v28
v_max3_f32 v251, v251, v59, v60
v_max3_f32 v249, v249, v29, v30
v_max3_f32 v251, v251, v61, v62
v_max3_f32 v249, v249, v31, v63
v_max_f32 v249, v249, v251
v_mov_b32 v251, v249
s_nop 1
v_permlane16_swap_b32 v249, v251
v_max_f32 v249, v249, v251
v_mov_b32 v251, v249
s_nop 1
v_permlane32_swap_b32 v249, v251
v_max_f32 v249, v249, v251
v_log_f32 v250, v247
s_nop 0
v_max3_f32 v249, 0, v249, v250
v_sub_f32 v250, 0, v249
EX v250,v250
v_sub_f32 v140, v140, v249
v_sub_f32 v141, v141, v249
v_sub_f32 v142, v142, v249
v_sub_f32 v143, v143, v249
v_sub_f32 v88, v88, v249
v_sub_f32 v24, v24, v249
v_sub_f32 v89, v89, v249
v_sub_f32 v25, v25, v249
v_sub_f32 v90, v90, v249
v_sub_f32 v26, v26, v249
v_sub_f32 v91, v91, v249
v_sub_f32 v27, v27, v249
v_sub_f32 v92, v92, v249
v_sub_f32 v28, v28, v249
v_sub_f32 v93, v93, v249
v_sub_f32 v29, v29, v249
v_sub_f32 v94, v94, v249
v_sub_f32 v30, v30, v249
v_sub_f32 v95, v95, v249
v_sub_f32 v31, v31, v249
v_sub_f32 v120, v120, v249
v_sub_f32 v56, v56, v249
v_sub_f32 v121, v121, v249
v_sub_f32 v57, v57, v249
v_sub_f32 v122, v122, v249
v_sub_f32 v58, v58, v249
v_sub_f32 v123, v123, v249
v_sub_f32 v59, v59, v249
v_sub_f32 v124, v124, v249
v_sub_f32 v60, v60, v249
v_sub_f32 v125, v125, v249
v_sub_f32 v61, v61, v249
v_sub_f32 v126, v126, v249
v_sub_f32 v62, v62, v249
v_sub_f32 v127, v127, v249
v_sub_f32 v63, v63, v249
v_mul_f32 v156, v247, v250
v_mov_b32 v157, 0
v_bfe_u32 v249, v241, 24, 8
v_lshl_or_b32 v249, v249, 1, 1
v_lshlrev_b32 v249, 22, v249
v_and_b32 v241, 0xffffff, v241
AR v254,a24
v_sub_f32 v254, v254, v249
v_mul_f32 v254, v254, v250
AW a24,v254
AR v251,a25
v_sub_f32 v251, v251, v249
v_mul_f32 v251, v251, v250
AW a25,v251
AR v254,a26
v_sub_f32 v254, v254, v249
v_mul_f32 v254, v254, v250
AW a26,v254
AR v251,a27
v_sub_f32 v251, v251, v249
v_mul_f32 v251, v251, v250
AW a27,v251
AR v254,a28
v_sub_f32 v254, v254, v249
v_mul_f32 v254, v254, v250
AW a28,v254
AR v251,a29
v_sub_f32 v251, v251, v249
v_mul_f32 v251, v251, v250
AW a29,v251
AR v254,a30
v_sub_f32 v254, v254, v249
v_mul_f32 v254, v254, v250
AW a30,v254
AR v251,a31
v_sub_f32 v251, v251, v249
v_mul_f32 v251, v251, v250
AW a31,v251
AR v254,a56
v_sub_f32 v254, v254, v249
v_mul_f32 v254, v254, v250
AW a56,v254
AR v251,a57
v_sub_f32 v251, v251, v249
v_mul_f32 v251, v251, v250
AW a57,v251
AR v254,a58
v_sub_f32 v254, v254, v249
v_mul_f32 v254, v254, v250
AW a58,v254
AR v251,a59
v_sub_f32 v251, v251, v249
v_mul_f32 v251, v251, v250
AW a59,v251
AR v254,a60
v_sub_f32 v254, v254, v249
v_mul_f32 v254, v254, v250
AW a60,v254
AR v251,a61
v_sub_f32 v251, v251, v249
)ASM"
R"ASM(
v_mul_f32 v251, v251, v250
AW a61,v251
AR v254,a62
v_sub_f32 v254, v254, v249
v_mul_f32 v254, v254, v250
AW a62,v254
AR v251,a63
v_sub_f32 v251, v251, v249
v_mul_f32 v251, v251, v250
AW a63,v251
AR v254,a88
v_sub_f32 v254, v254, v249
v_mul_f32 v254, v254, v250
AW a88,v254
AR v251,a89
v_sub_f32 v251, v251, v249
v_mul_f32 v251, v251, v250
AW a89,v251
AR v254,a90
v_sub_f32 v254, v254, v249
v_mul_f32 v254, v254, v250
AW a90,v254
AR v251,a91
v_sub_f32 v251, v251, v249
v_mul_f32 v251, v251, v250
AW a91,v251
AR v254,a92
v_sub_f32 v254, v254, v249
v_mul_f32 v254, v254, v250
AW a92,v254
AR v251,a93
v_sub_f32 v251, v251, v249
v_mul_f32 v251, v251, v250
AW a93,v251
AR v254,a94
v_sub_f32 v254, v254, v249
v_mul_f32 v254, v254, v250
AW a94,v254
AR v251,a95
v_sub_f32 v251, v251, v249
v_mul_f32 v251, v251, v250
AW a95,v251
AR v254,a120
v_sub_f32 v254, v254, v249
v_mul_f32 v254, v254, v250
AW a120,v254
AR v251,a121
v_sub_f32 v251, v251, v249
v_mul_f32 v251, v251, v250
AW a121,v251
AR v254,a122
v_sub_f32 v254, v254, v249
v_mul_f32 v254, v254, v250
AW a122,v254
AR v251,a123
v_sub_f32 v251, v251, v249
v_mul_f32 v251, v251, v250
AW a123,v251
AR v254,a124
v_sub_f32 v254, v254, v249
v_mul_f32 v254, v254, v250
AW a124,v254
AR v251,a125
v_sub_f32 v251, v251, v249
v_mul_f32 v251, v251, v250
AW a125,v251
AR v254,a126
v_sub_f32 v254, v254, v249
v_mul_f32 v254, v254, v250
AW a126,v254
AR v251,a127
v_sub_f32 v251, v251, v249
v_mul_f32 v251, v251, v250
AW a127,v251
EX v0,v0
EX v1,v1
EX v2,v2
EX v3,v3
EX v4,v4
EX v5,v5
EX v6,v6
EX v7,v7
EX v8,v8
EX v9,v9
EX v10,v10
EX v11,v11
EX v12,v12
EX v13,v13
EX v14,v14
EX v15,v15
EX v16,v16
EX v17,v17
EX v18,v18
EX v19,v19
EX v20,v20
EX v21,v21
EX v22,v22
EX v23,v23
EX v24,v24
EX v25,v25
EX v26,v26
EX v27,v27
EX v28,v28
EX v29,v29
EX v30,v30
EX v31,v31
EX v32,v32
EX v33,v33
EX v34,v34
EX v35,v35
EX v36,v36
EX v37,v37
EX v38,v38
EX v39,v39
EX v40,v40
EX v41,v41
EX v42,v42
EX v43,v43
EX v44,v44
EX v45,v45
EX v46,v46
EX v47,v47
EX v48,v48
EX v49,v49
EX v50,v50
EX v51,v51
EX v52,v52
EX v53,v53
EX v54,v54
EX v55,v55
EX v56,v56
EX v57,v57
EX v58,v58
EX v59,v59
EX v60,v60
EX v61,v61
EX v62,v62
EX v63,v63
CV v0,v0,v1
CV v1,v2,v3
CV v2,v4,v5
CV v3,v6,v7
CV v8,v8,v9
CV v9,v10,v11
CV v10,v12,v13
CV v11,v14,v15
CV v16,v16,v17
CV v17,v18,v19
CV v18,v20,v21
CV v19,v22,v23
CV v24,v24,v25
CV v25,v26,v27
CV v26,v28,v29
CV v27,v30,v31
CV v4,v32,v33
CV v5,v34,v35
CV v6,v36,v37
CV v7,v38,v39
CV v12,v40,v41
CV v13,v42,v43
CV v14,v44,v45
CV v15,v46,v47
CV v20,v48,v49
CV v21,v50,v51
CV v22,v52,v53
CV v23,v54,v55
CV v28,v56,v57
CV v29,v58,v59
CV v30,v60,v61
CV v31,v62,v63
v_smfmac_f32_16x16x64_bf16 v[144:147], v[236:239], v[0:7], v234
v_smfmac_f32_16x16x64_bf16 v[148:151], v[236:239], v[8:15], v234
v_smfmac_f32_16x16x64_bf16 v[152:155], v[236:239], v[16:23], v234
v_smfmac_f32_16x16x64_bf16 v[156:159], v[236:239], v[24:31], v234
s_nop 9
s_branch LRET_%=
LRARE104_%=:
s_nop 15
s_waitcnt vmcnt(0) lgkmcnt(0)
v_add_u32 v249, s74, v224
RD a[192:195],v249 offset:0
RD a[208:211],v249 offset:4096
RD a[224:227],v249 offset:8192
RD a[240:243],v249 offset:12288
v_add_u32 v250, s74, v225
RD a[196:199],v250 offset:0
RD a[212:215],v250 offset:4096
RD a[228:231],v250 offset:8192
RD a[244:247],v250 offset:12288
v_add_u32 v251, s74, v226
RD a[200:203],v251 offset:0
RD a[216:219],v251 offset:4096
RD a[232:235],v251 offset:8192
RD a[248:251],v251 offset:12288
v_add_u32 v254, s74, v227
RD a[204:207],v254 offset:0
RD a[220:223],v254 offset:4096
RD a[236:239],v254 offset:8192
RD a[252:255],v254 offset:12288
s_waitcnt lgkmcnt(0)
MM v[64:67],a[192:195],v[160:163],v[128:131]
MM v[64:67],a[196:199],v[176:179],v[64:67]
MM v[64:67],a[200:203],v[192:195],v[64:67]
MM v[64:67],a[204:207],v[208:211],v[64:67]
MM v[68:71],a[220:223],v[208:211],v[128:131]
MM v[68:71],a[216:219],v[192:195],v[68:71]
MM v[68:71],a[212:215],v[176:179],v[68:71]
MM v[68:71],a[208:211],v[160:163],v[68:71]
MM v[76:79],a[208:211],v[164:167],v[132:135]
MM v[76:79],a[212:215],v[180:183],v[76:79]
MM v[76:79],a[216:219],v[196:199],v[76:79]
MM v[76:79],a[220:223],v[212:215],v[76:79]
MM v[72:75],a[204:207],v[212:215],v[132:135]
MM v[72:75],a[200:203],v[196:199],v[72:75]
MM v[72:75],a[196:199],v[180:183],v[72:75]
MM v[72:75],a[192:195],v[164:167],v[72:75]
MM v[80:83],a[192:195],v[168:171],v[136:139]
MM v[80:83],a[196:199],v[184:187],v[80:83]
MM v[80:83],a[200:203],v[200:203],v[80:83]
MM v[80:83],a[204:207],v[216:219],v[80:83]
MM v[84:87],a[220:223],v[216:219],v[136:139]
MM v[84:87],a[216:219],v[200:203],v[84:87]
MM v[84:87],a[212:215],v[184:187],v[84:87]
MM v[84:87],a[208:211],v[168:171],v[84:87]
MM v[92:95],a[208:211],v[172:175],v[140:143]
MM v[92:95],a[212:215],v[188:191],v[92:95]
MM v[92:95],a[216:219],v[204:207],v[92:95]
MM v[92:95],a[220:223],v[220:223],v[92:95]
MM v[88:91],a[204:207],v[220:223],v[140:143]
MM v[88:91],a[200:203],v[204:207],v[88:91]
MM v[88:91],a[196:199],v[188:191],v[88:91]
MM v[88:91],a[192:195],v[172:175],v[88:91]
MM v[96:99],a[224:227],v[160:163],v[128:131]
MM v[96:99],a[228:231],v[176:179],v[96:99]
MM v[96:99],a[232:235],v[192:195],v[96:99]
MM v[96:99],a[236:239],v[208:211],v[96:99]
MM v[100:103],a[252:255],v[208:211],v[128:131]
MM v[100:103],a[248:251],v[192:195],v[100:103]
MM v[100:103],a[244:247],v[176:179],v[100:103]
MM v[100:103],a[240:243],v[160:163],v[100:103]
MM v[108:111],a[240:243],v[164:167],v[132:135]
MM v[108:111],a[244:247],v[180:183],v[108:111]
MM v[108:111],a[248:251],v[196:199],v[108:111]
MM v[108:111],a[252:255],v[212:215],v[108:111]
MM v[104:107],a[236:239],v[212:215],v[132:135]
MM v[104:107],a[232:235],v[196:199],v[104:107]
MM v[104:107],a[228:231],v[180:183],v[104:107]
MM v[104:107],a[224:227],v[164:167],v[104:107]
MM v[112:115],a[224:227],v[168:171],v[136:139]
MM v[112:115],a[228:231],v[184:187],v[112:115]
MM v[112:115],a[232:235],v[200:203],v[112:115]
MM v[112:115],a[236:239],v[216:219],v[112:115]
MM v[116:119],a[252:255],v[216:219],v[136:139]
MM v[116:119],a[248:251],v[200:203],v[116:119]
MM v[116:119],a[244:247],v[184:187],v[116:119]
MM v[116:119],a[240:243],v[168:171],v[116:119]
MM v[124:127],a[240:243],v[172:175],v[140:143]
MM v[124:127],a[244:247],v[188:191],v[124:127]
MM v[124:127],a[248:251],v[204:207],v[124:127]
MM v[124:127],a[252:255],v[220:223],v[124:127]
MM v[120:123],a[236:239],v[220:223],v[140:143]
MM v[120:123],a[232:235],v[204:207],v[120:123]
MM v[120:123],a[228:231],v[188:191],v[120:123]
MM v[120:123],a[224:227],v[172:175],v[120:123]
s_nop 15
v_add_u32 v249, s75, v224
RD a[192:195],v249 offset:0
RD a[208:211],v249 offset:4096
RD a[224:227],v249 offset:8192
RD a[240:243],v249 offset:12288
v_add_u32 v250, s75, v225
RD a[196:199],v250 offset:0
RD a[212:215],v250 offset:4096
RD a[228:231],v250 offset:8192
RD a[244:247],v250 offset:12288
v_add_u32 v251, s75, v226
RD a[200:203],v251 offset:0
RD a[216:219],v251 offset:4096
RD a[232:235],v251 offset:8192
RD a[248:251],v251 offset:12288
v_add_u32 v254, s75, v227
RD a[204:207],v254 offset:0
RD a[220:223],v254 offset:4096
RD a[236:239],v254 offset:8192
RD a[252:255],v254 offset:12288
s_waitcnt lgkmcnt(0)
v_max3_f32 v249, v64, v65, v66
v_max3_f32 v251, v96, v97, v98
v_max3_f32 v249, v249, v67, v68
v_max3_f32 v251, v251, v99, v100
v_max3_f32 v249, v249, v69, v70
v_max3_f32 v251, v251, v101, v102
v_max3_f32 v249, v249, v71, v103
v_max_f32 v249, v249, v251
v_mov_b32 v251, v249
s_nop 1
v_permlane16_swap_b32 v249, v251
v_max_f32 v249, v249, v251
v_mov_b32 v251, v249
s_nop 1
v_permlane32_swap_b32 v249, v251
v_max_f32 v249, v249, v251
v_log_f32 v250, v244
s_nop 0
v_max3_f32 v249, 0, v249, v250
v_sub_f32 v250, 0, v249
EX v250,v250
v_sub_f32 v128, v128, v249
v_sub_f32 v129, v129, v249
v_sub_f32 v130, v130, v249
v_sub_f32 v131, v131, v249
v_sub_f32 v0, v0, v249
v_sub_f32 v64, v64, v249
v_sub_f32 v1, v1, v249
v_sub_f32 v65, v65, v249
v_sub_f32 v2, v2, v249
v_sub_f32 v66, v66, v249
v_sub_f32 v3, v3, v249
v_sub_f32 v67, v67, v249
v_sub_f32 v4, v4, v249
v_sub_f32 v68, v68, v249
v_sub_f32 v5, v5, v249
v_sub_f32 v69, v69, v249
v_sub_f32 v6, v6, v249
v_sub_f32 v70, v70, v249
v_sub_f32 v7, v7, v249
v_sub_f32 v71, v71, v249
v_sub_f32 v32, v32, v249
v_sub_f32 v96, v96, v249
v_sub_f32 v33, v33, v249
v_sub_f32 v97, v97, v249
v_sub_f32 v34, v34, v249
v_sub_f32 v98, v98, v249
v_sub_f32 v35, v35, v249
v_sub_f32 v99, v99, v249
v_sub_f32 v36, v36, v249
v_sub_f32 v100, v100, v249
v_sub_f32 v37, v37, v249
v_sub_f32 v101, v101, v249
v_sub_f32 v38, v38, v249
v_sub_f32 v102, v102, v249
v_sub_f32 v39, v39, v249
v_sub_f32 v103, v103, v249
v_mul_f32 v144, v244, v250
v_mov_b32 v145, 0
v_bfe_u32 v249, v241, 0, 8
v_lshl_or_b32 v249, v249, 1, 1
v_lshlrev_b32 v249, 22, v249
v_and_b32 v241, 0xffffff00, v241
AR v254,a0
v_sub_f32 v254, v254, v249
v_mul_f32 v254, v254, v250
AW a0,v254
AR v251,a1
v_sub_f32 v251, v251, v249
v_mul_f32 v251, v251, v250
AW a1,v251
AR v254,a2
v_sub_f32 v254, v254, v249
v_mul_f32 v254, v254, v250
AW a2,v254
AR v251,a3
v_sub_f32 v251, v251, v249
v_mul_f32 v251, v251, v250
AW a3,v251
AR v254,a4
v_sub_f32 v254, v254, v249
v_mul_f32 v254, v254, v250
AW a4,v254
AR v251,a5
v_sub_f32 v251, v251, v249
v_mul_f32 v251, v251, v250
AW a5,v251
AR v254,a6
v_sub_f32 v254, v254, v249
v_mul_f32 v254, v254, v250
AW a6,v254
AR v251,a7
v_sub_f32 v251, v251, v249
v_mul_f32 v251, v251, v250
AW a7,v251
AR v254,a32
v_sub_f32 v254, v254, v249
v_mul_f32 v254, v254, v250
AW a32,v254
AR v251,a33
v_sub_f32 v251, v251, v249
v_mul_f32 v251, v251, v250
AW a33,v251
AR v254,a34
v_sub_f32 v254, v254, v249
v_mul_f32 v254, v254, v250
AW a34,v254
AR v251,a35
v_sub_f32 v251, v251, v249
v_mul_f32 v251, v251, v250
AW a35,v251
AR v254,a36
v_sub_f32 v254, v254, v249
v_mul_f32 v254, v254, v250
AW a36,v254
AR v251,a37
v_sub_f32 v251, v251, v249
v_mul_f32 v251, v251, v250
AW a37,v251
AR v254,a38
v_sub_f32 v254, v254, v249
v_mul_f32 v254, v254, v250
AW a38,v254
AR v251,a39
v_sub_f32 v251, v251, v249
v_mul_f32 v251, v251, v250
AW a39,v251
AR v254,a64
v_sub_f32 v254, v254, v249
v_mul_f32 v254, v254, v250
AW a64,v254
AR v251,a65
v_sub_f32 v251, v251, v249
v_mul_f32 v251, v251, v250
AW a65,v251
AR v254,a66
v_sub_f32 v254, v254, v249
v_mul_f32 v254, v254, v250
AW a66,v254
AR v251,a67
v_sub_f32 v251, v251, v249
v_mul_f32 v251, v251, v250
AW a67,v251
AR v254,a68
v_sub_f32 v254, v254, v249
v_mul_f32 v254, v254, v250
AW a68,v254
AR v251,a69
v_sub_f32 v251, v251, v249
v_mul_f32 v251, v251, v250
AW a69,v251
AR v254,a70
v_sub_f32 v254, v254, v249
v_mul_f32 v254, v254, v250
AW a70,v254
AR v251,a71
v_sub_f32 v251, v251, v249
v_mul_f32 v251, v251, v250
AW a71,v251
AR v254,a96
v_sub_f32 v254, v254, v249
v_mul_f32 v254, v254, v250
AW a96,v254
AR v251,a97
v_sub_f32 v251, v251, v249
v_mul_f32 v251, v251, v250
AW a97,v251
AR v254,a98
v_sub_f32 v254, v254, v249
v_mul_f32 v254, v254, v250
AW a98,v254
AR v251,a99
v_sub_f32 v251, v251, v249
v_mul_f32 v251, v251, v250
AW a99,v251
AR v254,a100
v_sub_f32 v254, v254, v249
v_mul_f32 v254, v254, v250
AW a100,v254
AR v251,a101
v_sub_f32 v251, v251, v249
v_mul_f32 v251, v251, v250
AW a101,v251
AR v254,a102
v_sub_f32 v254, v254, v249
v_mul_f32 v254, v254, v250
AW a102,v254
AR v251,a103
v_sub_f32 v251, v251, v249
v_mul_f32 v251, v251, v250
AW a103,v251
v_max3_f32 v249, v72, v73, v74
v_max3_f32 v251, v104, v105, v106
v_max3_f32 v249, v249, v75, v76
v_max3_f32 v251, v251, v107, v108
v_max3_f32 v249, v249, v77, v78
v_max3_f32 v251, v251, v109, v110
v_max3_f32 v249, v249, v79, v111
v_max_f32 v249, v249, v251
v_mov_b32 v251, v249
s_nop 1
v_permlane16_swap_b32 v249, v251
v_max_f32 v249, v249, v251
v_mov_b32 v251, v249
s_nop 1
v_permlane32_swap_b32 v249, v251
v_max_f32 v249, v249, v251
v_log_f32 v250, v245
s_nop 0
v_max3_f32 v249, 0, v249, v250
v_sub_f32 v250, 0, v249
EX v250,v250
v_sub_f32 v132, v132, v249
v_sub_f32 v133, v133, v249
v_sub_f32 v134, v134, v249
v_sub_f32 v135, v135, v249
v_sub_f32 v8, v8, v249
v_sub_f32 v72, v72, v249
v_sub_f32 v9, v9, v249
v_sub_f32 v73, v73, v249
v_sub_f32 v10, v10, v249
v_sub_f32 v74, v74, v249
v_sub_f32 v11, v11, v249
v_sub_f32 v75, v75, v249
v_sub_f32 v12, v12, v249
v_sub_f32 v76, v76, v249
)ASM"
R"ASM(
v_sub_f32 v13, v13, v249
v_sub_f32 v77, v77, v249
v_sub_f32 v14, v14, v249
v_sub_f32 v78, v78, v249
v_sub_f32 v15, v15, v249
v_sub_f32 v79, v79, v249
v_sub_f32 v40, v40, v249
v_sub_f32 v104, v104, v249
v_sub_f32 v41, v41, v249
v_sub_f32 v105, v105, v249
v_sub_f32 v42, v42, v249
v_sub_f32 v106, v106, v249
v_sub_f32 v43, v43, v249
v_sub_f32 v107, v107, v249
v_sub_f32 v44, v44, v249
v_sub_f32 v108, v108, v249
v_sub_f32 v45, v45, v249
v_sub_f32 v109, v109, v249
v_sub_f32 v46, v46, v249
v_sub_f32 v110, v110, v249
v_sub_f32 v47, v47, v249
v_sub_f32 v111, v111, v249
v_mul_f32 v148, v245, v250
v_mov_b32 v149, 0
v_bfe_u32 v249, v241, 8, 8
v_lshl_or_b32 v249, v249, 1, 1
v_lshlrev_b32 v249, 22, v249
v_and_b32 v241, 0xffff00ff, v241
AR v254,a8
v_sub_f32 v254, v254, v249
v_mul_f32 v254, v254, v250
AW a8,v254
AR v251,a9
v_sub_f32 v251, v251, v249
v_mul_f32 v251, v251, v250
AW a9,v251
AR v254,a10
v_sub_f32 v254, v254, v249
v_mul_f32 v254, v254, v250
AW a10,v254
AR v251,a11
v_sub_f32 v251, v251, v249
v_mul_f32 v251, v251, v250
AW a11,v251
AR v254,a12
v_sub_f32 v254, v254, v249
v_mul_f32 v254, v254, v250
AW a12,v254
AR v251,a13
v_sub_f32 v251, v251, v249
v_mul_f32 v251, v251, v250
AW a13,v251
AR v254,a14
v_sub_f32 v254, v254, v249
v_mul_f32 v254, v254, v250
AW a14,v254
AR v251,a15
v_sub_f32 v251, v251, v249
v_mul_f32 v251, v251, v250
AW a15,v251
AR v254,a40
v_sub_f32 v254, v254, v249
v_mul_f32 v254, v254, v250
AW a40,v254
AR v251,a41
v_sub_f32 v251, v251, v249
v_mul_f32 v251, v251, v250
AW a41,v251
AR v254,a42
v_sub_f32 v254, v254, v249
v_mul_f32 v254, v254, v250
AW a42,v254
AR v251,a43
v_sub_f32 v251, v251, v249
v_mul_f32 v251, v251, v250
AW a43,v251
AR v254,a44
v_sub_f32 v254, v254, v249
v_mul_f32 v254, v254, v250
AW a44,v254
AR v251,a45
v_sub_f32 v251, v251, v249
v_mul_f32 v251, v251, v250
AW a45,v251
AR v254,a46
v_sub_f32 v254, v254, v249
v_mul_f32 v254, v254, v250
AW a46,v254
AR v251,a47
v_sub_f32 v251, v251, v249
v_mul_f32 v251, v251, v250
AW a47,v251
AR v254,a72
v_sub_f32 v254, v254, v249
v_mul_f32 v254, v254, v250
AW a72,v254
AR v251,a73
v_sub_f32 v251, v251, v249
v_mul_f32 v251, v251, v250
AW a73,v251
AR v254,a74
v_sub_f32 v254, v254, v249
v_mul_f32 v254, v254, v250
AW a74,v254
AR v251,a75
v_sub_f32 v251, v251, v249
v_mul_f32 v251, v251, v250
AW a75,v251
AR v254,a76
v_sub_f32 v254, v254, v249
v_mul_f32 v254, v254, v250
AW a76,v254
AR v251,a77
v_sub_f32 v251, v251, v249
v_mul_f32 v251, v251, v250
AW a77,v251
AR v254,a78
v_sub_f32 v254, v254, v249
v_mul_f32 v254, v254, v250
AW a78,v254
AR v251,a79
v_sub_f32 v251, v251, v249
v_mul_f32 v251, v251, v250
AW a79,v251
AR v254,a104
v_sub_f32 v254, v254, v249
v_mul_f32 v254, v254, v250
AW a104,v254
AR v251,a105
v_sub_f32 v251, v251, v249
v_mul_f32 v251, v251, v250
AW a105,v251
AR v254,a106
v_sub_f32 v254, v254, v249
v_mul_f32 v254, v254, v250
AW a106,v254
AR v251,a107
v_sub_f32 v251, v251, v249
v_mul_f32 v251, v251, v250
AW a107,v251
AR v254,a108
v_sub_f32 v254, v254, v249
v_mul_f32 v254, v254, v250
AW a108,v254
AR v251,a109
v_sub_f32 v251, v251, v249
v_mul_f32 v251, v251, v250
AW a109,v251
AR v254,a110
v_sub_f32 v254, v254, v249
v_mul_f32 v254, v254, v250
AW a110,v254
AR v251,a111
v_sub_f32 v251, v251, v249
v_mul_f32 v251, v251, v250
AW a111,v251
v_max3_f32 v249, v80, v81, v82
v_max3_f32 v251, v112, v113, v114
v_max3_f32 v249, v249, v83, v84
v_max3_f32 v251, v251, v115, v116
v_max3_f32 v249, v249, v85, v86
v_max3_f32 v251, v251, v117, v118
v_max3_f32 v249, v249, v87, v119
v_max_f32 v249, v249, v251
v_mov_b32 v251, v249
s_nop 1
v_permlane16_swap_b32 v249, v251
v_max_f32 v249, v249, v251
v_mov_b32 v251, v249
s_nop 1
v_permlane32_swap_b32 v249, v251
v_max_f32 v249, v249, v251
v_log_f32 v250, v246
s_nop 0
v_max3_f32 v249, 0, v249, v250
v_sub_f32 v250, 0, v249
EX v250,v250
v_sub_f32 v136, v136, v249
v_sub_f32 v137, v137, v249
v_sub_f32 v138, v138, v249
v_sub_f32 v139, v139, v249
v_sub_f32 v16, v16, v249
v_sub_f32 v80, v80, v249
v_sub_f32 v17, v17, v249
v_sub_f32 v81, v81, v249
v_sub_f32 v18, v18, v249
v_sub_f32 v82, v82, v249
v_sub_f32 v19, v19, v249
v_sub_f32 v83, v83, v249
v_sub_f32 v20, v20, v249
v_sub_f32 v84, v84, v249
v_sub_f32 v21, v21, v249
v_sub_f32 v85, v85, v249
v_sub_f32 v22, v22, v249
v_sub_f32 v86, v86, v249
v_sub_f32 v23, v23, v249
v_sub_f32 v87, v87, v249
v_sub_f32 v48, v48, v249
v_sub_f32 v112, v112, v249
v_sub_f32 v49, v49, v249
v_sub_f32 v113, v113, v249
v_sub_f32 v50, v50, v249
v_sub_f32 v114, v114, v249
v_sub_f32 v51, v51, v249
v_sub_f32 v115, v115, v249
v_sub_f32 v52, v52, v249
v_sub_f32 v116, v116, v249
v_sub_f32 v53, v53, v249
v_sub_f32 v117, v117, v249
v_sub_f32 v54, v54, v249
v_sub_f32 v118, v118, v249
v_sub_f32 v55, v55, v249
v_sub_f32 v119, v119, v249
v_mul_f32 v152, v246, v250
v_mov_b32 v153, 0
v_bfe_u32 v249, v241, 16, 8
v_lshl_or_b32 v249, v249, 1, 1
v_lshlrev_b32 v249, 22, v249
v_and_b32 v241, 0xff00ffff, v241
AR v254,a16
v_sub_f32 v254, v254, v249
v_mul_f32 v254, v254, v250
AW a16,v254
AR v251,a17
v_sub_f32 v251, v251, v249
v_mul_f32 v251, v251, v250
AW a17,v251
AR v254,a18
v_sub_f32 v254, v254, v249
v_mul_f32 v254, v254, v250
AW a18,v254
AR v251,a19
v_sub_f32 v251, v251, v249
v_mul_f32 v251, v251, v250
AW a19,v251
AR v254,a20
v_sub_f32 v254, v254, v249
v_mul_f32 v254, v254, v250
AW a20,v254
AR v251,a21
v_sub_f32 v251, v251, v249
v_mul_f32 v251, v251, v250
AW a21,v251
AR v254,a22
v_sub_f32 v254, v254, v249
v_mul_f32 v254, v254, v250
AW a22,v254
AR v251,a23
v_sub_f32 v251, v251, v249
v_mul_f32 v251, v251, v250
AW a23,v251
AR v254,a48
v_sub_f32 v254, v254, v249
v_mul_f32 v254, v254, v250
AW a48,v254
AR v251,a49
v_sub_f32 v251, v251, v249
v_mul_f32 v251, v251, v250
AW a49,v251
AR v254,a50
v_sub_f32 v254, v254, v249
v_mul_f32 v254, v254, v250
AW a50,v254
AR v251,a51
v_sub_f32 v251, v251, v249
v_mul_f32 v251, v251, v250
AW a51,v251
AR v254,a52
v_sub_f32 v254, v254, v249
v_mul_f32 v254, v254, v250
AW a52,v254
AR v251,a53
v_sub_f32 v251, v251, v249
v_mul_f32 v251, v251, v250
AW a53,v251
AR v254,a54
v_sub_f32 v254, v254, v249
v_mul_f32 v254, v254, v250
AW a54,v254
AR v251,a55
v_sub_f32 v251, v251, v249
v_mul_f32 v251, v251, v250
AW a55,v251
AR v254,a80
v_sub_f32 v254, v254, v249
v_mul_f32 v254, v254, v250
AW a80,v254
AR v251,a81
v_sub_f32 v251, v251, v249
v_mul_f32 v251, v251, v250
AW a81,v251
AR v254,a82
v_sub_f32 v254, v254, v249
v_mul_f32 v254, v254, v250
AW a82,v254
AR v251,a83
v_sub_f32 v251, v251, v249
v_mul_f32 v251, v251, v250
AW a83,v251
AR v254,a84
v_sub_f32 v254, v254, v249
v_mul_f32 v254, v254, v250
AW a84,v254
AR v251,a85
v_sub_f32 v251, v251, v249
v_mul_f32 v251, v251, v250
AW a85,v251
AR v254,a86
v_sub_f32 v254, v254, v249
v_mul_f32 v254, v254, v250
AW a86,v254
AR v251,a87
v_sub_f32 v251, v251, v249
v_mul_f32 v251, v251, v250
AW a87,v251
AR v254,a112
v_sub_f32 v254, v254, v249
v_mul_f32 v254, v254, v250
AW a112,v254
AR v251,a113
v_sub_f32 v251, v251, v249
v_mul_f32 v251, v251, v250
AW a113,v251
AR v254,a114
v_sub_f32 v254, v254, v249
v_mul_f32 v254, v254, v250
AW a114,v254
AR v251,a115
v_sub_f32 v251, v251, v249
v_mul_f32 v251, v251, v250
AW a115,v251
AR v254,a116
v_sub_f32 v254, v254, v249
v_mul_f32 v254, v254, v250
AW a116,v254
AR v251,a117
v_sub_f32 v251, v251, v249
v_mul_f32 v251, v251, v250
AW a117,v251
AR v254,a118
v_sub_f32 v254, v254, v249
v_mul_f32 v254, v254, v250
AW a118,v254
AR v251,a119
v_sub_f32 v251, v251, v249
v_mul_f32 v251, v251, v250
AW a119,v251
v_max3_f32 v249, v88, v89, v90
v_max3_f32 v251, v120, v121, v122
v_max3_f32 v249, v249, v91, v92
v_max3_f32 v251, v251, v123, v124
v_max3_f32 v249, v249, v93, v94
v_max3_f32 v251, v251, v125, v126
v_max3_f32 v249, v249, v95, v127
v_max_f32 v249, v249, v251
v_mov_b32 v251, v249
s_nop 1
v_permlane16_swap_b32 v249, v251
v_max_f32 v249, v249, v251
v_mov_b32 v251, v249
s_nop 1
v_permlane32_swap_b32 v249, v251
v_max_f32 v249, v249, v251
v_log_f32 v250, v247
s_nop 0
v_max3_f32 v249, 0, v249, v250
v_sub_f32 v250, 0, v249
EX v250,v250
v_sub_f32 v140, v140, v249
v_sub_f32 v141, v141, v249
v_sub_f32 v142, v142, v249
v_sub_f32 v143, v143, v249
v_sub_f32 v24, v24, v249
v_sub_f32 v88, v88, v249
v_sub_f32 v25, v25, v249
v_sub_f32 v89, v89, v249
v_sub_f32 v26, v26, v249
v_sub_f32 v90, v90, v249
v_sub_f32 v27, v27, v249
v_sub_f32 v91, v91, v249
v_sub_f32 v28, v28, v249
v_sub_f32 v92, v92, v249
v_sub_f32 v29, v29, v249
v_sub_f32 v93, v93, v249
v_sub_f32 v30, v30, v249
v_sub_f32 v94, v94, v249
v_sub_f32 v31, v31, v249
v_sub_f32 v95, v95, v249
v_sub_f32 v56, v56, v249
v_sub_f32 v120, v120, v249
v_sub_f32 v57, v57, v249
v_sub_f32 v121, v121, v249
v_sub_f32 v58, v58, v249
v_sub_f32 v122, v122, v249
v_sub_f32 v59, v59, v249
v_sub_f32 v123, v123, v249
v_sub_f32 v60, v60, v249
v_sub_f32 v124, v124, v249
v_sub_f32 v61, v61, v249
v_sub_f32 v125, v125, v249
v_sub_f32 v62, v62, v249
v_sub_f32 v126, v126, v249
v_sub_f32 v63, v63, v249
v_sub_f32 v127, v127, v249
v_mul_f32 v156, v247, v250
v_mov_b32 v157, 0
v_bfe_u32 v249, v241, 24, 8
v_lshl_or_b32 v249, v249, 1, 1
v_lshlrev_b32 v249, 22, v249
v_and_b32 v241, 0xffffff, v241
AR v254,a24
v_sub_f32 v254, v254, v249
v_mul_f32 v254, v254, v250
AW a24,v254
AR v251,a25
v_sub_f32 v251, v251, v249
v_mul_f32 v251, v251, v250
AW a25,v251
AR v254,a26
v_sub_f32 v254, v254, v249
v_mul_f32 v254, v254, v250
AW a26,v254
AR v251,a27
v_sub_f32 v251, v251, v249
v_mul_f32 v251, v251, v250
AW a27,v251
AR v254,a28
v_sub_f32 v254, v254, v249
v_mul_f32 v254, v254, v250
AW a28,v254
AR v251,a29
v_sub_f32 v251, v251, v249
v_mul_f32 v251, v251, v250
AW a29,v251
AR v254,a30
v_sub_f32 v254, v254, v249
v_mul_f32 v254, v254, v250
AW a30,v254
AR v251,a31
v_sub_f32 v251, v251, v249
v_mul_f32 v251, v251, v250
AW a31,v251
AR v254,a56
v_sub_f32 v254, v254, v249
v_mul_f32 v254, v254, v250
AW a56,v254
AR v251,a57
v_sub_f32 v251, v251, v249
v_mul_f32 v251, v251, v250
AW a57,v251
AR v254,a58
v_sub_f32 v254, v254, v249
v_mul_f32 v254, v254, v250
AW a58,v254
AR v251,a59
v_sub_f32 v251, v251, v249
v_mul_f32 v251, v251, v250
AW a59,v251
AR v254,a60
v_sub_f32 v254, v254, v249
v_mul_f32 v254, v254, v250
AW a60,v254
AR v251,a61
v_sub_f32 v251, v251, v249
v_mul_f32 v251, v251, v250
AW a61,v251
AR v254,a62
v_sub_f32 v254, v254, v249
v_mul_f32 v254, v254, v250
AW a62,v254
AR v251,a63
v_sub_f32 v251, v251, v249
v_mul_f32 v251, v251, v250
AW a63,v251
AR v254,a88
v_sub_f32 v254, v254, v249
v_mul_f32 v254, v254, v250
AW a88,v254
AR v251,a89
v_sub_f32 v251, v251, v249
v_mul_f32 v251, v251, v250
AW a89,v251
AR v254,a90
v_sub_f32 v254, v254, v249
v_mul_f32 v254, v254, v250
AW a90,v254
AR v251,a91
v_sub_f32 v251, v251, v249
v_mul_f32 v251, v251, v250
AW a91,v251
AR v254,a92
v_sub_f32 v254, v254, v249
v_mul_f32 v254, v254, v250
AW a92,v254
AR v251,a93
v_sub_f32 v251, v251, v249
v_mul_f32 v251, v251, v250
AW a93,v251
AR v254,a94
v_sub_f32 v254, v254, v249
v_mul_f32 v254, v254, v250
AW a94,v254
AR v251,a95
v_sub_f32 v251, v251, v249
v_mul_f32 v251, v251, v250
AW a95,v251
AR v254,a120
v_sub_f32 v254, v254, v249
v_mul_f32 v254, v254, v250
AW a120,v254
AR v251,a121
v_sub_f32 v251, v251, v249
v_mul_f32 v251, v251, v250
AW a121,v251
AR v254,a122
v_sub_f32 v254, v254, v249
v_mul_f32 v254, v254, v250
AW a122,v254
AR v251,a123
v_sub_f32 v251, v251, v249
v_mul_f32 v251, v251, v250
AW a123,v251
AR v254,a124
v_sub_f32 v254, v254, v249
v_mul_f32 v254, v254, v250
AW a124,v254
AR v251,a125
v_sub_f32 v251, v251, v249
v_mul_f32 v251, v251, v250
AW a125,v251
AR v254,a126
v_sub_f32 v254, v254, v249
v_mul_f32 v254, v254, v250
AW a126,v254
AR v251,a127
v_sub_f32 v251, v251, v249
v_mul_f32 v251, v251, v250
AW a127,v251
EX v64,v64
EX v65,v65
EX v66,v66
EX v67,v67
EX v68,v68
EX v69,v69
EX v70,v70
EX v71,v71
EX v72,v72
EX v73,v73
EX v74,v74
EX v75,v75
EX v76,v76
EX v77,v77
EX v78,v78
EX v79,v79
EX v80,v80
EX v81,v81
EX v82,v82
EX v83,v83
EX v84,v84
EX v85,v85
EX v86,v86
EX v87,v87
EX v88,v88
EX v89,v89
EX v90,v90
EX v91,v91
EX v92,v92
EX v93,v93
EX v94,v94
EX v95,v95
EX v96,v96
EX v97,v97
EX v98,v98
EX v99,v99
EX v100,v100
EX v101,v101
EX v102,v102
EX v103,v103
EX v104,v104
EX v105,v105
EX v106,v106
EX v107,v107
)ASM"
R"ASM(
EX v108,v108
EX v109,v109
EX v110,v110
EX v111,v111
EX v112,v112
EX v113,v113
EX v114,v114
EX v115,v115
EX v116,v116
EX v117,v117
EX v118,v118
EX v119,v119
EX v120,v120
EX v121,v121
EX v122,v122
EX v123,v123
EX v124,v124
EX v125,v125
EX v126,v126
EX v127,v127
CV v64,v64,v65
CV v65,v66,v67
CV v66,v68,v69
CV v67,v70,v71
CV v72,v72,v73
CV v73,v74,v75
CV v74,v76,v77
CV v75,v78,v79
CV v80,v80,v81
CV v81,v82,v83
CV v82,v84,v85
CV v83,v86,v87
CV v88,v88,v89
CV v89,v90,v91
CV v90,v92,v93
CV v91,v94,v95
CV v68,v96,v97
CV v69,v98,v99
CV v70,v100,v101
CV v71,v102,v103
CV v76,v104,v105
CV v77,v106,v107
CV v78,v108,v109
CV v79,v110,v111
CV v84,v112,v113
CV v85,v114,v115
CV v86,v116,v117
CV v87,v118,v119
CV v92,v120,v121
CV v93,v122,v123
CV v94,v124,v125
CV v95,v126,v127
v_smfmac_f32_16x16x64_bf16 v[144:147], v[236:239], v[64:71], v234
v_smfmac_f32_16x16x64_bf16 v[148:151], v[236:239], v[72:79], v234
v_smfmac_f32_16x16x64_bf16 v[152:155], v[236:239], v[80:87], v234
v_smfmac_f32_16x16x64_bf16 v[156:159], v[236:239], v[88:95], v234
s_nop 9
s_branch LRET_%=
LRARE022_%=:
s_nop 15
s_waitcnt vmcnt(0) lgkmcnt(0)
v_add_u32 v249, s74, v224
RD a[192:195],v249 offset:0
RD a[208:211],v249 offset:4096
RD a[224:227],v249 offset:8192
RD a[240:243],v249 offset:12288
v_add_u32 v250, s74, v225
RD a[196:199],v250 offset:0
RD a[212:215],v250 offset:4096
RD a[228:231],v250 offset:8192
RD a[244:247],v250 offset:12288
v_add_u32 v251, s74, v226
RD a[200:203],v251 offset:0
RD a[216:219],v251 offset:4096
RD a[232:235],v251 offset:8192
RD a[248:251],v251 offset:12288
v_add_u32 v254, s74, v227
RD a[204:207],v254 offset:0
RD a[220:223],v254 offset:4096
RD a[236:239],v254 offset:8192
RD a[252:255],v254 offset:12288
s_waitcnt lgkmcnt(0)
MM v[16:19],a[192:195],v[168:171],v[136:139]
MM v[16:19],a[196:199],v[184:187],v[16:19]
MM v[16:19],a[200:203],v[200:203],v[16:19]
MM v[16:19],a[204:207],v[216:219],v[16:19]
MM v[20:23],a[220:223],v[216:219],v[136:139]
MM v[20:23],a[216:219],v[200:203],v[20:23]
MM v[20:23],a[212:215],v[184:187],v[20:23]
MM v[20:23],a[208:211],v[168:171],v[20:23]
MM v[28:31],a[208:211],v[172:175],v[140:143]
MM v[28:31],a[212:215],v[188:191],v[28:31]
MM v[28:31],a[216:219],v[204:207],v[28:31]
MM v[28:31],a[220:223],v[220:223],v[28:31]
MM v[24:27],a[204:207],v[220:223],v[140:143]
MM v[24:27],a[200:203],v[204:207],v[24:27]
MM v[24:27],a[196:199],v[188:191],v[24:27]
MM v[24:27],a[192:195],v[172:175],v[24:27]
MM v[48:51],a[224:227],v[168:171],v[136:139]
MM v[48:51],a[228:231],v[184:187],v[48:51]
MM v[48:51],a[232:235],v[200:203],v[48:51]
MM v[48:51],a[236:239],v[216:219],v[48:51]
MM v[52:55],a[252:255],v[216:219],v[136:139]
MM v[52:55],a[248:251],v[200:203],v[52:55]
MM v[52:55],a[244:247],v[184:187],v[52:55]
MM v[52:55],a[240:243],v[168:171],v[52:55]
MM v[60:63],a[240:243],v[172:175],v[140:143]
MM v[60:63],a[244:247],v[188:191],v[60:63]
MM v[60:63],a[248:251],v[204:207],v[60:63]
MM v[60:63],a[252:255],v[220:223],v[60:63]
MM v[56:59],a[236:239],v[220:223],v[140:143]
MM v[56:59],a[232:235],v[204:207],v[56:59]
MM v[56:59],a[228:231],v[188:191],v[56:59]
MM v[56:59],a[224:227],v[172:175],v[56:59]
s_nop 15
v_add_u32 v249, s75, v224
RD a[192:195],v249 offset:0
RD a[208:211],v249 offset:4096
RD a[224:227],v249 offset:8192
RD a[240:243],v249 offset:12288
v_add_u32 v250, s75, v225
RD a[196:199],v250 offset:0
RD a[212:215],v250 offset:4096
RD a[228:231],v250 offset:8192
RD a[244:247],v250 offset:12288
v_add_u32 v251, s75, v226
RD a[200:203],v251 offset:0
RD a[216:219],v251 offset:4096
RD a[232:235],v251 offset:8192
RD a[248:251],v251 offset:12288
v_add_u32 v254, s75, v227
RD a[204:207],v254 offset:0
RD a[220:223],v254 offset:4096
RD a[236:239],v254 offset:8192
RD a[252:255],v254 offset:12288
s_waitcnt lgkmcnt(0)
v_max3_f32 v249, v16, v17, v18
v_max3_f32 v251, v48, v49, v50
v_max3_f32 v249, v249, v19, v20
v_max3_f32 v251, v251, v51, v52
v_max3_f32 v249, v249, v21, v22
v_max3_f32 v251, v251, v53, v54
v_max3_f32 v249, v249, v23, v55
v_max_f32 v249, v249, v251
v_mov_b32 v251, v249
s_nop 1
v_permlane16_swap_b32 v249, v251
v_max_f32 v249, v249, v251
v_mov_b32 v251, v249
s_nop 1
v_permlane32_swap_b32 v249, v251
v_max_f32 v249, v249, v251
v_log_f32 v250, v246
s_nop 0
v_max3_f32 v249, 0, v249, v250
v_sub_f32 v250, 0, v249
EX v250,v250
v_sub_f32 v136, v136, v249
v_sub_f32 v137, v137, v249
v_sub_f32 v138, v138, v249
v_sub_f32 v139, v139, v249
v_sub_f32 v80, v80, v249
v_sub_f32 v16, v16, v249
v_sub_f32 v81, v81, v249
v_sub_f32 v17, v17, v249
v_sub_f32 v82, v82, v249
v_sub_f32 v18, v18, v249
v_sub_f32 v83, v83, v249
v_sub_f32 v19, v19, v249
v_sub_f32 v84, v84, v249
v_sub_f32 v20, v20, v249
v_sub_f32 v85, v85, v249
v_sub_f32 v21, v21, v249
v_sub_f32 v86, v86, v249
v_sub_f32 v22, v22, v249
v_sub_f32 v87, v87, v249
v_sub_f32 v23, v23, v249
v_sub_f32 v112, v112, v249
v_sub_f32 v48, v48, v249
v_sub_f32 v113, v113, v249
v_sub_f32 v49, v49, v249
v_sub_f32 v114, v114, v249
v_sub_f32 v50, v50, v249
v_sub_f32 v115, v115, v249
v_sub_f32 v51, v51, v249
v_sub_f32 v116, v116, v249
v_sub_f32 v52, v52, v249
v_sub_f32 v117, v117, v249
v_sub_f32 v53, v53, v249
v_sub_f32 v118, v118, v249
v_sub_f32 v54, v54, v249
v_sub_f32 v119, v119, v249
v_sub_f32 v55, v55, v249
v_mul_f32 v152, v246, v250
v_mov_b32 v153, 0
v_bfe_u32 v249, v241, 16, 8
v_lshl_or_b32 v249, v249, 1, 1
v_lshlrev_b32 v249, 22, v249
v_and_b32 v241, 0xff00ffff, v241
AR v254,a16
v_sub_f32 v254, v254, v249
v_mul_f32 v254, v254, v250
AW a16,v254
AR v251,a17
v_sub_f32 v251, v251, v249
v_mul_f32 v251, v251, v250
AW a17,v251
AR v254,a18
v_sub_f32 v254, v254, v249
v_mul_f32 v254, v254, v250
AW a18,v254
AR v251,a19
v_sub_f32 v251, v251, v249
v_mul_f32 v251, v251, v250
AW a19,v251
AR v254,a20
v_sub_f32 v254, v254, v249
v_mul_f32 v254, v254, v250
AW a20,v254
AR v251,a21
v_sub_f32 v251, v251, v249
v_mul_f32 v251, v251, v250
AW a21,v251
AR v254,a22
v_sub_f32 v254, v254, v249
v_mul_f32 v254, v254, v250
AW a22,v254
AR v251,a23
v_sub_f32 v251, v251, v249
v_mul_f32 v251, v251, v250
AW a23,v251
AR v254,a48
v_sub_f32 v254, v254, v249
v_mul_f32 v254, v254, v250
AW a48,v254
AR v251,a49
v_sub_f32 v251, v251, v249
v_mul_f32 v251, v251, v250
AW a49,v251
AR v254,a50
v_sub_f32 v254, v254, v249
v_mul_f32 v254, v254, v250
AW a50,v254
AR v251,a51
v_sub_f32 v251, v251, v249
v_mul_f32 v251, v251, v250
AW a51,v251
AR v254,a52
v_sub_f32 v254, v254, v249
v_mul_f32 v254, v254, v250
AW a52,v254
AR v251,a53
v_sub_f32 v251, v251, v249
v_mul_f32 v251, v251, v250
AW a53,v251
AR v254,a54
v_sub_f32 v254, v254, v249
v_mul_f32 v254, v254, v250
AW a54,v254
AR v251,a55
v_sub_f32 v251, v251, v249
v_mul_f32 v251, v251, v250
AW a55,v251
AR v254,a80
v_sub_f32 v254, v254, v249
v_mul_f32 v254, v254, v250
AW a80,v254
AR v251,a81
v_sub_f32 v251, v251, v249
v_mul_f32 v251, v251, v250
AW a81,v251
AR v254,a82
v_sub_f32 v254, v254, v249
v_mul_f32 v254, v254, v250
AW a82,v254
AR v251,a83
v_sub_f32 v251, v251, v249
v_mul_f32 v251, v251, v250
AW a83,v251
AR v254,a84
v_sub_f32 v254, v254, v249
v_mul_f32 v254, v254, v250
AW a84,v254
AR v251,a85
v_sub_f32 v251, v251, v249
v_mul_f32 v251, v251, v250
AW a85,v251
AR v254,a86
v_sub_f32 v254, v254, v249
v_mul_f32 v254, v254, v250
AW a86,v254
AR v251,a87
v_sub_f32 v251, v251, v249
v_mul_f32 v251, v251, v250
AW a87,v251
AR v254,a112
v_sub_f32 v254, v254, v249
v_mul_f32 v254, v254, v250
AW a112,v254
AR v251,a113
v_sub_f32 v251, v251, v249
v_mul_f32 v251, v251, v250
AW a113,v251
AR v254,a114
v_sub_f32 v254, v254, v249
v_mul_f32 v254, v254, v250
AW a114,v254
AR v251,a115
v_sub_f32 v251, v251, v249
v_mul_f32 v251, v251, v250
AW a115,v251
AR v254,a116
v_sub_f32 v254, v254, v249
v_mul_f32 v254, v254, v250
AW a116,v254
AR v251,a117
v_sub_f32 v251, v251, v249
v_mul_f32 v251, v251, v250
AW a117,v251
AR v254,a118
v_sub_f32 v254, v254, v249
v_mul_f32 v254, v254, v250
AW a118,v254
AR v251,a119
v_sub_f32 v251, v251, v249
v_mul_f32 v251, v251, v250
AW a119,v251
v_max3_f32 v249, v24, v25, v26
v_max3_f32 v251, v56, v57, v58
v_max3_f32 v249, v249, v27, v28
v_max3_f32 v251, v251, v59, v60
v_max3_f32 v249, v249, v29, v30
v_max3_f32 v251, v251, v61, v62
v_max3_f32 v249, v249, v31, v63
v_max_f32 v249, v249, v251
v_mov_b32 v251, v249
s_nop 1
v_permlane16_swap_b32 v249, v251
v_max_f32 v249, v249, v251
v_mov_b32 v251, v249
s_nop 1
v_permlane32_swap_b32 v249, v251
v_max_f32 v249, v249, v251
v_log_f32 v250, v247
s_nop 0
v_max3_f32 v249, 0, v249, v250
v_sub_f32 v250, 0, v249
EX v250,v250
v_sub_f32 v140, v140, v249
v_sub_f32 v141, v141, v249
v_sub_f32 v142, v142, v249
v_sub_f32 v143, v143, v249
v_sub_f32 v88, v88, v249
v_sub_f32 v24, v24, v249
v_sub_f32 v89, v89, v249
v_sub_f32 v25, v25, v249
v_sub_f32 v90, v90, v249
v_sub_f32 v26, v26, v249
v_sub_f32 v91, v91, v249
v_sub_f32 v27, v27, v249
v_sub_f32 v92, v92, v249
v_sub_f32 v28, v28, v249
v_sub_f32 v93, v93, v249
v_sub_f32 v29, v29, v249
v_sub_f32 v94, v94, v249
v_sub_f32 v30, v30, v249
v_sub_f32 v95, v95, v249
v_sub_f32 v31, v31, v249
v_sub_f32 v120, v120, v249
v_sub_f32 v56, v56, v249
v_sub_f32 v121, v121, v249
v_sub_f32 v57, v57, v249
v_sub_f32 v122, v122, v249
v_sub_f32 v58, v58, v249
v_sub_f32 v123, v123, v249
v_sub_f32 v59, v59, v249
v_sub_f32 v124, v124, v249
v_sub_f32 v60, v60, v249
v_sub_f32 v125, v125, v249
v_sub_f32 v61, v61, v249
v_sub_f32 v126, v126, v249
v_sub_f32 v62, v62, v249
v_sub_f32 v127, v127, v249
v_sub_f32 v63, v63, v249
v_mul_f32 v156, v247, v250
v_mov_b32 v157, 0
v_bfe_u32 v249, v241, 24, 8
v_lshl_or_b32 v249, v249, 1, 1
v_lshlrev_b32 v249, 22, v249
v_and_b32 v241, 0xffffff, v241
AR v254,a24
v_sub_f32 v254, v254, v249
v_mul_f32 v254, v254, v250
AW a24,v254
AR v251,a25
v_sub_f32 v251, v251, v249
v_mul_f32 v251, v251, v250
AW a25,v251
AR v254,a26
v_sub_f32 v254, v254, v249
v_mul_f32 v254, v254, v250
AW a26,v254
AR v251,a27
v_sub_f32 v251, v251, v249
v_mul_f32 v251, v251, v250
AW a27,v251
AR v254,a28
v_sub_f32 v254, v254, v249
v_mul_f32 v254, v254, v250
AW a28,v254
AR v251,a29
v_sub_f32 v251, v251, v249
v_mul_f32 v251, v251, v250
AW a29,v251
AR v254,a30
v_sub_f32 v254, v254, v249
v_mul_f32 v254, v254, v250
AW a30,v254
AR v251,a31
v_sub_f32 v251, v251, v249
v_mul_f32 v251, v251, v250
AW a31,v251
AR v254,a56
v_sub_f32 v254, v254, v249
v_mul_f32 v254, v254, v250
AW a56,v254
AR v251,a57
v_sub_f32 v251, v251, v249
v_mul_f32 v251, v251, v250
AW a57,v251
AR v254,a58
v_sub_f32 v254, v254, v249
v_mul_f32 v254, v254, v250
AW a58,v254
AR v251,a59
v_sub_f32 v251, v251, v249
v_mul_f32 v251, v251, v250
AW a59,v251
AR v254,a60
v_sub_f32 v254, v254, v249
v_mul_f32 v254, v254, v250
AW a60,v254
AR v251,a61
v_sub_f32 v251, v251, v249
v_mul_f32 v251, v251, v250
AW a61,v251
AR v254,a62
v_sub_f32 v254, v254, v249
v_mul_f32 v254, v254, v250
AW a62,v254
AR v251,a63
v_sub_f32 v251, v251, v249
v_mul_f32 v251, v251, v250
AW a63,v251
AR v254,a88
v_sub_f32 v254, v254, v249
v_mul_f32 v254, v254, v250
AW a88,v254
AR v251,a89
v_sub_f32 v251, v251, v249
v_mul_f32 v251, v251, v250
AW a89,v251
AR v254,a90
v_sub_f32 v254, v254, v249
v_mul_f32 v254, v254, v250
AW a90,v254
AR v251,a91
v_sub_f32 v251, v251, v249
v_mul_f32 v251, v251, v250
AW a91,v251
AR v254,a92
v_sub_f32 v254, v254, v249
v_mul_f32 v254, v254, v250
AW a92,v254
AR v251,a93
v_sub_f32 v251, v251, v249
v_mul_f32 v251, v251, v250
AW a93,v251
AR v254,a94
v_sub_f32 v254, v254, v249
v_mul_f32 v254, v254, v250
AW a94,v254
AR v251,a95
v_sub_f32 v251, v251, v249
v_mul_f32 v251, v251, v250
AW a95,v251
AR v254,a120
v_sub_f32 v254, v254, v249
v_mul_f32 v254, v254, v250
AW a120,v254
AR v251,a121
v_sub_f32 v251, v251, v249
v_mul_f32 v251, v251, v250
AW a121,v251
AR v254,a122
v_sub_f32 v254, v254, v249
v_mul_f32 v254, v254, v250
AW a122,v254
AR v251,a123
v_sub_f32 v251, v251, v249
v_mul_f32 v251, v251, v250
AW a123,v251
AR v254,a124
v_sub_f32 v254, v254, v249
v_mul_f32 v254, v254, v250
AW a124,v254
AR v251,a125
v_sub_f32 v251, v251, v249
v_mul_f32 v251, v251, v250
AW a125,v251
AR v254,a126
v_sub_f32 v254, v254, v249
v_mul_f32 v254, v254, v250
AW a126,v254
AR v251,a127
v_sub_f32 v251, v251, v249
v_mul_f32 v251, v251, v250
AW a127,v251
EX v16,v16
EX v17,v17
)ASM"
R"ASM(
EX v18,v18
EX v19,v19
EX v20,v20
EX v21,v21
EX v22,v22
EX v23,v23
EX v24,v24
EX v25,v25
EX v26,v26
EX v27,v27
EX v28,v28
EX v29,v29
EX v30,v30
EX v31,v31
EX v48,v48
EX v49,v49
EX v50,v50
EX v51,v51
EX v52,v52
EX v53,v53
EX v54,v54
EX v55,v55
EX v56,v56
EX v57,v57
EX v58,v58
EX v59,v59
EX v60,v60
EX v61,v61
EX v62,v62
EX v63,v63
CV v16,v16,v17
CV v17,v18,v19
CV v18,v20,v21
CV v19,v22,v23
CV v24,v24,v25
CV v25,v26,v27
CV v26,v28,v29
CV v27,v30,v31
CV v20,v48,v49
CV v21,v50,v51
CV v22,v52,v53
CV v23,v54,v55
CV v28,v56,v57
CV v29,v58,v59
CV v30,v60,v61
CV v31,v62,v63
v_smfmac_f32_16x16x64_bf16 v[152:155], v[236:239], v[16:23], v234
s_nop 0
v_smfmac_f32_16x16x64_bf16 v[156:159], v[236:239], v[24:31], v234
s_nop 9
s_branch LRET_%=
LRARE122_%=:
s_nop 15
s_waitcnt vmcnt(0) lgkmcnt(0)
v_add_u32 v249, s74, v224
RD a[192:195],v249 offset:0
RD a[208:211],v249 offset:4096
RD a[224:227],v249 offset:8192
RD a[240:243],v249 offset:12288
v_add_u32 v250, s74, v225
RD a[196:199],v250 offset:0
RD a[212:215],v250 offset:4096
RD a[228:231],v250 offset:8192
RD a[244:247],v250 offset:12288
v_add_u32 v251, s74, v226
RD a[200:203],v251 offset:0
RD a[216:219],v251 offset:4096
RD a[232:235],v251 offset:8192
RD a[248:251],v251 offset:12288
v_add_u32 v254, s74, v227
RD a[204:207],v254 offset:0
RD a[220:223],v254 offset:4096
RD a[236:239],v254 offset:8192
RD a[252:255],v254 offset:12288
s_waitcnt lgkmcnt(0)
MM v[80:83],a[192:195],v[168:171],v[136:139]
MM v[80:83],a[196:199],v[184:187],v[80:83]
MM v[80:83],a[200:203],v[200:203],v[80:83]
MM v[80:83],a[204:207],v[216:219],v[80:83]
MM v[84:87],a[220:223],v[216:219],v[136:139]
MM v[84:87],a[216:219],v[200:203],v[84:87]
MM v[84:87],a[212:215],v[184:187],v[84:87]
MM v[84:87],a[208:211],v[168:171],v[84:87]
MM v[92:95],a[208:211],v[172:175],v[140:143]
MM v[92:95],a[212:215],v[188:191],v[92:95]
MM v[92:95],a[216:219],v[204:207],v[92:95]
MM v[92:95],a[220:223],v[220:223],v[92:95]
MM v[88:91],a[204:207],v[220:223],v[140:143]
MM v[88:91],a[200:203],v[204:207],v[88:91]
MM v[88:91],a[196:199],v[188:191],v[88:91]
MM v[88:91],a[192:195],v[172:175],v[88:91]
MM v[112:115],a[224:227],v[168:171],v[136:139]
MM v[112:115],a[228:231],v[184:187],v[112:115]
MM v[112:115],a[232:235],v[200:203],v[112:115]
MM v[112:115],a[236:239],v[216:219],v[112:115]
MM v[116:119],a[252:255],v[216:219],v[136:139]
MM v[116:119],a[248:251],v[200:203],v[116:119]
MM v[116:119],a[244:247],v[184:187],v[116:119]
MM v[116:119],a[240:243],v[168:171],v[116:119]
MM v[124:127],a[240:243],v[172:175],v[140:143]
MM v[124:127],a[244:247],v[188:191],v[124:127]
MM v[124:127],a[248:251],v[204:207],v[124:127]
MM v[124:127],a[252:255],v[220:223],v[124:127]
MM v[120:123],a[236:239],v[220:223],v[140:143]
MM v[120:123],a[232:235],v[204:207],v[120:123]
MM v[120:123],a[228:231],v[188:191],v[120:123]
MM v[120:123],a[224:227],v[172:175],v[120:123]
s_nop 15
v_add_u32 v249, s75, v224
RD a[192:195],v249 offset:0
RD a[208:211],v249 offset:4096
RD a[224:227],v249 offset:8192
RD a[240:243],v249 offset:12288
v_add_u32 v250, s75, v225
RD a[196:199],v250 offset:0
RD a[212:215],v250 offset:4096
RD a[228:231],v250 offset:8192
RD a[244:247],v250 offset:12288
v_add_u32 v251, s75, v226
RD a[200:203],v251 offset:0
RD a[216:219],v251 offset:4096
RD a[232:235],v251 offset:8192
RD a[248:251],v251 offset:12288
v_add_u32 v254, s75, v227
RD a[204:207],v254 offset:0
RD a[220:223],v254 offset:4096
RD a[236:239],v254 offset:8192
RD a[252:255],v254 offset:12288
s_waitcnt lgkmcnt(0)
v_max3_f32 v249, v80, v81, v82
v_max3_f32 v251, v112, v113, v114
v_max3_f32 v249, v249, v83, v84
v_max3_f32 v251, v251, v115, v116
v_max3_f32 v249, v249, v85, v86
v_max3_f32 v251, v251, v117, v118
v_max3_f32 v249, v249, v87, v119
v_max_f32 v249, v249, v251
v_mov_b32 v251, v249
s_nop 1
v_permlane16_swap_b32 v249, v251
v_max_f32 v249, v249, v251
v_mov_b32 v251, v249
s_nop 1
v_permlane32_swap_b32 v249, v251
v_max_f32 v249, v249, v251
v_log_f32 v250, v246
s_nop 0
v_max3_f32 v249, 0, v249, v250
v_sub_f32 v250, 0, v249
EX v250,v250
v_sub_f32 v136, v136, v249
v_sub_f32 v137, v137, v249
v_sub_f32 v138, v138, v249
v_sub_f32 v139, v139, v249
v_sub_f32 v16, v16, v249
v_sub_f32 v80, v80, v249
v_sub_f32 v17, v17, v249
v_sub_f32 v81, v81, v249
v_sub_f32 v18, v18, v249
v_sub_f32 v82, v82, v249
v_sub_f32 v19, v19, v249
v_sub_f32 v83, v83, v249
v_sub_f32 v20, v20, v249
v_sub_f32 v84, v84, v249
v_sub_f32 v21, v21, v249
v_sub_f32 v85, v85, v249
v_sub_f32 v22, v22, v249
v_sub_f32 v86, v86, v249
v_sub_f32 v23, v23, v249
v_sub_f32 v87, v87, v249
v_sub_f32 v48, v48, v249
v_sub_f32 v112, v112, v249
v_sub_f32 v49, v49, v249
v_sub_f32 v113, v113, v249
v_sub_f32 v50, v50, v249
v_sub_f32 v114, v114, v249
v_sub_f32 v51, v51, v249
v_sub_f32 v115, v115, v249
v_sub_f32 v52, v52, v249
v_sub_f32 v116, v116, v249
v_sub_f32 v53, v53, v249
v_sub_f32 v117, v117, v249
v_sub_f32 v54, v54, v249
v_sub_f32 v118, v118, v249
v_sub_f32 v55, v55, v249
v_sub_f32 v119, v119, v249
v_mul_f32 v152, v246, v250
v_mov_b32 v153, 0
v_bfe_u32 v249, v241, 16, 8
v_lshl_or_b32 v249, v249, 1, 1
v_lshlrev_b32 v249, 22, v249
v_and_b32 v241, 0xff00ffff, v241
AR v254,a16
v_sub_f32 v254, v254, v249
v_mul_f32 v254, v254, v250
AW a16,v254
AR v251,a17
v_sub_f32 v251, v251, v249
v_mul_f32 v251, v251, v250
AW a17,v251
AR v254,a18
v_sub_f32 v254, v254, v249
v_mul_f32 v254, v254, v250
AW a18,v254
AR v251,a19
v_sub_f32 v251, v251, v249
v_mul_f32 v251, v251, v250
AW a19,v251
AR v254,a20
v_sub_f32 v254, v254, v249
v_mul_f32 v254, v254, v250
AW a20,v254
AR v251,a21
v_sub_f32 v251, v251, v249
v_mul_f32 v251, v251, v250
AW a21,v251
AR v254,a22
v_sub_f32 v254, v254, v249
v_mul_f32 v254, v254, v250
AW a22,v254
AR v251,a23
v_sub_f32 v251, v251, v249
v_mul_f32 v251, v251, v250
AW a23,v251
AR v254,a48
v_sub_f32 v254, v254, v249
v_mul_f32 v254, v254, v250
AW a48,v254
AR v251,a49
v_sub_f32 v251, v251, v249
v_mul_f32 v251, v251, v250
AW a49,v251
AR v254,a50
v_sub_f32 v254, v254, v249
v_mul_f32 v254, v254, v250
AW a50,v254
AR v251,a51
v_sub_f32 v251, v251, v249
v_mul_f32 v251, v251, v250
AW a51,v251
AR v254,a52
v_sub_f32 v254, v254, v249
v_mul_f32 v254, v254, v250
AW a52,v254
AR v251,a53
v_sub_f32 v251, v251, v249
v_mul_f32 v251, v251, v250
AW a53,v251
AR v254,a54
v_sub_f32 v254, v254, v249
v_mul_f32 v254, v254, v250
AW a54,v254
AR v251,a55
v_sub_f32 v251, v251, v249
v_mul_f32 v251, v251, v250
AW a55,v251
AR v254,a80
v_sub_f32 v254, v254, v249
v_mul_f32 v254, v254, v250
AW a80,v254
AR v251,a81
v_sub_f32 v251, v251, v249
v_mul_f32 v251, v251, v250
AW a81,v251
AR v254,a82
v_sub_f32 v254, v254, v249
v_mul_f32 v254, v254, v250
AW a82,v254
AR v251,a83
v_sub_f32 v251, v251, v249
v_mul_f32 v251, v251, v250
AW a83,v251
AR v254,a84
v_sub_f32 v254, v254, v249
v_mul_f32 v254, v254, v250
AW a84,v254
AR v251,a85
v_sub_f32 v251, v251, v249
v_mul_f32 v251, v251, v250
AW a85,v251
AR v254,a86
v_sub_f32 v254, v254, v249
v_mul_f32 v254, v254, v250
AW a86,v254
AR v251,a87
v_sub_f32 v251, v251, v249
v_mul_f32 v251, v251, v250
AW a87,v251
AR v254,a112
v_sub_f32 v254, v254, v249
v_mul_f32 v254, v254, v250
AW a112,v254
AR v251,a113
v_sub_f32 v251, v251, v249
v_mul_f32 v251, v251, v250
AW a113,v251
AR v254,a114
v_sub_f32 v254, v254, v249
v_mul_f32 v254, v254, v250
AW a114,v254
AR v251,a115
v_sub_f32 v251, v251, v249
v_mul_f32 v251, v251, v250
AW a115,v251
AR v254,a116
v_sub_f32 v254, v254, v249
v_mul_f32 v254, v254, v250
AW a116,v254
AR v251,a117
v_sub_f32 v251, v251, v249
v_mul_f32 v251, v251, v250
AW a117,v251
AR v254,a118
v_sub_f32 v254, v254, v249
v_mul_f32 v254, v254, v250
AW a118,v254
AR v251,a119
v_sub_f32 v251, v251, v249
v_mul_f32 v251, v251, v250
AW a119,v251
v_max3_f32 v249, v88, v89, v90
v_max3_f32 v251, v120, v121, v122
v_max3_f32 v249, v249, v91, v92
v_max3_f32 v251, v251, v123, v124
v_max3_f32 v249, v249, v93, v94
v_max3_f32 v251, v251, v125, v126
v_max3_f32 v249, v249, v95, v127
v_max_f32 v249, v249, v251
v_mov_b32 v251, v249
s_nop 1
v_permlane16_swap_b32 v249, v251
v_max_f32 v249, v249, v251
v_mov_b32 v251, v249
s_nop 1
v_permlane32_swap_b32 v249, v251
v_max_f32 v249, v249, v251
v_log_f32 v250, v247
s_nop 0
v_max3_f32 v249, 0, v249, v250
v_sub_f32 v250, 0, v249
EX v250,v250
v_sub_f32 v140, v140, v249
v_sub_f32 v141, v141, v249
v_sub_f32 v142, v142, v249
v_sub_f32 v143, v143, v249
v_sub_f32 v24, v24, v249
v_sub_f32 v88, v88, v249
v_sub_f32 v25, v25, v249
v_sub_f32 v89, v89, v249
v_sub_f32 v26, v26, v249
v_sub_f32 v90, v90, v249
v_sub_f32 v27, v27, v249
v_sub_f32 v91, v91, v249
v_sub_f32 v28, v28, v249
v_sub_f32 v92, v92, v249
v_sub_f32 v29, v29, v249
v_sub_f32 v93, v93, v249
v_sub_f32 v30, v30, v249
v_sub_f32 v94, v94, v249
v_sub_f32 v31, v31, v249
v_sub_f32 v95, v95, v249
v_sub_f32 v56, v56, v249
v_sub_f32 v120, v120, v249
v_sub_f32 v57, v57, v249
v_sub_f32 v121, v121, v249
v_sub_f32 v58, v58, v249
v_sub_f32 v122, v122, v249
v_sub_f32 v59, v59, v249
v_sub_f32 v123, v123, v249
v_sub_f32 v60, v60, v249
v_sub_f32 v124, v124, v249
v_sub_f32 v61, v61, v249
v_sub_f32 v125, v125, v249
v_sub_f32 v62, v62, v249
v_sub_f32 v126, v126, v249
v_sub_f32 v63, v63, v249
v_sub_f32 v127, v127, v249
v_mul_f32 v156, v247, v250
v_mov_b32 v157, 0
v_bfe_u32 v249, v241, 24, 8
v_lshl_or_b32 v249, v249, 1, 1
v_lshlrev_b32 v249, 22, v249
v_and_b32 v241, 0xffffff, v241
AR v254,a24
v_sub_f32 v254, v254, v249
v_mul_f32 v254, v254, v250
AW a24,v254
AR v251,a25
v_sub_f32 v251, v251, v249
v_mul_f32 v251, v251, v250
AW a25,v251
AR v254,a26
v_sub_f32 v254, v254, v249
v_mul_f32 v254, v254, v250
AW a26,v254
AR v251,a27
v_sub_f32 v251, v251, v249
v_mul_f32 v251, v251, v250
AW a27,v251
AR v254,a28
v_sub_f32 v254, v254, v249
v_mul_f32 v254, v254, v250
AW a28,v254
AR v251,a29
v_sub_f32 v251, v251, v249
v_mul_f32 v251, v251, v250
AW a29,v251
AR v254,a30
v_sub_f32 v254, v254, v249
v_mul_f32 v254, v254, v250
AW a30,v254
AR v251,a31
v_sub_f32 v251, v251, v249
v_mul_f32 v251, v251, v250
AW a31,v251
AR v254,a56
v_sub_f32 v254, v254, v249
v_mul_f32 v254, v254, v250
AW a56,v254
AR v251,a57
v_sub_f32 v251, v251, v249
v_mul_f32 v251, v251, v250
AW a57,v251
AR v254,a58
v_sub_f32 v254, v254, v249
v_mul_f32 v254, v254, v250
AW a58,v254
AR v251,a59
v_sub_f32 v251, v251, v249
v_mul_f32 v251, v251, v250
AW a59,v251
AR v254,a60
v_sub_f32 v254, v254, v249
v_mul_f32 v254, v254, v250
AW a60,v254
AR v251,a61
v_sub_f32 v251, v251, v249
v_mul_f32 v251, v251, v250
AW a61,v251
AR v254,a62
v_sub_f32 v254, v254, v249
v_mul_f32 v254, v254, v250
AW a62,v254
AR v251,a63
v_sub_f32 v251, v251, v249
v_mul_f32 v251, v251, v250
AW a63,v251
AR v254,a88
v_sub_f32 v254, v254, v249
v_mul_f32 v254, v254, v250
AW a88,v254
AR v251,a89
v_sub_f32 v251, v251, v249
v_mul_f32 v251, v251, v250
AW a89,v251
AR v254,a90
v_sub_f32 v254, v254, v249
v_mul_f32 v254, v254, v250
AW a90,v254
AR v251,a91
v_sub_f32 v251, v251, v249
v_mul_f32 v251, v251, v250
AW a91,v251
AR v254,a92
v_sub_f32 v254, v254, v249
v_mul_f32 v254, v254, v250
AW a92,v254
AR v251,a93
v_sub_f32 v251, v251, v249
v_mul_f32 v251, v251, v250
AW a93,v251
AR v254,a94
v_sub_f32 v254, v254, v249
v_mul_f32 v254, v254, v250
AW a94,v254
AR v251,a95
v_sub_f32 v251, v251, v249
v_mul_f32 v251, v251, v250
AW a95,v251
AR v254,a120
v_sub_f32 v254, v254, v249
v_mul_f32 v254, v254, v250
AW a120,v254
AR v251,a121
v_sub_f32 v251, v251, v249
v_mul_f32 v251, v251, v250
AW a121,v251
AR v254,a122
v_sub_f32 v254, v254, v249
v_mul_f32 v254, v254, v250
AW a122,v254
AR v251,a123
v_sub_f32 v251, v251, v249
v_mul_f32 v251, v251, v250
AW a123,v251
AR v254,a124
v_sub_f32 v254, v254, v249
v_mul_f32 v254, v254, v250
AW a124,v254
AR v251,a125
v_sub_f32 v251, v251, v249
v_mul_f32 v251, v251, v250
AW a125,v251
AR v254,a126
v_sub_f32 v254, v254, v249
v_mul_f32 v254, v254, v250
AW a126,v254
AR v251,a127
v_sub_f32 v251, v251, v249
v_mul_f32 v251, v251, v250
AW a127,v251
EX v80,v80
EX v81,v81
EX v82,v82
EX v83,v83
EX v84,v84
EX v85,v85
EX v86,v86
EX v87,v87
EX v88,v88
EX v89,v89
EX v90,v90
EX v91,v91
EX v92,v92
EX v93,v93
EX v94,v94
EX v95,v95
EX v112,v112
EX v113,v113
EX v114,v114
EX v115,v115
EX v116,v116
EX v117,v117
EX v118,v118
)ASM"
R"ASM(
EX v119,v119
EX v120,v120
EX v121,v121
EX v122,v122
EX v123,v123
EX v124,v124
EX v125,v125
EX v126,v126
EX v127,v127
CV v80,v80,v81
CV v81,v82,v83
CV v82,v84,v85
CV v83,v86,v87
CV v88,v88,v89
CV v89,v90,v91
CV v90,v92,v93
CV v91,v94,v95
CV v84,v112,v113
CV v85,v114,v115
CV v86,v116,v117
CV v87,v118,v119
CV v92,v120,v121
CV v93,v122,v123
CV v94,v124,v125
CV v95,v126,v127
v_smfmac_f32_16x16x64_bf16 v[152:155], v[236:239], v[80:87], v234
s_nop 0
v_smfmac_f32_16x16x64_bf16 v[156:159], v[236:239], v[88:95], v234
s_nop 9
s_branch LRET_%=
LEND_%=:
)ASM"

    :: [ka] "s"(__builtin_amdgcn_kernarg_segment_ptr()), [slds] "s"(slds), [swave] "s"(w), [sb] "s"(blockIdx.x)
    : "memory", "vcc", "scc", CLOB_V, CLOB_A, CLOB_S);
}
constexpr int DYN_LDS = 163840;

extern "C" void kernel_launch(void* const* d_in, const int* in_sizes, int n_in, void* d_out, int out_size, void* d_ws, size_t ws_size, hipStream_t stream) {
#if !defined(__HIP_DEVICE_COMPILE__)
  if (DYN_LDS) hipFuncSetAttribute((const void*)ka_fused, hipFuncAttributeMaxDynamicSharedMemorySize, DYN_LDS);
  ka_fused<<<256, 256, DYN_LDS, stream>>>(d_in[3], d_in[0], d_ws, d_in[1], d_in[6], d_in[2], d_in[4], d_in[7], d_in[8], d_in[9], d_out);
#endif
}
```

```cpp
#include <hip/hip_runtime.h>
#include <stdint.h>

using f32x4  = __attribute__((ext_vector_type(4))) float;
using u32x4  = __attribute__((ext_vector_type(4))) unsigned;
using u32x2  = __attribute__((ext_vector_type(2))) unsigned;

constexpr int SEQ = 2048, DM = 640, NH = 32, DH = 128, HD = NH * DH, FF = 1728, TOPC = 8;

constexpr unsigned WS_FO  = 0;
constexpr unsigned WS_EP  = 32768;
constexpr unsigned WS_G1  = 36864;
constexpr unsigned WS_G2  = 53248;
constexpr unsigned WS_FH  = 69632;
constexpr unsigned WS_FY  = 73728;
constexpr unsigned WS_FG  = 77824;
constexpr unsigned WS_FR  = 81920;
constexpr unsigned WS_RK  = 86016;
constexpr unsigned WS_XN  = 131072;
constexpr unsigned WS_OF  = WS_XN + SEQ * DM * 2;
constexpr unsigned WS_H   = WS_OF + (18u << 20);
constexpr unsigned WS_Y   = WS_H + 32768;
constexpr unsigned WS_P   = WS_Y + 1048576;
constexpr unsigned WS_SPAN = 64u << 20;

#define CLOB_V "v0","v1","v2","v3","v4","v5","v6","v7","v8","v9","v10","v11","v12","v13","v14","v15","v16","v17","v18","v19","v20","v21","v22","v23","v24","v25","v26","v27","v28","v29","v30","v31","v32","v33","v34","v35","v36","v37","v38","v39","v40","v41","v42","v43","v44","v45","v46","v47","v48","v49","v50","v51","v52","v53","v54","v55","v56","v57","v58","v59","v60","v61","v62","v63","v64","v65","v66","v67","v68","v69","v70","v71","v72","v73","v74","v75","v76","v77","v78","v79","v80","v81","v82","v83","v84","v85","v86","v87","v88","v89","v90","v91","v92","v93","v94","v95","v96","v97","v98","v99","v100","v101","v102","v103","v104","v105","v106","v107","v108","v109","v110","v111","v112","v113","v114","v115","v116","v117","v118","v119","v120","v121","v122","v123","v124","v125","v126","v127","v128","v129","v130","v131","v132","v133","v134","v135","v136","v137","v138","v139","v140","v141","v142","v143","v144","v145","v146","v147","v148","v149","v150","v151","v152","v153","v154","v155","v156","v157","v158","v159","v160","v161","v162","v163","v164","v165","v166","v167","v168","v169","v170","v171","v172","v173","v174","v175","v176","v177","v178","v179","v180","v181","v182","v183","v184","v185","v186","v187","v188","v189","v190","v191","v192","v193","v194","v195","v196","v197","v198","v199","v200","v201","v202","v203","v204","v205","v206","v207","v208","v209","v210","v211","v212","v213","v214","v215","v216","v217","v218","v219","v220","v221","v222","v223","v224","v225","v226","v227","v228","v229","v230","v231","v232","v233","v234","v235","v236","v237","v238","v239","v240","v241","v242","v243","v244","v245","v246","v247","v248","v249","v250","v251","v252","v253","v254","v255"
#define CLOB_A "a0","a1","a2","a3","a4","a5","a6","a7","a8","a9","a10","a11","a12","a13","a14","a15","a16","a17","a18","a19","a20","a21","a22","a23","a24","a25","a26","a27","a28","a29","a30","a31","a32","a33","a34","a35","a36","a37","a38","a39","a40","a41","a42","a43","a44","a45","a46","a47","a48","a49","a50","a51","a52","a53","a54","a55","a56","a57","a58","a59","a60","a61","a62","a63","a64","a65","a66","a67","a68","a69","a70","a71","a72","a73","a74","a75","a76","a77","a78","a79","a80","a81","a82","a83","a84","a85","a86","a87","a88","a89","a90","a91","a92","a93","a94","a95","a96","a97","a98","a99","a100","a101","a102","a103","a104","a105","a106","a107","a108","a109","a110","a111","a112","a113","a114","a115","a116","a117","a118","a119","a120","a121","a122","a123","a124","a125","a126","a127","a128","a129","a130","a131","a132","a133","a134","a135","a136","a137","a138","a139","a140","a141","a142","a143","a144","a145","a146","a147","a148","a149","a150","a151","a152","a153","a154","a155","a156","a157","a158","a159","a160","a161","a162","a163","a164","a165","a166","a167","a168","a169","a170","a171","a172","a173","a174","a175","a176","a177","a178","a179","a180","a181","a182","a183","a184","a185","a186","a187","a188","a189","a190","a191","a192","a193","a194","a195","a196","a197","a198","a199","a200","a201","a202","a203","a204","a205","a206","a207","a208","a209","a210","a211","a212","a213","a214","a215","a216","a217","a218","a219","a220","a221","a222","a223","a224","a225","a226","a227","a228","a229","a230","a231","a232","a233","a234","a235","a236","a237","a238","a239","a240","a241","a242","a243","a244","a245","a246","a247","a248","a249","a250","a251","a252","a253","a254","a255"
#define CLOB_S "s8","s9","s10","s11","s12","s13","s14","s15","s16","s17","s18","s19","s20","s21","s22","s23","s24","s25","s26","s27","s28","s29","s30","s31","s32","s33","s34","s35","s36","s37","s38","s39","s40","s41","s42","s43","s44","s45","s46","s47","s48","s49","s50","s51","s52","s53","s54","s55","s56","s57","s58","s59","s60","s61","s62","s63","s64","s65","s66","s67","s68","s69","s70","s71","s72","s73","s74","s75","s76","s77","s78","s79","s80","s81","s82","s83","s84","s85","s86","s87","s88","s89","s90","s91","s92","s93","s94","s95","s96","s97","s98","s99"
__global__ __launch_bounds__(256, 1) __attribute__((aligned(4096))) void ka_fused(const void* wq, const void* x, void* ws, const void* Kc, const void* wr, const void* Vc, const void* wo, const void* wg, const void* wu, const void* wd, void* out) {
  extern __shared__ __attribute__((aligned(16))) char lds[];
  const unsigned w = __builtin_amdgcn_readfirstlane(threadIdx.x >> 6);
  const unsigned slds = __builtin_amdgcn_readfirstlane((unsigned)(size_t)lds);
  asm volatile(
R"ASM(
.macro MM a, b, c, d:vararg
v_mfma_f32_16x16x32_bf16 \a, \b, \c, \d
.endm
.macro TR a, b:vararg
ds_read_b64_tr_b16 \a, \b
.endm
.macro CV a, b, c:vararg
v_cvt_pk_bf16_f32 \a, \b, \c
.endm
.macro AR a, b:vararg
v_accvgpr_read_b32 \a, \b
.endm
.macro AW a, b:vararg
v_accvgpr_write_b32 \a, \b
.endm
.macro EX a, b:vararg
v_exp_f32 \a, \b
.endm
.macro RD a, b:vararg
ds_read_b128 \a, \b
.endm
s_branch LPG0_%=
.p2align 6
LPG0_%=:
s_load_dwordx2 s[16:17], %[ka], 0
s_load_dwordx2 s[32:33], %[ka], 8
s_load_dwordx2 s[20:21], %[ka], 16
s_load_dwordx2 s[8:9], %[ka], 24
s_load_dwordx2 s[12:13], %[ka], 40
s_load_dwordx2 s[24:25], %[ka], 48
s_load_dwordx2 s[44:45], %[ka], 32
s_load_dwordx2 s[28:29], %[ka], 72
s_load_dwordx2 s[40:41], %[ka], 80
s_lshl_b32 s77, %[swave], 2
s_and_b32 s77, s77, 8
s_add_u32 s77, s77, 56
s_load_dwordx2 s[36:37], %[ka], s77
v_mbcnt_lo_u32_b32 v127, -1, 0
v_mbcnt_hi_u32_b32 v127, -1, v127
v_lshlrev_b32 v248, 4, v127
s_lshr_b32 s77, %[sb], 3
s_lshl_b32 s77, s77, 2
s_add_u32 s77, s77, %[swave]
s_lshl_b32 s77, s77, 10
v_add_u32 v118, s77, v248
s_mov_b32 s77, LKEND_ka-LPC_%=-16
v_min_u32 v118, s77, v118
s_getpc_b64 s[98:99]
LPC_%=:
global_load_dwordx4 a[252:255], v118, s[98:99]
s_and_b32 s77, %[sb], 7
s_lshl_b32 s77, s77, 2
s_bfe_u32 s84, %[sb], 131075
s_add_u32 s49, s77, s84
s_lshr_b32 s50, %[sb], 5
s_lshl_b32 s76, %[sb], 2
s_add_u32 s76, s76, %[swave]
v_lshrrev_b32 v180, 5, v127
s_mul_i32 s82, s76, 2560
v_and_b32 v182, 496, v248
v_mul_u32_u24 v181, 1280, v180
v_add_u32 v168, v181, v182
v_lshrrev_b32 v210, 1, v182
v_add_u32 v181, v181, v210
s_lshl_b32 s77, %[swave], 12
s_add_u32 s65, s77, %[slds]
s_lshl_b32 s89, %[swave], 17
v_bfe_u32 v210, v127, 2, 3
v_and_b32 v212, 3, v127
v_lshlrev_b32 v213, 4, v212
v_lshl_add_u32 v213, v180, 6, v213
v_lshrrev_b32 v253, 3, v127
v_lshlrev_b32 v253, 13, v253
v_and_b32 v254, 112, v248
v_or_b32 v253, v253, v254
v_and_b32 v254, 16, v127
v_lshlrev_b32 v254, 2, v254
v_xor_b32 v253, v253, v254
v_add_u32 v254, 128, v253
s_mul_i32 s96, %[swave], 4112
s_add_u32 s96, s96, %[slds]
s_lshl_b32 s77, s50, 2
s_add_u32 s77, s77, %[swave]
s_mul_i32 s68, s77, 81920
s_and_b32 s62, %[sb], 1
s_mul_i32 s63, s62, 40960
s_add_u32 s68, s68, s63
s_lshl_b32 s63, s63, 1
s_mul_i32 s83, s77, 560128
s_add_u32 s83, s83, 2752512
s_lshl_b32 s93, s77, 9
s_lshl_b32 s94, %[swave], 13
s_add_u32 s94, s94, %[slds]
v_lshrrev_b32 v211, 3, v127
v_mul_u32_u24 v220, 1280, v211
v_lshrrev_b32 v212, 4, v127
v_and_b32 v211, 7, v127
v_xor_b32 v211, v211, v212
v_lshl_add_u32 v220, v211, 4, v220
v_xor_b32 v221, 64, v220
s_bitcmp1_b32 s84, 0
s_mov_b32 s77, 0x77552200
s_cselect_b32 s77, 0x77552200, s77
s_lshl_b32 s91, s50, 2
s_lshr_b32 s77, s77, s91
s_and_b32 s77, s77, 15
s_lshl_b32 s86, s77, 7
s_lshl_b32 s85, s77, 19
v_and_b32 v211, 15, v127
v_bfe_u32 v222, v127, 1, 3
v_xor_b32 v222, v222, v212
v_lshlrev_b32 v222, 4, v222
v_lshl_add_u32 v222, v211, 7, v222
v_xor_b32 v223, 64, v222
v_add_u32 v222, s94, v222
v_add_u32 v223, s94, v223
v_and_b32 v214, 1, v212
v_lshlrev_b32 v215, 5, v214
v_xor_b32 v215, v215, v213
v_lshl_add_u32 v235, v210, 8, v215
v_add_u32 v235, 128, v235
v_and_b32 v234, 1, v127
v_mul_u32_u24 v234, 43690, v234
v_add_u32 v234, 17476, v234
v_bfe_u32 v215, v127, 2, 2
v_and_b32 v216, 3, v127
v_lshlrev_b32 v252, 7, v215
v_lshl_add_u32 v252, v216, 4, v252
v_and_b32 v251, 8, v127
v_lshl_add_u32 v252, v251, 3, v252
v_mul_u32_u24 v251, 2056, v212
s_xor_b32 s77, %[swave], 0
s_and_b32 s91, s77, 1
s_lshl_b32 s91, s91, 6
s_lshr_b32 s77, s77, 1
s_lshl_b32 s77, s77, 10
s_add_u32 s77, s77, %[slds]
s_add_u32 s77, s77, 65536
v_xor_b32 v192, s91, v252
v_add3_u32 v192, s77, v192, v251
v_add_u32 v196, 49344, v192
s_xor_b32 s77, %[swave], 1
s_and_b32 s91, s77, 1
s_lshl_b32 s91, s91, 6
s_lshr_b32 s77, s77, 1
s_lshl_b32 s77, s77, 10
s_add_u32 s77, s77, %[slds]
s_add_u32 s77, s77, 65536
v_xor_b32 v193, s91, v252
v_add3_u32 v193, s77, v193, v251
v_add_u32 v197, 49344, v193
s_xor_b32 s77, %[swave], 2
s_and_b32 s91, s77, 1
s_lshl_b32 s91, s91, 6
s_lshr_b32 s77, s77, 1
s_lshl_b32 s77, s77, 10
s_add_u32 s77, s77, %[slds]
s_add_u32 s77, s77, 65536
v_xor_b32 v194, s91, v252
v_add3_u32 v194, s77, v194, v251
v_add_u32 v198, 49344, v194
s_xor_b32 s77, %[swave], 3
s_and_b32 s91, s77, 1
s_lshl_b32 s91, s91, 6
s_lshr_b32 s77, s77, 1
s_lshl_b32 s77, s77, 10
s_add_u32 s77, s77, %[slds]
s_add_u32 s77, s77, 65536
v_xor_b32 v195, s91, v252
v_add3_u32 v195, s77, v195, v251
v_add_u32 v199, 49344, v195
v_mov_b32 v236, 0x3f803f80
v_mov_b32 v184, 0
v_mov_b32 v237, 0x3f803f80
v_mov_b32 v185, 0
v_mov_b32 v238, 0x3f803f80
v_mov_b32 v188, 0
v_mov_b32 v239, 0x3f803f80
v_mov_b32 v189, 0
s_lshl_b32 s90, s49, 8
s_waitcnt lgkmcnt(0)
s_add_u32 s16, s16, s90
s_addc_u32 s17, s17, 0
s_sub_u32 s18, 5242880, s90
s_mov_b32 s19, 131072
s_mov_b32 s34, 2621440
s_mov_b32 s35, 131072
s_mov_b32 s22, 67108864
s_mov_b32 s23, 131072
s_add_u32 s87, s89, s85
s_add_u32 s85, s85, 524288
s_cmp_ge_u32 s85, 5242880
s_cselect_b32 s85, 0, s85
s_add_u32 m0, s96, 65536
s_nop 0
buffer_load_dwordx4 v253, s[16:19], s87 offen offset:0 lds
s_add_u32 m0, s96, 66560
s_nop 0
buffer_load_dwordx4 v254, s[16:19], s87 offen offset:0 lds
s_add_u32 s87, s87, 65536
s_add_u32 m0, s96, 67592
s_nop 0
buffer_load_dwordx4 v253, s[16:19], s87 offen offset:0 lds
s_add_u32 m0, s96, 68616
s_nop 0
buffer_load_dwordx4 v254, s[16:19], s87 offen offset:0 lds
s_add_u32 s88, s68, s86
s_add_u32 s86, s86, 128
s_cmp_ge_u32 s86, 1280
s_cselect_b32 s86, 0, s86
s_add_u32 m0, s94, 0
s_add_u32 s95, s88, 0
buffer_load_dwordx4 v220, s[32:35], s95 offen offset:0 lds
s_add_u32 s95, s88, 9216
buffer_load_dwordx4 v221, s[32:35], s95 offen offset:1024 lds
s_add_u32 s95, s88, 18432
buffer_load_dwordx4 v220, s[32:35], s95 offen offset:2048 lds
s_add_u32 s95, s88, 27648
buffer_load_dwordx4 v221, s[32:35], s95 offen offset:3072 lds
s_mov_b32 s66, 32768
buffer_load_dword v169, off, s[20:23], s66 sc1
buffer_load_dwordx4 v[128:131], v168, s[32:35], s82 offen offset:0
buffer_load_dwordx4 v[132:135], v168, s[32:35], s82 offen offset:512
buffer_load_dwordx2 v[136:137], v181, s[32:35], s82 offen offset:1024
s_add_u32 s82, s82, 131072
s_sub_u32 s88, s88, s63
s_add_u32 m0, s94, 4096
s_add_u32 s95, s88, 40960
buffer_load_dwordx4 v220, s[32:35], s95 offen offset:0 lds
s_add_u32 s95, s88, 50176
buffer_load_dwordx4 v221, s[32:35], s95 offen offset:1024 lds
s_add_u32 s95, s88, 59392
buffer_load_dwordx4 v220, s[32:35], s95 offen offset:2048 lds
s_add_u32 s95, s88, 68608
buffer_load_dwordx4 v221, s[32:35], s95 offen offset:3072 lds
s_getpc_b64 s[98:99]
LTA0_%=:
s_min_u32 s73, %[swave], 3
s_lshl_b32 s73, s73, 12
s_add_u32 s73, s73, LPG1_%=-LTA0_%=
s_add_u32 s98, s98, s73
s_addc_u32 s99, s99, 0
s_mov_b64 exec, 0
s_setpc_b64 s[98:99]
LTR0_%=:
s_mov_b64 exec, -1
s_add_u32 s87, s89, s85
s_add_u32 s85, s85, 524288
s_cmp_ge_u32 s85, 5242880
s_cselect_b32 s85, 0, s85
s_add_u32 m0, s96, 81984
s_nop 0
buffer_load_dwordx4 v253, s[16:19], s87 offen offset:0 lds
s_add_u32 m0, s96, 83008
s_nop 0
buffer_load_dwordx4 v254, s[16:19], s87 offen offset:0 lds
s_add_u32 s87, s87, 65536
s_add_u32 m0, s96, 84040
s_nop 0
buffer_load_dwordx4 v253, s[16:19], s87 offen offset:0 lds
s_add_u32 m0, s96, 85064
s_nop 0
buffer_load_dwordx4 v254, s[16:19], s87 offen offset:0 lds
s_add_u32 s88, s68, s86
s_add_u32 s86, s86, 128
s_cmp_ge_u32 s86, 1280
s_cselect_b32 s86, 0, s86
s_add_u32 m0, s94, 32768
s_add_u32 s95, s88, 0
buffer_load_dwordx4 v220, s[32:35], s95 offen offset:0 lds
s_add_u32 s95, s88, 9216
buffer_load_dwordx4 v221, s[32:35], s95 offen offset:1024 lds
s_add_u32 s95, s88, 18432
buffer_load_dwordx4 v220, s[32:35], s95 offen offset:2048 lds
s_add_u32 s95, s88, 27648
buffer_load_dwordx4 v221, s[32:35], s95 offen offset:3072 lds
s_sub_u32 s88, s88, s63
s_add_u32 m0, s94, 36864
s_add_u32 s95, s88, 40960
buffer_load_dwordx4 v220, s[32:35], s95 offen offset:0 lds
s_add_u32 s95, s88, 50176
buffer_load_dwordx4 v221, s[32:35], s95 offen offset:1024 lds
s_add_u32 s95, s88, 59392
buffer_load_dwordx4 v220, s[32:35], s95 offen offset:2048 lds
s_add_u32 s95, s88, 68608
buffer_load_dwordx4 v221, s[32:35], s95 offen offset:3072 lds
s_waitcnt vmcnt(20)
RD a[128:131],v222 offset:0
RD a[132:135],v223 offset:0
RD a[136:139],v222 offset:2048
RD a[140:143],v223 offset:2048
s_barrier
TR a[0:1],v192 offset:0
TR a[2:3],v192 offset:512
TR a[4:5],v192 offset:8224
TR a[6:7],v192 offset:8736
TR a[8:9],v192 offset:8
TR a[10:11],v192 offset:520
TR a[12:13],v192 offset:8232
TR a[14:15],v192 offset:8744
TR a[16:17],v193 offset:0
TR a[18:19],v193 offset:512
TR a[20:21],v193 offset:8224
TR a[22:23],v193 offset:8736
TR a[24:25],v193 offset:8
TR a[26:27],v193 offset:520
TR a[28:29],v193 offset:8232
TR a[30:31],v193 offset:8744
s_waitcnt lgkmcnt(15)
s_waitcnt lgkmcnt(12)
MM v[0:3],a[0:3],a[128:131],0
s_add_u32 s87, s89, s85
TR a[32:33],v194 offset:0
TR a[34:35],v194 offset:512
MM v[0:3],a[4:7],a[132:135],v[0:3]
s_add_u32 s85, s85, 524288
TR a[36:37],v194 offset:8224
TR a[38:39],v194 offset:8736
MM v[8:11],a[4:7],a[140:143],0
s_waitcnt vmcnt(16)
TR a[40:41],v194 offset:8
TR a[42:43],v194 offset:520
MM v[8:11],a[0:3],a[136:139],v[8:11]
s_waitcnt lgkmcnt(14)
TR a[44:45],v194 offset:8232
TR a[46:47],v194 offset:8744
s_cmp_ge_u32 s85, 5242880
s_cselect_b32 s85, 0, s85
MM v[4:7],a[8:11],a[128:131],0
s_add_u32 m0, s96, 98432
v_max3_f32 v200, |v0|, |v1|, |v2|
v_max_f32 v200, v200, |v3|
v_readfirstlane_b32 s69, v169
MM v[4:7],a[12:15],a[132:135],v[4:7]
s_lshl_b32 s90, s76, 1
TR a[48:49],v195 offset:0
TR a[50:51],v195 offset:512
MM v[12:15],a[12:15],a[140:143],0
s_and_b32 s77, s90, 15
buffer_load_dwordx4 v253, s[16:19], s87 offen offset:0 lds
v_max3_f32 v201, |v8|, |v9|, |v10|
v_max_f32 v201, v201, |v11|
MM v[12:15],a[8:11],a[136:139],v[12:15]
s_add_u32 m0, s96, 99456
TR a[52:53],v195 offset:8224
v_min_f32 v200, v200, v201
v_and_b32 v200, 0x7f800000, v200
v_lshlrev_b32 v148, 16, v128
v_smfmac_f32_16x16x64_bf16 v[184:187], v[236:239], a[0:7], v234
s_lshl_b32 s77, s77, 1
v_add_u32 v200, 0x2c00000, v200
v_smfmac_f32_16x16x64_bf16 v[188:191], v[236:239], a[8:15], v234
s_waitcnt lgkmcnt(15)
v_min_u32 v200, 0x78400000, v200
v_mov_b32 v201, v200
v_mov_b32 v202, v200
v_mov_b32 v203, v200
s_nop 1
MM v[32:35],a[16:19],a[128:131],v[200:203]
s_bfe_u32 s91, s90, 65540
TR a[54:55],v195 offset:8736
buffer_load_dwordx4 v254, s[16:19], s87 offen offset:0 lds
v_mov_b32 v250, v200
v_and_b32 v149, 0xffff0000, v128
MM v[32:35],a[20:23],a[132:135],v[32:35]
s_add_u32 s87, s87, 65536
TR a[56:57],v195 offset:8
TR a[58:59],v195 offset:520
MM v[40:43],a[20:23],a[140:143],v[200:203]
s_add_u32 m0, s96, 100488
TR a[60:61],v195 offset:8232
TR a[62:63],v195 offset:8744
MM v[40:43],a[16:19],a[136:139],v[40:43]
s_waitcnt lgkmcnt(15)
buffer_load_dwordx4 v253, s[16:19], s87 offen offset:0 lds
v_lshlrev_b32 v150, 16, v129
MM v[36:39],a[24:27],a[128:131],v[200:203]
s_add_u32 m0, s96, 101512
v_and_b32 v151, 0xffff0000, v129
v_lshlrev_b32 v152, 16, v130
MM v[36:39],a[28:31],a[132:135],v[36:39]
s_add_u32 s77, s77, s91
v_and_b32 v153, 0xffff0000, v130
v_lshlrev_b32 v154, 16, v131
MM v[44:47],a[28:31],a[140:143],v[200:203]
s_and_b32 s91, s90, 0xffffffe0
buffer_load_dwordx4 v254, s[16:19], s87 offen offset:0 lds
MM v[44:47],a[24:27],a[136:139],v[44:47]
s_waitcnt lgkmcnt(12)
s_waitcnt vmcnt(16)
RD a[144:147],v222 offset:4096
v_and_b32 v155, 0xffff0000, v131
MM v[64:67],a[32:35],a[128:131],v[200:203]
s_add_u32 s77, s77, s91
RD a[148:151],v223 offset:4096
RD a[152:155],v222 offset:6144
MM v[64:67],a[36:39],a[132:135],v[64:67]
s_lshl_b32 s77, s77, 3
RD a[156:159],v223 offset:6144
v_lshlrev_b32 v156, 16, v132
MM v[72:75],a[36:39],a[140:143],v[200:203]
s_add_u32 s97, s77, 36864
s_waitcnt vmcnt(12)
s_barrier
v_and_b32 v157, 0xffff0000, v132
MM v[72:75],a[32:35],a[136:139],v[72:75]
s_waitcnt lgkmcnt(12)
global_load_dwordx4 v[138:141], v182, s[44:45] offset:0
MM v[68:71],a[40:43],a[128:131],v[200:203]
s_sub_u32 s79, 65535, s90
v_lshlrev_b32 v158, 16, v133
v_and_b32 v159, 0xffff0000, v133
MM v[68:71],a[44:47],a[132:135],v[68:71]
s_lshl_b32 s78, s90, 2
v_lshlrev_b32 v160, 16, v134
v_and_b32 v161, 0xffff0000, v134
MM v[76:79],a[44:47],a[140:143],v[200:203]
s_add_u32 s78, s78, 53248
global_load_dwordx4 v[142:145], v182, s[44:45] offset:512
MM v[76:79],a[40:43],a[136:139],v[76:79]
s_waitcnt lgkmcnt(8)
v_lshrrev_b32 v182, 1, v182
v_lshlrev_b32 v162, 16, v135
MM v[96:99],a[48:51],a[128:131],v[200:203]
s_lshl_b32 s80, s76, 2
global_load_dwordx2 v[146:147], v182, s[44:45] offset:1024
)ASM"
R"ASM(
MM v[96:99],a[52:55],a[132:135],v[96:99]
s_waitcnt lgkmcnt(0)
s_add_u32 s88, s68, s86
s_add_u32 s86, s86, 128
v_and_b32 v163, 0xffff0000, v135
MM v[104:107],a[52:55],a[140:143],v[200:203]
s_add_u32 s80, s80, 81920
s_cmp_ge_u32 s86, 1280
s_cselect_b32 s86, 0, s86
MM v[104:107],a[48:51],a[136:139],v[104:107]
s_add_u32 m0, s94, 0
s_add_u32 s95, s88, 0
v_lshlrev_b32 v164, 16, v136
MM v[100:103],a[56:59],a[128:131],v[200:203]
buffer_load_dwordx4 v220, s[32:35], s95 offen offset:0 lds
MM v[100:103],a[60:63],a[132:135],v[100:103]
s_add_u32 s95, s88, 9216
v_and_b32 v165, 0xffff0000, v136
v_lshlrev_b32 v166, 16, v137
MM v[108:111],a[60:63],a[140:143],v[200:203]
buffer_load_dwordx4 v221, s[32:35], s95 offen offset:1024 lds
MM v[108:111],a[56:59],a[136:139],v[108:111]
s_add_u32 s95, s88, 18432
v_and_b32 v167, 0xffff0000, v137
s_add_u32 s69, s69, 1
MM v[16:19],a[0:3],a[144:147],0
v_add_f32 v170, v148, v149
v_add_f32 v171, v150, v151
MM v[16:19],a[4:7],a[148:151],v[16:19]
v_add_f32 v170, v170, v152
v_add_f32 v171, v171, v153
MM v[24:27],a[4:7],a[156:159],0
buffer_load_dwordx4 v220, s[32:35], s95 offen offset:2048 lds
MM v[24:27],a[0:3],a[152:155],v[24:27]
s_add_u32 s95, s88, 27648
v_add_f32 v170, v170, v154
v_add_f32 v171, v171, v155
MM v[20:23],a[8:11],a[144:147],0
v_max3_f32 v204, |v16|, |v17|, |v18|
v_max_f32 v204, v204, |v19|
MM v[20:23],a[12:15],a[148:151],v[20:23]
TR a[0:1],v192 offset:16448
TR a[2:3],v192 offset:16960
v_add_f32 v170, v170, v156
MM v[28:31],a[12:15],a[156:159],0
v_max3_f32 v205, |v24|, |v25|, |v26|
v_max_f32 v205, v205, |v27|
MM v[28:31],a[8:11],a[152:155],v[28:31]
TR a[4:5],v192 offset:24672
v_min_f32 v204, v204, v205
v_and_b32 v204, 0x7f800000, v204
v_add_u32 v204, 0x2c00000, v204
v_min_u32 v204, 0x78400000, v204
v_mov_b32 v205, v204
v_mov_b32 v206, v204
v_mov_b32 v207, v204
s_nop 1
MM v[48:51],a[16:19],a[144:147],v[204:207]
TR a[6:7],v192 offset:25184
buffer_load_dwordx4 v221, s[32:35], s95 offen offset:3072 lds
v_mov_b32 v252, v204
v_add_f32 v171, v171, v157
MM v[48:51],a[20:23],a[148:151],v[48:51]
s_sub_u32 s88, s88, s63
TR a[8:9],v192 offset:16456
TR a[10:11],v192 offset:16968
MM v[56:59],a[20:23],a[156:159],v[204:207]
s_add_u32 m0, s94, 4096
TR a[12:13],v192 offset:24680
TR a[14:15],v192 offset:25192
MM v[56:59],a[16:19],a[152:155],v[56:59]
s_add_u32 s95, s88, 40960
buffer_load_dwordx4 v220, s[32:35], s95 offen offset:0 lds
v_add_f32 v170, v170, v158
MM v[52:55],a[24:27],a[144:147],v[204:207]
s_add_u32 s95, s88, 50176
TR a[16:17],v193 offset:16448
TR a[18:19],v193 offset:16960
MM v[52:55],a[28:31],a[148:151],v[52:55]
TR a[20:21],v193 offset:24672
TR a[22:23],v193 offset:25184
MM v[60:63],a[28:31],a[156:159],v[204:207]
buffer_load_dwordx4 v221, s[32:35], s95 offen offset:1024 lds
v_add_f32 v171, v171, v159
MM v[60:63],a[24:27],a[152:155],v[60:63]
s_add_u32 s95, s88, 59392
v_add_f32 v170, v170, v160
v_add_f32 v171, v171, v161
MM v[80:83],a[32:35],a[144:147],v[204:207]
TR a[24:25],v193 offset:16456
TR a[26:27],v193 offset:16968
MM v[80:83],a[36:39],a[148:151],v[80:83]
TR a[28:29],v193 offset:24680
TR a[30:31],v193 offset:25192
v_add_f32 v170, v170, v162
MM v[88:91],a[36:39],a[156:159],v[204:207]
buffer_load_dwordx4 v220, s[32:35], s95 offen offset:2048 lds
MM v[88:91],a[32:35],a[152:155],v[88:91]
s_add_u32 s95, s88, 68608
v_add_f32 v171, v171, v163
v_add_f32 v170, v170, v164
MM v[84:87],a[40:43],a[144:147],v[204:207]
TR a[32:33],v194 offset:16448
TR a[34:35],v194 offset:16960
MM v[84:87],a[44:47],a[148:151],v[84:87]
TR a[36:37],v194 offset:24672
TR a[38:39],v194 offset:25184
v_add_f32 v171, v171, v165
MM v[92:95],a[44:47],a[156:159],v[204:207]
buffer_load_dwordx4 v221, s[32:35], s95 offen offset:3072 lds
MM v[92:95],a[40:43],a[152:155],v[92:95]
s_add_u32 s87, s89, s85
s_add_u32 s85, s85, 524288
v_add_f32 v170, v170, v166
MM v[112:115],a[48:51],a[144:147],v[204:207]
TR a[40:41],v194 offset:16456
TR a[42:43],v194 offset:16968
MM v[112:115],a[52:55],a[148:151],v[112:115]
TR a[44:45],v194 offset:24680
TR a[46:47],v194 offset:25192
s_cmp_ge_u32 s85, 5242880
s_cselect_b32 s85, 0, s85
v_add_f32 v171, v171, v167
MM v[120:123],a[52:55],a[156:159],v[204:207]
s_add_u32 m0, s96, 114880
s_nop 0
buffer_load_dwordx4 v253, s[16:19], s87 offen offset:0 lds
MM v[120:123],a[48:51],a[152:155],v[120:123]
s_add_u32 m0, s96, 115904
v_add_f32 v172, v170, v171
v_lshlrev_b32 v232, 11, v180
MM v[116:119],a[56:59],a[144:147],v[204:207]
TR a[48:49],v195 offset:16448
TR a[50:51],v195 offset:16960
MM v[116:119],a[60:63],a[148:151],v[116:119]
TR a[52:53],v195 offset:24672
TR a[54:55],v195 offset:25184
MM v[124:127],a[60:63],a[156:159],v[204:207]
buffer_load_dwordx4 v254, s[16:19], s87 offen offset:0 lds
v_add_f32_dpp v172, v172, v172 row_ror:8 row_mask:0xf bank_mask:0xf
MM v[124:127],a[56:59],a[152:155],v[124:127]
s_add_u32 s87, s87, 65536
s_add_u32 m0, s96, 116936
v_add_f32_dpp v172, v172, v172 row_ror:4 row_mask:0xf bank_mask:0xf
buffer_load_dwordx4 v253, s[16:19], s87 offen offset:0 lds
s_add_u32 m0, s96, 117960
s_nop 0
buffer_load_dwordx4 v254, s[16:19], s87 offen offset:0 lds
s_waitcnt vmcnt(19)
RD a[160:163],v222 offset:32768
RD a[164:167],v223 offset:32768
RD a[168:171],v222 offset:34816
RD a[172:175],v223 offset:34816
RD a[176:179],v222 offset:36864
RD a[180:183],v223 offset:36864
RD a[184:187],v222 offset:38912
RD a[188:191],v223 offset:38912
s_waitcnt vmcnt(15) lgkmcnt(0)
s_barrier
MM v[0:3],a[0:3],a[160:163],v[0:3]
TR a[56:57],v195 offset:16456
TR a[58:59],v195 offset:16968
MM v[0:3],a[4:7],a[164:167],v[0:3]
TR a[60:61],v195 offset:24680
TR a[62:63],v195 offset:25192
MM v[8:11],a[4:7],a[172:175],v[8:11]
v_add_f32_dpp v172, v172, v172 row_ror:2 row_mask:0xf bank_mask:0xf
v_lshl_add_u32 v232, v214, 8, v232
MM v[8:11],a[0:3],a[168:171],v[8:11]
s_add_u32 s88, s68, s86
s_add_u32 s86, s86, 128
MM v[16:19],a[0:3],a[176:179],v[16:19]
s_cmp_ge_u32 s86, 1280
s_cselect_b32 s86, 0, s86
s_add_u32 m0, s94, 32768
MM v[16:19],a[4:7],a[180:183],v[16:19]
s_add_u32 s95, s88, 0
buffer_load_dwordx4 v220, s[32:35], s95 offen offset:0 lds
v_add_f32_dpp v172, v172, v172 row_ror:1 row_mask:0xf bank_mask:0xf
MM v[24:27],a[4:7],a[188:191],v[24:27]
s_add_u32 s95, s88, 9216
v_mov_b32 v173, v172
v_lshl_add_u32 v232, v215, 6, v232
MM v[24:27],a[0:3],a[184:187],v[24:27]
buffer_load_dwordx4 v221, s[32:35], s95 offen offset:1024 lds
MM v[4:7],a[8:11],a[160:163],v[4:7]
s_add_u32 s95, s88, 18432
v_permlane16_swap_b32 v172, v173
v_add_f32 v172, v172, v173
MM v[4:7],a[12:15],a[164:167],v[4:7]
v_mul_f32 v172, 0x3acccccd, v172
v_sub_f32 v176, v148, v172
MM v[12:15],a[12:15],a[172:175],v[12:15]
v_sub_f32 v177, v149, v172
v_mul_f32 v170, v176, v176
MM v[12:15],a[8:11],a[168:171],v[12:15]
buffer_load_dwordx4 v220, s[32:35], s95 offen offset:2048 lds
MM v[20:23],a[8:11],a[176:179],v[20:23]
s_add_u32 s95, s88, 27648
v_mul_f32 v171, v177, v177
v_sub_f32 v176, v150, v172
MM v[20:23],a[12:15],a[180:183],v[20:23]
v_sub_f32 v177, v151, v172
v_fma_f32 v170, v176, v176, v170
MM v[28:31],a[12:15],a[188:191],v[28:31]
v_fma_f32 v171, v177, v177, v171
v_sub_f32 v176, v152, v172
MM v[28:31],a[8:11],a[184:187],v[28:31]
buffer_load_dwordx4 v221, s[32:35], s95 offen offset:3072 lds
v_smfmac_f32_16x16x64_bf16 v[184:187], v[236:239], a[0:7], v234
s_sub_u32 s88, s88, s63
s_add_u32 m0, s94, 36864
v_sub_f32 v177, v153, v172
v_smfmac_f32_16x16x64_bf16 v[188:191], v[236:239], a[8:15], v234
s_add_u32 s95, s88, 40960
v_fma_f32 v170, v176, v176, v170
MM v[32:35],a[16:19],a[160:163],v[32:35]
TR a[0:1],v192 offset:32896
s_nop 0
LPG1_%=:
s_cbranch_execz LPG5_%=
TR a[2:3],v192 offset:33408
MM v[32:35],a[20:23],a[164:167],v[32:35]
TR a[4:5],v192 offset:41120
TR a[6:7],v192 offset:41632
v_fma_f32 v171, v177, v177, v171
MM v[40:43],a[20:23],a[172:175],v[40:43]
TR a[8:9],v192 offset:32904
TR a[10:11],v192 offset:33416
MM v[40:43],a[16:19],a[168:171],v[40:43]
TR a[12:13],v192 offset:41128
TR a[14:15],v192 offset:41640
buffer_load_dwordx4 v220, s[32:35], s95 offen offset:0 lds
MM v[48:51],a[16:19],a[176:179],v[48:51]
s_add_u32 s95, s88, 50176
v_sub_f32 v176, v154, v172
v_sub_f32 v177, v155, v172
MM v[48:51],a[20:23],a[180:183],v[48:51]
buffer_load_dwordx4 v221, s[32:35], s95 offen offset:1024 lds
MM v[56:59],a[20:23],a[188:191],v[56:59]
s_add_u32 s95, s88, 59392
v_fma_f32 v170, v176, v176, v170
v_fma_f32 v171, v177, v177, v171
MM v[56:59],a[16:19],a[184:187],v[56:59]
v_sub_f32 v176, v156, v172
v_sub_f32 v177, v157, v172
MM v[36:39],a[24:27],a[160:163],v[36:39]
TR a[16:17],v193 offset:32896
TR a[18:19],v193 offset:33408
MM v[36:39],a[28:31],a[164:167],v[36:39]
TR a[20:21],v193 offset:41120
TR a[22:23],v193 offset:41632
v_fma_f32 v170, v176, v176, v170
MM v[44:47],a[28:31],a[172:175],v[44:47]
buffer_load_dwordx4 v220, s[32:35], s95 offen offset:2048 lds
MM v[44:47],a[24:27],a[168:171],v[44:47]
s_add_u32 s95, s88, 68608
v_fma_f32 v171, v177, v177, v171
v_sub_f32 v176, v158, v172
MM v[52:55],a[24:27],a[176:179],v[52:55]
v_sub_f32 v177, v159, v172
v_fma_f32 v170, v176, v176, v170
MM v[52:55],a[28:31],a[180:183],v[52:55]
buffer_load_dwordx4 v221, s[32:35], s95 offen offset:3072 lds
MM v[60:63],a[28:31],a[188:191],v[60:63]
s_add_u32 s87, s89, s85
s_add_u32 s85, s85, 524288
v_fma_f32 v171, v177, v177, v171
MM v[60:63],a[24:27],a[184:187],v[60:63]
s_cmp_ge_u32 s85, 5242880
s_cselect_b32 s85, 0, s85
s_add_u32 m0, s96, 131328
MM v[64:67],a[32:35],a[160:163],v[64:67]
TR a[24:25],v193 offset:32904
TR a[26:27],v193 offset:33416
v_sub_f32 v176, v160, v172
MM v[64:67],a[36:39],a[164:167],v[64:67]
TR a[28:29],v193 offset:41128
TR a[30:31],v193 offset:41640
MM v[72:75],a[36:39],a[172:175],v[72:75]
buffer_load_dwordx4 v253, s[16:19], s87 offen offset:0 lds
MM v[72:75],a[32:35],a[168:171],v[72:75]
s_add_u32 m0, s96, 132352
v_sub_f32 v177, v161, v172
v_fma_f32 v170, v176, v176, v170
MM v[80:83],a[32:35],a[176:179],v[80:83]
v_fma_f32 v171, v177, v177, v171
v_sub_f32 v176, v162, v172
MM v[80:83],a[36:39],a[180:183],v[80:83]
buffer_load_dwordx4 v254, s[16:19], s87 offen offset:0 lds
MM v[88:91],a[36:39],a[188:191],v[88:91]
s_add_u32 s87, s87, 65536
s_add_u32 m0, s96, 133384
v_sub_f32 v177, v163, v172
MM v[88:91],a[32:35],a[184:187],v[88:91]
v_fma_f32 v170, v176, v176, v170
v_fma_f32 v171, v177, v177, v171
MM v[68:71],a[40:43],a[160:163],v[68:71]
TR a[32:33],v194 offset:32896
TR a[34:35],v194 offset:33408
MM v[68:71],a[44:47],a[164:167],v[68:71]
TR a[36:37],v194 offset:41120
TR a[38:39],v194 offset:41632
v_sub_f32 v176, v164, v172
MM v[76:79],a[44:47],a[172:175],v[76:79]
buffer_load_dwordx4 v253, s[16:19], s87 offen offset:0 lds
MM v[76:79],a[40:43],a[168:171],v[76:79]
s_add_u32 m0, s96, 134408
v_sub_f32 v177, v165, v172
v_fma_f32 v170, v176, v176, v170
MM v[84:87],a[40:43],a[176:179],v[84:87]
v_fma_f32 v171, v177, v177, v171
v_sub_f32 v176, v166, v172
MM v[84:87],a[44:47],a[180:183],v[84:87]
buffer_load_dwordx4 v254, s[16:19], s87 offen offset:0 lds
MM v[92:95],a[44:47],a[188:191],v[92:95]
s_waitcnt vmcnt(16)
RD a[192:195],v222 offset:0
v_sub_f32 v177, v167, v172
MM v[92:95],a[40:43],a[184:187],v[92:95]
RD a[196:199],v223 offset:0
v_fma_f32 v170, v176, v176, v170
MM v[96:99],a[48:51],a[160:163],v[96:99]
TR a[40:41],v194 offset:32904
TR a[42:43],v194 offset:33416
MM v[96:99],a[52:55],a[164:167],v[96:99]
TR a[44:45],v194 offset:41128
TR a[46:47],v194 offset:41640
v_fma_f32 v171, v177, v177, v171
MM v[104:107],a[52:55],a[172:175],v[104:107]
RD a[200:203],v222 offset:2048
v_add_f32 v170, v170, v171
MM v[104:107],a[48:51],a[168:171],v[104:107]
RD a[204:207],v223 offset:2048
v_add_f32_dpp v170, v170, v170 row_ror:8 row_mask:0xf bank_mask:0xf
MM v[112:115],a[48:51],a[176:179],v[112:115]
RD a[208:211],v222 offset:4096
v_add_f32_dpp v170, v170, v170 row_ror:4 row_mask:0xf bank_mask:0xf
MM v[112:115],a[52:55],a[180:183],v[112:115]
RD a[212:215],v223 offset:4096
v_add_f32_dpp v170, v170, v170 row_ror:2 row_mask:0xf bank_mask:0xf
MM v[120:123],a[52:55],a[188:191],v[120:123]
RD a[216:219],v222 offset:6144
v_add_f32_dpp v170, v170, v170 row_ror:1 row_mask:0xf bank_mask:0xf
MM v[120:123],a[48:51],a[184:187],v[120:123]
)ASM"
R"ASM(
s_waitcnt lgkmcnt(15)
RD a[220:223],v223 offset:6144
v_mov_b32 v173, v170
MM v[100:103],a[56:59],a[160:163],v[100:103]
TR a[48:49],v195 offset:32896
TR a[50:51],v195 offset:33408
MM v[100:103],a[60:63],a[164:167],v[100:103]
TR a[52:53],v195 offset:41120
TR a[54:55],v195 offset:41632
MM v[108:111],a[60:63],a[172:175],v[108:111]
v_permlane16_swap_b32 v170, v173
v_add_f32 v170, v170, v173
MM v[108:111],a[56:59],a[168:171],v[108:111]
s_waitcnt lgkmcnt(4)
s_add_u32 s88, s68, s86
s_add_u32 s86, s86, 128
MM v[116:119],a[56:59],a[176:179],v[116:119]
s_cmp_ge_u32 s86, 1280
s_cselect_b32 s86, 0, s86
s_add_u32 m0, s94, 0
v_mul_f32 v170, 0x3acccccd, v170
MM v[116:119],a[60:63],a[180:183],v[116:119]
s_add_u32 s95, s88, 0
buffer_load_dwordx4 v220, s[32:35], s95 offen offset:0 lds
MM v[124:127],a[60:63],a[188:191],v[124:127]
s_add_u32 s95, s88, 9216
CV v178,v172,v170
v_lshlrev_b32 v174, 16, v178
MM v[124:127],a[56:59],a[184:187],v[124:127]
buffer_load_dwordx4 v221, s[32:35], s95 offen offset:1024 lds
s_waitcnt vmcnt(14)
s_barrier
MM v[0:3],a[0:3],a[192:195],v[0:3]
s_add_u32 s95, s88, 18432
TR a[56:57],v195 offset:32904
TR a[58:59],v195 offset:33416
v_and_b32 v170, 0xffff0000, v178
MM v[0:3],a[4:7],a[196:199],v[0:3]
TR a[60:61],v195 offset:41128
TR a[62:63],v195 offset:41640
MM v[8:11],a[4:7],a[204:207],v[8:11]
v_add_f32 v170, 0x3727c5ac, v170
CV v170,v170,v170
MM v[8:11],a[0:3],a[200:203],v[8:11]
buffer_load_dwordx4 v220, s[32:35], s95 offen offset:2048 lds
MM v[16:19],a[0:3],a[208:211],v[16:19]
s_add_u32 s95, s88, 27648
v_and_b32 v170, 0xffff0000, v170
v_lshl_add_u32 v232, v216, 3, v232
MM v[16:19],a[4:7],a[212:215],v[16:19]
v_sqrt_f32 v170, v170
MM v[24:27],a[4:7],a[220:223],v[24:27]
v_mov_b32 v179, s69
CV v178,v174,v170
MM v[24:27],a[0:3],a[216:219],v[24:27]
buffer_load_dwordx4 v221, s[32:35], s95 offen offset:3072 lds
MM v[4:7],a[8:11],a[192:195],v[4:7]
s_sub_u32 s88, s88, s63
s_add_u32 m0, s94, 4096
s_add_u32 s95, s88, 40960
v_and_b32 v170, 0xffff0000, v178
MM v[4:7],a[12:15],a[196:199],v[4:7]
v_rcp_f32 v175, v170
MM v[12:15],a[12:15],a[204:207],v[12:15]
v_lshlrev_b32 v173, 4, v180
v_lshl_add_u32 v232, v214, 5, v232
MM v[12:15],a[8:11],a[200:203],v[12:15]
buffer_load_dwordx4 v220, s[32:35], s95 offen offset:0 lds
MM v[20:23],a[8:11],a[208:211],v[20:23]
s_add_u32 s95, s88, 50176
s_mov_b32 exec_lo, 1
s_mov_b32 exec_hi, 1
buffer_store_dwordx2 v[178:179], v173, s[20:23], s97 offen offset:0 sc1
s_mov_b64 exec, -1
MM v[20:23],a[12:15],a[212:215],v[20:23]
v_sub_f32 v148, v148, v174
v_sub_f32 v149, v149, v174
MM v[28:31],a[12:15],a[220:223],v[28:31]
v_sub_f32 v150, v150, v174
v_sub_f32 v151, v151, v174
MM v[28:31],a[8:11],a[216:219],v[28:31]
buffer_load_dwordx4 v221, s[32:35], s95 offen offset:1024 lds
v_smfmac_f32_16x16x64_bf16 v[184:187], v[236:239], a[0:7], v234
s_add_u32 s95, s88, 59392
v_sub_f32 v152, v152, v174
v_smfmac_f32_16x16x64_bf16 v[188:191], v[236:239], a[8:15], v234
v_sub_f32 v153, v153, v174
MM v[32:35],a[16:19],a[192:195],v[32:35]
TR a[0:1],v196 offset:0
TR a[2:3],v196 offset:512
MM v[32:35],a[20:23],a[196:199],v[32:35]
TR a[4:5],v196 offset:8224
TR a[6:7],v196 offset:8736
v_sub_f32 v154, v154, v174
MM v[40:43],a[20:23],a[204:207],v[40:43]
TR a[8:9],v196 offset:8
TR a[10:11],v196 offset:520
MM v[40:43],a[16:19],a[200:203],v[40:43]
TR a[12:13],v196 offset:8232
TR a[14:15],v196 offset:8744
buffer_load_dwordx4 v220, s[32:35], s95 offen offset:2048 lds
MM v[48:51],a[16:19],a[208:211],v[48:51]
s_add_u32 s95, s88, 68608
v_sub_f32 v155, v155, v174
v_sub_f32 v156, v156, v174
MM v[48:51],a[20:23],a[212:215],v[48:51]
buffer_load_dwordx4 v221, s[32:35], s95 offen offset:3072 lds
MM v[56:59],a[20:23],a[220:223],v[56:59]
s_add_u32 s87, s89, s85
s_add_u32 s85, s85, 524288
v_sub_f32 v157, v157, v174
MM v[56:59],a[16:19],a[216:219],v[56:59]
s_cmp_ge_u32 s85, 5242880
s_cselect_b32 s85, 0, s85
s_add_u32 m0, s96, 65536
MM v[36:39],a[24:27],a[192:195],v[36:39]
TR a[16:17],v197 offset:0
TR a[18:19],v197 offset:512
v_sub_f32 v158, v158, v174
MM v[36:39],a[28:31],a[196:199],v[36:39]
TR a[20:21],v197 offset:8224
TR a[22:23],v197 offset:8736
MM v[44:47],a[28:31],a[204:207],v[44:47]
buffer_load_dwordx4 v253, s[16:19], s87 offen offset:0 lds
MM v[44:47],a[24:27],a[200:203],v[44:47]
s_add_u32 m0, s96, 66560
v_sub_f32 v159, v159, v174
v_sub_f32 v160, v160, v174
MM v[52:55],a[24:27],a[208:211],v[52:55]
v_sub_f32 v161, v161, v174
v_sub_f32 v162, v162, v174
MM v[52:55],a[28:31],a[212:215],v[52:55]
buffer_load_dwordx4 v254, s[16:19], s87 offen offset:0 lds
MM v[60:63],a[28:31],a[220:223],v[60:63]
s_add_u32 s87, s87, 65536
s_add_u32 m0, s96, 67592
v_sub_f32 v163, v163, v174
MM v[60:63],a[24:27],a[216:219],v[60:63]
v_sub_f32 v164, v164, v174
v_sub_f32 v165, v165, v174
MM v[64:67],a[32:35],a[192:195],v[64:67]
TR a[24:25],v197 offset:8
TR a[26:27],v197 offset:520
MM v[64:67],a[36:39],a[196:199],v[64:67]
TR a[28:29],v197 offset:8232
TR a[30:31],v197 offset:8744
v_sub_f32 v166, v166, v174
MM v[72:75],a[36:39],a[204:207],v[72:75]
buffer_load_dwordx4 v253, s[16:19], s87 offen offset:0 lds
MM v[72:75],a[32:35],a[200:203],v[72:75]
s_add_u32 m0, s96, 68616
v_sub_f32 v167, v167, v174
CV v128,v148,v149
MM v[80:83],a[32:35],a[208:211],v[80:83]
CV v129,v150,v151
CV v130,v152,v153
MM v[80:83],a[36:39],a[212:215],v[80:83]
buffer_load_dwordx4 v254, s[16:19], s87 offen offset:0 lds
MM v[88:91],a[36:39],a[220:223],v[88:91]
s_waitcnt vmcnt(17)
RD a[224:227],v222 offset:32768
CV v131,v154,v155
MM v[88:91],a[32:35],a[216:219],v[88:91]
RD a[228:231],v223 offset:32768
CV v132,v156,v157
MM v[68:71],a[40:43],a[192:195],v[68:71]
TR a[32:33],v198 offset:0
TR a[34:35],v198 offset:512
MM v[68:71],a[44:47],a[196:199],v[68:71]
TR a[36:37],v198 offset:8224
TR a[38:39],v198 offset:8736
CV v133,v158,v159
MM v[76:79],a[44:47],a[204:207],v[76:79]
RD a[232:235],v222 offset:34816
CV v134,v160,v161
MM v[76:79],a[40:43],a[200:203],v[76:79]
RD a[236:239],v223 offset:34816
CV v135,v162,v163
MM v[84:87],a[40:43],a[208:211],v[84:87]
RD a[240:243],v222 offset:36864
CV v136,v164,v165
MM v[84:87],a[44:47],a[212:215],v[84:87]
RD a[244:247],v223 offset:36864
CV v137,v166,v167
MM v[92:95],a[44:47],a[220:223],v[92:95]
RD a[248:251],v222 offset:38912
v_lshlrev_b32 v148, 16, v128
MM v[92:95],a[40:43],a[216:219],v[92:95]
s_waitcnt lgkmcnt(15)
RD a[252:255],v223 offset:38912
v_and_b32 v149, 0xffff0000, v128
MM v[96:99],a[48:51],a[192:195],v[96:99]
TR a[40:41],v198 offset:8
TR a[42:43],v198 offset:520
MM v[96:99],a[52:55],a[196:199],v[96:99]
TR a[44:45],v198 offset:8232
TR a[46:47],v198 offset:8744
v_lshlrev_b32 v150, 16, v129
MM v[104:107],a[52:55],a[204:207],v[104:107]
v_and_b32 v151, 0xffff0000, v129
v_lshlrev_b32 v152, 16, v130
MM v[104:107],a[48:51],a[200:203],v[104:107]
s_waitcnt lgkmcnt(4)
s_add_u32 s88, s68, s86
s_add_u32 s86, s86, 128
MM v[112:115],a[48:51],a[208:211],v[112:115]
s_cmp_ge_u32 s86, 1280
s_cselect_b32 s86, 0, s86
s_add_u32 m0, s94, 32768
v_and_b32 v153, 0xffff0000, v130
MM v[112:115],a[52:55],a[212:215],v[112:115]
s_add_u32 s95, s88, 0
buffer_load_dwordx4 v220, s[32:35], s95 offen offset:0 lds
MM v[120:123],a[52:55],a[220:223],v[120:123]
s_add_u32 s95, s88, 9216
v_lshlrev_b32 v154, 16, v131
v_and_b32 v155, 0xffff0000, v131
MM v[120:123],a[48:51],a[216:219],v[120:123]
buffer_load_dwordx4 v221, s[32:35], s95 offen offset:1024 lds
MM v[100:103],a[56:59],a[192:195],v[100:103]
s_add_u32 s95, s88, 18432
TR a[48:49],v199 offset:0
TR a[50:51],v199 offset:512
v_lshlrev_b32 v156, 16, v132
MM v[100:103],a[60:63],a[196:199],v[100:103]
TR a[52:53],v199 offset:8224
TR a[54:55],v199 offset:8736
MM v[108:111],a[60:63],a[204:207],v[108:111]
v_and_b32 v157, 0xffff0000, v132
v_lshlrev_b32 v158, 16, v133
MM v[108:111],a[56:59],a[200:203],v[108:111]
buffer_load_dwordx4 v220, s[32:35], s95 offen offset:2048 lds
MM v[116:119],a[56:59],a[208:211],v[116:119]
s_add_u32 s95, s88, 27648
v_and_b32 v159, 0xffff0000, v133
v_lshlrev_b32 v160, 16, v134
MM v[116:119],a[60:63],a[212:215],v[116:119]
v_and_b32 v161, 0xffff0000, v134
v_lshlrev_b32 v162, 16, v135
MM v[124:127],a[60:63],a[220:223],v[124:127]
v_and_b32 v163, 0xffff0000, v135
v_lshlrev_b32 v164, 16, v136
MM v[124:127],a[56:59],a[216:219],v[124:127]
buffer_load_dwordx4 v221, s[32:35], s95 offen offset:3072 lds
s_waitcnt vmcnt(17)
s_barrier
MM v[0:3],a[0:3],a[224:227],v[0:3]
s_sub_u32 s88, s88, s63
TR a[56:57],v199 offset:8
TR a[58:59],v199 offset:520
v_and_b32 v165, 0xffff0000, v136
MM v[0:3],a[4:7],a[228:231],v[0:3]
s_add_u32 m0, s94, 36864
TR a[60:61],v199 offset:8232
TR a[62:63],v199 offset:8744
MM v[8:11],a[4:7],a[236:239],v[8:11]
s_add_u32 s95, s88, 40960
v_lshlrev_b32 v166, 16, v137
v_and_b32 v167, 0xffff0000, v137
MM v[8:11],a[0:3],a[232:235],v[8:11]
buffer_load_dwordx4 v220, s[32:35], s95 offen offset:0 lds
MM v[16:19],a[0:3],a[240:243],v[16:19]
s_add_u32 s95, s88, 50176
v_mul_f32 v148, v148, v175
v_mul_f32 v149, v149, v175
MM v[16:19],a[4:7],a[244:247],v[16:19]
v_mul_f32 v150, v150, v175
v_mul_f32 v151, v151, v175
MM v[24:27],a[4:7],a[252:255],v[24:27]
v_mul_f32 v152, v152, v175
v_mul_f32 v153, v153, v175
MM v[24:27],a[0:3],a[248:251],v[24:27]
buffer_load_dwordx4 v221, s[32:35], s95 offen offset:1024 lds
MM v[4:7],a[8:11],a[224:227],v[4:7]
s_add_u32 s95, s88, 59392
v_mul_f32 v154, v154, v175
v_mul_f32 v155, v155, v175
MM v[4:7],a[12:15],a[228:231],v[4:7]
v_mul_f32 v156, v156, v175
v_mul_f32 v157, v157, v175
MM v[12:15],a[12:15],a[236:239],v[12:15]
v_mul_f32 v158, v158, v175
v_mul_f32 v159, v159, v175
MM v[12:15],a[8:11],a[232:235],v[12:15]
buffer_load_dwordx4 v220, s[32:35], s95 offen offset:2048 lds
MM v[20:23],a[8:11],a[240:243],v[20:23]
s_add_u32 s95, s88, 68608
v_mul_f32 v160, v160, v175
v_mul_f32 v161, v161, v175
MM v[20:23],a[12:15],a[244:247],v[20:23]
v_mul_f32 v162, v162, v175
v_mul_f32 v163, v163, v175
MM v[28:31],a[12:15],a[252:255],v[28:31]
v_mul_f32 v164, v164, v175
v_mul_f32 v165, v165, v175
MM v[28:31],a[8:11],a[248:251],v[28:31]
buffer_load_dwordx4 v221, s[32:35], s95 offen offset:3072 lds
v_smfmac_f32_16x16x64_bf16 v[184:187], v[236:239], a[0:7], v234
s_add_u32 s87, s89, s85
s_add_u32 s85, s85, 524288
v_mul_f32 v166, v166, v175
v_smfmac_f32_16x16x64_bf16 v[188:191], v[236:239], a[8:15], v234
s_cmp_ge_u32 s85, 5242880
s_cselect_b32 s85, 0, s85
MM v[32:35],a[16:19],a[224:227],v[32:35]
s_add_u32 m0, s96, 81984
TR a[0:1],v196 offset:16448
TR a[2:3],v196 offset:16960
MM v[32:35],a[20:23],a[228:231],v[32:35]
TR a[4:5],v196 offset:24672
TR a[6:7],v196 offset:25184
v_mul_f32 v167, v167, v175
MM v[40:43],a[20:23],a[236:239],v[40:43]
TR a[8:9],v196 offset:16456
TR a[10:11],v196 offset:16968
MM v[40:43],a[16:19],a[232:235],v[40:43]
TR a[12:13],v196 offset:24680
TR a[14:15],v196 offset:25192
buffer_load_dwordx4 v253, s[16:19], s87 offen offset:0 lds
MM v[48:51],a[16:19],a[240:243],v[48:51]
s_add_u32 m0, s96, 83008
CV v128,v148,v149
CV v129,v150,v151
MM v[48:51],a[20:23],a[244:247],v[48:51]
buffer_load_dwordx4 v254, s[16:19], s87 offen offset:0 lds
MM v[56:59],a[20:23],a[252:255],v[56:59]
s_add_u32 s87, s87, 65536
s_add_u32 m0, s96, 84040
CV v130,v152,v153
MM v[56:59],a[16:19],a[248:251],v[56:59]
CV v131,v154,v155
CV v132,v156,v157
MM v[36:39],a[24:27],a[224:227],v[36:39]
TR a[16:17],v197 offset:16448
TR a[18:19],v197 offset:16960
MM v[36:39],a[28:31],a[228:231],v[36:39]
TR a[20:21],v197 offset:24672
TR a[22:23],v197 offset:25184
CV v133,v158,v159
MM v[44:47],a[28:31],a[236:239],v[44:47]
buffer_load_dwordx4 v253, s[16:19], s87 offen offset:0 lds
MM v[44:47],a[24:27],a[232:235],v[44:47]
s_add_u32 m0, s96, 85064
CV v134,v160,v161
CV v135,v162,v163
MM v[52:55],a[24:27],a[240:243],v[52:55]
CV v136,v164,v165
CV v137,v166,v167
MM v[52:55],a[28:31],a[244:247],v[52:55]
buffer_load_dwordx4 v254, s[16:19], s87 offen offset:0 lds
MM v[60:63],a[28:31],a[252:255],v[60:63]
s_waitcnt vmcnt(16)
RD a[128:131],v222 offset:0
v_xor_b32 v233, 32, v232
MM v[60:63],a[24:27],a[248:251],v[60:63]
s_add_u32 s77, %[slds], 65536
RD a[132:135],v223 offset:0
v_add_u32 v232, s77, v232
MM v[64:67],a[32:35],a[224:227],v[64:67]
TR a[24:25],v197 offset:16456
)ASM"
R"ASM(
TR a[26:27],v197 offset:16968
MM v[64:67],a[36:39],a[228:231],v[64:67]
TR a[28:29],v197 offset:24680
TR a[30:31],v197 offset:25192
MM v[72:75],a[36:39],a[236:239],v[72:75]
RD a[136:139],v222 offset:2048
v_add_u32 v233, s77, v233
MM v[72:75],a[32:35],a[232:235],v[72:75]
RD a[140:143],v223 offset:2048
buffer_store_dwordx4 v[128:131], v168, s[20:23], s82 offen offset:0 sc1
v_lshlrev_b32 v214, 8, v211
MM v[80:83],a[32:35],a[240:243],v[80:83]
RD a[144:147],v222 offset:4096
v_add_u32 v214, %[slds], v214
MM v[80:83],a[36:39],a[244:247],v[80:83]
RD a[148:151],v223 offset:4096
v_xor_b32 v215, v211, v212
MM v[88:91],a[36:39],a[252:255],v[88:91]
s_lshl_b32 s77, %[swave], 2
RD a[152:155],v222 offset:6144
v_xor_b32 v213, s77, v215
MM v[88:91],a[32:35],a[248:251],v[88:91]
s_waitcnt lgkmcnt(15)
RD a[156:159],v223 offset:6144
buffer_store_dwordx4 v[132:135], v168, s[20:23], s82 offen offset:512 sc1
v_xor_b32 v216, 0, v213
MM v[68:71],a[40:43],a[224:227],v[68:71]
TR a[32:33],v198 offset:16448
TR a[34:35],v198 offset:16960
MM v[68:71],a[44:47],a[228:231],v[68:71]
TR a[36:37],v198 offset:24672
TR a[38:39],v198 offset:25184
MM v[76:79],a[44:47],a[236:239],v[76:79]
buffer_store_dwordx2 v[136:137], v181, s[20:23], s82 offen offset:1024 sc1
MM v[76:79],a[40:43],a[232:235],v[76:79]
s_waitcnt lgkmcnt(4)
s_add_u32 s88, s68, s86
s_add_u32 s86, s86, 128
MM v[84:87],a[40:43],a[240:243],v[84:87]
s_cmp_ge_u32 s86, 1280
s_cselect_b32 s86, 0, s86
MM v[84:87],a[44:47],a[244:247],v[84:87]
s_add_u32 m0, s94, 0
s_add_u32 s95, s88, 0
v_lshlrev_b32 v148, 16, v128
MM v[92:95],a[44:47],a[252:255],v[92:95]
buffer_load_dwordx4 v220, s[32:35], s95 offen offset:0 lds
MM v[92:95],a[40:43],a[248:251],v[92:95]
s_add_u32 s95, s88, 9216
v_and_b32 v149, 0xffff0000, v128
v_lshlrev_b32 v176, 16, v138
MM v[96:99],a[48:51],a[224:227],v[96:99]
TR a[40:41],v198 offset:16456
TR a[42:43],v198 offset:16968
MM v[96:99],a[52:55],a[228:231],v[96:99]
TR a[44:45],v198 offset:24680
TR a[46:47],v198 offset:25192
v_and_b32 v177, 0xffff0000, v138
MM v[104:107],a[52:55],a[236:239],v[104:107]
buffer_load_dwordx4 v221, s[32:35], s95 offen offset:1024 lds
MM v[104:107],a[48:51],a[232:235],v[104:107]
s_add_u32 s95, s88, 18432
v_mul_f32 v170, v148, v176
v_mul_f32 v171, v149, v177
MM v[112:115],a[48:51],a[240:243],v[112:115]
buffer_load_dwordx4 v220, s[32:35], s95 offen offset:2048 lds
MM v[112:115],a[52:55],a[244:247],v[112:115]
s_add_u32 s95, s88, 27648
v_lshlrev_b32 v148, 16, v129
v_and_b32 v149, 0xffff0000, v129
MM v[120:123],a[52:55],a[252:255],v[120:123]
v_lshlrev_b32 v176, 16, v139
v_and_b32 v177, 0xffff0000, v139
MM v[120:123],a[48:51],a[248:251],v[120:123]
v_fma_f32 v170, v148, v176, v170
v_fma_f32 v171, v149, v177, v171
MM v[100:103],a[56:59],a[224:227],v[100:103]
TR a[48:49],v199 offset:16448
TR a[50:51],v199 offset:16960
MM v[100:103],a[60:63],a[228:231],v[100:103]
TR a[52:53],v199 offset:24672
TR a[54:55],v199 offset:25184
v_lshlrev_b32 v148, 16, v130
MM v[108:111],a[60:63],a[236:239],v[108:111]
buffer_load_dwordx4 v221, s[32:35], s95 offen offset:3072 lds
MM v[108:111],a[56:59],a[232:235],v[108:111]
s_sub_u32 s88, s88, s63
s_add_u32 m0, s94, 4096
s_add_u32 s95, s88, 40960
MM v[116:119],a[56:59],a[240:243],v[116:119]
LPG2_%=:
s_cbranch_execz LPG6_%=
buffer_load_dwordx4 v220, s[32:35], s95 offen offset:0 lds
v_and_b32 v149, 0xffff0000, v130
MM v[116:119],a[60:63],a[244:247],v[116:119]
s_add_u32 s95, s88, 50176
v_lshlrev_b32 v176, 16, v140
v_and_b32 v177, 0xffff0000, v140
MM v[124:127],a[60:63],a[252:255],v[124:127]
v_fma_f32 v170, v148, v176, v170
v_fma_f32 v171, v149, v177, v171
MM v[124:127],a[56:59],a[248:251],v[124:127]
v_lshlrev_b32 v148, 16, v131
v_and_b32 v149, 0xffff0000, v131
s_waitcnt vmcnt(20)
s_barrier
MM v[0:3],a[0:3],a[128:131],v[0:3]
TR a[56:57],v199 offset:16456
TR a[58:59],v199 offset:16968
MM v[0:3],a[4:7],a[132:135],v[0:3]
TR a[60:61],v199 offset:24680
TR a[62:63],v199 offset:25192
v_lshlrev_b32 v176, 16, v141
MM v[8:11],a[4:7],a[140:143],v[8:11]
buffer_load_dwordx4 v221, s[32:35], s95 offen offset:1024 lds
MM v[8:11],a[0:3],a[136:139],v[8:11]
s_add_u32 s95, s88, 59392
v_and_b32 v177, 0xffff0000, v141
v_fma_f32 v170, v148, v176, v170
MM v[16:19],a[0:3],a[144:147],v[16:19]
buffer_load_dwordx4 v220, s[32:35], s95 offen offset:2048 lds
MM v[16:19],a[4:7],a[148:151],v[16:19]
s_add_u32 s95, s88, 68608
v_fma_f32 v171, v149, v177, v171
v_lshlrev_b32 v148, 16, v132
MM v[24:27],a[4:7],a[156:159],v[24:27]
v_and_b32 v149, 0xffff0000, v132
v_lshlrev_b32 v176, 16, v142
MM v[24:27],a[0:3],a[152:155],v[24:27]
v_and_b32 v177, 0xffff0000, v142
v_fma_f32 v170, v148, v176, v170
MM v[4:7],a[8:11],a[128:131],v[4:7]
buffer_load_dwordx4 v221, s[32:35], s95 offen offset:3072 lds
MM v[4:7],a[12:15],a[132:135],v[4:7]
s_add_u32 s87, s89, s85
s_add_u32 s85, s85, 524288
v_fma_f32 v171, v149, v177, v171
MM v[12:15],a[12:15],a[140:143],v[12:15]
s_cmp_ge_u32 s85, 5242880
s_cselect_b32 s85, 0, s85
s_add_u32 m0, s96, 98432
MM v[12:15],a[8:11],a[136:139],v[12:15]
v_lshlrev_b32 v148, 16, v133
v_and_b32 v149, 0xffff0000, v133
MM v[20:23],a[8:11],a[144:147],v[20:23]
buffer_load_dwordx4 v253, s[16:19], s87 offen offset:0 lds
MM v[20:23],a[12:15],a[148:151],v[20:23]
s_add_u32 m0, s96, 99456
v_lshlrev_b32 v176, 16, v143
v_and_b32 v177, 0xffff0000, v143
MM v[28:31],a[12:15],a[156:159],v[28:31]
v_fma_f32 v170, v148, v176, v170
v_fma_f32 v171, v149, v177, v171
MM v[28:31],a[8:11],a[152:155],v[28:31]
v_lshlrev_b32 v148, 16, v134
v_and_b32 v149, 0xffff0000, v134
v_smfmac_f32_16x16x64_bf16 v[184:187], v[236:239], a[0:7], v234
v_lshlrev_b32 v176, 16, v144
v_smfmac_f32_16x16x64_bf16 v[188:191], v[236:239], a[8:15], v234
v_and_b32 v177, 0xffff0000, v144
MM v[32:35],a[16:19],a[128:131],v[32:35]
TR a[0:1],v192 offset:0
TR a[2:3],v192 offset:512
buffer_load_dwordx4 v254, s[16:19], s87 offen offset:0 lds
MM v[32:35],a[20:23],a[132:135],v[32:35]
s_add_u32 s87, s87, 65536
TR a[4:5],v192 offset:8224
TR a[6:7],v192 offset:8736
v_fma_f32 v170, v148, v176, v170
MM v[40:43],a[20:23],a[140:143],v[40:43]
s_add_u32 m0, s96, 100488
TR a[8:9],v192 offset:8
TR a[10:11],v192 offset:520
MM v[40:43],a[16:19],a[136:139],v[40:43]
TR a[12:13],v192 offset:8232
TR a[14:15],v192 offset:8744
MM v[48:51],a[16:19],a[144:147],v[48:51]
buffer_load_dwordx4 v253, s[16:19], s87 offen offset:0 lds
v_fma_f32 v171, v149, v177, v171
MM v[48:51],a[20:23],a[148:151],v[48:51]
s_add_u32 m0, s96, 101512
v_lshlrev_b32 v148, 16, v135
v_and_b32 v149, 0xffff0000, v135
MM v[56:59],a[20:23],a[156:159],v[56:59]
buffer_load_dwordx4 v254, s[16:19], s87 offen offset:0 lds
MM v[56:59],a[16:19],a[152:155],v[56:59]
s_waitcnt vmcnt(19)
RD a[160:163],v222 offset:32768
v_lshlrev_b32 v176, 16, v145
MM v[36:39],a[24:27],a[128:131],v[36:39]
TR a[16:17],v193 offset:0
TR a[18:19],v193 offset:512
MM v[36:39],a[28:31],a[132:135],v[36:39]
TR a[20:21],v193 offset:8224
TR a[22:23],v193 offset:8736
v_and_b32 v177, 0xffff0000, v145
MM v[44:47],a[28:31],a[140:143],v[44:47]
RD a[164:167],v223 offset:32768
v_fma_f32 v170, v148, v176, v170
MM v[44:47],a[24:27],a[136:139],v[44:47]
RD a[168:171],v222 offset:34816
v_fma_f32 v171, v149, v177, v171
MM v[52:55],a[24:27],a[144:147],v[52:55]
RD a[172:175],v223 offset:34816
v_lshlrev_b32 v148, 16, v136
MM v[52:55],a[28:31],a[148:151],v[52:55]
RD a[176:179],v222 offset:36864
v_and_b32 v149, 0xffff0000, v136
MM v[60:63],a[28:31],a[156:159],v[60:63]
RD a[180:183],v223 offset:36864
v_lshlrev_b32 v176, 16, v146
MM v[60:63],a[24:27],a[152:155],v[60:63]
s_waitcnt lgkmcnt(15)
RD a[184:187],v222 offset:38912
v_and_b32 v177, 0xffff0000, v146
MM v[64:67],a[32:35],a[128:131],v[64:67]
TR a[24:25],v193 offset:8
TR a[26:27],v193 offset:520
MM v[64:67],a[36:39],a[132:135],v[64:67]
TR a[28:29],v193 offset:8232
TR a[30:31],v193 offset:8744
v_fma_f32 v170, v148, v176, v170
MM v[72:75],a[36:39],a[140:143],v[72:75]
RD a[188:191],v223 offset:38912
v_fma_f32 v171, v149, v177, v171
MM v[72:75],a[32:35],a[136:139],v[72:75]
v_lshlrev_b32 v148, 16, v137
v_and_b32 v149, 0xffff0000, v137
MM v[80:83],a[32:35],a[144:147],v[80:83]
v_lshlrev_b32 v176, 16, v147
v_and_b32 v177, 0xffff0000, v147
MM v[80:83],a[36:39],a[148:151],v[80:83]
v_fma_f32 v170, v148, v176, v170
v_fma_f32 v171, v149, v177, v171
MM v[88:91],a[36:39],a[156:159],v[88:91]
s_waitcnt lgkmcnt(0)
s_add_u32 s88, s68, s86
s_add_u32 s86, s86, 128
MM v[88:91],a[32:35],a[152:155],v[88:91]
s_cmp_ge_u32 s86, 1280
s_cselect_b32 s86, 0, s86
s_add_u32 m0, s94, 32768
v_add_f32 v170, v170, v171
MM v[68:71],a[40:43],a[128:131],v[68:71]
s_add_u32 s95, s88, 0
TR a[32:33],v194 offset:0
TR a[34:35],v194 offset:512
MM v[68:71],a[44:47],a[132:135],v[68:71]
TR a[36:37],v194 offset:8224
TR a[38:39],v194 offset:8736
MM v[76:79],a[44:47],a[140:143],v[76:79]
buffer_load_dwordx4 v220, s[32:35], s95 offen offset:0 lds
v_add_f32_dpp v170, v170, v170 row_ror:8 row_mask:0xf bank_mask:0xf
MM v[76:79],a[40:43],a[136:139],v[76:79]
s_add_u32 s95, s88, 9216
v_add_f32_dpp v170, v170, v170 row_ror:4 row_mask:0xf bank_mask:0xf
v_lshl_add_u32 v224, v216, 4, v214
MM v[84:87],a[40:43],a[144:147],v[84:87]
buffer_load_dwordx4 v221, s[32:35], s95 offen offset:1024 lds
MM v[84:87],a[44:47],a[148:151],v[84:87]
s_add_u32 s95, s88, 18432
v_add_f32_dpp v170, v170, v170 row_ror:2 row_mask:0xf bank_mask:0xf
v_xor_b32 v216, 4, v213
MM v[92:95],a[44:47],a[156:159],v[92:95]
v_add_f32_dpp v170, v170, v170 row_ror:1 row_mask:0xf bank_mask:0xf
v_mov_b32 v173, v170
MM v[92:95],a[40:43],a[152:155],v[92:95]
s_nop 0
v_permlane16_swap_b32 v170, v173
v_add_f32 v170, v170, v173
MM v[96:99],a[48:51],a[128:131],v[96:99]
TR a[40:41],v194 offset:8
TR a[42:43],v194 offset:520
MM v[96:99],a[52:55],a[132:135],v[96:99]
TR a[44:45],v194 offset:8232
TR a[46:47],v194 offset:8744
CV v170,v170,v170
MM v[104:107],a[52:55],a[140:143],v[104:107]
buffer_load_dwordx4 v220, s[32:35], s95 offen offset:2048 lds
MM v[104:107],a[48:51],a[136:139],v[104:107]
s_add_u32 s95, s88, 27648
v_ashrrev_i32 v171, 31, v170
v_or_b32 v171, 0x80000000, v171
MM v[112:115],a[48:51],a[144:147],v[112:115]
buffer_load_dwordx4 v221, s[32:35], s95 offen offset:3072 lds
MM v[112:115],a[52:55],a[148:151],v[112:115]
s_sub_u32 s88, s88, s63
s_add_u32 m0, s94, 36864
s_add_u32 s95, s88, 40960
v_xor_b32 v170, v170, v171
MM v[120:123],a[52:55],a[156:159],v[120:123]
v_and_b32 v170, 0xffff0000, v170
v_sub_u32 v171, s79, v180
MM v[120:123],a[48:51],a[152:155],v[120:123]
v_or_b32 v170, v170, v171
v_lshlrev_b32 v173, 2, v180
MM v[100:103],a[56:59],a[128:131],v[100:103]
TR a[48:49],v195 offset:0
TR a[50:51],v195 offset:512
MM v[100:103],a[60:63],a[132:135],v[100:103]
TR a[52:53],v195 offset:8224
TR a[54:55],v195 offset:8736
MM v[108:111],a[60:63],a[140:143],v[108:111]
buffer_load_dwordx4 v220, s[32:35], s95 offen offset:0 lds
s_mov_b32 exec_lo, 1
s_mov_b32 exec_hi, 1
buffer_store_dword v170, v173, s[20:23], s78 offen offset:0 sc1
s_mov_b64 exec, -1
MM v[108:111],a[56:59],a[136:139],v[108:111]
s_add_u32 s95, s88, 50176
v_mov_b32 v171, s69
v_lshl_add_u32 v225, v216, 4, v214
MM v[116:119],a[56:59],a[144:147],v[116:119]
buffer_load_dwordx4 v221, s[32:35], s95 offen offset:1024 lds
MM v[116:119],a[60:63],a[148:151],v[116:119]
s_add_u32 s95, s88, 59392
v_xor_b32 v216, 8, v213
v_lshl_add_u32 v226, v216, 4, v214
MM v[124:127],a[60:63],a[156:159],v[124:127]
v_xor_b32 v216, 12, v213
v_lshl_add_u32 v227, v216, 4, v214
MM v[124:127],a[56:59],a[152:155],v[124:127]
v_lshlrev_b32 v214, 8, v212
v_xor_b32 v216, 0, v215
s_waitcnt vmcnt(22)
s_barrier
MM v[0:3],a[0:3],a[160:163],v[0:3]
TR a[56:57],v195 offset:8
TR a[58:59],v195 offset:520
MM v[0:3],a[4:7],a[164:167],v[0:3]
TR a[60:61],v195 offset:8232
TR a[62:63],v195 offset:8744
MM v[8:11],a[4:7],a[172:175],v[8:11]
buffer_load_dwordx4 v220, s[32:35], s95 offen offset:2048 lds
MM v[8:11],a[0:3],a[168:171],v[8:11]
s_add_u32 s95, s88, 68608
v_lshl_add_u32 v228, v216, 4, v214
v_xor_b32 v216, 4, v215
MM v[16:19],a[0:3],a[176:179],v[16:19]
buffer_load_dwordx4 v221, s[32:35], s95 offen offset:3072 lds
MM v[16:19],a[4:7],a[180:183],v[16:19]
s_add_u32 s87, s89, s85
)ASM"
R"ASM(
s_add_u32 s85, s85, 524288
v_lshl_add_u32 v229, v216, 4, v214
MM v[24:27],a[4:7],a[188:191],v[24:27]
s_cmp_ge_u32 s85, 5242880
s_cselect_b32 s85, 0, s85
s_add_u32 m0, s96, 114880
MM v[24:27],a[0:3],a[184:187],v[24:27]
v_xor_b32 v216, 8, v215
v_lshl_add_u32 v230, v216, 4, v214
MM v[4:7],a[8:11],a[160:163],v[4:7]
buffer_load_dwordx4 v253, s[16:19], s87 offen offset:0 lds
MM v[4:7],a[12:15],a[164:167],v[4:7]
s_add_u32 m0, s96, 115904
v_xor_b32 v216, 12, v215
v_lshl_add_u32 v231, v216, 4, v214
MM v[12:15],a[12:15],a[172:175],v[12:15]
s_lshl_b32 s90, s49, 19
s_add_u32 s8, s8, s90
s_addc_u32 s9, s9, 0
MM v[12:15],a[8:11],a[168:171],v[12:15]
s_mov_b32 s10, 524288
s_mov_b32 s11, 131072
MM v[20:23],a[8:11],a[176:179],v[20:23]
buffer_load_dwordx4 v254, s[16:19], s87 offen offset:0 lds
MM v[20:23],a[12:15],a[180:183],v[20:23]
s_add_u32 s87, s87, 65536
s_add_u32 s12, s12, s90
s_addc_u32 s13, s13, 0
MM v[28:31],a[12:15],a[188:191],v[28:31]
s_add_u32 m0, s96, 116936
s_mov_b32 s14, 525312
s_mov_b32 s15, 131072
MM v[28:31],a[8:11],a[184:187],v[28:31]
s_sub_u32 s12, s12, 1024
s_subb_u32 s13, s13, 0
s_lshr_b32 s90, s49, 3
v_smfmac_f32_16x16x64_bf16 v[184:187], v[236:239], a[0:7], v234
s_mul_i32 s90, s90, 1310720
s_mul_i32 s77, s84, 320
v_smfmac_f32_16x16x64_bf16 v[188:191], v[236:239], a[8:15], v234
s_add_u32 s90, s90, s77
MM v[32:35],a[16:19],a[160:163],v[32:35]
TR a[0:1],v192 offset:16448
TR a[2:3],v192 offset:16960
buffer_load_dwordx4 v253, s[16:19], s87 offen offset:0 lds
MM v[32:35],a[20:23],a[164:167],v[32:35]
s_add_u32 m0, s96, 117960
TR a[4:5],v192 offset:24672
TR a[6:7],v192 offset:25184
MM v[40:43],a[20:23],a[172:175],v[40:43]
TR a[8:9],v192 offset:16456
TR a[10:11],v192 offset:16968
MM v[40:43],a[16:19],a[168:171],v[40:43]
TR a[12:13],v192 offset:24680
TR a[14:15],v192 offset:25192
MM v[48:51],a[16:19],a[176:179],v[48:51]
buffer_load_dwordx4 v254, s[16:19], s87 offen offset:0 lds
MM v[48:51],a[20:23],a[180:183],v[48:51]
s_waitcnt vmcnt(17)
s_add_u32 s24, s24, s90
s_addc_u32 s25, s25, 0
MM v[56:59],a[20:23],a[188:191],v[56:59]
s_sub_u32 s26, 5242880, s90
RD a[192:195],v222 offset:0
s_mov_b32 s27, 131072
MM v[56:59],a[16:19],a[184:187],v[56:59]
s_mov_b32 s38, 2211840
RD a[196:199],v223 offset:0
s_mov_b32 s39, 131072
MM v[36:39],a[24:27],a[160:163],v[36:39]
s_mov_b32 s42, 2621440
TR a[16:17],v193 offset:16448
TR a[18:19],v193 offset:16960
MM v[36:39],a[28:31],a[164:167],v[36:39]
s_mov_b32 s43, 131072
TR a[20:21],v193 offset:24672
TR a[22:23],v193 offset:25184
MM v[44:47],a[28:31],a[172:175],v[44:47]
s_mov_b32 s71, 0x4e800000
RD a[200:203],v222 offset:2048
s_mov_b32 s72, 0x3e0293ee
MM v[44:47],a[24:27],a[168:171],v[44:47]
s_lshl_b32 s64, %[swave], 12
RD a[204:207],v223 offset:2048
MM v[52:55],a[24:27],a[176:179],v[52:55]
RD a[208:211],v222 offset:4096
MM v[52:55],a[28:31],a[180:183],v[52:55]
RD a[212:215],v223 offset:4096
MM v[60:63],a[28:31],a[188:191],v[60:63]
RD a[216:219],v222 offset:6144
MM v[60:63],a[24:27],a[184:187],v[60:63]
s_waitcnt lgkmcnt(15)
RD a[220:223],v223 offset:6144
MM v[64:67],a[32:35],a[160:163],v[64:67]
TR a[24:25],v193 offset:16456
TR a[26:27],v193 offset:16968
MM v[64:67],a[36:39],a[164:167],v[64:67]
TR a[28:29],v193 offset:24680
TR a[30:31],v193 offset:25192
MM v[72:75],a[36:39],a[172:175],v[72:75]
MM v[72:75],a[32:35],a[168:171],v[72:75]
s_waitcnt lgkmcnt(4)
s_add_u32 s88, s68, s86
s_add_u32 s86, s86, 128
MM v[80:83],a[32:35],a[176:179],v[80:83]
s_cmp_ge_u32 s86, 1280
s_cselect_b32 s86, 0, s86
s_add_u32 m0, s94, 0
MM v[80:83],a[36:39],a[180:183],v[80:83]
s_add_u32 s95, s88, 0
buffer_load_dwordx4 v220, s[32:35], s95 offen offset:0 lds
MM v[88:91],a[36:39],a[188:191],v[88:91]
s_add_u32 s95, s88, 9216
MM v[88:91],a[32:35],a[184:187],v[88:91]
buffer_load_dwordx4 v221, s[32:35], s95 offen offset:1024 lds
MM v[68:71],a[40:43],a[160:163],v[68:71]
s_add_u32 s95, s88, 18432
TR a[32:33],v194 offset:16448
TR a[34:35],v194 offset:16960
MM v[68:71],a[44:47],a[164:167],v[68:71]
TR a[36:37],v194 offset:24672
TR a[38:39],v194 offset:25184
MM v[76:79],a[44:47],a[172:175],v[76:79]
MM v[76:79],a[40:43],a[168:171],v[76:79]
buffer_load_dwordx4 v220, s[32:35], s95 offen offset:2048 lds
MM v[84:87],a[40:43],a[176:179],v[84:87]
s_add_u32 s95, s88, 27648
MM v[84:87],a[44:47],a[180:183],v[84:87]
MM v[92:95],a[44:47],a[188:191],v[92:95]
MM v[92:95],a[40:43],a[184:187],v[92:95]
buffer_load_dwordx4 v221, s[32:35], s95 offen offset:3072 lds
MM v[96:99],a[48:51],a[160:163],v[96:99]
s_sub_u32 s88, s88, s63
TR a[40:41],v194 offset:16456
TR a[42:43],v194 offset:16968
MM v[96:99],a[52:55],a[164:167],v[96:99]
s_add_u32 m0, s94, 4096
TR a[44:45],v194 offset:24680
TR a[46:47],v194 offset:25192
MM v[104:107],a[52:55],a[172:175],v[104:107]
s_add_u32 s95, s88, 40960
MM v[104:107],a[48:51],a[168:171],v[104:107]
buffer_load_dwordx4 v220, s[32:35], s95 offen offset:0 lds
MM v[112:115],a[48:51],a[176:179],v[112:115]
s_add_u32 s95, s88, 50176
MM v[112:115],a[52:55],a[180:183],v[112:115]
MM v[120:123],a[52:55],a[188:191],v[120:123]
MM v[120:123],a[48:51],a[184:187],v[120:123]
buffer_load_dwordx4 v221, s[32:35], s95 offen offset:1024 lds
MM v[100:103],a[56:59],a[160:163],v[100:103]
s_add_u32 s95, s88, 59392
TR a[48:49],v195 offset:16448
TR a[50:51],v195 offset:16960
MM v[100:103],a[60:63],a[164:167],v[100:103]
TR a[52:53],v195 offset:24672
TR a[54:55],v195 offset:25184
MM v[108:111],a[60:63],a[172:175],v[108:111]
MM v[108:111],a[56:59],a[168:171],v[108:111]
buffer_load_dwordx4 v220, s[32:35], s95 offen offset:2048 lds
MM v[116:119],a[56:59],a[176:179],v[116:119]
s_add_u32 s95, s88, 68608
MM v[116:119],a[60:63],a[180:183],v[116:119]
MM v[124:127],a[60:63],a[188:191],v[124:127]
MM v[124:127],a[56:59],a[184:187],v[124:127]
buffer_load_dwordx4 v221, s[32:35], s95 offen offset:3072 lds
s_waitcnt vmcnt(21)
s_barrier
MM v[0:3],a[0:3],a[192:195],v[0:3]
s_add_u32 s87, s89, s85
TR a[56:57],v195 offset:16456
TR a[58:59],v195 offset:16968
MM v[0:3],a[4:7],a[196:199],v[0:3]
s_add_u32 s85, s85, 524288
TR a[60:61],v195 offset:24680
TR a[62:63],v195 offset:25192
MM v[8:11],a[4:7],a[204:207],v[8:11]
s_cmp_ge_u32 s85, 5242880
s_cselect_b32 s85, 0, s85
s_add_u32 m0, s96, 131328
v_and_b32 v208, 240, v248
MM v[8:11],a[0:3],a[200:203],v[8:11]
s_lshl_b32 s62, s62, 8
buffer_load_dwordx4 v253, s[16:19], s87 offen offset:0 lds
MM v[16:19],a[0:3],a[208:211],v[16:19]
s_add_u32 m0, s96, 132352
s_add_u32 s93, s93, 36864
s_add_u32 s62, s62, s93
MM v[16:19],a[4:7],a[212:215],v[16:19]
s_xor_b32 s93, s62, 256
MM v[24:27],a[4:7],a[220:223],v[24:27]
MM v[24:27],a[0:3],a[216:219],v[24:27]
buffer_load_dwordx4 v254, s[16:19], s87 offen offset:0 lds
MM v[4:7],a[8:11],a[192:195],v[4:7]
s_add_u32 s87, s87, 65536
s_add_u32 m0, s96, 133384
MM v[4:7],a[12:15],a[196:199],v[4:7]
MM v[12:15],a[12:15],a[204:207],v[12:15]
MM v[12:15],a[8:11],a[200:203],v[12:15]
buffer_load_dwordx4 v253, s[16:19], s87 offen offset:0 lds
MM v[20:23],a[8:11],a[208:211],v[20:23]
s_add_u32 m0, s96, 134408
MM v[20:23],a[12:15],a[212:215],v[20:23]
MM v[28:31],a[12:15],a[220:223],v[28:31]
MM v[28:31],a[8:11],a[216:219],v[28:31]
buffer_load_dwordx4 v254, s[16:19], s87 offen offset:0 lds
v_smfmac_f32_16x16x64_bf16 v[184:187], v[236:239], a[0:7], v234
s_waitcnt vmcnt(16)
RD a[224:227],v222 offset:32768
v_smfmac_f32_16x16x64_bf16 v[188:191], v[236:239], a[8:15], v234
RD a[228:231],v223 offset:32768
MM v[32:35],a[16:19],a[192:195],v[32:35]
TR a[0:1],v192 offset:32896
TR a[2:3],v192 offset:33408
MM v[32:35],a[20:23],a[196:199],v[32:35]
TR a[4:5],v192 offset:41120
TR a[6:7],v192 offset:41632
MM v[40:43],a[20:23],a[204:207],v[40:43]
TR a[8:9],v192 offset:32904
TR a[10:11],v192 offset:33416
RD a[232:235],v222 offset:34816
MM v[40:43],a[16:19],a[200:203],v[40:43]
TR a[12:13],v192 offset:41128
TR a[14:15],v192 offset:41640
buffer_load_dwordx4 v[200:203], v208, s[20:23], s62 offen offset:0 sc1
MM v[48:51],a[16:19],a[208:211],v[48:51]
RD a[236:239],v223 offset:34816
MM v[48:51],a[20:23],a[212:215],v[48:51]
buffer_load_dwordx4 v[204:207], v208, s[20:23], s93 offen offset:0 sc1
MM v[56:59],a[20:23],a[220:223],v[56:59]
RD a[240:243],v222 offset:36864
MM v[56:59],a[16:19],a[216:219],v[56:59]
s_waitcnt lgkmcnt(15)
RD a[244:247],v223 offset:36864
MM v[36:39],a[24:27],a[192:195],v[36:39]
TR a[16:17],v193 offset:32896
TR a[18:19],v193 offset:33408
MM v[36:39],a[28:31],a[196:199],v[36:39]
TR a[20:21],v193 offset:41120
TR a[22:23],v193 offset:41632
MM v[44:47],a[28:31],a[204:207],v[44:47]
RD a[248:251],v222 offset:38912
MM v[44:47],a[24:27],a[200:203],v[44:47]
RD a[252:255],v223 offset:38912
MM v[52:55],a[24:27],a[208:211],v[52:55]
MM v[52:55],a[28:31],a[212:215],v[52:55]
MM v[60:63],a[28:31],a[220:223],v[60:63]
MM v[60:63],a[24:27],a[216:219],v[60:63]
s_waitcnt lgkmcnt(0)
s_add_u32 s88, s68, s86
s_add_u32 s86, s86, 128
MM v[64:67],a[32:35],a[192:195],v[64:67]
TR a[24:25],v193 offset:32904
TR a[26:27],v193 offset:33416
MM v[64:67],a[36:39],a[196:199],v[64:67]
TR a[28:29],v193 offset:41128
TR a[30:31],v193 offset:41640
s_cmp_ge_u32 s86, 1280
s_cselect_b32 s86, 0, s86
MM v[72:75],a[36:39],a[204:207],v[72:75]
s_add_u32 m0, s94, 32768
s_add_u32 s95, s88, 0
MM v[72:75],a[32:35],a[200:203],v[72:75]
buffer_load_dwordx4 v220, s[32:35], s95 offen offset:0 lds
MM v[80:83],a[32:35],a[208:211],v[80:83]
s_add_u32 s95, s88, 9216
MM v[80:83],a[36:39],a[212:215],v[80:83]
buffer_load_dwordx4 v221, s[32:35], s95 offen offset:1024 lds
MM v[88:91],a[36:39],a[220:223],v[88:91]
s_add_u32 s95, s88, 18432
MM v[88:91],a[32:35],a[216:219],v[88:91]
MM v[68:71],a[40:43],a[192:195],v[68:71]
TR a[32:33],v194 offset:32896
TR a[34:35],v194 offset:33408
MM v[68:71],a[44:47],a[196:199],v[68:71]
TR a[36:37],v194 offset:41120
TR a[38:39],v194 offset:41632
MM v[76:79],a[44:47],a[204:207],v[76:79]
buffer_load_dwordx4 v220, s[32:35], s95 offen offset:2048 lds
MM v[76:79],a[40:43],a[200:203],v[76:79]
s_add_u32 s95, s88, 27648
MM v[84:87],a[40:43],a[208:211],v[84:87]
MM v[84:87],a[44:47],a[212:215],v[84:87]
buffer_load_dwordx4 v221, s[32:35], s95 offen offset:3072 lds
MM v[92:95],a[44:47],a[220:223],v[92:95]
s_waitcnt vmcnt(10)
RD a[128:131],v222 offset:0
MM v[92:95],a[40:43],a[216:219],v[92:95]
RD a[132:135],v223 offset:0
MM v[96:99],a[48:51],a[192:195],v[96:99]
TR a[40:41],v194 offset:32904
TR a[42:43],v194 offset:33416
MM v[96:99],a[52:55],a[196:199],v[96:99]
TR a[44:45],v194 offset:41128
TR a[46:47],v194 offset:41640
MM v[104:107],a[52:55],a[204:207],v[104:107]
RD a[136:139],v222 offset:2048
MM v[104:107],a[48:51],a[200:203],v[104:107]
RD a[140:143],v223 offset:2048
MM v[112:115],a[48:51],a[208:211],v[112:115]
RD a[144:147],v222 offset:4096
MM v[112:115],a[52:55],a[212:215],v[112:115]
RD a[148:151],v223 offset:4096
MM v[120:123],a[52:55],a[220:223],v[120:123]
s_nop 0
LPG3_%=:
s_cbranch_execz LPG7_%=
RD a[152:155],v222 offset:6144
MM v[120:123],a[48:51],a[216:219],v[120:123]
RD a[156:159],v223 offset:6144
MM v[100:103],a[56:59],a[192:195],v[100:103]
s_sub_u32 s88, s88, s63
TR a[48:49],v195 offset:32896
TR a[50:51],v195 offset:33408
MM v[100:103],a[60:63],a[196:199],v[100:103]
s_add_u32 m0, s94, 36864
TR a[52:53],v195 offset:41120
TR a[54:55],v195 offset:41632
MM v[108:111],a[60:63],a[204:207],v[108:111]
s_add_u32 s95, s88, 40960
buffer_load_dwordx4 v220, s[32:35], s95 offen offset:0 lds
MM v[108:111],a[56:59],a[200:203],v[108:111]
s_add_u32 s95, s88, 50176
MM v[116:119],a[56:59],a[208:211],v[116:119]
buffer_load_dwordx4 v221, s[32:35], s95 offen offset:1024 lds
MM v[116:119],a[60:63],a[212:215],v[116:119]
s_add_u32 s95, s88, 59392
MM v[124:127],a[60:63],a[220:223],v[124:127]
MM v[124:127],a[56:59],a[216:219],v[124:127]
buffer_load_dwordx4 v220, s[32:35], s95 offen offset:2048 lds
s_add_u32 s95, s88, 68608
buffer_load_dwordx4 v221, s[32:35], s95 offen offset:3072 lds
s_waitcnt lgkmcnt(4)
s_waitcnt vmcnt(10)
s_barrier
MM v[0:3],a[0:3],a[224:227],v[0:3]
s_add_u32 s77, s64, 0
TR a[56:57],v195 offset:32904
TR a[58:59],v195 offset:33416
MM v[0:3],a[4:7],a[228:231],v[0:3]
s_add_u32 m0, s65, 0
TR a[60:61],v195 offset:41128
TR a[62:63],v195 offset:41640
)ASM"
R"ASM(
MM v[8:11],a[4:7],a[236:239],v[8:11]
s_waitcnt vmcnt(8)
TR a[64:65],v196 offset:0
TR a[66:67],v196 offset:512
MM v[8:11],a[0:3],a[232:235],v[8:11]
s_lshl_b32 s76, %[swave], 7
buffer_load_dwordx4 v228, s[8:11], s77 offen offset:0 lds
v_lshrrev_b32 v253, 8, v248
v_lshlrev_b32 v253, 5, v253
MM v[16:19],a[0:3],a[240:243],v[16:19]
s_add_u32 s76, s76, %[slds]
TR a[68:69],v196 offset:8224
TR a[70:71],v196 offset:8736
v_xor_b32 v201, s69, v201
MM v[16:19],a[4:7],a[244:247],v[16:19]
s_mov_b64 exec, 1
buffer_store_dword v171, off, s[20:23], s80 sc1
s_mov_b64 exec, -1
v_add_u32 v253, s76, v253
v_add_u32 v253, 151552, v253
MM v[24:27],a[4:7],a[252:255],v[24:27]
TR a[72:73],v196 offset:16448
TR a[74:75],v196 offset:16960
MM v[24:27],a[0:3],a[248:251],v[24:27]
TR a[76:77],v196 offset:24672
TR a[78:79],v196 offset:25184
buffer_load_dwordx4 v229, s[8:11], s77 offen offset:1024 lds
v_xor_b32 v203, s69, v203
MM v[4:7],a[8:11],a[224:227],v[4:7]
TR a[80:81],v196 offset:8
TR a[82:83],v196 offset:520
MM v[4:7],a[12:15],a[228:231],v[4:7]
TR a[84:85],v196 offset:8232
TR a[86:87],v196 offset:8744
MM v[12:15],a[12:15],a[236:239],v[12:15]
TR a[88:89],v196 offset:16456
TR a[90:91],v196 offset:16968
v_xor_b32 v205, s69, v205
MM v[12:15],a[8:11],a[232:235],v[12:15]
TR a[92:93],v196 offset:24680
TR a[94:95],v196 offset:25192
buffer_load_dwordx4 v230, s[8:11], s77 offen offset:2048 lds
MM v[20:23],a[8:11],a[240:243],v[20:23]
TR a[96:97],v197 offset:0
TR a[98:99],v197 offset:512
MM v[20:23],a[12:15],a[244:247],v[20:23]
TR a[100:101],v197 offset:8224
TR a[102:103],v197 offset:8736
v_xor_b32 v207, s69, v207
MM v[28:31],a[12:15],a[252:255],v[28:31]
TR a[104:105],v197 offset:16448
TR a[106:107],v197 offset:16960
MM v[28:31],a[8:11],a[248:251],v[28:31]
TR a[108:109],v197 offset:24672
TR a[110:111],v197 offset:25184
buffer_load_dwordx4 v231, s[8:11], s77 offen offset:3072 lds
v_smfmac_f32_16x16x64_bf16 v[184:187], v[236:239], a[0:7], v234
s_add_u32 s77, s64, 16384
TR a[112:113],v197 offset:8
v_or3_b32 v209, v201, v203, v205
v_smfmac_f32_16x16x64_bf16 v[188:191], v[236:239], a[8:15], v234
s_add_u32 m0, s65, 16384
TR a[114:115],v197 offset:520
MM v[32:35],a[16:19],a[224:227],v[32:35]
TR a[116:117],v197 offset:8232
TR a[118:119],v197 offset:8744
MM v[32:35],a[20:23],a[228:231],v[32:35]
TR a[120:121],v197 offset:16456
TR a[122:123],v197 offset:16968
v_or_b32 v209, v209, v207
MM v[40:43],a[20:23],a[236:239],v[40:43]
TR a[124:125],v197 offset:24680
TR a[126:127],v197 offset:25192
MM v[40:43],a[16:19],a[232:235],v[40:43]
TR a[0:1],v198 offset:0
TR a[2:3],v198 offset:512
buffer_load_dwordx4 v228, s[8:11], s77 offen offset:0 lds
MM v[48:51],a[16:19],a[240:243],v[48:51]
TR a[4:5],v198 offset:8224
TR a[6:7],v198 offset:8736
v_cmp_ne_u32 vcc, 0, v209
s_cbranch_vccnz LRG1o_%=
LRG1ob_%=:
MM v[48:51],a[20:23],a[244:247],v[48:51]
TR a[8:9],v198 offset:16448
TR a[10:11],v198 offset:16960
MM v[56:59],a[20:23],a[252:255],v[56:59]
TR a[12:13],v198 offset:24672
TR a[14:15],v198 offset:25184
MM v[56:59],a[16:19],a[248:251],v[56:59]
v_lshlrev_b32 v240, 16, v200
v_and_b32 v200, 0xffff0000, v200
MM v[36:39],a[24:27],a[224:227],v[36:39]
v_rcp_f32 v244, v200
MM v[36:39],a[28:31],a[228:231],v[36:39]
v_xor_b32 v240, 0x80000000, v240
v_mul_f32 v244, s72, v244
MM v[44:47],a[28:31],a[236:239],v[44:47]
v_lshlrev_b32 v241, 16, v202
v_and_b32 v202, 0xffff0000, v202
MM v[44:47],a[24:27],a[232:235],v[44:47]
buffer_load_dwordx4 v229, s[8:11], s77 offen offset:1024 lds
MM v[52:55],a[24:27],a[240:243],v[52:55]
v_rcp_f32 v245, v202
MM v[52:55],a[28:31],a[244:247],v[52:55]
v_xor_b32 v241, 0x80000000, v241
v_mul_f32 v245, s72, v245
MM v[60:63],a[28:31],a[252:255],v[60:63]
v_lshlrev_b32 v242, 16, v204
v_and_b32 v204, 0xffff0000, v204
MM v[60:63],a[24:27],a[248:251],v[60:63]
v_rcp_f32 v246, v204
MM v[64:67],a[32:35],a[224:227],v[64:67]
TR a[16:17],v198 offset:8
TR a[18:19],v198 offset:520
MM v[64:67],a[36:39],a[228:231],v[64:67]
TR a[20:21],v198 offset:8232
TR a[22:23],v198 offset:8744
v_xor_b32 v242, 0x80000000, v242
MM v[72:75],a[36:39],a[236:239],v[72:75]
TR a[24:25],v198 offset:16456
TR a[26:27],v198 offset:16968
MM v[72:75],a[32:35],a[232:235],v[72:75]
TR a[28:29],v198 offset:24680
TR a[30:31],v198 offset:25192
MM v[80:83],a[32:35],a[240:243],v[80:83]
v_mul_f32 v246, s72, v246
v_lshlrev_b32 v243, 16, v206
MM v[80:83],a[36:39],a[244:247],v[80:83]
buffer_load_dwordx4 v230, s[8:11], s77 offen offset:2048 lds
MM v[88:91],a[36:39],a[252:255],v[88:91]
v_and_b32 v206, 0xffff0000, v206
MM v[88:91],a[32:35],a[248:251],v[88:91]
s_waitcnt lgkmcnt(15)
v_rcp_f32 v247, v206
v_smfmac_f32_16x16x64_bf16 v[184:187], v[236:239], a[64:71], v234
v_xor_b32 v243, 0x80000000, v243
v_smfmac_f32_16x16x64_bf16 v[184:187], v[236:239], a[72:79], v234
v_mul_f32 v247, s72, v247
v_smfmac_f32_16x16x64_bf16 v[188:191], v[236:239], a[80:87], v234
v_smfmac_f32_16x16x64_bf16 v[188:191], v[236:239], a[88:95], v234
MM v[68:71],a[40:43],a[224:227],v[68:71]
v_bfe_u32 v183, v248, 6, 2
v_lshlrev_b32 v183, 5, v183
MM v[68:71],a[44:47],a[228:231],v[68:71]
v_bfe_u32 v182, v248, 4, 2
v_lshl_add_u32 v183, v182, 2, v183
MM v[76:79],a[44:47],a[236:239],v[76:79]
v_add_u32 v183, s76, v183
v_add_u32 v183, 151552, v183
MM v[76:79],a[40:43],a[232:235],v[76:79]
buffer_load_dwordx4 v231, s[8:11], s77 offen offset:3072 lds
MM v[84:87],a[40:43],a[240:243],v[84:87]
s_waitcnt vmcnt(9)
RD a[160:163],v222 offset:32768
MM v[84:87],a[44:47],a[244:247],v[84:87]
RD a[164:167],v223 offset:32768
MM v[92:95],a[44:47],a[252:255],v[92:95]
RD a[168:171],v222 offset:34816
MM v[92:95],a[40:43],a[248:251],v[92:95]
v_add_f32 v184, v184, v185
v_add_f32 v188, v188, v189
MM v[96:99],a[48:51],a[224:227],v[96:99]
TR a[32:33],v199 offset:0
TR a[34:35],v199 offset:512
MM v[96:99],a[52:55],a[228:231],v[96:99]
RD a[172:175],v223 offset:34816
ds_write_b32 v183, v184 offset:0
ds_write_b32 v183, v188 offset:16
MM v[104:107],a[52:55],a[236:239],v[104:107]
TR a[36:37],v199 offset:8224
TR a[38:39],v199 offset:8736
MM v[104:107],a[48:51],a[232:235],v[104:107]
TR a[40:41],v199 offset:16448
TR a[42:43],v199 offset:16960
MM v[112:115],a[48:51],a[240:243],v[112:115]
TR a[44:45],v199 offset:24672
TR a[46:47],v199 offset:25184
RD a[176:179],v222 offset:36864
MM v[112:115],a[52:55],a[244:247],v[112:115]
RD a[180:183],v223 offset:36864
MM v[120:123],a[52:55],a[252:255],v[120:123]
RD a[184:187],v222 offset:38912
MM v[120:123],a[48:51],a[248:251],v[120:123]
RD a[188:191],v223 offset:38912
MM v[100:103],a[56:59],a[224:227],v[100:103]
s_add_u32 s77, s64, 0
s_add_u32 s81, s77, 896
s_add_u32 m0, s65, 65536
MM v[100:103],a[60:63],a[228:231],v[100:103]
buffer_load_dwordx4 v235, s[12:15], s81 offen offset:0 lds
MM v[108:111],a[60:63],a[236:239],v[108:111]
MM v[108:111],a[56:59],a[232:235],v[108:111]
MM v[116:119],a[56:59],a[240:243],v[116:119]
MM v[116:119],a[60:63],a[244:247],v[116:119]
MM v[124:127],a[60:63],a[252:255],v[124:127]
MM v[124:127],a[56:59],a[248:251],v[124:127]
buffer_load_dwordx4 v235, s[12:15], s77 offen offset:1024 lds
s_waitcnt lgkmcnt(0)
s_barrier
MM v[0:3],a[64:67],a[128:131],v[0:3]
s_memrealtime s[52:53]
TR a[48:49],v199 offset:8
TR a[50:51],v199 offset:520
MM v[0:3],a[68:71],a[132:135],v[0:3]
s_memtime s[54:55]
TR a[52:53],v199 offset:8232
TR a[54:55],v199 offset:8744
MM v[0:3],a[72:75],a[160:163],v[0:3]
TR a[56:57],v199 offset:16456
TR a[58:59],v199 offset:16968
MM v[0:3],a[76:79],a[164:167],v[0:3]
TR a[60:61],v199 offset:24680
TR a[62:63],v199 offset:25192
MM v[8:11],a[76:79],a[172:175],v[8:11]
v_xor_b32 v151, 0, v253
v_xor_b32 v143, 0, v253
MM v[8:11],a[72:75],a[168:171],v[8:11]
RD v[144:147],v151 offset:0
RD v[148:151],v151 offset:16
MM v[8:11],a[68:71],a[140:143],v[8:11]
RD v[136:139],v143 offset:0
RD v[140:143],v143 offset:16
MM v[8:11],a[64:67],a[136:139],v[8:11]
MM v[4:7],a[80:83],a[128:131],v[4:7]
MM v[4:7],a[84:87],a[132:135],v[4:7]
s_waitcnt lgkmcnt(2)
buffer_load_dwordx4 v235, s[12:15], s81 offen offset:2048 lds
MM v[4:7],a[88:91],a[160:163],v[4:7]
s_waitcnt lgkmcnt(0)
MM v[4:7],a[92:95],a[164:167],v[4:7]
MM v[12:15],a[92:95],a[172:175],v[12:15]
MM v[12:15],a[88:91],a[168:171],v[12:15]
MM v[12:15],a[84:87],a[140:143],v[12:15]
MM v[12:15],a[80:83],a[136:139],v[12:15]
MM v[32:35],a[96:99],a[128:131],v[32:35]
MM v[32:35],a[100:103],a[132:135],v[32:35]
MM v[32:35],a[104:107],a[160:163],v[32:35]
MM v[32:35],a[108:111],a[164:167],v[32:35]
buffer_load_dwordx4 v235, s[12:15], s77 offen offset:3072 lds
v_fmac_f32 v0, v240, v144
v_fmac_f32 v1, v240, v145
v_fmac_f32 v2, v240, v146
MM v[40:43],a[108:111],a[172:175],v[40:43]
s_add_u32 s77, s64, 32768
s_add_u32 m0, s65, 32768
v_fmac_f32 v3, v240, v147
v_fmac_f32 v4, v240, v148
MM v[40:43],a[104:107],a[168:171],v[40:43]
v_fmac_f32 v5, v240, v149
v_fmac_f32 v6, v240, v150
MM v[40:43],a[100:103],a[140:143],v[40:43]
v_fmac_f32 v7, v240, v151
v_mul_f32 v0, v244, v0
v_mul_f32 v1, v244, v1
MM v[40:43],a[96:99],a[136:139],v[40:43]
v_mul_f32 v2, v244, v2
v_mul_f32 v3, v244, v3
MM v[36:39],a[112:115],a[128:131],v[36:39]
v_mul_f32 v4, v244, v4
v_mul_f32 v5, v244, v5
MM v[36:39],a[116:119],a[132:135],v[36:39]
v_mul_f32 v6, v244, v6
v_mul_f32 v7, v244, v7
CV v160,v0,v1
MM v[36:39],a[120:123],a[160:163],v[36:39]
CV v161,v2,v3
CV v162,v4,v5
MM v[36:39],a[124:127],a[164:167],v[36:39]
CV v163,v6,v7
v_fmac_f32 v8, v241, v144
MM v[44:47],a[124:127],a[172:175],v[44:47]
v_fmac_f32 v9, v241, v145
v_fmac_f32 v10, v241, v146
v_fmac_f32 v11, v241, v147
MM v[44:47],a[120:123],a[168:171],v[44:47]
buffer_load_dwordx4 v228, s[8:11], s77 offen offset:0 lds
v_fmac_f32 v12, v241, v148
v_fmac_f32 v13, v241, v149
MM v[44:47],a[116:119],a[140:143],v[44:47]
v_fmac_f32 v14, v241, v150
v_fmac_f32 v15, v241, v151
v_mul_f32 v8, v245, v8
MM v[44:47],a[112:115],a[136:139],v[44:47]
v_mul_f32 v9, v245, v9
v_mul_f32 v10, v245, v10
MM v[64:67],a[0:3],a[128:131],v[64:67]
v_mul_f32 v11, v245, v11
v_mul_f32 v12, v245, v12
MM v[64:67],a[4:7],a[132:135],v[64:67]
v_mul_f32 v13, v245, v13
v_mul_f32 v14, v245, v14
v_mul_f32 v15, v245, v15
MM v[64:67],a[8:11],a[160:163],v[64:67]
CV v164,v8,v9
CV v165,v10,v11
MM v[64:67],a[12:15],a[164:167],v[64:67]
CV v166,v12,v13
CV v167,v14,v15
MM v[72:75],a[12:15],a[172:175],v[72:75]
v_xor_b32 v159, 128, v253
MM v[72:75],a[8:11],a[168:171],v[72:75]
RD v[152:155],v159 offset:0
RD v[156:159],v159 offset:16
MM v[72:75],a[4:7],a[140:143],v[72:75]
MM v[72:75],a[0:3],a[136:139],v[72:75]
buffer_load_dwordx4 v229, s[8:11], s77 offen offset:1024 lds
MM v[68:71],a[16:19],a[128:131],v[68:71]
MM v[68:71],a[20:23],a[132:135],v[68:71]
s_waitcnt lgkmcnt(0)
v_fmac_f32 v32, v240, v152
v_fmac_f32 v33, v240, v153
v_fmac_f32 v34, v240, v154
v_fmac_f32 v35, v240, v155
v_fmac_f32 v36, v240, v156
v_fmac_f32 v37, v240, v157
v_fmac_f32 v38, v240, v158
v_fmac_f32 v39, v240, v159
v_mul_f32 v179, -v250, v244
v_fma_f32 v32, v244, v32, v179
v_fma_f32 v33, v244, v33, v179
v_fma_f32 v34, v244, v34, v179
MM v[68:71],a[24:27],a[160:163],v[68:71]
v_fma_f32 v35, v244, v35, v179
v_fma_f32 v36, v244, v36, v179
MM v[68:71],a[28:31],a[164:167],v[68:71]
v_fma_f32 v37, v244, v37, v179
v_fma_f32 v38, v244, v38, v179
v_fma_f32 v39, v244, v39, v179
MM v[76:79],a[28:31],a[172:175],v[76:79]
CV v176,v32,v33
CV v177,v34,v35
MM v[76:79],a[24:27],a[168:171],v[76:79]
CV v178,v36,v37
CV v179,v38,v39
v_fmac_f32 v40, v241, v152
MM v[76:79],a[20:23],a[140:143],v[76:79]
v_fmac_f32 v41, v241, v153
v_fmac_f32 v42, v241, v154
v_fmac_f32 v43, v241, v155
MM v[76:79],a[16:19],a[136:139],v[76:79]
v_fmac_f32 v44, v241, v156
v_fmac_f32 v45, v241, v157
MM v[96:99],a[32:35],a[128:131],v[96:99]
v_fmac_f32 v46, v241, v158
v_fmac_f32 v47, v241, v159
MM v[96:99],a[36:39],a[132:135],v[96:99]
buffer_load_dwordx4 v230, s[8:11], s77 offen offset:2048 lds
v_mul_f32 v183, -v250, v245
v_fma_f32 v40, v245, v40, v183
v_fma_f32 v41, v245, v41, v183
MM v[96:99],a[40:43],a[160:163],v[96:99]
v_fma_f32 v42, v245, v42, v183
v_fma_f32 v43, v245, v43, v183
MM v[96:99],a[44:47],a[164:167],v[96:99]
v_fma_f32 v44, v245, v44, v183
v_fma_f32 v45, v245, v45, v183
MM v[104:107],a[44:47],a[172:175],v[104:107]
)ASM"
R"ASM(
v_fma_f32 v46, v245, v46, v183
v_fma_f32 v47, v245, v47, v183
CV v180,v40,v41
MM v[104:107],a[40:43],a[168:171],v[104:107]
CV v181,v42,v43
CV v182,v44,v45
MM v[104:107],a[36:39],a[140:143],v[104:107]
CV v183,v46,v47
v_xor_b32 v151, 256, v253
MM v[104:107],a[32:35],a[136:139],v[104:107]
RD v[144:147],v151 offset:0
RD v[148:151],v151 offset:16
MM v[100:103],a[48:51],a[128:131],v[100:103]
MM v[100:103],a[52:55],a[132:135],v[100:103]
MM v[100:103],a[56:59],a[160:163],v[100:103]
MM v[100:103],a[60:63],a[164:167],v[100:103]
s_waitcnt lgkmcnt(0)
buffer_load_dwordx4 v231, s[8:11], s77 offen offset:3072 lds
v_fmac_f32 v64, v240, v144
v_fmac_f32 v65, v240, v145
v_fmac_f32 v66, v240, v146
v_fmac_f32 v67, v240, v147
v_fmac_f32 v68, v240, v148
v_fmac_f32 v69, v240, v149
v_fmac_f32 v70, v240, v150
v_fmac_f32 v71, v240, v151
v_mul_f32 v195, -v250, v244
v_fma_f32 v64, v244, v64, v195
v_fma_f32 v65, v244, v65, v195
MM v[108:111],a[60:63],a[172:175],v[108:111]
s_add_u32 s77, s64, 16384
s_add_u32 s81, s77, 896
s_add_u32 m0, s65, 81920
v_fma_f32 v66, v244, v66, v195
v_fma_f32 v67, v244, v67, v195
MM v[108:111],a[56:59],a[168:171],v[108:111]
v_fma_f32 v68, v244, v68, v195
v_fma_f32 v69, v244, v69, v195
MM v[108:111],a[52:55],a[140:143],v[108:111]
v_fma_f32 v70, v244, v70, v195
v_fma_f32 v71, v244, v71, v195
CV v192,v64,v65
MM v[108:111],a[48:51],a[136:139],v[108:111]
CV v193,v66,v67
CV v194,v68,v69
MM v[16:19],a[64:67],a[144:147],v[16:19]
CV v195,v70,v71
v_fmac_f32 v72, v241, v144
v_fmac_f32 v73, v241, v145
MM v[16:19],a[68:71],a[148:151],v[16:19]
v_fmac_f32 v74, v241, v146
v_fmac_f32 v75, v241, v147
v_fmac_f32 v76, v241, v148
MM v[16:19],a[72:75],a[176:179],v[16:19]
v_fmac_f32 v77, v241, v149
v_fmac_f32 v78, v241, v150
MM v[16:19],a[76:79],a[180:183],v[16:19]
v_fmac_f32 v79, v241, v151
v_mul_f32 v199, -v250, v245
v_fma_f32 v72, v245, v72, v199
MM v[24:27],a[76:79],a[188:191],v[24:27]
v_fma_f32 v73, v245, v73, v199
v_fma_f32 v74, v245, v74, v199
MM v[24:27],a[72:75],a[184:187],v[24:27]
buffer_load_dwordx4 v235, s[12:15], s81 offen offset:0 lds
v_fma_f32 v75, v245, v75, v199
v_fma_f32 v76, v245, v76, v199
MM v[24:27],a[68:71],a[156:159],v[24:27]
s_waitcnt vmcnt(13)
s_barrier
RD a[192:195],v224 offset:0
v_fma_f32 v77, v245, v77, v199
v_fma_f32 v78, v245, v78, v199
v_fma_f32 v79, v245, v79, v199
MM v[24:27],a[64:67],a[152:155],v[24:27]
RD a[196:199],v225 offset:0
RD a[200:203],v226 offset:0
CV v196,v72,v73
CV v197,v74,v75
MM v[20:23],a[80:83],a[144:147],v[20:23]
RD a[204:207],v227 offset:0
RD a[208:211],v224 offset:4096
CV v198,v76,v77
CV v199,v78,v79
MM v[20:23],a[84:87],a[148:151],v[20:23]
RD a[212:215],v225 offset:4096
RD a[216:219],v226 offset:4096
v_xor_b32 v159, 384, v253
MM v[20:23],a[88:91],a[176:179],v[20:23]
RD a[220:223],v227 offset:4096
RD a[224:227],v224 offset:8192
RD v[152:155],v159 offset:0
RD v[156:159],v159 offset:16
MM v[20:23],a[92:95],a[180:183],v[20:23]
RD a[228:231],v225 offset:8192
RD a[232:235],v226 offset:8192
MM v[28:31],a[92:95],a[188:191],v[28:31]
RD a[236:239],v227 offset:8192
RD a[240:243],v224 offset:12288
MM v[28:31],a[88:91],a[184:187],v[28:31]
RD a[244:247],v225 offset:12288
RD a[248:251],v226 offset:12288
MM v[28:31],a[84:87],a[156:159],v[28:31]
s_waitcnt lgkmcnt(6)
RD a[252:255],v227 offset:12288
v_fmac_f32 v96, v240, v152
v_fmac_f32 v97, v240, v153
v_fmac_f32 v98, v240, v154
v_fmac_f32 v99, v240, v155
v_fmac_f32 v100, v240, v156
v_fmac_f32 v101, v240, v157
v_fmac_f32 v102, v240, v158
v_fmac_f32 v103, v240, v159
v_mul_f32 v211, -v250, v244
v_fma_f32 v96, v244, v96, v211
v_fma_f32 v97, v244, v97, v211
v_fma_f32 v98, v244, v98, v211
MM v[28:31],a[80:83],a[152:155],v[28:31]
buffer_load_dwordx4 v235, s[12:15], s77 offen offset:1024 lds
v_fma_f32 v99, v244, v99, v211
v_fma_f32 v100, v244, v100, v211
MM v[48:51],a[96:99],a[144:147],v[48:51]
v_fma_f32 v101, v244, v101, v211
v_fma_f32 v102, v244, v102, v211
v_fma_f32 v103, v244, v103, v211
MM v[48:51],a[100:103],a[148:151],v[48:51]
CV v208,v96,v97
CV v209,v98,v99
MM v[48:51],a[104:107],a[176:179],v[48:51]
CV v210,v100,v101
CV v211,v102,v103
v_fmac_f32 v104, v241, v152
MM v[48:51],a[108:111],a[180:183],v[48:51]
v_fmac_f32 v105, v241, v153
v_fmac_f32 v106, v241, v154
v_fmac_f32 v107, v241, v155
MM v[56:59],a[108:111],a[188:191],v[56:59]
v_fmac_f32 v108, v241, v156
v_fmac_f32 v109, v241, v157
MM v[56:59],a[104:107],a[184:187],v[56:59]
v_fmac_f32 v110, v241, v158
v_fmac_f32 v111, v241, v159
MM v[56:59],a[100:103],a[156:159],v[56:59]
v_mul_f32 v215, -v250, v245
v_fma_f32 v104, v245, v104, v215
v_fma_f32 v105, v245, v105, v215
MM v[56:59],a[96:99],a[152:155],v[56:59]
v_fma_f32 v106, v245, v106, v215
v_fma_f32 v107, v245, v107, v215
MM v[52:55],a[112:115],a[144:147],v[52:55]
v_fma_f32 v108, v245, v108, v215
v_fma_f32 v109, v245, v109, v215
MM v[52:55],a[116:119],a[148:151],v[52:55]
buffer_load_dwordx4 v235, s[12:15], s81 offen offset:2048 lds
v_fma_f32 v110, v245, v110, v215
v_fma_f32 v111, v245, v111, v215
CV v212,v104,v105
MM v[52:55],a[120:123],a[176:179],v[52:55]
CV v213,v106,v107
CV v214,v108,v109
CV v215,v110,v111
MM v[52:55],a[124:127],a[180:183],v[52:55]
v_mov_b32 v241, 0
v_mov_b32 v244, 0
s_waitcnt lgkmcnt(0)
MM v[0:3],a[192:195],v[160:163],0
v_mov_b32 v144, 0
v_mov_b32 v145, 0
MM v[0:3],a[196:199],v[176:179],v[0:3]
v_mov_b32 v245, 0
v_mov_b32 v148, 0
MM v[0:3],a[200:203],v[192:195],v[0:3]
v_mov_b32 v149, 0
v_fmac_f32 v16, v242, v136
MM v[0:3],a[204:207],v[208:211],v[0:3]
v_fmac_f32 v17, v242, v137
v_fmac_f32 v18, v242, v138
MM v[8:11],a[192:195],v[164:167],0
v_fmac_f32 v19, v242, v139
v_fmac_f32 v20, v242, v140
MM v[8:11],a[196:199],v[180:183],v[8:11]
v_fmac_f32 v21, v242, v141
v_fmac_f32 v22, v242, v142
MM v[8:11],a[200:203],v[196:199],v[8:11]
v_fmac_f32 v23, v242, v143
v_mul_f32 v16, v246, v16
MM v[8:11],a[204:207],v[212:215],v[8:11]
buffer_load_dwordx4 v235, s[12:15], s77 offen offset:3072 lds
MM v[4:7],a[208:211],v[160:163],0
s_add_u32 s77, s64, 32768
s_add_u32 s81, s77, 896
s_add_u32 m0, s65, 98304
s_nop 0
LPG4_%=:
s_cbranch_execz LPG8_%=
MM v[4:7],a[212:215],v[176:179],v[4:7]
v_max3_f32 v146, v0, v1, v2
v_max_f32 v146, v146, v3
MM v[4:7],a[216:219],v[192:195],v[4:7]
v_mov_b32 v147, v146
v_mul_f32 v17, v246, v17
MM v[4:7],a[220:223],v[208:211],v[4:7]
v_permlane16_swap_b32 v146, v147
v_max_f32 v146, v146, v147
MM v[12:15],a[208:211],v[164:167],0
s_waitcnt vmcnt(8)
s_barrier
RD a[192:195],v224 offset:16384
MM v[12:15],a[212:215],v[180:183],v[12:15]
RD a[196:199],v225 offset:16384
RD a[200:203],v226 offset:16384
MM v[12:15],a[216:219],v[196:199],v[12:15]
RD a[204:207],v227 offset:16384
v_mov_b32 v147, v146
MM v[12:15],a[220:223],v[212:215],v[12:15]
s_nop 0
v_permlane32_swap_b32 v146, v147
v_max_f32 v146, v146, v147
v_sub_f32 v128, 0xc1600000, v146
v_sub_f32 v129, 0xc1600000, v146
MM v[32:35],a[224:227],v[160:163],0
RD a[208:211],v224 offset:20480
RD a[212:215],v225 offset:20480
v_sub_f32 v130, 0xc1600000, v146
v_sub_f32 v131, 0xc1600000, v146
v_max3_f32 v150, v8, v9, v10
MM v[32:35],a[228:231],v[176:179],v[32:35]
buffer_load_dwordx4 v235, s[12:15], s81 offen offset:0 lds
v_max_f32 v150, v150, v11
v_mov_b32 v151, v150
MM v[32:35],a[232:235],v[192:195],v[32:35]
RD a[216:219],v226 offset:20480
RD a[220:223],v227 offset:20480
v_permlane16_swap_b32 v150, v151
v_max_f32 v150, v150, v151
v_mov_b32 v151, v150
MM v[32:35],a[236:239],v[208:211],v[32:35]
s_nop 0
v_permlane32_swap_b32 v150, v151
v_max_f32 v150, v150, v151
v_add_f32 v0, v0, v128
v_sub_f32 v132, 0xc1600000, v150
MM v[40:43],a[224:227],v[164:167],0
v_sub_f32 v133, 0xc1600000, v150
v_sub_f32 v134, 0xc1600000, v150
v_sub_f32 v135, 0xc1600000, v150
v_add_f32 v1, v1, v128
EX v0,v0
MM v[40:43],a[228:231],v[180:183],v[40:43]
EX v1,v1
MM v[40:43],a[232:235],v[196:199],v[40:43]
v_add_f32 v2, v2, v128
v_add_f32 v3, v3, v128
MM v[40:43],a[236:239],v[212:215],v[40:43]
EX v2,v2
MM v[36:39],a[240:243],v[160:163],v[128:131]
RD a[224:227],v224 offset:24576
RD a[228:231],v225 offset:24576
EX v3,v3
MM v[36:39],a[244:247],v[176:179],v[36:39]
RD a[232:235],v226 offset:24576
RD a[236:239],v227 offset:24576
CV v0,v0,v1
v_add_f32 v4, v4, v128
MM v[36:39],a[248:251],v[192:195],v[36:39]
v_add_f32 v5, v5, v128
v_mul_f32 v18, v246, v18
EX v4,v4
MM v[36:39],a[252:255],v[208:211],v[36:39]
buffer_load_dwordx4 v235, s[12:15], s77 offen offset:1024 lds
EX v5,v5
MM v[44:47],a[240:243],v[164:167],v[132:135]
CV v1,v2,v3
v_add_f32 v6, v6, v128
MM v[44:47],a[244:247],v[180:183],v[44:47]
v_add_f32 v7, v7, v128
v_mul_f32 v19, v246, v19
MM v[44:47],a[248:251],v[196:199],v[44:47]
EX v6,v6
EX v7,v7
MM v[44:47],a[252:255],v[212:215],v[44:47]
CV v2,v4,v5
v_add_f32 v8, v8, v132
MM v[60:63],a[124:127],a[188:191],v[60:63]
RD a[240:243],v224 offset:28672
RD a[244:247],v225 offset:28672
v_add_f32 v9, v9, v132
MM v[60:63],a[120:123],a[184:187],v[60:63]
RD a[248:251],v226 offset:28672
RD a[252:255],v227 offset:28672
EX v8,v8
MM v[60:63],a[116:119],a[156:159],v[60:63]
EX v9,v9
MM v[60:63],a[112:115],a[152:155],v[60:63]
CV v3,v6,v7
v_add_f32 v10, v10, v132
MM v[80:83],a[0:3],a[144:147],v[80:83]
v_add_f32 v11, v11, v132
v_mul_f32 v20, v246, v20
EX v10,v10
MM v[80:83],a[4:7],a[148:151],v[80:83]
buffer_load_dwordx4 v235, s[12:15], s81 offen offset:2048 lds
EX v11,v11
MM v[80:83],a[8:11],a[176:179],v[80:83]
CV v8,v8,v9
v_add_f32 v12, v12, v132
MM v[80:83],a[12:15],a[180:183],v[80:83]
v_add_f32 v13, v13, v132
v_mul_f32 v21, v246, v21
EX v12,v12
MM v[88:91],a[12:15],a[188:191],v[88:91]
EX v13,v13
MM v[88:91],a[8:11],a[184:187],v[88:91]
CV v9,v10,v11
v_add_f32 v14, v14, v132
MM v[88:91],a[4:7],a[156:159],v[88:91]
v_add_f32 v15, v15, v132
v_mul_f32 v22, v246, v22
EX v14,v14
MM v[88:91],a[0:3],a[152:155],v[88:91]
EX v15,v15
MM v[84:87],a[16:19],a[144:147],v[84:87]
CV v10,v12,v13
v_add_f32 v32, v32, v128
MM v[84:87],a[20:23],a[148:151],v[84:87]
v_add_f32 v33, v33, v128
v_mul_f32 v23, v246, v23
MM v[84:87],a[24:27],a[176:179],v[84:87]
EX v32,v32
MM v[84:87],a[28:31],a[180:183],v[84:87]
buffer_load_dwordx4 v235, s[12:15], s77 offen offset:3072 lds
EX v33,v33
MM v[92:95],a[28:31],a[188:191],v[92:95]
s_add_u32 s77, s64, 49152
s_add_u32 m0, s65, 49152
CV v11,v14,v15
v_add_f32 v34, v34, v128
v_add_f32 v35, v35, v128
MM v[92:95],a[24:27],a[184:187],v[92:95]
buffer_load_dwordx4 v228, s[8:11], s77 offen offset:0 lds
EX v34,v34
MM v[92:95],a[20:23],a[156:159],v[92:95]
EX v35,v35
MM v[92:95],a[16:19],a[152:155],v[92:95]
CV v4,v32,v33
CV v168,v16,v17
EX v36,v36
MM v[112:115],a[32:35],a[144:147],v[112:115]
EX v37,v37
MM v[112:115],a[36:39],a[148:151],v[112:115]
buffer_load_dwordx4 v229, s[8:11], s77 offen offset:1024 lds
CV v5,v34,v35
MM v[112:115],a[40:43],a[176:179],v[112:115]
EX v38,v38
EX v39,v39
MM v[112:115],a[44:47],a[180:183],v[112:115]
CV v6,v36,v37
v_add_f32 v40, v40, v132
MM v[120:123],a[44:47],a[188:191],v[120:123]
v_add_f32 v41, v41, v132
CV v169,v18,v19
MM v[120:123],a[40:43],a[184:187],v[120:123]
buffer_load_dwordx4 v230, s[8:11], s77 offen offset:2048 lds
EX v40,v40
MM v[120:123],a[36:39],a[156:159],v[120:123]
EX v41,v41
MM v[120:123],a[32:35],a[152:155],v[120:123]
CV v7,v38,v39
v_add_f32 v42, v42, v132
MM v[116:119],a[48:51],a[144:147],v[116:119]
v_add_f32 v43, v43, v132
CV v170,v20,v21
EX v42,v42
MM v[116:119],a[52:55],a[148:151],v[116:119]
buffer_load_dwordx4 v231, s[8:11], s77 offen offset:3072 lds
EX v43,v43
MM v[116:119],a[56:59],a[176:179],v[116:119]
s_add_u32 s77, s64, 49152
s_add_u32 s81, s77, 896
s_add_u32 m0, s65, 114688
CV v12,v40,v41
EX v44,v44
MM v[116:119],a[60:63],a[180:183],v[116:119]
EX v45,v45
MM v[124:127],a[60:63],a[188:191],v[124:127]
CV v13,v42,v43
CV v171,v22,v23
MM v[124:127],a[56:59],a[184:187],v[124:127]
buffer_load_dwordx4 v235, s[12:15], s81 offen offset:0 lds
EX v46,v46
MM v[124:127],a[52:55],a[156:159],v[124:127]
EX v47,v47
CV v14,v44,v45
CV v15,v46,v47
MM v[124:127],a[48:51],a[152:155],v[124:127]
v_fmac_f32 v24, v243, v136
v_fmac_f32 v25, v243, v137
v_smfmac_f32_16x16x64_bf16 v[144:147], v[236:239], v[0:7], v234
v_fmac_f32 v26, v243, v138
v_smfmac_f32_16x16x64_bf16 v[148:151], v[236:239], v[8:15], v234
)ASM"
R"ASM(
s_waitcnt lgkmcnt(0)
v_fmac_f32 v27, v243, v139
buffer_load_dwordx4 v235, s[12:15], s77 offen offset:1024 lds
buffer_load_dwordx4 v235, s[12:15], s81 offen offset:2048 lds
buffer_load_dwordx4 v235, s[12:15], s77 offen offset:3072 lds
s_add_u32 s64, s64, 49152
MM v[64:67],a[192:195],v[160:163],v[128:131]
s_add_u32 s64, s64, 16384
v_fmac_f32 v28, v243, v140
v_fmac_f32 v29, v243, v141
MM v[64:67],a[196:199],v[176:179],v[64:67]
s_add_u32 s81, s64, 896
TR a[128:129],v232 offset:0
v_fmac_f32 v30, v243, v142
MM v[64:67],a[200:203],v[192:195],v[64:67]
v_fmac_f32 v31, v243, v143
v_mul_f32 v24, v247, v24
MM v[64:67],a[204:207],v[208:211],v[64:67]
v_max3_f32 v249, v144, v145, v148
v_max_f32 v249, v249, v149
v_mul_f32 v25, v247, v25
MM v[68:71],a[220:223],v[208:211],v[128:131]
TR a[130:131],v232 offset:4096
TR a[132:133],v232 offset:8192
v_cmp_lt_f32 vcc, s71, v249
v_mul_f32 v26, v247, v26
v_mul_f32 v27, v247, v27
MM v[68:71],a[216:219],v[192:195],v[68:71]
s_cbranch_vccnz LSITE0_%=
LBACK0_%=:
TR a[134:135],v232 offset:12288
v_add_f32 v244, v144, v145
v_mul_f32 v28, v247, v28
v_mul_f32 v29, v247, v29
v_mul_f32 v30, v247, v30
MM v[68:71],a[212:215],v[176:179],v[68:71]
TR a[136:137],v233 offset:0
v_add_f32 v245, v148, v149
v_mul_f32 v31, v247, v31
CV v172,v24,v25
MM v[68:71],a[208:211],v[160:163],v[68:71]
TR a[138:139],v233 offset:4096
CV v173,v26,v27
CV v174,v28,v29
MM v[76:79],a[208:211],v[164:167],v[132:135]
TR a[140:141],v233 offset:8192
TR a[142:143],v233 offset:12288
CV v175,v30,v31
v_xor_b32 v143, 128, v253
MM v[76:79],a[212:215],v[180:183],v[76:79]
RD v[136:139],v143 offset:0
RD v[140:143],v143 offset:16
TR a[144:145],v232 offset:512
MM v[76:79],a[216:219],v[196:199],v[76:79]
s_waitcnt lgkmcnt(9)
TR a[146:147],v232 offset:4608
TR a[148:149],v232 offset:8704
MM v[76:79],a[220:223],v[212:215],v[76:79]
EX v64,v64
MM v[32:35],a[128:131],v[0:3],0
MM v[36:39],a[128:131],v[8:11],0
MM v[72:75],a[204:207],v[212:215],v[132:135]
TR a[150:151],v232 offset:12800
TR a[152:153],v233 offset:512
MM v[72:75],a[200:203],v[196:199],v[72:75]
s_waitcnt lgkmcnt(5)
TR a[154:155],v233 offset:4608
v_fmac_f32 v48, v242, v136
v_fmac_f32 v49, v242, v137
v_fmac_f32 v50, v242, v138
v_fmac_f32 v51, v242, v139
v_fmac_f32 v52, v242, v140
v_fmac_f32 v53, v242, v141
v_fmac_f32 v54, v242, v142
v_fmac_f32 v55, v242, v143
v_mul_f32 v187, -v252, v246
v_fma_f32 v48, v246, v48, v187
v_fma_f32 v49, v246, v49, v187
EX v65,v65
MM v[72:75],a[196:199],v[180:183],v[72:75]
TR a[156:157],v233 offset:8704
v_fma_f32 v50, v246, v50, v187
v_fma_f32 v51, v246, v51, v187
v_fma_f32 v52, v246, v52, v187
MM v[72:75],a[192:195],v[164:167],v[72:75]
TR a[158:159],v233 offset:12800
v_fma_f32 v53, v246, v53, v187
EX v66,v66
v_fma_f32 v54, v246, v54, v187
MM v[96:99],a[224:227],v[160:163],v[128:131]
TR a[160:161],v232 offset:1024
v_max3_f32 v32, |v32|, |v33|, |v34|
v_max_f32 v32, v32, |v35|
v_fma_f32 v55, v246, v55, v187
CV v184,v48,v49
EX v67,v67
MM v[96:99],a[228:231],v[176:179],v[96:99]
TR a[162:163],v232 offset:5120
TR a[164:165],v232 offset:9216
v_bfe_u32 v32, v32, 23, 8
v_add_u32 v32, 7, v32
CV v185,v50,v51
CV v186,v52,v53
CV v187,v54,v55
v_fmac_f32 v56, v243, v136
MM v[96:99],a[232:235],v[192:195],v[96:99]
TR a[166:167],v232 offset:13312
v_min_u32 v32, 240, v32
v_lshl_or_b32 v241, v32, 0, v241
v_fmac_f32 v57, v243, v137
v_fmac_f32 v58, v243, v138
EX v68,v68
MM v[96:99],a[236:239],v[208:211],v[96:99]
TR a[168:169],v233 offset:1024
v_lshl_or_b32 v32, v32, 1, 1
v_lshlrev_b32 v32, 22, v32
v_fmac_f32 v59, v243, v139
v_fmac_f32 v60, v243, v140
MM v[100:103],a[252:255],v[208:211],v[128:131]
TR a[170:171],v233 offset:5120
AW a100,v32
AW a101,v32
v_fmac_f32 v61, v243, v141
v_fmac_f32 v62, v243, v142
EX v69,v69
v_fmac_f32 v63, v243, v143
MM v[100:103],a[248:251],v[192:195],v[100:103]
TR a[172:173],v233 offset:9216
TR a[174:175],v233 offset:13312
AW a102,v32
AW a103,v32
v_mul_f32 v191, -v252, v247
v_fma_f32 v56, v247, v56, v191
MM v[100:103],a[244:247],v[176:179],v[100:103]
TR a[176:177],v232 offset:1536
v_max3_f32 v36, |v36|, |v37|, |v38|
v_max_f32 v36, v36, |v39|
v_fma_f32 v57, v247, v57, v191
EX v70,v70
v_fma_f32 v58, v247, v58, v191
MM v[100:103],a[240:243],v[160:163],v[100:103]
TR a[178:179],v232 offset:5632
v_bfe_u32 v36, v36, 23, 8
v_add_u32 v36, 7, v36
v_fma_f32 v59, v247, v59, v191
v_fma_f32 v60, v247, v60, v191
EX v71,v71
v_fma_f32 v61, v247, v61, v191
MM v[108:111],a[240:243],v[164:167],v[132:135]
TR a[180:181],v232 offset:9728
v_min_u32 v36, 240, v36
v_lshl_or_b32 v241, v36, 8, v241
v_fma_f32 v62, v247, v62, v191
v_fma_f32 v63, v247, v63, v191
CV v64,v64,v65
MM v[108:111],a[244:247],v[180:183],v[108:111]
TR a[182:183],v232 offset:13824
v_lshl_or_b32 v36, v36, 1, 1
v_lshlrev_b32 v36, 22, v36
CV v188,v56,v57
CV v189,v58,v59
EX v72,v72
CV v190,v60,v61
MM v[108:111],a[248:251],v[196:199],v[108:111]
TR a[184:185],v233 offset:1536
TR a[186:187],v233 offset:5632
TR a[188:189],v233 offset:9728
TR a[190:191],v233 offset:13824
AW a108,v36
AW a109,v36
CV v191,v62,v63
v_xor_b32 v143, 256, v253
EX v73,v73
MM v[108:111],a[252:255],v[212:215],v[108:111]
RD v[136:139],v143 offset:0
RD v[140:143],v143 offset:16
AW a110,v36
AW a111,v36
CV v65,v66,v67
MM v[104:107],a[236:239],v[212:215],v[132:135]
EX v74,v74
MM v[104:107],a[232:235],v[196:199],v[104:107]
EX v75,v75
MM v[104:107],a[228:231],v[180:183],v[104:107]
EX v76,v76
CV v66,v68,v69
MM v[104:107],a[224:227],v[164:167],v[104:107]
s_waitcnt vmcnt(12) lgkmcnt(0)
s_barrier
v_fmac_f32 v80, v242, v136
v_fmac_f32 v81, v242, v137
v_fmac_f32 v82, v242, v138
v_fmac_f32 v83, v242, v139
v_fmac_f32 v84, v242, v140
v_fmac_f32 v85, v242, v141
v_fmac_f32 v86, v242, v142
v_fmac_f32 v87, v242, v143
v_mul_f32 v203, -v252, v246
v_fma_f32 v80, v246, v80, v203
MM a[0:3],a[128:131],v[0:3],a[100:103]
s_add_u32 m0, s65, 0
v_fma_f32 v81, v246, v81, v203
v_fma_f32 v82, v246, v82, v203
v_fma_f32 v83, v246, v83, v203
MM a[0:3],a[132:135],v[4:7],a[0:3]
v_fma_f32 v84, v246, v84, v203
v_fma_f32 v85, v246, v85, v203
EX v77,v77
CV v67,v70,v71
MM a[8:11],a[132:135],v[12:15],a[108:111]
v_fma_f32 v86, v246, v86, v203
v_fma_f32 v87, v246, v87, v203
MM a[8:11],a[128:131],v[8:11],a[8:11]
CV v200,v80,v81
CV v201,v82,v83
EX v78,v78
CV v202,v84,v85
MM a[12:15],a[136:139],v[8:11],a[108:111]
CV v203,v86,v87
v_fmac_f32 v88, v243, v136
v_fmac_f32 v89, v243, v137
EX v79,v79
MM a[12:15],a[140:143],v[12:15],a[12:15]
v_fmac_f32 v90, v243, v138
v_fmac_f32 v91, v243, v139
CV v72,v72,v73
MM a[4:7],a[140:143],v[4:7],a[100:103]
v_fmac_f32 v92, v243, v140
v_fmac_f32 v93, v243, v141
EX v96,v96
v_fmac_f32 v94, v243, v142
MM a[4:7],a[136:139],v[0:3],a[4:7]
v_fmac_f32 v95, v243, v143
v_mul_f32 v207, -v252, v247
MM a[32:35],a[144:147],v[0:3],a[100:103]
RD a[192:195],v224 offset:32768
v_fma_f32 v88, v247, v88, v207
v_fma_f32 v89, v247, v89, v207
EX v97,v97
CV v73,v74,v75
v_fma_f32 v90, v247, v90, v207
MM a[32:35],a[148:151],v[4:7],a[32:35]
buffer_load_dwordx4 v228, s[8:11], s64 offen offset:0 lds
v_fma_f32 v91, v247, v91, v207
v_fma_f32 v92, v247, v92, v207
EX v98,v98
MM a[40:43],a[148:151],v[12:15],a[108:111]
RD a[196:199],v225 offset:32768
v_fma_f32 v93, v247, v93, v207
v_fma_f32 v94, v247, v94, v207
MM a[40:43],a[144:147],v[8:11],a[40:43]
RD a[200:203],v226 offset:32768
v_fma_f32 v95, v247, v95, v207
CV v204,v88,v89
EX v99,v99
CV v74,v76,v77
CV v205,v90,v91
MM a[44:47],a[152:155],v[8:11],a[108:111]
CV v206,v92,v93
CV v207,v94,v95
MM a[44:47],a[156:159],v[12:15],a[44:47]
RD a[204:207],v227 offset:32768
buffer_load_dwordx4 v229, s[8:11], s64 offen offset:1024 lds
v_xor_b32 v143, 384, v253
EX v100,v100
MM a[36:39],a[156:159],v[4:7],a[100:103]
RD v[136:139],v143 offset:0
RD v[140:143],v143 offset:16
EX v101,v101
CV v75,v78,v79
MM a[36:39],a[152:155],v[0:3],a[36:39]
RD a[208:211],v224 offset:36864
RD a[212:215],v225 offset:36864
MM a[64:67],a[160:163],v[0:3],a[100:103]
EX v102,v102
MM a[64:67],a[164:167],v[4:7],a[64:67]
buffer_load_dwordx4 v230, s[8:11], s64 offen offset:2048 lds
MM a[72:75],a[164:167],v[12:15],a[108:111]
s_waitcnt lgkmcnt(2)
v_fmac_f32 v112, v242, v136
v_fmac_f32 v113, v242, v137
v_fmac_f32 v114, v242, v138
v_fmac_f32 v115, v242, v139
v_fmac_f32 v116, v242, v140
v_fmac_f32 v117, v242, v141
v_fmac_f32 v118, v242, v142
v_fmac_f32 v119, v242, v143
v_mul_f32 v219, -v252, v246
v_fma_f32 v112, v246, v112, v219
v_fma_f32 v113, v246, v113, v219
EX v103,v103
CV v68,v96,v97
v_fma_f32 v114, v246, v114, v219
MM a[72:75],a[160:163],v[8:11],a[72:75]
RD a[216:219],v226 offset:36864
v_fma_f32 v115, v246, v115, v219
v_fma_f32 v116, v246, v116, v219
EX v104,v104
MM a[76:79],a[168:171],v[8:11],a[108:111]
buffer_load_dwordx4 v231, s[8:11], s64 offen offset:3072 lds
RD a[220:223],v227 offset:36864
v_fma_f32 v117, v246, v117, v219
v_fma_f32 v118, v246, v118, v219
MM a[76:79],a[172:175],v[12:15],a[76:79]
s_add_u32 m0, s65, 65536
v_fma_f32 v119, v246, v119, v219
CV v216,v112,v113
EX v105,v105
CV v217,v114,v115
CV v69,v98,v99
MM a[68:71],a[172:175],v[4:7],a[100:103]
RD a[224:227],v224 offset:40960
CV v218,v116,v117
CV v219,v118,v119
v_fmac_f32 v120, v243, v136
MM a[68:71],a[168:171],v[0:3],a[68:71]
v_fmac_f32 v121, v243, v137
v_fmac_f32 v122, v243, v138
EX v106,v106
v_fmac_f32 v123, v243, v139
MM a[96:99],a[176:179],v[0:3],a[100:103]
RD a[228:231],v225 offset:40960
buffer_load_dwordx4 v235, s[12:15], s81 offen offset:0 lds
v_fmac_f32 v124, v243, v140
v_fmac_f32 v125, v243, v141
EX v107,v107
CV v70,v100,v101
MM a[96:99],a[180:183],v[4:7],a[96:99]
v_fmac_f32 v126, v243, v142
v_fmac_f32 v127, v243, v143
MM a[104:107],a[180:183],v[12:15],a[108:111]
RD a[232:235],v226 offset:40960
v_mul_f32 v223, -v252, v247
v_fma_f32 v120, v247, v120, v223
EX v108,v108
v_fma_f32 v121, v247, v121, v223
MM a[104:107],a[176:179],v[8:11],a[104:107]
v_fma_f32 v122, v247, v122, v223
v_fma_f32 v123, v247, v123, v223
CV v71,v102,v103
MM a[108:111],a[184:187],v[8:11],a[108:111]
RD a[236:239],v227 offset:40960
buffer_load_dwordx4 v235, s[12:15], s64 offen offset:1024 lds
v_fma_f32 v124, v247, v124, v223
v_fma_f32 v125, v247, v125, v223
EX v109,v109
MM a[108:111],a[188:191],v[12:15],a[108:111]
v_fma_f32 v126, v247, v126, v223
v_fma_f32 v127, v247, v127, v223
EX v110,v110
CV v220,v120,v121
MM a[100:103],a[188:191],v[4:7],a[100:103]
RD a[240:243],v224 offset:45056
RD a[244:247],v225 offset:45056
RD a[248:251],v226 offset:45056
RD a[252:255],v227 offset:45056
CV v221,v122,v123
EX v111,v111
CV v222,v124,v125
CV v223,v126,v127
MM a[100:103],a[184:187],v[0:3],a[100:103]
buffer_load_dwordx4 v235, s[12:15], s81 offen offset:2048 lds
CV v76,v104,v105
v_smfmac_f32_16x16x64_bf16 v[144:147], v[236:239], v[64:71], v234
CV v77,v106,v107
CV v78,v108,v109
CV v79,v110,v111
s_nop 1
v_smfmac_f32_16x16x64_bf16 v[148:151], v[236:239], v[72:79], v234
s_waitcnt lgkmcnt(0)
buffer_load_dwordx4 v235, s[12:15], s64 offen offset:3072 lds
MM v[0:3],a[192:195],v[160:163],v[128:131]
s_add_u32 s64, s64, 16384
TR a[128:129],v232 offset:16384
TR a[130:131],v232 offset:20480
MM v[0:3],a[196:199],v[176:179],v[0:3]
s_add_u32 s81, s64, 896
TR a[132:133],v232 offset:24576
TR a[134:135],v232 offset:28672
MM v[0:3],a[200:203],v[192:195],v[0:3]
TR a[136:137],v233 offset:16384
TR a[138:139],v233 offset:20480
MM v[0:3],a[204:207],v[208:211],v[0:3]
v_max3_f32 v249, v144, v145, v148
v_max_f32 v249, v249, v149
MM v[4:7],a[220:223],v[208:211],v[128:131]
TR a[140:141],v233 offset:24576
v_cmp_lt_f32 vcc, s71, v249
MM v[4:7],a[216:219],v[192:195],v[4:7]
s_cbranch_vccnz LSITE1_%=
LBACK1_%=:
TR a[142:143],v233 offset:28672
v_add_f32 v244, v144, v145
MM v[4:7],a[212:215],v[176:179],v[4:7]
TR a[144:145],v232 offset:16896
v_add_f32 v245, v148, v149
MM v[4:7],a[208:211],v[160:163],v[4:7]
TR a[146:147],v232 offset:20992
TR a[148:149],v232 offset:25088
MM v[12:15],a[208:211],v[164:167],v[132:135]
EX v0,v0
MM v[12:15],a[212:215],v[180:183],v[12:15]
EX v1,v1
MM v[12:15],a[216:219],v[196:199],v[12:15]
TR a[150:151],v232 offset:29184
CV v0,v0,v1
MM v[12:15],a[220:223],v[212:215],v[12:15]
TR a[152:153],v233 offset:16896
TR a[154:155],v233 offset:20992
s_nop 0
LPG5_%=:
)ASM"
R"ASM(
s_cbranch_execz LPG9_%=
MM v[8:11],a[204:207],v[212:215],v[132:135]
EX v2,v2
MM v[8:11],a[200:203],v[196:199],v[8:11]
EX v3,v3
MM v[8:11],a[196:199],v[180:183],v[8:11]
TR a[156:157],v233 offset:25088
CV v1,v2,v3
MM v[8:11],a[192:195],v[164:167],v[8:11]
TR a[158:159],v233 offset:29184
TR a[160:161],v232 offset:17408
MM v[32:35],a[224:227],v[160:163],v[128:131]
EX v4,v4
MM v[32:35],a[228:231],v[176:179],v[32:35]
TR a[162:163],v232 offset:21504
TR a[164:165],v232 offset:25600
MM v[32:35],a[232:235],v[192:195],v[32:35]
EX v5,v5
MM v[32:35],a[236:239],v[208:211],v[32:35]
TR a[166:167],v232 offset:29696
CV v2,v4,v5
MM v[36:39],a[252:255],v[208:211],v[128:131]
EX v6,v6
MM v[36:39],a[248:251],v[192:195],v[36:39]
TR a[168:169],v233 offset:17408
TR a[170:171],v233 offset:21504
MM v[36:39],a[244:247],v[176:179],v[36:39]
EX v7,v7
MM v[36:39],a[240:243],v[160:163],v[36:39]
TR a[172:173],v233 offset:25600
CV v3,v6,v7
MM v[44:47],a[240:243],v[164:167],v[132:135]
EX v8,v8
MM v[44:47],a[244:247],v[180:183],v[44:47]
TR a[174:175],v233 offset:29696
TR a[176:177],v232 offset:17920
MM v[44:47],a[248:251],v[196:199],v[44:47]
EX v9,v9
MM v[44:47],a[252:255],v[212:215],v[44:47]
TR a[178:179],v232 offset:22016
TR a[180:181],v232 offset:26112
MM v[40:43],a[236:239],v[212:215],v[132:135]
EX v10,v10
MM v[40:43],a[232:235],v[196:199],v[40:43]
TR a[182:183],v232 offset:30208
CV v8,v8,v9
MM v[40:43],a[228:231],v[180:183],v[40:43]
EX v11,v11
MM v[40:43],a[224:227],v[164:167],v[40:43]
TR a[184:185],v233 offset:17920
TR a[186:187],v233 offset:22016
MM v[16:19],a[192:195],v[168:171],0
EX v12,v12
MM v[16:19],a[196:199],v[184:187],v[16:19]
TR a[188:189],v233 offset:26112
TR a[190:191],v233 offset:30208
MM v[16:19],a[200:203],v[200:203],v[16:19]
EX v13,v13
MM v[16:19],a[204:207],v[216:219],v[16:19]
EX v14,v14
MM v[24:27],a[192:195],v[172:175],0
EX v15,v15
MM v[24:27],a[196:199],v[188:191],v[24:27]
EX v32,v32
MM v[24:27],a[200:203],v[204:207],v[24:27]
EX v33,v33
MM v[24:27],a[204:207],v[220:223],v[24:27]
s_waitcnt vmcnt(12) lgkmcnt(0)
s_barrier
CV v9,v10,v11
MM a[0:3],a[128:131],v[64:67],a[0:3]
s_add_u32 m0, s65, 16384
CV v10,v12,v13
CV v11,v14,v15
MM a[0:3],a[132:135],v[68:71],a[0:3]
RD a[192:195],v224 offset:49152
CV v4,v32,v33
MM a[8:11],a[132:135],v[76:79],a[8:11]
v_max3_f32 v154, v16, v17, v18
v_max_f32 v154, v154, v19
MM a[8:11],a[128:131],v[72:75],a[8:11]
buffer_load_dwordx4 v228, s[8:11], s64 offen offset:0 lds
MM a[12:15],a[136:139],v[72:75],a[12:15]
EX v34,v34
MM a[12:15],a[140:143],v[76:79],a[12:15]
RD a[196:199],v225 offset:49152
v_mov_b32 v155, v154
MM a[4:7],a[140:143],v[68:71],a[4:7]
s_nop 0
v_permlane16_swap_b32 v154, v155
v_max_f32 v154, v154, v155
MM a[4:7],a[136:139],v[64:67],a[4:7]
EX v35,v35
MM a[32:35],a[144:147],v[64:67],a[32:35]
RD a[200:203],v226 offset:49152
v_mov_b32 v155, v154
MM a[32:35],a[148:151],v[68:71],a[32:35]
s_nop 0
v_permlane32_swap_b32 v154, v155
v_max_f32 v154, v154, v155
MM a[40:43],a[148:151],v[76:79],a[40:43]
EX v36,v36
MM a[40:43],a[144:147],v[72:75],a[40:43]
RD a[204:207],v227 offset:49152
v_sub_f32 v136, 0xc1600000, v154
MM a[44:47],a[152:155],v[72:75],a[44:47]
EX v37,v37
MM a[44:47],a[156:159],v[76:79],a[44:47]
buffer_load_dwordx4 v229, s[8:11], s64 offen offset:1024 lds
v_sub_f32 v137, 0xc1600000, v154
v_sub_f32 v138, 0xc1600000, v154
MM a[36:39],a[156:159],v[68:71],a[36:39]
EX v38,v38
MM a[36:39],a[152:155],v[64:67],a[36:39]
RD a[208:211],v224 offset:53248
v_sub_f32 v139, 0xc1600000, v154
v_mov_b32 v246, 0
MM a[64:67],a[160:163],v[64:67],a[64:67]
EX v39,v39
v_mov_b32 v152, 0
MM a[64:67],a[164:167],v[68:71],a[64:67]
buffer_load_dwordx4 v230, s[8:11], s64 offen offset:2048 lds
RD a[212:215],v225 offset:53248
v_mov_b32 v153, 0
MM a[72:75],a[164:167],v[76:79],a[72:75]
EX v40,v40
v_max3_f32 v158, v24, v25, v26
MM a[72:75],a[160:163],v[72:75],a[72:75]
RD a[216:219],v226 offset:53248
v_max_f32 v158, v158, v27
CV v5,v34,v35
MM a[76:79],a[168:171],v[72:75],a[76:79]
buffer_load_dwordx4 v231, s[8:11], s64 offen offset:3072 lds
RD a[220:223],v227 offset:53248
EX v41,v41
v_mov_b32 v159, v158
MM a[76:79],a[172:175],v[76:79],a[76:79]
s_add_u32 m0, s65, 81920
v_permlane16_swap_b32 v158, v159
v_max_f32 v158, v158, v159
MM a[68:71],a[172:175],v[68:71],a[68:71]
RD a[224:227],v224 offset:57344
EX v42,v42
v_mov_b32 v159, v158
MM a[68:71],a[168:171],v[64:67],a[68:71]
s_nop 0
v_permlane32_swap_b32 v158, v159
CV v6,v36,v37
EX v43,v43
MM a[96:99],a[176:179],v[64:67],a[96:99]
RD a[228:231],v225 offset:57344
buffer_load_dwordx4 v235, s[12:15], s81 offen offset:0 lds
v_max_f32 v158, v158, v159
MM a[96:99],a[180:183],v[68:71],a[96:99]
v_sub_f32 v140, 0xc1600000, v158
CV v7,v38,v39
EX v44,v44
MM a[104:107],a[180:183],v[76:79],a[104:107]
RD a[232:235],v226 offset:57344
v_sub_f32 v141, 0xc1600000, v158
MM a[104:107],a[176:179],v[72:75],a[104:107]
v_sub_f32 v142, 0xc1600000, v158
v_sub_f32 v143, 0xc1600000, v158
EX v45,v45
MM a[108:111],a[184:187],v[72:75],a[108:111]
RD a[236:239],v227 offset:57344
buffer_load_dwordx4 v235, s[12:15], s64 offen offset:1024 lds
v_mov_b32 v247, 0
v_mov_b32 v156, 0
v_mov_b32 v157, 0
MM a[108:111],a[188:191],v[76:79],a[108:111]
EX v46,v46
MM a[100:103],a[188:191],v[68:71],a[100:103]
RD a[240:243],v224 offset:61440
RD a[244:247],v225 offset:61440
RD a[248:251],v226 offset:61440
RD a[252:255],v227 offset:61440
EX v47,v47
MM a[100:103],a[184:187],v[64:67],a[100:103]
buffer_load_dwordx4 v235, s[12:15], s81 offen offset:2048 lds
CV v12,v40,v41
v_smfmac_f32_16x16x64_bf16 v[144:147], v[236:239], v[0:7], v234
CV v13,v42,v43
CV v14,v44,v45
CV v15,v46,v47
s_nop 1
v_smfmac_f32_16x16x64_bf16 v[148:151], v[236:239], v[8:15], v234
s_waitcnt lgkmcnt(0)
buffer_load_dwordx4 v235, s[12:15], s64 offen offset:3072 lds
MM v[64:67],a[192:195],v[160:163],v[128:131]
s_add_u32 s64, s64, 16384
TR a[128:129],v232 offset:32768
s_add_u32 s81, s64, 896
MM v[64:67],a[196:199],v[176:179],v[64:67]
TR a[130:131],v232 offset:36864
TR a[132:133],v232 offset:40960
MM v[64:67],a[200:203],v[192:195],v[64:67]
TR a[134:135],v232 offset:45056
TR a[136:137],v233 offset:32768
MM v[64:67],a[204:207],v[208:211],v[64:67]
v_max3_f32 v249, v144, v145, v148
v_max_f32 v249, v249, v149
MM v[68:71],a[220:223],v[208:211],v[128:131]
TR a[138:139],v233 offset:36864
v_cmp_lt_f32 vcc, s71, v249
MM v[68:71],a[216:219],v[192:195],v[68:71]
s_cbranch_vccnz LSITE2_%=
LBACK2_%=:
TR a[140:141],v233 offset:40960
v_add_f32 v244, v144, v145
MM v[68:71],a[212:215],v[176:179],v[68:71]
TR a[142:143],v233 offset:45056
v_add_f32 v245, v148, v149
MM v[68:71],a[208:211],v[160:163],v[68:71]
TR a[144:145],v232 offset:33280
TR a[146:147],v232 offset:37376
MM v[76:79],a[208:211],v[164:167],v[132:135]
s_lshl_b32 s90, %[swave], 10
s_add_u32 s90, s90, 81920
TR a[148:149],v232 offset:41472
MM v[76:79],a[212:215],v[180:183],v[76:79]
buffer_load_dwordx4 v[48:51], v248, s[20:23], s90 offen offset:0 sc1
MM v[76:79],a[216:219],v[196:199],v[76:79]
EX v64,v64
MM v[76:79],a[220:223],v[212:215],v[76:79]
EX v65,v65
MM v[72:75],a[204:207],v[212:215],v[132:135]
EX v66,v66
MM v[72:75],a[200:203],v[196:199],v[72:75]
EX v67,v67
MM v[72:75],a[196:199],v[180:183],v[72:75]
CV v64,v64,v65
CV v65,v66,v67
MM v[72:75],a[192:195],v[164:167],v[72:75]
EX v68,v68
MM v[80:83],a[192:195],v[168:171],v[136:139]
EX v69,v69
MM v[80:83],a[196:199],v[184:187],v[80:83]
EX v70,v70
MM v[80:83],a[200:203],v[200:203],v[80:83]
EX v71,v71
MM v[80:83],a[204:207],v[216:219],v[80:83]
CV v66,v68,v69
CV v67,v70,v71
MM v[84:87],a[220:223],v[216:219],v[136:139]
EX v72,v72
MM v[84:87],a[216:219],v[200:203],v[84:87]
EX v73,v73
MM v[84:87],a[212:215],v[184:187],v[84:87]
TR a[150:151],v232 offset:45568
TR a[152:153],v233 offset:33280
MM v[84:87],a[208:211],v[168:171],v[84:87]
EX v74,v74
MM v[92:95],a[208:211],v[172:175],v[140:143]
EX v75,v75
MM v[92:95],a[212:215],v[188:191],v[92:95]
TR a[154:155],v233 offset:37376
CV v72,v72,v73
MM v[92:95],a[216:219],v[204:207],v[92:95]
EX v76,v76
MM v[92:95],a[220:223],v[220:223],v[92:95]
EX v77,v77
MM v[88:91],a[204:207],v[220:223],v[140:143]
TR a[156:157],v233 offset:41472
CV v73,v74,v75
MM v[88:91],a[200:203],v[204:207],v[88:91]
EX v78,v78
MM v[88:91],a[196:199],v[188:191],v[88:91]
EX v79,v79
MM v[88:91],a[192:195],v[172:175],v[88:91]
TR a[158:159],v233 offset:45568
CV v74,v76,v77
MM v[96:99],a[224:227],v[160:163],v[128:131]
EX v80,v80
MM v[96:99],a[228:231],v[176:179],v[96:99]
EX v81,v81
MM v[96:99],a[232:235],v[192:195],v[96:99]
TR a[160:161],v232 offset:33792
TR a[162:163],v232 offset:37888
MM v[96:99],a[236:239],v[208:211],v[96:99]
EX v82,v82
MM v[100:103],a[252:255],v[208:211],v[128:131]
EX v83,v83
MM v[100:103],a[248:251],v[192:195],v[100:103]
TR a[164:165],v232 offset:41984
CV v75,v78,v79
MM v[100:103],a[244:247],v[176:179],v[100:103]
EX v84,v84
MM v[100:103],a[240:243],v[160:163],v[100:103]
EX v85,v85
MM v[108:111],a[240:243],v[164:167],v[132:135]
TR a[166:167],v232 offset:46080
CV v80,v80,v81
MM v[108:111],a[244:247],v[180:183],v[108:111]
EX v86,v86
MM v[108:111],a[248:251],v[196:199],v[108:111]
EX v87,v87
MM v[108:111],a[252:255],v[212:215],v[108:111]
TR a[168:169],v233 offset:33792
TR a[170:171],v233 offset:37888
MM v[104:107],a[236:239],v[212:215],v[132:135]
EX v88,v88
MM v[104:107],a[232:235],v[196:199],v[104:107]
EX v89,v89
MM v[104:107],a[228:231],v[180:183],v[104:107]
TR a[172:173],v233 offset:41984
CV v81,v82,v83
EX v90,v90
MM v[104:107],a[224:227],v[164:167],v[104:107]
TR a[174:175],v233 offset:46080
CV v82,v84,v85
MM v[112:115],a[224:227],v[168:171],v[136:139]
EX v91,v91
MM v[112:115],a[228:231],v[184:187],v[112:115]
EX v92,v92
MM v[112:115],a[232:235],v[200:203],v[112:115]
TR a[176:177],v232 offset:34304
EX v93,v93
MM v[112:115],a[236:239],v[216:219],v[112:115]
TR a[178:179],v232 offset:38400
CV v83,v86,v87
MM v[116:119],a[252:255],v[216:219],v[136:139]
EX v94,v94
MM v[116:119],a[248:251],v[200:203],v[116:119]
EX v95,v95
MM v[116:119],a[244:247],v[184:187],v[116:119]
TR a[180:181],v232 offset:42496
CV v88,v88,v89
MM v[116:119],a[240:243],v[168:171],v[116:119]
s_memrealtime s[56:57]
EX v96,v96
MM v[124:127],a[240:243],v[172:175],v[140:143]
s_memtime s[58:59]
TR a[182:183],v232 offset:46592
EX v97,v97
CV v89,v90,v91
MM v[124:127],a[244:247],v[188:191],v[124:127]
EX v98,v98
MM v[124:127],a[248:251],v[204:207],v[124:127]
TR a[184:185],v233 offset:34304
TR a[186:187],v233 offset:38400
TR a[188:189],v233 offset:42496
TR a[190:191],v233 offset:46592
MM v[124:127],a[252:255],v[220:223],v[124:127]
EX v99,v99
CV v90,v92,v93
MM v[120:123],a[236:239],v[220:223],v[140:143]
EX v100,v100
MM v[120:123],a[232:235],v[204:207],v[120:123]
EX v101,v101
CV v91,v94,v95
MM v[120:123],a[228:231],v[188:191],v[120:123]
EX v102,v102
MM v[120:123],a[224:227],v[172:175],v[120:123]
s_waitcnt vmcnt(13) lgkmcnt(0)
s_barrier
CV v68,v96,v97
MM a[0:3],a[128:131],v[0:3],a[0:3]
s_sub_u32 s52, s56, s52
s_sub_u32 s54, s58, s54
v_cvt_f32_u32 v250, s52
EX v103,v103
MM a[0:3],a[132:135],v[4:7],a[0:3]
s_add_u32 m0, s65, 32768
v_cvt_f32_u32 v251, s54
CV v69,v98,v99
EX v104,v104
MM a[8:11],a[132:135],v[12:15],a[8:11]
v_rcp_f32 v251, v251
MM a[8:11],a[128:131],v[8:11],a[8:11]
v_mul_f32 v250, 0x47abe000, v250
s_lshl_b32 s52, %[swave], 11
EX v105,v105
MM a[12:15],a[136:139],v[8:11],a[12:15]
v_mul_f32 v250, v250, v251
s_lshl_b32 s54, %[sb], 3
EX v106,v106
MM a[12:15],a[140:143],v[12:15],a[12:15]
v_cvt_u32_f32 v250, v250
s_add_u32 s52, s52, s54
EX v107,v107
MM a[4:7],a[140:143],v[4:7],a[4:7]
s_waitcnt vmcnt(0)
v_add_u32 v250, s56, v250
v_mov_b32 v251, s69
CV v70,v100,v101
MM a[4:7],a[136:139],v[0:3],a[4:7]
s_add_u32 s52, s52, 86016
s_mov_b64 exec, 1
buffer_store_dwordx2 v[250:251], off, s[20:23], s52 sc1
s_mov_b64 exec, -1
s_nop 0
EX v108,v108
MM a[32:35],a[144:147],v[0:3],a[32:35]
RD a[192:195],v224 offset:0
v_xor_b32 v48, s69, v48
v_xor_b32 v49, s69, v49
EX v109,v109
CV v71,v102,v103
MM a[32:35],a[148:151],v[4:7],a[32:35]
buffer_load_dwordx4 v228, s[8:11], s64 offen offset:0 lds
v_xor_b32 v50, s69, v50
v_xor_b32 v51, s69, v51
MM a[40:43],a[148:151],v[12:15],a[40:43]
RD a[196:199],v225 offset:0
)ASM"
R"ASM(
v_or3_b32 v252, v48, v49, v50
v_or_b32 v252, v252, v51
EX v110,v110
MM a[40:43],a[144:147],v[8:11],a[40:43]
v_cmp_ne_u32 vcc, 0, v252
s_cbranch_vccnz LRFR_%=
LRFRb_%=:
EX v111,v111
CV v76,v104,v105
MM a[44:47],a[152:155],v[8:11],a[44:47]
s_lshl_b32 s90, %[swave], 11
s_add_u32 s78, s90, %[slds]
s_add_u32 s78, s78, 155648
RD a[200:203],v226 offset:0
EX v112,v112
MM a[44:47],a[156:159],v[12:15],a[44:47]
s_add_u32 s90, s90, 53248
buffer_load_dwordx4 v229, s[8:11], s64 offen offset:1024 lds
MM a[36:39],a[156:159],v[4:7],a[36:39]
RD a[204:207],v227 offset:0
EX v113,v113
CV v77,v106,v107
MM a[36:39],a[152:155],v[0:3],a[36:39]
EX v114,v114
MM a[64:67],a[160:163],v[0:3],a[64:67]
RD a[208:211],v224 offset:4096
EX v115,v115
CV v78,v108,v109
MM a[64:67],a[164:167],v[4:7],a[64:67]
buffer_load_dwordx4 v230, s[8:11], s64 offen offset:2048 lds
MM a[72:75],a[164:167],v[12:15],a[72:75]
RD a[212:215],v225 offset:4096
EX v116,v116
MM a[72:75],a[160:163],v[8:11],a[72:75]
EX v117,v117
CV v79,v110,v111
MM a[76:79],a[168:171],v[8:11],a[76:79]
RD a[216:219],v226 offset:4096
CV v84,v112,v113
EX v118,v118
MM a[76:79],a[172:175],v[12:15],a[76:79]
buffer_load_dwordx4 v231, s[8:11], s64 offen offset:3072 lds
MM a[68:71],a[172:175],v[4:7],a[68:71]
s_add_u32 m0, s65, 98304
RD a[220:223],v227 offset:4096
EX v119,v119
MM a[68:71],a[168:171],v[0:3],a[68:71]
EX v120,v120
MM a[96:99],a[176:179],v[0:3],a[96:99]
RD a[224:227],v224 offset:8192
CV v85,v114,v115
EX v121,v121
MM a[96:99],a[180:183],v[4:7],a[96:99]
RD a[228:231],v225 offset:8192
CV v86,v116,v117
MM a[104:107],a[180:183],v[12:15],a[104:107]
buffer_load_dwordx4 v235, s[12:15], s81 offen offset:0 lds
EX v122,v122
MM a[104:107],a[176:179],v[8:11],a[104:107]
EX v123,v123
MM a[108:111],a[184:187],v[8:11],a[108:111]
RD a[232:235],v226 offset:8192
CV v87,v118,v119
MM a[108:111],a[188:191],v[12:15],a[108:111]
EX v124,v124
MM a[100:103],a[188:191],v[4:7],a[100:103]
RD a[236:239],v227 offset:8192
buffer_load_dwordx4 v235, s[12:15], s64 offen offset:1024 lds
EX v125,v125
MM a[100:103],a[184:187],v[0:3],a[100:103]
EX v126,v126
v_smfmac_f32_16x16x64_bf16 v[144:147], v[236:239], v[64:71], v234
RD a[240:243],v224 offset:12288
RD a[244:247],v225 offset:12288
RD a[248:251],v226 offset:12288
RD a[252:255],v227 offset:12288
EX v127,v127
v_smfmac_f32_16x16x64_bf16 v[148:151], v[236:239], v[72:79], v234
CV v92,v120,v121
v_smfmac_f32_16x16x64_bf16 v[152:155], v[236:239], v[80:87], v234
buffer_load_dwordx4 v235, s[12:15], s81 offen offset:2048 lds
CV v93,v122,v123
CV v94,v124,v125
CV v95,v126,v127
s_nop 1
v_smfmac_f32_16x16x64_bf16 v[156:159], v[236:239], v[88:95], v234
s_waitcnt lgkmcnt(0)
buffer_load_dwordx4 v235, s[12:15], s64 offen offset:3072 lds
MM v[0:3],a[192:195],v[160:163],v[128:131]
s_add_u32 s64, s64, 16384
TR a[128:129],v232 offset:49152
s_add_u32 s81, s64, 896
MM v[0:3],a[196:199],v[176:179],v[0:3]
TR a[130:131],v232 offset:53248
v_max3_f32 v249, v144, v145, v148
MM v[0:3],a[200:203],v[192:195],v[0:3]
TR a[132:133],v232 offset:57344
v_max3_f32 v249, v249, v149, v152
MM v[0:3],a[204:207],v[208:211],v[0:3]
TR a[134:135],v232 offset:61440
v_max3_f32 v249, v249, v153, v156
MM v[4:7],a[220:223],v[208:211],v[128:131]
TR a[136:137],v233 offset:49152
v_max_f32 v249, v249, v157
MM v[4:7],a[216:219],v[192:195],v[4:7]
TR a[138:139],v233 offset:53248
v_cmp_lt_f32 vcc, s71, v249
MM v[4:7],a[212:215],v[176:179],v[4:7]
TR a[140:141],v233 offset:57344
TR a[142:143],v233 offset:61440
MM v[4:7],a[208:211],v[160:163],v[4:7]
s_cbranch_vccnz LSITE3_%=
LBACK3_%=:
MM v[12:15],a[208:211],v[164:167],v[132:135]
s_mov_b32 m0, s78
EX v0,v0
MM v[12:15],a[212:215],v[180:183],v[12:15]
buffer_load_dwordx4 v248, s[20:23], s90 offen offset:0 sc1 lds
MM v[12:15],a[216:219],v[196:199],v[12:15]
s_waitcnt lgkmcnt(6)
buffer_load_dwordx4 v248, s[20:23], s90 offen offset:1024 sc1 lds
MM v[12:15],a[220:223],v[212:215],v[12:15]
EX v1,v1
MM v[112:115],a[128:131],v[80:83],0
MM v[116:119],a[128:131],v[88:91],0
MM v[8:11],a[204:207],v[212:215],v[132:135]
EX v2,v2
MM v[8:11],a[200:203],v[196:199],v[8:11]
TR a[144:145],v232 offset:49664
CV v0,v0,v1
MM v[8:11],a[196:199],v[180:183],v[8:11]
EX v3,v3
MM v[8:11],a[192:195],v[164:167],v[8:11]
TR a[146:147],v232 offset:53760
CV v1,v2,v3
MM v[16:19],a[192:195],v[168:171],v[136:139]
v_max3_f32 v112, |v112|, |v113|, |v114|
v_max_f32 v112, v112, |v115|
MM v[16:19],a[196:199],v[184:187],v[16:19]
v_bfe_u32 v112, v112, 23, 8
v_add_u32 v112, 7, v112
MM v[16:19],a[200:203],v[200:203],v[16:19]
v_min_u32 v112, 240, v112
v_lshl_or_b32 v241, v112, 16, v241
MM v[16:19],a[204:207],v[216:219],v[16:19]
v_lshl_or_b32 v112, v112, 1, 1
v_lshlrev_b32 v112, 22, v112
EX v4,v4
MM v[20:23],a[220:223],v[216:219],v[136:139]
AW a116,v112
AW a117,v112
MM v[20:23],a[216:219],v[200:203],v[20:23]
AW a118,v112
AW a119,v112
EX v5,v5
MM v[20:23],a[212:215],v[184:187],v[20:23]
v_max3_f32 v116, |v116|, |v117|, |v118|
v_max_f32 v116, v116, |v119|
MM v[20:23],a[208:211],v[168:171],v[20:23]
v_bfe_u32 v116, v116, 23, 8
v_add_u32 v116, 7, v116
EX v6,v6
MM v[28:31],a[208:211],v[172:175],v[140:143]
v_min_u32 v116, 240, v116
v_lshl_or_b32 v241, v116, 24, v241
MM v[28:31],a[212:215],v[188:191],v[28:31]
TR a[148:149],v232 offset:57856
v_lshl_or_b32 v116, v116, 1, 1
v_lshlrev_b32 v116, 22, v116
EX v7,v7
MM v[28:31],a[216:219],v[204:207],v[28:31]
AW a124,v116
AW a125,v116
MM v[28:31],a[220:223],v[220:223],v[28:31]
TR a[150:151],v232 offset:61952
AW a126,v116
AW a127,v116
EX v8,v8
MM v[24:27],a[204:207],v[220:223],v[140:143]
TR a[152:153],v233 offset:49664
TR a[154:155],v233 offset:53760
MM v[24:27],a[200:203],v[204:207],v[24:27]
EX v9,v9
MM v[24:27],a[196:199],v[188:191],v[24:27]
EX v10,v10
MM v[24:27],a[192:195],v[172:175],v[24:27]
TR a[156:157],v233 offset:57856
CV v2,v4,v5
MM v[32:35],a[224:227],v[160:163],v[128:131]
EX v11,v11
MM v[32:35],a[228:231],v[176:179],v[32:35]
TR a[158:159],v233 offset:61952
CV v3,v6,v7
MM v[32:35],a[232:235],v[192:195],v[32:35]
EX v12,v12
MM v[32:35],a[236:239],v[208:211],v[32:35]
TR a[160:161],v232 offset:50176
TR a[162:163],v232 offset:54272
MM v[36:39],a[252:255],v[208:211],v[128:131]
EX v13,v13
LPG6_%=:
s_cbranch_execz LPG10_%=
MM v[36:39],a[248:251],v[192:195],v[36:39]
EX v14,v14
MM v[36:39],a[244:247],v[176:179],v[36:39]
TR a[164:165],v232 offset:58368
CV v8,v8,v9
MM v[36:39],a[240:243],v[160:163],v[36:39]
EX v15,v15
MM v[44:47],a[240:243],v[164:167],v[132:135]
TR a[166:167],v232 offset:62464
CV v9,v10,v11
MM v[44:47],a[244:247],v[180:183],v[44:47]
EX v16,v16
MM v[44:47],a[248:251],v[196:199],v[44:47]
TR a[168:169],v233 offset:50176
TR a[170:171],v233 offset:54272
MM v[44:47],a[252:255],v[212:215],v[44:47]
EX v17,v17
MM v[40:43],a[236:239],v[212:215],v[132:135]
TR a[172:173],v233 offset:58368
CV v10,v12,v13
MM v[40:43],a[232:235],v[196:199],v[40:43]
EX v18,v18
MM v[40:43],a[228:231],v[180:183],v[40:43]
EX v19,v19
MM v[40:43],a[224:227],v[164:167],v[40:43]
TR a[174:175],v233 offset:62464
CV v11,v14,v15
MM v[48:51],a[224:227],v[168:171],v[136:139]
EX v20,v20
MM v[48:51],a[228:231],v[184:187],v[48:51]
TR a[176:177],v232 offset:50688
TR a[178:179],v232 offset:54784
MM v[48:51],a[232:235],v[200:203],v[48:51]
EX v21,v21
MM v[48:51],a[236:239],v[216:219],v[48:51]
TR a[180:181],v232 offset:58880
CV v16,v16,v17
MM v[52:55],a[252:255],v[216:219],v[136:139]
EX v22,v22
MM v[52:55],a[248:251],v[200:203],v[52:55]
TR a[182:183],v232 offset:62976
CV v17,v18,v19
MM v[52:55],a[244:247],v[184:187],v[52:55]
EX v23,v23
MM v[52:55],a[240:243],v[168:171],v[52:55]
TR a[184:185],v233 offset:50688
CV v18,v20,v21
MM v[60:63],a[240:243],v[172:175],v[140:143]
EX v24,v24
MM v[60:63],a[244:247],v[188:191],v[60:63]
TR a[186:187],v233 offset:54784
TR a[188:189],v233 offset:58880
MM v[60:63],a[248:251],v[204:207],v[60:63]
TR a[190:191],v233 offset:62976
CV v19,v22,v23
EX v25,v25
MM v[60:63],a[252:255],v[220:223],v[60:63]
EX v26,v26
MM v[56:59],a[236:239],v[220:223],v[140:143]
EX v27,v27
MM v[56:59],a[232:235],v[204:207],v[56:59]
EX v28,v28
MM v[56:59],a[228:231],v[188:191],v[56:59]
EX v29,v29
MM v[56:59],a[224:227],v[172:175],v[56:59]
s_waitcnt lgkmcnt(0)
s_barrier
MM a[0:3],a[128:131],v[64:67],a[0:3]
s_add_u32 m0, s65, 49152
CV v24,v24,v25
CV v25,v26,v27
MM a[0:3],a[132:135],v[68:71],a[0:3]
EX v30,v30
MM a[8:11],a[132:135],v[76:79],a[8:11]
RD a[192:195],v224 offset:16384
CV v26,v28,v29
MM a[8:11],a[128:131],v[72:75],a[8:11]
EX v31,v31
MM a[16:19],a[128:131],v[80:83],a[116:119]
RD a[196:199],v225 offset:16384
CV v27,v30,v31
MM a[16:19],a[132:135],v[84:87],a[16:19]
EX v32,v32
MM a[24:27],a[132:135],v[92:95],a[124:127]
buffer_load_dwordx4 v228, s[8:11], s64 offen offset:0 lds
MM a[24:27],a[128:131],v[88:91],a[24:27]
EX v33,v33
MM a[28:31],a[136:139],v[88:91],a[124:127]
EX v34,v34
MM a[28:31],a[140:143],v[92:95],a[28:31]
RD a[200:203],v226 offset:16384
CV v4,v32,v33
MM a[20:23],a[140:143],v[84:87],a[116:119]
EX v35,v35
MM a[20:23],a[136:139],v[80:83],a[20:23]
RD a[204:207],v227 offset:16384
CV v5,v34,v35
MM a[12:15],a[136:139],v[72:75],a[12:15]
EX v36,v36
MM a[12:15],a[140:143],v[76:79],a[12:15]
buffer_load_dwordx4 v229, s[8:11], s64 offen offset:1024 lds
MM a[4:7],a[140:143],v[68:71],a[4:7]
EX v37,v37
MM a[4:7],a[136:139],v[64:67],a[4:7]
RD a[208:211],v224 offset:20480
CV v6,v36,v37
MM a[32:35],a[144:147],v[64:67],a[32:35]
EX v38,v38
MM a[32:35],a[148:151],v[68:71],a[32:35]
EX v39,v39
MM a[40:43],a[148:151],v[76:79],a[40:43]
RD a[212:215],v225 offset:20480
CV v7,v38,v39
MM a[40:43],a[144:147],v[72:75],a[40:43]
EX v40,v40
MM a[48:51],a[144:147],v[80:83],a[116:119]
buffer_load_dwordx4 v230, s[8:11], s64 offen offset:2048 lds
MM a[48:51],a[148:151],v[84:87],a[48:51]
EX v41,v41
MM a[56:59],a[148:151],v[92:95],a[124:127]
RD a[216:219],v226 offset:20480
CV v12,v40,v41
MM a[56:59],a[144:147],v[88:91],a[56:59]
EX v42,v42
MM a[60:63],a[152:155],v[88:91],a[124:127]
EX v43,v43
MM a[60:63],a[156:159],v[92:95],a[60:63]
RD a[220:223],v227 offset:20480
CV v13,v42,v43
MM a[52:55],a[156:159],v[84:87],a[116:119]
EX v44,v44
MM a[52:55],a[152:155],v[80:83],a[52:55]
buffer_load_dwordx4 v231, s[8:11], s64 offen offset:3072 lds
MM a[44:47],a[152:155],v[72:75],a[44:47]
s_add_u32 m0, s65, 114688
EX v45,v45
MM a[44:47],a[156:159],v[76:79],a[44:47]
RD a[224:227],v224 offset:24576
CV v14,v44,v45
MM a[36:39],a[156:159],v[68:71],a[36:39]
EX v46,v46
MM a[36:39],a[152:155],v[64:67],a[36:39]
EX v47,v47
MM a[64:67],a[160:163],v[64:67],a[64:67]
RD a[228:231],v225 offset:24576
CV v15,v46,v47
MM a[64:67],a[164:167],v[68:71],a[64:67]
EX v48,v48
MM a[72:75],a[164:167],v[76:79],a[72:75]
buffer_load_dwordx4 v235, s[12:15], s81 offen offset:0 lds
MM a[72:75],a[160:163],v[72:75],a[72:75]
EX v49,v49
MM a[80:83],a[160:163],v[80:83],a[116:119]
RD a[232:235],v226 offset:24576
CV v20,v48,v49
MM a[80:83],a[164:167],v[84:87],a[80:83]
EX v50,v50
MM a[88:91],a[164:167],v[92:95],a[124:127]
EX v51,v51
MM a[88:91],a[160:163],v[88:91],a[88:91]
RD a[236:239],v227 offset:24576
CV v21,v50,v51
MM a[92:95],a[168:171],v[88:91],a[124:127]
EX v52,v52
MM a[92:95],a[172:175],v[92:95],a[92:95]
buffer_load_dwordx4 v235, s[12:15], s64 offen offset:1024 lds
MM a[84:87],a[172:175],v[84:87],a[116:119]
EX v53,v53
MM a[84:87],a[168:171],v[80:83],a[84:87]
RD a[240:243],v224 offset:28672
CV v22,v52,v53
MM a[76:79],a[168:171],v[72:75],a[76:79]
EX v54,v54
MM a[76:79],a[172:175],v[76:79],a[76:79]
EX v55,v55
MM a[68:71],a[172:175],v[68:71],a[68:71]
RD a[244:247],v225 offset:28672
CV v23,v54,v55
MM a[68:71],a[168:171],v[64:67],a[68:71]
buffer_load_dwordx4 v235, s[12:15], s81 offen offset:2048 lds
MM a[96:99],a[176:179],v[64:67],a[96:99]
EX v56,v56
MM a[96:99],a[180:183],v[68:71],a[96:99]
EX v57,v57
MM a[104:107],a[180:183],v[76:79],a[104:107]
RD a[248:251],v226 offset:28672
CV v28,v56,v57
MM a[104:107],a[176:179],v[72:75],a[104:107]
EX v58,v58
MM a[112:115],a[176:179],v[80:83],a[116:119]
EX v59,v59
MM a[112:115],a[180:183],v[84:87],a[112:115]
RD a[252:255],v227 offset:28672
CV v29,v58,v59
MM a[120:123],a[180:183],v[92:95],a[124:127]
buffer_load_dwordx4 v235, s[12:15], s64 offen offset:3072 lds
)ASM"
R"ASM(
MM a[120:123],a[176:179],v[88:91],a[120:123]
EX v60,v60
MM a[124:127],a[184:187],v[88:91],a[124:127]
v_add_f32 v244, v144, v145
v_add_f32 v245, v148, v149
MM a[124:127],a[188:191],v[92:95],a[124:127]
v_add_f32 v246, v152, v153
v_add_f32 v247, v156, v157
MM a[116:119],a[188:191],v[84:87],a[116:119]
EX v61,v61
MM a[116:119],a[184:187],v[80:83],a[116:119]
EX v62,v62
MM a[108:111],a[184:187],v[72:75],a[108:111]
EX v63,v63
MM a[108:111],a[188:191],v[76:79],a[108:111]
CV v30,v60,v61
CV v31,v62,v63
MM a[100:103],a[188:191],v[68:71],a[100:103]
MM a[100:103],a[184:187],v[64:67],a[100:103]
v_smfmac_f32_16x16x64_bf16 v[144:147], v[236:239], v[0:7], v234
v_smfmac_f32_16x16x64_bf16 v[148:151], v[236:239], v[8:15], v234
v_smfmac_f32_16x16x64_bf16 v[152:155], v[236:239], v[16:23], v234
v_smfmac_f32_16x16x64_bf16 v[156:159], v[236:239], v[24:31], v234
s_waitcnt lgkmcnt(0)
MM v[64:67],a[192:195],v[160:163],v[128:131]
s_add_u32 s64, s64, 16384
TR a[128:129],v232 offset:0
s_add_u32 s81, s64, 896
MM v[64:67],a[196:199],v[176:179],v[64:67]
TR a[130:131],v232 offset:4096
v_max3_f32 v249, v144, v145, v148
MM v[64:67],a[200:203],v[192:195],v[64:67]
TR a[132:133],v232 offset:8192
v_max3_f32 v249, v249, v149, v152
MM v[64:67],a[204:207],v[208:211],v[64:67]
TR a[134:135],v232 offset:12288
v_max3_f32 v249, v249, v153, v156
MM v[68:71],a[220:223],v[208:211],v[128:131]
TR a[136:137],v233 offset:0
v_max_f32 v249, v249, v157
MM v[68:71],a[216:219],v[192:195],v[68:71]
TR a[138:139],v233 offset:4096
v_cmp_lt_f32 vcc, s71, v249
MM v[68:71],a[212:215],v[176:179],v[68:71]
TR a[140:141],v233 offset:8192
TR a[142:143],v233 offset:12288
MM v[68:71],a[208:211],v[160:163],v[68:71]
s_cbranch_vccnz LSITE4_%=
LBACK4_%=:
MM v[76:79],a[208:211],v[164:167],v[132:135]
s_cmp_lg_u32 %[sb], 0
s_cbranch_scc1 LEP_%=
v_mov_b32 v250, s69
s_mov_b32 s76, 32768
s_mov_b64 exec, 1
buffer_store_dword v250, off, s[20:23], s76 sc1
s_mov_b64 exec, -1
s_nop 1
LEP_%=:
MM v[76:79],a[212:215],v[180:183],v[76:79]
s_lshl_b32 s76, s50, 1
EX v64,v64
MM v[76:79],a[216:219],v[196:199],v[76:79]
s_bfe_u32 s77, s49, 65538
EX v65,v65
MM v[76:79],a[220:223],v[212:215],v[76:79]
s_add_u32 s76, s76, s77
TR a[144:145],v232 offset:512
CV v64,v64,v65
MM v[72:75],a[204:207],v[212:215],v[132:135]
s_lshl_b32 s76, s76, 2
EX v66,v66
MM v[72:75],a[200:203],v[196:199],v[72:75]
s_add_u32 s76, s76, s84
EX v67,v67
MM v[72:75],a[196:199],v[180:183],v[72:75]
s_lshl_b32 s76, s76, 2
TR a[146:147],v232 offset:4608
CV v65,v66,v67
MM v[72:75],a[192:195],v[164:167],v[72:75]
s_lshr_b32 s77, s49, 3
EX v68,v68
MM v[80:83],a[192:195],v[168:171],v[136:139]
s_add_u32 s76, s76, s77
TR a[148:149],v232 offset:8704
TR a[150:151],v232 offset:12800
MM v[80:83],a[196:199],v[184:187],v[80:83]
s_mul_i32 s95, s76, 33792
EX v69,v69
MM v[80:83],a[200:203],v[200:203],v[80:83]
s_lshl_b32 s76, %[swave], 13
TR a[152:153],v233 offset:512
CV v66,v68,v69
MM v[80:83],a[204:207],v[216:219],v[80:83]
s_add_u32 s95, s95, s76
EX v70,v70
MM v[84:87],a[220:223],v[216:219],v[136:139]
s_add_u32 s95, s95, 22708224
TR a[154:155],v233 offset:4608
v_mov_b32 v240, -1
MM v[84:87],a[216:219],v[200:203],v[84:87]
s_mov_b32 s67, s95
EX v71,v71
MM v[84:87],a[212:215],v[184:187],v[84:87]
s_add_u32 s87, s95, 6144
buffer_store_dword v240, v248, s[20:23], s87 offen offset:12 sc1
MM v[84:87],a[208:211],v[168:171],v[84:87]
TR a[156:157],v233 offset:8704
CV v67,v70,v71
MM v[92:95],a[208:211],v[172:175],v[140:143]
EX v72,v72
MM v[92:95],a[212:215],v[188:191],v[92:95]
EX v73,v73
MM v[92:95],a[216:219],v[204:207],v[92:95]
TR a[158:159],v233 offset:12800
CV v72,v72,v73
MM v[92:95],a[220:223],v[220:223],v[92:95]
EX v74,v74
MM v[88:91],a[204:207],v[220:223],v[140:143]
s_add_u32 s87, s95, 7168
buffer_store_dword v240, v248, s[20:23], s87 offen offset:12 sc1
MM v[88:91],a[200:203],v[204:207],v[88:91]
EX v75,v75
MM v[88:91],a[196:199],v[188:191],v[88:91]
TR a[160:161],v232 offset:1024
v_lshrrev_b32 v34, 1, v248
MM v[88:91],a[192:195],v[172:175],v[88:91]
s_mov_b32 s90, 86016
buffer_load_dwordx2 v[32:33], v34, s[20:23], s90 offen offset:0 sc1
MM v[96:99],a[224:227],v[160:163],v[128:131]
EX v76,v76
MM v[96:99],a[228:231],v[176:179],v[96:99]
TR a[162:163],v232 offset:5120
CV v73,v74,v75
MM v[96:99],a[232:235],v[192:195],v[96:99]
EX v77,v77
MM v[96:99],a[236:239],v[208:211],v[96:99]
TR a[164:165],v232 offset:9216
TR a[166:167],v232 offset:13312
MM v[100:103],a[252:255],v[208:211],v[128:131]
EX v78,v78
MM v[100:103],a[248:251],v[192:195],v[100:103]
EX v79,v79
MM v[100:103],a[244:247],v[176:179],v[100:103]
TR a[168:169],v233 offset:1024
CV v74,v76,v77
MM v[100:103],a[240:243],v[160:163],v[100:103]
EX v80,v80
MM v[108:111],a[240:243],v[164:167],v[132:135]
TR a[170:171],v233 offset:5120
CV v75,v78,v79
MM v[108:111],a[244:247],v[180:183],v[108:111]
EX v81,v81
MM v[108:111],a[248:251],v[196:199],v[108:111]
TR a[172:173],v233 offset:9216
TR a[174:175],v233 offset:13312
MM v[108:111],a[252:255],v[212:215],v[108:111]
EX v82,v82
MM v[104:107],a[236:239],v[212:215],v[132:135]
TR a[176:177],v232 offset:1536
CV v80,v80,v81
MM v[104:107],a[232:235],v[196:199],v[104:107]
EX v83,v83
MM v[104:107],a[228:231],v[180:183],v[104:107]
TR a[178:179],v232 offset:5632
CV v81,v82,v83
MM v[104:107],a[224:227],v[164:167],v[104:107]
EX v84,v84
MM v[112:115],a[224:227],v[168:171],v[136:139]
EX v85,v85
MM v[112:115],a[228:231],v[184:187],v[112:115]
TR a[180:181],v232 offset:9728
TR a[182:183],v232 offset:13824
MM v[112:115],a[232:235],v[200:203],v[112:115]
EX v86,v86
MM v[112:115],a[236:239],v[216:219],v[112:115]
TR a[184:185],v233 offset:1536
CV v82,v84,v85
MM v[116:119],a[252:255],v[216:219],v[136:139]
EX v87,v87
MM v[116:119],a[248:251],v[200:203],v[116:119]
TR a[186:187],v233 offset:5632
CV v83,v86,v87
MM v[116:119],a[244:247],v[184:187],v[116:119]
EX v88,v88
MM v[116:119],a[240:243],v[168:171],v[116:119]
TR a[188:189],v233 offset:9728
TR a[190:191],v233 offset:13824
MM v[124:127],a[240:243],v[172:175],v[140:143]
EX v89,v89
MM v[124:127],a[244:247],v[188:191],v[124:127]
EX v90,v90
MM v[124:127],a[248:251],v[204:207],v[124:127]
EX v91,v91
MM v[124:127],a[252:255],v[220:223],v[124:127]
EX v92,v92
MM v[120:123],a[236:239],v[220:223],v[140:143]
EX v93,v93
MM v[120:123],a[232:235],v[204:207],v[120:123]
EX v94,v94
MM v[120:123],a[228:231],v[188:191],v[120:123]
EX v95,v95
MM v[120:123],a[224:227],v[172:175],v[120:123]
s_waitcnt vmcnt(11) lgkmcnt(0)
s_barrier
MM a[0:3],a[128:131],v[0:3],a[0:3]
s_add_u32 m0, s65, 0
CV v88,v88,v89
CV v89,v90,v91
MM a[0:3],a[132:135],v[4:7],a[0:3]
RD a[192:195],v224 offset:32768
CV v90,v92,v93
MM a[8:11],a[132:135],v[12:15],a[8:11]
RD a[196:199],v225 offset:32768
CV v91,v94,v95
MM a[8:11],a[128:131],v[8:11],a[8:11]
buffer_load_dwordx4 v228, s[8:11], s64 offen offset:0 lds
MM a[16:19],a[128:131],v[16:19],a[16:19]
EX v96,v96
MM a[16:19],a[132:135],v[20:23],a[16:19]
EX v97,v97
MM a[24:27],a[132:135],v[28:31],a[24:27]
RD a[200:203],v226 offset:32768
CV v68,v96,v97
MM a[24:27],a[128:131],v[24:27],a[24:27]
EX v98,v98
MM a[28:31],a[136:139],v[24:27],a[28:31]
buffer_load_dwordx4 v229, s[8:11], s64 offen offset:1024 lds
MM a[28:31],a[140:143],v[28:31],a[28:31]
EX v99,v99
MM a[20:23],a[140:143],v[20:23],a[20:23]
RD a[204:207],v227 offset:32768
CV v69,v98,v99
MM a[20:23],a[136:139],v[16:19],a[20:23]
EX v100,v100
MM a[12:15],a[136:139],v[8:11],a[12:15]
EX v101,v101
MM a[12:15],a[140:143],v[12:15],a[12:15]
RD a[208:211],v224 offset:36864
CV v70,v100,v101
MM a[4:7],a[140:143],v[4:7],a[4:7]
EX v102,v102
MM a[4:7],a[136:139],v[0:3],a[4:7]
buffer_load_dwordx4 v230, s[8:11], s64 offen offset:2048 lds
MM a[32:35],a[144:147],v[0:3],a[32:35]
EX v103,v103
MM a[32:35],a[148:151],v[4:7],a[32:35]
RD a[212:215],v225 offset:36864
CV v71,v102,v103
MM a[40:43],a[148:151],v[12:15],a[40:43]
EX v104,v104
MM a[40:43],a[144:147],v[8:11],a[40:43]
RD a[216:219],v226 offset:36864
RD a[220:223],v227 offset:36864
MM a[48:51],a[144:147],v[16:19],a[48:51]
EX v105,v105
MM a[48:51],a[148:151],v[20:23],a[48:51]
RD a[224:227],v224 offset:40960
CV v76,v104,v105
MM a[56:59],a[148:151],v[28:31],a[56:59]
EX v106,v106
MM a[56:59],a[144:147],v[24:27],a[56:59]
EX v107,v107
MM a[60:63],a[152:155],v[24:27],a[60:63]
RD a[228:231],v225 offset:40960
CV v77,v106,v107
MM a[60:63],a[156:159],v[28:31],a[60:63]
buffer_load_dwordx4 v231, s[8:11], s64 offen offset:3072 lds
MM a[52:55],a[156:159],v[20:23],a[52:55]
s_add_u32 m0, s65, 65536
EX v108,v108
MM a[52:55],a[152:155],v[16:19],a[52:55]
EX v109,v109
MM a[44:47],a[152:155],v[8:11],a[44:47]
RD a[232:235],v226 offset:40960
CV v78,v108,v109
MM a[44:47],a[156:159],v[12:15],a[44:47]
EX v110,v110
MM a[36:39],a[156:159],v[4:7],a[36:39]
buffer_load_dwordx4 v235, s[12:15], s81 offen offset:0 lds
MM a[36:39],a[152:155],v[0:3],a[36:39]
EX v111,v111
MM a[64:67],a[160:163],v[0:3],a[64:67]
s_waitcnt vmcnt(5)
v_cmp_ne_u32 vcc, s69, v33
s_cbranch_vccnz LRRK_%=
LRRKb_%=:
MM a[64:67],a[164:167],v[4:7],a[64:67]
v_add_u32_dpp v32, v32, v32 row_ror:8 row_mask:0xf bank_mask:0xf
CV v79,v110,v111
MM a[72:75],a[164:167],v[12:15],a[72:75]
s_and_b32 s76, %[sb], 7
RD a[236:239],v227 offset:40960
v_mov_b32 v35, v32
MM a[72:75],a[160:163],v[8:11],a[72:75]
s_nop 0
v_permlane16_swap_b32 v32, v35
v_add_u32 v32, v32, v35
MM a[80:83],a[160:163],v[16:19],a[80:83]
RD a[240:243],v224 offset:45056
v_mov_b32 v35, v32
MM a[80:83],a[164:167],v[20:23],a[80:83]
s_nop 0
v_permlane32_swap_b32 v32, v35
v_add_u32 v32, v32, v35
MM a[88:91],a[164:167],v[28:31],a[88:91]
RD a[244:247],v225 offset:45056
v_readlane_b32 s77, v32, s76
MM a[88:91],a[160:163],v[24:27],a[88:91]
s_bfm_b32 s76, s76, 0
EX v112,v112
MM a[92:95],a[168:171],v[24:27],a[92:95]
v_cmp_gt_u32 s[60:61], s77, v32
v_cmp_eq_u32 s[62:63], s77, v32
EX v113,v113
MM a[92:95],a[172:175],v[28:31],a[92:95]
s_and_b32 s62, s62, s76
EX v114,v114
MM a[84:87],a[172:175],v[20:23],a[84:87]
s_or_b32 s60, s60, s62
EX v115,v115
MM a[84:87],a[168:171],v[16:19],a[84:87]
s_and_b32 s60, s60, 255
EX v116,v116
MM a[76:79],a[168:171],v[8:11],a[76:79]
s_bcnt1_i32_b32 s51, s60
s_cmp_lt_u32 s51, 4
s_cselect_b32 s82, 3, 99
MM a[76:79],a[172:175],v[12:15],a[76:79]
s_lshr_b32 s16, %[sb], 3
EX v117,v117
MM a[68:71],a[172:175],v[4:7],a[68:71]
s_lshl_b32 s76, s51, 5
CV v84,v112,v113
CV v85,v114,v115
MM a[68:71],a[168:171],v[0:3],a[68:71]
s_add_u32 s16, s16, s76
EX v118,v118
MM a[96:99],a[176:179],v[0:3],a[96:99]
EX v119,v119
MM a[96:99],a[180:183],v[4:7],a[96:99]
CV v86,v116,v117
CV v87,v118,v119
MM a[104:107],a[180:183],v[12:15],a[104:107]
EX v120,v120
MM a[104:107],a[176:179],v[8:11],a[104:107]
buffer_load_dwordx4 v235, s[12:15], s64 offen offset:1024 lds
MM a[112:115],a[176:179],v[16:19],a[112:115]
EX v121,v121
MM a[112:115],a[180:183],v[20:23],a[112:115]
EX v122,v122
MM a[120:123],a[180:183],v[28:31],a[120:123]
RD a[248:251],v226 offset:45056
CV v92,v120,v121
MM a[120:123],a[176:179],v[24:27],a[120:123]
EX v123,v123
MM a[124:127],a[184:187],v[24:27],a[124:127]
v_add_f32 v244, v144, v145
v_add_f32 v245, v148, v149
MM a[124:127],a[188:191],v[28:31],a[124:127]
v_add_f32 v246, v152, v153
v_add_f32 v247, v156, v157
MM a[116:119],a[188:191],v[20:23],a[116:119]
EX v124,v124
MM a[116:119],a[184:187],v[16:19],a[116:119]
buffer_load_dwordx4 v235, s[12:15], s81 offen offset:2048 lds
MM a[108:111],a[184:187],v[8:11],a[108:111]
EX v125,v125
MM a[108:111],a[188:191],v[12:15],a[108:111]
CV v93,v122,v123
CV v94,v124,v125
MM a[100:103],a[188:191],v[4:7],a[100:103]
EX v126,v126
MM a[100:103],a[184:187],v[0:3],a[100:103]
EX v127,v127
v_smfmac_f32_16x16x64_bf16 v[144:147], v[236:239], v[64:71], v234
RD a[252:255],v227 offset:45056
v_smfmac_f32_16x16x64_bf16 v[148:151], v[236:239], v[72:79], v234
CV v95,v126,v127
v_smfmac_f32_16x16x64_bf16 v[152:155], v[236:239], v[80:87], v234
s_nop 0
v_smfmac_f32_16x16x64_bf16 v[156:159], v[236:239], v[88:95], v234
s_waitcnt lgkmcnt(0)
buffer_load_dwordx4 v235, s[12:15], s64 offen offset:3072 lds
s_cmp_lt_u32 s51, 4
s_cbranch_scc1 LLAR_%=
s_mov_b32 s70, 6
LLAP_%=:
MM v[0:3],a[192:195],v[160:163],v[128:131]
s_add_u32 s64, s64, 16384
TR a[128:129],v232 offset:16384
)ASM"
R"ASM(
s_add_u32 s81, s64, 896
MM v[0:3],a[196:199],v[176:179],v[0:3]
TR a[130:131],v232 offset:20480
v_max3_f32 v249, v144, v145, v148
MM v[0:3],a[200:203],v[192:195],v[0:3]
TR a[132:133],v232 offset:24576
v_max3_f32 v249, v249, v149, v152
MM v[0:3],a[204:207],v[208:211],v[0:3]
TR a[134:135],v232 offset:28672
v_max3_f32 v249, v249, v153, v156
MM v[4:7],a[220:223],v[208:211],v[128:131]
TR a[136:137],v233 offset:16384
v_max_f32 v249, v249, v157
MM v[4:7],a[216:219],v[192:195],v[4:7]
TR a[138:139],v233 offset:20480
v_cmp_lt_f32 vcc, s71, v249
s_nop 0
LPG7_%=:
s_cbranch_execz LPG11_%=
MM v[4:7],a[212:215],v[176:179],v[4:7]
TR a[140:141],v233 offset:24576
TR a[142:143],v233 offset:28672
MM v[4:7],a[208:211],v[160:163],v[4:7]
s_cbranch_vccnz LSITE5_%=
LBACK5_%=:
MM v[12:15],a[208:211],v[164:167],v[132:135]
EX v0,v0
MM v[12:15],a[212:215],v[180:183],v[12:15]
EX v1,v1
MM v[12:15],a[216:219],v[196:199],v[12:15]
TR a[144:145],v232 offset:16896
CV v0,v0,v1
MM v[12:15],a[220:223],v[212:215],v[12:15]
EX v2,v2
MM v[8:11],a[204:207],v[212:215],v[132:135]
EX v3,v3
MM v[8:11],a[200:203],v[196:199],v[8:11]
TR a[146:147],v232 offset:20992
CV v1,v2,v3
MM v[8:11],a[196:199],v[180:183],v[8:11]
EX v4,v4
MM v[8:11],a[192:195],v[164:167],v[8:11]
TR a[148:149],v232 offset:25088
TR a[150:151],v232 offset:29184
MM v[16:19],a[192:195],v[168:171],v[136:139]
EX v5,v5
MM v[16:19],a[196:199],v[184:187],v[16:19]
TR a[152:153],v233 offset:16896
CV v2,v4,v5
MM v[16:19],a[200:203],v[200:203],v[16:19]
EX v6,v6
MM v[16:19],a[204:207],v[216:219],v[16:19]
TR a[154:155],v233 offset:20992
TR a[156:157],v233 offset:25088
MM v[20:23],a[220:223],v[216:219],v[136:139]
s_cmp_lg_u32 s70, s82
s_cbranch_scc1 LWW_%=
v_lshlrev_b32 v250, 3, v248
v_min_u32 v250, 4992, v250
s_sub_u32 s91, 5242880, s26
s_sub_u32 s60, s24, s91
s_subb_u32 s61, s25, 0
s_mov_b32 s62, 5242880
s_mov_b32 s63, 131072
s_lshl_b32 s90, s16, 2
s_add_u32 s90, s90, %[swave]
s_mul_i32 s90, s90, 10240
s_add_u32 s90, s90, 0
s_add_u32 s91, %[slds], 135168
s_mov_b32 m0, s91
s_nop 0
buffer_load_dword v250, s[60:63], s90 offen offset:0 lds
LWW_%=:
MM v[20:23],a[216:219],v[200:203],v[20:23]
EX v7,v7
MM v[20:23],a[212:215],v[184:187],v[20:23]
TR a[158:159],v233 offset:29184
CV v3,v6,v7
MM v[20:23],a[208:211],v[168:171],v[20:23]
EX v8,v8
MM v[28:31],a[208:211],v[172:175],v[140:143]
EX v9,v9
MM v[28:31],a[212:215],v[188:191],v[28:31]
TR a[160:161],v232 offset:17408
CV v8,v8,v9
MM v[28:31],a[216:219],v[204:207],v[28:31]
EX v10,v10
MM v[28:31],a[220:223],v[220:223],v[28:31]
EX v11,v11
MM v[24:27],a[204:207],v[220:223],v[140:143]
TR a[162:163],v232 offset:21504
CV v9,v10,v11
MM v[24:27],a[200:203],v[204:207],v[24:27]
TR a[164:165],v232 offset:25600
TR a[166:167],v232 offset:29696
MM v[24:27],a[196:199],v[188:191],v[24:27]
EX v12,v12
MM v[24:27],a[192:195],v[172:175],v[24:27]
EX v13,v13
MM v[32:35],a[224:227],v[160:163],v[128:131]
TR a[168:169],v233 offset:17408
CV v10,v12,v13
MM v[32:35],a[228:231],v[176:179],v[32:35]
EX v14,v14
MM v[32:35],a[232:235],v[192:195],v[32:35]
TR a[170:171],v233 offset:21504
TR a[172:173],v233 offset:25600
MM v[32:35],a[236:239],v[208:211],v[32:35]
EX v15,v15
MM v[36:39],a[252:255],v[208:211],v[128:131]
TR a[174:175],v233 offset:29696
CV v11,v14,v15
MM v[36:39],a[248:251],v[192:195],v[36:39]
EX v16,v16
MM v[36:39],a[244:247],v[176:179],v[36:39]
EX v17,v17
MM v[36:39],a[240:243],v[160:163],v[36:39]
TR a[176:177],v232 offset:17920
CV v16,v16,v17
MM v[44:47],a[240:243],v[164:167],v[132:135]
EX v18,v18
MM v[44:47],a[244:247],v[180:183],v[44:47]
TR a[178:179],v232 offset:22016
TR a[180:181],v232 offset:26112
MM v[44:47],a[248:251],v[196:199],v[44:47]
EX v19,v19
MM v[44:47],a[252:255],v[212:215],v[44:47]
TR a[182:183],v232 offset:30208
CV v17,v18,v19
MM v[40:43],a[236:239],v[212:215],v[132:135]
EX v20,v20
MM v[40:43],a[232:235],v[196:199],v[40:43]
EX v21,v21
MM v[40:43],a[228:231],v[180:183],v[40:43]
TR a[184:185],v233 offset:17920
CV v18,v20,v21
MM v[40:43],a[224:227],v[164:167],v[40:43]
EX v22,v22
MM v[48:51],a[224:227],v[168:171],v[136:139]
TR a[186:187],v233 offset:22016
TR a[188:189],v233 offset:26112
MM v[48:51],a[228:231],v[184:187],v[48:51]
EX v23,v23
MM v[48:51],a[232:235],v[200:203],v[48:51]
TR a[190:191],v233 offset:30208
CV v19,v22,v23
MM v[48:51],a[236:239],v[216:219],v[48:51]
EX v24,v24
MM v[52:55],a[252:255],v[216:219],v[136:139]
EX v25,v25
MM v[52:55],a[248:251],v[200:203],v[52:55]
EX v26,v26
MM v[52:55],a[244:247],v[184:187],v[52:55]
EX v27,v27
MM v[52:55],a[240:243],v[168:171],v[52:55]
EX v28,v28
MM v[60:63],a[240:243],v[172:175],v[140:143]
EX v29,v29
MM v[60:63],a[244:247],v[188:191],v[60:63]
EX v30,v30
MM v[60:63],a[248:251],v[204:207],v[60:63]
EX v31,v31
MM v[60:63],a[252:255],v[220:223],v[60:63]
EX v32,v32
MM v[56:59],a[236:239],v[220:223],v[140:143]
EX v33,v33
MM v[56:59],a[232:235],v[204:207],v[56:59]
EX v34,v34
MM v[56:59],a[228:231],v[188:191],v[56:59]
EX v35,v35
MM v[56:59],a[224:227],v[172:175],v[56:59]
s_waitcnt vmcnt(12) lgkmcnt(0)
s_barrier
MM a[0:3],a[128:131],v[64:67],a[0:3]
s_add_u32 m0, s65, 16384
CV v24,v24,v25
CV v25,v26,v27
MM a[0:3],a[132:135],v[68:71],a[0:3]
RD a[192:195],v224 offset:49152
CV v26,v28,v29
MM a[8:11],a[132:135],v[76:79],a[8:11]
RD a[196:199],v225 offset:49152
CV v27,v30,v31
MM a[8:11],a[128:131],v[72:75],a[8:11]
buffer_load_dwordx4 v228, s[8:11], s64 offen offset:0 lds
MM a[16:19],a[128:131],v[80:83],a[16:19]
RD a[200:203],v226 offset:49152
CV v4,v32,v33
MM a[16:19],a[132:135],v[84:87],a[16:19]
RD a[204:207],v227 offset:49152
CV v5,v34,v35
MM a[24:27],a[132:135],v[92:95],a[24:27]
buffer_load_dwordx4 v229, s[8:11], s64 offen offset:1024 lds
MM a[24:27],a[128:131],v[88:91],a[24:27]
RD a[208:211],v224 offset:53248
RD a[212:215],v225 offset:53248
MM a[28:31],a[136:139],v[88:91],a[28:31]
buffer_load_dwordx4 v230, s[8:11], s64 offen offset:2048 lds
MM a[28:31],a[140:143],v[92:95],a[28:31]
EX v36,v36
MM a[20:23],a[140:143],v[84:87],a[20:23]
EX v37,v37
MM a[20:23],a[136:139],v[80:83],a[20:23]
RD a[216:219],v226 offset:53248
CV v6,v36,v37
MM a[12:15],a[136:139],v[72:75],a[12:15]
EX v38,v38
MM a[12:15],a[140:143],v[76:79],a[12:15]
RD a[220:223],v227 offset:53248
RD a[224:227],v224 offset:57344
MM a[4:7],a[140:143],v[68:71],a[4:7]
EX v39,v39
MM a[4:7],a[136:139],v[64:67],a[4:7]
RD a[228:231],v225 offset:57344
CV v7,v38,v39
MM a[32:35],a[144:147],v[64:67],a[32:35]
EX v40,v40
MM a[32:35],a[148:151],v[68:71],a[32:35]
buffer_load_dwordx4 v231, s[8:11], s64 offen offset:3072 lds
MM a[40:43],a[148:151],v[76:79],a[40:43]
s_add_u32 m0, s65, 81920
EX v41,v41
MM a[40:43],a[144:147],v[72:75],a[40:43]
RD a[232:235],v226 offset:57344
CV v12,v40,v41
MM a[48:51],a[144:147],v[80:83],a[48:51]
EX v42,v42
MM a[48:51],a[148:151],v[84:87],a[48:51]
EX v43,v43
MM a[56:59],a[148:151],v[92:95],a[56:59]
RD a[236:239],v227 offset:57344
CV v13,v42,v43
MM a[56:59],a[144:147],v[88:91],a[56:59]
EX v44,v44
MM a[60:63],a[152:155],v[88:91],a[60:63]
buffer_load_dwordx4 v235, s[12:15], s81 offen offset:0 lds
MM a[60:63],a[156:159],v[92:95],a[60:63]
EX v45,v45
MM a[52:55],a[156:159],v[84:87],a[52:55]
RD a[240:243],v224 offset:61440
CV v14,v44,v45
MM a[52:55],a[152:155],v[80:83],a[52:55]
EX v46,v46
MM a[44:47],a[152:155],v[72:75],a[44:47]
EX v47,v47
MM a[44:47],a[156:159],v[76:79],a[44:47]
RD a[244:247],v225 offset:61440
CV v15,v46,v47
MM a[36:39],a[156:159],v[68:71],a[36:39]
buffer_load_dwordx4 v235, s[12:15], s64 offen offset:1024 lds
MM a[36:39],a[152:155],v[64:67],a[36:39]
EX v48,v48
MM a[64:67],a[160:163],v[64:67],a[64:67]
EX v49,v49
MM a[64:67],a[164:167],v[68:71],a[64:67]
RD a[248:251],v226 offset:61440
CV v20,v48,v49
MM a[72:75],a[164:167],v[76:79],a[72:75]
EX v50,v50
MM a[72:75],a[160:163],v[72:75],a[72:75]
EX v51,v51
MM a[80:83],a[160:163],v[80:83],a[80:83]
RD a[252:255],v227 offset:61440
CV v21,v50,v51
MM a[80:83],a[164:167],v[84:87],a[80:83]
buffer_load_dwordx4 v235, s[12:15], s81 offen offset:2048 lds
MM a[88:91],a[164:167],v[92:95],a[88:91]
EX v52,v52
MM a[88:91],a[160:163],v[88:91],a[88:91]
EX v53,v53
MM a[92:95],a[168:171],v[88:91],a[92:95]
EX v54,v54
MM a[92:95],a[172:175],v[92:95],a[92:95]
buffer_load_dwordx4 v235, s[12:15], s64 offen offset:3072 lds
MM a[84:87],a[172:175],v[84:87],a[84:87]
EX v55,v55
MM a[84:87],a[168:171],v[80:83],a[84:87]
EX v56,v56
MM a[76:79],a[168:171],v[72:75],a[76:79]
EX v57,v57
MM a[76:79],a[172:175],v[76:79],a[76:79]
EX v58,v58
MM a[68:71],a[172:175],v[68:71],a[68:71]
EX v59,v59
MM a[68:71],a[168:171],v[64:67],a[68:71]
EX v60,v60
MM a[96:99],a[176:179],v[64:67],a[96:99]
EX v61,v61
MM a[96:99],a[180:183],v[68:71],a[96:99]
EX v62,v62
MM a[104:107],a[180:183],v[76:79],a[104:107]
EX v63,v63
MM a[104:107],a[176:179],v[72:75],a[104:107]
CV v22,v52,v53
CV v23,v54,v55
MM a[112:115],a[176:179],v[80:83],a[112:115]
CV v28,v56,v57
CV v29,v58,v59
MM a[112:115],a[180:183],v[84:87],a[112:115]
CV v30,v60,v61
CV v31,v62,v63
MM a[120:123],a[180:183],v[92:95],a[120:123]
MM a[120:123],a[176:179],v[88:91],a[120:123]
MM a[124:127],a[184:187],v[88:91],a[124:127]
v_add_f32 v244, v144, v145
v_add_f32 v245, v148, v149
MM a[124:127],a[188:191],v[92:95],a[124:127]
v_add_f32 v246, v152, v153
v_add_f32 v247, v156, v157
MM a[116:119],a[188:191],v[84:87],a[116:119]
MM a[116:119],a[184:187],v[80:83],a[116:119]
MM a[108:111],a[184:187],v[72:75],a[108:111]
MM a[108:111],a[188:191],v[76:79],a[108:111]
MM a[100:103],a[188:191],v[68:71],a[100:103]
MM a[100:103],a[184:187],v[64:67],a[100:103]
v_smfmac_f32_16x16x64_bf16 v[144:147], v[236:239], v[0:7], v234
v_smfmac_f32_16x16x64_bf16 v[148:151], v[236:239], v[8:15], v234
v_smfmac_f32_16x16x64_bf16 v[152:155], v[236:239], v[16:23], v234
v_smfmac_f32_16x16x64_bf16 v[156:159], v[236:239], v[24:31], v234
s_waitcnt lgkmcnt(0)
MM v[64:67],a[192:195],v[160:163],v[128:131]
s_add_u32 s64, s64, 16384
TR a[128:129],v232 offset:32768
s_add_u32 s81, s64, 896
MM v[64:67],a[196:199],v[176:179],v[64:67]
TR a[130:131],v232 offset:36864
v_max3_f32 v249, v144, v145, v148
MM v[64:67],a[200:203],v[192:195],v[64:67]
TR a[132:133],v232 offset:40960
v_max3_f32 v249, v249, v149, v152
MM v[64:67],a[204:207],v[208:211],v[64:67]
TR a[134:135],v232 offset:45056
v_max3_f32 v249, v249, v153, v156
MM v[68:71],a[220:223],v[208:211],v[128:131]
TR a[136:137],v233 offset:32768
v_max_f32 v249, v249, v157
MM v[68:71],a[216:219],v[192:195],v[68:71]
TR a[138:139],v233 offset:36864
v_cmp_lt_f32 vcc, s71, v249
MM v[68:71],a[212:215],v[176:179],v[68:71]
TR a[140:141],v233 offset:40960
TR a[142:143],v233 offset:45056
MM v[68:71],a[208:211],v[160:163],v[68:71]
s_cbranch_vccnz LSITE6_%=
LBACK6_%=:
MM v[76:79],a[208:211],v[164:167],v[132:135]
EX v64,v64
MM v[76:79],a[212:215],v[180:183],v[76:79]
EX v65,v65
MM v[76:79],a[216:219],v[196:199],v[76:79]
TR a[144:145],v232 offset:33280
CV v64,v64,v65
MM v[76:79],a[220:223],v[212:215],v[76:79]
EX v66,v66
MM v[72:75],a[204:207],v[212:215],v[132:135]
EX v67,v67
MM v[72:75],a[200:203],v[196:199],v[72:75]
TR a[146:147],v232 offset:37376
CV v65,v66,v67
MM v[72:75],a[196:199],v[180:183],v[72:75]
EX v68,v68
MM v[72:75],a[192:195],v[164:167],v[72:75]
TR a[148:149],v232 offset:41472
TR a[150:151],v232 offset:45568
MM v[80:83],a[192:195],v[168:171],v[136:139]
EX v69,v69
MM v[80:83],a[196:199],v[184:187],v[80:83]
TR a[152:153],v233 offset:33280
CV v66,v68,v69
MM v[80:83],a[200:203],v[200:203],v[80:83]
EX v70,v70
MM v[80:83],a[204:207],v[216:219],v[80:83]
TR a[154:155],v233 offset:37376
TR a[156:157],v233 offset:41472
MM v[84:87],a[220:223],v[216:219],v[136:139]
EX v71,v71
MM v[84:87],a[216:219],v[200:203],v[84:87]
TR a[158:159],v233 offset:45568
CV v67,v70,v71
MM v[84:87],a[212:215],v[184:187],v[84:87]
EX v72,v72
MM v[84:87],a[208:211],v[168:171],v[84:87]
EX v73,v73
MM v[92:95],a[208:211],v[172:175],v[140:143]
TR a[160:161],v232 offset:33792
CV v72,v72,v73
MM v[92:95],a[212:215],v[188:191],v[92:95]
EX v74,v74
MM v[92:95],a[216:219],v[204:207],v[92:95]
TR a[162:163],v232 offset:37888
TR a[164:165],v232 offset:41984
MM v[92:95],a[220:223],v[220:223],v[92:95]
EX v75,v75
MM v[88:91],a[204:207],v[220:223],v[140:143]
)ASM"
R"ASM(
TR a[166:167],v232 offset:46080
CV v73,v74,v75
MM v[88:91],a[200:203],v[204:207],v[88:91]
EX v76,v76
MM v[88:91],a[196:199],v[188:191],v[88:91]
EX v77,v77
MM v[88:91],a[192:195],v[172:175],v[88:91]
TR a[168:169],v233 offset:33792
CV v74,v76,v77
MM v[96:99],a[224:227],v[160:163],v[128:131]
EX v78,v78
MM v[96:99],a[228:231],v[176:179],v[96:99]
TR a[170:171],v233 offset:37888
TR a[172:173],v233 offset:41984
MM v[96:99],a[232:235],v[192:195],v[96:99]
EX v79,v79
MM v[96:99],a[236:239],v[208:211],v[96:99]
TR a[174:175],v233 offset:46080
CV v75,v78,v79
MM v[100:103],a[252:255],v[208:211],v[128:131]
EX v80,v80
MM v[100:103],a[248:251],v[192:195],v[100:103]
EX v81,v81
MM v[100:103],a[244:247],v[176:179],v[100:103]
TR a[176:177],v232 offset:34304
CV v80,v80,v81
MM v[100:103],a[240:243],v[160:163],v[100:103]
EX v82,v82
MM v[108:111],a[240:243],v[164:167],v[132:135]
buffer_store_dword v240, v248, s[20:23], s67 offen offset:12 sc1
MM v[108:111],a[244:247],v[180:183],v[108:111]
s_add_u32 s67, s67, 1024
TR a[178:179],v232 offset:38400
TR a[180:181],v232 offset:42496
MM v[108:111],a[248:251],v[196:199],v[108:111]
EX v83,v83
MM v[108:111],a[252:255],v[212:215],v[108:111]
TR a[182:183],v232 offset:46592
CV v81,v82,v83
MM v[104:107],a[236:239],v[212:215],v[132:135]
EX v84,v84
MM v[104:107],a[232:235],v[196:199],v[104:107]
EX v85,v85
MM v[104:107],a[228:231],v[180:183],v[104:107]
TR a[184:185],v233 offset:34304
CV v82,v84,v85
MM v[104:107],a[224:227],v[164:167],v[104:107]
EX v86,v86
MM v[112:115],a[224:227],v[168:171],v[136:139]
TR a[186:187],v233 offset:38400
TR a[188:189],v233 offset:42496
MM v[112:115],a[228:231],v[184:187],v[112:115]
EX v87,v87
MM v[112:115],a[232:235],v[200:203],v[112:115]
TR a[190:191],v233 offset:46592
CV v83,v86,v87
MM v[112:115],a[236:239],v[216:219],v[112:115]
EX v88,v88
MM v[116:119],a[252:255],v[216:219],v[136:139]
EX v89,v89
MM v[116:119],a[248:251],v[200:203],v[116:119]
EX v90,v90
MM v[116:119],a[244:247],v[184:187],v[116:119]
EX v91,v91
MM v[116:119],a[240:243],v[168:171],v[116:119]
EX v92,v92
MM v[124:127],a[240:243],v[172:175],v[140:143]
EX v93,v93
MM v[124:127],a[244:247],v[188:191],v[124:127]
EX v94,v94
MM v[124:127],a[248:251],v[204:207],v[124:127]
EX v95,v95
MM v[124:127],a[252:255],v[220:223],v[124:127]
EX v96,v96
MM v[120:123],a[236:239],v[220:223],v[140:143]
EX v97,v97
MM v[120:123],a[232:235],v[204:207],v[120:123]
EX v98,v98
MM v[120:123],a[228:231],v[188:191],v[120:123]
EX v99,v99
MM v[120:123],a[224:227],v[172:175],v[120:123]
s_waitcnt vmcnt(12) lgkmcnt(0)
s_barrier
MM a[0:3],a[128:131],v[0:3],a[0:3]
s_add_u32 m0, s65, 32768
CV v88,v88,v89
CV v89,v90,v91
MM a[0:3],a[132:135],v[4:7],a[0:3]
RD a[192:195],v224 offset:0
CV v90,v92,v93
MM a[8:11],a[132:135],v[12:15],a[8:11]
RD a[196:199],v225 offset:0
CV v91,v94,v95
MM a[8:11],a[128:131],v[8:11],a[8:11]
buffer_load_dwordx4 v228, s[8:11], s64 offen offset:0 lds
MM a[16:19],a[128:131],v[16:19],a[16:19]
RD a[200:203],v226 offset:0
CV v68,v96,v97
MM a[16:19],a[132:135],v[20:23],a[16:19]
RD a[204:207],v227 offset:0
CV v69,v98,v99
MM a[24:27],a[132:135],v[28:31],a[24:27]
buffer_load_dwordx4 v229, s[8:11], s64 offen offset:1024 lds
MM a[24:27],a[128:131],v[24:27],a[24:27]
RD a[208:211],v224 offset:4096
RD a[212:215],v225 offset:4096
MM a[28:31],a[136:139],v[24:27],a[28:31]
buffer_load_dwordx4 v230, s[8:11], s64 offen offset:2048 lds
MM a[28:31],a[140:143],v[28:31],a[28:31]
EX v100,v100
MM a[20:23],a[140:143],v[20:23],a[20:23]
EX v101,v101
MM a[20:23],a[136:139],v[16:19],a[20:23]
RD a[216:219],v226 offset:4096
CV v70,v100,v101
MM a[12:15],a[136:139],v[8:11],a[12:15]
EX v102,v102
MM a[12:15],a[140:143],v[12:15],a[12:15]
RD a[220:223],v227 offset:4096
RD a[224:227],v224 offset:8192
MM a[4:7],a[140:143],v[4:7],a[4:7]
EX v103,v103
MM a[4:7],a[136:139],v[0:3],a[4:7]
RD a[228:231],v225 offset:8192
CV v71,v102,v103
MM a[32:35],a[144:147],v[0:3],a[32:35]
EX v104,v104
MM a[32:35],a[148:151],v[4:7],a[32:35]
buffer_load_dwordx4 v231, s[8:11], s64 offen offset:3072 lds
MM a[40:43],a[148:151],v[12:15],a[40:43]
s_add_u32 m0, s65, 98304
EX v105,v105
MM a[40:43],a[144:147],v[8:11],a[40:43]
RD a[232:235],v226 offset:8192
CV v76,v104,v105
MM a[48:51],a[144:147],v[16:19],a[48:51]
EX v106,v106
MM a[48:51],a[148:151],v[20:23],a[48:51]
EX v107,v107
MM a[56:59],a[148:151],v[28:31],a[56:59]
RD a[236:239],v227 offset:8192
CV v77,v106,v107
MM a[56:59],a[144:147],v[24:27],a[56:59]
EX v108,v108
MM a[60:63],a[152:155],v[24:27],a[60:63]
buffer_load_dwordx4 v235, s[12:15], s81 offen offset:0 lds
MM a[60:63],a[156:159],v[28:31],a[60:63]
EX v109,v109
MM a[52:55],a[156:159],v[20:23],a[52:55]
RD a[240:243],v224 offset:12288
CV v78,v108,v109
MM a[52:55],a[152:155],v[16:19],a[52:55]
EX v110,v110
MM a[44:47],a[152:155],v[8:11],a[44:47]
EX v111,v111
MM a[44:47],a[156:159],v[12:15],a[44:47]
RD a[244:247],v225 offset:12288
CV v79,v110,v111
MM a[36:39],a[156:159],v[4:7],a[36:39]
buffer_load_dwordx4 v235, s[12:15], s64 offen offset:1024 lds
MM a[36:39],a[152:155],v[0:3],a[36:39]
EX v112,v112
MM a[64:67],a[160:163],v[0:3],a[64:67]
EX v113,v113
MM a[64:67],a[164:167],v[4:7],a[64:67]
RD a[248:251],v226 offset:12288
CV v84,v112,v113
MM a[72:75],a[164:167],v[12:15],a[72:75]
EX v114,v114
MM a[72:75],a[160:163],v[8:11],a[72:75]
EX v115,v115
MM a[80:83],a[160:163],v[16:19],a[80:83]
RD a[252:255],v227 offset:12288
CV v85,v114,v115
MM a[80:83],a[164:167],v[20:23],a[80:83]
buffer_load_dwordx4 v235, s[12:15], s81 offen offset:2048 lds
MM a[88:91],a[164:167],v[28:31],a[88:91]
EX v116,v116
MM a[88:91],a[160:163],v[24:27],a[88:91]
EX v117,v117
MM a[92:95],a[168:171],v[24:27],a[92:95]
EX v118,v118
MM a[92:95],a[172:175],v[28:31],a[92:95]
buffer_load_dwordx4 v235, s[12:15], s64 offen offset:3072 lds
MM a[84:87],a[172:175],v[20:23],a[84:87]
EX v119,v119
MM a[84:87],a[168:171],v[16:19],a[84:87]
EX v120,v120
MM a[76:79],a[168:171],v[8:11],a[76:79]
EX v121,v121
MM a[76:79],a[172:175],v[12:15],a[76:79]
EX v122,v122
MM a[68:71],a[172:175],v[4:7],a[68:71]
EX v123,v123
MM a[68:71],a[168:171],v[0:3],a[68:71]
EX v124,v124
MM a[96:99],a[176:179],v[0:3],a[96:99]
EX v125,v125
MM a[96:99],a[180:183],v[4:7],a[96:99]
EX v126,v126
MM a[104:107],a[180:183],v[12:15],a[104:107]
EX v127,v127
MM a[104:107],a[176:179],v[8:11],a[104:107]
CV v86,v116,v117
CV v87,v118,v119
MM a[112:115],a[176:179],v[16:19],a[112:115]
CV v92,v120,v121
CV v93,v122,v123
MM a[112:115],a[180:183],v[20:23],a[112:115]
CV v94,v124,v125
CV v95,v126,v127
MM a[120:123],a[180:183],v[28:31],a[120:123]
MM a[120:123],a[176:179],v[24:27],a[120:123]
MM a[124:127],a[184:187],v[24:27],a[124:127]
v_add_f32 v244, v144, v145
v_add_f32 v245, v148, v149
LPG8_%=:
s_cbranch_execz LPG12_%=
MM a[124:127],a[188:191],v[28:31],a[124:127]
v_add_f32 v246, v152, v153
v_add_f32 v247, v156, v157
MM a[116:119],a[188:191],v[20:23],a[116:119]
MM a[116:119],a[184:187],v[16:19],a[116:119]
MM a[108:111],a[184:187],v[8:11],a[108:111]
MM a[108:111],a[188:191],v[12:15],a[108:111]
MM a[100:103],a[188:191],v[4:7],a[100:103]
MM a[100:103],a[184:187],v[0:3],a[100:103]
v_smfmac_f32_16x16x64_bf16 v[144:147], v[236:239], v[64:71], v234
v_smfmac_f32_16x16x64_bf16 v[148:151], v[236:239], v[72:79], v234
v_smfmac_f32_16x16x64_bf16 v[152:155], v[236:239], v[80:87], v234
v_smfmac_f32_16x16x64_bf16 v[156:159], v[236:239], v[88:95], v234
s_waitcnt lgkmcnt(0)
MM v[0:3],a[192:195],v[160:163],v[128:131]
s_add_u32 s64, s64, 16384
TR a[128:129],v232 offset:49152
s_add_u32 s81, s64, 896
MM v[0:3],a[196:199],v[176:179],v[0:3]
TR a[130:131],v232 offset:53248
v_max3_f32 v249, v144, v145, v148
MM v[0:3],a[200:203],v[192:195],v[0:3]
TR a[132:133],v232 offset:57344
v_max3_f32 v249, v249, v149, v152
MM v[0:3],a[204:207],v[208:211],v[0:3]
TR a[134:135],v232 offset:61440
v_max3_f32 v249, v249, v153, v156
MM v[4:7],a[220:223],v[208:211],v[128:131]
TR a[136:137],v233 offset:49152
v_max_f32 v249, v249, v157
MM v[4:7],a[216:219],v[192:195],v[4:7]
TR a[138:139],v233 offset:53248
v_cmp_lt_f32 vcc, s71, v249
MM v[4:7],a[212:215],v[176:179],v[4:7]
TR a[140:141],v233 offset:57344
TR a[142:143],v233 offset:61440
MM v[4:7],a[208:211],v[160:163],v[4:7]
s_cbranch_vccnz LSITE7_%=
LBACK7_%=:
MM v[12:15],a[208:211],v[164:167],v[132:135]
EX v0,v0
MM v[12:15],a[212:215],v[180:183],v[12:15]
EX v1,v1
MM v[12:15],a[216:219],v[196:199],v[12:15]
TR a[144:145],v232 offset:49664
CV v0,v0,v1
MM v[12:15],a[220:223],v[212:215],v[12:15]
EX v2,v2
MM v[8:11],a[204:207],v[212:215],v[132:135]
EX v3,v3
MM v[8:11],a[200:203],v[196:199],v[8:11]
TR a[146:147],v232 offset:53760
CV v1,v2,v3
MM v[8:11],a[196:199],v[180:183],v[8:11]
EX v4,v4
MM v[8:11],a[192:195],v[164:167],v[8:11]
TR a[148:149],v232 offset:57856
TR a[150:151],v232 offset:61952
MM v[16:19],a[192:195],v[168:171],v[136:139]
EX v5,v5
MM v[16:19],a[196:199],v[184:187],v[16:19]
TR a[152:153],v233 offset:49664
CV v2,v4,v5
MM v[16:19],a[200:203],v[200:203],v[16:19]
EX v6,v6
MM v[16:19],a[204:207],v[216:219],v[16:19]
TR a[154:155],v233 offset:53760
TR a[156:157],v233 offset:57856
MM v[20:23],a[220:223],v[216:219],v[136:139]
s_cmp_lg_u32 s70, 2
s_cbranch_scc1 LIP_%=
s_lshr_b32 s77, %[sb], 3
s_lshl_b32 s77, s77, 2
s_add_u32 s77, s77, %[swave]
s_lshl_b32 s77, s77, 9
s_add_u32 s77, s77, LLAJ_%=-LPDP_%=
v_lshrrev_b32 v253, 1, v248
v_add_u32 v253, s77, v253
s_mov_b32 s77, LKEND_ka-LPDP_%=-16
v_min_u32 v253, s77, v253
s_getpc_b64 s[98:99]
LPDP_%=:
global_load_dword v253, v253, s[98:99]
LIP_%=:
MM v[20:23],a[216:219],v[200:203],v[20:23]
EX v7,v7
MM v[20:23],a[212:215],v[184:187],v[20:23]
TR a[158:159],v233 offset:61952
CV v3,v6,v7
MM v[20:23],a[208:211],v[168:171],v[20:23]
EX v8,v8
MM v[28:31],a[208:211],v[172:175],v[140:143]
EX v9,v9
MM v[28:31],a[212:215],v[188:191],v[28:31]
TR a[160:161],v232 offset:50176
CV v8,v8,v9
MM v[28:31],a[216:219],v[204:207],v[28:31]
EX v10,v10
MM v[28:31],a[220:223],v[220:223],v[28:31]
EX v11,v11
MM v[24:27],a[204:207],v[220:223],v[140:143]
TR a[162:163],v232 offset:54272
CV v9,v10,v11
MM v[24:27],a[200:203],v[204:207],v[24:27]
TR a[164:165],v232 offset:58368
TR a[166:167],v232 offset:62464
MM v[24:27],a[196:199],v[188:191],v[24:27]
EX v12,v12
MM v[24:27],a[192:195],v[172:175],v[24:27]
EX v13,v13
MM v[32:35],a[224:227],v[160:163],v[128:131]
TR a[168:169],v233 offset:50176
CV v10,v12,v13
MM v[32:35],a[228:231],v[176:179],v[32:35]
EX v14,v14
MM v[32:35],a[232:235],v[192:195],v[32:35]
TR a[170:171],v233 offset:54272
TR a[172:173],v233 offset:58368
MM v[32:35],a[236:239],v[208:211],v[32:35]
EX v15,v15
MM v[36:39],a[252:255],v[208:211],v[128:131]
TR a[174:175],v233 offset:62464
CV v11,v14,v15
MM v[36:39],a[248:251],v[192:195],v[36:39]
EX v16,v16
MM v[36:39],a[244:247],v[176:179],v[36:39]
EX v17,v17
MM v[36:39],a[240:243],v[160:163],v[36:39]
TR a[176:177],v232 offset:50688
CV v16,v16,v17
MM v[44:47],a[240:243],v[164:167],v[132:135]
EX v18,v18
MM v[44:47],a[244:247],v[180:183],v[44:47]
TR a[178:179],v232 offset:54784
TR a[180:181],v232 offset:58880
MM v[44:47],a[248:251],v[196:199],v[44:47]
EX v19,v19
MM v[44:47],a[252:255],v[212:215],v[44:47]
TR a[182:183],v232 offset:62976
CV v17,v18,v19
MM v[40:43],a[236:239],v[212:215],v[132:135]
EX v20,v20
MM v[40:43],a[232:235],v[196:199],v[40:43]
EX v21,v21
MM v[40:43],a[228:231],v[180:183],v[40:43]
TR a[184:185],v233 offset:50688
CV v18,v20,v21
MM v[40:43],a[224:227],v[164:167],v[40:43]
EX v22,v22
MM v[48:51],a[224:227],v[168:171],v[136:139]
TR a[186:187],v233 offset:54784
TR a[188:189],v233 offset:58880
MM v[48:51],a[228:231],v[184:187],v[48:51]
EX v23,v23
MM v[48:51],a[232:235],v[200:203],v[48:51]
TR a[190:191],v233 offset:62976
CV v19,v22,v23
MM v[48:51],a[236:239],v[216:219],v[48:51]
EX v24,v24
MM v[52:55],a[252:255],v[216:219],v[136:139]
EX v25,v25
MM v[52:55],a[248:251],v[200:203],v[52:55]
EX v26,v26
MM v[52:55],a[244:247],v[184:187],v[52:55]
EX v27,v27
MM v[52:55],a[240:243],v[168:171],v[52:55]
EX v28,v28
MM v[60:63],a[240:243],v[172:175],v[140:143]
EX v29,v29
MM v[60:63],a[244:247],v[188:191],v[60:63]
EX v30,v30
MM v[60:63],a[248:251],v[204:207],v[60:63]
EX v31,v31
MM v[60:63],a[252:255],v[220:223],v[60:63]
)ASM"
R"ASM(
EX v32,v32
MM v[56:59],a[236:239],v[220:223],v[140:143]
EX v33,v33
MM v[56:59],a[232:235],v[204:207],v[56:59]
EX v34,v34
MM v[56:59],a[228:231],v[188:191],v[56:59]
EX v35,v35
MM v[56:59],a[224:227],v[172:175],v[56:59]
s_waitcnt vmcnt(12) lgkmcnt(0)
s_barrier
MM a[0:3],a[128:131],v[64:67],a[0:3]
s_add_u32 m0, s65, 49152
CV v24,v24,v25
CV v25,v26,v27
MM a[0:3],a[132:135],v[68:71],a[0:3]
RD a[192:195],v224 offset:16384
CV v26,v28,v29
MM a[8:11],a[132:135],v[76:79],a[8:11]
RD a[196:199],v225 offset:16384
CV v27,v30,v31
MM a[8:11],a[128:131],v[72:75],a[8:11]
buffer_load_dwordx4 v228, s[8:11], s64 offen offset:0 lds
MM a[16:19],a[128:131],v[80:83],a[16:19]
RD a[200:203],v226 offset:16384
CV v4,v32,v33
MM a[16:19],a[132:135],v[84:87],a[16:19]
RD a[204:207],v227 offset:16384
CV v5,v34,v35
MM a[24:27],a[132:135],v[92:95],a[24:27]
buffer_load_dwordx4 v229, s[8:11], s64 offen offset:1024 lds
MM a[24:27],a[128:131],v[88:91],a[24:27]
RD a[208:211],v224 offset:20480
RD a[212:215],v225 offset:20480
MM a[28:31],a[136:139],v[88:91],a[28:31]
buffer_load_dwordx4 v230, s[8:11], s64 offen offset:2048 lds
MM a[28:31],a[140:143],v[92:95],a[28:31]
EX v36,v36
MM a[20:23],a[140:143],v[84:87],a[20:23]
EX v37,v37
MM a[20:23],a[136:139],v[80:83],a[20:23]
RD a[216:219],v226 offset:20480
CV v6,v36,v37
MM a[12:15],a[136:139],v[72:75],a[12:15]
EX v38,v38
MM a[12:15],a[140:143],v[76:79],a[12:15]
RD a[220:223],v227 offset:20480
RD a[224:227],v224 offset:24576
MM a[4:7],a[140:143],v[68:71],a[4:7]
EX v39,v39
MM a[4:7],a[136:139],v[64:67],a[4:7]
RD a[228:231],v225 offset:24576
CV v7,v38,v39
MM a[32:35],a[144:147],v[64:67],a[32:35]
EX v40,v40
MM a[32:35],a[148:151],v[68:71],a[32:35]
buffer_load_dwordx4 v231, s[8:11], s64 offen offset:3072 lds
MM a[40:43],a[148:151],v[76:79],a[40:43]
s_add_u32 m0, s65, 114688
EX v41,v41
MM a[40:43],a[144:147],v[72:75],a[40:43]
RD a[232:235],v226 offset:24576
CV v12,v40,v41
MM a[48:51],a[144:147],v[80:83],a[48:51]
EX v42,v42
MM a[48:51],a[148:151],v[84:87],a[48:51]
EX v43,v43
MM a[56:59],a[148:151],v[92:95],a[56:59]
RD a[236:239],v227 offset:24576
CV v13,v42,v43
MM a[56:59],a[144:147],v[88:91],a[56:59]
EX v44,v44
MM a[60:63],a[152:155],v[88:91],a[60:63]
buffer_load_dwordx4 v235, s[12:15], s81 offen offset:0 lds
MM a[60:63],a[156:159],v[92:95],a[60:63]
EX v45,v45
MM a[52:55],a[156:159],v[84:87],a[52:55]
RD a[240:243],v224 offset:28672
CV v14,v44,v45
MM a[52:55],a[152:155],v[80:83],a[52:55]
EX v46,v46
MM a[44:47],a[152:155],v[72:75],a[44:47]
EX v47,v47
MM a[44:47],a[156:159],v[76:79],a[44:47]
RD a[244:247],v225 offset:28672
CV v15,v46,v47
MM a[36:39],a[156:159],v[68:71],a[36:39]
buffer_load_dwordx4 v235, s[12:15], s64 offen offset:1024 lds
MM a[36:39],a[152:155],v[64:67],a[36:39]
EX v48,v48
MM a[64:67],a[160:163],v[64:67],a[64:67]
EX v49,v49
MM a[64:67],a[164:167],v[68:71],a[64:67]
RD a[248:251],v226 offset:28672
CV v20,v48,v49
MM a[72:75],a[164:167],v[76:79],a[72:75]
EX v50,v50
MM a[72:75],a[160:163],v[72:75],a[72:75]
EX v51,v51
MM a[80:83],a[160:163],v[80:83],a[80:83]
RD a[252:255],v227 offset:28672
CV v21,v50,v51
MM a[80:83],a[164:167],v[84:87],a[80:83]
buffer_load_dwordx4 v235, s[12:15], s81 offen offset:2048 lds
MM a[88:91],a[164:167],v[92:95],a[88:91]
EX v52,v52
MM a[88:91],a[160:163],v[88:91],a[88:91]
EX v53,v53
MM a[92:95],a[168:171],v[88:91],a[92:95]
EX v54,v54
MM a[92:95],a[172:175],v[92:95],a[92:95]
buffer_load_dwordx4 v235, s[12:15], s64 offen offset:3072 lds
MM a[84:87],a[172:175],v[84:87],a[84:87]
EX v55,v55
MM a[84:87],a[168:171],v[80:83],a[84:87]
EX v56,v56
MM a[76:79],a[168:171],v[72:75],a[76:79]
EX v57,v57
MM a[76:79],a[172:175],v[76:79],a[76:79]
EX v58,v58
MM a[68:71],a[172:175],v[68:71],a[68:71]
EX v59,v59
MM a[68:71],a[168:171],v[64:67],a[68:71]
EX v60,v60
MM a[96:99],a[176:179],v[64:67],a[96:99]
EX v61,v61
MM a[96:99],a[180:183],v[68:71],a[96:99]
EX v62,v62
MM a[104:107],a[180:183],v[76:79],a[104:107]
EX v63,v63
MM a[104:107],a[176:179],v[72:75],a[104:107]
CV v22,v52,v53
CV v23,v54,v55
MM a[112:115],a[176:179],v[80:83],a[112:115]
CV v28,v56,v57
CV v29,v58,v59
MM a[112:115],a[180:183],v[84:87],a[112:115]
CV v30,v60,v61
CV v31,v62,v63
MM a[120:123],a[180:183],v[92:95],a[120:123]
MM a[120:123],a[176:179],v[88:91],a[120:123]
MM a[124:127],a[184:187],v[88:91],a[124:127]
v_add_f32 v244, v144, v145
v_add_f32 v245, v148, v149
MM a[124:127],a[188:191],v[92:95],a[124:127]
v_add_f32 v246, v152, v153
v_add_f32 v247, v156, v157
MM a[116:119],a[188:191],v[84:87],a[116:119]
MM a[116:119],a[184:187],v[80:83],a[116:119]
MM a[108:111],a[184:187],v[72:75],a[108:111]
MM a[108:111],a[188:191],v[76:79],a[108:111]
MM a[100:103],a[188:191],v[68:71],a[100:103]
MM a[100:103],a[184:187],v[64:67],a[100:103]
v_smfmac_f32_16x16x64_bf16 v[144:147], v[236:239], v[0:7], v234
v_smfmac_f32_16x16x64_bf16 v[148:151], v[236:239], v[8:15], v234
v_smfmac_f32_16x16x64_bf16 v[152:155], v[236:239], v[16:23], v234
v_smfmac_f32_16x16x64_bf16 v[156:159], v[236:239], v[24:31], v234
s_waitcnt lgkmcnt(0)
MM v[64:67],a[192:195],v[160:163],v[128:131]
s_add_u32 s64, s64, 16384
TR a[128:129],v232 offset:0
s_and_b32 s64, s64, 524287
MM v[64:67],a[196:199],v[176:179],v[64:67]
s_add_u32 s81, s64, 896
TR a[130:131],v232 offset:4096
v_max3_f32 v249, v144, v145, v148
MM v[64:67],a[200:203],v[192:195],v[64:67]
TR a[132:133],v232 offset:8192
v_max3_f32 v249, v249, v149, v152
MM v[64:67],a[204:207],v[208:211],v[64:67]
TR a[134:135],v232 offset:12288
v_max3_f32 v249, v249, v153, v156
MM v[68:71],a[220:223],v[208:211],v[128:131]
TR a[136:137],v233 offset:0
v_max_f32 v249, v249, v157
MM v[68:71],a[216:219],v[192:195],v[68:71]
TR a[138:139],v233 offset:4096
v_cmp_lt_f32 vcc, s71, v249
MM v[68:71],a[212:215],v[176:179],v[68:71]
TR a[140:141],v233 offset:8192
TR a[142:143],v233 offset:12288
MM v[68:71],a[208:211],v[160:163],v[68:71]
s_cbranch_vccnz LSITE8_%=
LBACK8_%=:
MM v[76:79],a[208:211],v[164:167],v[132:135]
EX v64,v64
MM v[76:79],a[212:215],v[180:183],v[76:79]
EX v65,v65
MM v[76:79],a[216:219],v[196:199],v[76:79]
TR a[144:145],v232 offset:512
CV v64,v64,v65
MM v[76:79],a[220:223],v[212:215],v[76:79]
EX v66,v66
MM v[72:75],a[204:207],v[212:215],v[132:135]
EX v67,v67
MM v[72:75],a[200:203],v[196:199],v[72:75]
TR a[146:147],v232 offset:4608
CV v65,v66,v67
MM v[72:75],a[196:199],v[180:183],v[72:75]
EX v68,v68
MM v[72:75],a[192:195],v[164:167],v[72:75]
TR a[148:149],v232 offset:8704
TR a[150:151],v232 offset:12800
MM v[80:83],a[192:195],v[168:171],v[136:139]
EX v69,v69
MM v[80:83],a[196:199],v[184:187],v[80:83]
TR a[152:153],v233 offset:512
CV v66,v68,v69
MM v[80:83],a[200:203],v[200:203],v[80:83]
EX v70,v70
MM v[80:83],a[204:207],v[216:219],v[80:83]
TR a[154:155],v233 offset:4608
TR a[156:157],v233 offset:8704
MM v[84:87],a[220:223],v[216:219],v[136:139]
s_cmp_lg_u32 s70, s82
s_cbranch_scc1 LWV_%=
v_lshlrev_b32 v250, 3, v248
v_min_u32 v250, 4992, v250
s_sub_u32 s91, 5242880, s26
s_sub_u32 s60, s24, s91
s_subb_u32 s61, s25, 0
s_mov_b32 s62, 5242880
s_mov_b32 s63, 131072
s_lshl_b32 s90, s16, 2
s_add_u32 s90, s90, %[swave]
s_mul_i32 s90, s90, 10240
s_add_u32 s90, s90, 5120
s_add_u32 s91, %[slds], 135168
s_mov_b32 m0, s91
s_nop 0
buffer_load_dword v250, s[60:63], s90 offen offset:0 lds
LWV_%=:
MM v[84:87],a[216:219],v[200:203],v[84:87]
EX v71,v71
MM v[84:87],a[212:215],v[184:187],v[84:87]
TR a[158:159],v233 offset:12800
CV v67,v70,v71
MM v[84:87],a[208:211],v[168:171],v[84:87]
EX v72,v72
MM v[92:95],a[208:211],v[172:175],v[140:143]
EX v73,v73
MM v[92:95],a[212:215],v[188:191],v[92:95]
TR a[160:161],v232 offset:1024
CV v72,v72,v73
MM v[92:95],a[216:219],v[204:207],v[92:95]
EX v74,v74
MM v[92:95],a[220:223],v[220:223],v[92:95]
EX v75,v75
MM v[88:91],a[204:207],v[220:223],v[140:143]
TR a[162:163],v232 offset:5120
CV v73,v74,v75
MM v[88:91],a[200:203],v[204:207],v[88:91]
TR a[164:165],v232 offset:9216
TR a[166:167],v232 offset:13312
MM v[88:91],a[196:199],v[188:191],v[88:91]
EX v76,v76
MM v[88:91],a[192:195],v[172:175],v[88:91]
EX v77,v77
MM v[96:99],a[224:227],v[160:163],v[128:131]
TR a[168:169],v233 offset:1024
CV v74,v76,v77
MM v[96:99],a[228:231],v[176:179],v[96:99]
EX v78,v78
MM v[96:99],a[232:235],v[192:195],v[96:99]
TR a[170:171],v233 offset:5120
TR a[172:173],v233 offset:9216
MM v[96:99],a[236:239],v[208:211],v[96:99]
EX v79,v79
MM v[100:103],a[252:255],v[208:211],v[128:131]
TR a[174:175],v233 offset:13312
CV v75,v78,v79
MM v[100:103],a[248:251],v[192:195],v[100:103]
EX v80,v80
MM v[100:103],a[244:247],v[176:179],v[100:103]
EX v81,v81
MM v[100:103],a[240:243],v[160:163],v[100:103]
TR a[176:177],v232 offset:1536
CV v80,v80,v81
MM v[108:111],a[240:243],v[164:167],v[132:135]
EX v82,v82
MM v[108:111],a[244:247],v[180:183],v[108:111]
TR a[178:179],v232 offset:5632
TR a[180:181],v232 offset:9728
MM v[108:111],a[248:251],v[196:199],v[108:111]
EX v83,v83
MM v[108:111],a[252:255],v[212:215],v[108:111]
TR a[182:183],v232 offset:13824
CV v81,v82,v83
MM v[104:107],a[236:239],v[212:215],v[132:135]
EX v84,v84
MM v[104:107],a[232:235],v[196:199],v[104:107]
EX v85,v85
MM v[104:107],a[228:231],v[180:183],v[104:107]
TR a[184:185],v233 offset:1536
CV v82,v84,v85
MM v[104:107],a[224:227],v[164:167],v[104:107]
EX v86,v86
MM v[112:115],a[224:227],v[168:171],v[136:139]
TR a[186:187],v233 offset:5632
TR a[188:189],v233 offset:9728
MM v[112:115],a[228:231],v[184:187],v[112:115]
EX v87,v87
MM v[112:115],a[232:235],v[200:203],v[112:115]
TR a[190:191],v233 offset:13824
CV v83,v86,v87
MM v[112:115],a[236:239],v[216:219],v[112:115]
EX v88,v88
MM v[116:119],a[252:255],v[216:219],v[136:139]
EX v89,v89
MM v[116:119],a[248:251],v[200:203],v[116:119]
EX v90,v90
MM v[116:119],a[244:247],v[184:187],v[116:119]
EX v91,v91
MM v[116:119],a[240:243],v[168:171],v[116:119]
EX v92,v92
MM v[124:127],a[240:243],v[172:175],v[140:143]
EX v93,v93
MM v[124:127],a[244:247],v[188:191],v[124:127]
EX v94,v94
MM v[124:127],a[248:251],v[204:207],v[124:127]
EX v95,v95
MM v[124:127],a[252:255],v[220:223],v[124:127]
EX v96,v96
MM v[120:123],a[236:239],v[220:223],v[140:143]
EX v97,v97
MM v[120:123],a[232:235],v[204:207],v[120:123]
EX v98,v98
MM v[120:123],a[228:231],v[188:191],v[120:123]
s_cmp_lg_u32 s70, 1
s_cbranch_scc1 LTP_%=
s_getpc_b64 s[98:99]
LTA1_%=:
s_min_u32 s73, %[swave], 3
s_lshl_b32 s73, s73, 12
s_add_u32 s73, s73, LPG23_%=-LTA1_%=
s_add_u32 s98, s98, s73
s_addc_u32 s99, s99, 0
s_mov_b64 exec, 0
s_setpc_b64 s[98:99]
LTR1_%=:
s_mov_b64 exec, -1
LTP_%=:
MM v[120:123],a[224:227],v[172:175],v[120:123]
s_waitcnt vmcnt(12) lgkmcnt(0)
s_barrier
MM a[0:3],a[128:131],v[0:3],a[0:3]
s_add_u32 m0, s65, 0
CV v88,v88,v89
CV v89,v90,v91
MM a[0:3],a[132:135],v[4:7],a[0:3]
RD a[192:195],v224 offset:32768
CV v90,v92,v93
MM a[8:11],a[132:135],v[12:15],a[8:11]
RD a[196:199],v225 offset:32768
CV v91,v94,v95
MM a[8:11],a[128:131],v[8:11],a[8:11]
buffer_load_dwordx4 v228, s[8:11], s64 offen offset:0 lds
MM a[16:19],a[128:131],v[16:19],a[16:19]
RD a[200:203],v226 offset:32768
CV v68,v96,v97
MM a[16:19],a[132:135],v[20:23],a[16:19]
RD a[204:207],v227 offset:32768
RD a[208:211],v224 offset:36864
MM a[24:27],a[132:135],v[28:31],a[24:27]
buffer_load_dwordx4 v229, s[8:11], s64 offen offset:1024 lds
MM a[24:27],a[128:131],v[24:27],a[24:27]
EX v99,v99
MM a[28:31],a[136:139],v[24:27],a[28:31]
RD a[212:215],v225 offset:36864
CV v69,v98,v99
MM a[28:31],a[140:143],v[28:31],a[28:31]
EX v100,v100
MM a[20:23],a[140:143],v[20:23],a[20:23]
buffer_load_dwordx4 v230, s[8:11], s64 offen offset:2048 lds
MM a[20:23],a[136:139],v[16:19],a[20:23]
EX v101,v101
MM a[12:15],a[136:139],v[8:11],a[12:15]
RD a[216:219],v226 offset:36864
CV v70,v100,v101
MM a[12:15],a[140:143],v[12:15],a[12:15]
EX v102,v102
MM a[4:7],a[140:143],v[4:7],a[4:7]
EX v103,v103
MM a[4:7],a[136:139],v[0:3],a[4:7]
RD a[220:223],v227 offset:36864
CV v71,v102,v103
MM a[32:35],a[144:147],v[0:3],a[32:35]
EX v104,v104
MM a[32:35],a[148:151],v[4:7],a[32:35]
buffer_load_dwordx4 v231, s[8:11], s64 offen offset:3072 lds
MM a[40:43],a[148:151],v[12:15],a[40:43]
s_add_u32 m0, s65, 65536
EX v105,v105
MM a[40:43],a[144:147],v[8:11],a[40:43]
RD a[224:227],v224 offset:40960
)ASM"
R"ASM(
CV v76,v104,v105
MM a[48:51],a[144:147],v[16:19],a[48:51]
EX v106,v106
MM a[48:51],a[148:151],v[20:23],a[48:51]
EX v107,v107
MM a[56:59],a[148:151],v[28:31],a[56:59]
RD a[228:231],v225 offset:40960
CV v77,v106,v107
MM a[56:59],a[144:147],v[24:27],a[56:59]
EX v108,v108
MM a[60:63],a[152:155],v[24:27],a[60:63]
buffer_load_dwordx4 v235, s[12:15], s81 offen offset:0 lds
MM a[60:63],a[156:159],v[28:31],a[60:63]
EX v109,v109
MM a[52:55],a[156:159],v[20:23],a[52:55]
RD a[232:235],v226 offset:40960
CV v78,v108,v109
MM a[52:55],a[152:155],v[16:19],a[52:55]
EX v110,v110
MM a[44:47],a[152:155],v[8:11],a[44:47]
EX v111,v111
MM a[44:47],a[156:159],v[12:15],a[44:47]
RD a[236:239],v227 offset:40960
CV v79,v110,v111
MM a[36:39],a[156:159],v[4:7],a[36:39]
EX v112,v112
MM a[36:39],a[152:155],v[0:3],a[36:39]
buffer_load_dwordx4 v235, s[12:15], s64 offen offset:1024 lds
MM a[64:67],a[160:163],v[0:3],a[64:67]
EX v113,v113
MM a[64:67],a[164:167],v[4:7],a[64:67]
RD a[240:243],v224 offset:45056
s_nop 0
LPG9_%=:
s_cbranch_execz LTR0_%=
CV v84,v112,v113
MM a[72:75],a[164:167],v[12:15],a[72:75]
EX v114,v114
MM a[72:75],a[160:163],v[8:11],a[72:75]
RD a[244:247],v225 offset:45056
RD a[248:251],v226 offset:45056
MM a[80:83],a[160:163],v[16:19],a[80:83]
EX v115,v115
MM a[80:83],a[164:167],v[20:23],a[80:83]
RD a[252:255],v227 offset:45056
CV v85,v114,v115
MM a[88:91],a[164:167],v[28:31],a[88:91]
EX v116,v116
MM a[88:91],a[160:163],v[24:27],a[88:91]
buffer_load_dwordx4 v235, s[12:15], s81 offen offset:2048 lds
MM a[92:95],a[168:171],v[24:27],a[92:95]
EX v117,v117
MM a[92:95],a[172:175],v[28:31],a[92:95]
EX v118,v118
MM a[84:87],a[172:175],v[20:23],a[84:87]
EX v119,v119
MM a[84:87],a[168:171],v[16:19],a[84:87]
buffer_load_dwordx4 v235, s[12:15], s64 offen offset:3072 lds
MM a[76:79],a[168:171],v[8:11],a[76:79]
EX v120,v120
MM a[76:79],a[172:175],v[12:15],a[76:79]
EX v121,v121
MM a[68:71],a[172:175],v[4:7],a[68:71]
EX v122,v122
MM a[68:71],a[168:171],v[0:3],a[68:71]
EX v123,v123
MM a[96:99],a[176:179],v[0:3],a[96:99]
EX v124,v124
MM a[96:99],a[180:183],v[4:7],a[96:99]
EX v125,v125
MM a[104:107],a[180:183],v[12:15],a[104:107]
EX v126,v126
MM a[104:107],a[176:179],v[8:11],a[104:107]
EX v127,v127
MM a[112:115],a[176:179],v[16:19],a[112:115]
CV v86,v116,v117
CV v87,v118,v119
MM a[112:115],a[180:183],v[20:23],a[112:115]
CV v92,v120,v121
CV v93,v122,v123
MM a[120:123],a[180:183],v[28:31],a[120:123]
CV v94,v124,v125
CV v95,v126,v127
MM a[120:123],a[176:179],v[24:27],a[120:123]
MM a[124:127],a[184:187],v[24:27],a[124:127]
v_add_f32 v244, v144, v145
v_add_f32 v245, v148, v149
MM a[124:127],a[188:191],v[28:31],a[124:127]
v_add_f32 v246, v152, v153
v_add_f32 v247, v156, v157
MM a[116:119],a[188:191],v[20:23],a[116:119]
MM a[116:119],a[184:187],v[16:19],a[116:119]
MM a[108:111],a[184:187],v[8:11],a[108:111]
MM a[108:111],a[188:191],v[12:15],a[108:111]
MM a[100:103],a[188:191],v[4:7],a[100:103]
MM a[100:103],a[184:187],v[0:3],a[100:103]
v_smfmac_f32_16x16x64_bf16 v[144:147], v[236:239], v[64:71], v234
v_smfmac_f32_16x16x64_bf16 v[148:151], v[236:239], v[72:79], v234
v_smfmac_f32_16x16x64_bf16 v[152:155], v[236:239], v[80:87], v234
v_smfmac_f32_16x16x64_bf16 v[156:159], v[236:239], v[88:95], v234
s_waitcnt lgkmcnt(0)
s_sub_u32 s70, s70, 1
s_cmp_lg_u32 s70, 0
s_cbranch_scc1 LLAP_%=
s_branch LLAJ_%=
s_nop 0
s_nop 0
s_nop 0
s_nop 0
s_nop 0
s_nop 0
s_nop 0
s_nop 0
s_nop 0
s_nop 0
s_nop 0
s_nop 0
s_nop 0
s_nop 0
s_nop 0
s_nop 0
s_nop 0
s_nop 0
s_nop 0
s_nop 0
s_nop 0
s_nop 0
s_nop 0
s_nop 0
s_nop 0
s_nop 0
s_nop 0
s_nop 0
s_nop 0
s_nop 0
s_nop 0
s_nop 0
s_nop 0
s_nop 0
s_nop 0
s_nop 0
s_nop 0
s_nop 0
s_nop 0
s_nop 0
s_nop 0
s_nop 0
s_nop 0
s_nop 0
s_nop 0
s_nop 0
s_nop 0
s_nop 0
s_nop 0
s_nop 0
s_nop 0
s_nop 0
s_nop 0
s_nop 0
s_nop 0
s_nop 0
s_nop 0
s_nop 0
s_nop 0
s_nop 0
s_nop 0
s_nop 0
s_nop 0
s_nop 0
s_nop 0
s_nop 0
s_nop 0
s_nop 0
s_nop 0
s_nop 0
s_nop 0
s_nop 0
s_nop 0
s_nop 0
s_nop 0
s_nop 0
s_nop 0
s_nop 0
s_nop 0
s_nop 0
s_nop 0
s_nop 0
s_nop 0
s_nop 0
s_nop 0
s_nop 0
s_nop 0
s_nop 0
s_nop 0
s_nop 0
s_nop 0
s_nop 0
s_nop 0
s_nop 0
s_nop 0
s_nop 0
s_nop 0
s_nop 0
s_nop 0
s_nop 0
s_nop 0
s_nop 0
s_nop 0
s_nop 0
s_nop 0
s_nop 0
s_nop 0
s_nop 0
s_nop 0
s_nop 0
s_nop 0
s_nop 0
s_nop 0
s_nop 0
s_nop 0
s_nop 0
s_nop 0
s_nop 0
s_nop 0
s_nop 0
s_nop 0
s_nop 0
s_nop 0
s_nop 0
s_nop 0
s_nop 0
s_nop 0
s_nop 0
s_nop 0
s_nop 0
s_nop 0
s_nop 0
s_nop 0
s_nop 0
s_nop 0
s_nop 0
s_nop 0
s_nop 0
s_nop 0
s_nop 0
s_nop 0
s_nop 0
s_nop 0
s_nop 0
s_nop 0
s_nop 0
s_nop 0
s_nop 0
s_nop 0
s_nop 0
s_nop 0
s_nop 0
s_nop 0
s_nop 0
s_nop 0
s_nop 0
s_nop 0
s_nop 0
s_nop 0
s_nop 0
s_nop 0
s_nop 0
s_nop 0
s_nop 0
s_nop 0
s_nop 0
s_nop 0
s_nop 0
s_nop 0
s_nop 0
s_nop 0
s_nop 0
s_nop 0
s_nop 0
s_nop 0
s_nop 0
s_nop 0
s_nop 0
s_nop 0
s_nop 0
s_nop 0
s_nop 0
s_nop 0
s_nop 0
s_nop 0
s_nop 0
s_nop 0
s_nop 0
s_nop 0
s_nop 0
s_nop 0
s_nop 0
s_nop 0
s_nop 0
s_nop 0
s_nop 0
s_nop 0
s_nop 0
s_nop 0
s_nop 0
s_nop 0
s_nop 0
s_nop 0
s_nop 0
s_nop 0
s_nop 0
s_nop 0
s_nop 0
s_nop 0
s_nop 0
s_nop 0
s_nop 0
s_nop 0
s_nop 0
s_nop 0
s_nop 0
s_nop 0
s_nop 0
s_nop 0
s_nop 0
s_nop 0
s_nop 0
s_nop 0
s_nop 0
s_nop 0
s_nop 0
s_nop 0
s_nop 0
s_nop 0
s_nop 0
s_nop 0
s_nop 0
s_nop 0
s_nop 0
s_nop 0
s_nop 0
s_nop 0
s_nop 0
s_nop 0
s_nop 0
s_nop 0
s_nop 0
s_nop 0
s_nop 0
s_nop 0
s_nop 0
s_nop 0
s_nop 0
s_nop 0
s_nop 0
s_nop 0
s_nop 0
s_nop 0
s_nop 0
s_nop 0
s_nop 0
s_nop 0
s_nop 0
s_nop 0
s_nop 0
s_nop 0
s_nop 0
s_nop 0
s_nop 0
s_nop 0
s_nop 0
s_nop 0
s_nop 0
s_nop 0
s_nop 0
s_nop 0
s_nop 0
s_nop 0
s_nop 0
s_nop 0
s_nop 0
s_nop 0
s_nop 0
s_nop 0
s_nop 0
s_nop 0
s_nop 0
s_nop 0
s_nop 0
s_nop 0
s_nop 0
s_nop 0
s_nop 0
s_nop 0
s_nop 0
s_nop 0
s_nop 0
s_nop 0
s_nop 0
s_nop 0
s_nop 0
s_nop 0
s_nop 0
s_nop 0
s_nop 0
s_nop 0
s_nop 0
s_nop 0
s_nop 0
s_nop 0
s_nop 0
s_nop 0
s_nop 0
s_nop 0
s_nop 0
s_nop 0
s_nop 0
s_nop 0
s_nop 0
s_nop 0
s_nop 0
s_nop 0
s_nop 0
s_nop 0
s_nop 0
s_nop 0
s_nop 0
s_nop 0
s_nop 0
s_nop 0
s_nop 0
s_nop 0
s_nop 0
s_nop 0
s_nop 0
s_nop 0
s_nop 0
s_nop 0
s_nop 0
s_nop 0
s_nop 0
s_nop 0
s_nop 0
s_nop 0
s_nop 0
s_nop 0
s_nop 0
s_nop 0
s_nop 0
s_nop 0
s_nop 0
s_nop 0
s_nop 0
s_nop 0
s_nop 0
s_nop 0
s_nop 0
s_nop 0
s_nop 0
s_nop 0
s_nop 0
s_nop 0
s_nop 0
s_nop 0
s_nop 0
s_nop 0
s_nop 0
s_nop 0
s_nop 0
s_nop 0
s_nop 0
s_nop 0
s_nop 0
s_nop 0
s_nop 0
s_nop 0
s_nop 0
s_nop 0
s_nop 0
s_nop 0
s_nop 0
s_nop 0
s_nop 0
s_nop 0
s_nop 0
s_nop 0
s_nop 0
s_nop 0
s_nop 0
s_nop 0
s_nop 0
s_nop 0
s_nop 0
s_nop 0
s_nop 0
s_nop 0
s_nop 0
s_nop 0
s_nop 0
s_nop 0
s_nop 0
s_nop 0
s_nop 0
s_nop 0
s_nop 0
s_nop 0
s_nop 0
s_nop 0
s_nop 0
s_nop 0
s_nop 0
s_nop 0
s_nop 0
s_nop 0
s_nop 0
s_nop 0
s_nop 0
s_nop 0
s_nop 0
s_nop 0
s_nop 0
s_nop 0
s_nop 0
s_nop 0
s_nop 0
s_nop 0
s_nop 0
s_nop 0
s_nop 0
s_nop 0
s_nop 0
s_nop 0
s_nop 0
s_nop 0
s_nop 0
s_nop 0
s_nop 0
s_nop 0
s_nop 0
s_nop 0
s_nop 0
s_nop 0
s_nop 0
s_nop 0
s_nop 0
s_nop 0
s_nop 0
s_nop 0
s_nop 0
s_nop 0
s_nop 0
s_nop 0
s_nop 0
s_nop 0
s_nop 0
s_nop 0
s_nop 0
s_nop 0
s_nop 0
s_nop 0
s_nop 0
s_nop 0
s_nop 0
s_nop 0
s_nop 0
s_nop 0
s_nop 0
s_nop 0
s_nop 0
s_nop 0
s_nop 0
s_nop 0
s_nop 0
s_nop 0
s_nop 0
s_nop 0
s_nop 0
s_nop 0
s_nop 0
s_nop 0
s_nop 0
s_nop 0
s_nop 0
s_nop 0
s_nop 0
s_nop 0
LLAR_%=:
MM v[0:3],a[192:195],v[160:163],v[128:131]
s_add_u32 s64, s64, 16384
TR a[128:129],v232 offset:16384
s_add_u32 s81, s64, 896
MM v[0:3],a[196:199],v[176:179],v[0:3]
TR a[130:131],v232 offset:20480
v_max3_f32 v249, v144, v145, v148
MM v[0:3],a[200:203],v[192:195],v[0:3]
TR a[132:133],v232 offset:24576
v_max3_f32 v249, v249, v149, v152
MM v[0:3],a[204:207],v[208:211],v[0:3]
TR a[134:135],v232 offset:28672
v_max3_f32 v249, v249, v153, v156
MM v[4:7],a[220:223],v[208:211],v[128:131]
TR a[136:137],v233 offset:16384
v_max_f32 v249, v249, v157
MM v[4:7],a[216:219],v[192:195],v[4:7]
TR a[138:139],v233 offset:20480
v_cmp_lt_f32 vcc, s71, v249
MM v[4:7],a[212:215],v[176:179],v[4:7]
TR a[140:141],v233 offset:24576
TR a[142:143],v233 offset:28672
MM v[4:7],a[208:211],v[160:163],v[4:7]
s_cbranch_vccnz LSITE9_%=
LBACK9_%=:
MM v[12:15],a[208:211],v[164:167],v[132:135]
s_lshl_b32 s17, s16, 2
EX v0,v0
MM v[12:15],a[212:215],v[180:183],v[12:15]
s_add_u32 s17, s17, %[swave]
EX v1,v1
MM v[12:15],a[216:219],v[196:199],v[12:15]
s_sub_u32 s90, s16, 108
TR a[144:145],v232 offset:16896
CV v0,v0,v1
MM v[12:15],a[220:223],v[212:215],v[12:15]
s_min_u32 s90, s90, s16
EX v2,v2
MM v[8:11],a[204:207],v[212:215],v[132:135]
s_lshl_b32 s48, s90, 5
EX v3,v3
MM v[8:11],a[200:203],v[196:199],v[8:11]
s_add_u32 s76, %[slds], 155648
v_add_u32 v250, s76, v248
CV v1,v2,v3
MM v[8:11],a[196:199],v[180:183],v[8:11]
s_lshl_b32 s77, s17, 4
TR a[146:147],v232 offset:20992
TR a[148:149],v232 offset:25088
MM v[8:11],a[192:195],v[164:167],v[8:11]
s_add_u32 s76, s76, s77
TR a[150:151],v232 offset:29184
v_mov_b32 v251, s76
MM v[16:19],a[192:195],v[168:171],v[136:139]
RD v[124:127],v251 offset:0
RD v[96:99],v250 offset:0
MM v[16:19],a[196:199],v[184:187],v[16:19]
RD v[100:103],v250 offset:1024
RD v[104:107],v250 offset:2048
MM v[16:19],a[200:203],v[200:203],v[16:19]
RD v[108:111],v250 offset:3072
RD v[112:115],v250 offset:4096
MM v[16:19],a[204:207],v[216:219],v[16:19]
RD v[116:119],v250 offset:5120
RD v[120:123],v250 offset:6144
MM v[20:23],a[220:223],v[216:219],v[136:139]
EX v4,v4
MM v[20:23],a[216:219],v[200:203],v[20:23]
EX v5,v5
MM v[20:23],a[212:215],v[184:187],v[20:23]
s_waitcnt lgkmcnt(7)
v_readfirstlane_b32 s52, v124
v_readfirstlane_b32 s53, v125
MM v[20:23],a[208:211],v[168:171],v[20:23]
v_readfirstlane_b32 s54, v126
v_readfirstlane_b32 s55, v127
EX v6,v6
MM v[28:31],a[208:211],v[172:175],v[140:143]
RD v[124:127],v250 offset:7168
TR a[152:153],v233 offset:16896
MM v[28:31],a[212:215],v[188:191],v[28:31]
s_waitcnt lgkmcnt(2)
v_max3_u32 v253, v96, v97, v98
v_max3_u32 v251, v99, v100, v101
EX v7,v7
MM v[28:31],a[216:219],v[204:207],v[28:31]
v_max3_u32 v253, v253, v102, v103
v_max3_u32 v251, v251, v104, v105
MM v[28:31],a[220:223],v[220:223],v[28:31]
v_max3_u32 v253, v253, v106, v107
v_max3_u32 v251, v251, v108, v109
EX v8,v8
MM v[24:27],a[204:207],v[220:223],v[140:143]
v_max3_u32 v253, v253, v110, v111
v_max3_u32 v251, v251, v112, v113
MM v[24:27],a[200:203],v[204:207],v[24:27]
v_max3_u32 v253, v253, v114, v115
v_max3_u32 v251, v251, v116, v117
EX v9,v9
MM v[24:27],a[196:199],v[188:191],v[24:27]
TR a[154:155],v233 offset:20992
v_max3_u32 v253, v253, v118, v119
v_max3_u32 v251, v251, v120, v121
MM v[24:27],a[192:195],v[172:175],v[24:27]
v_max3_u32 v253, v253, v122, v123
CV v2,v4,v5
EX v10,v10
MM v[32:35],a[224:227],v[160:163],v[128:131]
s_waitcnt lgkmcnt(2)
TR a[156:157],v233 offset:25088
v_max3_u32 v251, v251, v124, v125
v_max3_u32 v253, v253, v126, v127
EX v11,v11
MM v[32:35],a[228:231],v[176:179],v[32:35]
v_max_u32 v253, v253, v251
v_cmp_lt_u32 s[60:61], s52, v253
MM v[32:35],a[232:235],v[192:195],v[32:35]
TR a[158:159],v233 offset:29184
v_cmp_lt_u32 s[62:63], s53, v253
v_cmp_lt_u32 s[56:57], s54, v253
EX v12,v12
MM v[32:35],a[236:239],v[208:211],v[32:35]
v_cmp_lt_u32 s[58:59], s55, v253
CV v3,v6,v7
MM v[36:39],a[252:255],v[208:211],v[128:131]
TR a[160:161],v232 offset:17408
EX v13,v13
MM v[36:39],a[248:251],v[192:195],v[36:39]
s_bcnt1_i32_b64 s60, s[60:61]
TR a[162:163],v232 offset:21504
CV v8,v8,v9
MM v[36:39],a[244:247],v[176:179],v[36:39]
s_bcnt1_i32_b64 s62, s[62:63]
EX v14,v14
MM v[36:39],a[240:243],v[160:163],v[36:39]
s_bcnt1_i32_b64 s56, s[56:57]
TR a[164:165],v232 offset:25600
TR a[166:167],v232 offset:29696
MM v[44:47],a[240:243],v[164:167],v[132:135]
s_bcnt1_i32_b64 s58, s[58:59]
EX v15,v15
MM v[44:47],a[244:247],v[180:183],v[44:47]
s_min_u32 s76, s60, s62
EX v16,v16
MM v[44:47],a[248:251],v[196:199],v[44:47]
s_min_u32 s77, s56, s58
TR a[168:169],v233 offset:17408
CV v9,v10,v11
MM v[44:47],a[252:255],v[212:215],v[44:47]
s_min_u32 s76, s76, s77
s_cmp_lt_u32 s76, 8
s_cbranch_scc1 LRK_%=
LRKb_%=:
EX v17,v17
MM v[40:43],a[236:239],v[212:215],v[132:135]
v_lshrrev_b32 v252, 6, v248
v_mul_u32_u24 v252, 3456, v252
MM v[40:43],a[232:235],v[196:199],v[40:43]
TR a[170:171],v233 offset:21504
)ASM"
R"ASM(
v_and_b32 v251, 48, v248
v_add_u32 v252, v252, v251
EX v18,v18
MM v[40:43],a[228:231],v[180:183],v[40:43]
s_and_b32 s90, %[swave], 1
TR a[172:173],v233 offset:25600
CV v10,v12,v13
MM v[40:43],a[224:227],v[164:167],v[40:43]
s_mul_i32 s90, s90, 276480
EX v19,v19
MM v[48:51],a[224:227],v[168:171],v[136:139]
s_bfe_u32 s91, s48, 131077
TR a[174:175],v233 offset:29696
CV v11,v14,v15
MM v[48:51],a[228:231],v[184:187],v[48:51]
s_mul_i32 s91, s91, 552960
EX v20,v20
MM v[48:51],a[232:235],v[200:203],v[48:51]
s_add_u32 s90, s90, s91
TR a[176:177],v232 offset:17920
TR a[178:179],v232 offset:22016
MM v[48:51],a[236:239],v[216:219],v[48:51]
s_and_b32 s91, s48, 0xffffff80
EX v21,v21
MM v[52:55],a[252:255],v[216:219],v[136:139]
s_add_u32 s90, s90, s91
EX v22,v22
MM v[52:55],a[248:251],v[200:203],v[52:55]
s_and_b32 s91, s48, 16
TR a[180:181],v232 offset:26112
CV v16,v16,v17
MM v[52:55],a[244:247],v[184:187],v[52:55]
s_lshl_b32 s91, s91, 2
EX v23,v23
MM v[52:55],a[240:243],v[168:171],v[52:55]
s_add_u32 s66, s90, s91
TR a[182:183],v232 offset:30208
CV v17,v18,v19
MM v[60:63],a[240:243],v[172:175],v[140:143]
s_mul_i32 s91, %[swave], 5120
EX v24,v24
MM v[60:63],a[244:247],v[188:191],v[60:63]
s_add_u32 s91, s91, %[slds]
TR a[184:185],v233 offset:17920
TR a[186:187],v233 offset:22016
MM v[60:63],a[248:251],v[204:207],v[60:63]
s_add_u32 s67, s91, 131072
TR a[188:189],v233 offset:26112
TR a[190:191],v233 offset:30208
EX v25,v25
MM v[60:63],a[252:255],v[220:223],v[60:63]
EX v26,v26
MM v[56:59],a[236:239],v[220:223],v[140:143]
EX v27,v27
MM v[56:59],a[232:235],v[204:207],v[56:59]
EX v28,v28
MM v[56:59],a[228:231],v[188:191],v[56:59]
EX v29,v29
CV v18,v20,v21
MM v[56:59],a[224:227],v[172:175],v[56:59]
s_waitcnt lgkmcnt(0)
s_barrier
MM a[0:3],a[128:131],v[64:67],a[0:3]
s_add_u32 m0, s65, 16384
CV v19,v22,v23
CV v24,v24,v25
MM a[0:3],a[132:135],v[68:71],a[0:3]
CV v25,v26,v27
CV v26,v28,v29
MM a[8:11],a[132:135],v[76:79],a[8:11]
EX v30,v30
MM a[8:11],a[128:131],v[72:75],a[8:11]
EX v31,v31
MM a[16:19],a[128:131],v[80:83],a[16:19]
RD a[192:195],v224 offset:49152
CV v27,v30,v31
MM a[16:19],a[132:135],v[84:87],a[16:19]
EX v32,v32
MM a[24:27],a[132:135],v[92:95],a[24:27]
buffer_load_dwordx4 v228, s[8:11], s64 offen offset:0 lds
MM a[24:27],a[128:131],v[88:91],a[24:27]
EX v33,v33
MM a[28:31],a[136:139],v[88:91],a[28:31]
RD a[196:199],v225 offset:49152
CV v4,v32,v33
MM a[28:31],a[140:143],v[92:95],a[28:31]
EX v34,v34
MM a[20:23],a[140:143],v[84:87],a[20:23]
EX v35,v35
MM a[20:23],a[136:139],v[80:83],a[20:23]
RD a[200:203],v226 offset:49152
CV v5,v34,v35
MM a[12:15],a[136:139],v[72:75],a[12:15]
EX v36,v36
LPG10_%=:
s_cbranch_execz LTR0_%=
MM a[12:15],a[140:143],v[76:79],a[12:15]
buffer_load_dwordx4 v229, s[8:11], s64 offen offset:1024 lds
MM a[4:7],a[140:143],v[68:71],a[4:7]
EX v37,v37
MM a[4:7],a[136:139],v[64:67],a[4:7]
RD a[204:207],v227 offset:49152
CV v6,v36,v37
MM a[32:35],a[144:147],v[64:67],a[32:35]
EX v38,v38
MM a[32:35],a[148:151],v[68:71],a[32:35]
EX v39,v39
MM a[40:43],a[148:151],v[76:79],a[40:43]
RD a[208:211],v224 offset:53248
CV v7,v38,v39
MM a[40:43],a[144:147],v[72:75],a[40:43]
EX v40,v40
MM a[48:51],a[144:147],v[80:83],a[48:51]
buffer_load_dwordx4 v230, s[8:11], s64 offen offset:2048 lds
MM a[48:51],a[148:151],v[84:87],a[48:51]
EX v41,v41
MM a[56:59],a[148:151],v[92:95],a[56:59]
RD a[212:215],v225 offset:53248
CV v12,v40,v41
MM a[56:59],a[144:147],v[88:91],a[56:59]
EX v42,v42
MM a[60:63],a[152:155],v[88:91],a[60:63]
EX v43,v43
MM a[60:63],a[156:159],v[92:95],a[60:63]
RD a[216:219],v226 offset:53248
CV v13,v42,v43
MM a[52:55],a[156:159],v[84:87],a[52:55]
EX v44,v44
MM a[52:55],a[152:155],v[80:83],a[52:55]
buffer_load_dwordx4 v231, s[8:11], s64 offen offset:3072 lds
MM a[44:47],a[152:155],v[72:75],a[44:47]
s_add_u32 m0, s65, 81920
EX v45,v45
MM a[44:47],a[156:159],v[76:79],a[44:47]
RD a[220:223],v227 offset:53248
CV v14,v44,v45
MM a[36:39],a[156:159],v[68:71],a[36:39]
EX v46,v46
MM a[36:39],a[152:155],v[64:67],a[36:39]
RD a[224:227],v224 offset:57344
RD a[228:231],v225 offset:57344
MM a[64:67],a[160:163],v[64:67],a[64:67]
EX v47,v47
MM a[64:67],a[164:167],v[68:71],a[64:67]
RD a[232:235],v226 offset:57344
CV v15,v46,v47
MM a[72:75],a[164:167],v[76:79],a[72:75]
EX v48,v48
MM a[72:75],a[160:163],v[72:75],a[72:75]
buffer_load_dwordx4 v235, s[12:15], s81 offen offset:0 lds
MM a[80:83],a[160:163],v[80:83],a[80:83]
s_add_u32 m0, s67, 0
s_add_u32 s85, s66, 0
buffer_load_dwordx4 v252, s[36:39], s85 offen offset:0 lds
MM a[80:83],a[164:167],v[84:87],a[80:83]
EX v49,v49
MM a[88:91],a[164:167],v[92:95],a[88:91]
EX v50,v50
MM a[88:91],a[160:163],v[88:91],a[88:91]
RD a[236:239],v227 offset:57344
CV v20,v48,v49
MM a[92:95],a[168:171],v[88:91],a[92:95]
EX v51,v51
MM a[92:95],a[172:175],v[92:95],a[92:95]
RD a[240:243],v224 offset:61440
CV v21,v50,v51
MM a[84:87],a[172:175],v[84:87],a[84:87]
EX v52,v52
MM a[84:87],a[168:171],v[80:83],a[84:87]
EX v53,v53
MM a[76:79],a[168:171],v[72:75],a[76:79]
RD a[244:247],v225 offset:61440
CV v22,v52,v53
MM a[76:79],a[172:175],v[76:79],a[76:79]
EX v54,v54
MM a[68:71],a[172:175],v[68:71],a[68:71]
s_add_u32 m0, s65, 81920
s_nop 0
buffer_load_dwordx4 v235, s[12:15], s64 offen offset:1024 lds
MM a[68:71],a[168:171],v[64:67],a[68:71]
EX v55,v55
MM a[96:99],a[176:179],v[64:67],a[96:99]
RD a[248:251],v226 offset:61440
CV v23,v54,v55
MM a[96:99],a[180:183],v[68:71],a[96:99]
EX v56,v56
MM a[104:107],a[180:183],v[76:79],a[104:107]
EX v57,v57
MM a[104:107],a[176:179],v[72:75],a[104:107]
RD a[252:255],v227 offset:61440
CV v28,v56,v57
MM a[112:115],a[176:179],v[80:83],a[112:115]
buffer_load_dwordx4 v235, s[12:15], s81 offen offset:2048 lds
MM a[112:115],a[180:183],v[84:87],a[112:115]
EX v58,v58
MM a[120:123],a[180:183],v[92:95],a[120:123]
EX v59,v59
MM a[120:123],a[176:179],v[88:91],a[120:123]
EX v60,v60
MM a[124:127],a[184:187],v[88:91],a[124:127]
v_add_f32 v244, v144, v145
v_add_f32 v245, v148, v149
MM a[124:127],a[188:191],v[92:95],a[124:127]
v_add_f32 v246, v152, v153
v_add_f32 v247, v156, v157
MM a[116:119],a[188:191],v[84:87],a[116:119]
EX v61,v61
MM a[116:119],a[184:187],v[80:83],a[116:119]
buffer_load_dwordx4 v235, s[12:15], s64 offen offset:3072 lds
MM a[108:111],a[184:187],v[72:75],a[108:111]
EX v62,v62
MM a[108:111],a[188:191],v[76:79],a[108:111]
EX v63,v63
MM a[100:103],a[188:191],v[68:71],a[100:103]
CV v29,v58,v59
CV v30,v60,v61
MM a[100:103],a[184:187],v[64:67],a[100:103]
CV v31,v62,v63
v_smfmac_f32_16x16x64_bf16 v[144:147], v[236:239], v[0:7], v234
v_smfmac_f32_16x16x64_bf16 v[148:151], v[236:239], v[8:15], v234
v_smfmac_f32_16x16x64_bf16 v[152:155], v[236:239], v[16:23], v234
v_smfmac_f32_16x16x64_bf16 v[156:159], v[236:239], v[24:31], v234
s_waitcnt lgkmcnt(0)
MM v[64:67],a[192:195],v[160:163],v[128:131]
s_add_u32 s64, s64, 16384
TR a[128:129],v232 offset:32768
s_add_u32 s81, s64, 896
MM v[64:67],a[196:199],v[176:179],v[64:67]
TR a[130:131],v232 offset:36864
v_max3_f32 v249, v144, v145, v148
MM v[64:67],a[200:203],v[192:195],v[64:67]
TR a[132:133],v232 offset:40960
v_max3_f32 v249, v249, v149, v152
MM v[64:67],a[204:207],v[208:211],v[64:67]
TR a[134:135],v232 offset:45056
v_max3_f32 v249, v249, v153, v156
MM v[68:71],a[220:223],v[208:211],v[128:131]
TR a[136:137],v233 offset:32768
v_max_f32 v249, v249, v157
MM v[68:71],a[216:219],v[192:195],v[68:71]
TR a[138:139],v233 offset:36864
v_cmp_lt_f32 vcc, s71, v249
MM v[68:71],a[212:215],v[176:179],v[68:71]
TR a[140:141],v233 offset:40960
TR a[142:143],v233 offset:45056
MM v[68:71],a[208:211],v[160:163],v[68:71]
s_cbranch_vccnz LSITE10_%=
LBACK10_%=:
MM v[76:79],a[208:211],v[164:167],v[132:135]
s_add_u32 m0, s67, 1024
s_add_u32 s85, s66, 55296
buffer_load_dwordx4 v252, s[36:39], s85 offen offset:0 lds
MM v[76:79],a[212:215],v[180:183],v[76:79]
EX v64,v64
MM v[76:79],a[216:219],v[196:199],v[76:79]
EX v65,v65
MM v[76:79],a[220:223],v[212:215],v[76:79]
TR a[144:145],v232 offset:33280
CV v64,v64,v65
MM v[72:75],a[204:207],v[212:215],v[132:135]
EX v66,v66
MM v[72:75],a[200:203],v[196:199],v[72:75]
EX v67,v67
MM v[72:75],a[196:199],v[180:183],v[72:75]
TR a[146:147],v232 offset:37376
CV v65,v66,v67
MM v[72:75],a[192:195],v[164:167],v[72:75]
EX v68,v68
MM v[80:83],a[192:195],v[168:171],v[136:139]
TR a[148:149],v232 offset:41472
TR a[150:151],v232 offset:45568
MM v[80:83],a[196:199],v[184:187],v[80:83]
EX v69,v69
MM v[80:83],a[200:203],v[200:203],v[80:83]
TR a[152:153],v233 offset:33280
CV v66,v68,v69
MM v[80:83],a[204:207],v[216:219],v[80:83]
EX v70,v70
MM v[84:87],a[220:223],v[216:219],v[136:139]
EX v71,v71
MM v[84:87],a[216:219],v[200:203],v[84:87]
TR a[154:155],v233 offset:37376
CV v67,v70,v71
MM v[84:87],a[212:215],v[184:187],v[84:87]
EX v72,v72
MM v[84:87],a[208:211],v[168:171],v[84:87]
TR a[156:157],v233 offset:41472
TR a[158:159],v233 offset:45568
MM v[92:95],a[208:211],v[172:175],v[140:143]
EX v73,v73
MM v[92:95],a[212:215],v[188:191],v[92:95]
TR a[160:161],v232 offset:33792
CV v72,v72,v73
MM v[92:95],a[216:219],v[204:207],v[92:95]
EX v74,v74
MM v[92:95],a[220:223],v[220:223],v[92:95]
EX v75,v75
MM v[88:91],a[204:207],v[220:223],v[140:143]
TR a[162:163],v232 offset:37888
CV v73,v74,v75
MM v[88:91],a[200:203],v[204:207],v[88:91]
TR a[164:165],v232 offset:41984
TR a[166:167],v232 offset:46080
MM v[88:91],a[196:199],v[188:191],v[88:91]
EX v76,v76
MM v[88:91],a[192:195],v[172:175],v[88:91]
EX v77,v77
MM v[96:99],a[224:227],v[160:163],v[128:131]
TR a[168:169],v233 offset:33792
CV v74,v76,v77
MM v[96:99],a[228:231],v[176:179],v[96:99]
EX v78,v78
MM v[96:99],a[232:235],v[192:195],v[96:99]
TR a[170:171],v233 offset:37888
TR a[172:173],v233 offset:41984
MM v[96:99],a[236:239],v[208:211],v[96:99]
EX v79,v79
MM v[100:103],a[252:255],v[208:211],v[128:131]
TR a[174:175],v233 offset:46080
CV v75,v78,v79
MM v[100:103],a[248:251],v[192:195],v[100:103]
EX v80,v80
MM v[100:103],a[244:247],v[176:179],v[100:103]
EX v81,v81
MM v[100:103],a[240:243],v[160:163],v[100:103]
TR a[176:177],v232 offset:34304
CV v80,v80,v81
MM v[108:111],a[240:243],v[164:167],v[132:135]
s_add_u32 m0, s67, 2048
s_add_u32 s85, s66, 110592
buffer_load_dwordx4 v252, s[36:39], s85 offen offset:0 lds
MM v[108:111],a[244:247],v[180:183],v[108:111]
EX v82,v82
MM v[108:111],a[248:251],v[196:199],v[108:111]
TR a[178:179],v232 offset:38400
TR a[180:181],v232 offset:42496
MM v[108:111],a[252:255],v[212:215],v[108:111]
EX v83,v83
MM v[104:107],a[236:239],v[212:215],v[132:135]
TR a[182:183],v232 offset:46592
CV v81,v82,v83
MM v[104:107],a[232:235],v[196:199],v[104:107]
EX v84,v84
MM v[104:107],a[228:231],v[180:183],v[104:107]
EX v85,v85
MM v[104:107],a[224:227],v[164:167],v[104:107]
TR a[184:185],v233 offset:34304
CV v82,v84,v85
MM v[112:115],a[224:227],v[168:171],v[136:139]
EX v86,v86
MM v[112:115],a[228:231],v[184:187],v[112:115]
TR a[186:187],v233 offset:38400
TR a[188:189],v233 offset:42496
MM v[112:115],a[232:235],v[200:203],v[112:115]
EX v87,v87
MM v[112:115],a[236:239],v[216:219],v[112:115]
TR a[190:191],v233 offset:46592
CV v83,v86,v87
MM v[116:119],a[252:255],v[216:219],v[136:139]
EX v88,v88
MM v[116:119],a[248:251],v[200:203],v[116:119]
EX v89,v89
MM v[116:119],a[244:247],v[184:187],v[116:119]
EX v90,v90
MM v[116:119],a[240:243],v[168:171],v[116:119]
EX v91,v91
MM v[124:127],a[240:243],v[172:175],v[140:143]
EX v92,v92
MM v[124:127],a[244:247],v[188:191],v[124:127]
EX v93,v93
MM v[124:127],a[248:251],v[204:207],v[124:127]
EX v94,v94
MM v[124:127],a[252:255],v[220:223],v[124:127]
EX v95,v95
MM v[120:123],a[236:239],v[220:223],v[140:143]
EX v96,v96
MM v[120:123],a[232:235],v[204:207],v[120:123]
EX v97,v97
MM v[120:123],a[228:231],v[188:191],v[120:123]
EX v98,v98
MM v[120:123],a[224:227],v[172:175],v[120:123]
s_waitcnt vmcnt(15) lgkmcnt(0)
s_barrier
MM a[0:3],a[128:131],v[0:3],a[0:3]
s_add_u32 m0, s65, 32768
CV v88,v88,v89
CV v89,v90,v91
MM a[0:3],a[132:135],v[4:7],a[0:3]
RD a[192:195],v224 offset:0
CV v90,v92,v93
MM a[8:11],a[132:135],v[12:15],a[8:11]
RD a[196:199],v225 offset:0
CV v91,v94,v95
MM a[8:11],a[128:131],v[8:11],a[8:11]
buffer_load_dwordx4 v228, s[8:11], s64 offen offset:0 lds
)ASM"
R"ASM(
MM a[16:19],a[128:131],v[16:19],a[16:19]
RD a[200:203],v226 offset:0
CV v68,v96,v97
MM a[16:19],a[132:135],v[20:23],a[16:19]
buffer_load_dwordx4 v229, s[8:11], s64 offen offset:1024 lds
MM a[24:27],a[132:135],v[28:31],a[24:27]
RD a[204:207],v227 offset:0
RD a[208:211],v224 offset:4096
MM a[24:27],a[128:131],v[24:27],a[24:27]
EX v99,v99
MM a[28:31],a[136:139],v[24:27],a[28:31]
RD a[212:215],v225 offset:4096
CV v69,v98,v99
MM a[28:31],a[140:143],v[28:31],a[28:31]
EX v100,v100
MM a[20:23],a[140:143],v[20:23],a[20:23]
buffer_load_dwordx4 v230, s[8:11], s64 offen offset:2048 lds
MM a[20:23],a[136:139],v[16:19],a[20:23]
EX v101,v101
MM a[12:15],a[136:139],v[8:11],a[12:15]
RD a[216:219],v226 offset:4096
CV v70,v100,v101
MM a[12:15],a[140:143],v[12:15],a[12:15]
EX v102,v102
MM a[4:7],a[140:143],v[4:7],a[4:7]
EX v103,v103
MM a[4:7],a[136:139],v[0:3],a[4:7]
RD a[220:223],v227 offset:4096
CV v71,v102,v103
MM a[32:35],a[144:147],v[0:3],a[32:35]
EX v104,v104
MM a[32:35],a[148:151],v[4:7],a[32:35]
buffer_load_dwordx4 v231, s[8:11], s64 offen offset:3072 lds
MM a[40:43],a[148:151],v[12:15],a[40:43]
s_add_u32 m0, s65, 98304
EX v105,v105
MM a[40:43],a[144:147],v[8:11],a[40:43]
RD a[224:227],v224 offset:8192
CV v76,v104,v105
MM a[48:51],a[144:147],v[16:19],a[48:51]
EX v106,v106
MM a[48:51],a[148:151],v[20:23],a[48:51]
EX v107,v107
MM a[56:59],a[148:151],v[28:31],a[56:59]
RD a[228:231],v225 offset:8192
CV v77,v106,v107
MM a[56:59],a[144:147],v[24:27],a[56:59]
EX v108,v108
MM a[60:63],a[152:155],v[24:27],a[60:63]
buffer_load_dwordx4 v235, s[12:15], s81 offen offset:0 lds
MM a[60:63],a[156:159],v[28:31],a[60:63]
EX v109,v109
MM a[52:55],a[156:159],v[20:23],a[52:55]
RD a[232:235],v226 offset:8192
CV v78,v108,v109
MM a[52:55],a[152:155],v[16:19],a[52:55]
EX v110,v110
MM a[44:47],a[152:155],v[8:11],a[44:47]
EX v111,v111
MM a[44:47],a[156:159],v[12:15],a[44:47]
RD a[236:239],v227 offset:8192
CV v79,v110,v111
MM a[36:39],a[156:159],v[4:7],a[36:39]
buffer_load_dwordx4 v235, s[12:15], s64 offen offset:1024 lds
MM a[36:39],a[152:155],v[0:3],a[36:39]
EX v112,v112
MM a[64:67],a[160:163],v[0:3],a[64:67]
EX v113,v113
MM a[64:67],a[164:167],v[4:7],a[64:67]
RD a[240:243],v224 offset:12288
CV v84,v112,v113
MM a[72:75],a[164:167],v[12:15],a[72:75]
EX v114,v114
MM a[72:75],a[160:163],v[8:11],a[72:75]
RD a[244:247],v225 offset:12288
RD a[248:251],v226 offset:12288
MM a[80:83],a[160:163],v[16:19],a[80:83]
s_add_u32 m0, s67, 3072
s_add_u32 s85, s66, 165888
buffer_load_dwordx4 v252, s[36:39], s85 offen offset:0 lds
MM a[80:83],a[164:167],v[20:23],a[80:83]
EX v115,v115
MM a[88:91],a[164:167],v[28:31],a[88:91]
RD a[252:255],v227 offset:12288
CV v85,v114,v115
MM a[88:91],a[160:163],v[24:27],a[88:91]
EX v116,v116
MM a[92:95],a[168:171],v[24:27],a[92:95]
EX v117,v117
MM a[92:95],a[172:175],v[28:31],a[92:95]
s_add_u32 m0, s65, 98304
s_nop 0
buffer_load_dwordx4 v235, s[12:15], s81 offen offset:2048 lds
MM a[84:87],a[172:175],v[20:23],a[84:87]
EX v118,v118
MM a[84:87],a[168:171],v[16:19],a[84:87]
EX v119,v119
MM a[76:79],a[168:171],v[8:11],a[76:79]
buffer_load_dwordx4 v235, s[12:15], s64 offen offset:3072 lds
MM a[76:79],a[172:175],v[12:15],a[76:79]
EX v120,v120
MM a[68:71],a[172:175],v[4:7],a[68:71]
EX v121,v121
MM a[68:71],a[168:171],v[0:3],a[68:71]
EX v122,v122
MM a[96:99],a[176:179],v[0:3],a[96:99]
EX v123,v123
MM a[96:99],a[180:183],v[4:7],a[96:99]
EX v124,v124
MM a[104:107],a[180:183],v[12:15],a[104:107]
EX v125,v125
MM a[104:107],a[176:179],v[8:11],a[104:107]
EX v126,v126
MM a[112:115],a[176:179],v[16:19],a[112:115]
EX v127,v127
MM a[112:115],a[180:183],v[20:23],a[112:115]
CV v86,v116,v117
CV v87,v118,v119
MM a[120:123],a[180:183],v[28:31],a[120:123]
CV v92,v120,v121
CV v93,v122,v123
MM a[120:123],a[176:179],v[24:27],a[120:123]
CV v94,v124,v125
CV v95,v126,v127
MM a[124:127],a[184:187],v[24:27],a[124:127]
v_add_f32 v244, v144, v145
v_add_f32 v245, v148, v149
MM a[124:127],a[188:191],v[28:31],a[124:127]
v_add_f32 v246, v152, v153
v_add_f32 v247, v156, v157
MM a[116:119],a[188:191],v[20:23],a[116:119]
MM a[116:119],a[184:187],v[16:19],a[116:119]
MM a[108:111],a[184:187],v[8:11],a[108:111]
MM a[108:111],a[188:191],v[12:15],a[108:111]
MM a[100:103],a[188:191],v[4:7],a[100:103]
MM a[100:103],a[184:187],v[0:3],a[100:103]
v_smfmac_f32_16x16x64_bf16 v[144:147], v[236:239], v[64:71], v234
v_smfmac_f32_16x16x64_bf16 v[148:151], v[236:239], v[72:79], v234
v_smfmac_f32_16x16x64_bf16 v[152:155], v[236:239], v[80:87], v234
v_smfmac_f32_16x16x64_bf16 v[156:159], v[236:239], v[88:95], v234
s_waitcnt lgkmcnt(0)
MM v[0:3],a[192:195],v[160:163],v[128:131]
s_add_u32 s64, s64, 16384
TR a[128:129],v232 offset:49152
s_add_u32 s81, s64, 896
MM v[0:3],a[196:199],v[176:179],v[0:3]
TR a[130:131],v232 offset:53248
v_max3_f32 v249, v144, v145, v148
MM v[0:3],a[200:203],v[192:195],v[0:3]
TR a[132:133],v232 offset:57344
v_max3_f32 v249, v249, v149, v152
MM v[0:3],a[204:207],v[208:211],v[0:3]
TR a[134:135],v232 offset:61440
v_max3_f32 v249, v249, v153, v156
MM v[4:7],a[220:223],v[208:211],v[128:131]
TR a[136:137],v233 offset:49152
v_max_f32 v249, v249, v157
MM v[4:7],a[216:219],v[192:195],v[4:7]
TR a[138:139],v233 offset:53248
v_cmp_lt_f32 vcc, s71, v249
MM v[4:7],a[212:215],v[176:179],v[4:7]
TR a[140:141],v233 offset:57344
TR a[142:143],v233 offset:61440
MM v[4:7],a[208:211],v[160:163],v[4:7]
s_cbranch_vccnz LSITE11_%=
LBACK11_%=:
MM v[12:15],a[208:211],v[164:167],v[132:135]
s_add_u32 m0, s67, 4096
s_add_u32 s85, s66, 221184
buffer_load_dwordx4 v252, s[36:39], s85 offen offset:0 lds
MM v[12:15],a[212:215],v[180:183],v[12:15]
EX v0,v0
MM v[12:15],a[216:219],v[196:199],v[12:15]
EX v1,v1
MM v[12:15],a[220:223],v[212:215],v[12:15]
TR a[144:145],v232 offset:49664
CV v0,v0,v1
MM v[8:11],a[204:207],v[212:215],v[132:135]
EX v2,v2
MM v[8:11],a[200:203],v[196:199],v[8:11]
EX v3,v3
MM v[8:11],a[196:199],v[180:183],v[8:11]
TR a[146:147],v232 offset:53760
CV v1,v2,v3
MM v[8:11],a[192:195],v[164:167],v[8:11]
EX v4,v4
MM v[16:19],a[192:195],v[168:171],v[136:139]
TR a[148:149],v232 offset:57856
TR a[150:151],v232 offset:61952
MM v[16:19],a[196:199],v[184:187],v[16:19]
EX v5,v5
MM v[16:19],a[200:203],v[200:203],v[16:19]
TR a[152:153],v233 offset:49664
CV v2,v4,v5
MM v[16:19],a[204:207],v[216:219],v[16:19]
EX v6,v6
MM v[20:23],a[220:223],v[216:219],v[136:139]
EX v7,v7
MM v[20:23],a[216:219],v[200:203],v[20:23]
TR a[154:155],v233 offset:53760
CV v3,v6,v7
MM v[20:23],a[212:215],v[184:187],v[20:23]
EX v8,v8
MM v[20:23],a[208:211],v[168:171],v[20:23]
TR a[156:157],v233 offset:57856
TR a[158:159],v233 offset:61952
MM v[28:31],a[208:211],v[172:175],v[140:143]
EX v9,v9
MM v[28:31],a[212:215],v[188:191],v[28:31]
TR a[160:161],v232 offset:50176
CV v8,v8,v9
MM v[28:31],a[216:219],v[204:207],v[28:31]
EX v10,v10
MM v[28:31],a[220:223],v[220:223],v[28:31]
EX v11,v11
MM v[24:27],a[204:207],v[220:223],v[140:143]
TR a[162:163],v232 offset:54272
CV v9,v10,v11
MM v[24:27],a[200:203],v[204:207],v[24:27]
TR a[164:165],v232 offset:58368
TR a[166:167],v232 offset:62464
MM v[24:27],a[196:199],v[188:191],v[24:27]
EX v12,v12
MM v[24:27],a[192:195],v[172:175],v[24:27]
EX v13,v13
MM v[32:35],a[224:227],v[160:163],v[128:131]
TR a[168:169],v233 offset:50176
CV v10,v12,v13
MM v[32:35],a[228:231],v[176:179],v[32:35]
EX v14,v14
MM v[32:35],a[232:235],v[192:195],v[32:35]
TR a[170:171],v233 offset:54272
TR a[172:173],v233 offset:58368
MM v[32:35],a[236:239],v[208:211],v[32:35]
EX v15,v15
MM v[36:39],a[252:255],v[208:211],v[128:131]
TR a[174:175],v233 offset:62464
CV v11,v14,v15
MM v[36:39],a[248:251],v[192:195],v[36:39]
EX v16,v16
MM v[36:39],a[244:247],v[176:179],v[36:39]
EX v17,v17
MM v[36:39],a[240:243],v[160:163],v[36:39]
TR a[176:177],v232 offset:50688
CV v16,v16,v17
MM v[44:47],a[240:243],v[164:167],v[132:135]
EX v18,v18
MM v[44:47],a[244:247],v[180:183],v[44:47]
TR a[178:179],v232 offset:54784
TR a[180:181],v232 offset:58880
MM v[44:47],a[248:251],v[196:199],v[44:47]
EX v19,v19
MM v[44:47],a[252:255],v[212:215],v[44:47]
TR a[182:183],v232 offset:62976
CV v17,v18,v19
MM v[40:43],a[236:239],v[212:215],v[132:135]
EX v20,v20
MM v[40:43],a[232:235],v[196:199],v[40:43]
EX v21,v21
MM v[40:43],a[228:231],v[180:183],v[40:43]
TR a[184:185],v233 offset:50688
CV v18,v20,v21
MM v[40:43],a[224:227],v[164:167],v[40:43]
EX v22,v22
MM v[48:51],a[224:227],v[168:171],v[136:139]
TR a[186:187],v233 offset:54784
TR a[188:189],v233 offset:58880
MM v[48:51],a[228:231],v[184:187],v[48:51]
EX v23,v23
MM v[48:51],a[232:235],v[200:203],v[48:51]
TR a[190:191],v233 offset:62976
CV v19,v22,v23
MM v[48:51],a[236:239],v[216:219],v[48:51]
EX v24,v24
MM v[52:55],a[252:255],v[216:219],v[136:139]
EX v25,v25
MM v[52:55],a[248:251],v[200:203],v[52:55]
EX v26,v26
MM v[52:55],a[244:247],v[184:187],v[52:55]
EX v27,v27
MM v[52:55],a[240:243],v[168:171],v[52:55]
EX v28,v28
MM v[60:63],a[240:243],v[172:175],v[140:143]
EX v29,v29
MM v[60:63],a[244:247],v[188:191],v[60:63]
EX v30,v30
s_nop 0
LPG11_%=:
s_cbranch_execz LTR0_%=
MM v[60:63],a[248:251],v[204:207],v[60:63]
EX v31,v31
MM v[60:63],a[252:255],v[220:223],v[60:63]
EX v32,v32
MM v[56:59],a[236:239],v[220:223],v[140:143]
EX v33,v33
MM v[56:59],a[232:235],v[204:207],v[56:59]
EX v34,v34
MM v[56:59],a[228:231],v[188:191],v[56:59]
EX v35,v35
MM v[56:59],a[224:227],v[172:175],v[56:59]
s_waitcnt vmcnt(17) lgkmcnt(0)
s_barrier
MM a[0:3],a[128:131],v[64:67],a[0:3]
s_mov_b32 s86, 94208
v_bfe_u32 v251, v248, 4, 3
v_lshlrev_b32 v251, 3, v251
buffer_load_dwordx2 v[252:253], v251, s[20:23], s86 offen offset:0 sc1
MM a[0:3],a[132:135],v[68:71],a[0:3]
s_add_u32 m0, s65, 49152
CV v24,v24,v25
CV v25,v26,v27
MM a[8:11],a[132:135],v[76:79],a[8:11]
RD a[192:195],v224 offset:16384
CV v26,v28,v29
MM a[8:11],a[128:131],v[72:75],a[8:11]
RD a[196:199],v225 offset:16384
CV v27,v30,v31
MM a[16:19],a[128:131],v[80:83],a[16:19]
buffer_load_dwordx4 v228, s[8:11], s64 offen offset:0 lds
MM a[16:19],a[132:135],v[84:87],a[16:19]
RD a[200:203],v226 offset:16384
CV v4,v32,v33
MM a[24:27],a[132:135],v[92:95],a[24:27]
RD a[204:207],v227 offset:16384
CV v5,v34,v35
MM a[24:27],a[128:131],v[88:91],a[24:27]
buffer_load_dwordx4 v229, s[8:11], s64 offen offset:1024 lds
MM a[28:31],a[136:139],v[88:91],a[28:31]
RD a[208:211],v224 offset:20480
RD a[212:215],v225 offset:20480
MM a[28:31],a[140:143],v[92:95],a[28:31]
EX v36,v36
MM a[20:23],a[140:143],v[84:87],a[20:23]
buffer_load_dwordx4 v230, s[8:11], s64 offen offset:2048 lds
MM a[20:23],a[136:139],v[80:83],a[20:23]
EX v37,v37
MM a[12:15],a[136:139],v[72:75],a[12:15]
RD a[216:219],v226 offset:20480
CV v6,v36,v37
MM a[12:15],a[140:143],v[76:79],a[12:15]
EX v38,v38
MM a[4:7],a[140:143],v[68:71],a[4:7]
EX v39,v39
MM a[4:7],a[136:139],v[64:67],a[4:7]
RD a[220:223],v227 offset:20480
CV v7,v38,v39
MM a[32:35],a[144:147],v[64:67],a[32:35]
EX v40,v40
MM a[32:35],a[148:151],v[68:71],a[32:35]
buffer_load_dwordx4 v231, s[8:11], s64 offen offset:3072 lds
MM a[40:43],a[148:151],v[76:79],a[40:43]
s_add_u32 m0, s65, 114688
EX v41,v41
MM a[40:43],a[144:147],v[72:75],a[40:43]
RD a[224:227],v224 offset:24576
CV v12,v40,v41
MM a[48:51],a[144:147],v[80:83],a[48:51]
EX v42,v42
MM a[48:51],a[148:151],v[84:87],a[48:51]
EX v43,v43
MM a[56:59],a[148:151],v[92:95],a[56:59]
RD a[228:231],v225 offset:24576
CV v13,v42,v43
MM a[56:59],a[144:147],v[88:91],a[56:59]
EX v44,v44
MM a[60:63],a[152:155],v[88:91],a[60:63]
buffer_load_dwordx4 v235, s[12:15], s81 offen offset:0 lds
MM a[60:63],a[156:159],v[92:95],a[60:63]
EX v45,v45
MM a[52:55],a[156:159],v[84:87],a[52:55]
RD a[232:235],v226 offset:24576
CV v14,v44,v45
MM a[52:55],a[152:155],v[80:83],a[52:55]
EX v46,v46
MM a[44:47],a[152:155],v[72:75],a[44:47]
EX v47,v47
MM a[44:47],a[156:159],v[76:79],a[44:47]
RD a[236:239],v227 offset:24576
CV v15,v46,v47
MM a[36:39],a[156:159],v[68:71],a[36:39]
buffer_load_dwordx4 v235, s[12:15], s64 offen offset:1024 lds
MM a[36:39],a[152:155],v[64:67],a[36:39]
EX v48,v48
MM a[64:67],a[160:163],v[64:67],a[64:67]
EX v49,v49
MM a[64:67],a[164:167],v[68:71],a[64:67]
RD a[240:243],v224 offset:28672
CV v20,v48,v49
MM a[72:75],a[164:167],v[76:79],a[72:75]
)ASM"
R"ASM(
EX v50,v50
MM a[72:75],a[160:163],v[72:75],a[72:75]
RD a[244:247],v225 offset:28672
RD a[248:251],v226 offset:28672
MM a[80:83],a[160:163],v[80:83],a[80:83]
EX v51,v51
MM a[80:83],a[164:167],v[84:87],a[80:83]
RD a[252:255],v227 offset:28672
CV v21,v50,v51
MM a[88:91],a[164:167],v[92:95],a[88:91]
EX v52,v52
MM a[88:91],a[160:163],v[88:91],a[88:91]
buffer_load_dwordx4 v235, s[12:15], s81 offen offset:2048 lds
MM a[92:95],a[168:171],v[88:91],a[92:95]
EX v53,v53
MM a[92:95],a[172:175],v[92:95],a[92:95]
EX v54,v54
MM a[84:87],a[172:175],v[84:87],a[84:87]
buffer_load_dwordx4 v235, s[12:15], s64 offen offset:3072 lds
MM a[84:87],a[168:171],v[80:83],a[84:87]
EX v55,v55
MM a[76:79],a[168:171],v[72:75],a[76:79]
EX v56,v56
MM a[76:79],a[172:175],v[76:79],a[76:79]
EX v57,v57
MM a[68:71],a[172:175],v[68:71],a[68:71]
EX v58,v58
MM a[68:71],a[168:171],v[64:67],a[68:71]
EX v59,v59
MM a[96:99],a[176:179],v[64:67],a[96:99]
EX v60,v60
MM a[96:99],a[180:183],v[68:71],a[96:99]
EX v61,v61
MM a[104:107],a[180:183],v[76:79],a[104:107]
EX v62,v62
MM a[104:107],a[176:179],v[72:75],a[104:107]
EX v63,v63
MM a[112:115],a[176:179],v[80:83],a[112:115]
CV v22,v52,v53
CV v23,v54,v55
MM a[112:115],a[180:183],v[84:87],a[112:115]
CV v28,v56,v57
CV v29,v58,v59
MM a[120:123],a[180:183],v[92:95],a[120:123]
CV v30,v60,v61
CV v31,v62,v63
MM a[120:123],a[176:179],v[88:91],a[120:123]
MM a[124:127],a[184:187],v[88:91],a[124:127]
v_add_f32 v244, v144, v145
v_add_f32 v245, v148, v149
MM a[124:127],a[188:191],v[92:95],a[124:127]
v_add_f32 v246, v152, v153
v_add_f32 v247, v156, v157
MM a[116:119],a[188:191],v[84:87],a[116:119]
MM a[116:119],a[184:187],v[80:83],a[116:119]
MM a[108:111],a[184:187],v[72:75],a[108:111]
MM a[108:111],a[188:191],v[76:79],a[108:111]
MM a[100:103],a[188:191],v[68:71],a[100:103]
MM a[100:103],a[184:187],v[64:67],a[100:103]
v_smfmac_f32_16x16x64_bf16 v[144:147], v[236:239], v[0:7], v234
v_smfmac_f32_16x16x64_bf16 v[148:151], v[236:239], v[8:15], v234
v_smfmac_f32_16x16x64_bf16 v[152:155], v[236:239], v[16:23], v234
v_smfmac_f32_16x16x64_bf16 v[156:159], v[236:239], v[24:31], v234
s_waitcnt lgkmcnt(0)
MM v[64:67],a[192:195],v[160:163],v[128:131]
s_add_u32 s64, s64, 16384
TR a[128:129],v232 offset:0
s_add_u32 s81, s64, 896
MM v[64:67],a[196:199],v[176:179],v[64:67]
TR a[130:131],v232 offset:4096
v_max3_f32 v249, v144, v145, v148
MM v[64:67],a[200:203],v[192:195],v[64:67]
TR a[132:133],v232 offset:8192
v_max3_f32 v249, v249, v149, v152
MM v[64:67],a[204:207],v[208:211],v[64:67]
TR a[134:135],v232 offset:12288
v_max3_f32 v249, v249, v153, v156
MM v[68:71],a[220:223],v[208:211],v[128:131]
TR a[136:137],v233 offset:0
v_max_f32 v249, v249, v157
MM v[68:71],a[216:219],v[192:195],v[68:71]
TR a[138:139],v233 offset:4096
v_cmp_lt_f32 vcc, s71, v249
MM v[68:71],a[212:215],v[176:179],v[68:71]
TR a[140:141],v233 offset:8192
TR a[142:143],v233 offset:12288
MM v[68:71],a[208:211],v[160:163],v[68:71]
s_cbranch_vccnz LSITE12_%=
LBACK12_%=:
MM v[76:79],a[208:211],v[164:167],v[132:135]
s_waitcnt vmcnt(8)
v_cmp_ne_u32 vcc, s69, v253
s_cbranch_vccnz LRIX_%=
LRIXb_%=:
MM v[76:79],a[212:215],v[180:183],v[76:79]
s_and_b32 s90, s17, 7
v_and_b32 v253, 65535, v252
v_readlane_b32 s19, v252, s90
MM v[76:79],a[216:219],v[196:199],v[76:79]
v_sub_u32 v253, 65535, v253
v_mul_u32_u24 v253, 1280, v253
MM v[76:79],a[220:223],v[212:215],v[76:79]
v_lshrrev_b32 v252, 7, v248
v_lshl_add_u32 v253, v252, 4, v253
MM v[72:75],a[204:207],v[212:215],v[132:135]
s_mul_i32 s91, %[swave], 3072
EX v64,v64
MM v[72:75],a[200:203],v[196:199],v[72:75]
s_add_u32 s91, s91, %[slds]
EX v65,v65
MM v[72:75],a[196:199],v[180:183],v[72:75]
s_bfe_u32 s90, s48, 131077
EX v66,v66
MM v[72:75],a[192:195],v[164:167],v[72:75]
s_mul_i32 s90, s90, 320
EX v67,v67
MM v[80:83],a[192:195],v[168:171],v[136:139]
s_add_u32 s90, s90, 131072
s_add_u32 m0, s91, 151552
s_nop 0
buffer_load_dwordx4 v253, s[20:23], s90 offen offset:0 sc1 lds
MM v[80:83],a[196:199],v[184:187],v[80:83]
TR a[144:145],v232 offset:512
CV v64,v64,v65
MM v[80:83],a[200:203],v[200:203],v[80:83]
s_add_u32 m0, s91, 152448
s_nop 0
buffer_load_dwordx4 v253, s[20:23], s90 offen offset:128 sc1 lds
MM v[80:83],a[204:207],v[216:219],v[80:83]
EX v68,v68
MM v[84:87],a[220:223],v[216:219],v[136:139]
s_add_u32 m0, s91, 153344
s_nop 0
buffer_load_dwordx4 v253, s[20:23], s90 offen offset:256 sc1 lds
MM v[84:87],a[216:219],v[200:203],v[84:87]
EX v69,v69
MM v[84:87],a[212:215],v[184:187],v[84:87]
s_lshr_b32 s77, %[sb], 3
TR a[146:147],v232 offset:4608
TR a[148:149],v232 offset:8704
MM v[84:87],a[208:211],v[168:171],v[84:87]
s_lshl_b32 s77, s77, 2
EX v70,v70
MM v[92:95],a[208:211],v[172:175],v[140:143]
s_add_u32 s77, s77, %[swave]
EX v71,v71
MM v[92:95],a[212:215],v[188:191],v[92:95]
s_lshl_b32 s77, s77, 9
TR a[150:151],v232 offset:12800
CV v65,v66,v67
MM v[92:95],a[216:219],v[204:207],v[92:95]
s_add_u32 s77, s77, 0
v_lshrrev_b32 v253, 1, v248
v_add_u32 v253, s77, v253
MM v[92:95],a[220:223],v[220:223],v[92:95]
s_mov_b32 s77, LKEND_ka-LPD0_%=-16
TR a[152:153],v233 offset:512
v_min_u32 v253, s77, v253
EX v72,v72
MM v[88:91],a[204:207],v[220:223],v[140:143]
s_getpc_b64 s[98:99]
LPD0_%=:
global_load_dword v253, v253, s[98:99]
MM v[88:91],a[200:203],v[204:207],v[88:91]
s_add_u32 s67, s95, 1024
EX v73,v73
MM v[88:91],a[196:199],v[188:191],v[88:91]
s_mov_b32 s70, 5
TR a[154:155],v233 offset:4608
CV v66,v68,v69
MM v[88:91],a[192:195],v[172:175],v[88:91]
EX v74,v74
MM v[96:99],a[224:227],v[160:163],v[128:131]
TR a[156:157],v233 offset:8704
TR a[158:159],v233 offset:12800
EX v75,v75
MM v[96:99],a[228:231],v[176:179],v[96:99]
TR a[160:161],v232 offset:1024
CV v67,v70,v71
MM v[96:99],a[232:235],v[192:195],v[96:99]
EX v76,v76
MM v[96:99],a[236:239],v[208:211],v[96:99]
EX v77,v77
MM v[100:103],a[252:255],v[208:211],v[128:131]
TR a[162:163],v232 offset:5120
CV v72,v72,v73
MM v[100:103],a[248:251],v[192:195],v[100:103]
EX v78,v78
MM v[100:103],a[244:247],v[176:179],v[100:103]
TR a[164:165],v232 offset:9216
CV v73,v74,v75
MM v[100:103],a[240:243],v[160:163],v[100:103]
EX v79,v79
MM v[108:111],a[240:243],v[164:167],v[132:135]
TR a[166:167],v232 offset:13312
TR a[168:169],v233 offset:1024
MM v[108:111],a[244:247],v[180:183],v[108:111]
EX v80,v80
MM v[108:111],a[248:251],v[196:199],v[108:111]
EX v81,v81
MM v[108:111],a[252:255],v[212:215],v[108:111]
TR a[170:171],v233 offset:5120
CV v74,v76,v77
MM v[104:107],a[236:239],v[212:215],v[132:135]
EX v82,v82
MM v[104:107],a[232:235],v[196:199],v[104:107]
TR a[172:173],v233 offset:9216
CV v75,v78,v79
MM v[104:107],a[228:231],v[180:183],v[104:107]
EX v83,v83
MM v[104:107],a[224:227],v[164:167],v[104:107]
TR a[174:175],v233 offset:13312
TR a[176:177],v232 offset:1536
MM v[112:115],a[224:227],v[168:171],v[136:139]
EX v84,v84
MM v[112:115],a[228:231],v[184:187],v[112:115]
TR a[178:179],v232 offset:5632
CV v80,v80,v81
MM v[112:115],a[232:235],v[200:203],v[112:115]
EX v85,v85
MM v[112:115],a[236:239],v[216:219],v[112:115]
TR a[180:181],v232 offset:9728
CV v81,v82,v83
MM v[116:119],a[252:255],v[216:219],v[136:139]
EX v86,v86
MM v[116:119],a[248:251],v[200:203],v[116:119]
TR a[182:183],v232 offset:13824
TR a[184:185],v233 offset:1536
MM v[116:119],a[244:247],v[184:187],v[116:119]
EX v87,v87
MM v[116:119],a[240:243],v[168:171],v[116:119]
EX v88,v88
MM v[124:127],a[240:243],v[172:175],v[140:143]
TR a[186:187],v233 offset:5632
CV v82,v84,v85
MM v[124:127],a[244:247],v[188:191],v[124:127]
EX v89,v89
MM v[124:127],a[248:251],v[204:207],v[124:127]
TR a[188:189],v233 offset:9728
TR a[190:191],v233 offset:13824
MM v[124:127],a[252:255],v[220:223],v[124:127]
EX v90,v90
MM v[120:123],a[236:239],v[220:223],v[140:143]
CV v83,v86,v87
CV v88,v88,v89
MM v[120:123],a[232:235],v[204:207],v[120:123]
EX v91,v91
MM v[120:123],a[228:231],v[188:191],v[120:123]
EX v92,v92
MM v[120:123],a[224:227],v[172:175],v[120:123]
s_waitcnt lgkmcnt(0)
s_barrier
MM a[0:3],a[128:131],v[0:3],a[0:3]
s_add_u32 m0, s65, 0
EX v93,v93
MM a[0:3],a[132:135],v[4:7],a[0:3]
CV v89,v90,v91
CV v90,v92,v93
MM a[8:11],a[132:135],v[12:15],a[8:11]
EX v94,v94
MM a[8:11],a[128:131],v[8:11],a[8:11]
EX v95,v95
MM a[16:19],a[128:131],v[16:19],a[16:19]
RD a[192:195],v224 offset:32768
CV v91,v94,v95
MM a[16:19],a[132:135],v[20:23],a[16:19]
EX v96,v96
MM a[24:27],a[132:135],v[28:31],a[24:27]
buffer_load_dwordx4 v228, s[8:11], s64 offen offset:0 lds
MM a[24:27],a[128:131],v[24:27],a[24:27]
EX v97,v97
MM a[28:31],a[136:139],v[24:27],a[28:31]
RD a[196:199],v225 offset:32768
CV v68,v96,v97
MM a[28:31],a[140:143],v[28:31],a[28:31]
EX v98,v98
MM a[20:23],a[140:143],v[20:23],a[20:23]
EX v99,v99
MM a[20:23],a[136:139],v[16:19],a[20:23]
RD a[200:203],v226 offset:32768
CV v69,v98,v99
MM a[12:15],a[136:139],v[8:11],a[12:15]
EX v100,v100
MM a[12:15],a[140:143],v[12:15],a[12:15]
buffer_load_dwordx4 v229, s[8:11], s64 offen offset:1024 lds
MM a[4:7],a[140:143],v[4:7],a[4:7]
EX v101,v101
MM a[4:7],a[136:139],v[0:3],a[4:7]
RD a[204:207],v227 offset:32768
CV v70,v100,v101
MM a[32:35],a[144:147],v[0:3],a[32:35]
EX v102,v102
MM a[32:35],a[148:151],v[4:7],a[32:35]
EX v103,v103
MM a[40:43],a[148:151],v[12:15],a[40:43]
RD a[208:211],v224 offset:36864
CV v71,v102,v103
MM a[40:43],a[144:147],v[8:11],a[40:43]
EX v104,v104
MM a[48:51],a[144:147],v[16:19],a[48:51]
buffer_load_dwordx4 v230, s[8:11], s64 offen offset:2048 lds
MM a[48:51],a[148:151],v[20:23],a[48:51]
EX v105,v105
MM a[56:59],a[148:151],v[28:31],a[56:59]
RD a[212:215],v225 offset:36864
CV v76,v104,v105
MM a[56:59],a[144:147],v[24:27],a[56:59]
EX v106,v106
MM a[60:63],a[152:155],v[24:27],a[60:63]
EX v107,v107
MM a[60:63],a[156:159],v[28:31],a[60:63]
RD a[216:219],v226 offset:36864
CV v77,v106,v107
MM a[52:55],a[156:159],v[20:23],a[52:55]
EX v108,v108
MM a[52:55],a[152:155],v[16:19],a[52:55]
buffer_load_dwordx4 v231, s[8:11], s64 offen offset:3072 lds
MM a[44:47],a[152:155],v[8:11],a[44:47]
s_add_u32 m0, s65, 65536
EX v109,v109
MM a[44:47],a[156:159],v[12:15],a[44:47]
RD a[220:223],v227 offset:36864
CV v78,v108,v109
MM a[36:39],a[156:159],v[4:7],a[36:39]
EX v110,v110
MM a[36:39],a[152:155],v[0:3],a[36:39]
RD a[224:227],v224 offset:40960
RD a[228:231],v225 offset:40960
MM a[64:67],a[160:163],v[0:3],a[64:67]
EX v111,v111
MM a[64:67],a[164:167],v[4:7],a[64:67]
RD a[232:235],v226 offset:40960
CV v79,v110,v111
MM a[72:75],a[164:167],v[12:15],a[72:75]
EX v112,v112
MM a[72:75],a[160:163],v[8:11],a[72:75]
buffer_load_dwordx4 v235, s[12:15], s81 offen offset:0 lds
MM a[80:83],a[160:163],v[16:19],a[80:83]
EX v113,v113
MM a[80:83],a[164:167],v[20:23],a[80:83]
RD a[236:239],v227 offset:40960
CV v84,v112,v113
MM a[88:91],a[164:167],v[28:31],a[88:91]
EX v114,v114
MM a[88:91],a[160:163],v[24:27],a[88:91]
EX v115,v115
MM a[92:95],a[168:171],v[24:27],a[92:95]
RD a[240:243],v224 offset:45056
CV v85,v114,v115
MM a[92:95],a[172:175],v[28:31],a[92:95]
EX v116,v116
MM a[84:87],a[172:175],v[20:23],a[84:87]
buffer_load_dwordx4 v235, s[12:15], s64 offen offset:1024 lds
MM a[84:87],a[168:171],v[16:19],a[84:87]
EX v117,v117
MM a[76:79],a[168:171],v[8:11],a[76:79]
RD a[244:247],v225 offset:45056
CV v86,v116,v117
MM a[76:79],a[172:175],v[12:15],a[76:79]
EX v118,v118
MM a[68:71],a[172:175],v[4:7],a[68:71]
EX v119,v119
MM a[68:71],a[168:171],v[0:3],a[68:71]
RD a[248:251],v226 offset:45056
CV v87,v118,v119
MM a[96:99],a[176:179],v[0:3],a[96:99]
EX v120,v120
MM a[96:99],a[180:183],v[4:7],a[96:99]
buffer_load_dwordx4 v235, s[12:15], s81 offen offset:2048 lds
MM a[104:107],a[180:183],v[12:15],a[104:107]
EX v121,v121
MM a[104:107],a[176:179],v[8:11],a[104:107]
RD a[252:255],v227 offset:45056
CV v92,v120,v121
MM a[112:115],a[176:179],v[16:19],a[112:115]
EX v122,v122
MM a[112:115],a[180:183],v[20:23],a[112:115]
EX v123,v123
MM a[120:123],a[180:183],v[28:31],a[120:123]
s_add_u32 s87, s95, 0
buffer_store_dword v240, v248, s[20:23], s87 offen offset:12 sc1
MM a[120:123],a[176:179],v[24:27],a[120:123]
EX v124,v124
MM a[124:127],a[184:187],v[24:27],a[124:127]
v_add_f32 v244, v144, v145
v_add_f32 v245, v148, v149
MM a[124:127],a[188:191],v[28:31],a[124:127]
v_add_f32 v246, v152, v153
)ASM"
R"ASM(
v_add_f32 v247, v156, v157
MM a[116:119],a[188:191],v[20:23],a[116:119]
EX v125,v125
MM a[116:119],a[184:187],v[16:19],a[116:119]
buffer_load_dwordx4 v235, s[12:15], s64 offen offset:3072 lds
MM a[108:111],a[184:187],v[8:11],a[108:111]
EX v126,v126
MM a[108:111],a[188:191],v[12:15],a[108:111]
EX v127,v127
MM a[100:103],a[188:191],v[4:7],a[100:103]
CV v93,v122,v123
CV v94,v124,v125
MM a[100:103],a[184:187],v[0:3],a[100:103]
CV v95,v126,v127
v_smfmac_f32_16x16x64_bf16 v[144:147], v[236:239], v[64:71], v234
v_smfmac_f32_16x16x64_bf16 v[148:151], v[236:239], v[72:79], v234
v_smfmac_f32_16x16x64_bf16 v[152:155], v[236:239], v[80:87], v234
v_smfmac_f32_16x16x64_bf16 v[156:159], v[236:239], v[88:95], v234
s_waitcnt lgkmcnt(0)
s_cmp_ge_u32 s16, 108
s_cbranch_scc1 LLAP_%=
MM v[0:3],a[192:195],v[160:163],v[128:131]
s_add_u32 s64, s64, 16384
TR a[128:129],v232 offset:16384
s_add_u32 s81, s64, 896
MM v[0:3],a[196:199],v[176:179],v[0:3]
TR a[130:131],v232 offset:20480
v_max3_f32 v249, v144, v145, v148
MM v[0:3],a[200:203],v[192:195],v[0:3]
TR a[132:133],v232 offset:24576
v_max3_f32 v249, v249, v149, v152
MM v[0:3],a[204:207],v[208:211],v[0:3]
TR a[134:135],v232 offset:28672
v_max3_f32 v249, v249, v153, v156
MM v[4:7],a[220:223],v[208:211],v[128:131]
TR a[136:137],v233 offset:16384
v_max_f32 v249, v249, v157
MM v[4:7],a[216:219],v[192:195],v[4:7]
TR a[138:139],v233 offset:20480
v_cmp_lt_f32 vcc, s71, v249
MM v[4:7],a[212:215],v[176:179],v[4:7]
TR a[140:141],v233 offset:24576
TR a[142:143],v233 offset:28672
MM v[4:7],a[208:211],v[160:163],v[4:7]
s_cbranch_vccnz LSITE13_%=
LBACK13_%=:
MM v[12:15],a[208:211],v[164:167],v[132:135]
v_lshrrev_b32 v127, 8, v248
v_lshlrev_b32 v124, 9, v127
MM v[12:15],a[212:215],v[180:183],v[12:15]
v_and_b32 v126, 192, v248
v_add_u32 v124, v124, v126
MM v[12:15],a[216:219],v[196:199],v[12:15]
v_bfe_u32 v126, v248, 4, 2
v_lshl_add_u32 v124, v126, 3, v124
MM v[12:15],a[220:223],v[212:215],v[12:15]
s_lshr_b32 s76, %[swave], 1
EX v0,v0
MM v[8:11],a[204:207],v[212:215],v[132:135]
s_mul_i32 s76, s76, 10240
EX v1,v1
MM v[8:11],a[200:203],v[196:199],v[8:11]
s_and_b32 s77, %[swave], 1
EX v2,v2
MM v[8:11],a[196:199],v[180:183],v[8:11]
s_lshl_b32 s77, s77, 5
TR a[144:145],v232 offset:16896
CV v0,v0,v1
MM v[8:11],a[192:195],v[164:167],v[8:11]
s_add_u32 s76, s76, s77
EX v3,v3
MM v[16:19],a[192:195],v[168:171],v[136:139]
s_add_u32 s76, s76, %[slds]
EX v4,v4
MM v[16:19],a[196:199],v[184:187],v[16:19]
s_add_u32 s76, s76, 131072
v_add_u32 v124, s76, v124
v_bfe_u32 v126, v248, 4, 3
MM v[16:19],a[200:203],v[200:203],v[16:19]
v_lshl_add_u32 v126, v127, 3, v126
v_lshlrev_b32 v126, 4, v126
MM v[16:19],a[204:207],v[216:219],v[16:19]
s_mul_i32 s76, %[swave], 3072
EX v5,v5
MM v[20:23],a[220:223],v[216:219],v[136:139]
s_add_u32 s76, s76, %[slds]
TR a[146:147],v232 offset:20992
CV v1,v2,v3
MM v[20:23],a[216:219],v[200:203],v[20:23]
s_add_u32 s76, s76, 151552
TR a[148:149],v232 offset:25088
v_add_u32 v125, s76, v126
MM v[20:23],a[212:215],v[184:187],v[20:23]
s_lshr_b32 s76, s48, 7
EX v6,v6
MM v[20:23],a[208:211],v[168:171],v[20:23]
s_lshl_b32 s76, s76, 5
EX v7,v7
MM v[28:31],a[208:211],v[172:175],v[140:143]
s_and_b32 s77, s48, 16
TR a[150:151],v232 offset:29184
TR a[152:153],v233 offset:16896
MM v[28:31],a[212:215],v[188:191],v[28:31]
s_add_u32 s76, s76, s77
EX v8,v8
MM v[28:31],a[216:219],v[204:207],v[28:31]
s_bfe_u32 s77, s48, 131077
TR a[154:155],v233 offset:20992
CV v2,v4,v5
MM v[28:31],a[220:223],v[220:223],v[28:31]
LPG12_%=:
s_cbranch_execz LTR0_%=
s_lshl_b32 s77, s77, 2
EX v9,v9
MM v[24:27],a[204:207],v[220:223],v[140:143]
s_add_u32 s76, s76, s77
TR a[156:157],v233 offset:25088
CV v3,v6,v7
MM v[24:27],a[200:203],v[204:207],v[24:27]
s_add_u32 s76, s76, %[swave]
EX v10,v10
MM v[24:27],a[196:199],v[188:191],v[24:27]
s_lshl_b32 s97, s76, 2
TR a[158:159],v233 offset:29184
CV v8,v8,v9
MM v[24:27],a[192:195],v[172:175],v[24:27]
s_add_u32 s97, s97, 77824
EX v11,v11
MM v[32:35],a[224:227],v[160:163],v[128:131]
s_lshl_b32 s76, s76, 9
TR a[160:161],v232 offset:17408
TR a[162:163],v232 offset:21504
MM v[32:35],a[228:231],v[176:179],v[32:35]
s_add_u32 s76, s76, 22183936
EX v12,v12
MM v[32:35],a[232:235],v[192:195],v[32:35]
EX v13,v13
MM v[32:35],a[236:239],v[208:211],v[32:35]
TR a[164:165],v232 offset:25600
CV v9,v10,v11
MM v[36:39],a[252:255],v[208:211],v[128:131]
EX v14,v14
MM v[36:39],a[248:251],v[192:195],v[36:39]
TR a[166:167],v232 offset:29696
CV v10,v12,v13
MM v[36:39],a[244:247],v[176:179],v[36:39]
EX v15,v15
MM v[36:39],a[240:243],v[160:163],v[36:39]
TR a[168:169],v233 offset:17408
TR a[170:171],v233 offset:21504
MM v[44:47],a[240:243],v[164:167],v[132:135]
EX v16,v16
MM v[44:47],a[244:247],v[180:183],v[44:47]
EX v17,v17
MM v[44:47],a[248:251],v[196:199],v[44:47]
TR a[172:173],v233 offset:25600
CV v11,v14,v15
MM v[44:47],a[252:255],v[212:215],v[44:47]
EX v18,v18
MM v[40:43],a[236:239],v[212:215],v[132:135]
TR a[174:175],v233 offset:29696
CV v16,v16,v17
MM v[40:43],a[232:235],v[196:199],v[40:43]
EX v19,v19
MM v[40:43],a[228:231],v[180:183],v[40:43]
TR a[176:177],v232 offset:17920
TR a[178:179],v232 offset:22016
MM v[40:43],a[224:227],v[164:167],v[40:43]
EX v20,v20
MM v[48:51],a[224:227],v[168:171],v[136:139]
EX v21,v21
MM v[48:51],a[228:231],v[184:187],v[48:51]
TR a[180:181],v232 offset:26112
CV v17,v18,v19
MM v[48:51],a[232:235],v[200:203],v[48:51]
EX v22,v22
MM v[48:51],a[236:239],v[216:219],v[48:51]
TR a[182:183],v232 offset:30208
CV v18,v20,v21
MM v[52:55],a[252:255],v[216:219],v[136:139]
EX v23,v23
MM v[52:55],a[248:251],v[200:203],v[52:55]
TR a[184:185],v233 offset:17920
TR a[186:187],v233 offset:22016
MM v[52:55],a[244:247],v[184:187],v[52:55]
EX v24,v24
MM v[52:55],a[240:243],v[168:171],v[52:55]
TR a[188:189],v233 offset:26112
CV v19,v22,v23
MM v[60:63],a[240:243],v[172:175],v[140:143]
EX v25,v25
MM v[60:63],a[244:247],v[188:191],v[60:63]
TR a[190:191],v233 offset:30208
CV v24,v24,v25
MM v[60:63],a[248:251],v[204:207],v[60:63]
EX v26,v26
MM v[60:63],a[252:255],v[220:223],v[60:63]
EX v27,v27
MM v[56:59],a[236:239],v[220:223],v[140:143]
EX v28,v28
MM v[56:59],a[232:235],v[204:207],v[56:59]
EX v29,v29
MM v[56:59],a[228:231],v[188:191],v[56:59]
s_getpc_b64 s[98:99]
LTA2_%=:
s_min_u32 s73, %[swave], 3
s_lshl_b32 s73, s73, 12
s_add_u32 s73, s73, LPG13_%=-LTA2_%=
s_add_u32 s98, s98, s73
s_addc_u32 s99, s99, 0
s_mov_b64 exec, 0
s_setpc_b64 s[98:99]
LTR2_%=:
s_mov_b64 exec, -1
CV v25,v26,v27
MM v[56:59],a[224:227],v[172:175],v[56:59]
s_waitcnt vmcnt(17) lgkmcnt(0)
s_barrier
MM a[0:3],a[128:131],v[64:67],a[0:3]
s_add_u32 m0, s65, 16384
RD a[192:195],v224 offset:49152
CV v26,v28,v29
MM a[0:3],a[132:135],v[68:71],a[0:3]
EX v30,v30
MM a[8:11],a[132:135],v[76:79],a[8:11]
EX v31,v31
MM a[8:11],a[128:131],v[72:75],a[8:11]
RD a[196:199],v225 offset:49152
CV v27,v30,v31
MM a[16:19],a[128:131],v[80:83],a[16:19]
s_waitcnt vmcnt(10)
TR v[96:97],v124 offset:0
TR v[98:99],v124 offset:256
MM a[16:19],a[132:135],v[84:87],a[16:19]
RD v[108:111],v125 offset:0
TR v[100:101],v124 offset:2048
MM a[24:27],a[132:135],v[92:95],a[24:27]
TR v[102:103],v124 offset:2304
RD v[112:115],v125 offset:512
MM a[24:27],a[128:131],v[88:91],a[24:27]
TR v[104:105],v124 offset:4096
TR v[106:107],v124 offset:4352
MM a[28:31],a[136:139],v[88:91],a[28:31]
RD v[116:119],v125 offset:1024
RD a[200:203],v226 offset:49152
EX v32,v32
MM a[28:31],a[140:143],v[92:95],a[28:31]
EX v33,v33
MM a[20:23],a[140:143],v[84:87],a[20:23]
EX v34,v34
MM a[20:23],a[136:139],v[80:83],a[20:23]
buffer_load_dwordx4 v228, s[8:11], s64 offen offset:0 lds
MM a[12:15],a[136:139],v[72:75],a[12:15]
EX v35,v35
MM a[12:15],a[140:143],v[76:79],a[12:15]
EX v36,v36
MM a[4:7],a[140:143],v[68:71],a[4:7]
CV v4,v32,v33
CV v5,v34,v35
MM a[4:7],a[136:139],v[64:67],a[4:7]
EX v37,v37
MM a[32:35],a[144:147],v[64:67],a[32:35]
EX v38,v38
MM a[32:35],a[148:151],v[68:71],a[32:35]
s_waitcnt lgkmcnt(1)
buffer_load_dwordx4 v229, s[8:11], s64 offen offset:1024 lds
MM a[40:43],a[148:151],v[76:79],a[40:43]
EX v39,v39
MM v[120:123],v[96:99],v[108:111],0
MM v[120:123],v[100:103],v[112:115],v[120:123]
MM v[120:123],v[104:107],v[116:119],v[120:123]
MM a[40:43],a[144:147],v[72:75],a[40:43]
TR v[96:97],v124 offset:6144
TR v[98:99],v124 offset:6400
MM a[48:51],a[144:147],v[80:83],a[48:51]
RD v[108:111],v125 offset:1536
TR v[100:101],v124 offset:8192
MM a[48:51],a[148:151],v[84:87],a[48:51]
TR v[102:103],v124 offset:8448
RD v[112:115],v125 offset:2048
MM a[56:59],a[148:151],v[92:95],a[56:59]
EX v40,v40
MM a[56:59],a[144:147],v[88:91],a[56:59]
EX v41,v41
MM a[60:63],a[152:155],v[88:91],a[60:63]
RD a[204:207],v227 offset:49152
CV v6,v36,v37
MM a[60:63],a[156:159],v[92:95],a[60:63]
EX v42,v42
MM a[52:55],a[156:159],v[84:87],a[52:55]
RD a[208:211],v224 offset:53248
CV v7,v38,v39
MM a[52:55],a[152:155],v[80:83],a[52:55]
EX v43,v43
MM a[44:47],a[152:155],v[72:75],a[44:47]
buffer_load_dwordx4 v230, s[8:11], s64 offen offset:2048 lds
MM a[44:47],a[156:159],v[76:79],a[44:47]
EX v44,v44
MM a[36:39],a[156:159],v[68:71],a[36:39]
s_waitcnt lgkmcnt(2)
EX v45,v45
MM a[36:39],a[152:155],v[64:67],a[36:39]
RD a[212:215],v225 offset:53248
CV v12,v40,v41
MM v[120:123],v[96:99],v[108:111],v[120:123]
MM v[120:123],v[100:103],v[112:115],v[120:123]
MM a[64:67],a[160:163],v[64:67],a[64:67]
EX v46,v46
MM a[64:67],a[164:167],v[68:71],a[64:67]
RD a[216:219],v226 offset:53248
CV v13,v42,v43
MM a[72:75],a[164:167],v[76:79],a[72:75]
EX v47,v47
MM a[72:75],a[160:163],v[72:75],a[72:75]
buffer_load_dwordx4 v231, s[8:11], s64 offen offset:3072 lds
MM a[80:83],a[160:163],v[80:83],a[80:83]
s_add_u32 m0, s65, 81920
EX v48,v48
MM a[80:83],a[164:167],v[84:87],a[80:83]
RD a[220:223],v227 offset:53248
CV v14,v44,v45
MM a[88:91],a[164:167],v[92:95],a[88:91]
s_mov_b32 exec_lo, 0x00ff00ff
s_mov_b32 exec_hi, 0x00ff00ff
buffer_store_dwordx4 v[120:123], v126, s[20:23], s76 offen offset:0 sc1
s_mov_b64 exec, -1
s_nop 0
MM a[88:91],a[160:163],v[88:91],a[88:91]
EX v49,v49
MM a[92:95],a[168:171],v[88:91],a[92:95]
EX v50,v50
MM a[92:95],a[172:175],v[92:95],a[92:95]
RD a[224:227],v224 offset:57344
CV v15,v46,v47
MM a[84:87],a[172:175],v[84:87],a[84:87]
EX v51,v51
MM a[84:87],a[168:171],v[80:83],a[84:87]
EX v52,v52
MM a[76:79],a[168:171],v[72:75],a[76:79]
buffer_load_dwordx4 v235, s[12:15], s81 offen offset:0 lds
MM a[76:79],a[172:175],v[76:79],a[76:79]
EX v53,v53
MM a[68:71],a[172:175],v[68:71],a[68:71]
RD a[228:231],v225 offset:57344
CV v20,v48,v49
MM a[68:71],a[168:171],v[64:67],a[68:71]
EX v54,v54
MM a[96:99],a[176:179],v[64:67],a[96:99]
RD a[232:235],v226 offset:57344
CV v21,v50,v51
MM a[96:99],a[180:183],v[68:71],a[96:99]
EX v55,v55
MM a[104:107],a[180:183],v[76:79],a[104:107]
RD a[236:239],v227 offset:57344
CV v22,v52,v53
MM a[104:107],a[176:179],v[72:75],a[104:107]
EX v56,v56
MM a[112:115],a[176:179],v[80:83],a[112:115]
buffer_load_dwordx4 v235, s[12:15], s64 offen offset:1024 lds
MM a[112:115],a[180:183],v[84:87],a[112:115]
EX v57,v57
MM a[120:123],a[180:183],v[92:95],a[120:123]
EX v58,v58
MM a[120:123],a[176:179],v[88:91],a[120:123]
RD a[240:243],v224 offset:61440
CV v23,v54,v55
MM a[124:127],a[184:187],v[88:91],a[124:127]
v_add_f32 v244, v144, v145
v_add_f32 v245, v148, v149
MM a[124:127],a[188:191],v[92:95],a[124:127]
v_add_f32 v246, v152, v153
v_add_f32 v247, v156, v157
MM a[116:119],a[188:191],v[84:87],a[116:119]
EX v59,v59
MM a[116:119],a[184:187],v[80:83],a[116:119]
EX v60,v60
MM a[108:111],a[184:187],v[72:75],a[108:111]
EX v61,v61
MM a[108:111],a[188:191],v[76:79],a[108:111]
RD a[244:247],v225 offset:61440
CV v28,v56,v57
MM a[100:103],a[188:191],v[68:71],a[100:103]
buffer_load_dwordx4 v235, s[12:15], s81 offen offset:2048 lds
MM a[100:103],a[184:187],v[64:67],a[100:103]
EX v62,v62
v_smfmac_f32_16x16x64_bf16 v[144:147], v[236:239], v[0:7], v234
RD a[248:251],v226 offset:61440
RD a[252:255],v227 offset:61440
EX v63,v63
v_smfmac_f32_16x16x64_bf16 v[148:151], v[236:239], v[8:15], v234
CV v29,v58,v59
v_smfmac_f32_16x16x64_bf16 v[152:155], v[236:239], v[16:23], v234
CV v30,v60,v61
CV v31,v62,v63
s_nop 1
v_smfmac_f32_16x16x64_bf16 v[156:159], v[236:239], v[24:31], v234
)ASM"
R"ASM(
s_waitcnt lgkmcnt(0)
buffer_load_dwordx4 v235, s[12:15], s64 offen offset:3072 lds
MM v[64:67],a[192:195],v[160:163],v[128:131]
s_add_u32 s64, s64, 16384
TR a[128:129],v232 offset:32768
s_add_u32 s81, s64, 896
MM v[64:67],a[196:199],v[176:179],v[64:67]
TR a[130:131],v232 offset:36864
v_max3_f32 v249, v144, v145, v148
MM v[64:67],a[200:203],v[192:195],v[64:67]
TR a[132:133],v232 offset:40960
v_max3_f32 v249, v249, v149, v152
MM v[64:67],a[204:207],v[208:211],v[64:67]
TR a[134:135],v232 offset:45056
v_max3_f32 v249, v249, v153, v156
MM v[68:71],a[220:223],v[208:211],v[128:131]
TR a[136:137],v233 offset:32768
v_max_f32 v249, v249, v157
MM v[68:71],a[216:219],v[192:195],v[68:71]
TR a[138:139],v233 offset:36864
v_cmp_lt_f32 vcc, s71, v249
MM v[68:71],a[212:215],v[176:179],v[68:71]
TR a[140:141],v233 offset:40960
TR a[142:143],v233 offset:45056
MM v[68:71],a[208:211],v[160:163],v[68:71]
s_cbranch_vccnz LSITE14_%=
LBACK14_%=:
MM v[76:79],a[208:211],v[164:167],v[132:135]
s_add_u32 s48, s48, 16
v_lshrrev_b32 v252, 6, v248
v_mul_u32_u24 v252, 3456, v252
MM v[76:79],a[212:215],v[180:183],v[76:79]
v_and_b32 v251, 48, v248
v_add_u32 v252, v252, v251
MM v[76:79],a[216:219],v[196:199],v[76:79]
s_and_b32 s90, %[swave], 1
EX v64,v64
MM v[76:79],a[220:223],v[212:215],v[76:79]
s_mul_i32 s90, s90, 276480
EX v65,v65
MM v[72:75],a[204:207],v[212:215],v[132:135]
s_bfe_u32 s91, s48, 131077
EX v66,v66
MM v[72:75],a[200:203],v[196:199],v[72:75]
s_mul_i32 s91, s91, 552960
TR a[144:145],v232 offset:33280
CV v64,v64,v65
MM v[72:75],a[196:199],v[180:183],v[72:75]
s_add_u32 s90, s90, s91
EX v67,v67
MM v[72:75],a[192:195],v[164:167],v[72:75]
s_and_b32 s91, s48, 0xffffff80
TR a[146:147],v232 offset:37376
CV v65,v66,v67
MM v[80:83],a[192:195],v[168:171],v[136:139]
s_add_u32 s90, s90, s91
EX v68,v68
MM v[80:83],a[196:199],v[184:187],v[80:83]
s_and_b32 s91, s48, 16
TR a[148:149],v232 offset:41472
TR a[150:151],v232 offset:45568
MM v[80:83],a[200:203],v[200:203],v[80:83]
s_lshl_b32 s91, s91, 2
EX v69,v69
MM v[80:83],a[204:207],v[216:219],v[80:83]
s_add_u32 s66, s90, s91
TR a[152:153],v233 offset:33280
CV v66,v68,v69
MM v[84:87],a[220:223],v[216:219],v[136:139]
s_mul_i32 s91, %[swave], 5120
EX v70,v70
MM v[84:87],a[216:219],v[200:203],v[84:87]
s_add_u32 s91, s91, %[slds]
EX v71,v71
MM v[84:87],a[212:215],v[184:187],v[84:87]
s_add_u32 s67, s91, 131072
TR a[154:155],v233 offset:37376
CV v67,v70,v71
MM v[84:87],a[208:211],v[168:171],v[84:87]
s_and_b32 s18, s17, 255
EX v72,v72
MM v[92:95],a[208:211],v[172:175],v[140:143]
s_sub_u32 s90, s18, 40
s_cmp_ge_u32 s18, 216
s_cselect_b32 s18, s90, s18
MM v[92:95],a[212:215],v[188:191],v[92:95]
s_lshl_b32 s18, s18, 4
EX v73,v73
MM v[92:95],a[216:219],v[204:207],v[92:95]
s_sub_u32 s90, s16, 90
TR a[156:157],v233 offset:41472
CV v72,v72,v73
MM v[92:95],a[220:223],v[220:223],v[92:95]
s_min_u32 s47, s90, s16
EX v74,v74
MM v[88:91],a[204:207],v[220:223],v[140:143]
s_mul_i32 s90, s47, 205
TR a[158:159],v233 offset:45568
TR a[160:161],v232 offset:33792
MM v[88:91],a[200:203],v[204:207],v[88:91]
s_lshr_b32 s46, s90, 11
EX v75,v75
MM v[88:91],a[196:199],v[188:191],v[88:91]
s_mul_i32 s90, s46, 244480
TR a[162:163],v232 offset:37888
CV v73,v74,v75
MM v[88:91],a[192:195],v[172:175],v[88:91]
s_lshl_b32 s91, s47, 7
EX v76,v76
MM v[96:99],a[224:227],v[160:163],v[128:131]
s_add_u32 s90, s90, s91
s_add_u32 s28, s28, s90
s_addc_u32 s29, s29, 0
MM v[96:99],a[228:231],v[176:179],v[96:99]
s_sub_u32 s30, 2211840, s90
EX v77,v77
MM v[96:99],a[232:235],v[192:195],v[96:99]
s_mov_b32 s31, 131072
TR a[164:165],v232 offset:41984
CV v74,v76,v77
MM v[96:99],a[236:239],v[208:211],v[96:99]
EX v78,v78
MM v[100:103],a[252:255],v[208:211],v[128:131]
TR a[166:167],v232 offset:46080
TR a[168:169],v233 offset:33792
MM v[100:103],a[248:251],v[192:195],v[100:103]
EX v79,v79
MM v[100:103],a[244:247],v[176:179],v[100:103]
TR a[170:171],v233 offset:37888
CV v75,v78,v79
MM v[100:103],a[240:243],v[160:163],v[100:103]
EX v80,v80
MM v[108:111],a[240:243],v[164:167],v[132:135]
TR a[172:173],v233 offset:41984
TR a[174:175],v233 offset:46080
MM v[108:111],a[244:247],v[180:183],v[108:111]
EX v81,v81
MM v[108:111],a[248:251],v[196:199],v[108:111]
EX v82,v82
MM v[108:111],a[252:255],v[212:215],v[108:111]
TR a[176:177],v232 offset:34304
CV v80,v80,v81
MM v[104:107],a[236:239],v[212:215],v[132:135]
EX v83,v83
MM v[104:107],a[232:235],v[196:199],v[104:107]
TR a[178:179],v232 offset:38400
CV v81,v82,v83
MM v[104:107],a[228:231],v[180:183],v[104:107]
EX v84,v84
MM v[104:107],a[224:227],v[164:167],v[104:107]
TR a[180:181],v232 offset:42496
TR a[182:183],v232 offset:46592
MM v[112:115],a[224:227],v[168:171],v[136:139]
EX v85,v85
MM v[112:115],a[228:231],v[184:187],v[112:115]
TR a[184:185],v233 offset:34304
CV v82,v84,v85
MM v[112:115],a[232:235],v[200:203],v[112:115]
EX v86,v86
MM v[112:115],a[236:239],v[216:219],v[112:115]
EX v87,v87
MM v[116:119],a[252:255],v[216:219],v[136:139]
TR a[186:187],v233 offset:38400
CV v83,v86,v87
MM v[116:119],a[248:251],v[200:203],v[116:119]
EX v88,v88
MM v[116:119],a[244:247],v[184:187],v[116:119]
TR a[188:189],v233 offset:42496
TR a[190:191],v233 offset:46592
MM v[116:119],a[240:243],v[168:171],v[116:119]
EX v89,v89
MM v[124:127],a[240:243],v[172:175],v[140:143]
EX v90,v90
MM v[124:127],a[244:247],v[188:191],v[124:127]
EX v91,v91
MM v[124:127],a[248:251],v[204:207],v[124:127]
EX v92,v92
MM v[124:127],a[252:255],v[220:223],v[124:127]
EX v93,v93
MM v[120:123],a[236:239],v[220:223],v[140:143]
EX v94,v94
MM v[120:123],a[232:235],v[204:207],v[120:123]
EX v95,v95
MM v[120:123],a[228:231],v[188:191],v[120:123]
EX v96,v96
MM v[120:123],a[224:227],v[172:175],v[120:123]
s_waitcnt vmcnt(14) lgkmcnt(0)
s_barrier
MM a[0:3],a[128:131],v[0:3],a[0:3]
s_add_u32 m0, s65, 32768
CV v88,v88,v89
CV v89,v90,v91
MM a[0:3],a[132:135],v[4:7],a[0:3]
RD a[192:195],v224 offset:0
CV v90,v92,v93
MM a[8:11],a[132:135],v[12:15],a[8:11]
RD a[196:199],v225 offset:0
CV v91,v94,v95
MM a[8:11],a[128:131],v[8:11],a[8:11]
buffer_load_dwordx4 v228, s[8:11], s64 offen offset:0 lds
MM a[16:19],a[128:131],v[16:19],a[16:19]
RD a[200:203],v226 offset:0
RD a[204:207],v227 offset:0
MM a[16:19],a[132:135],v[20:23],a[16:19]
EX v97,v97
MM a[24:27],a[132:135],v[28:31],a[24:27]
RD a[208:211],v224 offset:4096
CV v68,v96,v97
MM a[24:27],a[128:131],v[24:27],a[24:27]
EX v98,v98
MM a[28:31],a[136:139],v[24:27],a[28:31]
buffer_load_dwordx4 v229, s[8:11], s64 offen offset:1024 lds
MM a[28:31],a[140:143],v[28:31],a[28:31]
EX v99,v99
MM a[20:23],a[140:143],v[20:23],a[20:23]
RD a[212:215],v225 offset:4096
CV v69,v98,v99
MM a[20:23],a[136:139],v[16:19],a[20:23]
EX v100,v100
MM a[12:15],a[136:139],v[8:11],a[12:15]
EX v101,v101
MM a[12:15],a[140:143],v[12:15],a[12:15]
RD a[216:219],v226 offset:4096
CV v70,v100,v101
MM a[4:7],a[140:143],v[4:7],a[4:7]
EX v102,v102
MM a[4:7],a[136:139],v[0:3],a[4:7]
buffer_load_dwordx4 v230, s[8:11], s64 offen offset:2048 lds
MM a[32:35],a[144:147],v[0:3],a[32:35]
EX v103,v103
MM a[32:35],a[148:151],v[4:7],a[32:35]
RD a[220:223],v227 offset:4096
CV v71,v102,v103
MM a[40:43],a[148:151],v[12:15],a[40:43]
EX v104,v104
MM a[40:43],a[144:147],v[8:11],a[40:43]
EX v105,v105
MM a[48:51],a[144:147],v[16:19],a[48:51]
RD a[224:227],v224 offset:8192
CV v76,v104,v105
MM a[48:51],a[148:151],v[20:23],a[48:51]
EX v106,v106
MM a[56:59],a[148:151],v[28:31],a[56:59]
buffer_load_dwordx4 v231, s[8:11], s64 offen offset:3072 lds
MM a[56:59],a[144:147],v[24:27],a[56:59]
s_add_u32 m0, s65, 98304
EX v107,v107
MM a[60:63],a[152:155],v[24:27],a[60:63]
RD a[228:231],v225 offset:8192
CV v77,v106,v107
MM a[60:63],a[156:159],v[28:31],a[60:63]
EX v108,v108
MM a[52:55],a[156:159],v[20:23],a[52:55]
EX v109,v109
MM a[52:55],a[152:155],v[16:19],a[52:55]
RD a[232:235],v226 offset:8192
CV v78,v108,v109
MM a[44:47],a[152:155],v[8:11],a[44:47]
EX v110,v110
MM a[44:47],a[156:159],v[12:15],a[44:47]
buffer_load_dwordx4 v235, s[12:15], s81 offen offset:0 lds
MM a[36:39],a[156:159],v[4:7],a[36:39]
EX v111,v111
MM a[36:39],a[152:155],v[0:3],a[36:39]
RD a[236:239],v227 offset:8192
CV v79,v110,v111
MM a[64:67],a[160:163],v[0:3],a[64:67]
EX v112,v112
MM a[64:67],a[164:167],v[4:7],a[64:67]
EX v113,v113
MM a[72:75],a[164:167],v[12:15],a[72:75]
RD a[240:243],v224 offset:12288
CV v84,v112,v113
MM a[72:75],a[160:163],v[8:11],a[72:75]
buffer_load_dwordx4 v235, s[12:15], s64 offen offset:1024 lds
MM a[80:83],a[160:163],v[16:19],a[80:83]
s_waitcnt vmcnt(10)
v_mov_b32 v250, s69
s_mov_b64 exec, 1
buffer_store_dword v250, off, s[20:23], s97 offset:0 sc1
s_mov_b64 exec, -1
MM a[80:83],a[164:167],v[20:23],a[80:83]
s_add_u32 m0, s67, 0
s_add_u32 s85, s66, 0
buffer_load_dwordx4 v252, s[36:39], s85 offen offset:0 lds
MM a[88:91],a[164:167],v[28:31],a[88:91]
EX v114,v114
MM a[88:91],a[160:163],v[24:27],a[88:91]
EX v115,v115
MM a[92:95],a[168:171],v[24:27],a[92:95]
RD a[244:247],v225 offset:12288
CV v85,v114,v115
MM a[92:95],a[172:175],v[28:31],a[92:95]
EX v116,v116
MM a[84:87],a[172:175],v[20:23],a[84:87]
EX v117,v117
MM a[84:87],a[168:171],v[16:19],a[84:87]
RD a[248:251],v226 offset:12288
CV v86,v116,v117
MM a[76:79],a[168:171],v[8:11],a[76:79]
EX v118,v118
MM a[76:79],a[172:175],v[12:15],a[76:79]
EX v119,v119
MM a[68:71],a[172:175],v[4:7],a[68:71]
RD a[252:255],v227 offset:12288
CV v87,v118,v119
MM a[68:71],a[168:171],v[0:3],a[68:71]
s_add_u32 m0, s65, 98304
s_nop 0
buffer_load_dwordx4 v235, s[12:15], s81 offen offset:2048 lds
MM a[96:99],a[176:179],v[0:3],a[96:99]
EX v120,v120
MM a[96:99],a[180:183],v[4:7],a[96:99]
EX v121,v121
MM a[104:107],a[180:183],v[12:15],a[104:107]
EX v122,v122
s_nop 0
LPG13_%=:
s_cbranch_execz LTR2_%=
MM a[104:107],a[176:179],v[8:11],a[104:107]
buffer_load_dwordx4 v235, s[12:15], s64 offen offset:3072 lds
MM a[112:115],a[176:179],v[16:19],a[112:115]
EX v123,v123
MM a[112:115],a[180:183],v[20:23],a[112:115]
EX v124,v124
MM a[120:123],a[180:183],v[28:31],a[120:123]
EX v125,v125
MM a[120:123],a[176:179],v[24:27],a[120:123]
EX v126,v126
MM a[124:127],a[184:187],v[24:27],a[124:127]
v_add_f32 v244, v144, v145
v_add_f32 v245, v148, v149
MM a[124:127],a[188:191],v[28:31],a[124:127]
v_add_f32 v246, v152, v153
v_add_f32 v247, v156, v157
MM a[116:119],a[188:191],v[20:23],a[116:119]
EX v127,v127
MM a[116:119],a[184:187],v[16:19],a[116:119]
CV v92,v120,v121
CV v93,v122,v123
MM a[108:111],a[184:187],v[8:11],a[108:111]
CV v94,v124,v125
CV v95,v126,v127
MM a[108:111],a[188:191],v[12:15],a[108:111]
MM a[100:103],a[188:191],v[4:7],a[100:103]
MM a[100:103],a[184:187],v[0:3],a[100:103]
v_smfmac_f32_16x16x64_bf16 v[144:147], v[236:239], v[64:71], v234
v_smfmac_f32_16x16x64_bf16 v[148:151], v[236:239], v[72:79], v234
v_smfmac_f32_16x16x64_bf16 v[152:155], v[236:239], v[80:87], v234
v_smfmac_f32_16x16x64_bf16 v[156:159], v[236:239], v[88:95], v234
s_waitcnt lgkmcnt(0)
MM v[0:3],a[192:195],v[160:163],v[128:131]
s_add_u32 s64, s64, 16384
TR a[128:129],v232 offset:49152
s_add_u32 s81, s64, 896
MM v[0:3],a[196:199],v[176:179],v[0:3]
TR a[130:131],v232 offset:53248
v_max3_f32 v249, v144, v145, v148
MM v[0:3],a[200:203],v[192:195],v[0:3]
TR a[132:133],v232 offset:57344
v_max3_f32 v249, v249, v149, v152
MM v[0:3],a[204:207],v[208:211],v[0:3]
TR a[134:135],v232 offset:61440
v_max3_f32 v249, v249, v153, v156
MM v[4:7],a[220:223],v[208:211],v[128:131]
TR a[136:137],v233 offset:49152
v_max_f32 v249, v249, v157
MM v[4:7],a[216:219],v[192:195],v[4:7]
TR a[138:139],v233 offset:53248
v_cmp_lt_f32 vcc, s71, v249
MM v[4:7],a[212:215],v[176:179],v[4:7]
TR a[140:141],v233 offset:57344
TR a[142:143],v233 offset:61440
MM v[4:7],a[208:211],v[160:163],v[4:7]
s_cbranch_vccnz LSITE15_%=
LBACK15_%=:
MM v[12:15],a[208:211],v[164:167],v[132:135]
s_add_u32 m0, s67, 1024
s_add_u32 s85, s66, 55296
buffer_load_dwordx4 v252, s[36:39], s85 offen offset:0 lds
MM v[12:15],a[212:215],v[180:183],v[12:15]
EX v0,v0
MM v[12:15],a[216:219],v[196:199],v[12:15]
EX v1,v1
MM v[12:15],a[220:223],v[212:215],v[12:15]
TR a[144:145],v232 offset:49664
CV v0,v0,v1
MM v[8:11],a[204:207],v[212:215],v[132:135]
EX v2,v2
MM v[8:11],a[200:203],v[196:199],v[8:11]
)ASM"
R"ASM(
EX v3,v3
MM v[8:11],a[196:199],v[180:183],v[8:11]
TR a[146:147],v232 offset:53760
CV v1,v2,v3
MM v[8:11],a[192:195],v[164:167],v[8:11]
EX v4,v4
MM v[16:19],a[192:195],v[168:171],v[136:139]
TR a[148:149],v232 offset:57856
TR a[150:151],v232 offset:61952
MM v[16:19],a[196:199],v[184:187],v[16:19]
EX v5,v5
MM v[16:19],a[200:203],v[200:203],v[16:19]
TR a[152:153],v233 offset:49664
CV v2,v4,v5
MM v[16:19],a[204:207],v[216:219],v[16:19]
EX v6,v6
MM v[20:23],a[220:223],v[216:219],v[136:139]
EX v7,v7
MM v[20:23],a[216:219],v[200:203],v[20:23]
TR a[154:155],v233 offset:53760
CV v3,v6,v7
MM v[20:23],a[212:215],v[184:187],v[20:23]
EX v8,v8
MM v[20:23],a[208:211],v[168:171],v[20:23]
TR a[156:157],v233 offset:57856
TR a[158:159],v233 offset:61952
MM v[28:31],a[208:211],v[172:175],v[140:143]
EX v9,v9
MM v[28:31],a[212:215],v[188:191],v[28:31]
TR a[160:161],v232 offset:50176
CV v8,v8,v9
MM v[28:31],a[216:219],v[204:207],v[28:31]
EX v10,v10
MM v[28:31],a[220:223],v[220:223],v[28:31]
EX v11,v11
MM v[24:27],a[204:207],v[220:223],v[140:143]
TR a[162:163],v232 offset:54272
CV v9,v10,v11
MM v[24:27],a[200:203],v[204:207],v[24:27]
TR a[164:165],v232 offset:58368
TR a[166:167],v232 offset:62464
MM v[24:27],a[196:199],v[188:191],v[24:27]
EX v12,v12
MM v[24:27],a[192:195],v[172:175],v[24:27]
EX v13,v13
MM v[32:35],a[224:227],v[160:163],v[128:131]
TR a[168:169],v233 offset:50176
CV v10,v12,v13
MM v[32:35],a[228:231],v[176:179],v[32:35]
EX v14,v14
MM v[32:35],a[232:235],v[192:195],v[32:35]
TR a[170:171],v233 offset:54272
TR a[172:173],v233 offset:58368
MM v[32:35],a[236:239],v[208:211],v[32:35]
EX v15,v15
MM v[36:39],a[252:255],v[208:211],v[128:131]
TR a[174:175],v233 offset:62464
CV v11,v14,v15
MM v[36:39],a[248:251],v[192:195],v[36:39]
EX v16,v16
MM v[36:39],a[244:247],v[176:179],v[36:39]
EX v17,v17
MM v[36:39],a[240:243],v[160:163],v[36:39]
TR a[176:177],v232 offset:50688
CV v16,v16,v17
MM v[44:47],a[240:243],v[164:167],v[132:135]
s_add_u32 m0, s67, 2048
s_add_u32 s85, s66, 110592
buffer_load_dwordx4 v252, s[36:39], s85 offen offset:0 lds
MM v[44:47],a[244:247],v[180:183],v[44:47]
EX v18,v18
MM v[44:47],a[248:251],v[196:199],v[44:47]
TR a[178:179],v232 offset:54784
TR a[180:181],v232 offset:58880
MM v[44:47],a[252:255],v[212:215],v[44:47]
EX v19,v19
MM v[40:43],a[236:239],v[212:215],v[132:135]
TR a[182:183],v232 offset:62976
CV v17,v18,v19
MM v[40:43],a[232:235],v[196:199],v[40:43]
EX v20,v20
MM v[40:43],a[228:231],v[180:183],v[40:43]
EX v21,v21
MM v[40:43],a[224:227],v[164:167],v[40:43]
TR a[184:185],v233 offset:50688
CV v18,v20,v21
MM v[48:51],a[224:227],v[168:171],v[136:139]
EX v22,v22
MM v[48:51],a[228:231],v[184:187],v[48:51]
TR a[186:187],v233 offset:54784
TR a[188:189],v233 offset:58880
MM v[48:51],a[232:235],v[200:203],v[48:51]
EX v23,v23
MM v[48:51],a[236:239],v[216:219],v[48:51]
TR a[190:191],v233 offset:62976
CV v19,v22,v23
MM v[52:55],a[252:255],v[216:219],v[136:139]
EX v24,v24
MM v[52:55],a[248:251],v[200:203],v[52:55]
EX v25,v25
MM v[52:55],a[244:247],v[184:187],v[52:55]
EX v26,v26
MM v[52:55],a[240:243],v[168:171],v[52:55]
EX v27,v27
MM v[60:63],a[240:243],v[172:175],v[140:143]
EX v28,v28
MM v[60:63],a[244:247],v[188:191],v[60:63]
EX v29,v29
MM v[60:63],a[248:251],v[204:207],v[60:63]
EX v30,v30
MM v[60:63],a[252:255],v[220:223],v[60:63]
EX v31,v31
MM v[56:59],a[236:239],v[220:223],v[140:143]
EX v32,v32
MM v[56:59],a[232:235],v[204:207],v[56:59]
EX v33,v33
MM v[56:59],a[228:231],v[188:191],v[56:59]
EX v34,v34
MM v[56:59],a[224:227],v[172:175],v[56:59]
s_waitcnt lgkmcnt(0)
s_barrier
MM a[0:3],a[128:131],v[64:67],a[0:3]
s_add_u32 m0, s65, 49152
CV v24,v24,v25
CV v25,v26,v27
MM a[0:3],a[132:135],v[68:71],a[0:3]
RD a[192:195],v224 offset:16384
CV v26,v28,v29
MM a[8:11],a[132:135],v[76:79],a[8:11]
RD a[196:199],v225 offset:16384
CV v27,v30,v31
MM a[8:11],a[128:131],v[72:75],a[8:11]
buffer_load_dwordx4 v228, s[8:11], s64 offen offset:0 lds
MM a[16:19],a[128:131],v[80:83],a[16:19]
s_add_u32 m0, s67, 3072
s_add_u32 s85, s66, 165888
buffer_load_dwordx4 v252, s[36:39], s85 offen offset:0 lds
MM a[16:19],a[132:135],v[84:87],a[16:19]
RD a[200:203],v226 offset:16384
CV v4,v32,v33
MM a[24:27],a[132:135],v[92:95],a[24:27]
RD a[204:207],v227 offset:16384
RD a[208:211],v224 offset:20480
MM a[24:27],a[128:131],v[88:91],a[24:27]
s_add_u32 m0, s65, 49152
s_nop 0
buffer_load_dwordx4 v229, s[8:11], s64 offen offset:1024 lds
MM a[28:31],a[136:139],v[88:91],a[28:31]
EX v35,v35
MM a[28:31],a[140:143],v[92:95],a[28:31]
RD a[212:215],v225 offset:20480
CV v5,v34,v35
MM a[20:23],a[140:143],v[84:87],a[20:23]
EX v36,v36
MM a[20:23],a[136:139],v[80:83],a[20:23]
EX v37,v37
MM a[12:15],a[136:139],v[72:75],a[12:15]
RD a[216:219],v226 offset:20480
CV v6,v36,v37
MM a[12:15],a[140:143],v[76:79],a[12:15]
buffer_load_dwordx4 v230, s[8:11], s64 offen offset:2048 lds
MM a[4:7],a[140:143],v[68:71],a[4:7]
EX v38,v38
MM a[4:7],a[136:139],v[64:67],a[4:7]
EX v39,v39
MM a[32:35],a[144:147],v[64:67],a[32:35]
RD a[220:223],v227 offset:20480
CV v7,v38,v39
MM a[32:35],a[148:151],v[68:71],a[32:35]
EX v40,v40
MM a[40:43],a[148:151],v[76:79],a[40:43]
EX v41,v41
MM a[40:43],a[144:147],v[72:75],a[40:43]
RD a[224:227],v224 offset:24576
CV v12,v40,v41
MM a[48:51],a[144:147],v[80:83],a[48:51]
buffer_load_dwordx4 v231, s[8:11], s64 offen offset:3072 lds
MM a[48:51],a[148:151],v[84:87],a[48:51]
s_add_u32 m0, s65, 114688
EX v42,v42
MM a[56:59],a[148:151],v[92:95],a[56:59]
EX v43,v43
MM a[56:59],a[144:147],v[88:91],a[56:59]
RD a[228:231],v225 offset:24576
CV v13,v42,v43
MM a[60:63],a[152:155],v[88:91],a[60:63]
EX v44,v44
MM a[60:63],a[156:159],v[92:95],a[60:63]
buffer_load_dwordx4 v235, s[12:15], s81 offen offset:0 lds
MM a[52:55],a[156:159],v[84:87],a[52:55]
EX v45,v45
MM a[52:55],a[152:155],v[80:83],a[52:55]
RD a[232:235],v226 offset:24576
CV v14,v44,v45
MM a[44:47],a[152:155],v[72:75],a[44:47]
EX v46,v46
MM a[44:47],a[156:159],v[76:79],a[44:47]
EX v47,v47
MM a[36:39],a[156:159],v[68:71],a[36:39]
RD a[236:239],v227 offset:24576
CV v15,v46,v47
MM a[36:39],a[152:155],v[64:67],a[36:39]
EX v48,v48
MM a[64:67],a[160:163],v[64:67],a[64:67]
buffer_load_dwordx4 v235, s[12:15], s64 offen offset:1024 lds
MM a[64:67],a[164:167],v[68:71],a[64:67]
EX v49,v49
MM a[72:75],a[164:167],v[76:79],a[72:75]
RD a[240:243],v224 offset:28672
CV v20,v48,v49
MM a[72:75],a[160:163],v[72:75],a[72:75]
EX v50,v50
MM a[80:83],a[160:163],v[80:83],a[80:83]
EX v51,v51
MM a[80:83],a[164:167],v[84:87],a[80:83]
RD a[244:247],v225 offset:28672
CV v21,v50,v51
MM a[88:91],a[164:167],v[92:95],a[88:91]
EX v52,v52
MM a[88:91],a[160:163],v[88:91],a[88:91]
buffer_load_dwordx4 v235, s[12:15], s81 offen offset:2048 lds
MM a[92:95],a[168:171],v[88:91],a[92:95]
EX v53,v53
MM a[92:95],a[172:175],v[92:95],a[92:95]
RD a[248:251],v226 offset:28672
CV v22,v52,v53
MM a[84:87],a[172:175],v[84:87],a[84:87]
EX v54,v54
MM a[84:87],a[168:171],v[80:83],a[84:87]
EX v55,v55
MM a[76:79],a[168:171],v[72:75],a[76:79]
RD a[252:255],v227 offset:28672
CV v23,v54,v55
MM a[76:79],a[172:175],v[76:79],a[76:79]
EX v56,v56
MM a[68:71],a[172:175],v[68:71],a[68:71]
buffer_load_dwordx4 v235, s[12:15], s64 offen offset:3072 lds
MM a[68:71],a[168:171],v[64:67],a[68:71]
EX v57,v57
MM a[96:99],a[176:179],v[64:67],a[96:99]
EX v58,v58
MM a[96:99],a[180:183],v[68:71],a[96:99]
EX v59,v59
MM a[104:107],a[180:183],v[76:79],a[104:107]
EX v60,v60
MM a[104:107],a[176:179],v[72:75],a[104:107]
EX v61,v61
MM a[112:115],a[176:179],v[80:83],a[112:115]
EX v62,v62
MM a[112:115],a[180:183],v[84:87],a[112:115]
EX v63,v63
MM a[120:123],a[180:183],v[92:95],a[120:123]
s_add_u32 m0, s67, 4096
s_add_u32 s85, s66, 221184
buffer_load_dwordx4 v252, s[36:39], s85 offen offset:0 lds
MM a[120:123],a[176:179],v[88:91],a[120:123]
CV v28,v56,v57
CV v29,v58,v59
MM a[124:127],a[184:187],v[88:91],a[124:127]
v_add_f32 v244, v144, v145
v_add_f32 v245, v148, v149
MM a[124:127],a[188:191],v[92:95],a[124:127]
v_add_f32 v246, v152, v153
v_add_f32 v247, v156, v157
MM a[116:119],a[188:191],v[84:87],a[116:119]
CV v30,v60,v61
CV v31,v62,v63
MM a[116:119],a[184:187],v[80:83],a[116:119]
MM a[108:111],a[184:187],v[72:75],a[108:111]
MM a[108:111],a[188:191],v[76:79],a[108:111]
MM a[100:103],a[188:191],v[68:71],a[100:103]
MM a[100:103],a[184:187],v[64:67],a[100:103]
v_smfmac_f32_16x16x64_bf16 v[144:147], v[236:239], v[0:7], v234
v_smfmac_f32_16x16x64_bf16 v[148:151], v[236:239], v[8:15], v234
v_smfmac_f32_16x16x64_bf16 v[152:155], v[236:239], v[16:23], v234
v_smfmac_f32_16x16x64_bf16 v[156:159], v[236:239], v[24:31], v234
s_waitcnt lgkmcnt(0)
MM v[64:67],a[192:195],v[160:163],v[128:131]
s_add_u32 s64, s64, 16384
TR a[128:129],v232 offset:0
s_add_u32 s81, s64, 896
MM v[64:67],a[196:199],v[176:179],v[64:67]
TR a[130:131],v232 offset:4096
v_max3_f32 v249, v144, v145, v148
MM v[64:67],a[200:203],v[192:195],v[64:67]
TR a[132:133],v232 offset:8192
v_max3_f32 v249, v249, v149, v152
MM v[64:67],a[204:207],v[208:211],v[64:67]
TR a[134:135],v232 offset:12288
v_max3_f32 v249, v249, v153, v156
MM v[68:71],a[220:223],v[208:211],v[128:131]
TR a[136:137],v233 offset:0
v_max_f32 v249, v249, v157
MM v[68:71],a[216:219],v[192:195],v[68:71]
TR a[138:139],v233 offset:4096
v_cmp_lt_f32 vcc, s71, v249
MM v[68:71],a[212:215],v[176:179],v[68:71]
TR a[140:141],v233 offset:8192
TR a[142:143],v233 offset:12288
MM v[68:71],a[208:211],v[160:163],v[68:71]
s_cbranch_vccnz LSITE16_%=
LBACK16_%=:
MM v[76:79],a[208:211],v[164:167],v[132:135]
EX v64,v64
MM v[76:79],a[212:215],v[180:183],v[76:79]
EX v65,v65
MM v[76:79],a[216:219],v[196:199],v[76:79]
TR a[144:145],v232 offset:512
CV v64,v64,v65
MM v[76:79],a[220:223],v[212:215],v[76:79]
EX v66,v66
MM v[72:75],a[204:207],v[212:215],v[132:135]
EX v67,v67
MM v[72:75],a[200:203],v[196:199],v[72:75]
TR a[146:147],v232 offset:4608
CV v65,v66,v67
MM v[72:75],a[196:199],v[180:183],v[72:75]
EX v68,v68
MM v[72:75],a[192:195],v[164:167],v[72:75]
TR a[148:149],v232 offset:8704
TR a[150:151],v232 offset:12800
MM v[80:83],a[192:195],v[168:171],v[136:139]
EX v69,v69
MM v[80:83],a[196:199],v[184:187],v[80:83]
TR a[152:153],v233 offset:512
CV v66,v68,v69
MM v[80:83],a[200:203],v[200:203],v[80:83]
EX v70,v70
MM v[80:83],a[204:207],v[216:219],v[80:83]
TR a[154:155],v233 offset:4608
TR a[156:157],v233 offset:8704
MM v[84:87],a[220:223],v[216:219],v[136:139]
s_add_u32 s87, s95, 1024
buffer_store_dword v240, v248, s[20:23], s87 offen offset:12 sc1
MM v[84:87],a[216:219],v[200:203],v[84:87]
EX v71,v71
MM v[84:87],a[212:215],v[184:187],v[84:87]
TR a[158:159],v233 offset:12800
CV v67,v70,v71
MM v[84:87],a[208:211],v[168:171],v[84:87]
EX v72,v72
MM v[92:95],a[208:211],v[172:175],v[140:143]
EX v73,v73
MM v[92:95],a[212:215],v[188:191],v[92:95]
TR a[160:161],v232 offset:1024
CV v72,v72,v73
MM v[92:95],a[216:219],v[204:207],v[92:95]
EX v74,v74
MM v[92:95],a[220:223],v[220:223],v[92:95]
EX v75,v75
MM v[88:91],a[204:207],v[220:223],v[140:143]
TR a[162:163],v232 offset:5120
CV v73,v74,v75
MM v[88:91],a[200:203],v[204:207],v[88:91]
TR a[164:165],v232 offset:9216
TR a[166:167],v232 offset:13312
MM v[88:91],a[196:199],v[188:191],v[88:91]
EX v76,v76
MM v[88:91],a[192:195],v[172:175],v[88:91]
EX v77,v77
MM v[96:99],a[224:227],v[160:163],v[128:131]
TR a[168:169],v233 offset:1024
CV v74,v76,v77
MM v[96:99],a[228:231],v[176:179],v[96:99]
EX v78,v78
MM v[96:99],a[232:235],v[192:195],v[96:99]
TR a[170:171],v233 offset:5120
TR a[172:173],v233 offset:9216
MM v[96:99],a[236:239],v[208:211],v[96:99]
EX v79,v79
MM v[100:103],a[252:255],v[208:211],v[128:131]
TR a[174:175],v233 offset:13312
CV v75,v78,v79
MM v[100:103],a[248:251],v[192:195],v[100:103]
EX v80,v80
MM v[100:103],a[244:247],v[176:179],v[100:103]
EX v81,v81
MM v[100:103],a[240:243],v[160:163],v[100:103]
TR a[176:177],v232 offset:1536
CV v80,v80,v81
MM v[108:111],a[240:243],v[164:167],v[132:135]
EX v82,v82
MM v[108:111],a[244:247],v[180:183],v[108:111]
TR a[178:179],v232 offset:5632
TR a[180:181],v232 offset:9728
MM v[108:111],a[248:251],v[196:199],v[108:111]
EX v83,v83
MM v[108:111],a[252:255],v[212:215],v[108:111]
TR a[182:183],v232 offset:13824
)ASM"
R"ASM(
CV v81,v82,v83
MM v[104:107],a[236:239],v[212:215],v[132:135]
EX v84,v84
MM v[104:107],a[232:235],v[196:199],v[104:107]
EX v85,v85
MM v[104:107],a[228:231],v[180:183],v[104:107]
TR a[184:185],v233 offset:1536
CV v82,v84,v85
MM v[104:107],a[224:227],v[164:167],v[104:107]
EX v86,v86
MM v[112:115],a[224:227],v[168:171],v[136:139]
TR a[186:187],v233 offset:5632
TR a[188:189],v233 offset:9728
MM v[112:115],a[228:231],v[184:187],v[112:115]
EX v87,v87
MM v[112:115],a[232:235],v[200:203],v[112:115]
TR a[190:191],v233 offset:13824
CV v83,v86,v87
MM v[112:115],a[236:239],v[216:219],v[112:115]
EX v88,v88
MM v[116:119],a[252:255],v[216:219],v[136:139]
EX v89,v89
MM v[116:119],a[248:251],v[200:203],v[116:119]
EX v90,v90
MM v[116:119],a[244:247],v[184:187],v[116:119]
EX v91,v91
MM v[116:119],a[240:243],v[168:171],v[116:119]
EX v92,v92
MM v[124:127],a[240:243],v[172:175],v[140:143]
EX v93,v93
MM v[124:127],a[244:247],v[188:191],v[124:127]
EX v94,v94
MM v[124:127],a[248:251],v[204:207],v[124:127]
EX v95,v95
MM v[124:127],a[252:255],v[220:223],v[124:127]
EX v96,v96
MM v[120:123],a[236:239],v[220:223],v[140:143]
EX v97,v97
MM v[120:123],a[232:235],v[204:207],v[120:123]
EX v98,v98
MM v[120:123],a[228:231],v[188:191],v[120:123]
EX v99,v99
MM v[120:123],a[224:227],v[172:175],v[120:123]
s_waitcnt vmcnt(19) lgkmcnt(0)
s_barrier
MM a[0:3],a[128:131],v[0:3],a[0:3]
s_add_u32 m0, s65, 0
CV v88,v88,v89
CV v89,v90,v91
MM a[0:3],a[132:135],v[4:7],a[0:3]
RD a[192:195],v224 offset:32768
CV v90,v92,v93
MM a[8:11],a[132:135],v[12:15],a[8:11]
RD a[196:199],v225 offset:32768
CV v91,v94,v95
MM a[8:11],a[128:131],v[8:11],a[8:11]
buffer_load_dwordx4 v228, s[8:11], s64 offen offset:0 lds
MM a[16:19],a[128:131],v[16:19],a[16:19]
RD a[200:203],v226 offset:32768
CV v68,v96,v97
MM a[16:19],a[132:135],v[20:23],a[16:19]
RD a[204:207],v227 offset:32768
CV v69,v98,v99
MM a[24:27],a[132:135],v[28:31],a[24:27]
buffer_load_dwordx4 v229, s[8:11], s64 offen offset:1024 lds
MM a[24:27],a[128:131],v[24:27],a[24:27]
RD a[208:211],v224 offset:36864
RD a[212:215],v225 offset:36864
MM a[28:31],a[136:139],v[24:27],a[28:31]
buffer_load_dwordx4 v230, s[8:11], s64 offen offset:2048 lds
MM a[28:31],a[140:143],v[28:31],a[28:31]
EX v100,v100
MM a[20:23],a[140:143],v[20:23],a[20:23]
EX v101,v101
MM a[20:23],a[136:139],v[16:19],a[20:23]
RD a[216:219],v226 offset:36864
CV v70,v100,v101
MM a[12:15],a[136:139],v[8:11],a[12:15]
EX v102,v102
MM a[12:15],a[140:143],v[12:15],a[12:15]
RD a[220:223],v227 offset:36864
RD a[224:227],v224 offset:40960
MM a[4:7],a[140:143],v[4:7],a[4:7]
EX v103,v103
MM a[4:7],a[136:139],v[0:3],a[4:7]
RD a[228:231],v225 offset:40960
CV v71,v102,v103
MM a[32:35],a[144:147],v[0:3],a[32:35]
EX v104,v104
MM a[32:35],a[148:151],v[4:7],a[32:35]
buffer_load_dwordx4 v231, s[8:11], s64 offen offset:3072 lds
MM a[40:43],a[148:151],v[12:15],a[40:43]
s_add_u32 m0, s65, 65536
EX v105,v105
MM a[40:43],a[144:147],v[8:11],a[40:43]
RD a[232:235],v226 offset:40960
CV v76,v104,v105
MM a[48:51],a[144:147],v[16:19],a[48:51]
EX v106,v106
MM a[48:51],a[148:151],v[20:23],a[48:51]
EX v107,v107
MM a[56:59],a[148:151],v[28:31],a[56:59]
RD a[236:239],v227 offset:40960
CV v77,v106,v107
MM a[56:59],a[144:147],v[24:27],a[56:59]
EX v108,v108
MM a[60:63],a[152:155],v[24:27],a[60:63]
buffer_load_dwordx4 v235, s[12:15], s81 offen offset:0 lds
MM a[60:63],a[156:159],v[28:31],a[60:63]
EX v109,v109
MM a[52:55],a[156:159],v[20:23],a[52:55]
s_add_u32 s87, s95, 2048
buffer_store_dword v240, v248, s[20:23], s87 offen offset:12 sc1
MM a[52:55],a[152:155],v[16:19],a[52:55]
RD a[240:243],v224 offset:45056
CV v78,v108,v109
MM a[44:47],a[152:155],v[8:11],a[44:47]
EX v110,v110
MM a[44:47],a[156:159],v[12:15],a[44:47]
EX v111,v111
MM a[36:39],a[156:159],v[4:7],a[36:39]
RD a[244:247],v225 offset:45056
CV v79,v110,v111
LPG14_%=:
s_cbranch_execz LTR2_%=
MM a[36:39],a[152:155],v[0:3],a[36:39]
EX v112,v112
MM a[64:67],a[160:163],v[0:3],a[64:67]
buffer_load_dwordx4 v235, s[12:15], s64 offen offset:1024 lds
MM a[64:67],a[164:167],v[4:7],a[64:67]
EX v113,v113
MM a[72:75],a[164:167],v[12:15],a[72:75]
RD a[248:251],v226 offset:45056
CV v84,v112,v113
MM a[72:75],a[160:163],v[8:11],a[72:75]
EX v114,v114
MM a[80:83],a[160:163],v[16:19],a[80:83]
EX v115,v115
MM a[80:83],a[164:167],v[20:23],a[80:83]
RD a[252:255],v227 offset:45056
CV v85,v114,v115
MM a[88:91],a[164:167],v[28:31],a[88:91]
EX v116,v116
MM a[88:91],a[160:163],v[24:27],a[88:91]
buffer_load_dwordx4 v235, s[12:15], s81 offen offset:2048 lds
MM a[92:95],a[168:171],v[24:27],a[92:95]
EX v117,v117
MM a[92:95],a[172:175],v[28:31],a[92:95]
EX v118,v118
MM a[84:87],a[172:175],v[20:23],a[84:87]
buffer_load_dwordx4 v235, s[12:15], s64 offen offset:3072 lds
MM a[84:87],a[168:171],v[16:19],a[84:87]
EX v119,v119
MM a[76:79],a[168:171],v[8:11],a[76:79]
EX v120,v120
MM a[76:79],a[172:175],v[12:15],a[76:79]
EX v121,v121
MM a[68:71],a[172:175],v[4:7],a[68:71]
EX v122,v122
MM a[68:71],a[168:171],v[0:3],a[68:71]
EX v123,v123
MM a[96:99],a[176:179],v[0:3],a[96:99]
EX v124,v124
MM a[96:99],a[180:183],v[4:7],a[96:99]
EX v125,v125
MM a[104:107],a[180:183],v[12:15],a[104:107]
EX v126,v126
MM a[104:107],a[176:179],v[8:11],a[104:107]
EX v127,v127
MM a[112:115],a[176:179],v[16:19],a[112:115]
CV v86,v116,v117
CV v87,v118,v119
MM a[112:115],a[180:183],v[20:23],a[112:115]
CV v92,v120,v121
CV v93,v122,v123
MM a[120:123],a[180:183],v[28:31],a[120:123]
CV v94,v124,v125
CV v95,v126,v127
MM a[120:123],a[176:179],v[24:27],a[120:123]
MM a[124:127],a[184:187],v[24:27],a[124:127]
v_add_f32 v244, v144, v145
v_add_f32 v245, v148, v149
MM a[124:127],a[188:191],v[28:31],a[124:127]
v_add_f32 v246, v152, v153
v_add_f32 v247, v156, v157
MM a[116:119],a[188:191],v[20:23],a[116:119]
MM a[116:119],a[184:187],v[16:19],a[116:119]
MM a[108:111],a[184:187],v[8:11],a[108:111]
MM a[108:111],a[188:191],v[12:15],a[108:111]
MM a[100:103],a[188:191],v[4:7],a[100:103]
MM a[100:103],a[184:187],v[0:3],a[100:103]
v_smfmac_f32_16x16x64_bf16 v[144:147], v[236:239], v[64:71], v234
v_smfmac_f32_16x16x64_bf16 v[148:151], v[236:239], v[72:79], v234
v_smfmac_f32_16x16x64_bf16 v[152:155], v[236:239], v[80:87], v234
v_smfmac_f32_16x16x64_bf16 v[156:159], v[236:239], v[88:95], v234
s_waitcnt lgkmcnt(0)
MM v[0:3],a[192:195],v[160:163],v[128:131]
s_add_u32 s64, s64, 16384
TR a[128:129],v232 offset:16384
s_add_u32 s81, s64, 896
MM v[0:3],a[196:199],v[176:179],v[0:3]
TR a[130:131],v232 offset:20480
v_max3_f32 v249, v144, v145, v148
MM v[0:3],a[200:203],v[192:195],v[0:3]
TR a[132:133],v232 offset:24576
v_max3_f32 v249, v249, v149, v152
MM v[0:3],a[204:207],v[208:211],v[0:3]
TR a[134:135],v232 offset:28672
v_max3_f32 v249, v249, v153, v156
MM v[4:7],a[220:223],v[208:211],v[128:131]
TR a[136:137],v233 offset:16384
v_max_f32 v249, v249, v157
MM v[4:7],a[216:219],v[192:195],v[4:7]
TR a[138:139],v233 offset:20480
v_cmp_lt_f32 vcc, s71, v249
MM v[4:7],a[212:215],v[176:179],v[4:7]
TR a[140:141],v233 offset:24576
TR a[142:143],v233 offset:28672
MM v[4:7],a[208:211],v[160:163],v[4:7]
s_cbranch_vccnz LSITE17_%=
LBACK17_%=:
MM v[12:15],a[208:211],v[164:167],v[132:135]
v_lshrrev_b32 v127, 8, v248
v_lshlrev_b32 v124, 9, v127
MM v[12:15],a[212:215],v[180:183],v[12:15]
v_and_b32 v126, 192, v248
v_add_u32 v124, v124, v126
MM v[12:15],a[216:219],v[196:199],v[12:15]
v_bfe_u32 v126, v248, 4, 2
v_lshl_add_u32 v124, v126, 3, v124
MM v[12:15],a[220:223],v[212:215],v[12:15]
s_lshr_b32 s76, %[swave], 1
EX v0,v0
MM v[8:11],a[204:207],v[212:215],v[132:135]
s_mul_i32 s76, s76, 10240
EX v1,v1
MM v[8:11],a[200:203],v[196:199],v[8:11]
s_and_b32 s77, %[swave], 1
EX v2,v2
MM v[8:11],a[196:199],v[180:183],v[8:11]
s_lshl_b32 s77, s77, 5
TR a[144:145],v232 offset:16896
CV v0,v0,v1
MM v[8:11],a[192:195],v[164:167],v[8:11]
s_add_u32 s76, s76, s77
EX v3,v3
MM v[16:19],a[192:195],v[168:171],v[136:139]
s_add_u32 s76, s76, %[slds]
EX v4,v4
MM v[16:19],a[196:199],v[184:187],v[16:19]
s_add_u32 s76, s76, 131072
v_add_u32 v124, s76, v124
v_bfe_u32 v126, v248, 4, 3
MM v[16:19],a[200:203],v[200:203],v[16:19]
v_lshl_add_u32 v126, v127, 3, v126
v_lshlrev_b32 v126, 4, v126
MM v[16:19],a[204:207],v[216:219],v[16:19]
s_mul_i32 s76, %[swave], 3072
EX v5,v5
MM v[20:23],a[220:223],v[216:219],v[136:139]
s_add_u32 s76, s76, %[slds]
TR a[146:147],v232 offset:20992
CV v1,v2,v3
MM v[20:23],a[216:219],v[200:203],v[20:23]
s_add_u32 s76, s76, 151552
TR a[148:149],v232 offset:25088
v_add_u32 v125, s76, v126
MM v[20:23],a[212:215],v[184:187],v[20:23]
s_lshr_b32 s76, s48, 7
EX v6,v6
MM v[20:23],a[208:211],v[168:171],v[20:23]
s_lshl_b32 s76, s76, 5
EX v7,v7
MM v[28:31],a[208:211],v[172:175],v[140:143]
s_and_b32 s77, s48, 16
TR a[150:151],v232 offset:29184
TR a[152:153],v233 offset:16896
MM v[28:31],a[212:215],v[188:191],v[28:31]
s_add_u32 s76, s76, s77
EX v8,v8
MM v[28:31],a[216:219],v[204:207],v[28:31]
s_bfe_u32 s77, s48, 131077
TR a[154:155],v233 offset:20992
CV v2,v4,v5
MM v[28:31],a[220:223],v[220:223],v[28:31]
s_lshl_b32 s77, s77, 2
EX v9,v9
MM v[24:27],a[204:207],v[220:223],v[140:143]
s_add_u32 s76, s76, s77
TR a[156:157],v233 offset:25088
CV v3,v6,v7
MM v[24:27],a[200:203],v[204:207],v[24:27]
s_add_u32 s76, s76, %[swave]
EX v10,v10
MM v[24:27],a[196:199],v[188:191],v[24:27]
s_lshl_b32 s97, s76, 2
TR a[158:159],v233 offset:29184
CV v8,v8,v9
MM v[24:27],a[192:195],v[172:175],v[24:27]
s_add_u32 s97, s97, 77824
EX v11,v11
MM v[32:35],a[224:227],v[160:163],v[128:131]
s_lshl_b32 s76, s76, 9
TR a[160:161],v232 offset:17408
TR a[162:163],v232 offset:21504
MM v[32:35],a[228:231],v[176:179],v[32:35]
s_add_u32 s76, s76, 22183936
EX v12,v12
MM v[32:35],a[232:235],v[192:195],v[32:35]
EX v13,v13
MM v[32:35],a[236:239],v[208:211],v[32:35]
TR a[164:165],v232 offset:25600
CV v9,v10,v11
MM v[36:39],a[252:255],v[208:211],v[128:131]
EX v14,v14
MM v[36:39],a[248:251],v[192:195],v[36:39]
TR a[166:167],v232 offset:29696
CV v10,v12,v13
MM v[36:39],a[244:247],v[176:179],v[36:39]
EX v15,v15
MM v[36:39],a[240:243],v[160:163],v[36:39]
TR a[168:169],v233 offset:17408
TR a[170:171],v233 offset:21504
MM v[44:47],a[240:243],v[164:167],v[132:135]
EX v16,v16
MM v[44:47],a[244:247],v[180:183],v[44:47]
EX v17,v17
MM v[44:47],a[248:251],v[196:199],v[44:47]
TR a[172:173],v233 offset:25600
CV v11,v14,v15
MM v[44:47],a[252:255],v[212:215],v[44:47]
EX v18,v18
MM v[40:43],a[236:239],v[212:215],v[132:135]
TR a[174:175],v233 offset:29696
CV v16,v16,v17
MM v[40:43],a[232:235],v[196:199],v[40:43]
EX v19,v19
MM v[40:43],a[228:231],v[180:183],v[40:43]
TR a[176:177],v232 offset:17920
TR a[178:179],v232 offset:22016
MM v[40:43],a[224:227],v[164:167],v[40:43]
EX v20,v20
MM v[48:51],a[224:227],v[168:171],v[136:139]
EX v21,v21
MM v[48:51],a[228:231],v[184:187],v[48:51]
TR a[180:181],v232 offset:26112
CV v17,v18,v19
MM v[48:51],a[232:235],v[200:203],v[48:51]
EX v22,v22
MM v[48:51],a[236:239],v[216:219],v[48:51]
TR a[182:183],v232 offset:30208
CV v18,v20,v21
MM v[52:55],a[252:255],v[216:219],v[136:139]
EX v23,v23
MM v[52:55],a[248:251],v[200:203],v[52:55]
TR a[184:185],v233 offset:17920
TR a[186:187],v233 offset:22016
MM v[52:55],a[244:247],v[184:187],v[52:55]
EX v24,v24
MM v[52:55],a[240:243],v[168:171],v[52:55]
TR a[188:189],v233 offset:26112
CV v19,v22,v23
MM v[60:63],a[240:243],v[172:175],v[140:143]
EX v25,v25
MM v[60:63],a[244:247],v[188:191],v[60:63]
TR a[190:191],v233 offset:30208
CV v24,v24,v25
MM v[60:63],a[248:251],v[204:207],v[60:63]
EX v26,v26
MM v[60:63],a[252:255],v[220:223],v[60:63]
EX v27,v27
MM v[56:59],a[236:239],v[220:223],v[140:143]
EX v28,v28
MM v[56:59],a[232:235],v[204:207],v[56:59]
EX v29,v29
MM v[56:59],a[228:231],v[188:191],v[56:59]
EX v30,v30
MM v[56:59],a[224:227],v[172:175],v[56:59]
s_waitcnt vmcnt(10) lgkmcnt(0)
s_barrier
MM a[0:3],a[128:131],v[64:67],a[0:3]
s_add_u32 m0, s65, 16384
CV v25,v26,v27
CV v26,v28,v29
MM a[0:3],a[132:135],v[68:71],a[0:3]
RD a[192:195],v224 offset:49152
RD a[196:199],v225 offset:49152
MM a[8:11],a[132:135],v[76:79],a[8:11]
EX v31,v31
MM a[8:11],a[128:131],v[72:75],a[8:11]
RD a[200:203],v226 offset:49152
CV v27,v30,v31
MM a[16:19],a[128:131],v[80:83],a[16:19]
)ASM"
R"ASM(
TR v[96:97],v124 offset:0
TR v[98:99],v124 offset:256
MM a[16:19],a[132:135],v[84:87],a[16:19]
RD v[108:111],v125 offset:0
TR v[100:101],v124 offset:2048
MM a[24:27],a[132:135],v[92:95],a[24:27]
TR v[102:103],v124 offset:2304
RD v[112:115],v125 offset:512
MM a[24:27],a[128:131],v[88:91],a[24:27]
TR v[104:105],v124 offset:4096
TR v[106:107],v124 offset:4352
MM a[28:31],a[136:139],v[88:91],a[28:31]
RD v[116:119],v125 offset:1024
RD a[204:207],v227 offset:49152
EX v32,v32
MM a[28:31],a[140:143],v[92:95],a[28:31]
EX v33,v33
MM a[20:23],a[140:143],v[84:87],a[20:23]
EX v34,v34
MM a[20:23],a[136:139],v[80:83],a[20:23]
buffer_load_dwordx4 v228, s[8:11], s64 offen offset:0 lds
MM a[12:15],a[136:139],v[72:75],a[12:15]
EX v35,v35
MM a[12:15],a[140:143],v[76:79],a[12:15]
EX v36,v36
MM a[4:7],a[140:143],v[68:71],a[4:7]
CV v4,v32,v33
CV v5,v34,v35
MM a[4:7],a[136:139],v[64:67],a[4:7]
EX v37,v37
MM a[32:35],a[144:147],v[64:67],a[32:35]
EX v38,v38
MM a[32:35],a[148:151],v[68:71],a[32:35]
s_waitcnt lgkmcnt(1)
buffer_load_dwordx4 v229, s[8:11], s64 offen offset:1024 lds
MM a[40:43],a[148:151],v[76:79],a[40:43]
EX v39,v39
MM v[120:123],v[96:99],v[108:111],0
MM v[120:123],v[100:103],v[112:115],v[120:123]
MM v[120:123],v[104:107],v[116:119],v[120:123]
MM a[40:43],a[144:147],v[72:75],a[40:43]
TR v[96:97],v124 offset:6144
TR v[98:99],v124 offset:6400
MM a[48:51],a[144:147],v[80:83],a[48:51]
RD v[108:111],v125 offset:1536
TR v[100:101],v124 offset:8192
MM a[48:51],a[148:151],v[84:87],a[48:51]
TR v[102:103],v124 offset:8448
RD v[112:115],v125 offset:2048
MM a[56:59],a[148:151],v[92:95],a[56:59]
EX v40,v40
MM a[56:59],a[144:147],v[88:91],a[56:59]
EX v41,v41
MM a[60:63],a[152:155],v[88:91],a[60:63]
CV v6,v36,v37
CV v7,v38,v39
MM a[60:63],a[156:159],v[92:95],a[60:63]
EX v42,v42
MM a[52:55],a[156:159],v[84:87],a[52:55]
RD a[208:211],v224 offset:53248
RD a[212:215],v225 offset:53248
MM a[52:55],a[152:155],v[80:83],a[52:55]
EX v43,v43
MM a[44:47],a[152:155],v[72:75],a[44:47]
buffer_load_dwordx4 v230, s[8:11], s64 offen offset:2048 lds
MM a[44:47],a[156:159],v[76:79],a[44:47]
EX v44,v44
MM a[36:39],a[156:159],v[68:71],a[36:39]
s_waitcnt lgkmcnt(2)
EX v45,v45
MM a[36:39],a[152:155],v[64:67],a[36:39]
CV v12,v40,v41
CV v13,v42,v43
MM v[120:123],v[96:99],v[108:111],v[120:123]
MM v[120:123],v[100:103],v[112:115],v[120:123]
MM a[64:67],a[160:163],v[64:67],a[64:67]
EX v46,v46
MM a[64:67],a[164:167],v[68:71],a[64:67]
RD a[216:219],v226 offset:53248
CV v14,v44,v45
MM a[72:75],a[164:167],v[76:79],a[72:75]
EX v47,v47
MM a[72:75],a[160:163],v[72:75],a[72:75]
buffer_load_dwordx4 v231, s[8:11], s64 offen offset:3072 lds
MM a[80:83],a[160:163],v[80:83],a[80:83]
s_add_u32 m0, s65, 81920
EX v48,v48
MM a[80:83],a[164:167],v[84:87],a[80:83]
RD a[220:223],v227 offset:53248
CV v15,v46,v47
MM a[88:91],a[164:167],v[92:95],a[88:91]
s_mov_b32 exec_lo, 0x00ff00ff
s_mov_b32 exec_hi, 0x00ff00ff
buffer_store_dwordx4 v[120:123], v126, s[20:23], s76 offen offset:0 sc1
s_mov_b64 exec, -1
s_nop 0
MM a[88:91],a[160:163],v[88:91],a[88:91]
EX v49,v49
MM a[92:95],a[168:171],v[88:91],a[92:95]
EX v50,v50
MM a[92:95],a[172:175],v[92:95],a[92:95]
EX v51,v51
MM a[84:87],a[172:175],v[84:87],a[84:87]
RD a[224:227],v224 offset:57344
CV v20,v48,v49
MM a[84:87],a[168:171],v[80:83],a[84:87]
EX v52,v52
MM a[76:79],a[168:171],v[72:75],a[76:79]
buffer_load_dwordx4 v235, s[12:15], s81 offen offset:0 lds
MM a[76:79],a[172:175],v[76:79],a[76:79]
EX v53,v53
MM a[68:71],a[172:175],v[68:71],a[68:71]
RD a[228:231],v225 offset:57344
CV v21,v50,v51
MM a[68:71],a[168:171],v[64:67],a[68:71]
EX v54,v54
MM a[96:99],a[176:179],v[64:67],a[96:99]
RD a[232:235],v226 offset:57344
CV v22,v52,v53
MM a[96:99],a[180:183],v[68:71],a[96:99]
EX v55,v55
MM a[104:107],a[180:183],v[76:79],a[104:107]
EX v56,v56
MM a[104:107],a[176:179],v[72:75],a[104:107]
buffer_load_dwordx4 v235, s[12:15], s64 offen offset:1024 lds
MM a[112:115],a[176:179],v[80:83],a[112:115]
EX v57,v57
MM a[112:115],a[180:183],v[84:87],a[112:115]
RD a[236:239],v227 offset:57344
CV v23,v54,v55
MM a[120:123],a[180:183],v[92:95],a[120:123]
EX v58,v58
MM a[120:123],a[176:179],v[88:91],a[120:123]
RD a[240:243],v224 offset:61440
CV v28,v56,v57
MM a[124:127],a[184:187],v[88:91],a[124:127]
v_add_f32 v244, v144, v145
v_add_f32 v245, v148, v149
MM a[124:127],a[188:191],v[92:95],a[124:127]
v_add_f32 v246, v152, v153
v_add_f32 v247, v156, v157
MM a[116:119],a[188:191],v[84:87],a[116:119]
EX v59,v59
MM a[116:119],a[184:187],v[80:83],a[116:119]
EX v60,v60
MM a[108:111],a[184:187],v[72:75],a[108:111]
EX v61,v61
MM a[108:111],a[188:191],v[76:79],a[108:111]
buffer_load_dwordx4 v235, s[12:15], s81 offen offset:2048 lds
MM a[100:103],a[188:191],v[68:71],a[100:103]
EX v62,v62
MM a[100:103],a[184:187],v[64:67],a[100:103]
RD a[244:247],v225 offset:61440
CV v29,v58,v59
v_smfmac_f32_16x16x64_bf16 v[144:147], v[236:239], v[0:7], v234
RD a[248:251],v226 offset:61440
RD a[252:255],v227 offset:61440
EX v63,v63
v_smfmac_f32_16x16x64_bf16 v[148:151], v[236:239], v[8:15], v234
CV v30,v60,v61
v_smfmac_f32_16x16x64_bf16 v[152:155], v[236:239], v[16:23], v234
CV v31,v62,v63
s_nop 1
v_smfmac_f32_16x16x64_bf16 v[156:159], v[236:239], v[24:31], v234
s_waitcnt lgkmcnt(0)
buffer_load_dwordx4 v235, s[12:15], s64 offen offset:3072 lds
MM v[64:67],a[192:195],v[160:163],v[128:131]
s_add_u32 s64, s64, 16384
TR a[128:129],v232 offset:32768
s_add_u32 s81, s64, 896
MM v[64:67],a[196:199],v[176:179],v[64:67]
TR a[130:131],v232 offset:36864
v_max3_f32 v249, v144, v145, v148
MM v[64:67],a[200:203],v[192:195],v[64:67]
TR a[132:133],v232 offset:40960
v_max3_f32 v249, v249, v149, v152
MM v[64:67],a[204:207],v[208:211],v[64:67]
TR a[134:135],v232 offset:45056
v_max3_f32 v249, v249, v153, v156
MM v[68:71],a[220:223],v[208:211],v[128:131]
TR a[136:137],v233 offset:32768
v_max_f32 v249, v249, v157
MM v[68:71],a[216:219],v[192:195],v[68:71]
TR a[138:139],v233 offset:36864
v_cmp_lt_f32 vcc, s71, v249
MM v[68:71],a[212:215],v[176:179],v[68:71]
TR a[140:141],v233 offset:40960
TR a[142:143],v233 offset:45056
MM v[68:71],a[208:211],v[160:163],v[68:71]
s_cbranch_vccnz LSITE18_%=
LBACK18_%=:
MM v[76:79],a[208:211],v[164:167],v[132:135]
EX v64,v64
MM v[76:79],a[212:215],v[180:183],v[76:79]
EX v65,v65
MM v[76:79],a[216:219],v[196:199],v[76:79]
TR a[144:145],v232 offset:33280
CV v64,v64,v65
MM v[76:79],a[220:223],v[212:215],v[76:79]
EX v66,v66
MM v[72:75],a[204:207],v[212:215],v[132:135]
EX v67,v67
MM v[72:75],a[200:203],v[196:199],v[72:75]
TR a[146:147],v232 offset:37376
CV v65,v66,v67
MM v[72:75],a[196:199],v[180:183],v[72:75]
EX v68,v68
MM v[72:75],a[192:195],v[164:167],v[72:75]
TR a[148:149],v232 offset:41472
TR a[150:151],v232 offset:45568
MM v[80:83],a[192:195],v[168:171],v[136:139]
EX v69,v69
MM v[80:83],a[196:199],v[184:187],v[80:83]
TR a[152:153],v233 offset:33280
CV v66,v68,v69
MM v[80:83],a[200:203],v[200:203],v[80:83]
EX v70,v70
MM v[80:83],a[204:207],v[216:219],v[80:83]
TR a[154:155],v233 offset:37376
TR a[156:157],v233 offset:41472
MM v[84:87],a[220:223],v[216:219],v[136:139]
EX v71,v71
MM v[84:87],a[216:219],v[200:203],v[84:87]
TR a[158:159],v233 offset:45568
CV v67,v70,v71
MM v[84:87],a[212:215],v[184:187],v[84:87]
EX v72,v72
MM v[84:87],a[208:211],v[168:171],v[84:87]
EX v73,v73
MM v[92:95],a[208:211],v[172:175],v[140:143]
TR a[160:161],v232 offset:33792
CV v72,v72,v73
MM v[92:95],a[212:215],v[188:191],v[92:95]
EX v74,v74
MM v[92:95],a[216:219],v[204:207],v[92:95]
TR a[162:163],v232 offset:37888
TR a[164:165],v232 offset:41984
MM v[92:95],a[220:223],v[220:223],v[92:95]
EX v75,v75
MM v[88:91],a[204:207],v[220:223],v[140:143]
TR a[166:167],v232 offset:46080
CV v73,v74,v75
MM v[88:91],a[200:203],v[204:207],v[88:91]
EX v76,v76
MM v[88:91],a[196:199],v[188:191],v[88:91]
EX v77,v77
MM v[88:91],a[192:195],v[172:175],v[88:91]
TR a[168:169],v233 offset:33792
CV v74,v76,v77
MM v[96:99],a[224:227],v[160:163],v[128:131]
EX v78,v78
MM v[96:99],a[228:231],v[176:179],v[96:99]
TR a[170:171],v233 offset:37888
TR a[172:173],v233 offset:41984
MM v[96:99],a[232:235],v[192:195],v[96:99]
EX v79,v79
MM v[96:99],a[236:239],v[208:211],v[96:99]
TR a[174:175],v233 offset:46080
CV v75,v78,v79
MM v[100:103],a[252:255],v[208:211],v[128:131]
EX v80,v80
MM v[100:103],a[248:251],v[192:195],v[100:103]
EX v81,v81
MM v[100:103],a[244:247],v[176:179],v[100:103]
TR a[176:177],v232 offset:34304
CV v80,v80,v81
MM v[100:103],a[240:243],v[160:163],v[100:103]
EX v82,v82
MM v[108:111],a[240:243],v[164:167],v[132:135]
v_lshrrev_b32 v253, 7, v248
v_mul_u32_u24 v253, 1280, v253
MM v[108:111],a[244:247],v[180:183],v[108:111]
v_and_b32 v252, 112, v248
v_add_u32 v253, v253, v252
MM v[108:111],a[248:251],v[196:199],v[108:111]
s_mul_i32 s90, %[swave], 61440
TR a[178:179],v232 offset:38400
TR a[180:181],v232 offset:42496
MM v[108:111],a[252:255],v[212:215],v[108:111]
s_mul_i32 s91, %[swave], 6144
EX v83,v83
MM v[104:107],a[236:239],v[212:215],v[132:135]
s_add_u32 s91, s91, %[slds]
TR a[182:183],v232 offset:46592
CV v81,v82,v83
MM v[104:107],a[232:235],v[196:199],v[104:107]
EX v84,v84
MM v[104:107],a[228:231],v[180:183],v[104:107]
EX v85,v85
MM v[104:107],a[224:227],v[164:167],v[104:107]
TR a[184:185],v233 offset:34304
CV v82,v84,v85
MM v[112:115],a[224:227],v[168:171],v[136:139]
EX v86,v86
MM v[112:115],a[228:231],v[184:187],v[112:115]
TR a[186:187],v233 offset:38400
TR a[188:189],v233 offset:42496
MM v[112:115],a[232:235],v[200:203],v[112:115]
EX v87,v87
MM v[112:115],a[236:239],v[216:219],v[112:115]
TR a[190:191],v233 offset:46592
CV v83,v86,v87
MM v[116:119],a[252:255],v[216:219],v[136:139]
EX v88,v88
MM v[116:119],a[248:251],v[200:203],v[116:119]
EX v89,v89
MM v[116:119],a[244:247],v[184:187],v[116:119]
EX v90,v90
MM v[116:119],a[240:243],v[168:171],v[116:119]
EX v91,v91
MM v[124:127],a[240:243],v[172:175],v[140:143]
EX v92,v92
MM v[124:127],a[244:247],v[188:191],v[124:127]
EX v93,v93
MM v[124:127],a[248:251],v[204:207],v[124:127]
EX v94,v94
MM v[124:127],a[252:255],v[220:223],v[124:127]
EX v95,v95
MM v[120:123],a[236:239],v[220:223],v[140:143]
EX v96,v96
MM v[120:123],a[232:235],v[204:207],v[120:123]
EX v97,v97
MM v[120:123],a[228:231],v[188:191],v[120:123]
EX v98,v98
MM v[120:123],a[224:227],v[172:175],v[120:123]
s_waitcnt vmcnt(14) lgkmcnt(0)
s_barrier
s_nop 0
LPG15_%=:
s_cbranch_execz LTR2_%=
MM a[0:3],a[128:131],v[0:3],a[0:3]
s_add_u32 m0, s65, 32768
CV v88,v88,v89
CV v89,v90,v91
MM a[0:3],a[132:135],v[4:7],a[0:3]
RD a[192:195],v224 offset:0
CV v90,v92,v93
MM a[8:11],a[132:135],v[12:15],a[8:11]
RD a[196:199],v225 offset:0
CV v91,v94,v95
MM a[8:11],a[128:131],v[8:11],a[8:11]
buffer_load_dwordx4 v228, s[8:11], s64 offen offset:0 lds
MM a[16:19],a[128:131],v[16:19],a[16:19]
RD a[200:203],v226 offset:0
CV v68,v96,v97
MM a[16:19],a[132:135],v[20:23],a[16:19]
buffer_load_dwordx4 v229, s[8:11], s64 offen offset:1024 lds
MM a[24:27],a[132:135],v[28:31],a[24:27]
RD a[204:207],v227 offset:0
RD a[208:211],v224 offset:4096
MM a[24:27],a[128:131],v[24:27],a[24:27]
EX v99,v99
MM a[28:31],a[136:139],v[24:27],a[28:31]
RD a[212:215],v225 offset:4096
CV v69,v98,v99
MM a[28:31],a[140:143],v[28:31],a[28:31]
EX v100,v100
MM a[20:23],a[140:143],v[20:23],a[20:23]
buffer_load_dwordx4 v230, s[8:11], s64 offen offset:2048 lds
MM a[20:23],a[136:139],v[16:19],a[20:23]
EX v101,v101
MM a[12:15],a[136:139],v[8:11],a[12:15]
RD a[216:219],v226 offset:4096
CV v70,v100,v101
MM a[12:15],a[140:143],v[12:15],a[12:15]
EX v102,v102
MM a[4:7],a[140:143],v[4:7],a[4:7]
EX v103,v103
MM a[4:7],a[136:139],v[0:3],a[4:7]
RD a[220:223],v227 offset:4096
CV v71,v102,v103
MM a[32:35],a[144:147],v[0:3],a[32:35]
EX v104,v104
MM a[32:35],a[148:151],v[4:7],a[32:35]
buffer_load_dwordx4 v231, s[8:11], s64 offen offset:3072 lds
MM a[40:43],a[148:151],v[12:15],a[40:43]
s_add_u32 m0, s65, 98304
EX v105,v105
MM a[40:43],a[144:147],v[8:11],a[40:43]
RD a[224:227],v224 offset:8192
CV v76,v104,v105
MM a[48:51],a[144:147],v[16:19],a[48:51]
EX v106,v106
MM a[48:51],a[148:151],v[20:23],a[48:51]
EX v107,v107
MM a[56:59],a[148:151],v[28:31],a[56:59]
RD a[228:231],v225 offset:8192
)ASM"
R"ASM(
CV v77,v106,v107
MM a[56:59],a[144:147],v[24:27],a[56:59]
EX v108,v108
MM a[60:63],a[152:155],v[24:27],a[60:63]
buffer_load_dwordx4 v235, s[12:15], s81 offen offset:0 lds
MM a[60:63],a[156:159],v[28:31],a[60:63]
EX v109,v109
MM a[52:55],a[156:159],v[20:23],a[52:55]
RD a[232:235],v226 offset:8192
CV v78,v108,v109
MM a[52:55],a[152:155],v[16:19],a[52:55]
EX v110,v110
MM a[44:47],a[152:155],v[8:11],a[44:47]
EX v111,v111
MM a[44:47],a[156:159],v[12:15],a[44:47]
RD a[236:239],v227 offset:8192
CV v79,v110,v111
MM a[36:39],a[156:159],v[4:7],a[36:39]
buffer_load_dwordx4 v235, s[12:15], s64 offen offset:1024 lds
MM a[36:39],a[152:155],v[0:3],a[36:39]
EX v112,v112
MM a[64:67],a[160:163],v[0:3],a[64:67]
EX v113,v113
MM a[64:67],a[164:167],v[4:7],a[64:67]
RD a[240:243],v224 offset:12288
CV v84,v112,v113
MM a[72:75],a[164:167],v[12:15],a[72:75]
EX v114,v114
MM a[72:75],a[160:163],v[8:11],a[72:75]
RD a[244:247],v225 offset:12288
RD a[248:251],v226 offset:12288
MM a[80:83],a[160:163],v[16:19],a[80:83]
s_waitcnt vmcnt(10)
v_mov_b32 v250, s69
s_mov_b64 exec, 1
buffer_store_dword v250, off, s[20:23], s97 offset:0 sc1
s_mov_b64 exec, -1
MM a[80:83],a[164:167],v[20:23],a[80:83]
s_add_u32 m0, s91, 139264
s_add_u32 s77, s90, 0
buffer_load_dwordx4 v253, s[28:31], s77 offen offset:0 lds
MM a[88:91],a[164:167],v[28:31],a[88:91]
EX v115,v115
MM a[88:91],a[160:163],v[24:27],a[88:91]
RD a[252:255],v227 offset:12288
CV v85,v114,v115
MM a[92:95],a[168:171],v[24:27],a[92:95]
EX v116,v116
MM a[92:95],a[172:175],v[28:31],a[92:95]
EX v117,v117
MM a[84:87],a[172:175],v[20:23],a[84:87]
s_add_u32 m0, s65, 98304
s_nop 0
buffer_load_dwordx4 v235, s[12:15], s81 offen offset:2048 lds
MM a[84:87],a[168:171],v[16:19],a[84:87]
EX v118,v118
MM a[76:79],a[168:171],v[8:11],a[76:79]
EX v119,v119
MM a[76:79],a[172:175],v[12:15],a[76:79]
EX v120,v120
MM a[68:71],a[172:175],v[4:7],a[68:71]
buffer_load_dwordx4 v235, s[12:15], s64 offen offset:3072 lds
MM a[68:71],a[168:171],v[0:3],a[68:71]
EX v121,v121
MM a[96:99],a[176:179],v[0:3],a[96:99]
EX v122,v122
MM a[96:99],a[180:183],v[4:7],a[96:99]
EX v123,v123
MM a[104:107],a[180:183],v[12:15],a[104:107]
EX v124,v124
MM a[104:107],a[176:179],v[8:11],a[104:107]
EX v125,v125
MM a[112:115],a[176:179],v[16:19],a[112:115]
EX v126,v126
MM a[112:115],a[180:183],v[20:23],a[112:115]
EX v127,v127
MM a[120:123],a[180:183],v[28:31],a[120:123]
CV v86,v116,v117
CV v87,v118,v119
MM a[120:123],a[176:179],v[24:27],a[120:123]
CV v92,v120,v121
CV v93,v122,v123
MM a[124:127],a[184:187],v[24:27],a[124:127]
v_add_f32 v244, v144, v145
v_add_f32 v245, v148, v149
MM a[124:127],a[188:191],v[28:31],a[124:127]
v_add_f32 v246, v152, v153
v_add_f32 v247, v156, v157
MM a[116:119],a[188:191],v[20:23],a[116:119]
CV v94,v124,v125
CV v95,v126,v127
MM a[116:119],a[184:187],v[16:19],a[116:119]
MM a[108:111],a[184:187],v[8:11],a[108:111]
MM a[108:111],a[188:191],v[12:15],a[108:111]
MM a[100:103],a[188:191],v[4:7],a[100:103]
MM a[100:103],a[184:187],v[0:3],a[100:103]
v_smfmac_f32_16x16x64_bf16 v[144:147], v[236:239], v[64:71], v234
v_smfmac_f32_16x16x64_bf16 v[148:151], v[236:239], v[72:79], v234
v_smfmac_f32_16x16x64_bf16 v[152:155], v[236:239], v[80:87], v234
v_smfmac_f32_16x16x64_bf16 v[156:159], v[236:239], v[88:95], v234
s_waitcnt lgkmcnt(0)
MM v[0:3],a[192:195],v[160:163],v[128:131]
s_add_u32 s64, s64, 16384
TR a[128:129],v232 offset:49152
s_add_u32 s81, s64, 896
MM v[0:3],a[196:199],v[176:179],v[0:3]
TR a[130:131],v232 offset:53248
v_max3_f32 v249, v144, v145, v148
MM v[0:3],a[200:203],v[192:195],v[0:3]
TR a[132:133],v232 offset:57344
v_max3_f32 v249, v249, v149, v152
MM v[0:3],a[204:207],v[208:211],v[0:3]
TR a[134:135],v232 offset:61440
v_max3_f32 v249, v249, v153, v156
MM v[4:7],a[220:223],v[208:211],v[128:131]
TR a[136:137],v233 offset:49152
v_max_f32 v249, v249, v157
MM v[4:7],a[216:219],v[192:195],v[4:7]
TR a[138:139],v233 offset:53248
v_cmp_lt_f32 vcc, s71, v249
MM v[4:7],a[212:215],v[176:179],v[4:7]
TR a[140:141],v233 offset:57344
TR a[142:143],v233 offset:61440
MM v[4:7],a[208:211],v[160:163],v[4:7]
s_cbranch_vccnz LSITE19_%=
LBACK19_%=:
MM v[12:15],a[208:211],v[164:167],v[132:135]
s_add_u32 m0, s91, 140288
s_add_u32 s77, s90, 10240
buffer_load_dwordx4 v253, s[28:31], s77 offen offset:0 lds
MM v[12:15],a[212:215],v[180:183],v[12:15]
EX v0,v0
MM v[12:15],a[216:219],v[196:199],v[12:15]
EX v1,v1
MM v[12:15],a[220:223],v[212:215],v[12:15]
TR a[144:145],v232 offset:49664
CV v0,v0,v1
MM v[8:11],a[204:207],v[212:215],v[132:135]
EX v2,v2
MM v[8:11],a[200:203],v[196:199],v[8:11]
EX v3,v3
MM v[8:11],a[196:199],v[180:183],v[8:11]
TR a[146:147],v232 offset:53760
CV v1,v2,v3
MM v[8:11],a[192:195],v[164:167],v[8:11]
EX v4,v4
MM v[16:19],a[192:195],v[168:171],v[136:139]
TR a[148:149],v232 offset:57856
TR a[150:151],v232 offset:61952
MM v[16:19],a[196:199],v[184:187],v[16:19]
EX v5,v5
MM v[16:19],a[200:203],v[200:203],v[16:19]
TR a[152:153],v233 offset:49664
CV v2,v4,v5
MM v[16:19],a[204:207],v[216:219],v[16:19]
EX v6,v6
MM v[20:23],a[220:223],v[216:219],v[136:139]
EX v7,v7
MM v[20:23],a[216:219],v[200:203],v[20:23]
TR a[154:155],v233 offset:53760
CV v3,v6,v7
MM v[20:23],a[212:215],v[184:187],v[20:23]
EX v8,v8
MM v[20:23],a[208:211],v[168:171],v[20:23]
TR a[156:157],v233 offset:57856
TR a[158:159],v233 offset:61952
MM v[28:31],a[208:211],v[172:175],v[140:143]
EX v9,v9
MM v[28:31],a[212:215],v[188:191],v[28:31]
TR a[160:161],v232 offset:50176
CV v8,v8,v9
MM v[28:31],a[216:219],v[204:207],v[28:31]
EX v10,v10
MM v[28:31],a[220:223],v[220:223],v[28:31]
EX v11,v11
MM v[24:27],a[204:207],v[220:223],v[140:143]
TR a[162:163],v232 offset:54272
CV v9,v10,v11
MM v[24:27],a[200:203],v[204:207],v[24:27]
TR a[164:165],v232 offset:58368
TR a[166:167],v232 offset:62464
MM v[24:27],a[196:199],v[188:191],v[24:27]
EX v12,v12
MM v[24:27],a[192:195],v[172:175],v[24:27]
EX v13,v13
MM v[32:35],a[224:227],v[160:163],v[128:131]
TR a[168:169],v233 offset:50176
CV v10,v12,v13
MM v[32:35],a[228:231],v[176:179],v[32:35]
EX v14,v14
MM v[32:35],a[232:235],v[192:195],v[32:35]
TR a[170:171],v233 offset:54272
TR a[172:173],v233 offset:58368
MM v[32:35],a[236:239],v[208:211],v[32:35]
EX v15,v15
MM v[36:39],a[252:255],v[208:211],v[128:131]
TR a[174:175],v233 offset:62464
CV v11,v14,v15
MM v[36:39],a[248:251],v[192:195],v[36:39]
EX v16,v16
MM v[36:39],a[244:247],v[176:179],v[36:39]
EX v17,v17
MM v[36:39],a[240:243],v[160:163],v[36:39]
TR a[176:177],v232 offset:50688
CV v16,v16,v17
MM v[44:47],a[240:243],v[164:167],v[132:135]
s_add_u32 m0, s91, 141312
s_add_u32 s77, s90, 20480
buffer_load_dwordx4 v253, s[28:31], s77 offen offset:0 lds
MM v[44:47],a[244:247],v[180:183],v[44:47]
EX v18,v18
MM v[44:47],a[248:251],v[196:199],v[44:47]
TR a[178:179],v232 offset:54784
TR a[180:181],v232 offset:58880
MM v[44:47],a[252:255],v[212:215],v[44:47]
EX v19,v19
MM v[40:43],a[236:239],v[212:215],v[132:135]
TR a[182:183],v232 offset:62976
CV v17,v18,v19
MM v[40:43],a[232:235],v[196:199],v[40:43]
EX v20,v20
MM v[40:43],a[228:231],v[180:183],v[40:43]
EX v21,v21
MM v[40:43],a[224:227],v[164:167],v[40:43]
TR a[184:185],v233 offset:50688
CV v18,v20,v21
MM v[48:51],a[224:227],v[168:171],v[136:139]
EX v22,v22
MM v[48:51],a[228:231],v[184:187],v[48:51]
TR a[186:187],v233 offset:54784
TR a[188:189],v233 offset:58880
MM v[48:51],a[232:235],v[200:203],v[48:51]
EX v23,v23
MM v[48:51],a[236:239],v[216:219],v[48:51]
TR a[190:191],v233 offset:62976
CV v19,v22,v23
MM v[52:55],a[252:255],v[216:219],v[136:139]
EX v24,v24
MM v[52:55],a[248:251],v[200:203],v[52:55]
EX v25,v25
MM v[52:55],a[244:247],v[184:187],v[52:55]
EX v26,v26
MM v[52:55],a[240:243],v[168:171],v[52:55]
EX v27,v27
MM v[60:63],a[240:243],v[172:175],v[140:143]
EX v28,v28
MM v[60:63],a[244:247],v[188:191],v[60:63]
EX v29,v29
MM v[60:63],a[248:251],v[204:207],v[60:63]
EX v30,v30
MM v[60:63],a[252:255],v[220:223],v[60:63]
EX v31,v31
MM v[56:59],a[236:239],v[220:223],v[140:143]
EX v32,v32
MM v[56:59],a[232:235],v[204:207],v[56:59]
EX v33,v33
MM v[56:59],a[228:231],v[188:191],v[56:59]
EX v34,v34
MM v[56:59],a[224:227],v[172:175],v[56:59]
s_waitcnt lgkmcnt(0)
s_barrier
MM a[0:3],a[128:131],v[64:67],a[0:3]
s_add_u32 m0, s65, 49152
CV v24,v24,v25
CV v25,v26,v27
MM a[0:3],a[132:135],v[68:71],a[0:3]
RD a[192:195],v224 offset:16384
CV v26,v28,v29
MM a[8:11],a[132:135],v[76:79],a[8:11]
RD a[196:199],v225 offset:16384
CV v27,v30,v31
MM a[8:11],a[128:131],v[72:75],a[8:11]
buffer_load_dwordx4 v228, s[8:11], s64 offen offset:0 lds
MM a[16:19],a[128:131],v[80:83],a[16:19]
s_add_u32 m0, s91, 142336
s_add_u32 s77, s90, 30720
buffer_load_dwordx4 v253, s[28:31], s77 offen offset:0 lds
MM a[16:19],a[132:135],v[84:87],a[16:19]
RD a[200:203],v226 offset:16384
CV v4,v32,v33
MM a[24:27],a[132:135],v[92:95],a[24:27]
RD a[204:207],v227 offset:16384
RD a[208:211],v224 offset:20480
MM a[24:27],a[128:131],v[88:91],a[24:27]
s_add_u32 m0, s65, 49152
s_nop 0
buffer_load_dwordx4 v229, s[8:11], s64 offen offset:1024 lds
MM a[28:31],a[136:139],v[88:91],a[28:31]
EX v35,v35
MM a[28:31],a[140:143],v[92:95],a[28:31]
RD a[212:215],v225 offset:20480
CV v5,v34,v35
MM a[20:23],a[140:143],v[84:87],a[20:23]
EX v36,v36
MM a[20:23],a[136:139],v[80:83],a[20:23]
EX v37,v37
MM a[12:15],a[136:139],v[72:75],a[12:15]
RD a[216:219],v226 offset:20480
CV v6,v36,v37
MM a[12:15],a[140:143],v[76:79],a[12:15]
buffer_load_dwordx4 v230, s[8:11], s64 offen offset:2048 lds
MM a[4:7],a[140:143],v[68:71],a[4:7]
EX v38,v38
MM a[4:7],a[136:139],v[64:67],a[4:7]
EX v39,v39
MM a[32:35],a[144:147],v[64:67],a[32:35]
RD a[220:223],v227 offset:20480
CV v7,v38,v39
MM a[32:35],a[148:151],v[68:71],a[32:35]
EX v40,v40
MM a[40:43],a[148:151],v[76:79],a[40:43]
EX v41,v41
MM a[40:43],a[144:147],v[72:75],a[40:43]
RD a[224:227],v224 offset:24576
CV v12,v40,v41
MM a[48:51],a[144:147],v[80:83],a[48:51]
buffer_load_dwordx4 v231, s[8:11], s64 offen offset:3072 lds
MM a[48:51],a[148:151],v[84:87],a[48:51]
s_add_u32 m0, s65, 114688
EX v42,v42
MM a[56:59],a[148:151],v[92:95],a[56:59]
EX v43,v43
MM a[56:59],a[144:147],v[88:91],a[56:59]
RD a[228:231],v225 offset:24576
CV v13,v42,v43
MM a[60:63],a[152:155],v[88:91],a[60:63]
EX v44,v44
MM a[60:63],a[156:159],v[92:95],a[60:63]
buffer_load_dwordx4 v235, s[12:15], s81 offen offset:0 lds
MM a[52:55],a[156:159],v[84:87],a[52:55]
EX v45,v45
MM a[52:55],a[152:155],v[80:83],a[52:55]
RD a[232:235],v226 offset:24576
CV v14,v44,v45
MM a[44:47],a[152:155],v[72:75],a[44:47]
EX v46,v46
MM a[44:47],a[156:159],v[76:79],a[44:47]
EX v47,v47
MM a[36:39],a[156:159],v[68:71],a[36:39]
RD a[236:239],v227 offset:24576
CV v15,v46,v47
MM a[36:39],a[152:155],v[64:67],a[36:39]
EX v48,v48
MM a[64:67],a[160:163],v[64:67],a[64:67]
buffer_load_dwordx4 v235, s[12:15], s64 offen offset:1024 lds
MM a[64:67],a[164:167],v[68:71],a[64:67]
EX v49,v49
MM a[72:75],a[164:167],v[76:79],a[72:75]
RD a[240:243],v224 offset:28672
CV v20,v48,v49
MM a[72:75],a[160:163],v[72:75],a[72:75]
EX v50,v50
MM a[80:83],a[160:163],v[80:83],a[80:83]
s_add_u32 m0, s91, 143360
s_add_u32 s77, s90, 40960
buffer_load_dwordx4 v253, s[28:31], s77 offen offset:0 lds
MM a[80:83],a[164:167],v[84:87],a[80:83]
EX v51,v51
MM a[88:91],a[164:167],v[92:95],a[88:91]
RD a[244:247],v225 offset:28672
CV v21,v50,v51
MM a[88:91],a[160:163],v[88:91],a[88:91]
EX v52,v52
MM a[92:95],a[168:171],v[88:91],a[92:95]
EX v53,v53
MM a[92:95],a[172:175],v[92:95],a[92:95]
RD a[248:251],v226 offset:28672
CV v22,v52,v53
MM a[84:87],a[172:175],v[84:87],a[84:87]
s_add_u32 m0, s65, 114688
s_nop 0
buffer_load_dwordx4 v235, s[12:15], s81 offen offset:2048 lds
MM a[84:87],a[168:171],v[80:83],a[84:87]
EX v54,v54
MM a[76:79],a[168:171],v[72:75],a[76:79]
EX v55,v55
MM a[76:79],a[172:175],v[76:79],a[76:79]
RD a[252:255],v227 offset:28672
CV v23,v54,v55
MM a[68:71],a[172:175],v[68:71],a[68:71]
EX v56,v56
MM a[68:71],a[168:171],v[64:67],a[68:71]
EX v57,v57
MM a[96:99],a[176:179],v[64:67],a[96:99]
buffer_load_dwordx4 v235, s[12:15], s64 offen offset:3072 lds
MM a[96:99],a[180:183],v[68:71],a[96:99]
EX v58,v58
MM a[104:107],a[180:183],v[76:79],a[104:107]
EX v59,v59
MM a[104:107],a[176:179],v[72:75],a[104:107]
)ASM"
R"ASM(
EX v60,v60
MM a[112:115],a[176:179],v[80:83],a[112:115]
EX v61,v61
MM a[112:115],a[180:183],v[84:87],a[112:115]
EX v62,v62
MM a[120:123],a[180:183],v[92:95],a[120:123]
s_add_u32 m0, s91, 144384
s_add_u32 s77, s90, 51200
buffer_load_dwordx4 v253, s[28:31], s77 offen offset:0 lds
MM a[120:123],a[176:179],v[88:91],a[120:123]
EX v63,v63
MM a[124:127],a[184:187],v[88:91],a[124:127]
v_add_f32 v244, v144, v145
v_add_f32 v245, v148, v149
MM a[124:127],a[188:191],v[92:95],a[124:127]
v_add_f32 v246, v152, v153
v_add_f32 v247, v156, v157
MM a[116:119],a[188:191],v[84:87],a[116:119]
CV v28,v56,v57
CV v29,v58,v59
MM a[116:119],a[184:187],v[80:83],a[116:119]
CV v30,v60,v61
CV v31,v62,v63
MM a[108:111],a[184:187],v[72:75],a[108:111]
MM a[108:111],a[188:191],v[76:79],a[108:111]
MM a[100:103],a[188:191],v[68:71],a[100:103]
MM a[100:103],a[184:187],v[64:67],a[100:103]
v_smfmac_f32_16x16x64_bf16 v[144:147], v[236:239], v[0:7], v234
v_smfmac_f32_16x16x64_bf16 v[148:151], v[236:239], v[8:15], v234
v_smfmac_f32_16x16x64_bf16 v[152:155], v[236:239], v[16:23], v234
v_smfmac_f32_16x16x64_bf16 v[156:159], v[236:239], v[24:31], v234
s_waitcnt lgkmcnt(0)
MM v[64:67],a[192:195],v[160:163],v[128:131]
s_add_u32 s64, s64, 16384
TR a[128:129],v232 offset:0
s_add_u32 s81, s64, 896
MM v[64:67],a[196:199],v[176:179],v[64:67]
TR a[130:131],v232 offset:4096
v_max3_f32 v249, v144, v145, v148
MM v[64:67],a[200:203],v[192:195],v[64:67]
TR a[132:133],v232 offset:8192
v_max3_f32 v249, v249, v149, v152
MM v[64:67],a[204:207],v[208:211],v[64:67]
TR a[134:135],v232 offset:12288
v_max3_f32 v249, v249, v153, v156
MM v[68:71],a[220:223],v[208:211],v[128:131]
TR a[136:137],v233 offset:0
v_max_f32 v249, v249, v157
MM v[68:71],a[216:219],v[192:195],v[68:71]
TR a[138:139],v233 offset:4096
v_cmp_lt_f32 vcc, s71, v249
MM v[68:71],a[212:215],v[176:179],v[68:71]
TR a[140:141],v233 offset:8192
TR a[142:143],v233 offset:12288
MM v[68:71],a[208:211],v[160:163],v[68:71]
s_cbranch_vccnz LSITE20_%=
LBACK20_%=:
MM v[76:79],a[208:211],v[164:167],v[132:135]
v_bfe_u32 v253, v248, 4, 4
v_lshlrev_b32 v253, 2, v253
MM v[76:79],a[212:215],v[180:183],v[76:79]
s_and_b32 s76, s18, 0xffffffc0
EX v64,v64
MM v[76:79],a[216:219],v[196:199],v[76:79]
s_add_u32 s76, s76, 77824
buffer_load_dword v252, v253, s[20:23], s76 offen offset:0 sc1
MM v[76:79],a[220:223],v[212:215],v[76:79]
EX v65,v65
MM v[72:75],a[204:207],v[212:215],v[132:135]
EX v66,v66
MM v[72:75],a[200:203],v[196:199],v[72:75]
TR a[144:145],v232 offset:512
CV v64,v64,v65
MM v[72:75],a[196:199],v[180:183],v[72:75]
EX v67,v67
MM v[72:75],a[192:195],v[164:167],v[72:75]
TR a[146:147],v232 offset:4608
CV v65,v66,v67
MM v[80:83],a[192:195],v[168:171],v[136:139]
EX v68,v68
MM v[80:83],a[196:199],v[184:187],v[80:83]
TR a[148:149],v232 offset:8704
TR a[150:151],v232 offset:12800
MM v[80:83],a[200:203],v[200:203],v[80:83]
EX v69,v69
MM v[80:83],a[204:207],v[216:219],v[80:83]
TR a[152:153],v233 offset:512
CV v66,v68,v69
MM v[84:87],a[220:223],v[216:219],v[136:139]
EX v70,v70
MM v[84:87],a[216:219],v[200:203],v[84:87]
EX v71,v71
MM v[84:87],a[212:215],v[184:187],v[84:87]
TR a[154:155],v233 offset:4608
CV v67,v70,v71
MM v[84:87],a[208:211],v[168:171],v[84:87]
EX v72,v72
MM v[92:95],a[208:211],v[172:175],v[140:143]
TR a[156:157],v233 offset:8704
TR a[158:159],v233 offset:12800
MM v[92:95],a[212:215],v[188:191],v[92:95]
EX v73,v73
MM v[92:95],a[216:219],v[204:207],v[92:95]
TR a[160:161],v232 offset:1024
CV v72,v72,v73
MM v[92:95],a[220:223],v[220:223],v[92:95]
EX v74,v74
MM v[88:91],a[204:207],v[220:223],v[140:143]
EX v75,v75
MM v[88:91],a[200:203],v[204:207],v[88:91]
TR a[162:163],v232 offset:5120
CV v73,v74,v75
MM v[88:91],a[196:199],v[188:191],v[88:91]
EX v76,v76
MM v[88:91],a[192:195],v[172:175],v[88:91]
TR a[164:165],v232 offset:9216
TR a[166:167],v232 offset:13312
MM v[96:99],a[224:227],v[160:163],v[128:131]
EX v77,v77
MM v[96:99],a[228:231],v[176:179],v[96:99]
TR a[168:169],v233 offset:1024
CV v74,v76,v77
MM v[96:99],a[232:235],v[192:195],v[96:99]
EX v78,v78
MM v[96:99],a[236:239],v[208:211],v[96:99]
EX v79,v79
MM v[100:103],a[252:255],v[208:211],v[128:131]
TR a[170:171],v233 offset:5120
CV v75,v78,v79
MM v[100:103],a[248:251],v[192:195],v[100:103]
EX v80,v80
MM v[100:103],a[244:247],v[176:179],v[100:103]
TR a[172:173],v233 offset:9216
TR a[174:175],v233 offset:13312
MM v[100:103],a[240:243],v[160:163],v[100:103]
LPG16_%=:
s_cbranch_execz LTR2_%=
EX v81,v81
MM v[108:111],a[240:243],v[164:167],v[132:135]
TR a[176:177],v232 offset:1536
CV v80,v80,v81
MM v[108:111],a[244:247],v[180:183],v[108:111]
EX v82,v82
MM v[108:111],a[248:251],v[196:199],v[108:111]
TR a[178:179],v232 offset:5632
TR a[180:181],v232 offset:9728
MM v[108:111],a[252:255],v[212:215],v[108:111]
EX v83,v83
MM v[104:107],a[236:239],v[212:215],v[132:135]
TR a[182:183],v232 offset:13824
CV v81,v82,v83
MM v[104:107],a[232:235],v[196:199],v[104:107]
EX v84,v84
MM v[104:107],a[228:231],v[180:183],v[104:107]
EX v85,v85
MM v[104:107],a[224:227],v[164:167],v[104:107]
TR a[184:185],v233 offset:1536
CV v82,v84,v85
MM v[112:115],a[224:227],v[168:171],v[136:139]
EX v86,v86
MM v[112:115],a[228:231],v[184:187],v[112:115]
TR a[186:187],v233 offset:5632
TR a[188:189],v233 offset:9728
MM v[112:115],a[232:235],v[200:203],v[112:115]
EX v87,v87
MM v[112:115],a[236:239],v[216:219],v[112:115]
TR a[190:191],v233 offset:13824
CV v83,v86,v87
MM v[116:119],a[252:255],v[216:219],v[136:139]
EX v88,v88
MM v[116:119],a[248:251],v[200:203],v[116:119]
EX v89,v89
MM v[116:119],a[244:247],v[184:187],v[116:119]
EX v90,v90
MM v[116:119],a[240:243],v[168:171],v[116:119]
EX v91,v91
MM v[124:127],a[240:243],v[172:175],v[140:143]
EX v92,v92
MM v[124:127],a[244:247],v[188:191],v[124:127]
EX v93,v93
MM v[124:127],a[248:251],v[204:207],v[124:127]
EX v94,v94
MM v[124:127],a[252:255],v[220:223],v[124:127]
EX v95,v95
MM v[120:123],a[236:239],v[220:223],v[140:143]
EX v96,v96
MM v[120:123],a[232:235],v[204:207],v[120:123]
EX v97,v97
MM v[120:123],a[228:231],v[188:191],v[120:123]
s_getpc_b64 s[98:99]
LTA3_%=:
s_min_u32 s73, %[swave], 3
s_lshl_b32 s73, s73, 12
s_add_u32 s73, s73, LPG17_%=-LTA3_%=
s_add_u32 s98, s98, s73
s_addc_u32 s99, s99, 0
s_mov_b64 exec, 0
s_setpc_b64 s[98:99]
LTR3_%=:
s_mov_b64 exec, -1
CV v88,v88,v89
MM v[120:123],a[224:227],v[172:175],v[120:123]
s_waitcnt vmcnt(20) lgkmcnt(0)
s_barrier
MM a[0:3],a[128:131],v[0:3],a[0:3]
s_add_u32 m0, s65, 0
CV v89,v90,v91
CV v90,v92,v93
MM a[0:3],a[132:135],v[4:7],a[0:3]
RD a[192:195],v224 offset:32768
CV v91,v94,v95
MM a[8:11],a[132:135],v[12:15],a[8:11]
buffer_load_dwordx4 v228, s[8:11], s64 offen offset:0 lds
MM a[8:11],a[128:131],v[8:11],a[8:11]
RD a[196:199],v225 offset:32768
CV v68,v96,v97
MM a[16:19],a[128:131],v[16:19],a[16:19]
RD a[200:203],v226 offset:32768
RD a[204:207],v227 offset:32768
MM a[16:19],a[132:135],v[20:23],a[16:19]
buffer_load_dwordx4 v229, s[8:11], s64 offen offset:1024 lds
MM a[24:27],a[132:135],v[28:31],a[24:27]
EX v98,v98
MM a[24:27],a[128:131],v[24:27],a[24:27]
RD a[208:211],v224 offset:36864
RD a[212:215],v225 offset:36864
MM a[28:31],a[136:139],v[24:27],a[28:31]
EX v99,v99
MM a[28:31],a[140:143],v[28:31],a[28:31]
RD a[216:219],v226 offset:36864
CV v69,v98,v99
MM a[20:23],a[140:143],v[20:23],a[20:23]
EX v100,v100
MM a[20:23],a[136:139],v[16:19],a[20:23]
EX v101,v101
MM a[12:15],a[136:139],v[8:11],a[12:15]
RD a[220:223],v227 offset:36864
CV v70,v100,v101
MM a[12:15],a[140:143],v[12:15],a[12:15]
buffer_load_dwordx4 v230, s[8:11], s64 offen offset:2048 lds
MM a[4:7],a[140:143],v[4:7],a[4:7]
EX v102,v102
MM a[4:7],a[136:139],v[0:3],a[4:7]
EX v103,v103
MM a[32:35],a[144:147],v[0:3],a[32:35]
RD a[224:227],v224 offset:40960
CV v71,v102,v103
MM a[32:35],a[148:151],v[4:7],a[32:35]
EX v104,v104
MM a[40:43],a[148:151],v[12:15],a[40:43]
EX v105,v105
MM a[40:43],a[144:147],v[8:11],a[40:43]
RD a[228:231],v225 offset:40960
CV v76,v104,v105
MM a[48:51],a[144:147],v[16:19],a[48:51]
buffer_load_dwordx4 v231, s[8:11], s64 offen offset:3072 lds
MM a[48:51],a[148:151],v[20:23],a[48:51]
s_add_u32 m0, s65, 65536
EX v106,v106
MM a[56:59],a[148:151],v[28:31],a[56:59]
EX v107,v107
MM a[56:59],a[144:147],v[24:27],a[56:59]
RD a[232:235],v226 offset:40960
CV v77,v106,v107
MM a[60:63],a[152:155],v[24:27],a[60:63]
EX v108,v108
MM a[60:63],a[156:159],v[28:31],a[60:63]
buffer_load_dwordx4 v235, s[12:15], s81 offen offset:0 lds
MM a[52:55],a[156:159],v[20:23],a[52:55]
EX v109,v109
MM a[52:55],a[152:155],v[16:19],a[52:55]
RD a[236:239],v227 offset:40960
CV v78,v108,v109
MM a[44:47],a[152:155],v[8:11],a[44:47]
EX v110,v110
MM a[44:47],a[156:159],v[12:15],a[44:47]
EX v111,v111
MM a[36:39],a[156:159],v[4:7],a[36:39]
RD a[240:243],v224 offset:45056
CV v79,v110,v111
MM a[36:39],a[152:155],v[0:3],a[36:39]
EX v112,v112
MM a[64:67],a[160:163],v[0:3],a[64:67]
buffer_load_dwordx4 v235, s[12:15], s64 offen offset:1024 lds
MM a[64:67],a[164:167],v[4:7],a[64:67]
EX v113,v113
MM a[72:75],a[164:167],v[12:15],a[72:75]
RD a[244:247],v225 offset:45056
CV v84,v112,v113
MM a[72:75],a[160:163],v[8:11],a[72:75]
EX v114,v114
MM a[80:83],a[160:163],v[16:19],a[80:83]
EX v115,v115
MM a[80:83],a[164:167],v[20:23],a[80:83]
RD a[248:251],v226 offset:45056
CV v85,v114,v115
MM a[88:91],a[164:167],v[28:31],a[88:91]
EX v116,v116
MM a[88:91],a[160:163],v[24:27],a[88:91]
buffer_load_dwordx4 v235, s[12:15], s81 offen offset:2048 lds
MM a[92:95],a[168:171],v[24:27],a[92:95]
EX v117,v117
MM a[92:95],a[172:175],v[28:31],a[92:95]
RD a[252:255],v227 offset:45056
CV v86,v116,v117
MM a[84:87],a[172:175],v[20:23],a[84:87]
EX v118,v118
MM a[84:87],a[168:171],v[16:19],a[84:87]
EX v119,v119
MM a[76:79],a[168:171],v[8:11],a[76:79]
CV v87,v118,v119
MM a[76:79],a[172:175],v[12:15],a[76:79]
EX v120,v120
MM a[68:71],a[172:175],v[4:7],a[68:71]
buffer_load_dwordx4 v235, s[12:15], s64 offen offset:3072 lds
MM a[68:71],a[168:171],v[0:3],a[68:71]
EX v121,v121
MM a[96:99],a[176:179],v[0:3],a[96:99]
EX v122,v122
MM a[96:99],a[180:183],v[4:7],a[96:99]
EX v123,v123
MM a[104:107],a[180:183],v[12:15],a[104:107]
EX v124,v124
MM a[104:107],a[176:179],v[8:11],a[104:107]
EX v125,v125
MM a[112:115],a[176:179],v[16:19],a[112:115]
EX v126,v126
MM a[112:115],a[180:183],v[20:23],a[112:115]
EX v127,v127
MM a[120:123],a[180:183],v[28:31],a[120:123]
s_waitcnt vmcnt(8)
v_cmp_ne_u32 vcc, s69, v252
s_cbranch_vccnz LRFG_%=
LRFGb_%=:
MM a[120:123],a[176:179],v[24:27],a[120:123]
v_lshrrev_b32 v253, 8, v248
v_lshlrev_b32 v253, 10, v253
MM a[124:127],a[184:187],v[24:27],a[124:127]
v_add_f32 v244, v144, v145
v_add_f32 v245, v148, v149
MM a[124:127],a[188:191],v[28:31],a[124:127]
v_add_f32 v246, v152, v153
v_add_f32 v247, v156, v157
v_and_b32 v252, 240, v248
MM a[116:119],a[188:191],v[20:23],a[116:119]
v_add_u32 v253, v253, v252
CV v92,v120,v121
MM a[116:119],a[184:187],v[16:19],a[116:119]
s_lshr_b32 s76, s18, 6
CV v93,v122,v123
CV v94,v124,v125
MM a[108:111],a[184:187],v[8:11],a[108:111]
s_lshl_b32 s76, s76, 13
s_bfe_u32 s90, s18, 131076
CV v95,v126,v127
MM a[108:111],a[188:191],v[12:15],a[108:111]
s_lshl_b32 s90, s90, 8
s_add_u32 s76, s76, s90
s_add_u32 s76, s76, 22183936
MM a[100:103],a[188:191],v[4:7],a[100:103]
s_lshl_b32 s90, %[swave], 11
s_add_u32 s90, s90, %[slds]
MM a[100:103],a[184:187],v[0:3],a[100:103]
s_add_u32 m0, s90, 131072
s_nop 0
buffer_load_dwordx4 v253, s[20:23], s76 offen offset:0 sc1 lds
v_smfmac_f32_16x16x64_bf16 v[144:147], v[236:239], v[64:71], v234
v_smfmac_f32_16x16x64_bf16 v[148:151], v[236:239], v[72:79], v234
v_smfmac_f32_16x16x64_bf16 v[152:155], v[236:239], v[80:87], v234
s_add_u32 m0, s90, 132096
s_add_u32 s76, s76, 4096
buffer_load_dwordx4 v253, s[20:23], s76 offen offset:0 sc1 lds
v_smfmac_f32_16x16x64_bf16 v[156:159], v[236:239], v[88:95], v234
s_waitcnt lgkmcnt(0)
s_add_u32 s67, s95, 3072
s_mov_b32 s70, 3
s_cmp_ge_u32 s16, 90
s_cbranch_scc1 LLAP_%=
MM v[0:3],a[192:195],v[160:163],v[128:131]
s_add_u32 s64, s64, 16384
TR a[128:129],v232 offset:16384
s_add_u32 s81, s64, 896
MM v[0:3],a[196:199],v[176:179],v[0:3]
TR a[130:131],v232 offset:20480
v_max3_f32 v249, v144, v145, v148
MM v[0:3],a[200:203],v[192:195],v[0:3]
)ASM"
R"ASM(
TR a[132:133],v232 offset:24576
v_max3_f32 v249, v249, v149, v152
MM v[0:3],a[204:207],v[208:211],v[0:3]
TR a[134:135],v232 offset:28672
v_max3_f32 v249, v249, v153, v156
MM v[4:7],a[220:223],v[208:211],v[128:131]
TR a[136:137],v233 offset:16384
v_max_f32 v249, v249, v157
MM v[4:7],a[216:219],v[192:195],v[4:7]
TR a[138:139],v233 offset:20480
v_cmp_lt_f32 vcc, s71, v249
MM v[4:7],a[212:215],v[176:179],v[4:7]
TR a[140:141],v233 offset:24576
TR a[142:143],v233 offset:28672
MM v[4:7],a[208:211],v[160:163],v[4:7]
s_cbranch_vccnz LSITE21_%=
LBACK21_%=:
MM v[12:15],a[208:211],v[164:167],v[132:135]
EX v0,v0
MM v[12:15],a[212:215],v[180:183],v[12:15]
EX v1,v1
MM v[12:15],a[216:219],v[196:199],v[12:15]
TR a[144:145],v232 offset:16896
CV v0,v0,v1
MM v[12:15],a[220:223],v[212:215],v[12:15]
EX v2,v2
MM v[8:11],a[204:207],v[212:215],v[132:135]
EX v3,v3
MM v[8:11],a[200:203],v[196:199],v[8:11]
TR a[146:147],v232 offset:20992
CV v1,v2,v3
MM v[8:11],a[196:199],v[180:183],v[8:11]
EX v4,v4
MM v[8:11],a[192:195],v[164:167],v[8:11]
TR a[148:149],v232 offset:25088
TR a[150:151],v232 offset:29184
MM v[16:19],a[192:195],v[168:171],v[136:139]
EX v5,v5
MM v[16:19],a[196:199],v[184:187],v[16:19]
TR a[152:153],v233 offset:16896
CV v2,v4,v5
MM v[16:19],a[200:203],v[200:203],v[16:19]
EX v6,v6
MM v[16:19],a[204:207],v[216:219],v[16:19]
TR a[154:155],v233 offset:20992
TR a[156:157],v233 offset:25088
MM v[20:23],a[220:223],v[216:219],v[136:139]
s_add_u32 s87, s95, 3072
buffer_store_dword v240, v248, s[20:23], s87 offen offset:12 sc1
MM v[20:23],a[216:219],v[200:203],v[20:23]
EX v7,v7
MM v[20:23],a[212:215],v[184:187],v[20:23]
TR a[158:159],v233 offset:29184
CV v3,v6,v7
MM v[20:23],a[208:211],v[168:171],v[20:23]
EX v8,v8
MM v[28:31],a[208:211],v[172:175],v[140:143]
EX v9,v9
MM v[28:31],a[212:215],v[188:191],v[28:31]
TR a[160:161],v232 offset:17408
CV v8,v8,v9
MM v[28:31],a[216:219],v[204:207],v[28:31]
EX v10,v10
MM v[28:31],a[220:223],v[220:223],v[28:31]
EX v11,v11
MM v[24:27],a[204:207],v[220:223],v[140:143]
TR a[162:163],v232 offset:21504
CV v9,v10,v11
MM v[24:27],a[200:203],v[204:207],v[24:27]
TR a[164:165],v232 offset:25600
TR a[166:167],v232 offset:29696
MM v[24:27],a[196:199],v[188:191],v[24:27]
EX v12,v12
MM v[24:27],a[192:195],v[172:175],v[24:27]
EX v13,v13
MM v[32:35],a[224:227],v[160:163],v[128:131]
TR a[168:169],v233 offset:17408
CV v10,v12,v13
MM v[32:35],a[228:231],v[176:179],v[32:35]
EX v14,v14
MM v[32:35],a[232:235],v[192:195],v[32:35]
TR a[170:171],v233 offset:21504
TR a[172:173],v233 offset:25600
MM v[32:35],a[236:239],v[208:211],v[32:35]
EX v15,v15
MM v[36:39],a[252:255],v[208:211],v[128:131]
TR a[174:175],v233 offset:29696
CV v11,v14,v15
MM v[36:39],a[248:251],v[192:195],v[36:39]
EX v16,v16
MM v[36:39],a[244:247],v[176:179],v[36:39]
EX v17,v17
MM v[36:39],a[240:243],v[160:163],v[36:39]
TR a[176:177],v232 offset:17920
CV v16,v16,v17
MM v[44:47],a[240:243],v[164:167],v[132:135]
EX v18,v18
MM v[44:47],a[244:247],v[180:183],v[44:47]
TR a[178:179],v232 offset:22016
TR a[180:181],v232 offset:26112
MM v[44:47],a[248:251],v[196:199],v[44:47]
EX v19,v19
MM v[44:47],a[252:255],v[212:215],v[44:47]
TR a[182:183],v232 offset:30208
CV v17,v18,v19
MM v[40:43],a[236:239],v[212:215],v[132:135]
EX v20,v20
MM v[40:43],a[232:235],v[196:199],v[40:43]
EX v21,v21
MM v[40:43],a[228:231],v[180:183],v[40:43]
TR a[184:185],v233 offset:17920
CV v18,v20,v21
MM v[40:43],a[224:227],v[164:167],v[40:43]
EX v22,v22
MM v[48:51],a[224:227],v[168:171],v[136:139]
TR a[186:187],v233 offset:22016
TR a[188:189],v233 offset:26112
MM v[48:51],a[228:231],v[184:187],v[48:51]
EX v23,v23
MM v[48:51],a[232:235],v[200:203],v[48:51]
TR a[190:191],v233 offset:30208
CV v19,v22,v23
MM v[48:51],a[236:239],v[216:219],v[48:51]
EX v24,v24
MM v[52:55],a[252:255],v[216:219],v[136:139]
EX v25,v25
MM v[52:55],a[248:251],v[200:203],v[52:55]
EX v26,v26
MM v[52:55],a[244:247],v[184:187],v[52:55]
EX v27,v27
MM v[52:55],a[240:243],v[168:171],v[52:55]
EX v28,v28
MM v[60:63],a[240:243],v[172:175],v[140:143]
EX v29,v29
MM v[60:63],a[244:247],v[188:191],v[60:63]
EX v30,v30
MM v[60:63],a[248:251],v[204:207],v[60:63]
EX v31,v31
MM v[60:63],a[252:255],v[220:223],v[60:63]
EX v32,v32
MM v[56:59],a[236:239],v[220:223],v[140:143]
EX v33,v33
MM v[56:59],a[232:235],v[204:207],v[56:59]
EX v34,v34
MM v[56:59],a[228:231],v[188:191],v[56:59]
EX v35,v35
MM v[56:59],a[224:227],v[172:175],v[56:59]
s_waitcnt lgkmcnt(0)
s_barrier
MM a[0:3],a[128:131],v[64:67],a[0:3]
s_add_u32 m0, s65, 16384
CV v24,v24,v25
CV v25,v26,v27
MM a[0:3],a[132:135],v[68:71],a[0:3]
RD a[192:195],v224 offset:49152
CV v26,v28,v29
MM a[8:11],a[132:135],v[76:79],a[8:11]
RD a[196:199],v225 offset:49152
CV v27,v30,v31
MM a[8:11],a[128:131],v[72:75],a[8:11]
buffer_load_dwordx4 v228, s[8:11], s64 offen offset:0 lds
MM a[16:19],a[128:131],v[80:83],a[16:19]
RD a[200:203],v226 offset:49152
CV v4,v32,v33
MM a[16:19],a[132:135],v[84:87],a[16:19]
RD a[204:207],v227 offset:49152
CV v5,v34,v35
MM a[24:27],a[132:135],v[92:95],a[24:27]
buffer_load_dwordx4 v229, s[8:11], s64 offen offset:1024 lds
MM a[24:27],a[128:131],v[88:91],a[24:27]
RD a[208:211],v224 offset:53248
RD a[212:215],v225 offset:53248
MM a[28:31],a[136:139],v[88:91],a[28:31]
buffer_load_dwordx4 v230, s[8:11], s64 offen offset:2048 lds
MM a[28:31],a[140:143],v[92:95],a[28:31]
EX v36,v36
MM a[20:23],a[140:143],v[84:87],a[20:23]
EX v37,v37
MM a[20:23],a[136:139],v[80:83],a[20:23]
RD a[216:219],v226 offset:53248
CV v6,v36,v37
MM a[12:15],a[136:139],v[72:75],a[12:15]
EX v38,v38
MM a[12:15],a[140:143],v[76:79],a[12:15]
RD a[220:223],v227 offset:53248
RD a[224:227],v224 offset:57344
MM a[4:7],a[140:143],v[68:71],a[4:7]
EX v39,v39
MM a[4:7],a[136:139],v[64:67],a[4:7]
RD a[228:231],v225 offset:57344
CV v7,v38,v39
MM a[32:35],a[144:147],v[64:67],a[32:35]
EX v40,v40
MM a[32:35],a[148:151],v[68:71],a[32:35]
buffer_load_dwordx4 v231, s[8:11], s64 offen offset:3072 lds
MM a[40:43],a[148:151],v[76:79],a[40:43]
s_add_u32 m0, s65, 81920
EX v41,v41
MM a[40:43],a[144:147],v[72:75],a[40:43]
RD a[232:235],v226 offset:57344
CV v12,v40,v41
MM a[48:51],a[144:147],v[80:83],a[48:51]
EX v42,v42
MM a[48:51],a[148:151],v[84:87],a[48:51]
EX v43,v43
MM a[56:59],a[148:151],v[92:95],a[56:59]
RD a[236:239],v227 offset:57344
CV v13,v42,v43
MM a[56:59],a[144:147],v[88:91],a[56:59]
EX v44,v44
MM a[60:63],a[152:155],v[88:91],a[60:63]
buffer_load_dwordx4 v235, s[12:15], s81 offen offset:0 lds
MM a[60:63],a[156:159],v[92:95],a[60:63]
EX v45,v45
MM a[52:55],a[156:159],v[84:87],a[52:55]
RD a[240:243],v224 offset:61440
CV v14,v44,v45
MM a[52:55],a[152:155],v[80:83],a[52:55]
EX v46,v46
MM a[44:47],a[152:155],v[72:75],a[44:47]
EX v47,v47
MM a[44:47],a[156:159],v[76:79],a[44:47]
RD a[244:247],v225 offset:61440
CV v15,v46,v47
MM a[36:39],a[156:159],v[68:71],a[36:39]
buffer_load_dwordx4 v235, s[12:15], s64 offen offset:1024 lds
MM a[36:39],a[152:155],v[64:67],a[36:39]
EX v48,v48
MM a[64:67],a[160:163],v[64:67],a[64:67]
EX v49,v49
MM a[64:67],a[164:167],v[68:71],a[64:67]
RD a[248:251],v226 offset:61440
CV v20,v48,v49
MM a[72:75],a[164:167],v[76:79],a[72:75]
EX v50,v50
MM a[72:75],a[160:163],v[72:75],a[72:75]
EX v51,v51
MM a[80:83],a[160:163],v[80:83],a[80:83]
RD a[252:255],v227 offset:61440
CV v21,v50,v51
MM a[80:83],a[164:167],v[84:87],a[80:83]
buffer_load_dwordx4 v235, s[12:15], s81 offen offset:2048 lds
MM a[88:91],a[164:167],v[92:95],a[88:91]
EX v52,v52
MM a[88:91],a[160:163],v[88:91],a[88:91]
EX v53,v53
MM a[92:95],a[168:171],v[88:91],a[92:95]
EX v54,v54
MM a[92:95],a[172:175],v[92:95],a[92:95]
buffer_load_dwordx4 v235, s[12:15], s64 offen offset:3072 lds
MM a[84:87],a[172:175],v[84:87],a[84:87]
EX v55,v55
MM a[84:87],a[168:171],v[80:83],a[84:87]
EX v56,v56
MM a[76:79],a[168:171],v[72:75],a[76:79]
EX v57,v57
MM a[76:79],a[172:175],v[76:79],a[76:79]
EX v58,v58
MM a[68:71],a[172:175],v[68:71],a[68:71]
EX v59,v59
MM a[68:71],a[168:171],v[64:67],a[68:71]
EX v60,v60
MM a[96:99],a[176:179],v[64:67],a[96:99]
EX v61,v61
MM a[96:99],a[180:183],v[68:71],a[96:99]
EX v62,v62
MM a[104:107],a[180:183],v[76:79],a[104:107]
EX v63,v63
MM a[104:107],a[176:179],v[72:75],a[104:107]
CV v22,v52,v53
CV v23,v54,v55
MM a[112:115],a[176:179],v[80:83],a[112:115]
CV v28,v56,v57
CV v29,v58,v59
MM a[112:115],a[180:183],v[84:87],a[112:115]
CV v30,v60,v61
CV v31,v62,v63
MM a[120:123],a[180:183],v[92:95],a[120:123]
MM a[120:123],a[176:179],v[88:91],a[120:123]
MM a[124:127],a[184:187],v[88:91],a[124:127]
v_add_f32 v244, v144, v145
v_add_f32 v245, v148, v149
MM a[124:127],a[188:191],v[92:95],a[124:127]
v_add_f32 v246, v152, v153
v_add_f32 v247, v156, v157
MM a[116:119],a[188:191],v[84:87],a[116:119]
MM a[116:119],a[184:187],v[80:83],a[116:119]
MM a[108:111],a[184:187],v[72:75],a[108:111]
MM a[108:111],a[188:191],v[76:79],a[108:111]
MM a[100:103],a[188:191],v[68:71],a[100:103]
MM a[100:103],a[184:187],v[64:67],a[100:103]
v_smfmac_f32_16x16x64_bf16 v[144:147], v[236:239], v[0:7], v234
v_smfmac_f32_16x16x64_bf16 v[148:151], v[236:239], v[8:15], v234
v_smfmac_f32_16x16x64_bf16 v[152:155], v[236:239], v[16:23], v234
v_smfmac_f32_16x16x64_bf16 v[156:159], v[236:239], v[24:31], v234
s_waitcnt lgkmcnt(0)
MM v[64:67],a[192:195],v[160:163],v[128:131]
s_add_u32 s64, s64, 16384
TR a[128:129],v232 offset:32768
s_add_u32 s81, s64, 896
MM v[64:67],a[196:199],v[176:179],v[64:67]
TR a[130:131],v232 offset:36864
v_max3_f32 v249, v144, v145, v148
MM v[64:67],a[200:203],v[192:195],v[64:67]
TR a[132:133],v232 offset:40960
v_max3_f32 v249, v249, v149, v152
MM v[64:67],a[204:207],v[208:211],v[64:67]
TR a[134:135],v232 offset:45056
v_max3_f32 v249, v249, v153, v156
MM v[68:71],a[220:223],v[208:211],v[128:131]
TR a[136:137],v233 offset:32768
v_max_f32 v249, v249, v157
MM v[68:71],a[216:219],v[192:195],v[68:71]
TR a[138:139],v233 offset:36864
v_cmp_lt_f32 vcc, s71, v249
MM v[68:71],a[212:215],v[176:179],v[68:71]
TR a[140:141],v233 offset:40960
TR a[142:143],v233 offset:45056
MM v[68:71],a[208:211],v[160:163],v[68:71]
s_cbranch_vccnz LSITE22_%=
LBACK22_%=:
MM v[76:79],a[208:211],v[164:167],v[132:135]
v_lshrrev_b32 v43, 7, v248
v_bfe_u32 v42, v248, 6, 1
MM v[76:79],a[212:215],v[180:183],v[76:79]
v_lshl_add_u32 v40, v42, 3, v43
v_lshlrev_b32 v40, 4, v40
MM v[76:79],a[216:219],v[196:199],v[76:79]
v_bfe_u32 v42, v248, 4, 2
v_lshl_add_u32 v40, v42, 2, v40
MM v[76:79],a[220:223],v[212:215],v[76:79]
s_lshl_b32 s76, %[swave], 11
EX v64,v64
MM v[72:75],a[204:207],v[212:215],v[132:135]
s_add_u32 s76, s76, %[slds]
EX v65,v65
MM v[72:75],a[200:203],v[196:199],v[72:75]
s_add_u32 s76, s76, 131072
v_add_u32 v40, s76, v40
CV v64,v64,v65
MM v[72:75],a[196:199],v[180:183],v[72:75]
s_waitcnt vmcnt(9)
ds_read_b32 v32, v40 offset:0
ds_read_b32 v36, v40 offset:256
MM v[72:75],a[192:195],v[164:167],v[72:75]
ds_read_b32 v33, v40 offset:512
ds_read_b32 v37, v40 offset:768
MM v[80:83],a[192:195],v[168:171],v[136:139]
ds_read_b32 v34, v40 offset:1024
LPG17_%=:
s_cbranch_execz LTR3_%=
ds_read_b32 v38, v40 offset:1280
EX v66,v66
MM v[80:83],a[196:199],v[184:187],v[80:83]
ds_read_b32 v35, v40 offset:1536
ds_read_b32 v39, v40 offset:1792
EX v67,v67
MM v[80:83],a[200:203],v[200:203],v[80:83]
v_mul_u32_u24 v41, 3456, v43
v_bfe_u32 v42, v248, 4, 3
MM v[80:83],a[204:207],v[216:219],v[80:83]
TR a[144:145],v232 offset:33280
v_lshl_add_u32 v41, v42, 1, v41
EX v68,v68
MM v[84:87],a[220:223],v[216:219],v[136:139]
s_add_u32 s76, s18, 21626880
EX v69,v69
MM v[84:87],a[216:219],v[200:203],v[84:87]
TR a[146:147],v232 offset:37376
TR a[148:149],v232 offset:41472
MM v[84:87],a[212:215],v[184:187],v[84:87]
EX v70,v70
MM v[84:87],a[208:211],v[168:171],v[84:87]
TR a[150:151],v232 offset:45568
CV v65,v66,v67
MM v[92:95],a[208:211],v[172:175],v[140:143]
EX v71,v71
MM v[92:95],a[212:215],v[188:191],v[92:95]
EX v72,v72
MM v[92:95],a[216:219],v[204:207],v[92:95]
TR a[152:153],v233 offset:33280
CV v66,v68,v69
MM v[92:95],a[220:223],v[220:223],v[92:95]
EX v73,v73
MM v[88:91],a[204:207],v[220:223],v[140:143]
s_waitcnt lgkmcnt(5)
v_add_f32 v32, v32, v33
v_add_f32 v36, v36, v37
MM v[88:91],a[200:203],v[204:207],v[88:91]
)ASM"
R"ASM(
v_add_f32 v34, v34, v35
v_add_f32 v38, v38, v39
MM v[88:91],a[196:199],v[188:191],v[88:91]
TR a[154:155],v233 offset:37376
v_add_f32 v32, v32, v34
v_add_f32 v36, v36, v38
MM v[88:91],a[192:195],v[172:175],v[88:91]
CV v42,v32,v36
v_lshlrev_b32 v32, 16, v42
EX v74,v74
MM v[96:99],a[224:227],v[160:163],v[128:131]
TR a[156:157],v233 offset:41472
v_and_b32 v36, 0xffff0000, v42
v_mul_f32 v42, 0xbfb8aa3b, v32
EX v75,v75
MM v[96:99],a[228:231],v[176:179],v[96:99]
EX v42,v42
MM v[96:99],a[232:235],v[192:195],v[96:99]
TR a[158:159],v233 offset:45568
v_add_f32 v42, 1.0, v42
EX v76,v76
MM v[96:99],a[236:239],v[208:211],v[96:99]
v_rcp_f32 v42, v42
MM v[100:103],a[252:255],v[208:211],v[128:131]
TR a[160:161],v232 offset:33792
v_mul_f32 v42, v32, v42
CV v42,v42,v42
EX v77,v77
CV v67,v70,v71
MM v[100:103],a[248:251],v[192:195],v[100:103]
v_lshlrev_b32 v42, 16, v42
v_mul_f32 v32, v42, v36
MM v[100:103],a[244:247],v[176:179],v[100:103]
TR a[162:163],v232 offset:37888
CV v43,v32,v32
EX v78,v78
MM v[100:103],a[240:243],v[160:163],v[100:103]
buffer_store_short v43, v41, s[20:23], s76 offen offset:0 sc1
s_nop 1
TR a[164:165],v232 offset:41984
MM v[108:111],a[240:243],v[164:167],v[132:135]
EX v79,v79
CV v72,v72,v73
MM v[108:111],a[244:247],v[180:183],v[108:111]
TR a[166:167],v232 offset:46080
CV v73,v74,v75
MM v[108:111],a[248:251],v[196:199],v[108:111]
EX v80,v80
MM v[108:111],a[252:255],v[212:215],v[108:111]
TR a[168:169],v233 offset:33792
TR a[170:171],v233 offset:37888
EX v81,v81
MM v[104:107],a[236:239],v[212:215],v[132:135]
TR a[172:173],v233 offset:41984
CV v74,v76,v77
MM v[104:107],a[232:235],v[196:199],v[104:107]
EX v82,v82
MM v[104:107],a[228:231],v[180:183],v[104:107]
EX v83,v83
MM v[104:107],a[224:227],v[164:167],v[104:107]
TR a[174:175],v233 offset:46080
CV v75,v78,v79
MM v[112:115],a[224:227],v[168:171],v[136:139]
EX v84,v84
MM v[112:115],a[228:231],v[184:187],v[112:115]
TR a[176:177],v232 offset:34304
TR a[178:179],v232 offset:38400
MM v[112:115],a[232:235],v[200:203],v[112:115]
EX v85,v85
MM v[112:115],a[236:239],v[216:219],v[112:115]
TR a[180:181],v232 offset:42496
CV v80,v80,v81
MM v[116:119],a[252:255],v[216:219],v[136:139]
EX v86,v86
MM v[116:119],a[248:251],v[200:203],v[116:119]
TR a[182:183],v232 offset:46592
CV v81,v82,v83
MM v[116:119],a[244:247],v[184:187],v[116:119]
EX v87,v87
MM v[116:119],a[240:243],v[168:171],v[116:119]
TR a[184:185],v233 offset:34304
CV v82,v84,v85
MM v[124:127],a[240:243],v[172:175],v[140:143]
EX v88,v88
MM v[124:127],a[244:247],v[188:191],v[124:127]
TR a[186:187],v233 offset:38400
TR a[188:189],v233 offset:42496
MM v[124:127],a[248:251],v[204:207],v[124:127]
TR a[190:191],v233 offset:46592
CV v83,v86,v87
EX v89,v89
MM v[124:127],a[252:255],v[220:223],v[124:127]
EX v90,v90
MM v[120:123],a[236:239],v[220:223],v[140:143]
EX v91,v91
MM v[120:123],a[232:235],v[204:207],v[120:123]
EX v92,v92
MM v[120:123],a[228:231],v[188:191],v[120:123]
EX v93,v93
MM v[120:123],a[224:227],v[172:175],v[120:123]
s_waitcnt lgkmcnt(0)
s_barrier
MM a[0:3],a[128:131],v[0:3],a[0:3]
s_add_u32 m0, s65, 32768
CV v88,v88,v89
CV v89,v90,v91
MM a[0:3],a[132:135],v[4:7],a[0:3]
EX v94,v94
MM a[8:11],a[132:135],v[12:15],a[8:11]
RD a[192:195],v224 offset:0
CV v90,v92,v93
MM a[8:11],a[128:131],v[8:11],a[8:11]
EX v95,v95
MM a[16:19],a[128:131],v[16:19],a[16:19]
RD a[196:199],v225 offset:0
CV v91,v94,v95
MM a[16:19],a[132:135],v[20:23],a[16:19]
EX v96,v96
MM a[24:27],a[132:135],v[28:31],a[24:27]
buffer_load_dwordx4 v228, s[8:11], s64 offen offset:0 lds
MM a[24:27],a[128:131],v[24:27],a[24:27]
EX v97,v97
MM a[28:31],a[136:139],v[24:27],a[28:31]
EX v98,v98
MM a[28:31],a[140:143],v[28:31],a[28:31]
RD a[200:203],v226 offset:0
CV v68,v96,v97
MM a[20:23],a[140:143],v[20:23],a[20:23]
EX v99,v99
MM a[20:23],a[136:139],v[16:19],a[20:23]
RD a[204:207],v227 offset:0
CV v69,v98,v99
MM a[12:15],a[136:139],v[8:11],a[12:15]
EX v100,v100
MM a[12:15],a[140:143],v[12:15],a[12:15]
buffer_load_dwordx4 v229, s[8:11], s64 offen offset:1024 lds
MM a[4:7],a[140:143],v[4:7],a[4:7]
EX v101,v101
MM a[4:7],a[136:139],v[0:3],a[4:7]
RD a[208:211],v224 offset:4096
CV v70,v100,v101
MM a[32:35],a[144:147],v[0:3],a[32:35]
EX v102,v102
MM a[32:35],a[148:151],v[4:7],a[32:35]
EX v103,v103
MM a[40:43],a[148:151],v[12:15],a[40:43]
RD a[212:215],v225 offset:4096
CV v71,v102,v103
MM a[40:43],a[144:147],v[8:11],a[40:43]
EX v104,v104
MM a[48:51],a[144:147],v[16:19],a[48:51]
buffer_load_dwordx4 v230, s[8:11], s64 offen offset:2048 lds
MM a[48:51],a[148:151],v[20:23],a[48:51]
EX v105,v105
MM a[56:59],a[148:151],v[28:31],a[56:59]
RD a[216:219],v226 offset:4096
CV v76,v104,v105
MM a[56:59],a[144:147],v[24:27],a[56:59]
EX v106,v106
MM a[60:63],a[152:155],v[24:27],a[60:63]
EX v107,v107
MM a[60:63],a[156:159],v[28:31],a[60:63]
RD a[220:223],v227 offset:4096
CV v77,v106,v107
MM a[52:55],a[156:159],v[20:23],a[52:55]
EX v108,v108
MM a[52:55],a[152:155],v[16:19],a[52:55]
buffer_load_dwordx4 v231, s[8:11], s64 offen offset:3072 lds
MM a[44:47],a[152:155],v[8:11],a[44:47]
s_add_u32 m0, s65, 98304
EX v109,v109
MM a[44:47],a[156:159],v[12:15],a[44:47]
RD a[224:227],v224 offset:8192
CV v78,v108,v109
MM a[36:39],a[156:159],v[4:7],a[36:39]
EX v110,v110
MM a[36:39],a[152:155],v[0:3],a[36:39]
EX v111,v111
MM a[64:67],a[160:163],v[0:3],a[64:67]
RD a[228:231],v225 offset:8192
CV v79,v110,v111
MM a[64:67],a[164:167],v[4:7],a[64:67]
EX v112,v112
MM a[72:75],a[164:167],v[12:15],a[72:75]
buffer_load_dwordx4 v235, s[12:15], s81 offen offset:0 lds
MM a[72:75],a[160:163],v[8:11],a[72:75]
EX v113,v113
MM a[80:83],a[160:163],v[16:19],a[80:83]
RD a[232:235],v226 offset:8192
CV v84,v112,v113
MM a[80:83],a[164:167],v[20:23],a[80:83]
EX v114,v114
MM a[88:91],a[164:167],v[28:31],a[88:91]
EX v115,v115
MM a[88:91],a[160:163],v[24:27],a[88:91]
RD a[236:239],v227 offset:8192
CV v85,v114,v115
MM a[92:95],a[168:171],v[24:27],a[92:95]
EX v116,v116
MM a[92:95],a[172:175],v[28:31],a[92:95]
buffer_load_dwordx4 v235, s[12:15], s64 offen offset:1024 lds
MM a[84:87],a[172:175],v[20:23],a[84:87]
EX v117,v117
MM a[84:87],a[168:171],v[16:19],a[84:87]
RD a[240:243],v224 offset:12288
CV v86,v116,v117
MM a[76:79],a[168:171],v[8:11],a[76:79]
EX v118,v118
MM a[76:79],a[172:175],v[12:15],a[76:79]
EX v119,v119
MM a[68:71],a[172:175],v[4:7],a[68:71]
RD a[244:247],v225 offset:12288
CV v87,v118,v119
MM a[68:71],a[168:171],v[0:3],a[68:71]
buffer_load_dwordx4 v235, s[12:15], s81 offen offset:2048 lds
MM a[96:99],a[176:179],v[0:3],a[96:99]
EX v120,v120
MM a[96:99],a[180:183],v[4:7],a[96:99]
EX v121,v121
MM a[104:107],a[180:183],v[12:15],a[104:107]
RD a[248:251],v226 offset:12288
CV v92,v120,v121
MM a[104:107],a[176:179],v[8:11],a[104:107]
EX v122,v122
MM a[112:115],a[176:179],v[16:19],a[112:115]
EX v123,v123
MM a[112:115],a[180:183],v[20:23],a[112:115]
RD a[252:255],v227 offset:12288
CV v93,v122,v123
MM a[120:123],a[180:183],v[28:31],a[120:123]
buffer_load_dwordx4 v235, s[12:15], s64 offen offset:3072 lds
MM a[120:123],a[176:179],v[24:27],a[120:123]
EX v124,v124
MM a[124:127],a[184:187],v[24:27],a[124:127]
v_add_f32 v244, v144, v145
v_add_f32 v245, v148, v149
MM a[124:127],a[188:191],v[28:31],a[124:127]
v_add_f32 v246, v152, v153
v_add_f32 v247, v156, v157
MM a[116:119],a[188:191],v[20:23],a[116:119]
EX v125,v125
MM a[116:119],a[184:187],v[16:19],a[116:119]
EX v126,v126
MM a[108:111],a[184:187],v[8:11],a[108:111]
EX v127,v127
MM a[108:111],a[188:191],v[12:15],a[108:111]
CV v94,v124,v125
CV v95,v126,v127
MM a[100:103],a[188:191],v[4:7],a[100:103]
MM a[100:103],a[184:187],v[0:3],a[100:103]
v_smfmac_f32_16x16x64_bf16 v[144:147], v[236:239], v[64:71], v234
v_smfmac_f32_16x16x64_bf16 v[148:151], v[236:239], v[72:79], v234
v_smfmac_f32_16x16x64_bf16 v[152:155], v[236:239], v[80:87], v234
v_smfmac_f32_16x16x64_bf16 v[156:159], v[236:239], v[88:95], v234
s_waitcnt lgkmcnt(0)
MM v[0:3],a[192:195],v[160:163],v[128:131]
s_add_u32 s64, s64, 16384
TR a[128:129],v232 offset:49152
s_add_u32 s81, s64, 896
MM v[0:3],a[196:199],v[176:179],v[0:3]
TR a[130:131],v232 offset:53248
v_max3_f32 v249, v144, v145, v148
MM v[0:3],a[200:203],v[192:195],v[0:3]
TR a[132:133],v232 offset:57344
v_max3_f32 v249, v249, v149, v152
MM v[0:3],a[204:207],v[208:211],v[0:3]
TR a[134:135],v232 offset:61440
v_max3_f32 v249, v249, v153, v156
MM v[4:7],a[220:223],v[208:211],v[128:131]
TR a[136:137],v233 offset:49152
v_max_f32 v249, v249, v157
MM v[4:7],a[216:219],v[192:195],v[4:7]
TR a[138:139],v233 offset:53248
v_cmp_lt_f32 vcc, s71, v249
MM v[4:7],a[212:215],v[176:179],v[4:7]
TR a[140:141],v233 offset:57344
TR a[142:143],v233 offset:61440
MM v[4:7],a[208:211],v[160:163],v[4:7]
s_cbranch_vccnz LSITE23_%=
LBACK23_%=:
MM v[12:15],a[208:211],v[164:167],v[132:135]
EX v0,v0
MM v[12:15],a[212:215],v[180:183],v[12:15]
EX v1,v1
MM v[12:15],a[216:219],v[196:199],v[12:15]
TR a[144:145],v232 offset:49664
CV v0,v0,v1
MM v[12:15],a[220:223],v[212:215],v[12:15]
EX v2,v2
MM v[8:11],a[204:207],v[212:215],v[132:135]
EX v3,v3
MM v[8:11],a[200:203],v[196:199],v[8:11]
TR a[146:147],v232 offset:53760
CV v1,v2,v3
MM v[8:11],a[196:199],v[180:183],v[8:11]
EX v4,v4
MM v[8:11],a[192:195],v[164:167],v[8:11]
TR a[148:149],v232 offset:57856
TR a[150:151],v232 offset:61952
MM v[16:19],a[192:195],v[168:171],v[136:139]
EX v5,v5
MM v[16:19],a[196:199],v[184:187],v[16:19]
TR a[152:153],v233 offset:49664
CV v2,v4,v5
MM v[16:19],a[200:203],v[200:203],v[16:19]
EX v6,v6
MM v[16:19],a[204:207],v[216:219],v[16:19]
TR a[154:155],v233 offset:53760
TR a[156:157],v233 offset:57856
MM v[20:23],a[220:223],v[216:219],v[136:139]
EX v7,v7
MM v[20:23],a[216:219],v[200:203],v[20:23]
TR a[158:159],v233 offset:61952
CV v3,v6,v7
MM v[20:23],a[212:215],v[184:187],v[20:23]
EX v8,v8
MM v[20:23],a[208:211],v[168:171],v[20:23]
EX v9,v9
MM v[28:31],a[208:211],v[172:175],v[140:143]
TR a[160:161],v232 offset:50176
CV v8,v8,v9
MM v[28:31],a[212:215],v[188:191],v[28:31]
EX v10,v10
MM v[28:31],a[216:219],v[204:207],v[28:31]
TR a[162:163],v232 offset:54272
TR a[164:165],v232 offset:58368
MM v[28:31],a[220:223],v[220:223],v[28:31]
EX v11,v11
MM v[24:27],a[204:207],v[220:223],v[140:143]
TR a[166:167],v232 offset:62464
CV v9,v10,v11
MM v[24:27],a[200:203],v[204:207],v[24:27]
EX v12,v12
MM v[24:27],a[196:199],v[188:191],v[24:27]
EX v13,v13
MM v[24:27],a[192:195],v[172:175],v[24:27]
TR a[168:169],v233 offset:50176
CV v10,v12,v13
MM v[32:35],a[224:227],v[160:163],v[128:131]
EX v14,v14
MM v[32:35],a[228:231],v[176:179],v[32:35]
TR a[170:171],v233 offset:54272
TR a[172:173],v233 offset:58368
MM v[32:35],a[232:235],v[192:195],v[32:35]
EX v15,v15
MM v[32:35],a[236:239],v[208:211],v[32:35]
TR a[174:175],v233 offset:62464
CV v11,v14,v15
MM v[36:39],a[252:255],v[208:211],v[128:131]
EX v16,v16
MM v[36:39],a[248:251],v[192:195],v[36:39]
EX v17,v17
MM v[36:39],a[244:247],v[176:179],v[36:39]
TR a[176:177],v232 offset:50688
CV v16,v16,v17
MM v[36:39],a[240:243],v[160:163],v[36:39]
EX v18,v18
MM v[44:47],a[240:243],v[164:167],v[132:135]
TR a[178:179],v232 offset:54784
TR a[180:181],v232 offset:58880
MM v[44:47],a[244:247],v[180:183],v[44:47]
EX v19,v19
MM v[44:47],a[248:251],v[196:199],v[44:47]
TR a[182:183],v232 offset:62976
CV v17,v18,v19
MM v[44:47],a[252:255],v[212:215],v[44:47]
EX v20,v20
MM v[40:43],a[236:239],v[212:215],v[132:135]
TR a[184:185],v233 offset:50688
TR a[186:187],v233 offset:54784
MM v[40:43],a[232:235],v[196:199],v[40:43]
EX v21,v21
MM v[40:43],a[228:231],v[180:183],v[40:43]
TR a[188:189],v233 offset:58880
CV v18,v20,v21
MM v[40:43],a[224:227],v[164:167],v[40:43]
EX v22,v22
MM v[48:51],a[224:227],v[168:171],v[136:139]
EX v23,v23
MM v[48:51],a[228:231],v[184:187],v[48:51]
TR a[190:191],v233 offset:62976
CV v19,v22,v23
MM v[48:51],a[232:235],v[200:203],v[48:51]
EX v24,v24
MM v[48:51],a[236:239],v[216:219],v[48:51]
EX v25,v25
MM v[52:55],a[252:255],v[216:219],v[136:139]
EX v26,v26
MM v[52:55],a[248:251],v[200:203],v[52:55]
EX v27,v27
MM v[52:55],a[244:247],v[184:187],v[52:55]
EX v28,v28
MM v[52:55],a[240:243],v[168:171],v[52:55]
EX v29,v29
MM v[60:63],a[240:243],v[172:175],v[140:143]
EX v30,v30
MM v[60:63],a[244:247],v[188:191],v[60:63]
)ASM"
R"ASM(
EX v31,v31
MM v[60:63],a[248:251],v[204:207],v[60:63]
EX v32,v32
MM v[60:63],a[252:255],v[220:223],v[60:63]
EX v33,v33
MM v[56:59],a[236:239],v[220:223],v[140:143]
EX v34,v34
MM v[56:59],a[232:235],v[204:207],v[56:59]
EX v35,v35
MM v[56:59],a[228:231],v[188:191],v[56:59]
EX v36,v36
MM v[56:59],a[224:227],v[172:175],v[56:59]
s_waitcnt vmcnt(13) lgkmcnt(0)
s_barrier
MM a[0:3],a[128:131],v[64:67],a[0:3]
s_add_u32 m0, s65, 49152
CV v24,v24,v25
CV v25,v26,v27
MM a[0:3],a[132:135],v[68:71],a[0:3]
RD a[192:195],v224 offset:16384
CV v26,v28,v29
MM a[8:11],a[132:135],v[76:79],a[8:11]
RD a[196:199],v225 offset:16384
CV v27,v30,v31
MM a[8:11],a[128:131],v[72:75],a[8:11]
buffer_load_dwordx4 v228, s[8:11], s64 offen offset:0 lds
MM a[16:19],a[128:131],v[80:83],a[16:19]
RD a[200:203],v226 offset:16384
CV v4,v32,v33
MM a[16:19],a[132:135],v[84:87],a[16:19]
RD a[204:207],v227 offset:16384
CV v5,v34,v35
MM a[24:27],a[132:135],v[92:95],a[24:27]
buffer_load_dwordx4 v229, s[8:11], s64 offen offset:1024 lds
MM a[24:27],a[128:131],v[88:91],a[24:27]
RD a[208:211],v224 offset:20480
RD a[212:215],v225 offset:20480
MM a[28:31],a[136:139],v[88:91],a[28:31]
buffer_load_dwordx4 v230, s[8:11], s64 offen offset:2048 lds
MM a[28:31],a[140:143],v[92:95],a[28:31]
RD a[216:219],v226 offset:20480
RD a[220:223],v227 offset:20480
MM a[20:23],a[140:143],v[84:87],a[20:23]
EX v37,v37
MM a[20:23],a[136:139],v[80:83],a[20:23]
RD a[224:227],v224 offset:24576
CV v6,v36,v37
MM a[12:15],a[136:139],v[72:75],a[12:15]
EX v38,v38
MM a[12:15],a[140:143],v[76:79],a[12:15]
buffer_load_dwordx4 v231, s[8:11], s64 offen offset:3072 lds
MM a[4:7],a[140:143],v[68:71],a[4:7]
s_add_u32 m0, s65, 114688
EX v39,v39
MM a[4:7],a[136:139],v[64:67],a[4:7]
RD a[228:231],v225 offset:24576
CV v7,v38,v39
MM a[32:35],a[144:147],v[64:67],a[32:35]
EX v40,v40
MM a[32:35],a[148:151],v[68:71],a[32:35]
EX v41,v41
MM a[40:43],a[148:151],v[76:79],a[40:43]
RD a[232:235],v226 offset:24576
CV v12,v40,v41
MM a[40:43],a[144:147],v[72:75],a[40:43]
EX v42,v42
MM a[48:51],a[144:147],v[80:83],a[48:51]
buffer_load_dwordx4 v235, s[12:15], s81 offen offset:0 lds
MM a[48:51],a[148:151],v[84:87],a[48:51]
EX v43,v43
MM a[56:59],a[148:151],v[92:95],a[56:59]
RD a[236:239],v227 offset:24576
CV v13,v42,v43
MM a[56:59],a[144:147],v[88:91],a[56:59]
EX v44,v44
MM a[60:63],a[152:155],v[88:91],a[60:63]
EX v45,v45
MM a[60:63],a[156:159],v[92:95],a[60:63]
RD a[240:243],v224 offset:28672
CV v14,v44,v45
MM a[52:55],a[156:159],v[84:87],a[52:55]
EX v46,v46
MM a[52:55],a[152:155],v[80:83],a[52:55]
buffer_load_dwordx4 v235, s[12:15], s64 offen offset:1024 lds
MM a[44:47],a[152:155],v[72:75],a[44:47]
EX v47,v47
MM a[44:47],a[156:159],v[76:79],a[44:47]
RD a[244:247],v225 offset:28672
CV v15,v46,v47
MM a[36:39],a[156:159],v[68:71],a[36:39]
EX v48,v48
MM a[36:39],a[152:155],v[64:67],a[36:39]
EX v49,v49
MM a[64:67],a[160:163],v[64:67],a[64:67]
RD a[248:251],v226 offset:28672
CV v20,v48,v49
MM a[64:67],a[164:167],v[68:71],a[64:67]
EX v50,v50
MM a[72:75],a[164:167],v[76:79],a[72:75]
buffer_load_dwordx4 v235, s[12:15], s81 offen offset:2048 lds
MM a[72:75],a[160:163],v[72:75],a[72:75]
EX v51,v51
MM a[80:83],a[160:163],v[80:83],a[80:83]
RD a[252:255],v227 offset:28672
CV v21,v50,v51
MM a[80:83],a[164:167],v[84:87],a[80:83]
EX v52,v52
MM a[88:91],a[164:167],v[92:95],a[88:91]
EX v53,v53
MM a[88:91],a[160:163],v[88:91],a[88:91]
buffer_load_dwordx4 v235, s[12:15], s64 offen offset:3072 lds
MM a[92:95],a[168:171],v[88:91],a[92:95]
EX v54,v54
MM a[92:95],a[172:175],v[92:95],a[92:95]
EX v55,v55
MM a[84:87],a[172:175],v[84:87],a[84:87]
EX v56,v56
MM a[84:87],a[168:171],v[80:83],a[84:87]
EX v57,v57
MM a[76:79],a[168:171],v[72:75],a[76:79]
EX v58,v58
MM a[76:79],a[172:175],v[76:79],a[76:79]
EX v59,v59
MM a[68:71],a[172:175],v[68:71],a[68:71]
EX v60,v60
MM a[68:71],a[168:171],v[64:67],a[68:71]
EX v61,v61
MM a[96:99],a[176:179],v[64:67],a[96:99]
EX v62,v62
MM a[96:99],a[180:183],v[68:71],a[96:99]
EX v63,v63
MM a[104:107],a[180:183],v[76:79],a[104:107]
CV v22,v52,v53
CV v23,v54,v55
MM a[104:107],a[176:179],v[72:75],a[104:107]
CV v28,v56,v57
CV v29,v58,v59
MM a[112:115],a[176:179],v[80:83],a[112:115]
CV v30,v60,v61
CV v31,v62,v63
MM a[112:115],a[180:183],v[84:87],a[112:115]
MM a[120:123],a[180:183],v[92:95],a[120:123]
s_lshr_b32 s91, s18, 2
s_add_u32 s91, s91, 69632
s_waitcnt vmcnt(16)
MM a[120:123],a[176:179],v[88:91],a[120:123]
v_mov_b32 v250, s69
s_mov_b64 exec, 1
buffer_store_dword v250, off, s[20:23], s91 offset:0 sc1
s_mov_b64 exec, -1
MM a[124:127],a[184:187],v[88:91],a[124:127]
v_add_f32 v244, v144, v145
v_add_f32 v245, v148, v149
MM a[124:127],a[188:191],v[92:95],a[124:127]
v_add_f32 v246, v152, v153
v_add_f32 v247, v156, v157
v_lshlrev_b32 v96, 3, v248
MM a[116:119],a[188:191],v[84:87],a[116:119]
v_min_u32 v96, 4992, v96
LPG18_%=:
s_cbranch_execz LTR3_%=
s_sub_u32 s91, 5242880, s26
MM a[116:119],a[184:187],v[80:83],a[116:119]
s_sub_u32 s60, s24, s91
s_subb_u32 s61, s25, 0
s_mov_b32 s62, 5242880
MM a[108:111],a[184:187],v[72:75],a[108:111]
s_mov_b32 s63, 131072
s_lshl_b32 s90, s16, 2
s_add_u32 s90, s90, %[swave]
MM a[108:111],a[188:191],v[76:79],a[108:111]
s_mul_i32 s90, s90, 10240
s_add_u32 s90, s90, 0
s_add_u32 s91, %[slds], 135168
MM a[100:103],a[188:191],v[68:71],a[100:103]
s_mov_b32 m0, s91
s_nop 0
buffer_load_dword v96, s[60:63], s90 offen offset:0 lds
MM a[100:103],a[184:187],v[64:67],a[100:103]
s_lshr_b32 s77, %[sb], 3
s_lshl_b32 s77, s77, 2
s_add_u32 s77, s77, %[swave]
v_smfmac_f32_16x16x64_bf16 v[144:147], v[236:239], v[0:7], v234
s_lshl_b32 s77, s77, 9
s_add_u32 s77, s77, 0
v_smfmac_f32_16x16x64_bf16 v[148:151], v[236:239], v[8:15], v234
v_lshrrev_b32 v253, 1, v248
v_smfmac_f32_16x16x64_bf16 v[152:155], v[236:239], v[16:23], v234
v_add_u32 v253, s77, v253
v_smfmac_f32_16x16x64_bf16 v[156:159], v[236:239], v[24:31], v234
s_waitcnt lgkmcnt(0)
s_mov_b32 s77, LKEND_ka-LPD1_%=-16
v_min_u32 v253, s77, v253
s_getpc_b64 s[98:99]
LPD1_%=:
global_load_dword v253, v253, s[98:99]
MM v[64:67],a[192:195],v[160:163],v[128:131]
s_add_u32 s64, s64, 16384
TR a[128:129],v232 offset:0
s_add_u32 s81, s64, 896
MM v[64:67],a[196:199],v[176:179],v[64:67]
TR a[130:131],v232 offset:4096
v_max3_f32 v249, v144, v145, v148
MM v[64:67],a[200:203],v[192:195],v[64:67]
TR a[132:133],v232 offset:8192
v_max3_f32 v249, v249, v149, v152
MM v[64:67],a[204:207],v[208:211],v[64:67]
TR a[134:135],v232 offset:12288
v_max3_f32 v249, v249, v153, v156
MM v[68:71],a[220:223],v[208:211],v[128:131]
TR a[136:137],v233 offset:0
v_max_f32 v249, v249, v157
MM v[68:71],a[216:219],v[192:195],v[68:71]
TR a[138:139],v233 offset:4096
v_cmp_lt_f32 vcc, s71, v249
MM v[68:71],a[212:215],v[176:179],v[68:71]
TR a[140:141],v233 offset:8192
TR a[142:143],v233 offset:12288
MM v[68:71],a[208:211],v[160:163],v[68:71]
s_cbranch_vccnz LSITE24_%=
LBACK24_%=:
MM v[76:79],a[208:211],v[164:167],v[132:135]
EX v64,v64
MM v[76:79],a[212:215],v[180:183],v[76:79]
EX v65,v65
MM v[76:79],a[216:219],v[196:199],v[76:79]
TR a[144:145],v232 offset:512
CV v64,v64,v65
MM v[76:79],a[220:223],v[212:215],v[76:79]
EX v66,v66
MM v[72:75],a[204:207],v[212:215],v[132:135]
EX v67,v67
MM v[72:75],a[200:203],v[196:199],v[72:75]
TR a[146:147],v232 offset:4608
CV v65,v66,v67
MM v[72:75],a[196:199],v[180:183],v[72:75]
EX v68,v68
MM v[72:75],a[192:195],v[164:167],v[72:75]
TR a[148:149],v232 offset:8704
TR a[150:151],v232 offset:12800
MM v[80:83],a[192:195],v[168:171],v[136:139]
EX v69,v69
MM v[80:83],a[196:199],v[184:187],v[80:83]
TR a[152:153],v233 offset:512
CV v66,v68,v69
MM v[80:83],a[200:203],v[200:203],v[80:83]
EX v70,v70
MM v[80:83],a[204:207],v[216:219],v[80:83]
TR a[154:155],v233 offset:4608
TR a[156:157],v233 offset:8704
MM v[84:87],a[220:223],v[216:219],v[136:139]
s_add_u32 s87, s95, 4096
buffer_store_dword v240, v248, s[20:23], s87 offen offset:12 sc1
MM v[84:87],a[216:219],v[200:203],v[84:87]
EX v71,v71
MM v[84:87],a[212:215],v[184:187],v[84:87]
TR a[158:159],v233 offset:12800
CV v67,v70,v71
MM v[84:87],a[208:211],v[168:171],v[84:87]
EX v72,v72
MM v[92:95],a[208:211],v[172:175],v[140:143]
EX v73,v73
MM v[92:95],a[212:215],v[188:191],v[92:95]
TR a[160:161],v232 offset:1024
CV v72,v72,v73
MM v[92:95],a[216:219],v[204:207],v[92:95]
EX v74,v74
MM v[92:95],a[220:223],v[220:223],v[92:95]
EX v75,v75
MM v[88:91],a[204:207],v[220:223],v[140:143]
TR a[162:163],v232 offset:5120
CV v73,v74,v75
MM v[88:91],a[200:203],v[204:207],v[88:91]
TR a[164:165],v232 offset:9216
TR a[166:167],v232 offset:13312
MM v[88:91],a[196:199],v[188:191],v[88:91]
EX v76,v76
MM v[88:91],a[192:195],v[172:175],v[88:91]
EX v77,v77
MM v[96:99],a[224:227],v[160:163],v[128:131]
TR a[168:169],v233 offset:1024
CV v74,v76,v77
MM v[96:99],a[228:231],v[176:179],v[96:99]
EX v78,v78
MM v[96:99],a[232:235],v[192:195],v[96:99]
TR a[170:171],v233 offset:5120
TR a[172:173],v233 offset:9216
MM v[96:99],a[236:239],v[208:211],v[96:99]
EX v79,v79
MM v[100:103],a[252:255],v[208:211],v[128:131]
TR a[174:175],v233 offset:13312
CV v75,v78,v79
MM v[100:103],a[248:251],v[192:195],v[100:103]
EX v80,v80
MM v[100:103],a[244:247],v[176:179],v[100:103]
EX v81,v81
MM v[100:103],a[240:243],v[160:163],v[100:103]
TR a[176:177],v232 offset:1536
CV v80,v80,v81
MM v[108:111],a[240:243],v[164:167],v[132:135]
EX v82,v82
MM v[108:111],a[244:247],v[180:183],v[108:111]
TR a[178:179],v232 offset:5632
TR a[180:181],v232 offset:9728
MM v[108:111],a[248:251],v[196:199],v[108:111]
EX v83,v83
MM v[108:111],a[252:255],v[212:215],v[108:111]
TR a[182:183],v232 offset:13824
CV v81,v82,v83
MM v[104:107],a[236:239],v[212:215],v[132:135]
EX v84,v84
MM v[104:107],a[232:235],v[196:199],v[104:107]
EX v85,v85
MM v[104:107],a[228:231],v[180:183],v[104:107]
TR a[184:185],v233 offset:1536
CV v82,v84,v85
MM v[104:107],a[224:227],v[164:167],v[104:107]
EX v86,v86
MM v[112:115],a[224:227],v[168:171],v[136:139]
TR a[186:187],v233 offset:5632
TR a[188:189],v233 offset:9728
MM v[112:115],a[228:231],v[184:187],v[112:115]
EX v87,v87
MM v[112:115],a[232:235],v[200:203],v[112:115]
TR a[190:191],v233 offset:13824
CV v83,v86,v87
MM v[112:115],a[236:239],v[216:219],v[112:115]
EX v88,v88
MM v[116:119],a[252:255],v[216:219],v[136:139]
EX v89,v89
MM v[116:119],a[248:251],v[200:203],v[116:119]
EX v90,v90
MM v[116:119],a[244:247],v[184:187],v[116:119]
EX v91,v91
MM v[116:119],a[240:243],v[168:171],v[116:119]
EX v92,v92
MM v[124:127],a[240:243],v[172:175],v[140:143]
EX v93,v93
MM v[124:127],a[244:247],v[188:191],v[124:127]
EX v94,v94
MM v[124:127],a[248:251],v[204:207],v[124:127]
EX v95,v95
MM v[124:127],a[252:255],v[220:223],v[124:127]
EX v96,v96
MM v[120:123],a[236:239],v[220:223],v[140:143]
EX v97,v97
MM v[120:123],a[232:235],v[204:207],v[120:123]
EX v98,v98
MM v[120:123],a[228:231],v[188:191],v[120:123]
EX v99,v99
MM v[120:123],a[224:227],v[172:175],v[120:123]
s_waitcnt vmcnt(16) lgkmcnt(0)
s_barrier
MM a[0:3],a[128:131],v[0:3],a[0:3]
s_add_u32 m0, s65, 0
CV v88,v88,v89
CV v89,v90,v91
MM a[0:3],a[132:135],v[4:7],a[0:3]
RD a[192:195],v224 offset:32768
CV v90,v92,v93
MM a[8:11],a[132:135],v[12:15],a[8:11]
RD a[196:199],v225 offset:32768
CV v91,v94,v95
MM a[8:11],a[128:131],v[8:11],a[8:11]
buffer_load_dwordx4 v228, s[8:11], s64 offen offset:0 lds
MM a[16:19],a[128:131],v[16:19],a[16:19]
RD a[200:203],v226 offset:32768
CV v68,v96,v97
MM a[16:19],a[132:135],v[20:23],a[16:19]
RD a[204:207],v227 offset:32768
CV v69,v98,v99
MM a[24:27],a[132:135],v[28:31],a[24:27]
buffer_load_dwordx4 v229, s[8:11], s64 offen offset:1024 lds
MM a[24:27],a[128:131],v[24:27],a[24:27]
RD a[208:211],v224 offset:36864
RD a[212:215],v225 offset:36864
MM a[28:31],a[136:139],v[24:27],a[28:31]
buffer_load_dwordx4 v230, s[8:11], s64 offen offset:2048 lds
MM a[28:31],a[140:143],v[28:31],a[28:31]
EX v100,v100
MM a[20:23],a[140:143],v[20:23],a[20:23]
EX v101,v101
MM a[20:23],a[136:139],v[16:19],a[20:23]
RD a[216:219],v226 offset:36864
CV v70,v100,v101
MM a[12:15],a[136:139],v[8:11],a[12:15]
EX v102,v102
MM a[12:15],a[140:143],v[12:15],a[12:15]
RD a[220:223],v227 offset:36864
RD a[224:227],v224 offset:40960
MM a[4:7],a[140:143],v[4:7],a[4:7]
)ASM"
R"ASM(
EX v103,v103
MM a[4:7],a[136:139],v[0:3],a[4:7]
RD a[228:231],v225 offset:40960
CV v71,v102,v103
MM a[32:35],a[144:147],v[0:3],a[32:35]
EX v104,v104
MM a[32:35],a[148:151],v[4:7],a[32:35]
buffer_load_dwordx4 v231, s[8:11], s64 offen offset:3072 lds
MM a[40:43],a[148:151],v[12:15],a[40:43]
s_add_u32 m0, s65, 65536
EX v105,v105
MM a[40:43],a[144:147],v[8:11],a[40:43]
RD a[232:235],v226 offset:40960
CV v76,v104,v105
MM a[48:51],a[144:147],v[16:19],a[48:51]
EX v106,v106
MM a[48:51],a[148:151],v[20:23],a[48:51]
EX v107,v107
MM a[56:59],a[148:151],v[28:31],a[56:59]
RD a[236:239],v227 offset:40960
CV v77,v106,v107
MM a[56:59],a[144:147],v[24:27],a[56:59]
EX v108,v108
MM a[60:63],a[152:155],v[24:27],a[60:63]
buffer_load_dwordx4 v235, s[12:15], s81 offen offset:0 lds
MM a[60:63],a[156:159],v[28:31],a[60:63]
EX v109,v109
MM a[52:55],a[156:159],v[20:23],a[52:55]
RD a[240:243],v224 offset:45056
CV v78,v108,v109
MM a[52:55],a[152:155],v[16:19],a[52:55]
EX v110,v110
MM a[44:47],a[152:155],v[8:11],a[44:47]
EX v111,v111
MM a[44:47],a[156:159],v[12:15],a[44:47]
RD a[244:247],v225 offset:45056
CV v79,v110,v111
MM a[36:39],a[156:159],v[4:7],a[36:39]
buffer_load_dwordx4 v235, s[12:15], s64 offen offset:1024 lds
MM a[36:39],a[152:155],v[0:3],a[36:39]
EX v112,v112
MM a[64:67],a[160:163],v[0:3],a[64:67]
EX v113,v113
MM a[64:67],a[164:167],v[4:7],a[64:67]
RD a[248:251],v226 offset:45056
CV v84,v112,v113
MM a[72:75],a[164:167],v[12:15],a[72:75]
EX v114,v114
MM a[72:75],a[160:163],v[8:11],a[72:75]
EX v115,v115
MM a[80:83],a[160:163],v[16:19],a[80:83]
RD a[252:255],v227 offset:45056
CV v85,v114,v115
MM a[80:83],a[164:167],v[20:23],a[80:83]
buffer_load_dwordx4 v235, s[12:15], s81 offen offset:2048 lds
MM a[88:91],a[164:167],v[28:31],a[88:91]
EX v116,v116
MM a[88:91],a[160:163],v[24:27],a[88:91]
EX v117,v117
MM a[92:95],a[168:171],v[24:27],a[92:95]
EX v118,v118
MM a[92:95],a[172:175],v[28:31],a[92:95]
buffer_load_dwordx4 v235, s[12:15], s64 offen offset:3072 lds
MM a[84:87],a[172:175],v[20:23],a[84:87]
EX v119,v119
MM a[84:87],a[168:171],v[16:19],a[84:87]
EX v120,v120
MM a[76:79],a[168:171],v[8:11],a[76:79]
EX v121,v121
MM a[76:79],a[172:175],v[12:15],a[76:79]
EX v122,v122
MM a[68:71],a[172:175],v[4:7],a[68:71]
EX v123,v123
MM a[68:71],a[168:171],v[0:3],a[68:71]
EX v124,v124
MM a[96:99],a[176:179],v[0:3],a[96:99]
EX v125,v125
MM a[96:99],a[180:183],v[4:7],a[96:99]
EX v126,v126
MM a[104:107],a[180:183],v[12:15],a[104:107]
EX v127,v127
MM a[104:107],a[176:179],v[8:11],a[104:107]
CV v86,v116,v117
CV v87,v118,v119
MM a[112:115],a[176:179],v[16:19],a[112:115]
CV v92,v120,v121
CV v93,v122,v123
MM a[112:115],a[180:183],v[20:23],a[112:115]
CV v94,v124,v125
CV v95,v126,v127
MM a[120:123],a[180:183],v[28:31],a[120:123]
MM a[120:123],a[176:179],v[24:27],a[120:123]
MM a[124:127],a[184:187],v[24:27],a[124:127]
v_add_f32 v244, v144, v145
v_add_f32 v245, v148, v149
MM a[124:127],a[188:191],v[28:31],a[124:127]
v_add_f32 v246, v152, v153
v_add_f32 v247, v156, v157
MM a[116:119],a[188:191],v[20:23],a[116:119]
MM a[116:119],a[184:187],v[16:19],a[116:119]
MM a[108:111],a[184:187],v[8:11],a[108:111]
MM a[108:111],a[188:191],v[12:15],a[108:111]
MM a[100:103],a[188:191],v[4:7],a[100:103]
MM a[100:103],a[184:187],v[0:3],a[100:103]
v_smfmac_f32_16x16x64_bf16 v[144:147], v[236:239], v[64:71], v234
v_smfmac_f32_16x16x64_bf16 v[148:151], v[236:239], v[72:79], v234
v_smfmac_f32_16x16x64_bf16 v[152:155], v[236:239], v[80:87], v234
v_smfmac_f32_16x16x64_bf16 v[156:159], v[236:239], v[88:95], v234
s_waitcnt lgkmcnt(0)
MM v[0:3],a[192:195],v[160:163],v[128:131]
s_add_u32 s64, s64, 16384
TR a[128:129],v232 offset:16384
s_add_u32 s81, s64, 896
MM v[0:3],a[196:199],v[176:179],v[0:3]
TR a[130:131],v232 offset:20480
v_max3_f32 v249, v144, v145, v148
MM v[0:3],a[200:203],v[192:195],v[0:3]
TR a[132:133],v232 offset:24576
v_max3_f32 v249, v249, v149, v152
MM v[0:3],a[204:207],v[208:211],v[0:3]
TR a[134:135],v232 offset:28672
v_max3_f32 v249, v249, v153, v156
MM v[4:7],a[220:223],v[208:211],v[128:131]
TR a[136:137],v233 offset:16384
v_max_f32 v249, v249, v157
MM v[4:7],a[216:219],v[192:195],v[4:7]
TR a[138:139],v233 offset:20480
v_cmp_lt_f32 vcc, s71, v249
MM v[4:7],a[212:215],v[176:179],v[4:7]
TR a[140:141],v233 offset:24576
TR a[142:143],v233 offset:28672
MM v[4:7],a[208:211],v[160:163],v[4:7]
s_cbranch_vccnz LSITE25_%=
LBACK25_%=:
MM v[12:15],a[208:211],v[164:167],v[132:135]
s_waitcnt vmcnt(9)
EX v0,v0
MM v[12:15],a[212:215],v[180:183],v[12:15]
s_mul_i32 s76, s46, 96
EX v1,v1
MM v[12:15],a[216:219],v[196:199],v[12:15]
s_add_u32 s76, s76, 69632
v_lshrrev_b32 v253, 2, v248
v_min_u32 v253, 92, v253
MM v[12:15],a[220:223],v[212:215],v[12:15]
buffer_load_dword v252, v253, s[20:23], s76 offen offset:0 sc1
MM v[8:11],a[204:207],v[212:215],v[132:135]
EX v2,v2
MM v[8:11],a[200:203],v[196:199],v[8:11]
TR a[144:145],v232 offset:16896
CV v0,v0,v1
MM v[8:11],a[196:199],v[180:183],v[8:11]
EX v3,v3
MM v[8:11],a[192:195],v[164:167],v[8:11]
TR a[146:147],v232 offset:20992
CV v1,v2,v3
MM v[16:19],a[192:195],v[168:171],v[136:139]
EX v4,v4
MM v[16:19],a[196:199],v[184:187],v[16:19]
TR a[148:149],v232 offset:25088
TR a[150:151],v232 offset:29184
MM v[16:19],a[200:203],v[200:203],v[16:19]
EX v5,v5
MM v[16:19],a[204:207],v[216:219],v[16:19]
TR a[152:153],v233 offset:16896
CV v2,v4,v5
MM v[20:23],a[220:223],v[216:219],v[136:139]
EX v6,v6
MM v[20:23],a[216:219],v[200:203],v[20:23]
EX v7,v7
MM v[20:23],a[212:215],v[184:187],v[20:23]
TR a[154:155],v233 offset:20992
CV v3,v6,v7
MM v[20:23],a[208:211],v[168:171],v[20:23]
EX v8,v8
MM v[28:31],a[208:211],v[172:175],v[140:143]
TR a[156:157],v233 offset:25088
TR a[158:159],v233 offset:29184
MM v[28:31],a[212:215],v[188:191],v[28:31]
EX v9,v9
MM v[28:31],a[216:219],v[204:207],v[28:31]
TR a[160:161],v232 offset:17408
CV v8,v8,v9
MM v[28:31],a[220:223],v[220:223],v[28:31]
EX v10,v10
MM v[24:27],a[204:207],v[220:223],v[140:143]
EX v11,v11
MM v[24:27],a[200:203],v[204:207],v[24:27]
TR a[162:163],v232 offset:21504
CV v9,v10,v11
MM v[24:27],a[196:199],v[188:191],v[24:27]
EX v12,v12
MM v[24:27],a[192:195],v[172:175],v[24:27]
TR a[164:165],v232 offset:25600
TR a[166:167],v232 offset:29696
MM v[32:35],a[224:227],v[160:163],v[128:131]
EX v13,v13
MM v[32:35],a[228:231],v[176:179],v[32:35]
TR a[168:169],v233 offset:17408
CV v10,v12,v13
MM v[32:35],a[232:235],v[192:195],v[32:35]
EX v14,v14
MM v[32:35],a[236:239],v[208:211],v[32:35]
EX v15,v15
MM v[36:39],a[252:255],v[208:211],v[128:131]
TR a[170:171],v233 offset:21504
CV v11,v14,v15
MM v[36:39],a[248:251],v[192:195],v[36:39]
EX v16,v16
MM v[36:39],a[244:247],v[176:179],v[36:39]
TR a[172:173],v233 offset:25600
TR a[174:175],v233 offset:29696
MM v[36:39],a[240:243],v[160:163],v[36:39]
EX v17,v17
MM v[44:47],a[240:243],v[164:167],v[132:135]
TR a[176:177],v232 offset:17920
CV v16,v16,v17
MM v[44:47],a[244:247],v[180:183],v[44:47]
EX v18,v18
MM v[44:47],a[248:251],v[196:199],v[44:47]
TR a[178:179],v232 offset:22016
TR a[180:181],v232 offset:26112
MM v[44:47],a[252:255],v[212:215],v[44:47]
EX v19,v19
MM v[40:43],a[236:239],v[212:215],v[132:135]
TR a[182:183],v232 offset:30208
CV v17,v18,v19
MM v[40:43],a[232:235],v[196:199],v[40:43]
EX v20,v20
MM v[40:43],a[228:231],v[180:183],v[40:43]
EX v21,v21
MM v[40:43],a[224:227],v[164:167],v[40:43]
TR a[184:185],v233 offset:17920
CV v18,v20,v21
MM v[48:51],a[224:227],v[168:171],v[136:139]
EX v22,v22
MM v[48:51],a[228:231],v[184:187],v[48:51]
TR a[186:187],v233 offset:22016
TR a[188:189],v233 offset:26112
MM v[48:51],a[232:235],v[200:203],v[48:51]
EX v23,v23
MM v[48:51],a[236:239],v[216:219],v[48:51]
TR a[190:191],v233 offset:30208
CV v19,v22,v23
MM v[52:55],a[252:255],v[216:219],v[136:139]
EX v24,v24
MM v[52:55],a[248:251],v[200:203],v[52:55]
EX v25,v25
MM v[52:55],a[244:247],v[184:187],v[52:55]
EX v26,v26
MM v[52:55],a[240:243],v[168:171],v[52:55]
EX v27,v27
MM v[60:63],a[240:243],v[172:175],v[140:143]
EX v28,v28
MM v[60:63],a[244:247],v[188:191],v[60:63]
EX v29,v29
MM v[60:63],a[248:251],v[204:207],v[60:63]
EX v30,v30
MM v[60:63],a[252:255],v[220:223],v[60:63]
EX v31,v31
MM v[56:59],a[236:239],v[220:223],v[140:143]
EX v32,v32
MM v[56:59],a[232:235],v[204:207],v[56:59]
EX v33,v33
MM v[56:59],a[228:231],v[188:191],v[56:59]
EX v34,v34
MM v[56:59],a[224:227],v[172:175],v[56:59]
s_waitcnt lgkmcnt(0)
s_barrier
MM a[0:3],a[128:131],v[64:67],a[0:3]
s_add_u32 m0, s65, 16384
CV v24,v24,v25
CV v25,v26,v27
MM a[0:3],a[132:135],v[68:71],a[0:3]
RD a[192:195],v224 offset:49152
CV v26,v28,v29
MM a[8:11],a[132:135],v[76:79],a[8:11]
RD a[196:199],v225 offset:49152
CV v27,v30,v31
MM a[8:11],a[128:131],v[72:75],a[8:11]
buffer_load_dwordx4 v228, s[8:11], s64 offen offset:0 lds
MM a[16:19],a[128:131],v[80:83],a[16:19]
RD a[200:203],v226 offset:49152
CV v4,v32,v33
MM a[16:19],a[132:135],v[84:87],a[16:19]
buffer_load_dwordx4 v229, s[8:11], s64 offen offset:1024 lds
MM a[24:27],a[132:135],v[92:95],a[24:27]
RD a[204:207],v227 offset:49152
RD a[208:211],v224 offset:53248
MM a[24:27],a[128:131],v[88:91],a[24:27]
EX v35,v35
MM a[28:31],a[136:139],v[88:91],a[28:31]
RD a[212:215],v225 offset:53248
CV v5,v34,v35
MM a[28:31],a[140:143],v[92:95],a[28:31]
EX v36,v36
MM a[20:23],a[140:143],v[84:87],a[20:23]
buffer_load_dwordx4 v230, s[8:11], s64 offen offset:2048 lds
MM a[20:23],a[136:139],v[80:83],a[20:23]
EX v37,v37
MM a[12:15],a[136:139],v[72:75],a[12:15]
RD a[216:219],v226 offset:53248
CV v6,v36,v37
MM a[12:15],a[140:143],v[76:79],a[12:15]
EX v38,v38
MM a[4:7],a[140:143],v[68:71],a[4:7]
EX v39,v39
MM a[4:7],a[136:139],v[64:67],a[4:7]
RD a[220:223],v227 offset:53248
CV v7,v38,v39
MM a[32:35],a[144:147],v[64:67],a[32:35]
EX v40,v40
MM a[32:35],a[148:151],v[68:71],a[32:35]
buffer_load_dwordx4 v231, s[8:11], s64 offen offset:3072 lds
MM a[40:43],a[148:151],v[76:79],a[40:43]
s_add_u32 m0, s65, 81920
EX v41,v41
MM a[40:43],a[144:147],v[72:75],a[40:43]
RD a[224:227],v224 offset:57344
CV v12,v40,v41
MM a[48:51],a[144:147],v[80:83],a[48:51]
EX v42,v42
MM a[48:51],a[148:151],v[84:87],a[48:51]
EX v43,v43
MM a[56:59],a[148:151],v[92:95],a[56:59]
RD a[228:231],v225 offset:57344
CV v13,v42,v43
MM a[56:59],a[144:147],v[88:91],a[56:59]
EX v44,v44
MM a[60:63],a[152:155],v[88:91],a[60:63]
buffer_load_dwordx4 v235, s[12:15], s81 offen offset:0 lds
MM a[60:63],a[156:159],v[92:95],a[60:63]
EX v45,v45
MM a[52:55],a[156:159],v[84:87],a[52:55]
RD a[232:235],v226 offset:57344
CV v14,v44,v45
MM a[52:55],a[152:155],v[80:83],a[52:55]
EX v46,v46
MM a[44:47],a[152:155],v[72:75],a[44:47]
EX v47,v47
MM a[44:47],a[156:159],v[76:79],a[44:47]
RD a[236:239],v227 offset:57344
CV v15,v46,v47
MM a[36:39],a[156:159],v[68:71],a[36:39]
buffer_load_dwordx4 v235, s[12:15], s64 offen offset:1024 lds
MM a[36:39],a[152:155],v[64:67],a[36:39]
EX v48,v48
MM a[64:67],a[160:163],v[64:67],a[64:67]
EX v49,v49
MM a[64:67],a[164:167],v[68:71],a[64:67]
RD a[240:243],v224 offset:61440
CV v20,v48,v49
MM a[72:75],a[164:167],v[76:79],a[72:75]
EX v50,v50
MM a[72:75],a[160:163],v[72:75],a[72:75]
RD a[244:247],v225 offset:61440
RD a[248:251],v226 offset:61440
MM a[80:83],a[160:163],v[80:83],a[80:83]
EX v51,v51
MM a[80:83],a[164:167],v[84:87],a[80:83]
LPG19_%=:
s_cbranch_execz LTR3_%=
RD a[252:255],v227 offset:61440
CV v21,v50,v51
MM a[88:91],a[164:167],v[92:95],a[88:91]
EX v52,v52
MM a[88:91],a[160:163],v[88:91],a[88:91]
buffer_load_dwordx4 v235, s[12:15], s81 offen offset:2048 lds
MM a[92:95],a[168:171],v[88:91],a[92:95]
EX v53,v53
MM a[92:95],a[172:175],v[92:95],a[92:95]
EX v54,v54
MM a[84:87],a[172:175],v[84:87],a[84:87]
buffer_load_dwordx4 v235, s[12:15], s64 offen offset:3072 lds
MM a[84:87],a[168:171],v[80:83],a[84:87]
EX v55,v55
MM a[76:79],a[168:171],v[72:75],a[76:79]
EX v56,v56
MM a[76:79],a[172:175],v[76:79],a[76:79]
EX v57,v57
MM a[68:71],a[172:175],v[68:71],a[68:71]
EX v58,v58
MM a[68:71],a[168:171],v[64:67],a[68:71]
EX v59,v59
MM a[96:99],a[176:179],v[64:67],a[96:99]
EX v60,v60
MM a[96:99],a[180:183],v[68:71],a[96:99]
EX v61,v61
MM a[104:107],a[180:183],v[76:79],a[104:107]
EX v62,v62
MM a[104:107],a[176:179],v[72:75],a[104:107]
)ASM"
R"ASM(
EX v63,v63
MM a[112:115],a[176:179],v[80:83],a[112:115]
CV v22,v52,v53
CV v23,v54,v55
MM a[112:115],a[180:183],v[84:87],a[112:115]
CV v28,v56,v57
CV v29,v58,v59
MM a[120:123],a[180:183],v[92:95],a[120:123]
s_waitcnt vmcnt(8)
v_cmp_ne_u32 vcc, s69, v252
s_cbranch_vccnz LRFH_%=
LRFHb_%=:
MM a[120:123],a[176:179],v[88:91],a[120:123]
v_bfe_u32 v253, v248, 4, 3
v_mul_u32_u24 v253, 3456, v253
MM a[124:127],a[184:187],v[88:91],a[124:127]
v_add_f32 v244, v144, v145
v_add_f32 v245, v148, v149
MM a[124:127],a[188:191],v[92:95],a[124:127]
v_add_f32 v246, v152, v153
v_add_f32 v247, v156, v157
v_lshrrev_b32 v252, 7, v248
MM a[116:119],a[188:191],v[84:87],a[116:119]
v_lshl_add_u32 v253, v252, 4, v253
CV v30,v60,v61
MM a[116:119],a[184:187],v[80:83],a[116:119]
s_min_u32 s90, %[swave], 2
s_mul_i32 s76, s46, 384
CV v31,v62,v63
MM a[108:111],a[184:187],v[72:75],a[108:111]
s_lshl_b32 s91, s90, 7
s_add_u32 s76, s76, s91
s_add_u32 s76, s76, 21626880
MM a[108:111],a[188:191],v[76:79],a[108:111]
s_lshl_b32 s90, s90, 10
s_add_u32 s90, s90, %[slds]
MM a[100:103],a[188:191],v[68:71],a[100:103]
s_add_u32 m0, s90, 131072
s_nop 0
buffer_load_dwordx4 v253, s[20:23], s76 offen offset:0 sc1 lds
MM a[100:103],a[184:187],v[64:67],a[100:103]
v_smfmac_f32_16x16x64_bf16 v[144:147], v[236:239], v[0:7], v234
v_smfmac_f32_16x16x64_bf16 v[148:151], v[236:239], v[8:15], v234
v_smfmac_f32_16x16x64_bf16 v[152:155], v[236:239], v[16:23], v234
v_smfmac_f32_16x16x64_bf16 v[156:159], v[236:239], v[24:31], v234
s_waitcnt lgkmcnt(0)
MM v[64:67],a[192:195],v[160:163],v[128:131]
s_add_u32 s64, s64, 16384
TR a[128:129],v232 offset:32768
s_add_u32 s81, s64, 896
MM v[64:67],a[196:199],v[176:179],v[64:67]
TR a[130:131],v232 offset:36864
v_max3_f32 v249, v144, v145, v148
MM v[64:67],a[200:203],v[192:195],v[64:67]
TR a[132:133],v232 offset:40960
v_max3_f32 v249, v249, v149, v152
MM v[64:67],a[204:207],v[208:211],v[64:67]
TR a[134:135],v232 offset:45056
v_max3_f32 v249, v249, v153, v156
MM v[68:71],a[220:223],v[208:211],v[128:131]
TR a[136:137],v233 offset:32768
v_max_f32 v249, v249, v157
MM v[68:71],a[216:219],v[192:195],v[68:71]
TR a[138:139],v233 offset:36864
v_cmp_lt_f32 vcc, s71, v249
MM v[68:71],a[212:215],v[176:179],v[68:71]
TR a[140:141],v233 offset:40960
TR a[142:143],v233 offset:45056
MM v[68:71],a[208:211],v[160:163],v[68:71]
s_cbranch_vccnz LSITE26_%=
LBACK26_%=:
MM v[76:79],a[208:211],v[164:167],v[132:135]
EX v64,v64
MM v[76:79],a[212:215],v[180:183],v[76:79]
EX v65,v65
MM v[76:79],a[216:219],v[196:199],v[76:79]
TR a[144:145],v232 offset:33280
CV v64,v64,v65
MM v[76:79],a[220:223],v[212:215],v[76:79]
EX v66,v66
MM v[72:75],a[204:207],v[212:215],v[132:135]
EX v67,v67
MM v[72:75],a[200:203],v[196:199],v[72:75]
TR a[146:147],v232 offset:37376
CV v65,v66,v67
MM v[72:75],a[196:199],v[180:183],v[72:75]
EX v68,v68
MM v[72:75],a[192:195],v[164:167],v[72:75]
TR a[148:149],v232 offset:41472
TR a[150:151],v232 offset:45568
MM v[80:83],a[192:195],v[168:171],v[136:139]
EX v69,v69
MM v[80:83],a[196:199],v[184:187],v[80:83]
TR a[152:153],v233 offset:33280
CV v66,v68,v69
MM v[80:83],a[200:203],v[200:203],v[80:83]
EX v70,v70
MM v[80:83],a[204:207],v[216:219],v[80:83]
TR a[154:155],v233 offset:37376
TR a[156:157],v233 offset:41472
MM v[84:87],a[220:223],v[216:219],v[136:139]
EX v71,v71
MM v[84:87],a[216:219],v[200:203],v[84:87]
TR a[158:159],v233 offset:45568
CV v67,v70,v71
MM v[84:87],a[212:215],v[184:187],v[84:87]
EX v72,v72
MM v[84:87],a[208:211],v[168:171],v[84:87]
EX v73,v73
MM v[92:95],a[208:211],v[172:175],v[140:143]
TR a[160:161],v232 offset:33792
CV v72,v72,v73
MM v[92:95],a[212:215],v[188:191],v[92:95]
EX v74,v74
MM v[92:95],a[216:219],v[204:207],v[92:95]
TR a[162:163],v232 offset:37888
TR a[164:165],v232 offset:41984
MM v[92:95],a[220:223],v[220:223],v[92:95]
EX v75,v75
MM v[88:91],a[204:207],v[220:223],v[140:143]
TR a[166:167],v232 offset:46080
CV v73,v74,v75
MM v[88:91],a[200:203],v[204:207],v[88:91]
EX v76,v76
MM v[88:91],a[196:199],v[188:191],v[88:91]
EX v77,v77
MM v[88:91],a[192:195],v[172:175],v[88:91]
TR a[168:169],v233 offset:33792
CV v74,v76,v77
MM v[96:99],a[224:227],v[160:163],v[128:131]
EX v78,v78
MM v[96:99],a[228:231],v[176:179],v[96:99]
TR a[170:171],v233 offset:37888
TR a[172:173],v233 offset:41984
MM v[96:99],a[232:235],v[192:195],v[96:99]
EX v79,v79
MM v[96:99],a[236:239],v[208:211],v[96:99]
TR a[174:175],v233 offset:46080
CV v75,v78,v79
MM v[100:103],a[252:255],v[208:211],v[128:131]
EX v80,v80
MM v[100:103],a[248:251],v[192:195],v[100:103]
EX v81,v81
MM v[100:103],a[244:247],v[176:179],v[100:103]
TR a[176:177],v232 offset:34304
CV v80,v80,v81
MM v[100:103],a[240:243],v[160:163],v[100:103]
EX v82,v82
MM v[108:111],a[240:243],v[164:167],v[132:135]
TR a[178:179],v232 offset:38400
TR a[180:181],v232 offset:42496
MM v[108:111],a[244:247],v[180:183],v[108:111]
EX v83,v83
MM v[108:111],a[248:251],v[196:199],v[108:111]
TR a[182:183],v232 offset:46592
CV v81,v82,v83
MM v[108:111],a[252:255],v[212:215],v[108:111]
EX v84,v84
MM v[104:107],a[236:239],v[212:215],v[132:135]
TR a[184:185],v233 offset:34304
TR a[186:187],v233 offset:38400
MM v[104:107],a[232:235],v[196:199],v[104:107]
EX v85,v85
MM v[104:107],a[228:231],v[180:183],v[104:107]
TR a[188:189],v233 offset:42496
CV v82,v84,v85
MM v[104:107],a[224:227],v[164:167],v[104:107]
EX v86,v86
MM v[112:115],a[224:227],v[168:171],v[136:139]
EX v87,v87
MM v[112:115],a[228:231],v[184:187],v[112:115]
TR a[190:191],v233 offset:46592
CV v83,v86,v87
MM v[112:115],a[232:235],v[200:203],v[112:115]
EX v88,v88
MM v[112:115],a[236:239],v[216:219],v[112:115]
EX v89,v89
MM v[116:119],a[252:255],v[216:219],v[136:139]
EX v90,v90
MM v[116:119],a[248:251],v[200:203],v[116:119]
EX v91,v91
MM v[116:119],a[244:247],v[184:187],v[116:119]
EX v92,v92
MM v[116:119],a[240:243],v[168:171],v[116:119]
EX v93,v93
MM v[124:127],a[240:243],v[172:175],v[140:143]
EX v94,v94
MM v[124:127],a[244:247],v[188:191],v[124:127]
EX v95,v95
MM v[124:127],a[248:251],v[204:207],v[124:127]
EX v96,v96
MM v[124:127],a[252:255],v[220:223],v[124:127]
EX v97,v97
MM v[120:123],a[236:239],v[220:223],v[140:143]
EX v98,v98
MM v[120:123],a[232:235],v[204:207],v[120:123]
EX v99,v99
MM v[120:123],a[228:231],v[188:191],v[120:123]
s_getpc_b64 s[98:99]
LTA4_%=:
s_min_u32 s73, %[swave], 1
s_lshl_b32 s73, s73, 12
s_add_u32 s73, s73, LPG21_%=-LTA4_%=
s_add_u32 s98, s98, s73
s_addc_u32 s99, s99, 0
s_mov_b64 exec, 0
s_setpc_b64 s[98:99]
LTR4_%=:
s_mov_b64 exec, -1
CV v88,v88,v89
MM v[120:123],a[224:227],v[172:175],v[120:123]
s_waitcnt lgkmcnt(0)
s_barrier
MM a[0:3],a[128:131],v[0:3],a[0:3]
s_add_u32 m0, s65, 32768
CV v89,v90,v91
CV v90,v92,v93
MM a[0:3],a[132:135],v[4:7],a[0:3]
RD a[192:195],v224 offset:0
CV v91,v94,v95
MM a[8:11],a[132:135],v[12:15],a[8:11]
buffer_load_dwordx4 v228, s[8:11], s64 offen offset:0 lds
MM a[8:11],a[128:131],v[8:11],a[8:11]
RD a[196:199],v225 offset:0
CV v68,v96,v97
MM a[16:19],a[128:131],v[16:19],a[16:19]
RD a[200:203],v226 offset:0
CV v69,v98,v99
MM a[16:19],a[132:135],v[20:23],a[16:19]
buffer_load_dwordx4 v229, s[8:11], s64 offen offset:1024 lds
MM a[24:27],a[132:135],v[28:31],a[24:27]
RD a[204:207],v227 offset:0
RD a[208:211],v224 offset:4096
MM a[24:27],a[128:131],v[24:27],a[24:27]
RD a[212:215],v225 offset:4096
RD a[216:219],v226 offset:4096
MM a[28:31],a[136:139],v[24:27],a[28:31]
buffer_load_dwordx4 v230, s[8:11], s64 offen offset:2048 lds
MM a[28:31],a[140:143],v[28:31],a[28:31]
EX v100,v100
MM a[20:23],a[140:143],v[20:23],a[20:23]
EX v101,v101
MM a[20:23],a[136:139],v[16:19],a[20:23]
RD a[220:223],v227 offset:4096
CV v70,v100,v101
MM a[12:15],a[136:139],v[8:11],a[12:15]
EX v102,v102
MM a[12:15],a[140:143],v[12:15],a[12:15]
buffer_load_dwordx4 v231, s[8:11], s64 offen offset:3072 lds
MM a[4:7],a[140:143],v[4:7],a[4:7]
s_add_u32 m0, s65, 98304
EX v103,v103
MM a[4:7],a[136:139],v[0:3],a[4:7]
RD a[224:227],v224 offset:8192
CV v71,v102,v103
MM a[32:35],a[144:147],v[0:3],a[32:35]
EX v104,v104
MM a[32:35],a[148:151],v[4:7],a[32:35]
EX v105,v105
MM a[40:43],a[148:151],v[12:15],a[40:43]
RD a[228:231],v225 offset:8192
CV v76,v104,v105
MM a[40:43],a[144:147],v[8:11],a[40:43]
EX v106,v106
MM a[48:51],a[144:147],v[16:19],a[48:51]
buffer_load_dwordx4 v235, s[12:15], s81 offen offset:0 lds
MM a[48:51],a[148:151],v[20:23],a[48:51]
EX v107,v107
MM a[56:59],a[148:151],v[28:31],a[56:59]
RD a[232:235],v226 offset:8192
CV v77,v106,v107
MM a[56:59],a[144:147],v[24:27],a[56:59]
EX v108,v108
MM a[60:63],a[152:155],v[24:27],a[60:63]
EX v109,v109
MM a[60:63],a[156:159],v[28:31],a[60:63]
RD a[236:239],v227 offset:8192
CV v78,v108,v109
MM a[52:55],a[156:159],v[20:23],a[52:55]
EX v110,v110
MM a[52:55],a[152:155],v[16:19],a[52:55]
buffer_load_dwordx4 v235, s[12:15], s64 offen offset:1024 lds
MM a[44:47],a[152:155],v[8:11],a[44:47]
EX v111,v111
MM a[44:47],a[156:159],v[12:15],a[44:47]
RD a[240:243],v224 offset:12288
CV v79,v110,v111
MM a[36:39],a[156:159],v[4:7],a[36:39]
EX v112,v112
MM a[36:39],a[152:155],v[0:3],a[36:39]
EX v113,v113
MM a[64:67],a[160:163],v[0:3],a[64:67]
RD a[244:247],v225 offset:12288
CV v84,v112,v113
MM a[64:67],a[164:167],v[4:7],a[64:67]
EX v114,v114
MM a[72:75],a[164:167],v[12:15],a[72:75]
buffer_load_dwordx4 v235, s[12:15], s81 offen offset:2048 lds
MM a[72:75],a[160:163],v[8:11],a[72:75]
EX v115,v115
MM a[80:83],a[160:163],v[16:19],a[80:83]
RD a[248:251],v226 offset:12288
CV v85,v114,v115
MM a[80:83],a[164:167],v[20:23],a[80:83]
EX v116,v116
MM a[88:91],a[164:167],v[28:31],a[88:91]
EX v117,v117
MM a[88:91],a[160:163],v[24:27],a[88:91]
RD a[252:255],v227 offset:12288
CV v86,v116,v117
MM a[92:95],a[168:171],v[24:27],a[92:95]
EX v118,v118
MM a[92:95],a[172:175],v[28:31],a[92:95]
buffer_load_dwordx4 v235, s[12:15], s64 offen offset:3072 lds
MM a[84:87],a[172:175],v[20:23],a[84:87]
EX v119,v119
MM a[84:87],a[168:171],v[16:19],a[84:87]
CV v87,v118,v119
MM a[76:79],a[168:171],v[8:11],a[76:79]
EX v120,v120
MM a[76:79],a[172:175],v[12:15],a[76:79]
EX v121,v121
MM a[68:71],a[172:175],v[4:7],a[68:71]
EX v122,v122
MM a[68:71],a[168:171],v[0:3],a[68:71]
EX v123,v123
MM a[96:99],a[176:179],v[0:3],a[96:99]
EX v124,v124
MM a[96:99],a[180:183],v[4:7],a[96:99]
EX v125,v125
MM a[104:107],a[180:183],v[12:15],a[104:107]
EX v126,v126
MM a[104:107],a[176:179],v[8:11],a[104:107]
EX v127,v127
MM a[112:115],a[176:179],v[16:19],a[112:115]
CV v92,v120,v121
CV v93,v122,v123
MM a[112:115],a[180:183],v[20:23],a[112:115]
CV v94,v124,v125
CV v95,v126,v127
MM a[120:123],a[180:183],v[28:31],a[120:123]
MM a[120:123],a[176:179],v[24:27],a[120:123]
MM a[124:127],a[184:187],v[24:27],a[124:127]
v_add_f32 v244, v144, v145
v_add_f32 v245, v148, v149
MM a[124:127],a[188:191],v[28:31],a[124:127]
v_add_f32 v246, v152, v153
v_add_f32 v247, v156, v157
MM a[116:119],a[188:191],v[20:23],a[116:119]
MM a[116:119],a[184:187],v[16:19],a[116:119]
MM a[108:111],a[184:187],v[8:11],a[108:111]
MM a[108:111],a[188:191],v[12:15],a[108:111]
MM a[100:103],a[188:191],v[4:7],a[100:103]
MM a[100:103],a[184:187],v[0:3],a[100:103]
v_smfmac_f32_16x16x64_bf16 v[144:147], v[236:239], v[64:71], v234
v_smfmac_f32_16x16x64_bf16 v[148:151], v[236:239], v[72:79], v234
v_smfmac_f32_16x16x64_bf16 v[152:155], v[236:239], v[80:87], v234
v_smfmac_f32_16x16x64_bf16 v[156:159], v[236:239], v[88:95], v234
s_waitcnt lgkmcnt(0)
MM v[0:3],a[192:195],v[160:163],v[128:131]
s_add_u32 s64, s64, 16384
TR a[128:129],v232 offset:49152
s_add_u32 s81, s64, 896
MM v[0:3],a[196:199],v[176:179],v[0:3]
TR a[130:131],v232 offset:53248
v_max3_f32 v249, v144, v145, v148
MM v[0:3],a[200:203],v[192:195],v[0:3]
TR a[132:133],v232 offset:57344
v_max3_f32 v249, v249, v149, v152
MM v[0:3],a[204:207],v[208:211],v[0:3]
TR a[134:135],v232 offset:61440
v_max3_f32 v249, v249, v153, v156
MM v[4:7],a[220:223],v[208:211],v[128:131]
TR a[136:137],v233 offset:49152
v_max_f32 v249, v249, v157
MM v[4:7],a[216:219],v[192:195],v[4:7]
TR a[138:139],v233 offset:53248
v_cmp_lt_f32 vcc, s71, v249
MM v[4:7],a[212:215],v[176:179],v[4:7]
TR a[140:141],v233 offset:57344
TR a[142:143],v233 offset:61440
MM v[4:7],a[208:211],v[160:163],v[4:7]
)ASM"
R"ASM(
s_cbranch_vccnz LSITE27_%=
LBACK27_%=:
MM v[12:15],a[208:211],v[164:167],v[132:135]
v_lshrrev_b32 v125, 8, v248
v_lshlrev_b32 v124, 10, v125
MM v[12:15],a[212:215],v[180:183],v[12:15]
v_bfe_u32 v126, v248, 6, 2
v_lshl_add_u32 v124, v126, 7, v124
MM v[12:15],a[216:219],v[196:199],v[12:15]
v_bfe_u32 v126, v248, 4, 2
v_lshl_add_u32 v124, v126, 3, v124
MM v[12:15],a[220:223],v[212:215],v[12:15]
s_lshl_b32 s91, %[swave], 5
EX v0,v0
MM v[8:11],a[204:207],v[212:215],v[132:135]
s_add_u32 s91, s91, %[slds]
EX v1,v1
MM v[8:11],a[200:203],v[196:199],v[8:11]
s_add_u32 s91, s91, 139264
v_add_u32 v124, s91, v124
v_and_b32 v126, 112, v248
MM v[8:11],a[196:199],v[180:183],v[8:11]
s_add_u32 s91, %[slds], 131072
v_lshl_add_u32 v126, v125, 7, v126
v_add_u32 v126, s91, v126
MM v[8:11],a[192:195],v[164:167],v[8:11]
v_bfe_u32 v127, v248, 4, 3
v_mul_u32_u24 v127, 2560, v127
MM v[16:19],a[192:195],v[168:171],v[136:139]
v_lshl_add_u32 v127, v125, 4, v127
CV v0,v0,v1
EX v2,v2
MM v[16:19],a[196:199],v[184:187],v[16:19]
s_lshl_b32 s90, %[swave], 6
EX v3,v3
MM v[16:19],a[200:203],v[200:203],v[16:19]
s_lshl_b32 s91, s47, 8
EX v4,v4
MM v[16:19],a[204:207],v[216:219],v[16:19]
s_add_u32 s90, s90, s91
TR a[144:145],v232 offset:49664
TR a[146:147],v232 offset:53760
MM v[20:23],a[220:223],v[216:219],v[136:139]
s_mul_i32 s91, s46, 17920
EX v5,v5
MM v[20:23],a[216:219],v[200:203],v[20:23]
s_add_u32 s90, s90, s91
TR a[148:149],v232 offset:57856
CV v1,v2,v3
MM v[20:23],a[212:215],v[184:187],v[20:23]
s_add_u32 s90, s90, 21659648
EX v6,v6
MM v[20:23],a[208:211],v[168:171],v[20:23]
EX v7,v7
MM v[28:31],a[208:211],v[172:175],v[140:143]
TR a[150:151],v232 offset:61952
TR a[152:153],v233 offset:49664
MM v[28:31],a[212:215],v[188:191],v[28:31]
EX v8,v8
MM v[28:31],a[216:219],v[204:207],v[28:31]
TR a[154:155],v233 offset:53760
CV v2,v4,v5
MM v[28:31],a[220:223],v[220:223],v[28:31]
EX v9,v9
MM v[24:27],a[204:207],v[220:223],v[140:143]
TR a[156:157],v233 offset:57856
CV v3,v6,v7
MM v[24:27],a[200:203],v[204:207],v[24:27]
EX v10,v10
MM v[24:27],a[196:199],v[188:191],v[24:27]
TR a[158:159],v233 offset:61952
CV v8,v8,v9
MM v[24:27],a[192:195],v[172:175],v[24:27]
EX v11,v11
MM v[32:35],a[224:227],v[160:163],v[128:131]
TR a[160:161],v232 offset:50176
TR a[162:163],v232 offset:54272
MM v[32:35],a[228:231],v[176:179],v[32:35]
EX v12,v12
MM v[32:35],a[232:235],v[192:195],v[32:35]
EX v13,v13
MM v[32:35],a[236:239],v[208:211],v[32:35]
TR a[164:165],v232 offset:58368
CV v9,v10,v11
MM v[36:39],a[252:255],v[208:211],v[128:131]
EX v14,v14
MM v[36:39],a[248:251],v[192:195],v[36:39]
TR a[166:167],v232 offset:62464
CV v10,v12,v13
MM v[36:39],a[244:247],v[176:179],v[36:39]
EX v15,v15
MM v[36:39],a[240:243],v[160:163],v[36:39]
TR a[168:169],v233 offset:50176
TR a[170:171],v233 offset:54272
MM v[44:47],a[240:243],v[164:167],v[132:135]
EX v16,v16
MM v[44:47],a[244:247],v[180:183],v[44:47]
EX v17,v17
MM v[44:47],a[248:251],v[196:199],v[44:47]
TR a[172:173],v233 offset:58368
CV v11,v14,v15
MM v[44:47],a[252:255],v[212:215],v[44:47]
EX v18,v18
MM v[40:43],a[236:239],v[212:215],v[132:135]
TR a[174:175],v233 offset:62464
CV v16,v16,v17
MM v[40:43],a[232:235],v[196:199],v[40:43]
EX v19,v19
MM v[40:43],a[228:231],v[180:183],v[40:43]
TR a[176:177],v232 offset:50688
TR a[178:179],v232 offset:54784
MM v[40:43],a[224:227],v[164:167],v[40:43]
EX v20,v20
MM v[48:51],a[224:227],v[168:171],v[136:139]
EX v21,v21
MM v[48:51],a[228:231],v[184:187],v[48:51]
TR a[180:181],v232 offset:58880
CV v17,v18,v19
MM v[48:51],a[232:235],v[200:203],v[48:51]
EX v22,v22
MM v[48:51],a[236:239],v[216:219],v[48:51]
TR a[182:183],v232 offset:62976
CV v18,v20,v21
MM v[52:55],a[252:255],v[216:219],v[136:139]
EX v23,v23
MM v[52:55],a[248:251],v[200:203],v[52:55]
TR a[184:185],v233 offset:50688
TR a[186:187],v233 offset:54784
MM v[52:55],a[244:247],v[184:187],v[52:55]
EX v24,v24
MM v[52:55],a[240:243],v[168:171],v[52:55]
TR a[188:189],v233 offset:58880
CV v19,v22,v23
MM v[60:63],a[240:243],v[172:175],v[140:143]
EX v25,v25
MM v[60:63],a[244:247],v[188:191],v[60:63]
TR a[190:191],v233 offset:62976
CV v24,v24,v25
MM v[60:63],a[248:251],v[204:207],v[60:63]
EX v26,v26
MM v[60:63],a[252:255],v[220:223],v[60:63]
EX v27,v27
MM v[56:59],a[236:239],v[220:223],v[140:143]
EX v28,v28
MM v[56:59],a[232:235],v[204:207],v[56:59]
EX v29,v29
MM v[56:59],a[228:231],v[188:191],v[56:59]
EX v30,v30
MM v[56:59],a[224:227],v[172:175],v[56:59]
s_waitcnt vmcnt(8) lgkmcnt(0)
s_barrier
MM a[0:3],a[128:131],v[64:67],a[0:3]
s_add_u32 m0, s65, 49152
CV v25,v26,v27
CV v26,v28,v29
MM a[0:3],a[132:135],v[68:71],a[0:3]
RD a[192:195],v224 offset:16384
RD a[196:199],v225 offset:16384
MM a[8:11],a[132:135],v[76:79],a[8:11]
EX v31,v31
MM a[8:11],a[128:131],v[72:75],a[8:11]
RD a[200:203],v226 offset:16384
CV v27,v30,v31
MM a[16:19],a[128:131],v[80:83],a[16:19]
TR v[96:97],v124 offset:0
TR v[98:99],v124 offset:512
MM a[16:19],a[132:135],v[84:87],a[16:19]
RD v[108:111],v126 offset:0
TR v[100:101],v124 offset:4096
MM a[24:27],a[132:135],v[92:95],a[24:27]
TR v[102:103],v124 offset:4608
RD v[112:115],v126 offset:512
MM a[24:27],a[128:131],v[88:91],a[24:27]
TR v[104:105],v124 offset:8192
TR v[106:107],v124 offset:8704
MM a[28:31],a[136:139],v[88:91],a[28:31]
RD v[116:119],v126 offset:1024
RD a[204:207],v227 offset:16384
EX v32,v32
MM a[28:31],a[140:143],v[92:95],a[28:31]
EX v33,v33
MM a[20:23],a[140:143],v[84:87],a[20:23]
EX v34,v34
MM a[20:23],a[136:139],v[80:83],a[20:23]
buffer_load_dwordx4 v228, s[8:11], s64 offen offset:0 lds
LPG20_%=:
s_cbranch_execz LTR3_%=
MM a[12:15],a[136:139],v[72:75],a[12:15]
EX v35,v35
MM a[12:15],a[140:143],v[76:79],a[12:15]
EX v36,v36
MM a[4:7],a[140:143],v[68:71],a[4:7]
CV v4,v32,v33
CV v5,v34,v35
MM a[4:7],a[136:139],v[64:67],a[4:7]
EX v37,v37
MM a[32:35],a[144:147],v[64:67],a[32:35]
EX v38,v38
MM a[32:35],a[148:151],v[68:71],a[32:35]
buffer_load_dwordx4 v229, s[8:11], s64 offen offset:1024 lds
MM a[40:43],a[148:151],v[76:79],a[40:43]
EX v39,v39
MM a[40:43],a[144:147],v[72:75],a[40:43]
RD a[208:211],v224 offset:20480
CV v6,v36,v37
MM a[48:51],a[144:147],v[80:83],a[48:51]
s_waitcnt lgkmcnt(2)
EX v40,v40
MM a[48:51],a[148:151],v[84:87],a[48:51]
RD a[212:215],v225 offset:20480
CV v7,v38,v39
MM v[120:123],v[96:99],v[108:111],0
MM v[120:123],v[100:103],v[112:115],v[120:123]
MM v[120:123],v[104:107],v[116:119],v[120:123]
MM a[56:59],a[148:151],v[92:95],a[56:59]
TR v[96:97],v124 offset:12288
TR v[98:99],v124 offset:12800
MM a[56:59],a[144:147],v[88:91],a[56:59]
RD v[108:111],v126 offset:1536
TR v[100:101],v124 offset:16384
MM a[60:63],a[152:155],v[88:91],a[60:63]
TR v[102:103],v124 offset:16896
RD v[112:115],v126 offset:2048
EX v41,v41
MM a[60:63],a[156:159],v[92:95],a[60:63]
TR v[104:105],v124 offset:20480
TR v[106:107],v124 offset:20992
MM a[52:55],a[156:159],v[84:87],a[52:55]
RD v[116:119],v126 offset:2560
CV v12,v40,v41
EX v42,v42
MM a[52:55],a[152:155],v[80:83],a[52:55]
EX v43,v43
MM a[44:47],a[152:155],v[72:75],a[44:47]
buffer_load_dwordx4 v230, s[8:11], s64 offen offset:2048 lds
MM a[44:47],a[156:159],v[76:79],a[44:47]
EX v44,v44
MM a[36:39],a[156:159],v[68:71],a[36:39]
EX v45,v45
MM a[36:39],a[152:155],v[64:67],a[36:39]
RD a[216:219],v226 offset:20480
CV v13,v42,v43
MM a[64:67],a[160:163],v[64:67],a[64:67]
EX v46,v46
MM a[64:67],a[164:167],v[68:71],a[64:67]
EX v47,v47
MM a[72:75],a[164:167],v[76:79],a[72:75]
buffer_load_dwordx4 v231, s[8:11], s64 offen offset:3072 lds
MM a[72:75],a[160:163],v[72:75],a[72:75]
s_add_u32 m0, s65, 114688
RD a[220:223],v227 offset:20480
CV v14,v44,v45
MM a[80:83],a[160:163],v[80:83],a[80:83]
EX v48,v48
MM a[80:83],a[164:167],v[84:87],a[80:83]
EX v49,v49
MM a[88:91],a[164:167],v[92:95],a[88:91]
s_waitcnt lgkmcnt(2)
RD a[224:227],v224 offset:24576
CV v15,v46,v47
MM a[88:91],a[160:163],v[88:91],a[88:91]
EX v50,v50
MM v[120:123],v[96:99],v[108:111],v[120:123]
MM v[120:123],v[100:103],v[112:115],v[120:123]
MM v[120:123],v[104:107],v[116:119],v[120:123]
MM a[92:95],a[168:171],v[88:91],a[92:95]
EX v51,v51
MM a[92:95],a[172:175],v[92:95],a[92:95]
buffer_load_dwordx4 v235, s[12:15], s81 offen offset:0 lds
MM a[84:87],a[172:175],v[84:87],a[84:87]
RD a[228:231],v225 offset:24576
CV v20,v48,v49
MM a[84:87],a[168:171],v[80:83],a[84:87]
EX v52,v52
MM a[76:79],a[168:171],v[72:75],a[76:79]
EX v53,v53
MM a[76:79],a[172:175],v[76:79],a[76:79]
RD a[232:235],v226 offset:24576
CV v21,v50,v51
MM a[68:71],a[172:175],v[68:71],a[68:71]
s_mov_b32 exec_lo, 0x00ff00ff
s_mov_b32 exec_hi, 0x00ff00ff
buffer_store_dwordx4 v[120:123], v127, s[20:23], s90 offen offset:0 sc1
s_mov_b64 exec, -1
s_nop 0
MM a[68:71],a[168:171],v[64:67],a[68:71]
EX v54,v54
MM a[96:99],a[176:179],v[64:67],a[96:99]
EX v55,v55
MM a[96:99],a[180:183],v[68:71],a[96:99]
RD a[236:239],v227 offset:24576
CV v22,v52,v53
MM a[104:107],a[180:183],v[76:79],a[104:107]
EX v56,v56
MM a[104:107],a[176:179],v[72:75],a[104:107]
buffer_load_dwordx4 v235, s[12:15], s64 offen offset:1024 lds
MM a[112:115],a[176:179],v[80:83],a[112:115]
EX v57,v57
MM a[112:115],a[180:183],v[84:87],a[112:115]
RD a[240:243],v224 offset:28672
CV v23,v54,v55
MM a[120:123],a[180:183],v[92:95],a[120:123]
EX v58,v58
MM a[120:123],a[176:179],v[88:91],a[120:123]
EX v59,v59
MM a[124:127],a[184:187],v[88:91],a[124:127]
v_add_f32 v244, v144, v145
v_add_f32 v245, v148, v149
MM a[124:127],a[188:191],v[92:95],a[124:127]
v_add_f32 v246, v152, v153
v_add_f32 v247, v156, v157
MM a[116:119],a[188:191],v[84:87],a[116:119]
EX v60,v60
MM a[116:119],a[184:187],v[80:83],a[116:119]
RD a[244:247],v225 offset:28672
CV v28,v56,v57
MM a[108:111],a[184:187],v[72:75],a[108:111]
EX v61,v61
MM a[108:111],a[188:191],v[76:79],a[108:111]
buffer_load_dwordx4 v235, s[12:15], s81 offen offset:2048 lds
MM a[100:103],a[188:191],v[68:71],a[100:103]
EX v62,v62
MM a[100:103],a[184:187],v[64:67],a[100:103]
RD a[248:251],v226 offset:28672
CV v29,v58,v59
v_smfmac_f32_16x16x64_bf16 v[144:147], v[236:239], v[0:7], v234
RD a[252:255],v227 offset:28672
EX v63,v63
v_smfmac_f32_16x16x64_bf16 v[148:151], v[236:239], v[8:15], v234
CV v30,v60,v61
v_smfmac_f32_16x16x64_bf16 v[152:155], v[236:239], v[16:23], v234
CV v31,v62,v63
s_nop 1
v_smfmac_f32_16x16x64_bf16 v[156:159], v[236:239], v[24:31], v234
s_waitcnt lgkmcnt(0)
buffer_load_dwordx4 v235, s[12:15], s64 offen offset:3072 lds
MM v[64:67],a[192:195],v[160:163],v[128:131]
s_add_u32 s64, s64, 16384
TR a[128:129],v232 offset:0
s_add_u32 s81, s64, 896
MM v[64:67],a[196:199],v[176:179],v[64:67]
TR a[130:131],v232 offset:4096
v_max3_f32 v249, v144, v145, v148
MM v[64:67],a[200:203],v[192:195],v[64:67]
TR a[132:133],v232 offset:8192
v_max3_f32 v249, v249, v149, v152
MM v[64:67],a[204:207],v[208:211],v[64:67]
TR a[134:135],v232 offset:12288
v_max3_f32 v249, v249, v153, v156
MM v[68:71],a[220:223],v[208:211],v[128:131]
TR a[136:137],v233 offset:0
v_max_f32 v249, v249, v157
MM v[68:71],a[216:219],v[192:195],v[68:71]
TR a[138:139],v233 offset:4096
v_cmp_lt_f32 vcc, s71, v249
MM v[68:71],a[212:215],v[176:179],v[68:71]
TR a[140:141],v233 offset:8192
TR a[142:143],v233 offset:12288
MM v[68:71],a[208:211],v[160:163],v[68:71]
s_cbranch_vccnz LSITE28_%=
LBACK28_%=:
MM v[76:79],a[208:211],v[164:167],v[132:135]
EX v64,v64
MM v[76:79],a[212:215],v[180:183],v[76:79]
EX v65,v65
MM v[76:79],a[216:219],v[196:199],v[76:79]
TR a[144:145],v232 offset:512
CV v64,v64,v65
MM v[76:79],a[220:223],v[212:215],v[76:79]
EX v66,v66
MM v[72:75],a[204:207],v[212:215],v[132:135]
EX v67,v67
MM v[72:75],a[200:203],v[196:199],v[72:75]
TR a[146:147],v232 offset:4608
CV v65,v66,v67
MM v[72:75],a[196:199],v[180:183],v[72:75]
EX v68,v68
MM v[72:75],a[192:195],v[164:167],v[72:75]
TR a[148:149],v232 offset:8704
TR a[150:151],v232 offset:12800
MM v[80:83],a[192:195],v[168:171],v[136:139]
EX v69,v69
MM v[80:83],a[196:199],v[184:187],v[80:83]
TR a[152:153],v233 offset:512
CV v66,v68,v69
MM v[80:83],a[200:203],v[200:203],v[80:83]
EX v70,v70
MM v[80:83],a[204:207],v[216:219],v[80:83]
TR a[154:155],v233 offset:4608
TR a[156:157],v233 offset:8704
MM v[84:87],a[220:223],v[216:219],v[136:139]
v_lshlrev_b32 v32, 3, v248
)ASM"
R"ASM(
v_min_u32 v32, 4992, v32
MM v[84:87],a[216:219],v[200:203],v[84:87]
s_sub_u32 s91, 5242880, s26
s_sub_u32 s60, s24, s91
s_subb_u32 s61, s25, 0
MM v[84:87],a[212:215],v[184:187],v[84:87]
s_mov_b32 s62, 5242880
EX v71,v71
MM v[84:87],a[208:211],v[168:171],v[84:87]
s_mov_b32 s63, 131072
TR a[158:159],v233 offset:12800
CV v67,v70,v71
MM v[92:95],a[208:211],v[172:175],v[140:143]
s_lshl_b32 s90, s16, 2
EX v72,v72
MM v[92:95],a[212:215],v[188:191],v[92:95]
s_add_u32 s90, s90, %[swave]
EX v73,v73
MM v[92:95],a[216:219],v[204:207],v[92:95]
s_mul_i32 s90, s90, 10240
TR a[160:161],v232 offset:1024
CV v72,v72,v73
MM v[92:95],a[220:223],v[220:223],v[92:95]
s_add_u32 s90, s90, 5120
EX v74,v74
MM v[88:91],a[204:207],v[220:223],v[140:143]
s_add_u32 s91, %[slds], 135168
s_mov_b32 m0, s91
s_nop 0
buffer_load_dword v32, s[60:63], s90 offen offset:0 lds
MM v[88:91],a[200:203],v[204:207],v[88:91]
EX v75,v75
MM v[88:91],a[196:199],v[188:191],v[88:91]
TR a[162:163],v232 offset:5120
CV v73,v74,v75
MM v[88:91],a[192:195],v[172:175],v[88:91]
EX v76,v76
MM v[96:99],a[224:227],v[160:163],v[128:131]
TR a[164:165],v232 offset:9216
TR a[166:167],v232 offset:13312
MM v[96:99],a[228:231],v[176:179],v[96:99]
EX v77,v77
MM v[96:99],a[232:235],v[192:195],v[96:99]
TR a[168:169],v233 offset:1024
CV v74,v76,v77
MM v[96:99],a[236:239],v[208:211],v[96:99]
EX v78,v78
MM v[100:103],a[252:255],v[208:211],v[128:131]
EX v79,v79
MM v[100:103],a[248:251],v[192:195],v[100:103]
TR a[170:171],v233 offset:5120
CV v75,v78,v79
MM v[100:103],a[244:247],v[176:179],v[100:103]
EX v80,v80
MM v[100:103],a[240:243],v[160:163],v[100:103]
TR a[172:173],v233 offset:9216
TR a[174:175],v233 offset:13312
MM v[108:111],a[240:243],v[164:167],v[132:135]
s_add_u32 s67, s95, 5120
EX v81,v81
MM v[108:111],a[244:247],v[180:183],v[108:111]
s_mov_b32 s70, 1
TR a[176:177],v232 offset:1536
CV v80,v80,v81
MM v[108:111],a[248:251],v[196:199],v[108:111]
EX v82,v82
MM v[108:111],a[252:255],v[212:215],v[108:111]
EX v83,v83
MM v[104:107],a[236:239],v[212:215],v[132:135]
TR a[178:179],v232 offset:5632
CV v81,v82,v83
MM v[104:107],a[232:235],v[196:199],v[104:107]
EX v84,v84
MM v[104:107],a[228:231],v[180:183],v[104:107]
TR a[180:181],v232 offset:9728
TR a[182:183],v232 offset:13824
MM v[104:107],a[224:227],v[164:167],v[104:107]
EX v85,v85
MM v[112:115],a[224:227],v[168:171],v[136:139]
TR a[184:185],v233 offset:1536
CV v82,v84,v85
MM v[112:115],a[228:231],v[184:187],v[112:115]
EX v86,v86
MM v[112:115],a[232:235],v[200:203],v[112:115]
EX v87,v87
MM v[112:115],a[236:239],v[216:219],v[112:115]
TR a[186:187],v233 offset:5632
CV v83,v86,v87
MM v[116:119],a[252:255],v[216:219],v[136:139]
EX v88,v88
MM v[116:119],a[248:251],v[200:203],v[116:119]
TR a[188:189],v233 offset:9728
TR a[190:191],v233 offset:13824
MM v[116:119],a[244:247],v[184:187],v[116:119]
EX v89,v89
MM v[116:119],a[240:243],v[168:171],v[116:119]
EX v90,v90
MM v[124:127],a[240:243],v[172:175],v[140:143]
EX v91,v91
MM v[124:127],a[244:247],v[188:191],v[124:127]
EX v92,v92
MM v[124:127],a[248:251],v[204:207],v[124:127]
EX v93,v93
MM v[124:127],a[252:255],v[220:223],v[124:127]
EX v94,v94
MM v[120:123],a[236:239],v[220:223],v[140:143]
EX v95,v95
MM v[120:123],a[232:235],v[204:207],v[120:123]
EX v96,v96
MM v[120:123],a[228:231],v[188:191],v[120:123]
EX v97,v97
MM v[120:123],a[224:227],v[172:175],v[120:123]
s_waitcnt vmcnt(14) lgkmcnt(0)
s_barrier
MM a[0:3],a[128:131],v[0:3],a[0:3]
s_add_u32 m0, s65, 0
CV v88,v88,v89
CV v89,v90,v91
MM a[0:3],a[132:135],v[4:7],a[0:3]
RD a[192:195],v224 offset:32768
CV v90,v92,v93
MM a[8:11],a[132:135],v[12:15],a[8:11]
RD a[196:199],v225 offset:32768
CV v91,v94,v95
MM a[8:11],a[128:131],v[8:11],a[8:11]
buffer_load_dwordx4 v228, s[8:11], s64 offen offset:0 lds
MM a[16:19],a[128:131],v[16:19],a[16:19]
RD a[200:203],v226 offset:32768
CV v68,v96,v97
MM a[16:19],a[132:135],v[20:23],a[16:19]
buffer_load_dwordx4 v229, s[8:11], s64 offen offset:1024 lds
MM a[24:27],a[132:135],v[28:31],a[24:27]
EX v98,v98
MM a[24:27],a[128:131],v[24:27],a[24:27]
RD a[204:207],v227 offset:32768
RD a[208:211],v224 offset:36864
MM a[28:31],a[136:139],v[24:27],a[28:31]
EX v99,v99
MM a[28:31],a[140:143],v[28:31],a[28:31]
RD a[212:215],v225 offset:36864
CV v69,v98,v99
MM a[20:23],a[140:143],v[20:23],a[20:23]
EX v100,v100
MM a[20:23],a[136:139],v[16:19],a[20:23]
EX v101,v101
MM a[12:15],a[136:139],v[8:11],a[12:15]
RD a[216:219],v226 offset:36864
CV v70,v100,v101
MM a[12:15],a[140:143],v[12:15],a[12:15]
buffer_load_dwordx4 v230, s[8:11], s64 offen offset:2048 lds
MM a[4:7],a[140:143],v[4:7],a[4:7]
EX v102,v102
MM a[4:7],a[136:139],v[0:3],a[4:7]
EX v103,v103
MM a[32:35],a[144:147],v[0:3],a[32:35]
RD a[220:223],v227 offset:36864
CV v71,v102,v103
MM a[32:35],a[148:151],v[4:7],a[32:35]
EX v104,v104
MM a[40:43],a[148:151],v[12:15],a[40:43]
EX v105,v105
MM a[40:43],a[144:147],v[8:11],a[40:43]
RD a[224:227],v224 offset:40960
CV v76,v104,v105
MM a[48:51],a[144:147],v[16:19],a[48:51]
buffer_load_dwordx4 v231, s[8:11], s64 offen offset:3072 lds
MM a[48:51],a[148:151],v[20:23],a[48:51]
s_add_u32 m0, s65, 65536
EX v106,v106
MM a[56:59],a[148:151],v[28:31],a[56:59]
EX v107,v107
MM a[56:59],a[144:147],v[24:27],a[56:59]
RD a[228:231],v225 offset:40960
CV v77,v106,v107
MM a[60:63],a[152:155],v[24:27],a[60:63]
EX v108,v108
MM a[60:63],a[156:159],v[28:31],a[60:63]
buffer_load_dwordx4 v235, s[12:15], s81 offen offset:0 lds
MM a[52:55],a[156:159],v[20:23],a[52:55]
EX v109,v109
MM a[52:55],a[152:155],v[16:19],a[52:55]
RD a[232:235],v226 offset:40960
CV v78,v108,v109
MM a[44:47],a[152:155],v[8:11],a[44:47]
EX v110,v110
MM a[44:47],a[156:159],v[12:15],a[44:47]
EX v111,v111
MM a[36:39],a[156:159],v[4:7],a[36:39]
RD a[236:239],v227 offset:40960
CV v79,v110,v111
MM a[36:39],a[152:155],v[0:3],a[36:39]
EX v112,v112
MM a[64:67],a[160:163],v[0:3],a[64:67]
buffer_load_dwordx4 v235, s[12:15], s64 offen offset:1024 lds
MM a[64:67],a[164:167],v[4:7],a[64:67]
EX v113,v113
MM a[72:75],a[164:167],v[12:15],a[72:75]
RD a[240:243],v224 offset:45056
CV v84,v112,v113
MM a[72:75],a[160:163],v[8:11],a[72:75]
EX v114,v114
MM a[80:83],a[160:163],v[16:19],a[80:83]
EX v115,v115
MM a[80:83],a[164:167],v[20:23],a[80:83]
RD a[244:247],v225 offset:45056
CV v85,v114,v115
MM a[88:91],a[164:167],v[28:31],a[88:91]
EX v116,v116
MM a[88:91],a[160:163],v[24:27],a[88:91]
buffer_load_dwordx4 v235, s[12:15], s81 offen offset:2048 lds
MM a[92:95],a[168:171],v[24:27],a[92:95]
EX v117,v117
MM a[92:95],a[172:175],v[28:31],a[92:95]
RD a[248:251],v226 offset:45056
CV v86,v116,v117
MM a[84:87],a[172:175],v[20:23],a[84:87]
EX v118,v118
MM a[84:87],a[168:171],v[16:19],a[84:87]
EX v119,v119
MM a[76:79],a[168:171],v[8:11],a[76:79]
RD a[252:255],v227 offset:45056
CV v87,v118,v119
MM a[76:79],a[172:175],v[12:15],a[76:79]
EX v120,v120
MM a[68:71],a[172:175],v[4:7],a[68:71]
buffer_load_dwordx4 v235, s[12:15], s64 offen offset:3072 lds
MM a[68:71],a[168:171],v[0:3],a[68:71]
EX v121,v121
MM a[96:99],a[176:179],v[0:3],a[96:99]
EX v122,v122
MM a[96:99],a[180:183],v[4:7],a[96:99]
EX v123,v123
MM a[104:107],a[180:183],v[12:15],a[104:107]
EX v124,v124
MM a[104:107],a[176:179],v[8:11],a[104:107]
EX v125,v125
MM a[112:115],a[176:179],v[16:19],a[112:115]
EX v126,v126
MM a[112:115],a[180:183],v[20:23],a[112:115]
EX v127,v127
MM a[120:123],a[180:183],v[28:31],a[120:123]
s_lshl_b32 s91, s47, 2
CV v92,v120,v121
CV v93,v122,v123
MM a[120:123],a[176:179],v[24:27],a[120:123]
s_add_u32 s91, s91, %[swave]
CV v94,v124,v125
CV v95,v126,v127
MM a[124:127],a[184:187],v[24:27],a[124:127]
s_lshl_b32 s91, s91, 2
v_add_f32 v244, v144, v145
v_add_f32 v245, v148, v149
MM a[124:127],a[188:191],v[28:31],a[124:127]
s_add_u32 s91, s91, 73728
v_add_f32 v246, v152, v153
v_add_f32 v247, v156, v157
MM a[116:119],a[188:191],v[20:23],a[116:119]
s_waitcnt vmcnt(12)
v_mov_b32 v250, s69
s_mov_b64 exec, 1
buffer_store_dword v250, off, s[20:23], s91 offset:0 sc1
s_mov_b64 exec, -1
MM a[116:119],a[184:187],v[16:19],a[116:119]
MM a[108:111],a[184:187],v[8:11],a[108:111]
MM a[108:111],a[188:191],v[12:15],a[108:111]
MM a[100:103],a[188:191],v[4:7],a[100:103]
MM a[100:103],a[184:187],v[0:3],a[100:103]
v_smfmac_f32_16x16x64_bf16 v[144:147], v[236:239], v[64:71], v234
v_smfmac_f32_16x16x64_bf16 v[148:151], v[236:239], v[72:79], v234
v_smfmac_f32_16x16x64_bf16 v[152:155], v[236:239], v[80:87], v234
v_smfmac_f32_16x16x64_bf16 v[156:159], v[236:239], v[88:95], v234
s_waitcnt lgkmcnt(0)
s_cmp_ge_u32 s16, 20
s_cbranch_scc1 LLAP_%=
MM v[0:3],a[192:195],v[160:163],v[128:131]
s_add_u32 s64, s64, 16384
TR a[128:129],v232 offset:16384
s_add_u32 s81, s64, 896
MM v[0:3],a[196:199],v[176:179],v[0:3]
TR a[130:131],v232 offset:20480
v_max3_f32 v249, v144, v145, v148
MM v[0:3],a[200:203],v[192:195],v[0:3]
TR a[132:133],v232 offset:24576
v_max3_f32 v249, v249, v149, v152
MM v[0:3],a[204:207],v[208:211],v[0:3]
TR a[134:135],v232 offset:28672
v_max3_f32 v249, v249, v153, v156
MM v[4:7],a[220:223],v[208:211],v[128:131]
TR a[136:137],v233 offset:16384
v_max_f32 v249, v249, v157
MM v[4:7],a[216:219],v[192:195],v[4:7]
TR a[138:139],v233 offset:20480
v_cmp_lt_f32 vcc, s71, v249
MM v[4:7],a[212:215],v[176:179],v[4:7]
TR a[140:141],v233 offset:24576
TR a[142:143],v233 offset:28672
MM v[4:7],a[208:211],v[160:163],v[4:7]
s_cbranch_vccnz LSITE29_%=
LBACK29_%=:
MM v[12:15],a[208:211],v[164:167],v[132:135]
EX v0,v0
MM v[12:15],a[212:215],v[180:183],v[12:15]
EX v1,v1
MM v[12:15],a[216:219],v[196:199],v[12:15]
TR a[144:145],v232 offset:16896
CV v0,v0,v1
MM v[12:15],a[220:223],v[212:215],v[12:15]
EX v2,v2
MM v[8:11],a[204:207],v[212:215],v[132:135]
EX v3,v3
MM v[8:11],a[200:203],v[196:199],v[8:11]
TR a[146:147],v232 offset:20992
CV v1,v2,v3
MM v[8:11],a[196:199],v[180:183],v[8:11]
EX v4,v4
MM v[8:11],a[192:195],v[164:167],v[8:11]
TR a[148:149],v232 offset:25088
TR a[150:151],v232 offset:29184
MM v[16:19],a[192:195],v[168:171],v[136:139]
EX v5,v5
MM v[16:19],a[196:199],v[184:187],v[16:19]
TR a[152:153],v233 offset:16896
CV v2,v4,v5
MM v[16:19],a[200:203],v[200:203],v[16:19]
EX v6,v6
MM v[16:19],a[204:207],v[216:219],v[16:19]
TR a[154:155],v233 offset:20992
TR a[156:157],v233 offset:25088
MM v[20:23],a[220:223],v[216:219],v[136:139]
s_add_u32 s87, s95, 5120
buffer_store_dword v240, v248, s[20:23], s87 offen offset:12 sc1
MM v[20:23],a[216:219],v[200:203],v[20:23]
EX v7,v7
MM v[20:23],a[212:215],v[184:187],v[20:23]
TR a[158:159],v233 offset:29184
CV v3,v6,v7
MM v[20:23],a[208:211],v[168:171],v[20:23]
EX v8,v8
MM v[28:31],a[208:211],v[172:175],v[140:143]
EX v9,v9
MM v[28:31],a[212:215],v[188:191],v[28:31]
TR a[160:161],v232 offset:17408
CV v8,v8,v9
MM v[28:31],a[216:219],v[204:207],v[28:31]
EX v10,v10
MM v[28:31],a[220:223],v[220:223],v[28:31]
EX v11,v11
MM v[24:27],a[204:207],v[220:223],v[140:143]
TR a[162:163],v232 offset:21504
CV v9,v10,v11
MM v[24:27],a[200:203],v[204:207],v[24:27]
TR a[164:165],v232 offset:25600
TR a[166:167],v232 offset:29696
MM v[24:27],a[196:199],v[188:191],v[24:27]
EX v12,v12
MM v[24:27],a[192:195],v[172:175],v[24:27]
EX v13,v13
MM v[32:35],a[224:227],v[160:163],v[128:131]
TR a[168:169],v233 offset:17408
CV v10,v12,v13
MM v[32:35],a[228:231],v[176:179],v[32:35]
EX v14,v14
MM v[32:35],a[232:235],v[192:195],v[32:35]
TR a[170:171],v233 offset:21504
TR a[172:173],v233 offset:25600
MM v[32:35],a[236:239],v[208:211],v[32:35]
EX v15,v15
MM v[36:39],a[252:255],v[208:211],v[128:131]
TR a[174:175],v233 offset:29696
CV v11,v14,v15
MM v[36:39],a[248:251],v[192:195],v[36:39]
EX v16,v16
MM v[36:39],a[244:247],v[176:179],v[36:39]
EX v17,v17
MM v[36:39],a[240:243],v[160:163],v[36:39]
TR a[176:177],v232 offset:17920
CV v16,v16,v17
MM v[44:47],a[240:243],v[164:167],v[132:135]
EX v18,v18
MM v[44:47],a[244:247],v[180:183],v[44:47]
TR a[178:179],v232 offset:22016
TR a[180:181],v232 offset:26112
MM v[44:47],a[248:251],v[196:199],v[44:47]
EX v19,v19
MM v[44:47],a[252:255],v[212:215],v[44:47]
s_nop 0
LPG21_%=:
s_cbranch_execz LTR4_%=
TR a[182:183],v232 offset:30208
CV v17,v18,v19
MM v[40:43],a[236:239],v[212:215],v[132:135]
EX v20,v20
MM v[40:43],a[232:235],v[196:199],v[40:43]
)ASM"
R"ASM(
EX v21,v21
MM v[40:43],a[228:231],v[180:183],v[40:43]
TR a[184:185],v233 offset:17920
CV v18,v20,v21
MM v[40:43],a[224:227],v[164:167],v[40:43]
EX v22,v22
MM v[48:51],a[224:227],v[168:171],v[136:139]
TR a[186:187],v233 offset:22016
TR a[188:189],v233 offset:26112
MM v[48:51],a[228:231],v[184:187],v[48:51]
EX v23,v23
MM v[48:51],a[232:235],v[200:203],v[48:51]
TR a[190:191],v233 offset:30208
CV v19,v22,v23
MM v[48:51],a[236:239],v[216:219],v[48:51]
EX v24,v24
MM v[52:55],a[252:255],v[216:219],v[136:139]
EX v25,v25
MM v[52:55],a[248:251],v[200:203],v[52:55]
EX v26,v26
MM v[52:55],a[244:247],v[184:187],v[52:55]
EX v27,v27
MM v[52:55],a[240:243],v[168:171],v[52:55]
EX v28,v28
MM v[60:63],a[240:243],v[172:175],v[140:143]
EX v29,v29
MM v[60:63],a[244:247],v[188:191],v[60:63]
EX v30,v30
MM v[60:63],a[248:251],v[204:207],v[60:63]
EX v31,v31
MM v[60:63],a[252:255],v[220:223],v[60:63]
EX v32,v32
MM v[56:59],a[236:239],v[220:223],v[140:143]
EX v33,v33
MM v[56:59],a[232:235],v[204:207],v[56:59]
EX v34,v34
MM v[56:59],a[228:231],v[188:191],v[56:59]
EX v35,v35
MM v[56:59],a[224:227],v[172:175],v[56:59]
s_waitcnt lgkmcnt(0)
s_barrier
MM a[0:3],a[128:131],v[64:67],a[0:3]
s_add_u32 m0, s65, 16384
CV v24,v24,v25
CV v25,v26,v27
MM a[0:3],a[132:135],v[68:71],a[0:3]
RD a[192:195],v224 offset:49152
CV v26,v28,v29
MM a[8:11],a[132:135],v[76:79],a[8:11]
RD a[196:199],v225 offset:49152
CV v27,v30,v31
MM a[8:11],a[128:131],v[72:75],a[8:11]
buffer_load_dwordx4 v228, s[8:11], s64 offen offset:0 lds
MM a[16:19],a[128:131],v[80:83],a[16:19]
RD a[200:203],v226 offset:49152
CV v4,v32,v33
MM a[16:19],a[132:135],v[84:87],a[16:19]
RD a[204:207],v227 offset:49152
CV v5,v34,v35
MM a[24:27],a[132:135],v[92:95],a[24:27]
buffer_load_dwordx4 v229, s[8:11], s64 offen offset:1024 lds
MM a[24:27],a[128:131],v[88:91],a[24:27]
RD a[208:211],v224 offset:53248
RD a[212:215],v225 offset:53248
MM a[28:31],a[136:139],v[88:91],a[28:31]
buffer_load_dwordx4 v230, s[8:11], s64 offen offset:2048 lds
MM a[28:31],a[140:143],v[92:95],a[28:31]
EX v36,v36
MM a[20:23],a[140:143],v[84:87],a[20:23]
EX v37,v37
MM a[20:23],a[136:139],v[80:83],a[20:23]
RD a[216:219],v226 offset:53248
CV v6,v36,v37
MM a[12:15],a[136:139],v[72:75],a[12:15]
EX v38,v38
MM a[12:15],a[140:143],v[76:79],a[12:15]
RD a[220:223],v227 offset:53248
RD a[224:227],v224 offset:57344
MM a[4:7],a[140:143],v[68:71],a[4:7]
EX v39,v39
MM a[4:7],a[136:139],v[64:67],a[4:7]
RD a[228:231],v225 offset:57344
CV v7,v38,v39
MM a[32:35],a[144:147],v[64:67],a[32:35]
EX v40,v40
MM a[32:35],a[148:151],v[68:71],a[32:35]
buffer_load_dwordx4 v231, s[8:11], s64 offen offset:3072 lds
MM a[40:43],a[148:151],v[76:79],a[40:43]
s_add_u32 m0, s65, 81920
EX v41,v41
MM a[40:43],a[144:147],v[72:75],a[40:43]
RD a[232:235],v226 offset:57344
CV v12,v40,v41
MM a[48:51],a[144:147],v[80:83],a[48:51]
EX v42,v42
MM a[48:51],a[148:151],v[84:87],a[48:51]
EX v43,v43
MM a[56:59],a[148:151],v[92:95],a[56:59]
RD a[236:239],v227 offset:57344
CV v13,v42,v43
MM a[56:59],a[144:147],v[88:91],a[56:59]
EX v44,v44
MM a[60:63],a[152:155],v[88:91],a[60:63]
buffer_load_dwordx4 v235, s[12:15], s81 offen offset:0 lds
MM a[60:63],a[156:159],v[92:95],a[60:63]
EX v45,v45
MM a[52:55],a[156:159],v[84:87],a[52:55]
RD a[240:243],v224 offset:61440
CV v14,v44,v45
MM a[52:55],a[152:155],v[80:83],a[52:55]
EX v46,v46
MM a[44:47],a[152:155],v[72:75],a[44:47]
EX v47,v47
MM a[44:47],a[156:159],v[76:79],a[44:47]
RD a[244:247],v225 offset:61440
CV v15,v46,v47
MM a[36:39],a[156:159],v[68:71],a[36:39]
buffer_load_dwordx4 v235, s[12:15], s64 offen offset:1024 lds
MM a[36:39],a[152:155],v[64:67],a[36:39]
EX v48,v48
MM a[64:67],a[160:163],v[64:67],a[64:67]
EX v49,v49
MM a[64:67],a[164:167],v[68:71],a[64:67]
RD a[248:251],v226 offset:61440
CV v20,v48,v49
MM a[72:75],a[164:167],v[76:79],a[72:75]
EX v50,v50
MM a[72:75],a[160:163],v[72:75],a[72:75]
EX v51,v51
MM a[80:83],a[160:163],v[80:83],a[80:83]
RD a[252:255],v227 offset:61440
CV v21,v50,v51
MM a[80:83],a[164:167],v[84:87],a[80:83]
buffer_load_dwordx4 v235, s[12:15], s81 offen offset:2048 lds
MM a[88:91],a[164:167],v[92:95],a[88:91]
EX v52,v52
MM a[88:91],a[160:163],v[88:91],a[88:91]
EX v53,v53
MM a[92:95],a[168:171],v[88:91],a[92:95]
EX v54,v54
MM a[92:95],a[172:175],v[92:95],a[92:95]
buffer_load_dwordx4 v235, s[12:15], s64 offen offset:3072 lds
MM a[84:87],a[172:175],v[84:87],a[84:87]
EX v55,v55
MM a[84:87],a[168:171],v[80:83],a[84:87]
EX v56,v56
MM a[76:79],a[168:171],v[72:75],a[76:79]
EX v57,v57
MM a[76:79],a[172:175],v[76:79],a[76:79]
EX v58,v58
MM a[68:71],a[172:175],v[68:71],a[68:71]
EX v59,v59
MM a[68:71],a[168:171],v[64:67],a[68:71]
EX v60,v60
MM a[96:99],a[176:179],v[64:67],a[96:99]
EX v61,v61
MM a[96:99],a[180:183],v[68:71],a[96:99]
EX v62,v62
MM a[104:107],a[180:183],v[76:79],a[104:107]
EX v63,v63
MM a[104:107],a[176:179],v[72:75],a[104:107]
CV v22,v52,v53
CV v23,v54,v55
MM a[112:115],a[176:179],v[80:83],a[112:115]
CV v28,v56,v57
CV v29,v58,v59
MM a[112:115],a[180:183],v[84:87],a[112:115]
CV v30,v60,v61
CV v31,v62,v63
MM a[120:123],a[180:183],v[92:95],a[120:123]
MM a[120:123],a[176:179],v[88:91],a[120:123]
MM a[124:127],a[184:187],v[88:91],a[124:127]
v_add_f32 v244, v144, v145
v_add_f32 v245, v148, v149
MM a[124:127],a[188:191],v[92:95],a[124:127]
v_add_f32 v246, v152, v153
v_add_f32 v247, v156, v157
MM a[116:119],a[188:191],v[84:87],a[116:119]
MM a[116:119],a[184:187],v[80:83],a[116:119]
MM a[108:111],a[184:187],v[72:75],a[108:111]
MM a[108:111],a[188:191],v[76:79],a[108:111]
MM a[100:103],a[188:191],v[68:71],a[100:103]
MM a[100:103],a[184:187],v[64:67],a[100:103]
v_smfmac_f32_16x16x64_bf16 v[144:147], v[236:239], v[0:7], v234
v_smfmac_f32_16x16x64_bf16 v[148:151], v[236:239], v[8:15], v234
v_smfmac_f32_16x16x64_bf16 v[152:155], v[236:239], v[16:23], v234
v_smfmac_f32_16x16x64_bf16 v[156:159], v[236:239], v[24:31], v234
s_waitcnt lgkmcnt(0)
MM v[64:67],a[192:195],v[160:163],v[128:131]
s_add_u32 s64, s64, 16384
TR a[128:129],v232 offset:32768
s_add_u32 s81, s64, 896
MM v[64:67],a[196:199],v[176:179],v[64:67]
TR a[130:131],v232 offset:36864
v_max3_f32 v249, v144, v145, v148
MM v[64:67],a[200:203],v[192:195],v[64:67]
TR a[132:133],v232 offset:40960
v_max3_f32 v249, v249, v149, v152
MM v[64:67],a[204:207],v[208:211],v[64:67]
TR a[134:135],v232 offset:45056
v_max3_f32 v249, v249, v153, v156
MM v[68:71],a[220:223],v[208:211],v[128:131]
TR a[136:137],v233 offset:32768
v_max_f32 v249, v249, v157
MM v[68:71],a[216:219],v[192:195],v[68:71]
TR a[138:139],v233 offset:36864
v_cmp_lt_f32 vcc, s71, v249
MM v[68:71],a[212:215],v[176:179],v[68:71]
TR a[140:141],v233 offset:40960
TR a[142:143],v233 offset:45056
MM v[68:71],a[208:211],v[160:163],v[68:71]
s_cbranch_vccnz LSITE30_%=
LBACK30_%=:
MM v[76:79],a[208:211],v[164:167],v[132:135]
s_and_b32 s90, s17, 63
s_cmp_lt_u32 s17, 80
s_cselect_b32 s17, s17, s90
s_cselect_b64 s[44:45], -1, 0
MM v[76:79],a[212:215],v[180:183],v[76:79]
v_lshrrev_b32 v253, 4, v248
v_min_u32 v253, 35, v253
MM v[76:79],a[216:219],v[196:199],v[76:79]
v_lshrrev_b32 v252, 2, v253
v_mul_u32_u24 v252, 144, v252
MM v[76:79],a[220:223],v[212:215],v[76:79]
TR a[144:145],v232 offset:33280
v_lshl_add_u32 v253, v253, 2, v252
MM v[72:75],a[204:207],v[212:215],v[132:135]
s_lshr_b32 s90, s17, 3
EX v64,v64
MM v[72:75],a[200:203],v[196:199],v[72:75]
s_lshl_b32 s76, s90, 4
EX v65,v65
MM v[72:75],a[196:199],v[180:183],v[72:75]
s_add_u32 s76, s76, 73728
buffer_load_dword v252, v253, s[20:23], s76 offen offset:0 sc1
MM v[72:75],a[192:195],v[164:167],v[72:75]
EX v66,v66
MM v[80:83],a[192:195],v[168:171],v[136:139]
EX v67,v67
MM v[80:83],a[196:199],v[184:187],v[80:83]
EX v68,v68
MM v[80:83],a[200:203],v[200:203],v[80:83]
TR a[146:147],v232 offset:37376
CV v64,v64,v65
MM v[80:83],a[204:207],v[216:219],v[80:83]
EX v69,v69
MM v[84:87],a[220:223],v[216:219],v[136:139]
TR a[148:149],v232 offset:41472
CV v65,v66,v67
MM v[84:87],a[216:219],v[200:203],v[84:87]
EX v70,v70
MM v[84:87],a[212:215],v[184:187],v[84:87]
TR a[150:151],v232 offset:45568
CV v66,v68,v69
MM v[84:87],a[208:211],v[168:171],v[84:87]
EX v71,v71
MM v[92:95],a[208:211],v[172:175],v[140:143]
TR a[152:153],v233 offset:33280
TR a[154:155],v233 offset:37376
MM v[92:95],a[212:215],v[188:191],v[92:95]
EX v72,v72
MM v[92:95],a[216:219],v[204:207],v[92:95]
EX v73,v73
MM v[92:95],a[220:223],v[220:223],v[92:95]
TR a[156:157],v233 offset:41472
CV v67,v70,v71
MM v[88:91],a[204:207],v[220:223],v[140:143]
EX v74,v74
MM v[88:91],a[200:203],v[204:207],v[88:91]
TR a[158:159],v233 offset:45568
CV v72,v72,v73
MM v[88:91],a[196:199],v[188:191],v[88:91]
EX v75,v75
MM v[88:91],a[192:195],v[172:175],v[88:91]
TR a[160:161],v232 offset:33792
TR a[162:163],v232 offset:37888
MM v[96:99],a[224:227],v[160:163],v[128:131]
EX v76,v76
MM v[96:99],a[228:231],v[176:179],v[96:99]
TR a[164:165],v232 offset:41984
CV v73,v74,v75
MM v[96:99],a[232:235],v[192:195],v[96:99]
EX v77,v77
MM v[96:99],a[236:239],v[208:211],v[96:99]
EX v78,v78
MM v[100:103],a[252:255],v[208:211],v[128:131]
TR a[166:167],v232 offset:46080
CV v74,v76,v77
MM v[100:103],a[248:251],v[192:195],v[100:103]
EX v79,v79
MM v[100:103],a[244:247],v[176:179],v[100:103]
TR a[168:169],v233 offset:33792
CV v75,v78,v79
MM v[100:103],a[240:243],v[160:163],v[100:103]
EX v80,v80
MM v[108:111],a[240:243],v[164:167],v[132:135]
TR a[170:171],v233 offset:37888
TR a[172:173],v233 offset:41984
MM v[108:111],a[244:247],v[180:183],v[108:111]
EX v81,v81
MM v[108:111],a[248:251],v[196:199],v[108:111]
TR a[174:175],v233 offset:46080
CV v80,v80,v81
MM v[108:111],a[252:255],v[212:215],v[108:111]
EX v82,v82
MM v[104:107],a[236:239],v[212:215],v[132:135]
EX v83,v83
MM v[104:107],a[232:235],v[196:199],v[104:107]
TR a[176:177],v232 offset:34304
TR a[178:179],v232 offset:38400
MM v[104:107],a[228:231],v[180:183],v[104:107]
EX v84,v84
MM v[104:107],a[224:227],v[164:167],v[104:107]
TR a[180:181],v232 offset:42496
CV v81,v82,v83
MM v[112:115],a[224:227],v[168:171],v[136:139]
EX v85,v85
MM v[112:115],a[228:231],v[184:187],v[112:115]
TR a[182:183],v232 offset:46592
CV v82,v84,v85
MM v[112:115],a[232:235],v[200:203],v[112:115]
EX v86,v86
MM v[112:115],a[236:239],v[216:219],v[112:115]
TR a[184:185],v233 offset:34304
TR a[186:187],v233 offset:38400
MM v[116:119],a[252:255],v[216:219],v[136:139]
EX v87,v87
MM v[116:119],a[248:251],v[200:203],v[116:119]
TR a[188:189],v233 offset:42496
CV v83,v86,v87
MM v[116:119],a[244:247],v[184:187],v[116:119]
EX v88,v88
MM v[116:119],a[240:243],v[168:171],v[116:119]
EX v89,v89
MM v[124:127],a[240:243],v[172:175],v[140:143]
TR a[190:191],v233 offset:46592
CV v88,v88,v89
MM v[124:127],a[244:247],v[188:191],v[124:127]
EX v90,v90
MM v[124:127],a[248:251],v[204:207],v[124:127]
EX v91,v91
MM v[124:127],a[252:255],v[220:223],v[124:127]
EX v92,v92
MM v[120:123],a[236:239],v[220:223],v[140:143]
EX v93,v93
MM v[120:123],a[232:235],v[204:207],v[120:123]
EX v94,v94
MM v[120:123],a[228:231],v[188:191],v[120:123]
EX v95,v95
MM v[120:123],a[224:227],v[172:175],v[120:123]
s_waitcnt vmcnt(15) lgkmcnt(0)
s_barrier
MM a[0:3],a[128:131],v[0:3],a[0:3]
s_add_u32 m0, s65, 32768
CV v89,v90,v91
CV v90,v92,v93
MM a[0:3],a[132:135],v[4:7],a[0:3]
RD a[192:195],v224 offset:0
CV v91,v94,v95
MM a[8:11],a[132:135],v[12:15],a[8:11]
buffer_load_dwordx4 v228, s[8:11], s64 offen offset:0 lds
MM a[8:11],a[128:131],v[8:11],a[8:11]
RD a[196:199],v225 offset:0
RD a[200:203],v226 offset:0
MM a[16:19],a[128:131],v[16:19],a[16:19]
EX v96,v96
MM a[16:19],a[132:135],v[20:23],a[16:19]
EX v97,v97
MM a[24:27],a[132:135],v[28:31],a[24:27]
RD a[204:207],v227 offset:0
CV v68,v96,v97
MM a[24:27],a[128:131],v[24:27],a[24:27]
EX v98,v98
MM a[28:31],a[136:139],v[24:27],a[28:31]
buffer_load_dwordx4 v229, s[8:11], s64 offen offset:1024 lds
MM a[28:31],a[140:143],v[28:31],a[28:31]
EX v99,v99
MM a[20:23],a[140:143],v[20:23],a[20:23]
RD a[208:211],v224 offset:4096
CV v69,v98,v99
MM a[20:23],a[136:139],v[16:19],a[20:23]
EX v100,v100
MM a[12:15],a[136:139],v[8:11],a[12:15]
EX v101,v101
MM a[12:15],a[140:143],v[12:15],a[12:15]
RD a[212:215],v225 offset:4096
CV v70,v100,v101
MM a[4:7],a[140:143],v[4:7],a[4:7]
)ASM"
R"ASM(
EX v102,v102
MM a[4:7],a[136:139],v[0:3],a[4:7]
buffer_load_dwordx4 v230, s[8:11], s64 offen offset:2048 lds
MM a[32:35],a[144:147],v[0:3],a[32:35]
EX v103,v103
MM a[32:35],a[148:151],v[4:7],a[32:35]
RD a[216:219],v226 offset:4096
CV v71,v102,v103
MM a[40:43],a[148:151],v[12:15],a[40:43]
EX v104,v104
MM a[40:43],a[144:147],v[8:11],a[40:43]
EX v105,v105
MM a[48:51],a[144:147],v[16:19],a[48:51]
RD a[220:223],v227 offset:4096
CV v76,v104,v105
MM a[48:51],a[148:151],v[20:23],a[48:51]
EX v106,v106
MM a[56:59],a[148:151],v[28:31],a[56:59]
buffer_load_dwordx4 v231, s[8:11], s64 offen offset:3072 lds
MM a[56:59],a[144:147],v[24:27],a[56:59]
s_add_u32 m0, s65, 98304
EX v107,v107
MM a[60:63],a[152:155],v[24:27],a[60:63]
RD a[224:227],v224 offset:8192
CV v77,v106,v107
MM a[60:63],a[156:159],v[28:31],a[60:63]
EX v108,v108
MM a[52:55],a[156:159],v[20:23],a[52:55]
EX v109,v109
MM a[52:55],a[152:155],v[16:19],a[52:55]
RD a[228:231],v225 offset:8192
CV v78,v108,v109
MM a[44:47],a[152:155],v[8:11],a[44:47]
EX v110,v110
MM a[44:47],a[156:159],v[12:15],a[44:47]
buffer_load_dwordx4 v235, s[12:15], s81 offen offset:0 lds
MM a[36:39],a[156:159],v[4:7],a[36:39]
EX v111,v111
MM a[36:39],a[152:155],v[0:3],a[36:39]
RD a[232:235],v226 offset:8192
CV v79,v110,v111
MM a[64:67],a[160:163],v[0:3],a[64:67]
EX v112,v112
MM a[64:67],a[164:167],v[4:7],a[64:67]
EX v113,v113
MM a[72:75],a[164:167],v[12:15],a[72:75]
RD a[236:239],v227 offset:8192
CV v84,v112,v113
MM a[72:75],a[160:163],v[8:11],a[72:75]
buffer_load_dwordx4 v235, s[12:15], s64 offen offset:1024 lds
MM a[80:83],a[160:163],v[16:19],a[80:83]
EX v114,v114
MM a[80:83],a[164:167],v[20:23],a[80:83]
EX v115,v115
MM a[88:91],a[164:167],v[28:31],a[88:91]
RD a[240:243],v224 offset:12288
CV v85,v114,v115
MM a[88:91],a[160:163],v[24:27],a[88:91]
EX v116,v116
MM a[92:95],a[168:171],v[24:27],a[92:95]
EX v117,v117
MM a[92:95],a[172:175],v[28:31],a[92:95]
RD a[244:247],v225 offset:12288
CV v86,v116,v117
MM a[84:87],a[172:175],v[20:23],a[84:87]
buffer_load_dwordx4 v235, s[12:15], s81 offen offset:2048 lds
MM a[84:87],a[168:171],v[16:19],a[84:87]
EX v118,v118
MM a[76:79],a[168:171],v[8:11],a[76:79]
EX v119,v119
MM a[76:79],a[172:175],v[12:15],a[76:79]
RD a[248:251],v226 offset:12288
CV v87,v118,v119
MM a[68:71],a[172:175],v[4:7],a[68:71]
EX v120,v120
MM a[68:71],a[168:171],v[0:3],a[68:71]
EX v121,v121
MM a[96:99],a[176:179],v[0:3],a[96:99]
RD a[252:255],v227 offset:12288
CV v92,v120,v121
MM a[96:99],a[180:183],v[4:7],a[96:99]
buffer_load_dwordx4 v235, s[12:15], s64 offen offset:3072 lds
MM a[104:107],a[180:183],v[12:15],a[104:107]
EX v122,v122
MM a[104:107],a[176:179],v[8:11],a[104:107]
EX v123,v123
MM a[112:115],a[176:179],v[16:19],a[112:115]
EX v124,v124
MM a[112:115],a[180:183],v[20:23],a[112:115]
EX v125,v125
MM a[120:123],a[180:183],v[28:31],a[120:123]
EX v126,v126
MM a[120:123],a[176:179],v[24:27],a[120:123]
EX v127,v127
MM a[124:127],a[184:187],v[24:27],a[124:127]
v_add_f32 v244, v144, v145
v_add_f32 v245, v148, v149
MM a[124:127],a[188:191],v[28:31],a[124:127]
v_add_f32 v246, v152, v153
v_add_f32 v247, v156, v157
MM a[116:119],a[188:191],v[20:23],a[116:119]
CV v93,v122,v123
CV v94,v124,v125
MM a[116:119],a[184:187],v[16:19],a[116:119]
CV v95,v126,v127
MM a[108:111],a[184:187],v[8:11],a[108:111]
MM a[108:111],a[188:191],v[12:15],a[108:111]
MM a[100:103],a[188:191],v[4:7],a[100:103]
MM a[100:103],a[184:187],v[0:3],a[100:103]
v_smfmac_f32_16x16x64_bf16 v[144:147], v[236:239], v[64:71], v234
v_smfmac_f32_16x16x64_bf16 v[148:151], v[236:239], v[72:79], v234
v_smfmac_f32_16x16x64_bf16 v[152:155], v[236:239], v[80:87], v234
v_smfmac_f32_16x16x64_bf16 v[156:159], v[236:239], v[88:95], v234
s_waitcnt lgkmcnt(0)
MM v[0:3],a[192:195],v[160:163],v[128:131]
s_add_u32 s64, s64, 16384
TR a[128:129],v232 offset:49152
s_add_u32 s81, s64, 896
MM v[0:3],a[196:199],v[176:179],v[0:3]
TR a[130:131],v232 offset:53248
v_max3_f32 v249, v144, v145, v148
MM v[0:3],a[200:203],v[192:195],v[0:3]
TR a[132:133],v232 offset:57344
v_max3_f32 v249, v249, v149, v152
MM v[0:3],a[204:207],v[208:211],v[0:3]
TR a[134:135],v232 offset:61440
v_max3_f32 v249, v249, v153, v156
MM v[4:7],a[220:223],v[208:211],v[128:131]
TR a[136:137],v233 offset:49152
v_max_f32 v249, v249, v157
MM v[4:7],a[216:219],v[192:195],v[4:7]
TR a[138:139],v233 offset:53248
v_cmp_lt_f32 vcc, s71, v249
MM v[4:7],a[212:215],v[176:179],v[4:7]
TR a[140:141],v233 offset:57344
TR a[142:143],v233 offset:61440
MM v[4:7],a[208:211],v[160:163],v[4:7]
s_cbranch_vccnz LSITE31_%=
LBACK31_%=:
MM v[12:15],a[208:211],v[164:167],v[132:135]
s_waitcnt vmcnt(8)
v_cmp_ne_u32 vcc, s69, v252
s_cbranch_vccnz LRFY_%=
LRFYb_%=:
MM v[12:15],a[212:215],v[180:183],v[12:15]
v_lshrrev_b32 v109, 2, v248
v_lshrrev_b32 v110, 3, v248
MM v[12:15],a[216:219],v[196:199],v[12:15]
s_and_b32 s76, s17, 7
EX v0,v0
MM v[12:15],a[220:223],v[212:215],v[12:15]
s_mul_i32 s76, s76, 2560
EX v1,v1
MM v[8:11],a[204:207],v[212:215],v[132:135]
s_lshl_b32 s91, s90, 8
EX v2,v2
MM v[8:11],a[200:203],v[196:199],v[8:11]
s_add_u32 s76, s76, s91
TR a[144:145],v232 offset:49664
CV v0,v0,v1
MM v[8:11],a[196:199],v[180:183],v[8:11]
s_add_u32 s76, s76, 21659648
buffer_load_dword v96, v109, s[20:23], s76 offen offset:0 sc1
MM v[8:11],a[192:195],v[164:167],v[8:11]
s_add_u32 s76, s76, 20480
EX v3,v3
MM v[16:19],a[192:195],v[168:171],v[136:139]
buffer_load_dword v97, v109, s[20:23], s76 offen offset:0 sc1
MM v[16:19],a[196:199],v[184:187],v[16:19]
s_add_u32 s76, s76, 20480
EX v4,v4
MM v[16:19],a[200:203],v[200:203],v[16:19]
buffer_load_dword v98, v109, s[20:23], s76 offen offset:0 sc1
MM v[16:19],a[204:207],v[216:219],v[16:19]
s_add_u32 s76, s76, 20480
EX v5,v5
MM v[20:23],a[220:223],v[216:219],v[136:139]
buffer_load_dword v99, v109, s[20:23], s76 offen offset:0 sc1
MM v[20:23],a[216:219],v[200:203],v[20:23]
s_add_u32 s76, s76, 20480
TR a[146:147],v232 offset:53760
TR a[148:149],v232 offset:57856
MM v[20:23],a[212:215],v[184:187],v[20:23]
buffer_load_dword v100, v109, s[20:23], s76 offen offset:0 sc1
MM v[20:23],a[208:211],v[168:171],v[20:23]
s_add_u32 s76, s76, 20480
EX v6,v6
MM v[28:31],a[208:211],v[172:175],v[140:143]
buffer_load_dword v101, v109, s[20:23], s76 offen offset:0 sc1
MM v[28:31],a[212:215],v[188:191],v[28:31]
s_add_u32 s76, s76, 20480
EX v7,v7
MM v[28:31],a[216:219],v[204:207],v[28:31]
buffer_load_dword v102, v109, s[20:23], s76 offen offset:0 sc1
MM v[28:31],a[220:223],v[220:223],v[28:31]
s_add_u32 s76, s76, 20480
s_nop 0
LPG22_%=:
s_cbranch_execz LTR4_%=
TR a[150:151],v232 offset:61952
EX v8,v8
MM v[24:27],a[204:207],v[220:223],v[140:143]
buffer_load_dword v103, v109, s[20:23], s76 offen offset:0 sc1
MM v[24:27],a[200:203],v[204:207],v[24:27]
s_add_u32 s76, s76, 20480
TR a[152:153],v233 offset:49664
EX v9,v9
CV v1,v2,v3
MM v[24:27],a[196:199],v[188:191],v[24:27]
buffer_load_dword v104, v109, s[20:23], s76 offen offset:0 sc1
TR a[154:155],v233 offset:53760
MM v[24:27],a[192:195],v[172:175],v[24:27]
s_and_b32 s76, s19, 65535
EX v10,v10
MM v[32:35],a[224:227],v[160:163],v[128:131]
s_sub_u32 s76, 65535, s76
TR a[156:157],v233 offset:57856
CV v2,v4,v5
EX v11,v11
MM v[32:35],a[228:231],v[176:179],v[32:35]
s_mul_i32 s76, s76, 1280
TR a[158:159],v233 offset:61952
CV v3,v6,v7
MM v[32:35],a[232:235],v[192:195],v[32:35]
s_lshl_b32 s91, s90, 7
EX v12,v12
MM v[32:35],a[236:239],v[208:211],v[32:35]
s_add_u32 s76, s76, s91
TR a[160:161],v232 offset:50176
TR a[162:163],v232 offset:54272
MM v[36:39],a[252:255],v[208:211],v[128:131]
s_add_u32 s76, s76, 131072
buffer_load_ushort v108, v110, s[20:23], s76 offen sc1
EX v13,v13
MM v[36:39],a[248:251],v[192:195],v[36:39]
s_ashr_i32 s91, s19, 31
EX v14,v14
MM v[36:39],a[244:247],v[176:179],v[36:39]
s_not_b32 s91, s91
TR a[164:165],v232 offset:58368
CV v8,v8,v9
MM v[36:39],a[240:243],v[160:163],v[36:39]
s_or_b32 s91, s91, 0x80000000
EX v15,v15
MM v[44:47],a[240:243],v[164:167],v[132:135]
s_xor_b32 s19, s19, s91
TR a[166:167],v232 offset:62464
CV v9,v10,v11
MM v[44:47],a[244:247],v[180:183],v[44:47]
s_and_b32 s19, s19, 0xffff0000
v_mov_b32 v111, 0xbfb8aa3b
v_mul_f32 v111, s19, v111
MM v[44:47],a[248:251],v[196:199],v[44:47]
EX v111,v111
EX v16,v16
MM v[44:47],a[252:255],v[212:215],v[44:47]
TR a[168:169],v233 offset:50176
v_add_f32 v111, 1.0, v111
EX v17,v17
MM v[40:43],a[236:239],v[212:215],v[132:135]
v_rcp_f32 v111, v111
MM v[40:43],a[232:235],v[196:199],v[40:43]
TR a[170:171],v233 offset:54272
CV v111,v111,v111
v_and_b32 v111, 0xffff0000, v111
EX v18,v18
MM v[40:43],a[228:231],v[180:183],v[40:43]
TR a[172:173],v233 offset:58368
CV v10,v12,v13
MM v[40:43],a[224:227],v[164:167],v[40:43]
EX v19,v19
MM v[48:51],a[224:227],v[168:171],v[136:139]
TR a[174:175],v233 offset:62464
CV v11,v14,v15
MM v[48:51],a[228:231],v[184:187],v[48:51]
EX v20,v20
MM v[48:51],a[232:235],v[200:203],v[48:51]
TR a[176:177],v232 offset:50688
TR a[178:179],v232 offset:54784
MM v[48:51],a[236:239],v[216:219],v[48:51]
EX v21,v21
MM v[52:55],a[252:255],v[216:219],v[136:139]
EX v22,v22
MM v[52:55],a[248:251],v[200:203],v[52:55]
TR a[180:181],v232 offset:58880
CV v16,v16,v17
MM v[52:55],a[244:247],v[184:187],v[52:55]
EX v23,v23
MM v[52:55],a[240:243],v[168:171],v[52:55]
TR a[182:183],v232 offset:62976
CV v17,v18,v19
MM v[60:63],a[240:243],v[172:175],v[140:143]
EX v24,v24
MM v[60:63],a[244:247],v[188:191],v[60:63]
TR a[184:185],v233 offset:50688
TR a[186:187],v233 offset:54784
MM v[60:63],a[248:251],v[204:207],v[60:63]
TR a[188:189],v233 offset:58880
TR a[190:191],v233 offset:62976
EX v25,v25
MM v[60:63],a[252:255],v[220:223],v[60:63]
EX v26,v26
MM v[56:59],a[236:239],v[220:223],v[140:143]
EX v27,v27
MM v[56:59],a[232:235],v[204:207],v[56:59]
EX v28,v28
MM v[56:59],a[228:231],v[188:191],v[56:59]
EX v29,v29
CV v18,v20,v21
MM v[56:59],a[224:227],v[172:175],v[56:59]
s_waitcnt lgkmcnt(0)
s_barrier
MM a[0:3],a[128:131],v[64:67],a[0:3]
s_add_u32 m0, s65, 49152
CV v19,v22,v23
CV v24,v24,v25
MM a[0:3],a[132:135],v[68:71],a[0:3]
CV v25,v26,v27
CV v26,v28,v29
MM a[8:11],a[132:135],v[76:79],a[8:11]
EX v30,v30
MM a[8:11],a[128:131],v[72:75],a[8:11]
EX v31,v31
MM a[16:19],a[128:131],v[80:83],a[16:19]
RD a[192:195],v224 offset:16384
CV v27,v30,v31
MM a[16:19],a[132:135],v[84:87],a[16:19]
EX v32,v32
MM a[24:27],a[132:135],v[92:95],a[24:27]
buffer_load_dwordx4 v228, s[8:11], s64 offen offset:0 lds
MM a[24:27],a[128:131],v[88:91],a[24:27]
EX v33,v33
MM a[28:31],a[136:139],v[88:91],a[28:31]
RD a[196:199],v225 offset:16384
CV v4,v32,v33
MM a[28:31],a[140:143],v[92:95],a[28:31]
EX v34,v34
MM a[20:23],a[140:143],v[84:87],a[20:23]
EX v35,v35
MM a[20:23],a[136:139],v[80:83],a[20:23]
RD a[200:203],v226 offset:16384
CV v5,v34,v35
MM a[12:15],a[136:139],v[72:75],a[12:15]
EX v36,v36
MM a[12:15],a[140:143],v[76:79],a[12:15]
buffer_load_dwordx4 v229, s[8:11], s64 offen offset:1024 lds
MM a[4:7],a[140:143],v[68:71],a[4:7]
EX v37,v37
MM a[4:7],a[136:139],v[64:67],a[4:7]
RD a[204:207],v227 offset:16384
CV v6,v36,v37
MM a[32:35],a[144:147],v[64:67],a[32:35]
EX v38,v38
MM a[32:35],a[148:151],v[68:71],a[32:35]
EX v39,v39
MM a[40:43],a[148:151],v[76:79],a[40:43]
RD a[208:211],v224 offset:20480
CV v7,v38,v39
MM a[40:43],a[144:147],v[72:75],a[40:43]
EX v40,v40
MM a[48:51],a[144:147],v[80:83],a[48:51]
buffer_load_dwordx4 v230, s[8:11], s64 offen offset:2048 lds
MM a[48:51],a[148:151],v[84:87],a[48:51]
EX v41,v41
MM a[56:59],a[148:151],v[92:95],a[56:59]
RD a[212:215],v225 offset:20480
CV v12,v40,v41
MM a[56:59],a[144:147],v[88:91],a[56:59]
EX v42,v42
MM a[60:63],a[152:155],v[88:91],a[60:63]
EX v43,v43
MM a[60:63],a[156:159],v[92:95],a[60:63]
RD a[216:219],v226 offset:20480
CV v13,v42,v43
MM a[52:55],a[156:159],v[84:87],a[52:55]
EX v44,v44
MM a[52:55],a[152:155],v[80:83],a[52:55]
buffer_load_dwordx4 v231, s[8:11], s64 offen offset:3072 lds
MM a[44:47],a[152:155],v[72:75],a[44:47]
s_add_u32 m0, s65, 114688
EX v45,v45
MM a[44:47],a[156:159],v[76:79],a[44:47]
RD a[220:223],v227 offset:20480
CV v14,v44,v45
MM a[36:39],a[156:159],v[68:71],a[36:39]
EX v46,v46
MM a[36:39],a[152:155],v[64:67],a[36:39]
RD a[224:227],v224 offset:24576
)ASM"
R"ASM(
RD a[228:231],v225 offset:24576
MM a[64:67],a[160:163],v[64:67],a[64:67]
EX v47,v47
MM a[64:67],a[164:167],v[68:71],a[64:67]
RD a[232:235],v226 offset:24576
CV v15,v46,v47
MM a[72:75],a[164:167],v[76:79],a[72:75]
EX v48,v48
MM a[72:75],a[160:163],v[72:75],a[72:75]
buffer_load_dwordx4 v235, s[12:15], s81 offen offset:0 lds
MM a[80:83],a[160:163],v[80:83],a[80:83]
EX v49,v49
MM a[80:83],a[164:167],v[84:87],a[80:83]
RD a[236:239],v227 offset:24576
CV v20,v48,v49
MM a[88:91],a[164:167],v[92:95],a[88:91]
EX v50,v50
MM a[88:91],a[160:163],v[88:91],a[88:91]
EX v51,v51
MM a[92:95],a[168:171],v[88:91],a[92:95]
RD a[240:243],v224 offset:28672
CV v21,v50,v51
MM a[92:95],a[172:175],v[92:95],a[92:95]
EX v52,v52
MM a[84:87],a[172:175],v[84:87],a[84:87]
buffer_load_dwordx4 v235, s[12:15], s64 offen offset:1024 lds
MM a[84:87],a[168:171],v[80:83],a[84:87]
EX v53,v53
MM a[76:79],a[168:171],v[72:75],a[76:79]
RD a[244:247],v225 offset:28672
CV v22,v52,v53
MM a[76:79],a[172:175],v[76:79],a[76:79]
EX v54,v54
MM a[68:71],a[172:175],v[68:71],a[68:71]
s_waitcnt vmcnt(6)
v_add_f32 v96, v96, v100
v_add_f32 v97, v97, v101
MM a[68:71],a[168:171],v[64:67],a[68:71]
v_add_f32 v98, v98, v102
v_add_f32 v99, v99, v103
MM a[96:99],a[176:179],v[64:67],a[96:99]
v_add_f32 v96, v96, v98
v_add_f32 v97, v97, v99
MM a[96:99],a[180:183],v[68:71],a[96:99]
v_add_f32 v96, v96, v104
v_lshlrev_b32 v108, 16, v108
MM a[104:107],a[180:183],v[76:79],a[104:107]
v_add_f32 v96, v96, v97
CV v96,v96,v96
EX v55,v55
MM a[104:107],a[176:179],v[72:75],a[104:107]
v_and_b32 v96, 0xffff0000, v96
v_mul_f32 v96, v96, v111
MM a[112:115],a[176:179],v[80:83],a[112:115]
CV v96,v96,v96
v_and_b32 v96, 0xffff0000, v96
EX v56,v56
MM a[112:115],a[180:183],v[84:87],a[112:115]
v_add_f32 v96, v96, v108
CV v96,v96,v96
MM a[120:123],a[180:183],v[92:95],a[120:123]
s_mov_b64 exec, s[44:45]
buffer_store_short v96, v110, s[20:23], s76 offen offset:0 sc1
s_mov_b64 exec, -1
s_nop 0
EX v57,v57
MM a[120:123],a[176:179],v[88:91],a[120:123]
EX v58,v58
MM a[124:127],a[184:187],v[88:91],a[124:127]
v_add_f32 v244, v144, v145
v_add_f32 v245, v148, v149
MM a[124:127],a[188:191],v[92:95],a[124:127]
v_add_f32 v246, v152, v153
v_add_f32 v247, v156, v157
MM a[116:119],a[188:191],v[84:87],a[116:119]
EX v59,v59
MM a[116:119],a[184:187],v[80:83],a[116:119]
EX v60,v60
MM a[108:111],a[184:187],v[72:75],a[108:111]
CV v23,v54,v55
CV v28,v56,v57
MM a[108:111],a[188:191],v[76:79],a[108:111]
EX v61,v61
MM a[100:103],a[188:191],v[68:71],a[100:103]
buffer_load_dwordx4 v235, s[12:15], s81 offen offset:2048 lds
MM a[100:103],a[184:187],v[64:67],a[100:103]
EX v62,v62
v_smfmac_f32_16x16x64_bf16 v[144:147], v[236:239], v[0:7], v234
RD a[248:251],v226 offset:28672
RD a[252:255],v227 offset:28672
EX v63,v63
v_smfmac_f32_16x16x64_bf16 v[148:151], v[236:239], v[8:15], v234
CV v29,v58,v59
v_smfmac_f32_16x16x64_bf16 v[152:155], v[236:239], v[16:23], v234
CV v30,v60,v61
CV v31,v62,v63
s_nop 1
v_smfmac_f32_16x16x64_bf16 v[156:159], v[236:239], v[24:31], v234
s_waitcnt lgkmcnt(0)
buffer_load_dwordx4 v235, s[12:15], s64 offen offset:3072 lds
MM v[64:67],a[192:195],v[160:163],v[128:131]
s_sub_u32 s64, s64, 507904
TR a[128:129],v232 offset:0
s_add_u32 s81, s64, 896
MM v[64:67],a[196:199],v[176:179],v[64:67]
TR a[130:131],v232 offset:4096
v_max3_f32 v249, v144, v145, v148
MM v[64:67],a[200:203],v[192:195],v[64:67]
TR a[132:133],v232 offset:8192
v_max3_f32 v249, v249, v149, v152
MM v[64:67],a[204:207],v[208:211],v[64:67]
TR a[134:135],v232 offset:12288
v_max3_f32 v249, v249, v153, v156
MM v[68:71],a[220:223],v[208:211],v[128:131]
TR a[136:137],v233 offset:0
v_max_f32 v249, v249, v157
MM v[68:71],a[216:219],v[192:195],v[68:71]
TR a[138:139],v233 offset:4096
v_cmp_lt_f32 vcc, s71, v249
MM v[68:71],a[212:215],v[176:179],v[68:71]
TR a[140:141],v233 offset:8192
TR a[142:143],v233 offset:12288
MM v[68:71],a[208:211],v[160:163],v[68:71]
s_cbranch_vccnz LSITE32_%=
LBACK32_%=:
MM v[76:79],a[208:211],v[164:167],v[132:135]
EX v64,v64
MM v[76:79],a[212:215],v[180:183],v[76:79]
EX v65,v65
MM v[76:79],a[216:219],v[196:199],v[76:79]
TR a[144:145],v232 offset:512
CV v64,v64,v65
MM v[76:79],a[220:223],v[212:215],v[76:79]
EX v66,v66
MM v[72:75],a[204:207],v[212:215],v[132:135]
EX v67,v67
MM v[72:75],a[200:203],v[196:199],v[72:75]
TR a[146:147],v232 offset:4608
CV v65,v66,v67
MM v[72:75],a[196:199],v[180:183],v[72:75]
EX v68,v68
MM v[72:75],a[192:195],v[164:167],v[72:75]
TR a[148:149],v232 offset:8704
TR a[150:151],v232 offset:12800
MM v[80:83],a[192:195],v[168:171],v[136:139]
EX v69,v69
MM v[80:83],a[196:199],v[184:187],v[80:83]
TR a[152:153],v233 offset:512
CV v66,v68,v69
MM v[80:83],a[200:203],v[200:203],v[80:83]
EX v70,v70
MM v[80:83],a[204:207],v[216:219],v[80:83]
TR a[154:155],v233 offset:4608
TR a[156:157],v233 offset:8704
MM v[84:87],a[220:223],v[216:219],v[136:139]
EX v71,v71
MM v[84:87],a[216:219],v[200:203],v[84:87]
TR a[158:159],v233 offset:12800
CV v67,v70,v71
MM v[84:87],a[212:215],v[184:187],v[84:87]
EX v72,v72
MM v[84:87],a[208:211],v[168:171],v[84:87]
EX v73,v73
MM v[92:95],a[208:211],v[172:175],v[140:143]
TR a[160:161],v232 offset:1024
CV v72,v72,v73
MM v[92:95],a[212:215],v[188:191],v[92:95]
EX v74,v74
MM v[92:95],a[216:219],v[204:207],v[92:95]
TR a[162:163],v232 offset:5120
TR a[164:165],v232 offset:9216
MM v[92:95],a[220:223],v[220:223],v[92:95]
EX v75,v75
MM v[88:91],a[204:207],v[220:223],v[140:143]
TR a[166:167],v232 offset:13312
CV v73,v74,v75
MM v[88:91],a[200:203],v[204:207],v[88:91]
EX v76,v76
MM v[88:91],a[196:199],v[188:191],v[88:91]
EX v77,v77
MM v[88:91],a[192:195],v[172:175],v[88:91]
TR a[168:169],v233 offset:1024
CV v74,v76,v77
MM v[96:99],a[224:227],v[160:163],v[128:131]
EX v78,v78
MM v[96:99],a[228:231],v[176:179],v[96:99]
TR a[170:171],v233 offset:5120
TR a[172:173],v233 offset:9216
MM v[96:99],a[232:235],v[192:195],v[96:99]
EX v79,v79
MM v[96:99],a[236:239],v[208:211],v[96:99]
TR a[174:175],v233 offset:13312
CV v75,v78,v79
MM v[100:103],a[252:255],v[208:211],v[128:131]
EX v80,v80
MM v[100:103],a[248:251],v[192:195],v[100:103]
EX v81,v81
MM v[100:103],a[244:247],v[176:179],v[100:103]
TR a[176:177],v232 offset:1536
CV v80,v80,v81
MM v[100:103],a[240:243],v[160:163],v[100:103]
EX v82,v82
MM v[108:111],a[240:243],v[164:167],v[132:135]
TR a[178:179],v232 offset:5632
TR a[180:181],v232 offset:9728
MM v[108:111],a[244:247],v[180:183],v[108:111]
EX v83,v83
MM v[108:111],a[248:251],v[196:199],v[108:111]
TR a[182:183],v232 offset:13824
CV v81,v82,v83
MM v[108:111],a[252:255],v[212:215],v[108:111]
EX v84,v84
MM v[104:107],a[236:239],v[212:215],v[132:135]
TR a[184:185],v233 offset:1536
TR a[186:187],v233 offset:5632
MM v[104:107],a[232:235],v[196:199],v[104:107]
EX v85,v85
MM v[104:107],a[228:231],v[180:183],v[104:107]
TR a[188:189],v233 offset:9728
CV v82,v84,v85
MM v[104:107],a[224:227],v[164:167],v[104:107]
EX v86,v86
MM v[112:115],a[224:227],v[168:171],v[136:139]
EX v87,v87
MM v[112:115],a[228:231],v[184:187],v[112:115]
TR a[190:191],v233 offset:13824
CV v83,v86,v87
MM v[112:115],a[232:235],v[200:203],v[112:115]
EX v88,v88
MM v[112:115],a[236:239],v[216:219],v[112:115]
EX v89,v89
MM v[116:119],a[252:255],v[216:219],v[136:139]
EX v90,v90
MM v[116:119],a[248:251],v[200:203],v[116:119]
EX v91,v91
MM v[116:119],a[244:247],v[184:187],v[116:119]
EX v92,v92
MM v[116:119],a[240:243],v[168:171],v[116:119]
EX v93,v93
MM v[124:127],a[240:243],v[172:175],v[140:143]
EX v94,v94
MM v[124:127],a[244:247],v[188:191],v[124:127]
EX v95,v95
MM v[124:127],a[248:251],v[204:207],v[124:127]
EX v96,v96
MM v[124:127],a[252:255],v[220:223],v[124:127]
EX v97,v97
MM v[120:123],a[236:239],v[220:223],v[140:143]
EX v98,v98
MM v[120:123],a[232:235],v[204:207],v[120:123]
EX v99,v99
MM v[120:123],a[228:231],v[188:191],v[120:123]
EX v100,v100
MM v[120:123],a[224:227],v[172:175],v[120:123]
s_waitcnt lgkmcnt(0)
s_barrier
MM a[0:3],a[128:131],v[0:3],a[0:3]
s_add_u32 m0, s65, 0
CV v88,v88,v89
CV v89,v90,v91
MM a[0:3],a[132:135],v[4:7],a[0:3]
RD a[192:195],v224 offset:32768
CV v90,v92,v93
MM a[8:11],a[132:135],v[12:15],a[8:11]
RD a[196:199],v225 offset:32768
CV v91,v94,v95
MM a[8:11],a[128:131],v[8:11],a[8:11]
buffer_load_dwordx4 v228, s[8:11], s64 offen offset:0 lds
MM a[16:19],a[128:131],v[16:19],a[16:19]
RD a[200:203],v226 offset:32768
CV v68,v96,v97
MM a[16:19],a[132:135],v[20:23],a[16:19]
RD a[204:207],v227 offset:32768
CV v69,v98,v99
MM a[24:27],a[132:135],v[28:31],a[24:27]
buffer_load_dwordx4 v229, s[8:11], s64 offen offset:1024 lds
MM a[24:27],a[128:131],v[24:27],a[24:27]
RD a[208:211],v224 offset:36864
RD a[212:215],v225 offset:36864
MM a[28:31],a[136:139],v[24:27],a[28:31]
buffer_load_dwordx4 v230, s[8:11], s64 offen offset:2048 lds
MM a[28:31],a[140:143],v[28:31],a[28:31]
RD a[216:219],v226 offset:36864
RD a[220:223],v227 offset:36864
MM a[20:23],a[140:143],v[20:23],a[20:23]
EX v101,v101
MM a[20:23],a[136:139],v[16:19],a[20:23]
RD a[224:227],v224 offset:40960
CV v70,v100,v101
MM a[12:15],a[136:139],v[8:11],a[12:15]
EX v102,v102
MM a[12:15],a[140:143],v[12:15],a[12:15]
buffer_load_dwordx4 v231, s[8:11], s64 offen offset:3072 lds
MM a[4:7],a[140:143],v[4:7],a[4:7]
s_add_u32 m0, s65, 65536
EX v103,v103
MM a[4:7],a[136:139],v[0:3],a[4:7]
RD a[228:231],v225 offset:40960
CV v71,v102,v103
MM a[32:35],a[144:147],v[0:3],a[32:35]
EX v104,v104
MM a[32:35],a[148:151],v[4:7],a[32:35]
EX v105,v105
MM a[40:43],a[148:151],v[12:15],a[40:43]
RD a[232:235],v226 offset:40960
CV v76,v104,v105
MM a[40:43],a[144:147],v[8:11],a[40:43]
EX v106,v106
MM a[48:51],a[144:147],v[16:19],a[48:51]
buffer_load_dwordx4 v235, s[12:15], s81 offen offset:0 lds
MM a[48:51],a[148:151],v[20:23],a[48:51]
EX v107,v107
MM a[56:59],a[148:151],v[28:31],a[56:59]
RD a[236:239],v227 offset:40960
CV v77,v106,v107
MM a[56:59],a[144:147],v[24:27],a[56:59]
EX v108,v108
MM a[60:63],a[152:155],v[24:27],a[60:63]
EX v109,v109
MM a[60:63],a[156:159],v[28:31],a[60:63]
RD a[240:243],v224 offset:45056
CV v78,v108,v109
MM a[52:55],a[156:159],v[20:23],a[52:55]
EX v110,v110
MM a[52:55],a[152:155],v[16:19],a[52:55]
buffer_load_dwordx4 v235, s[12:15], s64 offen offset:1024 lds
MM a[44:47],a[152:155],v[8:11],a[44:47]
EX v111,v111
MM a[44:47],a[156:159],v[12:15],a[44:47]
RD a[244:247],v225 offset:45056
CV v79,v110,v111
MM a[36:39],a[156:159],v[4:7],a[36:39]
EX v112,v112
MM a[36:39],a[152:155],v[0:3],a[36:39]
EX v113,v113
MM a[64:67],a[160:163],v[0:3],a[64:67]
RD a[248:251],v226 offset:45056
CV v84,v112,v113
MM a[64:67],a[164:167],v[4:7],a[64:67]
EX v114,v114
MM a[72:75],a[164:167],v[12:15],a[72:75]
buffer_load_dwordx4 v235, s[12:15], s81 offen offset:2048 lds
MM a[72:75],a[160:163],v[8:11],a[72:75]
EX v115,v115
MM a[80:83],a[160:163],v[16:19],a[80:83]
RD a[252:255],v227 offset:45056
CV v85,v114,v115
MM a[80:83],a[164:167],v[20:23],a[80:83]
EX v116,v116
MM a[88:91],a[164:167],v[28:31],a[88:91]
EX v117,v117
MM a[88:91],a[160:163],v[24:27],a[88:91]
buffer_load_dwordx4 v235, s[12:15], s64 offen offset:3072 lds
MM a[92:95],a[168:171],v[24:27],a[92:95]
EX v118,v118
MM a[92:95],a[172:175],v[28:31],a[92:95]
EX v119,v119
MM a[84:87],a[172:175],v[20:23],a[84:87]
EX v120,v120
MM a[84:87],a[168:171],v[16:19],a[84:87]
EX v121,v121
MM a[76:79],a[168:171],v[8:11],a[76:79]
EX v122,v122
MM a[76:79],a[172:175],v[12:15],a[76:79]
EX v123,v123
MM a[68:71],a[172:175],v[4:7],a[68:71]
EX v124,v124
MM a[68:71],a[168:171],v[0:3],a[68:71]
EX v125,v125
MM a[96:99],a[176:179],v[0:3],a[96:99]
EX v126,v126
MM a[96:99],a[180:183],v[4:7],a[96:99]
EX v127,v127
MM a[104:107],a[180:183],v[12:15],a[104:107]
CV v86,v116,v117
CV v87,v118,v119
MM a[104:107],a[176:179],v[8:11],a[104:107]
CV v92,v120,v121
CV v93,v122,v123
MM a[112:115],a[176:179],v[16:19],a[112:115]
CV v94,v124,v125
CV v95,v126,v127
MM a[112:115],a[180:183],v[20:23],a[112:115]
MM a[120:123],a[180:183],v[28:31],a[120:123]
s_lshl_b32 s91, s17, 2
s_add_u32 s91, s91, 94464
s_and_b64 s[44:45], s[44:45], 1
MM a[120:123],a[176:179],v[24:27],a[120:123]
s_waitcnt vmcnt(10)
v_mov_b32 v250, s69
s_mov_b64 exec, s[44:45]
buffer_store_dword v250, off, s[20:23], s91 sc1
s_mov_b64 exec, -1
)ASM"
R"ASM(
MM a[124:127],a[184:187],v[24:27],a[124:127]
v_add_f32 v244, v144, v145
v_add_f32 v245, v148, v149
MM a[124:127],a[188:191],v[28:31],a[124:127]
v_add_f32 v246, v152, v153
v_add_f32 v247, v156, v157
MM a[116:119],a[188:191],v[20:23],a[116:119]
MM a[116:119],a[184:187],v[16:19],a[116:119]
MM a[108:111],a[184:187],v[8:11],a[108:111]
MM a[108:111],a[188:191],v[12:15],a[108:111]
MM a[100:103],a[188:191],v[4:7],a[100:103]
MM a[100:103],a[184:187],v[0:3],a[100:103]
v_smfmac_f32_16x16x64_bf16 v[144:147], v[236:239], v[64:71], v234
v_smfmac_f32_16x16x64_bf16 v[148:151], v[236:239], v[72:79], v234
s_nop 0
LPG23_%=:
s_cbranch_execz LPG27_%=
v_smfmac_f32_16x16x64_bf16 v[152:155], v[236:239], v[80:87], v234
v_smfmac_f32_16x16x64_bf16 v[156:159], v[236:239], v[88:95], v234
s_waitcnt lgkmcnt(0)
LLAJ_%=:
MM v[0:3],a[192:195],v[160:163],v[128:131]
s_add_u32 s64, s64, 16384
TR a[128:129],v232 offset:16384
s_add_u32 s81, s64, 896
MM v[0:3],a[196:199],v[176:179],v[0:3]
TR a[130:131],v232 offset:20480
v_max3_f32 v249, v144, v145, v148
MM v[0:3],a[200:203],v[192:195],v[0:3]
TR a[132:133],v232 offset:24576
v_max3_f32 v249, v249, v149, v152
MM v[0:3],a[204:207],v[208:211],v[0:3]
TR a[134:135],v232 offset:28672
v_max3_f32 v249, v249, v153, v156
MM v[4:7],a[220:223],v[208:211],v[128:131]
TR a[136:137],v233 offset:16384
v_max_f32 v249, v249, v157
MM v[4:7],a[216:219],v[192:195],v[4:7]
TR a[138:139],v233 offset:20480
v_cmp_lt_f32 vcc, s71, v249
MM v[4:7],a[212:215],v[176:179],v[4:7]
TR a[140:141],v233 offset:24576
TR a[142:143],v233 offset:28672
MM v[4:7],a[208:211],v[160:163],v[4:7]
s_cbranch_vccnz LSITE33_%=
LBACK33_%=:
MM v[12:15],a[208:211],v[164:167],v[132:135]
v_bfe_u32 v242, v248, 6, 4
v_mul_u32_u24 v242, 1280, v242
MM v[12:15],a[212:215],v[180:183],v[12:15]
v_and_b32 v252, 48, v248
v_bfe_u32 v243, v248, 8, 1
MM v[12:15],a[216:219],v[196:199],v[12:15]
v_lshlrev_b32 v243, 5, v243
v_xor_b32 v252, v252, v243
MM v[12:15],a[220:223],v[212:215],v[12:15]
v_add_u32 v252, v252, v242
v_lshrrev_b32 v242, 3, v248
MM v[8:11],a[204:207],v[212:215],v[132:135]
v_xor_b32 v243, v242, v248
v_and_b32 v243, 96, v243
MM v[8:11],a[200:203],v[196:199],v[8:11]
TR a[144:145],v232 offset:16896
v_and_or_b32 v243, v248, 16, v243
MM v[8:11],a[196:199],v[180:183],v[8:11]
s_and_b32 s66, s49, 8
EX v0,v0
MM v[8:11],a[192:195],v[164:167],v[8:11]
s_lshl_b32 s66, s66, 3
v_lshrrev_b32 v242, 7, v248
v_xor_b32 v243, s66, v243
EX v1,v1
MM v[16:19],a[192:195],v[168:171],v[136:139]
v_mul_u32_u24 v242, 1280, v242
v_add_u32 v240, v242, v243
EX v2,v2
MM v[16:19],a[196:199],v[184:187],v[16:19]
s_mul_i32 s88, %[swave], 40960
EX v3,v3
MM v[16:19],a[200:203],v[200:203],v[16:19]
s_and_b32 s66, s84, 1
EX v4,v4
MM v[16:19],a[204:207],v[216:219],v[16:19]
s_lshl_b32 s86, s66, 6
TR a[146:147],v232 offset:20992
CV v0,v0,v1
MM v[20:23],a[220:223],v[216:219],v[136:139]
s_add_u32 s86, s86, s88
EX v5,v5
MM v[20:23],a[216:219],v[200:203],v[20:23]
s_xor_b32 s66, s66, 1
TR a[148:149],v232 offset:25088
CV v1,v2,v3
MM v[20:23],a[212:215],v[184:187],v[20:23]
s_lshl_b32 s66, s66, 8
EX v6,v6
MM v[20:23],a[208:211],v[168:171],v[20:23]
s_add_u32 s88, s88, s66
EX v7,v7
MM v[28:31],a[208:211],v[172:175],v[140:143]
s_and_b32 s66, s49, 16
TR a[150:151],v232 offset:29184
TR a[152:153],v233 offset:16896
MM v[28:31],a[212:215],v[188:191],v[28:31]
s_lshl_b32 s66, s66, 3
EX v8,v8
MM v[28:31],a[216:219],v[204:207],v[28:31]
s_sub_u32 s87, s86, s66
TR a[154:155],v233 offset:20992
CV v2,v4,v5
MM v[28:31],a[220:223],v[220:223],v[28:31]
s_add_u32 s87, s87, 128
EX v9,v9
MM v[24:27],a[204:207],v[220:223],v[140:143]
s_add_u32 s86, s86, s66
TR a[156:157],v233 offset:25088
CV v3,v6,v7
MM v[24:27],a[200:203],v[204:207],v[24:27]
s_and_b32 s94, s49, 7
EX v10,v10
MM v[24:27],a[196:199],v[188:191],v[24:27]
s_lshl_b32 s89, %[swave], 11
TR a[158:159],v233 offset:29184
CV v8,v8,v9
MM v[24:27],a[192:195],v[172:175],v[24:27]
s_add_u32 s89, s89, %[slds]
EX v11,v11
MM v[32:35],a[224:227],v[160:163],v[128:131]
s_xor_b32 s66, s94, 4
TR a[160:161],v232 offset:17408
TR a[162:163],v232 offset:21504
MM v[32:35],a[228:231],v[176:179],v[32:35]
s_mul_i32 s66, s66, 163840
EX v12,v12
MM v[32:35],a[232:235],v[192:195],v[32:35]
s_add_u32 s66, s66, s88
s_add_u32 m0, s89, 131072
s_nop 0
buffer_load_dwordx4 v252, s[24:27], s66 offen offset:0 lds
MM v[32:35],a[236:239],v[208:211],v[32:35]
s_add_u32 s66, s66, 19456
EX v13,v13
MM v[36:39],a[252:255],v[208:211],v[128:131]
TR a[164:165],v232 offset:25600
CV v9,v10,v11
MM v[36:39],a[248:251],v[192:195],v[36:39]
EX v14,v14
MM v[36:39],a[244:247],v[176:179],v[36:39]
EX v15,v15
MM v[36:39],a[240:243],v[160:163],v[36:39]
TR a[166:167],v232 offset:29696
TR a[168:169],v233 offset:17408
MM v[44:47],a[240:243],v[164:167],v[132:135]
EX v16,v16
MM v[44:47],a[244:247],v[180:183],v[44:47]
TR a[170:171],v233 offset:21504
CV v10,v12,v13
MM v[44:47],a[248:251],v[196:199],v[44:47]
EX v17,v17
MM v[44:47],a[252:255],v[212:215],v[44:47]
s_add_u32 m0, s89, 131072
s_nop 0
buffer_load_dwordx4 v252, s[24:27], s66 offen offset:1024 lds
MM v[40:43],a[236:239],v[212:215],v[132:135]
EX v18,v18
MM v[40:43],a[232:235],v[196:199],v[40:43]
TR a[172:173],v233 offset:25600
CV v11,v14,v15
MM v[40:43],a[228:231],v[180:183],v[40:43]
EX v19,v19
MM v[40:43],a[224:227],v[164:167],v[40:43]
TR a[174:175],v233 offset:29696
TR a[176:177],v232 offset:17920
MM v[48:51],a[224:227],v[168:171],v[136:139]
EX v20,v20
MM v[48:51],a[228:231],v[184:187],v[48:51]
TR a[178:179],v232 offset:22016
CV v16,v16,v17
MM v[48:51],a[232:235],v[200:203],v[48:51]
EX v21,v21
MM v[48:51],a[236:239],v[216:219],v[48:51]
s_lshl_b32 s60, s50, 2
TR a[180:181],v232 offset:26112
CV v17,v18,v19
MM v[52:55],a[252:255],v[216:219],v[136:139]
s_add_u32 s60, s60, %[swave]
EX v22,v22
MM v[52:55],a[248:251],v[200:203],v[52:55]
s_lshl_b32 s60, s60, 1
TR a[182:183],v232 offset:30208
TR a[184:185],v233 offset:17920
MM v[52:55],a[244:247],v[184:187],v[52:55]
s_bfe_u32 s61, s49, 65538
EX v23,v23
MM v[52:55],a[240:243],v[168:171],v[52:55]
s_xor_b32 s61, s61, 1
EX v24,v24
MM v[60:63],a[240:243],v[172:175],v[140:143]
s_add_u32 s60, s60, s61
TR a[186:187],v233 offset:22016
CV v18,v20,v21
MM v[60:63],a[244:247],v[188:191],v[60:63]
s_mul_i32 s18, s60, 40960
EX v25,v25
MM v[60:63],a[248:251],v[204:207],v[60:63]
s_mul_i32 s60, s84, 320
TR a[188:189],v233 offset:26112
TR a[190:191],v233 offset:30208
MM v[60:63],a[252:255],v[220:223],v[60:63]
s_add_u32 s18, s18, s60
EX v26,v26
MM v[56:59],a[236:239],v[220:223],v[140:143]
s_lshr_b32 s62, s49, 3
CV v19,v22,v23
CV v24,v24,v25
MM v[56:59],a[232:235],v[204:207],v[56:59]
s_mul_i32 s63, s62, 10240
EX v27,v27
MM v[56:59],a[228:231],v[188:191],v[56:59]
s_add_u32 s63, s63, s18
EX v28,v28
MM v[56:59],a[224:227],v[172:175],v[56:59]
s_waitcnt vmcnt(14) lgkmcnt(0)
s_barrier
MM a[0:3],a[128:131],v[64:67],a[0:3]
s_and_b32 s60, s84, 1
EX v29,v29
MM a[0:3],a[132:135],v[68:71],a[0:3]
s_add_u32 s61, s60, s62
CV v25,v26,v27
CV v26,v28,v29
MM a[8:11],a[132:135],v[76:79],a[8:11]
s_lshl_b32 s61, s61, 6
EX v30,v30
MM a[8:11],a[128:131],v[72:75],a[8:11]
s_add_u32 s18, s18, s61
EX v31,v31
MM a[16:19],a[128:131],v[80:83],a[16:19]
s_xor_b32 s60, s60, 1
RD a[192:195],v224 offset:49152
CV v27,v30,v31
MM a[16:19],a[132:135],v[84:87],a[16:19]
s_lshl_b32 s60, s60, 8
EX v32,v32
MM a[24:27],a[132:135],v[92:95],a[24:27]
s_add_u32 s63, s63, s60
s_add_u32 m0, s65, 16384
RD a[196:199],v225 offset:49152
MM a[24:27],a[128:131],v[88:91],a[24:27]
s_mul_i32 s60, s62, 33792
EX v33,v33
MM a[28:31],a[136:139],v[88:91],a[28:31]
s_sub_u32 s60, s95, s60
buffer_load_dwordx4 v228, s[8:11], s64 offen offset:0 lds
MM a[28:31],a[140:143],v[92:95],a[28:31]
s_xor_b32 s61, s62, 1
EX v34,v34
MM a[20:23],a[140:143],v[84:87],a[20:23]
s_mul_i32 s61, s61, 33792
EX v35,v35
MM a[20:23],a[136:139],v[80:83],a[20:23]
s_add_u32 s61, s61, s60
RD a[200:203],v226 offset:49152
CV v4,v32,v33
MM a[12:15],a[136:139],v[72:75],a[12:15]
s_add_u32 s36, s61, 2048
EX v36,v36
MM a[12:15],a[140:143],v[76:79],a[12:15]
s_xor_b32 s61, s62, 2
CV v5,v34,v35
MM a[4:7],a[140:143],v[68:71],a[4:7]
s_mul_i32 s61, s61, 33792
EX v37,v37
MM a[4:7],a[136:139],v[64:67],a[4:7]
s_add_u32 s61, s61, s60
buffer_load_dwordx4 v229, s[8:11], s64 offen offset:1024 lds
MM a[32:35],a[144:147],v[64:67],a[32:35]
s_add_u32 s37, s61, 4096
EX v38,v38
MM a[32:35],a[148:151],v[68:71],a[32:35]
s_xor_b32 s61, s62, 3
RD a[204:207],v227 offset:49152
CV v6,v36,v37
MM a[40:43],a[148:151],v[76:79],a[40:43]
s_mul_i32 s61, s61, 33792
EX v39,v39
MM a[40:43],a[144:147],v[72:75],a[40:43]
s_add_u32 s61, s61, s60
RD a[208:211],v224 offset:53248
CV v7,v38,v39
MM a[48:51],a[144:147],v[80:83],a[48:51]
s_add_u32 s38, s61, 6144
EX v40,v40
MM a[48:51],a[148:151],v[84:87],a[48:51]
s_lshl_b32 s61, s62, 9
EX v41,v41
MM a[56:59],a[148:151],v[92:95],a[56:59]
s_add_u32 s39, s60, s61
RD a[212:215],v225 offset:53248
MM a[56:59],a[144:147],v[88:91],a[56:59]
s_mov_b32 s17, 15
buffer_load_dwordx4 v230, s[8:11], s64 offen offset:2048 lds
MM a[60:63],a[152:155],v[88:91],a[60:63]
s_mov_b32 s58, 0
EX v42,v42
MM a[60:63],a[156:159],v[92:95],a[60:63]
s_lshl_b32 s60, s50, 1
EX v43,v43
MM a[52:55],a[156:159],v[84:87],a[52:55]
s_bfe_u32 s61, s49, 65538
RD a[216:219],v226 offset:53248
CV v12,v40,v41
MM a[52:55],a[152:155],v[80:83],a[52:55]
s_add_u32 s60, s60, s61
EX v44,v44
MM a[44:47],a[152:155],v[72:75],a[44:47]
s_lshl_b32 s60, s60, 2
CV v13,v42,v43
MM a[44:47],a[156:159],v[76:79],a[44:47]
s_add_u32 s60, s60, s84
EX v45,v45
MM a[36:39],a[156:159],v[68:71],a[36:39]
s_lshl_b32 s60, s60, 2
buffer_load_dwordx4 v231, s[8:11], s64 offen offset:3072 lds
MM a[36:39],a[152:155],v[64:67],a[36:39]
s_add_u32 s68, s60, %[swave]
EX v46,v46
MM a[64:67],a[160:163],v[64:67],a[64:67]
s_lshl_b32 s68, s68, 6
RD a[220:223],v227 offset:53248
CV v14,v44,v45
MM a[64:67],a[164:167],v[68:71],a[64:67]
s_add_u32 s68, s68, 16384
EX v47,v47
MM a[72:75],a[164:167],v[76:79],a[72:75]
s_lshr_b32 s61, s49, 3
EX v48,v48
MM a[72:75],a[160:163],v[72:75],a[72:75]
s_lshl_b32 s19, s61, 4
RD a[224:227],v224 offset:57344
CV v15,v46,v47
MM a[80:83],a[160:163],v[80:83],a[80:83]
s_add_u32 s19, s19, s68
EX v49,v49
MM a[80:83],a[164:167],v[84:87],a[80:83]
s_lshr_b32 s96, s49, 2
s_add_u32 m0, s65, 81920
CV v20,v48,v49
MM a[88:91],a[164:167],v[92:95],a[88:91]
s_lshl_b32 s97, s50, 3
EX v50,v50
MM a[88:91],a[160:163],v[88:91],a[88:91]
s_add_u32 s96, s96, s97
buffer_load_dwordx4 v235, s[12:15], s81 offen offset:0 lds
MM a[92:95],a[168:171],v[88:91],a[92:95]
s_lshl_b32 s96, s96, 2
EX v51,v51
MM a[92:95],a[172:175],v[92:95],a[92:95]
s_add_u32 s96, s96, %[swave]
RD a[228:231],v225 offset:57344
CV v21,v50,v51
MM a[84:87],a[172:175],v[84:87],a[84:87]
s_lshl_b32 s96, s96, 6
EX v52,v52
MM a[84:87],a[168:171],v[80:83],a[84:87]
s_add_u32 s96, s96, 0
RD a[232:235],v226 offset:57344
MM a[76:79],a[168:171],v[72:75],a[76:79]
s_xor_b32 s44, s96, 256
EX v53,v53
MM a[76:79],a[172:175],v[76:79],a[76:79]
s_xor_b32 s66, s94, 5
RD a[236:239],v227 offset:57344
CV v22,v52,v53
MM a[68:71],a[172:175],v[68:71],a[68:71]
s_mul_i32 s66, s66, 163840
EX v54,v54
MM a[68:71],a[168:171],v[64:67],a[68:71]
s_add_u32 s66, s66, s88
s_add_u32 m0, s89, 139264
s_nop 0
buffer_load_dwordx4 v252, s[24:27], s66 offen offset:0 lds
MM a[96:99],a[176:179],v[64:67],a[96:99]
s_add_u32 s66, s66, 19456
EX v55,v55
MM a[96:99],a[180:183],v[68:71],a[96:99]
s_add_u32 m0, s65, 81920
s_nop 0
buffer_load_dwordx4 v235, s[12:15], s64 offen offset:1024 lds
MM a[104:107],a[180:183],v[76:79],a[104:107]
EX v56,v56
MM a[104:107],a[176:179],v[72:75],a[104:107]
RD a[240:243],v224 offset:61440
CV v23,v54,v55
MM a[112:115],a[176:179],v[80:83],a[112:115]
EX v57,v57
MM a[112:115],a[180:183],v[84:87],a[112:115]
EX v58,v58
MM a[120:123],a[180:183],v[92:95],a[120:123]
RD a[244:247],v225 offset:61440
CV v28,v56,v57
MM a[120:123],a[176:179],v[88:91],a[120:123]
EX v59,v59
MM a[124:127],a[184:187],v[88:91],a[124:127]
v_add_f32 v244, v144, v145
v_add_f32 v245, v148, v149
MM a[124:127],a[188:191],v[92:95],a[124:127]
v_add_f32 v246, v152, v153
v_add_f32 v247, v156, v157
MM a[116:119],a[188:191],v[84:87],a[116:119]
)ASM"
R"ASM(
s_add_u32 m0, s89, 139264
s_nop 0
buffer_load_dwordx4 v252, s[24:27], s66 offen offset:1024 lds
MM a[116:119],a[184:187],v[80:83],a[116:119]
EX v60,v60
MM a[108:111],a[184:187],v[72:75],a[108:111]
EX v61,v61
MM a[108:111],a[188:191],v[76:79],a[108:111]
s_add_u32 m0, s65, 81920
s_nop 0
buffer_load_dwordx4 v235, s[12:15], s81 offen offset:2048 lds
MM a[100:103],a[188:191],v[68:71],a[100:103]
EX v62,v62
MM a[100:103],a[184:187],v[64:67],a[100:103]
RD a[248:251],v226 offset:61440
CV v29,v58,v59
v_smfmac_f32_16x16x64_bf16 v[144:147], v[236:239], v[0:7], v234
RD a[252:255],v227 offset:61440
EX v63,v63
v_smfmac_f32_16x16x64_bf16 v[148:151], v[236:239], v[8:15], v234
CV v30,v60,v61
v_smfmac_f32_16x16x64_bf16 v[152:155], v[236:239], v[16:23], v234
CV v31,v62,v63
s_nop 1
v_smfmac_f32_16x16x64_bf16 v[156:159], v[236:239], v[24:31], v234
s_waitcnt lgkmcnt(0)
buffer_load_dwordx4 v235, s[12:15], s64 offen offset:3072 lds
MM v[64:67],a[192:195],v[160:163],v[128:131]
s_add_u32 s64, s64, 16384
TR a[128:129],v232 offset:32768
s_add_u32 s81, s64, 896
MM v[64:67],a[196:199],v[176:179],v[64:67]
TR a[130:131],v232 offset:36864
v_max3_f32 v249, v144, v145, v148
MM v[64:67],a[200:203],v[192:195],v[64:67]
TR a[132:133],v232 offset:40960
v_max3_f32 v249, v249, v149, v152
MM v[64:67],a[204:207],v[208:211],v[64:67]
TR a[134:135],v232 offset:45056
v_max3_f32 v249, v249, v153, v156
MM v[68:71],a[220:223],v[208:211],v[128:131]
TR a[136:137],v233 offset:32768
v_max_f32 v249, v249, v157
MM v[68:71],a[216:219],v[192:195],v[68:71]
TR a[138:139],v233 offset:36864
v_cmp_lt_f32 vcc, s71, v249
MM v[68:71],a[212:215],v[176:179],v[68:71]
TR a[140:141],v233 offset:40960
TR a[142:143],v233 offset:45056
MM v[68:71],a[208:211],v[160:163],v[68:71]
s_cbranch_vccnz LSITE34_%=
LBACK34_%=:
MM v[76:79],a[208:211],v[164:167],v[132:135]
buffer_load_dword v242, off, s[20:23], s96 sc1
MM v[76:79],a[212:215],v[180:183],v[76:79]
buffer_load_dword v243, off, s[20:23], s19 sc1
MM v[76:79],a[216:219],v[196:199],v[76:79]
s_xor_b32 s66, s94, 6
v_and_b32 v32, 496, v248
v_mul_u32_u24 v32, 80, v32
MM v[76:79],a[220:223],v[212:215],v[76:79]
s_mul_i32 s66, s66, 163840
EX v64,v64
MM v[72:75],a[204:207],v[212:215],v[132:135]
s_add_u32 s66, s66, s88
s_add_u32 m0, s89, 147456
s_nop 0
buffer_load_dwordx4 v252, s[24:27], s66 offen offset:0 lds
MM v[72:75],a[200:203],v[196:199],v[72:75]
s_add_u32 s66, s66, 19456
EX v65,v65
MM v[72:75],a[196:199],v[180:183],v[72:75]
s_sub_u32 s60, s63, s18
v_add_u32 v32, s18, v32
CV v64,v64,v65
MM v[72:75],a[192:195],v[164:167],v[72:75]
s_mov_b32 s61, 131072
s_mov_b32 exec_lo, 0
v_add_u32 v32, s60, v32
s_mov_b64 exec, -1
MM v[80:83],a[192:195],v[168:171],v[136:139]
s_mov_b32 exec_hi, 0xff
buffer_load_dword v36, v32, s[32:35], 0 offen offset:0
s_mov_b64 exec, -1
EX v66,v66
MM v[80:83],a[196:199],v[184:187],v[80:83]
EX v67,v67
MM v[80:83],a[200:203],v[200:203],v[80:83]
EX v68,v68
MM v[80:83],a[204:207],v[216:219],v[80:83]
s_mov_b32 exec_hi, 0xff
buffer_load_dword v37, v32, s[20:23], s61 offen offset:0 sc1
s_mov_b64 exec, -1
MM v[84:87],a[220:223],v[216:219],v[136:139]
TR a[144:145],v232 offset:33280
TR a[146:147],v232 offset:37376
MM v[84:87],a[216:219],v[200:203],v[84:87]
s_add_u32 m0, s89, 147456
s_nop 0
buffer_load_dwordx4 v252, s[24:27], s66 offen offset:1024 lds
EX v69,v69
MM v[84:87],a[212:215],v[184:187],v[84:87]
EX v70,v70
MM v[84:87],a[208:211],v[168:171],v[84:87]
TR a[148:149],v232 offset:41472
CV v65,v66,v67
MM v[92:95],a[208:211],v[172:175],v[140:143]
EX v71,v71
MM v[92:95],a[212:215],v[188:191],v[92:95]
TR a[150:151],v232 offset:45568
TR a[152:153],v233 offset:33280
MM v[92:95],a[216:219],v[204:207],v[92:95]
EX v72,v72
MM v[92:95],a[220:223],v[220:223],v[92:95]
EX v73,v73
MM v[88:91],a[204:207],v[220:223],v[140:143]
TR a[154:155],v233 offset:37376
CV v66,v68,v69
MM v[88:91],a[200:203],v[204:207],v[88:91]
s_xor_b32 s66, s94, 7
EX v74,v74
MM v[88:91],a[196:199],v[188:191],v[88:91]
s_mul_i32 s66, s66, 163840
TR a[156:157],v233 offset:41472
CV v67,v70,v71
MM v[88:91],a[192:195],v[172:175],v[88:91]
s_add_u32 s66, s66, s88
s_add_u32 m0, s89, 155648
s_nop 0
buffer_load_dwordx4 v252, s[24:27], s66 offen offset:0 lds
MM v[96:99],a[224:227],v[160:163],v[128:131]
s_add_u32 s66, s66, 19456
EX v75,v75
MM v[96:99],a[228:231],v[176:179],v[96:99]
TR a[158:159],v233 offset:45568
TR a[160:161],v232 offset:33792
MM v[96:99],a[232:235],v[192:195],v[96:99]
EX v76,v76
MM v[96:99],a[236:239],v[208:211],v[96:99]
EX v77,v77
MM v[100:103],a[252:255],v[208:211],v[128:131]
TR a[162:163],v232 offset:37888
CV v72,v72,v73
MM v[100:103],a[248:251],v[192:195],v[100:103]
EX v78,v78
MM v[100:103],a[244:247],v[176:179],v[100:103]
TR a[164:165],v232 offset:41984
CV v73,v74,v75
MM v[100:103],a[240:243],v[160:163],v[100:103]
EX v79,v79
MM v[108:111],a[240:243],v[164:167],v[132:135]
s_add_u32 m0, s89, 155648
s_nop 0
buffer_load_dwordx4 v252, s[24:27], s66 offen offset:1024 lds
MM v[108:111],a[244:247],v[180:183],v[108:111]
TR a[166:167],v232 offset:46080
TR a[168:169],v233 offset:33792
MM v[108:111],a[248:251],v[196:199],v[108:111]
EX v80,v80
MM v[108:111],a[252:255],v[212:215],v[108:111]
EX v81,v81
MM v[104:107],a[236:239],v[212:215],v[132:135]
TR a[170:171],v233 offset:37888
CV v74,v76,v77
MM v[104:107],a[232:235],v[196:199],v[104:107]
EX v82,v82
MM v[104:107],a[228:231],v[180:183],v[104:107]
TR a[172:173],v233 offset:41984
TR a[174:175],v233 offset:46080
MM v[104:107],a[224:227],v[164:167],v[104:107]
EX v83,v83
MM v[112:115],a[224:227],v[168:171],v[136:139]
TR a[176:177],v232 offset:34304
CV v75,v78,v79
MM v[112:115],a[228:231],v[184:187],v[112:115]
EX v84,v84
MM v[112:115],a[232:235],v[200:203],v[112:115]
EX v85,v85
MM v[112:115],a[236:239],v[216:219],v[112:115]
TR a[178:179],v232 offset:38400
CV v80,v80,v81
MM v[116:119],a[252:255],v[216:219],v[136:139]
EX v86,v86
MM v[116:119],a[248:251],v[200:203],v[116:119]
TR a[180:181],v232 offset:42496
TR a[182:183],v232 offset:46592
MM v[116:119],a[244:247],v[184:187],v[116:119]
EX v87,v87
MM v[116:119],a[240:243],v[168:171],v[116:119]
TR a[184:185],v233 offset:34304
CV v81,v82,v83
MM v[124:127],a[240:243],v[172:175],v[140:143]
EX v88,v88
MM v[124:127],a[244:247],v[188:191],v[124:127]
TR a[186:187],v233 offset:38400
CV v82,v84,v85
MM v[124:127],a[248:251],v[204:207],v[124:127]
TR a[188:189],v233 offset:42496
TR a[190:191],v233 offset:46592
EX v89,v89
MM v[124:127],a[252:255],v[220:223],v[124:127]
EX v90,v90
MM v[120:123],a[236:239],v[220:223],v[140:143]
CV v83,v86,v87
CV v88,v88,v89
MM v[120:123],a[232:235],v[204:207],v[120:123]
EX v91,v91
MM v[120:123],a[228:231],v[188:191],v[120:123]
EX v92,v92
MM v[120:123],a[224:227],v[172:175],v[120:123]
s_waitcnt vmcnt(24) lgkmcnt(0)
s_barrier
MM a[0:3],a[128:131],v[0:3],a[0:3]
s_add_u32 m0, s65, 32768
EX v93,v93
MM a[0:3],a[132:135],v[4:7],a[0:3]
CV v89,v90,v91
CV v90,v92,v93
MM a[8:11],a[132:135],v[12:15],a[8:11]
EX v94,v94
MM a[8:11],a[128:131],v[8:11],a[8:11]
EX v95,v95
MM a[16:19],a[128:131],v[16:19],a[16:19]
RD a[192:195],v224 offset:0
LPG24_%=:
s_cbranch_execz LPG28_%=
CV v91,v94,v95
MM a[16:19],a[132:135],v[20:23],a[16:19]
EX v96,v96
MM a[24:27],a[132:135],v[28:31],a[24:27]
buffer_load_dwordx4 v228, s[8:11], s64 offen offset:0 lds
MM a[24:27],a[128:131],v[24:27],a[24:27]
EX v97,v97
MM a[28:31],a[136:139],v[24:27],a[28:31]
RD a[196:199],v225 offset:0
CV v68,v96,v97
MM a[28:31],a[140:143],v[28:31],a[28:31]
EX v98,v98
MM a[20:23],a[140:143],v[20:23],a[20:23]
EX v99,v99
MM a[20:23],a[136:139],v[16:19],a[20:23]
TR a[128:129],v232 offset:49152
RD a[200:203],v226 offset:0
MM a[12:15],a[136:139],v[8:11],a[12:15]
EX v100,v100
MM a[12:15],a[140:143],v[12:15],a[12:15]
TR a[130:131],v232 offset:53248
CV v69,v98,v99
MM a[4:7],a[140:143],v[4:7],a[4:7]
EX v101,v101
MM a[4:7],a[136:139],v[0:3],a[4:7]
TR a[132:133],v232 offset:57344
RD a[204:207],v227 offset:0
MM a[32:35],a[144:147],v[0:3],a[32:35]
EX v102,v102
MM a[32:35],a[148:151],v[4:7],a[32:35]
buffer_load_dwordx4 v229, s[8:11], s64 offen offset:1024 lds
MM a[40:43],a[148:151],v[12:15],a[40:43]
TR a[134:135],v232 offset:61440
CV v70,v100,v101
MM a[40:43],a[144:147],v[8:11],a[40:43]
EX v103,v103
MM a[48:51],a[144:147],v[16:19],a[48:51]
TR a[136:137],v233 offset:49152
TR a[138:139],v233 offset:53248
MM a[48:51],a[148:151],v[20:23],a[48:51]
EX v104,v104
MM a[56:59],a[148:151],v[28:31],a[56:59]
EX v105,v105
MM a[56:59],a[144:147],v[24:27],a[56:59]
TR a[140:141],v233 offset:57344
CV v71,v102,v103
MM a[60:63],a[152:155],v[24:27],a[60:63]
EX v106,v106
MM a[60:63],a[156:159],v[28:31],a[60:63]
RD a[208:211],v224 offset:4096
TR a[142:143],v233 offset:61440
MM a[52:55],a[156:159],v[20:23],a[52:55]
EX v107,v107
MM a[52:55],a[152:155],v[16:19],a[52:55]
buffer_load_dwordx4 v230, s[8:11], s64 offen offset:2048 lds
MM a[44:47],a[152:155],v[8:11],a[44:47]
TR a[144:145],v232 offset:49664
RD a[212:215],v225 offset:4096
MM a[44:47],a[156:159],v[12:15],a[44:47]
EX v108,v108
MM a[36:39],a[156:159],v[4:7],a[36:39]
TR a[146:147],v232 offset:53760
CV v76,v104,v105
MM a[36:39],a[152:155],v[0:3],a[36:39]
EX v109,v109
MM a[64:67],a[160:163],v[0:3],a[64:67]
TR a[148:149],v232 offset:57856
CV v77,v106,v107
MM a[64:67],a[164:167],v[4:7],a[64:67]
EX v110,v110
MM a[72:75],a[164:167],v[12:15],a[72:75]
EX v111,v111
MM a[72:75],a[160:163],v[8:11],a[72:75]
TR a[150:151],v232 offset:61952
RD a[216:219],v226 offset:4096
MM a[80:83],a[160:163],v[16:19],a[80:83]
TR a[152:153],v233 offset:49664
CV v78,v108,v109
MM a[80:83],a[164:167],v[20:23],a[80:83]
EX v112,v112
MM a[88:91],a[164:167],v[28:31],a[88:91]
EX v113,v113
MM a[88:91],a[160:163],v[24:27],a[88:91]
TR a[154:155],v233 offset:53760
RD a[220:223],v227 offset:4096
MM a[92:95],a[168:171],v[24:27],a[92:95]
buffer_load_dwordx4 v231, s[8:11], s64 offen offset:3072 lds
MM a[92:95],a[172:175],v[28:31],a[92:95]
s_add_u32 m0, s65, 98304
EX v114,v114
MM a[84:87],a[172:175],v[20:23],a[84:87]
TR a[156:157],v233 offset:57856
TR a[158:159],v233 offset:61952
MM a[84:87],a[168:171],v[16:19],a[84:87]
EX v115,v115
MM a[76:79],a[168:171],v[8:11],a[76:79]
EX v116,v116
MM a[76:79],a[172:175],v[12:15],a[76:79]
TR a[160:161],v232 offset:50176
CV v79,v110,v111
MM a[68:71],a[172:175],v[4:7],a[68:71]
EX v117,v117
MM a[68:71],a[168:171],v[0:3],a[68:71]
TR a[162:163],v232 offset:54272
RD a[224:227],v224 offset:8192
MM a[96:99],a[176:179],v[0:3],a[96:99]
EX v118,v118
MM a[96:99],a[180:183],v[4:7],a[96:99]
TR a[164:165],v232 offset:58368
CV v84,v112,v113
MM a[104:107],a[180:183],v[12:15],a[104:107]
buffer_load_dwordx4 v235, s[12:15], s81 offen offset:0 lds
TR a[166:167],v232 offset:62464
EX v119,v119
MM a[104:107],a[176:179],v[8:11],a[104:107]
RD a[228:231],v225 offset:8192
TR a[168:169],v233 offset:50176
MM a[112:115],a[176:179],v[16:19],a[112:115]
EX v120,v120
MM a[112:115],a[180:183],v[20:23],a[112:115]
TR a[170:171],v233 offset:54272
CV v85,v114,v115
MM a[120:123],a[180:183],v[28:31],a[120:123]
s_waitcnt vmcnt(8)
EX v121,v121
MM a[120:123],a[176:179],v[24:27],a[120:123]
s_lshr_b32 s77, %[sb], 3
RD a[232:235],v226 offset:8192
TR a[172:173],v233 offset:58368
MM a[124:127],a[184:187],v[24:27],a[124:127]
s_lshl_b32 s77, s77, 2
v_add_f32 v244, v144, v145
v_add_f32 v245, v148, v149
EX v122,v122
MM a[124:127],a[188:191],v[28:31],a[124:127]
s_add_u32 s77, s77, %[swave]
v_add_f32 v246, v152, v153
v_add_f32 v247, v156, v157
CV v86,v116,v117
MM a[116:119],a[188:191],v[20:23],a[116:119]
s_lshl_b32 s77, s77, 9
TR a[174:175],v233 offset:62464
buffer_load_dwordx4 v235, s[12:15], s64 offen offset:1024 lds
EX v123,v123
MM a[116:119],a[184:187],v[16:19],a[116:119]
s_add_u32 s77, s77, 0
TR a[176:177],v232 offset:50688
v_lshrrev_b32 v253, 1, v248
v_add_u32 v253, s77, v253
MM a[108:111],a[184:187],v[8:11],a[108:111]
s_mov_b32 s77, LKEND_ka-LPD2_%=-16
RD a[236:239],v227 offset:8192
v_min_u32 v253, s77, v253
EX v124,v124
CV v87,v118,v119
MM a[108:111],a[188:191],v[12:15],a[108:111]
s_getpc_b64 s[98:99]
LPD2_%=:
global_load_dword v253, v253, s[98:99]
TR a[178:179],v232 offset:54784
EX v125,v125
MM a[100:103],a[188:191],v[4:7],a[100:103]
TR a[180:181],v232 offset:58880
)ASM"
R"ASM(
RD a[240:243],v224 offset:12288
MM a[100:103],a[184:187],v[0:3],a[100:103]
EX v126,v126
v_smfmac_f32_16x16x64_bf16 v[144:147], v[236:239], v[64:71], v234
RD a[244:247],v225 offset:12288
RD a[248:251],v226 offset:12288
RD a[252:255],v227 offset:12288
EX v127,v127
v_smfmac_f32_16x16x64_bf16 v[148:151], v[236:239], v[72:79], v234
TR a[182:183],v232 offset:62976
CV v92,v120,v121
v_smfmac_f32_16x16x64_bf16 v[152:155], v[236:239], v[80:87], v234
buffer_load_dwordx4 v235, s[12:15], s81 offen offset:2048 lds
CV v93,v122,v123
CV v94,v124,v125
CV v95,v126,v127
s_nop 1
v_smfmac_f32_16x16x64_bf16 v[156:159], v[236:239], v[88:95], v234
TR a[184:185],v233 offset:50688
TR a[186:187],v233 offset:54784
TR a[188:189],v233 offset:58880
TR a[190:191],v233 offset:62976
s_waitcnt lgkmcnt(5)
buffer_load_dwordx4 v235, s[12:15], s64 offen offset:3072 lds
MM v[16:19],a[192:195],v[168:171],v[136:139]
MM v[16:19],a[196:199],v[184:187],v[16:19]
MM v[16:19],a[200:203],v[200:203],v[16:19]
v_max3_f32 v249, v144, v145, v148
v_max3_f32 v249, v249, v149, v152
MM v[16:19],a[204:207],v[216:219],v[16:19]
v_max3_f32 v249, v249, v153, v156
v_max_f32 v249, v249, v157
MM v[20:23],a[220:223],v[216:219],v[136:139]
v_cmp_lt_f32 vcc, s71, v249
MM v[20:23],a[216:219],v[200:203],v[20:23]
s_cbranch_vccnz LSITE35_%=
LBACK35_%=:
v_add_f32 v244, v144, v145
MM v[20:23],a[212:215],v[184:187],v[20:23]
v_add_f32 v245, v148, v149
v_add_f32 v246, v152, v153
MM v[20:23],a[208:211],v[168:171],v[20:23]
s_waitcnt lgkmcnt(0)
v_add_f32 v247, v156, v157
MM a[0:3],a[128:131],v[64:67],a[0:3]
EX v16,v16
MM a[0:3],a[132:135],v[68:71],a[0:3]
v_rcp_f32 v145, v244
MM a[8:11],a[132:135],v[76:79],a[8:11]
EX v17,v17
MM a[8:11],a[128:131],v[72:75],a[8:11]
EX v18,v18
MM a[12:15],a[136:139],v[72:75],a[12:15]
v_rcp_f32 v149, v245
MM a[12:15],a[140:143],v[76:79],a[12:15]
CV v16,v16,v17
v_bfe_u32 v144, v241, 0, 8
MM a[4:7],a[140:143],v[68:71],a[4:7]
EX v19,v19
MM a[4:7],a[136:139],v[64:67],a[4:7]
CV v17,v18,v19
v_lshl_or_b32 v144, v144, 1, 1
MM a[32:35],a[144:147],v[64:67],a[32:35]
EX v20,v20
MM a[32:35],a[148:151],v[68:71],a[32:35]
v_lshlrev_b32 v144, 22, v144
v_mul_f32 v144, -v144, v145
MM a[40:43],a[148:151],v[76:79],a[40:43]
v_bfe_u32 v148, v241, 8, 8
v_lshl_or_b32 v148, v148, 1, 1
MM a[40:43],a[144:147],v[72:75],a[40:43]
v_lshlrev_b32 v148, 22, v148
v_mul_f32 v148, -v148, v149
MM a[44:47],a[152:155],v[72:75],a[44:47]
s_mul_i32 s78, s49, 17408
s_add_u32 s78, s83, s78
s_add_u32 s78, s78, 0
s_add_u32 s79, s78, 4096
AR v0,a0
AR v1,a1
AR v2,a2
MM a[44:47],a[156:159],v[76:79],a[44:47]
AR v3,a3
AR v4,a4
AR v5,a5
MM a[36:39],a[156:159],v[68:71],a[36:39]
AR v6,a6
AR v7,a7
v_fma_f32 v0, v0, v145, v144
v_fma_f32 v1, v1, v145, v144
MM a[36:39],a[152:155],v[64:67],a[36:39]
v_fma_f32 v2, v2, v145, v144
v_fma_f32 v3, v3, v145, v144
v_fma_f32 v4, v4, v145, v144
v_fma_f32 v5, v5, v145, v144
MM a[64:67],a[160:163],v[64:67],a[64:67]
v_fma_f32 v6, v6, v145, v144
v_fma_f32 v7, v7, v145, v144
CV v160,v0,v1
CV v161,v2,v3
MM a[64:67],a[164:167],v[68:71],a[64:67]
CV v162,v4,v5
CV v163,v6,v7
MM a[72:75],a[164:167],v[76:79],a[72:75]
buffer_store_dwordx4 v[160:163], v248, s[20:23], s78 offen offset:0 sc1
AR v8,a8
AR v9,a9
AR v10,a10
MM a[72:75],a[160:163],v[72:75],a[72:75]
AR v11,a11
AR v12,a12
AR v13,a13
EX v21,v21
AR v14,a14
MM a[76:79],a[168:171],v[72:75],a[76:79]
AR v15,a15
v_fma_f32 v8, v8, v149, v148
v_fma_f32 v9, v9, v149, v148
v_fma_f32 v10, v10, v149, v148
MM a[76:79],a[172:175],v[76:79],a[76:79]
v_fma_f32 v11, v11, v149, v148
v_fma_f32 v12, v12, v149, v148
v_fma_f32 v13, v13, v149, v148
v_fma_f32 v14, v14, v149, v148
EX v22,v22
MM a[68:71],a[172:175],v[68:71],a[68:71]
v_fma_f32 v15, v15, v149, v148
CV v164,v8,v9
CV v165,v10,v11
CV v166,v12,v13
MM a[68:71],a[168:171],v[64:67],a[68:71]
CV v167,v14,v15
CV v18,v20,v21
MM a[96:99],a[176:179],v[64:67],a[96:99]
buffer_store_dwordx4 v[164:167], v248, s[20:23], s79 offen offset:0 sc1
AR v32,a32
AR v33,a33
AR v34,a34
EX v23,v23
AR v35,a35
MM a[96:99],a[180:183],v[68:71],a[96:99]
AR v36,a36
AR v37,a37
AR v38,a38
AR v39,a39
MM a[104:107],a[180:183],v[76:79],a[104:107]
v_fma_f32 v32, v32, v145, v144
v_fma_f32 v33, v33, v145, v144
v_fma_f32 v34, v34, v145, v144
v_fma_f32 v35, v35, v145, v144
MM a[104:107],a[176:179],v[72:75],a[104:107]
v_fma_f32 v36, v36, v145, v144
v_fma_f32 v37, v37, v145, v144
v_fma_f32 v38, v38, v145, v144
MM a[108:111],a[184:187],v[72:75],a[108:111]
v_fma_f32 v39, v39, v145, v144
CV v176,v32,v33
CV v177,v34,v35
CV v178,v36,v37
MM a[108:111],a[188:191],v[76:79],a[108:111]
CV v179,v38,v39
CV v19,v22,v23
MM a[100:103],a[188:191],v[68:71],a[100:103]
buffer_store_dwordx4 v[176:179], v248, s[20:23], s78 offen offset:1024 sc1
AR v40,a40
AR v41,a41
AR v42,a42
AR v43,a43
AR v44,a44
MM a[100:103],a[184:187],v[64:67],a[100:103]
AR v45,a45
AR v46,a46
AR v47,a47
v_fma_f32 v40, v40, v149, v148
MM v[28:31],a[208:211],v[172:175],v[140:143]
v_fma_f32 v41, v41, v149, v148
v_fma_f32 v42, v42, v149, v148
v_fma_f32 v43, v43, v149, v148
MM v[28:31],a[212:215],v[188:191],v[28:31]
v_fma_f32 v44, v44, v149, v148
v_fma_f32 v45, v45, v149, v148
v_fma_f32 v46, v46, v149, v148
v_fma_f32 v47, v47, v149, v148
MM v[28:31],a[216:219],v[204:207],v[28:31]
CV v180,v40,v41
CV v181,v42,v43
CV v182,v44,v45
CV v183,v46,v47
MM v[28:31],a[220:223],v[220:223],v[28:31]
buffer_store_dwordx4 v[180:183], v248, s[20:23], s79 offen offset:1024 sc1
AR v0,a64
AR v1,a65
MM v[24:27],a[204:207],v[220:223],v[140:143]
AR v2,a66
AR v3,a67
AR v4,a68
MM v[24:27],a[200:203],v[204:207],v[24:27]
AR v5,a69
AR v6,a70
AR v7,a71
v_fma_f32 v0, v0, v145, v144
MM v[24:27],a[196:199],v[188:191],v[24:27]
v_fma_f32 v1, v1, v145, v144
v_fma_f32 v2, v2, v145, v144
v_fma_f32 v3, v3, v145, v144
v_fma_f32 v4, v4, v145, v144
MM v[24:27],a[192:195],v[172:175],v[24:27]
v_fma_f32 v5, v5, v145, v144
v_fma_f32 v6, v6, v145, v144
v_fma_f32 v7, v7, v145, v144
CV v192,v0,v1
MM v[48:51],a[224:227],v[168:171],v[136:139]
CV v193,v2,v3
CV v194,v4,v5
CV v195,v6,v7
MM v[48:51],a[228:231],v[184:187],v[48:51]
buffer_store_dwordx4 v[192:195], v248, s[20:23], s78 offen offset:2048 sc1
AR v8,a72
AR v9,a73
MM v[48:51],a[232:235],v[200:203],v[48:51]
AR v10,a74
AR v11,a75
AR v12,a76
AR v13,a77
MM v[48:51],a[236:239],v[216:219],v[48:51]
AR v14,a78
AR v15,a79
v_fma_f32 v8, v8, v149, v148
v_fma_f32 v9, v9, v149, v148
MM v[52:55],a[252:255],v[216:219],v[136:139]
EX v24,v24
EX v25,v25
EX v26,v26
EX v27,v27
EX v28,v28
EX v29,v29
v_fma_f32 v10, v10, v149, v148
v_fma_f32 v11, v11, v149, v148
EX v30,v30
v_fma_f32 v12, v12, v149, v148
v_fma_f32 v13, v13, v149, v148
MM v[52:55],a[248:251],v[200:203],v[52:55]
v_fma_f32 v14, v14, v149, v148
v_fma_f32 v15, v15, v149, v148
CV v196,v8,v9
EX v31,v31
CV v24,v24,v25
CV v197,v10,v11
MM v[52:55],a[244:247],v[184:187],v[52:55]
CV v198,v12,v13
CV v199,v14,v15
MM v[52:55],a[240:243],v[168:171],v[52:55]
buffer_store_dwordx4 v[196:199], v248, s[20:23], s79 offen offset:2048 sc1
AR v32,a96
AR v33,a97
AR v34,a98
MM v[60:63],a[240:243],v[172:175],v[140:143]
AR v35,a99
AR v36,a100
AR v37,a101
EX v48,v48
AR v38,a102
MM v[60:63],a[244:247],v[188:191],v[60:63]
AR v39,a103
v_fma_f32 v32, v32, v145, v144
v_fma_f32 v33, v33, v145, v144
v_fma_f32 v34, v34, v145, v144
CV v25,v26,v27
MM v[60:63],a[248:251],v[204:207],v[60:63]
v_fma_f32 v35, v35, v145, v144
v_fma_f32 v36, v36, v145, v144
v_fma_f32 v37, v37, v145, v144
MM v[60:63],a[252:255],v[220:223],v[60:63]
v_fma_f32 v38, v38, v145, v144
v_fma_f32 v39, v39, v145, v144
CV v208,v32,v33
EX v49,v49
CV v209,v34,v35
MM v[56:59],a[236:239],v[220:223],v[140:143]
CV v210,v36,v37
CV v211,v38,v39
MM v[56:59],a[232:235],v[204:207],v[56:59]
buffer_store_dwordx4 v[208:211], v248, s[20:23], s78 offen offset:3072 sc1
AR v40,a104
AR v41,a105
AR v42,a106
AR v43,a107
EX v50,v50
MM v[56:59],a[228:231],v[188:191],v[56:59]
AR v44,a108
AR v45,a109
AR v46,a110
AR v47,a111
CV v26,v28,v29
MM v[56:59],a[224:227],v[172:175],v[56:59]
s_waitcnt lgkmcnt(0)
s_barrier
v_fma_f32 v40, v40, v149, v148
v_fma_f32 v41, v41, v149, v148
v_fma_f32 v42, v42, v149, v148
MM a[16:19],a[128:131],v[80:83],a[16:19]
s_xor_b32 s66, s94, 4
v_fma_f32 v43, v43, v149, v148
v_fma_f32 v44, v44, v149, v148
v_fma_f32 v45, v45, v149, v148
v_fma_f32 v46, v46, v149, v148
EX v51,v51
MM a[16:19],a[132:135],v[84:87],a[16:19]
s_mul_i32 s66, s66, 163840
v_fma_f32 v47, v47, v149, v148
CV v212,v40,v41
CV v213,v42,v43
CV v214,v44,v45
MM a[24:27],a[132:135],v[92:95],a[24:27]
s_add_u32 s66, s66, s87
CV v215,v46,v47
CV v27,v30,v31
MM a[24:27],a[128:131],v[88:91],a[24:27]
s_add_u32 m0, s65, 114688
buffer_store_dwordx4 v[212:215], v248, s[20:23], s79 offen offset:3072 sc1
EX v52,v52
MM a[28:31],a[136:139],v[88:91],a[28:31]
EX v53,v53
MM a[28:31],a[140:143],v[92:95],a[28:31]
RD a[192:195],v224 offset:16384
CV v20,v48,v49
MM a[20:23],a[140:143],v[84:87],a[20:23]
buffer_load_dwordx4 v240, s[24:27], s66 offen offset:0 lds
MM a[20:23],a[136:139],v[80:83],a[20:23]
s_add_u32 s66, s66, 9216
EX v54,v54
MM a[48:51],a[144:147],v[80:83],a[48:51]
RD a[196:199],v225 offset:16384
CV v21,v50,v51
MM a[48:51],a[148:151],v[84:87],a[48:51]
EX v55,v55
MM a[56:59],a[148:151],v[92:95],a[56:59]
RD a[200:203],v226 offset:16384
RD a[204:207],v227 offset:16384
MM a[56:59],a[144:147],v[88:91],a[56:59]
buffer_load_dwordx4 v240, s[24:27], s66 offen offset:1024 lds
MM a[60:63],a[152:155],v[88:91],a[60:63]
s_add_u32 s66, s66, 9216
EX v56,v56
MM a[60:63],a[156:159],v[92:95],a[60:63]
RD a[208:211],v224 offset:20480
CV v22,v52,v53
MM a[52:55],a[156:159],v[84:87],a[52:55]
buffer_load_dwordx4 v240, s[24:27], s66 offen offset:2048 lds
MM a[52:55],a[152:155],v[80:83],a[52:55]
s_add_u32 s66, s66, 9216
EX v57,v57
MM a[80:83],a[160:163],v[80:83],a[80:83]
RD a[212:215],v225 offset:20480
RD a[216:219],v226 offset:20480
MM a[80:83],a[164:167],v[84:87],a[80:83]
buffer_load_dwordx4 v240, s[24:27], s66 offen offset:3072 lds
MM a[88:91],a[164:167],v[92:95],a[88:91]
s_xor_b32 s66, s94, 4
EX v58,v58
MM a[88:91],a[160:163],v[88:91],a[88:91]
s_mul_i32 s66, s66, 163840
RD a[220:223],v227 offset:20480
CV v23,v54,v55
MM a[92:95],a[168:171],v[88:91],a[92:95]
s_add_u32 s66, s66, s86
EX v59,v59
MM a[92:95],a[172:175],v[92:95],a[92:95]
s_add_u32 m0, s65, 49152
RD a[224:227],v224 offset:24576
RD a[228:231],v225 offset:24576
MM a[84:87],a[172:175],v[84:87],a[84:87]
buffer_load_dwordx4 v240, s[24:27], s66 offen offset:0 lds
MM a[84:87],a[168:171],v[80:83],a[84:87]
s_add_u32 s66, s66, 9216
EX v60,v60
MM a[112:115],a[176:179],v[80:83],a[112:115]
RD a[232:235],v226 offset:24576
CV v28,v56,v57
MM a[112:115],a[180:183],v[84:87],a[112:115]
EX v61,v61
MM a[120:123],a[180:183],v[92:95],a[120:123]
buffer_load_dwordx4 v240, s[24:27], s66 offen offset:1024 lds
MM a[120:123],a[176:179],v[88:91],a[120:123]
s_add_u32 s66, s66, 9216
RD a[236:239],v227 offset:24576
RD a[240:243],v224 offset:28672
MM a[124:127],a[184:187],v[88:91],a[124:127]
EX v62,v62
MM a[124:127],a[188:191],v[92:95],a[124:127]
buffer_load_dwordx4 v240, s[24:27], s66 offen offset:2048 lds
MM a[116:119],a[188:191],v[84:87],a[116:119]
s_add_u32 s66, s66, 9216
RD a[244:247],v225 offset:28672
RD a[248:251],v226 offset:28672
RD a[252:255],v227 offset:28672
EX v63,v63
MM a[116:119],a[184:187],v[80:83],a[116:119]
buffer_load_dwordx4 v240, s[24:27], s66 offen offset:3072 lds
v_smfmac_f32_16x16x64_bf16 v[152:155], v[236:239], v[16:23], v234
s_nop 0
LPG25_%=:
s_cbranch_execz LPG29_%=
CV v29,v58,v59
CV v30,v60,v61
CV v31,v62,v63
s_nop 1
v_smfmac_f32_16x16x64_bf16 v[156:159], v[236:239], v[24:31], v234
s_waitcnt lgkmcnt(0)
MM v[80:83],a[192:195],v[168:171],v[136:139]
TR a[128:129],v232 offset:0
TR a[130:131],v232 offset:4096
MM v[80:83],a[196:199],v[184:187],v[80:83]
TR a[132:133],v232 offset:8192
TR a[134:135],v232 offset:12288
MM v[80:83],a[200:203],v[200:203],v[80:83]
TR a[136:137],v233 offset:0
TR a[138:139],v233 offset:4096
MM v[80:83],a[204:207],v[216:219],v[80:83]
v_max3_f32 v249, v152, v153, v156
v_max_f32 v249, v249, v157
MM v[84:87],a[220:223],v[216:219],v[136:139]
TR a[140:141],v233 offset:8192
v_cmp_lt_f32 vcc, s71, v249
MM v[84:87],a[216:219],v[200:203],v[84:87]
)ASM"
R"ASM(
s_cbranch_vccnz LSITE36_%=
LBACK36_%=:
TR a[142:143],v233 offset:12288
v_add_f32 v246, v152, v153
MM v[84:87],a[212:215],v[184:187],v[84:87]
TR a[144:145],v232 offset:512
v_add_f32 v247, v156, v157
MM v[84:87],a[208:211],v[168:171],v[84:87]
TR a[146:147],v232 offset:4608
TR a[148:149],v232 offset:8704
MM v[92:95],a[208:211],v[172:175],v[140:143]
TR a[150:151],v232 offset:12800
TR a[152:153],v233 offset:512
MM v[92:95],a[212:215],v[188:191],v[92:95]
EX v80,v80
MM v[92:95],a[216:219],v[204:207],v[92:95]
v_lshrrev_b32 v98, 1, v248
v_min_u32 v98, 312, v98
MM v[92:95],a[220:223],v[220:223],v[92:95]
s_mov_b32 s62, 94464
buffer_load_dwordx2 v[96:97], v98, s[20:23], s62 offen offset:0 sc1
MM v[88:91],a[204:207],v[220:223],v[140:143]
EX v81,v81
MM v[88:91],a[200:203],v[204:207],v[88:91]
TR a[154:155],v233 offset:4608
TR a[156:157],v233 offset:8704
MM v[88:91],a[196:199],v[188:191],v[88:91]
s_lshl_b32 s97, s84, 2
s_add_u32 s97, s97, s96
s_add_u32 s97, s97, 0
MM v[88:91],a[192:195],v[172:175],v[88:91]
s_waitcnt vmcnt(9)
v_mov_b32 v242, s69
s_mov_b64 exec, 1
buffer_store_dword v242, off, s[20:23], s97 offset:0 sc1
s_mov_b64 exec, -1
TR a[158:159],v233 offset:12800
EX v82,v82
MM v[112:115],a[224:227],v[168:171],v[136:139]
TR a[160:161],v232 offset:1024
TR a[162:163],v232 offset:5120
EX v83,v83
MM v[112:115],a[228:231],v[184:187],v[112:115]
TR a[164:165],v232 offset:9216
TR a[166:167],v232 offset:13312
MM v[112:115],a[232:235],v[200:203],v[112:115]
EX v84,v84
MM v[112:115],a[236:239],v[216:219],v[112:115]
TR a[168:169],v233 offset:1024
TR a[170:171],v233 offset:5120
MM v[116:119],a[252:255],v[216:219],v[136:139]
EX v85,v85
MM v[116:119],a[248:251],v[200:203],v[116:119]
TR a[172:173],v233 offset:9216
TR a[174:175],v233 offset:13312
MM v[116:119],a[244:247],v[184:187],v[116:119]
TR a[176:177],v232 offset:1536
EX v86,v86
MM v[116:119],a[240:243],v[168:171],v[116:119]
TR a[178:179],v232 offset:5632
CV v80,v80,v81
EX v87,v87
MM v[124:127],a[240:243],v[172:175],v[140:143]
TR a[180:181],v232 offset:9728
TR a[182:183],v232 offset:13824
MM v[124:127],a[244:247],v[188:191],v[124:127]
EX v88,v88
MM v[124:127],a[248:251],v[204:207],v[124:127]
TR a[184:185],v233 offset:1536
TR a[186:187],v233 offset:5632
TR a[188:189],v233 offset:9728
TR a[190:191],v233 offset:13824
EX v89,v89
MM v[124:127],a[252:255],v[220:223],v[124:127]
CV v81,v82,v83
CV v82,v84,v85
MM v[120:123],a[236:239],v[220:223],v[140:143]
EX v90,v90
MM v[120:123],a[232:235],v[204:207],v[120:123]
EX v91,v91
MM v[120:123],a[228:231],v[188:191],v[120:123]
CV v83,v86,v87
CV v88,v88,v89
MM v[120:123],a[224:227],v[172:175],v[120:123]
s_waitcnt lgkmcnt(0)
s_barrier
CV v89,v90,v91
EX v92,v92
MM a[16:19],a[128:131],v[16:19],a[16:19]
s_xor_b32 s66, s94, 5
EX v93,v93
MM a[16:19],a[132:135],v[20:23],a[16:19]
s_mul_i32 s66, s66, 163840
EX v94,v94
MM a[24:27],a[132:135],v[28:31],a[24:27]
s_add_u32 s66, s66, s87
EX v95,v95
MM a[24:27],a[128:131],v[24:27],a[24:27]
s_add_u32 m0, s65, 65536
RD a[192:195],v224 offset:32768
CV v90,v92,v93
MM a[28:31],a[136:139],v[24:27],a[28:31]
EX v112,v112
MM a[28:31],a[140:143],v[28:31],a[28:31]
buffer_load_dwordx4 v240, s[24:27], s66 offen offset:0 lds
MM a[20:23],a[140:143],v[20:23],a[20:23]
s_add_u32 s66, s66, 9216
EX v113,v113
MM a[20:23],a[136:139],v[16:19],a[20:23]
RD a[196:199],v225 offset:32768
CV v91,v94,v95
MM a[48:51],a[144:147],v[16:19],a[48:51]
EX v114,v114
MM a[48:51],a[148:151],v[20:23],a[48:51]
RD a[200:203],v226 offset:32768
RD a[204:207],v227 offset:32768
MM a[56:59],a[148:151],v[28:31],a[56:59]
EX v115,v115
MM a[56:59],a[144:147],v[24:27],a[56:59]
buffer_load_dwordx4 v240, s[24:27], s66 offen offset:1024 lds
MM a[60:63],a[152:155],v[24:27],a[60:63]
s_add_u32 s66, s66, 9216
EX v116,v116
MM a[60:63],a[156:159],v[28:31],a[60:63]
RD a[208:211],v224 offset:36864
CV v84,v112,v113
MM a[52:55],a[156:159],v[20:23],a[52:55]
EX v117,v117
MM a[52:55],a[152:155],v[16:19],a[52:55]
buffer_load_dwordx4 v240, s[24:27], s66 offen offset:2048 lds
MM a[80:83],a[160:163],v[16:19],a[80:83]
s_add_u32 s66, s66, 9216
v_lshrrev_b32 v243, 2, v248
v_min_u32 v243, 12, v243
EX v118,v118
MM a[80:83],a[164:167],v[20:23],a[80:83]
buffer_load_dword v242, v243, s[20:23], s44 offen offset:0 sc1
RD a[212:215],v225 offset:36864
MM a[88:91],a[164:167],v[28:31],a[88:91]
EX v119,v119
MM a[88:91],a[160:163],v[24:27],a[88:91]
buffer_load_dwordx4 v240, s[24:27], s66 offen offset:3072 lds
RD a[216:219],v226 offset:36864
EX v120,v120
MM a[92:95],a[168:171],v[24:27],a[92:95]
s_xor_b32 s66, s94, 5
RD a[220:223],v227 offset:36864
CV v85,v114,v115
MM a[92:95],a[172:175],v[28:31],a[92:95]
s_mul_i32 s66, s66, 163840
EX v121,v121
MM a[84:87],a[172:175],v[20:23],a[84:87]
s_add_u32 s66, s66, s86
RD a[224:227],v224 offset:40960
CV v86,v116,v117
MM a[84:87],a[168:171],v[16:19],a[84:87]
s_add_u32 m0, s65, 0
EX v122,v122
MM a[112:115],a[176:179],v[16:19],a[112:115]
buffer_load_dwordx4 v240, s[24:27], s66 offen offset:0 lds
RD a[228:231],v225 offset:40960
EX v123,v123
MM a[112:115],a[180:183],v[20:23],a[112:115]
s_add_u32 s66, s66, 9216
RD a[232:235],v226 offset:40960
CV v87,v118,v119
MM a[120:123],a[180:183],v[28:31],a[120:123]
EX v124,v124
MM a[120:123],a[176:179],v[24:27],a[120:123]
buffer_load_dwordx4 v240, s[24:27], s66 offen offset:1024 lds
MM a[124:127],a[184:187],v[24:27],a[124:127]
s_add_u32 s66, s66, 9216
RD a[236:239],v227 offset:40960
EX v125,v125
MM a[124:127],a[188:191],v[28:31],a[124:127]
EX v126,v126
MM a[116:119],a[188:191],v[20:23],a[116:119]
RD a[240:243],v224 offset:45056
RD a[244:247],v225 offset:45056
RD a[248:251],v226 offset:45056
RD a[252:255],v227 offset:45056
EX v127,v127
MM a[116:119],a[184:187],v[16:19],a[116:119]
buffer_load_dwordx4 v240, s[24:27], s66 offen offset:2048 lds
CV v92,v120,v121
v_smfmac_f32_16x16x64_bf16 v[152:155], v[236:239], v[80:87], v234
s_add_u32 s66, s66, 9216
CV v93,v122,v123
CV v94,v124,v125
CV v95,v126,v127
s_nop 1
v_smfmac_f32_16x16x64_bf16 v[156:159], v[236:239], v[88:95], v234
s_waitcnt lgkmcnt(0)
buffer_load_dwordx4 v240, s[24:27], s66 offen offset:3072 lds
v_lshrrev_b32 v145, 9, v248
v_mul_u32_u24 v145, 1536, v145
v_sub_u32 v244, v232, v145
v_sub_u32 v235, v233, v145
v_add_u32 v244, 65536, v244
v_add_u32 v235, 65536, v235
v_and_b32 v145, 960, v248
v_lshlrev_b32 v145, 1, v145
v_bfe_u32 v146, v248, 7, 2
v_lshl_add_u32 v145, v146, 5, v145
v_bfe_u32 v146, v248, 4, 2
v_lshl_add_u32 v145, v146, 3, v145
v_add_u32 v228, %[slds], v145
v_xor_b32 v229, 32, v228
v_xor_b32 v230, 64, v228
v_xor_b32 v231, 96, v228
v_add_u32 v144, 65536, v228
v_add_u32 v145, 65536, v229
v_add_u32 v146, 65536, v230
v_add_u32 v147, 65536, v231
MM v[16:19],a[192:195],v[168:171],v[136:139]
TR a[128:129],v232 offset:16384
TR a[130:131],v232 offset:20480
MM v[16:19],a[196:199],v[184:187],v[16:19]
TR a[132:133],v232 offset:24576
TR a[134:135],v232 offset:28672
MM v[16:19],a[200:203],v[200:203],v[16:19]
TR a[136:137],v233 offset:16384
TR a[138:139],v233 offset:20480
MM v[16:19],a[204:207],v[216:219],v[16:19]
v_max3_f32 v249, v152, v153, v156
v_max_f32 v249, v249, v157
MM v[20:23],a[220:223],v[216:219],v[136:139]
TR a[140:141],v233 offset:24576
v_cmp_lt_f32 vcc, s71, v249
MM v[20:23],a[216:219],v[200:203],v[20:23]
s_cbranch_vccnz LSITE37_%=
LBACK37_%=:
TR a[142:143],v233 offset:28672
v_add_f32 v246, v152, v153
MM v[20:23],a[212:215],v[184:187],v[20:23]
TR a[144:145],v232 offset:16896
v_add_f32 v247, v156, v157
MM v[20:23],a[208:211],v[168:171],v[20:23]
TR a[146:147],v232 offset:20992
TR a[148:149],v232 offset:25088
MM v[28:31],a[208:211],v[172:175],v[140:143]
TR a[150:151],v232 offset:29184
TR a[152:153],v233 offset:16896
MM v[28:31],a[212:215],v[188:191],v[28:31]
EX v16,v16
MM v[28:31],a[216:219],v[204:207],v[28:31]
s_waitcnt vmcnt(10)
v_xor_b32 v96, s69, v96
v_xor_b32 v97, s69, v97
MM v[28:31],a[220:223],v[220:223],v[28:31]
TR a[154:155],v233 offset:20992
v_or_b32 v99, v96, v97
MM v[24:27],a[204:207],v[220:223],v[140:143]
v_cmp_ne_u32 vcc, 0, v99
s_cbranch_vccnz LRPW_%=
LRPWb_%=:
MM v[24:27],a[200:203],v[204:207],v[24:27]
EX v17,v17
MM v[24:27],a[196:199],v[188:191],v[24:27]
TR a[156:157],v233 offset:25088
TR a[158:159],v233 offset:29184
MM v[24:27],a[192:195],v[172:175],v[24:27]
EX v18,v18
MM v[48:51],a[224:227],v[168:171],v[136:139]
TR a[160:161],v232 offset:17408
TR a[162:163],v232 offset:21504
EX v19,v19
MM v[48:51],a[228:231],v[184:187],v[48:51]
TR a[164:165],v232 offset:25600
TR a[166:167],v232 offset:29696
MM v[48:51],a[232:235],v[200:203],v[48:51]
EX v20,v20
MM v[48:51],a[236:239],v[216:219],v[48:51]
TR a[168:169],v233 offset:17408
TR a[170:171],v233 offset:21504
MM v[52:55],a[252:255],v[216:219],v[136:139]
s_waitcnt vmcnt(5)
v_cmp_ne_u32 vcc, s69, v242
s_cbranch_vccnz LRPO0_%=
LRPO0b_%=:
EX v21,v21
MM v[52:55],a[248:251],v[200:203],v[52:55]
s_xor_b32 s92, s49, 4
s_mul_i32 s92, s92, 17408
s_add_u32 s92, s83, s92
TR a[172:173],v233 offset:25600
TR a[174:175],v233 offset:29696
MM v[52:55],a[244:247],v[184:187],v[52:55]
buffer_load_dwordx4 a[0:3], v248, s[20:23], s92 offen offset:0 sc1
TR a[176:177],v232 offset:17920
EX v22,v22
MM v[52:55],a[240:243],v[168:171],v[52:55]
buffer_load_dwordx4 a[32:35], v248, s[20:23], s92 offen offset:1024 sc1
TR a[178:179],v232 offset:22016
EX v23,v23
MM v[60:63],a[240:243],v[172:175],v[140:143]
TR a[180:181],v232 offset:26112
CV v16,v16,v17
MM v[60:63],a[244:247],v[188:191],v[60:63]
TR a[182:183],v232 offset:30208
TR a[184:185],v233 offset:17920
EX v24,v24
MM v[60:63],a[248:251],v[204:207],v[60:63]
buffer_load_dwordx4 a[64:67], v248, s[20:23], s92 offen offset:2048 sc1
TR a[186:187],v233 offset:22016
TR a[188:189],v233 offset:26112
TR a[190:191],v233 offset:30208
EX v25,v25
MM v[60:63],a[252:255],v[220:223],v[60:63]
EX v26,v26
CV v17,v18,v19
MM v[56:59],a[236:239],v[220:223],v[140:143]
EX v27,v27
MM v[56:59],a[232:235],v[204:207],v[56:59]
buffer_load_dwordx4 a[96:99], v248, s[20:23], s92 offen offset:3072 sc1
MM v[56:59],a[228:231],v[188:191],v[56:59]
s_add_u32 s92, s92, 4096
EX v28,v28
CV v18,v20,v21
MM v[56:59],a[224:227],v[172:175],v[56:59]
s_waitcnt lgkmcnt(0)
s_barrier
CV v19,v22,v23
MM a[16:19],a[128:131],v[80:83],a[16:19]
s_xor_b32 s66, s94, 6
buffer_load_dwordx4 a[4:7], v248, s[20:23], s92 offen offset:0 sc1
MM a[16:19],a[132:135],v[84:87],a[16:19]
s_mul_i32 s66, s66, 163840
EX v29,v29
MM a[24:27],a[132:135],v[92:95],a[24:27]
s_add_u32 s66, s66, s87
EX v30,v30
MM a[24:27],a[128:131],v[88:91],a[24:27]
s_add_u32 m0, s65, 81920
buffer_load_dwordx4 a[36:39], v248, s[20:23], s92 offen offset:1024 sc1
MM a[28:31],a[136:139],v[88:91],a[28:31]
EX v31,v31
MM a[28:31],a[140:143],v[92:95],a[28:31]
CV v24,v24,v25
CV v25,v26,v27
MM a[20:23],a[140:143],v[84:87],a[20:23]
buffer_load_dwordx4 a[68:71], v248, s[20:23], s92 offen offset:2048 sc1
EX v48,v48
MM a[20:23],a[136:139],v[80:83],a[20:23]
EX v49,v49
MM a[48:51],a[144:147],v[80:83],a[48:51]
CV v26,v28,v29
CV v27,v30,v31
MM a[48:51],a[148:151],v[84:87],a[48:51]
buffer_load_dwordx4 a[100:103], v248, s[20:23], s92 offen offset:3072 sc1
EX v50,v50
MM a[56:59],a[148:151],v[92:95],a[56:59]
EX v51,v51
MM a[56:59],a[144:147],v[88:91],a[56:59]
TR a[128:129],v232 offset:32768
CV v20,v48,v49
MM a[60:63],a[152:155],v[88:91],a[60:63]
buffer_load_dwordx4 v240, s[24:27], s66 offen offset:0 lds
s_add_u32 s66, s66, 9216
MM a[60:63],a[156:159],v[92:95],a[60:63]
TR a[130:131],v232 offset:36864
EX v52,v52
MM a[52:55],a[156:159],v[84:87],a[52:55]
TR a[132:133],v232 offset:40960
TR a[134:135],v232 offset:45056
EX v53,v53
MM a[52:55],a[152:155],v[80:83],a[52:55]
buffer_load_dwordx4 v240, s[24:27], s66 offen offset:1024 lds
s_add_u32 s66, s66, 9216
TR a[136:137],v233 offset:32768
MM a[80:83],a[160:163],v[80:83],a[80:83]
TR a[138:139],v233 offset:36864
TR a[140:141],v233 offset:40960
EX v54,v54
MM a[80:83],a[164:167],v[84:87],a[80:83]
TR a[142:143],v233 offset:45056
TR a[144:145],v232 offset:33280
MM a[88:91],a[164:167],v[92:95],a[88:91]
buffer_load_dwordx4 v240, s[24:27], s66 offen offset:2048 lds
s_add_u32 s66, s66, 9216
EX v55,v55
MM a[88:91],a[160:163],v[88:91],a[88:91]
TR a[146:147],v232 offset:37376
)ASM"
R"ASM(
TR a[148:149],v232 offset:41472
EX v56,v56
MM a[92:95],a[168:171],v[88:91],a[92:95]
TR a[150:151],v232 offset:45568
CV v21,v50,v51
MM a[92:95],a[172:175],v[92:95],a[92:95]
buffer_load_dwordx4 v240, s[24:27], s66 offen offset:3072 lds
TR a[152:153],v233 offset:33280
EX v57,v57
MM a[84:87],a[172:175],v[84:87],a[84:87]
s_xor_b32 s66, s94, 6
TR a[154:155],v233 offset:37376
TR a[156:157],v233 offset:41472
MM a[84:87],a[168:171],v[80:83],a[84:87]
s_mul_i32 s66, s66, 163840
TR a[158:159],v233 offset:45568
s_add_u32 s66, s66, s86
s_add_u32 m0, s65, 16384
EX v58,v58
MM a[112:115],a[176:179],v[80:83],a[112:115]
TR a[160:161],v232 offset:33792
buffer_load_dwordx4 v240, s[24:27], s66 offen offset:0 lds
CV v22,v52,v53
EX v59,v59
MM a[112:115],a[180:183],v[84:87],a[112:115]
s_add_u32 s66, s66, 9216
TR a[162:163],v232 offset:37888
TR a[164:165],v232 offset:41984
MM a[120:123],a[180:183],v[92:95],a[120:123]
TR a[166:167],v232 offset:46080
CV v23,v54,v55
EX v60,v60
MM a[120:123],a[176:179],v[88:91],a[120:123]
TR a[168:169],v233 offset:33792
TR a[170:171],v233 offset:37888
buffer_load_dwordx4 v240, s[24:27], s66 offen offset:1024 lds
MM a[124:127],a[184:187],v[88:91],a[124:127]
s_add_u32 s66, s66, 9216
TR a[172:173],v233 offset:41984
EX v61,v61
MM a[124:127],a[188:191],v[92:95],a[124:127]
TR a[174:175],v233 offset:46080
TR a[176:177],v232 offset:34304
EX v62,v62
MM a[116:119],a[188:191],v[84:87],a[116:119]
s_xor_b32 s92, s49, 5
s_mul_i32 s92, s92, 17408
s_add_u32 s92, s83, s92
TR a[178:179],v232 offset:38400
EX v63,v63
MM a[116:119],a[184:187],v[80:83],a[116:119]
buffer_load_dwordx4 a[8:11], v248, s[20:23], s92 offen offset:0 sc1
TR a[180:181],v232 offset:42496
CV v28,v56,v57
v_smfmac_f32_16x16x64_bf16 v[152:155], v[236:239], v[16:23], v234
TR a[182:183],v232 offset:46592
CV v29,v58,v59
CV v30,v60,v61
CV v31,v62,v63
s_nop 1
v_smfmac_f32_16x16x64_bf16 v[156:159], v[236:239], v[24:31], v234
TR a[184:185],v233 offset:34304
TR a[186:187],v233 offset:38400
TR a[188:189],v233 offset:42496
TR a[190:191],v233 offset:46592
s_waitcnt lgkmcnt(0)
buffer_load_dwordx4 v240, s[24:27], s66 offen offset:2048 lds
s_add_u32 s66, s66, 9216
buffer_load_dwordx4 v240, s[24:27], s66 offen offset:3072 lds
s_nop 6
v_max3_f32 v249, v152, v153, v156
v_max_f32 v249, v249, v157
v_cmp_lt_f32 vcc, s71, v249
s_cbranch_vccnz LSITE38_%=
LBACK38_%=:
v_add_f32 v246, v152, v153
v_add_f32 v247, v156, v157
s_waitcnt lgkmcnt(0)
MM a[16:19],a[128:131],v[16:19],a[16:19]
s_barrier
MM a[16:19],a[132:135],v[20:23],a[16:19]
v_rcp_f32 v153, v246
MM a[24:27],a[132:135],v[28:31],a[24:27]
buffer_load_dwordx4 a[40:43], v248, s[20:23], s92 offen offset:1024 sc1
MM a[24:27],a[128:131],v[24:27],a[24:27]
v_rcp_f32 v157, v247
MM a[28:31],a[136:139],v[24:27],a[28:31]
v_bfe_u32 v152, v241, 16, 8
v_lshl_or_b32 v152, v152, 1, 1
MM a[28:31],a[140:143],v[28:31],a[28:31]
v_lshlrev_b32 v152, 22, v152
v_mul_f32 v152, -v152, v153
MM a[20:23],a[140:143],v[20:23],a[20:23]
buffer_load_dwordx4 a[72:75], v248, s[20:23], s92 offen offset:2048 sc1
MM a[20:23],a[136:139],v[16:19],a[20:23]
v_bfe_u32 v156, v241, 24, 8
v_lshl_or_b32 v156, v156, 1, 1
MM a[48:51],a[144:147],v[16:19],a[48:51]
TR a[128:129],v244 offset:0
TR a[130:131],v244 offset:1024
MM a[48:51],a[148:151],v[20:23],a[48:51]
v_lshlrev_b32 v156, 22, v156
v_mul_f32 v156, -v156, v157
MM a[56:59],a[148:151],v[28:31],a[56:59]
s_mul_i32 s78, s49, 17408
TR a[132:133],v244 offset:2048
TR a[134:135],v244 offset:3072
MM a[56:59],a[144:147],v[24:27],a[56:59]
s_add_u32 s78, s83, s78
buffer_load_dwordx4 a[104:107], v248, s[20:23], s92 offen offset:3072 sc1
MM a[60:63],a[152:155],v[24:27],a[60:63]
s_add_u32 s78, s78, 8192
TR a[136:137],v244 offset:4096
TR a[138:139],v244 offset:5120
MM a[60:63],a[156:159],v[28:31],a[60:63]
s_add_u32 s92, s92, 4096
s_add_u32 s79, s78, 4096
AR v80,a16
MM a[52:55],a[156:159],v[20:23],a[52:55]
TR a[140:141],v244 offset:6144
TR a[142:143],v244 offset:7168
MM a[52:55],a[152:155],v[16:19],a[52:55]
buffer_load_dwordx4 a[12:15], v248, s[20:23], s92 offen offset:0 sc1
MM a[80:83],a[160:163],v[16:19],a[80:83]
TR a[144:145],v235 offset:0
TR a[146:147],v235 offset:1024
MM a[80:83],a[164:167],v[20:23],a[80:83]
AR v81,a17
AR v82,a18
MM a[88:91],a[164:167],v[28:31],a[88:91]
TR a[148:149],v235 offset:2048
TR a[150:151],v235 offset:3072
MM a[88:91],a[160:163],v[24:27],a[88:91]
buffer_load_dwordx4 a[44:47], v248, s[20:23], s92 offen offset:1024 sc1
AR v83,a19
MM a[92:95],a[168:171],v[24:27],a[92:95]
TR a[152:153],v235 offset:4096
TR a[154:155],v235 offset:5120
AR v84,a20
AR v85,a21
MM a[92:95],a[172:175],v[28:31],a[92:95]
s_xor_b32 s66, s94, 7
AR v86,a22
AR v87,a23
MM a[84:87],a[172:175],v[20:23],a[84:87]
s_mul_i32 s66, s66, 163840
TR a[156:157],v235 offset:6144
TR a[158:159],v235 offset:7168
v_fma_f32 v80, v80, v153, v152
MM a[84:87],a[168:171],v[16:19],a[84:87]
s_add_u32 s66, s66, s87
buffer_load_dwordx4 a[76:79], v248, s[20:23], s92 offen offset:2048 sc1
v_fma_f32 v81, v81, v153, v152
v_fma_f32 v82, v82, v153, v152
MM a[112:115],a[176:179],v[16:19],a[112:115]
s_add_u32 m0, s65, 98304
TR a[160:161],v144 offset:49152
TR a[162:163],v144 offset:51200
v_fma_f32 v83, v83, v153, v152
MM a[112:115],a[180:183],v[20:23],a[112:115]
v_fma_f32 v84, v84, v153, v152
v_fma_f32 v85, v85, v153, v152
MM a[120:123],a[180:183],v[28:31],a[120:123]
TR a[164:165],v144 offset:53248
TR a[166:167],v144 offset:55296
v_fma_f32 v86, v86, v153, v152
v_fma_f32 v87, v87, v153, v152
MM a[120:123],a[176:179],v[24:27],a[120:123]
buffer_load_dwordx4 a[108:111], v248, s[20:23], s92 offen offset:3072 sc1
CV v168,v80,v81
MM a[124:127],a[184:187],v[24:27],a[124:127]
TR a[168:169],v144 offset:57344
TR a[170:171],v144 offset:59392
CV v169,v82,v83
CV v170,v84,v85
MM a[124:127],a[188:191],v[28:31],a[124:127]
CV v171,v86,v87
MM a[116:119],a[188:191],v[20:23],a[116:119]
buffer_store_dwordx4 v[168:171], v248, s[20:23], s78 offen offset:0 sc1
TR a[172:173],v144 offset:61440
s_nop 0
LPG26_%=:
s_cbranch_execz LPG30_%=
TR a[174:175],v144 offset:63488
AR v88,a24
MM a[116:119],a[184:187],v[16:19],a[116:119]
AR v89,a25
AR v90,a26
s_waitcnt vmcnt(21)
s_waitcnt lgkmcnt(15)
MM v[128:131],a[128:131],a[0:3],0
TR a[176:177],v145 offset:49152
TR a[178:179],v145 offset:51200
AR v91,a27
MM v[128:131],a[132:135],a[32:35],v[128:131]
v_bfe_u32 v249, v248, 6, 2
v_lshlrev_b32 v249, 8, v249
AR v92,a28
AR v93,a29
MM v[128:131],a[136:139],a[64:67],v[128:131]
TR a[180:181],v145 offset:53248
TR a[182:183],v145 offset:55296
AR v94,a30
MM v[128:131],a[140:143],a[96:99],v[128:131]
s_waitcnt vmcnt(17)
buffer_load_dwordx4 v240, s[24:27], s66 offen offset:0 lds
v_lshrrev_b32 v250, 8, v248
v_lshl_add_u32 v249, v250, 6, v249
AR v95,a31
v_fma_f32 v88, v88, v157, v156
MM v[136:139],a[140:143],a[100:103],0
s_add_u32 s66, s66, 9216
TR a[184:185],v145 offset:57344
TR a[186:187],v145 offset:59392
v_fma_f32 v89, v89, v157, v156
v_fma_f32 v90, v90, v157, v156
MM v[136:139],a[136:139],a[68:71],v[136:139]
v_and_b32 v251, 48, v248
v_add_u32 v249, v249, v251
v_fma_f32 v91, v91, v157, v156
MM v[136:139],a[132:135],a[36:39],v[136:139]
s_lshr_b32 s60, s49, 3
TR a[188:189],v145 offset:61440
TR a[190:191],v145 offset:63488
v_fma_f32 v92, v92, v157, v156
v_fma_f32 v93, v93, v157, v156
MM v[136:139],a[128:131],a[4:7],v[136:139]
s_waitcnt lgkmcnt(15)
buffer_load_dwordx4 v240, s[24:27], s66 offen offset:1024 lds
v_fma_f32 v94, v94, v157, v156
v_fma_f32 v95, v95, v157, v156
MM v[140:143],a[144:147],a[4:7],0
s_add_u32 s66, s66, 9216
TR a[128:129],v146 offset:49152
TR a[130:131],v146 offset:51200
CV v172,v88,v89
CV v173,v90,v91
MM v[140:143],a[148:151],a[36:39],v[140:143]
CV v174,v92,v93
CV v175,v94,v95
MM v[140:143],a[152:155],a[68:71],v[140:143]
buffer_store_dwordx4 v[172:175], v248, s[20:23], s79 offen offset:0 sc1
TR a[132:133],v146 offset:53248
TR a[134:135],v146 offset:55296
MM v[140:143],a[156:159],a[100:103],v[140:143]
AR v112,a48
AR v113,a49
MM v[132:135],a[156:159],a[96:99],0
TR a[136:137],v146 offset:57344
TR a[138:139],v146 offset:59392
MM v[132:135],a[152:155],a[64:67],v[132:135]
AR v114,a50
AR v115,a51
MM v[132:135],a[148:151],a[32:35],v[132:135]
TR a[140:141],v146 offset:61440
TR a[142:143],v146 offset:63488
AR v116,a52
MM v[132:135],a[144:147],a[0:3],v[132:135]
s_waitcnt lgkmcnt(15)
buffer_load_dwordx4 v240, s[24:27], s66 offen offset:2048 lds
AR v117,a53
AR v118,a54
MM v[64:67],a[160:163],a[0:3],0
s_xor_b32 s92, s49, 6
TR a[144:145],v147 offset:49152
TR a[146:147],v147 offset:51200
AR v119,a55
v_fma_f32 v112, v112, v153, v152
MM v[64:67],a[164:167],a[32:35],v[64:67]
s_mul_i32 s92, s92, 17408
s_add_u32 s92, s83, s92
v_fma_f32 v113, v113, v153, v152
v_fma_f32 v114, v114, v153, v152
MM v[64:67],a[168:171],a[64:67],v[64:67]
s_add_u32 s66, s66, 9216
TR a[148:149],v147 offset:53248
TR a[150:151],v147 offset:55296
v_fma_f32 v115, v115, v153, v152
MM v[64:67],a[172:175],a[96:99],v[64:67]
buffer_load_dwordx4 v[160:163], v248, s[20:23], s92 offen offset:0 sc1
v_fma_f32 v116, v116, v153, v152
v_fma_f32 v117, v117, v153, v152
MM v[72:75],a[172:175],a[100:103],0
TR a[152:153],v147 offset:57344
TR a[154:155],v147 offset:59392
v_fma_f32 v118, v118, v153, v152
v_fma_f32 v119, v119, v153, v152
MM v[72:75],a[168:171],a[68:71],v[72:75]
CV v184,v112,v113
CV v185,v114,v115
MM v[72:75],a[164:167],a[36:39],v[72:75]
TR a[156:157],v147 offset:61440
TR a[158:159],v147 offset:63488
CV v186,v116,v117
MM v[72:75],a[160:163],a[4:7],v[72:75]
s_waitcnt lgkmcnt(15)
buffer_load_dwordx4 v[176:179], v248, s[20:23], s92 offen offset:1024 sc1
CV v187,v118,v119
MM v[76:79],a[176:179],a[4:7],0
TR a[160:161],v228 offset:49152
TR a[162:163],v228 offset:51200
MM v[76:79],a[180:183],a[36:39],v[76:79]
v_xor_b32 v253, s60, v250
v_and_b32 v238, 240, v248
MM v[76:79],a[184:187],a[68:71],v[76:79]
buffer_store_dwordx4 v[184:187], v248, s[20:23], s78 offen offset:1024 sc1
TR a[164:165],v228 offset:53248
TR a[166:167],v228 offset:55296
AR v120,a56
AR v121,a57
AR v122,a58
AR v123,a59
MM v[76:79],a[188:191],a[100:103],v[76:79]
AR v124,a60
AR v125,a61
MM v[68:71],a[188:191],a[96:99],0
TR a[168:169],v228 offset:57344
TR a[170:171],v228 offset:59392
AR v126,a62
MM v[68:71],a[184:187],a[64:67],v[68:71]
AR v127,a63
v_fma_f32 v120, v120, v157, v156
MM v[68:71],a[180:183],a[32:35],v[68:71]
TR a[172:173],v228 offset:61440
TR a[174:175],v228 offset:63488
v_fma_f32 v121, v121, v157, v156
v_fma_f32 v122, v122, v157, v156
MM v[68:71],a[176:179],a[0:3],v[68:71]
s_waitcnt lgkmcnt(15)
buffer_load_dwordx4 v[192:195], v248, s[20:23], s92 offen offset:2048 sc1
v_fma_f32 v123, v123, v157, v156
MM v[96:99],a[128:131],a[0:3],0
TR a[176:177],v229 offset:49152
TR a[178:179],v229 offset:51200
v_fma_f32 v124, v124, v157, v156
v_fma_f32 v125, v125, v157, v156
MM v[96:99],a[132:135],a[32:35],v[96:99]
v_fma_f32 v126, v126, v157, v156
v_fma_f32 v127, v127, v157, v156
MM v[96:99],a[136:139],a[64:67],v[96:99]
buffer_load_dwordx4 v240, s[24:27], s66 offen offset:3072 lds
TR a[180:181],v229 offset:53248
TR a[182:183],v229 offset:55296
CV v188,v120,v121
MM v[96:99],a[140:143],a[96:99],v[96:99]
CV v189,v122,v123
CV v190,v124,v125
MM v[104:107],a[140:143],a[100:103],0
s_xor_b32 s66, s94, 7
TR a[184:185],v229 offset:57344
TR a[186:187],v229 offset:59392
CV v191,v126,v127
MM v[104:107],a[136:139],a[68:71],v[104:107]
s_mul_i32 s66, s66, 163840
v_lshl_add_u32 v232, v250, 2, v238
s_add_u32 s61, s65, 131072
MM v[104:107],a[132:135],a[36:39],v[104:107]
s_add_u32 s66, s66, s86
buffer_store_dwordx4 v[188:191], v248, s[20:23], s79 offen offset:1024 sc1
TR a[188:189],v229 offset:61440
TR a[190:191],v229 offset:63488
AR v80,a80
AR v81,a81
MM v[104:107],a[128:131],a[4:7],v[104:107]
s_waitcnt lgkmcnt(15)
AR v82,a82
AR v83,a83
MM v[108:111],a[144:147],a[4:7],0
s_add_u32 m0, s65, 32768
TR a[128:129],v230 offset:49152
TR a[130:131],v230 offset:51200
AR v84,a84
MM v[108:111],a[148:151],a[36:39],v[108:111]
AR v85,a85
AR v86,a86
MM v[108:111],a[152:155],a[68:71],v[108:111]
TR a[132:133],v230 offset:53248
TR a[134:135],v230 offset:55296
AR v87,a87
v_fma_f32 v80, v80, v153, v152
MM v[108:111],a[156:159],a[100:103],v[108:111]
)ASM"
R"ASM(
buffer_load_dwordx4 v[208:211], v248, s[20:23], s92 offen offset:3072 sc1
v_fma_f32 v81, v81, v153, v152
v_fma_f32 v82, v82, v153, v152
MM v[100:103],a[156:159],a[96:99],0
TR a[136:137],v230 offset:57344
TR a[138:139],v230 offset:59392
v_fma_f32 v83, v83, v153, v152
MM v[100:103],a[152:155],a[64:67],v[100:103]
s_add_u32 s92, s92, 4096
v_fma_f32 v84, v84, v153, v152
v_fma_f32 v85, v85, v153, v152
MM v[100:103],a[148:151],a[32:35],v[100:103]
TR a[140:141],v230 offset:61440
TR a[142:143],v230 offset:63488
v_fma_f32 v86, v86, v153, v152
v_fma_f32 v87, v87, v153, v152
MM v[100:103],a[144:147],a[0:3],v[100:103]
s_waitcnt lgkmcnt(15)
buffer_load_dwordx4 v[164:167], v248, s[20:23], s92 offen offset:0 sc1
CV v200,v80,v81
MM v[0:3],a[160:163],a[0:3],0
TR a[144:145],v231 offset:49152
TR a[146:147],v231 offset:51200
CV v201,v82,v83
CV v202,v84,v85
MM v[0:3],a[164:167],a[32:35],v[0:3]
CV v203,v86,v87
v_mul_u32_u24 v224, 5, v238
MM v[0:3],a[168:171],a[64:67],v[0:3]
buffer_store_dwordx4 v[200:203], v248, s[20:23], s78 offen offset:2048 sc1
TR a[148:149],v231 offset:53248
TR a[150:151],v231 offset:55296
MM v[0:3],a[172:175],a[96:99],v[0:3]
AR v88,a88
AR v89,a89
MM v[8:11],a[172:175],a[100:103],0
TR a[152:153],v231 offset:57344
TR a[154:155],v231 offset:59392
AR v90,a90
AR v91,a91
MM v[8:11],a[168:171],a[68:71],v[8:11]
AR v92,a92
AR v93,a93
MM v[8:11],a[164:167],a[36:39],v[8:11]
buffer_load_dwordx4 v240, s[24:27], s66 offen offset:0 lds
TR a[156:157],v231 offset:61440
TR a[158:159],v231 offset:63488
AR v94,a94
MM v[8:11],a[160:163],a[4:7],v[8:11]
s_waitcnt lgkmcnt(15)
AR v95,a95
v_fma_f32 v88, v88, v157, v156
s_barrier
MM v[12:15],a[176:179],a[4:7],0
s_add_u32 s66, s66, 9216
TR a[160:161],v244 offset:8192
TR a[162:163],v244 offset:9216
v_fma_f32 v89, v89, v157, v156
v_fma_f32 v90, v90, v157, v156
MM v[12:15],a[180:183],a[36:39],v[12:15]
v_fma_f32 v91, v91, v157, v156
v_fma_f32 v92, v92, v157, v156
MM v[12:15],a[184:187],a[68:71],v[12:15]
TR a[164:165],v244 offset:10240
TR a[166:167],v244 offset:11264
v_fma_f32 v93, v93, v157, v156
MM v[12:15],a[188:191],a[100:103],v[12:15]
buffer_load_dwordx4 v[180:183], v248, s[20:23], s92 offen offset:1024 sc1
v_fma_f32 v94, v94, v157, v156
v_fma_f32 v95, v95, v157, v156
MM v[4:7],a[188:191],a[96:99],0
TR a[168:169],v244 offset:12288
TR a[170:171],v244 offset:13312
CV v204,v88,v89
MM v[4:7],a[184:187],a[64:67],v[4:7]
CV v205,v90,v91
CV v206,v92,v93
MM v[4:7],a[180:183],a[32:35],v[4:7]
TR a[172:173],v244 offset:14336
TR a[174:175],v244 offset:15360
CV v207,v94,v95
MM v[4:7],a[176:179],a[0:3],v[4:7]
s_waitcnt lgkmcnt(15)
buffer_store_dwordx4 v[204:207], v248, s[20:23], s79 offen offset:2048 sc1
MM v[32:35],a[128:131],a[0:3],0
TR a[176:177],v235 offset:8192
TR a[178:179],v235 offset:9216
AR v112,a112
AR v113,a113
MM v[32:35],a[132:135],a[32:35],v[32:35]
AR v114,a114
AR v115,a115
MM v[32:35],a[136:139],a[64:67],v[32:35]
buffer_load_dwordx4 v240, s[24:27], s66 offen offset:1024 lds
TR a[180:181],v235 offset:10240
TR a[182:183],v235 offset:11264
AR v116,a116
MM v[32:35],a[140:143],a[96:99],v[32:35]
s_add_u32 s66, s66, 9216
AR v117,a117
AR v118,a118
MM v[40:43],a[140:143],a[100:103],0
TR a[184:185],v235 offset:12288
TR a[186:187],v235 offset:13312
AR v119,a119
v_fma_f32 v112, v112, v153, v152
MM v[40:43],a[136:139],a[68:71],v[40:43]
v_fma_f32 v113, v113, v153, v152
v_fma_f32 v114, v114, v153, v152
MM v[40:43],a[132:135],a[36:39],v[40:43]
TR a[188:189],v235 offset:14336
TR a[190:191],v235 offset:15360
v_fma_f32 v115, v115, v153, v152
MM v[40:43],a[128:131],a[4:7],v[40:43]
s_waitcnt lgkmcnt(15)
buffer_load_dwordx4 v[196:199], v248, s[20:23], s92 offen offset:2048 sc1
v_fma_f32 v116, v116, v153, v152
v_fma_f32 v117, v117, v153, v152
MM v[44:47],a[144:147],a[4:7],0
TR a[128:129],v144 offset:0
TR a[130:131],v144 offset:2048
v_fma_f32 v118, v118, v153, v152
v_fma_f32 v119, v119, v153, v152
MM v[44:47],a[148:151],a[36:39],v[44:47]
CV v216,v112,v113
CV v217,v114,v115
MM v[44:47],a[152:155],a[68:71],v[44:47]
TR a[132:133],v144 offset:4096
TR a[134:135],v144 offset:6144
CV v218,v116,v117
MM v[44:47],a[156:159],a[100:103],v[44:47]
buffer_load_dwordx4 v[212:215], v248, s[20:23], s92 offen offset:3072 sc1
CV v219,v118,v119
MM v[36:39],a[156:159],a[96:99],0
TR a[136:137],v144 offset:8192
TR a[138:139],v144 offset:10240
MM v[36:39],a[152:155],a[64:67],v[36:39]
v_lshl_add_u32 v224, v250, 3, v224
v_add_u32 v224, s61, v224
MM v[36:39],a[148:151],a[32:35],v[36:39]
buffer_store_dwordx4 v[216:219], v248, s[20:23], s78 offen offset:3072 sc1
TR a[140:141],v144 offset:12288
TR a[142:143],v144 offset:14336
AR v120,a120
AR v121,a121
AR v122,a122
AR v123,a123
MM v[36:39],a[144:147],a[0:3],v[36:39]
s_waitcnt lgkmcnt(15)
AR v124,a124
AR v125,a125
s_waitcnt vmcnt(25)
MM v[128:131],a[160:163],a[8:11],v[128:131]
TR a[144:145],v145 offset:0
TR a[146:147],v145 offset:2048
AR v126,a126
MM v[128:131],a[164:167],a[40:43],v[128:131]
AR v127,a127
v_fma_f32 v120, v120, v157, v156
MM v[128:131],a[168:171],a[72:75],v[128:131]
buffer_load_dwordx4 v240, s[24:27], s66 offen offset:2048 lds
TR a[148:149],v145 offset:4096
TR a[150:151],v145 offset:6144
v_fma_f32 v121, v121, v157, v156
v_fma_f32 v122, v122, v157, v156
MM v[128:131],a[172:175],a[104:107],v[128:131]
s_waitcnt vmcnt(22)
v_fma_f32 v123, v123, v157, v156
v_fma_f32 v124, v124, v157, v156
MM v[136:139],a[172:175],a[108:111],v[136:139]
s_add_u32 s66, s66, 9216
TR a[152:153],v145 offset:8192
TR a[154:155],v145 offset:10240
v_fma_f32 v125, v125, v157, v156
MM v[136:139],a[168:171],a[76:79],v[136:139]
v_fma_f32 v126, v126, v157, v156
v_fma_f32 v127, v127, v157, v156
MM v[136:139],a[164:167],a[44:47],v[136:139]
TR a[156:157],v145 offset:12288
TR a[158:159],v145 offset:14336
CV v220,v120,v121
MM v[136:139],a[160:163],a[12:15],v[136:139]
s_waitcnt lgkmcnt(15)
buffer_load_dwordx4 v240, s[24:27], s66 offen offset:3072 lds
CV v221,v122,v123
CV v222,v124,v125
MM v[140:143],a[176:179],a[12:15],v[140:143]
TR a[160:161],v146 offset:0
TR a[162:163],v146 offset:2048
CV v223,v126,v127
MM v[140:143],a[180:183],a[44:47],v[140:143]
s_xor_b32 s66, s94, 0
v_mul_u32_u24 v254, 1280, v250
v_mul_u32_u24 v250, 33792, v253
MM v[140:143],a[184:187],a[76:79],v[140:143]
s_mul_i32 s66, s66, 163840
buffer_store_dwordx4 v[220:223], v248, s[20:23], s79 offen offset:3072 sc1
TR a[164:165],v146 offset:4096
TR a[166:167],v146 offset:6144
MM v[140:143],a[188:191],a[108:111],v[140:143]
s_add_u32 s66, s66, s88
v_add_u32 v250, v250, v238
v_lshlrev_b32 v253, 4, v253
MM v[132:135],a[188:191],a[104:107],v[132:135]
s_add_u32 m0, s89, 131072
TR a[168:169],v146 offset:8192
TR a[170:171],v146 offset:10240
MM v[132:135],a[184:187],a[72:75],v[132:135]
v_bfe_u32 v238, v248, 4, 3
v_lshl_add_u32 v254, v238, 3, v254
MM v[132:135],a[180:183],a[40:43],v[132:135]
TR a[172:173],v146 offset:12288
TR a[174:175],v146 offset:14336
MM v[132:135],a[176:179],a[8:11],v[132:135]
s_waitcnt lgkmcnt(15)
buffer_load_dwordx4 v252, s[24:27], s66 offen offset:0 lds
MM v[64:67],a[128:131],a[8:11],v[64:67]
s_xor_b32 s92, s49, 7
TR a[176:177],v147 offset:0
TR a[178:179],v147 offset:2048
MM v[64:67],a[132:135],a[40:43],v[64:67]
s_mul_i32 s92, s92, 17408
s_add_u32 s92, s83, s92
v_bfe_u32 v238, v248, 7, 1
MM v[64:67],a[136:139],a[72:75],v[64:67]
s_add_u32 s66, s66, 19456
TR a[180:181],v147 offset:4096
TR a[182:183],v147 offset:6144
MM v[64:67],a[140:143],a[104:107],v[64:67]
buffer_load_dwordx4 a[192:195], v248, s[20:23], s92 offen offset:0 sc1
MM v[72:75],a[140:143],a[108:111],v[72:75]
TR a[184:185],v147 offset:8192
TR a[186:187],v147 offset:10240
MM v[72:75],a[136:139],a[76:79],v[72:75]
v_mul_u32_u24 v238, 5120, v238
v_add_u32 v254, v254, v238
MM v[72:75],a[132:135],a[44:47],v[72:75]
TR a[188:189],v147 offset:12288
TR a[190:191],v147 offset:14336
MM v[72:75],a[128:131],a[12:15],v[72:75]
s_waitcnt lgkmcnt(15)
buffer_load_dwordx4 a[208:211], v248, s[20:23], s92 offen offset:1024 sc1
MM v[76:79],a[144:147],a[12:15],v[76:79]
TR a[128:129],v228 offset:0
TR a[130:131],v228 offset:2048
MM v[76:79],a[148:151],a[44:47],v[76:79]
v_lshrrev_b32 v238, 6, v248
v_mul_u32_u24 v225, 80, v238
MM v[76:79],a[152:155],a[76:79],v[76:79]
TR a[132:133],v228 offset:4096
TR a[134:135],v228 offset:6144
MM v[76:79],a[156:159],a[108:111],v[76:79]
buffer_load_dwordx4 a[224:227], v248, s[20:23], s92 offen offset:2048 sc1
MM v[68:71],a[156:159],a[104:107],v[68:71]
TR a[136:137],v228 offset:8192
TR a[138:139],v228 offset:10240
MM v[68:71],a[152:155],a[72:75],v[68:71]
v_add3_u32 v225, v225, v251, s61
v_mul_u32_u24 v238, 1280, v238
MM v[68:71],a[148:151],a[40:43],v[68:71]
buffer_load_dwordx4 v252, s[24:27], s66 offen offset:1024 lds
TR a[140:141],v228 offset:12288
TR a[142:143],v228 offset:14336
MM v[68:71],a[144:147],a[8:11],v[68:71]
s_waitcnt lgkmcnt(15)
v_add_u32 v251, v251, v238
MM v[96:99],a[160:163],a[8:11],v[96:99]
s_xor_b32 s66, s94, 0
TR a[144:145],v229 offset:0
TR a[146:147],v229 offset:2048
MM v[96:99],a[164:167],a[40:43],v[96:99]
s_mul_i32 s66, s66, 163840
s_add_u32 s66, s66, s87
s_add_u32 m0, s65, 114688
MM v[96:99],a[168:171],a[72:75],v[96:99]
TR a[148:149],v229 offset:4096
TR a[150:151],v229 offset:6144
MM v[96:99],a[172:175],a[104:107],v[96:99]
buffer_load_dwordx4 a[240:243], v248, s[20:23], s92 offen offset:3072 sc1
MM v[104:107],a[172:175],a[108:111],v[104:107]
TR a[152:153],v229 offset:8192
TR a[154:155],v229 offset:10240
MM v[104:107],a[168:171],a[76:79],v[104:107]
s_add_u32 s92, s92, 4096
MM v[104:107],a[164:167],a[44:47],v[104:107]
TR a[156:157],v229 offset:12288
TR a[158:159],v229 offset:14336
MM v[104:107],a[160:163],a[12:15],v[104:107]
s_waitcnt lgkmcnt(15)
buffer_load_dwordx4 a[196:199], v248, s[20:23], s92 offen offset:0 sc1
MM v[108:111],a[176:179],a[12:15],v[108:111]
TR a[160:161],v230 offset:0
TR a[162:163],v230 offset:2048
MM v[108:111],a[180:183],a[44:47],v[108:111]
MM v[108:111],a[184:187],a[76:79],v[108:111]
buffer_load_dwordx4 v240, s[24:27], s66 offen offset:0 lds
TR a[164:165],v230 offset:4096
TR a[166:167],v230 offset:6144
MM v[108:111],a[188:191],a[108:111],v[108:111]
s_add_u32 s66, s66, 9216
MM v[100:103],a[188:191],a[104:107],v[100:103]
TR a[168:169],v230 offset:8192
TR a[170:171],v230 offset:10240
MM v[100:103],a[184:187],a[72:75],v[100:103]
MM v[100:103],a[180:183],a[40:43],v[100:103]
TR a[172:173],v230 offset:12288
TR a[174:175],v230 offset:14336
MM v[100:103],a[176:179],a[8:11],v[100:103]
s_waitcnt lgkmcnt(15)
buffer_load_dwordx4 a[212:215], v248, s[20:23], s92 offen offset:1024 sc1
MM v[0:3],a[128:131],a[8:11],v[0:3]
TR a[176:177],v231 offset:0
TR a[178:179],v231 offset:2048
MM v[0:3],a[132:135],a[40:43],v[0:3]
MM v[0:3],a[136:139],a[72:75],v[0:3]
TR a[180:181],v231 offset:4096
TR a[182:183],v231 offset:6144
MM v[0:3],a[140:143],a[104:107],v[0:3]
buffer_load_dwordx4 a[228:231], v248, s[20:23], s92 offen offset:2048 sc1
MM v[8:11],a[140:143],a[108:111],v[8:11]
TR a[184:185],v231 offset:8192
TR a[186:187],v231 offset:10240
MM v[8:11],a[136:139],a[76:79],v[8:11]
MM v[8:11],a[132:135],a[44:47],v[8:11]
buffer_load_dwordx4 v240, s[24:27], s66 offen offset:1024 lds
TR a[188:189],v231 offset:12288
TR a[190:191],v231 offset:14336
MM v[8:11],a[128:131],a[12:15],v[8:11]
s_waitcnt lgkmcnt(15)
s_add_u32 s66, s66, 9216
s_barrier
MM v[12:15],a[144:147],a[12:15],v[12:15]
TR a[128:129],v244 offset:16384
TR a[130:131],v244 offset:17408
MM v[12:15],a[148:151],a[44:47],v[12:15]
MM v[12:15],a[152:155],a[76:79],v[12:15]
TR a[132:133],v244 offset:18432
LPG27_%=:
s_cbranch_execz LTR1_%=
TR a[134:135],v244 offset:19456
MM v[12:15],a[156:159],a[108:111],v[12:15]
buffer_load_dwordx4 a[244:247], v248, s[20:23], s92 offen offset:3072 sc1
MM v[4:7],a[156:159],a[104:107],v[4:7]
TR a[136:137],v244 offset:20480
TR a[138:139],v244 offset:21504
MM v[4:7],a[152:155],a[72:75],v[4:7]
MM v[4:7],a[148:151],a[40:43],v[4:7]
TR a[140:141],v244 offset:22528
TR a[142:143],v244 offset:23552
MM v[4:7],a[144:147],a[8:11],v[4:7]
s_waitcnt lgkmcnt(15)
buffer_load_dwordx4 v240, s[24:27], s66 offen offset:2048 lds
MM v[32:35],a[160:163],a[8:11],v[32:35]
s_add_u32 s66, s66, 9216
)ASM"
R"ASM(
TR a[144:145],v235 offset:16384
TR a[146:147],v235 offset:17408
MM v[32:35],a[164:167],a[40:43],v[32:35]
MM v[32:35],a[168:171],a[72:75],v[32:35]
TR a[148:149],v235 offset:18432
TR a[150:151],v235 offset:19456
MM v[32:35],a[172:175],a[104:107],v[32:35]
buffer_load_dwordx4 v240, s[24:27], s66 offen offset:3072 lds
MM v[40:43],a[172:175],a[108:111],v[40:43]
TR a[152:153],v235 offset:20480
TR a[154:155],v235 offset:21504
MM v[40:43],a[168:171],a[76:79],v[40:43]
s_xor_b32 s66, s94, 0
s_mul_i32 s66, s66, 163840
s_add_u32 s66, s66, s86
MM v[40:43],a[164:167],a[44:47],v[40:43]
s_add_u32 m0, s65, 49152
TR a[156:157],v235 offset:22528
TR a[158:159],v235 offset:23552
MM v[40:43],a[160:163],a[12:15],v[40:43]
s_waitcnt lgkmcnt(15)
buffer_load_dwordx4 v240, s[24:27], s66 offen offset:0 lds
MM v[44:47],a[176:179],a[12:15],v[44:47]
s_lshl_b32 s97, s84, 2
TR a[160:161],v144 offset:16384
TR a[162:163],v144 offset:18432
MM v[44:47],a[180:183],a[44:47],v[44:47]
s_add_u32 s97, s97, s96
s_add_u32 s97, s97, 16
s_waitcnt vmcnt(15)
MM v[44:47],a[184:187],a[76:79],v[44:47]
s_add_u32 s66, s66, 9216
TR a[164:165],v144 offset:20480
TR a[166:167],v144 offset:22528
MM v[44:47],a[188:191],a[108:111],v[44:47]
v_mov_b32 v242, s69
s_mov_b64 exec, 1
buffer_store_dword v242, off, s[20:23], s97 offset:0 sc1
s_mov_b64 exec, -1
MM v[36:39],a[188:191],a[104:107],v[36:39]
TR a[168:169],v144 offset:24576
TR a[170:171],v144 offset:26624
MM v[36:39],a[184:187],a[72:75],v[36:39]
buffer_load_dwordx4 v240, s[24:27], s66 offen offset:1024 lds
MM v[36:39],a[180:183],a[40:43],v[36:39]
s_add_u32 s66, s66, 9216
TR a[172:173],v144 offset:28672
TR a[174:175],v144 offset:30720
MM v[36:39],a[176:179],a[8:11],v[36:39]
s_waitcnt lgkmcnt(15)
MM v[128:131],a[128:131],v[160:163],v[128:131]
TR a[176:177],v145 offset:16384
TR a[178:179],v145 offset:18432
MM v[128:131],a[132:135],v[176:179],v[128:131]
buffer_load_dwordx4 v240, s[24:27], s66 offen offset:2048 lds
MM v[128:131],a[136:139],v[192:195],v[128:131]
s_add_u32 s66, s66, 9216
TR a[180:181],v145 offset:20480
TR a[182:183],v145 offset:22528
MM v[128:131],a[140:143],v[208:211],v[128:131]
MM v[136:139],a[140:143],v[212:215],v[136:139]
TR a[184:185],v145 offset:24576
TR a[186:187],v145 offset:26624
MM v[136:139],a[136:139],v[196:199],v[136:139]
buffer_load_dwordx4 v240, s[24:27], s66 offen offset:3072 lds
MM v[136:139],a[132:135],v[180:183],v[136:139]
TR a[188:189],v145 offset:28672
TR a[190:191],v145 offset:30720
MM v[136:139],a[128:131],v[164:167],v[136:139]
s_waitcnt lgkmcnt(15)
s_xor_b32 s66, s94, 1
s_mul_i32 s66, s66, 163840
MM v[140:143],a[144:147],v[164:167],v[140:143]
s_add_u32 s66, s66, s88
TR a[128:129],v146 offset:16384
TR a[130:131],v146 offset:18432
MM v[140:143],a[148:151],v[180:183],v[140:143]
s_add_u32 m0, s89, 139264
MM v[140:143],a[152:155],v[196:199],v[140:143]
TR a[132:133],v146 offset:20480
TR a[134:135],v146 offset:22528
MM v[140:143],a[156:159],v[212:215],v[140:143]
buffer_load_dwordx4 v252, s[24:27], s66 offen offset:0 lds
MM v[132:135],a[156:159],v[208:211],v[132:135]
s_add_u32 s66, s66, 19456
TR a[136:137],v146 offset:24576
TR a[138:139],v146 offset:26624
MM v[132:135],a[152:155],v[192:195],v[132:135]
MM v[132:135],a[148:151],v[176:179],v[132:135]
TR a[140:141],v146 offset:28672
TR a[142:143],v146 offset:30720
MM v[132:135],a[144:147],v[160:163],v[132:135]
s_waitcnt lgkmcnt(15)
buffer_load_dwordx4 v252, s[24:27], s66 offen offset:1024 lds
MM v[64:67],a[160:163],v[160:163],v[64:67]
TR a[144:145],v147 offset:16384
TR a[146:147],v147 offset:18432
MM v[64:67],a[164:167],v[176:179],v[64:67]
s_xor_b32 s66, s94, 1
s_mul_i32 s66, s66, 163840
s_add_u32 s66, s66, s87
MM v[64:67],a[168:171],v[192:195],v[64:67]
s_add_u32 m0, s65, 65536
TR a[148:149],v147 offset:20480
TR a[150:151],v147 offset:22528
MM v[64:67],a[172:175],v[208:211],v[64:67]
buffer_load_dwordx4 v240, s[24:27], s66 offen offset:0 lds
MM v[72:75],a[172:175],v[212:215],v[72:75]
s_add_u32 s66, s66, 9216
TR a[152:153],v147 offset:24576
TR a[154:155],v147 offset:26624
MM v[72:75],a[168:171],v[196:199],v[72:75]
MM v[72:75],a[164:167],v[180:183],v[72:75]
TR a[156:157],v147 offset:28672
TR a[158:159],v147 offset:30720
MM v[72:75],a[160:163],v[164:167],v[72:75]
s_waitcnt lgkmcnt(15)
buffer_load_dwordx4 v240, s[24:27], s66 offen offset:1024 lds
MM v[76:79],a[176:179],v[164:167],v[76:79]
s_add_u32 s66, s66, 9216
TR a[160:161],v228 offset:16384
TR a[162:163],v228 offset:18432
MM v[76:79],a[180:183],v[180:183],v[76:79]
MM v[76:79],a[184:187],v[196:199],v[76:79]
TR a[164:165],v228 offset:20480
TR a[166:167],v228 offset:22528
MM v[76:79],a[188:191],v[212:215],v[76:79]
buffer_load_dwordx4 v240, s[24:27], s66 offen offset:2048 lds
MM v[68:71],a[188:191],v[208:211],v[68:71]
s_add_u32 s66, s66, 9216
TR a[168:169],v228 offset:24576
TR a[170:171],v228 offset:26624
MM v[68:71],a[184:187],v[192:195],v[68:71]
MM v[68:71],a[180:183],v[176:179],v[68:71]
TR a[172:173],v228 offset:28672
TR a[174:175],v228 offset:30720
MM v[68:71],a[176:179],v[160:163],v[68:71]
s_waitcnt lgkmcnt(15)
buffer_load_dwordx4 v240, s[24:27], s66 offen offset:3072 lds
MM v[96:99],a[128:131],v[160:163],v[96:99]
s_add_u32 s80, s18, 131072
TR a[176:177],v229 offset:16384
TR a[178:179],v229 offset:18432
MM v[96:99],a[132:135],v[176:179],v[96:99]
buffer_load_dwordx4 v[16:19], v251, s[20:23], s80 offen offset:0 sc1
MM v[96:99],a[136:139],v[192:195],v[96:99]
TR a[180:181],v229 offset:20480
TR a[182:183],v229 offset:22528
MM v[96:99],a[140:143],v[208:211],v[96:99]
v_mov_b32 v237, 0x3f80
v_and_b32 v236, 16, v248
MM v[104:107],a[140:143],v[212:215],v[104:107]
TR a[184:185],v229 offset:24576
TR a[186:187],v229 offset:26624
MM v[104:107],a[136:139],v[196:199],v[104:107]
s_add_u32 s80, s18, 151552
buffer_load_dwordx4 v[20:23], v251, s[20:23], s80 offen offset:0 sc1
MM v[104:107],a[132:135],v[180:183],v[104:107]
TR a[188:189],v229 offset:28672
TR a[190:191],v229 offset:30720
MM v[104:107],a[128:131],v[164:167],v[104:107]
s_waitcnt lgkmcnt(15)
v_lshlrev_b32 v236, v236, v237
s_mov_b32 s28, 0x30003
MM v[108:111],a[144:147],v[164:167],v[108:111]
s_mov_b32 s29, s28
TR a[128:129],v230 offset:16384
TR a[130:131],v230 offset:18432
MM v[108:111],a[148:151],v[180:183],v[108:111]
s_add_u32 s80, s63, 131072
v_lshrrev_b32 v243, 2, v248
v_min_u32 v243, 12, v243
MM v[108:111],a[152:155],v[196:199],v[108:111]
TR a[132:133],v230 offset:20480
TR a[134:135],v230 offset:22528
MM v[108:111],a[156:159],v[212:215],v[108:111]
buffer_load_dword v242, v243, s[20:23], s96 offen offset:16 sc1
buffer_load_dwordx2 v[150:151], v254, s[20:23], s80 offen offset:0 sc1
MM v[100:103],a[156:159],v[208:211],v[100:103]
TR a[136:137],v230 offset:24576
TR a[138:139],v230 offset:26624
MM v[100:103],a[152:155],v[192:195],v[100:103]
v_cndmask_b32 v48, 0, v236, s[28:29]
s_lshl_b64 s[28:29], s[28:29], 2
MM v[100:103],a[148:151],v[176:179],v[100:103]
TR a[140:141],v230 offset:28672
TR a[142:143],v230 offset:30720
MM v[100:103],a[144:147],v[160:163],v[100:103]
s_waitcnt lgkmcnt(15)
v_cndmask_b32 v49, 0, v236, s[28:29]
s_lshl_b64 s[28:29], s[28:29], 2
MM v[0:3],a[160:163],v[160:163],v[0:3]
TR a[144:145],v231 offset:16384
TR a[146:147],v231 offset:18432
MM v[0:3],a[164:167],v[176:179],v[0:3]
v_cndmask_b32 v50, 0, v236, s[28:29]
s_lshl_b64 s[28:29], s[28:29], 2
MM v[0:3],a[168:171],v[192:195],v[0:3]
TR a[148:149],v231 offset:20480
TR a[150:151],v231 offset:22528
MM v[0:3],a[172:175],v[208:211],v[0:3]
v_cndmask_b32 v51, 0, v236, s[28:29]
s_lshl_b64 s[28:29], s[28:29], 2
MM v[8:11],a[172:175],v[212:215],v[8:11]
TR a[152:153],v231 offset:24576
TR a[154:155],v231 offset:26624
MM v[8:11],a[168:171],v[196:199],v[8:11]
v_cndmask_b32 v52, 0, v236, s[28:29]
s_lshl_b64 s[28:29], s[28:29], 2
MM v[8:11],a[164:167],v[180:183],v[8:11]
TR a[156:157],v231 offset:28672
TR a[158:159],v231 offset:30720
MM v[8:11],a[160:163],v[164:167],v[8:11]
s_waitcnt lgkmcnt(15)
v_cndmask_b32 v53, 0, v236, s[28:29]
s_lshl_b64 s[28:29], s[28:29], 2
s_barrier
MM v[12:15],a[176:179],v[164:167],v[12:15]
s_xor_b32 s66, s94, 2
TR a[160:161],v244 offset:24576
TR a[162:163],v244 offset:25600
MM v[12:15],a[180:183],v[180:183],v[12:15]
s_mul_i32 s66, s66, 163840
v_cndmask_b32 v54, 0, v236, s[28:29]
s_lshl_b64 s[28:29], s[28:29], 2
MM v[12:15],a[184:187],v[196:199],v[12:15]
s_add_u32 s66, s66, s88
TR a[164:165],v244 offset:26624
TR a[166:167],v244 offset:27648
MM v[12:15],a[188:191],v[212:215],v[12:15]
s_add_u32 m0, s89, 147456
v_cndmask_b32 v55, 0, v236, s[28:29]
s_mov_b32 s28, 0x300003
MM v[4:7],a[188:191],v[208:211],v[4:7]
s_mov_b32 s29, 0x30000300
TR a[168:169],v244 offset:28672
TR a[170:171],v244 offset:29696
MM v[4:7],a[184:187],v[192:195],v[4:7]
buffer_load_dwordx4 v252, s[24:27], s66 offen offset:0 lds
MM v[4:7],a[180:183],v[176:179],v[4:7]
s_add_u32 s66, s66, 19456
TR a[172:173],v244 offset:30720
TR a[174:175],v244 offset:31744
MM v[4:7],a[176:179],v[160:163],v[4:7]
s_waitcnt lgkmcnt(15)
v_mov_b32 v56, 0
v_mov_b32 v57, 0
MM v[32:35],a[128:131],v[160:163],v[32:35]
TR a[176:177],v235 offset:24576
TR a[178:179],v235 offset:25600
MM v[32:35],a[132:135],v[176:179],v[32:35]
buffer_load_dwordx4 v252, s[24:27], s66 offen offset:1024 lds
MM v[32:35],a[136:139],v[192:195],v[32:35]
TR a[180:181],v235 offset:26624
TR a[182:183],v235 offset:27648
MM v[32:35],a[140:143],v[208:211],v[32:35]
s_xor_b32 s66, s94, 2
v_mov_b32 v60, 0
v_mov_b32 v61, 0
MM v[40:43],a[140:143],v[212:215],v[40:43]
s_mul_i32 s66, s66, 163840
TR a[184:185],v235 offset:28672
TR a[186:187],v235 offset:29696
MM v[40:43],a[136:139],v[196:199],v[40:43]
s_add_u32 s66, s66, s87
v_cndmask_b32 v58, 0, v236, s[28:29]
s_lshl_b64 s[28:29], s[28:29], 2
MM v[40:43],a[132:135],v[180:183],v[40:43]
s_add_u32 m0, s65, 81920
TR a[188:189],v235 offset:30720
TR a[190:191],v235 offset:31744
MM v[40:43],a[128:131],v[164:167],v[40:43]
s_waitcnt lgkmcnt(15)
buffer_load_dwordx4 v240, s[24:27], s66 offen offset:0 lds
MM v[44:47],a[144:147],v[164:167],v[44:47]
s_add_u32 s66, s66, 9216
TR a[128:129],v144 offset:32768
TR a[130:131],v144 offset:34816
MM v[44:47],a[148:151],v[180:183],v[44:47]
v_cndmask_b32 v59, 0, v236, s[28:29]
MM v[44:47],a[152:155],v[196:199],v[44:47]
TR a[132:133],v144 offset:36864
TR a[134:135],v144 offset:38912
MM v[44:47],a[156:159],v[212:215],v[44:47]
buffer_load_dwordx4 v240, s[24:27], s66 offen offset:1024 lds
MM v[36:39],a[156:159],v[208:211],v[36:39]
s_add_u32 s66, s66, 9216
TR a[136:137],v144 offset:40960
TR a[138:139],v144 offset:43008
MM v[36:39],a[152:155],v[192:195],v[36:39]
MM v[36:39],a[148:151],v[176:179],v[36:39]
TR a[140:141],v144 offset:45056
TR a[142:143],v144 offset:47104
MM v[36:39],a[144:147],v[160:163],v[36:39]
s_waitcnt lgkmcnt(15)
buffer_load_dwordx4 v240, s[24:27], s66 offen offset:2048 lds
s_waitcnt vmcnt(28)
MM v[128:131],a[160:163],a[192:195],v[128:131]
s_add_u32 s80, s18, 0
TR a[144:145],v145 offset:32768
TR a[146:147],v145 offset:34816
MM v[128:131],a[164:167],a[208:211],v[128:131]
s_add_u32 s66, s66, 9216
buffer_load_dwordx4 v[24:27], v251, s[32:35], s80 offen offset:0
MM v[128:131],a[168:171],a[224:227],v[128:131]
TR a[148:149],v145 offset:36864
TR a[150:151],v145 offset:38912
MM v[128:131],a[172:175],a[240:243],v[128:131]
s_waitcnt vmcnt(23)
MM v[136:139],a[172:175],a[244:247],v[136:139]
TR a[152:153],v145 offset:40960
TR a[154:155],v145 offset:43008
MM v[136:139],a[168:171],a[228:231],v[136:139]
s_add_u32 s80, s18, 20480
buffer_load_dwordx4 v[28:31], v251, s[32:35], s80 offen offset:0
MM v[136:139],a[164:167],a[212:215],v[136:139]
TR a[156:157],v145 offset:45056
TR a[158:159],v145 offset:47104
MM v[136:139],a[160:163],a[196:199],v[136:139]
s_waitcnt lgkmcnt(15)
MM v[140:143],a[176:179],a[196:199],v[140:143]
TR a[160:161],v146 offset:32768
TR a[162:163],v146 offset:34816
MM v[140:143],a[180:183],a[212:215],v[140:143]
s_add_u32 s80, s63, 0
buffer_load_dwordx2 v[62:63], v254, s[32:35], s80 offen offset:0
MM v[140:143],a[184:187],a[228:231],v[140:143]
TR a[164:165],v146 offset:36864
TR a[166:167],v146 offset:38912
MM v[140:143],a[188:191],a[244:247],v[140:143]
buffer_load_dwordx4 v240, s[24:27], s66 offen offset:3072 lds
)ASM"
R"ASM(
MM v[132:135],a[188:191],a[240:243],v[132:135]
TR a[168:169],v146 offset:40960
TR a[170:171],v146 offset:43008
MM v[132:135],a[184:187],a[224:227],v[132:135]
MM v[132:135],a[180:183],a[208:211],v[132:135]
TR a[172:173],v146 offset:45056
TR a[174:175],v146 offset:47104
MM v[132:135],a[176:179],a[192:195],v[132:135]
s_waitcnt lgkmcnt(15)
MM v[64:67],a[128:131],a[192:195],v[64:67]
s_waitcnt vmcnt(10)
TR a[176:177],v147 offset:32768
TR a[178:179],v147 offset:34816
MM v[64:67],a[132:135],a[208:211],v[64:67]
v_cmp_ne_u32 vcc, s69, v242
s_cbranch_vccnz LRPO1_%=
LRPO1b_%=:
MM v[64:67],a[136:139],a[224:227],v[64:67]
s_xor_b32 s92, s49, 1
TR a[180:181],v147 offset:36864
TR a[182:183],v147 offset:38912
MM v[64:67],a[140:143],a[240:243],v[64:67]
s_mul_i32 s92, s92, 17408
s_add_u32 s92, s83, s92
s_add_u32 s92, s92, 8192
MM v[72:75],a[140:143],a[244:247],v[72:75]
TR a[184:185],v147 offset:40960
TR a[186:187],v147 offset:43008
MM v[72:75],a[136:139],a[228:231],v[72:75]
buffer_load_dwordx4 a[16:19], v248, s[20:23], s92 offen offset:0 sc1
MM v[72:75],a[132:135],a[212:215],v[72:75]
TR a[188:189],v147 offset:45056
TR a[190:191],v147 offset:47104
MM v[72:75],a[128:131],a[196:199],v[72:75]
s_waitcnt lgkmcnt(15)
buffer_load_dwordx4 a[48:51], v248, s[20:23], s92 offen offset:1024 sc1
MM v[76:79],a[144:147],a[196:199],v[76:79]
TR a[128:129],v228 offset:32768
TR a[130:131],v228 offset:34816
MM v[76:79],a[148:151],a[212:215],v[76:79]
MM v[76:79],a[152:155],a[228:231],v[76:79]
TR a[132:133],v228 offset:36864
TR a[134:135],v228 offset:38912
MM v[76:79],a[156:159],a[244:247],v[76:79]
buffer_load_dwordx4 a[80:83], v248, s[20:23], s92 offen offset:2048 sc1
MM v[68:71],a[156:159],a[240:243],v[68:71]
TR a[136:137],v228 offset:40960
TR a[138:139],v228 offset:43008
MM v[68:71],a[152:155],a[224:227],v[68:71]
MM v[68:71],a[148:151],a[208:211],v[68:71]
TR a[140:141],v228 offset:45056
TR a[142:143],v228 offset:47104
MM v[68:71],a[144:147],a[192:195],v[68:71]
s_waitcnt lgkmcnt(15)
buffer_load_dwordx4 a[112:115], v248, s[20:23], s92 offen offset:3072 sc1
MM v[96:99],a[160:163],a[192:195],v[96:99]
s_add_u32 s92, s92, 4096
TR a[144:145],v229 offset:32768
TR a[146:147],v229 offset:34816
MM v[96:99],a[164:167],a[208:211],v[96:99]
MM v[96:99],a[168:171],a[224:227],v[96:99]
TR a[148:149],v229 offset:36864
TR a[150:151],v229 offset:38912
MM v[96:99],a[172:175],a[240:243],v[96:99]
buffer_load_dwordx4 a[20:23], v248, s[20:23], s92 offen offset:0 sc1
MM v[104:107],a[172:175],a[244:247],v[104:107]
TR a[152:153],v229 offset:40960
TR a[154:155],v229 offset:43008
MM v[104:107],a[168:171],a[228:231],v[104:107]
MM v[104:107],a[164:167],a[212:215],v[104:107]
TR a[156:157],v229 offset:45056
TR a[158:159],v229 offset:47104
MM v[104:107],a[160:163],a[196:199],v[104:107]
s_waitcnt lgkmcnt(15)
buffer_load_dwordx4 a[52:55], v248, s[20:23], s92 offen offset:1024 sc1
MM v[108:111],a[176:179],a[196:199],v[108:111]
TR a[160:161],v230 offset:32768
TR a[162:163],v230 offset:34816
MM v[108:111],a[180:183],a[212:215],v[108:111]
MM v[108:111],a[184:187],a[228:231],v[108:111]
TR a[164:165],v230 offset:36864
TR a[166:167],v230 offset:38912
MM v[108:111],a[188:191],a[244:247],v[108:111]
buffer_load_dwordx4 a[84:87], v248, s[20:23], s92 offen offset:2048 sc1
MM v[100:103],a[188:191],a[240:243],v[100:103]
TR a[168:169],v230 offset:40960
TR a[170:171],v230 offset:43008
MM v[100:103],a[184:187],a[224:227],v[100:103]
MM v[100:103],a[180:183],a[208:211],v[100:103]
TR a[172:173],v230 offset:45056
TR a[174:175],v230 offset:47104
MM v[100:103],a[176:179],a[192:195],v[100:103]
s_waitcnt lgkmcnt(15)
buffer_load_dwordx4 a[116:119], v248, s[20:23], s92 offen offset:3072 sc1
MM v[0:3],a[128:131],a[192:195],v[0:3]
TR a[176:177],v231 offset:32768
TR a[178:179],v231 offset:34816
MM v[0:3],a[132:135],a[208:211],v[0:3]
MM v[0:3],a[136:139],a[224:227],v[0:3]
TR a[180:181],v231 offset:36864
TR a[182:183],v231 offset:38912
MM v[0:3],a[140:143],a[240:243],v[0:3]
MM v[8:11],a[140:143],a[244:247],v[8:11]
TR a[184:185],v231 offset:40960
TR a[186:187],v231 offset:43008
MM v[8:11],a[136:139],a[228:231],v[8:11]
MM v[8:11],a[132:135],a[212:215],v[8:11]
TR a[188:189],v231 offset:45056
TR a[190:191],v231 offset:47104
MM v[8:11],a[128:131],a[196:199],v[8:11]
s_waitcnt lgkmcnt(15)
s_barrier
MM v[12:15],a[144:147],a[196:199],v[12:15]
s_xor_b32 s66, s94, 3
TR a[128:129],v244 offset:0
TR a[130:131],v244 offset:1024
MM v[12:15],a[148:151],a[212:215],v[12:15]
s_mul_i32 s66, s66, 163840
s_add_u32 s66, s66, s88
s_add_u32 m0, s89, 155648
MM v[12:15],a[152:155],a[228:231],v[12:15]
TR a[132:133],v244 offset:2048
TR a[134:135],v244 offset:3072
MM v[12:15],a[156:159],a[244:247],v[12:15]
buffer_load_dwordx4 v252, s[24:27], s66 offen offset:0 lds
MM v[4:7],a[156:159],a[240:243],v[4:7]
s_add_u32 s66, s66, 19456
TR a[136:137],v244 offset:4096
TR a[138:139],v244 offset:5120
MM v[4:7],a[152:155],a[224:227],v[4:7]
MM v[4:7],a[148:151],a[208:211],v[4:7]
TR a[140:141],v244 offset:6144
TR a[142:143],v244 offset:7168
MM v[4:7],a[144:147],a[192:195],v[4:7]
s_waitcnt lgkmcnt(15)
buffer_load_dwordx4 v252, s[24:27], s66 offen offset:1024 lds
MM v[32:35],a[160:163],a[192:195],v[32:35]
TR a[144:145],v235 offset:0
TR a[146:147],v235 offset:1024
MM v[32:35],a[164:167],a[208:211],v[32:35]
s_xor_b32 s66, s94, 3
s_mul_i32 s66, s66, 163840
s_add_u32 s66, s66, s87
MM v[32:35],a[168:171],a[224:227],v[32:35]
s_add_u32 m0, s65, 98304
TR a[148:149],v235 offset:2048
TR a[150:151],v235 offset:3072
MM v[32:35],a[172:175],a[240:243],v[32:35]
buffer_load_dwordx4 v240, s[24:27], s66 offen offset:0 lds
MM v[40:43],a[172:175],a[244:247],v[40:43]
s_add_u32 s66, s66, 9216
TR a[152:153],v235 offset:4096
TR a[154:155],v235 offset:5120
MM v[40:43],a[168:171],a[228:231],v[40:43]
MM v[40:43],a[164:167],a[212:215],v[40:43]
TR a[156:157],v235 offset:6144
TR a[158:159],v235 offset:7168
MM v[40:43],a[160:163],a[196:199],v[40:43]
s_waitcnt lgkmcnt(15)
buffer_load_dwordx4 v240, s[24:27], s66 offen offset:1024 lds
MM v[44:47],a[176:179],a[196:199],v[44:47]
s_add_u32 s66, s66, 9216
TR a[160:161],v144 offset:49152
TR a[162:163],v144 offset:51200
MM v[44:47],a[180:183],a[212:215],v[44:47]
MM v[44:47],a[184:187],a[228:231],v[44:47]
TR a[164:165],v144 offset:53248
TR a[166:167],v144 offset:55296
MM v[44:47],a[188:191],a[244:247],v[44:47]
buffer_load_dwordx4 v240, s[24:27], s66 offen offset:2048 lds
MM v[36:39],a[188:191],a[240:243],v[36:39]
s_add_u32 s66, s66, 9216
TR a[168:169],v144 offset:57344
TR a[170:171],v144 offset:59392
MM v[36:39],a[184:187],a[224:227],v[36:39]
MM v[36:39],a[180:183],a[208:211],v[36:39]
TR a[172:173],v144 offset:61440
TR a[174:175],v144 offset:63488
MM v[36:39],a[176:179],a[192:195],v[36:39]
s_waitcnt lgkmcnt(15)
buffer_load_dwordx4 v240, s[24:27], s66 offen offset:3072 lds
MM v[128:131],a[128:131],v[168:171],v[128:131]
TR a[176:177],v145 offset:49152
TR a[178:179],v145 offset:51200
MM v[128:131],a[132:135],v[184:187],v[128:131]
MM v[128:131],a[136:139],v[200:203],v[128:131]
TR a[180:181],v145 offset:53248
TR a[182:183],v145 offset:55296
MM v[128:131],a[140:143],v[216:219],v[128:131]
MM v[136:139],a[140:143],v[220:223],v[136:139]
TR a[184:185],v145 offset:57344
TR a[186:187],v145 offset:59392
MM v[136:139],a[136:139],v[204:207],v[136:139]
MM v[136:139],a[132:135],v[188:191],v[136:139]
TR a[188:189],v145 offset:61440
TR a[190:191],v145 offset:63488
MM v[136:139],a[128:131],v[172:175],v[136:139]
s_waitcnt lgkmcnt(15)
MM v[140:143],a[144:147],v[172:175],v[140:143]
s_xor_b32 s92, s49, 2
TR a[128:129],v146 offset:49152
TR a[130:131],v146 offset:51200
MM v[140:143],a[148:151],v[188:191],v[140:143]
s_nop 0
LPG28_%=:
s_cbranch_execz LTR1_%=
s_mul_i32 s92, s92, 17408
s_add_u32 s92, s83, s92
s_add_u32 s92, s92, 8192
MM v[140:143],a[152:155],v[204:207],v[140:143]
TR a[132:133],v146 offset:53248
TR a[134:135],v146 offset:55296
MM v[140:143],a[156:159],v[220:223],v[140:143]
buffer_load_dwordx4 a[24:27], v248, s[20:23], s92 offen offset:0 sc1
MM v[132:135],a[156:159],v[216:219],v[132:135]
TR a[136:137],v146 offset:57344
TR a[138:139],v146 offset:59392
MM v[132:135],a[152:155],v[200:203],v[132:135]
buffer_load_dwordx4 a[56:59], v248, s[20:23], s92 offen offset:1024 sc1
MM v[132:135],a[148:151],v[184:187],v[132:135]
TR a[140:141],v146 offset:61440
TR a[142:143],v146 offset:63488
MM v[132:135],a[144:147],v[168:171],v[132:135]
s_waitcnt lgkmcnt(15)
MM v[64:67],a[160:163],v[168:171],v[64:67]
TR a[144:145],v147 offset:49152
TR a[146:147],v147 offset:51200
MM v[64:67],a[164:167],v[184:187],v[64:67]
buffer_load_dwordx4 a[88:91], v248, s[20:23], s92 offen offset:2048 sc1
MM v[64:67],a[168:171],v[200:203],v[64:67]
TR a[148:149],v147 offset:53248
TR a[150:151],v147 offset:55296
MM v[64:67],a[172:175],v[216:219],v[64:67]
MM v[72:75],a[172:175],v[220:223],v[72:75]
TR a[152:153],v147 offset:57344
TR a[154:155],v147 offset:59392
MM v[72:75],a[168:171],v[204:207],v[72:75]
buffer_load_dwordx4 a[120:123], v248, s[20:23], s92 offen offset:3072 sc1
MM v[72:75],a[164:167],v[188:191],v[72:75]
s_add_u32 s92, s92, 4096
TR a[156:157],v147 offset:61440
TR a[158:159],v147 offset:63488
MM v[72:75],a[160:163],v[172:175],v[72:75]
s_waitcnt lgkmcnt(15)
MM v[76:79],a[176:179],v[172:175],v[76:79]
TR a[160:161],v230 offset:49152
TR a[162:163],v230 offset:51200
MM v[76:79],a[180:183],v[188:191],v[76:79]
buffer_load_dwordx4 a[28:31], v248, s[20:23], s92 offen offset:0 sc1
MM v[76:79],a[184:187],v[204:207],v[76:79]
TR a[164:165],v230 offset:53248
TR a[166:167],v230 offset:55296
MM v[76:79],a[188:191],v[220:223],v[76:79]
MM v[68:71],a[188:191],v[216:219],v[68:71]
TR a[168:169],v230 offset:57344
TR a[170:171],v230 offset:59392
MM v[68:71],a[184:187],v[200:203],v[68:71]
buffer_load_dwordx4 a[60:63], v248, s[20:23], s92 offen offset:1024 sc1
MM v[68:71],a[180:183],v[184:187],v[68:71]
TR a[172:173],v230 offset:61440
TR a[174:175],v230 offset:63488
MM v[68:71],a[176:179],v[168:171],v[68:71]
s_waitcnt lgkmcnt(15)
MM v[96:99],a[128:131],v[168:171],v[96:99]
TR a[176:177],v231 offset:49152
TR a[178:179],v231 offset:51200
MM v[96:99],a[132:135],v[184:187],v[96:99]
buffer_load_dwordx4 a[92:95], v248, s[20:23], s92 offen offset:2048 sc1
MM v[96:99],a[136:139],v[200:203],v[96:99]
TR a[180:181],v231 offset:53248
TR a[182:183],v231 offset:55296
MM v[96:99],a[140:143],v[216:219],v[96:99]
MM v[104:107],a[140:143],v[220:223],v[104:107]
TR a[184:185],v231 offset:57344
TR a[186:187],v231 offset:59392
MM v[104:107],a[136:139],v[204:207],v[104:107]
buffer_load_dwordx4 a[124:127], v248, s[20:23], s92 offen offset:3072 sc1
MM v[104:107],a[132:135],v[188:191],v[104:107]
s_xor_b32 s92, s49, 3
TR a[188:189],v231 offset:61440
TR a[190:191],v231 offset:63488
MM v[104:107],a[128:131],v[172:175],v[104:107]
s_waitcnt lgkmcnt(15)
s_mul_i32 s92, s92, 17408
s_add_u32 s92, s83, s92
s_barrier
MM v[108:111],a[144:147],v[172:175],v[108:111]
s_add_u32 s92, s92, 8192
TR a[128:129],v244 offset:8192
TR a[130:131],v244 offset:9216
MM v[108:111],a[148:151],v[188:191],v[108:111]
buffer_load_dwordx4 a[200:203], v248, s[20:23], s92 offen offset:0 sc1
MM v[108:111],a[152:155],v[204:207],v[108:111]
TR a[132:133],v244 offset:10240
TR a[134:135],v244 offset:11264
MM v[108:111],a[156:159],v[220:223],v[108:111]
MM v[100:103],a[156:159],v[216:219],v[100:103]
TR a[136:137],v244 offset:12288
TR a[138:139],v244 offset:13312
MM v[100:103],a[152:155],v[200:203],v[100:103]
buffer_load_dwordx4 a[216:219], v248, s[20:23], s92 offen offset:1024 sc1
MM v[100:103],a[148:151],v[184:187],v[100:103]
TR a[140:141],v244 offset:14336
TR a[142:143],v244 offset:15360
MM v[100:103],a[144:147],v[168:171],v[100:103]
s_waitcnt lgkmcnt(15)
MM v[32:35],a[160:163],v[168:171],v[32:35]
TR a[144:145],v235 offset:8192
TR a[146:147],v235 offset:9216
MM v[32:35],a[164:167],v[184:187],v[32:35]
buffer_load_dwordx4 a[232:235], v248, s[20:23], s92 offen offset:2048 sc1
MM v[32:35],a[168:171],v[200:203],v[32:35]
TR a[148:149],v235 offset:10240
TR a[150:151],v235 offset:11264
MM v[32:35],a[172:175],v[216:219],v[32:35]
MM v[40:43],a[172:175],v[220:223],v[40:43]
)ASM"
R"ASM(
TR a[152:153],v235 offset:12288
TR a[154:155],v235 offset:13312
MM v[40:43],a[168:171],v[204:207],v[40:43]
buffer_load_dwordx4 a[248:251], v248, s[20:23], s92 offen offset:3072 sc1
MM v[40:43],a[164:167],v[188:191],v[40:43]
s_add_u32 s92, s92, 4096
TR a[156:157],v235 offset:14336
TR a[158:159],v235 offset:15360
MM v[40:43],a[160:163],v[172:175],v[40:43]
s_waitcnt lgkmcnt(15)
MM v[44:47],a[176:179],v[172:175],v[44:47]
TR a[160:161],v144 offset:0
TR a[162:163],v144 offset:2048
MM v[44:47],a[180:183],v[188:191],v[44:47]
buffer_load_dwordx4 a[204:207], v248, s[20:23], s92 offen offset:0 sc1
MM v[44:47],a[184:187],v[204:207],v[44:47]
TR a[164:165],v144 offset:4096
TR a[166:167],v144 offset:6144
MM v[44:47],a[188:191],v[220:223],v[44:47]
MM v[36:39],a[188:191],v[216:219],v[36:39]
TR a[168:169],v144 offset:8192
TR a[170:171],v144 offset:10240
MM v[36:39],a[184:187],v[200:203],v[36:39]
buffer_load_dwordx4 a[220:223], v248, s[20:23], s92 offen offset:1024 sc1
MM v[36:39],a[180:183],v[184:187],v[36:39]
TR a[172:173],v144 offset:12288
TR a[174:175],v144 offset:14336
MM v[36:39],a[176:179],v[168:171],v[36:39]
s_waitcnt lgkmcnt(15)
s_waitcnt vmcnt(24)
MM v[128:131],a[128:131],a[16:19],v[128:131]
TR a[176:177],v145 offset:0
TR a[178:179],v145 offset:2048
MM v[128:131],a[132:135],a[48:51],v[128:131]
buffer_load_dwordx4 a[236:239], v248, s[20:23], s92 offen offset:2048 sc1
MM v[128:131],a[136:139],a[80:83],v[128:131]
TR a[180:181],v145 offset:4096
TR a[182:183],v145 offset:6144
MM v[128:131],a[140:143],a[112:115],v[128:131]
s_waitcnt vmcnt(21)
MM v[136:139],a[140:143],a[116:119],v[136:139]
TR a[184:185],v145 offset:8192
TR a[186:187],v145 offset:10240
MM v[136:139],a[136:139],a[84:87],v[136:139]
buffer_load_dwordx4 a[252:255], v248, s[20:23], s92 offen offset:3072 sc1
MM v[136:139],a[132:135],a[52:55],v[136:139]
TR a[188:189],v145 offset:12288
TR a[190:191],v145 offset:14336
MM v[136:139],a[128:131],a[20:23],v[136:139]
s_waitcnt lgkmcnt(15)
MM v[140:143],a[144:147],a[20:23],v[140:143]
TR a[128:129],v146 offset:0
TR a[130:131],v146 offset:2048
MM v[140:143],a[148:151],a[52:55],v[140:143]
MM v[140:143],a[152:155],a[84:87],v[140:143]
TR a[132:133],v146 offset:4096
TR a[134:135],v146 offset:6144
MM v[140:143],a[156:159],a[116:119],v[140:143]
MM v[132:135],a[156:159],a[112:115],v[132:135]
TR a[136:137],v146 offset:8192
TR a[138:139],v146 offset:10240
MM v[132:135],a[152:155],a[80:83],v[132:135]
MM v[132:135],a[148:151],a[48:51],v[132:135]
TR a[140:141],v146 offset:12288
TR a[142:143],v146 offset:14336
MM v[132:135],a[144:147],a[16:19],v[132:135]
s_waitcnt lgkmcnt(15)
MM v[64:67],a[160:163],a[16:19],v[64:67]
TR a[144:145],v147 offset:0
TR a[146:147],v147 offset:2048
MM v[64:67],a[164:167],a[48:51],v[64:67]
MM v[64:67],a[168:171],a[80:83],v[64:67]
TR a[148:149],v147 offset:4096
TR a[150:151],v147 offset:6144
MM v[64:67],a[172:175],a[112:115],v[64:67]
MM v[72:75],a[172:175],a[116:119],v[72:75]
TR a[152:153],v147 offset:8192
TR a[154:155],v147 offset:10240
MM v[72:75],a[168:171],a[84:87],v[72:75]
MM v[72:75],a[164:167],a[52:55],v[72:75]
TR a[156:157],v147 offset:12288
TR a[158:159],v147 offset:14336
MM v[72:75],a[160:163],a[20:23],v[72:75]
s_waitcnt lgkmcnt(15)
s_waitcnt vmcnt(16)
s_barrier
MM v[76:79],a[176:179],a[20:23],v[76:79]
s_xor_b32 s66, s94, 1
TR a[160:161],v244 offset:16384
TR a[162:163],v244 offset:17408
MM v[76:79],a[180:183],a[52:55],v[76:79]
s_mul_i32 s66, s66, 163840
s_add_u32 s66, s66, s86
s_add_u32 m0, s65, 0
MM v[76:79],a[184:187],a[84:87],v[76:79]
TR a[164:165],v244 offset:18432
TR a[166:167],v244 offset:19456
MM v[76:79],a[188:191],a[116:119],v[76:79]
buffer_load_dwordx4 v240, s[24:27], s66 offen offset:0 lds
MM v[68:71],a[188:191],a[112:115],v[68:71]
s_add_u32 s66, s66, 9216
TR a[168:169],v244 offset:20480
TR a[170:171],v244 offset:21504
MM v[68:71],a[184:187],a[80:83],v[68:71]
MM v[68:71],a[180:183],a[48:51],v[68:71]
TR a[172:173],v244 offset:22528
TR a[174:175],v244 offset:23552
MM v[68:71],a[176:179],a[16:19],v[68:71]
s_waitcnt lgkmcnt(15)
buffer_load_dwordx4 v240, s[24:27], s66 offen offset:1024 lds
MM v[96:99],a[128:131],a[16:19],v[96:99]
s_add_u32 s66, s66, 9216
TR a[176:177],v244 offset:24576
TR a[178:179],v244 offset:25600
MM v[96:99],a[132:135],a[48:51],v[96:99]
MM v[96:99],a[136:139],a[80:83],v[96:99]
TR a[180:181],v244 offset:26624
TR a[182:183],v244 offset:27648
MM v[96:99],a[140:143],a[112:115],v[96:99]
buffer_load_dwordx4 v240, s[24:27], s66 offen offset:2048 lds
MM v[104:107],a[140:143],a[116:119],v[104:107]
s_add_u32 s66, s66, 9216
TR a[184:185],v244 offset:28672
TR a[186:187],v244 offset:29696
MM v[104:107],a[136:139],a[84:87],v[104:107]
MM v[104:107],a[132:135],a[52:55],v[104:107]
TR a[188:189],v244 offset:30720
TR a[190:191],v244 offset:31744
MM v[104:107],a[128:131],a[20:23],v[104:107]
s_waitcnt lgkmcnt(15)
buffer_load_dwordx4 v240, s[24:27], s66 offen offset:3072 lds
MM v[108:111],a[144:147],a[20:23],v[108:111]
TR a[128:129],v235 offset:16384
TR a[130:131],v235 offset:17408
MM v[108:111],a[148:151],a[52:55],v[108:111]
s_xor_b32 s66, s94, 2
s_mul_i32 s66, s66, 163840
s_add_u32 s66, s66, s86
MM v[108:111],a[152:155],a[84:87],v[108:111]
s_add_u32 m0, s65, 16384
TR a[132:133],v235 offset:18432
TR a[134:135],v235 offset:19456
MM v[108:111],a[156:159],a[116:119],v[108:111]
buffer_load_dwordx4 v240, s[24:27], s66 offen offset:0 lds
MM v[100:103],a[156:159],a[112:115],v[100:103]
s_add_u32 s66, s66, 9216
TR a[136:137],v235 offset:20480
TR a[138:139],v235 offset:21504
MM v[100:103],a[152:155],a[80:83],v[100:103]
MM v[100:103],a[148:151],a[48:51],v[100:103]
TR a[140:141],v235 offset:22528
TR a[142:143],v235 offset:23552
MM v[100:103],a[144:147],a[16:19],v[100:103]
s_waitcnt lgkmcnt(15)
buffer_load_dwordx4 v240, s[24:27], s66 offen offset:1024 lds
s_waitcnt vmcnt(18)
MM v[128:131],a[160:163],a[24:27],v[128:131]
s_add_u32 s66, s66, 9216
TR a[144:145],v235 offset:24576
TR a[146:147],v235 offset:25600
MM v[128:131],a[164:167],a[56:59],v[128:131]
MM v[128:131],a[168:171],a[88:91],v[128:131]
TR a[148:149],v235 offset:26624
TR a[150:151],v235 offset:27648
MM v[128:131],a[172:175],a[120:123],v[128:131]
s_waitcnt vmcnt(14)
buffer_load_dwordx4 v240, s[24:27], s66 offen offset:2048 lds
MM v[136:139],a[172:175],a[124:127],v[136:139]
s_add_u32 s66, s66, 9216
TR a[152:153],v235 offset:28672
TR a[154:155],v235 offset:29696
MM v[136:139],a[168:171],a[92:95],v[136:139]
MM v[136:139],a[164:167],a[60:63],v[136:139]
TR a[156:157],v235 offset:30720
TR a[158:159],v235 offset:31744
MM v[136:139],a[160:163],a[28:31],v[136:139]
s_waitcnt lgkmcnt(15)
buffer_load_dwordx4 v240, s[24:27], s66 offen offset:3072 lds
s_waitcnt vmcnt(12)
MM v[128:131],a[176:179],a[200:203],v[128:131]
s_memrealtime s[56:57]
TR a[160:161],v144 offset:16384
TR a[162:163],v144 offset:18432
MM v[128:131],a[180:183],a[216:219],v[128:131]
s_xor_b32 s66, s94, 3
s_mul_i32 s66, s66, 163840
s_add_u32 s66, s66, s86
MM v[128:131],a[184:187],a[232:235],v[128:131]
s_add_u32 m0, s65, 32768
TR a[164:165],v144 offset:20480
TR a[166:167],v144 offset:22528
MM v[128:131],a[188:191],a[248:251],v[128:131]
s_waitcnt vmcnt(8)
buffer_load_dwordx4 v240, s[24:27], s66 offen offset:0 lds
MM v[136:139],a[188:191],a[252:255],v[136:139]
s_add_u32 s66, s66, 9216
TR a[168:169],v144 offset:24576
TR a[170:171],v144 offset:26624
MM v[136:139],a[184:187],a[236:239],v[136:139]
MM v[136:139],a[180:183],a[220:223],v[136:139]
TR a[172:173],v144 offset:28672
TR a[174:175],v144 offset:30720
MM v[136:139],a[176:179],a[204:207],v[136:139]
s_waitcnt lgkmcnt(0)
buffer_load_dwordx4 v240, s[24:27], s66 offen offset:1024 lds
MM v[140:143],a[128:131],a[28:31],v[140:143]
s_add_u32 s66, s66, 9216
TR a[176:177],v144 offset:32768
TR a[178:179],v144 offset:34816
MM v[140:143],a[132:135],a[60:63],v[140:143]
v_mov_b32 v158, s56
v_mov_b32 v159, s69
MM v[140:143],a[136:139],a[92:95],v[140:143]
TR a[180:181],v144 offset:36864
TR a[182:183],v144 offset:38912
MM v[140:143],a[140:143],a[124:127],v[140:143]
s_add_u32 s67, s95, 0
CV v128,v128,v129
CV v129,v130,v131
MM v[132:135],a[140:143],a[120:123],v[132:135]
buffer_load_dwordx4 v240, s[24:27], s66 offen offset:2048 lds
s_mov_b64 exec, 1
buffer_store_dwordx2 v[158:159], off, s[20:23], s19 offset:8 sc1
s_mov_b64 exec, -1
s_nop 0
TR a[184:185],v144 offset:40960
TR a[186:187],v144 offset:43008
MM v[132:135],a[136:139],a[88:91],v[132:135]
s_add_u32 s66, s66, 9216
CV v136,v136,v137
CV v137,v138,v139
MM v[132:135],a[132:135],a[56:59],v[132:135]
TR a[188:189],v144 offset:45056
TR a[190:191],v144 offset:47104
MM v[132:135],a[128:131],a[24:27],v[132:135]
buffer_load_dwordx4 v240, s[24:27], s66 offen offset:3072 lds
MM v[132:135],a[144:147],a[200:203],v[132:135]
TR a[128:129],v145 offset:16384
TR a[130:131],v145 offset:18432
MM v[132:135],a[148:151],a[216:219],v[132:135]
MM v[132:135],a[152:155],a[232:235],v[132:135]
TR a[132:133],v145 offset:20480
TR a[134:135],v145 offset:22528
MM v[132:135],a[156:159],a[248:251],v[132:135]
MM v[140:143],a[156:159],a[252:255],v[140:143]
TR a[136:137],v145 offset:24576
TR a[138:139],v145 offset:26624
MM v[140:143],a[152:155],a[236:239],v[140:143]
MM v[140:143],a[148:151],a[220:223],v[140:143]
TR a[140:141],v145 offset:28672
TR a[142:143],v145 offset:30720
MM v[140:143],a[144:147],a[204:207],v[140:143]
CV v130,v132,v133
CV v131,v134,v135
MM v[72:75],a[160:163],a[28:31],v[72:75]
TR a[144:145],v145 offset:32768
TR a[146:147],v145 offset:34816
MM v[72:75],a[164:167],a[60:63],v[72:75]
v_min_u32 v131, -2, v131
MM v[72:75],a[168:171],a[92:95],v[72:75]
TR a[148:149],v145 offset:36864
TR a[150:151],v145 offset:38912
MM v[72:75],a[172:175],a[124:127],v[72:75]
buffer_store_dwordx4 v[128:131], v249, s[20:23], s67 offen offset:0 sc1
MM v[64:67],a[172:175],a[120:123],v[64:67]
TR a[152:153],v145 offset:40960
TR a[154:155],v145 offset:43008
MM v[64:67],a[168:171],a[88:91],v[64:67]
CV v138,v140,v141
CV v139,v142,v143
MM v[64:67],a[164:167],a[56:59],v[64:67]
TR a[156:157],v145 offset:45056
TR a[158:159],v145 offset:47104
MM v[64:67],a[160:163],a[24:27],v[64:67]
s_waitcnt lgkmcnt(15)
v_min_u32 v139, -2, v139
MM v[64:67],a[176:179],a[200:203],v[64:67]
TR a[160:161],v146 offset:16384
TR a[162:163],v146 offset:18432
MM v[64:67],a[180:183],a[216:219],v[64:67]
buffer_store_dwordx4 v[136:139], v249, s[20:23], s67 offen offset:1024 sc1
MM v[64:67],a[184:187],a[232:235],v[64:67]
TR a[164:165],v146 offset:20480
TR a[166:167],v146 offset:22528
MM v[64:67],a[188:191],a[248:251],v[64:67]
MM v[72:75],a[188:191],a[252:255],v[72:75]
TR a[168:169],v146 offset:24576
TR a[170:171],v146 offset:26624
MM v[72:75],a[184:187],a[236:239],v[72:75]
MM v[72:75],a[180:183],a[220:223],v[72:75]
TR a[172:173],v146 offset:28672
TR a[174:175],v146 offset:30720
MM v[72:75],a[176:179],a[204:207],v[72:75]
s_waitcnt lgkmcnt(15)
MM v[76:79],a[128:131],a[28:31],v[76:79]
TR a[176:177],v146 offset:32768
TR a[178:179],v146 offset:34816
MM v[76:79],a[132:135],a[60:63],v[76:79]
MM v[76:79],a[136:139],a[92:95],v[76:79]
TR a[180:181],v146 offset:36864
TR a[182:183],v146 offset:38912
MM v[76:79],a[140:143],a[124:127],v[76:79]
s_add_u32 s67, s95, 4096
CV v64,v64,v65
CV v65,v66,v67
MM v[68:71],a[140:143],a[120:123],v[68:71]
TR a[184:185],v146 offset:40960
TR a[186:187],v146 offset:43008
MM v[68:71],a[136:139],a[88:91],v[68:71]
CV v72,v72,v73
CV v73,v74,v75
MM v[68:71],a[132:135],a[56:59],v[68:71]
TR a[188:189],v146 offset:45056
TR a[190:191],v146 offset:47104
MM v[68:71],a[128:131],a[24:27],v[68:71]
s_waitcnt lgkmcnt(15)
MM v[68:71],a[144:147],a[200:203],v[68:71]
TR a[128:129],v147 offset:16384
TR a[130:131],v147 offset:18432
MM v[68:71],a[148:151],a[216:219],v[68:71]
buffer_load_dwordx2 v[148:149], v253, s[20:23], s68 offen offset:8 sc1
MM v[68:71],a[152:155],a[232:235],v[68:71]
TR a[132:133],v147 offset:20480
TR a[134:135],v147 offset:22528
MM v[68:71],a[156:159],a[248:251],v[68:71]
MM v[76:79],a[156:159],a[252:255],v[76:79]
TR a[136:137],v147 offset:24576
TR a[138:139],v147 offset:26624
MM v[76:79],a[152:155],a[236:239],v[76:79]
MM v[76:79],a[148:151],a[220:223],v[76:79]
)ASM"
R"ASM(
TR a[140:141],v147 offset:28672
TR a[142:143],v147 offset:30720
MM v[76:79],a[144:147],a[204:207],v[76:79]
s_waitcnt lgkmcnt(15)
CV v66,v68,v69
CV v67,v70,v71
s_waitcnt vmcnt(3)
s_barrier
MM v[104:107],a[160:163],a[28:31],v[104:107]
TR a[144:145],v147 offset:32768
TR a[146:147],v147 offset:34816
MM v[104:107],a[164:167],a[60:63],v[104:107]
v_min_u32 v67, -2, v67
MM v[104:107],a[168:171],a[92:95],v[104:107]
TR a[148:149],v147 offset:36864
TR a[150:151],v147 offset:38912
MM v[104:107],a[172:175],a[124:127],v[104:107]
buffer_store_dwordx4 v[64:67], v248, s[20:23], s67 offen offset:0 sc1
MM v[96:99],a[172:175],a[120:123],v[96:99]
TR a[152:153],v147 offset:40960
TR a[154:155],v147 offset:43008
MM v[96:99],a[168:171],a[88:91],v[96:99]
CV v74,v76,v77
CV v75,v78,v79
MM v[96:99],a[164:167],a[56:59],v[96:99]
TR a[156:157],v147 offset:45056
TR a[158:159],v147 offset:47104
MM v[96:99],a[160:163],a[24:27],v[96:99]
s_waitcnt lgkmcnt(15)
v_min_u32 v75, -2, v75
MM v[96:99],a[176:179],a[200:203],v[96:99]
TR a[160:161],v230 offset:0
TR a[162:163],v230 offset:2048
MM v[96:99],a[180:183],a[216:219],v[96:99]
buffer_store_dwordx4 v[72:75], v248, s[20:23], s67 offen offset:1024 sc1
MM v[96:99],a[184:187],a[232:235],v[96:99]
TR a[164:165],v230 offset:4096
TR a[166:167],v230 offset:6144
MM v[96:99],a[188:191],a[248:251],v[96:99]
MM v[104:107],a[188:191],a[252:255],v[104:107]
TR a[168:169],v230 offset:8192
TR a[170:171],v230 offset:10240
MM v[104:107],a[184:187],a[236:239],v[104:107]
MM v[104:107],a[180:183],a[220:223],v[104:107]
TR a[172:173],v230 offset:12288
TR a[174:175],v230 offset:14336
MM v[104:107],a[176:179],a[204:207],v[104:107]
s_waitcnt lgkmcnt(15)
MM v[108:111],a[128:131],a[28:31],v[108:111]
TR a[176:177],v230 offset:16384
TR a[178:179],v230 offset:18432
MM v[108:111],a[132:135],a[60:63],v[108:111]
MM v[108:111],a[136:139],a[92:95],v[108:111]
TR a[180:181],v230 offset:20480
TR a[182:183],v230 offset:22528
MM v[108:111],a[140:143],a[124:127],v[108:111]
s_add_u32 s67, s95, 4096
CV v96,v96,v97
CV v97,v98,v99
MM v[100:103],a[140:143],a[120:123],v[100:103]
TR a[184:185],v230 offset:24576
TR a[186:187],v230 offset:26624
MM v[100:103],a[136:139],a[88:91],v[100:103]
CV v104,v104,v105
CV v105,v106,v107
MM v[100:103],a[132:135],a[56:59],v[100:103]
TR a[188:189],v230 offset:28672
TR a[190:191],v230 offset:30720
MM v[100:103],a[128:131],a[24:27],v[100:103]
s_waitcnt lgkmcnt(15)
MM v[100:103],a[144:147],a[200:203],v[100:103]
TR a[128:129],v230 offset:32768
TR a[130:131],v230 offset:34816
MM v[100:103],a[148:151],a[216:219],v[100:103]
MM v[100:103],a[152:155],a[232:235],v[100:103]
TR a[132:133],v230 offset:36864
TR a[134:135],v230 offset:38912
MM v[100:103],a[156:159],a[248:251],v[100:103]
MM v[108:111],a[156:159],a[252:255],v[108:111]
TR a[136:137],v230 offset:40960
TR a[138:139],v230 offset:43008
MM v[108:111],a[152:155],a[236:239],v[108:111]
MM v[108:111],a[148:151],a[220:223],v[108:111]
TR a[140:141],v230 offset:45056
TR a[142:143],v230 offset:47104
MM v[108:111],a[144:147],a[204:207],v[108:111]
s_waitcnt lgkmcnt(15)
CV v98,v100,v101
CV v99,v102,v103
MM v[40:43],a[160:163],a[20:23],v[40:43]
TR a[144:145],v231 offset:0
TR a[146:147],v231 offset:2048
MM v[40:43],a[164:167],a[52:55],v[40:43]
v_min_u32 v99, -2, v99
v_lshlrev_b32 v136, 16, v150
MM v[40:43],a[168:171],a[84:87],v[40:43]
TR a[148:149],v231 offset:4096
TR a[150:151],v231 offset:6144
MM v[40:43],a[172:175],a[116:119],v[40:43]
s_nop 0
LPG29_%=:
s_cbranch_execz LTR1_%=
buffer_store_dwordx4 v[96:99], v248, s[20:23], s67 offen offset:2048 sc1
MM v[32:35],a[172:175],a[112:115],v[32:35]
TR a[152:153],v231 offset:8192
TR a[154:155],v231 offset:10240
MM v[32:35],a[168:171],a[80:83],v[32:35]
CV v106,v108,v109
CV v107,v110,v111
MM v[32:35],a[164:167],a[48:51],v[32:35]
TR a[156:157],v231 offset:12288
TR a[158:159],v231 offset:14336
MM v[32:35],a[160:163],a[16:19],v[32:35]
s_waitcnt lgkmcnt(15)
v_min_u32 v107, -2, v107
v_and_b32 v137, 0xffff0000, v150
MM v[32:35],a[176:179],a[24:27],v[32:35]
TR a[160:161],v231 offset:16384
TR a[162:163],v231 offset:18432
MM v[32:35],a[180:183],a[56:59],v[32:35]
buffer_store_dwordx4 v[104:107], v248, s[20:23], s67 offen offset:3072 sc1
MM v[32:35],a[184:187],a[88:91],v[32:35]
TR a[164:165],v231 offset:20480
TR a[166:167],v231 offset:22528
MM v[32:35],a[188:191],a[120:123],v[32:35]
v_lshlrev_b32 v138, 16, v151
v_and_b32 v139, 0xffff0000, v151
MM v[40:43],a[188:191],a[124:127],v[40:43]
TR a[168:169],v231 offset:24576
TR a[170:171],v231 offset:26624
MM v[40:43],a[184:187],a[92:95],v[40:43]
v_mul_f32 v140, 0xbfb8aa3b, v136
v_mul_f32 v141, 0xbfb8aa3b, v137
MM v[40:43],a[180:183],a[60:63],v[40:43]
TR a[172:173],v231 offset:28672
TR a[174:175],v231 offset:30720
MM v[40:43],a[176:179],a[28:31],v[40:43]
s_waitcnt lgkmcnt(15)
v_mul_f32 v142, 0xbfb8aa3b, v138
v_mul_f32 v143, 0xbfb8aa3b, v139
MM v[40:43],a[128:131],a[204:207],v[40:43]
TR a[176:177],v231 offset:32768
TR a[178:179],v231 offset:34816
MM v[40:43],a[132:135],a[220:223],v[40:43]
EX v140,v140
MM v[40:43],a[136:139],a[236:239],v[40:43]
TR a[180:181],v231 offset:36864
TR a[182:183],v231 offset:38912
MM v[40:43],a[140:143],a[252:255],v[40:43]
EX v141,v141
MM v[32:35],a[140:143],a[248:251],v[32:35]
TR a[184:185],v231 offset:40960
TR a[186:187],v231 offset:43008
MM v[32:35],a[136:139],a[232:235],v[32:35]
EX v142,v142
MM v[32:35],a[132:135],a[216:219],v[32:35]
TR a[188:189],v231 offset:45056
TR a[190:191],v231 offset:47104
MM v[32:35],a[128:131],a[200:203],v[32:35]
s_waitcnt lgkmcnt(15)
EX v143,v143
MM v[36:39],a[144:147],a[16:19],v[36:39]
TR a[128:129],v228 offset:49152
TR a[130:131],v228 offset:51200
MM v[36:39],a[148:151],a[48:51],v[36:39]
v_add_f32 v140, 1.0, v140
v_add_f32 v141, 1.0, v141
MM v[36:39],a[152:155],a[80:83],v[36:39]
TR a[132:133],v228 offset:53248
TR a[134:135],v228 offset:55296
MM v[36:39],a[156:159],a[112:115],v[36:39]
s_add_u32 s67, s95, 0
CV v32,v32,v33
CV v33,v34,v35
MM v[44:47],a[156:159],a[116:119],v[44:47]
TR a[136:137],v228 offset:57344
TR a[138:139],v228 offset:59392
MM v[44:47],a[152:155],a[84:87],v[44:47]
CV v40,v40,v41
CV v41,v42,v43
MM v[44:47],a[148:151],a[52:55],v[44:47]
TR a[140:141],v228 offset:61440
TR a[142:143],v228 offset:63488
MM v[44:47],a[144:147],a[20:23],v[44:47]
s_waitcnt lgkmcnt(15)
v_add_f32 v142, 1.0, v142
v_add_f32 v143, 1.0, v143
MM v[44:47],a[160:163],a[28:31],v[44:47]
s_waitcnt vmcnt(4)
TR a[144:145],v228 offset:0
TR a[146:147],v228 offset:2048
MM v[44:47],a[164:167],a[60:63],v[44:47]
v_cmp_eq_u32 s[8:9], s69, v149
v_subrev_u32 v226, s56, v148
MM v[44:47],a[168:171],a[92:95],v[44:47]
TR a[148:149],v228 offset:4096
TR a[150:151],v228 offset:6144
MM v[44:47],a[172:175],a[124:127],v[44:47]
v_rcp_f32 v140, v140
MM v[36:39],a[172:175],a[120:123],v[36:39]
TR a[152:153],v228 offset:8192
TR a[154:155],v228 offset:10240
v_cndmask_b32 v226, 4.0, v226, s[8:9]
MM v[36:39],a[168:171],a[88:91],v[36:39]
v_readlane_b32 s13, v226, 16
v_readlane_b32 s14, v226, 32
MM v[36:39],a[164:167],a[56:59],v[36:39]
TR a[156:157],v228 offset:12288
TR a[158:159],v228 offset:14336
MM v[36:39],a[160:163],a[24:27],v[36:39]
s_waitcnt lgkmcnt(15)
v_rcp_f32 v141, v141
v_readlane_b32 s15, v226, 48
MM v[36:39],a[176:179],a[200:203],v[36:39]
s_max_i32 s12, s13, s14
TR a[160:161],v228 offset:16384
TR a[162:163],v228 offset:18432
MM v[36:39],a[180:183],a[216:219],v[36:39]
s_max_i32 s12, s12, s15
v_rcp_f32 v142, v142
MM v[36:39],a[184:187],a[232:235],v[36:39]
s_max_i32 s12, s12, 0
TR a[164:165],v228 offset:20480
TR a[166:167],v228 offset:22528
MM v[36:39],a[188:191],a[248:251],v[36:39]
buffer_load_dwordx2 v[148:149], v253, s[20:23], s68 offen offset:8 sc1
MM v[44:47],a[188:191],a[252:255],v[44:47]
TR a[168:169],v228 offset:24576
TR a[170:171],v228 offset:26624
MM v[44:47],a[184:187],a[236:239],v[44:47]
v_rcp_f32 v143, v143
MM v[44:47],a[180:183],a[220:223],v[44:47]
TR a[172:173],v228 offset:28672
TR a[174:175],v228 offset:30720
MM v[44:47],a[176:179],a[204:207],v[44:47]
s_waitcnt lgkmcnt(15)
CV v34,v36,v37
CV v35,v38,v39
v_min_u32 v35, -2, v35
buffer_store_dwordx4 v[32:35], v248, s[20:23], s67 offen offset:2048 sc1
MM v[8:11],a[128:131],v[172:175],v[8:11]
TR a[176:177],v228 offset:32768
TR a[178:179],v228 offset:34816
MM v[8:11],a[132:135],v[188:191],v[8:11]
v_mul_f32 v140, v136, v140
v_mul_f32 v141, v137, v141
MM v[8:11],a[136:139],v[204:207],v[8:11]
TR a[180:181],v228 offset:36864
TR a[182:183],v228 offset:38912
MM v[8:11],a[140:143],v[220:223],v[8:11]
CV v42,v44,v45
CV v43,v46,v47
v_min_u32 v43, -2, v43
buffer_store_dwordx4 v[40:43], v248, s[20:23], s67 offen offset:3072 sc1
MM v[0:3],a[140:143],v[216:219],v[0:3]
TR a[184:185],v228 offset:40960
TR a[186:187],v228 offset:43008
MM v[0:3],a[136:139],v[200:203],v[0:3]
v_mul_f32 v142, v138, v142
v_mul_f32 v143, v139, v143
MM v[0:3],a[132:135],v[184:187],v[0:3]
TR a[188:189],v228 offset:45056
TR a[190:191],v228 offset:47104
MM v[0:3],a[128:131],v[168:171],v[0:3]
s_waitcnt lgkmcnt(15)
CV v150,v140,v141
CV v151,v142,v143
MM v[0:3],a[144:147],a[16:19],v[0:3]
TR a[128:129],v229 offset:49152
TR a[130:131],v229 offset:51200
MM v[0:3],a[148:151],a[48:51],v[0:3]
s_cmp_le_i32 s12, 76
s_cselect_b32 s60, 1, 0
s_cselect_b64 s[8:9], -1, 0
MM v[0:3],a[152:155],a[80:83],v[0:3]
s_or_b32 s58, s58, s60
TR a[132:133],v229 offset:53248
TR a[134:135],v229 offset:55296
MM v[0:3],a[156:159],a[112:115],v[0:3]
s_mov_b64 exec, s[8:9]
buffer_load_dwordx4 v[120:123], v250, s[20:23], s39 offen offset:0 sc1
s_mov_b64 exec, -1
MM v[8:11],a[156:159],a[116:119],v[8:11]
TR a[136:137],v229 offset:57344
TR a[138:139],v229 offset:59392
MM v[8:11],a[152:155],a[84:87],v[8:11]
s_mov_b64 exec, s[8:9]
buffer_load_dwordx4 v[124:127], v250, s[20:23], s39 offen offset:256 sc1
s_mov_b64 exec, -1
MM v[8:11],a[148:151],a[52:55],v[8:11]
TR a[140:141],v229 offset:61440
TR a[142:143],v229 offset:63488
MM v[8:11],a[144:147],a[20:23],v[8:11]
s_waitcnt lgkmcnt(15)
v_lshlrev_b32 v160, 16, v16
v_and_b32 v161, 0xffff0000, v16
MM v[8:11],a[160:163],a[28:31],v[8:11]
TR a[144:145],v229 offset:0
TR a[146:147],v229 offset:2048
MM v[8:11],a[164:167],a[60:63],v[8:11]
s_cmp_le_i32 s14, 51
s_cselect_b32 s60, 4, 0
s_cselect_b64 s[8:9], -1, 0
MM v[8:11],a[168:171],a[92:95],v[8:11]
s_or_b32 s58, s58, s60
TR a[148:149],v229 offset:4096
TR a[150:151],v229 offset:6144
MM v[8:11],a[172:175],a[124:127],v[8:11]
s_mov_b64 exec, s[8:9]
buffer_load_dwordx4 v[88:91], v248, s[20:23], s37 offen offset:0 sc1
s_mov_b64 exec, -1
MM v[0:3],a[172:175],a[120:123],v[0:3]
TR a[152:153],v229 offset:8192
TR a[154:155],v229 offset:10240
MM v[0:3],a[168:171],a[88:91],v[0:3]
s_mov_b64 exec, s[8:9]
buffer_load_dwordx4 v[92:95], v248, s[20:23], s37 offen offset:1024 sc1
s_mov_b64 exec, -1
MM v[0:3],a[164:167],a[56:59],v[0:3]
TR a[156:157],v229 offset:12288
TR a[158:159],v229 offset:14336
MM v[0:3],a[160:163],a[24:27],v[0:3]
s_waitcnt lgkmcnt(15)
v_lshlrev_b32 v162, 16, v17
v_and_b32 v163, 0xffff0000, v17
MM v[0:3],a[176:179],a[200:203],v[0:3]
TR a[160:161],v229 offset:16384
TR a[162:163],v229 offset:18432
MM v[0:3],a[180:183],a[216:219],v[0:3]
s_cmp_le_i32 s15, 25
s_cselect_b32 s60, 8, 0
s_cselect_b64 s[8:9], -1, 0
MM v[0:3],a[184:187],a[232:235],v[0:3]
s_or_b32 s58, s58, s60
TR a[164:165],v229 offset:20480
TR a[166:167],v229 offset:22528
MM v[0:3],a[188:191],a[248:251],v[0:3]
s_mov_b64 exec, s[8:9]
buffer_load_dwordx4 v[112:115], v248, s[20:23], s38 offen offset:0 sc1
s_mov_b64 exec, -1
MM v[8:11],a[188:191],a[252:255],v[8:11]
TR a[168:169],v229 offset:24576
TR a[170:171],v229 offset:26624
MM v[8:11],a[184:187],a[236:239],v[8:11]
s_mov_b64 exec, s[8:9]
buffer_load_dwordx4 v[116:119], v248, s[20:23], s38 offen offset:1024 sc1
s_mov_b64 exec, -1
MM v[8:11],a[180:183],a[220:223],v[8:11]
TR a[172:173],v229 offset:28672
TR a[174:175],v229 offset:30720
MM v[8:11],a[176:179],a[204:207],v[8:11]
s_waitcnt lgkmcnt(15)
v_lshlrev_b32 v164, 16, v18
v_and_b32 v165, 0xffff0000, v18
MM v[12:15],a[128:131],v[172:175],v[12:15]
TR a[176:177],v229 offset:32768
TR a[178:179],v229 offset:34816
MM v[12:15],a[132:135],v[188:191],v[12:15]
)ASM"
R"ASM(
v_lshlrev_b32 v166, 16, v19
v_and_b32 v167, 0xffff0000, v19
MM v[12:15],a[136:139],v[204:207],v[12:15]
TR a[180:181],v229 offset:36864
TR a[182:183],v229 offset:38912
MM v[12:15],a[140:143],v[220:223],v[12:15]
v_mul_f32 v136, 0xbfb8aa3b, v160
v_mul_f32 v137, 0xbfb8aa3b, v161
MM v[4:7],a[140:143],v[216:219],v[4:7]
TR a[184:185],v229 offset:40960
TR a[186:187],v229 offset:43008
MM v[4:7],a[136:139],v[200:203],v[4:7]
v_mul_f32 v138, 0xbfb8aa3b, v162
v_mul_f32 v139, 0xbfb8aa3b, v163
MM v[4:7],a[132:135],v[184:187],v[4:7]
TR a[188:189],v229 offset:45056
TR a[190:191],v229 offset:47104
MM v[4:7],a[128:131],v[168:171],v[4:7]
s_waitcnt lgkmcnt(15)
v_mul_f32 v140, 0xbfb8aa3b, v164
v_mul_f32 v141, 0xbfb8aa3b, v165
MM v[4:7],a[144:147],a[16:19],v[4:7]
s_cmp_le_i32 s13, -8
s_cselect_b32 s60, 2, 0
s_cselect_b64 s[8:9], -1, 0
MM v[4:7],a[148:151],a[48:51],v[4:7]
s_or_b32 s58, s58, s60
s_mov_b64 exec, s[8:9]
buffer_load_dwordx4 v[80:83], v248, s[20:23], s36 offen offset:0 sc1
s_mov_b64 exec, -1
MM v[4:7],a[152:155],a[80:83],v[4:7]
v_mul_f32 v142, 0xbfb8aa3b, v166
v_mul_f32 v143, 0xbfb8aa3b, v167
MM v[4:7],a[156:159],a[112:115],v[4:7]
s_mov_b64 exec, s[8:9]
buffer_load_dwordx4 v[84:87], v248, s[20:23], s36 offen offset:1024 sc1
s_mov_b64 exec, -1
MM v[12:15],a[156:159],a[116:119],v[12:15]
EX v136,v136
MM v[12:15],a[152:155],a[84:87],v[12:15]
EX v137,v137
MM v[12:15],a[148:151],a[52:55],v[12:15]
EX v138,v138
MM v[12:15],a[144:147],a[20:23],v[12:15]
s_waitcnt lgkmcnt(8)
EX v139,v139
MM v[12:15],a[160:163],a[28:31],v[12:15]
EX v140,v140
MM v[12:15],a[164:167],a[60:63],v[12:15]
EX v141,v141
MM v[12:15],a[168:171],a[92:95],v[12:15]
EX v142,v142
MM v[12:15],a[172:175],a[124:127],v[12:15]
EX v143,v143
MM v[4:7],a[172:175],a[120:123],v[4:7]
v_add_f32 v136, 1.0, v136
v_add_f32 v137, 1.0, v137
MM v[4:7],a[168:171],a[88:91],v[4:7]
v_add_f32 v138, 1.0, v138
v_add_f32 v139, 1.0, v139
MM v[4:7],a[164:167],a[56:59],v[4:7]
v_add_f32 v140, 1.0, v140
v_add_f32 v141, 1.0, v141
MM v[4:7],a[160:163],a[24:27],v[4:7]
s_waitcnt lgkmcnt(0)
v_add_f32 v142, 1.0, v142
v_add_f32 v143, 1.0, v143
MM v[4:7],a[176:179],a[200:203],v[4:7]
v_rcp_f32 v136, v136
MM v[4:7],a[180:183],a[216:219],v[4:7]
v_rcp_f32 v137, v137
MM v[4:7],a[184:187],a[232:235],v[4:7]
v_rcp_f32 v138, v138
MM v[4:7],a[188:191],a[248:251],v[4:7]
v_rcp_f32 v139, v139
MM v[12:15],a[188:191],a[252:255],v[12:15]
v_rcp_f32 v140, v140
MM v[12:15],a[184:187],a[236:239],v[12:15]
v_rcp_f32 v141, v141
MM v[12:15],a[180:183],a[220:223],v[12:15]
v_rcp_f32 v142, v142
MM v[12:15],a[176:179],a[204:207],v[12:15]
v_rcp_f32 v143, v143
s_memrealtime s[54:55]
s_cmp_eq_u32 s58, 15
s_cbranch_scc1 LSK1_%=
s_memrealtime s[98:99]
s_andn2_b32 s91, s17, s58
s_mov_b32 s77, 0x7fffffff
s_waitcnt lgkmcnt(0)
s_sub_u32 s76, s98, s54
s_bitcmp0_b32 s91, 1
s_cbranch_scc1 LSM11_%=
s_sub_u32 s90, s13, s76
s_sub_u32 s90, s90, 12
s_cmp_gt_i32 s90, 0
s_cbranch_scc1 LSM11_%=
buffer_load_dwordx4 v[80:83], v248, s[20:23], s36 offen offset:0 sc1
buffer_load_dwordx4 v[84:87], v248, s[20:23], s36 offen offset:1024 sc1
s_bitset1_b32 s58, 1
LSM11_%=:
s_bitcmp0_b32 s91, 3
s_cbranch_scc1 LSM13_%=
s_sub_u32 s90, s15, s76
s_sub_u32 s90, s90, 63
s_cmp_gt_i32 s90, 0
s_cbranch_scc1 LSM13_%=
buffer_load_dwordx4 v[112:115], v248, s[20:23], s38 offen offset:0 sc1
buffer_load_dwordx4 v[116:119], v248, s[20:23], s38 offen offset:1024 sc1
s_bitset1_b32 s58, 3
LSM13_%=:
s_bitcmp0_b32 s91, 2
s_cbranch_scc1 LSM12_%=
s_sub_u32 s90, s14, s76
s_sub_u32 s90, s90, 97
s_cmp_gt_i32 s90, 0
s_cbranch_scc1 LSM12_%=
buffer_load_dwordx4 v[88:91], v248, s[20:23], s37 offen offset:0 sc1
buffer_load_dwordx4 v[92:95], v248, s[20:23], s37 offen offset:1024 sc1
s_bitset1_b32 s58, 2
LSM12_%=:
s_bitcmp0_b32 s91, 0
s_cbranch_scc1 LSM10_%=
s_sub_u32 s90, s12, s76
s_sub_u32 s90, s90, 131
s_cmp_gt_i32 s90, 0
s_cbranch_scc1 LSM10_%=
buffer_load_dwordx4 v[120:123], v250, s[20:23], s39 offen offset:0 sc1
buffer_load_dwordx4 v[124:127], v250, s[20:23], s39 offen offset:256 sc1
s_bitset1_b32 s58, 0
LSM10_%=:
LSK1_%=:
v_mul_f32 v136, v160, v136
v_mul_f32 v137, v161, v137
v_mul_f32 v138, v162, v138
v_mul_f32 v139, v163, v139
v_mul_f32 v140, v164, v140
v_mul_f32 v141, v165, v141
v_mul_f32 v142, v166, v142
v_mul_f32 v143, v167, v143
CV v16,v136,v137
CV v17,v138,v139
CV v18,v140,v141
CV v19,v142,v143
v_lshlrev_b32 v160, 16, v16
v_and_b32 v161, 0xffff0000, v16
v_lshlrev_b32 v162, 16, v17
v_and_b32 v163, 0xffff0000, v17
v_lshlrev_b32 v164, 16, v18
v_and_b32 v165, 0xffff0000, v18
v_lshlrev_b32 v166, 16, v19
v_and_b32 v167, 0xffff0000, v19
v_lshlrev_b32 v176, 16, v20
v_and_b32 v177, 0xffff0000, v20
v_lshlrev_b32 v178, 16, v21
v_and_b32 v179, 0xffff0000, v21
v_lshlrev_b32 v180, 16, v22
v_and_b32 v181, 0xffff0000, v22
v_lshlrev_b32 v182, 16, v23
v_and_b32 v183, 0xffff0000, v23
v_mul_f32 v136, 0xbfb8aa3b, v176
v_mul_f32 v137, 0xbfb8aa3b, v177
v_mul_f32 v138, 0xbfb8aa3b, v178
v_mul_f32 v139, 0xbfb8aa3b, v179
v_mul_f32 v140, 0xbfb8aa3b, v180
v_mul_f32 v141, 0xbfb8aa3b, v181
v_mul_f32 v142, 0xbfb8aa3b, v182
v_mul_f32 v143, 0xbfb8aa3b, v183
EX v136,v136
s_waitcnt vmcnt(10)
v_cmp_eq_u32 s[8:9], s69, v149
v_subrev_u32 v226, s56, v148
s_nop 0
v_cndmask_b32 v226, 4.0, v226, s[8:9]
s_nop 0
v_readlane_b32 s13, v226, 16
v_readlane_b32 s14, v226, 32
v_readlane_b32 s15, v226, 48
s_max_i32 s12, s13, s14
s_max_i32 s12, s12, s15
s_max_i32 s12, s12, 0
s_cmp_eq_u32 s58, 15
s_cbranch_scc1 LSK2_%=
s_memrealtime s[98:99]
s_andn2_b32 s91, s17, s58
s_mov_b32 s77, 0x7fffffff
s_waitcnt lgkmcnt(0)
s_sub_u32 s76, s98, s54
s_bitcmp0_b32 s91, 1
s_cbranch_scc1 LSM21_%=
s_sub_u32 s90, s13, s76
s_sub_u32 s90, s90, 12
s_cmp_gt_i32 s90, 0
s_cbranch_scc1 LSM21_%=
buffer_load_dwordx4 v[80:83], v248, s[20:23], s36 offen offset:0 sc1
buffer_load_dwordx4 v[84:87], v248, s[20:23], s36 offen offset:1024 sc1
s_bitset1_b32 s58, 1
LSM21_%=:
s_bitcmp0_b32 s91, 3
s_cbranch_scc1 LSM23_%=
s_sub_u32 s90, s15, s76
s_sub_u32 s90, s90, 63
s_cmp_gt_i32 s90, 0
s_cbranch_scc1 LSM23_%=
buffer_load_dwordx4 v[112:115], v248, s[20:23], s38 offen offset:0 sc1
buffer_load_dwordx4 v[116:119], v248, s[20:23], s38 offen offset:1024 sc1
s_bitset1_b32 s58, 3
LSM23_%=:
s_bitcmp0_b32 s91, 2
s_cbranch_scc1 LSM22_%=
s_sub_u32 s90, s14, s76
s_sub_u32 s90, s90, 97
s_cmp_gt_i32 s90, 0
s_cbranch_scc1 LSM22_%=
buffer_load_dwordx4 v[88:91], v248, s[20:23], s37 offen offset:0 sc1
buffer_load_dwordx4 v[92:95], v248, s[20:23], s37 offen offset:1024 sc1
s_bitset1_b32 s58, 2
LSM22_%=:
s_bitcmp0_b32 s91, 0
s_cbranch_scc1 LSM20_%=
s_sub_u32 s90, s12, s76
s_sub_u32 s90, s90, 131
s_cmp_gt_i32 s90, 0
s_cbranch_scc1 LSM20_%=
buffer_load_dwordx4 v[120:123], v250, s[20:23], s39 offen offset:0 sc1
buffer_load_dwordx4 v[124:127], v250, s[20:23], s39 offen offset:256 sc1
s_bitset1_b32 s58, 0
LSM20_%=:
LSK2_%=:
EX v137,v137
EX v138,v138
EX v139,v139
EX v140,v140
EX v141,v141
EX v142,v142
EX v143,v143
v_add_f32 v136, 1.0, v136
v_add_f32 v137, 1.0, v137
v_add_f32 v138, 1.0, v138
v_add_f32 v139, 1.0, v139
v_add_f32 v140, 1.0, v140
v_add_f32 v141, 1.0, v141
v_add_f32 v142, 1.0, v142
v_add_f32 v143, 1.0, v143
v_rcp_f32 v136, v136
v_rcp_f32 v137, v137
v_rcp_f32 v138, v138
v_rcp_f32 v139, v139
v_rcp_f32 v140, v140
v_rcp_f32 v141, v141
v_rcp_f32 v142, v142
v_rcp_f32 v143, v143
s_cmp_eq_u32 s58, 15
s_cbranch_scc1 LSK3_%=
s_memrealtime s[98:99]
s_andn2_b32 s91, s17, s58
s_mov_b32 s77, 0x7fffffff
s_waitcnt lgkmcnt(0)
s_sub_u32 s76, s98, s54
s_bitcmp0_b32 s91, 1
s_cbranch_scc1 LSM31_%=
s_sub_u32 s90, s13, s76
s_sub_u32 s90, s90, 12
s_cmp_gt_i32 s90, 0
s_cbranch_scc1 LSM31_%=
buffer_load_dwordx4 v[80:83], v248, s[20:23], s36 offen offset:0 sc1
buffer_load_dwordx4 v[84:87], v248, s[20:23], s36 offen offset:1024 sc1
s_bitset1_b32 s58, 1
LSM31_%=:
s_bitcmp0_b32 s91, 3
s_cbranch_scc1 LSM33_%=
s_sub_u32 s90, s15, s76
s_sub_u32 s90, s90, 63
s_cmp_gt_i32 s90, 0
s_cbranch_scc1 LSM33_%=
buffer_load_dwordx4 v[112:115], v248, s[20:23], s38 offen offset:0 sc1
buffer_load_dwordx4 v[116:119], v248, s[20:23], s38 offen offset:1024 sc1
s_bitset1_b32 s58, 3
LSM33_%=:
s_bitcmp0_b32 s91, 2
s_cbranch_scc1 LSM32_%=
s_sub_u32 s90, s14, s76
s_sub_u32 s90, s90, 97
s_cmp_gt_i32 s90, 0
s_cbranch_scc1 LSM32_%=
buffer_load_dwordx4 v[88:91], v248, s[20:23], s37 offen offset:0 sc1
buffer_load_dwordx4 v[92:95], v248, s[20:23], s37 offen offset:1024 sc1
s_bitset1_b32 s58, 2
LSM32_%=:
s_bitcmp0_b32 s91, 0
s_cbranch_scc1 LSM30_%=
s_sub_u32 s90, s12, s76
s_sub_u32 s90, s90, 131
s_cmp_gt_i32 s90, 0
s_cbranch_scc1 LSM30_%=
buffer_load_dwordx4 v[120:123], v250, s[20:23], s39 offen offset:0 sc1
buffer_load_dwordx4 v[124:127], v250, s[20:23], s39 offen offset:256 sc1
s_bitset1_b32 s58, 0
LSM30_%=:
LSK3_%=:
v_mul_f32 v136, v176, v136
v_mul_f32 v137, v177, v137
v_mul_f32 v138, v178, v138
v_mul_f32 v139, v179, v139
v_mul_f32 v140, v180, v140
v_mul_f32 v141, v181, v141
v_mul_f32 v142, v182, v142
v_mul_f32 v143, v183, v143
CV v20,v136,v137
CV v21,v138,v139
CV v22,v140,v141
CV v23,v142,v143
v_lshlrev_b32 v176, 16, v20
v_and_b32 v177, 0xffff0000, v20
v_lshlrev_b32 v178, 16, v21
v_and_b32 v179, 0xffff0000, v21
v_lshlrev_b32 v180, 16, v22
v_and_b32 v181, 0xffff0000, v22
v_lshlrev_b32 v182, 16, v23
v_and_b32 v183, 0xffff0000, v23
v_lshlrev_b32 v192, 16, v24
v_and_b32 v193, 0xffff0000, v24
v_lshlrev_b32 v194, 16, v25
v_and_b32 v195, 0xffff0000, v25
v_lshlrev_b32 v196, 16, v26
v_and_b32 v197, 0xffff0000, v26
v_lshlrev_b32 v198, 16, v27
v_and_b32 v199, 0xffff0000, v27
v_lshlrev_b32 v208, 16, v28
v_and_b32 v209, 0xffff0000, v28
v_lshlrev_b32 v210, 16, v29
v_and_b32 v211, 0xffff0000, v29
v_lshlrev_b32 v212, 16, v30
v_and_b32 v213, 0xffff0000, v30
v_lshlrev_b32 v214, 16, v31
v_and_b32 v215, 0xffff0000, v31
s_mov_b32 s73, 30000
s_cmp_lg_u32 s58, 15
s_cbranch_scc1 LSI_%=
s_waitcnt vmcnt(0)
v_max3_u32 v236, v123, v127, v83
v_max3_u32 v237, v87, v91, v95
v_max3_u32 v236, v236, v115, v119
v_max_u32 v236, v236, v237
v_cmp_eq_u32 vcc, -1, v236
s_cbranch_vccnz LSI_%=
LSF_%=:
s_waitcnt lgkmcnt(0)
MM v[152:155],v[48:51],v[120:123],0
MM v[152:155],v[52:55],v[124:127],v[152:155]
MM v[0:3],v[58:61],v[88:91],v[0:3]
v_lshlrev_b32 v128, 16, v150
v_and_b32 v129, 0xffff0000, v150
MM v[0:3],v[58:61],v[112:115],v[0:3]
v_lshlrev_b32 v130, 16, v151
v_and_b32 v131, 0xffff0000, v151
MM v[0:3],v[58:61],v[80:83],v[0:3]
v_lshlrev_b32 v132, 16, v62
v_and_b32 v133, 0xffff0000, v62
MM v[4:7],v[56:59],v[88:91],v[4:7]
v_lshlrev_b32 v134, 16, v63
v_and_b32 v135, 0xffff0000, v63
MM v[4:7],v[56:59],v[112:115],v[4:7]
CV v236,v152,v153
CV v237,v154,v155
MM v[4:7],v[56:59],v[80:83],v[4:7]
ds_bpermute_b32 v236, v232, v236
ds_bpermute_b32 v237, v232, v237
MM v[8:11],v[58:61],v[92:95],v[8:11]
MM v[8:11],v[58:61],v[116:119],v[8:11]
MM v[8:11],v[58:61],v[84:87],v[8:11]
s_add_u32 s80, s18, 20480
MM v[12:15],v[56:59],v[92:95],v[12:15]
MM v[12:15],v[56:59],v[116:119],v[12:15]
CV v32,v0,v1
CV v33,v2,v3
MM v[12:15],v[56:59],v[84:87],v[12:15]
CV v34,v4,v5
CV v35,v6,v7
ds_write2_b64 v224, v[32:33], v[34:35] offset0:0 offset1:4
RD v[40:43],v225 offset:0
CV v36,v8,v9
CV v37,v10,v11
s_nop 1
CV v38,v12,v13
CV v39,v14,v15
ds_write2_b64 v224, v[36:37], v[38:39] offset0:160 offset1:164
RD v[44:47],v225 offset:1280
s_waitcnt lgkmcnt(4)
v_lshlrev_b32 v152, 16, v236
v_and_b32 v153, 0xffff0000, v236
v_lshlrev_b32 v154, 16, v237
v_and_b32 v155, 0xffff0000, v237
v_pk_mul_f32 v[152:153], v[152:153], v[128:129]
v_pk_mul_f32 v[154:155], v[154:155], v[130:131]
CV v236,v152,v153
CV v237,v154,v155
v_lshlrev_b32 v152, 16, v236
v_and_b32 v153, 0xffff0000, v236
v_lshlrev_b32 v154, 16, v237
v_and_b32 v155, 0xffff0000, v237
v_pk_add_f32 v[152:153], v[152:153], v[132:133]
v_pk_add_f32 v[154:155], v[154:155], v[134:135]
CV v238,v152,v153
CV v239,v154,v155
buffer_store_dwordx2 v[238:239], v254, s[40:43], s63 offen offset:0 nt
s_nop 0
s_waitcnt lgkmcnt(2)
v_lshlrev_b32 v0, 16, v40
v_and_b32 v1, 0xffff0000, v40
v_lshlrev_b32 v2, 16, v41
LPG30_%=:
s_cbranch_execz LTR1_%=
v_and_b32 v3, 0xffff0000, v41
v_lshlrev_b32 v4, 16, v42
v_and_b32 v5, 0xffff0000, v42
v_lshlrev_b32 v6, 16, v43
v_and_b32 v7, 0xffff0000, v43
)ASM"
R"ASM(
v_pk_mul_f32 v[0:1], v[0:1], v[160:161]
v_pk_mul_f32 v[2:3], v[2:3], v[162:163]
v_pk_mul_f32 v[4:5], v[4:5], v[164:165]
v_pk_mul_f32 v[6:7], v[6:7], v[166:167]
CV v40,v0,v1
CV v41,v2,v3
CV v42,v4,v5
CV v43,v6,v7
v_lshlrev_b32 v0, 16, v40
v_and_b32 v1, 0xffff0000, v40
v_lshlrev_b32 v2, 16, v41
v_and_b32 v3, 0xffff0000, v41
v_lshlrev_b32 v4, 16, v42
v_and_b32 v5, 0xffff0000, v42
v_lshlrev_b32 v6, 16, v43
v_and_b32 v7, 0xffff0000, v43
v_pk_add_f32 v[0:1], v[0:1], v[192:193]
v_pk_add_f32 v[2:3], v[2:3], v[194:195]
v_pk_add_f32 v[4:5], v[4:5], v[196:197]
v_pk_add_f32 v[6:7], v[6:7], v[198:199]
CV v40,v0,v1
CV v41,v2,v3
CV v42,v4,v5
CV v43,v6,v7
buffer_store_dwordx4 v[40:43], v251, s[40:43], s18 offen offset:0 nt
s_waitcnt lgkmcnt(0)
v_lshlrev_b32 v8, 16, v44
v_and_b32 v9, 0xffff0000, v44
v_lshlrev_b32 v10, 16, v45
v_and_b32 v11, 0xffff0000, v45
v_lshlrev_b32 v12, 16, v46
v_and_b32 v13, 0xffff0000, v46
v_lshlrev_b32 v14, 16, v47
v_and_b32 v15, 0xffff0000, v47
v_pk_mul_f32 v[8:9], v[8:9], v[176:177]
v_pk_mul_f32 v[10:11], v[10:11], v[178:179]
v_pk_mul_f32 v[12:13], v[12:13], v[180:181]
v_pk_mul_f32 v[14:15], v[14:15], v[182:183]
CV v44,v8,v9
CV v45,v10,v11
CV v46,v12,v13
CV v47,v14,v15
v_lshlrev_b32 v8, 16, v44
v_and_b32 v9, 0xffff0000, v44
v_lshlrev_b32 v10, 16, v45
v_and_b32 v11, 0xffff0000, v45
v_lshlrev_b32 v12, 16, v46
v_and_b32 v13, 0xffff0000, v46
v_lshlrev_b32 v14, 16, v47
v_and_b32 v15, 0xffff0000, v47
v_pk_add_f32 v[8:9], v[8:9], v[208:209]
v_pk_add_f32 v[10:11], v[10:11], v[210:211]
v_pk_add_f32 v[12:13], v[12:13], v[212:213]
v_pk_add_f32 v[14:15], v[14:15], v[214:215]
CV v44,v8,v9
CV v45,v10,v11
CV v46,v12,v13
CV v47,v14,v15
buffer_store_dwordx4 v[44:47], v251, s[40:43], s80 offen offset:0 nt
s_waitcnt vmcnt(0)
s_endpgm
LSI_%=:
s_memrealtime s[98:99]
s_andn2_b32 s91, s17, s58
s_mov_b32 s77, 0x7fffffff
s_waitcnt lgkmcnt(0)
s_sub_u32 s76, s98, s54
s_bitcmp0_b32 s91, 1
s_cbranch_scc1 LSM1_%=
s_sub_u32 s90, s13, s76
s_sub_u32 s90, s90, 12
s_cmp_gt_i32 s90, 0
s_cbranch_scc1 LSN1_%=
buffer_load_dwordx4 v[80:83], v248, s[20:23], s36 offen offset:0 sc1
buffer_load_dwordx4 v[84:87], v248, s[20:23], s36 offen offset:1024 sc1
s_bitset1_b32 s58, 1
s_branch LSM1_%=
LSN1_%=:
s_min_i32 s77, s77, s90
LSM1_%=:
s_bitcmp0_b32 s91, 3
s_cbranch_scc1 LSM3_%=
s_sub_u32 s90, s15, s76
s_sub_u32 s90, s90, 63
s_cmp_gt_i32 s90, 0
s_cbranch_scc1 LSN3_%=
buffer_load_dwordx4 v[112:115], v248, s[20:23], s38 offen offset:0 sc1
buffer_load_dwordx4 v[116:119], v248, s[20:23], s38 offen offset:1024 sc1
s_bitset1_b32 s58, 3
s_branch LSM3_%=
LSN3_%=:
s_min_i32 s77, s77, s90
LSM3_%=:
s_bitcmp0_b32 s91, 2
s_cbranch_scc1 LSM2_%=
s_sub_u32 s90, s14, s76
s_sub_u32 s90, s90, 97
s_cmp_gt_i32 s90, 0
s_cbranch_scc1 LSN2_%=
buffer_load_dwordx4 v[88:91], v248, s[20:23], s37 offen offset:0 sc1
buffer_load_dwordx4 v[92:95], v248, s[20:23], s37 offen offset:1024 sc1
s_bitset1_b32 s58, 2
s_branch LSM2_%=
LSN2_%=:
s_min_i32 s77, s77, s90
LSM2_%=:
s_bitcmp0_b32 s91, 0
s_cbranch_scc1 LSM0_%=
s_sub_u32 s90, s12, s76
s_sub_u32 s90, s90, 131
s_cmp_gt_i32 s90, 0
s_cbranch_scc1 LSN0_%=
buffer_load_dwordx4 v[120:123], v250, s[20:23], s39 offen offset:0 sc1
buffer_load_dwordx4 v[124:127], v250, s[20:23], s39 offen offset:256 sc1
s_bitset1_b32 s58, 0
s_branch LSM0_%=
LSN0_%=:
s_min_i32 s77, s77, s90
LSM0_%=:
s_cmp_lt_i32 s77, 100
s_cbranch_scc1 LSI_%=
buffer_load_dwordx2 v[148:149], v253, s[20:23], s68 offen offset:8 sc1
s_waitcnt vmcnt(1)
s_bitcmp0_b32 s58, 1
s_cbranch_scc1 LSB1_%=
v_max_u32 v237, v83, v87
v_cmp_eq_u32 vcc, -1, v237
s_cbranch_vccnz LSB1_%=
s_bitset0_b32 s17, 1
LSB1_%=:
s_bitcmp0_b32 s58, 3
s_cbranch_scc1 LSB3_%=
v_max_u32 v239, v115, v119
v_cmp_eq_u32 vcc, -1, v239
s_cbranch_vccnz LSB3_%=
s_bitset0_b32 s17, 3
LSB3_%=:
s_bitcmp0_b32 s58, 2
s_cbranch_scc1 LSB2_%=
v_max_u32 v238, v91, v95
v_cmp_eq_u32 vcc, -1, v238
s_cbranch_vccnz LSB2_%=
s_bitset0_b32 s17, 2
LSB2_%=:
s_bitcmp0_b32 s58, 0
s_cbranch_scc1 LSB0_%=
v_max_u32 v236, v123, v127
v_cmp_eq_u32 vcc, -1, v236
s_cbranch_vccnz LSB0_%=
s_bitset0_b32 s17, 0
LSB0_%=:
s_cmp_eq_u32 s17, 0
s_cbranch_scc1 LSF_%=
s_mov_b32 s58, 0
s_waitcnt vmcnt(0)
v_cmp_eq_u32 s[8:9], s69, v149
v_subrev_u32 v226, s56, v148
s_nop 0
v_cndmask_b32 v226, 4.0, v226, s[8:9]
s_nop 0
v_readlane_b32 s13, v226, 16
v_readlane_b32 s14, v226, 32
v_readlane_b32 s15, v226, 48
s_max_i32 s12, s13, s14
s_max_i32 s12, s12, s15
s_max_i32 s12, s12, 0
s_sub_u32 s73, s73, 1
s_cbranch_scc0 LSI_%=
LSE_%=:
s_waitcnt vmcnt(0)
s_endpgm
LRG1o_%=:
s_mov_b32 s70, 30000
LRG1ol_%=:
s_sleep 2
buffer_load_dwordx4 v[200:203], v208, s[20:23], s62 offen offset:0 sc1
buffer_load_dwordx4 v[204:207], v208, s[20:23], s93 offen offset:0 sc1
s_waitcnt vmcnt(0)
v_xor_b32 v201, s69, v201
v_xor_b32 v203, s69, v203
v_xor_b32 v205, s69, v205
v_xor_b32 v207, s69, v207
v_or3_b32 v209, v201, v203, v205
v_or_b32 v209, v209, v207
v_cmp_ne_u32 vcc, 0, v209
s_cbranch_vccz LRG1ob_%=
s_sub_u32 s70, s70, 1
s_cbranch_scc0 LRG1ol_%=
s_branch LRG1ob_%=
LRFR_%=:
s_mov_b32 s91, 30000
LRFRl_%=:
s_sleep 2
buffer_load_dwordx4 v[48:51], v248, s[20:23], s90 offen offset:0 sc1
s_waitcnt vmcnt(0)
v_xor_b32 v48, s69, v48
v_xor_b32 v49, s69, v49
v_xor_b32 v50, s69, v50
v_xor_b32 v51, s69, v51
v_or3_b32 v252, v48, v49, v50
v_or_b32 v252, v252, v51
v_cmp_ne_u32 vcc, 0, v252
s_cbranch_vccz LRFRb_%=
s_sub_u32 s91, s91, 1
s_cbranch_scc0 LRFRl_%=
s_branch LRFRb_%=
LRRK_%=:
s_mov_b32 s91, 30000
LRRKl_%=:
s_sleep 2
buffer_load_dwordx2 v[32:33], v34, s[20:23], s90 offen offset:0 sc1
s_waitcnt vmcnt(0)
v_cmp_ne_u32 vcc, s69, v33
s_cbranch_vccz LRRKb_%=
s_sub_u32 s91, s91, 1
s_cbranch_scc0 LRRKl_%=
s_branch LRRKb_%=
LRK_%=:
s_cmp_ge_u32 s60, 8
s_cbranch_scc1 LRK0_%=
v_mov_b32 v253, 0
v_cmp_lt_u32 vcc, s52, v96
v_cmp_lt_u32 s[98:99], s52, v97
s_nop 0
v_addc_co_u32 v253, vcc, 0, v253, vcc
v_addc_co_u32 v253, s[98:99], 0, v253, s[98:99]
v_cmp_lt_u32 vcc, s52, v98
v_cmp_lt_u32 s[98:99], s52, v99
s_nop 0
v_addc_co_u32 v253, vcc, 0, v253, vcc
v_addc_co_u32 v253, s[98:99], 0, v253, s[98:99]
v_cmp_lt_u32 vcc, s52, v100
v_cmp_lt_u32 s[98:99], s52, v101
s_nop 0
v_addc_co_u32 v253, vcc, 0, v253, vcc
v_addc_co_u32 v253, s[98:99], 0, v253, s[98:99]
v_cmp_lt_u32 vcc, s52, v102
v_cmp_lt_u32 s[98:99], s52, v103
s_nop 0
v_addc_co_u32 v253, vcc, 0, v253, vcc
v_addc_co_u32 v253, s[98:99], 0, v253, s[98:99]
v_cmp_lt_u32 vcc, s52, v104
v_cmp_lt_u32 s[98:99], s52, v105
s_nop 0
v_addc_co_u32 v253, vcc, 0, v253, vcc
v_addc_co_u32 v253, s[98:99], 0, v253, s[98:99]
v_cmp_lt_u32 vcc, s52, v106
v_cmp_lt_u32 s[98:99], s52, v107
s_nop 0
v_addc_co_u32 v253, vcc, 0, v253, vcc
v_addc_co_u32 v253, s[98:99], 0, v253, s[98:99]
v_cmp_lt_u32 vcc, s52, v108
v_cmp_lt_u32 s[98:99], s52, v109
s_nop 0
v_addc_co_u32 v253, vcc, 0, v253, vcc
v_addc_co_u32 v253, s[98:99], 0, v253, s[98:99]
v_cmp_lt_u32 vcc, s52, v110
v_cmp_lt_u32 s[98:99], s52, v111
s_nop 0
v_addc_co_u32 v253, vcc, 0, v253, vcc
v_addc_co_u32 v253, s[98:99], 0, v253, s[98:99]
v_cmp_lt_u32 vcc, s52, v112
v_cmp_lt_u32 s[98:99], s52, v113
s_nop 0
v_addc_co_u32 v253, vcc, 0, v253, vcc
v_addc_co_u32 v253, s[98:99], 0, v253, s[98:99]
v_cmp_lt_u32 vcc, s52, v114
v_cmp_lt_u32 s[98:99], s52, v115
s_nop 0
v_addc_co_u32 v253, vcc, 0, v253, vcc
v_addc_co_u32 v253, s[98:99], 0, v253, s[98:99]
v_cmp_lt_u32 vcc, s52, v116
v_cmp_lt_u32 s[98:99], s52, v117
s_nop 0
v_addc_co_u32 v253, vcc, 0, v253, vcc
v_addc_co_u32 v253, s[98:99], 0, v253, s[98:99]
v_cmp_lt_u32 vcc, s52, v118
v_cmp_lt_u32 s[98:99], s52, v119
s_nop 0
v_addc_co_u32 v253, vcc, 0, v253, vcc
v_addc_co_u32 v253, s[98:99], 0, v253, s[98:99]
v_cmp_lt_u32 vcc, s52, v120
v_cmp_lt_u32 s[98:99], s52, v121
s_nop 0
v_addc_co_u32 v253, vcc, 0, v253, vcc
v_addc_co_u32 v253, s[98:99], 0, v253, s[98:99]
v_cmp_lt_u32 vcc, s52, v122
v_cmp_lt_u32 s[98:99], s52, v123
s_nop 0
v_addc_co_u32 v253, vcc, 0, v253, vcc
v_addc_co_u32 v253, s[98:99], 0, v253, s[98:99]
v_cmp_lt_u32 vcc, s52, v124
v_cmp_lt_u32 s[98:99], s52, v125
s_nop 0
v_addc_co_u32 v253, vcc, 0, v253, vcc
v_addc_co_u32 v253, s[98:99], 0, v253, s[98:99]
v_cmp_lt_u32 vcc, s52, v126
v_cmp_lt_u32 s[98:99], s52, v127
s_nop 0
v_addc_co_u32 v253, vcc, 0, v253, vcc
v_addc_co_u32 v253, s[98:99], 0, v253, s[98:99]
s_nop 1
v_add_u32_dpp v253, v253, v253 row_ror:8 row_mask:0xf bank_mask:0xf
s_nop 1
v_add_u32_dpp v253, v253, v253 row_ror:4 row_mask:0xf bank_mask:0xf
s_nop 1
v_add_u32_dpp v253, v253, v253 row_ror:2 row_mask:0xf bank_mask:0xf
s_nop 1
v_add_u32_dpp v253, v253, v253 row_ror:1 row_mask:0xf bank_mask:0xf
v_mov_b32 v250, v253
s_nop 1
v_permlane16_swap_b32 v253, v250
v_add_u32 v253, v253, v250
v_mov_b32 v250, v253
s_nop 1
v_permlane32_swap_b32 v253, v250
v_add_u32 v253, v253, v250
v_cmp_gt_u32 vcc, 8, v253
v_mov_b32 v250, s52
v_mov_b32 v251, s69
v_lshlrev_b32 v253, 3, v253
s_mov_b32 s76, 94208
s_cbranch_vccz LRK0_%=
s_mov_b64 exec, 1
buffer_store_dwordx2 v[250:251], v253, s[20:23], s76 offen offset:0 sc1
s_mov_b64 exec, -1
s_nop 1
LRK0_%=:
s_cmp_ge_u32 s62, 8
s_cbranch_scc1 LRK1_%=
v_mov_b32 v253, 0
v_cmp_lt_u32 vcc, s53, v96
v_cmp_lt_u32 s[98:99], s53, v97
s_nop 0
v_addc_co_u32 v253, vcc, 0, v253, vcc
v_addc_co_u32 v253, s[98:99], 0, v253, s[98:99]
v_cmp_lt_u32 vcc, s53, v98
v_cmp_lt_u32 s[98:99], s53, v99
s_nop 0
v_addc_co_u32 v253, vcc, 0, v253, vcc
v_addc_co_u32 v253, s[98:99], 0, v253, s[98:99]
v_cmp_lt_u32 vcc, s53, v100
v_cmp_lt_u32 s[98:99], s53, v101
s_nop 0
v_addc_co_u32 v253, vcc, 0, v253, vcc
v_addc_co_u32 v253, s[98:99], 0, v253, s[98:99]
v_cmp_lt_u32 vcc, s53, v102
v_cmp_lt_u32 s[98:99], s53, v103
s_nop 0
v_addc_co_u32 v253, vcc, 0, v253, vcc
v_addc_co_u32 v253, s[98:99], 0, v253, s[98:99]
v_cmp_lt_u32 vcc, s53, v104
v_cmp_lt_u32 s[98:99], s53, v105
s_nop 0
v_addc_co_u32 v253, vcc, 0, v253, vcc
v_addc_co_u32 v253, s[98:99], 0, v253, s[98:99]
v_cmp_lt_u32 vcc, s53, v106
v_cmp_lt_u32 s[98:99], s53, v107
s_nop 0
v_addc_co_u32 v253, vcc, 0, v253, vcc
v_addc_co_u32 v253, s[98:99], 0, v253, s[98:99]
v_cmp_lt_u32 vcc, s53, v108
v_cmp_lt_u32 s[98:99], s53, v109
s_nop 0
v_addc_co_u32 v253, vcc, 0, v253, vcc
v_addc_co_u32 v253, s[98:99], 0, v253, s[98:99]
v_cmp_lt_u32 vcc, s53, v110
v_cmp_lt_u32 s[98:99], s53, v111
s_nop 0
v_addc_co_u32 v253, vcc, 0, v253, vcc
v_addc_co_u32 v253, s[98:99], 0, v253, s[98:99]
v_cmp_lt_u32 vcc, s53, v112
v_cmp_lt_u32 s[98:99], s53, v113
s_nop 0
v_addc_co_u32 v253, vcc, 0, v253, vcc
v_addc_co_u32 v253, s[98:99], 0, v253, s[98:99]
v_cmp_lt_u32 vcc, s53, v114
v_cmp_lt_u32 s[98:99], s53, v115
s_nop 0
v_addc_co_u32 v253, vcc, 0, v253, vcc
v_addc_co_u32 v253, s[98:99], 0, v253, s[98:99]
v_cmp_lt_u32 vcc, s53, v116
v_cmp_lt_u32 s[98:99], s53, v117
s_nop 0
v_addc_co_u32 v253, vcc, 0, v253, vcc
v_addc_co_u32 v253, s[98:99], 0, v253, s[98:99]
v_cmp_lt_u32 vcc, s53, v118
v_cmp_lt_u32 s[98:99], s53, v119
s_nop 0
v_addc_co_u32 v253, vcc, 0, v253, vcc
v_addc_co_u32 v253, s[98:99], 0, v253, s[98:99]
v_cmp_lt_u32 vcc, s53, v120
v_cmp_lt_u32 s[98:99], s53, v121
s_nop 0
v_addc_co_u32 v253, vcc, 0, v253, vcc
v_addc_co_u32 v253, s[98:99], 0, v253, s[98:99]
v_cmp_lt_u32 vcc, s53, v122
v_cmp_lt_u32 s[98:99], s53, v123
s_nop 0
v_addc_co_u32 v253, vcc, 0, v253, vcc
v_addc_co_u32 v253, s[98:99], 0, v253, s[98:99]
v_cmp_lt_u32 vcc, s53, v124
v_cmp_lt_u32 s[98:99], s53, v125
s_nop 0
v_addc_co_u32 v253, vcc, 0, v253, vcc
v_addc_co_u32 v253, s[98:99], 0, v253, s[98:99]
v_cmp_lt_u32 vcc, s53, v126
v_cmp_lt_u32 s[98:99], s53, v127
s_nop 0
v_addc_co_u32 v253, vcc, 0, v253, vcc
v_addc_co_u32 v253, s[98:99], 0, v253, s[98:99]
s_nop 1
v_add_u32_dpp v253, v253, v253 row_ror:8 row_mask:0xf bank_mask:0xf
s_nop 1
v_add_u32_dpp v253, v253, v253 row_ror:4 row_mask:0xf bank_mask:0xf
s_nop 1
v_add_u32_dpp v253, v253, v253 row_ror:2 row_mask:0xf bank_mask:0xf
s_nop 1
v_add_u32_dpp v253, v253, v253 row_ror:1 row_mask:0xf bank_mask:0xf
v_mov_b32 v250, v253
s_nop 1
v_permlane16_swap_b32 v253, v250
v_add_u32 v253, v253, v250
v_mov_b32 v250, v253
s_nop 1
v_permlane32_swap_b32 v253, v250
v_add_u32 v253, v253, v250
v_cmp_gt_u32 vcc, 8, v253
v_mov_b32 v250, s53
v_mov_b32 v251, s69
)ASM"
R"ASM(
v_lshlrev_b32 v253, 3, v253
s_mov_b32 s76, 94208
s_cbranch_vccz LRK1_%=
s_mov_b64 exec, 1
buffer_store_dwordx2 v[250:251], v253, s[20:23], s76 offen offset:0 sc1
s_mov_b64 exec, -1
s_nop 1
LRK1_%=:
s_cmp_ge_u32 s56, 8
s_cbranch_scc1 LRK2_%=
v_mov_b32 v253, 0
v_cmp_lt_u32 vcc, s54, v96
v_cmp_lt_u32 s[98:99], s54, v97
s_nop 0
v_addc_co_u32 v253, vcc, 0, v253, vcc
v_addc_co_u32 v253, s[98:99], 0, v253, s[98:99]
v_cmp_lt_u32 vcc, s54, v98
v_cmp_lt_u32 s[98:99], s54, v99
s_nop 0
v_addc_co_u32 v253, vcc, 0, v253, vcc
v_addc_co_u32 v253, s[98:99], 0, v253, s[98:99]
v_cmp_lt_u32 vcc, s54, v100
v_cmp_lt_u32 s[98:99], s54, v101
s_nop 0
v_addc_co_u32 v253, vcc, 0, v253, vcc
v_addc_co_u32 v253, s[98:99], 0, v253, s[98:99]
v_cmp_lt_u32 vcc, s54, v102
v_cmp_lt_u32 s[98:99], s54, v103
s_nop 0
v_addc_co_u32 v253, vcc, 0, v253, vcc
v_addc_co_u32 v253, s[98:99], 0, v253, s[98:99]
v_cmp_lt_u32 vcc, s54, v104
v_cmp_lt_u32 s[98:99], s54, v105
s_nop 0
v_addc_co_u32 v253, vcc, 0, v253, vcc
v_addc_co_u32 v253, s[98:99], 0, v253, s[98:99]
v_cmp_lt_u32 vcc, s54, v106
v_cmp_lt_u32 s[98:99], s54, v107
s_nop 0
v_addc_co_u32 v253, vcc, 0, v253, vcc
v_addc_co_u32 v253, s[98:99], 0, v253, s[98:99]
v_cmp_lt_u32 vcc, s54, v108
v_cmp_lt_u32 s[98:99], s54, v109
s_nop 0
v_addc_co_u32 v253, vcc, 0, v253, vcc
v_addc_co_u32 v253, s[98:99], 0, v253, s[98:99]
v_cmp_lt_u32 vcc, s54, v110
v_cmp_lt_u32 s[98:99], s54, v111
s_nop 0
v_addc_co_u32 v253, vcc, 0, v253, vcc
v_addc_co_u32 v253, s[98:99], 0, v253, s[98:99]
v_cmp_lt_u32 vcc, s54, v112
v_cmp_lt_u32 s[98:99], s54, v113
s_nop 0
v_addc_co_u32 v253, vcc, 0, v253, vcc
v_addc_co_u32 v253, s[98:99], 0, v253, s[98:99]
v_cmp_lt_u32 vcc, s54, v114
v_cmp_lt_u32 s[98:99], s54, v115
s_nop 0
v_addc_co_u32 v253, vcc, 0, v253, vcc
v_addc_co_u32 v253, s[98:99], 0, v253, s[98:99]
v_cmp_lt_u32 vcc, s54, v116
v_cmp_lt_u32 s[98:99], s54, v117
s_nop 0
v_addc_co_u32 v253, vcc, 0, v253, vcc
v_addc_co_u32 v253, s[98:99], 0, v253, s[98:99]
v_cmp_lt_u32 vcc, s54, v118
v_cmp_lt_u32 s[98:99], s54, v119
s_nop 0
v_addc_co_u32 v253, vcc, 0, v253, vcc
v_addc_co_u32 v253, s[98:99], 0, v253, s[98:99]
v_cmp_lt_u32 vcc, s54, v120
v_cmp_lt_u32 s[98:99], s54, v121
s_nop 0
v_addc_co_u32 v253, vcc, 0, v253, vcc
v_addc_co_u32 v253, s[98:99], 0, v253, s[98:99]
v_cmp_lt_u32 vcc, s54, v122
v_cmp_lt_u32 s[98:99], s54, v123
s_nop 0
v_addc_co_u32 v253, vcc, 0, v253, vcc
v_addc_co_u32 v253, s[98:99], 0, v253, s[98:99]
v_cmp_lt_u32 vcc, s54, v124
v_cmp_lt_u32 s[98:99], s54, v125
s_nop 0
v_addc_co_u32 v253, vcc, 0, v253, vcc
v_addc_co_u32 v253, s[98:99], 0, v253, s[98:99]
v_cmp_lt_u32 vcc, s54, v126
v_cmp_lt_u32 s[98:99], s54, v127
s_nop 0
v_addc_co_u32 v253, vcc, 0, v253, vcc
v_addc_co_u32 v253, s[98:99], 0, v253, s[98:99]
s_nop 1
v_add_u32_dpp v253, v253, v253 row_ror:8 row_mask:0xf bank_mask:0xf
s_nop 1
v_add_u32_dpp v253, v253, v253 row_ror:4 row_mask:0xf bank_mask:0xf
s_nop 1
v_add_u32_dpp v253, v253, v253 row_ror:2 row_mask:0xf bank_mask:0xf
s_nop 1
v_add_u32_dpp v253, v253, v253 row_ror:1 row_mask:0xf bank_mask:0xf
v_mov_b32 v250, v253
s_nop 1
v_permlane16_swap_b32 v253, v250
v_add_u32 v253, v253, v250
v_mov_b32 v250, v253
s_nop 1
v_permlane32_swap_b32 v253, v250
v_add_u32 v253, v253, v250
v_cmp_gt_u32 vcc, 8, v253
v_mov_b32 v250, s54
v_mov_b32 v251, s69
v_lshlrev_b32 v253, 3, v253
s_mov_b32 s76, 94208
s_cbranch_vccz LRK2_%=
s_mov_b64 exec, 1
buffer_store_dwordx2 v[250:251], v253, s[20:23], s76 offen offset:0 sc1
s_mov_b64 exec, -1
s_nop 1
LRK2_%=:
s_cmp_ge_u32 s58, 8
s_cbranch_scc1 LRK3_%=
v_mov_b32 v253, 0
v_cmp_lt_u32 vcc, s55, v96
v_cmp_lt_u32 s[98:99], s55, v97
s_nop 0
v_addc_co_u32 v253, vcc, 0, v253, vcc
v_addc_co_u32 v253, s[98:99], 0, v253, s[98:99]
v_cmp_lt_u32 vcc, s55, v98
v_cmp_lt_u32 s[98:99], s55, v99
s_nop 0
v_addc_co_u32 v253, vcc, 0, v253, vcc
v_addc_co_u32 v253, s[98:99], 0, v253, s[98:99]
v_cmp_lt_u32 vcc, s55, v100
v_cmp_lt_u32 s[98:99], s55, v101
s_nop 0
v_addc_co_u32 v253, vcc, 0, v253, vcc
v_addc_co_u32 v253, s[98:99], 0, v253, s[98:99]
v_cmp_lt_u32 vcc, s55, v102
v_cmp_lt_u32 s[98:99], s55, v103
s_nop 0
v_addc_co_u32 v253, vcc, 0, v253, vcc
v_addc_co_u32 v253, s[98:99], 0, v253, s[98:99]
v_cmp_lt_u32 vcc, s55, v104
v_cmp_lt_u32 s[98:99], s55, v105
s_nop 0
v_addc_co_u32 v253, vcc, 0, v253, vcc
v_addc_co_u32 v253, s[98:99], 0, v253, s[98:99]
v_cmp_lt_u32 vcc, s55, v106
v_cmp_lt_u32 s[98:99], s55, v107
s_nop 0
v_addc_co_u32 v253, vcc, 0, v253, vcc
v_addc_co_u32 v253, s[98:99], 0, v253, s[98:99]
v_cmp_lt_u32 vcc, s55, v108
v_cmp_lt_u32 s[98:99], s55, v109
s_nop 0
v_addc_co_u32 v253, vcc, 0, v253, vcc
v_addc_co_u32 v253, s[98:99], 0, v253, s[98:99]
v_cmp_lt_u32 vcc, s55, v110
v_cmp_lt_u32 s[98:99], s55, v111
s_nop 0
v_addc_co_u32 v253, vcc, 0, v253, vcc
v_addc_co_u32 v253, s[98:99], 0, v253, s[98:99]
v_cmp_lt_u32 vcc, s55, v112
v_cmp_lt_u32 s[98:99], s55, v113
s_nop 0
v_addc_co_u32 v253, vcc, 0, v253, vcc
v_addc_co_u32 v253, s[98:99], 0, v253, s[98:99]
v_cmp_lt_u32 vcc, s55, v114
v_cmp_lt_u32 s[98:99], s55, v115
s_nop 0
v_addc_co_u32 v253, vcc, 0, v253, vcc
v_addc_co_u32 v253, s[98:99], 0, v253, s[98:99]
v_cmp_lt_u32 vcc, s55, v116
v_cmp_lt_u32 s[98:99], s55, v117
s_nop 0
v_addc_co_u32 v253, vcc, 0, v253, vcc
v_addc_co_u32 v253, s[98:99], 0, v253, s[98:99]
v_cmp_lt_u32 vcc, s55, v118
v_cmp_lt_u32 s[98:99], s55, v119
s_nop 0
v_addc_co_u32 v253, vcc, 0, v253, vcc
v_addc_co_u32 v253, s[98:99], 0, v253, s[98:99]
v_cmp_lt_u32 vcc, s55, v120
v_cmp_lt_u32 s[98:99], s55, v121
s_nop 0
v_addc_co_u32 v253, vcc, 0, v253, vcc
v_addc_co_u32 v253, s[98:99], 0, v253, s[98:99]
v_cmp_lt_u32 vcc, s55, v122
v_cmp_lt_u32 s[98:99], s55, v123
s_nop 0
v_addc_co_u32 v253, vcc, 0, v253, vcc
v_addc_co_u32 v253, s[98:99], 0, v253, s[98:99]
v_cmp_lt_u32 vcc, s55, v124
v_cmp_lt_u32 s[98:99], s55, v125
s_nop 0
v_addc_co_u32 v253, vcc, 0, v253, vcc
v_addc_co_u32 v253, s[98:99], 0, v253, s[98:99]
v_cmp_lt_u32 vcc, s55, v126
v_cmp_lt_u32 s[98:99], s55, v127
s_nop 0
v_addc_co_u32 v253, vcc, 0, v253, vcc
v_addc_co_u32 v253, s[98:99], 0, v253, s[98:99]
s_nop 1
v_add_u32_dpp v253, v253, v253 row_ror:8 row_mask:0xf bank_mask:0xf
s_nop 1
v_add_u32_dpp v253, v253, v253 row_ror:4 row_mask:0xf bank_mask:0xf
s_nop 1
v_add_u32_dpp v253, v253, v253 row_ror:2 row_mask:0xf bank_mask:0xf
s_nop 1
v_add_u32_dpp v253, v253, v253 row_ror:1 row_mask:0xf bank_mask:0xf
v_mov_b32 v250, v253
s_nop 1
v_permlane16_swap_b32 v253, v250
v_add_u32 v253, v253, v250
v_mov_b32 v250, v253
s_nop 1
v_permlane32_swap_b32 v253, v250
v_add_u32 v253, v253, v250
v_cmp_gt_u32 vcc, 8, v253
v_mov_b32 v250, s55
v_mov_b32 v251, s69
v_lshlrev_b32 v253, 3, v253
s_mov_b32 s76, 94208
s_cbranch_vccz LRK3_%=
s_mov_b64 exec, 1
buffer_store_dwordx2 v[250:251], v253, s[20:23], s76 offen offset:0 sc1
s_mov_b64 exec, -1
s_nop 1
LRK3_%=:
s_branch LRKb_%=
LRIX_%=:
s_mov_b32 s91, 30000
LRIXl_%=:
s_sleep 2
v_bfe_u32 v251, v248, 4, 3
v_lshlrev_b32 v251, 3, v251
buffer_load_dwordx2 v[252:253], v251, s[20:23], s86 offen offset:0 sc1
s_waitcnt vmcnt(0)
v_cmp_ne_u32 vcc, s69, v253
s_cbranch_vccz LRIXb_%=
s_sub_u32 s91, s91, 1
s_cbranch_scc0 LRIXl_%=
s_branch LRIXb_%=
LRFG_%=:
s_mov_b32 s91, 30000
LRFGl_%=:
s_sleep 2
buffer_load_dword v252, v253, s[20:23], s76 offen offset:0 sc1
s_waitcnt vmcnt(0)
v_cmp_ne_u32 vcc, s69, v252
s_cbranch_vccz LRFGb_%=
s_sub_u32 s91, s91, 1
s_cbranch_scc0 LRFGl_%=
s_branch LRFGb_%=
LRFH_%=:
s_mov_b32 s91, 30000
LRFHl_%=:
s_sleep 2
buffer_load_dword v252, v253, s[20:23], s76 offen offset:0 sc1
s_waitcnt vmcnt(0)
v_cmp_ne_u32 vcc, s69, v252
s_cbranch_vccz LRFHb_%=
s_sub_u32 s91, s91, 1
s_cbranch_scc0 LRFHl_%=
s_branch LRFHb_%=
LRFY_%=:
s_mov_b32 s91, 30000
LRFYl_%=:
s_sleep 2
buffer_load_dword v252, v253, s[20:23], s76 offen offset:0 sc1
s_waitcnt vmcnt(0)
v_cmp_ne_u32 vcc, s69, v252
s_cbranch_vccz LRFYb_%=
s_sub_u32 s91, s91, 1
s_cbranch_scc0 LRFYl_%=
s_branch LRFYb_%=
LRPO0_%=:
s_mov_b32 s97, 30000
LRPO0l_%=:
s_sleep 2
buffer_load_dword v242, v243, s[20:23], s44 offen offset:0 sc1
s_waitcnt vmcnt(0)
v_cmp_ne_u32 vcc, s69, v242
s_cbranch_vccz LRPO0b_%=
s_sub_u32 s97, s97, 1
s_cbranch_scc0 LRPO0l_%=
s_branch LRPO0b_%=
LRPW_%=:
s_mov_b32 s61, 30000
LRPWl_%=:
s_sleep 2
buffer_load_dwordx2 v[96:97], v98, s[20:23], s62 offen offset:0 sc1
s_waitcnt vmcnt(0)
v_xor_b32 v96, s69, v96
v_xor_b32 v97, s69, v97
v_or_b32 v99, v96, v97
v_cmp_ne_u32 vcc, 0, v99
s_cbranch_vccz LRPWb_%=
s_sub_u32 s61, s61, 1
s_cbranch_scc0 LRPWl_%=
s_branch LRPWb_%=
LRPO1_%=:
s_mov_b32 s97, 30000
LRPO1l_%=:
s_sleep 2
buffer_load_dword v242, v243, s[20:23], s96 offen offset:16 sc1
s_waitcnt vmcnt(0)
v_cmp_ne_u32 vcc, s69, v242
s_cbranch_vccz LRPO1b_%=
s_sub_u32 s97, s97, 1
s_cbranch_scc0 LRPO1l_%=
s_branch LRPO1b_%=
LKEND_ka:
LRET_%=:
s_cmp_eq_u32 s73, 0
s_cbranch_scc1 LBACK0_%=
s_cmp_eq_u32 s73, 2
s_cbranch_scc1 LBACK2_%=
s_cmp_eq_u32 s73, 1
s_cbranch_scc1 LBACK1_%=
s_cmp_eq_u32 s73, 3
s_cbranch_scc1 LBACK3_%=
s_cmp_eq_u32 s73, 5
s_cbranch_scc1 LBACK5_%=
s_cmp_eq_u32 s73, 7
s_cbranch_scc1 LBACK7_%=
s_cmp_eq_u32 s73, 9
s_cbranch_scc1 LBACK9_%=
s_cmp_eq_u32 s73, 11
s_cbranch_scc1 LBACK11_%=
s_cmp_eq_u32 s73, 13
s_cbranch_scc1 LBACK13_%=
s_cmp_eq_u32 s73, 15
s_cbranch_scc1 LBACK15_%=
s_cmp_eq_u32 s73, 17
s_cbranch_scc1 LBACK17_%=
s_cmp_eq_u32 s73, 19
s_cbranch_scc1 LBACK19_%=
s_cmp_eq_u32 s73, 21
s_cbranch_scc1 LBACK21_%=
s_cmp_eq_u32 s73, 23
s_cbranch_scc1 LBACK23_%=
s_cmp_eq_u32 s73, 25
s_cbranch_scc1 LBACK25_%=
s_cmp_eq_u32 s73, 27
s_cbranch_scc1 LBACK27_%=
s_cmp_eq_u32 s73, 29
s_cbranch_scc1 LBACK29_%=
s_cmp_eq_u32 s73, 31
s_cbranch_scc1 LBACK31_%=
s_cmp_eq_u32 s73, 33
s_cbranch_scc1 LBACK33_%=
s_cmp_eq_u32 s73, 35
s_cbranch_scc1 LBACK35_%=
s_cmp_eq_u32 s73, 4
s_cbranch_scc1 LBACK4_%=
s_cmp_eq_u32 s73, 6
s_cbranch_scc1 LBACK6_%=
s_cmp_eq_u32 s73, 8
s_cbranch_scc1 LBACK8_%=
s_cmp_eq_u32 s73, 10
s_cbranch_scc1 LBACK10_%=
s_cmp_eq_u32 s73, 12
s_cbranch_scc1 LBACK12_%=
s_cmp_eq_u32 s73, 14
s_cbranch_scc1 LBACK14_%=
s_cmp_eq_u32 s73, 16
s_cbranch_scc1 LBACK16_%=
s_cmp_eq_u32 s73, 18
s_cbranch_scc1 LBACK18_%=
s_cmp_eq_u32 s73, 20
s_cbranch_scc1 LBACK20_%=
s_cmp_eq_u32 s73, 22
s_cbranch_scc1 LBACK22_%=
s_cmp_eq_u32 s73, 24
s_cbranch_scc1 LBACK24_%=
s_cmp_eq_u32 s73, 26
s_cbranch_scc1 LBACK26_%=
s_cmp_eq_u32 s73, 28
s_cbranch_scc1 LBACK28_%=
s_cmp_eq_u32 s73, 30
s_cbranch_scc1 LBACK30_%=
s_cmp_eq_u32 s73, 32
s_cbranch_scc1 LBACK32_%=
s_cmp_eq_u32 s73, 34
s_cbranch_scc1 LBACK34_%=
s_cmp_eq_u32 s73, 36
s_cbranch_scc1 LBACK36_%=
s_cmp_eq_u32 s73, 38
s_cbranch_scc1 LBACK38_%=
s_cmp_eq_u32 s73, 37
s_cbranch_scc1 LBACK37_%=
s_endpgm
LSITE0_%=:
s_mov_b32 s73, 0
s_mov_b32 s74, 0
s_mov_b32 s75, 16384
s_branch LRARE002_%=
LSITE2_%=:
s_mov_b32 s73, 2
s_mov_b32 s74, 32768
s_mov_b32 s75, 49152
s_branch LRARE002_%=
LSITE1_%=:
s_mov_b32 s73, 1
s_mov_b32 s74, 16384
s_mov_b32 s75, 32768
s_branch LRARE102_%=
LSITE3_%=:
s_mov_b32 s73, 3
s_mov_b32 s74, 49152
s_mov_b32 s75, 0
s_branch LRARE104_%=
LSITE5_%=:
s_mov_b32 s73, 5
s_mov_b32 s74, 16384
s_mov_b32 s75, 32768
s_branch LRARE104_%=
LSITE7_%=:
s_mov_b32 s73, 7
s_mov_b32 s74, 49152
s_mov_b32 s75, 0
s_branch LRARE104_%=
LSITE9_%=:
s_mov_b32 s73, 9
s_mov_b32 s74, 16384
s_mov_b32 s75, 32768
s_branch LRARE104_%=
LSITE11_%=:
s_mov_b32 s73, 11
s_mov_b32 s74, 49152
s_mov_b32 s75, 0
s_branch LRARE104_%=
LSITE13_%=:
s_mov_b32 s73, 13
s_mov_b32 s74, 16384
s_mov_b32 s75, 32768
s_branch LRARE104_%=
LSITE15_%=:
s_mov_b32 s73, 15
s_mov_b32 s74, 49152
s_mov_b32 s75, 0
s_branch LRARE104_%=
LSITE17_%=:
s_mov_b32 s73, 17
s_mov_b32 s74, 16384
s_mov_b32 s75, 32768
s_branch LRARE104_%=
LSITE19_%=:
s_mov_b32 s73, 19
s_mov_b32 s74, 49152
s_mov_b32 s75, 0
s_branch LRARE104_%=
LSITE21_%=:
s_mov_b32 s73, 21
s_mov_b32 s74, 16384
s_mov_b32 s75, 32768
s_branch LRARE104_%=
LSITE23_%=:
s_mov_b32 s73, 23
s_mov_b32 s74, 49152
s_mov_b32 s75, 0
s_branch LRARE104_%=
LSITE25_%=:
s_mov_b32 s73, 25
s_mov_b32 s74, 16384
s_mov_b32 s75, 32768
s_branch LRARE104_%=
LSITE27_%=:
s_mov_b32 s73, 27
s_mov_b32 s74, 49152
s_mov_b32 s75, 0
s_branch LRARE104_%=
LSITE29_%=:
s_mov_b32 s73, 29
s_mov_b32 s74, 16384
s_mov_b32 s75, 32768
)ASM"
R"ASM(
s_branch LRARE104_%=
LSITE31_%=:
s_mov_b32 s73, 31
s_mov_b32 s74, 49152
s_mov_b32 s75, 0
s_branch LRARE104_%=
LSITE33_%=:
s_mov_b32 s73, 33
s_mov_b32 s74, 16384
s_mov_b32 s75, 32768
s_branch LRARE104_%=
LSITE35_%=:
s_mov_b32 s73, 35
s_mov_b32 s74, 49152
s_mov_b32 s75, 0
s_branch LRARE104_%=
LSITE4_%=:
s_mov_b32 s73, 4
s_mov_b32 s74, 0
s_mov_b32 s75, 16384
s_branch LRARE004_%=
LSITE6_%=:
s_mov_b32 s73, 6
s_mov_b32 s74, 32768
s_mov_b32 s75, 49152
s_branch LRARE004_%=
LSITE8_%=:
s_mov_b32 s73, 8
s_mov_b32 s74, 0
s_mov_b32 s75, 16384
s_branch LRARE004_%=
LSITE10_%=:
s_mov_b32 s73, 10
s_mov_b32 s74, 32768
s_mov_b32 s75, 49152
s_branch LRARE004_%=
LSITE12_%=:
s_mov_b32 s73, 12
s_mov_b32 s74, 0
s_mov_b32 s75, 16384
s_branch LRARE004_%=
LSITE14_%=:
s_mov_b32 s73, 14
s_mov_b32 s74, 32768
s_mov_b32 s75, 49152
s_branch LRARE004_%=
LSITE16_%=:
s_mov_b32 s73, 16
s_mov_b32 s74, 0
s_mov_b32 s75, 16384
s_branch LRARE004_%=
LSITE18_%=:
s_mov_b32 s73, 18
s_mov_b32 s74, 32768
s_mov_b32 s75, 49152
s_branch LRARE004_%=
LSITE20_%=:
s_mov_b32 s73, 20
s_mov_b32 s74, 0
s_mov_b32 s75, 16384
s_branch LRARE004_%=
LSITE22_%=:
s_mov_b32 s73, 22
s_mov_b32 s74, 32768
s_mov_b32 s75, 49152
s_branch LRARE004_%=
LSITE24_%=:
s_mov_b32 s73, 24
s_mov_b32 s74, 0
s_mov_b32 s75, 16384
s_branch LRARE004_%=
LSITE26_%=:
s_mov_b32 s73, 26
s_mov_b32 s74, 32768
s_mov_b32 s75, 49152
s_branch LRARE004_%=
LSITE28_%=:
s_mov_b32 s73, 28
s_mov_b32 s74, 0
s_mov_b32 s75, 16384
s_branch LRARE004_%=
LSITE30_%=:
s_mov_b32 s73, 30
s_mov_b32 s74, 32768
s_mov_b32 s75, 49152
s_branch LRARE004_%=
LSITE32_%=:
s_mov_b32 s73, 32
s_mov_b32 s74, 0
s_mov_b32 s75, 16384
s_branch LRARE004_%=
LSITE34_%=:
s_mov_b32 s73, 34
s_mov_b32 s74, 32768
s_mov_b32 s75, 49152
s_branch LRARE004_%=
LSITE36_%=:
s_mov_b32 s73, 36
s_mov_b32 s74, 0
s_mov_b32 s75, 16384
s_branch LRARE022_%=
LSITE38_%=:
s_mov_b32 s73, 38
s_mov_b32 s74, 32768
s_mov_b32 s75, 49152
s_branch LRARE022_%=
LSITE37_%=:
s_mov_b32 s73, 37
s_mov_b32 s74, 16384
s_mov_b32 s75, 32768
s_branch LRARE122_%=
LRARE002_%=:
s_nop 15
s_waitcnt vmcnt(0) lgkmcnt(0)
v_add_u32 v249, s74, v224
RD a[192:195],v249 offset:0
RD a[208:211],v249 offset:4096
RD a[224:227],v249 offset:8192
RD a[240:243],v249 offset:12288
v_add_u32 v250, s74, v225
RD a[196:199],v250 offset:0
RD a[212:215],v250 offset:4096
RD a[228:231],v250 offset:8192
RD a[244:247],v250 offset:12288
v_add_u32 v251, s74, v226
RD a[200:203],v251 offset:0
RD a[216:219],v251 offset:4096
RD a[232:235],v251 offset:8192
RD a[248:251],v251 offset:12288
v_add_u32 v254, s74, v227
RD a[204:207],v254 offset:0
RD a[220:223],v254 offset:4096
RD a[236:239],v254 offset:8192
RD a[252:255],v254 offset:12288
s_waitcnt lgkmcnt(0)
MM v[0:3],a[192:195],v[160:163],v[128:131]
MM v[0:3],a[196:199],v[176:179],v[0:3]
MM v[0:3],a[200:203],v[192:195],v[0:3]
MM v[0:3],a[204:207],v[208:211],v[0:3]
MM v[4:7],a[220:223],v[208:211],v[128:131]
MM v[4:7],a[216:219],v[192:195],v[4:7]
MM v[4:7],a[212:215],v[176:179],v[4:7]
MM v[4:7],a[208:211],v[160:163],v[4:7]
MM v[12:15],a[208:211],v[164:167],v[132:135]
MM v[12:15],a[212:215],v[180:183],v[12:15]
MM v[12:15],a[216:219],v[196:199],v[12:15]
MM v[12:15],a[220:223],v[212:215],v[12:15]
MM v[8:11],a[204:207],v[212:215],v[132:135]
MM v[8:11],a[200:203],v[196:199],v[8:11]
MM v[8:11],a[196:199],v[180:183],v[8:11]
MM v[8:11],a[192:195],v[164:167],v[8:11]
MM v[32:35],a[224:227],v[160:163],v[128:131]
MM v[32:35],a[228:231],v[176:179],v[32:35]
MM v[32:35],a[232:235],v[192:195],v[32:35]
MM v[32:35],a[236:239],v[208:211],v[32:35]
MM v[36:39],a[252:255],v[208:211],v[128:131]
MM v[36:39],a[248:251],v[192:195],v[36:39]
MM v[36:39],a[244:247],v[176:179],v[36:39]
MM v[36:39],a[240:243],v[160:163],v[36:39]
MM v[44:47],a[240:243],v[164:167],v[132:135]
MM v[44:47],a[244:247],v[180:183],v[44:47]
MM v[44:47],a[248:251],v[196:199],v[44:47]
MM v[44:47],a[252:255],v[212:215],v[44:47]
MM v[40:43],a[236:239],v[212:215],v[132:135]
MM v[40:43],a[232:235],v[196:199],v[40:43]
MM v[40:43],a[228:231],v[180:183],v[40:43]
MM v[40:43],a[224:227],v[164:167],v[40:43]
s_nop 15
v_add_u32 v249, s75, v224
RD a[192:195],v249 offset:0
RD a[208:211],v249 offset:4096
RD a[224:227],v249 offset:8192
RD a[240:243],v249 offset:12288
v_add_u32 v250, s75, v225
RD a[196:199],v250 offset:0
RD a[212:215],v250 offset:4096
RD a[228:231],v250 offset:8192
RD a[244:247],v250 offset:12288
v_add_u32 v251, s75, v226
RD a[200:203],v251 offset:0
RD a[216:219],v251 offset:4096
RD a[232:235],v251 offset:8192
RD a[248:251],v251 offset:12288
v_add_u32 v254, s75, v227
RD a[204:207],v254 offset:0
RD a[220:223],v254 offset:4096
RD a[236:239],v254 offset:8192
RD a[252:255],v254 offset:12288
s_waitcnt lgkmcnt(0)
v_max3_f32 v249, v0, v1, v2
v_max3_f32 v251, v32, v33, v34
v_max3_f32 v249, v249, v3, v4
v_max3_f32 v251, v251, v35, v36
v_max3_f32 v249, v249, v5, v6
v_max3_f32 v251, v251, v37, v38
v_max3_f32 v249, v249, v7, v39
v_max_f32 v249, v249, v251
v_mov_b32 v251, v249
s_nop 1
v_permlane16_swap_b32 v249, v251
v_max_f32 v249, v249, v251
v_mov_b32 v251, v249
s_nop 1
v_permlane32_swap_b32 v249, v251
v_max_f32 v249, v249, v251
v_log_f32 v250, v244
s_nop 0
v_max3_f32 v249, 0, v249, v250
v_sub_f32 v250, 0, v249
EX v250,v250
v_sub_f32 v128, v128, v249
v_sub_f32 v129, v129, v249
v_sub_f32 v130, v130, v249
v_sub_f32 v131, v131, v249
v_sub_f32 v64, v64, v249
v_sub_f32 v0, v0, v249
v_sub_f32 v65, v65, v249
v_sub_f32 v1, v1, v249
v_sub_f32 v66, v66, v249
v_sub_f32 v2, v2, v249
v_sub_f32 v67, v67, v249
v_sub_f32 v3, v3, v249
v_sub_f32 v68, v68, v249
v_sub_f32 v4, v4, v249
v_sub_f32 v69, v69, v249
v_sub_f32 v5, v5, v249
v_sub_f32 v70, v70, v249
v_sub_f32 v6, v6, v249
v_sub_f32 v71, v71, v249
v_sub_f32 v7, v7, v249
v_sub_f32 v96, v96, v249
v_sub_f32 v32, v32, v249
v_sub_f32 v97, v97, v249
v_sub_f32 v33, v33, v249
v_sub_f32 v98, v98, v249
v_sub_f32 v34, v34, v249
v_sub_f32 v99, v99, v249
v_sub_f32 v35, v35, v249
v_sub_f32 v100, v100, v249
v_sub_f32 v36, v36, v249
v_sub_f32 v101, v101, v249
v_sub_f32 v37, v37, v249
v_sub_f32 v102, v102, v249
v_sub_f32 v38, v38, v249
v_sub_f32 v103, v103, v249
v_sub_f32 v39, v39, v249
v_mul_f32 v144, v244, v250
v_mov_b32 v145, 0
v_bfe_u32 v249, v241, 0, 8
v_lshl_or_b32 v249, v249, 1, 1
v_lshlrev_b32 v249, 22, v249
v_and_b32 v241, 0xffffff00, v241
AR v254,a0
v_sub_f32 v254, v254, v249
v_mul_f32 v254, v254, v250
AW a0,v254
AR v251,a1
v_sub_f32 v251, v251, v249
v_mul_f32 v251, v251, v250
AW a1,v251
AR v254,a2
v_sub_f32 v254, v254, v249
v_mul_f32 v254, v254, v250
AW a2,v254
AR v251,a3
v_sub_f32 v251, v251, v249
v_mul_f32 v251, v251, v250
AW a3,v251
AR v254,a4
v_sub_f32 v254, v254, v249
v_mul_f32 v254, v254, v250
AW a4,v254
AR v251,a5
v_sub_f32 v251, v251, v249
v_mul_f32 v251, v251, v250
AW a5,v251
AR v254,a6
v_sub_f32 v254, v254, v249
v_mul_f32 v254, v254, v250
AW a6,v254
AR v251,a7
v_sub_f32 v251, v251, v249
v_mul_f32 v251, v251, v250
AW a7,v251
AR v254,a32
v_sub_f32 v254, v254, v249
v_mul_f32 v254, v254, v250
AW a32,v254
AR v251,a33
v_sub_f32 v251, v251, v249
v_mul_f32 v251, v251, v250
AW a33,v251
AR v254,a34
v_sub_f32 v254, v254, v249
v_mul_f32 v254, v254, v250
AW a34,v254
AR v251,a35
v_sub_f32 v251, v251, v249
v_mul_f32 v251, v251, v250
AW a35,v251
AR v254,a36
v_sub_f32 v254, v254, v249
v_mul_f32 v254, v254, v250
AW a36,v254
AR v251,a37
v_sub_f32 v251, v251, v249
v_mul_f32 v251, v251, v250
AW a37,v251
AR v254,a38
v_sub_f32 v254, v254, v249
v_mul_f32 v254, v254, v250
AW a38,v254
AR v251,a39
v_sub_f32 v251, v251, v249
v_mul_f32 v251, v251, v250
AW a39,v251
AR v254,a64
v_sub_f32 v254, v254, v249
v_mul_f32 v254, v254, v250
AW a64,v254
AR v251,a65
v_sub_f32 v251, v251, v249
v_mul_f32 v251, v251, v250
AW a65,v251
AR v254,a66
v_sub_f32 v254, v254, v249
v_mul_f32 v254, v254, v250
AW a66,v254
AR v251,a67
v_sub_f32 v251, v251, v249
v_mul_f32 v251, v251, v250
AW a67,v251
AR v254,a68
v_sub_f32 v254, v254, v249
v_mul_f32 v254, v254, v250
AW a68,v254
AR v251,a69
v_sub_f32 v251, v251, v249
v_mul_f32 v251, v251, v250
AW a69,v251
AR v254,a70
v_sub_f32 v254, v254, v249
v_mul_f32 v254, v254, v250
AW a70,v254
AR v251,a71
v_sub_f32 v251, v251, v249
v_mul_f32 v251, v251, v250
AW a71,v251
AR v254,a96
v_sub_f32 v254, v254, v249
v_mul_f32 v254, v254, v250
AW a96,v254
AR v251,a97
v_sub_f32 v251, v251, v249
v_mul_f32 v251, v251, v250
AW a97,v251
AR v254,a98
v_sub_f32 v254, v254, v249
v_mul_f32 v254, v254, v250
AW a98,v254
AR v251,a99
v_sub_f32 v251, v251, v249
v_mul_f32 v251, v251, v250
AW a99,v251
AR v254,a100
v_sub_f32 v254, v254, v249
v_mul_f32 v254, v254, v250
AW a100,v254
AR v251,a101
v_sub_f32 v251, v251, v249
v_mul_f32 v251, v251, v250
AW a101,v251
AR v254,a102
v_sub_f32 v254, v254, v249
v_mul_f32 v254, v254, v250
AW a102,v254
AR v251,a103
v_sub_f32 v251, v251, v249
v_mul_f32 v251, v251, v250
AW a103,v251
v_max3_f32 v249, v8, v9, v10
v_max3_f32 v251, v40, v41, v42
v_max3_f32 v249, v249, v11, v12
v_max3_f32 v251, v251, v43, v44
v_max3_f32 v249, v249, v13, v14
v_max3_f32 v251, v251, v45, v46
v_max3_f32 v249, v249, v15, v47
v_max_f32 v249, v249, v251
v_mov_b32 v251, v249
s_nop 1
v_permlane16_swap_b32 v249, v251
v_max_f32 v249, v249, v251
v_mov_b32 v251, v249
s_nop 1
v_permlane32_swap_b32 v249, v251
v_max_f32 v249, v249, v251
v_log_f32 v250, v245
s_nop 0
v_max3_f32 v249, 0, v249, v250
v_sub_f32 v250, 0, v249
EX v250,v250
v_sub_f32 v132, v132, v249
v_sub_f32 v133, v133, v249
v_sub_f32 v134, v134, v249
v_sub_f32 v135, v135, v249
v_sub_f32 v72, v72, v249
v_sub_f32 v8, v8, v249
v_sub_f32 v73, v73, v249
v_sub_f32 v9, v9, v249
v_sub_f32 v74, v74, v249
v_sub_f32 v10, v10, v249
v_sub_f32 v75, v75, v249
v_sub_f32 v11, v11, v249
v_sub_f32 v76, v76, v249
v_sub_f32 v12, v12, v249
v_sub_f32 v77, v77, v249
v_sub_f32 v13, v13, v249
v_sub_f32 v78, v78, v249
v_sub_f32 v14, v14, v249
v_sub_f32 v79, v79, v249
v_sub_f32 v15, v15, v249
v_sub_f32 v104, v104, v249
v_sub_f32 v40, v40, v249
v_sub_f32 v105, v105, v249
v_sub_f32 v41, v41, v249
v_sub_f32 v106, v106, v249
v_sub_f32 v42, v42, v249
v_sub_f32 v107, v107, v249
v_sub_f32 v43, v43, v249
v_sub_f32 v108, v108, v249
v_sub_f32 v44, v44, v249
v_sub_f32 v109, v109, v249
v_sub_f32 v45, v45, v249
v_sub_f32 v110, v110, v249
v_sub_f32 v46, v46, v249
v_sub_f32 v111, v111, v249
v_sub_f32 v47, v47, v249
v_mul_f32 v148, v245, v250
v_mov_b32 v149, 0
v_bfe_u32 v249, v241, 8, 8
v_lshl_or_b32 v249, v249, 1, 1
v_lshlrev_b32 v249, 22, v249
v_and_b32 v241, 0xffff00ff, v241
AR v254,a8
v_sub_f32 v254, v254, v249
v_mul_f32 v254, v254, v250
AW a8,v254
AR v251,a9
v_sub_f32 v251, v251, v249
v_mul_f32 v251, v251, v250
AW a9,v251
AR v254,a10
v_sub_f32 v254, v254, v249
v_mul_f32 v254, v254, v250
AW a10,v254
AR v251,a11
v_sub_f32 v251, v251, v249
v_mul_f32 v251, v251, v250
AW a11,v251
AR v254,a12
v_sub_f32 v254, v254, v249
v_mul_f32 v254, v254, v250
AW a12,v254
AR v251,a13
v_sub_f32 v251, v251, v249
v_mul_f32 v251, v251, v250
AW a13,v251
AR v254,a14
v_sub_f32 v254, v254, v249
v_mul_f32 v254, v254, v250
AW a14,v254
AR v251,a15
v_sub_f32 v251, v251, v249
v_mul_f32 v251, v251, v250
AW a15,v251
AR v254,a40
v_sub_f32 v254, v254, v249
v_mul_f32 v254, v254, v250
AW a40,v254
AR v251,a41
v_sub_f32 v251, v251, v249
v_mul_f32 v251, v251, v250
AW a41,v251
AR v254,a42
v_sub_f32 v254, v254, v249
v_mul_f32 v254, v254, v250
AW a42,v254
AR v251,a43
v_sub_f32 v251, v251, v249
v_mul_f32 v251, v251, v250
AW a43,v251
AR v254,a44
v_sub_f32 v254, v254, v249
v_mul_f32 v254, v254, v250
AW a44,v254
AR v251,a45
v_sub_f32 v251, v251, v249
v_mul_f32 v251, v251, v250
AW a45,v251
AR v254,a46
v_sub_f32 v254, v254, v249
v_mul_f32 v254, v254, v250
AW a46,v254
AR v251,a47
v_sub_f32 v251, v251, v249
v_mul_f32 v251, v251, v250
AW a47,v251
AR v254,a72
v_sub_f32 v254, v254, v249
v_mul_f32 v254, v254, v250
AW a72,v254
AR v251,a73
v_sub_f32 v251, v251, v249
v_mul_f32 v251, v251, v250
AW a73,v251
AR v254,a74
v_sub_f32 v254, v254, v249
v_mul_f32 v254, v254, v250
AW a74,v254
AR v251,a75
v_sub_f32 v251, v251, v249
v_mul_f32 v251, v251, v250
AW a75,v251
AR v254,a76
v_sub_f32 v254, v254, v249
v_mul_f32 v254, v254, v250
AW a76,v254
AR v251,a77
v_sub_f32 v251, v251, v249
)ASM"
R"ASM(
v_mul_f32 v251, v251, v250
AW a77,v251
AR v254,a78
v_sub_f32 v254, v254, v249
v_mul_f32 v254, v254, v250
AW a78,v254
AR v251,a79
v_sub_f32 v251, v251, v249
v_mul_f32 v251, v251, v250
AW a79,v251
AR v254,a104
v_sub_f32 v254, v254, v249
v_mul_f32 v254, v254, v250
AW a104,v254
AR v251,a105
v_sub_f32 v251, v251, v249
v_mul_f32 v251, v251, v250
AW a105,v251
AR v254,a106
v_sub_f32 v254, v254, v249
v_mul_f32 v254, v254, v250
AW a106,v254
AR v251,a107
v_sub_f32 v251, v251, v249
v_mul_f32 v251, v251, v250
AW a107,v251
AR v254,a108
v_sub_f32 v254, v254, v249
v_mul_f32 v254, v254, v250
AW a108,v254
AR v251,a109
v_sub_f32 v251, v251, v249
v_mul_f32 v251, v251, v250
AW a109,v251
AR v254,a110
v_sub_f32 v254, v254, v249
v_mul_f32 v254, v254, v250
AW a110,v254
AR v251,a111
v_sub_f32 v251, v251, v249
v_mul_f32 v251, v251, v250
AW a111,v251
EX v0,v0
EX v1,v1
EX v2,v2
EX v3,v3
EX v4,v4
EX v5,v5
EX v6,v6
EX v7,v7
EX v8,v8
EX v9,v9
EX v10,v10
EX v11,v11
EX v12,v12
EX v13,v13
EX v14,v14
EX v15,v15
EX v32,v32
EX v33,v33
EX v34,v34
EX v35,v35
EX v36,v36
EX v37,v37
EX v38,v38
EX v39,v39
EX v40,v40
EX v41,v41
EX v42,v42
EX v43,v43
EX v44,v44
EX v45,v45
EX v46,v46
EX v47,v47
CV v0,v0,v1
CV v1,v2,v3
CV v2,v4,v5
CV v3,v6,v7
CV v8,v8,v9
CV v9,v10,v11
CV v10,v12,v13
CV v11,v14,v15
CV v4,v32,v33
CV v5,v34,v35
CV v6,v36,v37
CV v7,v38,v39
CV v12,v40,v41
CV v13,v42,v43
CV v14,v44,v45
CV v15,v46,v47
v_smfmac_f32_16x16x64_bf16 v[144:147], v[236:239], v[0:7], v234
s_nop 0
v_smfmac_f32_16x16x64_bf16 v[148:151], v[236:239], v[8:15], v234
s_nop 9
s_branch LRET_%=
LRARE102_%=:
s_nop 15
s_waitcnt vmcnt(0) lgkmcnt(0)
v_add_u32 v249, s74, v224
RD a[192:195],v249 offset:0
RD a[208:211],v249 offset:4096
RD a[224:227],v249 offset:8192
RD a[240:243],v249 offset:12288
v_add_u32 v250, s74, v225
RD a[196:199],v250 offset:0
RD a[212:215],v250 offset:4096
RD a[228:231],v250 offset:8192
RD a[244:247],v250 offset:12288
v_add_u32 v251, s74, v226
RD a[200:203],v251 offset:0
RD a[216:219],v251 offset:4096
RD a[232:235],v251 offset:8192
RD a[248:251],v251 offset:12288
v_add_u32 v254, s74, v227
RD a[204:207],v254 offset:0
RD a[220:223],v254 offset:4096
RD a[236:239],v254 offset:8192
RD a[252:255],v254 offset:12288
s_waitcnt lgkmcnt(0)
MM v[64:67],a[192:195],v[160:163],v[128:131]
MM v[64:67],a[196:199],v[176:179],v[64:67]
MM v[64:67],a[200:203],v[192:195],v[64:67]
MM v[64:67],a[204:207],v[208:211],v[64:67]
MM v[68:71],a[220:223],v[208:211],v[128:131]
MM v[68:71],a[216:219],v[192:195],v[68:71]
MM v[68:71],a[212:215],v[176:179],v[68:71]
MM v[68:71],a[208:211],v[160:163],v[68:71]
MM v[76:79],a[208:211],v[164:167],v[132:135]
MM v[76:79],a[212:215],v[180:183],v[76:79]
MM v[76:79],a[216:219],v[196:199],v[76:79]
MM v[76:79],a[220:223],v[212:215],v[76:79]
MM v[72:75],a[204:207],v[212:215],v[132:135]
MM v[72:75],a[200:203],v[196:199],v[72:75]
MM v[72:75],a[196:199],v[180:183],v[72:75]
MM v[72:75],a[192:195],v[164:167],v[72:75]
MM v[96:99],a[224:227],v[160:163],v[128:131]
MM v[96:99],a[228:231],v[176:179],v[96:99]
MM v[96:99],a[232:235],v[192:195],v[96:99]
MM v[96:99],a[236:239],v[208:211],v[96:99]
MM v[100:103],a[252:255],v[208:211],v[128:131]
MM v[100:103],a[248:251],v[192:195],v[100:103]
MM v[100:103],a[244:247],v[176:179],v[100:103]
MM v[100:103],a[240:243],v[160:163],v[100:103]
MM v[108:111],a[240:243],v[164:167],v[132:135]
MM v[108:111],a[244:247],v[180:183],v[108:111]
MM v[108:111],a[248:251],v[196:199],v[108:111]
MM v[108:111],a[252:255],v[212:215],v[108:111]
MM v[104:107],a[236:239],v[212:215],v[132:135]
MM v[104:107],a[232:235],v[196:199],v[104:107]
MM v[104:107],a[228:231],v[180:183],v[104:107]
MM v[104:107],a[224:227],v[164:167],v[104:107]
s_nop 15
v_add_u32 v249, s75, v224
RD a[192:195],v249 offset:0
RD a[208:211],v249 offset:4096
RD a[224:227],v249 offset:8192
RD a[240:243],v249 offset:12288
v_add_u32 v250, s75, v225
RD a[196:199],v250 offset:0
RD a[212:215],v250 offset:4096
RD a[228:231],v250 offset:8192
RD a[244:247],v250 offset:12288
v_add_u32 v251, s75, v226
RD a[200:203],v251 offset:0
RD a[216:219],v251 offset:4096
RD a[232:235],v251 offset:8192
RD a[248:251],v251 offset:12288
v_add_u32 v254, s75, v227
RD a[204:207],v254 offset:0
RD a[220:223],v254 offset:4096
RD a[236:239],v254 offset:8192
RD a[252:255],v254 offset:12288
s_waitcnt lgkmcnt(0)
v_max3_f32 v249, v64, v65, v66
v_max3_f32 v251, v96, v97, v98
v_max3_f32 v249, v249, v67, v68
v_max3_f32 v251, v251, v99, v100
v_max3_f32 v249, v249, v69, v70
v_max3_f32 v251, v251, v101, v102
v_max3_f32 v249, v249, v71, v103
v_max_f32 v249, v249, v251
v_mov_b32 v251, v249
s_nop 1
v_permlane16_swap_b32 v249, v251
v_max_f32 v249, v249, v251
v_mov_b32 v251, v249
s_nop 1
v_permlane32_swap_b32 v249, v251
v_max_f32 v249, v249, v251
v_log_f32 v250, v244
s_nop 0
v_max3_f32 v249, 0, v249, v250
v_sub_f32 v250, 0, v249
EX v250,v250
v_sub_f32 v128, v128, v249
v_sub_f32 v129, v129, v249
v_sub_f32 v130, v130, v249
v_sub_f32 v131, v131, v249
v_sub_f32 v0, v0, v249
v_sub_f32 v64, v64, v249
v_sub_f32 v1, v1, v249
v_sub_f32 v65, v65, v249
v_sub_f32 v2, v2, v249
v_sub_f32 v66, v66, v249
v_sub_f32 v3, v3, v249
v_sub_f32 v67, v67, v249
v_sub_f32 v4, v4, v249
v_sub_f32 v68, v68, v249
v_sub_f32 v5, v5, v249
v_sub_f32 v69, v69, v249
v_sub_f32 v6, v6, v249
v_sub_f32 v70, v70, v249
v_sub_f32 v7, v7, v249
v_sub_f32 v71, v71, v249
v_sub_f32 v32, v32, v249
v_sub_f32 v96, v96, v249
v_sub_f32 v33, v33, v249
v_sub_f32 v97, v97, v249
v_sub_f32 v34, v34, v249
v_sub_f32 v98, v98, v249
v_sub_f32 v35, v35, v249
v_sub_f32 v99, v99, v249
v_sub_f32 v36, v36, v249
v_sub_f32 v100, v100, v249
v_sub_f32 v37, v37, v249
v_sub_f32 v101, v101, v249
v_sub_f32 v38, v38, v249
v_sub_f32 v102, v102, v249
v_sub_f32 v39, v39, v249
v_sub_f32 v103, v103, v249
v_mul_f32 v144, v244, v250
v_mov_b32 v145, 0
v_bfe_u32 v249, v241, 0, 8
v_lshl_or_b32 v249, v249, 1, 1
v_lshlrev_b32 v249, 22, v249
v_and_b32 v241, 0xffffff00, v241
AR v254,a0
v_sub_f32 v254, v254, v249
v_mul_f32 v254, v254, v250
AW a0,v254
AR v251,a1
v_sub_f32 v251, v251, v249
v_mul_f32 v251, v251, v250
AW a1,v251
AR v254,a2
v_sub_f32 v254, v254, v249
v_mul_f32 v254, v254, v250
AW a2,v254
AR v251,a3
v_sub_f32 v251, v251, v249
v_mul_f32 v251, v251, v250
AW a3,v251
AR v254,a4
v_sub_f32 v254, v254, v249
v_mul_f32 v254, v254, v250
AW a4,v254
AR v251,a5
v_sub_f32 v251, v251, v249
v_mul_f32 v251, v251, v250
AW a5,v251
AR v254,a6
v_sub_f32 v254, v254, v249
v_mul_f32 v254, v254, v250
AW a6,v254
AR v251,a7
v_sub_f32 v251, v251, v249
v_mul_f32 v251, v251, v250
AW a7,v251
AR v254,a32
v_sub_f32 v254, v254, v249
v_mul_f32 v254, v254, v250
AW a32,v254
AR v251,a33
v_sub_f32 v251, v251, v249
v_mul_f32 v251, v251, v250
AW a33,v251
AR v254,a34
v_sub_f32 v254, v254, v249
v_mul_f32 v254, v254, v250
AW a34,v254
AR v251,a35
v_sub_f32 v251, v251, v249
v_mul_f32 v251, v251, v250
AW a35,v251
AR v254,a36
v_sub_f32 v254, v254, v249
v_mul_f32 v254, v254, v250
AW a36,v254
AR v251,a37
v_sub_f32 v251, v251, v249
v_mul_f32 v251, v251, v250
AW a37,v251
AR v254,a38
v_sub_f32 v254, v254, v249
v_mul_f32 v254, v254, v250
AW a38,v254
AR v251,a39
v_sub_f32 v251, v251, v249
v_mul_f32 v251, v251, v250
AW a39,v251
AR v254,a64
v_sub_f32 v254, v254, v249
v_mul_f32 v254, v254, v250
AW a64,v254
AR v251,a65
v_sub_f32 v251, v251, v249
v_mul_f32 v251, v251, v250
AW a65,v251
AR v254,a66
v_sub_f32 v254, v254, v249
v_mul_f32 v254, v254, v250
AW a66,v254
AR v251,a67
v_sub_f32 v251, v251, v249
v_mul_f32 v251, v251, v250
AW a67,v251
AR v254,a68
v_sub_f32 v254, v254, v249
v_mul_f32 v254, v254, v250
AW a68,v254
AR v251,a69
v_sub_f32 v251, v251, v249
v_mul_f32 v251, v251, v250
AW a69,v251
AR v254,a70
v_sub_f32 v254, v254, v249
v_mul_f32 v254, v254, v250
AW a70,v254
AR v251,a71
v_sub_f32 v251, v251, v249
v_mul_f32 v251, v251, v250
AW a71,v251
AR v254,a96
v_sub_f32 v254, v254, v249
v_mul_f32 v254, v254, v250
AW a96,v254
AR v251,a97
v_sub_f32 v251, v251, v249
v_mul_f32 v251, v251, v250
AW a97,v251
AR v254,a98
v_sub_f32 v254, v254, v249
v_mul_f32 v254, v254, v250
AW a98,v254
AR v251,a99
v_sub_f32 v251, v251, v249
v_mul_f32 v251, v251, v250
AW a99,v251
AR v254,a100
v_sub_f32 v254, v254, v249
v_mul_f32 v254, v254, v250
AW a100,v254
AR v251,a101
v_sub_f32 v251, v251, v249
v_mul_f32 v251, v251, v250
AW a101,v251
AR v254,a102
v_sub_f32 v254, v254, v249
v_mul_f32 v254, v254, v250
AW a102,v254
AR v251,a103
v_sub_f32 v251, v251, v249
v_mul_f32 v251, v251, v250
AW a103,v251
v_max3_f32 v249, v72, v73, v74
v_max3_f32 v251, v104, v105, v106
v_max3_f32 v249, v249, v75, v76
v_max3_f32 v251, v251, v107, v108
v_max3_f32 v249, v249, v77, v78
v_max3_f32 v251, v251, v109, v110
v_max3_f32 v249, v249, v79, v111
v_max_f32 v249, v249, v251
v_mov_b32 v251, v249
s_nop 1
v_permlane16_swap_b32 v249, v251
v_max_f32 v249, v249, v251
v_mov_b32 v251, v249
s_nop 1
v_permlane32_swap_b32 v249, v251
v_max_f32 v249, v249, v251
v_log_f32 v250, v245
s_nop 0
v_max3_f32 v249, 0, v249, v250
v_sub_f32 v250, 0, v249
EX v250,v250
v_sub_f32 v132, v132, v249
v_sub_f32 v133, v133, v249
v_sub_f32 v134, v134, v249
v_sub_f32 v135, v135, v249
v_sub_f32 v8, v8, v249
v_sub_f32 v72, v72, v249
v_sub_f32 v9, v9, v249
v_sub_f32 v73, v73, v249
v_sub_f32 v10, v10, v249
v_sub_f32 v74, v74, v249
v_sub_f32 v11, v11, v249
v_sub_f32 v75, v75, v249
v_sub_f32 v12, v12, v249
v_sub_f32 v76, v76, v249
v_sub_f32 v13, v13, v249
v_sub_f32 v77, v77, v249
v_sub_f32 v14, v14, v249
v_sub_f32 v78, v78, v249
v_sub_f32 v15, v15, v249
v_sub_f32 v79, v79, v249
v_sub_f32 v40, v40, v249
v_sub_f32 v104, v104, v249
v_sub_f32 v41, v41, v249
v_sub_f32 v105, v105, v249
v_sub_f32 v42, v42, v249
v_sub_f32 v106, v106, v249
v_sub_f32 v43, v43, v249
v_sub_f32 v107, v107, v249
v_sub_f32 v44, v44, v249
v_sub_f32 v108, v108, v249
v_sub_f32 v45, v45, v249
v_sub_f32 v109, v109, v249
v_sub_f32 v46, v46, v249
v_sub_f32 v110, v110, v249
v_sub_f32 v47, v47, v249
v_sub_f32 v111, v111, v249
v_mul_f32 v148, v245, v250
v_mov_b32 v149, 0
v_bfe_u32 v249, v241, 8, 8
v_lshl_or_b32 v249, v249, 1, 1
v_lshlrev_b32 v249, 22, v249
v_and_b32 v241, 0xffff00ff, v241
AR v254,a8
v_sub_f32 v254, v254, v249
v_mul_f32 v254, v254, v250
AW a8,v254
AR v251,a9
v_sub_f32 v251, v251, v249
v_mul_f32 v251, v251, v250
AW a9,v251
AR v254,a10
v_sub_f32 v254, v254, v249
v_mul_f32 v254, v254, v250
AW a10,v254
AR v251,a11
v_sub_f32 v251, v251, v249
v_mul_f32 v251, v251, v250
AW a11,v251
AR v254,a12
v_sub_f32 v254, v254, v249
v_mul_f32 v254, v254, v250
AW a12,v254
AR v251,a13
v_sub_f32 v251, v251, v249
v_mul_f32 v251, v251, v250
AW a13,v251
AR v254,a14
v_sub_f32 v254, v254, v249
v_mul_f32 v254, v254, v250
AW a14,v254
AR v251,a15
v_sub_f32 v251, v251, v249
v_mul_f32 v251, v251, v250
AW a15,v251
AR v254,a40
v_sub_f32 v254, v254, v249
v_mul_f32 v254, v254, v250
AW a40,v254
AR v251,a41
v_sub_f32 v251, v251, v249
v_mul_f32 v251, v251, v250
AW a41,v251
AR v254,a42
v_sub_f32 v254, v254, v249
v_mul_f32 v254, v254, v250
AW a42,v254
AR v251,a43
v_sub_f32 v251, v251, v249
v_mul_f32 v251, v251, v250
AW a43,v251
AR v254,a44
v_sub_f32 v254, v254, v249
v_mul_f32 v254, v254, v250
AW a44,v254
AR v251,a45
v_sub_f32 v251, v251, v249
v_mul_f32 v251, v251, v250
AW a45,v251
AR v254,a46
v_sub_f32 v254, v254, v249
v_mul_f32 v254, v254, v250
AW a46,v254
AR v251,a47
v_sub_f32 v251, v251, v249
v_mul_f32 v251, v251, v250
AW a47,v251
AR v254,a72
v_sub_f32 v254, v254, v249
v_mul_f32 v254, v254, v250
AW a72,v254
AR v251,a73
v_sub_f32 v251, v251, v249
v_mul_f32 v251, v251, v250
AW a73,v251
AR v254,a74
v_sub_f32 v254, v254, v249
v_mul_f32 v254, v254, v250
AW a74,v254
AR v251,a75
v_sub_f32 v251, v251, v249
v_mul_f32 v251, v251, v250
AW a75,v251
AR v254,a76
v_sub_f32 v254, v254, v249
v_mul_f32 v254, v254, v250
AW a76,v254
AR v251,a77
v_sub_f32 v251, v251, v249
v_mul_f32 v251, v251, v250
AW a77,v251
AR v254,a78
v_sub_f32 v254, v254, v249
v_mul_f32 v254, v254, v250
AW a78,v254
AR v251,a79
v_sub_f32 v251, v251, v249
v_mul_f32 v251, v251, v250
AW a79,v251
AR v254,a104
v_sub_f32 v254, v254, v249
v_mul_f32 v254, v254, v250
AW a104,v254
AR v251,a105
v_sub_f32 v251, v251, v249
v_mul_f32 v251, v251, v250
AW a105,v251
AR v254,a106
)ASM"
R"ASM(
v_sub_f32 v254, v254, v249
v_mul_f32 v254, v254, v250
AW a106,v254
AR v251,a107
v_sub_f32 v251, v251, v249
v_mul_f32 v251, v251, v250
AW a107,v251
AR v254,a108
v_sub_f32 v254, v254, v249
v_mul_f32 v254, v254, v250
AW a108,v254
AR v251,a109
v_sub_f32 v251, v251, v249
v_mul_f32 v251, v251, v250
AW a109,v251
AR v254,a110
v_sub_f32 v254, v254, v249
v_mul_f32 v254, v254, v250
AW a110,v254
AR v251,a111
v_sub_f32 v251, v251, v249
v_mul_f32 v251, v251, v250
AW a111,v251
EX v64,v64
EX v65,v65
EX v66,v66
EX v67,v67
EX v68,v68
EX v69,v69
EX v70,v70
EX v71,v71
EX v72,v72
EX v73,v73
EX v74,v74
EX v75,v75
EX v76,v76
EX v77,v77
EX v78,v78
EX v79,v79
EX v96,v96
EX v97,v97
EX v98,v98
EX v99,v99
EX v100,v100
EX v101,v101
EX v102,v102
EX v103,v103
EX v104,v104
EX v105,v105
EX v106,v106
EX v107,v107
EX v108,v108
EX v109,v109
EX v110,v110
EX v111,v111
CV v64,v64,v65
CV v65,v66,v67
CV v66,v68,v69
CV v67,v70,v71
CV v72,v72,v73
CV v73,v74,v75
CV v74,v76,v77
CV v75,v78,v79
CV v68,v96,v97
CV v69,v98,v99
CV v70,v100,v101
CV v71,v102,v103
CV v76,v104,v105
CV v77,v106,v107
CV v78,v108,v109
CV v79,v110,v111
v_smfmac_f32_16x16x64_bf16 v[144:147], v[236:239], v[64:71], v234
s_nop 0
v_smfmac_f32_16x16x64_bf16 v[148:151], v[236:239], v[72:79], v234
s_nop 9
s_branch LRET_%=
LRARE104_%=:
s_nop 15
s_waitcnt vmcnt(0) lgkmcnt(0)
v_add_u32 v249, s74, v224
RD a[192:195],v249 offset:0
RD a[208:211],v249 offset:4096
RD a[224:227],v249 offset:8192
RD a[240:243],v249 offset:12288
v_add_u32 v250, s74, v225
RD a[196:199],v250 offset:0
RD a[212:215],v250 offset:4096
RD a[228:231],v250 offset:8192
RD a[244:247],v250 offset:12288
v_add_u32 v251, s74, v226
RD a[200:203],v251 offset:0
RD a[216:219],v251 offset:4096
RD a[232:235],v251 offset:8192
RD a[248:251],v251 offset:12288
v_add_u32 v254, s74, v227
RD a[204:207],v254 offset:0
RD a[220:223],v254 offset:4096
RD a[236:239],v254 offset:8192
RD a[252:255],v254 offset:12288
s_waitcnt lgkmcnt(0)
MM v[64:67],a[192:195],v[160:163],v[128:131]
MM v[64:67],a[196:199],v[176:179],v[64:67]
MM v[64:67],a[200:203],v[192:195],v[64:67]
MM v[64:67],a[204:207],v[208:211],v[64:67]
MM v[68:71],a[220:223],v[208:211],v[128:131]
MM v[68:71],a[216:219],v[192:195],v[68:71]
MM v[68:71],a[212:215],v[176:179],v[68:71]
MM v[68:71],a[208:211],v[160:163],v[68:71]
MM v[76:79],a[208:211],v[164:167],v[132:135]
MM v[76:79],a[212:215],v[180:183],v[76:79]
MM v[76:79],a[216:219],v[196:199],v[76:79]
MM v[76:79],a[220:223],v[212:215],v[76:79]
MM v[72:75],a[204:207],v[212:215],v[132:135]
MM v[72:75],a[200:203],v[196:199],v[72:75]
MM v[72:75],a[196:199],v[180:183],v[72:75]
MM v[72:75],a[192:195],v[164:167],v[72:75]
MM v[80:83],a[192:195],v[168:171],v[136:139]
MM v[80:83],a[196:199],v[184:187],v[80:83]
MM v[80:83],a[200:203],v[200:203],v[80:83]
MM v[80:83],a[204:207],v[216:219],v[80:83]
MM v[84:87],a[220:223],v[216:219],v[136:139]
MM v[84:87],a[216:219],v[200:203],v[84:87]
MM v[84:87],a[212:215],v[184:187],v[84:87]
MM v[84:87],a[208:211],v[168:171],v[84:87]
MM v[92:95],a[208:211],v[172:175],v[140:143]
MM v[92:95],a[212:215],v[188:191],v[92:95]
MM v[92:95],a[216:219],v[204:207],v[92:95]
MM v[92:95],a[220:223],v[220:223],v[92:95]
MM v[88:91],a[204:207],v[220:223],v[140:143]
MM v[88:91],a[200:203],v[204:207],v[88:91]
MM v[88:91],a[196:199],v[188:191],v[88:91]
MM v[88:91],a[192:195],v[172:175],v[88:91]
MM v[96:99],a[224:227],v[160:163],v[128:131]
MM v[96:99],a[228:231],v[176:179],v[96:99]
MM v[96:99],a[232:235],v[192:195],v[96:99]
MM v[96:99],a[236:239],v[208:211],v[96:99]
MM v[100:103],a[252:255],v[208:211],v[128:131]
MM v[100:103],a[248:251],v[192:195],v[100:103]
MM v[100:103],a[244:247],v[176:179],v[100:103]
MM v[100:103],a[240:243],v[160:163],v[100:103]
MM v[108:111],a[240:243],v[164:167],v[132:135]
MM v[108:111],a[244:247],v[180:183],v[108:111]
MM v[108:111],a[248:251],v[196:199],v[108:111]
MM v[108:111],a[252:255],v[212:215],v[108:111]
MM v[104:107],a[236:239],v[212:215],v[132:135]
MM v[104:107],a[232:235],v[196:199],v[104:107]
MM v[104:107],a[228:231],v[180:183],v[104:107]
MM v[104:107],a[224:227],v[164:167],v[104:107]
MM v[112:115],a[224:227],v[168:171],v[136:139]
MM v[112:115],a[228:231],v[184:187],v[112:115]
MM v[112:115],a[232:235],v[200:203],v[112:115]
MM v[112:115],a[236:239],v[216:219],v[112:115]
MM v[116:119],a[252:255],v[216:219],v[136:139]
MM v[116:119],a[248:251],v[200:203],v[116:119]
MM v[116:119],a[244:247],v[184:187],v[116:119]
MM v[116:119],a[240:243],v[168:171],v[116:119]
MM v[124:127],a[240:243],v[172:175],v[140:143]
MM v[124:127],a[244:247],v[188:191],v[124:127]
MM v[124:127],a[248:251],v[204:207],v[124:127]
MM v[124:127],a[252:255],v[220:223],v[124:127]
MM v[120:123],a[236:239],v[220:223],v[140:143]
MM v[120:123],a[232:235],v[204:207],v[120:123]
MM v[120:123],a[228:231],v[188:191],v[120:123]
MM v[120:123],a[224:227],v[172:175],v[120:123]
s_nop 15
v_add_u32 v249, s75, v224
RD a[192:195],v249 offset:0
RD a[208:211],v249 offset:4096
RD a[224:227],v249 offset:8192
RD a[240:243],v249 offset:12288
v_add_u32 v250, s75, v225
RD a[196:199],v250 offset:0
RD a[212:215],v250 offset:4096
RD a[228:231],v250 offset:8192
RD a[244:247],v250 offset:12288
v_add_u32 v251, s75, v226
RD a[200:203],v251 offset:0
RD a[216:219],v251 offset:4096
RD a[232:235],v251 offset:8192
RD a[248:251],v251 offset:12288
v_add_u32 v254, s75, v227
RD a[204:207],v254 offset:0
RD a[220:223],v254 offset:4096
RD a[236:239],v254 offset:8192
RD a[252:255],v254 offset:12288
s_waitcnt lgkmcnt(0)
v_max3_f32 v249, v64, v65, v66
v_max3_f32 v251, v96, v97, v98
v_max3_f32 v249, v249, v67, v68
v_max3_f32 v251, v251, v99, v100
v_max3_f32 v249, v249, v69, v70
v_max3_f32 v251, v251, v101, v102
v_max3_f32 v249, v249, v71, v103
v_max_f32 v249, v249, v251
v_mov_b32 v251, v249
s_nop 1
v_permlane16_swap_b32 v249, v251
v_max_f32 v249, v249, v251
v_mov_b32 v251, v249
s_nop 1
v_permlane32_swap_b32 v249, v251
v_max_f32 v249, v249, v251
v_log_f32 v250, v244
s_nop 0
v_max3_f32 v249, 0, v249, v250
v_sub_f32 v250, 0, v249
EX v250,v250
v_sub_f32 v128, v128, v249
v_sub_f32 v129, v129, v249
v_sub_f32 v130, v130, v249
v_sub_f32 v131, v131, v249
v_sub_f32 v0, v0, v249
v_sub_f32 v64, v64, v249
v_sub_f32 v1, v1, v249
v_sub_f32 v65, v65, v249
v_sub_f32 v2, v2, v249
v_sub_f32 v66, v66, v249
v_sub_f32 v3, v3, v249
v_sub_f32 v67, v67, v249
v_sub_f32 v4, v4, v249
v_sub_f32 v68, v68, v249
v_sub_f32 v5, v5, v249
v_sub_f32 v69, v69, v249
v_sub_f32 v6, v6, v249
v_sub_f32 v70, v70, v249
v_sub_f32 v7, v7, v249
v_sub_f32 v71, v71, v249
v_sub_f32 v32, v32, v249
v_sub_f32 v96, v96, v249
v_sub_f32 v33, v33, v249
v_sub_f32 v97, v97, v249
v_sub_f32 v34, v34, v249
v_sub_f32 v98, v98, v249
v_sub_f32 v35, v35, v249
v_sub_f32 v99, v99, v249
v_sub_f32 v36, v36, v249
v_sub_f32 v100, v100, v249
v_sub_f32 v37, v37, v249
v_sub_f32 v101, v101, v249
v_sub_f32 v38, v38, v249
v_sub_f32 v102, v102, v249
v_sub_f32 v39, v39, v249
v_sub_f32 v103, v103, v249
v_mul_f32 v144, v244, v250
v_mov_b32 v145, 0
v_bfe_u32 v249, v241, 0, 8
v_lshl_or_b32 v249, v249, 1, 1
v_lshlrev_b32 v249, 22, v249
v_and_b32 v241, 0xffffff00, v241
AR v254,a0
v_sub_f32 v254, v254, v249
v_mul_f32 v254, v254, v250
AW a0,v254
AR v251,a1
v_sub_f32 v251, v251, v249
v_mul_f32 v251, v251, v250
AW a1,v251
AR v254,a2
v_sub_f32 v254, v254, v249
v_mul_f32 v254, v254, v250
AW a2,v254
AR v251,a3
v_sub_f32 v251, v251, v249
v_mul_f32 v251, v251, v250
AW a3,v251
AR v254,a4
v_sub_f32 v254, v254, v249
v_mul_f32 v254, v254, v250
AW a4,v254
AR v251,a5
v_sub_f32 v251, v251, v249
v_mul_f32 v251, v251, v250
AW a5,v251
AR v254,a6
v_sub_f32 v254, v254, v249
v_mul_f32 v254, v254, v250
AW a6,v254
AR v251,a7
v_sub_f32 v251, v251, v249
v_mul_f32 v251, v251, v250
AW a7,v251
AR v254,a32
v_sub_f32 v254, v254, v249
v_mul_f32 v254, v254, v250
AW a32,v254
AR v251,a33
v_sub_f32 v251, v251, v249
v_mul_f32 v251, v251, v250
AW a33,v251
AR v254,a34
v_sub_f32 v254, v254, v249
v_mul_f32 v254, v254, v250
AW a34,v254
AR v251,a35
v_sub_f32 v251, v251, v249
v_mul_f32 v251, v251, v250
AW a35,v251
AR v254,a36
v_sub_f32 v254, v254, v249
v_mul_f32 v254, v254, v250
AW a36,v254
AR v251,a37
v_sub_f32 v251, v251, v249
v_mul_f32 v251, v251, v250
AW a37,v251
AR v254,a38
v_sub_f32 v254, v254, v249
v_mul_f32 v254, v254, v250
AW a38,v254
AR v251,a39
v_sub_f32 v251, v251, v249
v_mul_f32 v251, v251, v250
AW a39,v251
AR v254,a64
v_sub_f32 v254, v254, v249
v_mul_f32 v254, v254, v250
AW a64,v254
AR v251,a65
v_sub_f32 v251, v251, v249
v_mul_f32 v251, v251, v250
AW a65,v251
AR v254,a66
v_sub_f32 v254, v254, v249
v_mul_f32 v254, v254, v250
AW a66,v254
AR v251,a67
v_sub_f32 v251, v251, v249
v_mul_f32 v251, v251, v250
AW a67,v251
AR v254,a68
v_sub_f32 v254, v254, v249
v_mul_f32 v254, v254, v250
AW a68,v254
AR v251,a69
v_sub_f32 v251, v251, v249
v_mul_f32 v251, v251, v250
AW a69,v251
AR v254,a70
v_sub_f32 v254, v254, v249
v_mul_f32 v254, v254, v250
AW a70,v254
AR v251,a71
v_sub_f32 v251, v251, v249
v_mul_f32 v251, v251, v250
AW a71,v251
AR v254,a96
v_sub_f32 v254, v254, v249
v_mul_f32 v254, v254, v250
AW a96,v254
AR v251,a97
v_sub_f32 v251, v251, v249
v_mul_f32 v251, v251, v250
AW a97,v251
AR v254,a98
v_sub_f32 v254, v254, v249
v_mul_f32 v254, v254, v250
AW a98,v254
AR v251,a99
v_sub_f32 v251, v251, v249
v_mul_f32 v251, v251, v250
AW a99,v251
AR v254,a100
v_sub_f32 v254, v254, v249
v_mul_f32 v254, v254, v250
AW a100,v254
AR v251,a101
v_sub_f32 v251, v251, v249
v_mul_f32 v251, v251, v250
AW a101,v251
AR v254,a102
v_sub_f32 v254, v254, v249
v_mul_f32 v254, v254, v250
AW a102,v254
AR v251,a103
v_sub_f32 v251, v251, v249
v_mul_f32 v251, v251, v250
AW a103,v251
v_max3_f32 v249, v72, v73, v74
v_max3_f32 v251, v104, v105, v106
v_max3_f32 v249, v249, v75, v76
v_max3_f32 v251, v251, v107, v108
v_max3_f32 v249, v249, v77, v78
v_max3_f32 v251, v251, v109, v110
v_max3_f32 v249, v249, v79, v111
v_max_f32 v249, v249, v251
v_mov_b32 v251, v249
s_nop 1
v_permlane16_swap_b32 v249, v251
v_max_f32 v249, v249, v251
v_mov_b32 v251, v249
s_nop 1
v_permlane32_swap_b32 v249, v251
v_max_f32 v249, v249, v251
v_log_f32 v250, v245
s_nop 0
v_max3_f32 v249, 0, v249, v250
v_sub_f32 v250, 0, v249
EX v250,v250
v_sub_f32 v132, v132, v249
v_sub_f32 v133, v133, v249
v_sub_f32 v134, v134, v249
v_sub_f32 v135, v135, v249
v_sub_f32 v8, v8, v249
v_sub_f32 v72, v72, v249
v_sub_f32 v9, v9, v249
v_sub_f32 v73, v73, v249
v_sub_f32 v10, v10, v249
v_sub_f32 v74, v74, v249
v_sub_f32 v11, v11, v249
v_sub_f32 v75, v75, v249
v_sub_f32 v12, v12, v249
v_sub_f32 v76, v76, v249
v_sub_f32 v13, v13, v249
v_sub_f32 v77, v77, v249
v_sub_f32 v14, v14, v249
v_sub_f32 v78, v78, v249
v_sub_f32 v15, v15, v249
v_sub_f32 v79, v79, v249
v_sub_f32 v40, v40, v249
v_sub_f32 v104, v104, v249
v_sub_f32 v41, v41, v249
v_sub_f32 v105, v105, v249
v_sub_f32 v42, v42, v249
v_sub_f32 v106, v106, v249
v_sub_f32 v43, v43, v249
v_sub_f32 v107, v107, v249
v_sub_f32 v44, v44, v249
v_sub_f32 v108, v108, v249
v_sub_f32 v45, v45, v249
v_sub_f32 v109, v109, v249
v_sub_f32 v46, v46, v249
v_sub_f32 v110, v110, v249
v_sub_f32 v47, v47, v249
v_sub_f32 v111, v111, v249
v_mul_f32 v148, v245, v250
v_mov_b32 v149, 0
v_bfe_u32 v249, v241, 8, 8
v_lshl_or_b32 v249, v249, 1, 1
v_lshlrev_b32 v249, 22, v249
v_and_b32 v241, 0xffff00ff, v241
AR v254,a8
v_sub_f32 v254, v254, v249
v_mul_f32 v254, v254, v250
AW a8,v254
AR v251,a9
v_sub_f32 v251, v251, v249
v_mul_f32 v251, v251, v250
AW a9,v251
AR v254,a10
v_sub_f32 v254, v254, v249
v_mul_f32 v254, v254, v250
AW a10,v254
AR v251,a11
v_sub_f32 v251, v251, v249
v_mul_f32 v251, v251, v250
AW a11,v251
AR v254,a12
v_sub_f32 v254, v254, v249
v_mul_f32 v254, v254, v250
AW a12,v254
AR v251,a13
v_sub_f32 v251, v251, v249
v_mul_f32 v251, v251, v250
AW a13,v251
AR v254,a14
v_sub_f32 v254, v254, v249
v_mul_f32 v254, v254, v250
AW a14,v254
AR v251,a15
v_sub_f32 v251, v251, v249
v_mul_f32 v251, v251, v250
AW a15,v251
AR v254,a40
v_sub_f32 v254, v254, v249
v_mul_f32 v254, v254, v250
AW a40,v254
AR v251,a41
v_sub_f32 v251, v251, v249
v_mul_f32 v251, v251, v250
AW a41,v251
AR v254,a42
v_sub_f32 v254, v254, v249
v_mul_f32 v254, v254, v250
AW a42,v254
AR v251,a43
v_sub_f32 v251, v251, v249
)ASM"
R"ASM(
v_mul_f32 v251, v251, v250
AW a43,v251
AR v254,a44
v_sub_f32 v254, v254, v249
v_mul_f32 v254, v254, v250
AW a44,v254
AR v251,a45
v_sub_f32 v251, v251, v249
v_mul_f32 v251, v251, v250
AW a45,v251
AR v254,a46
v_sub_f32 v254, v254, v249
v_mul_f32 v254, v254, v250
AW a46,v254
AR v251,a47
v_sub_f32 v251, v251, v249
v_mul_f32 v251, v251, v250
AW a47,v251
AR v254,a72
v_sub_f32 v254, v254, v249
v_mul_f32 v254, v254, v250
AW a72,v254
AR v251,a73
v_sub_f32 v251, v251, v249
v_mul_f32 v251, v251, v250
AW a73,v251
AR v254,a74
v_sub_f32 v254, v254, v249
v_mul_f32 v254, v254, v250
AW a74,v254
AR v251,a75
v_sub_f32 v251, v251, v249
v_mul_f32 v251, v251, v250
AW a75,v251
AR v254,a76
v_sub_f32 v254, v254, v249
v_mul_f32 v254, v254, v250
AW a76,v254
AR v251,a77
v_sub_f32 v251, v251, v249
v_mul_f32 v251, v251, v250
AW a77,v251
AR v254,a78
v_sub_f32 v254, v254, v249
v_mul_f32 v254, v254, v250
AW a78,v254
AR v251,a79
v_sub_f32 v251, v251, v249
v_mul_f32 v251, v251, v250
AW a79,v251
AR v254,a104
v_sub_f32 v254, v254, v249
v_mul_f32 v254, v254, v250
AW a104,v254
AR v251,a105
v_sub_f32 v251, v251, v249
v_mul_f32 v251, v251, v250
AW a105,v251
AR v254,a106
v_sub_f32 v254, v254, v249
v_mul_f32 v254, v254, v250
AW a106,v254
AR v251,a107
v_sub_f32 v251, v251, v249
v_mul_f32 v251, v251, v250
AW a107,v251
AR v254,a108
v_sub_f32 v254, v254, v249
v_mul_f32 v254, v254, v250
AW a108,v254
AR v251,a109
v_sub_f32 v251, v251, v249
v_mul_f32 v251, v251, v250
AW a109,v251
AR v254,a110
v_sub_f32 v254, v254, v249
v_mul_f32 v254, v254, v250
AW a110,v254
AR v251,a111
v_sub_f32 v251, v251, v249
v_mul_f32 v251, v251, v250
AW a111,v251
v_max3_f32 v249, v80, v81, v82
v_max3_f32 v251, v112, v113, v114
v_max3_f32 v249, v249, v83, v84
v_max3_f32 v251, v251, v115, v116
v_max3_f32 v249, v249, v85, v86
v_max3_f32 v251, v251, v117, v118
v_max3_f32 v249, v249, v87, v119
v_max_f32 v249, v249, v251
v_mov_b32 v251, v249
s_nop 1
v_permlane16_swap_b32 v249, v251
v_max_f32 v249, v249, v251
v_mov_b32 v251, v249
s_nop 1
v_permlane32_swap_b32 v249, v251
v_max_f32 v249, v249, v251
v_log_f32 v250, v246
s_nop 0
v_max3_f32 v249, 0, v249, v250
v_sub_f32 v250, 0, v249
EX v250,v250
v_sub_f32 v136, v136, v249
v_sub_f32 v137, v137, v249
v_sub_f32 v138, v138, v249
v_sub_f32 v139, v139, v249
v_sub_f32 v16, v16, v249
v_sub_f32 v80, v80, v249
v_sub_f32 v17, v17, v249
v_sub_f32 v81, v81, v249
v_sub_f32 v18, v18, v249
v_sub_f32 v82, v82, v249
v_sub_f32 v19, v19, v249
v_sub_f32 v83, v83, v249
v_sub_f32 v20, v20, v249
v_sub_f32 v84, v84, v249
v_sub_f32 v21, v21, v249
v_sub_f32 v85, v85, v249
v_sub_f32 v22, v22, v249
v_sub_f32 v86, v86, v249
v_sub_f32 v23, v23, v249
v_sub_f32 v87, v87, v249
v_sub_f32 v48, v48, v249
v_sub_f32 v112, v112, v249
v_sub_f32 v49, v49, v249
v_sub_f32 v113, v113, v249
v_sub_f32 v50, v50, v249
v_sub_f32 v114, v114, v249
v_sub_f32 v51, v51, v249
v_sub_f32 v115, v115, v249
v_sub_f32 v52, v52, v249
v_sub_f32 v116, v116, v249
v_sub_f32 v53, v53, v249
v_sub_f32 v117, v117, v249
v_sub_f32 v54, v54, v249
v_sub_f32 v118, v118, v249
v_sub_f32 v55, v55, v249
v_sub_f32 v119, v119, v249
v_mul_f32 v152, v246, v250
v_mov_b32 v153, 0
v_bfe_u32 v249, v241, 16, 8
v_lshl_or_b32 v249, v249, 1, 1
v_lshlrev_b32 v249, 22, v249
v_and_b32 v241, 0xff00ffff, v241
AR v254,a16
v_sub_f32 v254, v254, v249
v_mul_f32 v254, v254, v250
AW a16,v254
AR v251,a17
v_sub_f32 v251, v251, v249
v_mul_f32 v251, v251, v250
AW a17,v251
AR v254,a18
v_sub_f32 v254, v254, v249
v_mul_f32 v254, v254, v250
AW a18,v254
AR v251,a19
v_sub_f32 v251, v251, v249
v_mul_f32 v251, v251, v250
AW a19,v251
AR v254,a20
v_sub_f32 v254, v254, v249
v_mul_f32 v254, v254, v250
AW a20,v254
AR v251,a21
v_sub_f32 v251, v251, v249
v_mul_f32 v251, v251, v250
AW a21,v251
AR v254,a22
v_sub_f32 v254, v254, v249
v_mul_f32 v254, v254, v250
AW a22,v254
AR v251,a23
v_sub_f32 v251, v251, v249
v_mul_f32 v251, v251, v250
AW a23,v251
AR v254,a48
v_sub_f32 v254, v254, v249
v_mul_f32 v254, v254, v250
AW a48,v254
AR v251,a49
v_sub_f32 v251, v251, v249
v_mul_f32 v251, v251, v250
AW a49,v251
AR v254,a50
v_sub_f32 v254, v254, v249
v_mul_f32 v254, v254, v250
AW a50,v254
AR v251,a51
v_sub_f32 v251, v251, v249
v_mul_f32 v251, v251, v250
AW a51,v251
AR v254,a52
v_sub_f32 v254, v254, v249
v_mul_f32 v254, v254, v250
AW a52,v254
AR v251,a53
v_sub_f32 v251, v251, v249
v_mul_f32 v251, v251, v250
AW a53,v251
AR v254,a54
v_sub_f32 v254, v254, v249
v_mul_f32 v254, v254, v250
AW a54,v254
AR v251,a55
v_sub_f32 v251, v251, v249
v_mul_f32 v251, v251, v250
AW a55,v251
AR v254,a80
v_sub_f32 v254, v254, v249
v_mul_f32 v254, v254, v250
AW a80,v254
AR v251,a81
v_sub_f32 v251, v251, v249
v_mul_f32 v251, v251, v250
AW a81,v251
AR v254,a82
v_sub_f32 v254, v254, v249
v_mul_f32 v254, v254, v250
AW a82,v254
AR v251,a83
v_sub_f32 v251, v251, v249
v_mul_f32 v251, v251, v250
AW a83,v251
AR v254,a84
v_sub_f32 v254, v254, v249
v_mul_f32 v254, v254, v250
AW a84,v254
AR v251,a85
v_sub_f32 v251, v251, v249
v_mul_f32 v251, v251, v250
AW a85,v251
AR v254,a86
v_sub_f32 v254, v254, v249
v_mul_f32 v254, v254, v250
AW a86,v254
AR v251,a87
v_sub_f32 v251, v251, v249
v_mul_f32 v251, v251, v250
AW a87,v251
AR v254,a112
v_sub_f32 v254, v254, v249
v_mul_f32 v254, v254, v250
AW a112,v254
AR v251,a113
v_sub_f32 v251, v251, v249
v_mul_f32 v251, v251, v250
AW a113,v251
AR v254,a114
v_sub_f32 v254, v254, v249
v_mul_f32 v254, v254, v250
AW a114,v254
AR v251,a115
v_sub_f32 v251, v251, v249
v_mul_f32 v251, v251, v250
AW a115,v251
AR v254,a116
v_sub_f32 v254, v254, v249
v_mul_f32 v254, v254, v250
AW a116,v254
AR v251,a117
v_sub_f32 v251, v251, v249
v_mul_f32 v251, v251, v250
AW a117,v251
AR v254,a118
v_sub_f32 v254, v254, v249
v_mul_f32 v254, v254, v250
AW a118,v254
AR v251,a119
v_sub_f32 v251, v251, v249
v_mul_f32 v251, v251, v250
AW a119,v251
v_max3_f32 v249, v88, v89, v90
v_max3_f32 v251, v120, v121, v122
v_max3_f32 v249, v249, v91, v92
v_max3_f32 v251, v251, v123, v124
v_max3_f32 v249, v249, v93, v94
v_max3_f32 v251, v251, v125, v126
v_max3_f32 v249, v249, v95, v127
v_max_f32 v249, v249, v251
v_mov_b32 v251, v249
s_nop 1
v_permlane16_swap_b32 v249, v251
v_max_f32 v249, v249, v251
v_mov_b32 v251, v249
s_nop 1
v_permlane32_swap_b32 v249, v251
v_max_f32 v249, v249, v251
v_log_f32 v250, v247
s_nop 0
v_max3_f32 v249, 0, v249, v250
v_sub_f32 v250, 0, v249
EX v250,v250
v_sub_f32 v140, v140, v249
v_sub_f32 v141, v141, v249
v_sub_f32 v142, v142, v249
v_sub_f32 v143, v143, v249
v_sub_f32 v24, v24, v249
v_sub_f32 v88, v88, v249
v_sub_f32 v25, v25, v249
v_sub_f32 v89, v89, v249
v_sub_f32 v26, v26, v249
v_sub_f32 v90, v90, v249
v_sub_f32 v27, v27, v249
v_sub_f32 v91, v91, v249
v_sub_f32 v28, v28, v249
v_sub_f32 v92, v92, v249
v_sub_f32 v29, v29, v249
v_sub_f32 v93, v93, v249
v_sub_f32 v30, v30, v249
v_sub_f32 v94, v94, v249
v_sub_f32 v31, v31, v249
v_sub_f32 v95, v95, v249
v_sub_f32 v56, v56, v249
v_sub_f32 v120, v120, v249
v_sub_f32 v57, v57, v249
v_sub_f32 v121, v121, v249
v_sub_f32 v58, v58, v249
v_sub_f32 v122, v122, v249
v_sub_f32 v59, v59, v249
v_sub_f32 v123, v123, v249
v_sub_f32 v60, v60, v249
v_sub_f32 v124, v124, v249
v_sub_f32 v61, v61, v249
v_sub_f32 v125, v125, v249
v_sub_f32 v62, v62, v249
v_sub_f32 v126, v126, v249
v_sub_f32 v63, v63, v249
v_sub_f32 v127, v127, v249
v_mul_f32 v156, v247, v250
v_mov_b32 v157, 0
v_bfe_u32 v249, v241, 24, 8
v_lshl_or_b32 v249, v249, 1, 1
v_lshlrev_b32 v249, 22, v249
v_and_b32 v241, 0xffffff, v241
AR v254,a24
v_sub_f32 v254, v254, v249
v_mul_f32 v254, v254, v250
AW a24,v254
AR v251,a25
v_sub_f32 v251, v251, v249
v_mul_f32 v251, v251, v250
AW a25,v251
AR v254,a26
v_sub_f32 v254, v254, v249
v_mul_f32 v254, v254, v250
AW a26,v254
AR v251,a27
v_sub_f32 v251, v251, v249
v_mul_f32 v251, v251, v250
AW a27,v251
AR v254,a28
v_sub_f32 v254, v254, v249
v_mul_f32 v254, v254, v250
AW a28,v254
AR v251,a29
v_sub_f32 v251, v251, v249
v_mul_f32 v251, v251, v250
AW a29,v251
AR v254,a30
v_sub_f32 v254, v254, v249
v_mul_f32 v254, v254, v250
AW a30,v254
AR v251,a31
v_sub_f32 v251, v251, v249
v_mul_f32 v251, v251, v250
AW a31,v251
AR v254,a56
v_sub_f32 v254, v254, v249
v_mul_f32 v254, v254, v250
AW a56,v254
AR v251,a57
v_sub_f32 v251, v251, v249
v_mul_f32 v251, v251, v250
AW a57,v251
AR v254,a58
v_sub_f32 v254, v254, v249
v_mul_f32 v254, v254, v250
AW a58,v254
AR v251,a59
v_sub_f32 v251, v251, v249
v_mul_f32 v251, v251, v250
AW a59,v251
AR v254,a60
v_sub_f32 v254, v254, v249
v_mul_f32 v254, v254, v250
AW a60,v254
AR v251,a61
v_sub_f32 v251, v251, v249
v_mul_f32 v251, v251, v250
AW a61,v251
AR v254,a62
v_sub_f32 v254, v254, v249
v_mul_f32 v254, v254, v250
AW a62,v254
AR v251,a63
v_sub_f32 v251, v251, v249
v_mul_f32 v251, v251, v250
AW a63,v251
AR v254,a88
v_sub_f32 v254, v254, v249
v_mul_f32 v254, v254, v250
AW a88,v254
AR v251,a89
v_sub_f32 v251, v251, v249
v_mul_f32 v251, v251, v250
AW a89,v251
AR v254,a90
v_sub_f32 v254, v254, v249
v_mul_f32 v254, v254, v250
AW a90,v254
AR v251,a91
v_sub_f32 v251, v251, v249
v_mul_f32 v251, v251, v250
AW a91,v251
AR v254,a92
v_sub_f32 v254, v254, v249
v_mul_f32 v254, v254, v250
AW a92,v254
AR v251,a93
v_sub_f32 v251, v251, v249
v_mul_f32 v251, v251, v250
AW a93,v251
AR v254,a94
v_sub_f32 v254, v254, v249
v_mul_f32 v254, v254, v250
AW a94,v254
AR v251,a95
v_sub_f32 v251, v251, v249
v_mul_f32 v251, v251, v250
AW a95,v251
AR v254,a120
v_sub_f32 v254, v254, v249
v_mul_f32 v254, v254, v250
AW a120,v254
AR v251,a121
v_sub_f32 v251, v251, v249
v_mul_f32 v251, v251, v250
AW a121,v251
AR v254,a122
v_sub_f32 v254, v254, v249
v_mul_f32 v254, v254, v250
AW a122,v254
AR v251,a123
v_sub_f32 v251, v251, v249
v_mul_f32 v251, v251, v250
AW a123,v251
AR v254,a124
v_sub_f32 v254, v254, v249
v_mul_f32 v254, v254, v250
AW a124,v254
AR v251,a125
v_sub_f32 v251, v251, v249
v_mul_f32 v251, v251, v250
AW a125,v251
AR v254,a126
v_sub_f32 v254, v254, v249
v_mul_f32 v254, v254, v250
AW a126,v254
AR v251,a127
v_sub_f32 v251, v251, v249
v_mul_f32 v251, v251, v250
AW a127,v251
EX v64,v64
EX v65,v65
EX v66,v66
EX v67,v67
EX v68,v68
EX v69,v69
EX v70,v70
EX v71,v71
EX v72,v72
EX v73,v73
EX v74,v74
EX v75,v75
EX v76,v76
EX v77,v77
EX v78,v78
EX v79,v79
EX v80,v80
EX v81,v81
EX v82,v82
EX v83,v83
EX v84,v84
EX v85,v85
EX v86,v86
EX v87,v87
EX v88,v88
EX v89,v89
EX v90,v90
EX v91,v91
EX v92,v92
EX v93,v93
EX v94,v94
EX v95,v95
EX v96,v96
EX v97,v97
EX v98,v98
EX v99,v99
EX v100,v100
EX v101,v101
EX v102,v102
EX v103,v103
EX v104,v104
EX v105,v105
EX v106,v106
EX v107,v107
EX v108,v108
EX v109,v109
EX v110,v110
EX v111,v111
EX v112,v112
EX v113,v113
EX v114,v114
EX v115,v115
EX v116,v116
EX v117,v117
EX v118,v118
EX v119,v119
EX v120,v120
EX v121,v121
EX v122,v122
EX v123,v123
EX v124,v124
EX v125,v125
EX v126,v126
EX v127,v127
CV v64,v64,v65
CV v65,v66,v67
CV v66,v68,v69
CV v67,v70,v71
CV v72,v72,v73
CV v73,v74,v75
CV v74,v76,v77
CV v75,v78,v79
CV v80,v80,v81
CV v81,v82,v83
CV v82,v84,v85
CV v83,v86,v87
CV v88,v88,v89
CV v89,v90,v91
CV v90,v92,v93
CV v91,v94,v95
CV v68,v96,v97
CV v69,v98,v99
CV v70,v100,v101
CV v71,v102,v103
CV v76,v104,v105
CV v77,v106,v107
CV v78,v108,v109
CV v79,v110,v111
CV v84,v112,v113
CV v85,v114,v115
CV v86,v116,v117
CV v87,v118,v119
CV v92,v120,v121
CV v93,v122,v123
CV v94,v124,v125
CV v95,v126,v127
v_smfmac_f32_16x16x64_bf16 v[144:147], v[236:239], v[64:71], v234
v_smfmac_f32_16x16x64_bf16 v[148:151], v[236:239], v[72:79], v234
v_smfmac_f32_16x16x64_bf16 v[152:155], v[236:239], v[80:87], v234
v_smfmac_f32_16x16x64_bf16 v[156:159], v[236:239], v[88:95], v234
s_nop 9
s_branch LRET_%=
LRARE004_%=:
s_nop 15
s_waitcnt vmcnt(0) lgkmcnt(0)
v_add_u32 v249, s74, v224
RD a[192:195],v249 offset:0
RD a[208:211],v249 offset:4096
RD a[224:227],v249 offset:8192
RD a[240:243],v249 offset:12288
v_add_u32 v250, s74, v225
RD a[196:199],v250 offset:0
RD a[212:215],v250 offset:4096
RD a[228:231],v250 offset:8192
RD a[244:247],v250 offset:12288
v_add_u32 v251, s74, v226
RD a[200:203],v251 offset:0
RD a[216:219],v251 offset:4096
RD a[232:235],v251 offset:8192
RD a[248:251],v251 offset:12288
v_add_u32 v254, s74, v227
RD a[204:207],v254 offset:0
RD a[220:223],v254 offset:4096
)ASM"
R"ASM(
RD a[236:239],v254 offset:8192
RD a[252:255],v254 offset:12288
s_waitcnt lgkmcnt(0)
MM v[0:3],a[192:195],v[160:163],v[128:131]
MM v[0:3],a[196:199],v[176:179],v[0:3]
MM v[0:3],a[200:203],v[192:195],v[0:3]
MM v[0:3],a[204:207],v[208:211],v[0:3]
MM v[4:7],a[220:223],v[208:211],v[128:131]
MM v[4:7],a[216:219],v[192:195],v[4:7]
MM v[4:7],a[212:215],v[176:179],v[4:7]
MM v[4:7],a[208:211],v[160:163],v[4:7]
MM v[12:15],a[208:211],v[164:167],v[132:135]
MM v[12:15],a[212:215],v[180:183],v[12:15]
MM v[12:15],a[216:219],v[196:199],v[12:15]
MM v[12:15],a[220:223],v[212:215],v[12:15]
MM v[8:11],a[204:207],v[212:215],v[132:135]
MM v[8:11],a[200:203],v[196:199],v[8:11]
MM v[8:11],a[196:199],v[180:183],v[8:11]
MM v[8:11],a[192:195],v[164:167],v[8:11]
MM v[16:19],a[192:195],v[168:171],v[136:139]
MM v[16:19],a[196:199],v[184:187],v[16:19]
MM v[16:19],a[200:203],v[200:203],v[16:19]
MM v[16:19],a[204:207],v[216:219],v[16:19]
MM v[20:23],a[220:223],v[216:219],v[136:139]
MM v[20:23],a[216:219],v[200:203],v[20:23]
MM v[20:23],a[212:215],v[184:187],v[20:23]
MM v[20:23],a[208:211],v[168:171],v[20:23]
MM v[28:31],a[208:211],v[172:175],v[140:143]
MM v[28:31],a[212:215],v[188:191],v[28:31]
MM v[28:31],a[216:219],v[204:207],v[28:31]
MM v[28:31],a[220:223],v[220:223],v[28:31]
MM v[24:27],a[204:207],v[220:223],v[140:143]
MM v[24:27],a[200:203],v[204:207],v[24:27]
MM v[24:27],a[196:199],v[188:191],v[24:27]
MM v[24:27],a[192:195],v[172:175],v[24:27]
MM v[32:35],a[224:227],v[160:163],v[128:131]
MM v[32:35],a[228:231],v[176:179],v[32:35]
MM v[32:35],a[232:235],v[192:195],v[32:35]
MM v[32:35],a[236:239],v[208:211],v[32:35]
MM v[36:39],a[252:255],v[208:211],v[128:131]
MM v[36:39],a[248:251],v[192:195],v[36:39]
MM v[36:39],a[244:247],v[176:179],v[36:39]
MM v[36:39],a[240:243],v[160:163],v[36:39]
MM v[44:47],a[240:243],v[164:167],v[132:135]
MM v[44:47],a[244:247],v[180:183],v[44:47]
MM v[44:47],a[248:251],v[196:199],v[44:47]
MM v[44:47],a[252:255],v[212:215],v[44:47]
MM v[40:43],a[236:239],v[212:215],v[132:135]
MM v[40:43],a[232:235],v[196:199],v[40:43]
MM v[40:43],a[228:231],v[180:183],v[40:43]
MM v[40:43],a[224:227],v[164:167],v[40:43]
MM v[48:51],a[224:227],v[168:171],v[136:139]
MM v[48:51],a[228:231],v[184:187],v[48:51]
MM v[48:51],a[232:235],v[200:203],v[48:51]
MM v[48:51],a[236:239],v[216:219],v[48:51]
MM v[52:55],a[252:255],v[216:219],v[136:139]
MM v[52:55],a[248:251],v[200:203],v[52:55]
MM v[52:55],a[244:247],v[184:187],v[52:55]
MM v[52:55],a[240:243],v[168:171],v[52:55]
MM v[60:63],a[240:243],v[172:175],v[140:143]
MM v[60:63],a[244:247],v[188:191],v[60:63]
MM v[60:63],a[248:251],v[204:207],v[60:63]
MM v[60:63],a[252:255],v[220:223],v[60:63]
MM v[56:59],a[236:239],v[220:223],v[140:143]
MM v[56:59],a[232:235],v[204:207],v[56:59]
MM v[56:59],a[228:231],v[188:191],v[56:59]
MM v[56:59],a[224:227],v[172:175],v[56:59]
s_nop 15
v_add_u32 v249, s75, v224
RD a[192:195],v249 offset:0
RD a[208:211],v249 offset:4096
RD a[224:227],v249 offset:8192
RD a[240:243],v249 offset:12288
v_add_u32 v250, s75, v225
RD a[196:199],v250 offset:0
RD a[212:215],v250 offset:4096
RD a[228:231],v250 offset:8192
RD a[244:247],v250 offset:12288
v_add_u32 v251, s75, v226
RD a[200:203],v251 offset:0
RD a[216:219],v251 offset:4096
RD a[232:235],v251 offset:8192
RD a[248:251],v251 offset:12288
v_add_u32 v254, s75, v227
RD a[204:207],v254 offset:0
RD a[220:223],v254 offset:4096
RD a[236:239],v254 offset:8192
RD a[252:255],v254 offset:12288
s_waitcnt lgkmcnt(0)
v_max3_f32 v249, v0, v1, v2
v_max3_f32 v251, v32, v33, v34
v_max3_f32 v249, v249, v3, v4
v_max3_f32 v251, v251, v35, v36
v_max3_f32 v249, v249, v5, v6
v_max3_f32 v251, v251, v37, v38
v_max3_f32 v249, v249, v7, v39
v_max_f32 v249, v249, v251
v_mov_b32 v251, v249
s_nop 1
v_permlane16_swap_b32 v249, v251
v_max_f32 v249, v249, v251
v_mov_b32 v251, v249
s_nop 1
v_permlane32_swap_b32 v249, v251
v_max_f32 v249, v249, v251
v_log_f32 v250, v244
s_nop 0
v_max3_f32 v249, 0, v249, v250
v_sub_f32 v250, 0, v249
EX v250,v250
v_sub_f32 v128, v128, v249
v_sub_f32 v129, v129, v249
v_sub_f32 v130, v130, v249
v_sub_f32 v131, v131, v249
v_sub_f32 v64, v64, v249
v_sub_f32 v0, v0, v249
v_sub_f32 v65, v65, v249
v_sub_f32 v1, v1, v249
v_sub_f32 v66, v66, v249
v_sub_f32 v2, v2, v249
v_sub_f32 v67, v67, v249
v_sub_f32 v3, v3, v249
v_sub_f32 v68, v68, v249
v_sub_f32 v4, v4, v249
v_sub_f32 v69, v69, v249
v_sub_f32 v5, v5, v249
v_sub_f32 v70, v70, v249
v_sub_f32 v6, v6, v249
v_sub_f32 v71, v71, v249
v_sub_f32 v7, v7, v249
v_sub_f32 v96, v96, v249
v_sub_f32 v32, v32, v249
v_sub_f32 v97, v97, v249
v_sub_f32 v33, v33, v249
v_sub_f32 v98, v98, v249
v_sub_f32 v34, v34, v249
v_sub_f32 v99, v99, v249
v_sub_f32 v35, v35, v249
v_sub_f32 v100, v100, v249
v_sub_f32 v36, v36, v249
v_sub_f32 v101, v101, v249
v_sub_f32 v37, v37, v249
v_sub_f32 v102, v102, v249
v_sub_f32 v38, v38, v249
v_sub_f32 v103, v103, v249
v_sub_f32 v39, v39, v249
v_mul_f32 v144, v244, v250
v_mov_b32 v145, 0
v_bfe_u32 v249, v241, 0, 8
v_lshl_or_b32 v249, v249, 1, 1
v_lshlrev_b32 v249, 22, v249
v_and_b32 v241, 0xffffff00, v241
AR v254,a0
v_sub_f32 v254, v254, v249
v_mul_f32 v254, v254, v250
AW a0,v254
AR v251,a1
v_sub_f32 v251, v251, v249
v_mul_f32 v251, v251, v250
AW a1,v251
AR v254,a2
v_sub_f32 v254, v254, v249
v_mul_f32 v254, v254, v250
AW a2,v254
AR v251,a3
v_sub_f32 v251, v251, v249
v_mul_f32 v251, v251, v250
AW a3,v251
AR v254,a4
v_sub_f32 v254, v254, v249
v_mul_f32 v254, v254, v250
AW a4,v254
AR v251,a5
v_sub_f32 v251, v251, v249
v_mul_f32 v251, v251, v250
AW a5,v251
AR v254,a6
v_sub_f32 v254, v254, v249
v_mul_f32 v254, v254, v250
AW a6,v254
AR v251,a7
v_sub_f32 v251, v251, v249
v_mul_f32 v251, v251, v250
AW a7,v251
AR v254,a32
v_sub_f32 v254, v254, v249
v_mul_f32 v254, v254, v250
AW a32,v254
AR v251,a33
v_sub_f32 v251, v251, v249
v_mul_f32 v251, v251, v250
AW a33,v251
AR v254,a34
v_sub_f32 v254, v254, v249
v_mul_f32 v254, v254, v250
AW a34,v254
AR v251,a35
v_sub_f32 v251, v251, v249
v_mul_f32 v251, v251, v250
AW a35,v251
AR v254,a36
v_sub_f32 v254, v254, v249
v_mul_f32 v254, v254, v250
AW a36,v254
AR v251,a37
v_sub_f32 v251, v251, v249
v_mul_f32 v251, v251, v250
AW a37,v251
AR v254,a38
v_sub_f32 v254, v254, v249
v_mul_f32 v254, v254, v250
AW a38,v254
AR v251,a39
v_sub_f32 v251, v251, v249
v_mul_f32 v251, v251, v250
AW a39,v251
AR v254,a64
v_sub_f32 v254, v254, v249
v_mul_f32 v254, v254, v250
AW a64,v254
AR v251,a65
v_sub_f32 v251, v251, v249
v_mul_f32 v251, v251, v250
AW a65,v251
AR v254,a66
v_sub_f32 v254, v254, v249
v_mul_f32 v254, v254, v250
AW a66,v254
AR v251,a67
v_sub_f32 v251, v251, v249
v_mul_f32 v251, v251, v250
AW a67,v251
AR v254,a68
v_sub_f32 v254, v254, v249
v_mul_f32 v254, v254, v250
AW a68,v254
AR v251,a69
v_sub_f32 v251, v251, v249
v_mul_f32 v251, v251, v250
AW a69,v251
AR v254,a70
v_sub_f32 v254, v254, v249
v_mul_f32 v254, v254, v250
AW a70,v254
AR v251,a71
v_sub_f32 v251, v251, v249
v_mul_f32 v251, v251, v250
AW a71,v251
AR v254,a96
v_sub_f32 v254, v254, v249
v_mul_f32 v254, v254, v250
AW a96,v254
AR v251,a97
v_sub_f32 v251, v251, v249
v_mul_f32 v251, v251, v250
AW a97,v251
AR v254,a98
v_sub_f32 v254, v254, v249
v_mul_f32 v254, v254, v250
AW a98,v254
AR v251,a99
v_sub_f32 v251, v251, v249
v_mul_f32 v251, v251, v250
AW a99,v251
AR v254,a100
v_sub_f32 v254, v254, v249
v_mul_f32 v254, v254, v250
AW a100,v254
AR v251,a101
v_sub_f32 v251, v251, v249
v_mul_f32 v251, v251, v250
AW a101,v251
AR v254,a102
v_sub_f32 v254, v254, v249
v_mul_f32 v254, v254, v250
AW a102,v254
AR v251,a103
v_sub_f32 v251, v251, v249
v_mul_f32 v251, v251, v250
AW a103,v251
v_max3_f32 v249, v8, v9, v10
v_max3_f32 v251, v40, v41, v42
v_max3_f32 v249, v249, v11, v12
v_max3_f32 v251, v251, v43, v44
v_max3_f32 v249, v249, v13, v14
v_max3_f32 v251, v251, v45, v46
v_max3_f32 v249, v249, v15, v47
v_max_f32 v249, v249, v251
v_mov_b32 v251, v249
s_nop 1
v_permlane16_swap_b32 v249, v251
v_max_f32 v249, v249, v251
v_mov_b32 v251, v249
s_nop 1
v_permlane32_swap_b32 v249, v251
v_max_f32 v249, v249, v251
v_log_f32 v250, v245
s_nop 0
v_max3_f32 v249, 0, v249, v250
v_sub_f32 v250, 0, v249
EX v250,v250
v_sub_f32 v132, v132, v249
v_sub_f32 v133, v133, v249
v_sub_f32 v134, v134, v249
v_sub_f32 v135, v135, v249
v_sub_f32 v72, v72, v249
v_sub_f32 v8, v8, v249
v_sub_f32 v73, v73, v249
v_sub_f32 v9, v9, v249
v_sub_f32 v74, v74, v249
v_sub_f32 v10, v10, v249
v_sub_f32 v75, v75, v249
v_sub_f32 v11, v11, v249
v_sub_f32 v76, v76, v249
v_sub_f32 v12, v12, v249
v_sub_f32 v77, v77, v249
v_sub_f32 v13, v13, v249
v_sub_f32 v78, v78, v249
v_sub_f32 v14, v14, v249
v_sub_f32 v79, v79, v249
v_sub_f32 v15, v15, v249
v_sub_f32 v104, v104, v249
v_sub_f32 v40, v40, v249
v_sub_f32 v105, v105, v249
v_sub_f32 v41, v41, v249
v_sub_f32 v106, v106, v249
v_sub_f32 v42, v42, v249
v_sub_f32 v107, v107, v249
v_sub_f32 v43, v43, v249
v_sub_f32 v108, v108, v249
v_sub_f32 v44, v44, v249
v_sub_f32 v109, v109, v249
v_sub_f32 v45, v45, v249
v_sub_f32 v110, v110, v249
v_sub_f32 v46, v46, v249
v_sub_f32 v111, v111, v249
v_sub_f32 v47, v47, v249
v_mul_f32 v148, v245, v250
v_mov_b32 v149, 0
v_bfe_u32 v249, v241, 8, 8
v_lshl_or_b32 v249, v249, 1, 1
v_lshlrev_b32 v249, 22, v249
v_and_b32 v241, 0xffff00ff, v241
AR v254,a8
v_sub_f32 v254, v254, v249
v_mul_f32 v254, v254, v250
AW a8,v254
AR v251,a9
v_sub_f32 v251, v251, v249
v_mul_f32 v251, v251, v250
AW a9,v251
AR v254,a10
v_sub_f32 v254, v254, v249
v_mul_f32 v254, v254, v250
AW a10,v254
AR v251,a11
v_sub_f32 v251, v251, v249
v_mul_f32 v251, v251, v250
AW a11,v251
AR v254,a12
v_sub_f32 v254, v254, v249
v_mul_f32 v254, v254, v250
AW a12,v254
AR v251,a13
v_sub_f32 v251, v251, v249
v_mul_f32 v251, v251, v250
AW a13,v251
AR v254,a14
v_sub_f32 v254, v254, v249
v_mul_f32 v254, v254, v250
AW a14,v254
AR v251,a15
v_sub_f32 v251, v251, v249
v_mul_f32 v251, v251, v250
AW a15,v251
AR v254,a40
v_sub_f32 v254, v254, v249
v_mul_f32 v254, v254, v250
AW a40,v254
AR v251,a41
v_sub_f32 v251, v251, v249
v_mul_f32 v251, v251, v250
AW a41,v251
AR v254,a42
v_sub_f32 v254, v254, v249
v_mul_f32 v254, v254, v250
AW a42,v254
AR v251,a43
v_sub_f32 v251, v251, v249
v_mul_f32 v251, v251, v250
AW a43,v251
AR v254,a44
v_sub_f32 v254, v254, v249
v_mul_f32 v254, v254, v250
AW a44,v254
AR v251,a45
v_sub_f32 v251, v251, v249
v_mul_f32 v251, v251, v250
AW a45,v251
AR v254,a46
v_sub_f32 v254, v254, v249
v_mul_f32 v254, v254, v250
AW a46,v254
AR v251,a47
v_sub_f32 v251, v251, v249
v_mul_f32 v251, v251, v250
AW a47,v251
AR v254,a72
v_sub_f32 v254, v254, v249
v_mul_f32 v254, v254, v250
AW a72,v254
AR v251,a73
v_sub_f32 v251, v251, v249
v_mul_f32 v251, v251, v250
AW a73,v251
AR v254,a74
v_sub_f32 v254, v254, v249
v_mul_f32 v254, v254, v250
AW a74,v254
AR v251,a75
v_sub_f32 v251, v251, v249
v_mul_f32 v251, v251, v250
AW a75,v251
AR v254,a76
v_sub_f32 v254, v254, v249
v_mul_f32 v254, v254, v250
AW a76,v254
AR v251,a77
v_sub_f32 v251, v251, v249
v_mul_f32 v251, v251, v250
AW a77,v251
AR v254,a78
v_sub_f32 v254, v254, v249
v_mul_f32 v254, v254, v250
AW a78,v254
AR v251,a79
v_sub_f32 v251, v251, v249
v_mul_f32 v251, v251, v250
AW a79,v251
AR v254,a104
v_sub_f32 v254, v254, v249
v_mul_f32 v254, v254, v250
AW a104,v254
AR v251,a105
v_sub_f32 v251, v251, v249
v_mul_f32 v251, v251, v250
AW a105,v251
AR v254,a106
v_sub_f32 v254, v254, v249
v_mul_f32 v254, v254, v250
AW a106,v254
AR v251,a107
v_sub_f32 v251, v251, v249
v_mul_f32 v251, v251, v250
AW a107,v251
AR v254,a108
v_sub_f32 v254, v254, v249
v_mul_f32 v254, v254, v250
AW a108,v254
AR v251,a109
v_sub_f32 v251, v251, v249
v_mul_f32 v251, v251, v250
AW a109,v251
AR v254,a110
v_sub_f32 v254, v254, v249
v_mul_f32 v254, v254, v250
AW a110,v254
AR v251,a111
v_sub_f32 v251, v251, v249
v_mul_f32 v251, v251, v250
AW a111,v251
v_max3_f32 v249, v16, v17, v18
v_max3_f32 v251, v48, v49, v50
v_max3_f32 v249, v249, v19, v20
v_max3_f32 v251, v251, v51, v52
v_max3_f32 v249, v249, v21, v22
v_max3_f32 v251, v251, v53, v54
v_max3_f32 v249, v249, v23, v55
v_max_f32 v249, v249, v251
v_mov_b32 v251, v249
s_nop 1
v_permlane16_swap_b32 v249, v251
v_max_f32 v249, v249, v251
v_mov_b32 v251, v249
s_nop 1
)ASM"
R"ASM(
v_permlane32_swap_b32 v249, v251
v_max_f32 v249, v249, v251
v_log_f32 v250, v246
s_nop 0
v_max3_f32 v249, 0, v249, v250
v_sub_f32 v250, 0, v249
EX v250,v250
v_sub_f32 v136, v136, v249
v_sub_f32 v137, v137, v249
v_sub_f32 v138, v138, v249
v_sub_f32 v139, v139, v249
v_sub_f32 v80, v80, v249
v_sub_f32 v16, v16, v249
v_sub_f32 v81, v81, v249
v_sub_f32 v17, v17, v249
v_sub_f32 v82, v82, v249
v_sub_f32 v18, v18, v249
v_sub_f32 v83, v83, v249
v_sub_f32 v19, v19, v249
v_sub_f32 v84, v84, v249
v_sub_f32 v20, v20, v249
v_sub_f32 v85, v85, v249
v_sub_f32 v21, v21, v249
v_sub_f32 v86, v86, v249
v_sub_f32 v22, v22, v249
v_sub_f32 v87, v87, v249
v_sub_f32 v23, v23, v249
v_sub_f32 v112, v112, v249
v_sub_f32 v48, v48, v249
v_sub_f32 v113, v113, v249
v_sub_f32 v49, v49, v249
v_sub_f32 v114, v114, v249
v_sub_f32 v50, v50, v249
v_sub_f32 v115, v115, v249
v_sub_f32 v51, v51, v249
v_sub_f32 v116, v116, v249
v_sub_f32 v52, v52, v249
v_sub_f32 v117, v117, v249
v_sub_f32 v53, v53, v249
v_sub_f32 v118, v118, v249
v_sub_f32 v54, v54, v249
v_sub_f32 v119, v119, v249
v_sub_f32 v55, v55, v249
v_mul_f32 v152, v246, v250
v_mov_b32 v153, 0
v_bfe_u32 v249, v241, 16, 8
v_lshl_or_b32 v249, v249, 1, 1
v_lshlrev_b32 v249, 22, v249
v_and_b32 v241, 0xff00ffff, v241
AR v254,a16
v_sub_f32 v254, v254, v249
v_mul_f32 v254, v254, v250
AW a16,v254
AR v251,a17
v_sub_f32 v251, v251, v249
v_mul_f32 v251, v251, v250
AW a17,v251
AR v254,a18
v_sub_f32 v254, v254, v249
v_mul_f32 v254, v254, v250
AW a18,v254
AR v251,a19
v_sub_f32 v251, v251, v249
v_mul_f32 v251, v251, v250
AW a19,v251
AR v254,a20
v_sub_f32 v254, v254, v249
v_mul_f32 v254, v254, v250
AW a20,v254
AR v251,a21
v_sub_f32 v251, v251, v249
v_mul_f32 v251, v251, v250
AW a21,v251
AR v254,a22
v_sub_f32 v254, v254, v249
v_mul_f32 v254, v254, v250
AW a22,v254
AR v251,a23
v_sub_f32 v251, v251, v249
v_mul_f32 v251, v251, v250
AW a23,v251
AR v254,a48
v_sub_f32 v254, v254, v249
v_mul_f32 v254, v254, v250
AW a48,v254
AR v251,a49
v_sub_f32 v251, v251, v249
v_mul_f32 v251, v251, v250
AW a49,v251
AR v254,a50
v_sub_f32 v254, v254, v249
v_mul_f32 v254, v254, v250
AW a50,v254
AR v251,a51
v_sub_f32 v251, v251, v249
v_mul_f32 v251, v251, v250
AW a51,v251
AR v254,a52
v_sub_f32 v254, v254, v249
v_mul_f32 v254, v254, v250
AW a52,v254
AR v251,a53
v_sub_f32 v251, v251, v249
v_mul_f32 v251, v251, v250
AW a53,v251
AR v254,a54
v_sub_f32 v254, v254, v249
v_mul_f32 v254, v254, v250
AW a54,v254
AR v251,a55
v_sub_f32 v251, v251, v249
v_mul_f32 v251, v251, v250
AW a55,v251
AR v254,a80
v_sub_f32 v254, v254, v249
v_mul_f32 v254, v254, v250
AW a80,v254
AR v251,a81
v_sub_f32 v251, v251, v249
v_mul_f32 v251, v251, v250
AW a81,v251
AR v254,a82
v_sub_f32 v254, v254, v249
v_mul_f32 v254, v254, v250
AW a82,v254
AR v251,a83
v_sub_f32 v251, v251, v249
v_mul_f32 v251, v251, v250
AW a83,v251
AR v254,a84
v_sub_f32 v254, v254, v249
v_mul_f32 v254, v254, v250
AW a84,v254
AR v251,a85
v_sub_f32 v251, v251, v249
v_mul_f32 v251, v251, v250
AW a85,v251
AR v254,a86
v_sub_f32 v254, v254, v249
v_mul_f32 v254, v254, v250
AW a86,v254
AR v251,a87
v_sub_f32 v251, v251, v249
v_mul_f32 v251, v251, v250
AW a87,v251
AR v254,a112
v_sub_f32 v254, v254, v249
v_mul_f32 v254, v254, v250
AW a112,v254
AR v251,a113
v_sub_f32 v251, v251, v249
v_mul_f32 v251, v251, v250
AW a113,v251
AR v254,a114
v_sub_f32 v254, v254, v249
v_mul_f32 v254, v254, v250
AW a114,v254
AR v251,a115
v_sub_f32 v251, v251, v249
v_mul_f32 v251, v251, v250
AW a115,v251
AR v254,a116
v_sub_f32 v254, v254, v249
v_mul_f32 v254, v254, v250
AW a116,v254
AR v251,a117
v_sub_f32 v251, v251, v249
v_mul_f32 v251, v251, v250
AW a117,v251
AR v254,a118
v_sub_f32 v254, v254, v249
v_mul_f32 v254, v254, v250
AW a118,v254
AR v251,a119
v_sub_f32 v251, v251, v249
v_mul_f32 v251, v251, v250
AW a119,v251
v_max3_f32 v249, v24, v25, v26
v_max3_f32 v251, v56, v57, v58
v_max3_f32 v249, v249, v27, v28
v_max3_f32 v251, v251, v59, v60
v_max3_f32 v249, v249, v29, v30
v_max3_f32 v251, v251, v61, v62
v_max3_f32 v249, v249, v31, v63
v_max_f32 v249, v249, v251
v_mov_b32 v251, v249
s_nop 1
v_permlane16_swap_b32 v249, v251
v_max_f32 v249, v249, v251
v_mov_b32 v251, v249
s_nop 1
v_permlane32_swap_b32 v249, v251
v_max_f32 v249, v249, v251
v_log_f32 v250, v247
s_nop 0
v_max3_f32 v249, 0, v249, v250
v_sub_f32 v250, 0, v249
EX v250,v250
v_sub_f32 v140, v140, v249
v_sub_f32 v141, v141, v249
v_sub_f32 v142, v142, v249
v_sub_f32 v143, v143, v249
v_sub_f32 v88, v88, v249
v_sub_f32 v24, v24, v249
v_sub_f32 v89, v89, v249
v_sub_f32 v25, v25, v249
v_sub_f32 v90, v90, v249
v_sub_f32 v26, v26, v249
v_sub_f32 v91, v91, v249
v_sub_f32 v27, v27, v249
v_sub_f32 v92, v92, v249
v_sub_f32 v28, v28, v249
v_sub_f32 v93, v93, v249
v_sub_f32 v29, v29, v249
v_sub_f32 v94, v94, v249
v_sub_f32 v30, v30, v249
v_sub_f32 v95, v95, v249
v_sub_f32 v31, v31, v249
v_sub_f32 v120, v120, v249
v_sub_f32 v56, v56, v249
v_sub_f32 v121, v121, v249
v_sub_f32 v57, v57, v249
v_sub_f32 v122, v122, v249
v_sub_f32 v58, v58, v249
v_sub_f32 v123, v123, v249
v_sub_f32 v59, v59, v249
v_sub_f32 v124, v124, v249
v_sub_f32 v60, v60, v249
v_sub_f32 v125, v125, v249
v_sub_f32 v61, v61, v249
v_sub_f32 v126, v126, v249
v_sub_f32 v62, v62, v249
v_sub_f32 v127, v127, v249
v_sub_f32 v63, v63, v249
v_mul_f32 v156, v247, v250
v_mov_b32 v157, 0
v_bfe_u32 v249, v241, 24, 8
v_lshl_or_b32 v249, v249, 1, 1
v_lshlrev_b32 v249, 22, v249
v_and_b32 v241, 0xffffff, v241
AR v254,a24
v_sub_f32 v254, v254, v249
v_mul_f32 v254, v254, v250
AW a24,v254
AR v251,a25
v_sub_f32 v251, v251, v249
v_mul_f32 v251, v251, v250
AW a25,v251
AR v254,a26
v_sub_f32 v254, v254, v249
v_mul_f32 v254, v254, v250
AW a26,v254
AR v251,a27
v_sub_f32 v251, v251, v249
v_mul_f32 v251, v251, v250
AW a27,v251
AR v254,a28
v_sub_f32 v254, v254, v249
v_mul_f32 v254, v254, v250
AW a28,v254
AR v251,a29
v_sub_f32 v251, v251, v249
v_mul_f32 v251, v251, v250
AW a29,v251
AR v254,a30
v_sub_f32 v254, v254, v249
v_mul_f32 v254, v254, v250
AW a30,v254
AR v251,a31
v_sub_f32 v251, v251, v249
v_mul_f32 v251, v251, v250
AW a31,v251
AR v254,a56
v_sub_f32 v254, v254, v249
v_mul_f32 v254, v254, v250
AW a56,v254
AR v251,a57
v_sub_f32 v251, v251, v249
v_mul_f32 v251, v251, v250
AW a57,v251
AR v254,a58
v_sub_f32 v254, v254, v249
v_mul_f32 v254, v254, v250
AW a58,v254
AR v251,a59
v_sub_f32 v251, v251, v249
v_mul_f32 v251, v251, v250
AW a59,v251
AR v254,a60
v_sub_f32 v254, v254, v249
v_mul_f32 v254, v254, v250
AW a60,v254
AR v251,a61
v_sub_f32 v251, v251, v249
v_mul_f32 v251, v251, v250
AW a61,v251
AR v254,a62
v_sub_f32 v254, v254, v249
v_mul_f32 v254, v254, v250
AW a62,v254
AR v251,a63
v_sub_f32 v251, v251, v249
v_mul_f32 v251, v251, v250
AW a63,v251
AR v254,a88
v_sub_f32 v254, v254, v249
v_mul_f32 v254, v254, v250
AW a88,v254
AR v251,a89
v_sub_f32 v251, v251, v249
v_mul_f32 v251, v251, v250
AW a89,v251
AR v254,a90
v_sub_f32 v254, v254, v249
v_mul_f32 v254, v254, v250
AW a90,v254
AR v251,a91
v_sub_f32 v251, v251, v249
v_mul_f32 v251, v251, v250
AW a91,v251
AR v254,a92
v_sub_f32 v254, v254, v249
v_mul_f32 v254, v254, v250
AW a92,v254
AR v251,a93
v_sub_f32 v251, v251, v249
v_mul_f32 v251, v251, v250
AW a93,v251
AR v254,a94
v_sub_f32 v254, v254, v249
v_mul_f32 v254, v254, v250
AW a94,v254
AR v251,a95
v_sub_f32 v251, v251, v249
v_mul_f32 v251, v251, v250
AW a95,v251
AR v254,a120
v_sub_f32 v254, v254, v249
v_mul_f32 v254, v254, v250
AW a120,v254
AR v251,a121
v_sub_f32 v251, v251, v249
v_mul_f32 v251, v251, v250
AW a121,v251
AR v254,a122
v_sub_f32 v254, v254, v249
v_mul_f32 v254, v254, v250
AW a122,v254
AR v251,a123
v_sub_f32 v251, v251, v249
v_mul_f32 v251, v251, v250
AW a123,v251
AR v254,a124
v_sub_f32 v254, v254, v249
v_mul_f32 v254, v254, v250
AW a124,v254
AR v251,a125
v_sub_f32 v251, v251, v249
v_mul_f32 v251, v251, v250
AW a125,v251
AR v254,a126
v_sub_f32 v254, v254, v249
v_mul_f32 v254, v254, v250
AW a126,v254
AR v251,a127
v_sub_f32 v251, v251, v249
v_mul_f32 v251, v251, v250
AW a127,v251
EX v0,v0
EX v1,v1
EX v2,v2
EX v3,v3
EX v4,v4
EX v5,v5
EX v6,v6
EX v7,v7
EX v8,v8
EX v9,v9
EX v10,v10
EX v11,v11
EX v12,v12
EX v13,v13
EX v14,v14
EX v15,v15
EX v16,v16
EX v17,v17
EX v18,v18
EX v19,v19
EX v20,v20
EX v21,v21
EX v22,v22
EX v23,v23
EX v24,v24
EX v25,v25
EX v26,v26
EX v27,v27
EX v28,v28
EX v29,v29
EX v30,v30
EX v31,v31
EX v32,v32
EX v33,v33
EX v34,v34
EX v35,v35
EX v36,v36
EX v37,v37
EX v38,v38
EX v39,v39
EX v40,v40
EX v41,v41
EX v42,v42
EX v43,v43
EX v44,v44
EX v45,v45
EX v46,v46
EX v47,v47
EX v48,v48
EX v49,v49
EX v50,v50
EX v51,v51
EX v52,v52
EX v53,v53
EX v54,v54
EX v55,v55
EX v56,v56
EX v57,v57
EX v58,v58
EX v59,v59
EX v60,v60
EX v61,v61
EX v62,v62
EX v63,v63
CV v0,v0,v1
CV v1,v2,v3
CV v2,v4,v5
CV v3,v6,v7
CV v8,v8,v9
CV v9,v10,v11
CV v10,v12,v13
CV v11,v14,v15
CV v16,v16,v17
CV v17,v18,v19
CV v18,v20,v21
CV v19,v22,v23
CV v24,v24,v25
CV v25,v26,v27
CV v26,v28,v29
CV v27,v30,v31
CV v4,v32,v33
CV v5,v34,v35
CV v6,v36,v37
CV v7,v38,v39
CV v12,v40,v41
CV v13,v42,v43
CV v14,v44,v45
CV v15,v46,v47
CV v20,v48,v49
CV v21,v50,v51
CV v22,v52,v53
CV v23,v54,v55
CV v28,v56,v57
CV v29,v58,v59
CV v30,v60,v61
CV v31,v62,v63
v_smfmac_f32_16x16x64_bf16 v[144:147], v[236:239], v[0:7], v234
v_smfmac_f32_16x16x64_bf16 v[148:151], v[236:239], v[8:15], v234
v_smfmac_f32_16x16x64_bf16 v[152:155], v[236:239], v[16:23], v234
v_smfmac_f32_16x16x64_bf16 v[156:159], v[236:239], v[24:31], v234
s_nop 9
s_branch LRET_%=
LRARE022_%=:
s_nop 15
s_waitcnt vmcnt(0) lgkmcnt(0)
v_add_u32 v249, s74, v224
RD a[192:195],v249 offset:0
RD a[208:211],v249 offset:4096
RD a[224:227],v249 offset:8192
RD a[240:243],v249 offset:12288
v_add_u32 v250, s74, v225
RD a[196:199],v250 offset:0
RD a[212:215],v250 offset:4096
RD a[228:231],v250 offset:8192
RD a[244:247],v250 offset:12288
v_add_u32 v251, s74, v226
RD a[200:203],v251 offset:0
RD a[216:219],v251 offset:4096
RD a[232:235],v251 offset:8192
RD a[248:251],v251 offset:12288
v_add_u32 v254, s74, v227
RD a[204:207],v254 offset:0
RD a[220:223],v254 offset:4096
RD a[236:239],v254 offset:8192
RD a[252:255],v254 offset:12288
s_waitcnt lgkmcnt(0)
MM v[16:19],a[192:195],v[168:171],v[136:139]
MM v[16:19],a[196:199],v[184:187],v[16:19]
MM v[16:19],a[200:203],v[200:203],v[16:19]
MM v[16:19],a[204:207],v[216:219],v[16:19]
MM v[20:23],a[220:223],v[216:219],v[136:139]
MM v[20:23],a[216:219],v[200:203],v[20:23]
MM v[20:23],a[212:215],v[184:187],v[20:23]
MM v[20:23],a[208:211],v[168:171],v[20:23]
MM v[28:31],a[208:211],v[172:175],v[140:143]
MM v[28:31],a[212:215],v[188:191],v[28:31]
MM v[28:31],a[216:219],v[204:207],v[28:31]
MM v[28:31],a[220:223],v[220:223],v[28:31]
MM v[24:27],a[204:207],v[220:223],v[140:143]
MM v[24:27],a[200:203],v[204:207],v[24:27]
MM v[24:27],a[196:199],v[188:191],v[24:27]
MM v[24:27],a[192:195],v[172:175],v[24:27]
MM v[48:51],a[224:227],v[168:171],v[136:139]
MM v[48:51],a[228:231],v[184:187],v[48:51]
MM v[48:51],a[232:235],v[200:203],v[48:51]
MM v[48:51],a[236:239],v[216:219],v[48:51]
MM v[52:55],a[252:255],v[216:219],v[136:139]
MM v[52:55],a[248:251],v[200:203],v[52:55]
MM v[52:55],a[244:247],v[184:187],v[52:55]
MM v[52:55],a[240:243],v[168:171],v[52:55]
MM v[60:63],a[240:243],v[172:175],v[140:143]
MM v[60:63],a[244:247],v[188:191],v[60:63]
MM v[60:63],a[248:251],v[204:207],v[60:63]
MM v[60:63],a[252:255],v[220:223],v[60:63]
MM v[56:59],a[236:239],v[220:223],v[140:143]
MM v[56:59],a[232:235],v[204:207],v[56:59]
MM v[56:59],a[228:231],v[188:191],v[56:59]
MM v[56:59],a[224:227],v[172:175],v[56:59]
s_nop 15
v_add_u32 v249, s75, v224
RD a[192:195],v249 offset:0
RD a[208:211],v249 offset:4096
RD a[224:227],v249 offset:8192
RD a[240:243],v249 offset:12288
v_add_u32 v250, s75, v225
RD a[196:199],v250 offset:0
RD a[212:215],v250 offset:4096
RD a[228:231],v250 offset:8192
RD a[244:247],v250 offset:12288
v_add_u32 v251, s75, v226
RD a[200:203],v251 offset:0
RD a[216:219],v251 offset:4096
RD a[232:235],v251 offset:8192
RD a[248:251],v251 offset:12288
v_add_u32 v254, s75, v227
RD a[204:207],v254 offset:0
RD a[220:223],v254 offset:4096
RD a[236:239],v254 offset:8192
RD a[252:255],v254 offset:12288
s_waitcnt lgkmcnt(0)
v_max3_f32 v249, v16, v17, v18
)ASM"
R"ASM(
v_max3_f32 v251, v48, v49, v50
v_max3_f32 v249, v249, v19, v20
v_max3_f32 v251, v251, v51, v52
v_max3_f32 v249, v249, v21, v22
v_max3_f32 v251, v251, v53, v54
v_max3_f32 v249, v249, v23, v55
v_max_f32 v249, v249, v251
v_mov_b32 v251, v249
s_nop 1
v_permlane16_swap_b32 v249, v251
v_max_f32 v249, v249, v251
v_mov_b32 v251, v249
s_nop 1
v_permlane32_swap_b32 v249, v251
v_max_f32 v249, v249, v251
v_log_f32 v250, v246
s_nop 0
v_max3_f32 v249, 0, v249, v250
v_sub_f32 v250, 0, v249
EX v250,v250
v_sub_f32 v136, v136, v249
v_sub_f32 v137, v137, v249
v_sub_f32 v138, v138, v249
v_sub_f32 v139, v139, v249
v_sub_f32 v80, v80, v249
v_sub_f32 v16, v16, v249
v_sub_f32 v81, v81, v249
v_sub_f32 v17, v17, v249
v_sub_f32 v82, v82, v249
v_sub_f32 v18, v18, v249
v_sub_f32 v83, v83, v249
v_sub_f32 v19, v19, v249
v_sub_f32 v84, v84, v249
v_sub_f32 v20, v20, v249
v_sub_f32 v85, v85, v249
v_sub_f32 v21, v21, v249
v_sub_f32 v86, v86, v249
v_sub_f32 v22, v22, v249
v_sub_f32 v87, v87, v249
v_sub_f32 v23, v23, v249
v_sub_f32 v112, v112, v249
v_sub_f32 v48, v48, v249
v_sub_f32 v113, v113, v249
v_sub_f32 v49, v49, v249
v_sub_f32 v114, v114, v249
v_sub_f32 v50, v50, v249
v_sub_f32 v115, v115, v249
v_sub_f32 v51, v51, v249
v_sub_f32 v116, v116, v249
v_sub_f32 v52, v52, v249
v_sub_f32 v117, v117, v249
v_sub_f32 v53, v53, v249
v_sub_f32 v118, v118, v249
v_sub_f32 v54, v54, v249
v_sub_f32 v119, v119, v249
v_sub_f32 v55, v55, v249
v_mul_f32 v152, v246, v250
v_mov_b32 v153, 0
v_bfe_u32 v249, v241, 16, 8
v_lshl_or_b32 v249, v249, 1, 1
v_lshlrev_b32 v249, 22, v249
v_and_b32 v241, 0xff00ffff, v241
AR v254,a16
v_sub_f32 v254, v254, v249
v_mul_f32 v254, v254, v250
AW a16,v254
AR v251,a17
v_sub_f32 v251, v251, v249
v_mul_f32 v251, v251, v250
AW a17,v251
AR v254,a18
v_sub_f32 v254, v254, v249
v_mul_f32 v254, v254, v250
AW a18,v254
AR v251,a19
v_sub_f32 v251, v251, v249
v_mul_f32 v251, v251, v250
AW a19,v251
AR v254,a20
v_sub_f32 v254, v254, v249
v_mul_f32 v254, v254, v250
AW a20,v254
AR v251,a21
v_sub_f32 v251, v251, v249
v_mul_f32 v251, v251, v250
AW a21,v251
AR v254,a22
v_sub_f32 v254, v254, v249
v_mul_f32 v254, v254, v250
AW a22,v254
AR v251,a23
v_sub_f32 v251, v251, v249
v_mul_f32 v251, v251, v250
AW a23,v251
AR v254,a48
v_sub_f32 v254, v254, v249
v_mul_f32 v254, v254, v250
AW a48,v254
AR v251,a49
v_sub_f32 v251, v251, v249
v_mul_f32 v251, v251, v250
AW a49,v251
AR v254,a50
v_sub_f32 v254, v254, v249
v_mul_f32 v254, v254, v250
AW a50,v254
AR v251,a51
v_sub_f32 v251, v251, v249
v_mul_f32 v251, v251, v250
AW a51,v251
AR v254,a52
v_sub_f32 v254, v254, v249
v_mul_f32 v254, v254, v250
AW a52,v254
AR v251,a53
v_sub_f32 v251, v251, v249
v_mul_f32 v251, v251, v250
AW a53,v251
AR v254,a54
v_sub_f32 v254, v254, v249
v_mul_f32 v254, v254, v250
AW a54,v254
AR v251,a55
v_sub_f32 v251, v251, v249
v_mul_f32 v251, v251, v250
AW a55,v251
AR v254,a80
v_sub_f32 v254, v254, v249
v_mul_f32 v254, v254, v250
AW a80,v254
AR v251,a81
v_sub_f32 v251, v251, v249
v_mul_f32 v251, v251, v250
AW a81,v251
AR v254,a82
v_sub_f32 v254, v254, v249
v_mul_f32 v254, v254, v250
AW a82,v254
AR v251,a83
v_sub_f32 v251, v251, v249
v_mul_f32 v251, v251, v250
AW a83,v251
AR v254,a84
v_sub_f32 v254, v254, v249
v_mul_f32 v254, v254, v250
AW a84,v254
AR v251,a85
v_sub_f32 v251, v251, v249
v_mul_f32 v251, v251, v250
AW a85,v251
AR v254,a86
v_sub_f32 v254, v254, v249
v_mul_f32 v254, v254, v250
AW a86,v254
AR v251,a87
v_sub_f32 v251, v251, v249
v_mul_f32 v251, v251, v250
AW a87,v251
AR v254,a112
v_sub_f32 v254, v254, v249
v_mul_f32 v254, v254, v250
AW a112,v254
AR v251,a113
v_sub_f32 v251, v251, v249
v_mul_f32 v251, v251, v250
AW a113,v251
AR v254,a114
v_sub_f32 v254, v254, v249
v_mul_f32 v254, v254, v250
AW a114,v254
AR v251,a115
v_sub_f32 v251, v251, v249
v_mul_f32 v251, v251, v250
AW a115,v251
AR v254,a116
v_sub_f32 v254, v254, v249
v_mul_f32 v254, v254, v250
AW a116,v254
AR v251,a117
v_sub_f32 v251, v251, v249
v_mul_f32 v251, v251, v250
AW a117,v251
AR v254,a118
v_sub_f32 v254, v254, v249
v_mul_f32 v254, v254, v250
AW a118,v254
AR v251,a119
v_sub_f32 v251, v251, v249
v_mul_f32 v251, v251, v250
AW a119,v251
v_max3_f32 v249, v24, v25, v26
v_max3_f32 v251, v56, v57, v58
v_max3_f32 v249, v249, v27, v28
v_max3_f32 v251, v251, v59, v60
v_max3_f32 v249, v249, v29, v30
v_max3_f32 v251, v251, v61, v62
v_max3_f32 v249, v249, v31, v63
v_max_f32 v249, v249, v251
v_mov_b32 v251, v249
s_nop 1
v_permlane16_swap_b32 v249, v251
v_max_f32 v249, v249, v251
v_mov_b32 v251, v249
s_nop 1
v_permlane32_swap_b32 v249, v251
v_max_f32 v249, v249, v251
v_log_f32 v250, v247
s_nop 0
v_max3_f32 v249, 0, v249, v250
v_sub_f32 v250, 0, v249
EX v250,v250
v_sub_f32 v140, v140, v249
v_sub_f32 v141, v141, v249
v_sub_f32 v142, v142, v249
v_sub_f32 v143, v143, v249
v_sub_f32 v88, v88, v249
v_sub_f32 v24, v24, v249
v_sub_f32 v89, v89, v249
v_sub_f32 v25, v25, v249
v_sub_f32 v90, v90, v249
v_sub_f32 v26, v26, v249
v_sub_f32 v91, v91, v249
v_sub_f32 v27, v27, v249
v_sub_f32 v92, v92, v249
v_sub_f32 v28, v28, v249
v_sub_f32 v93, v93, v249
v_sub_f32 v29, v29, v249
v_sub_f32 v94, v94, v249
v_sub_f32 v30, v30, v249
v_sub_f32 v95, v95, v249
v_sub_f32 v31, v31, v249
v_sub_f32 v120, v120, v249
v_sub_f32 v56, v56, v249
v_sub_f32 v121, v121, v249
v_sub_f32 v57, v57, v249
v_sub_f32 v122, v122, v249
v_sub_f32 v58, v58, v249
v_sub_f32 v123, v123, v249
v_sub_f32 v59, v59, v249
v_sub_f32 v124, v124, v249
v_sub_f32 v60, v60, v249
v_sub_f32 v125, v125, v249
v_sub_f32 v61, v61, v249
v_sub_f32 v126, v126, v249
v_sub_f32 v62, v62, v249
v_sub_f32 v127, v127, v249
v_sub_f32 v63, v63, v249
v_mul_f32 v156, v247, v250
v_mov_b32 v157, 0
v_bfe_u32 v249, v241, 24, 8
v_lshl_or_b32 v249, v249, 1, 1
v_lshlrev_b32 v249, 22, v249
v_and_b32 v241, 0xffffff, v241
AR v254,a24
v_sub_f32 v254, v254, v249
v_mul_f32 v254, v254, v250
AW a24,v254
AR v251,a25
v_sub_f32 v251, v251, v249
v_mul_f32 v251, v251, v250
AW a25,v251
AR v254,a26
v_sub_f32 v254, v254, v249
v_mul_f32 v254, v254, v250
AW a26,v254
AR v251,a27
v_sub_f32 v251, v251, v249
v_mul_f32 v251, v251, v250
AW a27,v251
AR v254,a28
v_sub_f32 v254, v254, v249
v_mul_f32 v254, v254, v250
AW a28,v254
AR v251,a29
v_sub_f32 v251, v251, v249
v_mul_f32 v251, v251, v250
AW a29,v251
AR v254,a30
v_sub_f32 v254, v254, v249
v_mul_f32 v254, v254, v250
AW a30,v254
AR v251,a31
v_sub_f32 v251, v251, v249
v_mul_f32 v251, v251, v250
AW a31,v251
AR v254,a56
v_sub_f32 v254, v254, v249
v_mul_f32 v254, v254, v250
AW a56,v254
AR v251,a57
v_sub_f32 v251, v251, v249
v_mul_f32 v251, v251, v250
AW a57,v251
AR v254,a58
v_sub_f32 v254, v254, v249
v_mul_f32 v254, v254, v250
AW a58,v254
AR v251,a59
v_sub_f32 v251, v251, v249
v_mul_f32 v251, v251, v250
AW a59,v251
AR v254,a60
v_sub_f32 v254, v254, v249
v_mul_f32 v254, v254, v250
AW a60,v254
AR v251,a61
v_sub_f32 v251, v251, v249
v_mul_f32 v251, v251, v250
AW a61,v251
AR v254,a62
v_sub_f32 v254, v254, v249
v_mul_f32 v254, v254, v250
AW a62,v254
AR v251,a63
v_sub_f32 v251, v251, v249
v_mul_f32 v251, v251, v250
AW a63,v251
AR v254,a88
v_sub_f32 v254, v254, v249
v_mul_f32 v254, v254, v250
AW a88,v254
AR v251,a89
v_sub_f32 v251, v251, v249
v_mul_f32 v251, v251, v250
AW a89,v251
AR v254,a90
v_sub_f32 v254, v254, v249
v_mul_f32 v254, v254, v250
AW a90,v254
AR v251,a91
v_sub_f32 v251, v251, v249
v_mul_f32 v251, v251, v250
AW a91,v251
AR v254,a92
v_sub_f32 v254, v254, v249
v_mul_f32 v254, v254, v250
AW a92,v254
AR v251,a93
v_sub_f32 v251, v251, v249
v_mul_f32 v251, v251, v250
AW a93,v251
AR v254,a94
v_sub_f32 v254, v254, v249
v_mul_f32 v254, v254, v250
AW a94,v254
AR v251,a95
v_sub_f32 v251, v251, v249
v_mul_f32 v251, v251, v250
AW a95,v251
AR v254,a120
v_sub_f32 v254, v254, v249
v_mul_f32 v254, v254, v250
AW a120,v254
AR v251,a121
v_sub_f32 v251, v251, v249
v_mul_f32 v251, v251, v250
AW a121,v251
AR v254,a122
v_sub_f32 v254, v254, v249
v_mul_f32 v254, v254, v250
AW a122,v254
AR v251,a123
v_sub_f32 v251, v251, v249
v_mul_f32 v251, v251, v250
AW a123,v251
AR v254,a124
v_sub_f32 v254, v254, v249
v_mul_f32 v254, v254, v250
AW a124,v254
AR v251,a125
v_sub_f32 v251, v251, v249
v_mul_f32 v251, v251, v250
AW a125,v251
AR v254,a126
v_sub_f32 v254, v254, v249
v_mul_f32 v254, v254, v250
AW a126,v254
AR v251,a127
v_sub_f32 v251, v251, v249
v_mul_f32 v251, v251, v250
AW a127,v251
EX v16,v16
EX v17,v17
EX v18,v18
EX v19,v19
EX v20,v20
EX v21,v21
EX v22,v22
EX v23,v23
EX v24,v24
EX v25,v25
EX v26,v26
EX v27,v27
EX v28,v28
EX v29,v29
EX v30,v30
EX v31,v31
EX v48,v48
EX v49,v49
EX v50,v50
EX v51,v51
EX v52,v52
EX v53,v53
EX v54,v54
EX v55,v55
EX v56,v56
EX v57,v57
EX v58,v58
EX v59,v59
EX v60,v60
EX v61,v61
EX v62,v62
EX v63,v63
CV v16,v16,v17
CV v17,v18,v19
CV v18,v20,v21
CV v19,v22,v23
CV v24,v24,v25
CV v25,v26,v27
CV v26,v28,v29
CV v27,v30,v31
CV v20,v48,v49
CV v21,v50,v51
CV v22,v52,v53
CV v23,v54,v55
CV v28,v56,v57
CV v29,v58,v59
CV v30,v60,v61
CV v31,v62,v63
v_smfmac_f32_16x16x64_bf16 v[152:155], v[236:239], v[16:23], v234
s_nop 0
v_smfmac_f32_16x16x64_bf16 v[156:159], v[236:239], v[24:31], v234
s_nop 9
s_branch LRET_%=
LRARE122_%=:
s_nop 15
s_waitcnt vmcnt(0) lgkmcnt(0)
v_add_u32 v249, s74, v224
RD a[192:195],v249 offset:0
RD a[208:211],v249 offset:4096
RD a[224:227],v249 offset:8192
RD a[240:243],v249 offset:12288
v_add_u32 v250, s74, v225
RD a[196:199],v250 offset:0
RD a[212:215],v250 offset:4096
RD a[228:231],v250 offset:8192
RD a[244:247],v250 offset:12288
v_add_u32 v251, s74, v226
RD a[200:203],v251 offset:0
RD a[216:219],v251 offset:4096
RD a[232:235],v251 offset:8192
RD a[248:251],v251 offset:12288
v_add_u32 v254, s74, v227
RD a[204:207],v254 offset:0
RD a[220:223],v254 offset:4096
RD a[236:239],v254 offset:8192
RD a[252:255],v254 offset:12288
s_waitcnt lgkmcnt(0)
MM v[80:83],a[192:195],v[168:171],v[136:139]
MM v[80:83],a[196:199],v[184:187],v[80:83]
MM v[80:83],a[200:203],v[200:203],v[80:83]
MM v[80:83],a[204:207],v[216:219],v[80:83]
MM v[84:87],a[220:223],v[216:219],v[136:139]
MM v[84:87],a[216:219],v[200:203],v[84:87]
MM v[84:87],a[212:215],v[184:187],v[84:87]
MM v[84:87],a[208:211],v[168:171],v[84:87]
MM v[92:95],a[208:211],v[172:175],v[140:143]
MM v[92:95],a[212:215],v[188:191],v[92:95]
MM v[92:95],a[216:219],v[204:207],v[92:95]
MM v[92:95],a[220:223],v[220:223],v[92:95]
MM v[88:91],a[204:207],v[220:223],v[140:143]
MM v[88:91],a[200:203],v[204:207],v[88:91]
MM v[88:91],a[196:199],v[188:191],v[88:91]
MM v[88:91],a[192:195],v[172:175],v[88:91]
MM v[112:115],a[224:227],v[168:171],v[136:139]
MM v[112:115],a[228:231],v[184:187],v[112:115]
MM v[112:115],a[232:235],v[200:203],v[112:115]
MM v[112:115],a[236:239],v[216:219],v[112:115]
MM v[116:119],a[252:255],v[216:219],v[136:139]
MM v[116:119],a[248:251],v[200:203],v[116:119]
MM v[116:119],a[244:247],v[184:187],v[116:119]
MM v[116:119],a[240:243],v[168:171],v[116:119]
MM v[124:127],a[240:243],v[172:175],v[140:143]
MM v[124:127],a[244:247],v[188:191],v[124:127]
MM v[124:127],a[248:251],v[204:207],v[124:127]
MM v[124:127],a[252:255],v[220:223],v[124:127]
MM v[120:123],a[236:239],v[220:223],v[140:143]
MM v[120:123],a[232:235],v[204:207],v[120:123]
MM v[120:123],a[228:231],v[188:191],v[120:123]
MM v[120:123],a[224:227],v[172:175],v[120:123]
s_nop 15
v_add_u32 v249, s75, v224
RD a[192:195],v249 offset:0
RD a[208:211],v249 offset:4096
RD a[224:227],v249 offset:8192
RD a[240:243],v249 offset:12288
v_add_u32 v250, s75, v225
RD a[196:199],v250 offset:0
RD a[212:215],v250 offset:4096
RD a[228:231],v250 offset:8192
RD a[244:247],v250 offset:12288
v_add_u32 v251, s75, v226
RD a[200:203],v251 offset:0
RD a[216:219],v251 offset:4096
RD a[232:235],v251 offset:8192
RD a[248:251],v251 offset:12288
v_add_u32 v254, s75, v227
RD a[204:207],v254 offset:0
RD a[220:223],v254 offset:4096
RD a[236:239],v254 offset:8192
RD a[252:255],v254 offset:12288
s_waitcnt lgkmcnt(0)
v_max3_f32 v249, v80, v81, v82
v_max3_f32 v251, v112, v113, v114
v_max3_f32 v249, v249, v83, v84
v_max3_f32 v251, v251, v115, v116
v_max3_f32 v249, v249, v85, v86
v_max3_f32 v251, v251, v117, v118
v_max3_f32 v249, v249, v87, v119
v_max_f32 v249, v249, v251
v_mov_b32 v251, v249
s_nop 1
v_permlane16_swap_b32 v249, v251
)ASM"
R"ASM(
v_max_f32 v249, v249, v251
v_mov_b32 v251, v249
s_nop 1
v_permlane32_swap_b32 v249, v251
v_max_f32 v249, v249, v251
v_log_f32 v250, v246
s_nop 0
v_max3_f32 v249, 0, v249, v250
v_sub_f32 v250, 0, v249
EX v250,v250
v_sub_f32 v136, v136, v249
v_sub_f32 v137, v137, v249
v_sub_f32 v138, v138, v249
v_sub_f32 v139, v139, v249
v_sub_f32 v16, v16, v249
v_sub_f32 v80, v80, v249
v_sub_f32 v17, v17, v249
v_sub_f32 v81, v81, v249
v_sub_f32 v18, v18, v249
v_sub_f32 v82, v82, v249
v_sub_f32 v19, v19, v249
v_sub_f32 v83, v83, v249
v_sub_f32 v20, v20, v249
v_sub_f32 v84, v84, v249
v_sub_f32 v21, v21, v249
v_sub_f32 v85, v85, v249
v_sub_f32 v22, v22, v249
v_sub_f32 v86, v86, v249
v_sub_f32 v23, v23, v249
v_sub_f32 v87, v87, v249
v_sub_f32 v48, v48, v249
v_sub_f32 v112, v112, v249
v_sub_f32 v49, v49, v249
v_sub_f32 v113, v113, v249
v_sub_f32 v50, v50, v249
v_sub_f32 v114, v114, v249
v_sub_f32 v51, v51, v249
v_sub_f32 v115, v115, v249
v_sub_f32 v52, v52, v249
v_sub_f32 v116, v116, v249
v_sub_f32 v53, v53, v249
v_sub_f32 v117, v117, v249
v_sub_f32 v54, v54, v249
v_sub_f32 v118, v118, v249
v_sub_f32 v55, v55, v249
v_sub_f32 v119, v119, v249
v_mul_f32 v152, v246, v250
v_mov_b32 v153, 0
v_bfe_u32 v249, v241, 16, 8
v_lshl_or_b32 v249, v249, 1, 1
v_lshlrev_b32 v249, 22, v249
v_and_b32 v241, 0xff00ffff, v241
AR v254,a16
v_sub_f32 v254, v254, v249
v_mul_f32 v254, v254, v250
AW a16,v254
AR v251,a17
v_sub_f32 v251, v251, v249
v_mul_f32 v251, v251, v250
AW a17,v251
AR v254,a18
v_sub_f32 v254, v254, v249
v_mul_f32 v254, v254, v250
AW a18,v254
AR v251,a19
v_sub_f32 v251, v251, v249
v_mul_f32 v251, v251, v250
AW a19,v251
AR v254,a20
v_sub_f32 v254, v254, v249
v_mul_f32 v254, v254, v250
AW a20,v254
AR v251,a21
v_sub_f32 v251, v251, v249
v_mul_f32 v251, v251, v250
AW a21,v251
AR v254,a22
v_sub_f32 v254, v254, v249
v_mul_f32 v254, v254, v250
AW a22,v254
AR v251,a23
v_sub_f32 v251, v251, v249
v_mul_f32 v251, v251, v250
AW a23,v251
AR v254,a48
v_sub_f32 v254, v254, v249
v_mul_f32 v254, v254, v250
AW a48,v254
AR v251,a49
v_sub_f32 v251, v251, v249
v_mul_f32 v251, v251, v250
AW a49,v251
AR v254,a50
v_sub_f32 v254, v254, v249
v_mul_f32 v254, v254, v250
AW a50,v254
AR v251,a51
v_sub_f32 v251, v251, v249
v_mul_f32 v251, v251, v250
AW a51,v251
AR v254,a52
v_sub_f32 v254, v254, v249
v_mul_f32 v254, v254, v250
AW a52,v254
AR v251,a53
v_sub_f32 v251, v251, v249
v_mul_f32 v251, v251, v250
AW a53,v251
AR v254,a54
v_sub_f32 v254, v254, v249
v_mul_f32 v254, v254, v250
AW a54,v254
AR v251,a55
v_sub_f32 v251, v251, v249
v_mul_f32 v251, v251, v250
AW a55,v251
AR v254,a80
v_sub_f32 v254, v254, v249
v_mul_f32 v254, v254, v250
AW a80,v254
AR v251,a81
v_sub_f32 v251, v251, v249
v_mul_f32 v251, v251, v250
AW a81,v251
AR v254,a82
v_sub_f32 v254, v254, v249
v_mul_f32 v254, v254, v250
AW a82,v254
AR v251,a83
v_sub_f32 v251, v251, v249
v_mul_f32 v251, v251, v250
AW a83,v251
AR v254,a84
v_sub_f32 v254, v254, v249
v_mul_f32 v254, v254, v250
AW a84,v254
AR v251,a85
v_sub_f32 v251, v251, v249
v_mul_f32 v251, v251, v250
AW a85,v251
AR v254,a86
v_sub_f32 v254, v254, v249
v_mul_f32 v254, v254, v250
AW a86,v254
AR v251,a87
v_sub_f32 v251, v251, v249
v_mul_f32 v251, v251, v250
AW a87,v251
AR v254,a112
v_sub_f32 v254, v254, v249
v_mul_f32 v254, v254, v250
AW a112,v254
AR v251,a113
v_sub_f32 v251, v251, v249
v_mul_f32 v251, v251, v250
AW a113,v251
AR v254,a114
v_sub_f32 v254, v254, v249
v_mul_f32 v254, v254, v250
AW a114,v254
AR v251,a115
v_sub_f32 v251, v251, v249
v_mul_f32 v251, v251, v250
AW a115,v251
AR v254,a116
v_sub_f32 v254, v254, v249
v_mul_f32 v254, v254, v250
AW a116,v254
AR v251,a117
v_sub_f32 v251, v251, v249
v_mul_f32 v251, v251, v250
AW a117,v251
AR v254,a118
v_sub_f32 v254, v254, v249
v_mul_f32 v254, v254, v250
AW a118,v254
AR v251,a119
v_sub_f32 v251, v251, v249
v_mul_f32 v251, v251, v250
AW a119,v251
v_max3_f32 v249, v88, v89, v90
v_max3_f32 v251, v120, v121, v122
v_max3_f32 v249, v249, v91, v92
v_max3_f32 v251, v251, v123, v124
v_max3_f32 v249, v249, v93, v94
v_max3_f32 v251, v251, v125, v126
v_max3_f32 v249, v249, v95, v127
v_max_f32 v249, v249, v251
v_mov_b32 v251, v249
s_nop 1
v_permlane16_swap_b32 v249, v251
v_max_f32 v249, v249, v251
v_mov_b32 v251, v249
s_nop 1
v_permlane32_swap_b32 v249, v251
v_max_f32 v249, v249, v251
v_log_f32 v250, v247
s_nop 0
v_max3_f32 v249, 0, v249, v250
v_sub_f32 v250, 0, v249
EX v250,v250
v_sub_f32 v140, v140, v249
v_sub_f32 v141, v141, v249
v_sub_f32 v142, v142, v249
v_sub_f32 v143, v143, v249
v_sub_f32 v24, v24, v249
v_sub_f32 v88, v88, v249
v_sub_f32 v25, v25, v249
v_sub_f32 v89, v89, v249
v_sub_f32 v26, v26, v249
v_sub_f32 v90, v90, v249
v_sub_f32 v27, v27, v249
v_sub_f32 v91, v91, v249
v_sub_f32 v28, v28, v249
v_sub_f32 v92, v92, v249
v_sub_f32 v29, v29, v249
v_sub_f32 v93, v93, v249
v_sub_f32 v30, v30, v249
v_sub_f32 v94, v94, v249
v_sub_f32 v31, v31, v249
v_sub_f32 v95, v95, v249
v_sub_f32 v56, v56, v249
v_sub_f32 v120, v120, v249
v_sub_f32 v57, v57, v249
v_sub_f32 v121, v121, v249
v_sub_f32 v58, v58, v249
v_sub_f32 v122, v122, v249
v_sub_f32 v59, v59, v249
v_sub_f32 v123, v123, v249
v_sub_f32 v60, v60, v249
v_sub_f32 v124, v124, v249
v_sub_f32 v61, v61, v249
v_sub_f32 v125, v125, v249
v_sub_f32 v62, v62, v249
v_sub_f32 v126, v126, v249
v_sub_f32 v63, v63, v249
v_sub_f32 v127, v127, v249
v_mul_f32 v156, v247, v250
v_mov_b32 v157, 0
v_bfe_u32 v249, v241, 24, 8
v_lshl_or_b32 v249, v249, 1, 1
v_lshlrev_b32 v249, 22, v249
v_and_b32 v241, 0xffffff, v241
AR v254,a24
v_sub_f32 v254, v254, v249
v_mul_f32 v254, v254, v250
AW a24,v254
AR v251,a25
v_sub_f32 v251, v251, v249
v_mul_f32 v251, v251, v250
AW a25,v251
AR v254,a26
v_sub_f32 v254, v254, v249
v_mul_f32 v254, v254, v250
AW a26,v254
AR v251,a27
v_sub_f32 v251, v251, v249
v_mul_f32 v251, v251, v250
AW a27,v251
AR v254,a28
v_sub_f32 v254, v254, v249
v_mul_f32 v254, v254, v250
AW a28,v254
AR v251,a29
v_sub_f32 v251, v251, v249
v_mul_f32 v251, v251, v250
AW a29,v251
AR v254,a30
v_sub_f32 v254, v254, v249
v_mul_f32 v254, v254, v250
AW a30,v254
AR v251,a31
v_sub_f32 v251, v251, v249
v_mul_f32 v251, v251, v250
AW a31,v251
AR v254,a56
v_sub_f32 v254, v254, v249
v_mul_f32 v254, v254, v250
AW a56,v254
AR v251,a57
v_sub_f32 v251, v251, v249
v_mul_f32 v251, v251, v250
AW a57,v251
AR v254,a58
v_sub_f32 v254, v254, v249
v_mul_f32 v254, v254, v250
AW a58,v254
AR v251,a59
v_sub_f32 v251, v251, v249
v_mul_f32 v251, v251, v250
AW a59,v251
AR v254,a60
v_sub_f32 v254, v254, v249
v_mul_f32 v254, v254, v250
AW a60,v254
AR v251,a61
v_sub_f32 v251, v251, v249
v_mul_f32 v251, v251, v250
AW a61,v251
AR v254,a62
v_sub_f32 v254, v254, v249
v_mul_f32 v254, v254, v250
AW a62,v254
AR v251,a63
v_sub_f32 v251, v251, v249
v_mul_f32 v251, v251, v250
AW a63,v251
AR v254,a88
v_sub_f32 v254, v254, v249
v_mul_f32 v254, v254, v250
AW a88,v254
AR v251,a89
v_sub_f32 v251, v251, v249
v_mul_f32 v251, v251, v250
AW a89,v251
AR v254,a90
v_sub_f32 v254, v254, v249
v_mul_f32 v254, v254, v250
AW a90,v254
AR v251,a91
v_sub_f32 v251, v251, v249
v_mul_f32 v251, v251, v250
AW a91,v251
AR v254,a92
v_sub_f32 v254, v254, v249
v_mul_f32 v254, v254, v250
AW a92,v254
AR v251,a93
v_sub_f32 v251, v251, v249
v_mul_f32 v251, v251, v250
AW a93,v251
AR v254,a94
v_sub_f32 v254, v254, v249
v_mul_f32 v254, v254, v250
AW a94,v254
AR v251,a95
v_sub_f32 v251, v251, v249
v_mul_f32 v251, v251, v250
AW a95,v251
AR v254,a120
v_sub_f32 v254, v254, v249
v_mul_f32 v254, v254, v250
AW a120,v254
AR v251,a121
v_sub_f32 v251, v251, v249
v_mul_f32 v251, v251, v250
AW a121,v251
AR v254,a122
v_sub_f32 v254, v254, v249
v_mul_f32 v254, v254, v250
AW a122,v254
AR v251,a123
v_sub_f32 v251, v251, v249
v_mul_f32 v251, v251, v250
AW a123,v251
AR v254,a124
v_sub_f32 v254, v254, v249
v_mul_f32 v254, v254, v250
AW a124,v254
AR v251,a125
v_sub_f32 v251, v251, v249
v_mul_f32 v251, v251, v250
AW a125,v251
AR v254,a126
v_sub_f32 v254, v254, v249
v_mul_f32 v254, v254, v250
AW a126,v254
AR v251,a127
v_sub_f32 v251, v251, v249
v_mul_f32 v251, v251, v250
AW a127,v251
EX v80,v80
EX v81,v81
EX v82,v82
EX v83,v83
EX v84,v84
EX v85,v85
EX v86,v86
EX v87,v87
EX v88,v88
EX v89,v89
EX v90,v90
EX v91,v91
EX v92,v92
EX v93,v93
EX v94,v94
EX v95,v95
EX v112,v112
EX v113,v113
EX v114,v114
EX v115,v115
EX v116,v116
EX v117,v117
EX v118,v118
EX v119,v119
EX v120,v120
EX v121,v121
EX v122,v122
EX v123,v123
EX v124,v124
EX v125,v125
EX v126,v126
EX v127,v127
CV v80,v80,v81
CV v81,v82,v83
CV v82,v84,v85
CV v83,v86,v87
CV v88,v88,v89
CV v89,v90,v91
CV v90,v92,v93
CV v91,v94,v95
CV v84,v112,v113
CV v85,v114,v115
CV v86,v116,v117
CV v87,v118,v119
CV v92,v120,v121
CV v93,v122,v123
CV v94,v124,v125
CV v95,v126,v127
v_smfmac_f32_16x16x64_bf16 v[152:155], v[236:239], v[80:87], v234
s_nop 0
v_smfmac_f32_16x16x64_bf16 v[156:159], v[236:239], v[88:95], v234
s_nop 9
s_branch LRET_%=
LEND_%=:
)ASM"

    :: [ka] "s"(__builtin_amdgcn_kernarg_segment_ptr()), [slds] "s"(slds), [swave] "s"(w), [sb] "s"(blockIdx.x)
    : "memory", "vcc", "scc", CLOB_V, CLOB_A, CLOB_S);
}
constexpr int DYN_LDS = 163840;

extern "C" void kernel_launch(void* const* d_in, const int* in_sizes, int n_in, void* d_out, int out_size, void* d_ws, size_t ws_size, hipStream_t stream) {
#if !defined(__HIP_DEVICE_COMPILE__)
  if (DYN_LDS) hipFuncSetAttribute((const void*)ka_fused, hipFuncAttributeMaxDynamicSharedMemorySize, DYN_LDS);
  ka_fused<<<256, 256, DYN_LDS, stream>>>(d_in[3], d_in[0], d_ws, d_in[1], d_in[6], d_in[2], d_in[4], d_in[7], d_in[8], d_in[9], d_out);
#endif
}
```

```cpp
#include <hip/hip_runtime.h>
#include <stdint.h>

using f32x4  = __attribute__((ext_vector_type(4))) float;
using u32x4  = __attribute__((ext_vector_type(4))) unsigned;
using u32x2  = __attribute__((ext_vector_type(2))) unsigned;

constexpr int SEQ = 2048, DM = 640, NH = 32, DH = 128, HD = NH * DH, FF = 1728, TOPC = 8;

constexpr unsigned WS_FO  = 0;
constexpr unsigned WS_EP  = 32768;
constexpr unsigned WS_G1  = 36864;
constexpr unsigned WS_G2  = 53248;
constexpr unsigned WS_FH  = 69632;
constexpr unsigned WS_FY  = 73728;
constexpr unsigned WS_FG  = 77824;
constexpr unsigned WS_FR  = 81920;
constexpr unsigned WS_RK  = 86016;
constexpr unsigned WS_XN  = 131072;
constexpr unsigned WS_OF  = WS_XN + SEQ * DM * 2;
constexpr unsigned WS_H   = WS_OF + (18u << 20);
constexpr unsigned WS_Y   = WS_H + 32768;
constexpr unsigned WS_P   = WS_Y + 1048576;
constexpr unsigned WS_SPAN = 64u << 20;

#define CLOB_V "v0","v1","v2","v3","v4","v5","v6","v7","v8","v9","v10","v11","v12","v13","v14","v15","v16","v17","v18","v19","v20","v21","v22","v23","v24","v25","v26","v27","v28","v29","v30","v31","v32","v33","v34","v35","v36","v37","v38","v39","v40","v41","v42","v43","v44","v45","v46","v47","v48","v49","v50","v51","v52","v53","v54","v55","v56","v57","v58","v59","v60","v61","v62","v63","v64","v65","v66","v67","v68","v69","v70","v71","v72","v73","v74","v75","v76","v77","v78","v79","v80","v81","v82","v83","v84","v85","v86","v87","v88","v89","v90","v91","v92","v93","v94","v95","v96","v97","v98","v99","v100","v101","v102","v103","v104","v105","v106","v107","v108","v109","v110","v111","v112","v113","v114","v115","v116","v117","v118","v119","v120","v121","v122","v123","v124","v125","v126","v127","v128","v129","v130","v131","v132","v133","v134","v135","v136","v137","v138","v139","v140","v141","v142","v143","v144","v145","v146","v147","v148","v149","v150","v151","v152","v153","v154","v155","v156","v157","v158","v159","v160","v161","v162","v163","v164","v165","v166","v167","v168","v169","v170","v171","v172","v173","v174","v175","v176","v177","v178","v179","v180","v181","v182","v183","v184","v185","v186","v187","v188","v189","v190","v191","v192","v193","v194","v195","v196","v197","v198","v199","v200","v201","v202","v203","v204","v205","v206","v207","v208","v209","v210","v211","v212","v213","v214","v215","v216","v217","v218","v219","v220","v221","v222","v223","v224","v225","v226","v227","v228","v229","v230","v231","v232","v233","v234","v235","v236","v237","v238","v239","v240","v241","v242","v243","v244","v245","v246","v247","v248","v249","v250","v251","v252","v253","v254","v255"
#define CLOB_A "a0","a1","a2","a3","a4","a5","a6","a7","a8","a9","a10","a11","a12","a13","a14","a15","a16","a17","a18","a19","a20","a21","a22","a23","a24","a25","a26","a27","a28","a29","a30","a31","a32","a33","a34","a35","a36","a37","a38","a39","a40","a41","a42","a43","a44","a45","a46","a47","a48","a49","a50","a51","a52","a53","a54","a55","a56","a57","a58","a59","a60","a61","a62","a63","a64","a65","a66","a67","a68","a69","a70","a71","a72","a73","a74","a75","a76","a77","a78","a79","a80","a81","a82","a83","a84","a85","a86","a87","a88","a89","a90","a91","a92","a93","a94","a95","a96","a97","a98","a99","a100","a101","a102","a103","a104","a105","a106","a107","a108","a109","a110","a111","a112","a113","a114","a115","a116","a117","a118","a119","a120","a121","a122","a123","a124","a125","a126","a127","a128","a129","a130","a131","a132","a133","a134","a135","a136","a137","a138","a139","a140","a141","a142","a143","a144","a145","a146","a147","a148","a149","a150","a151","a152","a153","a154","a155","a156","a157","a158","a159","a160","a161","a162","a163","a164","a165","a166","a167","a168","a169","a170","a171","a172","a173","a174","a175","a176","a177","a178","a179","a180","a181","a182","a183","a184","a185","a186","a187","a188","a189","a190","a191","a192","a193","a194","a195","a196","a197","a198","a199","a200","a201","a202","a203","a204","a205","a206","a207","a208","a209","a210","a211","a212","a213","a214","a215","a216","a217","a218","a219","a220","a221","a222","a223","a224","a225","a226","a227","a228","a229","a230","a231","a232","a233","a234","a235","a236","a237","a238","a239","a240","a241","a242","a243","a244","a245","a246","a247","a248","a249","a250","a251","a252","a253","a254","a255"
#define CLOB_S "s8","s9","s10","s11","s12","s13","s14","s15","s16","s17","s18","s19","s20","s21","s22","s23","s24","s25","s26","s27","s28","s29","s30","s31","s32","s33","s34","s35","s36","s37","s38","s39","s40","s41","s42","s43","s44","s45","s46","s47","s48","s49","s50","s51","s52","s53","s54","s55","s56","s57","s58","s59","s60","s61","s62","s63","s64","s65","s66","s67","s68","s69","s70","s71","s72","s73","s74","s75","s76","s77","s78","s79","s80","s81","s82","s83","s84","s85","s86","s87","s88","s89","s90","s91","s92","s93","s94","s95","s96","s97","s98","s99"
__global__ __launch_bounds__(256, 1) __attribute__((aligned(4096))) void ka_fused(const void* wq, const void* x, void* ws, const void* Kc, const void* wr, const void* Vc, const void* wo, const void* wg, const void* wu, const void* wd, void* out) {
  extern __shared__ __attribute__((aligned(16))) char lds[];
  const unsigned w = __builtin_amdgcn_readfirstlane(threadIdx.x >> 6);
  const unsigned slds = __builtin_amdgcn_readfirstlane((unsigned)(size_t)lds);
  asm volatile(
R"ASM(
.macro MM a, b, c, d:vararg
v_mfma_f32_16x16x32_bf16 \a, \b, \c, \d
.endm
.macro TR a, b:vararg
ds_read_b64_tr_b16 \a, \b
.endm
.macro CV a, b, c:vararg
v_cvt_pk_bf16_f32 \a, \b, \c
.endm
.macro AR a, b:vararg
v_accvgpr_read_b32 \a, \b
.endm
.macro AW a, b:vararg
v_accvgpr_write_b32 \a, \b
.endm
.macro EX a, b:vararg
v_exp_f32 \a, \b
.endm
.macro RD a, b:vararg
ds_read_b128 \a, \b
.endm
s_branch LPG0_%=
.p2align 6
LPG0_%=:
s_load_dwordx2 s[16:17], %[ka], 0
s_load_dwordx2 s[32:33], %[ka], 8
s_load_dwordx2 s[20:21], %[ka], 16
s_load_dwordx2 s[8:9], %[ka], 24
s_load_dwordx2 s[12:13], %[ka], 40
s_load_dwordx2 s[24:25], %[ka], 48
s_load_dwordx2 s[44:45], %[ka], 32
s_load_dwordx2 s[28:29], %[ka], 72
s_load_dwordx2 s[40:41], %[ka], 80
s_lshl_b32 s77, %[swave], 2
s_and_b32 s77, s77, 8
s_add_u32 s77, s77, 56
s_load_dwordx2 s[36:37], %[ka], s77
v_mbcnt_lo_u32_b32 v127, -1, 0
v_mbcnt_hi_u32_b32 v127, -1, v127
v_lshlrev_b32 v248, 4, v127
s_lshr_b32 s77, %[sb], 3
s_lshl_b32 s77, s77, 2
s_add_u32 s77, s77, %[swave]
s_lshl_b32 s77, s77, 10
v_add_u32 v118, s77, v248
s_mov_b32 s77, LKEND_ka-LPC_%=-16
v_min_u32 v118, s77, v118
s_getpc_b64 s[98:99]
LPC_%=:
global_load_dwordx4 a[252:255], v118, s[98:99]
s_and_b32 s77, %[sb], 7
s_lshl_b32 s77, s77, 2
s_bfe_u32 s84, %[sb], 131075
s_add_u32 s49, s77, s84
s_lshr_b32 s50, %[sb], 5
s_lshl_b32 s76, %[sb], 2
s_add_u32 s76, s76, %[swave]
v_lshrrev_b32 v180, 5, v127
s_mul_i32 s82, s76, 2560
v_and_b32 v182, 496, v248
v_mul_u32_u24 v181, 1280, v180
v_add_u32 v168, v181, v182
v_lshrrev_b32 v210, 1, v182
v_add_u32 v181, v181, v210
s_lshl_b32 s77, %[swave], 12
s_add_u32 s65, s77, %[slds]
s_lshl_b32 s89, %[swave], 17
v_bfe_u32 v210, v127, 2, 3
v_and_b32 v212, 3, v127
v_lshlrev_b32 v213, 4, v212
v_lshl_add_u32 v213, v180, 6, v213
v_lshrrev_b32 v253, 3, v127
v_lshlrev_b32 v253, 13, v253
v_and_b32 v254, 112, v248
v_or_b32 v253, v253, v254
v_and_b32 v254, 16, v127
v_lshlrev_b32 v254, 2, v254
v_xor_b32 v253, v253, v254
v_add_u32 v254, 128, v253
s_mul_i32 s96, %[swave], 4112
s_add_u32 s96, s96, %[slds]
s_lshl_b32 s77, s50, 2
s_add_u32 s77, s77, %[swave]
s_mul_i32 s68, s77, 81920
s_and_b32 s62, %[sb], 1
s_mul_i32 s63, s62, 40960
s_add_u32 s68, s68, s63
s_lshl_b32 s63, s63, 1
s_mul_i32 s83, s77, 560128
s_add_u32 s83, s83, 2752512
s_lshl_b32 s93, s77, 9
s_lshl_b32 s94, %[swave], 13
s_add_u32 s94, s94, %[slds]
v_lshrrev_b32 v211, 3, v127
v_mul_u32_u24 v220, 1280, v211
v_lshrrev_b32 v212, 4, v127
v_and_b32 v211, 7, v127
v_xor_b32 v211, v211, v212
v_lshl_add_u32 v220, v211, 4, v220
v_xor_b32 v221, 64, v220
s_bitcmp1_b32 s84, 0
s_mov_b32 s77, 0x77552200
s_cselect_b32 s77, 0x77552200, s77
s_lshl_b32 s91, s50, 2
s_lshr_b32 s77, s77, s91
s_and_b32 s77, s77, 15
s_lshl_b32 s86, s77, 7
s_lshl_b32 s85, s77, 19
v_and_b32 v211, 15, v127
v_bfe_u32 v222, v127, 1, 3
v_xor_b32 v222, v222, v212
v_lshlrev_b32 v222, 4, v222
v_lshl_add_u32 v222, v211, 7, v222
v_xor_b32 v223, 64, v222
v_add_u32 v222, s94, v222
v_add_u32 v223, s94, v223
v_and_b32 v214, 1, v212
v_lshlrev_b32 v215, 5, v214
v_xor_b32 v215, v215, v213
v_lshl_add_u32 v235, v210, 8, v215
v_add_u32 v235, 128, v235
v_and_b32 v234, 1, v127
v_mul_u32_u24 v234, 43690, v234
v_add_u32 v234, 17476, v234
v_bfe_u32 v215, v127, 2, 2
v_and_b32 v216, 3, v127
v_lshlrev_b32 v252, 7, v215
v_lshl_add_u32 v252, v216, 4, v252
v_and_b32 v251, 8, v127
v_lshl_add_u32 v252, v251, 3, v252
v_mul_u32_u24 v251, 2056, v212
s_xor_b32 s77, %[swave], 0
s_and_b32 s91, s77, 1
s_lshl_b32 s91, s91, 6
s_lshr_b32 s77, s77, 1
s_lshl_b32 s77, s77, 10
s_add_u32 s77, s77, %[slds]
s_add_u32 s77, s77, 65536
v_xor_b32 v192, s91, v252
v_add3_u32 v192, s77, v192, v251
v_add_u32 v196, 49344, v192
s_xor_b32 s77, %[swave], 1
s_and_b32 s91, s77, 1
s_lshl_b32 s91, s91, 6
s_lshr_b32 s77, s77, 1
s_lshl_b32 s77, s77, 10
s_add_u32 s77, s77, %[slds]
s_add_u32 s77, s77, 65536
v_xor_b32 v193, s91, v252
v_add3_u32 v193, s77, v193, v251
v_add_u32 v197, 49344, v193
s_xor_b32 s77, %[swave], 2
s_and_b32 s91, s77, 1
s_lshl_b32 s91, s91, 6
s_lshr_b32 s77, s77, 1
s_lshl_b32 s77, s77, 10
s_add_u32 s77, s77, %[slds]
s_add_u32 s77, s77, 65536
v_xor_b32 v194, s91, v252
v_add3_u32 v194, s77, v194, v251
v_add_u32 v198, 49344, v194
s_xor_b32 s77, %[swave], 3
s_and_b32 s91, s77, 1
s_lshl_b32 s91, s91, 6
s_lshr_b32 s77, s77, 1
s_lshl_b32 s77, s77, 10
s_add_u32 s77, s77, %[slds]
s_add_u32 s77, s77, 65536
v_xor_b32 v195, s91, v252
v_add3_u32 v195, s77, v195, v251
v_add_u32 v199, 49344, v195
v_mov_b32 v236, 0x3f803f80
v_mov_b32 v184, 0
v_mov_b32 v237, 0x3f803f80
v_mov_b32 v185, 0
v_mov_b32 v238, 0x3f803f80
v_mov_b32 v188, 0
v_mov_b32 v239, 0x3f803f80
v_mov_b32 v189, 0
s_lshl_b32 s90, s49, 8
s_waitcnt lgkmcnt(0)
s_add_u32 s16, s16, s90
s_addc_u32 s17, s17, 0
s_sub_u32 s18, 5242880, s90
s_mov_b32 s19, 131072
s_mov_b32 s34, 2621440
s_mov_b32 s35, 131072
s_mov_b32 s22, 67108864
s_mov_b32 s23, 131072
s_add_u32 s87, s89, s85
s_add_u32 s85, s85, 524288
s_cmp_ge_u32 s85, 5242880
s_cselect_b32 s85, 0, s85
s_add_u32 m0, s96, 65536
s_nop 0
buffer_load_dwordx4 v253, s[16:19], s87 offen offset:0 lds
s_add_u32 m0, s96, 66560
s_nop 0
buffer_load_dwordx4 v254, s[16:19], s87 offen offset:0 lds
s_add_u32 s87, s87, 65536
s_add_u32 m0, s96, 67592
s_nop 0
buffer_load_dwordx4 v253, s[16:19], s87 offen offset:0 lds
s_add_u32 m0, s96, 68616
s_nop 0
buffer_load_dwordx4 v254, s[16:19], s87 offen offset:0 lds
s_add_u32 s88, s68, s86
s_add_u32 s86, s86, 128
s_cmp_ge_u32 s86, 1280
s_cselect_b32 s86, 0, s86
s_add_u32 m0, s94, 0
s_add_u32 s95, s88, 0
buffer_load_dwordx4 v220, s[32:35], s95 offen offset:0 lds
s_add_u32 s95, s88, 9216
buffer_load_dwordx4 v221, s[32:35], s95 offen offset:1024 lds
s_add_u32 s95, s88, 18432
buffer_load_dwordx4 v220, s[32:35], s95 offen offset:2048 lds
s_add_u32 s95, s88, 27648
buffer_load_dwordx4 v221, s[32:35], s95 offen offset:3072 lds
s_mov_b32 s66, 32768
buffer_load_dword v169, off, s[20:23], s66 sc1
buffer_load_dwordx4 v[128:131], v168, s[32:35], s82 offen offset:0
buffer_load_dwordx4 v[132:135], v168, s[32:35], s82 offen offset:512
buffer_load_dwordx2 v[136:137], v181, s[32:35], s82 offen offset:1024
s_add_u32 s82, s82, 131072
s_sub_u32 s88, s88, s63
s_add_u32 m0, s94, 4096
s_add_u32 s95, s88, 40960
buffer_load_dwordx4 v220, s[32:35], s95 offen offset:0 lds
s_add_u32 s95, s88, 50176
buffer_load_dwordx4 v221, s[32:35], s95 offen offset:1024 lds
s_add_u32 s95, s88, 59392
buffer_load_dwordx4 v220, s[32:35], s95 offen offset:2048 lds
s_add_u32 s95, s88, 68608
buffer_load_dwordx4 v221, s[32:35], s95 offen offset:3072 lds
s_getpc_b64 s[98:99]
LTA0_%=:
s_min_u32 s73, %[swave], 3
s_lshl_b32 s73, s73, 12
s_add_u32 s73, s73, LPG1_%=-LTA0_%=
s_add_u32 s98, s98, s73
s_addc_u32 s99, s99, 0
s_mov_b64 exec, 0
s_setpc_b64 s[98:99]
LTR0_%=:
s_mov_b64 exec, -1
s_add_u32 s87, s89, s85
s_add_u32 s85, s85, 524288
s_cmp_ge_u32 s85, 5242880
s_cselect_b32 s85, 0, s85
s_add_u32 m0, s96, 81984
s_nop 0
buffer_load_dwordx4 v253, s[16:19], s87 offen offset:0 lds
s_add_u32 m0, s96, 83008
s_nop 0
buffer_load_dwordx4 v254, s[16:19], s87 offen offset:0 lds
s_add_u32 s87, s87, 65536
s_add_u32 m0, s96, 84040
s_nop 0
buffer_load_dwordx4 v253, s[16:19], s87 offen offset:0 lds
s_add_u32 m0, s96, 85064
s_nop 0
buffer_load_dwordx4 v254, s[16:19], s87 offen offset:0 lds
s_add_u32 s88, s68, s86
s_add_u32 s86, s86, 128
s_cmp_ge_u32 s86, 1280
s_cselect_b32 s86, 0, s86
s_add_u32 m0, s94, 32768
s_add_u32 s95, s88, 0
buffer_load_dwordx4 v220, s[32:35], s95 offen offset:0 lds
s_add_u32 s95, s88, 9216
buffer_load_dwordx4 v221, s[32:35], s95 offen offset:1024 lds
s_add_u32 s95, s88, 18432
buffer_load_dwordx4 v220, s[32:35], s95 offen offset:2048 lds
s_add_u32 s95, s88, 27648
buffer_load_dwordx4 v221, s[32:35], s95 offen offset:3072 lds
s_sub_u32 s88, s88, s63
s_add_u32 m0, s94, 36864
s_add_u32 s95, s88, 40960
buffer_load_dwordx4 v220, s[32:35], s95 offen offset:0 lds
s_add_u32 s95, s88, 50176
buffer_load_dwordx4 v221, s[32:35], s95 offen offset:1024 lds
s_add_u32 s95, s88, 59392
buffer_load_dwordx4 v220, s[32:35], s95 offen offset:2048 lds
s_add_u32 s95, s88, 68608
buffer_load_dwordx4 v221, s[32:35], s95 offen offset:3072 lds
s_waitcnt vmcnt(20)
RD a[128:131],v222 offset:0
RD a[132:135],v223 offset:0
RD a[136:139],v222 offset:2048
RD a[140:143],v223 offset:2048
s_barrier
TR a[0:1],v192 offset:0
TR a[2:3],v192 offset:512
TR a[4:5],v192 offset:8224
TR a[6:7],v192 offset:8736
TR a[8:9],v192 offset:8
TR a[10:11],v192 offset:520
TR a[12:13],v192 offset:8232
TR a[14:15],v192 offset:8744
TR a[16:17],v193 offset:0
TR a[18:19],v193 offset:512
TR a[20:21],v193 offset:8224
TR a[22:23],v193 offset:8736
TR a[24:25],v193 offset:8
TR a[26:27],v193 offset:520
TR a[28:29],v193 offset:8232
TR a[30:31],v193 offset:8744
s_waitcnt lgkmcnt(15)
s_waitcnt lgkmcnt(12)
MM v[0:3],a[0:3],a[128:131],0
s_add_u32 s87, s89, s85
TR a[32:33],v194 offset:0
TR a[34:35],v194 offset:512
MM v[0:3],a[4:7],a[132:135],v[0:3]
s_add_u32 s85, s85, 524288
TR a[36:37],v194 offset:8224
TR a[38:39],v194 offset:8736
MM v[8:11],a[4:7],a[140:143],0
s_waitcnt vmcnt(16)
TR a[40:41],v194 offset:8
TR a[42:43],v194 offset:520
MM v[8:11],a[0:3],a[136:139],v[8:11]
s_waitcnt lgkmcnt(14)
TR a[44:45],v194 offset:8232
TR a[46:47],v194 offset:8744
s_cmp_ge_u32 s85, 5242880
s_cselect_b32 s85, 0, s85
MM v[4:7],a[8:11],a[128:131],0
s_add_u32 m0, s96, 98432
v_max3_f32 v200, |v0|, |v1|, |v2|
v_max_f32 v200, v200, |v3|
v_readfirstlane_b32 s69, v169
MM v[4:7],a[12:15],a[132:135],v[4:7]
s_lshl_b32 s90, s76, 1
TR a[48:49],v195 offset:0
TR a[50:51],v195 offset:512
MM v[12:15],a[12:15],a[140:143],0
s_and_b32 s77, s90, 15
buffer_load_dwordx4 v253, s[16:19], s87 offen offset:0 lds
v_max3_f32 v201, |v8|, |v9|, |v10|
v_max_f32 v201, v201, |v11|
MM v[12:15],a[8:11],a[136:139],v[12:15]
s_add_u32 m0, s96, 99456
TR a[52:53],v195 offset:8224
v_min_f32 v200, v200, v201
v_and_b32 v200, 0x7f800000, v200
v_lshlrev_b32 v148, 16, v128
v_smfmac_f32_16x16x64_bf16 v[184:187], v[236:239], a[0:7], v234
s_lshl_b32 s77, s77, 1
v_add_u32 v200, 0x2c00000, v200
v_smfmac_f32_16x16x64_bf16 v[188:191], v[236:239], a[8:15], v234
s_waitcnt lgkmcnt(15)
v_min_u32 v200, 0x78400000, v200
v_mov_b32 v201, v200
v_mov_b32 v202, v200
v_mov_b32 v203, v200
s_nop 1
MM v[32:35],a[16:19],a[128:131],v[200:203]
s_bfe_u32 s91, s90, 65540
TR a[54:55],v195 offset:8736
buffer_load_dwordx4 v254, s[16:19], s87 offen offset:0 lds
v_mov_b32 v250, v200
v_and_b32 v149, 0xffff0000, v128
MM v[32:35],a[20:23],a[132:135],v[32:35]
s_add_u32 s87, s87, 65536
TR a[56:57],v195 offset:8
TR a[58:59],v195 offset:520
MM v[40:43],a[20:23],a[140:143],v[200:203]
s_add_u32 m0, s96, 100488
TR a[60:61],v195 offset:8232
TR a[62:63],v195 offset:8744
MM v[40:43],a[16:19],a[136:139],v[40:43]
s_waitcnt lgkmcnt(15)
buffer_load_dwordx4 v253, s[16:19], s87 offen offset:0 lds
v_lshlrev_b32 v150, 16, v129
MM v[36:39],a[24:27],a[128:131],v[200:203]
s_add_u32 m0, s96, 101512
v_and_b32 v151, 0xffff0000, v129
v_lshlrev_b32 v152, 16, v130
MM v[36:39],a[28:31],a[132:135],v[36:39]
s_add_u32 s77, s77, s91
v_and_b32 v153, 0xffff0000, v130
v_lshlrev_b32 v154, 16, v131
MM v[44:47],a[28:31],a[140:143],v[200:203]
s_and_b32 s91, s90, 0xffffffe0
buffer_load_dwordx4 v254, s[16:19], s87 offen offset:0 lds
MM v[44:47],a[24:27],a[136:139],v[44:47]
s_waitcnt lgkmcnt(12)
s_waitcnt vmcnt(16)
RD a[144:147],v222 offset:4096
v_and_b32 v155, 0xffff0000, v131
MM v[64:67],a[32:35],a[128:131],v[200:203]
s_add_u32 s77, s77, s91
RD a[148:151],v223 offset:4096
RD a[152:155],v222 offset:6144
MM v[64:67],a[36:39],a[132:135],v[64:67]
s_lshl_b32 s77, s77, 3
RD a[156:159],v223 offset:6144
v_lshlrev_b32 v156, 16, v132
MM v[72:75],a[36:39],a[140:143],v[200:203]
s_add_u32 s97, s77, 36864
s_waitcnt vmcnt(12)
s_barrier
v_and_b32 v157, 0xffff0000, v132
MM v[72:75],a[32:35],a[136:139],v[72:75]
s_waitcnt lgkmcnt(12)
global_load_dwordx4 v[138:141], v182, s[44:45] offset:0
MM v[68:71],a[40:43],a[128:131],v[200:203]
s_sub_u32 s79, 65535, s90
v_lshlrev_b32 v158, 16, v133
v_and_b32 v159, 0xffff0000, v133
MM v[68:71],a[44:47],a[132:135],v[68:71]
s_lshl_b32 s78, s90, 2
v_lshlrev_b32 v160, 16, v134
v_and_b32 v161, 0xffff0000, v134
MM v[76:79],a[44:47],a[140:143],v[200:203]
s_add_u32 s78, s78, 53248
global_load_dwordx4 v[142:145], v182, s[44:45] offset:512
MM v[76:79],a[40:43],a[136:139],v[76:79]
s_waitcnt lgkmcnt(8)
v_lshrrev_b32 v182, 1, v182
v_lshlrev_b32 v162, 16, v135
MM v[96:99],a[48:51],a[128:131],v[200:203]
s_lshl_b32 s80, s76, 2
global_load_dwordx2 v[146:147], v182, s[44:45] offset:1024
)ASM"
R"ASM(
MM v[96:99],a[52:55],a[132:135],v[96:99]
s_waitcnt lgkmcnt(0)
s_add_u32 s88, s68, s86
s_add_u32 s86, s86, 128
v_and_b32 v163, 0xffff0000, v135
MM v[104:107],a[52:55],a[140:143],v[200:203]
s_add_u32 s80, s80, 81920
s_cmp_ge_u32 s86, 1280
s_cselect_b32 s86, 0, s86
MM v[104:107],a[48:51],a[136:139],v[104:107]
s_add_u32 m0, s94, 0
s_add_u32 s95, s88, 0
v_lshlrev_b32 v164, 16, v136
MM v[100:103],a[56:59],a[128:131],v[200:203]
buffer_load_dwordx4 v220, s[32:35], s95 offen offset:0 lds
MM v[100:103],a[60:63],a[132:135],v[100:103]
s_add_u32 s95, s88, 9216
v_and_b32 v165, 0xffff0000, v136
v_lshlrev_b32 v166, 16, v137
MM v[108:111],a[60:63],a[140:143],v[200:203]
buffer_load_dwordx4 v221, s[32:35], s95 offen offset:1024 lds
MM v[108:111],a[56:59],a[136:139],v[108:111]
s_add_u32 s95, s88, 18432
v_and_b32 v167, 0xffff0000, v137
s_add_u32 s69, s69, 1
MM v[16:19],a[0:3],a[144:147],0
v_add_f32 v170, v148, v149
v_add_f32 v171, v150, v151
MM v[16:19],a[4:7],a[148:151],v[16:19]
v_add_f32 v170, v170, v152
v_add_f32 v171, v171, v153
MM v[24:27],a[4:7],a[156:159],0
buffer_load_dwordx4 v220, s[32:35], s95 offen offset:2048 lds
MM v[24:27],a[0:3],a[152:155],v[24:27]
s_add_u32 s95, s88, 27648
v_add_f32 v170, v170, v154
v_add_f32 v171, v171, v155
MM v[20:23],a[8:11],a[144:147],0
v_max3_f32 v204, |v16|, |v17|, |v18|
v_max_f32 v204, v204, |v19|
MM v[20:23],a[12:15],a[148:151],v[20:23]
TR a[0:1],v192 offset:16448
TR a[2:3],v192 offset:16960
v_add_f32 v170, v170, v156
MM v[28:31],a[12:15],a[156:159],0
v_max3_f32 v205, |v24|, |v25|, |v26|
v_max_f32 v205, v205, |v27|
MM v[28:31],a[8:11],a[152:155],v[28:31]
TR a[4:5],v192 offset:24672
v_min_f32 v204, v204, v205
v_and_b32 v204, 0x7f800000, v204
v_add_u32 v204, 0x2c00000, v204
v_min_u32 v204, 0x78400000, v204
v_mov_b32 v205, v204
v_mov_b32 v206, v204
v_mov_b32 v207, v204
s_nop 1
MM v[48:51],a[16:19],a[144:147],v[204:207]
TR a[6:7],v192 offset:25184
buffer_load_dwordx4 v221, s[32:35], s95 offen offset:3072 lds
v_mov_b32 v252, v204
v_add_f32 v171, v171, v157
MM v[48:51],a[20:23],a[148:151],v[48:51]
s_sub_u32 s88, s88, s63
TR a[8:9],v192 offset:16456
TR a[10:11],v192 offset:16968
MM v[56:59],a[20:23],a[156:159],v[204:207]
s_add_u32 m0, s94, 4096
TR a[12:13],v192 offset:24680
TR a[14:15],v192 offset:25192
MM v[56:59],a[16:19],a[152:155],v[56:59]
s_add_u32 s95, s88, 40960
buffer_load_dwordx4 v220, s[32:35], s95 offen offset:0 lds
v_add_f32 v170, v170, v158
MM v[52:55],a[24:27],a[144:147],v[204:207]
s_add_u32 s95, s88, 50176
TR a[16:17],v193 offset:16448
TR a[18:19],v193 offset:16960
MM v[52:55],a[28:31],a[148:151],v[52:55]
TR a[20:21],v193 offset:24672
TR a[22:23],v193 offset:25184
MM v[60:63],a[28:31],a[156:159],v[204:207]
buffer_load_dwordx4 v221, s[32:35], s95 offen offset:1024 lds
v_add_f32 v171, v171, v159
MM v[60:63],a[24:27],a[152:155],v[60:63]
s_add_u32 s95, s88, 59392
v_add_f32 v170, v170, v160
v_add_f32 v171, v171, v161
MM v[80:83],a[32:35],a[144:147],v[204:207]
TR a[24:25],v193 offset:16456
TR a[26:27],v193 offset:16968
MM v[80:83],a[36:39],a[148:151],v[80:83]
TR a[28:29],v193 offset:24680
TR a[30:31],v193 offset:25192
v_add_f32 v170, v170, v162
MM v[88:91],a[36:39],a[156:159],v[204:207]
buffer_load_dwordx4 v220, s[32:35], s95 offen offset:2048 lds
MM v[88:91],a[32:35],a[152:155],v[88:91]
s_add_u32 s95, s88, 68608
v_add_f32 v171, v171, v163
v_add_f32 v170, v170, v164
MM v[84:87],a[40:43],a[144:147],v[204:207]
TR a[32:33],v194 offset:16448
TR a[34:35],v194 offset:16960
MM v[84:87],a[44:47],a[148:151],v[84:87]
TR a[36:37],v194 offset:24672
TR a[38:39],v194 offset:25184
v_add_f32 v171, v171, v165
MM v[92:95],a[44:47],a[156:159],v[204:207]
buffer_load_dwordx4 v221, s[32:35], s95 offen offset:3072 lds
MM v[92:95],a[40:43],a[152:155],v[92:95]
s_add_u32 s87, s89, s85
s_add_u32 s85, s85, 524288
v_add_f32 v170, v170, v166
MM v[112:115],a[48:51],a[144:147],v[204:207]
TR a[40:41],v194 offset:16456
TR a[42:43],v194 offset:16968
MM v[112:115],a[52:55],a[148:151],v[112:115]
TR a[44:45],v194 offset:24680
TR a[46:47],v194 offset:25192
s_cmp_ge_u32 s85, 5242880
s_cselect_b32 s85, 0, s85
v_add_f32 v171, v171, v167
MM v[120:123],a[52:55],a[156:159],v[204:207]
s_add_u32 m0, s96, 114880
s_nop 0
buffer_load_dwordx4 v253, s[16:19], s87 offen offset:0 lds
MM v[120:123],a[48:51],a[152:155],v[120:123]
s_add_u32 m0, s96, 115904
v_add_f32 v172, v170, v171
v_lshlrev_b32 v232, 11, v180
MM v[116:119],a[56:59],a[144:147],v[204:207]
TR a[48:49],v195 offset:16448
TR a[50:51],v195 offset:16960
MM v[116:119],a[60:63],a[148:151],v[116:119]
TR a[52:53],v195 offset:24672
TR a[54:55],v195 offset:25184
MM v[124:127],a[60:63],a[156:159],v[204:207]
buffer_load_dwordx4 v254, s[16:19], s87 offen offset:0 lds
v_add_f32_dpp v172, v172, v172 row_ror:8 row_mask:0xf bank_mask:0xf
MM v[124:127],a[56:59],a[152:155],v[124:127]
s_add_u32 s87, s87, 65536
s_add_u32 m0, s96, 116936
v_add_f32_dpp v172, v172, v172 row_ror:4 row_mask:0xf bank_mask:0xf
buffer_load_dwordx4 v253, s[16:19], s87 offen offset:0 lds
s_add_u32 m0, s96, 117960
s_nop 0
buffer_load_dwordx4 v254, s[16:19], s87 offen offset:0 lds
s_waitcnt vmcnt(19)
RD a[160:163],v222 offset:32768
RD a[164:167],v223 offset:32768
RD a[168:171],v222 offset:34816
RD a[172:175],v223 offset:34816
RD a[176:179],v222 offset:36864
RD a[180:183],v223 offset:36864
RD a[184:187],v222 offset:38912
RD a[188:191],v223 offset:38912
s_waitcnt vmcnt(15) lgkmcnt(0)
s_barrier
MM v[0:3],a[0:3],a[160:163],v[0:3]
TR a[56:57],v195 offset:16456
TR a[58:59],v195 offset:16968
MM v[0:3],a[4:7],a[164:167],v[0:3]
TR a[60:61],v195 offset:24680
TR a[62:63],v195 offset:25192
MM v[8:11],a[4:7],a[172:175],v[8:11]
v_add_f32_dpp v172, v172, v172 row_ror:2 row_mask:0xf bank_mask:0xf
v_lshl_add_u32 v232, v214, 8, v232
MM v[8:11],a[0:3],a[168:171],v[8:11]
s_add_u32 s88, s68, s86
s_add_u32 s86, s86, 128
MM v[16:19],a[0:3],a[176:179],v[16:19]
s_cmp_ge_u32 s86, 1280
s_cselect_b32 s86, 0, s86
s_add_u32 m0, s94, 32768
MM v[16:19],a[4:7],a[180:183],v[16:19]
s_add_u32 s95, s88, 0
buffer_load_dwordx4 v220, s[32:35], s95 offen offset:0 lds
v_add_f32_dpp v172, v172, v172 row_ror:1 row_mask:0xf bank_mask:0xf
MM v[24:27],a[4:7],a[188:191],v[24:27]
s_add_u32 s95, s88, 9216
v_mov_b32 v173, v172
v_lshl_add_u32 v232, v215, 6, v232
MM v[24:27],a[0:3],a[184:187],v[24:27]
buffer_load_dwordx4 v221, s[32:35], s95 offen offset:1024 lds
MM v[4:7],a[8:11],a[160:163],v[4:7]
s_add_u32 s95, s88, 18432
v_permlane16_swap_b32 v172, v173
v_add_f32 v172, v172, v173
MM v[4:7],a[12:15],a[164:167],v[4:7]
v_mul_f32 v172, 0x3acccccd, v172
v_sub_f32 v176, v148, v172
MM v[12:15],a[12:15],a[172:175],v[12:15]
v_sub_f32 v177, v149, v172
v_mul_f32 v170, v176, v176
MM v[12:15],a[8:11],a[168:171],v[12:15]
buffer_load_dwordx4 v220, s[32:35], s95 offen offset:2048 lds
MM v[20:23],a[8:11],a[176:179],v[20:23]
s_add_u32 s95, s88, 27648
v_mul_f32 v171, v177, v177
v_sub_f32 v176, v150, v172
MM v[20:23],a[12:15],a[180:183],v[20:23]
v_sub_f32 v177, v151, v172
v_fma_f32 v170, v176, v176, v170
MM v[28:31],a[12:15],a[188:191],v[28:31]
v_fma_f32 v171, v177, v177, v171
v_sub_f32 v176, v152, v172
MM v[28:31],a[8:11],a[184:187],v[28:31]
buffer_load_dwordx4 v221, s[32:35], s95 offen offset:3072 lds
v_smfmac_f32_16x16x64_bf16 v[184:187], v[236:239], a[0:7], v234
s_sub_u32 s88, s88, s63
s_add_u32 m0, s94, 36864
v_sub_f32 v177, v153, v172
v_smfmac_f32_16x16x64_bf16 v[188:191], v[236:239], a[8:15], v234
s_add_u32 s95, s88, 40960
v_fma_f32 v170, v176, v176, v170
MM v[32:35],a[16:19],a[160:163],v[32:35]
TR a[0:1],v192 offset:32896
s_nop 0
LPG1_%=:
s_cbranch_execz LPG5_%=
TR a[2:3],v192 offset:33408
MM v[32:35],a[20:23],a[164:167],v[32:35]
TR a[4:5],v192 offset:41120
TR a[6:7],v192 offset:41632
v_fma_f32 v171, v177, v177, v171
MM v[40:43],a[20:23],a[172:175],v[40:43]
TR a[8:9],v192 offset:32904
TR a[10:11],v192 offset:33416
MM v[40:43],a[16:19],a[168:171],v[40:43]
TR a[12:13],v192 offset:41128
TR a[14:15],v192 offset:41640
buffer_load_dwordx4 v220, s[32:35], s95 offen offset:0 lds
MM v[48:51],a[16:19],a[176:179],v[48:51]
s_add_u32 s95, s88, 50176
v_sub_f32 v176, v154, v172
v_sub_f32 v177, v155, v172
MM v[48:51],a[20:23],a[180:183],v[48:51]
buffer_load_dwordx4 v221, s[32:35], s95 offen offset:1024 lds
MM v[56:59],a[20:23],a[188:191],v[56:59]
s_add_u32 s95, s88, 59392
v_fma_f32 v170, v176, v176, v170
v_fma_f32 v171, v177, v177, v171
MM v[56:59],a[16:19],a[184:187],v[56:59]
v_sub_f32 v176, v156, v172
v_sub_f32 v177, v157, v172
MM v[36:39],a[24:27],a[160:163],v[36:39]
TR a[16:17],v193 offset:32896
TR a[18:19],v193 offset:33408
MM v[36:39],a[28:31],a[164:167],v[36:39]
TR a[20:21],v193 offset:41120
TR a[22:23],v193 offset:41632
v_fma_f32 v170, v176, v176, v170
MM v[44:47],a[28:31],a[172:175],v[44:47]
buffer_load_dwordx4 v220, s[32:35], s95 offen offset:2048 lds
MM v[44:47],a[24:27],a[168:171],v[44:47]
s_add_u32 s95, s88, 68608
v_fma_f32 v171, v177, v177, v171
v_sub_f32 v176, v158, v172
MM v[52:55],a[24:27],a[176:179],v[52:55]
v_sub_f32 v177, v159, v172
v_fma_f32 v170, v176, v176, v170
MM v[52:55],a[28:31],a[180:183],v[52:55]
buffer_load_dwordx4 v221, s[32:35], s95 offen offset:3072 lds
MM v[60:63],a[28:31],a[188:191],v[60:63]
s_add_u32 s87, s89, s85
s_add_u32 s85, s85, 524288
v_fma_f32 v171, v177, v177, v171
MM v[60:63],a[24:27],a[184:187],v[60:63]
s_cmp_ge_u32 s85, 5242880
s_cselect_b32 s85, 0, s85
s_add_u32 m0, s96, 131328
MM v[64:67],a[32:35],a[160:163],v[64:67]
TR a[24:25],v193 offset:32904
TR a[26:27],v193 offset:33416
v_sub_f32 v176, v160, v172
MM v[64:67],a[36:39],a[164:167],v[64:67]
TR a[28:29],v193 offset:41128
TR a[30:31],v193 offset:41640
MM v[72:75],a[36:39],a[172:175],v[72:75]
buffer_load_dwordx4 v253, s[16:19], s87 offen offset:0 lds
MM v[72:75],a[32:35],a[168:171],v[72:75]
s_add_u32 m0, s96, 132352
v_sub_f32 v177, v161, v172
v_fma_f32 v170, v176, v176, v170
MM v[80:83],a[32:35],a[176:179],v[80:83]
v_fma_f32 v171, v177, v177, v171
v_sub_f32 v176, v162, v172
MM v[80:83],a[36:39],a[180:183],v[80:83]
buffer_load_dwordx4 v254, s[16:19], s87 offen offset:0 lds
MM v[88:91],a[36:39],a[188:191],v[88:91]
s_add_u32 s87, s87, 65536
s_add_u32 m0, s96, 133384
v_sub_f32 v177, v163, v172
MM v[88:91],a[32:35],a[184:187],v[88:91]
v_fma_f32 v170, v176, v176, v170
v_fma_f32 v171, v177, v177, v171
MM v[68:71],a[40:43],a[160:163],v[68:71]
TR a[32:33],v194 offset:32896
TR a[34:35],v194 offset:33408
MM v[68:71],a[44:47],a[164:167],v[68:71]
TR a[36:37],v194 offset:41120
TR a[38:39],v194 offset:41632
v_sub_f32 v176, v164, v172
MM v[76:79],a[44:47],a[172:175],v[76:79]
buffer_load_dwordx4 v253, s[16:19], s87 offen offset:0 lds
MM v[76:79],a[40:43],a[168:171],v[76:79]
s_add_u32 m0, s96, 134408
v_sub_f32 v177, v165, v172
v_fma_f32 v170, v176, v176, v170
MM v[84:87],a[40:43],a[176:179],v[84:87]
v_fma_f32 v171, v177, v177, v171
v_sub_f32 v176, v166, v172
MM v[84:87],a[44:47],a[180:183],v[84:87]
buffer_load_dwordx4 v254, s[16:19], s87 offen offset:0 lds
MM v[92:95],a[44:47],a[188:191],v[92:95]
s_waitcnt vmcnt(16)
RD a[192:195],v222 offset:0
v_sub_f32 v177, v167, v172
MM v[92:95],a[40:43],a[184:187],v[92:95]
RD a[196:199],v223 offset:0
v_fma_f32 v170, v176, v176, v170
MM v[96:99],a[48:51],a[160:163],v[96:99]
TR a[40:41],v194 offset:32904
TR a[42:43],v194 offset:33416
MM v[96:99],a[52:55],a[164:167],v[96:99]
TR a[44:45],v194 offset:41128
TR a[46:47],v194 offset:41640
v_fma_f32 v171, v177, v177, v171
MM v[104:107],a[52:55],a[172:175],v[104:107]
RD a[200:203],v222 offset:2048
v_add_f32 v170, v170, v171
MM v[104:107],a[48:51],a[168:171],v[104:107]
RD a[204:207],v223 offset:2048
v_add_f32_dpp v170, v170, v170 row_ror:8 row_mask:0xf bank_mask:0xf
MM v[112:115],a[48:51],a[176:179],v[112:115]
RD a[208:211],v222 offset:4096
v_add_f32_dpp v170, v170, v170 row_ror:4 row_mask:0xf bank_mask:0xf
MM v[112:115],a[52:55],a[180:183],v[112:115]
RD a[212:215],v223 offset:4096
v_add_f32_dpp v170, v170, v170 row_ror:2 row_mask:0xf bank_mask:0xf
MM v[120:123],a[52:55],a[188:191],v[120:123]
RD a[216:219],v222 offset:6144
v_add_f32_dpp v170, v170, v170 row_ror:1 row_mask:0xf bank_mask:0xf
MM v[120:123],a[48:51],a[184:187],v[120:123]
)ASM"
R"ASM(
s_waitcnt lgkmcnt(15)
RD a[220:223],v223 offset:6144
v_mov_b32 v173, v170
MM v[100:103],a[56:59],a[160:163],v[100:103]
TR a[48:49],v195 offset:32896
TR a[50:51],v195 offset:33408
MM v[100:103],a[60:63],a[164:167],v[100:103]
TR a[52:53],v195 offset:41120
TR a[54:55],v195 offset:41632
MM v[108:111],a[60:63],a[172:175],v[108:111]
v_permlane16_swap_b32 v170, v173
v_add_f32 v170, v170, v173
MM v[108:111],a[56:59],a[168:171],v[108:111]
s_waitcnt lgkmcnt(4)
s_add_u32 s88, s68, s86
s_add_u32 s86, s86, 128
MM v[116:119],a[56:59],a[176:179],v[116:119]
s_cmp_ge_u32 s86, 1280
s_cselect_b32 s86, 0, s86
s_add_u32 m0, s94, 0
v_mul_f32 v170, 0x3acccccd, v170
MM v[116:119],a[60:63],a[180:183],v[116:119]
s_add_u32 s95, s88, 0
buffer_load_dwordx4 v220, s[32:35], s95 offen offset:0 lds
MM v[124:127],a[60:63],a[188:191],v[124:127]
s_add_u32 s95, s88, 9216
CV v178,v172,v170
v_lshlrev_b32 v174, 16, v178
MM v[124:127],a[56:59],a[184:187],v[124:127]
buffer_load_dwordx4 v221, s[32:35], s95 offen offset:1024 lds
s_waitcnt vmcnt(14)
s_barrier
MM v[0:3],a[0:3],a[192:195],v[0:3]
s_add_u32 s95, s88, 18432
TR a[56:57],v195 offset:32904
TR a[58:59],v195 offset:33416
v_and_b32 v170, 0xffff0000, v178
MM v[0:3],a[4:7],a[196:199],v[0:3]
TR a[60:61],v195 offset:41128
TR a[62:63],v195 offset:41640
MM v[8:11],a[4:7],a[204:207],v[8:11]
v_add_f32 v170, 0x3727c5ac, v170
CV v170,v170,v170
MM v[8:11],a[0:3],a[200:203],v[8:11]
buffer_load_dwordx4 v220, s[32:35], s95 offen offset:2048 lds
MM v[16:19],a[0:3],a[208:211],v[16:19]
s_add_u32 s95, s88, 27648
v_and_b32 v170, 0xffff0000, v170
v_lshl_add_u32 v232, v216, 3, v232
MM v[16:19],a[4:7],a[212:215],v[16:19]
v_sqrt_f32 v170, v170
MM v[24:27],a[4:7],a[220:223],v[24:27]
v_mov_b32 v179, s69
CV v178,v174,v170
MM v[24:27],a[0:3],a[216:219],v[24:27]
buffer_load_dwordx4 v221, s[32:35], s95 offen offset:3072 lds
MM v[4:7],a[8:11],a[192:195],v[4:7]
s_sub_u32 s88, s88, s63
s_add_u32 m0, s94, 4096
s_add_u32 s95, s88, 40960
v_and_b32 v170, 0xffff0000, v178
MM v[4:7],a[12:15],a[196:199],v[4:7]
v_rcp_f32 v175, v170
MM v[12:15],a[12:15],a[204:207],v[12:15]
v_lshlrev_b32 v173, 4, v180
v_lshl_add_u32 v232, v214, 5, v232
MM v[12:15],a[8:11],a[200:203],v[12:15]
buffer_load_dwordx4 v220, s[32:35], s95 offen offset:0 lds
MM v[20:23],a[8:11],a[208:211],v[20:23]
s_add_u32 s95, s88, 50176
s_mov_b32 exec_lo, 1
s_mov_b32 exec_hi, 1
buffer_store_dwordx2 v[178:179], v173, s[20:23], s97 offen offset:0 sc1
s_mov_b64 exec, -1
MM v[20:23],a[12:15],a[212:215],v[20:23]
v_sub_f32 v148, v148, v174
v_sub_f32 v149, v149, v174
MM v[28:31],a[12:15],a[220:223],v[28:31]
v_sub_f32 v150, v150, v174
v_sub_f32 v151, v151, v174
MM v[28:31],a[8:11],a[216:219],v[28:31]
buffer_load_dwordx4 v221, s[32:35], s95 offen offset:1024 lds
v_smfmac_f32_16x16x64_bf16 v[184:187], v[236:239], a[0:7], v234
s_add_u32 s95, s88, 59392
v_sub_f32 v152, v152, v174
v_smfmac_f32_16x16x64_bf16 v[188:191], v[236:239], a[8:15], v234
v_sub_f32 v153, v153, v174
MM v[32:35],a[16:19],a[192:195],v[32:35]
TR a[0:1],v196 offset:0
TR a[2:3],v196 offset:512
MM v[32:35],a[20:23],a[196:199],v[32:35]
TR a[4:5],v196 offset:8224
TR a[6:7],v196 offset:8736
v_sub_f32 v154, v154, v174
MM v[40:43],a[20:23],a[204:207],v[40:43]
TR a[8:9],v196 offset:8
TR a[10:11],v196 offset:520
MM v[40:43],a[16:19],a[200:203],v[40:43]
TR a[12:13],v196 offset:8232
TR a[14:15],v196 offset:8744
buffer_load_dwordx4 v220, s[32:35], s95 offen offset:2048 lds
MM v[48:51],a[16:19],a[208:211],v[48:51]
s_add_u32 s95, s88, 68608
v_sub_f32 v155, v155, v174
v_sub_f32 v156, v156, v174
MM v[48:51],a[20:23],a[212:215],v[48:51]
buffer_load_dwordx4 v221, s[32:35], s95 offen offset:3072 lds
MM v[56:59],a[20:23],a[220:223],v[56:59]
s_add_u32 s87, s89, s85
s_add_u32 s85, s85, 524288
v_sub_f32 v157, v157, v174
MM v[56:59],a[16:19],a[216:219],v[56:59]
s_cmp_ge_u32 s85, 5242880
s_cselect_b32 s85, 0, s85
s_add_u32 m0, s96, 65536
MM v[36:39],a[24:27],a[192:195],v[36:39]
TR a[16:17],v197 offset:0
TR a[18:19],v197 offset:512
v_sub_f32 v158, v158, v174
MM v[36:39],a[28:31],a[196:199],v[36:39]
TR a[20:21],v197 offset:8224
TR a[22:23],v197 offset:8736
MM v[44:47],a[28:31],a[204:207],v[44:47]
buffer_load_dwordx4 v253, s[16:19], s87 offen offset:0 lds
MM v[44:47],a[24:27],a[200:203],v[44:47]
s_add_u32 m0, s96, 66560
v_sub_f32 v159, v159, v174
v_sub_f32 v160, v160, v174
MM v[52:55],a[24:27],a[208:211],v[52:55]
v_sub_f32 v161, v161, v174
v_sub_f32 v162, v162, v174
MM v[52:55],a[28:31],a[212:215],v[52:55]
buffer_load_dwordx4 v254, s[16:19], s87 offen offset:0 lds
MM v[60:63],a[28:31],a[220:223],v[60:63]
s_add_u32 s87, s87, 65536
s_add_u32 m0, s96, 67592
v_sub_f32 v163, v163, v174
MM v[60:63],a[24:27],a[216:219],v[60:63]
v_sub_f32 v164, v164, v174
v_sub_f32 v165, v165, v174
MM v[64:67],a[32:35],a[192:195],v[64:67]
TR a[24:25],v197 offset:8
TR a[26:27],v197 offset:520
MM v[64:67],a[36:39],a[196:199],v[64:67]
TR a[28:29],v197 offset:8232
TR a[30:31],v197 offset:8744
v_sub_f32 v166, v166, v174
MM v[72:75],a[36:39],a[204:207],v[72:75]
buffer_load_dwordx4 v253, s[16:19], s87 offen offset:0 lds
MM v[72:75],a[32:35],a[200:203],v[72:75]
s_add_u32 m0, s96, 68616
v_sub_f32 v167, v167, v174
CV v128,v148,v149
MM v[80:83],a[32:35],a[208:211],v[80:83]
CV v129,v150,v151
CV v130,v152,v153
MM v[80:83],a[36:39],a[212:215],v[80:83]
buffer_load_dwordx4 v254, s[16:19], s87 offen offset:0 lds
MM v[88:91],a[36:39],a[220:223],v[88:91]
s_waitcnt vmcnt(17)
RD a[224:227],v222 offset:32768
CV v131,v154,v155
MM v[88:91],a[32:35],a[216:219],v[88:91]
RD a[228:231],v223 offset:32768
CV v132,v156,v157
MM v[68:71],a[40:43],a[192:195],v[68:71]
TR a[32:33],v198 offset:0
TR a[34:35],v198 offset:512
MM v[68:71],a[44:47],a[196:199],v[68:71]
TR a[36:37],v198 offset:8224
TR a[38:39],v198 offset:8736
CV v133,v158,v159
MM v[76:79],a[44:47],a[204:207],v[76:79]
RD a[232:235],v222 offset:34816
CV v134,v160,v161
MM v[76:79],a[40:43],a[200:203],v[76:79]
RD a[236:239],v223 offset:34816
CV v135,v162,v163
MM v[84:87],a[40:43],a[208:211],v[84:87]
RD a[240:243],v222 offset:36864
CV v136,v164,v165
MM v[84:87],a[44:47],a[212:215],v[84:87]
RD a[244:247],v223 offset:36864
CV v137,v166,v167
MM v[92:95],a[44:47],a[220:223],v[92:95]
RD a[248:251],v222 offset:38912
v_lshlrev_b32 v148, 16, v128
MM v[92:95],a[40:43],a[216:219],v[92:95]
s_waitcnt lgkmcnt(15)
RD a[252:255],v223 offset:38912
v_and_b32 v149, 0xffff0000, v128
MM v[96:99],a[48:51],a[192:195],v[96:99]
TR a[40:41],v198 offset:8
TR a[42:43],v198 offset:520
MM v[96:99],a[52:55],a[196:199],v[96:99]
TR a[44:45],v198 offset:8232
TR a[46:47],v198 offset:8744
v_lshlrev_b32 v150, 16, v129
MM v[104:107],a[52:55],a[204:207],v[104:107]
v_and_b32 v151, 0xffff0000, v129
v_lshlrev_b32 v152, 16, v130
MM v[104:107],a[48:51],a[200:203],v[104:107]
s_waitcnt lgkmcnt(4)
s_add_u32 s88, s68, s86
s_add_u32 s86, s86, 128
MM v[112:115],a[48:51],a[208:211],v[112:115]
s_cmp_ge_u32 s86, 1280
s_cselect_b32 s86, 0, s86
s_add_u32 m0, s94, 32768
v_and_b32 v153, 0xffff0000, v130
MM v[112:115],a[52:55],a[212:215],v[112:115]
s_add_u32 s95, s88, 0
buffer_load_dwordx4 v220, s[32:35], s95 offen offset:0 lds
MM v[120:123],a[52:55],a[220:223],v[120:123]
s_add_u32 s95, s88, 9216
v_lshlrev_b32 v154, 16, v131
v_and_b32 v155, 0xffff0000, v131
MM v[120:123],a[48:51],a[216:219],v[120:123]
buffer_load_dwordx4 v221, s[32:35], s95 offen offset:1024 lds
MM v[100:103],a[56:59],a[192:195],v[100:103]
s_add_u32 s95, s88, 18432
TR a[48:49],v199 offset:0
TR a[50:51],v199 offset:512
v_lshlrev_b32 v156, 16, v132
MM v[100:103],a[60:63],a[196:199],v[100:103]
TR a[52:53],v199 offset:8224
TR a[54:55],v199 offset:8736
MM v[108:111],a[60:63],a[204:207],v[108:111]
v_and_b32 v157, 0xffff0000, v132
v_lshlrev_b32 v158, 16, v133
MM v[108:111],a[56:59],a[200:203],v[108:111]
buffer_load_dwordx4 v220, s[32:35], s95 offen offset:2048 lds
MM v[116:119],a[56:59],a[208:211],v[116:119]
s_add_u32 s95, s88, 27648
v_and_b32 v159, 0xffff0000, v133
v_lshlrev_b32 v160, 16, v134
MM v[116:119],a[60:63],a[212:215],v[116:119]
v_and_b32 v161, 0xffff0000, v134
v_lshlrev_b32 v162, 16, v135
MM v[124:127],a[60:63],a[220:223],v[124:127]
v_and_b32 v163, 0xffff0000, v135
v_lshlrev_b32 v164, 16, v136
MM v[124:127],a[56:59],a[216:219],v[124:127]
buffer_load_dwordx4 v221, s[32:35], s95 offen offset:3072 lds
s_waitcnt vmcnt(17)
s_barrier
MM v[0:3],a[0:3],a[224:227],v[0:3]
s_sub_u32 s88, s88, s63
TR a[56:57],v199 offset:8
TR a[58:59],v199 offset:520
v_and_b32 v165, 0xffff0000, v136
MM v[0:3],a[4:7],a[228:231],v[0:3]
s_add_u32 m0, s94, 36864
TR a[60:61],v199 offset:8232
TR a[62:63],v199 offset:8744
MM v[8:11],a[4:7],a[236:239],v[8:11]
s_add_u32 s95, s88, 40960
v_lshlrev_b32 v166, 16, v137
v_and_b32 v167, 0xffff0000, v137
MM v[8:11],a[0:3],a[232:235],v[8:11]
buffer_load_dwordx4 v220, s[32:35], s95 offen offset:0 lds
MM v[16:19],a[0:3],a[240:243],v[16:19]
s_add_u32 s95, s88, 50176
v_mul_f32 v148, v148, v175
v_mul_f32 v149, v149, v175
MM v[16:19],a[4:7],a[244:247],v[16:19]
v_mul_f32 v150, v150, v175
v_mul_f32 v151, v151, v175
MM v[24:27],a[4:7],a[252:255],v[24:27]
v_mul_f32 v152, v152, v175
v_mul_f32 v153, v153, v175
MM v[24:27],a[0:3],a[248:251],v[24:27]
buffer_load_dwordx4 v221, s[32:35], s95 offen offset:1024 lds
MM v[4:7],a[8:11],a[224:227],v[4:7]
s_add_u32 s95, s88, 59392
v_mul_f32 v154, v154, v175
v_mul_f32 v155, v155, v175
MM v[4:7],a[12:15],a[228:231],v[4:7]
v_mul_f32 v156, v156, v175
v_mul_f32 v157, v157, v175
MM v[12:15],a[12:15],a[236:239],v[12:15]
v_mul_f32 v158, v158, v175
v_mul_f32 v159, v159, v175
MM v[12:15],a[8:11],a[232:235],v[12:15]
buffer_load_dwordx4 v220, s[32:35], s95 offen offset:2048 lds
MM v[20:23],a[8:11],a[240:243],v[20:23]
s_add_u32 s95, s88, 68608
v_mul_f32 v160, v160, v175
v_mul_f32 v161, v161, v175
MM v[20:23],a[12:15],a[244:247],v[20:23]
v_mul_f32 v162, v162, v175
v_mul_f32 v163, v163, v175
MM v[28:31],a[12:15],a[252:255],v[28:31]
v_mul_f32 v164, v164, v175
v_mul_f32 v165, v165, v175
MM v[28:31],a[8:11],a[248:251],v[28:31]
buffer_load_dwordx4 v221, s[32:35], s95 offen offset:3072 lds
v_smfmac_f32_16x16x64_bf16 v[184:187], v[236:239], a[0:7], v234
s_add_u32 s87, s89, s85
s_add_u32 s85, s85, 524288
v_mul_f32 v166, v166, v175
v_smfmac_f32_16x16x64_bf16 v[188:191], v[236:239], a[8:15], v234
s_cmp_ge_u32 s85, 5242880
s_cselect_b32 s85, 0, s85
MM v[32:35],a[16:19],a[224:227],v[32:35]
s_add_u32 m0, s96, 81984
TR a[0:1],v196 offset:16448
TR a[2:3],v196 offset:16960
MM v[32:35],a[20:23],a[228:231],v[32:35]
TR a[4:5],v196 offset:24672
TR a[6:7],v196 offset:25184
v_mul_f32 v167, v167, v175
MM v[40:43],a[20:23],a[236:239],v[40:43]
TR a[8:9],v196 offset:16456
TR a[10:11],v196 offset:16968
MM v[40:43],a[16:19],a[232:235],v[40:43]
TR a[12:13],v196 offset:24680
TR a[14:15],v196 offset:25192
buffer_load_dwordx4 v253, s[16:19], s87 offen offset:0 lds
MM v[48:51],a[16:19],a[240:243],v[48:51]
s_add_u32 m0, s96, 83008
CV v128,v148,v149
CV v129,v150,v151
MM v[48:51],a[20:23],a[244:247],v[48:51]
buffer_load_dwordx4 v254, s[16:19], s87 offen offset:0 lds
MM v[56:59],a[20:23],a[252:255],v[56:59]
s_add_u32 s87, s87, 65536
s_add_u32 m0, s96, 84040
CV v130,v152,v153
MM v[56:59],a[16:19],a[248:251],v[56:59]
CV v131,v154,v155
CV v132,v156,v157
MM v[36:39],a[24:27],a[224:227],v[36:39]
TR a[16:17],v197 offset:16448
TR a[18:19],v197 offset:16960
MM v[36:39],a[28:31],a[228:231],v[36:39]
TR a[20:21],v197 offset:24672
TR a[22:23],v197 offset:25184
CV v133,v158,v159
MM v[44:47],a[28:31],a[236:239],v[44:47]
buffer_load_dwordx4 v253, s[16:19], s87 offen offset:0 lds
MM v[44:47],a[24:27],a[232:235],v[44:47]
s_add_u32 m0, s96, 85064
CV v134,v160,v161
CV v135,v162,v163
MM v[52:55],a[24:27],a[240:243],v[52:55]
CV v136,v164,v165
CV v137,v166,v167
MM v[52:55],a[28:31],a[244:247],v[52:55]
buffer_load_dwordx4 v254, s[16:19], s87 offen offset:0 lds
MM v[60:63],a[28:31],a[252:255],v[60:63]
s_waitcnt vmcnt(16)
RD a[128:131],v222 offset:0
v_xor_b32 v233, 32, v232
MM v[60:63],a[24:27],a[248:251],v[60:63]
s_add_u32 s77, %[slds], 65536
RD a[132:135],v223 offset:0
v_add_u32 v232, s77, v232
MM v[64:67],a[32:35],a[224:227],v[64:67]
TR a[24:25],v197 offset:16456
)ASM"
R"ASM(
TR a[26:27],v197 offset:16968
MM v[64:67],a[36:39],a[228:231],v[64:67]
TR a[28:29],v197 offset:24680
TR a[30:31],v197 offset:25192
MM v[72:75],a[36:39],a[236:239],v[72:75]
RD a[136:139],v222 offset:2048
v_add_u32 v233, s77, v233
MM v[72:75],a[32:35],a[232:235],v[72:75]
RD a[140:143],v223 offset:2048
buffer_store_dwordx4 v[128:131], v168, s[20:23], s82 offen offset:0 sc1
v_lshlrev_b32 v214, 8, v211
MM v[80:83],a[32:35],a[240:243],v[80:83]
RD a[144:147],v222 offset:4096
v_add_u32 v214, %[slds], v214
MM v[80:83],a[36:39],a[244:247],v[80:83]
RD a[148:151],v223 offset:4096
v_xor_b32 v215, v211, v212
MM v[88:91],a[36:39],a[252:255],v[88:91]
s_lshl_b32 s77, %[swave], 2
RD a[152:155],v222 offset:6144
v_xor_b32 v213, s77, v215
MM v[88:91],a[32:35],a[248:251],v[88:91]
s_waitcnt lgkmcnt(15)
RD a[156:159],v223 offset:6144
buffer_store_dwordx4 v[132:135], v168, s[20:23], s82 offen offset:512 sc1
v_xor_b32 v216, 0, v213
MM v[68:71],a[40:43],a[224:227],v[68:71]
TR a[32:33],v198 offset:16448
TR a[34:35],v198 offset:16960
MM v[68:71],a[44:47],a[228:231],v[68:71]
TR a[36:37],v198 offset:24672
TR a[38:39],v198 offset:25184
MM v[76:79],a[44:47],a[236:239],v[76:79]
buffer_store_dwordx2 v[136:137], v181, s[20:23], s82 offen offset:1024 sc1
MM v[76:79],a[40:43],a[232:235],v[76:79]
s_waitcnt lgkmcnt(4)
s_add_u32 s88, s68, s86
s_add_u32 s86, s86, 128
MM v[84:87],a[40:43],a[240:243],v[84:87]
s_cmp_ge_u32 s86, 1280
s_cselect_b32 s86, 0, s86
MM v[84:87],a[44:47],a[244:247],v[84:87]
s_add_u32 m0, s94, 0
s_add_u32 s95, s88, 0
v_lshlrev_b32 v148, 16, v128
MM v[92:95],a[44:47],a[252:255],v[92:95]
buffer_load_dwordx4 v220, s[32:35], s95 offen offset:0 lds
MM v[92:95],a[40:43],a[248:251],v[92:95]
s_add_u32 s95, s88, 9216
v_and_b32 v149, 0xffff0000, v128
v_lshlrev_b32 v176, 16, v138
MM v[96:99],a[48:51],a[224:227],v[96:99]
TR a[40:41],v198 offset:16456
TR a[42:43],v198 offset:16968
MM v[96:99],a[52:55],a[228:231],v[96:99]
TR a[44:45],v198 offset:24680
TR a[46:47],v198 offset:25192
v_and_b32 v177, 0xffff0000, v138
MM v[104:107],a[52:55],a[236:239],v[104:107]
buffer_load_dwordx4 v221, s[32:35], s95 offen offset:1024 lds
MM v[104:107],a[48:51],a[232:235],v[104:107]
s_add_u32 s95, s88, 18432
v_mul_f32 v170, v148, v176
v_mul_f32 v171, v149, v177
MM v[112:115],a[48:51],a[240:243],v[112:115]
buffer_load_dwordx4 v220, s[32:35], s95 offen offset:2048 lds
MM v[112:115],a[52:55],a[244:247],v[112:115]
s_add_u32 s95, s88, 27648
v_lshlrev_b32 v148, 16, v129
v_and_b32 v149, 0xffff0000, v129
MM v[120:123],a[52:55],a[252:255],v[120:123]
v_lshlrev_b32 v176, 16, v139
v_and_b32 v177, 0xffff0000, v139
MM v[120:123],a[48:51],a[248:251],v[120:123]
v_fma_f32 v170, v148, v176, v170
v_fma_f32 v171, v149, v177, v171
MM v[100:103],a[56:59],a[224:227],v[100:103]
TR a[48:49],v199 offset:16448
TR a[50:51],v199 offset:16960
MM v[100:103],a[60:63],a[228:231],v[100:103]
TR a[52:53],v199 offset:24672
TR a[54:55],v199 offset:25184
v_lshlrev_b32 v148, 16, v130
MM v[108:111],a[60:63],a[236:239],v[108:111]
buffer_load_dwordx4 v221, s[32:35], s95 offen offset:3072 lds
MM v[108:111],a[56:59],a[232:235],v[108:111]
s_sub_u32 s88, s88, s63
s_add_u32 m0, s94, 4096
s_add_u32 s95, s88, 40960
MM v[116:119],a[56:59],a[240:243],v[116:119]
LPG2_%=:
s_cbranch_execz LPG6_%=
buffer_load_dwordx4 v220, s[32:35], s95 offen offset:0 lds
v_and_b32 v149, 0xffff0000, v130
MM v[116:119],a[60:63],a[244:247],v[116:119]
s_add_u32 s95, s88, 50176
v_lshlrev_b32 v176, 16, v140
v_and_b32 v177, 0xffff0000, v140
MM v[124:127],a[60:63],a[252:255],v[124:127]
v_fma_f32 v170, v148, v176, v170
v_fma_f32 v171, v149, v177, v171
MM v[124:127],a[56:59],a[248:251],v[124:127]
v_lshlrev_b32 v148, 16, v131
v_and_b32 v149, 0xffff0000, v131
s_waitcnt vmcnt(20)
s_barrier
MM v[0:3],a[0:3],a[128:131],v[0:3]
TR a[56:57],v199 offset:16456
TR a[58:59],v199 offset:16968
MM v[0:3],a[4:7],a[132:135],v[0:3]
TR a[60:61],v199 offset:24680
TR a[62:63],v199 offset:25192
v_lshlrev_b32 v176, 16, v141
MM v[8:11],a[4:7],a[140:143],v[8:11]
buffer_load_dwordx4 v221, s[32:35], s95 offen offset:1024 lds
MM v[8:11],a[0:3],a[136:139],v[8:11]
s_add_u32 s95, s88, 59392
v_and_b32 v177, 0xffff0000, v141
v_fma_f32 v170, v148, v176, v170
MM v[16:19],a[0:3],a[144:147],v[16:19]
buffer_load_dwordx4 v220, s[32:35], s95 offen offset:2048 lds
MM v[16:19],a[4:7],a[148:151],v[16:19]
s_add_u32 s95, s88, 68608
v_fma_f32 v171, v149, v177, v171
v_lshlrev_b32 v148, 16, v132
MM v[24:27],a[4:7],a[156:159],v[24:27]
v_and_b32 v149, 0xffff0000, v132
v_lshlrev_b32 v176, 16, v142
MM v[24:27],a[0:3],a[152:155],v[24:27]
v_and_b32 v177, 0xffff0000, v142
v_fma_f32 v170, v148, v176, v170
MM v[4:7],a[8:11],a[128:131],v[4:7]
buffer_load_dwordx4 v221, s[32:35], s95 offen offset:3072 lds
MM v[4:7],a[12:15],a[132:135],v[4:7]
s_add_u32 s87, s89, s85
s_add_u32 s85, s85, 524288
v_fma_f32 v171, v149, v177, v171
MM v[12:15],a[12:15],a[140:143],v[12:15]
s_cmp_ge_u32 s85, 5242880
s_cselect_b32 s85, 0, s85
s_add_u32 m0, s96, 98432
MM v[12:15],a[8:11],a[136:139],v[12:15]
v_lshlrev_b32 v148, 16, v133
v_and_b32 v149, 0xffff0000, v133
MM v[20:23],a[8:11],a[144:147],v[20:23]
buffer_load_dwordx4 v253, s[16:19], s87 offen offset:0 lds
MM v[20:23],a[12:15],a[148:151],v[20:23]
s_add_u32 m0, s96, 99456
v_lshlrev_b32 v176, 16, v143
v_and_b32 v177, 0xffff0000, v143
MM v[28:31],a[12:15],a[156:159],v[28:31]
v_fma_f32 v170, v148, v176, v170
v_fma_f32 v171, v149, v177, v171
MM v[28:31],a[8:11],a[152:155],v[28:31]
v_lshlrev_b32 v148, 16, v134
v_and_b32 v149, 0xffff0000, v134
v_smfmac_f32_16x16x64_bf16 v[184:187], v[236:239], a[0:7], v234
v_lshlrev_b32 v176, 16, v144
v_smfmac_f32_16x16x64_bf16 v[188:191], v[236:239], a[8:15], v234
v_and_b32 v177, 0xffff0000, v144
MM v[32:35],a[16:19],a[128:131],v[32:35]
TR a[0:1],v192 offset:0
TR a[2:3],v192 offset:512
buffer_load_dwordx4 v254, s[16:19], s87 offen offset:0 lds
MM v[32:35],a[20:23],a[132:135],v[32:35]
s_add_u32 s87, s87, 65536
TR a[4:5],v192 offset:8224
TR a[6:7],v192 offset:8736
v_fma_f32 v170, v148, v176, v170
MM v[40:43],a[20:23],a[140:143],v[40:43]
s_add_u32 m0, s96, 100488
TR a[8:9],v192 offset:8
TR a[10:11],v192 offset:520
MM v[40:43],a[16:19],a[136:139],v[40:43]
TR a[12:13],v192 offset:8232
TR a[14:15],v192 offset:8744
MM v[48:51],a[16:19],a[144:147],v[48:51]
buffer_load_dwordx4 v253, s[16:19], s87 offen offset:0 lds
v_fma_f32 v171, v149, v177, v171
MM v[48:51],a[20:23],a[148:151],v[48:51]
s_add_u32 m0, s96, 101512
v_lshlrev_b32 v148, 16, v135
v_and_b32 v149, 0xffff0000, v135
MM v[56:59],a[20:23],a[156:159],v[56:59]
buffer_load_dwordx4 v254, s[16:19], s87 offen offset:0 lds
MM v[56:59],a[16:19],a[152:155],v[56:59]
s_waitcnt vmcnt(19)
RD a[160:163],v222 offset:32768
v_lshlrev_b32 v176, 16, v145
MM v[36:39],a[24:27],a[128:131],v[36:39]
TR a[16:17],v193 offset:0
TR a[18:19],v193 offset:512
MM v[36:39],a[28:31],a[132:135],v[36:39]
TR a[20:21],v193 offset:8224
TR a[22:23],v193 offset:8736
v_and_b32 v177, 0xffff0000, v145
MM v[44:47],a[28:31],a[140:143],v[44:47]
RD a[164:167],v223 offset:32768
v_fma_f32 v170, v148, v176, v170
MM v[44:47],a[24:27],a[136:139],v[44:47]
RD a[168:171],v222 offset:34816
v_fma_f32 v171, v149, v177, v171
MM v[52:55],a[24:27],a[144:147],v[52:55]
RD a[172:175],v223 offset:34816
v_lshlrev_b32 v148, 16, v136
MM v[52:55],a[28:31],a[148:151],v[52:55]
RD a[176:179],v222 offset:36864
v_and_b32 v149, 0xffff0000, v136
MM v[60:63],a[28:31],a[156:159],v[60:63]
RD a[180:183],v223 offset:36864
v_lshlrev_b32 v176, 16, v146
MM v[60:63],a[24:27],a[152:155],v[60:63]
s_waitcnt lgkmcnt(15)
RD a[184:187],v222 offset:38912
v_and_b32 v177, 0xffff0000, v146
MM v[64:67],a[32:35],a[128:131],v[64:67]
TR a[24:25],v193 offset:8
TR a[26:27],v193 offset:520
MM v[64:67],a[36:39],a[132:135],v[64:67]
TR a[28:29],v193 offset:8232
TR a[30:31],v193 offset:8744
v_fma_f32 v170, v148, v176, v170
MM v[72:75],a[36:39],a[140:143],v[72:75]
RD a[188:191],v223 offset:38912
v_fma_f32 v171, v149, v177, v171
MM v[72:75],a[32:35],a[136:139],v[72:75]
v_lshlrev_b32 v148, 16, v137
v_and_b32 v149, 0xffff0000, v137
MM v[80:83],a[32:35],a[144:147],v[80:83]
v_lshlrev_b32 v176, 16, v147
v_and_b32 v177, 0xffff0000, v147
MM v[80:83],a[36:39],a[148:151],v[80:83]
v_fma_f32 v170, v148, v176, v170
v_fma_f32 v171, v149, v177, v171
MM v[88:91],a[36:39],a[156:159],v[88:91]
s_waitcnt lgkmcnt(0)
s_add_u32 s88, s68, s86
s_add_u32 s86, s86, 128
MM v[88:91],a[32:35],a[152:155],v[88:91]
s_cmp_ge_u32 s86, 1280
s_cselect_b32 s86, 0, s86
s_add_u32 m0, s94, 32768
v_add_f32 v170, v170, v171
MM v[68:71],a[40:43],a[128:131],v[68:71]
s_add_u32 s95, s88, 0
TR a[32:33],v194 offset:0
TR a[34:35],v194 offset:512
MM v[68:71],a[44:47],a[132:135],v[68:71]
TR a[36:37],v194 offset:8224
TR a[38:39],v194 offset:8736
MM v[76:79],a[44:47],a[140:143],v[76:79]
buffer_load_dwordx4 v220, s[32:35], s95 offen offset:0 lds
v_add_f32_dpp v170, v170, v170 row_ror:8 row_mask:0xf bank_mask:0xf
MM v[76:79],a[40:43],a[136:139],v[76:79]
s_add_u32 s95, s88, 9216
v_add_f32_dpp v170, v170, v170 row_ror:4 row_mask:0xf bank_mask:0xf
v_lshl_add_u32 v224, v216, 4, v214
MM v[84:87],a[40:43],a[144:147],v[84:87]
buffer_load_dwordx4 v221, s[32:35], s95 offen offset:1024 lds
MM v[84:87],a[44:47],a[148:151],v[84:87]
s_add_u32 s95, s88, 18432
v_add_f32_dpp v170, v170, v170 row_ror:2 row_mask:0xf bank_mask:0xf
v_xor_b32 v216, 4, v213
MM v[92:95],a[44:47],a[156:159],v[92:95]
v_add_f32_dpp v170, v170, v170 row_ror:1 row_mask:0xf bank_mask:0xf
v_mov_b32 v173, v170
MM v[92:95],a[40:43],a[152:155],v[92:95]
s_nop 0
v_permlane16_swap_b32 v170, v173
v_add_f32 v170, v170, v173
MM v[96:99],a[48:51],a[128:131],v[96:99]
TR a[40:41],v194 offset:8
TR a[42:43],v194 offset:520
MM v[96:99],a[52:55],a[132:135],v[96:99]
TR a[44:45],v194 offset:8232
TR a[46:47],v194 offset:8744
CV v170,v170,v170
MM v[104:107],a[52:55],a[140:143],v[104:107]
buffer_load_dwordx4 v220, s[32:35], s95 offen offset:2048 lds
MM v[104:107],a[48:51],a[136:139],v[104:107]
s_add_u32 s95, s88, 27648
v_ashrrev_i32 v171, 31, v170
v_or_b32 v171, 0x80000000, v171
MM v[112:115],a[48:51],a[144:147],v[112:115]
buffer_load_dwordx4 v221, s[32:35], s95 offen offset:3072 lds
MM v[112:115],a[52:55],a[148:151],v[112:115]
s_sub_u32 s88, s88, s63
s_add_u32 m0, s94, 36864
s_add_u32 s95, s88, 40960
v_xor_b32 v170, v170, v171
MM v[120:123],a[52:55],a[156:159],v[120:123]
v_and_b32 v170, 0xffff0000, v170
v_sub_u32 v171, s79, v180
MM v[120:123],a[48:51],a[152:155],v[120:123]
v_or_b32 v170, v170, v171
v_lshlrev_b32 v173, 2, v180
MM v[100:103],a[56:59],a[128:131],v[100:103]
TR a[48:49],v195 offset:0
TR a[50:51],v195 offset:512
MM v[100:103],a[60:63],a[132:135],v[100:103]
TR a[52:53],v195 offset:8224
TR a[54:55],v195 offset:8736
MM v[108:111],a[60:63],a[140:143],v[108:111]
buffer_load_dwordx4 v220, s[32:35], s95 offen offset:0 lds
s_mov_b32 exec_lo, 1
s_mov_b32 exec_hi, 1
buffer_store_dword v170, v173, s[20:23], s78 offen offset:0 sc1
s_mov_b64 exec, -1
MM v[108:111],a[56:59],a[136:139],v[108:111]
s_add_u32 s95, s88, 50176
v_mov_b32 v171, s69
v_lshl_add_u32 v225, v216, 4, v214
MM v[116:119],a[56:59],a[144:147],v[116:119]
buffer_load_dwordx4 v221, s[32:35], s95 offen offset:1024 lds
MM v[116:119],a[60:63],a[148:151],v[116:119]
s_add_u32 s95, s88, 59392
v_xor_b32 v216, 8, v213
v_lshl_add_u32 v226, v216, 4, v214
MM v[124:127],a[60:63],a[156:159],v[124:127]
v_xor_b32 v216, 12, v213
v_lshl_add_u32 v227, v216, 4, v214
MM v[124:127],a[56:59],a[152:155],v[124:127]
v_lshlrev_b32 v214, 8, v212
v_xor_b32 v216, 0, v215
s_waitcnt vmcnt(22)
s_barrier
MM v[0:3],a[0:3],a[160:163],v[0:3]
TR a[56:57],v195 offset:8
TR a[58:59],v195 offset:520
MM v[0:3],a[4:7],a[164:167],v[0:3]
TR a[60:61],v195 offset:8232
TR a[62:63],v195 offset:8744
MM v[8:11],a[4:7],a[172:175],v[8:11]
buffer_load_dwordx4 v220, s[32:35], s95 offen offset:2048 lds
MM v[8:11],a[0:3],a[168:171],v[8:11]
s_add_u32 s95, s88, 68608
v_lshl_add_u32 v228, v216, 4, v214
v_xor_b32 v216, 4, v215
MM v[16:19],a[0:3],a[176:179],v[16:19]
buffer_load_dwordx4 v221, s[32:35], s95 offen offset:3072 lds
MM v[16:19],a[4:7],a[180:183],v[16:19]
s_add_u32 s87, s89, s85
)ASM"
R"ASM(
s_add_u32 s85, s85, 524288
v_lshl_add_u32 v229, v216, 4, v214
MM v[24:27],a[4:7],a[188:191],v[24:27]
s_cmp_ge_u32 s85, 5242880
s_cselect_b32 s85, 0, s85
s_add_u32 m0, s96, 114880
MM v[24:27],a[0:3],a[184:187],v[24:27]
v_xor_b32 v216, 8, v215
v_lshl_add_u32 v230, v216, 4, v214
MM v[4:7],a[8:11],a[160:163],v[4:7]
buffer_load_dwordx4 v253, s[16:19], s87 offen offset:0 lds
MM v[4:7],a[12:15],a[164:167],v[4:7]
s_add_u32 m0, s96, 115904
v_xor_b32 v216, 12, v215
v_lshl_add_u32 v231, v216, 4, v214
MM v[12:15],a[12:15],a[172:175],v[12:15]
s_lshl_b32 s90, s49, 19
s_add_u32 s8, s8, s90
s_addc_u32 s9, s9, 0
MM v[12:15],a[8:11],a[168:171],v[12:15]
s_mov_b32 s10, 524288
s_mov_b32 s11, 131072
MM v[20:23],a[8:11],a[176:179],v[20:23]
buffer_load_dwordx4 v254, s[16:19], s87 offen offset:0 lds
MM v[20:23],a[12:15],a[180:183],v[20:23]
s_add_u32 s87, s87, 65536
s_add_u32 s12, s12, s90
s_addc_u32 s13, s13, 0
MM v[28:31],a[12:15],a[188:191],v[28:31]
s_add_u32 m0, s96, 116936
s_mov_b32 s14, 525312
s_mov_b32 s15, 131072
MM v[28:31],a[8:11],a[184:187],v[28:31]
s_sub_u32 s12, s12, 1024
s_subb_u32 s13, s13, 0
s_lshr_b32 s90, s49, 3
v_smfmac_f32_16x16x64_bf16 v[184:187], v[236:239], a[0:7], v234
s_mul_i32 s90, s90, 1310720
s_mul_i32 s77, s84, 320
v_smfmac_f32_16x16x64_bf16 v[188:191], v[236:239], a[8:15], v234
s_add_u32 s90, s90, s77
MM v[32:35],a[16:19],a[160:163],v[32:35]
TR a[0:1],v192 offset:16448
TR a[2:3],v192 offset:16960
buffer_load_dwordx4 v253, s[16:19], s87 offen offset:0 lds
MM v[32:35],a[20:23],a[164:167],v[32:35]
s_add_u32 m0, s96, 117960
TR a[4:5],v192 offset:24672
TR a[6:7],v192 offset:25184
MM v[40:43],a[20:23],a[172:175],v[40:43]
TR a[8:9],v192 offset:16456
TR a[10:11],v192 offset:16968
MM v[40:43],a[16:19],a[168:171],v[40:43]
TR a[12:13],v192 offset:24680
TR a[14:15],v192 offset:25192
MM v[48:51],a[16:19],a[176:179],v[48:51]
buffer_load_dwordx4 v254, s[16:19], s87 offen offset:0 lds
MM v[48:51],a[20:23],a[180:183],v[48:51]
s_waitcnt vmcnt(17)
s_add_u32 s24, s24, s90
s_addc_u32 s25, s25, 0
MM v[56:59],a[20:23],a[188:191],v[56:59]
s_sub_u32 s26, 5242880, s90
RD a[192:195],v222 offset:0
s_mov_b32 s27, 131072
MM v[56:59],a[16:19],a[184:187],v[56:59]
s_mov_b32 s38, 2211840
RD a[196:199],v223 offset:0
s_mov_b32 s39, 131072
MM v[36:39],a[24:27],a[160:163],v[36:39]
s_mov_b32 s42, 2621440
TR a[16:17],v193 offset:16448
TR a[18:19],v193 offset:16960
MM v[36:39],a[28:31],a[164:167],v[36:39]
s_mov_b32 s43, 131072
TR a[20:21],v193 offset:24672
TR a[22:23],v193 offset:25184
MM v[44:47],a[28:31],a[172:175],v[44:47]
s_mov_b32 s71, 0x4e800000
RD a[200:203],v222 offset:2048
s_mov_b32 s72, 0x3e0293ee
MM v[44:47],a[24:27],a[168:171],v[44:47]
s_lshl_b32 s64, %[swave], 12
RD a[204:207],v223 offset:2048
MM v[52:55],a[24:27],a[176:179],v[52:55]
RD a[208:211],v222 offset:4096
MM v[52:55],a[28:31],a[180:183],v[52:55]
RD a[212:215],v223 offset:4096
MM v[60:63],a[28:31],a[188:191],v[60:63]
RD a[216:219],v222 offset:6144
MM v[60:63],a[24:27],a[184:187],v[60:63]
s_waitcnt lgkmcnt(15)
RD a[220:223],v223 offset:6144
MM v[64:67],a[32:35],a[160:163],v[64:67]
TR a[24:25],v193 offset:16456
TR a[26:27],v193 offset:16968
MM v[64:67],a[36:39],a[164:167],v[64:67]
TR a[28:29],v193 offset:24680
TR a[30:31],v193 offset:25192
MM v[72:75],a[36:39],a[172:175],v[72:75]
MM v[72:75],a[32:35],a[168:171],v[72:75]
s_waitcnt lgkmcnt(4)
s_add_u32 s88, s68, s86
s_add_u32 s86, s86, 128
MM v[80:83],a[32:35],a[176:179],v[80:83]
s_cmp_ge_u32 s86, 1280
s_cselect_b32 s86, 0, s86
s_add_u32 m0, s94, 0
MM v[80:83],a[36:39],a[180:183],v[80:83]
s_add_u32 s95, s88, 0
buffer_load_dwordx4 v220, s[32:35], s95 offen offset:0 lds
MM v[88:91],a[36:39],a[188:191],v[88:91]
s_add_u32 s95, s88, 9216
MM v[88:91],a[32:35],a[184:187],v[88:91]
buffer_load_dwordx4 v221, s[32:35], s95 offen offset:1024 lds
MM v[68:71],a[40:43],a[160:163],v[68:71]
s_add_u32 s95, s88, 18432
TR a[32:33],v194 offset:16448
TR a[34:35],v194 offset:16960
MM v[68:71],a[44:47],a[164:167],v[68:71]
TR a[36:37],v194 offset:24672
TR a[38:39],v194 offset:25184
MM v[76:79],a[44:47],a[172:175],v[76:79]
MM v[76:79],a[40:43],a[168:171],v[76:79]
buffer_load_dwordx4 v220, s[32:35], s95 offen offset:2048 lds
MM v[84:87],a[40:43],a[176:179],v[84:87]
s_add_u32 s95, s88, 27648
MM v[84:87],a[44:47],a[180:183],v[84:87]
MM v[92:95],a[44:47],a[188:191],v[92:95]
MM v[92:95],a[40:43],a[184:187],v[92:95]
buffer_load_dwordx4 v221, s[32:35], s95 offen offset:3072 lds
MM v[96:99],a[48:51],a[160:163],v[96:99]
s_sub_u32 s88, s88, s63
TR a[40:41],v194 offset:16456
TR a[42:43],v194 offset:16968
MM v[96:99],a[52:55],a[164:167],v[96:99]
s_add_u32 m0, s94, 4096
TR a[44:45],v194 offset:24680
TR a[46:47],v194 offset:25192
MM v[104:107],a[52:55],a[172:175],v[104:107]
s_add_u32 s95, s88, 40960
MM v[104:107],a[48:51],a[168:171],v[104:107]
buffer_load_dwordx4 v220, s[32:35], s95 offen offset:0 lds
MM v[112:115],a[48:51],a[176:179],v[112:115]
s_add_u32 s95, s88, 50176
MM v[112:115],a[52:55],a[180:183],v[112:115]
MM v[120:123],a[52:55],a[188:191],v[120:123]
MM v[120:123],a[48:51],a[184:187],v[120:123]
buffer_load_dwordx4 v221, s[32:35], s95 offen offset:1024 lds
MM v[100:103],a[56:59],a[160:163],v[100:103]
s_add_u32 s95, s88, 59392
TR a[48:49],v195 offset:16448
TR a[50:51],v195 offset:16960
MM v[100:103],a[60:63],a[164:167],v[100:103]
TR a[52:53],v195 offset:24672
TR a[54:55],v195 offset:25184
MM v[108:111],a[60:63],a[172:175],v[108:111]
MM v[108:111],a[56:59],a[168:171],v[108:111]
buffer_load_dwordx4 v220, s[32:35], s95 offen offset:2048 lds
MM v[116:119],a[56:59],a[176:179],v[116:119]
s_add_u32 s95, s88, 68608
MM v[116:119],a[60:63],a[180:183],v[116:119]
MM v[124:127],a[60:63],a[188:191],v[124:127]
MM v[124:127],a[56:59],a[184:187],v[124:127]
buffer_load_dwordx4 v221, s[32:35], s95 offen offset:3072 lds
s_waitcnt vmcnt(21)
s_barrier
MM v[0:3],a[0:3],a[192:195],v[0:3]
s_add_u32 s87, s89, s85
TR a[56:57],v195 offset:16456
TR a[58:59],v195 offset:16968
MM v[0:3],a[4:7],a[196:199],v[0:3]
s_add_u32 s85, s85, 524288
TR a[60:61],v195 offset:24680
TR a[62:63],v195 offset:25192
MM v[8:11],a[4:7],a[204:207],v[8:11]
s_cmp_ge_u32 s85, 5242880
s_cselect_b32 s85, 0, s85
s_add_u32 m0, s96, 131328
v_and_b32 v208, 240, v248
MM v[8:11],a[0:3],a[200:203],v[8:11]
s_lshl_b32 s62, s62, 8
buffer_load_dwordx4 v253, s[16:19], s87 offen offset:0 lds
MM v[16:19],a[0:3],a[208:211],v[16:19]
s_add_u32 m0, s96, 132352
s_add_u32 s93, s93, 36864
s_add_u32 s62, s62, s93
MM v[16:19],a[4:7],a[212:215],v[16:19]
s_xor_b32 s93, s62, 256
MM v[24:27],a[4:7],a[220:223],v[24:27]
MM v[24:27],a[0:3],a[216:219],v[24:27]
buffer_load_dwordx4 v254, s[16:19], s87 offen offset:0 lds
MM v[4:7],a[8:11],a[192:195],v[4:7]
s_add_u32 s87, s87, 65536
s_add_u32 m0, s96, 133384
MM v[4:7],a[12:15],a[196:199],v[4:7]
MM v[12:15],a[12:15],a[204:207],v[12:15]
MM v[12:15],a[8:11],a[200:203],v[12:15]
buffer_load_dwordx4 v253, s[16:19], s87 offen offset:0 lds
MM v[20:23],a[8:11],a[208:211],v[20:23]
s_add_u32 m0, s96, 134408
MM v[20:23],a[12:15],a[212:215],v[20:23]
MM v[28:31],a[12:15],a[220:223],v[28:31]
MM v[28:31],a[8:11],a[216:219],v[28:31]
buffer_load_dwordx4 v254, s[16:19], s87 offen offset:0 lds
v_smfmac_f32_16x16x64_bf16 v[184:187], v[236:239], a[0:7], v234
s_waitcnt vmcnt(16)
RD a[224:227],v222 offset:32768
v_smfmac_f32_16x16x64_bf16 v[188:191], v[236:239], a[8:15], v234
RD a[228:231],v223 offset:32768
MM v[32:35],a[16:19],a[192:195],v[32:35]
TR a[0:1],v192 offset:32896
TR a[2:3],v192 offset:33408
MM v[32:35],a[20:23],a[196:199],v[32:35]
TR a[4:5],v192 offset:41120
TR a[6:7],v192 offset:41632
MM v[40:43],a[20:23],a[204:207],v[40:43]
TR a[8:9],v192 offset:32904
TR a[10:11],v192 offset:33416
RD a[232:235],v222 offset:34816
MM v[40:43],a[16:19],a[200:203],v[40:43]
TR a[12:13],v192 offset:41128
TR a[14:15],v192 offset:41640
buffer_load_dwordx4 v[200:203], v208, s[20:23], s62 offen offset:0 sc1
MM v[48:51],a[16:19],a[208:211],v[48:51]
RD a[236:239],v223 offset:34816
MM v[48:51],a[20:23],a[212:215],v[48:51]
buffer_load_dwordx4 v[204:207], v208, s[20:23], s93 offen offset:0 sc1
MM v[56:59],a[20:23],a[220:223],v[56:59]
RD a[240:243],v222 offset:36864
MM v[56:59],a[16:19],a[216:219],v[56:59]
s_waitcnt lgkmcnt(15)
RD a[244:247],v223 offset:36864
MM v[36:39],a[24:27],a[192:195],v[36:39]
TR a[16:17],v193 offset:32896
TR a[18:19],v193 offset:33408
MM v[36:39],a[28:31],a[196:199],v[36:39]
TR a[20:21],v193 offset:41120
TR a[22:23],v193 offset:41632
MM v[44:47],a[28:31],a[204:207],v[44:47]
RD a[248:251],v222 offset:38912
MM v[44:47],a[24:27],a[200:203],v[44:47]
RD a[252:255],v223 offset:38912
MM v[52:55],a[24:27],a[208:211],v[52:55]
MM v[52:55],a[28:31],a[212:215],v[52:55]
MM v[60:63],a[28:31],a[220:223],v[60:63]
MM v[60:63],a[24:27],a[216:219],v[60:63]
s_waitcnt lgkmcnt(0)
s_add_u32 s88, s68, s86
s_add_u32 s86, s86, 128
MM v[64:67],a[32:35],a[192:195],v[64:67]
TR a[24:25],v193 offset:32904
TR a[26:27],v193 offset:33416
MM v[64:67],a[36:39],a[196:199],v[64:67]
TR a[28:29],v193 offset:41128
TR a[30:31],v193 offset:41640
s_cmp_ge_u32 s86, 1280
s_cselect_b32 s86, 0, s86
MM v[72:75],a[36:39],a[204:207],v[72:75]
s_add_u32 m0, s94, 32768
s_add_u32 s95, s88, 0
MM v[72:75],a[32:35],a[200:203],v[72:75]
buffer_load_dwordx4 v220, s[32:35], s95 offen offset:0 lds
MM v[80:83],a[32:35],a[208:211],v[80:83]
s_add_u32 s95, s88, 9216
MM v[80:83],a[36:39],a[212:215],v[80:83]
buffer_load_dwordx4 v221, s[32:35], s95 offen offset:1024 lds
MM v[88:91],a[36:39],a[220:223],v[88:91]
s_add_u32 s95, s88, 18432
MM v[88:91],a[32:35],a[216:219],v[88:91]
MM v[68:71],a[40:43],a[192:195],v[68:71]
TR a[32:33],v194 offset:32896
TR a[34:35],v194 offset:33408
MM v[68:71],a[44:47],a[196:199],v[68:71]
TR a[36:37],v194 offset:41120
TR a[38:39],v194 offset:41632
MM v[76:79],a[44:47],a[204:207],v[76:79]
buffer_load_dwordx4 v220, s[32:35], s95 offen offset:2048 lds
MM v[76:79],a[40:43],a[200:203],v[76:79]
s_add_u32 s95, s88, 27648
MM v[84:87],a[40:43],a[208:211],v[84:87]
MM v[84:87],a[44:47],a[212:215],v[84:87]
buffer_load_dwordx4 v221, s[32:35], s95 offen offset:3072 lds
MM v[92:95],a[44:47],a[220:223],v[92:95]
s_waitcnt vmcnt(10)
RD a[128:131],v222 offset:0
MM v[92:95],a[40:43],a[216:219],v[92:95]
RD a[132:135],v223 offset:0
MM v[96:99],a[48:51],a[192:195],v[96:99]
TR a[40:41],v194 offset:32904
TR a[42:43],v194 offset:33416
MM v[96:99],a[52:55],a[196:199],v[96:99]
TR a[44:45],v194 offset:41128
TR a[46:47],v194 offset:41640
MM v[104:107],a[52:55],a[204:207],v[104:107]
RD a[136:139],v222 offset:2048
MM v[104:107],a[48:51],a[200:203],v[104:107]
RD a[140:143],v223 offset:2048
MM v[112:115],a[48:51],a[208:211],v[112:115]
RD a[144:147],v222 offset:4096
MM v[112:115],a[52:55],a[212:215],v[112:115]
RD a[148:151],v223 offset:4096
MM v[120:123],a[52:55],a[220:223],v[120:123]
s_nop 0
LPG3_%=:
s_cbranch_execz LPG7_%=
RD a[152:155],v222 offset:6144
MM v[120:123],a[48:51],a[216:219],v[120:123]
RD a[156:159],v223 offset:6144
MM v[100:103],a[56:59],a[192:195],v[100:103]
s_sub_u32 s88, s88, s63
TR a[48:49],v195 offset:32896
TR a[50:51],v195 offset:33408
MM v[100:103],a[60:63],a[196:199],v[100:103]
s_add_u32 m0, s94, 36864
TR a[52:53],v195 offset:41120
TR a[54:55],v195 offset:41632
MM v[108:111],a[60:63],a[204:207],v[108:111]
s_add_u32 s95, s88, 40960
buffer_load_dwordx4 v220, s[32:35], s95 offen offset:0 lds
MM v[108:111],a[56:59],a[200:203],v[108:111]
s_add_u32 s95, s88, 50176
MM v[116:119],a[56:59],a[208:211],v[116:119]
buffer_load_dwordx4 v221, s[32:35], s95 offen offset:1024 lds
MM v[116:119],a[60:63],a[212:215],v[116:119]
s_add_u32 s95, s88, 59392
MM v[124:127],a[60:63],a[220:223],v[124:127]
MM v[124:127],a[56:59],a[216:219],v[124:127]
buffer_load_dwordx4 v220, s[32:35], s95 offen offset:2048 lds
s_add_u32 s95, s88, 68608
buffer_load_dwordx4 v221, s[32:35], s95 offen offset:3072 lds
s_waitcnt lgkmcnt(4)
s_waitcnt vmcnt(10)
s_barrier
MM v[0:3],a[0:3],a[224:227],v[0:3]
s_add_u32 s77, s64, 0
TR a[56:57],v195 offset:32904
TR a[58:59],v195 offset:33416
MM v[0:3],a[4:7],a[228:231],v[0:3]
s_add_u32 m0, s65, 0
TR a[60:61],v195 offset:41128
TR a[62:63],v195 offset:41640
)ASM"
R"ASM(
MM v[8:11],a[4:7],a[236:239],v[8:11]
s_waitcnt vmcnt(8)
TR a[64:65],v196 offset:0
TR a[66:67],v196 offset:512
MM v[8:11],a[0:3],a[232:235],v[8:11]
s_lshl_b32 s76, %[swave], 7
buffer_load_dwordx4 v228, s[8:11], s77 offen offset:0 lds
v_lshrrev_b32 v253, 8, v248
v_lshlrev_b32 v253, 5, v253
MM v[16:19],a[0:3],a[240:243],v[16:19]
s_add_u32 s76, s76, %[slds]
TR a[68:69],v196 offset:8224
TR a[70:71],v196 offset:8736
v_xor_b32 v201, s69, v201
MM v[16:19],a[4:7],a[244:247],v[16:19]
s_mov_b64 exec, 1
buffer_store_dword v171, off, s[20:23], s80 sc1
s_mov_b64 exec, -1
v_add_u32 v253, s76, v253
v_add_u32 v253, 151552, v253
MM v[24:27],a[4:7],a[252:255],v[24:27]
TR a[72:73],v196 offset:16448
TR a[74:75],v196 offset:16960
MM v[24:27],a[0:3],a[248:251],v[24:27]
TR a[76:77],v196 offset:24672
TR a[78:79],v196 offset:25184
buffer_load_dwordx4 v229, s[8:11], s77 offen offset:1024 lds
v_xor_b32 v203, s69, v203
MM v[4:7],a[8:11],a[224:227],v[4:7]
TR a[80:81],v196 offset:8
TR a[82:83],v196 offset:520
MM v[4:7],a[12:15],a[228:231],v[4:7]
TR a[84:85],v196 offset:8232
TR a[86:87],v196 offset:8744
MM v[12:15],a[12:15],a[236:239],v[12:15]
TR a[88:89],v196 offset:16456
TR a[90:91],v196 offset:16968
v_xor_b32 v205, s69, v205
MM v[12:15],a[8:11],a[232:235],v[12:15]
TR a[92:93],v196 offset:24680
TR a[94:95],v196 offset:25192
buffer_load_dwordx4 v230, s[8:11], s77 offen offset:2048 lds
MM v[20:23],a[8:11],a[240:243],v[20:23]
TR a[96:97],v197 offset:0
TR a[98:99],v197 offset:512
MM v[20:23],a[12:15],a[244:247],v[20:23]
TR a[100:101],v197 offset:8224
TR a[102:103],v197 offset:8736
v_xor_b32 v207, s69, v207
MM v[28:31],a[12:15],a[252:255],v[28:31]
TR a[104:105],v197 offset:16448
TR a[106:107],v197 offset:16960
MM v[28:31],a[8:11],a[248:251],v[28:31]
TR a[108:109],v197 offset:24672
TR a[110:111],v197 offset:25184
buffer_load_dwordx4 v231, s[8:11], s77 offen offset:3072 lds
v_smfmac_f32_16x16x64_bf16 v[184:187], v[236:239], a[0:7], v234
s_add_u32 s77, s64, 16384
TR a[112:113],v197 offset:8
v_or3_b32 v209, v201, v203, v205
v_smfmac_f32_16x16x64_bf16 v[188:191], v[236:239], a[8:15], v234
s_add_u32 m0, s65, 16384
TR a[114:115],v197 offset:520
MM v[32:35],a[16:19],a[224:227],v[32:35]
TR a[116:117],v197 offset:8232
TR a[118:119],v197 offset:8744
MM v[32:35],a[20:23],a[228:231],v[32:35]
TR a[120:121],v197 offset:16456
TR a[122:123],v197 offset:16968
v_or_b32 v209, v209, v207
MM v[40:43],a[20:23],a[236:239],v[40:43]
TR a[124:125],v197 offset:24680
TR a[126:127],v197 offset:25192
MM v[40:43],a[16:19],a[232:235],v[40:43]
TR a[0:1],v198 offset:0
TR a[2:3],v198 offset:512
buffer_load_dwordx4 v228, s[8:11], s77 offen offset:0 lds
MM v[48:51],a[16:19],a[240:243],v[48:51]
TR a[4:5],v198 offset:8224
TR a[6:7],v198 offset:8736
v_cmp_ne_u32 vcc, 0, v209
s_cbranch_vccnz LRG1o_%=
LRG1ob_%=:
MM v[48:51],a[20:23],a[244:247],v[48:51]
TR a[8:9],v198 offset:16448
TR a[10:11],v198 offset:16960
MM v[56:59],a[20:23],a[252:255],v[56:59]
TR a[12:13],v198 offset:24672
TR a[14:15],v198 offset:25184
MM v[56:59],a[16:19],a[248:251],v[56:59]
v_lshlrev_b32 v240, 16, v200
v_and_b32 v200, 0xffff0000, v200
MM v[36:39],a[24:27],a[224:227],v[36:39]
v_rcp_f32 v244, v200
MM v[36:39],a[28:31],a[228:231],v[36:39]
v_xor_b32 v240, 0x80000000, v240
v_mul_f32 v244, s72, v244
MM v[44:47],a[28:31],a[236:239],v[44:47]
v_lshlrev_b32 v241, 16, v202
v_and_b32 v202, 0xffff0000, v202
MM v[44:47],a[24:27],a[232:235],v[44:47]
buffer_load_dwordx4 v229, s[8:11], s77 offen offset:1024 lds
MM v[52:55],a[24:27],a[240:243],v[52:55]
v_rcp_f32 v245, v202
MM v[52:55],a[28:31],a[244:247],v[52:55]
v_xor_b32 v241, 0x80000000, v241
v_mul_f32 v245, s72, v245
MM v[60:63],a[28:31],a[252:255],v[60:63]
v_lshlrev_b32 v242, 16, v204
v_and_b32 v204, 0xffff0000, v204
MM v[60:63],a[24:27],a[248:251],v[60:63]
v_rcp_f32 v246, v204
MM v[64:67],a[32:35],a[224:227],v[64:67]
TR a[16:17],v198 offset:8
TR a[18:19],v198 offset:520
MM v[64:67],a[36:39],a[228:231],v[64:67]
TR a[20:21],v198 offset:8232
TR a[22:23],v198 offset:8744
v_xor_b32 v242, 0x80000000, v242
MM v[72:75],a[36:39],a[236:239],v[72:75]
TR a[24:25],v198 offset:16456
TR a[26:27],v198 offset:16968
MM v[72:75],a[32:35],a[232:235],v[72:75]
TR a[28:29],v198 offset:24680
TR a[30:31],v198 offset:25192
MM v[80:83],a[32:35],a[240:243],v[80:83]
v_mul_f32 v246, s72, v246
v_lshlrev_b32 v243, 16, v206
MM v[80:83],a[36:39],a[244:247],v[80:83]
buffer_load_dwordx4 v230, s[8:11], s77 offen offset:2048 lds
MM v[88:91],a[36:39],a[252:255],v[88:91]
v_and_b32 v206, 0xffff0000, v206
MM v[88:91],a[32:35],a[248:251],v[88:91]
s_waitcnt lgkmcnt(15)
v_rcp_f32 v247, v206
v_smfmac_f32_16x16x64_bf16 v[184:187], v[236:239], a[64:71], v234
v_xor_b32 v243, 0x80000000, v243
v_smfmac_f32_16x16x64_bf16 v[184:187], v[236:239], a[72:79], v234
v_mul_f32 v247, s72, v247
v_smfmac_f32_16x16x64_bf16 v[188:191], v[236:239], a[80:87], v234
v_smfmac_f32_16x16x64_bf16 v[188:191], v[236:239], a[88:95], v234
MM v[68:71],a[40:43],a[224:227],v[68:71]
v_bfe_u32 v183, v248, 6, 2
v_lshlrev_b32 v183, 5, v183
MM v[68:71],a[44:47],a[228:231],v[68:71]
v_bfe_u32 v182, v248, 4, 2
v_lshl_add_u32 v183, v182, 2, v183
MM v[76:79],a[44:47],a[236:239],v[76:79]
v_add_u32 v183, s76, v183
v_add_u32 v183, 151552, v183
MM v[76:79],a[40:43],a[232:235],v[76:79]
buffer_load_dwordx4 v231, s[8:11], s77 offen offset:3072 lds
MM v[84:87],a[40:43],a[240:243],v[84:87]
s_waitcnt vmcnt(9)
RD a[160:163],v222 offset:32768
MM v[84:87],a[44:47],a[244:247],v[84:87]
RD a[164:167],v223 offset:32768
MM v[92:95],a[44:47],a[252:255],v[92:95]
RD a[168:171],v222 offset:34816
MM v[92:95],a[40:43],a[248:251],v[92:95]
v_add_f32 v184, v184, v185
v_add_f32 v188, v188, v189
MM v[96:99],a[48:51],a[224:227],v[96:99]
TR a[32:33],v199 offset:0
TR a[34:35],v199 offset:512
MM v[96:99],a[52:55],a[228:231],v[96:99]
RD a[172:175],v223 offset:34816
ds_write_b32 v183, v184 offset:0
ds_write_b32 v183, v188 offset:16
MM v[104:107],a[52:55],a[236:239],v[104:107]
TR a[36:37],v199 offset:8224
TR a[38:39],v199 offset:8736
MM v[104:107],a[48:51],a[232:235],v[104:107]
TR a[40:41],v199 offset:16448
TR a[42:43],v199 offset:16960
MM v[112:115],a[48:51],a[240:243],v[112:115]
TR a[44:45],v199 offset:24672
TR a[46:47],v199 offset:25184
RD a[176:179],v222 offset:36864
MM v[112:115],a[52:55],a[244:247],v[112:115]
RD a[180:183],v223 offset:36864
MM v[120:123],a[52:55],a[252:255],v[120:123]
RD a[184:187],v222 offset:38912
MM v[120:123],a[48:51],a[248:251],v[120:123]
RD a[188:191],v223 offset:38912
MM v[100:103],a[56:59],a[224:227],v[100:103]
s_add_u32 s77, s64, 0
s_add_u32 s81, s77, 896
s_add_u32 m0, s65, 65536
MM v[100:103],a[60:63],a[228:231],v[100:103]
buffer_load_dwordx4 v235, s[12:15], s81 offen offset:0 lds
MM v[108:111],a[60:63],a[236:239],v[108:111]
MM v[108:111],a[56:59],a[232:235],v[108:111]
MM v[116:119],a[56:59],a[240:243],v[116:119]
MM v[116:119],a[60:63],a[244:247],v[116:119]
MM v[124:127],a[60:63],a[252:255],v[124:127]
MM v[124:127],a[56:59],a[248:251],v[124:127]
buffer_load_dwordx4 v235, s[12:15], s77 offen offset:1024 lds
s_waitcnt lgkmcnt(0)
s_barrier
MM v[0:3],a[64:67],a[128:131],v[0:3]
s_memrealtime s[52:53]
TR a[48:49],v199 offset:8
TR a[50:51],v199 offset:520
MM v[0:3],a[68:71],a[132:135],v[0:3]
s_memtime s[54:55]
TR a[52:53],v199 offset:8232
TR a[54:55],v199 offset:8744
MM v[0:3],a[72:75],a[160:163],v[0:3]
TR a[56:57],v199 offset:16456
TR a[58:59],v199 offset:16968
MM v[0:3],a[76:79],a[164:167],v[0:3]
TR a[60:61],v199 offset:24680
TR a[62:63],v199 offset:25192
MM v[8:11],a[76:79],a[172:175],v[8:11]
v_xor_b32 v151, 0, v253
v_xor_b32 v143, 0, v253
MM v[8:11],a[72:75],a[168:171],v[8:11]
RD v[144:147],v151 offset:0
RD v[148:151],v151 offset:16
MM v[8:11],a[68:71],a[140:143],v[8:11]
RD v[136:139],v143 offset:0
RD v[140:143],v143 offset:16
MM v[8:11],a[64:67],a[136:139],v[8:11]
MM v[4:7],a[80:83],a[128:131],v[4:7]
MM v[4:7],a[84:87],a[132:135],v[4:7]
s_waitcnt lgkmcnt(2)
buffer_load_dwordx4 v235, s[12:15], s81 offen offset:2048 lds
MM v[4:7],a[88:91],a[160:163],v[4:7]
s_waitcnt lgkmcnt(0)
MM v[4:7],a[92:95],a[164:167],v[4:7]
MM v[12:15],a[92:95],a[172:175],v[12:15]
MM v[12:15],a[88:91],a[168:171],v[12:15]
MM v[12:15],a[84:87],a[140:143],v[12:15]
MM v[12:15],a[80:83],a[136:139],v[12:15]
MM v[32:35],a[96:99],a[128:131],v[32:35]
MM v[32:35],a[100:103],a[132:135],v[32:35]
MM v[32:35],a[104:107],a[160:163],v[32:35]
MM v[32:35],a[108:111],a[164:167],v[32:35]
buffer_load_dwordx4 v235, s[12:15], s77 offen offset:3072 lds
v_fmac_f32 v0, v240, v144
v_fmac_f32 v1, v240, v145
v_fmac_f32 v2, v240, v146
MM v[40:43],a[108:111],a[172:175],v[40:43]
s_add_u32 s77, s64, 32768
s_add_u32 m0, s65, 32768
v_fmac_f32 v3, v240, v147
v_fmac_f32 v4, v240, v148
MM v[40:43],a[104:107],a[168:171],v[40:43]
v_fmac_f32 v5, v240, v149
v_fmac_f32 v6, v240, v150
MM v[40:43],a[100:103],a[140:143],v[40:43]
v_fmac_f32 v7, v240, v151
v_mul_f32 v0, v244, v0
v_mul_f32 v1, v244, v1
MM v[40:43],a[96:99],a[136:139],v[40:43]
v_mul_f32 v2, v244, v2
v_mul_f32 v3, v244, v3
MM v[36:39],a[112:115],a[128:131],v[36:39]
v_mul_f32 v4, v244, v4
v_mul_f32 v5, v244, v5
MM v[36:39],a[116:119],a[132:135],v[36:39]
v_mul_f32 v6, v244, v6
v_mul_f32 v7, v244, v7
CV v160,v0,v1
MM v[36:39],a[120:123],a[160:163],v[36:39]
CV v161,v2,v3
CV v162,v4,v5
MM v[36:39],a[124:127],a[164:167],v[36:39]
CV v163,v6,v7
v_fmac_f32 v8, v241, v144
MM v[44:47],a[124:127],a[172:175],v[44:47]
v_fmac_f32 v9, v241, v145
v_fmac_f32 v10, v241, v146
v_fmac_f32 v11, v241, v147
MM v[44:47],a[120:123],a[168:171],v[44:47]
buffer_load_dwordx4 v228, s[8:11], s77 offen offset:0 lds
v_fmac_f32 v12, v241, v148
v_fmac_f32 v13, v241, v149
MM v[44:47],a[116:119],a[140:143],v[44:47]
v_fmac_f32 v14, v241, v150
v_fmac_f32 v15, v241, v151
v_mul_f32 v8, v245, v8
MM v[44:47],a[112:115],a[136:139],v[44:47]
v_mul_f32 v9, v245, v9
v_mul_f32 v10, v245, v10
MM v[64:67],a[0:3],a[128:131],v[64:67]
v_mul_f32 v11, v245, v11
v_mul_f32 v12, v245, v12
MM v[64:67],a[4:7],a[132:135],v[64:67]
v_mul_f32 v13, v245, v13
v_mul_f32 v14, v245, v14
v_mul_f32 v15, v245, v15
MM v[64:67],a[8:11],a[160:163],v[64:67]
CV v164,v8,v9
CV v165,v10,v11
MM v[64:67],a[12:15],a[164:167],v[64:67]
CV v166,v12,v13
CV v167,v14,v15
MM v[72:75],a[12:15],a[172:175],v[72:75]
v_xor_b32 v159, 128, v253
MM v[72:75],a[8:11],a[168:171],v[72:75]
RD v[152:155],v159 offset:0
RD v[156:159],v159 offset:16
MM v[72:75],a[4:7],a[140:143],v[72:75]
MM v[72:75],a[0:3],a[136:139],v[72:75]
buffer_load_dwordx4 v229, s[8:11], s77 offen offset:1024 lds
MM v[68:71],a[16:19],a[128:131],v[68:71]
MM v[68:71],a[20:23],a[132:135],v[68:71]
s_waitcnt lgkmcnt(0)
v_fmac_f32 v32, v240, v152
v_fmac_f32 v33, v240, v153
v_fmac_f32 v34, v240, v154
v_fmac_f32 v35, v240, v155
v_fmac_f32 v36, v240, v156
v_fmac_f32 v37, v240, v157
v_fmac_f32 v38, v240, v158
v_fmac_f32 v39, v240, v159
v_mul_f32 v179, -v250, v244
v_fma_f32 v32, v244, v32, v179
v_fma_f32 v33, v244, v33, v179
v_fma_f32 v34, v244, v34, v179
MM v[68:71],a[24:27],a[160:163],v[68:71]
v_fma_f32 v35, v244, v35, v179
v_fma_f32 v36, v244, v36, v179
MM v[68:71],a[28:31],a[164:167],v[68:71]
v_fma_f32 v37, v244, v37, v179
v_fma_f32 v38, v244, v38, v179
v_fma_f32 v39, v244, v39, v179
MM v[76:79],a[28:31],a[172:175],v[76:79]
CV v176,v32,v33
CV v177,v34,v35
MM v[76:79],a[24:27],a[168:171],v[76:79]
CV v178,v36,v37
CV v179,v38,v39
v_fmac_f32 v40, v241, v152
MM v[76:79],a[20:23],a[140:143],v[76:79]
v_fmac_f32 v41, v241, v153
v_fmac_f32 v42, v241, v154
v_fmac_f32 v43, v241, v155
MM v[76:79],a[16:19],a[136:139],v[76:79]
v_fmac_f32 v44, v241, v156
v_fmac_f32 v45, v241, v157
MM v[96:99],a[32:35],a[128:131],v[96:99]
v_fmac_f32 v46, v241, v158
v_fmac_f32 v47, v241, v159
MM v[96:99],a[36:39],a[132:135],v[96:99]
buffer_load_dwordx4 v230, s[8:11], s77 offen offset:2048 lds
v_mul_f32 v183, -v250, v245
v_fma_f32 v40, v245, v40, v183
v_fma_f32 v41, v245, v41, v183
MM v[96:99],a[40:43],a[160:163],v[96:99]
v_fma_f32 v42, v245, v42, v183
v_fma_f32 v43, v245, v43, v183
MM v[96:99],a[44:47],a[164:167],v[96:99]
v_fma_f32 v44, v245, v44, v183
v_fma_f32 v45, v245, v45, v183
MM v[104:107],a[44:47],a[172:175],v[104:107]
)ASM"
R"ASM(
v_fma_f32 v46, v245, v46, v183
v_fma_f32 v47, v245, v47, v183
CV v180,v40,v41
MM v[104:107],a[40:43],a[168:171],v[104:107]
CV v181,v42,v43
CV v182,v44,v45
MM v[104:107],a[36:39],a[140:143],v[104:107]
CV v183,v46,v47
v_xor_b32 v151, 256, v253
MM v[104:107],a[32:35],a[136:139],v[104:107]
RD v[144:147],v151 offset:0
RD v[148:151],v151 offset:16
MM v[100:103],a[48:51],a[128:131],v[100:103]
MM v[100:103],a[52:55],a[132:135],v[100:103]
MM v[100:103],a[56:59],a[160:163],v[100:103]
MM v[100:103],a[60:63],a[164:167],v[100:103]
s_waitcnt lgkmcnt(0)
buffer_load_dwordx4 v231, s[8:11], s77 offen offset:3072 lds
v_fmac_f32 v64, v240, v144
v_fmac_f32 v65, v240, v145
v_fmac_f32 v66, v240, v146
v_fmac_f32 v67, v240, v147
v_fmac_f32 v68, v240, v148
v_fmac_f32 v69, v240, v149
v_fmac_f32 v70, v240, v150
v_fmac_f32 v71, v240, v151
v_mul_f32 v195, -v250, v244
v_fma_f32 v64, v244, v64, v195
v_fma_f32 v65, v244, v65, v195
MM v[108:111],a[60:63],a[172:175],v[108:111]
s_add_u32 s77, s64, 16384
s_add_u32 s81, s77, 896
s_add_u32 m0, s65, 81920
v_fma_f32 v66, v244, v66, v195
v_fma_f32 v67, v244, v67, v195
MM v[108:111],a[56:59],a[168:171],v[108:111]
v_fma_f32 v68, v244, v68, v195
v_fma_f32 v69, v244, v69, v195
MM v[108:111],a[52:55],a[140:143],v[108:111]
v_fma_f32 v70, v244, v70, v195
v_fma_f32 v71, v244, v71, v195
CV v192,v64,v65
MM v[108:111],a[48:51],a[136:139],v[108:111]
CV v193,v66,v67
CV v194,v68,v69
MM v[16:19],a[64:67],a[144:147],v[16:19]
CV v195,v70,v71
v_fmac_f32 v72, v241, v144
v_fmac_f32 v73, v241, v145
MM v[16:19],a[68:71],a[148:151],v[16:19]
v_fmac_f32 v74, v241, v146
v_fmac_f32 v75, v241, v147
v_fmac_f32 v76, v241, v148
MM v[16:19],a[72:75],a[176:179],v[16:19]
v_fmac_f32 v77, v241, v149
v_fmac_f32 v78, v241, v150
MM v[16:19],a[76:79],a[180:183],v[16:19]
v_fmac_f32 v79, v241, v151
v_mul_f32 v199, -v250, v245
v_fma_f32 v72, v245, v72, v199
MM v[24:27],a[76:79],a[188:191],v[24:27]
v_fma_f32 v73, v245, v73, v199
v_fma_f32 v74, v245, v74, v199
MM v[24:27],a[72:75],a[184:187],v[24:27]
buffer_load_dwordx4 v235, s[12:15], s81 offen offset:0 lds
v_fma_f32 v75, v245, v75, v199
v_fma_f32 v76, v245, v76, v199
MM v[24:27],a[68:71],a[156:159],v[24:27]
s_waitcnt vmcnt(13)
s_barrier
RD a[192:195],v224 offset:0
v_fma_f32 v77, v245, v77, v199
v_fma_f32 v78, v245, v78, v199
v_fma_f32 v79, v245, v79, v199
MM v[24:27],a[64:67],a[152:155],v[24:27]
RD a[196:199],v225 offset:0
RD a[200:203],v226 offset:0
CV v196,v72,v73
CV v197,v74,v75
MM v[20:23],a[80:83],a[144:147],v[20:23]
RD a[204:207],v227 offset:0
RD a[208:211],v224 offset:4096
CV v198,v76,v77
CV v199,v78,v79
MM v[20:23],a[84:87],a[148:151],v[20:23]
RD a[212:215],v225 offset:4096
RD a[216:219],v226 offset:4096
v_xor_b32 v159, 384, v253
MM v[20:23],a[88:91],a[176:179],v[20:23]
RD a[220:223],v227 offset:4096
RD a[224:227],v224 offset:8192
RD v[152:155],v159 offset:0
RD v[156:159],v159 offset:16
MM v[20:23],a[92:95],a[180:183],v[20:23]
RD a[228:231],v225 offset:8192
RD a[232:235],v226 offset:8192
MM v[28:31],a[92:95],a[188:191],v[28:31]
RD a[236:239],v227 offset:8192
RD a[240:243],v224 offset:12288
MM v[28:31],a[88:91],a[184:187],v[28:31]
RD a[244:247],v225 offset:12288
RD a[248:251],v226 offset:12288
MM v[28:31],a[84:87],a[156:159],v[28:31]
s_waitcnt lgkmcnt(6)
RD a[252:255],v227 offset:12288
v_fmac_f32 v96, v240, v152
v_fmac_f32 v97, v240, v153
v_fmac_f32 v98, v240, v154
v_fmac_f32 v99, v240, v155
v_fmac_f32 v100, v240, v156
v_fmac_f32 v101, v240, v157
v_fmac_f32 v102, v240, v158
v_fmac_f32 v103, v240, v159
v_mul_f32 v211, -v250, v244
v_fma_f32 v96, v244, v96, v211
v_fma_f32 v97, v244, v97, v211
v_fma_f32 v98, v244, v98, v211
MM v[28:31],a[80:83],a[152:155],v[28:31]
buffer_load_dwordx4 v235, s[12:15], s77 offen offset:1024 lds
v_fma_f32 v99, v244, v99, v211
v_fma_f32 v100, v244, v100, v211
MM v[48:51],a[96:99],a[144:147],v[48:51]
v_fma_f32 v101, v244, v101, v211
v_fma_f32 v102, v244, v102, v211
v_fma_f32 v103, v244, v103, v211
MM v[48:51],a[100:103],a[148:151],v[48:51]
CV v208,v96,v97
CV v209,v98,v99
MM v[48:51],a[104:107],a[176:179],v[48:51]
CV v210,v100,v101
CV v211,v102,v103
v_fmac_f32 v104, v241, v152
MM v[48:51],a[108:111],a[180:183],v[48:51]
v_fmac_f32 v105, v241, v153
v_fmac_f32 v106, v241, v154
v_fmac_f32 v107, v241, v155
MM v[56:59],a[108:111],a[188:191],v[56:59]
v_fmac_f32 v108, v241, v156
v_fmac_f32 v109, v241, v157
MM v[56:59],a[104:107],a[184:187],v[56:59]
v_fmac_f32 v110, v241, v158
v_fmac_f32 v111, v241, v159
MM v[56:59],a[100:103],a[156:159],v[56:59]
v_mul_f32 v215, -v250, v245
v_fma_f32 v104, v245, v104, v215
v_fma_f32 v105, v245, v105, v215
MM v[56:59],a[96:99],a[152:155],v[56:59]
v_fma_f32 v106, v245, v106, v215
v_fma_f32 v107, v245, v107, v215
MM v[52:55],a[112:115],a[144:147],v[52:55]
v_fma_f32 v108, v245, v108, v215
v_fma_f32 v109, v245, v109, v215
MM v[52:55],a[116:119],a[148:151],v[52:55]
buffer_load_dwordx4 v235, s[12:15], s81 offen offset:2048 lds
v_fma_f32 v110, v245, v110, v215
v_fma_f32 v111, v245, v111, v215
CV v212,v104,v105
MM v[52:55],a[120:123],a[176:179],v[52:55]
CV v213,v106,v107
CV v214,v108,v109
CV v215,v110,v111
MM v[52:55],a[124:127],a[180:183],v[52:55]
v_mov_b32 v241, 0
v_mov_b32 v244, 0
s_waitcnt lgkmcnt(0)
MM v[0:3],a[192:195],v[160:163],0
v_mov_b32 v144, 0
v_mov_b32 v145, 0
MM v[0:3],a[196:199],v[176:179],v[0:3]
v_mov_b32 v245, 0
v_mov_b32 v148, 0
MM v[0:3],a[200:203],v[192:195],v[0:3]
v_mov_b32 v149, 0
v_fmac_f32 v16, v242, v136
MM v[0:3],a[204:207],v[208:211],v[0:3]
v_fmac_f32 v17, v242, v137
v_fmac_f32 v18, v242, v138
MM v[8:11],a[192:195],v[164:167],0
v_fmac_f32 v19, v242, v139
v_fmac_f32 v20, v242, v140
MM v[8:11],a[196:199],v[180:183],v[8:11]
v_fmac_f32 v21, v242, v141
v_fmac_f32 v22, v242, v142
MM v[8:11],a[200:203],v[196:199],v[8:11]
v_fmac_f32 v23, v242, v143
v_mul_f32 v16, v246, v16
MM v[8:11],a[204:207],v[212:215],v[8:11]
buffer_load_dwordx4 v235, s[12:15], s77 offen offset:3072 lds
MM v[4:7],a[208:211],v[160:163],0
s_add_u32 s77, s64, 32768
s_add_u32 s81, s77, 896
s_add_u32 m0, s65, 98304
s_nop 0
LPG4_%=:
s_cbranch_execz LPG8_%=
MM v[4:7],a[212:215],v[176:179],v[4:7]
v_max3_f32 v146, v0, v1, v2
v_max_f32 v146, v146, v3
MM v[4:7],a[216:219],v[192:195],v[4:7]
v_mov_b32 v147, v146
v_mul_f32 v17, v246, v17
MM v[4:7],a[220:223],v[208:211],v[4:7]
v_permlane16_swap_b32 v146, v147
v_max_f32 v146, v146, v147
MM v[12:15],a[208:211],v[164:167],0
s_waitcnt vmcnt(8)
s_barrier
RD a[192:195],v224 offset:16384
MM v[12:15],a[212:215],v[180:183],v[12:15]
RD a[196:199],v225 offset:16384
RD a[200:203],v226 offset:16384
MM v[12:15],a[216:219],v[196:199],v[12:15]
RD a[204:207],v227 offset:16384
v_mov_b32 v147, v146
MM v[12:15],a[220:223],v[212:215],v[12:15]
s_nop 0
v_permlane32_swap_b32 v146, v147
v_max_f32 v146, v146, v147
v_sub_f32 v128, 0xc1600000, v146
v_sub_f32 v129, 0xc1600000, v146
MM v[32:35],a[224:227],v[160:163],0
RD a[208:211],v224 offset:20480
RD a[212:215],v225 offset:20480
v_sub_f32 v130, 0xc1600000, v146
v_sub_f32 v131, 0xc1600000, v146
v_max3_f32 v150, v8, v9, v10
MM v[32:35],a[228:231],v[176:179],v[32:35]
buffer_load_dwordx4 v235, s[12:15], s81 offen offset:0 lds
v_max_f32 v150, v150, v11
v_mov_b32 v151, v150
MM v[32:35],a[232:235],v[192:195],v[32:35]
RD a[216:219],v226 offset:20480
RD a[220:223],v227 offset:20480
v_permlane16_swap_b32 v150, v151
v_max_f32 v150, v150, v151
v_mov_b32 v151, v150
MM v[32:35],a[236:239],v[208:211],v[32:35]
s_nop 0
v_permlane32_swap_b32 v150, v151
v_max_f32 v150, v150, v151
v_add_f32 v0, v0, v128
v_sub_f32 v132, 0xc1600000, v150
MM v[40:43],a[224:227],v[164:167],0
v_sub_f32 v133, 0xc1600000, v150
v_sub_f32 v134, 0xc1600000, v150
v_sub_f32 v135, 0xc1600000, v150
v_add_f32 v1, v1, v128
EX v0,v0
MM v[40:43],a[228:231],v[180:183],v[40:43]
EX v1,v1
MM v[40:43],a[232:235],v[196:199],v[40:43]
v_add_f32 v2, v2, v128
v_add_f32 v3, v3, v128
MM v[40:43],a[236:239],v[212:215],v[40:43]
EX v2,v2
MM v[36:39],a[240:243],v[160:163],v[128:131]
RD a[224:227],v224 offset:24576
RD a[228:231],v225 offset:24576
EX v3,v3
MM v[36:39],a[244:247],v[176:179],v[36:39]
RD a[232:235],v226 offset:24576
RD a[236:239],v227 offset:24576
CV v0,v0,v1
v_add_f32 v4, v4, v128
MM v[36:39],a[248:251],v[192:195],v[36:39]
v_add_f32 v5, v5, v128
v_mul_f32 v18, v246, v18
EX v4,v4
MM v[36:39],a[252:255],v[208:211],v[36:39]
buffer_load_dwordx4 v235, s[12:15], s77 offen offset:1024 lds
EX v5,v5
MM v[44:47],a[240:243],v[164:167],v[132:135]
CV v1,v2,v3
v_add_f32 v6, v6, v128
MM v[44:47],a[244:247],v[180:183],v[44:47]
v_add_f32 v7, v7, v128
v_mul_f32 v19, v246, v19
MM v[44:47],a[248:251],v[196:199],v[44:47]
EX v6,v6
EX v7,v7
MM v[44:47],a[252:255],v[212:215],v[44:47]
CV v2,v4,v5
v_add_f32 v8, v8, v132
MM v[60:63],a[124:127],a[188:191],v[60:63]
RD a[240:243],v224 offset:28672
RD a[244:247],v225 offset:28672
v_add_f32 v9, v9, v132
MM v[60:63],a[120:123],a[184:187],v[60:63]
RD a[248:251],v226 offset:28672
RD a[252:255],v227 offset:28672
EX v8,v8
MM v[60:63],a[116:119],a[156:159],v[60:63]
EX v9,v9
MM v[60:63],a[112:115],a[152:155],v[60:63]
CV v3,v6,v7
v_add_f32 v10, v10, v132
MM v[80:83],a[0:3],a[144:147],v[80:83]
v_add_f32 v11, v11, v132
v_mul_f32 v20, v246, v20
EX v10,v10
MM v[80:83],a[4:7],a[148:151],v[80:83]
buffer_load_dwordx4 v235, s[12:15], s81 offen offset:2048 lds
EX v11,v11
MM v[80:83],a[8:11],a[176:179],v[80:83]
CV v8,v8,v9
v_add_f32 v12, v12, v132
MM v[80:83],a[12:15],a[180:183],v[80:83]
v_add_f32 v13, v13, v132
v_mul_f32 v21, v246, v21
EX v12,v12
MM v[88:91],a[12:15],a[188:191],v[88:91]
EX v13,v13
MM v[88:91],a[8:11],a[184:187],v[88:91]
CV v9,v10,v11
v_add_f32 v14, v14, v132
MM v[88:91],a[4:7],a[156:159],v[88:91]
v_add_f32 v15, v15, v132
v_mul_f32 v22, v246, v22
EX v14,v14
MM v[88:91],a[0:3],a[152:155],v[88:91]
EX v15,v15
MM v[84:87],a[16:19],a[144:147],v[84:87]
CV v10,v12,v13
v_add_f32 v32, v32, v128
MM v[84:87],a[20:23],a[148:151],v[84:87]
v_add_f32 v33, v33, v128
v_mul_f32 v23, v246, v23
MM v[84:87],a[24:27],a[176:179],v[84:87]
EX v32,v32
MM v[84:87],a[28:31],a[180:183],v[84:87]
buffer_load_dwordx4 v235, s[12:15], s77 offen offset:3072 lds
EX v33,v33
MM v[92:95],a[28:31],a[188:191],v[92:95]
s_add_u32 s77, s64, 49152
s_add_u32 m0, s65, 49152
CV v11,v14,v15
v_add_f32 v34, v34, v128
v_add_f32 v35, v35, v128
MM v[92:95],a[24:27],a[184:187],v[92:95]
buffer_load_dwordx4 v228, s[8:11], s77 offen offset:0 lds
EX v34,v34
MM v[92:95],a[20:23],a[156:159],v[92:95]
EX v35,v35
MM v[92:95],a[16:19],a[152:155],v[92:95]
CV v4,v32,v33
CV v168,v16,v17
EX v36,v36
MM v[112:115],a[32:35],a[144:147],v[112:115]
EX v37,v37
MM v[112:115],a[36:39],a[148:151],v[112:115]
buffer_load_dwordx4 v229, s[8:11], s77 offen offset:1024 lds
CV v5,v34,v35
MM v[112:115],a[40:43],a[176:179],v[112:115]
EX v38,v38
EX v39,v39
MM v[112:115],a[44:47],a[180:183],v[112:115]
CV v6,v36,v37
v_add_f32 v40, v40, v132
MM v[120:123],a[44:47],a[188:191],v[120:123]
v_add_f32 v41, v41, v132
CV v169,v18,v19
MM v[120:123],a[40:43],a[184:187],v[120:123]
buffer_load_dwordx4 v230, s[8:11], s77 offen offset:2048 lds
EX v40,v40
MM v[120:123],a[36:39],a[156:159],v[120:123]
EX v41,v41
MM v[120:123],a[32:35],a[152:155],v[120:123]
CV v7,v38,v39
v_add_f32 v42, v42, v132
MM v[116:119],a[48:51],a[144:147],v[116:119]
v_add_f32 v43, v43, v132
CV v170,v20,v21
EX v42,v42
MM v[116:119],a[52:55],a[148:151],v[116:119]
buffer_load_dwordx4 v231, s[8:11], s77 offen offset:3072 lds
EX v43,v43
MM v[116:119],a[56:59],a[176:179],v[116:119]
s_add_u32 s77, s64, 49152
s_add_u32 s81, s77, 896
s_add_u32 m0, s65, 114688
CV v12,v40,v41
EX v44,v44
MM v[116:119],a[60:63],a[180:183],v[116:119]
EX v45,v45
MM v[124:127],a[60:63],a[188:191],v[124:127]
CV v13,v42,v43
CV v171,v22,v23
MM v[124:127],a[56:59],a[184:187],v[124:127]
buffer_load_dwordx4 v235, s[12:15], s81 offen offset:0 lds
EX v46,v46
MM v[124:127],a[52:55],a[156:159],v[124:127]
EX v47,v47
CV v14,v44,v45
CV v15,v46,v47
MM v[124:127],a[48:51],a[152:155],v[124:127]
v_fmac_f32 v24, v243, v136
v_fmac_f32 v25, v243, v137
v_smfmac_f32_16x16x64_bf16 v[144:147], v[236:239], v[0:7], v234
v_fmac_f32 v26, v243, v138
v_smfmac_f32_16x16x64_bf16 v[148:151], v[236:239], v[8:15], v234
)ASM"
R"ASM(
s_waitcnt lgkmcnt(0)
v_fmac_f32 v27, v243, v139
buffer_load_dwordx4 v235, s[12:15], s77 offen offset:1024 lds
buffer_load_dwordx4 v235, s[12:15], s81 offen offset:2048 lds
buffer_load_dwordx4 v235, s[12:15], s77 offen offset:3072 lds
s_add_u32 s64, s64, 49152
MM v[64:67],a[192:195],v[160:163],v[128:131]
s_add_u32 s64, s64, 16384
v_fmac_f32 v28, v243, v140
v_fmac_f32 v29, v243, v141
MM v[64:67],a[196:199],v[176:179],v[64:67]
s_add_u32 s81, s64, 896
TR a[128:129],v232 offset:0
v_fmac_f32 v30, v243, v142
MM v[64:67],a[200:203],v[192:195],v[64:67]
v_fmac_f32 v31, v243, v143
v_mul_f32 v24, v247, v24
MM v[64:67],a[204:207],v[208:211],v[64:67]
v_max3_f32 v249, v144, v145, v148
v_max_f32 v249, v249, v149
v_mul_f32 v25, v247, v25
MM v[68:71],a[220:223],v[208:211],v[128:131]
TR a[130:131],v232 offset:4096
TR a[132:133],v232 offset:8192
v_cmp_lt_f32 vcc, s71, v249
v_mul_f32 v26, v247, v26
v_mul_f32 v27, v247, v27
MM v[68:71],a[216:219],v[192:195],v[68:71]
s_cbranch_vccnz LSITE0_%=
LBACK0_%=:
TR a[134:135],v232 offset:12288
v_add_f32 v244, v144, v145
v_mul_f32 v28, v247, v28
v_mul_f32 v29, v247, v29
v_mul_f32 v30, v247, v30
MM v[68:71],a[212:215],v[176:179],v[68:71]
TR a[136:137],v233 offset:0
v_add_f32 v245, v148, v149
v_mul_f32 v31, v247, v31
CV v172,v24,v25
MM v[68:71],a[208:211],v[160:163],v[68:71]
TR a[138:139],v233 offset:4096
CV v173,v26,v27
CV v174,v28,v29
MM v[76:79],a[208:211],v[164:167],v[132:135]
TR a[140:141],v233 offset:8192
TR a[142:143],v233 offset:12288
CV v175,v30,v31
v_xor_b32 v143, 128, v253
MM v[76:79],a[212:215],v[180:183],v[76:79]
RD v[136:139],v143 offset:0
RD v[140:143],v143 offset:16
TR a[144:145],v232 offset:512
MM v[76:79],a[216:219],v[196:199],v[76:79]
s_waitcnt lgkmcnt(9)
TR a[146:147],v232 offset:4608
TR a[148:149],v232 offset:8704
MM v[76:79],a[220:223],v[212:215],v[76:79]
EX v64,v64
MM v[32:35],a[128:131],v[0:3],0
MM v[36:39],a[128:131],v[8:11],0
MM v[72:75],a[204:207],v[212:215],v[132:135]
TR a[150:151],v232 offset:12800
TR a[152:153],v233 offset:512
MM v[72:75],a[200:203],v[196:199],v[72:75]
s_waitcnt lgkmcnt(5)
TR a[154:155],v233 offset:4608
v_fmac_f32 v48, v242, v136
v_fmac_f32 v49, v242, v137
v_fmac_f32 v50, v242, v138
v_fmac_f32 v51, v242, v139
v_fmac_f32 v52, v242, v140
v_fmac_f32 v53, v242, v141
v_fmac_f32 v54, v242, v142
v_fmac_f32 v55, v242, v143
v_mul_f32 v187, -v252, v246
v_fma_f32 v48, v246, v48, v187
v_fma_f32 v49, v246, v49, v187
EX v65,v65
MM v[72:75],a[196:199],v[180:183],v[72:75]
TR a[156:157],v233 offset:8704
v_fma_f32 v50, v246, v50, v187
v_fma_f32 v51, v246, v51, v187
v_fma_f32 v52, v246, v52, v187
MM v[72:75],a[192:195],v[164:167],v[72:75]
TR a[158:159],v233 offset:12800
v_fma_f32 v53, v246, v53, v187
EX v66,v66
v_fma_f32 v54, v246, v54, v187
MM v[96:99],a[224:227],v[160:163],v[128:131]
TR a[160:161],v232 offset:1024
v_max3_f32 v32, |v32|, |v33|, |v34|
v_max_f32 v32, v32, |v35|
v_fma_f32 v55, v246, v55, v187
CV v184,v48,v49
EX v67,v67
MM v[96:99],a[228:231],v[176:179],v[96:99]
TR a[162:163],v232 offset:5120
TR a[164:165],v232 offset:9216
v_bfe_u32 v32, v32, 23, 8
v_add_u32 v32, 7, v32
CV v185,v50,v51
CV v186,v52,v53
CV v187,v54,v55
v_fmac_f32 v56, v243, v136
MM v[96:99],a[232:235],v[192:195],v[96:99]
TR a[166:167],v232 offset:13312
v_min_u32 v32, 240, v32
v_lshl_or_b32 v241, v32, 0, v241
v_fmac_f32 v57, v243, v137
v_fmac_f32 v58, v243, v138
EX v68,v68
MM v[96:99],a[236:239],v[208:211],v[96:99]
TR a[168:169],v233 offset:1024
v_lshl_or_b32 v32, v32, 1, 1
v_lshlrev_b32 v32, 22, v32
v_fmac_f32 v59, v243, v139
v_fmac_f32 v60, v243, v140
MM v[100:103],a[252:255],v[208:211],v[128:131]
TR a[170:171],v233 offset:5120
AW a100,v32
AW a101,v32
v_fmac_f32 v61, v243, v141
v_fmac_f32 v62, v243, v142
EX v69,v69
v_fmac_f32 v63, v243, v143
MM v[100:103],a[248:251],v[192:195],v[100:103]
TR a[172:173],v233 offset:9216
TR a[174:175],v233 offset:13312
AW a102,v32
AW a103,v32
v_mul_f32 v191, -v252, v247
v_fma_f32 v56, v247, v56, v191
MM v[100:103],a[244:247],v[176:179],v[100:103]
TR a[176:177],v232 offset:1536
v_max3_f32 v36, |v36|, |v37|, |v38|
v_max_f32 v36, v36, |v39|
v_fma_f32 v57, v247, v57, v191
EX v70,v70
v_fma_f32 v58, v247, v58, v191
MM v[100:103],a[240:243],v[160:163],v[100:103]
TR a[178:179],v232 offset:5632
v_bfe_u32 v36, v36, 23, 8
v_add_u32 v36, 7, v36
v_fma_f32 v59, v247, v59, v191
v_fma_f32 v60, v247, v60, v191
EX v71,v71
v_fma_f32 v61, v247, v61, v191
MM v[108:111],a[240:243],v[164:167],v[132:135]
TR a[180:181],v232 offset:9728
v_min_u32 v36, 240, v36
v_lshl_or_b32 v241, v36, 8, v241
v_fma_f32 v62, v247, v62, v191
v_fma_f32 v63, v247, v63, v191
CV v64,v64,v65
MM v[108:111],a[244:247],v[180:183],v[108:111]
TR a[182:183],v232 offset:13824
v_lshl_or_b32 v36, v36, 1, 1
v_lshlrev_b32 v36, 22, v36
CV v188,v56,v57
CV v189,v58,v59
EX v72,v72
CV v190,v60,v61
MM v[108:111],a[248:251],v[196:199],v[108:111]
TR a[184:185],v233 offset:1536
TR a[186:187],v233 offset:5632
TR a[188:189],v233 offset:9728
TR a[190:191],v233 offset:13824
AW a108,v36
AW a109,v36
CV v191,v62,v63
v_xor_b32 v143, 256, v253
EX v73,v73
MM v[108:111],a[252:255],v[212:215],v[108:111]
RD v[136:139],v143 offset:0
RD v[140:143],v143 offset:16
AW a110,v36
AW a111,v36
CV v65,v66,v67
MM v[104:107],a[236:239],v[212:215],v[132:135]
EX v74,v74
MM v[104:107],a[232:235],v[196:199],v[104:107]
EX v75,v75
MM v[104:107],a[228:231],v[180:183],v[104:107]
EX v76,v76
CV v66,v68,v69
MM v[104:107],a[224:227],v[164:167],v[104:107]
s_waitcnt vmcnt(12) lgkmcnt(0)
s_barrier
v_fmac_f32 v80, v242, v136
v_fmac_f32 v81, v242, v137
v_fmac_f32 v82, v242, v138
v_fmac_f32 v83, v242, v139
v_fmac_f32 v84, v242, v140
v_fmac_f32 v85, v242, v141
v_fmac_f32 v86, v242, v142
v_fmac_f32 v87, v242, v143
v_mul_f32 v203, -v252, v246
v_fma_f32 v80, v246, v80, v203
MM a[0:3],a[128:131],v[0:3],a[100:103]
s_add_u32 m0, s65, 0
v_fma_f32 v81, v246, v81, v203
v_fma_f32 v82, v246, v82, v203
v_fma_f32 v83, v246, v83, v203
MM a[0:3],a[132:135],v[4:7],a[0:3]
v_fma_f32 v84, v246, v84, v203
v_fma_f32 v85, v246, v85, v203
EX v77,v77
CV v67,v70,v71
MM a[8:11],a[132:135],v[12:15],a[108:111]
v_fma_f32 v86, v246, v86, v203
v_fma_f32 v87, v246, v87, v203
MM a[8:11],a[128:131],v[8:11],a[8:11]
CV v200,v80,v81
CV v201,v82,v83
EX v78,v78
CV v202,v84,v85
MM a[12:15],a[136:139],v[8:11],a[108:111]
CV v203,v86,v87
v_fmac_f32 v88, v243, v136
v_fmac_f32 v89, v243, v137
EX v79,v79
MM a[12:15],a[140:143],v[12:15],a[12:15]
v_fmac_f32 v90, v243, v138
v_fmac_f32 v91, v243, v139
CV v72,v72,v73
MM a[4:7],a[140:143],v[4:7],a[100:103]
v_fmac_f32 v92, v243, v140
v_fmac_f32 v93, v243, v141
EX v96,v96
v_fmac_f32 v94, v243, v142
MM a[4:7],a[136:139],v[0:3],a[4:7]
v_fmac_f32 v95, v243, v143
v_mul_f32 v207, -v252, v247
MM a[32:35],a[144:147],v[0:3],a[100:103]
RD a[192:195],v224 offset:32768
v_fma_f32 v88, v247, v88, v207
v_fma_f32 v89, v247, v89, v207
EX v97,v97
CV v73,v74,v75
v_fma_f32 v90, v247, v90, v207
MM a[32:35],a[148:151],v[4:7],a[32:35]
buffer_load_dwordx4 v228, s[8:11], s64 offen offset:0 lds
v_fma_f32 v91, v247, v91, v207
v_fma_f32 v92, v247, v92, v207
EX v98,v98
MM a[40:43],a[148:151],v[12:15],a[108:111]
RD a[196:199],v225 offset:32768
v_fma_f32 v93, v247, v93, v207
v_fma_f32 v94, v247, v94, v207
MM a[40:43],a[144:147],v[8:11],a[40:43]
RD a[200:203],v226 offset:32768
v_fma_f32 v95, v247, v95, v207
CV v204,v88,v89
EX v99,v99
CV v74,v76,v77
CV v205,v90,v91
MM a[44:47],a[152:155],v[8:11],a[108:111]
CV v206,v92,v93
CV v207,v94,v95
MM a[44:47],a[156:159],v[12:15],a[44:47]
RD a[204:207],v227 offset:32768
buffer_load_dwordx4 v229, s[8:11], s64 offen offset:1024 lds
v_xor_b32 v143, 384, v253
EX v100,v100
MM a[36:39],a[156:159],v[4:7],a[100:103]
RD v[136:139],v143 offset:0
RD v[140:143],v143 offset:16
EX v101,v101
CV v75,v78,v79
MM a[36:39],a[152:155],v[0:3],a[36:39]
RD a[208:211],v224 offset:36864
RD a[212:215],v225 offset:36864
MM a[64:67],a[160:163],v[0:3],a[100:103]
EX v102,v102
MM a[64:67],a[164:167],v[4:7],a[64:67]
buffer_load_dwordx4 v230, s[8:11], s64 offen offset:2048 lds
MM a[72:75],a[164:167],v[12:15],a[108:111]
s_waitcnt lgkmcnt(2)
v_fmac_f32 v112, v242, v136
v_fmac_f32 v113, v242, v137
v_fmac_f32 v114, v242, v138
v_fmac_f32 v115, v242, v139
v_fmac_f32 v116, v242, v140
v_fmac_f32 v117, v242, v141
v_fmac_f32 v118, v242, v142
v_fmac_f32 v119, v242, v143
v_mul_f32 v219, -v252, v246
v_fma_f32 v112, v246, v112, v219
v_fma_f32 v113, v246, v113, v219
EX v103,v103
CV v68,v96,v97
v_fma_f32 v114, v246, v114, v219
MM a[72:75],a[160:163],v[8:11],a[72:75]
RD a[216:219],v226 offset:36864
v_fma_f32 v115, v246, v115, v219
v_fma_f32 v116, v246, v116, v219
EX v104,v104
MM a[76:79],a[168:171],v[8:11],a[108:111]
buffer_load_dwordx4 v231, s[8:11], s64 offen offset:3072 lds
RD a[220:223],v227 offset:36864
v_fma_f32 v117, v246, v117, v219
v_fma_f32 v118, v246, v118, v219
MM a[76:79],a[172:175],v[12:15],a[76:79]
s_add_u32 m0, s65, 65536
v_fma_f32 v119, v246, v119, v219
CV v216,v112,v113
EX v105,v105
CV v217,v114,v115
CV v69,v98,v99
MM a[68:71],a[172:175],v[4:7],a[100:103]
RD a[224:227],v224 offset:40960
CV v218,v116,v117
CV v219,v118,v119
v_fmac_f32 v120, v243, v136
MM a[68:71],a[168:171],v[0:3],a[68:71]
v_fmac_f32 v121, v243, v137
v_fmac_f32 v122, v243, v138
EX v106,v106
v_fmac_f32 v123, v243, v139
MM a[96:99],a[176:179],v[0:3],a[100:103]
RD a[228:231],v225 offset:40960
buffer_load_dwordx4 v235, s[12:15], s81 offen offset:0 lds
v_fmac_f32 v124, v243, v140
v_fmac_f32 v125, v243, v141
EX v107,v107
CV v70,v100,v101
MM a[96:99],a[180:183],v[4:7],a[96:99]
v_fmac_f32 v126, v243, v142
v_fmac_f32 v127, v243, v143
MM a[104:107],a[180:183],v[12:15],a[108:111]
RD a[232:235],v226 offset:40960
v_mul_f32 v223, -v252, v247
v_fma_f32 v120, v247, v120, v223
EX v108,v108
v_fma_f32 v121, v247, v121, v223
MM a[104:107],a[176:179],v[8:11],a[104:107]
v_fma_f32 v122, v247, v122, v223
v_fma_f32 v123, v247, v123, v223
CV v71,v102,v103
MM a[108:111],a[184:187],v[8:11],a[108:111]
RD a[236:239],v227 offset:40960
buffer_load_dwordx4 v235, s[12:15], s64 offen offset:1024 lds
v_fma_f32 v124, v247, v124, v223
v_fma_f32 v125, v247, v125, v223
EX v109,v109
MM a[108:111],a[188:191],v[12:15],a[108:111]
v_fma_f32 v126, v247, v126, v223
v_fma_f32 v127, v247, v127, v223
EX v110,v110
CV v220,v120,v121
MM a[100:103],a[188:191],v[4:7],a[100:103]
RD a[240:243],v224 offset:45056
RD a[244:247],v225 offset:45056
RD a[248:251],v226 offset:45056
RD a[252:255],v227 offset:45056
CV v221,v122,v123
EX v111,v111
CV v222,v124,v125
CV v223,v126,v127
MM a[100:103],a[184:187],v[0:3],a[100:103]
buffer_load_dwordx4 v235, s[12:15], s81 offen offset:2048 lds
CV v76,v104,v105
v_smfmac_f32_16x16x64_bf16 v[144:147], v[236:239], v[64:71], v234
CV v77,v106,v107
CV v78,v108,v109
CV v79,v110,v111
s_nop 1
v_smfmac_f32_16x16x64_bf16 v[148:151], v[236:239], v[72:79], v234
s_waitcnt lgkmcnt(0)
buffer_load_dwordx4 v235, s[12:15], s64 offen offset:3072 lds
MM v[0:3],a[192:195],v[160:163],v[128:131]
s_add_u32 s64, s64, 16384
TR a[128:129],v232 offset:16384
TR a[130:131],v232 offset:20480
MM v[0:3],a[196:199],v[176:179],v[0:3]
s_add_u32 s81, s64, 896
TR a[132:133],v232 offset:24576
TR a[134:135],v232 offset:28672
MM v[0:3],a[200:203],v[192:195],v[0:3]
TR a[136:137],v233 offset:16384
TR a[138:139],v233 offset:20480
MM v[0:3],a[204:207],v[208:211],v[0:3]
v_max3_f32 v249, v144, v145, v148
v_max_f32 v249, v249, v149
MM v[4:7],a[220:223],v[208:211],v[128:131]
TR a[140:141],v233 offset:24576
v_cmp_lt_f32 vcc, s71, v249
MM v[4:7],a[216:219],v[192:195],v[4:7]
s_cbranch_vccnz LSITE1_%=
LBACK1_%=:
TR a[142:143],v233 offset:28672
v_add_f32 v244, v144, v145
MM v[4:7],a[212:215],v[176:179],v[4:7]
TR a[144:145],v232 offset:16896
v_add_f32 v245, v148, v149
MM v[4:7],a[208:211],v[160:163],v[4:7]
TR a[146:147],v232 offset:20992
TR a[148:149],v232 offset:25088
MM v[12:15],a[208:211],v[164:167],v[132:135]
EX v0,v0
MM v[12:15],a[212:215],v[180:183],v[12:15]
EX v1,v1
MM v[12:15],a[216:219],v[196:199],v[12:15]
TR a[150:151],v232 offset:29184
CV v0,v0,v1
MM v[12:15],a[220:223],v[212:215],v[12:15]
TR a[152:153],v233 offset:16896
TR a[154:155],v233 offset:20992
s_nop 0
LPG5_%=:
)ASM"
R"ASM(
s_cbranch_execz LPG9_%=
MM v[8:11],a[204:207],v[212:215],v[132:135]
EX v2,v2
MM v[8:11],a[200:203],v[196:199],v[8:11]
EX v3,v3
MM v[8:11],a[196:199],v[180:183],v[8:11]
TR a[156:157],v233 offset:25088
CV v1,v2,v3
MM v[8:11],a[192:195],v[164:167],v[8:11]
TR a[158:159],v233 offset:29184
TR a[160:161],v232 offset:17408
MM v[32:35],a[224:227],v[160:163],v[128:131]
EX v4,v4
MM v[32:35],a[228:231],v[176:179],v[32:35]
TR a[162:163],v232 offset:21504
TR a[164:165],v232 offset:25600
MM v[32:35],a[232:235],v[192:195],v[32:35]
EX v5,v5
MM v[32:35],a[236:239],v[208:211],v[32:35]
TR a[166:167],v232 offset:29696
CV v2,v4,v5
MM v[36:39],a[252:255],v[208:211],v[128:131]
EX v6,v6
MM v[36:39],a[248:251],v[192:195],v[36:39]
TR a[168:169],v233 offset:17408
TR a[170:171],v233 offset:21504
MM v[36:39],a[244:247],v[176:179],v[36:39]
EX v7,v7
MM v[36:39],a[240:243],v[160:163],v[36:39]
TR a[172:173],v233 offset:25600
CV v3,v6,v7
MM v[44:47],a[240:243],v[164:167],v[132:135]
EX v8,v8
MM v[44:47],a[244:247],v[180:183],v[44:47]
TR a[174:175],v233 offset:29696
TR a[176:177],v232 offset:17920
MM v[44:47],a[248:251],v[196:199],v[44:47]
EX v9,v9
MM v[44:47],a[252:255],v[212:215],v[44:47]
TR a[178:179],v232 offset:22016
TR a[180:181],v232 offset:26112
MM v[40:43],a[236:239],v[212:215],v[132:135]
EX v10,v10
MM v[40:43],a[232:235],v[196:199],v[40:43]
TR a[182:183],v232 offset:30208
CV v8,v8,v9
MM v[40:43],a[228:231],v[180:183],v[40:43]
EX v11,v11
MM v[40:43],a[224:227],v[164:167],v[40:43]
TR a[184:185],v233 offset:17920
TR a[186:187],v233 offset:22016
MM v[16:19],a[192:195],v[168:171],0
EX v12,v12
MM v[16:19],a[196:199],v[184:187],v[16:19]
TR a[188:189],v233 offset:26112
TR a[190:191],v233 offset:30208
MM v[16:19],a[200:203],v[200:203],v[16:19]
EX v13,v13
MM v[16:19],a[204:207],v[216:219],v[16:19]
EX v14,v14
MM v[24:27],a[192:195],v[172:175],0
EX v15,v15
MM v[24:27],a[196:199],v[188:191],v[24:27]
EX v32,v32
MM v[24:27],a[200:203],v[204:207],v[24:27]
EX v33,v33
MM v[24:27],a[204:207],v[220:223],v[24:27]
s_waitcnt vmcnt(12) lgkmcnt(0)
s_barrier
CV v9,v10,v11
MM a[0:3],a[128:131],v[64:67],a[0:3]
s_add_u32 m0, s65, 16384
CV v10,v12,v13
CV v11,v14,v15
MM a[0:3],a[132:135],v[68:71],a[0:3]
RD a[192:195],v224 offset:49152
CV v4,v32,v33
MM a[8:11],a[132:135],v[76:79],a[8:11]
v_max3_f32 v154, v16, v17, v18
v_max_f32 v154, v154, v19
MM a[8:11],a[128:131],v[72:75],a[8:11]
buffer_load_dwordx4 v228, s[8:11], s64 offen offset:0 lds
MM a[12:15],a[136:139],v[72:75],a[12:15]
EX v34,v34
MM a[12:15],a[140:143],v[76:79],a[12:15]
RD a[196:199],v225 offset:49152
v_mov_b32 v155, v154
MM a[4:7],a[140:143],v[68:71],a[4:7]
s_nop 0
v_permlane16_swap_b32 v154, v155
v_max_f32 v154, v154, v155
MM a[4:7],a[136:139],v[64:67],a[4:7]
EX v35,v35
MM a[32:35],a[144:147],v[64:67],a[32:35]
RD a[200:203],v226 offset:49152
v_mov_b32 v155, v154
MM a[32:35],a[148:151],v[68:71],a[32:35]
s_nop 0
v_permlane32_swap_b32 v154, v155
v_max_f32 v154, v154, v155
MM a[40:43],a[148:151],v[76:79],a[40:43]
EX v36,v36
MM a[40:43],a[144:147],v[72:75],a[40:43]
RD a[204:207],v227 offset:49152
v_sub_f32 v136, 0xc1600000, v154
MM a[44:47],a[152:155],v[72:75],a[44:47]
EX v37,v37
MM a[44:47],a[156:159],v[76:79],a[44:47]
buffer_load_dwordx4 v229, s[8:11], s64 offen offset:1024 lds
v_sub_f32 v137, 0xc1600000, v154
v_sub_f32 v138, 0xc1600000, v154
MM a[36:39],a[156:159],v[68:71],a[36:39]
EX v38,v38
MM a[36:39],a[152:155],v[64:67],a[36:39]
RD a[208:211],v224 offset:53248
v_sub_f32 v139, 0xc1600000, v154
v_mov_b32 v246, 0
MM a[64:67],a[160:163],v[64:67],a[64:67]
EX v39,v39
v_mov_b32 v152, 0
MM a[64:67],a[164:167],v[68:71],a[64:67]
buffer_load_dwordx4 v230, s[8:11], s64 offen offset:2048 lds
RD a[212:215],v225 offset:53248
v_mov_b32 v153, 0
MM a[72:75],a[164:167],v[76:79],a[72:75]
EX v40,v40
v_max3_f32 v158, v24, v25, v26
MM a[72:75],a[160:163],v[72:75],a[72:75]
RD a[216:219],v226 offset:53248
v_max_f32 v158, v158, v27
CV v5,v34,v35
MM a[76:79],a[168:171],v[72:75],a[76:79]
buffer_load_dwordx4 v231, s[8:11], s64 offen offset:3072 lds
RD a[220:223],v227 offset:53248
EX v41,v41
v_mov_b32 v159, v158
MM a[76:79],a[172:175],v[76:79],a[76:79]
s_add_u32 m0, s65, 81920
v_permlane16_swap_b32 v158, v159
v_max_f32 v158, v158, v159
MM a[68:71],a[172:175],v[68:71],a[68:71]
RD a[224:227],v224 offset:57344
EX v42,v42
v_mov_b32 v159, v158
MM a[68:71],a[168:171],v[64:67],a[68:71]
s_nop 0
v_permlane32_swap_b32 v158, v159
CV v6,v36,v37
EX v43,v43
MM a[96:99],a[176:179],v[64:67],a[96:99]
RD a[228:231],v225 offset:57344
buffer_load_dwordx4 v235, s[12:15], s81 offen offset:0 lds
v_max_f32 v158, v158, v159
MM a[96:99],a[180:183],v[68:71],a[96:99]
v_sub_f32 v140, 0xc1600000, v158
CV v7,v38,v39
EX v44,v44
MM a[104:107],a[180:183],v[76:79],a[104:107]
RD a[232:235],v226 offset:57344
v_sub_f32 v141, 0xc1600000, v158
MM a[104:107],a[176:179],v[72:75],a[104:107]
v_sub_f32 v142, 0xc1600000, v158
v_sub_f32 v143, 0xc1600000, v158
EX v45,v45
MM a[108:111],a[184:187],v[72:75],a[108:111]
RD a[236:239],v227 offset:57344
buffer_load_dwordx4 v235, s[12:15], s64 offen offset:1024 lds
v_mov_b32 v247, 0
v_mov_b32 v156, 0
v_mov_b32 v157, 0
MM a[108:111],a[188:191],v[76:79],a[108:111]
EX v46,v46
MM a[100:103],a[188:191],v[68:71],a[100:103]
RD a[240:243],v224 offset:61440
RD a[244:247],v225 offset:61440
RD a[248:251],v226 offset:61440
RD a[252:255],v227 offset:61440
EX v47,v47
MM a[100:103],a[184:187],v[64:67],a[100:103]
buffer_load_dwordx4 v235, s[12:15], s81 offen offset:2048 lds
CV v12,v40,v41
v_smfmac_f32_16x16x64_bf16 v[144:147], v[236:239], v[0:7], v234
CV v13,v42,v43
CV v14,v44,v45
CV v15,v46,v47
s_nop 1
v_smfmac_f32_16x16x64_bf16 v[148:151], v[236:239], v[8:15], v234
s_waitcnt lgkmcnt(0)
buffer_load_dwordx4 v235, s[12:15], s64 offen offset:3072 lds
MM v[64:67],a[192:195],v[160:163],v[128:131]
s_add_u32 s64, s64, 16384
TR a[128:129],v232 offset:32768
s_add_u32 s81, s64, 896
MM v[64:67],a[196:199],v[176:179],v[64:67]
TR a[130:131],v232 offset:36864
TR a[132:133],v232 offset:40960
MM v[64:67],a[200:203],v[192:195],v[64:67]
TR a[134:135],v232 offset:45056
TR a[136:137],v233 offset:32768
MM v[64:67],a[204:207],v[208:211],v[64:67]
v_max3_f32 v249, v144, v145, v148
v_max_f32 v249, v249, v149
MM v[68:71],a[220:223],v[208:211],v[128:131]
TR a[138:139],v233 offset:36864
v_cmp_lt_f32 vcc, s71, v249
MM v[68:71],a[216:219],v[192:195],v[68:71]
s_cbranch_vccnz LSITE2_%=
LBACK2_%=:
TR a[140:141],v233 offset:40960
v_add_f32 v244, v144, v145
MM v[68:71],a[212:215],v[176:179],v[68:71]
TR a[142:143],v233 offset:45056
v_add_f32 v245, v148, v149
MM v[68:71],a[208:211],v[160:163],v[68:71]
TR a[144:145],v232 offset:33280
TR a[146:147],v232 offset:37376
MM v[76:79],a[208:211],v[164:167],v[132:135]
s_lshl_b32 s90, %[swave], 10
s_add_u32 s90, s90, 81920
TR a[148:149],v232 offset:41472
MM v[76:79],a[212:215],v[180:183],v[76:79]
buffer_load_dwordx4 v[48:51], v248, s[20:23], s90 offen offset:0 sc1
MM v[76:79],a[216:219],v[196:199],v[76:79]
EX v64,v64
MM v[76:79],a[220:223],v[212:215],v[76:79]
EX v65,v65
MM v[72:75],a[204:207],v[212:215],v[132:135]
EX v66,v66
MM v[72:75],a[200:203],v[196:199],v[72:75]
EX v67,v67
MM v[72:75],a[196:199],v[180:183],v[72:75]
CV v64,v64,v65
CV v65,v66,v67
MM v[72:75],a[192:195],v[164:167],v[72:75]
EX v68,v68
MM v[80:83],a[192:195],v[168:171],v[136:139]
EX v69,v69
MM v[80:83],a[196:199],v[184:187],v[80:83]
EX v70,v70
MM v[80:83],a[200:203],v[200:203],v[80:83]
EX v71,v71
MM v[80:83],a[204:207],v[216:219],v[80:83]
CV v66,v68,v69
CV v67,v70,v71
MM v[84:87],a[220:223],v[216:219],v[136:139]
EX v72,v72
MM v[84:87],a[216:219],v[200:203],v[84:87]
EX v73,v73
MM v[84:87],a[212:215],v[184:187],v[84:87]
TR a[150:151],v232 offset:45568
TR a[152:153],v233 offset:33280
MM v[84:87],a[208:211],v[168:171],v[84:87]
EX v74,v74
MM v[92:95],a[208:211],v[172:175],v[140:143]
EX v75,v75
MM v[92:95],a[212:215],v[188:191],v[92:95]
TR a[154:155],v233 offset:37376
CV v72,v72,v73
MM v[92:95],a[216:219],v[204:207],v[92:95]
EX v76,v76
MM v[92:95],a[220:223],v[220:223],v[92:95]
EX v77,v77
MM v[88:91],a[204:207],v[220:223],v[140:143]
TR a[156:157],v233 offset:41472
CV v73,v74,v75
MM v[88:91],a[200:203],v[204:207],v[88:91]
EX v78,v78
MM v[88:91],a[196:199],v[188:191],v[88:91]
EX v79,v79
MM v[88:91],a[192:195],v[172:175],v[88:91]
TR a[158:159],v233 offset:45568
CV v74,v76,v77
MM v[96:99],a[224:227],v[160:163],v[128:131]
EX v80,v80
MM v[96:99],a[228:231],v[176:179],v[96:99]
EX v81,v81
MM v[96:99],a[232:235],v[192:195],v[96:99]
TR a[160:161],v232 offset:33792
TR a[162:163],v232 offset:37888
MM v[96:99],a[236:239],v[208:211],v[96:99]
EX v82,v82
MM v[100:103],a[252:255],v[208:211],v[128:131]
EX v83,v83
MM v[100:103],a[248:251],v[192:195],v[100:103]
TR a[164:165],v232 offset:41984
CV v75,v78,v79
MM v[100:103],a[244:247],v[176:179],v[100:103]
EX v84,v84
MM v[100:103],a[240:243],v[160:163],v[100:103]
EX v85,v85
MM v[108:111],a[240:243],v[164:167],v[132:135]
TR a[166:167],v232 offset:46080
CV v80,v80,v81
MM v[108:111],a[244:247],v[180:183],v[108:111]
EX v86,v86
MM v[108:111],a[248:251],v[196:199],v[108:111]
EX v87,v87
MM v[108:111],a[252:255],v[212:215],v[108:111]
TR a[168:169],v233 offset:33792
TR a[170:171],v233 offset:37888
MM v[104:107],a[236:239],v[212:215],v[132:135]
EX v88,v88
MM v[104:107],a[232:235],v[196:199],v[104:107]
EX v89,v89
MM v[104:107],a[228:231],v[180:183],v[104:107]
TR a[172:173],v233 offset:41984
CV v81,v82,v83
EX v90,v90
MM v[104:107],a[224:227],v[164:167],v[104:107]
TR a[174:175],v233 offset:46080
CV v82,v84,v85
MM v[112:115],a[224:227],v[168:171],v[136:139]
EX v91,v91
MM v[112:115],a[228:231],v[184:187],v[112:115]
EX v92,v92
MM v[112:115],a[232:235],v[200:203],v[112:115]
TR a[176:177],v232 offset:34304
EX v93,v93
MM v[112:115],a[236:239],v[216:219],v[112:115]
TR a[178:179],v232 offset:38400
CV v83,v86,v87
MM v[116:119],a[252:255],v[216:219],v[136:139]
EX v94,v94
MM v[116:119],a[248:251],v[200:203],v[116:119]
EX v95,v95
MM v[116:119],a[244:247],v[184:187],v[116:119]
TR a[180:181],v232 offset:42496
CV v88,v88,v89
MM v[116:119],a[240:243],v[168:171],v[116:119]
s_memrealtime s[56:57]
EX v96,v96
MM v[124:127],a[240:243],v[172:175],v[140:143]
s_memtime s[58:59]
TR a[182:183],v232 offset:46592
EX v97,v97
CV v89,v90,v91
MM v[124:127],a[244:247],v[188:191],v[124:127]
EX v98,v98
MM v[124:127],a[248:251],v[204:207],v[124:127]
TR a[184:185],v233 offset:34304
TR a[186:187],v233 offset:38400
TR a[188:189],v233 offset:42496
TR a[190:191],v233 offset:46592
MM v[124:127],a[252:255],v[220:223],v[124:127]
EX v99,v99
CV v90,v92,v93
MM v[120:123],a[236:239],v[220:223],v[140:143]
EX v100,v100
MM v[120:123],a[232:235],v[204:207],v[120:123]
EX v101,v101
CV v91,v94,v95
MM v[120:123],a[228:231],v[188:191],v[120:123]
EX v102,v102
MM v[120:123],a[224:227],v[172:175],v[120:123]
s_waitcnt vmcnt(13) lgkmcnt(0)
s_barrier
CV v68,v96,v97
MM a[0:3],a[128:131],v[0:3],a[0:3]
s_sub_u32 s52, s56, s52
s_sub_u32 s54, s58, s54
v_cvt_f32_u32 v250, s52
EX v103,v103
MM a[0:3],a[132:135],v[4:7],a[0:3]
s_add_u32 m0, s65, 32768
v_cvt_f32_u32 v251, s54
CV v69,v98,v99
EX v104,v104
MM a[8:11],a[132:135],v[12:15],a[8:11]
v_rcp_f32 v251, v251
MM a[8:11],a[128:131],v[8:11],a[8:11]
v_mul_f32 v250, 0x47abe000, v250
s_lshl_b32 s52, %[swave], 11
EX v105,v105
MM a[12:15],a[136:139],v[8:11],a[12:15]
v_mul_f32 v250, v250, v251
s_lshl_b32 s54, %[sb], 3
EX v106,v106
MM a[12:15],a[140:143],v[12:15],a[12:15]
v_cvt_u32_f32 v250, v250
s_add_u32 s52, s52, s54
EX v107,v107
MM a[4:7],a[140:143],v[4:7],a[4:7]
s_waitcnt vmcnt(0)
v_add_u32 v250, s56, v250
v_mov_b32 v251, s69
CV v70,v100,v101
MM a[4:7],a[136:139],v[0:3],a[4:7]
s_add_u32 s52, s52, 86016
s_mov_b64 exec, 1
buffer_store_dwordx2 v[250:251], off, s[20:23], s52 sc1
s_mov_b64 exec, -1
s_nop 0
EX v108,v108
MM a[32:35],a[144:147],v[0:3],a[32:35]
RD a[192:195],v224 offset:0
v_xor_b32 v48, s69, v48
v_xor_b32 v49, s69, v49
EX v109,v109
CV v71,v102,v103
MM a[32:35],a[148:151],v[4:7],a[32:35]
buffer_load_dwordx4 v228, s[8:11], s64 offen offset:0 lds
v_xor_b32 v50, s69, v50
v_xor_b32 v51, s69, v51
MM a[40:43],a[148:151],v[12:15],a[40:43]
RD a[196:199],v225 offset:0
)ASM"
R"ASM(
v_or3_b32 v252, v48, v49, v50
v_or_b32 v252, v252, v51
EX v110,v110
MM a[40:43],a[144:147],v[8:11],a[40:43]
v_cmp_ne_u32 vcc, 0, v252
s_cbranch_vccnz LRFR_%=
LRFRb_%=:
EX v111,v111
CV v76,v104,v105
MM a[44:47],a[152:155],v[8:11],a[44:47]
s_lshl_b32 s90, %[swave], 11
s_add_u32 s78, s90, %[slds]
s_add_u32 s78, s78, 155648
RD a[200:203],v226 offset:0
EX v112,v112
MM a[44:47],a[156:159],v[12:15],a[44:47]
s_add_u32 s90, s90, 53248
buffer_load_dwordx4 v229, s[8:11], s64 offen offset:1024 lds
MM a[36:39],a[156:159],v[4:7],a[36:39]
RD a[204:207],v227 offset:0
EX v113,v113
CV v77,v106,v107
MM a[36:39],a[152:155],v[0:3],a[36:39]
EX v114,v114
MM a[64:67],a[160:163],v[0:3],a[64:67]
RD a[208:211],v224 offset:4096
EX v115,v115
CV v78,v108,v109
MM a[64:67],a[164:167],v[4:7],a[64:67]
buffer_load_dwordx4 v230, s[8:11], s64 offen offset:2048 lds
MM a[72:75],a[164:167],v[12:15],a[72:75]
RD a[212:215],v225 offset:4096
EX v116,v116
MM a[72:75],a[160:163],v[8:11],a[72:75]
EX v117,v117
CV v79,v110,v111
MM a[76:79],a[168:171],v[8:11],a[76:79]
RD a[216:219],v226 offset:4096
CV v84,v112,v113
EX v118,v118
MM a[76:79],a[172:175],v[12:15],a[76:79]
buffer_load_dwordx4 v231, s[8:11], s64 offen offset:3072 lds
MM a[68:71],a[172:175],v[4:7],a[68:71]
s_add_u32 m0, s65, 98304
RD a[220:223],v227 offset:4096
EX v119,v119
MM a[68:71],a[168:171],v[0:3],a[68:71]
EX v120,v120
MM a[96:99],a[176:179],v[0:3],a[96:99]
RD a[224:227],v224 offset:8192
CV v85,v114,v115
EX v121,v121
MM a[96:99],a[180:183],v[4:7],a[96:99]
RD a[228:231],v225 offset:8192
CV v86,v116,v117
MM a[104:107],a[180:183],v[12:15],a[104:107]
buffer_load_dwordx4 v235, s[12:15], s81 offen offset:0 lds
EX v122,v122
MM a[104:107],a[176:179],v[8:11],a[104:107]
EX v123,v123
MM a[108:111],a[184:187],v[8:11],a[108:111]
RD a[232:235],v226 offset:8192
CV v87,v118,v119
MM a[108:111],a[188:191],v[12:15],a[108:111]
EX v124,v124
MM a[100:103],a[188:191],v[4:7],a[100:103]
RD a[236:239],v227 offset:8192
buffer_load_dwordx4 v235, s[12:15], s64 offen offset:1024 lds
EX v125,v125
MM a[100:103],a[184:187],v[0:3],a[100:103]
EX v126,v126
v_smfmac_f32_16x16x64_bf16 v[144:147], v[236:239], v[64:71], v234
RD a[240:243],v224 offset:12288
RD a[244:247],v225 offset:12288
RD a[248:251],v226 offset:12288
RD a[252:255],v227 offset:12288
EX v127,v127
v_smfmac_f32_16x16x64_bf16 v[148:151], v[236:239], v[72:79], v234
CV v92,v120,v121
v_smfmac_f32_16x16x64_bf16 v[152:155], v[236:239], v[80:87], v234
buffer_load_dwordx4 v235, s[12:15], s81 offen offset:2048 lds
CV v93,v122,v123
CV v94,v124,v125
CV v95,v126,v127
s_nop 1
v_smfmac_f32_16x16x64_bf16 v[156:159], v[236:239], v[88:95], v234
s_waitcnt lgkmcnt(0)
buffer_load_dwordx4 v235, s[12:15], s64 offen offset:3072 lds
MM v[0:3],a[192:195],v[160:163],v[128:131]
s_add_u32 s64, s64, 16384
TR a[128:129],v232 offset:49152
s_add_u32 s81, s64, 896
MM v[0:3],a[196:199],v[176:179],v[0:3]
TR a[130:131],v232 offset:53248
v_max3_f32 v249, v144, v145, v148
MM v[0:3],a[200:203],v[192:195],v[0:3]
TR a[132:133],v232 offset:57344
v_max3_f32 v249, v249, v149, v152
MM v[0:3],a[204:207],v[208:211],v[0:3]
TR a[134:135],v232 offset:61440
v_max3_f32 v249, v249, v153, v156
MM v[4:7],a[220:223],v[208:211],v[128:131]
TR a[136:137],v233 offset:49152
v_max_f32 v249, v249, v157
MM v[4:7],a[216:219],v[192:195],v[4:7]
TR a[138:139],v233 offset:53248
v_cmp_lt_f32 vcc, s71, v249
MM v[4:7],a[212:215],v[176:179],v[4:7]
TR a[140:141],v233 offset:57344
TR a[142:143],v233 offset:61440
MM v[4:7],a[208:211],v[160:163],v[4:7]
s_cbranch_vccnz LSITE3_%=
LBACK3_%=:
MM v[12:15],a[208:211],v[164:167],v[132:135]
s_mov_b32 m0, s78
EX v0,v0
MM v[12:15],a[212:215],v[180:183],v[12:15]
buffer_load_dwordx4 v248, s[20:23], s90 offen offset:0 sc1 lds
MM v[12:15],a[216:219],v[196:199],v[12:15]
s_waitcnt lgkmcnt(6)
buffer_load_dwordx4 v248, s[20:23], s90 offen offset:1024 sc1 lds
MM v[12:15],a[220:223],v[212:215],v[12:15]
EX v1,v1
MM v[112:115],a[128:131],v[80:83],0
MM v[116:119],a[128:131],v[88:91],0
MM v[8:11],a[204:207],v[212:215],v[132:135]
EX v2,v2
MM v[8:11],a[200:203],v[196:199],v[8:11]
TR a[144:145],v232 offset:49664
CV v0,v0,v1
MM v[8:11],a[196:199],v[180:183],v[8:11]
EX v3,v3
MM v[8:11],a[192:195],v[164:167],v[8:11]
TR a[146:147],v232 offset:53760
CV v1,v2,v3
MM v[16:19],a[192:195],v[168:171],v[136:139]
v_max3_f32 v112, |v112|, |v113|, |v114|
v_max_f32 v112, v112, |v115|
MM v[16:19],a[196:199],v[184:187],v[16:19]
v_bfe_u32 v112, v112, 23, 8
v_add_u32 v112, 7, v112
MM v[16:19],a[200:203],v[200:203],v[16:19]
v_min_u32 v112, 240, v112
v_lshl_or_b32 v241, v112, 16, v241
MM v[16:19],a[204:207],v[216:219],v[16:19]
v_lshl_or_b32 v112, v112, 1, 1
v_lshlrev_b32 v112, 22, v112
EX v4,v4
MM v[20:23],a[220:223],v[216:219],v[136:139]
AW a116,v112
AW a117,v112
MM v[20:23],a[216:219],v[200:203],v[20:23]
AW a118,v112
AW a119,v112
EX v5,v5
MM v[20:23],a[212:215],v[184:187],v[20:23]
v_max3_f32 v116, |v116|, |v117|, |v118|
v_max_f32 v116, v116, |v119|
MM v[20:23],a[208:211],v[168:171],v[20:23]
v_bfe_u32 v116, v116, 23, 8
v_add_u32 v116, 7, v116
EX v6,v6
MM v[28:31],a[208:211],v[172:175],v[140:143]
v_min_u32 v116, 240, v116
v_lshl_or_b32 v241, v116, 24, v241
MM v[28:31],a[212:215],v[188:191],v[28:31]
TR a[148:149],v232 offset:57856
v_lshl_or_b32 v116, v116, 1, 1
v_lshlrev_b32 v116, 22, v116
EX v7,v7
MM v[28:31],a[216:219],v[204:207],v[28:31]
AW a124,v116
AW a125,v116
MM v[28:31],a[220:223],v[220:223],v[28:31]
TR a[150:151],v232 offset:61952
AW a126,v116
AW a127,v116
EX v8,v8
MM v[24:27],a[204:207],v[220:223],v[140:143]
TR a[152:153],v233 offset:49664
TR a[154:155],v233 offset:53760
MM v[24:27],a[200:203],v[204:207],v[24:27]
EX v9,v9
MM v[24:27],a[196:199],v[188:191],v[24:27]
EX v10,v10
MM v[24:27],a[192:195],v[172:175],v[24:27]
TR a[156:157],v233 offset:57856
CV v2,v4,v5
MM v[32:35],a[224:227],v[160:163],v[128:131]
EX v11,v11
MM v[32:35],a[228:231],v[176:179],v[32:35]
TR a[158:159],v233 offset:61952
CV v3,v6,v7
MM v[32:35],a[232:235],v[192:195],v[32:35]
EX v12,v12
MM v[32:35],a[236:239],v[208:211],v[32:35]
TR a[160:161],v232 offset:50176
TR a[162:163],v232 offset:54272
MM v[36:39],a[252:255],v[208:211],v[128:131]
EX v13,v13
LPG6_%=:
s_cbranch_execz LPG10_%=
MM v[36:39],a[248:251],v[192:195],v[36:39]
EX v14,v14
MM v[36:39],a[244:247],v[176:179],v[36:39]
TR a[164:165],v232 offset:58368
CV v8,v8,v9
MM v[36:39],a[240:243],v[160:163],v[36:39]
EX v15,v15
MM v[44:47],a[240:243],v[164:167],v[132:135]
s_lshl_b32 s76, s50, 1
TR a[166:167],v232 offset:62464
CV v9,v10,v11
MM v[44:47],a[244:247],v[180:183],v[44:47]
s_bfe_u32 s77, s49, 65538
EX v16,v16
MM v[44:47],a[248:251],v[196:199],v[44:47]
s_add_u32 s76, s76, s77
TR a[168:169],v233 offset:50176
TR a[170:171],v233 offset:54272
MM v[44:47],a[252:255],v[212:215],v[44:47]
s_lshl_b32 s76, s76, 2
EX v17,v17
MM v[40:43],a[236:239],v[212:215],v[132:135]
s_add_u32 s76, s76, s84
TR a[172:173],v233 offset:58368
CV v10,v12,v13
MM v[40:43],a[232:235],v[196:199],v[40:43]
s_lshl_b32 s76, s76, 2
EX v18,v18
MM v[40:43],a[228:231],v[180:183],v[40:43]
s_lshr_b32 s77, s49, 3
EX v19,v19
MM v[40:43],a[224:227],v[164:167],v[40:43]
s_add_u32 s76, s76, s77
TR a[174:175],v233 offset:62464
CV v11,v14,v15
MM v[48:51],a[224:227],v[168:171],v[136:139]
s_mul_i32 s95, s76, 33792
EX v20,v20
MM v[48:51],a[228:231],v[184:187],v[48:51]
s_lshl_b32 s76, %[swave], 13
TR a[176:177],v232 offset:50688
TR a[178:179],v232 offset:54784
MM v[48:51],a[232:235],v[200:203],v[48:51]
s_add_u32 s95, s95, s76
EX v21,v21
MM v[48:51],a[236:239],v[216:219],v[48:51]
s_add_u32 s95, s95, 22708224
v_mov_b32 v240, -1
CV v16,v16,v17
MM v[52:55],a[252:255],v[216:219],v[136:139]
s_mov_b32 s67, s95
EX v22,v22
MM v[52:55],a[248:251],v[200:203],v[52:55]
s_add_u32 s87, s95, 6144
buffer_store_dword v240, v248, s[20:23], s87 offen offset:12 sc1
MM v[52:55],a[244:247],v[184:187],v[52:55]
TR a[180:181],v232 offset:58880
TR a[182:183],v232 offset:62976
MM v[52:55],a[240:243],v[168:171],v[52:55]
EX v23,v23
MM v[60:63],a[240:243],v[172:175],v[140:143]
EX v24,v24
MM v[60:63],a[244:247],v[188:191],v[60:63]
TR a[184:185],v233 offset:50688
CV v17,v18,v19
MM v[60:63],a[248:251],v[204:207],v[60:63]
TR a[186:187],v233 offset:54784
TR a[188:189],v233 offset:58880
TR a[190:191],v233 offset:62976
EX v25,v25
MM v[60:63],a[252:255],v[220:223],v[60:63]
s_add_u32 s87, s95, 7168
buffer_store_dword v240, v248, s[20:23], s87 offen offset:12 sc1
MM v[56:59],a[236:239],v[220:223],v[140:143]
EX v26,v26
MM v[56:59],a[232:235],v[204:207],v[56:59]
EX v27,v27
MM v[56:59],a[228:231],v[188:191],v[56:59]
EX v28,v28
CV v18,v20,v21
MM v[56:59],a[224:227],v[172:175],v[56:59]
s_waitcnt lgkmcnt(0)
s_barrier
MM a[0:3],a[128:131],v[64:67],a[0:3]
s_add_u32 m0, s65, 49152
CV v19,v22,v23
CV v24,v24,v25
MM a[0:3],a[132:135],v[68:71],a[0:3]
EX v29,v29
MM a[8:11],a[132:135],v[76:79],a[8:11]
EX v30,v30
MM a[8:11],a[128:131],v[72:75],a[8:11]
CV v25,v26,v27
CV v26,v28,v29
MM a[16:19],a[128:131],v[80:83],a[116:119]
EX v31,v31
MM a[16:19],a[132:135],v[84:87],a[16:19]
EX v32,v32
MM a[24:27],a[132:135],v[92:95],a[124:127]
RD a[192:195],v224 offset:16384
CV v27,v30,v31
MM a[24:27],a[128:131],v[88:91],a[24:27]
EX v33,v33
MM a[28:31],a[136:139],v[88:91],a[124:127]
buffer_load_dwordx4 v228, s[8:11], s64 offen offset:0 lds
MM a[28:31],a[140:143],v[92:95],a[28:31]
EX v34,v34
MM a[20:23],a[140:143],v[84:87],a[116:119]
RD a[196:199],v225 offset:16384
CV v4,v32,v33
MM a[20:23],a[136:139],v[80:83],a[20:23]
EX v35,v35
MM a[12:15],a[136:139],v[72:75],a[12:15]
EX v36,v36
MM a[12:15],a[140:143],v[76:79],a[12:15]
RD a[200:203],v226 offset:16384
CV v5,v34,v35
MM a[4:7],a[140:143],v[68:71],a[4:7]
EX v37,v37
MM a[4:7],a[136:139],v[64:67],a[4:7]
buffer_load_dwordx4 v229, s[8:11], s64 offen offset:1024 lds
MM a[32:35],a[144:147],v[64:67],a[32:35]
EX v38,v38
MM a[32:35],a[148:151],v[68:71],a[32:35]
RD a[204:207],v227 offset:16384
CV v6,v36,v37
MM a[40:43],a[148:151],v[76:79],a[40:43]
EX v39,v39
MM a[40:43],a[144:147],v[72:75],a[40:43]
RD a[208:211],v224 offset:20480
CV v7,v38,v39
MM a[48:51],a[144:147],v[80:83],a[116:119]
EX v40,v40
MM a[48:51],a[148:151],v[84:87],a[48:51]
EX v41,v41
MM a[56:59],a[148:151],v[92:95],a[124:127]
buffer_load_dwordx4 v230, s[8:11], s64 offen offset:2048 lds
MM a[56:59],a[144:147],v[88:91],a[56:59]
EX v42,v42
MM a[60:63],a[152:155],v[88:91],a[124:127]
RD a[212:215],v225 offset:20480
CV v12,v40,v41
MM a[60:63],a[156:159],v[92:95],a[60:63]
EX v43,v43
MM a[52:55],a[156:159],v[84:87],a[116:119]
RD a[216:219],v226 offset:20480
CV v13,v42,v43
MM a[52:55],a[152:155],v[80:83],a[52:55]
EX v44,v44
MM a[44:47],a[152:155],v[72:75],a[44:47]
EX v45,v45
MM a[44:47],a[156:159],v[76:79],a[44:47]
buffer_load_dwordx4 v231, s[8:11], s64 offen offset:3072 lds
MM a[36:39],a[156:159],v[68:71],a[36:39]
s_add_u32 m0, s65, 114688
RD a[220:223],v227 offset:20480
CV v14,v44,v45
MM a[36:39],a[152:155],v[64:67],a[36:39]
EX v46,v46
MM a[64:67],a[160:163],v[64:67],a[64:67]
EX v47,v47
MM a[64:67],a[164:167],v[68:71],a[64:67]
RD a[224:227],v224 offset:24576
CV v15,v46,v47
MM a[72:75],a[164:167],v[76:79],a[72:75]
EX v48,v48
MM a[72:75],a[160:163],v[72:75],a[72:75]
buffer_load_dwordx4 v235, s[12:15], s81 offen offset:0 lds
MM a[80:83],a[160:163],v[80:83],a[116:119]
EX v49,v49
MM a[80:83],a[164:167],v[84:87],a[80:83]
RD a[228:231],v225 offset:24576
CV v20,v48,v49
MM a[88:91],a[164:167],v[92:95],a[124:127]
EX v50,v50
MM a[88:91],a[160:163],v[88:91],a[88:91]
EX v51,v51
MM a[92:95],a[168:171],v[88:91],a[124:127]
RD a[232:235],v226 offset:24576
CV v21,v50,v51
MM a[92:95],a[172:175],v[92:95],a[92:95]
EX v52,v52
MM a[84:87],a[172:175],v[84:87],a[116:119]
buffer_load_dwordx4 v235, s[12:15], s64 offen offset:1024 lds
MM a[84:87],a[168:171],v[80:83],a[84:87]
EX v53,v53
MM a[76:79],a[168:171],v[72:75],a[76:79]
RD a[236:239],v227 offset:24576
CV v22,v52,v53
MM a[76:79],a[172:175],v[76:79],a[76:79]
EX v54,v54
MM a[68:71],a[172:175],v[68:71],a[68:71]
EX v55,v55
MM a[68:71],a[168:171],v[64:67],a[68:71]
RD a[240:243],v224 offset:28672
CV v23,v54,v55
MM a[96:99],a[176:179],v[64:67],a[96:99]
EX v56,v56
MM a[96:99],a[180:183],v[68:71],a[96:99]
)ASM"
R"ASM(
buffer_load_dwordx4 v235, s[12:15], s81 offen offset:2048 lds
MM a[104:107],a[180:183],v[76:79],a[104:107]
EX v57,v57
MM a[104:107],a[176:179],v[72:75],a[104:107]
RD a[244:247],v225 offset:28672
CV v28,v56,v57
MM a[112:115],a[176:179],v[80:83],a[116:119]
EX v58,v58
MM a[112:115],a[180:183],v[84:87],a[112:115]
RD a[248:251],v226 offset:28672
RD a[252:255],v227 offset:28672
MM a[120:123],a[180:183],v[92:95],a[124:127]
EX v59,v59
MM a[120:123],a[176:179],v[88:91],a[120:123]
EX v60,v60
MM a[124:127],a[184:187],v[88:91],a[124:127]
v_add_f32 v244, v144, v145
v_add_f32 v245, v148, v149
MM a[124:127],a[188:191],v[92:95],a[124:127]
v_add_f32 v246, v152, v153
v_add_f32 v247, v156, v157
MM a[116:119],a[188:191],v[84:87],a[116:119]
EX v61,v61
MM a[116:119],a[184:187],v[80:83],a[116:119]
buffer_load_dwordx4 v235, s[12:15], s64 offen offset:3072 lds
MM a[108:111],a[184:187],v[72:75],a[108:111]
EX v62,v62
MM a[108:111],a[188:191],v[76:79],a[108:111]
EX v63,v63
MM a[100:103],a[188:191],v[68:71],a[100:103]
CV v29,v58,v59
CV v30,v60,v61
MM a[100:103],a[184:187],v[64:67],a[100:103]
CV v31,v62,v63
v_smfmac_f32_16x16x64_bf16 v[144:147], v[236:239], v[0:7], v234
v_smfmac_f32_16x16x64_bf16 v[148:151], v[236:239], v[8:15], v234
v_smfmac_f32_16x16x64_bf16 v[152:155], v[236:239], v[16:23], v234
v_smfmac_f32_16x16x64_bf16 v[156:159], v[236:239], v[24:31], v234
s_waitcnt lgkmcnt(0)
MM v[64:67],a[192:195],v[160:163],v[128:131]
s_add_u32 s64, s64, 16384
TR a[128:129],v232 offset:0
s_add_u32 s81, s64, 896
MM v[64:67],a[196:199],v[176:179],v[64:67]
TR a[130:131],v232 offset:4096
v_max3_f32 v249, v144, v145, v148
MM v[64:67],a[200:203],v[192:195],v[64:67]
TR a[132:133],v232 offset:8192
v_max3_f32 v249, v249, v149, v152
MM v[64:67],a[204:207],v[208:211],v[64:67]
TR a[134:135],v232 offset:12288
v_max3_f32 v249, v249, v153, v156
MM v[68:71],a[220:223],v[208:211],v[128:131]
TR a[136:137],v233 offset:0
v_max_f32 v249, v249, v157
MM v[68:71],a[216:219],v[192:195],v[68:71]
TR a[138:139],v233 offset:4096
v_cmp_lt_f32 vcc, s71, v249
MM v[68:71],a[212:215],v[176:179],v[68:71]
TR a[140:141],v233 offset:8192
TR a[142:143],v233 offset:12288
MM v[68:71],a[208:211],v[160:163],v[68:71]
s_cbranch_vccnz LSITE4_%=
LBACK4_%=:
MM v[76:79],a[208:211],v[164:167],v[132:135]
s_cmp_lg_u32 %[sb], 0
s_cbranch_scc1 LEP_%=
v_mov_b32 v250, s69
s_mov_b32 s76, 32768
s_mov_b64 exec, 1
buffer_store_dword v250, off, s[20:23], s76 sc1
s_mov_b64 exec, -1
s_nop 1
LEP_%=:
MM v[76:79],a[212:215],v[180:183],v[76:79]
EX v64,v64
MM v[76:79],a[216:219],v[196:199],v[76:79]
EX v65,v65
MM v[76:79],a[220:223],v[212:215],v[76:79]
TR a[144:145],v232 offset:512
CV v64,v64,v65
MM v[72:75],a[204:207],v[212:215],v[132:135]
TR a[146:147],v232 offset:4608
v_lshrrev_b32 v34, 1, v248
MM v[72:75],a[200:203],v[196:199],v[72:75]
s_mov_b32 s90, 86016
buffer_load_dwordx2 v[32:33], v34, s[20:23], s90 offen offset:0 sc1
MM v[72:75],a[196:199],v[180:183],v[72:75]
EX v66,v66
MM v[72:75],a[192:195],v[164:167],v[72:75]
EX v67,v67
MM v[80:83],a[192:195],v[168:171],v[136:139]
EX v68,v68
MM v[80:83],a[196:199],v[184:187],v[80:83]
TR a[148:149],v232 offset:8704
CV v65,v66,v67
MM v[80:83],a[200:203],v[200:203],v[80:83]
EX v69,v69
MM v[80:83],a[204:207],v[216:219],v[80:83]
TR a[150:151],v232 offset:12800
CV v66,v68,v69
MM v[84:87],a[220:223],v[216:219],v[136:139]
EX v70,v70
MM v[84:87],a[216:219],v[200:203],v[84:87]
TR a[152:153],v233 offset:512
TR a[154:155],v233 offset:4608
MM v[84:87],a[212:215],v[184:187],v[84:87]
EX v71,v71
MM v[84:87],a[208:211],v[168:171],v[84:87]
TR a[156:157],v233 offset:8704
CV v67,v70,v71
MM v[92:95],a[208:211],v[172:175],v[140:143]
EX v72,v72
MM v[92:95],a[212:215],v[188:191],v[92:95]
EX v73,v73
MM v[92:95],a[216:219],v[204:207],v[92:95]
TR a[158:159],v233 offset:12800
CV v72,v72,v73
MM v[92:95],a[220:223],v[220:223],v[92:95]
EX v74,v74
MM v[88:91],a[204:207],v[220:223],v[140:143]
TR a[160:161],v232 offset:1024
TR a[162:163],v232 offset:5120
MM v[88:91],a[200:203],v[204:207],v[88:91]
EX v75,v75
MM v[88:91],a[196:199],v[188:191],v[88:91]
TR a[164:165],v232 offset:9216
CV v73,v74,v75
MM v[88:91],a[192:195],v[172:175],v[88:91]
EX v76,v76
MM v[96:99],a[224:227],v[160:163],v[128:131]
EX v77,v77
MM v[96:99],a[228:231],v[176:179],v[96:99]
TR a[166:167],v232 offset:13312
CV v74,v76,v77
MM v[96:99],a[232:235],v[192:195],v[96:99]
EX v78,v78
MM v[96:99],a[236:239],v[208:211],v[96:99]
TR a[168:169],v233 offset:1024
TR a[170:171],v233 offset:5120
MM v[100:103],a[252:255],v[208:211],v[128:131]
EX v79,v79
MM v[100:103],a[248:251],v[192:195],v[100:103]
TR a[172:173],v233 offset:9216
CV v75,v78,v79
MM v[100:103],a[244:247],v[176:179],v[100:103]
EX v80,v80
MM v[100:103],a[240:243],v[160:163],v[100:103]
EX v81,v81
MM v[108:111],a[240:243],v[164:167],v[132:135]
TR a[174:175],v233 offset:13312
CV v80,v80,v81
MM v[108:111],a[244:247],v[180:183],v[108:111]
EX v82,v82
MM v[108:111],a[248:251],v[196:199],v[108:111]
TR a[176:177],v232 offset:1536
TR a[178:179],v232 offset:5632
MM v[108:111],a[252:255],v[212:215],v[108:111]
EX v83,v83
MM v[104:107],a[236:239],v[212:215],v[132:135]
TR a[180:181],v232 offset:9728
CV v81,v82,v83
MM v[104:107],a[232:235],v[196:199],v[104:107]
EX v84,v84
MM v[104:107],a[228:231],v[180:183],v[104:107]
TR a[182:183],v232 offset:13824
TR a[184:185],v233 offset:1536
MM v[104:107],a[224:227],v[164:167],v[104:107]
EX v85,v85
MM v[112:115],a[224:227],v[168:171],v[136:139]
TR a[186:187],v233 offset:5632
CV v82,v84,v85
MM v[112:115],a[228:231],v[184:187],v[112:115]
EX v86,v86
MM v[112:115],a[232:235],v[200:203],v[112:115]
EX v87,v87
MM v[112:115],a[236:239],v[216:219],v[112:115]
TR a[188:189],v233 offset:9728
CV v83,v86,v87
MM v[116:119],a[252:255],v[216:219],v[136:139]
EX v88,v88
MM v[116:119],a[248:251],v[200:203],v[116:119]
EX v89,v89
MM v[116:119],a[244:247],v[184:187],v[116:119]
TR a[190:191],v233 offset:13824
CV v88,v88,v89
MM v[116:119],a[240:243],v[168:171],v[116:119]
EX v90,v90
MM v[124:127],a[240:243],v[172:175],v[140:143]
EX v91,v91
MM v[124:127],a[244:247],v[188:191],v[124:127]
EX v92,v92
MM v[124:127],a[248:251],v[204:207],v[124:127]
EX v93,v93
MM v[124:127],a[252:255],v[220:223],v[124:127]
EX v94,v94
MM v[120:123],a[236:239],v[220:223],v[140:143]
EX v95,v95
MM v[120:123],a[232:235],v[204:207],v[120:123]
EX v96,v96
MM v[120:123],a[228:231],v[188:191],v[120:123]
EX v97,v97
MM v[120:123],a[224:227],v[172:175],v[120:123]
s_waitcnt vmcnt(11) lgkmcnt(0)
s_barrier
MM a[0:3],a[128:131],v[0:3],a[0:3]
s_add_u32 m0, s65, 0
CV v89,v90,v91
CV v90,v92,v93
MM a[0:3],a[132:135],v[4:7],a[0:3]
RD a[192:195],v224 offset:32768
CV v91,v94,v95
MM a[8:11],a[132:135],v[12:15],a[8:11]
buffer_load_dwordx4 v228, s[8:11], s64 offen offset:0 lds
MM a[8:11],a[128:131],v[8:11],a[8:11]
RD a[196:199],v225 offset:32768
CV v68,v96,v97
MM a[16:19],a[128:131],v[16:19],a[16:19]
RD a[200:203],v226 offset:32768
RD a[204:207],v227 offset:32768
MM a[16:19],a[132:135],v[20:23],a[16:19]
buffer_load_dwordx4 v229, s[8:11], s64 offen offset:1024 lds
MM a[24:27],a[132:135],v[28:31],a[24:27]
EX v98,v98
MM a[24:27],a[128:131],v[24:27],a[24:27]
RD a[208:211],v224 offset:36864
RD a[212:215],v225 offset:36864
MM a[28:31],a[136:139],v[24:27],a[28:31]
EX v99,v99
MM a[28:31],a[140:143],v[28:31],a[28:31]
RD a[216:219],v226 offset:36864
CV v69,v98,v99
MM a[20:23],a[140:143],v[20:23],a[20:23]
EX v100,v100
MM a[20:23],a[136:139],v[16:19],a[20:23]
EX v101,v101
MM a[12:15],a[136:139],v[8:11],a[12:15]
s_waitcnt vmcnt(2)
v_cmp_ne_u32 vcc, s69, v33
s_cbranch_vccnz LRRK_%=
LRRKb_%=:
MM a[12:15],a[140:143],v[12:15],a[12:15]
v_add_u32_dpp v32, v32, v32 row_ror:8 row_mask:0xf bank_mask:0xf
CV v70,v100,v101
MM a[4:7],a[140:143],v[4:7],a[4:7]
s_and_b32 s76, %[sb], 7
RD a[220:223],v227 offset:36864
v_mov_b32 v35, v32
MM a[4:7],a[136:139],v[0:3],a[4:7]
s_nop 0
v_permlane16_swap_b32 v32, v35
v_add_u32 v32, v32, v35
MM a[32:35],a[144:147],v[0:3],a[32:35]
RD a[224:227],v224 offset:40960
v_mov_b32 v35, v32
MM a[32:35],a[148:151],v[4:7],a[32:35]
s_nop 0
v_permlane32_swap_b32 v32, v35
v_add_u32 v32, v32, v35
MM a[40:43],a[148:151],v[12:15],a[40:43]
RD a[228:231],v225 offset:40960
v_readlane_b32 s77, v32, s76
MM a[40:43],a[144:147],v[8:11],a[40:43]
s_bfm_b32 s76, s76, 0
EX v102,v102
MM a[48:51],a[144:147],v[16:19],a[48:51]
v_cmp_gt_u32 s[60:61], s77, v32
v_cmp_eq_u32 s[62:63], s77, v32
MM a[48:51],a[148:151],v[20:23],a[48:51]
s_and_b32 s62, s62, s76
EX v103,v103
MM a[56:59],a[148:151],v[28:31],a[56:59]
s_or_b32 s60, s60, s62
EX v104,v104
MM a[56:59],a[144:147],v[24:27],a[56:59]
s_and_b32 s60, s60, 255
EX v105,v105
MM a[60:63],a[152:155],v[24:27],a[60:63]
s_bcnt1_i32_b32 s51, s60
s_cmp_lt_u32 s51, 4
s_cselect_b32 s82, 3, 99
MM a[60:63],a[156:159],v[28:31],a[60:63]
s_lshr_b32 s16, %[sb], 3
EX v106,v106
MM a[52:55],a[156:159],v[20:23],a[52:55]
s_lshl_b32 s76, s51, 5
CV v71,v102,v103
CV v76,v104,v105
MM a[52:55],a[152:155],v[16:19],a[52:55]
s_add_u32 s16, s16, s76
EX v107,v107
MM a[44:47],a[152:155],v[8:11],a[44:47]
buffer_load_dwordx4 v230, s[8:11], s64 offen offset:2048 lds
MM a[44:47],a[156:159],v[12:15],a[44:47]
EX v108,v108
MM a[36:39],a[156:159],v[4:7],a[36:39]
EX v109,v109
MM a[36:39],a[152:155],v[0:3],a[36:39]
CV v77,v106,v107
CV v78,v108,v109
MM a[64:67],a[160:163],v[0:3],a[64:67]
EX v110,v110
MM a[64:67],a[164:167],v[4:7],a[64:67]
EX v111,v111
MM a[72:75],a[164:167],v[12:15],a[72:75]
buffer_load_dwordx4 v231, s[8:11], s64 offen offset:3072 lds
MM a[72:75],a[160:163],v[8:11],a[72:75]
s_add_u32 m0, s65, 65536
EX v112,v112
MM a[80:83],a[160:163],v[16:19],a[80:83]
RD a[232:235],v226 offset:40960
CV v79,v110,v111
MM a[80:83],a[164:167],v[20:23],a[80:83]
EX v113,v113
MM a[88:91],a[164:167],v[28:31],a[88:91]
EX v114,v114
MM a[88:91],a[160:163],v[24:27],a[88:91]
buffer_load_dwordx4 v235, s[12:15], s81 offen offset:0 lds
MM a[92:95],a[168:171],v[24:27],a[92:95]
EX v115,v115
MM a[92:95],a[172:175],v[28:31],a[92:95]
CV v84,v112,v113
CV v85,v114,v115
MM a[84:87],a[172:175],v[20:23],a[84:87]
EX v116,v116
MM a[84:87],a[168:171],v[16:19],a[84:87]
EX v117,v117
MM a[76:79],a[168:171],v[8:11],a[76:79]
RD a[236:239],v227 offset:40960
CV v86,v116,v117
MM a[76:79],a[172:175],v[12:15],a[76:79]
EX v118,v118
MM a[68:71],a[172:175],v[4:7],a[68:71]
buffer_load_dwordx4 v235, s[12:15], s64 offen offset:1024 lds
MM a[68:71],a[168:171],v[0:3],a[68:71]
EX v119,v119
MM a[96:99],a[176:179],v[0:3],a[96:99]
RD a[240:243],v224 offset:45056
CV v87,v118,v119
MM a[96:99],a[180:183],v[4:7],a[96:99]
EX v120,v120
MM a[104:107],a[180:183],v[12:15],a[104:107]
EX v121,v121
MM a[104:107],a[176:179],v[8:11],a[104:107]
RD a[244:247],v225 offset:45056
CV v92,v120,v121
MM a[112:115],a[176:179],v[16:19],a[112:115]
buffer_load_dwordx4 v235, s[12:15], s81 offen offset:2048 lds
MM a[112:115],a[180:183],v[20:23],a[112:115]
EX v122,v122
MM a[120:123],a[180:183],v[28:31],a[120:123]
EX v123,v123
MM a[120:123],a[176:179],v[24:27],a[120:123]
RD a[248:251],v226 offset:45056
CV v93,v122,v123
MM a[124:127],a[184:187],v[24:27],a[124:127]
v_add_f32 v244, v144, v145
v_add_f32 v245, v148, v149
MM a[124:127],a[188:191],v[28:31],a[124:127]
v_add_f32 v246, v152, v153
v_add_f32 v247, v156, v157
MM a[116:119],a[188:191],v[20:23],a[116:119]
EX v124,v124
MM a[116:119],a[184:187],v[16:19],a[116:119]
EX v125,v125
MM a[108:111],a[184:187],v[8:11],a[108:111]
EX v126,v126
MM a[108:111],a[188:191],v[12:15],a[108:111]
RD a[252:255],v227 offset:45056
CV v94,v124,v125
MM a[100:103],a[188:191],v[4:7],a[100:103]
EX v127,v127
MM a[100:103],a[184:187],v[0:3],a[100:103]
buffer_load_dwordx4 v235, s[12:15], s64 offen offset:3072 lds
v_smfmac_f32_16x16x64_bf16 v[144:147], v[236:239], v[64:71], v234
CV v95,v126,v127
v_smfmac_f32_16x16x64_bf16 v[148:151], v[236:239], v[72:79], v234
v_smfmac_f32_16x16x64_bf16 v[152:155], v[236:239], v[80:87], v234
v_smfmac_f32_16x16x64_bf16 v[156:159], v[236:239], v[88:95], v234
s_waitcnt lgkmcnt(0)
s_cmp_lt_u32 s51, 4
s_cbranch_scc1 LLAR_%=
s_mov_b32 s70, 6
s_branch LLAP_%=
s_nop 0
s_nop 0
s_nop 0
s_nop 0
s_nop 0
s_nop 0
s_nop 0
s_nop 0
s_nop 0
s_nop 0
s_nop 0
s_nop 0
s_nop 0
)ASM"
R"ASM(
s_nop 0
s_nop 0
s_nop 0
s_nop 0
s_nop 0
s_nop 0
s_nop 0
s_nop 0
s_nop 0
s_nop 0
s_nop 0
s_nop 0
s_nop 0
s_nop 0
s_nop 0
s_nop 0
s_nop 0
s_nop 0
s_nop 0
s_nop 0
s_nop 0
s_nop 0
s_nop 0
s_nop 0
LPG7_%=:
s_cbranch_execz LPG11_%=
s_nop 0
s_nop 0
s_nop 0
s_nop 0
s_nop 0
s_nop 0
s_nop 0
s_nop 0
s_nop 0
s_nop 0
s_nop 0
s_nop 0
s_nop 0
s_nop 0
s_nop 0
s_nop 0
s_nop 0
s_nop 0
s_nop 0
s_nop 0
s_nop 0
s_nop 0
s_nop 0
s_nop 0
s_nop 0
s_nop 0
s_nop 0
s_nop 0
s_nop 0
s_nop 0
s_nop 0
LLAP_%=:
MM v[0:3],a[192:195],v[160:163],v[128:131]
s_add_u32 s64, s64, 16384
TR a[128:129],v232 offset:16384
s_add_u32 s81, s64, 896
MM v[0:3],a[196:199],v[176:179],v[0:3]
TR a[130:131],v232 offset:20480
v_max3_f32 v249, v144, v145, v148
MM v[0:3],a[200:203],v[192:195],v[0:3]
TR a[132:133],v232 offset:24576
v_max3_f32 v249, v249, v149, v152
MM v[0:3],a[204:207],v[208:211],v[0:3]
TR a[134:135],v232 offset:28672
v_max3_f32 v249, v249, v153, v156
MM v[4:7],a[220:223],v[208:211],v[128:131]
TR a[136:137],v233 offset:16384
v_max_f32 v249, v249, v157
MM v[4:7],a[216:219],v[192:195],v[4:7]
TR a[138:139],v233 offset:20480
v_cmp_lt_f32 vcc, s71, v249
MM v[4:7],a[212:215],v[176:179],v[4:7]
TR a[140:141],v233 offset:24576
TR a[142:143],v233 offset:28672
MM v[4:7],a[208:211],v[160:163],v[4:7]
s_cbranch_vccnz LSITE5_%=
LBACK5_%=:
MM v[12:15],a[208:211],v[164:167],v[132:135]
EX v0,v0
MM v[12:15],a[212:215],v[180:183],v[12:15]
EX v1,v1
MM v[12:15],a[216:219],v[196:199],v[12:15]
TR a[144:145],v232 offset:16896
CV v0,v0,v1
MM v[12:15],a[220:223],v[212:215],v[12:15]
EX v2,v2
MM v[8:11],a[204:207],v[212:215],v[132:135]
EX v3,v3
MM v[8:11],a[200:203],v[196:199],v[8:11]
TR a[146:147],v232 offset:20992
CV v1,v2,v3
MM v[8:11],a[196:199],v[180:183],v[8:11]
EX v4,v4
MM v[8:11],a[192:195],v[164:167],v[8:11]
TR a[148:149],v232 offset:25088
TR a[150:151],v232 offset:29184
MM v[16:19],a[192:195],v[168:171],v[136:139]
EX v5,v5
MM v[16:19],a[196:199],v[184:187],v[16:19]
TR a[152:153],v233 offset:16896
CV v2,v4,v5
MM v[16:19],a[200:203],v[200:203],v[16:19]
EX v6,v6
MM v[16:19],a[204:207],v[216:219],v[16:19]
TR a[154:155],v233 offset:20992
TR a[156:157],v233 offset:25088
MM v[20:23],a[220:223],v[216:219],v[136:139]
EX v7,v7
MM v[20:23],a[216:219],v[200:203],v[20:23]
TR a[158:159],v233 offset:29184
CV v3,v6,v7
MM v[20:23],a[212:215],v[184:187],v[20:23]
EX v8,v8
MM v[20:23],a[208:211],v[168:171],v[20:23]
EX v9,v9
MM v[28:31],a[208:211],v[172:175],v[140:143]
TR a[160:161],v232 offset:17408
CV v8,v8,v9
MM v[28:31],a[212:215],v[188:191],v[28:31]
EX v10,v10
MM v[28:31],a[216:219],v[204:207],v[28:31]
TR a[162:163],v232 offset:21504
TR a[164:165],v232 offset:25600
MM v[28:31],a[220:223],v[220:223],v[28:31]
EX v11,v11
MM v[24:27],a[204:207],v[220:223],v[140:143]
TR a[166:167],v232 offset:29696
CV v9,v10,v11
MM v[24:27],a[200:203],v[204:207],v[24:27]
EX v12,v12
MM v[24:27],a[196:199],v[188:191],v[24:27]
EX v13,v13
MM v[24:27],a[192:195],v[172:175],v[24:27]
TR a[168:169],v233 offset:17408
CV v10,v12,v13
MM v[32:35],a[224:227],v[160:163],v[128:131]
EX v14,v14
MM v[32:35],a[228:231],v[176:179],v[32:35]
TR a[170:171],v233 offset:21504
TR a[172:173],v233 offset:25600
MM v[32:35],a[232:235],v[192:195],v[32:35]
EX v15,v15
MM v[32:35],a[236:239],v[208:211],v[32:35]
TR a[174:175],v233 offset:29696
CV v11,v14,v15
MM v[36:39],a[252:255],v[208:211],v[128:131]
EX v16,v16
MM v[36:39],a[248:251],v[192:195],v[36:39]
EX v17,v17
MM v[36:39],a[244:247],v[176:179],v[36:39]
TR a[176:177],v232 offset:17920
CV v16,v16,v17
MM v[36:39],a[240:243],v[160:163],v[36:39]
EX v18,v18
MM v[44:47],a[240:243],v[164:167],v[132:135]
TR a[178:179],v232 offset:22016
TR a[180:181],v232 offset:26112
MM v[44:47],a[244:247],v[180:183],v[44:47]
EX v19,v19
MM v[44:47],a[248:251],v[196:199],v[44:47]
TR a[182:183],v232 offset:30208
CV v17,v18,v19
MM v[44:47],a[252:255],v[212:215],v[44:47]
EX v20,v20
MM v[40:43],a[236:239],v[212:215],v[132:135]
TR a[184:185],v233 offset:17920
TR a[186:187],v233 offset:22016
MM v[40:43],a[232:235],v[196:199],v[40:43]
EX v21,v21
MM v[40:43],a[228:231],v[180:183],v[40:43]
TR a[188:189],v233 offset:26112
CV v18,v20,v21
MM v[40:43],a[224:227],v[164:167],v[40:43]
EX v22,v22
MM v[48:51],a[224:227],v[168:171],v[136:139]
EX v23,v23
MM v[48:51],a[228:231],v[184:187],v[48:51]
TR a[190:191],v233 offset:30208
CV v19,v22,v23
MM v[48:51],a[232:235],v[200:203],v[48:51]
EX v24,v24
MM v[48:51],a[236:239],v[216:219],v[48:51]
EX v25,v25
MM v[52:55],a[252:255],v[216:219],v[136:139]
EX v26,v26
MM v[52:55],a[248:251],v[200:203],v[52:55]
EX v27,v27
MM v[52:55],a[244:247],v[184:187],v[52:55]
EX v28,v28
MM v[52:55],a[240:243],v[168:171],v[52:55]
EX v29,v29
MM v[60:63],a[240:243],v[172:175],v[140:143]
EX v30,v30
MM v[60:63],a[244:247],v[188:191],v[60:63]
EX v31,v31
MM v[60:63],a[248:251],v[204:207],v[60:63]
EX v32,v32
MM v[60:63],a[252:255],v[220:223],v[60:63]
EX v33,v33
MM v[56:59],a[236:239],v[220:223],v[140:143]
EX v34,v34
MM v[56:59],a[232:235],v[204:207],v[56:59]
EX v35,v35
MM v[56:59],a[228:231],v[188:191],v[56:59]
EX v36,v36
MM v[56:59],a[224:227],v[172:175],v[56:59]
s_waitcnt vmcnt(12) lgkmcnt(0)
s_barrier
MM a[0:3],a[128:131],v[64:67],a[0:3]
s_add_u32 m0, s65, 16384
CV v24,v24,v25
CV v25,v26,v27
MM a[0:3],a[132:135],v[68:71],a[0:3]
RD a[192:195],v224 offset:49152
CV v26,v28,v29
MM a[8:11],a[132:135],v[76:79],a[8:11]
RD a[196:199],v225 offset:49152
CV v27,v30,v31
MM a[8:11],a[128:131],v[72:75],a[8:11]
buffer_load_dwordx4 v228, s[8:11], s64 offen offset:0 lds
MM a[16:19],a[128:131],v[80:83],a[16:19]
RD a[200:203],v226 offset:49152
CV v4,v32,v33
MM a[16:19],a[132:135],v[84:87],a[16:19]
RD a[204:207],v227 offset:49152
CV v5,v34,v35
MM a[24:27],a[132:135],v[92:95],a[24:27]
buffer_load_dwordx4 v229, s[8:11], s64 offen offset:1024 lds
MM a[24:27],a[128:131],v[88:91],a[24:27]
RD a[208:211],v224 offset:53248
RD a[212:215],v225 offset:53248
MM a[28:31],a[136:139],v[88:91],a[28:31]
buffer_load_dwordx4 v230, s[8:11], s64 offen offset:2048 lds
MM a[28:31],a[140:143],v[92:95],a[28:31]
RD a[216:219],v226 offset:53248
RD a[220:223],v227 offset:53248
MM a[20:23],a[140:143],v[84:87],a[20:23]
EX v37,v37
MM a[20:23],a[136:139],v[80:83],a[20:23]
RD a[224:227],v224 offset:57344
CV v6,v36,v37
MM a[12:15],a[136:139],v[72:75],a[12:15]
EX v38,v38
MM a[12:15],a[140:143],v[76:79],a[12:15]
buffer_load_dwordx4 v231, s[8:11], s64 offen offset:3072 lds
MM a[4:7],a[140:143],v[68:71],a[4:7]
s_add_u32 m0, s65, 81920
EX v39,v39
MM a[4:7],a[136:139],v[64:67],a[4:7]
RD a[228:231],v225 offset:57344
CV v7,v38,v39
MM a[32:35],a[144:147],v[64:67],a[32:35]
EX v40,v40
MM a[32:35],a[148:151],v[68:71],a[32:35]
EX v41,v41
MM a[40:43],a[148:151],v[76:79],a[40:43]
RD a[232:235],v226 offset:57344
CV v12,v40,v41
MM a[40:43],a[144:147],v[72:75],a[40:43]
EX v42,v42
MM a[48:51],a[144:147],v[80:83],a[48:51]
buffer_load_dwordx4 v235, s[12:15], s81 offen offset:0 lds
MM a[48:51],a[148:151],v[84:87],a[48:51]
EX v43,v43
MM a[56:59],a[148:151],v[92:95],a[56:59]
RD a[236:239],v227 offset:57344
CV v13,v42,v43
MM a[56:59],a[144:147],v[88:91],a[56:59]
EX v44,v44
MM a[60:63],a[152:155],v[88:91],a[60:63]
EX v45,v45
MM a[60:63],a[156:159],v[92:95],a[60:63]
RD a[240:243],v224 offset:61440
CV v14,v44,v45
MM a[52:55],a[156:159],v[84:87],a[52:55]
EX v46,v46
MM a[52:55],a[152:155],v[80:83],a[52:55]
buffer_load_dwordx4 v235, s[12:15], s64 offen offset:1024 lds
MM a[44:47],a[152:155],v[72:75],a[44:47]
EX v47,v47
MM a[44:47],a[156:159],v[76:79],a[44:47]
RD a[244:247],v225 offset:61440
CV v15,v46,v47
MM a[36:39],a[156:159],v[68:71],a[36:39]
EX v48,v48
MM a[36:39],a[152:155],v[64:67],a[36:39]
EX v49,v49
MM a[64:67],a[160:163],v[64:67],a[64:67]
RD a[248:251],v226 offset:61440
CV v20,v48,v49
MM a[64:67],a[164:167],v[68:71],a[64:67]
EX v50,v50
MM a[72:75],a[164:167],v[76:79],a[72:75]
buffer_load_dwordx4 v235, s[12:15], s81 offen offset:2048 lds
MM a[72:75],a[160:163],v[72:75],a[72:75]
EX v51,v51
MM a[80:83],a[160:163],v[80:83],a[80:83]
RD a[252:255],v227 offset:61440
CV v21,v50,v51
MM a[80:83],a[164:167],v[84:87],a[80:83]
EX v52,v52
MM a[88:91],a[164:167],v[92:95],a[88:91]
EX v53,v53
MM a[88:91],a[160:163],v[88:91],a[88:91]
buffer_load_dwordx4 v235, s[12:15], s64 offen offset:3072 lds
MM a[92:95],a[168:171],v[88:91],a[92:95]
EX v54,v54
MM a[92:95],a[172:175],v[92:95],a[92:95]
EX v55,v55
MM a[84:87],a[172:175],v[84:87],a[84:87]
EX v56,v56
MM a[84:87],a[168:171],v[80:83],a[84:87]
EX v57,v57
MM a[76:79],a[168:171],v[72:75],a[76:79]
EX v58,v58
MM a[76:79],a[172:175],v[76:79],a[76:79]
EX v59,v59
MM a[68:71],a[172:175],v[68:71],a[68:71]
EX v60,v60
MM a[68:71],a[168:171],v[64:67],a[68:71]
EX v61,v61
MM a[96:99],a[176:179],v[64:67],a[96:99]
EX v62,v62
MM a[96:99],a[180:183],v[68:71],a[96:99]
EX v63,v63
MM a[104:107],a[180:183],v[76:79],a[104:107]
CV v22,v52,v53
CV v23,v54,v55
MM a[104:107],a[176:179],v[72:75],a[104:107]
CV v28,v56,v57
CV v29,v58,v59
MM a[112:115],a[176:179],v[80:83],a[112:115]
CV v30,v60,v61
CV v31,v62,v63
MM a[112:115],a[180:183],v[84:87],a[112:115]
MM a[120:123],a[180:183],v[92:95],a[120:123]
MM a[120:123],a[176:179],v[88:91],a[120:123]
MM a[124:127],a[184:187],v[88:91],a[124:127]
v_add_f32 v244, v144, v145
v_add_f32 v245, v148, v149
MM a[124:127],a[188:191],v[92:95],a[124:127]
v_add_f32 v246, v152, v153
v_add_f32 v247, v156, v157
MM a[116:119],a[188:191],v[84:87],a[116:119]
s_cmp_eq_u32 s70, s82
s_cbranch_scc1 LWW_%=
LLALWW_%=:
MM a[116:119],a[184:187],v[80:83],a[116:119]
MM a[108:111],a[184:187],v[72:75],a[108:111]
MM a[108:111],a[188:191],v[76:79],a[108:111]
MM a[100:103],a[188:191],v[68:71],a[100:103]
MM a[100:103],a[184:187],v[64:67],a[100:103]
v_smfmac_f32_16x16x64_bf16 v[144:147], v[236:239], v[0:7], v234
v_smfmac_f32_16x16x64_bf16 v[148:151], v[236:239], v[8:15], v234
v_smfmac_f32_16x16x64_bf16 v[152:155], v[236:239], v[16:23], v234
v_smfmac_f32_16x16x64_bf16 v[156:159], v[236:239], v[24:31], v234
s_waitcnt lgkmcnt(0)
MM v[64:67],a[192:195],v[160:163],v[128:131]
s_add_u32 s64, s64, 16384
TR a[128:129],v232 offset:32768
s_add_u32 s81, s64, 896
MM v[64:67],a[196:199],v[176:179],v[64:67]
TR a[130:131],v232 offset:36864
v_max3_f32 v249, v144, v145, v148
MM v[64:67],a[200:203],v[192:195],v[64:67]
TR a[132:133],v232 offset:40960
v_max3_f32 v249, v249, v149, v152
MM v[64:67],a[204:207],v[208:211],v[64:67]
TR a[134:135],v232 offset:45056
v_max3_f32 v249, v249, v153, v156
MM v[68:71],a[220:223],v[208:211],v[128:131]
TR a[136:137],v233 offset:32768
v_max_f32 v249, v249, v157
MM v[68:71],a[216:219],v[192:195],v[68:71]
TR a[138:139],v233 offset:36864
v_cmp_lt_f32 vcc, s71, v249
MM v[68:71],a[212:215],v[176:179],v[68:71]
TR a[140:141],v233 offset:40960
TR a[142:143],v233 offset:45056
MM v[68:71],a[208:211],v[160:163],v[68:71]
s_cbranch_vccnz LSITE6_%=
LBACK6_%=:
MM v[76:79],a[208:211],v[164:167],v[132:135]
EX v64,v64
MM v[76:79],a[212:215],v[180:183],v[76:79]
EX v65,v65
MM v[76:79],a[216:219],v[196:199],v[76:79]
TR a[144:145],v232 offset:33280
CV v64,v64,v65
MM v[76:79],a[220:223],v[212:215],v[76:79]
EX v66,v66
MM v[72:75],a[204:207],v[212:215],v[132:135]
EX v67,v67
MM v[72:75],a[200:203],v[196:199],v[72:75]
TR a[146:147],v232 offset:37376
CV v65,v66,v67
MM v[72:75],a[196:199],v[180:183],v[72:75]
EX v68,v68
MM v[72:75],a[192:195],v[164:167],v[72:75]
TR a[148:149],v232 offset:41472
TR a[150:151],v232 offset:45568
MM v[80:83],a[192:195],v[168:171],v[136:139]
EX v69,v69
MM v[80:83],a[196:199],v[184:187],v[80:83]
TR a[152:153],v233 offset:33280
CV v66,v68,v69
MM v[80:83],a[200:203],v[200:203],v[80:83]
EX v70,v70
MM v[80:83],a[204:207],v[216:219],v[80:83]
TR a[154:155],v233 offset:37376
TR a[156:157],v233 offset:41472
MM v[84:87],a[220:223],v[216:219],v[136:139]
EX v71,v71
MM v[84:87],a[216:219],v[200:203],v[84:87]
TR a[158:159],v233 offset:45568
CV v67,v70,v71
MM v[84:87],a[212:215],v[184:187],v[84:87]
EX v72,v72
MM v[84:87],a[208:211],v[168:171],v[84:87]
EX v73,v73
MM v[92:95],a[208:211],v[172:175],v[140:143]
TR a[160:161],v232 offset:33792
CV v72,v72,v73
MM v[92:95],a[212:215],v[188:191],v[92:95]
EX v74,v74
MM v[92:95],a[216:219],v[204:207],v[92:95]
TR a[162:163],v232 offset:37888
)ASM"
R"ASM(
TR a[164:165],v232 offset:41984
MM v[92:95],a[220:223],v[220:223],v[92:95]
EX v75,v75
MM v[88:91],a[204:207],v[220:223],v[140:143]
TR a[166:167],v232 offset:46080
CV v73,v74,v75
MM v[88:91],a[200:203],v[204:207],v[88:91]
EX v76,v76
MM v[88:91],a[196:199],v[188:191],v[88:91]
EX v77,v77
MM v[88:91],a[192:195],v[172:175],v[88:91]
TR a[168:169],v233 offset:33792
CV v74,v76,v77
MM v[96:99],a[224:227],v[160:163],v[128:131]
EX v78,v78
MM v[96:99],a[228:231],v[176:179],v[96:99]
TR a[170:171],v233 offset:37888
TR a[172:173],v233 offset:41984
MM v[96:99],a[232:235],v[192:195],v[96:99]
EX v79,v79
MM v[96:99],a[236:239],v[208:211],v[96:99]
TR a[174:175],v233 offset:46080
CV v75,v78,v79
MM v[100:103],a[252:255],v[208:211],v[128:131]
EX v80,v80
MM v[100:103],a[248:251],v[192:195],v[100:103]
EX v81,v81
MM v[100:103],a[244:247],v[176:179],v[100:103]
TR a[176:177],v232 offset:34304
CV v80,v80,v81
MM v[100:103],a[240:243],v[160:163],v[100:103]
EX v82,v82
MM v[108:111],a[240:243],v[164:167],v[132:135]
buffer_store_dword v240, v248, s[20:23], s67 offen offset:12 sc1
MM v[108:111],a[244:247],v[180:183],v[108:111]
s_add_u32 s67, s67, 1024
TR a[178:179],v232 offset:38400
TR a[180:181],v232 offset:42496
MM v[108:111],a[248:251],v[196:199],v[108:111]
EX v83,v83
MM v[108:111],a[252:255],v[212:215],v[108:111]
TR a[182:183],v232 offset:46592
CV v81,v82,v83
MM v[104:107],a[236:239],v[212:215],v[132:135]
EX v84,v84
MM v[104:107],a[232:235],v[196:199],v[104:107]
EX v85,v85
MM v[104:107],a[228:231],v[180:183],v[104:107]
TR a[184:185],v233 offset:34304
CV v82,v84,v85
MM v[104:107],a[224:227],v[164:167],v[104:107]
EX v86,v86
MM v[112:115],a[224:227],v[168:171],v[136:139]
TR a[186:187],v233 offset:38400
TR a[188:189],v233 offset:42496
MM v[112:115],a[228:231],v[184:187],v[112:115]
EX v87,v87
MM v[112:115],a[232:235],v[200:203],v[112:115]
TR a[190:191],v233 offset:46592
CV v83,v86,v87
MM v[112:115],a[236:239],v[216:219],v[112:115]
EX v88,v88
MM v[116:119],a[252:255],v[216:219],v[136:139]
EX v89,v89
MM v[116:119],a[248:251],v[200:203],v[116:119]
EX v90,v90
MM v[116:119],a[244:247],v[184:187],v[116:119]
EX v91,v91
MM v[116:119],a[240:243],v[168:171],v[116:119]
EX v92,v92
MM v[124:127],a[240:243],v[172:175],v[140:143]
EX v93,v93
MM v[124:127],a[244:247],v[188:191],v[124:127]
EX v94,v94
MM v[124:127],a[248:251],v[204:207],v[124:127]
EX v95,v95
MM v[124:127],a[252:255],v[220:223],v[124:127]
EX v96,v96
MM v[120:123],a[236:239],v[220:223],v[140:143]
EX v97,v97
MM v[120:123],a[232:235],v[204:207],v[120:123]
EX v98,v98
MM v[120:123],a[228:231],v[188:191],v[120:123]
EX v99,v99
MM v[120:123],a[224:227],v[172:175],v[120:123]
s_waitcnt vmcnt(12) lgkmcnt(0)
s_barrier
MM a[0:3],a[128:131],v[0:3],a[0:3]
s_add_u32 m0, s65, 32768
CV v88,v88,v89
CV v89,v90,v91
MM a[0:3],a[132:135],v[4:7],a[0:3]
RD a[192:195],v224 offset:0
CV v90,v92,v93
MM a[8:11],a[132:135],v[12:15],a[8:11]
RD a[196:199],v225 offset:0
CV v91,v94,v95
MM a[8:11],a[128:131],v[8:11],a[8:11]
buffer_load_dwordx4 v228, s[8:11], s64 offen offset:0 lds
MM a[16:19],a[128:131],v[16:19],a[16:19]
RD a[200:203],v226 offset:0
CV v68,v96,v97
MM a[16:19],a[132:135],v[20:23],a[16:19]
RD a[204:207],v227 offset:0
CV v69,v98,v99
MM a[24:27],a[132:135],v[28:31],a[24:27]
buffer_load_dwordx4 v229, s[8:11], s64 offen offset:1024 lds
MM a[24:27],a[128:131],v[24:27],a[24:27]
RD a[208:211],v224 offset:4096
RD a[212:215],v225 offset:4096
MM a[28:31],a[136:139],v[24:27],a[28:31]
buffer_load_dwordx4 v230, s[8:11], s64 offen offset:2048 lds
MM a[28:31],a[140:143],v[28:31],a[28:31]
EX v100,v100
MM a[20:23],a[140:143],v[20:23],a[20:23]
EX v101,v101
MM a[20:23],a[136:139],v[16:19],a[20:23]
RD a[216:219],v226 offset:4096
CV v70,v100,v101
MM a[12:15],a[136:139],v[8:11],a[12:15]
EX v102,v102
MM a[12:15],a[140:143],v[12:15],a[12:15]
RD a[220:223],v227 offset:4096
RD a[224:227],v224 offset:8192
MM a[4:7],a[140:143],v[4:7],a[4:7]
EX v103,v103
MM a[4:7],a[136:139],v[0:3],a[4:7]
RD a[228:231],v225 offset:8192
CV v71,v102,v103
MM a[32:35],a[144:147],v[0:3],a[32:35]
EX v104,v104
MM a[32:35],a[148:151],v[4:7],a[32:35]
buffer_load_dwordx4 v231, s[8:11], s64 offen offset:3072 lds
MM a[40:43],a[148:151],v[12:15],a[40:43]
s_add_u32 m0, s65, 98304
EX v105,v105
MM a[40:43],a[144:147],v[8:11],a[40:43]
RD a[232:235],v226 offset:8192
CV v76,v104,v105
MM a[48:51],a[144:147],v[16:19],a[48:51]
EX v106,v106
MM a[48:51],a[148:151],v[20:23],a[48:51]
EX v107,v107
MM a[56:59],a[148:151],v[28:31],a[56:59]
RD a[236:239],v227 offset:8192
CV v77,v106,v107
MM a[56:59],a[144:147],v[24:27],a[56:59]
EX v108,v108
MM a[60:63],a[152:155],v[24:27],a[60:63]
buffer_load_dwordx4 v235, s[12:15], s81 offen offset:0 lds
MM a[60:63],a[156:159],v[28:31],a[60:63]
EX v109,v109
MM a[52:55],a[156:159],v[20:23],a[52:55]
RD a[240:243],v224 offset:12288
CV v78,v108,v109
MM a[52:55],a[152:155],v[16:19],a[52:55]
EX v110,v110
MM a[44:47],a[152:155],v[8:11],a[44:47]
EX v111,v111
MM a[44:47],a[156:159],v[12:15],a[44:47]
RD a[244:247],v225 offset:12288
CV v79,v110,v111
MM a[36:39],a[156:159],v[4:7],a[36:39]
buffer_load_dwordx4 v235, s[12:15], s64 offen offset:1024 lds
MM a[36:39],a[152:155],v[0:3],a[36:39]
EX v112,v112
MM a[64:67],a[160:163],v[0:3],a[64:67]
EX v113,v113
MM a[64:67],a[164:167],v[4:7],a[64:67]
RD a[248:251],v226 offset:12288
CV v84,v112,v113
MM a[72:75],a[164:167],v[12:15],a[72:75]
EX v114,v114
MM a[72:75],a[160:163],v[8:11],a[72:75]
EX v115,v115
MM a[80:83],a[160:163],v[16:19],a[80:83]
RD a[252:255],v227 offset:12288
CV v85,v114,v115
MM a[80:83],a[164:167],v[20:23],a[80:83]
buffer_load_dwordx4 v235, s[12:15], s81 offen offset:2048 lds
MM a[88:91],a[164:167],v[28:31],a[88:91]
EX v116,v116
MM a[88:91],a[160:163],v[24:27],a[88:91]
EX v117,v117
MM a[92:95],a[168:171],v[24:27],a[92:95]
EX v118,v118
MM a[92:95],a[172:175],v[28:31],a[92:95]
buffer_load_dwordx4 v235, s[12:15], s64 offen offset:3072 lds
MM a[84:87],a[172:175],v[20:23],a[84:87]
EX v119,v119
MM a[84:87],a[168:171],v[16:19],a[84:87]
EX v120,v120
MM a[76:79],a[168:171],v[8:11],a[76:79]
LPG8_%=:
s_cbranch_execz LPG12_%=
EX v121,v121
MM a[76:79],a[172:175],v[12:15],a[76:79]
EX v122,v122
MM a[68:71],a[172:175],v[4:7],a[68:71]
EX v123,v123
MM a[68:71],a[168:171],v[0:3],a[68:71]
EX v124,v124
MM a[96:99],a[176:179],v[0:3],a[96:99]
EX v125,v125
MM a[96:99],a[180:183],v[4:7],a[96:99]
EX v126,v126
MM a[104:107],a[180:183],v[12:15],a[104:107]
EX v127,v127
MM a[104:107],a[176:179],v[8:11],a[104:107]
CV v86,v116,v117
CV v87,v118,v119
MM a[112:115],a[176:179],v[16:19],a[112:115]
CV v92,v120,v121
CV v93,v122,v123
MM a[112:115],a[180:183],v[20:23],a[112:115]
CV v94,v124,v125
CV v95,v126,v127
MM a[120:123],a[180:183],v[28:31],a[120:123]
MM a[120:123],a[176:179],v[24:27],a[120:123]
MM a[124:127],a[184:187],v[24:27],a[124:127]
v_add_f32 v244, v144, v145
v_add_f32 v245, v148, v149
MM a[124:127],a[188:191],v[28:31],a[124:127]
v_add_f32 v246, v152, v153
v_add_f32 v247, v156, v157
MM a[116:119],a[188:191],v[20:23],a[116:119]
MM a[116:119],a[184:187],v[16:19],a[116:119]
MM a[108:111],a[184:187],v[8:11],a[108:111]
MM a[108:111],a[188:191],v[12:15],a[108:111]
MM a[100:103],a[188:191],v[4:7],a[100:103]
MM a[100:103],a[184:187],v[0:3],a[100:103]
v_smfmac_f32_16x16x64_bf16 v[144:147], v[236:239], v[64:71], v234
v_smfmac_f32_16x16x64_bf16 v[148:151], v[236:239], v[72:79], v234
v_smfmac_f32_16x16x64_bf16 v[152:155], v[236:239], v[80:87], v234
v_smfmac_f32_16x16x64_bf16 v[156:159], v[236:239], v[88:95], v234
s_waitcnt lgkmcnt(0)
MM v[0:3],a[192:195],v[160:163],v[128:131]
s_add_u32 s64, s64, 16384
TR a[128:129],v232 offset:49152
s_add_u32 s81, s64, 896
MM v[0:3],a[196:199],v[176:179],v[0:3]
TR a[130:131],v232 offset:53248
v_max3_f32 v249, v144, v145, v148
MM v[0:3],a[200:203],v[192:195],v[0:3]
TR a[132:133],v232 offset:57344
v_max3_f32 v249, v249, v149, v152
MM v[0:3],a[204:207],v[208:211],v[0:3]
TR a[134:135],v232 offset:61440
v_max3_f32 v249, v249, v153, v156
MM v[4:7],a[220:223],v[208:211],v[128:131]
TR a[136:137],v233 offset:49152
v_max_f32 v249, v249, v157
MM v[4:7],a[216:219],v[192:195],v[4:7]
TR a[138:139],v233 offset:53248
v_cmp_lt_f32 vcc, s71, v249
MM v[4:7],a[212:215],v[176:179],v[4:7]
TR a[140:141],v233 offset:57344
TR a[142:143],v233 offset:61440
MM v[4:7],a[208:211],v[160:163],v[4:7]
s_cbranch_vccnz LSITE7_%=
LBACK7_%=:
MM v[12:15],a[208:211],v[164:167],v[132:135]
EX v0,v0
MM v[12:15],a[212:215],v[180:183],v[12:15]
EX v1,v1
MM v[12:15],a[216:219],v[196:199],v[12:15]
TR a[144:145],v232 offset:49664
CV v0,v0,v1
MM v[12:15],a[220:223],v[212:215],v[12:15]
EX v2,v2
MM v[8:11],a[204:207],v[212:215],v[132:135]
EX v3,v3
MM v[8:11],a[200:203],v[196:199],v[8:11]
TR a[146:147],v232 offset:53760
CV v1,v2,v3
MM v[8:11],a[196:199],v[180:183],v[8:11]
EX v4,v4
MM v[8:11],a[192:195],v[164:167],v[8:11]
TR a[148:149],v232 offset:57856
TR a[150:151],v232 offset:61952
MM v[16:19],a[192:195],v[168:171],v[136:139]
EX v5,v5
MM v[16:19],a[196:199],v[184:187],v[16:19]
TR a[152:153],v233 offset:49664
CV v2,v4,v5
MM v[16:19],a[200:203],v[200:203],v[16:19]
EX v6,v6
MM v[16:19],a[204:207],v[216:219],v[16:19]
TR a[154:155],v233 offset:53760
TR a[156:157],v233 offset:57856
MM v[20:23],a[220:223],v[216:219],v[136:139]
EX v7,v7
MM v[20:23],a[216:219],v[200:203],v[20:23]
TR a[158:159],v233 offset:61952
CV v3,v6,v7
MM v[20:23],a[212:215],v[184:187],v[20:23]
EX v8,v8
MM v[20:23],a[208:211],v[168:171],v[20:23]
EX v9,v9
MM v[28:31],a[208:211],v[172:175],v[140:143]
TR a[160:161],v232 offset:50176
CV v8,v8,v9
MM v[28:31],a[212:215],v[188:191],v[28:31]
EX v10,v10
MM v[28:31],a[216:219],v[204:207],v[28:31]
TR a[162:163],v232 offset:54272
TR a[164:165],v232 offset:58368
MM v[28:31],a[220:223],v[220:223],v[28:31]
EX v11,v11
MM v[24:27],a[204:207],v[220:223],v[140:143]
TR a[166:167],v232 offset:62464
CV v9,v10,v11
MM v[24:27],a[200:203],v[204:207],v[24:27]
EX v12,v12
MM v[24:27],a[196:199],v[188:191],v[24:27]
EX v13,v13
MM v[24:27],a[192:195],v[172:175],v[24:27]
TR a[168:169],v233 offset:50176
CV v10,v12,v13
MM v[32:35],a[224:227],v[160:163],v[128:131]
EX v14,v14
MM v[32:35],a[228:231],v[176:179],v[32:35]
TR a[170:171],v233 offset:54272
TR a[172:173],v233 offset:58368
MM v[32:35],a[232:235],v[192:195],v[32:35]
EX v15,v15
MM v[32:35],a[236:239],v[208:211],v[32:35]
TR a[174:175],v233 offset:62464
CV v11,v14,v15
MM v[36:39],a[252:255],v[208:211],v[128:131]
EX v16,v16
MM v[36:39],a[248:251],v[192:195],v[36:39]
EX v17,v17
MM v[36:39],a[244:247],v[176:179],v[36:39]
TR a[176:177],v232 offset:50688
CV v16,v16,v17
MM v[36:39],a[240:243],v[160:163],v[36:39]
EX v18,v18
MM v[44:47],a[240:243],v[164:167],v[132:135]
TR a[178:179],v232 offset:54784
TR a[180:181],v232 offset:58880
MM v[44:47],a[244:247],v[180:183],v[44:47]
EX v19,v19
MM v[44:47],a[248:251],v[196:199],v[44:47]
TR a[182:183],v232 offset:62976
CV v17,v18,v19
MM v[44:47],a[252:255],v[212:215],v[44:47]
EX v20,v20
MM v[40:43],a[236:239],v[212:215],v[132:135]
TR a[184:185],v233 offset:50688
TR a[186:187],v233 offset:54784
MM v[40:43],a[232:235],v[196:199],v[40:43]
EX v21,v21
MM v[40:43],a[228:231],v[180:183],v[40:43]
TR a[188:189],v233 offset:58880
CV v18,v20,v21
MM v[40:43],a[224:227],v[164:167],v[40:43]
EX v22,v22
MM v[48:51],a[224:227],v[168:171],v[136:139]
EX v23,v23
MM v[48:51],a[228:231],v[184:187],v[48:51]
TR a[190:191],v233 offset:62976
CV v19,v22,v23
MM v[48:51],a[232:235],v[200:203],v[48:51]
EX v24,v24
MM v[48:51],a[236:239],v[216:219],v[48:51]
EX v25,v25
MM v[52:55],a[252:255],v[216:219],v[136:139]
EX v26,v26
MM v[52:55],a[248:251],v[200:203],v[52:55]
EX v27,v27
MM v[52:55],a[244:247],v[184:187],v[52:55]
EX v28,v28
MM v[52:55],a[240:243],v[168:171],v[52:55]
EX v29,v29
MM v[60:63],a[240:243],v[172:175],v[140:143]
EX v30,v30
MM v[60:63],a[244:247],v[188:191],v[60:63]
EX v31,v31
MM v[60:63],a[248:251],v[204:207],v[60:63]
EX v32,v32
MM v[60:63],a[252:255],v[220:223],v[60:63]
EX v33,v33
MM v[56:59],a[236:239],v[220:223],v[140:143]
EX v34,v34
MM v[56:59],a[232:235],v[204:207],v[56:59]
EX v35,v35
MM v[56:59],a[228:231],v[188:191],v[56:59]
EX v36,v36
MM v[56:59],a[224:227],v[172:175],v[56:59]
s_waitcnt vmcnt(12) lgkmcnt(0)
)ASM"
R"ASM(
s_barrier
MM a[0:3],a[128:131],v[64:67],a[0:3]
s_add_u32 m0, s65, 49152
CV v24,v24,v25
CV v25,v26,v27
MM a[0:3],a[132:135],v[68:71],a[0:3]
RD a[192:195],v224 offset:16384
CV v26,v28,v29
MM a[8:11],a[132:135],v[76:79],a[8:11]
RD a[196:199],v225 offset:16384
CV v27,v30,v31
MM a[8:11],a[128:131],v[72:75],a[8:11]
buffer_load_dwordx4 v228, s[8:11], s64 offen offset:0 lds
MM a[16:19],a[128:131],v[80:83],a[16:19]
RD a[200:203],v226 offset:16384
CV v4,v32,v33
MM a[16:19],a[132:135],v[84:87],a[16:19]
RD a[204:207],v227 offset:16384
CV v5,v34,v35
MM a[24:27],a[132:135],v[92:95],a[24:27]
buffer_load_dwordx4 v229, s[8:11], s64 offen offset:1024 lds
MM a[24:27],a[128:131],v[88:91],a[24:27]
RD a[208:211],v224 offset:20480
RD a[212:215],v225 offset:20480
MM a[28:31],a[136:139],v[88:91],a[28:31]
buffer_load_dwordx4 v230, s[8:11], s64 offen offset:2048 lds
MM a[28:31],a[140:143],v[92:95],a[28:31]
RD a[216:219],v226 offset:20480
RD a[220:223],v227 offset:20480
MM a[20:23],a[140:143],v[84:87],a[20:23]
EX v37,v37
MM a[20:23],a[136:139],v[80:83],a[20:23]
RD a[224:227],v224 offset:24576
CV v6,v36,v37
MM a[12:15],a[136:139],v[72:75],a[12:15]
EX v38,v38
MM a[12:15],a[140:143],v[76:79],a[12:15]
buffer_load_dwordx4 v231, s[8:11], s64 offen offset:3072 lds
MM a[4:7],a[140:143],v[68:71],a[4:7]
s_add_u32 m0, s65, 114688
EX v39,v39
MM a[4:7],a[136:139],v[64:67],a[4:7]
RD a[228:231],v225 offset:24576
CV v7,v38,v39
MM a[32:35],a[144:147],v[64:67],a[32:35]
EX v40,v40
MM a[32:35],a[148:151],v[68:71],a[32:35]
EX v41,v41
MM a[40:43],a[148:151],v[76:79],a[40:43]
RD a[232:235],v226 offset:24576
CV v12,v40,v41
MM a[40:43],a[144:147],v[72:75],a[40:43]
EX v42,v42
MM a[48:51],a[144:147],v[80:83],a[48:51]
buffer_load_dwordx4 v235, s[12:15], s81 offen offset:0 lds
MM a[48:51],a[148:151],v[84:87],a[48:51]
EX v43,v43
MM a[56:59],a[148:151],v[92:95],a[56:59]
RD a[236:239],v227 offset:24576
CV v13,v42,v43
MM a[56:59],a[144:147],v[88:91],a[56:59]
EX v44,v44
MM a[60:63],a[152:155],v[88:91],a[60:63]
EX v45,v45
MM a[60:63],a[156:159],v[92:95],a[60:63]
RD a[240:243],v224 offset:28672
CV v14,v44,v45
MM a[52:55],a[156:159],v[84:87],a[52:55]
EX v46,v46
MM a[52:55],a[152:155],v[80:83],a[52:55]
buffer_load_dwordx4 v235, s[12:15], s64 offen offset:1024 lds
MM a[44:47],a[152:155],v[72:75],a[44:47]
EX v47,v47
MM a[44:47],a[156:159],v[76:79],a[44:47]
RD a[244:247],v225 offset:28672
CV v15,v46,v47
MM a[36:39],a[156:159],v[68:71],a[36:39]
EX v48,v48
MM a[36:39],a[152:155],v[64:67],a[36:39]
EX v49,v49
MM a[64:67],a[160:163],v[64:67],a[64:67]
RD a[248:251],v226 offset:28672
CV v20,v48,v49
MM a[64:67],a[164:167],v[68:71],a[64:67]
EX v50,v50
MM a[72:75],a[164:167],v[76:79],a[72:75]
buffer_load_dwordx4 v235, s[12:15], s81 offen offset:2048 lds
MM a[72:75],a[160:163],v[72:75],a[72:75]
EX v51,v51
MM a[80:83],a[160:163],v[80:83],a[80:83]
RD a[252:255],v227 offset:28672
CV v21,v50,v51
MM a[80:83],a[164:167],v[84:87],a[80:83]
EX v52,v52
MM a[88:91],a[164:167],v[92:95],a[88:91]
EX v53,v53
MM a[88:91],a[160:163],v[88:91],a[88:91]
buffer_load_dwordx4 v235, s[12:15], s64 offen offset:3072 lds
MM a[92:95],a[168:171],v[88:91],a[92:95]
EX v54,v54
MM a[92:95],a[172:175],v[92:95],a[92:95]
EX v55,v55
MM a[84:87],a[172:175],v[84:87],a[84:87]
EX v56,v56
MM a[84:87],a[168:171],v[80:83],a[84:87]
EX v57,v57
MM a[76:79],a[168:171],v[72:75],a[76:79]
EX v58,v58
MM a[76:79],a[172:175],v[76:79],a[76:79]
EX v59,v59
MM a[68:71],a[172:175],v[68:71],a[68:71]
EX v60,v60
MM a[68:71],a[168:171],v[64:67],a[68:71]
EX v61,v61
MM a[96:99],a[176:179],v[64:67],a[96:99]
EX v62,v62
MM a[96:99],a[180:183],v[68:71],a[96:99]
EX v63,v63
MM a[104:107],a[180:183],v[76:79],a[104:107]
CV v22,v52,v53
CV v23,v54,v55
MM a[104:107],a[176:179],v[72:75],a[104:107]
CV v28,v56,v57
CV v29,v58,v59
MM a[112:115],a[176:179],v[80:83],a[112:115]
CV v30,v60,v61
CV v31,v62,v63
MM a[112:115],a[180:183],v[84:87],a[112:115]
MM a[120:123],a[180:183],v[92:95],a[120:123]
MM a[120:123],a[176:179],v[88:91],a[120:123]
MM a[124:127],a[184:187],v[88:91],a[124:127]
v_add_f32 v244, v144, v145
v_add_f32 v245, v148, v149
MM a[124:127],a[188:191],v[92:95],a[124:127]
v_add_f32 v246, v152, v153
v_add_f32 v247, v156, v157
MM a[116:119],a[188:191],v[84:87],a[116:119]
s_cmp_eq_u32 s70, 2
s_cbranch_scc1 LIP_%=
LLALIP_%=:
MM a[116:119],a[184:187],v[80:83],a[116:119]
MM a[108:111],a[184:187],v[72:75],a[108:111]
MM a[108:111],a[188:191],v[76:79],a[108:111]
MM a[100:103],a[188:191],v[68:71],a[100:103]
MM a[100:103],a[184:187],v[64:67],a[100:103]
v_smfmac_f32_16x16x64_bf16 v[144:147], v[236:239], v[0:7], v234
v_smfmac_f32_16x16x64_bf16 v[148:151], v[236:239], v[8:15], v234
v_smfmac_f32_16x16x64_bf16 v[152:155], v[236:239], v[16:23], v234
v_smfmac_f32_16x16x64_bf16 v[156:159], v[236:239], v[24:31], v234
s_waitcnt lgkmcnt(0)
MM v[64:67],a[192:195],v[160:163],v[128:131]
s_add_u32 s64, s64, 16384
TR a[128:129],v232 offset:0
s_and_b32 s64, s64, 524287
MM v[64:67],a[196:199],v[176:179],v[64:67]
s_add_u32 s81, s64, 896
TR a[130:131],v232 offset:4096
v_max3_f32 v249, v144, v145, v148
MM v[64:67],a[200:203],v[192:195],v[64:67]
TR a[132:133],v232 offset:8192
v_max3_f32 v249, v249, v149, v152
MM v[64:67],a[204:207],v[208:211],v[64:67]
TR a[134:135],v232 offset:12288
v_max3_f32 v249, v249, v153, v156
MM v[68:71],a[220:223],v[208:211],v[128:131]
TR a[136:137],v233 offset:0
v_max_f32 v249, v249, v157
MM v[68:71],a[216:219],v[192:195],v[68:71]
TR a[138:139],v233 offset:4096
v_cmp_lt_f32 vcc, s71, v249
MM v[68:71],a[212:215],v[176:179],v[68:71]
TR a[140:141],v233 offset:8192
TR a[142:143],v233 offset:12288
MM v[68:71],a[208:211],v[160:163],v[68:71]
s_cbranch_vccnz LSITE8_%=
LBACK8_%=:
MM v[76:79],a[208:211],v[164:167],v[132:135]
EX v64,v64
MM v[76:79],a[212:215],v[180:183],v[76:79]
EX v65,v65
MM v[76:79],a[216:219],v[196:199],v[76:79]
TR a[144:145],v232 offset:512
CV v64,v64,v65
MM v[76:79],a[220:223],v[212:215],v[76:79]
EX v66,v66
MM v[72:75],a[204:207],v[212:215],v[132:135]
EX v67,v67
MM v[72:75],a[200:203],v[196:199],v[72:75]
TR a[146:147],v232 offset:4608
CV v65,v66,v67
MM v[72:75],a[196:199],v[180:183],v[72:75]
EX v68,v68
MM v[72:75],a[192:195],v[164:167],v[72:75]
TR a[148:149],v232 offset:8704
TR a[150:151],v232 offset:12800
MM v[80:83],a[192:195],v[168:171],v[136:139]
EX v69,v69
MM v[80:83],a[196:199],v[184:187],v[80:83]
TR a[152:153],v233 offset:512
CV v66,v68,v69
MM v[80:83],a[200:203],v[200:203],v[80:83]
EX v70,v70
MM v[80:83],a[204:207],v[216:219],v[80:83]
TR a[154:155],v233 offset:4608
TR a[156:157],v233 offset:8704
MM v[84:87],a[220:223],v[216:219],v[136:139]
EX v71,v71
MM v[84:87],a[216:219],v[200:203],v[84:87]
TR a[158:159],v233 offset:12800
CV v67,v70,v71
MM v[84:87],a[212:215],v[184:187],v[84:87]
EX v72,v72
MM v[84:87],a[208:211],v[168:171],v[84:87]
EX v73,v73
MM v[92:95],a[208:211],v[172:175],v[140:143]
TR a[160:161],v232 offset:1024
CV v72,v72,v73
MM v[92:95],a[212:215],v[188:191],v[92:95]
EX v74,v74
MM v[92:95],a[216:219],v[204:207],v[92:95]
TR a[162:163],v232 offset:5120
TR a[164:165],v232 offset:9216
MM v[92:95],a[220:223],v[220:223],v[92:95]
EX v75,v75
MM v[88:91],a[204:207],v[220:223],v[140:143]
TR a[166:167],v232 offset:13312
CV v73,v74,v75
MM v[88:91],a[200:203],v[204:207],v[88:91]
EX v76,v76
MM v[88:91],a[196:199],v[188:191],v[88:91]
EX v77,v77
MM v[88:91],a[192:195],v[172:175],v[88:91]
TR a[168:169],v233 offset:1024
CV v74,v76,v77
MM v[96:99],a[224:227],v[160:163],v[128:131]
EX v78,v78
MM v[96:99],a[228:231],v[176:179],v[96:99]
TR a[170:171],v233 offset:5120
TR a[172:173],v233 offset:9216
MM v[96:99],a[232:235],v[192:195],v[96:99]
EX v79,v79
MM v[96:99],a[236:239],v[208:211],v[96:99]
TR a[174:175],v233 offset:13312
CV v75,v78,v79
MM v[100:103],a[252:255],v[208:211],v[128:131]
EX v80,v80
MM v[100:103],a[248:251],v[192:195],v[100:103]
EX v81,v81
MM v[100:103],a[244:247],v[176:179],v[100:103]
TR a[176:177],v232 offset:1536
CV v80,v80,v81
MM v[100:103],a[240:243],v[160:163],v[100:103]
EX v82,v82
MM v[108:111],a[240:243],v[164:167],v[132:135]
TR a[178:179],v232 offset:5632
TR a[180:181],v232 offset:9728
MM v[108:111],a[244:247],v[180:183],v[108:111]
EX v83,v83
MM v[108:111],a[248:251],v[196:199],v[108:111]
TR a[182:183],v232 offset:13824
CV v81,v82,v83
MM v[108:111],a[252:255],v[212:215],v[108:111]
EX v84,v84
MM v[104:107],a[236:239],v[212:215],v[132:135]
TR a[184:185],v233 offset:1536
TR a[186:187],v233 offset:5632
MM v[104:107],a[232:235],v[196:199],v[104:107]
EX v85,v85
MM v[104:107],a[228:231],v[180:183],v[104:107]
TR a[188:189],v233 offset:9728
CV v82,v84,v85
MM v[104:107],a[224:227],v[164:167],v[104:107]
EX v86,v86
MM v[112:115],a[224:227],v[168:171],v[136:139]
EX v87,v87
MM v[112:115],a[228:231],v[184:187],v[112:115]
TR a[190:191],v233 offset:13824
CV v83,v86,v87
MM v[112:115],a[232:235],v[200:203],v[112:115]
EX v88,v88
MM v[112:115],a[236:239],v[216:219],v[112:115]
EX v89,v89
MM v[116:119],a[252:255],v[216:219],v[136:139]
EX v90,v90
MM v[116:119],a[248:251],v[200:203],v[116:119]
EX v91,v91
MM v[116:119],a[244:247],v[184:187],v[116:119]
EX v92,v92
MM v[116:119],a[240:243],v[168:171],v[116:119]
EX v93,v93
MM v[124:127],a[240:243],v[172:175],v[140:143]
EX v94,v94
MM v[124:127],a[244:247],v[188:191],v[124:127]
EX v95,v95
MM v[124:127],a[248:251],v[204:207],v[124:127]
EX v96,v96
MM v[124:127],a[252:255],v[220:223],v[124:127]
EX v97,v97
MM v[120:123],a[236:239],v[220:223],v[140:143]
EX v98,v98
MM v[120:123],a[232:235],v[204:207],v[120:123]
EX v99,v99
MM v[120:123],a[228:231],v[188:191],v[120:123]
s_cmp_eq_u32 s70, 1
s_cbranch_scc1 LTP_%=
LLALTP_%=:
MM v[120:123],a[224:227],v[172:175],v[120:123]
s_waitcnt vmcnt(12) lgkmcnt(0)
s_barrier
MM a[0:3],a[128:131],v[0:3],a[0:3]
s_add_u32 m0, s65, 0
CV v88,v88,v89
CV v89,v90,v91
MM a[0:3],a[132:135],v[4:7],a[0:3]
RD a[192:195],v224 offset:32768
CV v90,v92,v93
MM a[8:11],a[132:135],v[12:15],a[8:11]
RD a[196:199],v225 offset:32768
CV v91,v94,v95
MM a[8:11],a[128:131],v[8:11],a[8:11]
buffer_load_dwordx4 v228, s[8:11], s64 offen offset:0 lds
MM a[16:19],a[128:131],v[16:19],a[16:19]
RD a[200:203],v226 offset:32768
CV v68,v96,v97
MM a[16:19],a[132:135],v[20:23],a[16:19]
RD a[204:207],v227 offset:32768
CV v69,v98,v99
MM a[24:27],a[132:135],v[28:31],a[24:27]
buffer_load_dwordx4 v229, s[8:11], s64 offen offset:1024 lds
MM a[24:27],a[128:131],v[24:27],a[24:27]
RD a[208:211],v224 offset:36864
RD a[212:215],v225 offset:36864
MM a[28:31],a[136:139],v[24:27],a[28:31]
buffer_load_dwordx4 v230, s[8:11], s64 offen offset:2048 lds
MM a[28:31],a[140:143],v[28:31],a[28:31]
EX v100,v100
MM a[20:23],a[140:143],v[20:23],a[20:23]
EX v101,v101
MM a[20:23],a[136:139],v[16:19],a[20:23]
RD a[216:219],v226 offset:36864
CV v70,v100,v101
MM a[12:15],a[136:139],v[8:11],a[12:15]
EX v102,v102
MM a[12:15],a[140:143],v[12:15],a[12:15]
RD a[220:223],v227 offset:36864
RD a[224:227],v224 offset:40960
MM a[4:7],a[140:143],v[4:7],a[4:7]
EX v103,v103
MM a[4:7],a[136:139],v[0:3],a[4:7]
RD a[228:231],v225 offset:40960
CV v71,v102,v103
MM a[32:35],a[144:147],v[0:3],a[32:35]
EX v104,v104
MM a[32:35],a[148:151],v[4:7],a[32:35]
buffer_load_dwordx4 v231, s[8:11], s64 offen offset:3072 lds
MM a[40:43],a[148:151],v[12:15],a[40:43]
s_add_u32 m0, s65, 65536
EX v105,v105
MM a[40:43],a[144:147],v[8:11],a[40:43]
RD a[232:235],v226 offset:40960
CV v76,v104,v105
MM a[48:51],a[144:147],v[16:19],a[48:51]
EX v106,v106
MM a[48:51],a[148:151],v[20:23],a[48:51]
EX v107,v107
MM a[56:59],a[148:151],v[28:31],a[56:59]
RD a[236:239],v227 offset:40960
CV v77,v106,v107
MM a[56:59],a[144:147],v[24:27],a[56:59]
EX v108,v108
MM a[60:63],a[152:155],v[24:27],a[60:63]
buffer_load_dwordx4 v235, s[12:15], s81 offen offset:0 lds
MM a[60:63],a[156:159],v[28:31],a[60:63]
EX v109,v109
MM a[52:55],a[156:159],v[20:23],a[52:55]
RD a[240:243],v224 offset:45056
CV v78,v108,v109
MM a[52:55],a[152:155],v[16:19],a[52:55]
EX v110,v110
MM a[44:47],a[152:155],v[8:11],a[44:47]
EX v111,v111
MM a[44:47],a[156:159],v[12:15],a[44:47]
RD a[244:247],v225 offset:45056
CV v79,v110,v111
MM a[36:39],a[156:159],v[4:7],a[36:39]
EX v112,v112
MM a[36:39],a[152:155],v[0:3],a[36:39]
buffer_load_dwordx4 v235, s[12:15], s64 offen offset:1024 lds
)ASM"
R"ASM(
MM a[64:67],a[160:163],v[0:3],a[64:67]
EX v113,v113
MM a[64:67],a[164:167],v[4:7],a[64:67]
RD a[248:251],v226 offset:45056
s_nop 0
LPG9_%=:
s_cbranch_execz LTR0_%=
CV v84,v112,v113
MM a[72:75],a[164:167],v[12:15],a[72:75]
EX v114,v114
MM a[72:75],a[160:163],v[8:11],a[72:75]
EX v115,v115
MM a[80:83],a[160:163],v[16:19],a[80:83]
RD a[252:255],v227 offset:45056
CV v85,v114,v115
MM a[80:83],a[164:167],v[20:23],a[80:83]
buffer_load_dwordx4 v235, s[12:15], s81 offen offset:2048 lds
MM a[88:91],a[164:167],v[28:31],a[88:91]
EX v116,v116
MM a[88:91],a[160:163],v[24:27],a[88:91]
EX v117,v117
MM a[92:95],a[168:171],v[24:27],a[92:95]
EX v118,v118
MM a[92:95],a[172:175],v[28:31],a[92:95]
buffer_load_dwordx4 v235, s[12:15], s64 offen offset:3072 lds
MM a[84:87],a[172:175],v[20:23],a[84:87]
EX v119,v119
MM a[84:87],a[168:171],v[16:19],a[84:87]
EX v120,v120
MM a[76:79],a[168:171],v[8:11],a[76:79]
EX v121,v121
MM a[76:79],a[172:175],v[12:15],a[76:79]
EX v122,v122
MM a[68:71],a[172:175],v[4:7],a[68:71]
EX v123,v123
MM a[68:71],a[168:171],v[0:3],a[68:71]
EX v124,v124
MM a[96:99],a[176:179],v[0:3],a[96:99]
EX v125,v125
MM a[96:99],a[180:183],v[4:7],a[96:99]
EX v126,v126
MM a[104:107],a[180:183],v[12:15],a[104:107]
EX v127,v127
MM a[104:107],a[176:179],v[8:11],a[104:107]
CV v86,v116,v117
CV v87,v118,v119
MM a[112:115],a[176:179],v[16:19],a[112:115]
CV v92,v120,v121
CV v93,v122,v123
MM a[112:115],a[180:183],v[20:23],a[112:115]
CV v94,v124,v125
CV v95,v126,v127
MM a[120:123],a[180:183],v[28:31],a[120:123]
MM a[120:123],a[176:179],v[24:27],a[120:123]
MM a[124:127],a[184:187],v[24:27],a[124:127]
v_add_f32 v244, v144, v145
v_add_f32 v245, v148, v149
MM a[124:127],a[188:191],v[28:31],a[124:127]
v_add_f32 v246, v152, v153
v_add_f32 v247, v156, v157
MM a[116:119],a[188:191],v[20:23],a[116:119]
s_cmp_eq_u32 s70, s82
s_cbranch_scc1 LWV_%=
LLALWV_%=:
MM a[116:119],a[184:187],v[16:19],a[116:119]
MM a[108:111],a[184:187],v[8:11],a[108:111]
MM a[108:111],a[188:191],v[12:15],a[108:111]
MM a[100:103],a[188:191],v[4:7],a[100:103]
MM a[100:103],a[184:187],v[0:3],a[100:103]
v_smfmac_f32_16x16x64_bf16 v[144:147], v[236:239], v[64:71], v234
v_smfmac_f32_16x16x64_bf16 v[148:151], v[236:239], v[72:79], v234
v_smfmac_f32_16x16x64_bf16 v[152:155], v[236:239], v[80:87], v234
v_smfmac_f32_16x16x64_bf16 v[156:159], v[236:239], v[88:95], v234
s_waitcnt lgkmcnt(0)
s_sub_u32 s70, s70, 1
s_cmp_lg_u32 s70, 0
s_cbranch_scc1 LLAP_%=
s_branch LLAJ_%=
LWW_%=:
v_lshlrev_b32 v250, 3, v248
v_min_u32 v250, 4992, v250
s_sub_u32 s91, 5242880, s26
s_sub_u32 s60, s24, s91
s_subb_u32 s61, s25, 0
s_mov_b32 s62, 5242880
s_mov_b32 s63, 131072
s_lshl_b32 s90, s16, 2
s_add_u32 s90, s90, %[swave]
s_mul_i32 s90, s90, 10240
s_add_u32 s90, s90, 0
s_add_u32 s91, %[slds], 135168
s_mov_b32 m0, s91
s_nop 0
buffer_load_dword v250, s[60:63], s90 offen offset:0 lds
s_branch LLALWW_%=
LIP_%=:
s_lshr_b32 s77, %[sb], 3
s_lshl_b32 s77, s77, 2
s_add_u32 s77, s77, %[swave]
s_lshl_b32 s77, s77, 9
s_add_u32 s77, s77, LLAJ_%=-LPDP_%=
v_lshrrev_b32 v253, 1, v248
v_add_u32 v253, s77, v253
s_mov_b32 s77, LKEND_ka-LPDP_%=-16
v_min_u32 v253, s77, v253
s_getpc_b64 s[98:99]
LPDP_%=:
global_load_dword v253, v253, s[98:99]
s_branch LLALIP_%=
LTP_%=:
s_getpc_b64 s[98:99]
LTA1_%=:
s_min_u32 s73, %[swave], 3
s_lshl_b32 s73, s73, 12
s_add_u32 s73, s73, LPG23_%=-LTA1_%=
s_add_u32 s98, s98, s73
s_addc_u32 s99, s99, 0
s_mov_b64 exec, 0
s_setpc_b64 s[98:99]
LTR1_%=:
s_mov_b64 exec, -1
s_branch LLALTP_%=
LWV_%=:
v_lshlrev_b32 v250, 3, v248
v_min_u32 v250, 4992, v250
s_sub_u32 s91, 5242880, s26
s_sub_u32 s60, s24, s91
s_subb_u32 s61, s25, 0
s_mov_b32 s62, 5242880
s_mov_b32 s63, 131072
s_lshl_b32 s90, s16, 2
s_add_u32 s90, s90, %[swave]
s_mul_i32 s90, s90, 10240
s_add_u32 s90, s90, 5120
s_add_u32 s91, %[slds], 135168
s_mov_b32 m0, s91
s_nop 0
buffer_load_dword v250, s[60:63], s90 offen offset:0 lds
s_branch LLALWV_%=
s_nop 0
s_nop 0
s_nop 0
s_nop 0
s_nop 0
s_nop 0
s_nop 0
s_nop 0
s_nop 0
s_nop 0
s_nop 0
s_nop 0
s_nop 0
s_nop 0
s_nop 0
s_nop 0
s_nop 0
s_nop 0
s_nop 0
s_nop 0
s_nop 0
s_nop 0
s_nop 0
s_nop 0
s_nop 0
s_nop 0
s_nop 0
s_nop 0
s_nop 0
s_nop 0
s_nop 0
s_nop 0
s_nop 0
s_nop 0
s_nop 0
s_nop 0
s_nop 0
s_nop 0
s_nop 0
s_nop 0
s_nop 0
s_nop 0
s_nop 0
s_nop 0
s_nop 0
s_nop 0
s_nop 0
s_nop 0
s_nop 0
s_nop 0
s_nop 0
s_nop 0
s_nop 0
s_nop 0
s_nop 0
s_nop 0
s_nop 0
s_nop 0
s_nop 0
s_nop 0
s_nop 0
s_nop 0
s_nop 0
s_nop 0
s_nop 0
s_nop 0
s_nop 0
s_nop 0
s_nop 0
s_nop 0
s_nop 0
s_nop 0
s_nop 0
s_nop 0
s_nop 0
s_nop 0
s_nop 0
s_nop 0
s_nop 0
s_nop 0
s_nop 0
s_nop 0
s_nop 0
s_nop 0
s_nop 0
s_nop 0
s_nop 0
s_nop 0
s_nop 0
s_nop 0
s_nop 0
s_nop 0
s_nop 0
s_nop 0
s_nop 0
s_nop 0
s_nop 0
s_nop 0
s_nop 0
s_nop 0
s_nop 0
s_nop 0
s_nop 0
s_nop 0
s_nop 0
s_nop 0
s_nop 0
s_nop 0
s_nop 0
s_nop 0
s_nop 0
s_nop 0
s_nop 0
s_nop 0
s_nop 0
s_nop 0
s_nop 0
s_nop 0
s_nop 0
s_nop 0
s_nop 0
s_nop 0
s_nop 0
s_nop 0
s_nop 0
s_nop 0
s_nop 0
s_nop 0
s_nop 0
s_nop 0
s_nop 0
s_nop 0
s_nop 0
s_nop 0
s_nop 0
s_nop 0
s_nop 0
s_nop 0
s_nop 0
s_nop 0
s_nop 0
s_nop 0
s_nop 0
s_nop 0
s_nop 0
s_nop 0
s_nop 0
s_nop 0
s_nop 0
s_nop 0
s_nop 0
s_nop 0
s_nop 0
s_nop 0
s_nop 0
s_nop 0
s_nop 0
s_nop 0
s_nop 0
s_nop 0
s_nop 0
s_nop 0
s_nop 0
s_nop 0
s_nop 0
s_nop 0
s_nop 0
s_nop 0
s_nop 0
s_nop 0
s_nop 0
s_nop 0
s_nop 0
s_nop 0
s_nop 0
s_nop 0
s_nop 0
s_nop 0
s_nop 0
s_nop 0
s_nop 0
s_nop 0
s_nop 0
s_nop 0
s_nop 0
s_nop 0
s_nop 0
s_nop 0
s_nop 0
s_nop 0
s_nop 0
s_nop 0
s_nop 0
s_nop 0
s_nop 0
s_nop 0
s_nop 0
s_nop 0
s_nop 0
s_nop 0
s_nop 0
s_nop 0
s_nop 0
s_nop 0
s_nop 0
s_nop 0
s_nop 0
s_nop 0
s_nop 0
s_nop 0
s_nop 0
s_nop 0
s_nop 0
s_nop 0
s_nop 0
s_nop 0
s_nop 0
s_nop 0
s_nop 0
s_nop 0
s_nop 0
s_nop 0
s_nop 0
s_nop 0
s_nop 0
s_nop 0
s_nop 0
s_nop 0
s_nop 0
s_nop 0
s_nop 0
s_nop 0
s_nop 0
s_nop 0
s_nop 0
s_nop 0
s_nop 0
s_nop 0
s_nop 0
s_nop 0
s_nop 0
s_nop 0
s_nop 0
s_nop 0
s_nop 0
s_nop 0
s_nop 0
s_nop 0
s_nop 0
s_nop 0
s_nop 0
s_nop 0
s_nop 0
s_nop 0
s_nop 0
s_nop 0
s_nop 0
s_nop 0
s_nop 0
s_nop 0
s_nop 0
s_nop 0
s_nop 0
s_nop 0
s_nop 0
s_nop 0
s_nop 0
s_nop 0
s_nop 0
s_nop 0
s_nop 0
s_nop 0
s_nop 0
s_nop 0
s_nop 0
s_nop 0
s_nop 0
s_nop 0
s_nop 0
s_nop 0
s_nop 0
s_nop 0
s_nop 0
s_nop 0
s_nop 0
s_nop 0
s_nop 0
s_nop 0
s_nop 0
s_nop 0
s_nop 0
s_nop 0
s_nop 0
s_nop 0
s_nop 0
s_nop 0
s_nop 0
s_nop 0
s_nop 0
s_nop 0
s_nop 0
s_nop 0
s_nop 0
s_nop 0
s_nop 0
s_nop 0
s_nop 0
s_nop 0
s_nop 0
s_nop 0
s_nop 0
s_nop 0
s_nop 0
s_nop 0
s_nop 0
s_nop 0
s_nop 0
s_nop 0
s_nop 0
s_nop 0
s_nop 0
s_nop 0
s_nop 0
s_nop 0
s_nop 0
s_nop 0
s_nop 0
s_nop 0
s_nop 0
s_nop 0
s_nop 0
s_nop 0
s_nop 0
s_nop 0
s_nop 0
s_nop 0
s_nop 0
s_nop 0
s_nop 0
s_nop 0
s_nop 0
s_nop 0
s_nop 0
s_nop 0
s_nop 0
s_nop 0
s_nop 0
s_nop 0
s_nop 0
s_nop 0
s_nop 0
s_nop 0
s_nop 0
s_nop 0
s_nop 0
s_nop 0
s_nop 0
s_nop 0
s_nop 0
s_nop 0
s_nop 0
s_nop 0
s_nop 0
s_nop 0
s_nop 0
s_nop 0
s_nop 0
s_nop 0
s_nop 0
s_nop 0
s_nop 0
s_nop 0
s_nop 0
s_nop 0
s_nop 0
s_nop 0
s_nop 0
s_nop 0
s_nop 0
s_nop 0
s_nop 0
s_nop 0
s_nop 0
s_nop 0
s_nop 0
s_nop 0
s_nop 0
s_nop 0
s_nop 0
s_nop 0
s_nop 0
s_nop 0
s_nop 0
s_nop 0
s_nop 0
s_nop 0
s_nop 0
s_nop 0
s_nop 0
s_nop 0
s_nop 0
s_nop 0
s_nop 0
s_nop 0
s_nop 0
s_nop 0
s_nop 0
LLAR_%=:
MM v[0:3],a[192:195],v[160:163],v[128:131]
s_add_u32 s64, s64, 16384
TR a[128:129],v232 offset:16384
s_add_u32 s81, s64, 896
MM v[0:3],a[196:199],v[176:179],v[0:3]
TR a[130:131],v232 offset:20480
v_max3_f32 v249, v144, v145, v148
MM v[0:3],a[200:203],v[192:195],v[0:3]
TR a[132:133],v232 offset:24576
v_max3_f32 v249, v249, v149, v152
MM v[0:3],a[204:207],v[208:211],v[0:3]
TR a[134:135],v232 offset:28672
v_max3_f32 v249, v249, v153, v156
MM v[4:7],a[220:223],v[208:211],v[128:131]
TR a[136:137],v233 offset:16384
v_max_f32 v249, v249, v157
MM v[4:7],a[216:219],v[192:195],v[4:7]
TR a[138:139],v233 offset:20480
v_cmp_lt_f32 vcc, s71, v249
MM v[4:7],a[212:215],v[176:179],v[4:7]
TR a[140:141],v233 offset:24576
TR a[142:143],v233 offset:28672
MM v[4:7],a[208:211],v[160:163],v[4:7]
s_cbranch_vccnz LSITE9_%=
LBACK9_%=:
MM v[12:15],a[208:211],v[164:167],v[132:135]
s_lshl_b32 s17, s16, 2
EX v0,v0
MM v[12:15],a[212:215],v[180:183],v[12:15]
s_add_u32 s17, s17, %[swave]
EX v1,v1
MM v[12:15],a[216:219],v[196:199],v[12:15]
s_sub_u32 s90, s16, 108
TR a[144:145],v232 offset:16896
CV v0,v0,v1
MM v[12:15],a[220:223],v[212:215],v[12:15]
s_min_u32 s90, s90, s16
EX v2,v2
MM v[8:11],a[204:207],v[212:215],v[132:135]
s_lshl_b32 s48, s90, 5
EX v3,v3
MM v[8:11],a[200:203],v[196:199],v[8:11]
s_add_u32 s76, %[slds], 155648
v_add_u32 v250, s76, v248
CV v1,v2,v3
MM v[8:11],a[196:199],v[180:183],v[8:11]
s_lshl_b32 s77, s17, 4
TR a[146:147],v232 offset:20992
TR a[148:149],v232 offset:25088
MM v[8:11],a[192:195],v[164:167],v[8:11]
s_add_u32 s76, s76, s77
TR a[150:151],v232 offset:29184
v_mov_b32 v251, s76
MM v[16:19],a[192:195],v[168:171],v[136:139]
RD v[124:127],v251 offset:0
RD v[96:99],v250 offset:0
MM v[16:19],a[196:199],v[184:187],v[16:19]
RD v[100:103],v250 offset:1024
RD v[104:107],v250 offset:2048
MM v[16:19],a[200:203],v[200:203],v[16:19]
RD v[108:111],v250 offset:3072
RD v[112:115],v250 offset:4096
MM v[16:19],a[204:207],v[216:219],v[16:19]
RD v[116:119],v250 offset:5120
RD v[120:123],v250 offset:6144
MM v[20:23],a[220:223],v[216:219],v[136:139]
EX v4,v4
MM v[20:23],a[216:219],v[200:203],v[20:23]
EX v5,v5
MM v[20:23],a[212:215],v[184:187],v[20:23]
s_waitcnt lgkmcnt(7)
v_readfirstlane_b32 s52, v124
v_readfirstlane_b32 s53, v125
MM v[20:23],a[208:211],v[168:171],v[20:23]
v_readfirstlane_b32 s54, v126
v_readfirstlane_b32 s55, v127
EX v6,v6
MM v[28:31],a[208:211],v[172:175],v[140:143]
RD v[124:127],v250 offset:7168
TR a[152:153],v233 offset:16896
MM v[28:31],a[212:215],v[188:191],v[28:31]
s_waitcnt lgkmcnt(2)
v_max3_u32 v253, v96, v97, v98
v_max3_u32 v251, v99, v100, v101
EX v7,v7
MM v[28:31],a[216:219],v[204:207],v[28:31]
v_max3_u32 v253, v253, v102, v103
v_max3_u32 v251, v251, v104, v105
MM v[28:31],a[220:223],v[220:223],v[28:31]
v_max3_u32 v253, v253, v106, v107
v_max3_u32 v251, v251, v108, v109
EX v8,v8
MM v[24:27],a[204:207],v[220:223],v[140:143]
v_max3_u32 v253, v253, v110, v111
v_max3_u32 v251, v251, v112, v113
MM v[24:27],a[200:203],v[204:207],v[24:27]
v_max3_u32 v253, v253, v114, v115
v_max3_u32 v251, v251, v116, v117
EX v9,v9
MM v[24:27],a[196:199],v[188:191],v[24:27]
TR a[154:155],v233 offset:20992
v_max3_u32 v253, v253, v118, v119
v_max3_u32 v251, v251, v120, v121
MM v[24:27],a[192:195],v[172:175],v[24:27]
v_max3_u32 v253, v253, v122, v123
CV v2,v4,v5
EX v10,v10
MM v[32:35],a[224:227],v[160:163],v[128:131]
s_waitcnt lgkmcnt(2)
TR a[156:157],v233 offset:25088
v_max3_u32 v251, v251, v124, v125
v_max3_u32 v253, v253, v126, v127
EX v11,v11
MM v[32:35],a[228:231],v[176:179],v[32:35]
v_max_u32 v253, v253, v251
v_cmp_lt_u32 s[60:61], s52, v253
MM v[32:35],a[232:235],v[192:195],v[32:35]
TR a[158:159],v233 offset:29184
v_cmp_lt_u32 s[62:63], s53, v253
v_cmp_lt_u32 s[56:57], s54, v253
EX v12,v12
MM v[32:35],a[236:239],v[208:211],v[32:35]
v_cmp_lt_u32 s[58:59], s55, v253
CV v3,v6,v7
MM v[36:39],a[252:255],v[208:211],v[128:131]
TR a[160:161],v232 offset:17408
EX v13,v13
MM v[36:39],a[248:251],v[192:195],v[36:39]
s_bcnt1_i32_b64 s60, s[60:61]
TR a[162:163],v232 offset:21504
CV v8,v8,v9
MM v[36:39],a[244:247],v[176:179],v[36:39]
s_bcnt1_i32_b64 s62, s[62:63]
EX v14,v14
MM v[36:39],a[240:243],v[160:163],v[36:39]
s_bcnt1_i32_b64 s56, s[56:57]
TR a[164:165],v232 offset:25600
TR a[166:167],v232 offset:29696
MM v[44:47],a[240:243],v[164:167],v[132:135]
s_bcnt1_i32_b64 s58, s[58:59]
EX v15,v15
MM v[44:47],a[244:247],v[180:183],v[44:47]
s_min_u32 s76, s60, s62
EX v16,v16
MM v[44:47],a[248:251],v[196:199],v[44:47]
s_min_u32 s77, s56, s58
TR a[168:169],v233 offset:17408
CV v9,v10,v11
MM v[44:47],a[252:255],v[212:215],v[44:47]
s_min_u32 s76, s76, s77
s_cmp_lt_u32 s76, 8
s_cbranch_scc1 LRK_%=
LRKb_%=:
EX v17,v17
MM v[40:43],a[236:239],v[212:215],v[132:135]
v_lshrrev_b32 v252, 6, v248
v_mul_u32_u24 v252, 3456, v252
MM v[40:43],a[232:235],v[196:199],v[40:43]
TR a[170:171],v233 offset:21504
v_and_b32 v251, 48, v248
)ASM"
R"ASM(
v_add_u32 v252, v252, v251
EX v18,v18
MM v[40:43],a[228:231],v[180:183],v[40:43]
s_and_b32 s90, %[swave], 1
TR a[172:173],v233 offset:25600
CV v10,v12,v13
MM v[40:43],a[224:227],v[164:167],v[40:43]
s_mul_i32 s90, s90, 276480
EX v19,v19
MM v[48:51],a[224:227],v[168:171],v[136:139]
s_bfe_u32 s91, s48, 131077
TR a[174:175],v233 offset:29696
CV v11,v14,v15
MM v[48:51],a[228:231],v[184:187],v[48:51]
s_mul_i32 s91, s91, 552960
EX v20,v20
MM v[48:51],a[232:235],v[200:203],v[48:51]
s_add_u32 s90, s90, s91
TR a[176:177],v232 offset:17920
TR a[178:179],v232 offset:22016
MM v[48:51],a[236:239],v[216:219],v[48:51]
s_and_b32 s91, s48, 0xffffff80
EX v21,v21
MM v[52:55],a[252:255],v[216:219],v[136:139]
s_add_u32 s90, s90, s91
EX v22,v22
MM v[52:55],a[248:251],v[200:203],v[52:55]
s_and_b32 s91, s48, 16
TR a[180:181],v232 offset:26112
CV v16,v16,v17
MM v[52:55],a[244:247],v[184:187],v[52:55]
s_lshl_b32 s91, s91, 2
EX v23,v23
MM v[52:55],a[240:243],v[168:171],v[52:55]
s_add_u32 s66, s90, s91
TR a[182:183],v232 offset:30208
CV v17,v18,v19
MM v[60:63],a[240:243],v[172:175],v[140:143]
s_mul_i32 s91, %[swave], 5120
EX v24,v24
MM v[60:63],a[244:247],v[188:191],v[60:63]
s_add_u32 s91, s91, %[slds]
TR a[184:185],v233 offset:17920
TR a[186:187],v233 offset:22016
MM v[60:63],a[248:251],v[204:207],v[60:63]
s_add_u32 s67, s91, 131072
TR a[188:189],v233 offset:26112
TR a[190:191],v233 offset:30208
EX v25,v25
MM v[60:63],a[252:255],v[220:223],v[60:63]
EX v26,v26
MM v[56:59],a[236:239],v[220:223],v[140:143]
EX v27,v27
MM v[56:59],a[232:235],v[204:207],v[56:59]
EX v28,v28
MM v[56:59],a[228:231],v[188:191],v[56:59]
EX v29,v29
CV v18,v20,v21
MM v[56:59],a[224:227],v[172:175],v[56:59]
s_waitcnt lgkmcnt(0)
s_barrier
MM a[0:3],a[128:131],v[64:67],a[0:3]
s_add_u32 m0, s65, 16384
CV v19,v22,v23
CV v24,v24,v25
MM a[0:3],a[132:135],v[68:71],a[0:3]
CV v25,v26,v27
CV v26,v28,v29
MM a[8:11],a[132:135],v[76:79],a[8:11]
EX v30,v30
MM a[8:11],a[128:131],v[72:75],a[8:11]
EX v31,v31
MM a[16:19],a[128:131],v[80:83],a[16:19]
RD a[192:195],v224 offset:49152
CV v27,v30,v31
MM a[16:19],a[132:135],v[84:87],a[16:19]
EX v32,v32
MM a[24:27],a[132:135],v[92:95],a[24:27]
buffer_load_dwordx4 v228, s[8:11], s64 offen offset:0 lds
MM a[24:27],a[128:131],v[88:91],a[24:27]
EX v33,v33
MM a[28:31],a[136:139],v[88:91],a[28:31]
RD a[196:199],v225 offset:49152
CV v4,v32,v33
MM a[28:31],a[140:143],v[92:95],a[28:31]
EX v34,v34
MM a[20:23],a[140:143],v[84:87],a[20:23]
EX v35,v35
MM a[20:23],a[136:139],v[80:83],a[20:23]
RD a[200:203],v226 offset:49152
CV v5,v34,v35
MM a[12:15],a[136:139],v[72:75],a[12:15]
EX v36,v36
MM a[12:15],a[140:143],v[76:79],a[12:15]
s_nop 0
LPG10_%=:
s_cbranch_execz LTR0_%=
buffer_load_dwordx4 v229, s[8:11], s64 offen offset:1024 lds
MM a[4:7],a[140:143],v[68:71],a[4:7]
EX v37,v37
MM a[4:7],a[136:139],v[64:67],a[4:7]
RD a[204:207],v227 offset:49152
CV v6,v36,v37
MM a[32:35],a[144:147],v[64:67],a[32:35]
EX v38,v38
MM a[32:35],a[148:151],v[68:71],a[32:35]
EX v39,v39
MM a[40:43],a[148:151],v[76:79],a[40:43]
RD a[208:211],v224 offset:53248
CV v7,v38,v39
MM a[40:43],a[144:147],v[72:75],a[40:43]
EX v40,v40
MM a[48:51],a[144:147],v[80:83],a[48:51]
buffer_load_dwordx4 v230, s[8:11], s64 offen offset:2048 lds
MM a[48:51],a[148:151],v[84:87],a[48:51]
EX v41,v41
MM a[56:59],a[148:151],v[92:95],a[56:59]
RD a[212:215],v225 offset:53248
CV v12,v40,v41
MM a[56:59],a[144:147],v[88:91],a[56:59]
EX v42,v42
MM a[60:63],a[152:155],v[88:91],a[60:63]
EX v43,v43
MM a[60:63],a[156:159],v[92:95],a[60:63]
RD a[216:219],v226 offset:53248
CV v13,v42,v43
MM a[52:55],a[156:159],v[84:87],a[52:55]
EX v44,v44
MM a[52:55],a[152:155],v[80:83],a[52:55]
buffer_load_dwordx4 v231, s[8:11], s64 offen offset:3072 lds
MM a[44:47],a[152:155],v[72:75],a[44:47]
s_add_u32 m0, s65, 81920
EX v45,v45
MM a[44:47],a[156:159],v[76:79],a[44:47]
RD a[220:223],v227 offset:53248
CV v14,v44,v45
MM a[36:39],a[156:159],v[68:71],a[36:39]
EX v46,v46
MM a[36:39],a[152:155],v[64:67],a[36:39]
RD a[224:227],v224 offset:57344
RD a[228:231],v225 offset:57344
MM a[64:67],a[160:163],v[64:67],a[64:67]
EX v47,v47
MM a[64:67],a[164:167],v[68:71],a[64:67]
RD a[232:235],v226 offset:57344
CV v15,v46,v47
MM a[72:75],a[164:167],v[76:79],a[72:75]
EX v48,v48
MM a[72:75],a[160:163],v[72:75],a[72:75]
buffer_load_dwordx4 v235, s[12:15], s81 offen offset:0 lds
MM a[80:83],a[160:163],v[80:83],a[80:83]
s_add_u32 m0, s67, 0
s_add_u32 s85, s66, 0
buffer_load_dwordx4 v252, s[36:39], s85 offen offset:0 lds
MM a[80:83],a[164:167],v[84:87],a[80:83]
EX v49,v49
MM a[88:91],a[164:167],v[92:95],a[88:91]
EX v50,v50
MM a[88:91],a[160:163],v[88:91],a[88:91]
RD a[236:239],v227 offset:57344
CV v20,v48,v49
MM a[92:95],a[168:171],v[88:91],a[92:95]
EX v51,v51
MM a[92:95],a[172:175],v[92:95],a[92:95]
RD a[240:243],v224 offset:61440
CV v21,v50,v51
MM a[84:87],a[172:175],v[84:87],a[84:87]
EX v52,v52
MM a[84:87],a[168:171],v[80:83],a[84:87]
EX v53,v53
MM a[76:79],a[168:171],v[72:75],a[76:79]
RD a[244:247],v225 offset:61440
CV v22,v52,v53
MM a[76:79],a[172:175],v[76:79],a[76:79]
EX v54,v54
MM a[68:71],a[172:175],v[68:71],a[68:71]
s_add_u32 m0, s65, 81920
s_nop 0
buffer_load_dwordx4 v235, s[12:15], s64 offen offset:1024 lds
MM a[68:71],a[168:171],v[64:67],a[68:71]
EX v55,v55
MM a[96:99],a[176:179],v[64:67],a[96:99]
RD a[248:251],v226 offset:61440
CV v23,v54,v55
MM a[96:99],a[180:183],v[68:71],a[96:99]
EX v56,v56
MM a[104:107],a[180:183],v[76:79],a[104:107]
EX v57,v57
MM a[104:107],a[176:179],v[72:75],a[104:107]
RD a[252:255],v227 offset:61440
CV v28,v56,v57
MM a[112:115],a[176:179],v[80:83],a[112:115]
buffer_load_dwordx4 v235, s[12:15], s81 offen offset:2048 lds
MM a[112:115],a[180:183],v[84:87],a[112:115]
EX v58,v58
MM a[120:123],a[180:183],v[92:95],a[120:123]
EX v59,v59
MM a[120:123],a[176:179],v[88:91],a[120:123]
EX v60,v60
MM a[124:127],a[184:187],v[88:91],a[124:127]
v_add_f32 v244, v144, v145
v_add_f32 v245, v148, v149
MM a[124:127],a[188:191],v[92:95],a[124:127]
v_add_f32 v246, v152, v153
v_add_f32 v247, v156, v157
MM a[116:119],a[188:191],v[84:87],a[116:119]
EX v61,v61
MM a[116:119],a[184:187],v[80:83],a[116:119]
buffer_load_dwordx4 v235, s[12:15], s64 offen offset:3072 lds
MM a[108:111],a[184:187],v[72:75],a[108:111]
EX v62,v62
MM a[108:111],a[188:191],v[76:79],a[108:111]
EX v63,v63
MM a[100:103],a[188:191],v[68:71],a[100:103]
CV v29,v58,v59
CV v30,v60,v61
MM a[100:103],a[184:187],v[64:67],a[100:103]
CV v31,v62,v63
v_smfmac_f32_16x16x64_bf16 v[144:147], v[236:239], v[0:7], v234
v_smfmac_f32_16x16x64_bf16 v[148:151], v[236:239], v[8:15], v234
v_smfmac_f32_16x16x64_bf16 v[152:155], v[236:239], v[16:23], v234
v_smfmac_f32_16x16x64_bf16 v[156:159], v[236:239], v[24:31], v234
s_waitcnt lgkmcnt(0)
MM v[64:67],a[192:195],v[160:163],v[128:131]
s_add_u32 s64, s64, 16384
TR a[128:129],v232 offset:32768
s_add_u32 s81, s64, 896
MM v[64:67],a[196:199],v[176:179],v[64:67]
TR a[130:131],v232 offset:36864
v_max3_f32 v249, v144, v145, v148
MM v[64:67],a[200:203],v[192:195],v[64:67]
TR a[132:133],v232 offset:40960
v_max3_f32 v249, v249, v149, v152
MM v[64:67],a[204:207],v[208:211],v[64:67]
TR a[134:135],v232 offset:45056
v_max3_f32 v249, v249, v153, v156
MM v[68:71],a[220:223],v[208:211],v[128:131]
TR a[136:137],v233 offset:32768
v_max_f32 v249, v249, v157
MM v[68:71],a[216:219],v[192:195],v[68:71]
TR a[138:139],v233 offset:36864
v_cmp_lt_f32 vcc, s71, v249
MM v[68:71],a[212:215],v[176:179],v[68:71]
TR a[140:141],v233 offset:40960
TR a[142:143],v233 offset:45056
MM v[68:71],a[208:211],v[160:163],v[68:71]
s_cbranch_vccnz LSITE10_%=
LBACK10_%=:
MM v[76:79],a[208:211],v[164:167],v[132:135]
s_add_u32 m0, s67, 1024
s_add_u32 s85, s66, 55296
buffer_load_dwordx4 v252, s[36:39], s85 offen offset:0 lds
MM v[76:79],a[212:215],v[180:183],v[76:79]
EX v64,v64
MM v[76:79],a[216:219],v[196:199],v[76:79]
EX v65,v65
MM v[76:79],a[220:223],v[212:215],v[76:79]
TR a[144:145],v232 offset:33280
CV v64,v64,v65
MM v[72:75],a[204:207],v[212:215],v[132:135]
EX v66,v66
MM v[72:75],a[200:203],v[196:199],v[72:75]
EX v67,v67
MM v[72:75],a[196:199],v[180:183],v[72:75]
TR a[146:147],v232 offset:37376
CV v65,v66,v67
MM v[72:75],a[192:195],v[164:167],v[72:75]
EX v68,v68
MM v[80:83],a[192:195],v[168:171],v[136:139]
TR a[148:149],v232 offset:41472
TR a[150:151],v232 offset:45568
MM v[80:83],a[196:199],v[184:187],v[80:83]
EX v69,v69
MM v[80:83],a[200:203],v[200:203],v[80:83]
TR a[152:153],v233 offset:33280
CV v66,v68,v69
MM v[80:83],a[204:207],v[216:219],v[80:83]
EX v70,v70
MM v[84:87],a[220:223],v[216:219],v[136:139]
EX v71,v71
MM v[84:87],a[216:219],v[200:203],v[84:87]
TR a[154:155],v233 offset:37376
CV v67,v70,v71
MM v[84:87],a[212:215],v[184:187],v[84:87]
EX v72,v72
MM v[84:87],a[208:211],v[168:171],v[84:87]
TR a[156:157],v233 offset:41472
TR a[158:159],v233 offset:45568
MM v[92:95],a[208:211],v[172:175],v[140:143]
EX v73,v73
MM v[92:95],a[212:215],v[188:191],v[92:95]
TR a[160:161],v232 offset:33792
CV v72,v72,v73
MM v[92:95],a[216:219],v[204:207],v[92:95]
EX v74,v74
MM v[92:95],a[220:223],v[220:223],v[92:95]
EX v75,v75
MM v[88:91],a[204:207],v[220:223],v[140:143]
TR a[162:163],v232 offset:37888
CV v73,v74,v75
MM v[88:91],a[200:203],v[204:207],v[88:91]
TR a[164:165],v232 offset:41984
TR a[166:167],v232 offset:46080
MM v[88:91],a[196:199],v[188:191],v[88:91]
EX v76,v76
MM v[88:91],a[192:195],v[172:175],v[88:91]
EX v77,v77
MM v[96:99],a[224:227],v[160:163],v[128:131]
TR a[168:169],v233 offset:33792
CV v74,v76,v77
MM v[96:99],a[228:231],v[176:179],v[96:99]
EX v78,v78
MM v[96:99],a[232:235],v[192:195],v[96:99]
TR a[170:171],v233 offset:37888
TR a[172:173],v233 offset:41984
MM v[96:99],a[236:239],v[208:211],v[96:99]
EX v79,v79
MM v[100:103],a[252:255],v[208:211],v[128:131]
TR a[174:175],v233 offset:46080
CV v75,v78,v79
MM v[100:103],a[248:251],v[192:195],v[100:103]
EX v80,v80
MM v[100:103],a[244:247],v[176:179],v[100:103]
EX v81,v81
MM v[100:103],a[240:243],v[160:163],v[100:103]
TR a[176:177],v232 offset:34304
CV v80,v80,v81
MM v[108:111],a[240:243],v[164:167],v[132:135]
s_add_u32 m0, s67, 2048
s_add_u32 s85, s66, 110592
buffer_load_dwordx4 v252, s[36:39], s85 offen offset:0 lds
MM v[108:111],a[244:247],v[180:183],v[108:111]
EX v82,v82
MM v[108:111],a[248:251],v[196:199],v[108:111]
TR a[178:179],v232 offset:38400
TR a[180:181],v232 offset:42496
MM v[108:111],a[252:255],v[212:215],v[108:111]
EX v83,v83
MM v[104:107],a[236:239],v[212:215],v[132:135]
TR a[182:183],v232 offset:46592
CV v81,v82,v83
MM v[104:107],a[232:235],v[196:199],v[104:107]
EX v84,v84
MM v[104:107],a[228:231],v[180:183],v[104:107]
EX v85,v85
MM v[104:107],a[224:227],v[164:167],v[104:107]
TR a[184:185],v233 offset:34304
CV v82,v84,v85
MM v[112:115],a[224:227],v[168:171],v[136:139]
EX v86,v86
MM v[112:115],a[228:231],v[184:187],v[112:115]
TR a[186:187],v233 offset:38400
TR a[188:189],v233 offset:42496
MM v[112:115],a[232:235],v[200:203],v[112:115]
EX v87,v87
MM v[112:115],a[236:239],v[216:219],v[112:115]
TR a[190:191],v233 offset:46592
CV v83,v86,v87
MM v[116:119],a[252:255],v[216:219],v[136:139]
EX v88,v88
MM v[116:119],a[248:251],v[200:203],v[116:119]
EX v89,v89
MM v[116:119],a[244:247],v[184:187],v[116:119]
EX v90,v90
MM v[116:119],a[240:243],v[168:171],v[116:119]
EX v91,v91
MM v[124:127],a[240:243],v[172:175],v[140:143]
EX v92,v92
MM v[124:127],a[244:247],v[188:191],v[124:127]
EX v93,v93
MM v[124:127],a[248:251],v[204:207],v[124:127]
EX v94,v94
MM v[124:127],a[252:255],v[220:223],v[124:127]
EX v95,v95
MM v[120:123],a[236:239],v[220:223],v[140:143]
EX v96,v96
MM v[120:123],a[232:235],v[204:207],v[120:123]
EX v97,v97
MM v[120:123],a[228:231],v[188:191],v[120:123]
EX v98,v98
MM v[120:123],a[224:227],v[172:175],v[120:123]
s_waitcnt vmcnt(15) lgkmcnt(0)
s_barrier
MM a[0:3],a[128:131],v[0:3],a[0:3]
s_add_u32 m0, s65, 32768
CV v88,v88,v89
CV v89,v90,v91
MM a[0:3],a[132:135],v[4:7],a[0:3]
RD a[192:195],v224 offset:0
CV v90,v92,v93
MM a[8:11],a[132:135],v[12:15],a[8:11]
RD a[196:199],v225 offset:0
CV v91,v94,v95
MM a[8:11],a[128:131],v[8:11],a[8:11]
buffer_load_dwordx4 v228, s[8:11], s64 offen offset:0 lds
MM a[16:19],a[128:131],v[16:19],a[16:19]
)ASM"
R"ASM(
RD a[200:203],v226 offset:0
CV v68,v96,v97
MM a[16:19],a[132:135],v[20:23],a[16:19]
buffer_load_dwordx4 v229, s[8:11], s64 offen offset:1024 lds
MM a[24:27],a[132:135],v[28:31],a[24:27]
RD a[204:207],v227 offset:0
RD a[208:211],v224 offset:4096
MM a[24:27],a[128:131],v[24:27],a[24:27]
EX v99,v99
MM a[28:31],a[136:139],v[24:27],a[28:31]
RD a[212:215],v225 offset:4096
CV v69,v98,v99
MM a[28:31],a[140:143],v[28:31],a[28:31]
EX v100,v100
MM a[20:23],a[140:143],v[20:23],a[20:23]
buffer_load_dwordx4 v230, s[8:11], s64 offen offset:2048 lds
MM a[20:23],a[136:139],v[16:19],a[20:23]
EX v101,v101
MM a[12:15],a[136:139],v[8:11],a[12:15]
RD a[216:219],v226 offset:4096
CV v70,v100,v101
MM a[12:15],a[140:143],v[12:15],a[12:15]
EX v102,v102
MM a[4:7],a[140:143],v[4:7],a[4:7]
EX v103,v103
MM a[4:7],a[136:139],v[0:3],a[4:7]
RD a[220:223],v227 offset:4096
CV v71,v102,v103
MM a[32:35],a[144:147],v[0:3],a[32:35]
EX v104,v104
MM a[32:35],a[148:151],v[4:7],a[32:35]
buffer_load_dwordx4 v231, s[8:11], s64 offen offset:3072 lds
MM a[40:43],a[148:151],v[12:15],a[40:43]
s_add_u32 m0, s65, 98304
EX v105,v105
MM a[40:43],a[144:147],v[8:11],a[40:43]
RD a[224:227],v224 offset:8192
CV v76,v104,v105
MM a[48:51],a[144:147],v[16:19],a[48:51]
EX v106,v106
MM a[48:51],a[148:151],v[20:23],a[48:51]
EX v107,v107
MM a[56:59],a[148:151],v[28:31],a[56:59]
RD a[228:231],v225 offset:8192
CV v77,v106,v107
MM a[56:59],a[144:147],v[24:27],a[56:59]
EX v108,v108
MM a[60:63],a[152:155],v[24:27],a[60:63]
buffer_load_dwordx4 v235, s[12:15], s81 offen offset:0 lds
MM a[60:63],a[156:159],v[28:31],a[60:63]
EX v109,v109
MM a[52:55],a[156:159],v[20:23],a[52:55]
RD a[232:235],v226 offset:8192
CV v78,v108,v109
MM a[52:55],a[152:155],v[16:19],a[52:55]
EX v110,v110
MM a[44:47],a[152:155],v[8:11],a[44:47]
EX v111,v111
MM a[44:47],a[156:159],v[12:15],a[44:47]
RD a[236:239],v227 offset:8192
CV v79,v110,v111
MM a[36:39],a[156:159],v[4:7],a[36:39]
buffer_load_dwordx4 v235, s[12:15], s64 offen offset:1024 lds
MM a[36:39],a[152:155],v[0:3],a[36:39]
EX v112,v112
MM a[64:67],a[160:163],v[0:3],a[64:67]
EX v113,v113
MM a[64:67],a[164:167],v[4:7],a[64:67]
RD a[240:243],v224 offset:12288
CV v84,v112,v113
MM a[72:75],a[164:167],v[12:15],a[72:75]
EX v114,v114
MM a[72:75],a[160:163],v[8:11],a[72:75]
RD a[244:247],v225 offset:12288
RD a[248:251],v226 offset:12288
MM a[80:83],a[160:163],v[16:19],a[80:83]
s_add_u32 m0, s67, 3072
s_add_u32 s85, s66, 165888
buffer_load_dwordx4 v252, s[36:39], s85 offen offset:0 lds
MM a[80:83],a[164:167],v[20:23],a[80:83]
EX v115,v115
MM a[88:91],a[164:167],v[28:31],a[88:91]
RD a[252:255],v227 offset:12288
CV v85,v114,v115
MM a[88:91],a[160:163],v[24:27],a[88:91]
EX v116,v116
MM a[92:95],a[168:171],v[24:27],a[92:95]
EX v117,v117
MM a[92:95],a[172:175],v[28:31],a[92:95]
s_add_u32 m0, s65, 98304
s_nop 0
buffer_load_dwordx4 v235, s[12:15], s81 offen offset:2048 lds
MM a[84:87],a[172:175],v[20:23],a[84:87]
EX v118,v118
MM a[84:87],a[168:171],v[16:19],a[84:87]
EX v119,v119
MM a[76:79],a[168:171],v[8:11],a[76:79]
buffer_load_dwordx4 v235, s[12:15], s64 offen offset:3072 lds
MM a[76:79],a[172:175],v[12:15],a[76:79]
EX v120,v120
MM a[68:71],a[172:175],v[4:7],a[68:71]
EX v121,v121
MM a[68:71],a[168:171],v[0:3],a[68:71]
EX v122,v122
MM a[96:99],a[176:179],v[0:3],a[96:99]
EX v123,v123
MM a[96:99],a[180:183],v[4:7],a[96:99]
EX v124,v124
MM a[104:107],a[180:183],v[12:15],a[104:107]
EX v125,v125
MM a[104:107],a[176:179],v[8:11],a[104:107]
EX v126,v126
MM a[112:115],a[176:179],v[16:19],a[112:115]
EX v127,v127
MM a[112:115],a[180:183],v[20:23],a[112:115]
CV v86,v116,v117
CV v87,v118,v119
MM a[120:123],a[180:183],v[28:31],a[120:123]
CV v92,v120,v121
CV v93,v122,v123
MM a[120:123],a[176:179],v[24:27],a[120:123]
CV v94,v124,v125
CV v95,v126,v127
MM a[124:127],a[184:187],v[24:27],a[124:127]
v_add_f32 v244, v144, v145
v_add_f32 v245, v148, v149
MM a[124:127],a[188:191],v[28:31],a[124:127]
v_add_f32 v246, v152, v153
v_add_f32 v247, v156, v157
MM a[116:119],a[188:191],v[20:23],a[116:119]
MM a[116:119],a[184:187],v[16:19],a[116:119]
MM a[108:111],a[184:187],v[8:11],a[108:111]
MM a[108:111],a[188:191],v[12:15],a[108:111]
MM a[100:103],a[188:191],v[4:7],a[100:103]
MM a[100:103],a[184:187],v[0:3],a[100:103]
v_smfmac_f32_16x16x64_bf16 v[144:147], v[236:239], v[64:71], v234
v_smfmac_f32_16x16x64_bf16 v[148:151], v[236:239], v[72:79], v234
v_smfmac_f32_16x16x64_bf16 v[152:155], v[236:239], v[80:87], v234
v_smfmac_f32_16x16x64_bf16 v[156:159], v[236:239], v[88:95], v234
s_waitcnt lgkmcnt(0)
MM v[0:3],a[192:195],v[160:163],v[128:131]
s_add_u32 s64, s64, 16384
TR a[128:129],v232 offset:49152
s_add_u32 s81, s64, 896
MM v[0:3],a[196:199],v[176:179],v[0:3]
TR a[130:131],v232 offset:53248
v_max3_f32 v249, v144, v145, v148
MM v[0:3],a[200:203],v[192:195],v[0:3]
TR a[132:133],v232 offset:57344
v_max3_f32 v249, v249, v149, v152
MM v[0:3],a[204:207],v[208:211],v[0:3]
TR a[134:135],v232 offset:61440
v_max3_f32 v249, v249, v153, v156
MM v[4:7],a[220:223],v[208:211],v[128:131]
TR a[136:137],v233 offset:49152
v_max_f32 v249, v249, v157
MM v[4:7],a[216:219],v[192:195],v[4:7]
TR a[138:139],v233 offset:53248
v_cmp_lt_f32 vcc, s71, v249
MM v[4:7],a[212:215],v[176:179],v[4:7]
TR a[140:141],v233 offset:57344
TR a[142:143],v233 offset:61440
MM v[4:7],a[208:211],v[160:163],v[4:7]
s_cbranch_vccnz LSITE11_%=
LBACK11_%=:
MM v[12:15],a[208:211],v[164:167],v[132:135]
s_add_u32 m0, s67, 4096
s_add_u32 s85, s66, 221184
buffer_load_dwordx4 v252, s[36:39], s85 offen offset:0 lds
MM v[12:15],a[212:215],v[180:183],v[12:15]
EX v0,v0
MM v[12:15],a[216:219],v[196:199],v[12:15]
EX v1,v1
MM v[12:15],a[220:223],v[212:215],v[12:15]
TR a[144:145],v232 offset:49664
CV v0,v0,v1
MM v[8:11],a[204:207],v[212:215],v[132:135]
EX v2,v2
MM v[8:11],a[200:203],v[196:199],v[8:11]
EX v3,v3
MM v[8:11],a[196:199],v[180:183],v[8:11]
TR a[146:147],v232 offset:53760
CV v1,v2,v3
MM v[8:11],a[192:195],v[164:167],v[8:11]
EX v4,v4
MM v[16:19],a[192:195],v[168:171],v[136:139]
TR a[148:149],v232 offset:57856
TR a[150:151],v232 offset:61952
MM v[16:19],a[196:199],v[184:187],v[16:19]
EX v5,v5
MM v[16:19],a[200:203],v[200:203],v[16:19]
TR a[152:153],v233 offset:49664
CV v2,v4,v5
MM v[16:19],a[204:207],v[216:219],v[16:19]
EX v6,v6
MM v[20:23],a[220:223],v[216:219],v[136:139]
EX v7,v7
MM v[20:23],a[216:219],v[200:203],v[20:23]
TR a[154:155],v233 offset:53760
CV v3,v6,v7
MM v[20:23],a[212:215],v[184:187],v[20:23]
EX v8,v8
MM v[20:23],a[208:211],v[168:171],v[20:23]
TR a[156:157],v233 offset:57856
TR a[158:159],v233 offset:61952
MM v[28:31],a[208:211],v[172:175],v[140:143]
EX v9,v9
MM v[28:31],a[212:215],v[188:191],v[28:31]
TR a[160:161],v232 offset:50176
CV v8,v8,v9
MM v[28:31],a[216:219],v[204:207],v[28:31]
EX v10,v10
MM v[28:31],a[220:223],v[220:223],v[28:31]
EX v11,v11
MM v[24:27],a[204:207],v[220:223],v[140:143]
TR a[162:163],v232 offset:54272
CV v9,v10,v11
MM v[24:27],a[200:203],v[204:207],v[24:27]
TR a[164:165],v232 offset:58368
TR a[166:167],v232 offset:62464
MM v[24:27],a[196:199],v[188:191],v[24:27]
EX v12,v12
MM v[24:27],a[192:195],v[172:175],v[24:27]
EX v13,v13
MM v[32:35],a[224:227],v[160:163],v[128:131]
TR a[168:169],v233 offset:50176
CV v10,v12,v13
MM v[32:35],a[228:231],v[176:179],v[32:35]
EX v14,v14
MM v[32:35],a[232:235],v[192:195],v[32:35]
TR a[170:171],v233 offset:54272
TR a[172:173],v233 offset:58368
MM v[32:35],a[236:239],v[208:211],v[32:35]
EX v15,v15
MM v[36:39],a[252:255],v[208:211],v[128:131]
TR a[174:175],v233 offset:62464
CV v11,v14,v15
MM v[36:39],a[248:251],v[192:195],v[36:39]
EX v16,v16
MM v[36:39],a[244:247],v[176:179],v[36:39]
EX v17,v17
MM v[36:39],a[240:243],v[160:163],v[36:39]
TR a[176:177],v232 offset:50688
CV v16,v16,v17
MM v[44:47],a[240:243],v[164:167],v[132:135]
EX v18,v18
MM v[44:47],a[244:247],v[180:183],v[44:47]
TR a[178:179],v232 offset:54784
TR a[180:181],v232 offset:58880
MM v[44:47],a[248:251],v[196:199],v[44:47]
EX v19,v19
MM v[44:47],a[252:255],v[212:215],v[44:47]
TR a[182:183],v232 offset:62976
CV v17,v18,v19
MM v[40:43],a[236:239],v[212:215],v[132:135]
EX v20,v20
MM v[40:43],a[232:235],v[196:199],v[40:43]
EX v21,v21
MM v[40:43],a[228:231],v[180:183],v[40:43]
TR a[184:185],v233 offset:50688
CV v18,v20,v21
MM v[40:43],a[224:227],v[164:167],v[40:43]
EX v22,v22
MM v[48:51],a[224:227],v[168:171],v[136:139]
TR a[186:187],v233 offset:54784
TR a[188:189],v233 offset:58880
MM v[48:51],a[228:231],v[184:187],v[48:51]
EX v23,v23
MM v[48:51],a[232:235],v[200:203],v[48:51]
TR a[190:191],v233 offset:62976
CV v19,v22,v23
MM v[48:51],a[236:239],v[216:219],v[48:51]
EX v24,v24
MM v[52:55],a[252:255],v[216:219],v[136:139]
EX v25,v25
MM v[52:55],a[248:251],v[200:203],v[52:55]
EX v26,v26
MM v[52:55],a[244:247],v[184:187],v[52:55]
EX v27,v27
MM v[52:55],a[240:243],v[168:171],v[52:55]
EX v28,v28
MM v[60:63],a[240:243],v[172:175],v[140:143]
EX v29,v29
MM v[60:63],a[244:247],v[188:191],v[60:63]
EX v30,v30
MM v[60:63],a[248:251],v[204:207],v[60:63]
EX v31,v31
LPG11_%=:
s_cbranch_execz LTR0_%=
MM v[60:63],a[252:255],v[220:223],v[60:63]
EX v32,v32
MM v[56:59],a[236:239],v[220:223],v[140:143]
EX v33,v33
MM v[56:59],a[232:235],v[204:207],v[56:59]
EX v34,v34
MM v[56:59],a[228:231],v[188:191],v[56:59]
EX v35,v35
MM v[56:59],a[224:227],v[172:175],v[56:59]
s_waitcnt vmcnt(17) lgkmcnt(0)
s_barrier
MM a[0:3],a[128:131],v[64:67],a[0:3]
s_mov_b32 s86, 94208
v_bfe_u32 v251, v248, 4, 3
v_lshlrev_b32 v251, 3, v251
buffer_load_dwordx2 v[252:253], v251, s[20:23], s86 offen offset:0 sc1
MM a[0:3],a[132:135],v[68:71],a[0:3]
s_add_u32 m0, s65, 49152
CV v24,v24,v25
CV v25,v26,v27
MM a[8:11],a[132:135],v[76:79],a[8:11]
RD a[192:195],v224 offset:16384
CV v26,v28,v29
MM a[8:11],a[128:131],v[72:75],a[8:11]
RD a[196:199],v225 offset:16384
CV v27,v30,v31
MM a[16:19],a[128:131],v[80:83],a[16:19]
buffer_load_dwordx4 v228, s[8:11], s64 offen offset:0 lds
MM a[16:19],a[132:135],v[84:87],a[16:19]
RD a[200:203],v226 offset:16384
CV v4,v32,v33
MM a[24:27],a[132:135],v[92:95],a[24:27]
RD a[204:207],v227 offset:16384
CV v5,v34,v35
MM a[24:27],a[128:131],v[88:91],a[24:27]
buffer_load_dwordx4 v229, s[8:11], s64 offen offset:1024 lds
MM a[28:31],a[136:139],v[88:91],a[28:31]
RD a[208:211],v224 offset:20480
RD a[212:215],v225 offset:20480
MM a[28:31],a[140:143],v[92:95],a[28:31]
EX v36,v36
MM a[20:23],a[140:143],v[84:87],a[20:23]
buffer_load_dwordx4 v230, s[8:11], s64 offen offset:2048 lds
MM a[20:23],a[136:139],v[80:83],a[20:23]
EX v37,v37
MM a[12:15],a[136:139],v[72:75],a[12:15]
RD a[216:219],v226 offset:20480
CV v6,v36,v37
MM a[12:15],a[140:143],v[76:79],a[12:15]
EX v38,v38
MM a[4:7],a[140:143],v[68:71],a[4:7]
EX v39,v39
MM a[4:7],a[136:139],v[64:67],a[4:7]
RD a[220:223],v227 offset:20480
CV v7,v38,v39
MM a[32:35],a[144:147],v[64:67],a[32:35]
EX v40,v40
MM a[32:35],a[148:151],v[68:71],a[32:35]
buffer_load_dwordx4 v231, s[8:11], s64 offen offset:3072 lds
MM a[40:43],a[148:151],v[76:79],a[40:43]
s_add_u32 m0, s65, 114688
EX v41,v41
MM a[40:43],a[144:147],v[72:75],a[40:43]
RD a[224:227],v224 offset:24576
CV v12,v40,v41
MM a[48:51],a[144:147],v[80:83],a[48:51]
EX v42,v42
MM a[48:51],a[148:151],v[84:87],a[48:51]
EX v43,v43
MM a[56:59],a[148:151],v[92:95],a[56:59]
RD a[228:231],v225 offset:24576
CV v13,v42,v43
MM a[56:59],a[144:147],v[88:91],a[56:59]
EX v44,v44
MM a[60:63],a[152:155],v[88:91],a[60:63]
buffer_load_dwordx4 v235, s[12:15], s81 offen offset:0 lds
MM a[60:63],a[156:159],v[92:95],a[60:63]
EX v45,v45
MM a[52:55],a[156:159],v[84:87],a[52:55]
RD a[232:235],v226 offset:24576
CV v14,v44,v45
MM a[52:55],a[152:155],v[80:83],a[52:55]
EX v46,v46
MM a[44:47],a[152:155],v[72:75],a[44:47]
EX v47,v47
MM a[44:47],a[156:159],v[76:79],a[44:47]
RD a[236:239],v227 offset:24576
CV v15,v46,v47
MM a[36:39],a[156:159],v[68:71],a[36:39]
buffer_load_dwordx4 v235, s[12:15], s64 offen offset:1024 lds
MM a[36:39],a[152:155],v[64:67],a[36:39]
EX v48,v48
MM a[64:67],a[160:163],v[64:67],a[64:67]
EX v49,v49
MM a[64:67],a[164:167],v[68:71],a[64:67]
RD a[240:243],v224 offset:28672
CV v20,v48,v49
MM a[72:75],a[164:167],v[76:79],a[72:75]
EX v50,v50
MM a[72:75],a[160:163],v[72:75],a[72:75]
)ASM"
R"ASM(
RD a[244:247],v225 offset:28672
RD a[248:251],v226 offset:28672
MM a[80:83],a[160:163],v[80:83],a[80:83]
EX v51,v51
MM a[80:83],a[164:167],v[84:87],a[80:83]
RD a[252:255],v227 offset:28672
CV v21,v50,v51
MM a[88:91],a[164:167],v[92:95],a[88:91]
EX v52,v52
MM a[88:91],a[160:163],v[88:91],a[88:91]
buffer_load_dwordx4 v235, s[12:15], s81 offen offset:2048 lds
MM a[92:95],a[168:171],v[88:91],a[92:95]
EX v53,v53
MM a[92:95],a[172:175],v[92:95],a[92:95]
EX v54,v54
MM a[84:87],a[172:175],v[84:87],a[84:87]
buffer_load_dwordx4 v235, s[12:15], s64 offen offset:3072 lds
MM a[84:87],a[168:171],v[80:83],a[84:87]
EX v55,v55
MM a[76:79],a[168:171],v[72:75],a[76:79]
EX v56,v56
MM a[76:79],a[172:175],v[76:79],a[76:79]
EX v57,v57
MM a[68:71],a[172:175],v[68:71],a[68:71]
EX v58,v58
MM a[68:71],a[168:171],v[64:67],a[68:71]
EX v59,v59
MM a[96:99],a[176:179],v[64:67],a[96:99]
EX v60,v60
MM a[96:99],a[180:183],v[68:71],a[96:99]
EX v61,v61
MM a[104:107],a[180:183],v[76:79],a[104:107]
EX v62,v62
MM a[104:107],a[176:179],v[72:75],a[104:107]
EX v63,v63
MM a[112:115],a[176:179],v[80:83],a[112:115]
CV v22,v52,v53
CV v23,v54,v55
MM a[112:115],a[180:183],v[84:87],a[112:115]
CV v28,v56,v57
CV v29,v58,v59
MM a[120:123],a[180:183],v[92:95],a[120:123]
CV v30,v60,v61
CV v31,v62,v63
MM a[120:123],a[176:179],v[88:91],a[120:123]
MM a[124:127],a[184:187],v[88:91],a[124:127]
v_add_f32 v244, v144, v145
v_add_f32 v245, v148, v149
MM a[124:127],a[188:191],v[92:95],a[124:127]
v_add_f32 v246, v152, v153
v_add_f32 v247, v156, v157
MM a[116:119],a[188:191],v[84:87],a[116:119]
MM a[116:119],a[184:187],v[80:83],a[116:119]
MM a[108:111],a[184:187],v[72:75],a[108:111]
MM a[108:111],a[188:191],v[76:79],a[108:111]
MM a[100:103],a[188:191],v[68:71],a[100:103]
MM a[100:103],a[184:187],v[64:67],a[100:103]
v_smfmac_f32_16x16x64_bf16 v[144:147], v[236:239], v[0:7], v234
v_smfmac_f32_16x16x64_bf16 v[148:151], v[236:239], v[8:15], v234
v_smfmac_f32_16x16x64_bf16 v[152:155], v[236:239], v[16:23], v234
v_smfmac_f32_16x16x64_bf16 v[156:159], v[236:239], v[24:31], v234
s_waitcnt lgkmcnt(0)
MM v[64:67],a[192:195],v[160:163],v[128:131]
s_add_u32 s64, s64, 16384
TR a[128:129],v232 offset:0
s_add_u32 s81, s64, 896
MM v[64:67],a[196:199],v[176:179],v[64:67]
TR a[130:131],v232 offset:4096
v_max3_f32 v249, v144, v145, v148
MM v[64:67],a[200:203],v[192:195],v[64:67]
TR a[132:133],v232 offset:8192
v_max3_f32 v249, v249, v149, v152
MM v[64:67],a[204:207],v[208:211],v[64:67]
TR a[134:135],v232 offset:12288
v_max3_f32 v249, v249, v153, v156
MM v[68:71],a[220:223],v[208:211],v[128:131]
TR a[136:137],v233 offset:0
v_max_f32 v249, v249, v157
MM v[68:71],a[216:219],v[192:195],v[68:71]
TR a[138:139],v233 offset:4096
v_cmp_lt_f32 vcc, s71, v249
MM v[68:71],a[212:215],v[176:179],v[68:71]
TR a[140:141],v233 offset:8192
TR a[142:143],v233 offset:12288
MM v[68:71],a[208:211],v[160:163],v[68:71]
s_cbranch_vccnz LSITE12_%=
LBACK12_%=:
MM v[76:79],a[208:211],v[164:167],v[132:135]
s_waitcnt vmcnt(8)
v_cmp_ne_u32 vcc, s69, v253
s_cbranch_vccnz LRIX_%=
LRIXb_%=:
MM v[76:79],a[212:215],v[180:183],v[76:79]
s_and_b32 s90, s17, 7
v_and_b32 v253, 65535, v252
v_readlane_b32 s19, v252, s90
MM v[76:79],a[216:219],v[196:199],v[76:79]
v_sub_u32 v253, 65535, v253
v_mul_u32_u24 v253, 1280, v253
MM v[76:79],a[220:223],v[212:215],v[76:79]
v_lshrrev_b32 v252, 7, v248
v_lshl_add_u32 v253, v252, 4, v253
MM v[72:75],a[204:207],v[212:215],v[132:135]
s_mul_i32 s91, %[swave], 3072
EX v64,v64
MM v[72:75],a[200:203],v[196:199],v[72:75]
s_add_u32 s91, s91, %[slds]
EX v65,v65
MM v[72:75],a[196:199],v[180:183],v[72:75]
s_bfe_u32 s90, s48, 131077
EX v66,v66
MM v[72:75],a[192:195],v[164:167],v[72:75]
s_mul_i32 s90, s90, 320
EX v67,v67
MM v[80:83],a[192:195],v[168:171],v[136:139]
s_add_u32 s90, s90, 131072
s_add_u32 m0, s91, 151552
s_nop 0
buffer_load_dwordx4 v253, s[20:23], s90 offen offset:0 sc1 lds
MM v[80:83],a[196:199],v[184:187],v[80:83]
TR a[144:145],v232 offset:512
CV v64,v64,v65
MM v[80:83],a[200:203],v[200:203],v[80:83]
s_add_u32 m0, s91, 152448
s_nop 0
buffer_load_dwordx4 v253, s[20:23], s90 offen offset:128 sc1 lds
MM v[80:83],a[204:207],v[216:219],v[80:83]
EX v68,v68
MM v[84:87],a[220:223],v[216:219],v[136:139]
s_add_u32 m0, s91, 153344
s_nop 0
buffer_load_dwordx4 v253, s[20:23], s90 offen offset:256 sc1 lds
MM v[84:87],a[216:219],v[200:203],v[84:87]
EX v69,v69
MM v[84:87],a[212:215],v[184:187],v[84:87]
s_lshr_b32 s77, %[sb], 3
TR a[146:147],v232 offset:4608
TR a[148:149],v232 offset:8704
MM v[84:87],a[208:211],v[168:171],v[84:87]
s_lshl_b32 s77, s77, 2
EX v70,v70
MM v[92:95],a[208:211],v[172:175],v[140:143]
s_add_u32 s77, s77, %[swave]
EX v71,v71
MM v[92:95],a[212:215],v[188:191],v[92:95]
s_lshl_b32 s77, s77, 9
TR a[150:151],v232 offset:12800
CV v65,v66,v67
MM v[92:95],a[216:219],v[204:207],v[92:95]
s_add_u32 s77, s77, 0
v_lshrrev_b32 v253, 1, v248
v_add_u32 v253, s77, v253
MM v[92:95],a[220:223],v[220:223],v[92:95]
s_mov_b32 s77, LKEND_ka-LPD0_%=-16
TR a[152:153],v233 offset:512
v_min_u32 v253, s77, v253
EX v72,v72
MM v[88:91],a[204:207],v[220:223],v[140:143]
s_getpc_b64 s[98:99]
LPD0_%=:
global_load_dword v253, v253, s[98:99]
MM v[88:91],a[200:203],v[204:207],v[88:91]
s_add_u32 s67, s95, 1024
EX v73,v73
MM v[88:91],a[196:199],v[188:191],v[88:91]
s_mov_b32 s70, 5
TR a[154:155],v233 offset:4608
CV v66,v68,v69
MM v[88:91],a[192:195],v[172:175],v[88:91]
EX v74,v74
MM v[96:99],a[224:227],v[160:163],v[128:131]
TR a[156:157],v233 offset:8704
TR a[158:159],v233 offset:12800
EX v75,v75
MM v[96:99],a[228:231],v[176:179],v[96:99]
TR a[160:161],v232 offset:1024
CV v67,v70,v71
MM v[96:99],a[232:235],v[192:195],v[96:99]
EX v76,v76
MM v[96:99],a[236:239],v[208:211],v[96:99]
EX v77,v77
MM v[100:103],a[252:255],v[208:211],v[128:131]
TR a[162:163],v232 offset:5120
CV v72,v72,v73
MM v[100:103],a[248:251],v[192:195],v[100:103]
EX v78,v78
MM v[100:103],a[244:247],v[176:179],v[100:103]
TR a[164:165],v232 offset:9216
CV v73,v74,v75
MM v[100:103],a[240:243],v[160:163],v[100:103]
EX v79,v79
MM v[108:111],a[240:243],v[164:167],v[132:135]
TR a[166:167],v232 offset:13312
TR a[168:169],v233 offset:1024
MM v[108:111],a[244:247],v[180:183],v[108:111]
EX v80,v80
MM v[108:111],a[248:251],v[196:199],v[108:111]
EX v81,v81
MM v[108:111],a[252:255],v[212:215],v[108:111]
TR a[170:171],v233 offset:5120
CV v74,v76,v77
MM v[104:107],a[236:239],v[212:215],v[132:135]
EX v82,v82
MM v[104:107],a[232:235],v[196:199],v[104:107]
TR a[172:173],v233 offset:9216
CV v75,v78,v79
MM v[104:107],a[228:231],v[180:183],v[104:107]
EX v83,v83
MM v[104:107],a[224:227],v[164:167],v[104:107]
TR a[174:175],v233 offset:13312
TR a[176:177],v232 offset:1536
MM v[112:115],a[224:227],v[168:171],v[136:139]
EX v84,v84
MM v[112:115],a[228:231],v[184:187],v[112:115]
TR a[178:179],v232 offset:5632
CV v80,v80,v81
MM v[112:115],a[232:235],v[200:203],v[112:115]
EX v85,v85
MM v[112:115],a[236:239],v[216:219],v[112:115]
TR a[180:181],v232 offset:9728
CV v81,v82,v83
MM v[116:119],a[252:255],v[216:219],v[136:139]
EX v86,v86
MM v[116:119],a[248:251],v[200:203],v[116:119]
TR a[182:183],v232 offset:13824
TR a[184:185],v233 offset:1536
MM v[116:119],a[244:247],v[184:187],v[116:119]
EX v87,v87
MM v[116:119],a[240:243],v[168:171],v[116:119]
EX v88,v88
MM v[124:127],a[240:243],v[172:175],v[140:143]
TR a[186:187],v233 offset:5632
CV v82,v84,v85
MM v[124:127],a[244:247],v[188:191],v[124:127]
EX v89,v89
MM v[124:127],a[248:251],v[204:207],v[124:127]
TR a[188:189],v233 offset:9728
TR a[190:191],v233 offset:13824
MM v[124:127],a[252:255],v[220:223],v[124:127]
EX v90,v90
MM v[120:123],a[236:239],v[220:223],v[140:143]
CV v83,v86,v87
CV v88,v88,v89
MM v[120:123],a[232:235],v[204:207],v[120:123]
EX v91,v91
MM v[120:123],a[228:231],v[188:191],v[120:123]
EX v92,v92
MM v[120:123],a[224:227],v[172:175],v[120:123]
s_waitcnt lgkmcnt(0)
s_barrier
MM a[0:3],a[128:131],v[0:3],a[0:3]
s_add_u32 m0, s65, 0
EX v93,v93
MM a[0:3],a[132:135],v[4:7],a[0:3]
CV v89,v90,v91
CV v90,v92,v93
MM a[8:11],a[132:135],v[12:15],a[8:11]
EX v94,v94
MM a[8:11],a[128:131],v[8:11],a[8:11]
EX v95,v95
MM a[16:19],a[128:131],v[16:19],a[16:19]
RD a[192:195],v224 offset:32768
CV v91,v94,v95
MM a[16:19],a[132:135],v[20:23],a[16:19]
EX v96,v96
MM a[24:27],a[132:135],v[28:31],a[24:27]
buffer_load_dwordx4 v228, s[8:11], s64 offen offset:0 lds
MM a[24:27],a[128:131],v[24:27],a[24:27]
EX v97,v97
MM a[28:31],a[136:139],v[24:27],a[28:31]
RD a[196:199],v225 offset:32768
CV v68,v96,v97
MM a[28:31],a[140:143],v[28:31],a[28:31]
EX v98,v98
MM a[20:23],a[140:143],v[20:23],a[20:23]
EX v99,v99
MM a[20:23],a[136:139],v[16:19],a[20:23]
RD a[200:203],v226 offset:32768
CV v69,v98,v99
MM a[12:15],a[136:139],v[8:11],a[12:15]
EX v100,v100
MM a[12:15],a[140:143],v[12:15],a[12:15]
buffer_load_dwordx4 v229, s[8:11], s64 offen offset:1024 lds
MM a[4:7],a[140:143],v[4:7],a[4:7]
EX v101,v101
MM a[4:7],a[136:139],v[0:3],a[4:7]
RD a[204:207],v227 offset:32768
CV v70,v100,v101
MM a[32:35],a[144:147],v[0:3],a[32:35]
EX v102,v102
MM a[32:35],a[148:151],v[4:7],a[32:35]
EX v103,v103
MM a[40:43],a[148:151],v[12:15],a[40:43]
RD a[208:211],v224 offset:36864
CV v71,v102,v103
MM a[40:43],a[144:147],v[8:11],a[40:43]
EX v104,v104
MM a[48:51],a[144:147],v[16:19],a[48:51]
buffer_load_dwordx4 v230, s[8:11], s64 offen offset:2048 lds
MM a[48:51],a[148:151],v[20:23],a[48:51]
EX v105,v105
MM a[56:59],a[148:151],v[28:31],a[56:59]
RD a[212:215],v225 offset:36864
CV v76,v104,v105
MM a[56:59],a[144:147],v[24:27],a[56:59]
EX v106,v106
MM a[60:63],a[152:155],v[24:27],a[60:63]
EX v107,v107
MM a[60:63],a[156:159],v[28:31],a[60:63]
RD a[216:219],v226 offset:36864
CV v77,v106,v107
MM a[52:55],a[156:159],v[20:23],a[52:55]
EX v108,v108
MM a[52:55],a[152:155],v[16:19],a[52:55]
buffer_load_dwordx4 v231, s[8:11], s64 offen offset:3072 lds
MM a[44:47],a[152:155],v[8:11],a[44:47]
s_add_u32 m0, s65, 65536
EX v109,v109
MM a[44:47],a[156:159],v[12:15],a[44:47]
RD a[220:223],v227 offset:36864
CV v78,v108,v109
MM a[36:39],a[156:159],v[4:7],a[36:39]
EX v110,v110
MM a[36:39],a[152:155],v[0:3],a[36:39]
RD a[224:227],v224 offset:40960
RD a[228:231],v225 offset:40960
MM a[64:67],a[160:163],v[0:3],a[64:67]
EX v111,v111
MM a[64:67],a[164:167],v[4:7],a[64:67]
RD a[232:235],v226 offset:40960
CV v79,v110,v111
MM a[72:75],a[164:167],v[12:15],a[72:75]
EX v112,v112
MM a[72:75],a[160:163],v[8:11],a[72:75]
buffer_load_dwordx4 v235, s[12:15], s81 offen offset:0 lds
MM a[80:83],a[160:163],v[16:19],a[80:83]
EX v113,v113
MM a[80:83],a[164:167],v[20:23],a[80:83]
RD a[236:239],v227 offset:40960
CV v84,v112,v113
MM a[88:91],a[164:167],v[28:31],a[88:91]
EX v114,v114
MM a[88:91],a[160:163],v[24:27],a[88:91]
EX v115,v115
MM a[92:95],a[168:171],v[24:27],a[92:95]
RD a[240:243],v224 offset:45056
CV v85,v114,v115
MM a[92:95],a[172:175],v[28:31],a[92:95]
EX v116,v116
MM a[84:87],a[172:175],v[20:23],a[84:87]
buffer_load_dwordx4 v235, s[12:15], s64 offen offset:1024 lds
MM a[84:87],a[168:171],v[16:19],a[84:87]
EX v117,v117
MM a[76:79],a[168:171],v[8:11],a[76:79]
RD a[244:247],v225 offset:45056
CV v86,v116,v117
MM a[76:79],a[172:175],v[12:15],a[76:79]
EX v118,v118
MM a[68:71],a[172:175],v[4:7],a[68:71]
EX v119,v119
MM a[68:71],a[168:171],v[0:3],a[68:71]
RD a[248:251],v226 offset:45056
CV v87,v118,v119
MM a[96:99],a[176:179],v[0:3],a[96:99]
EX v120,v120
MM a[96:99],a[180:183],v[4:7],a[96:99]
buffer_load_dwordx4 v235, s[12:15], s81 offen offset:2048 lds
MM a[104:107],a[180:183],v[12:15],a[104:107]
EX v121,v121
MM a[104:107],a[176:179],v[8:11],a[104:107]
RD a[252:255],v227 offset:45056
CV v92,v120,v121
MM a[112:115],a[176:179],v[16:19],a[112:115]
EX v122,v122
MM a[112:115],a[180:183],v[20:23],a[112:115]
EX v123,v123
MM a[120:123],a[180:183],v[28:31],a[120:123]
s_add_u32 s87, s95, 0
buffer_store_dword v240, v248, s[20:23], s87 offen offset:12 sc1
MM a[120:123],a[176:179],v[24:27],a[120:123]
EX v124,v124
MM a[124:127],a[184:187],v[24:27],a[124:127]
v_add_f32 v244, v144, v145
v_add_f32 v245, v148, v149
MM a[124:127],a[188:191],v[28:31],a[124:127]
v_add_f32 v246, v152, v153
v_add_f32 v247, v156, v157
MM a[116:119],a[188:191],v[20:23],a[116:119]
)ASM"
R"ASM(
EX v125,v125
MM a[116:119],a[184:187],v[16:19],a[116:119]
buffer_load_dwordx4 v235, s[12:15], s64 offen offset:3072 lds
MM a[108:111],a[184:187],v[8:11],a[108:111]
EX v126,v126
MM a[108:111],a[188:191],v[12:15],a[108:111]
EX v127,v127
MM a[100:103],a[188:191],v[4:7],a[100:103]
CV v93,v122,v123
CV v94,v124,v125
MM a[100:103],a[184:187],v[0:3],a[100:103]
CV v95,v126,v127
v_smfmac_f32_16x16x64_bf16 v[144:147], v[236:239], v[64:71], v234
v_smfmac_f32_16x16x64_bf16 v[148:151], v[236:239], v[72:79], v234
v_smfmac_f32_16x16x64_bf16 v[152:155], v[236:239], v[80:87], v234
v_smfmac_f32_16x16x64_bf16 v[156:159], v[236:239], v[88:95], v234
s_waitcnt lgkmcnt(0)
s_cmp_ge_u32 s16, 108
s_cbranch_scc1 LLAP_%=
MM v[0:3],a[192:195],v[160:163],v[128:131]
s_add_u32 s64, s64, 16384
TR a[128:129],v232 offset:16384
s_add_u32 s81, s64, 896
MM v[0:3],a[196:199],v[176:179],v[0:3]
TR a[130:131],v232 offset:20480
v_max3_f32 v249, v144, v145, v148
MM v[0:3],a[200:203],v[192:195],v[0:3]
TR a[132:133],v232 offset:24576
v_max3_f32 v249, v249, v149, v152
MM v[0:3],a[204:207],v[208:211],v[0:3]
TR a[134:135],v232 offset:28672
v_max3_f32 v249, v249, v153, v156
MM v[4:7],a[220:223],v[208:211],v[128:131]
TR a[136:137],v233 offset:16384
v_max_f32 v249, v249, v157
MM v[4:7],a[216:219],v[192:195],v[4:7]
TR a[138:139],v233 offset:20480
v_cmp_lt_f32 vcc, s71, v249
MM v[4:7],a[212:215],v[176:179],v[4:7]
TR a[140:141],v233 offset:24576
TR a[142:143],v233 offset:28672
MM v[4:7],a[208:211],v[160:163],v[4:7]
s_cbranch_vccnz LSITE13_%=
LBACK13_%=:
MM v[12:15],a[208:211],v[164:167],v[132:135]
v_lshrrev_b32 v127, 8, v248
v_lshlrev_b32 v124, 9, v127
MM v[12:15],a[212:215],v[180:183],v[12:15]
v_and_b32 v126, 192, v248
v_add_u32 v124, v124, v126
MM v[12:15],a[216:219],v[196:199],v[12:15]
v_bfe_u32 v126, v248, 4, 2
v_lshl_add_u32 v124, v126, 3, v124
MM v[12:15],a[220:223],v[212:215],v[12:15]
s_lshr_b32 s76, %[swave], 1
EX v0,v0
MM v[8:11],a[204:207],v[212:215],v[132:135]
s_mul_i32 s76, s76, 10240
EX v1,v1
MM v[8:11],a[200:203],v[196:199],v[8:11]
s_and_b32 s77, %[swave], 1
EX v2,v2
MM v[8:11],a[196:199],v[180:183],v[8:11]
s_lshl_b32 s77, s77, 5
TR a[144:145],v232 offset:16896
CV v0,v0,v1
MM v[8:11],a[192:195],v[164:167],v[8:11]
s_add_u32 s76, s76, s77
EX v3,v3
MM v[16:19],a[192:195],v[168:171],v[136:139]
s_add_u32 s76, s76, %[slds]
EX v4,v4
MM v[16:19],a[196:199],v[184:187],v[16:19]
s_add_u32 s76, s76, 131072
v_add_u32 v124, s76, v124
v_bfe_u32 v126, v248, 4, 3
MM v[16:19],a[200:203],v[200:203],v[16:19]
v_lshl_add_u32 v126, v127, 3, v126
v_lshlrev_b32 v126, 4, v126
MM v[16:19],a[204:207],v[216:219],v[16:19]
s_mul_i32 s76, %[swave], 3072
EX v5,v5
MM v[20:23],a[220:223],v[216:219],v[136:139]
s_add_u32 s76, s76, %[slds]
TR a[146:147],v232 offset:20992
CV v1,v2,v3
MM v[20:23],a[216:219],v[200:203],v[20:23]
s_add_u32 s76, s76, 151552
TR a[148:149],v232 offset:25088
v_add_u32 v125, s76, v126
MM v[20:23],a[212:215],v[184:187],v[20:23]
s_lshr_b32 s76, s48, 7
EX v6,v6
MM v[20:23],a[208:211],v[168:171],v[20:23]
s_lshl_b32 s76, s76, 5
EX v7,v7
MM v[28:31],a[208:211],v[172:175],v[140:143]
s_and_b32 s77, s48, 16
TR a[150:151],v232 offset:29184
TR a[152:153],v233 offset:16896
MM v[28:31],a[212:215],v[188:191],v[28:31]
s_add_u32 s76, s76, s77
EX v8,v8
MM v[28:31],a[216:219],v[204:207],v[28:31]
s_bfe_u32 s77, s48, 131077
TR a[154:155],v233 offset:20992
CV v2,v4,v5
MM v[28:31],a[220:223],v[220:223],v[28:31]
s_lshl_b32 s77, s77, 2
EX v9,v9
s_nop 0
LPG12_%=:
s_cbranch_execz LTR0_%=
MM v[24:27],a[204:207],v[220:223],v[140:143]
s_add_u32 s76, s76, s77
TR a[156:157],v233 offset:25088
CV v3,v6,v7
MM v[24:27],a[200:203],v[204:207],v[24:27]
s_add_u32 s76, s76, %[swave]
EX v10,v10
MM v[24:27],a[196:199],v[188:191],v[24:27]
s_lshl_b32 s97, s76, 2
TR a[158:159],v233 offset:29184
CV v8,v8,v9
MM v[24:27],a[192:195],v[172:175],v[24:27]
s_add_u32 s97, s97, 77824
EX v11,v11
MM v[32:35],a[224:227],v[160:163],v[128:131]
s_lshl_b32 s76, s76, 9
TR a[160:161],v232 offset:17408
TR a[162:163],v232 offset:21504
MM v[32:35],a[228:231],v[176:179],v[32:35]
s_add_u32 s76, s76, 22183936
EX v12,v12
MM v[32:35],a[232:235],v[192:195],v[32:35]
EX v13,v13
MM v[32:35],a[236:239],v[208:211],v[32:35]
TR a[164:165],v232 offset:25600
CV v9,v10,v11
MM v[36:39],a[252:255],v[208:211],v[128:131]
EX v14,v14
MM v[36:39],a[248:251],v[192:195],v[36:39]
TR a[166:167],v232 offset:29696
CV v10,v12,v13
MM v[36:39],a[244:247],v[176:179],v[36:39]
EX v15,v15
MM v[36:39],a[240:243],v[160:163],v[36:39]
TR a[168:169],v233 offset:17408
TR a[170:171],v233 offset:21504
MM v[44:47],a[240:243],v[164:167],v[132:135]
EX v16,v16
MM v[44:47],a[244:247],v[180:183],v[44:47]
EX v17,v17
MM v[44:47],a[248:251],v[196:199],v[44:47]
TR a[172:173],v233 offset:25600
CV v11,v14,v15
MM v[44:47],a[252:255],v[212:215],v[44:47]
EX v18,v18
MM v[40:43],a[236:239],v[212:215],v[132:135]
TR a[174:175],v233 offset:29696
CV v16,v16,v17
MM v[40:43],a[232:235],v[196:199],v[40:43]
EX v19,v19
MM v[40:43],a[228:231],v[180:183],v[40:43]
TR a[176:177],v232 offset:17920
TR a[178:179],v232 offset:22016
MM v[40:43],a[224:227],v[164:167],v[40:43]
EX v20,v20
MM v[48:51],a[224:227],v[168:171],v[136:139]
EX v21,v21
MM v[48:51],a[228:231],v[184:187],v[48:51]
TR a[180:181],v232 offset:26112
CV v17,v18,v19
MM v[48:51],a[232:235],v[200:203],v[48:51]
EX v22,v22
MM v[48:51],a[236:239],v[216:219],v[48:51]
TR a[182:183],v232 offset:30208
CV v18,v20,v21
MM v[52:55],a[252:255],v[216:219],v[136:139]
EX v23,v23
MM v[52:55],a[248:251],v[200:203],v[52:55]
TR a[184:185],v233 offset:17920
TR a[186:187],v233 offset:22016
MM v[52:55],a[244:247],v[184:187],v[52:55]
EX v24,v24
MM v[52:55],a[240:243],v[168:171],v[52:55]
TR a[188:189],v233 offset:26112
CV v19,v22,v23
MM v[60:63],a[240:243],v[172:175],v[140:143]
EX v25,v25
MM v[60:63],a[244:247],v[188:191],v[60:63]
TR a[190:191],v233 offset:30208
CV v24,v24,v25
MM v[60:63],a[248:251],v[204:207],v[60:63]
EX v26,v26
MM v[60:63],a[252:255],v[220:223],v[60:63]
EX v27,v27
MM v[56:59],a[236:239],v[220:223],v[140:143]
EX v28,v28
MM v[56:59],a[232:235],v[204:207],v[56:59]
EX v29,v29
MM v[56:59],a[228:231],v[188:191],v[56:59]
s_getpc_b64 s[98:99]
LTA2_%=:
s_min_u32 s73, %[swave], 3
s_lshl_b32 s73, s73, 12
s_add_u32 s73, s73, LPG13_%=-LTA2_%=
s_add_u32 s98, s98, s73
s_addc_u32 s99, s99, 0
s_mov_b64 exec, 0
s_setpc_b64 s[98:99]
LTR2_%=:
s_mov_b64 exec, -1
CV v25,v26,v27
MM v[56:59],a[224:227],v[172:175],v[56:59]
s_waitcnt vmcnt(17) lgkmcnt(0)
s_barrier
MM a[0:3],a[128:131],v[64:67],a[0:3]
s_add_u32 m0, s65, 16384
RD a[192:195],v224 offset:49152
CV v26,v28,v29
MM a[0:3],a[132:135],v[68:71],a[0:3]
EX v30,v30
MM a[8:11],a[132:135],v[76:79],a[8:11]
EX v31,v31
MM a[8:11],a[128:131],v[72:75],a[8:11]
RD a[196:199],v225 offset:49152
CV v27,v30,v31
MM a[16:19],a[128:131],v[80:83],a[16:19]
s_waitcnt vmcnt(10)
TR v[96:97],v124 offset:0
TR v[98:99],v124 offset:256
MM a[16:19],a[132:135],v[84:87],a[16:19]
RD v[108:111],v125 offset:0
TR v[100:101],v124 offset:2048
MM a[24:27],a[132:135],v[92:95],a[24:27]
TR v[102:103],v124 offset:2304
RD v[112:115],v125 offset:512
MM a[24:27],a[128:131],v[88:91],a[24:27]
TR v[104:105],v124 offset:4096
TR v[106:107],v124 offset:4352
MM a[28:31],a[136:139],v[88:91],a[28:31]
RD v[116:119],v125 offset:1024
RD a[200:203],v226 offset:49152
EX v32,v32
MM a[28:31],a[140:143],v[92:95],a[28:31]
EX v33,v33
MM a[20:23],a[140:143],v[84:87],a[20:23]
EX v34,v34
MM a[20:23],a[136:139],v[80:83],a[20:23]
buffer_load_dwordx4 v228, s[8:11], s64 offen offset:0 lds
MM a[12:15],a[136:139],v[72:75],a[12:15]
EX v35,v35
MM a[12:15],a[140:143],v[76:79],a[12:15]
EX v36,v36
MM a[4:7],a[140:143],v[68:71],a[4:7]
CV v4,v32,v33
CV v5,v34,v35
MM a[4:7],a[136:139],v[64:67],a[4:7]
EX v37,v37
MM a[32:35],a[144:147],v[64:67],a[32:35]
EX v38,v38
MM a[32:35],a[148:151],v[68:71],a[32:35]
s_waitcnt lgkmcnt(1)
buffer_load_dwordx4 v229, s[8:11], s64 offen offset:1024 lds
MM a[40:43],a[148:151],v[76:79],a[40:43]
EX v39,v39
MM v[120:123],v[96:99],v[108:111],0
MM v[120:123],v[100:103],v[112:115],v[120:123]
MM v[120:123],v[104:107],v[116:119],v[120:123]
MM a[40:43],a[144:147],v[72:75],a[40:43]
TR v[96:97],v124 offset:6144
TR v[98:99],v124 offset:6400
MM a[48:51],a[144:147],v[80:83],a[48:51]
RD v[108:111],v125 offset:1536
TR v[100:101],v124 offset:8192
MM a[48:51],a[148:151],v[84:87],a[48:51]
TR v[102:103],v124 offset:8448
RD v[112:115],v125 offset:2048
MM a[56:59],a[148:151],v[92:95],a[56:59]
EX v40,v40
MM a[56:59],a[144:147],v[88:91],a[56:59]
EX v41,v41
MM a[60:63],a[152:155],v[88:91],a[60:63]
RD a[204:207],v227 offset:49152
CV v6,v36,v37
MM a[60:63],a[156:159],v[92:95],a[60:63]
EX v42,v42
MM a[52:55],a[156:159],v[84:87],a[52:55]
RD a[208:211],v224 offset:53248
CV v7,v38,v39
MM a[52:55],a[152:155],v[80:83],a[52:55]
EX v43,v43
MM a[44:47],a[152:155],v[72:75],a[44:47]
buffer_load_dwordx4 v230, s[8:11], s64 offen offset:2048 lds
MM a[44:47],a[156:159],v[76:79],a[44:47]
EX v44,v44
MM a[36:39],a[156:159],v[68:71],a[36:39]
s_waitcnt lgkmcnt(2)
EX v45,v45
MM a[36:39],a[152:155],v[64:67],a[36:39]
RD a[212:215],v225 offset:53248
CV v12,v40,v41
MM v[120:123],v[96:99],v[108:111],v[120:123]
MM v[120:123],v[100:103],v[112:115],v[120:123]
MM a[64:67],a[160:163],v[64:67],a[64:67]
EX v46,v46
MM a[64:67],a[164:167],v[68:71],a[64:67]
RD a[216:219],v226 offset:53248
CV v13,v42,v43
MM a[72:75],a[164:167],v[76:79],a[72:75]
EX v47,v47
MM a[72:75],a[160:163],v[72:75],a[72:75]
buffer_load_dwordx4 v231, s[8:11], s64 offen offset:3072 lds
MM a[80:83],a[160:163],v[80:83],a[80:83]
s_add_u32 m0, s65, 81920
EX v48,v48
MM a[80:83],a[164:167],v[84:87],a[80:83]
RD a[220:223],v227 offset:53248
CV v14,v44,v45
MM a[88:91],a[164:167],v[92:95],a[88:91]
s_mov_b32 exec_lo, 0x00ff00ff
s_mov_b32 exec_hi, 0x00ff00ff
buffer_store_dwordx4 v[120:123], v126, s[20:23], s76 offen offset:0 sc1
s_mov_b64 exec, -1
s_nop 0
MM a[88:91],a[160:163],v[88:91],a[88:91]
EX v49,v49
MM a[92:95],a[168:171],v[88:91],a[92:95]
EX v50,v50
MM a[92:95],a[172:175],v[92:95],a[92:95]
RD a[224:227],v224 offset:57344
CV v15,v46,v47
MM a[84:87],a[172:175],v[84:87],a[84:87]
EX v51,v51
MM a[84:87],a[168:171],v[80:83],a[84:87]
EX v52,v52
MM a[76:79],a[168:171],v[72:75],a[76:79]
buffer_load_dwordx4 v235, s[12:15], s81 offen offset:0 lds
MM a[76:79],a[172:175],v[76:79],a[76:79]
EX v53,v53
MM a[68:71],a[172:175],v[68:71],a[68:71]
RD a[228:231],v225 offset:57344
CV v20,v48,v49
MM a[68:71],a[168:171],v[64:67],a[68:71]
EX v54,v54
MM a[96:99],a[176:179],v[64:67],a[96:99]
RD a[232:235],v226 offset:57344
CV v21,v50,v51
MM a[96:99],a[180:183],v[68:71],a[96:99]
EX v55,v55
MM a[104:107],a[180:183],v[76:79],a[104:107]
RD a[236:239],v227 offset:57344
CV v22,v52,v53
MM a[104:107],a[176:179],v[72:75],a[104:107]
EX v56,v56
MM a[112:115],a[176:179],v[80:83],a[112:115]
buffer_load_dwordx4 v235, s[12:15], s64 offen offset:1024 lds
MM a[112:115],a[180:183],v[84:87],a[112:115]
EX v57,v57
MM a[120:123],a[180:183],v[92:95],a[120:123]
EX v58,v58
MM a[120:123],a[176:179],v[88:91],a[120:123]
RD a[240:243],v224 offset:61440
CV v23,v54,v55
MM a[124:127],a[184:187],v[88:91],a[124:127]
v_add_f32 v244, v144, v145
v_add_f32 v245, v148, v149
MM a[124:127],a[188:191],v[92:95],a[124:127]
v_add_f32 v246, v152, v153
v_add_f32 v247, v156, v157
MM a[116:119],a[188:191],v[84:87],a[116:119]
EX v59,v59
MM a[116:119],a[184:187],v[80:83],a[116:119]
EX v60,v60
MM a[108:111],a[184:187],v[72:75],a[108:111]
EX v61,v61
MM a[108:111],a[188:191],v[76:79],a[108:111]
RD a[244:247],v225 offset:61440
CV v28,v56,v57
MM a[100:103],a[188:191],v[68:71],a[100:103]
buffer_load_dwordx4 v235, s[12:15], s81 offen offset:2048 lds
MM a[100:103],a[184:187],v[64:67],a[100:103]
EX v62,v62
v_smfmac_f32_16x16x64_bf16 v[144:147], v[236:239], v[0:7], v234
RD a[248:251],v226 offset:61440
RD a[252:255],v227 offset:61440
EX v63,v63
v_smfmac_f32_16x16x64_bf16 v[148:151], v[236:239], v[8:15], v234
CV v29,v58,v59
v_smfmac_f32_16x16x64_bf16 v[152:155], v[236:239], v[16:23], v234
CV v30,v60,v61
CV v31,v62,v63
s_nop 1
v_smfmac_f32_16x16x64_bf16 v[156:159], v[236:239], v[24:31], v234
s_waitcnt lgkmcnt(0)
buffer_load_dwordx4 v235, s[12:15], s64 offen offset:3072 lds
)ASM"
R"ASM(
MM v[64:67],a[192:195],v[160:163],v[128:131]
s_add_u32 s64, s64, 16384
TR a[128:129],v232 offset:32768
s_add_u32 s81, s64, 896
MM v[64:67],a[196:199],v[176:179],v[64:67]
TR a[130:131],v232 offset:36864
v_max3_f32 v249, v144, v145, v148
MM v[64:67],a[200:203],v[192:195],v[64:67]
TR a[132:133],v232 offset:40960
v_max3_f32 v249, v249, v149, v152
MM v[64:67],a[204:207],v[208:211],v[64:67]
TR a[134:135],v232 offset:45056
v_max3_f32 v249, v249, v153, v156
MM v[68:71],a[220:223],v[208:211],v[128:131]
TR a[136:137],v233 offset:32768
v_max_f32 v249, v249, v157
MM v[68:71],a[216:219],v[192:195],v[68:71]
TR a[138:139],v233 offset:36864
v_cmp_lt_f32 vcc, s71, v249
MM v[68:71],a[212:215],v[176:179],v[68:71]
TR a[140:141],v233 offset:40960
TR a[142:143],v233 offset:45056
MM v[68:71],a[208:211],v[160:163],v[68:71]
s_cbranch_vccnz LSITE14_%=
LBACK14_%=:
MM v[76:79],a[208:211],v[164:167],v[132:135]
s_add_u32 s48, s48, 16
v_lshrrev_b32 v252, 6, v248
v_mul_u32_u24 v252, 3456, v252
MM v[76:79],a[212:215],v[180:183],v[76:79]
v_and_b32 v251, 48, v248
v_add_u32 v252, v252, v251
MM v[76:79],a[216:219],v[196:199],v[76:79]
s_and_b32 s90, %[swave], 1
EX v64,v64
MM v[76:79],a[220:223],v[212:215],v[76:79]
s_mul_i32 s90, s90, 276480
EX v65,v65
MM v[72:75],a[204:207],v[212:215],v[132:135]
s_bfe_u32 s91, s48, 131077
EX v66,v66
MM v[72:75],a[200:203],v[196:199],v[72:75]
s_mul_i32 s91, s91, 552960
TR a[144:145],v232 offset:33280
CV v64,v64,v65
MM v[72:75],a[196:199],v[180:183],v[72:75]
s_add_u32 s90, s90, s91
EX v67,v67
MM v[72:75],a[192:195],v[164:167],v[72:75]
s_and_b32 s91, s48, 0xffffff80
TR a[146:147],v232 offset:37376
CV v65,v66,v67
MM v[80:83],a[192:195],v[168:171],v[136:139]
s_add_u32 s90, s90, s91
EX v68,v68
MM v[80:83],a[196:199],v[184:187],v[80:83]
s_and_b32 s91, s48, 16
TR a[148:149],v232 offset:41472
TR a[150:151],v232 offset:45568
MM v[80:83],a[200:203],v[200:203],v[80:83]
s_lshl_b32 s91, s91, 2
EX v69,v69
MM v[80:83],a[204:207],v[216:219],v[80:83]
s_add_u32 s66, s90, s91
TR a[152:153],v233 offset:33280
CV v66,v68,v69
MM v[84:87],a[220:223],v[216:219],v[136:139]
s_mul_i32 s91, %[swave], 5120
EX v70,v70
MM v[84:87],a[216:219],v[200:203],v[84:87]
s_add_u32 s91, s91, %[slds]
EX v71,v71
MM v[84:87],a[212:215],v[184:187],v[84:87]
s_add_u32 s67, s91, 131072
TR a[154:155],v233 offset:37376
CV v67,v70,v71
MM v[84:87],a[208:211],v[168:171],v[84:87]
s_and_b32 s18, s17, 255
EX v72,v72
MM v[92:95],a[208:211],v[172:175],v[140:143]
s_sub_u32 s90, s18, 40
s_cmp_ge_u32 s18, 216
s_cselect_b32 s18, s90, s18
MM v[92:95],a[212:215],v[188:191],v[92:95]
s_lshl_b32 s18, s18, 4
EX v73,v73
MM v[92:95],a[216:219],v[204:207],v[92:95]
s_sub_u32 s90, s16, 90
TR a[156:157],v233 offset:41472
CV v72,v72,v73
MM v[92:95],a[220:223],v[220:223],v[92:95]
s_min_u32 s47, s90, s16
EX v74,v74
MM v[88:91],a[204:207],v[220:223],v[140:143]
s_mul_i32 s90, s47, 205
TR a[158:159],v233 offset:45568
TR a[160:161],v232 offset:33792
MM v[88:91],a[200:203],v[204:207],v[88:91]
s_lshr_b32 s46, s90, 11
EX v75,v75
MM v[88:91],a[196:199],v[188:191],v[88:91]
s_mul_i32 s90, s46, 244480
TR a[162:163],v232 offset:37888
CV v73,v74,v75
MM v[88:91],a[192:195],v[172:175],v[88:91]
s_lshl_b32 s91, s47, 7
EX v76,v76
MM v[96:99],a[224:227],v[160:163],v[128:131]
s_add_u32 s90, s90, s91
s_add_u32 s28, s28, s90
s_addc_u32 s29, s29, 0
MM v[96:99],a[228:231],v[176:179],v[96:99]
s_sub_u32 s30, 2211840, s90
EX v77,v77
MM v[96:99],a[232:235],v[192:195],v[96:99]
s_mov_b32 s31, 131072
TR a[164:165],v232 offset:41984
CV v74,v76,v77
MM v[96:99],a[236:239],v[208:211],v[96:99]
EX v78,v78
MM v[100:103],a[252:255],v[208:211],v[128:131]
TR a[166:167],v232 offset:46080
TR a[168:169],v233 offset:33792
MM v[100:103],a[248:251],v[192:195],v[100:103]
EX v79,v79
MM v[100:103],a[244:247],v[176:179],v[100:103]
TR a[170:171],v233 offset:37888
CV v75,v78,v79
MM v[100:103],a[240:243],v[160:163],v[100:103]
EX v80,v80
MM v[108:111],a[240:243],v[164:167],v[132:135]
TR a[172:173],v233 offset:41984
TR a[174:175],v233 offset:46080
MM v[108:111],a[244:247],v[180:183],v[108:111]
EX v81,v81
MM v[108:111],a[248:251],v[196:199],v[108:111]
EX v82,v82
MM v[108:111],a[252:255],v[212:215],v[108:111]
TR a[176:177],v232 offset:34304
CV v80,v80,v81
MM v[104:107],a[236:239],v[212:215],v[132:135]
EX v83,v83
MM v[104:107],a[232:235],v[196:199],v[104:107]
TR a[178:179],v232 offset:38400
CV v81,v82,v83
MM v[104:107],a[228:231],v[180:183],v[104:107]
EX v84,v84
MM v[104:107],a[224:227],v[164:167],v[104:107]
TR a[180:181],v232 offset:42496
TR a[182:183],v232 offset:46592
MM v[112:115],a[224:227],v[168:171],v[136:139]
EX v85,v85
MM v[112:115],a[228:231],v[184:187],v[112:115]
TR a[184:185],v233 offset:34304
CV v82,v84,v85
MM v[112:115],a[232:235],v[200:203],v[112:115]
EX v86,v86
MM v[112:115],a[236:239],v[216:219],v[112:115]
EX v87,v87
MM v[116:119],a[252:255],v[216:219],v[136:139]
TR a[186:187],v233 offset:38400
CV v83,v86,v87
MM v[116:119],a[248:251],v[200:203],v[116:119]
EX v88,v88
MM v[116:119],a[244:247],v[184:187],v[116:119]
TR a[188:189],v233 offset:42496
TR a[190:191],v233 offset:46592
MM v[116:119],a[240:243],v[168:171],v[116:119]
EX v89,v89
MM v[124:127],a[240:243],v[172:175],v[140:143]
EX v90,v90
MM v[124:127],a[244:247],v[188:191],v[124:127]
EX v91,v91
MM v[124:127],a[248:251],v[204:207],v[124:127]
EX v92,v92
MM v[124:127],a[252:255],v[220:223],v[124:127]
EX v93,v93
MM v[120:123],a[236:239],v[220:223],v[140:143]
EX v94,v94
MM v[120:123],a[232:235],v[204:207],v[120:123]
EX v95,v95
MM v[120:123],a[228:231],v[188:191],v[120:123]
EX v96,v96
MM v[120:123],a[224:227],v[172:175],v[120:123]
s_waitcnt vmcnt(14) lgkmcnt(0)
s_barrier
MM a[0:3],a[128:131],v[0:3],a[0:3]
s_add_u32 m0, s65, 32768
CV v88,v88,v89
CV v89,v90,v91
MM a[0:3],a[132:135],v[4:7],a[0:3]
RD a[192:195],v224 offset:0
CV v90,v92,v93
MM a[8:11],a[132:135],v[12:15],a[8:11]
RD a[196:199],v225 offset:0
CV v91,v94,v95
MM a[8:11],a[128:131],v[8:11],a[8:11]
buffer_load_dwordx4 v228, s[8:11], s64 offen offset:0 lds
MM a[16:19],a[128:131],v[16:19],a[16:19]
RD a[200:203],v226 offset:0
RD a[204:207],v227 offset:0
MM a[16:19],a[132:135],v[20:23],a[16:19]
EX v97,v97
MM a[24:27],a[132:135],v[28:31],a[24:27]
RD a[208:211],v224 offset:4096
CV v68,v96,v97
MM a[24:27],a[128:131],v[24:27],a[24:27]
EX v98,v98
MM a[28:31],a[136:139],v[24:27],a[28:31]
buffer_load_dwordx4 v229, s[8:11], s64 offen offset:1024 lds
MM a[28:31],a[140:143],v[28:31],a[28:31]
EX v99,v99
MM a[20:23],a[140:143],v[20:23],a[20:23]
RD a[212:215],v225 offset:4096
CV v69,v98,v99
MM a[20:23],a[136:139],v[16:19],a[20:23]
EX v100,v100
MM a[12:15],a[136:139],v[8:11],a[12:15]
EX v101,v101
MM a[12:15],a[140:143],v[12:15],a[12:15]
RD a[216:219],v226 offset:4096
CV v70,v100,v101
MM a[4:7],a[140:143],v[4:7],a[4:7]
EX v102,v102
MM a[4:7],a[136:139],v[0:3],a[4:7]
buffer_load_dwordx4 v230, s[8:11], s64 offen offset:2048 lds
MM a[32:35],a[144:147],v[0:3],a[32:35]
EX v103,v103
MM a[32:35],a[148:151],v[4:7],a[32:35]
RD a[220:223],v227 offset:4096
CV v71,v102,v103
MM a[40:43],a[148:151],v[12:15],a[40:43]
EX v104,v104
MM a[40:43],a[144:147],v[8:11],a[40:43]
EX v105,v105
MM a[48:51],a[144:147],v[16:19],a[48:51]
RD a[224:227],v224 offset:8192
CV v76,v104,v105
MM a[48:51],a[148:151],v[20:23],a[48:51]
EX v106,v106
MM a[56:59],a[148:151],v[28:31],a[56:59]
buffer_load_dwordx4 v231, s[8:11], s64 offen offset:3072 lds
MM a[56:59],a[144:147],v[24:27],a[56:59]
s_add_u32 m0, s65, 98304
EX v107,v107
MM a[60:63],a[152:155],v[24:27],a[60:63]
RD a[228:231],v225 offset:8192
CV v77,v106,v107
MM a[60:63],a[156:159],v[28:31],a[60:63]
EX v108,v108
MM a[52:55],a[156:159],v[20:23],a[52:55]
EX v109,v109
MM a[52:55],a[152:155],v[16:19],a[52:55]
RD a[232:235],v226 offset:8192
CV v78,v108,v109
MM a[44:47],a[152:155],v[8:11],a[44:47]
EX v110,v110
MM a[44:47],a[156:159],v[12:15],a[44:47]
buffer_load_dwordx4 v235, s[12:15], s81 offen offset:0 lds
MM a[36:39],a[156:159],v[4:7],a[36:39]
EX v111,v111
MM a[36:39],a[152:155],v[0:3],a[36:39]
RD a[236:239],v227 offset:8192
CV v79,v110,v111
MM a[64:67],a[160:163],v[0:3],a[64:67]
EX v112,v112
MM a[64:67],a[164:167],v[4:7],a[64:67]
EX v113,v113
MM a[72:75],a[164:167],v[12:15],a[72:75]
RD a[240:243],v224 offset:12288
CV v84,v112,v113
MM a[72:75],a[160:163],v[8:11],a[72:75]
buffer_load_dwordx4 v235, s[12:15], s64 offen offset:1024 lds
MM a[80:83],a[160:163],v[16:19],a[80:83]
s_waitcnt vmcnt(10)
v_mov_b32 v250, s69
s_mov_b64 exec, 1
buffer_store_dword v250, off, s[20:23], s97 offset:0 sc1
s_mov_b64 exec, -1
MM a[80:83],a[164:167],v[20:23],a[80:83]
s_add_u32 m0, s67, 0
s_add_u32 s85, s66, 0
buffer_load_dwordx4 v252, s[36:39], s85 offen offset:0 lds
MM a[88:91],a[164:167],v[28:31],a[88:91]
EX v114,v114
MM a[88:91],a[160:163],v[24:27],a[88:91]
EX v115,v115
MM a[92:95],a[168:171],v[24:27],a[92:95]
RD a[244:247],v225 offset:12288
CV v85,v114,v115
MM a[92:95],a[172:175],v[28:31],a[92:95]
EX v116,v116
MM a[84:87],a[172:175],v[20:23],a[84:87]
EX v117,v117
MM a[84:87],a[168:171],v[16:19],a[84:87]
RD a[248:251],v226 offset:12288
CV v86,v116,v117
MM a[76:79],a[168:171],v[8:11],a[76:79]
EX v118,v118
MM a[76:79],a[172:175],v[12:15],a[76:79]
EX v119,v119
MM a[68:71],a[172:175],v[4:7],a[68:71]
RD a[252:255],v227 offset:12288
CV v87,v118,v119
MM a[68:71],a[168:171],v[0:3],a[68:71]
s_add_u32 m0, s65, 98304
s_nop 0
buffer_load_dwordx4 v235, s[12:15], s81 offen offset:2048 lds
MM a[96:99],a[176:179],v[0:3],a[96:99]
EX v120,v120
MM a[96:99],a[180:183],v[4:7],a[96:99]
EX v121,v121
MM a[104:107],a[180:183],v[12:15],a[104:107]
EX v122,v122
MM a[104:107],a[176:179],v[8:11],a[104:107]
s_nop 0
LPG13_%=:
s_cbranch_execz LTR2_%=
buffer_load_dwordx4 v235, s[12:15], s64 offen offset:3072 lds
MM a[112:115],a[176:179],v[16:19],a[112:115]
EX v123,v123
MM a[112:115],a[180:183],v[20:23],a[112:115]
EX v124,v124
MM a[120:123],a[180:183],v[28:31],a[120:123]
EX v125,v125
MM a[120:123],a[176:179],v[24:27],a[120:123]
EX v126,v126
MM a[124:127],a[184:187],v[24:27],a[124:127]
v_add_f32 v244, v144, v145
v_add_f32 v245, v148, v149
MM a[124:127],a[188:191],v[28:31],a[124:127]
v_add_f32 v246, v152, v153
v_add_f32 v247, v156, v157
MM a[116:119],a[188:191],v[20:23],a[116:119]
EX v127,v127
MM a[116:119],a[184:187],v[16:19],a[116:119]
CV v92,v120,v121
CV v93,v122,v123
MM a[108:111],a[184:187],v[8:11],a[108:111]
CV v94,v124,v125
CV v95,v126,v127
MM a[108:111],a[188:191],v[12:15],a[108:111]
MM a[100:103],a[188:191],v[4:7],a[100:103]
MM a[100:103],a[184:187],v[0:3],a[100:103]
v_smfmac_f32_16x16x64_bf16 v[144:147], v[236:239], v[64:71], v234
v_smfmac_f32_16x16x64_bf16 v[148:151], v[236:239], v[72:79], v234
v_smfmac_f32_16x16x64_bf16 v[152:155], v[236:239], v[80:87], v234
v_smfmac_f32_16x16x64_bf16 v[156:159], v[236:239], v[88:95], v234
s_waitcnt lgkmcnt(0)
MM v[0:3],a[192:195],v[160:163],v[128:131]
s_add_u32 s64, s64, 16384
TR a[128:129],v232 offset:49152
s_add_u32 s81, s64, 896
MM v[0:3],a[196:199],v[176:179],v[0:3]
TR a[130:131],v232 offset:53248
v_max3_f32 v249, v144, v145, v148
MM v[0:3],a[200:203],v[192:195],v[0:3]
TR a[132:133],v232 offset:57344
v_max3_f32 v249, v249, v149, v152
MM v[0:3],a[204:207],v[208:211],v[0:3]
TR a[134:135],v232 offset:61440
v_max3_f32 v249, v249, v153, v156
MM v[4:7],a[220:223],v[208:211],v[128:131]
TR a[136:137],v233 offset:49152
v_max_f32 v249, v249, v157
MM v[4:7],a[216:219],v[192:195],v[4:7]
TR a[138:139],v233 offset:53248
v_cmp_lt_f32 vcc, s71, v249
MM v[4:7],a[212:215],v[176:179],v[4:7]
TR a[140:141],v233 offset:57344
TR a[142:143],v233 offset:61440
MM v[4:7],a[208:211],v[160:163],v[4:7]
s_cbranch_vccnz LSITE15_%=
LBACK15_%=:
MM v[12:15],a[208:211],v[164:167],v[132:135]
s_add_u32 m0, s67, 1024
s_add_u32 s85, s66, 55296
buffer_load_dwordx4 v252, s[36:39], s85 offen offset:0 lds
MM v[12:15],a[212:215],v[180:183],v[12:15]
EX v0,v0
MM v[12:15],a[216:219],v[196:199],v[12:15]
EX v1,v1
MM v[12:15],a[220:223],v[212:215],v[12:15]
TR a[144:145],v232 offset:49664
CV v0,v0,v1
MM v[8:11],a[204:207],v[212:215],v[132:135]
EX v2,v2
MM v[8:11],a[200:203],v[196:199],v[8:11]
EX v3,v3
MM v[8:11],a[196:199],v[180:183],v[8:11]
TR a[146:147],v232 offset:53760
)ASM"
R"ASM(
CV v1,v2,v3
MM v[8:11],a[192:195],v[164:167],v[8:11]
EX v4,v4
MM v[16:19],a[192:195],v[168:171],v[136:139]
TR a[148:149],v232 offset:57856
TR a[150:151],v232 offset:61952
MM v[16:19],a[196:199],v[184:187],v[16:19]
EX v5,v5
MM v[16:19],a[200:203],v[200:203],v[16:19]
TR a[152:153],v233 offset:49664
CV v2,v4,v5
MM v[16:19],a[204:207],v[216:219],v[16:19]
EX v6,v6
MM v[20:23],a[220:223],v[216:219],v[136:139]
EX v7,v7
MM v[20:23],a[216:219],v[200:203],v[20:23]
TR a[154:155],v233 offset:53760
CV v3,v6,v7
MM v[20:23],a[212:215],v[184:187],v[20:23]
EX v8,v8
MM v[20:23],a[208:211],v[168:171],v[20:23]
TR a[156:157],v233 offset:57856
TR a[158:159],v233 offset:61952
MM v[28:31],a[208:211],v[172:175],v[140:143]
EX v9,v9
MM v[28:31],a[212:215],v[188:191],v[28:31]
TR a[160:161],v232 offset:50176
CV v8,v8,v9
MM v[28:31],a[216:219],v[204:207],v[28:31]
EX v10,v10
MM v[28:31],a[220:223],v[220:223],v[28:31]
EX v11,v11
MM v[24:27],a[204:207],v[220:223],v[140:143]
TR a[162:163],v232 offset:54272
CV v9,v10,v11
MM v[24:27],a[200:203],v[204:207],v[24:27]
TR a[164:165],v232 offset:58368
TR a[166:167],v232 offset:62464
MM v[24:27],a[196:199],v[188:191],v[24:27]
EX v12,v12
MM v[24:27],a[192:195],v[172:175],v[24:27]
EX v13,v13
MM v[32:35],a[224:227],v[160:163],v[128:131]
TR a[168:169],v233 offset:50176
CV v10,v12,v13
MM v[32:35],a[228:231],v[176:179],v[32:35]
EX v14,v14
MM v[32:35],a[232:235],v[192:195],v[32:35]
TR a[170:171],v233 offset:54272
TR a[172:173],v233 offset:58368
MM v[32:35],a[236:239],v[208:211],v[32:35]
EX v15,v15
MM v[36:39],a[252:255],v[208:211],v[128:131]
TR a[174:175],v233 offset:62464
CV v11,v14,v15
MM v[36:39],a[248:251],v[192:195],v[36:39]
EX v16,v16
MM v[36:39],a[244:247],v[176:179],v[36:39]
EX v17,v17
MM v[36:39],a[240:243],v[160:163],v[36:39]
TR a[176:177],v232 offset:50688
CV v16,v16,v17
MM v[44:47],a[240:243],v[164:167],v[132:135]
s_add_u32 m0, s67, 2048
s_add_u32 s85, s66, 110592
buffer_load_dwordx4 v252, s[36:39], s85 offen offset:0 lds
MM v[44:47],a[244:247],v[180:183],v[44:47]
EX v18,v18
MM v[44:47],a[248:251],v[196:199],v[44:47]
TR a[178:179],v232 offset:54784
TR a[180:181],v232 offset:58880
MM v[44:47],a[252:255],v[212:215],v[44:47]
EX v19,v19
MM v[40:43],a[236:239],v[212:215],v[132:135]
TR a[182:183],v232 offset:62976
CV v17,v18,v19
MM v[40:43],a[232:235],v[196:199],v[40:43]
EX v20,v20
MM v[40:43],a[228:231],v[180:183],v[40:43]
EX v21,v21
MM v[40:43],a[224:227],v[164:167],v[40:43]
TR a[184:185],v233 offset:50688
CV v18,v20,v21
MM v[48:51],a[224:227],v[168:171],v[136:139]
EX v22,v22
MM v[48:51],a[228:231],v[184:187],v[48:51]
TR a[186:187],v233 offset:54784
TR a[188:189],v233 offset:58880
MM v[48:51],a[232:235],v[200:203],v[48:51]
EX v23,v23
MM v[48:51],a[236:239],v[216:219],v[48:51]
TR a[190:191],v233 offset:62976
CV v19,v22,v23
MM v[52:55],a[252:255],v[216:219],v[136:139]
EX v24,v24
MM v[52:55],a[248:251],v[200:203],v[52:55]
EX v25,v25
MM v[52:55],a[244:247],v[184:187],v[52:55]
EX v26,v26
MM v[52:55],a[240:243],v[168:171],v[52:55]
EX v27,v27
MM v[60:63],a[240:243],v[172:175],v[140:143]
EX v28,v28
MM v[60:63],a[244:247],v[188:191],v[60:63]
EX v29,v29
MM v[60:63],a[248:251],v[204:207],v[60:63]
EX v30,v30
MM v[60:63],a[252:255],v[220:223],v[60:63]
EX v31,v31
MM v[56:59],a[236:239],v[220:223],v[140:143]
EX v32,v32
MM v[56:59],a[232:235],v[204:207],v[56:59]
EX v33,v33
MM v[56:59],a[228:231],v[188:191],v[56:59]
EX v34,v34
MM v[56:59],a[224:227],v[172:175],v[56:59]
s_waitcnt lgkmcnt(0)
s_barrier
MM a[0:3],a[128:131],v[64:67],a[0:3]
s_add_u32 m0, s65, 49152
CV v24,v24,v25
CV v25,v26,v27
MM a[0:3],a[132:135],v[68:71],a[0:3]
RD a[192:195],v224 offset:16384
CV v26,v28,v29
MM a[8:11],a[132:135],v[76:79],a[8:11]
RD a[196:199],v225 offset:16384
CV v27,v30,v31
MM a[8:11],a[128:131],v[72:75],a[8:11]
buffer_load_dwordx4 v228, s[8:11], s64 offen offset:0 lds
MM a[16:19],a[128:131],v[80:83],a[16:19]
s_add_u32 m0, s67, 3072
s_add_u32 s85, s66, 165888
buffer_load_dwordx4 v252, s[36:39], s85 offen offset:0 lds
MM a[16:19],a[132:135],v[84:87],a[16:19]
RD a[200:203],v226 offset:16384
CV v4,v32,v33
MM a[24:27],a[132:135],v[92:95],a[24:27]
RD a[204:207],v227 offset:16384
RD a[208:211],v224 offset:20480
MM a[24:27],a[128:131],v[88:91],a[24:27]
s_add_u32 m0, s65, 49152
s_nop 0
buffer_load_dwordx4 v229, s[8:11], s64 offen offset:1024 lds
MM a[28:31],a[136:139],v[88:91],a[28:31]
EX v35,v35
MM a[28:31],a[140:143],v[92:95],a[28:31]
RD a[212:215],v225 offset:20480
CV v5,v34,v35
MM a[20:23],a[140:143],v[84:87],a[20:23]
EX v36,v36
MM a[20:23],a[136:139],v[80:83],a[20:23]
EX v37,v37
MM a[12:15],a[136:139],v[72:75],a[12:15]
RD a[216:219],v226 offset:20480
CV v6,v36,v37
MM a[12:15],a[140:143],v[76:79],a[12:15]
buffer_load_dwordx4 v230, s[8:11], s64 offen offset:2048 lds
MM a[4:7],a[140:143],v[68:71],a[4:7]
EX v38,v38
MM a[4:7],a[136:139],v[64:67],a[4:7]
EX v39,v39
MM a[32:35],a[144:147],v[64:67],a[32:35]
RD a[220:223],v227 offset:20480
CV v7,v38,v39
MM a[32:35],a[148:151],v[68:71],a[32:35]
EX v40,v40
MM a[40:43],a[148:151],v[76:79],a[40:43]
EX v41,v41
MM a[40:43],a[144:147],v[72:75],a[40:43]
RD a[224:227],v224 offset:24576
CV v12,v40,v41
MM a[48:51],a[144:147],v[80:83],a[48:51]
buffer_load_dwordx4 v231, s[8:11], s64 offen offset:3072 lds
MM a[48:51],a[148:151],v[84:87],a[48:51]
s_add_u32 m0, s65, 114688
EX v42,v42
MM a[56:59],a[148:151],v[92:95],a[56:59]
EX v43,v43
MM a[56:59],a[144:147],v[88:91],a[56:59]
RD a[228:231],v225 offset:24576
CV v13,v42,v43
MM a[60:63],a[152:155],v[88:91],a[60:63]
EX v44,v44
MM a[60:63],a[156:159],v[92:95],a[60:63]
buffer_load_dwordx4 v235, s[12:15], s81 offen offset:0 lds
MM a[52:55],a[156:159],v[84:87],a[52:55]
EX v45,v45
MM a[52:55],a[152:155],v[80:83],a[52:55]
RD a[232:235],v226 offset:24576
CV v14,v44,v45
MM a[44:47],a[152:155],v[72:75],a[44:47]
EX v46,v46
MM a[44:47],a[156:159],v[76:79],a[44:47]
EX v47,v47
MM a[36:39],a[156:159],v[68:71],a[36:39]
RD a[236:239],v227 offset:24576
CV v15,v46,v47
MM a[36:39],a[152:155],v[64:67],a[36:39]
EX v48,v48
MM a[64:67],a[160:163],v[64:67],a[64:67]
buffer_load_dwordx4 v235, s[12:15], s64 offen offset:1024 lds
MM a[64:67],a[164:167],v[68:71],a[64:67]
EX v49,v49
MM a[72:75],a[164:167],v[76:79],a[72:75]
RD a[240:243],v224 offset:28672
CV v20,v48,v49
MM a[72:75],a[160:163],v[72:75],a[72:75]
EX v50,v50
MM a[80:83],a[160:163],v[80:83],a[80:83]
EX v51,v51
MM a[80:83],a[164:167],v[84:87],a[80:83]
RD a[244:247],v225 offset:28672
CV v21,v50,v51
MM a[88:91],a[164:167],v[92:95],a[88:91]
EX v52,v52
MM a[88:91],a[160:163],v[88:91],a[88:91]
buffer_load_dwordx4 v235, s[12:15], s81 offen offset:2048 lds
MM a[92:95],a[168:171],v[88:91],a[92:95]
EX v53,v53
MM a[92:95],a[172:175],v[92:95],a[92:95]
RD a[248:251],v226 offset:28672
CV v22,v52,v53
MM a[84:87],a[172:175],v[84:87],a[84:87]
EX v54,v54
MM a[84:87],a[168:171],v[80:83],a[84:87]
EX v55,v55
MM a[76:79],a[168:171],v[72:75],a[76:79]
RD a[252:255],v227 offset:28672
CV v23,v54,v55
MM a[76:79],a[172:175],v[76:79],a[76:79]
EX v56,v56
MM a[68:71],a[172:175],v[68:71],a[68:71]
buffer_load_dwordx4 v235, s[12:15], s64 offen offset:3072 lds
MM a[68:71],a[168:171],v[64:67],a[68:71]
EX v57,v57
MM a[96:99],a[176:179],v[64:67],a[96:99]
EX v58,v58
MM a[96:99],a[180:183],v[68:71],a[96:99]
EX v59,v59
MM a[104:107],a[180:183],v[76:79],a[104:107]
EX v60,v60
MM a[104:107],a[176:179],v[72:75],a[104:107]
EX v61,v61
MM a[112:115],a[176:179],v[80:83],a[112:115]
EX v62,v62
MM a[112:115],a[180:183],v[84:87],a[112:115]
EX v63,v63
MM a[120:123],a[180:183],v[92:95],a[120:123]
s_add_u32 m0, s67, 4096
s_add_u32 s85, s66, 221184
buffer_load_dwordx4 v252, s[36:39], s85 offen offset:0 lds
MM a[120:123],a[176:179],v[88:91],a[120:123]
CV v28,v56,v57
CV v29,v58,v59
MM a[124:127],a[184:187],v[88:91],a[124:127]
v_add_f32 v244, v144, v145
v_add_f32 v245, v148, v149
MM a[124:127],a[188:191],v[92:95],a[124:127]
v_add_f32 v246, v152, v153
v_add_f32 v247, v156, v157
MM a[116:119],a[188:191],v[84:87],a[116:119]
CV v30,v60,v61
CV v31,v62,v63
MM a[116:119],a[184:187],v[80:83],a[116:119]
MM a[108:111],a[184:187],v[72:75],a[108:111]
MM a[108:111],a[188:191],v[76:79],a[108:111]
MM a[100:103],a[188:191],v[68:71],a[100:103]
MM a[100:103],a[184:187],v[64:67],a[100:103]
v_smfmac_f32_16x16x64_bf16 v[144:147], v[236:239], v[0:7], v234
v_smfmac_f32_16x16x64_bf16 v[148:151], v[236:239], v[8:15], v234
v_smfmac_f32_16x16x64_bf16 v[152:155], v[236:239], v[16:23], v234
v_smfmac_f32_16x16x64_bf16 v[156:159], v[236:239], v[24:31], v234
s_waitcnt lgkmcnt(0)
MM v[64:67],a[192:195],v[160:163],v[128:131]
s_add_u32 s64, s64, 16384
TR a[128:129],v232 offset:0
s_add_u32 s81, s64, 896
MM v[64:67],a[196:199],v[176:179],v[64:67]
TR a[130:131],v232 offset:4096
v_max3_f32 v249, v144, v145, v148
MM v[64:67],a[200:203],v[192:195],v[64:67]
TR a[132:133],v232 offset:8192
v_max3_f32 v249, v249, v149, v152
MM v[64:67],a[204:207],v[208:211],v[64:67]
TR a[134:135],v232 offset:12288
v_max3_f32 v249, v249, v153, v156
MM v[68:71],a[220:223],v[208:211],v[128:131]
TR a[136:137],v233 offset:0
v_max_f32 v249, v249, v157
MM v[68:71],a[216:219],v[192:195],v[68:71]
TR a[138:139],v233 offset:4096
v_cmp_lt_f32 vcc, s71, v249
MM v[68:71],a[212:215],v[176:179],v[68:71]
TR a[140:141],v233 offset:8192
TR a[142:143],v233 offset:12288
MM v[68:71],a[208:211],v[160:163],v[68:71]
s_cbranch_vccnz LSITE16_%=
LBACK16_%=:
MM v[76:79],a[208:211],v[164:167],v[132:135]
EX v64,v64
MM v[76:79],a[212:215],v[180:183],v[76:79]
EX v65,v65
MM v[76:79],a[216:219],v[196:199],v[76:79]
TR a[144:145],v232 offset:512
CV v64,v64,v65
MM v[76:79],a[220:223],v[212:215],v[76:79]
EX v66,v66
MM v[72:75],a[204:207],v[212:215],v[132:135]
EX v67,v67
MM v[72:75],a[200:203],v[196:199],v[72:75]
TR a[146:147],v232 offset:4608
CV v65,v66,v67
MM v[72:75],a[196:199],v[180:183],v[72:75]
EX v68,v68
MM v[72:75],a[192:195],v[164:167],v[72:75]
TR a[148:149],v232 offset:8704
TR a[150:151],v232 offset:12800
MM v[80:83],a[192:195],v[168:171],v[136:139]
EX v69,v69
MM v[80:83],a[196:199],v[184:187],v[80:83]
TR a[152:153],v233 offset:512
CV v66,v68,v69
MM v[80:83],a[200:203],v[200:203],v[80:83]
EX v70,v70
MM v[80:83],a[204:207],v[216:219],v[80:83]
TR a[154:155],v233 offset:4608
TR a[156:157],v233 offset:8704
MM v[84:87],a[220:223],v[216:219],v[136:139]
s_add_u32 s87, s95, 1024
buffer_store_dword v240, v248, s[20:23], s87 offen offset:12 sc1
MM v[84:87],a[216:219],v[200:203],v[84:87]
EX v71,v71
MM v[84:87],a[212:215],v[184:187],v[84:87]
TR a[158:159],v233 offset:12800
CV v67,v70,v71
MM v[84:87],a[208:211],v[168:171],v[84:87]
EX v72,v72
MM v[92:95],a[208:211],v[172:175],v[140:143]
EX v73,v73
MM v[92:95],a[212:215],v[188:191],v[92:95]
TR a[160:161],v232 offset:1024
CV v72,v72,v73
MM v[92:95],a[216:219],v[204:207],v[92:95]
EX v74,v74
MM v[92:95],a[220:223],v[220:223],v[92:95]
EX v75,v75
MM v[88:91],a[204:207],v[220:223],v[140:143]
TR a[162:163],v232 offset:5120
CV v73,v74,v75
MM v[88:91],a[200:203],v[204:207],v[88:91]
TR a[164:165],v232 offset:9216
TR a[166:167],v232 offset:13312
MM v[88:91],a[196:199],v[188:191],v[88:91]
EX v76,v76
MM v[88:91],a[192:195],v[172:175],v[88:91]
EX v77,v77
MM v[96:99],a[224:227],v[160:163],v[128:131]
TR a[168:169],v233 offset:1024
CV v74,v76,v77
MM v[96:99],a[228:231],v[176:179],v[96:99]
EX v78,v78
MM v[96:99],a[232:235],v[192:195],v[96:99]
TR a[170:171],v233 offset:5120
TR a[172:173],v233 offset:9216
MM v[96:99],a[236:239],v[208:211],v[96:99]
EX v79,v79
MM v[100:103],a[252:255],v[208:211],v[128:131]
TR a[174:175],v233 offset:13312
CV v75,v78,v79
MM v[100:103],a[248:251],v[192:195],v[100:103]
EX v80,v80
MM v[100:103],a[244:247],v[176:179],v[100:103]
EX v81,v81
MM v[100:103],a[240:243],v[160:163],v[100:103]
TR a[176:177],v232 offset:1536
CV v80,v80,v81
MM v[108:111],a[240:243],v[164:167],v[132:135]
EX v82,v82
MM v[108:111],a[244:247],v[180:183],v[108:111]
TR a[178:179],v232 offset:5632
TR a[180:181],v232 offset:9728
MM v[108:111],a[248:251],v[196:199],v[108:111]
EX v83,v83
MM v[108:111],a[252:255],v[212:215],v[108:111]
TR a[182:183],v232 offset:13824
CV v81,v82,v83
MM v[104:107],a[236:239],v[212:215],v[132:135]
EX v84,v84
MM v[104:107],a[232:235],v[196:199],v[104:107]
)ASM"
R"ASM(
EX v85,v85
MM v[104:107],a[228:231],v[180:183],v[104:107]
TR a[184:185],v233 offset:1536
CV v82,v84,v85
MM v[104:107],a[224:227],v[164:167],v[104:107]
EX v86,v86
MM v[112:115],a[224:227],v[168:171],v[136:139]
TR a[186:187],v233 offset:5632
TR a[188:189],v233 offset:9728
MM v[112:115],a[228:231],v[184:187],v[112:115]
EX v87,v87
MM v[112:115],a[232:235],v[200:203],v[112:115]
TR a[190:191],v233 offset:13824
CV v83,v86,v87
MM v[112:115],a[236:239],v[216:219],v[112:115]
EX v88,v88
MM v[116:119],a[252:255],v[216:219],v[136:139]
EX v89,v89
MM v[116:119],a[248:251],v[200:203],v[116:119]
EX v90,v90
MM v[116:119],a[244:247],v[184:187],v[116:119]
EX v91,v91
MM v[116:119],a[240:243],v[168:171],v[116:119]
EX v92,v92
MM v[124:127],a[240:243],v[172:175],v[140:143]
EX v93,v93
MM v[124:127],a[244:247],v[188:191],v[124:127]
EX v94,v94
MM v[124:127],a[248:251],v[204:207],v[124:127]
EX v95,v95
MM v[124:127],a[252:255],v[220:223],v[124:127]
EX v96,v96
MM v[120:123],a[236:239],v[220:223],v[140:143]
EX v97,v97
MM v[120:123],a[232:235],v[204:207],v[120:123]
EX v98,v98
MM v[120:123],a[228:231],v[188:191],v[120:123]
EX v99,v99
MM v[120:123],a[224:227],v[172:175],v[120:123]
s_waitcnt vmcnt(19) lgkmcnt(0)
s_barrier
MM a[0:3],a[128:131],v[0:3],a[0:3]
s_add_u32 m0, s65, 0
CV v88,v88,v89
CV v89,v90,v91
MM a[0:3],a[132:135],v[4:7],a[0:3]
RD a[192:195],v224 offset:32768
CV v90,v92,v93
MM a[8:11],a[132:135],v[12:15],a[8:11]
RD a[196:199],v225 offset:32768
CV v91,v94,v95
MM a[8:11],a[128:131],v[8:11],a[8:11]
buffer_load_dwordx4 v228, s[8:11], s64 offen offset:0 lds
MM a[16:19],a[128:131],v[16:19],a[16:19]
RD a[200:203],v226 offset:32768
CV v68,v96,v97
MM a[16:19],a[132:135],v[20:23],a[16:19]
RD a[204:207],v227 offset:32768
CV v69,v98,v99
MM a[24:27],a[132:135],v[28:31],a[24:27]
buffer_load_dwordx4 v229, s[8:11], s64 offen offset:1024 lds
MM a[24:27],a[128:131],v[24:27],a[24:27]
RD a[208:211],v224 offset:36864
RD a[212:215],v225 offset:36864
MM a[28:31],a[136:139],v[24:27],a[28:31]
buffer_load_dwordx4 v230, s[8:11], s64 offen offset:2048 lds
MM a[28:31],a[140:143],v[28:31],a[28:31]
EX v100,v100
MM a[20:23],a[140:143],v[20:23],a[20:23]
EX v101,v101
MM a[20:23],a[136:139],v[16:19],a[20:23]
RD a[216:219],v226 offset:36864
CV v70,v100,v101
MM a[12:15],a[136:139],v[8:11],a[12:15]
EX v102,v102
MM a[12:15],a[140:143],v[12:15],a[12:15]
RD a[220:223],v227 offset:36864
RD a[224:227],v224 offset:40960
MM a[4:7],a[140:143],v[4:7],a[4:7]
EX v103,v103
MM a[4:7],a[136:139],v[0:3],a[4:7]
RD a[228:231],v225 offset:40960
CV v71,v102,v103
MM a[32:35],a[144:147],v[0:3],a[32:35]
EX v104,v104
MM a[32:35],a[148:151],v[4:7],a[32:35]
buffer_load_dwordx4 v231, s[8:11], s64 offen offset:3072 lds
MM a[40:43],a[148:151],v[12:15],a[40:43]
s_add_u32 m0, s65, 65536
EX v105,v105
MM a[40:43],a[144:147],v[8:11],a[40:43]
RD a[232:235],v226 offset:40960
CV v76,v104,v105
MM a[48:51],a[144:147],v[16:19],a[48:51]
EX v106,v106
MM a[48:51],a[148:151],v[20:23],a[48:51]
EX v107,v107
MM a[56:59],a[148:151],v[28:31],a[56:59]
RD a[236:239],v227 offset:40960
CV v77,v106,v107
MM a[56:59],a[144:147],v[24:27],a[56:59]
EX v108,v108
MM a[60:63],a[152:155],v[24:27],a[60:63]
buffer_load_dwordx4 v235, s[12:15], s81 offen offset:0 lds
MM a[60:63],a[156:159],v[28:31],a[60:63]
EX v109,v109
MM a[52:55],a[156:159],v[20:23],a[52:55]
s_add_u32 s87, s95, 2048
buffer_store_dword v240, v248, s[20:23], s87 offen offset:12 sc1
MM a[52:55],a[152:155],v[16:19],a[52:55]
RD a[240:243],v224 offset:45056
CV v78,v108,v109
MM a[44:47],a[152:155],v[8:11],a[44:47]
EX v110,v110
MM a[44:47],a[156:159],v[12:15],a[44:47]
EX v111,v111
MM a[36:39],a[156:159],v[4:7],a[36:39]
RD a[244:247],v225 offset:45056
CV v79,v110,v111
MM a[36:39],a[152:155],v[0:3],a[36:39]
LPG14_%=:
s_cbranch_execz LTR2_%=
EX v112,v112
MM a[64:67],a[160:163],v[0:3],a[64:67]
buffer_load_dwordx4 v235, s[12:15], s64 offen offset:1024 lds
MM a[64:67],a[164:167],v[4:7],a[64:67]
EX v113,v113
MM a[72:75],a[164:167],v[12:15],a[72:75]
RD a[248:251],v226 offset:45056
CV v84,v112,v113
MM a[72:75],a[160:163],v[8:11],a[72:75]
EX v114,v114
MM a[80:83],a[160:163],v[16:19],a[80:83]
EX v115,v115
MM a[80:83],a[164:167],v[20:23],a[80:83]
RD a[252:255],v227 offset:45056
CV v85,v114,v115
MM a[88:91],a[164:167],v[28:31],a[88:91]
EX v116,v116
MM a[88:91],a[160:163],v[24:27],a[88:91]
buffer_load_dwordx4 v235, s[12:15], s81 offen offset:2048 lds
MM a[92:95],a[168:171],v[24:27],a[92:95]
EX v117,v117
MM a[92:95],a[172:175],v[28:31],a[92:95]
EX v118,v118
MM a[84:87],a[172:175],v[20:23],a[84:87]
buffer_load_dwordx4 v235, s[12:15], s64 offen offset:3072 lds
MM a[84:87],a[168:171],v[16:19],a[84:87]
EX v119,v119
MM a[76:79],a[168:171],v[8:11],a[76:79]
EX v120,v120
MM a[76:79],a[172:175],v[12:15],a[76:79]
EX v121,v121
MM a[68:71],a[172:175],v[4:7],a[68:71]
EX v122,v122
MM a[68:71],a[168:171],v[0:3],a[68:71]
EX v123,v123
MM a[96:99],a[176:179],v[0:3],a[96:99]
EX v124,v124
MM a[96:99],a[180:183],v[4:7],a[96:99]
EX v125,v125
MM a[104:107],a[180:183],v[12:15],a[104:107]
EX v126,v126
MM a[104:107],a[176:179],v[8:11],a[104:107]
EX v127,v127
MM a[112:115],a[176:179],v[16:19],a[112:115]
CV v86,v116,v117
CV v87,v118,v119
MM a[112:115],a[180:183],v[20:23],a[112:115]
CV v92,v120,v121
CV v93,v122,v123
MM a[120:123],a[180:183],v[28:31],a[120:123]
CV v94,v124,v125
CV v95,v126,v127
MM a[120:123],a[176:179],v[24:27],a[120:123]
MM a[124:127],a[184:187],v[24:27],a[124:127]
v_add_f32 v244, v144, v145
v_add_f32 v245, v148, v149
MM a[124:127],a[188:191],v[28:31],a[124:127]
v_add_f32 v246, v152, v153
v_add_f32 v247, v156, v157
MM a[116:119],a[188:191],v[20:23],a[116:119]
MM a[116:119],a[184:187],v[16:19],a[116:119]
MM a[108:111],a[184:187],v[8:11],a[108:111]
MM a[108:111],a[188:191],v[12:15],a[108:111]
MM a[100:103],a[188:191],v[4:7],a[100:103]
MM a[100:103],a[184:187],v[0:3],a[100:103]
v_smfmac_f32_16x16x64_bf16 v[144:147], v[236:239], v[64:71], v234
v_smfmac_f32_16x16x64_bf16 v[148:151], v[236:239], v[72:79], v234
v_smfmac_f32_16x16x64_bf16 v[152:155], v[236:239], v[80:87], v234
v_smfmac_f32_16x16x64_bf16 v[156:159], v[236:239], v[88:95], v234
s_waitcnt lgkmcnt(0)
MM v[0:3],a[192:195],v[160:163],v[128:131]
s_add_u32 s64, s64, 16384
TR a[128:129],v232 offset:16384
s_add_u32 s81, s64, 896
MM v[0:3],a[196:199],v[176:179],v[0:3]
TR a[130:131],v232 offset:20480
v_max3_f32 v249, v144, v145, v148
MM v[0:3],a[200:203],v[192:195],v[0:3]
TR a[132:133],v232 offset:24576
v_max3_f32 v249, v249, v149, v152
MM v[0:3],a[204:207],v[208:211],v[0:3]
TR a[134:135],v232 offset:28672
v_max3_f32 v249, v249, v153, v156
MM v[4:7],a[220:223],v[208:211],v[128:131]
TR a[136:137],v233 offset:16384
v_max_f32 v249, v249, v157
MM v[4:7],a[216:219],v[192:195],v[4:7]
TR a[138:139],v233 offset:20480
v_cmp_lt_f32 vcc, s71, v249
MM v[4:7],a[212:215],v[176:179],v[4:7]
TR a[140:141],v233 offset:24576
TR a[142:143],v233 offset:28672
MM v[4:7],a[208:211],v[160:163],v[4:7]
s_cbranch_vccnz LSITE17_%=
LBACK17_%=:
MM v[12:15],a[208:211],v[164:167],v[132:135]
v_lshrrev_b32 v127, 8, v248
v_lshlrev_b32 v124, 9, v127
MM v[12:15],a[212:215],v[180:183],v[12:15]
v_and_b32 v126, 192, v248
v_add_u32 v124, v124, v126
MM v[12:15],a[216:219],v[196:199],v[12:15]
v_bfe_u32 v126, v248, 4, 2
v_lshl_add_u32 v124, v126, 3, v124
MM v[12:15],a[220:223],v[212:215],v[12:15]
s_lshr_b32 s76, %[swave], 1
EX v0,v0
MM v[8:11],a[204:207],v[212:215],v[132:135]
s_mul_i32 s76, s76, 10240
EX v1,v1
MM v[8:11],a[200:203],v[196:199],v[8:11]
s_and_b32 s77, %[swave], 1
EX v2,v2
MM v[8:11],a[196:199],v[180:183],v[8:11]
s_lshl_b32 s77, s77, 5
TR a[144:145],v232 offset:16896
CV v0,v0,v1
MM v[8:11],a[192:195],v[164:167],v[8:11]
s_add_u32 s76, s76, s77
EX v3,v3
MM v[16:19],a[192:195],v[168:171],v[136:139]
s_add_u32 s76, s76, %[slds]
EX v4,v4
MM v[16:19],a[196:199],v[184:187],v[16:19]
s_add_u32 s76, s76, 131072
v_add_u32 v124, s76, v124
v_bfe_u32 v126, v248, 4, 3
MM v[16:19],a[200:203],v[200:203],v[16:19]
v_lshl_add_u32 v126, v127, 3, v126
v_lshlrev_b32 v126, 4, v126
MM v[16:19],a[204:207],v[216:219],v[16:19]
s_mul_i32 s76, %[swave], 3072
EX v5,v5
MM v[20:23],a[220:223],v[216:219],v[136:139]
s_add_u32 s76, s76, %[slds]
TR a[146:147],v232 offset:20992
CV v1,v2,v3
MM v[20:23],a[216:219],v[200:203],v[20:23]
s_add_u32 s76, s76, 151552
TR a[148:149],v232 offset:25088
v_add_u32 v125, s76, v126
MM v[20:23],a[212:215],v[184:187],v[20:23]
s_lshr_b32 s76, s48, 7
EX v6,v6
MM v[20:23],a[208:211],v[168:171],v[20:23]
s_lshl_b32 s76, s76, 5
EX v7,v7
MM v[28:31],a[208:211],v[172:175],v[140:143]
s_and_b32 s77, s48, 16
TR a[150:151],v232 offset:29184
TR a[152:153],v233 offset:16896
MM v[28:31],a[212:215],v[188:191],v[28:31]
s_add_u32 s76, s76, s77
EX v8,v8
MM v[28:31],a[216:219],v[204:207],v[28:31]
s_bfe_u32 s77, s48, 131077
TR a[154:155],v233 offset:20992
CV v2,v4,v5
MM v[28:31],a[220:223],v[220:223],v[28:31]
s_lshl_b32 s77, s77, 2
EX v9,v9
MM v[24:27],a[204:207],v[220:223],v[140:143]
s_add_u32 s76, s76, s77
TR a[156:157],v233 offset:25088
CV v3,v6,v7
MM v[24:27],a[200:203],v[204:207],v[24:27]
s_add_u32 s76, s76, %[swave]
EX v10,v10
MM v[24:27],a[196:199],v[188:191],v[24:27]
s_lshl_b32 s97, s76, 2
TR a[158:159],v233 offset:29184
CV v8,v8,v9
MM v[24:27],a[192:195],v[172:175],v[24:27]
s_add_u32 s97, s97, 77824
EX v11,v11
MM v[32:35],a[224:227],v[160:163],v[128:131]
s_lshl_b32 s76, s76, 9
TR a[160:161],v232 offset:17408
TR a[162:163],v232 offset:21504
MM v[32:35],a[228:231],v[176:179],v[32:35]
s_add_u32 s76, s76, 22183936
EX v12,v12
MM v[32:35],a[232:235],v[192:195],v[32:35]
EX v13,v13
MM v[32:35],a[236:239],v[208:211],v[32:35]
TR a[164:165],v232 offset:25600
CV v9,v10,v11
MM v[36:39],a[252:255],v[208:211],v[128:131]
EX v14,v14
MM v[36:39],a[248:251],v[192:195],v[36:39]
TR a[166:167],v232 offset:29696
CV v10,v12,v13
MM v[36:39],a[244:247],v[176:179],v[36:39]
EX v15,v15
MM v[36:39],a[240:243],v[160:163],v[36:39]
TR a[168:169],v233 offset:17408
TR a[170:171],v233 offset:21504
MM v[44:47],a[240:243],v[164:167],v[132:135]
EX v16,v16
MM v[44:47],a[244:247],v[180:183],v[44:47]
EX v17,v17
MM v[44:47],a[248:251],v[196:199],v[44:47]
TR a[172:173],v233 offset:25600
CV v11,v14,v15
MM v[44:47],a[252:255],v[212:215],v[44:47]
EX v18,v18
MM v[40:43],a[236:239],v[212:215],v[132:135]
TR a[174:175],v233 offset:29696
CV v16,v16,v17
MM v[40:43],a[232:235],v[196:199],v[40:43]
EX v19,v19
MM v[40:43],a[228:231],v[180:183],v[40:43]
TR a[176:177],v232 offset:17920
TR a[178:179],v232 offset:22016
MM v[40:43],a[224:227],v[164:167],v[40:43]
EX v20,v20
MM v[48:51],a[224:227],v[168:171],v[136:139]
EX v21,v21
MM v[48:51],a[228:231],v[184:187],v[48:51]
TR a[180:181],v232 offset:26112
CV v17,v18,v19
MM v[48:51],a[232:235],v[200:203],v[48:51]
EX v22,v22
MM v[48:51],a[236:239],v[216:219],v[48:51]
TR a[182:183],v232 offset:30208
CV v18,v20,v21
MM v[52:55],a[252:255],v[216:219],v[136:139]
EX v23,v23
MM v[52:55],a[248:251],v[200:203],v[52:55]
TR a[184:185],v233 offset:17920
TR a[186:187],v233 offset:22016
MM v[52:55],a[244:247],v[184:187],v[52:55]
EX v24,v24
MM v[52:55],a[240:243],v[168:171],v[52:55]
TR a[188:189],v233 offset:26112
CV v19,v22,v23
MM v[60:63],a[240:243],v[172:175],v[140:143]
EX v25,v25
MM v[60:63],a[244:247],v[188:191],v[60:63]
TR a[190:191],v233 offset:30208
CV v24,v24,v25
MM v[60:63],a[248:251],v[204:207],v[60:63]
EX v26,v26
MM v[60:63],a[252:255],v[220:223],v[60:63]
EX v27,v27
MM v[56:59],a[236:239],v[220:223],v[140:143]
EX v28,v28
MM v[56:59],a[232:235],v[204:207],v[56:59]
EX v29,v29
MM v[56:59],a[228:231],v[188:191],v[56:59]
EX v30,v30
MM v[56:59],a[224:227],v[172:175],v[56:59]
s_waitcnt vmcnt(10) lgkmcnt(0)
s_barrier
MM a[0:3],a[128:131],v[64:67],a[0:3]
s_add_u32 m0, s65, 16384
CV v25,v26,v27
CV v26,v28,v29
MM a[0:3],a[132:135],v[68:71],a[0:3]
RD a[192:195],v224 offset:49152
RD a[196:199],v225 offset:49152
MM a[8:11],a[132:135],v[76:79],a[8:11]
EX v31,v31
MM a[8:11],a[128:131],v[72:75],a[8:11]
RD a[200:203],v226 offset:49152
CV v27,v30,v31
MM a[16:19],a[128:131],v[80:83],a[16:19]
TR v[96:97],v124 offset:0
TR v[98:99],v124 offset:256
MM a[16:19],a[132:135],v[84:87],a[16:19]
RD v[108:111],v125 offset:0
)ASM"
R"ASM(
TR v[100:101],v124 offset:2048
MM a[24:27],a[132:135],v[92:95],a[24:27]
TR v[102:103],v124 offset:2304
RD v[112:115],v125 offset:512
MM a[24:27],a[128:131],v[88:91],a[24:27]
TR v[104:105],v124 offset:4096
TR v[106:107],v124 offset:4352
MM a[28:31],a[136:139],v[88:91],a[28:31]
RD v[116:119],v125 offset:1024
RD a[204:207],v227 offset:49152
EX v32,v32
MM a[28:31],a[140:143],v[92:95],a[28:31]
EX v33,v33
MM a[20:23],a[140:143],v[84:87],a[20:23]
EX v34,v34
MM a[20:23],a[136:139],v[80:83],a[20:23]
buffer_load_dwordx4 v228, s[8:11], s64 offen offset:0 lds
MM a[12:15],a[136:139],v[72:75],a[12:15]
EX v35,v35
MM a[12:15],a[140:143],v[76:79],a[12:15]
EX v36,v36
MM a[4:7],a[140:143],v[68:71],a[4:7]
CV v4,v32,v33
CV v5,v34,v35
MM a[4:7],a[136:139],v[64:67],a[4:7]
EX v37,v37
MM a[32:35],a[144:147],v[64:67],a[32:35]
EX v38,v38
MM a[32:35],a[148:151],v[68:71],a[32:35]
s_waitcnt lgkmcnt(1)
buffer_load_dwordx4 v229, s[8:11], s64 offen offset:1024 lds
MM a[40:43],a[148:151],v[76:79],a[40:43]
EX v39,v39
MM v[120:123],v[96:99],v[108:111],0
MM v[120:123],v[100:103],v[112:115],v[120:123]
MM v[120:123],v[104:107],v[116:119],v[120:123]
MM a[40:43],a[144:147],v[72:75],a[40:43]
TR v[96:97],v124 offset:6144
TR v[98:99],v124 offset:6400
MM a[48:51],a[144:147],v[80:83],a[48:51]
RD v[108:111],v125 offset:1536
TR v[100:101],v124 offset:8192
MM a[48:51],a[148:151],v[84:87],a[48:51]
TR v[102:103],v124 offset:8448
RD v[112:115],v125 offset:2048
MM a[56:59],a[148:151],v[92:95],a[56:59]
EX v40,v40
MM a[56:59],a[144:147],v[88:91],a[56:59]
EX v41,v41
MM a[60:63],a[152:155],v[88:91],a[60:63]
CV v6,v36,v37
CV v7,v38,v39
MM a[60:63],a[156:159],v[92:95],a[60:63]
EX v42,v42
MM a[52:55],a[156:159],v[84:87],a[52:55]
RD a[208:211],v224 offset:53248
RD a[212:215],v225 offset:53248
MM a[52:55],a[152:155],v[80:83],a[52:55]
EX v43,v43
MM a[44:47],a[152:155],v[72:75],a[44:47]
buffer_load_dwordx4 v230, s[8:11], s64 offen offset:2048 lds
MM a[44:47],a[156:159],v[76:79],a[44:47]
EX v44,v44
MM a[36:39],a[156:159],v[68:71],a[36:39]
s_waitcnt lgkmcnt(2)
EX v45,v45
MM a[36:39],a[152:155],v[64:67],a[36:39]
CV v12,v40,v41
CV v13,v42,v43
MM v[120:123],v[96:99],v[108:111],v[120:123]
MM v[120:123],v[100:103],v[112:115],v[120:123]
MM a[64:67],a[160:163],v[64:67],a[64:67]
EX v46,v46
MM a[64:67],a[164:167],v[68:71],a[64:67]
RD a[216:219],v226 offset:53248
CV v14,v44,v45
MM a[72:75],a[164:167],v[76:79],a[72:75]
EX v47,v47
MM a[72:75],a[160:163],v[72:75],a[72:75]
buffer_load_dwordx4 v231, s[8:11], s64 offen offset:3072 lds
MM a[80:83],a[160:163],v[80:83],a[80:83]
s_add_u32 m0, s65, 81920
EX v48,v48
MM a[80:83],a[164:167],v[84:87],a[80:83]
RD a[220:223],v227 offset:53248
CV v15,v46,v47
MM a[88:91],a[164:167],v[92:95],a[88:91]
s_mov_b32 exec_lo, 0x00ff00ff
s_mov_b32 exec_hi, 0x00ff00ff
buffer_store_dwordx4 v[120:123], v126, s[20:23], s76 offen offset:0 sc1
s_mov_b64 exec, -1
s_nop 0
MM a[88:91],a[160:163],v[88:91],a[88:91]
EX v49,v49
MM a[92:95],a[168:171],v[88:91],a[92:95]
EX v50,v50
MM a[92:95],a[172:175],v[92:95],a[92:95]
EX v51,v51
MM a[84:87],a[172:175],v[84:87],a[84:87]
RD a[224:227],v224 offset:57344
CV v20,v48,v49
MM a[84:87],a[168:171],v[80:83],a[84:87]
EX v52,v52
MM a[76:79],a[168:171],v[72:75],a[76:79]
buffer_load_dwordx4 v235, s[12:15], s81 offen offset:0 lds
MM a[76:79],a[172:175],v[76:79],a[76:79]
EX v53,v53
MM a[68:71],a[172:175],v[68:71],a[68:71]
RD a[228:231],v225 offset:57344
CV v21,v50,v51
MM a[68:71],a[168:171],v[64:67],a[68:71]
EX v54,v54
MM a[96:99],a[176:179],v[64:67],a[96:99]
RD a[232:235],v226 offset:57344
CV v22,v52,v53
MM a[96:99],a[180:183],v[68:71],a[96:99]
EX v55,v55
MM a[104:107],a[180:183],v[76:79],a[104:107]
EX v56,v56
MM a[104:107],a[176:179],v[72:75],a[104:107]
buffer_load_dwordx4 v235, s[12:15], s64 offen offset:1024 lds
MM a[112:115],a[176:179],v[80:83],a[112:115]
EX v57,v57
MM a[112:115],a[180:183],v[84:87],a[112:115]
RD a[236:239],v227 offset:57344
CV v23,v54,v55
MM a[120:123],a[180:183],v[92:95],a[120:123]
EX v58,v58
MM a[120:123],a[176:179],v[88:91],a[120:123]
RD a[240:243],v224 offset:61440
CV v28,v56,v57
MM a[124:127],a[184:187],v[88:91],a[124:127]
v_add_f32 v244, v144, v145
v_add_f32 v245, v148, v149
MM a[124:127],a[188:191],v[92:95],a[124:127]
v_add_f32 v246, v152, v153
v_add_f32 v247, v156, v157
MM a[116:119],a[188:191],v[84:87],a[116:119]
EX v59,v59
MM a[116:119],a[184:187],v[80:83],a[116:119]
EX v60,v60
MM a[108:111],a[184:187],v[72:75],a[108:111]
EX v61,v61
MM a[108:111],a[188:191],v[76:79],a[108:111]
buffer_load_dwordx4 v235, s[12:15], s81 offen offset:2048 lds
MM a[100:103],a[188:191],v[68:71],a[100:103]
EX v62,v62
MM a[100:103],a[184:187],v[64:67],a[100:103]
RD a[244:247],v225 offset:61440
CV v29,v58,v59
v_smfmac_f32_16x16x64_bf16 v[144:147], v[236:239], v[0:7], v234
RD a[248:251],v226 offset:61440
RD a[252:255],v227 offset:61440
EX v63,v63
v_smfmac_f32_16x16x64_bf16 v[148:151], v[236:239], v[8:15], v234
CV v30,v60,v61
v_smfmac_f32_16x16x64_bf16 v[152:155], v[236:239], v[16:23], v234
CV v31,v62,v63
s_nop 1
v_smfmac_f32_16x16x64_bf16 v[156:159], v[236:239], v[24:31], v234
s_waitcnt lgkmcnt(0)
buffer_load_dwordx4 v235, s[12:15], s64 offen offset:3072 lds
MM v[64:67],a[192:195],v[160:163],v[128:131]
s_add_u32 s64, s64, 16384
TR a[128:129],v232 offset:32768
s_add_u32 s81, s64, 896
MM v[64:67],a[196:199],v[176:179],v[64:67]
TR a[130:131],v232 offset:36864
v_max3_f32 v249, v144, v145, v148
MM v[64:67],a[200:203],v[192:195],v[64:67]
TR a[132:133],v232 offset:40960
v_max3_f32 v249, v249, v149, v152
MM v[64:67],a[204:207],v[208:211],v[64:67]
TR a[134:135],v232 offset:45056
v_max3_f32 v249, v249, v153, v156
MM v[68:71],a[220:223],v[208:211],v[128:131]
TR a[136:137],v233 offset:32768
v_max_f32 v249, v249, v157
MM v[68:71],a[216:219],v[192:195],v[68:71]
TR a[138:139],v233 offset:36864
v_cmp_lt_f32 vcc, s71, v249
MM v[68:71],a[212:215],v[176:179],v[68:71]
TR a[140:141],v233 offset:40960
TR a[142:143],v233 offset:45056
MM v[68:71],a[208:211],v[160:163],v[68:71]
s_cbranch_vccnz LSITE18_%=
LBACK18_%=:
MM v[76:79],a[208:211],v[164:167],v[132:135]
EX v64,v64
MM v[76:79],a[212:215],v[180:183],v[76:79]
EX v65,v65
MM v[76:79],a[216:219],v[196:199],v[76:79]
TR a[144:145],v232 offset:33280
CV v64,v64,v65
MM v[76:79],a[220:223],v[212:215],v[76:79]
EX v66,v66
MM v[72:75],a[204:207],v[212:215],v[132:135]
EX v67,v67
MM v[72:75],a[200:203],v[196:199],v[72:75]
TR a[146:147],v232 offset:37376
CV v65,v66,v67
MM v[72:75],a[196:199],v[180:183],v[72:75]
EX v68,v68
MM v[72:75],a[192:195],v[164:167],v[72:75]
TR a[148:149],v232 offset:41472
TR a[150:151],v232 offset:45568
MM v[80:83],a[192:195],v[168:171],v[136:139]
EX v69,v69
MM v[80:83],a[196:199],v[184:187],v[80:83]
TR a[152:153],v233 offset:33280
CV v66,v68,v69
MM v[80:83],a[200:203],v[200:203],v[80:83]
EX v70,v70
MM v[80:83],a[204:207],v[216:219],v[80:83]
TR a[154:155],v233 offset:37376
TR a[156:157],v233 offset:41472
MM v[84:87],a[220:223],v[216:219],v[136:139]
EX v71,v71
MM v[84:87],a[216:219],v[200:203],v[84:87]
TR a[158:159],v233 offset:45568
CV v67,v70,v71
MM v[84:87],a[212:215],v[184:187],v[84:87]
EX v72,v72
MM v[84:87],a[208:211],v[168:171],v[84:87]
EX v73,v73
MM v[92:95],a[208:211],v[172:175],v[140:143]
TR a[160:161],v232 offset:33792
CV v72,v72,v73
MM v[92:95],a[212:215],v[188:191],v[92:95]
EX v74,v74
MM v[92:95],a[216:219],v[204:207],v[92:95]
TR a[162:163],v232 offset:37888
TR a[164:165],v232 offset:41984
MM v[92:95],a[220:223],v[220:223],v[92:95]
EX v75,v75
MM v[88:91],a[204:207],v[220:223],v[140:143]
TR a[166:167],v232 offset:46080
CV v73,v74,v75
MM v[88:91],a[200:203],v[204:207],v[88:91]
EX v76,v76
MM v[88:91],a[196:199],v[188:191],v[88:91]
EX v77,v77
MM v[88:91],a[192:195],v[172:175],v[88:91]
TR a[168:169],v233 offset:33792
CV v74,v76,v77
MM v[96:99],a[224:227],v[160:163],v[128:131]
EX v78,v78
MM v[96:99],a[228:231],v[176:179],v[96:99]
TR a[170:171],v233 offset:37888
TR a[172:173],v233 offset:41984
MM v[96:99],a[232:235],v[192:195],v[96:99]
EX v79,v79
MM v[96:99],a[236:239],v[208:211],v[96:99]
TR a[174:175],v233 offset:46080
CV v75,v78,v79
MM v[100:103],a[252:255],v[208:211],v[128:131]
EX v80,v80
MM v[100:103],a[248:251],v[192:195],v[100:103]
EX v81,v81
MM v[100:103],a[244:247],v[176:179],v[100:103]
TR a[176:177],v232 offset:34304
CV v80,v80,v81
MM v[100:103],a[240:243],v[160:163],v[100:103]
EX v82,v82
MM v[108:111],a[240:243],v[164:167],v[132:135]
v_lshrrev_b32 v253, 7, v248
v_mul_u32_u24 v253, 1280, v253
MM v[108:111],a[244:247],v[180:183],v[108:111]
v_and_b32 v252, 112, v248
v_add_u32 v253, v253, v252
MM v[108:111],a[248:251],v[196:199],v[108:111]
s_mul_i32 s90, %[swave], 61440
TR a[178:179],v232 offset:38400
TR a[180:181],v232 offset:42496
MM v[108:111],a[252:255],v[212:215],v[108:111]
s_mul_i32 s91, %[swave], 6144
EX v83,v83
MM v[104:107],a[236:239],v[212:215],v[132:135]
s_add_u32 s91, s91, %[slds]
TR a[182:183],v232 offset:46592
CV v81,v82,v83
MM v[104:107],a[232:235],v[196:199],v[104:107]
EX v84,v84
MM v[104:107],a[228:231],v[180:183],v[104:107]
EX v85,v85
MM v[104:107],a[224:227],v[164:167],v[104:107]
TR a[184:185],v233 offset:34304
CV v82,v84,v85
MM v[112:115],a[224:227],v[168:171],v[136:139]
EX v86,v86
MM v[112:115],a[228:231],v[184:187],v[112:115]
TR a[186:187],v233 offset:38400
TR a[188:189],v233 offset:42496
MM v[112:115],a[232:235],v[200:203],v[112:115]
EX v87,v87
MM v[112:115],a[236:239],v[216:219],v[112:115]
TR a[190:191],v233 offset:46592
CV v83,v86,v87
MM v[116:119],a[252:255],v[216:219],v[136:139]
EX v88,v88
MM v[116:119],a[248:251],v[200:203],v[116:119]
EX v89,v89
MM v[116:119],a[244:247],v[184:187],v[116:119]
EX v90,v90
MM v[116:119],a[240:243],v[168:171],v[116:119]
EX v91,v91
MM v[124:127],a[240:243],v[172:175],v[140:143]
EX v92,v92
MM v[124:127],a[244:247],v[188:191],v[124:127]
EX v93,v93
MM v[124:127],a[248:251],v[204:207],v[124:127]
EX v94,v94
MM v[124:127],a[252:255],v[220:223],v[124:127]
EX v95,v95
MM v[120:123],a[236:239],v[220:223],v[140:143]
EX v96,v96
MM v[120:123],a[232:235],v[204:207],v[120:123]
EX v97,v97
MM v[120:123],a[228:231],v[188:191],v[120:123]
EX v98,v98
MM v[120:123],a[224:227],v[172:175],v[120:123]
s_waitcnt vmcnt(14) lgkmcnt(0)
s_barrier
MM a[0:3],a[128:131],v[0:3],a[0:3]
s_nop 0
LPG15_%=:
s_cbranch_execz LTR2_%=
s_add_u32 m0, s65, 32768
CV v88,v88,v89
CV v89,v90,v91
MM a[0:3],a[132:135],v[4:7],a[0:3]
RD a[192:195],v224 offset:0
CV v90,v92,v93
MM a[8:11],a[132:135],v[12:15],a[8:11]
RD a[196:199],v225 offset:0
CV v91,v94,v95
MM a[8:11],a[128:131],v[8:11],a[8:11]
buffer_load_dwordx4 v228, s[8:11], s64 offen offset:0 lds
MM a[16:19],a[128:131],v[16:19],a[16:19]
RD a[200:203],v226 offset:0
CV v68,v96,v97
MM a[16:19],a[132:135],v[20:23],a[16:19]
buffer_load_dwordx4 v229, s[8:11], s64 offen offset:1024 lds
MM a[24:27],a[132:135],v[28:31],a[24:27]
RD a[204:207],v227 offset:0
RD a[208:211],v224 offset:4096
MM a[24:27],a[128:131],v[24:27],a[24:27]
EX v99,v99
MM a[28:31],a[136:139],v[24:27],a[28:31]
RD a[212:215],v225 offset:4096
CV v69,v98,v99
MM a[28:31],a[140:143],v[28:31],a[28:31]
EX v100,v100
MM a[20:23],a[140:143],v[20:23],a[20:23]
buffer_load_dwordx4 v230, s[8:11], s64 offen offset:2048 lds
MM a[20:23],a[136:139],v[16:19],a[20:23]
EX v101,v101
MM a[12:15],a[136:139],v[8:11],a[12:15]
RD a[216:219],v226 offset:4096
CV v70,v100,v101
MM a[12:15],a[140:143],v[12:15],a[12:15]
EX v102,v102
MM a[4:7],a[140:143],v[4:7],a[4:7]
EX v103,v103
MM a[4:7],a[136:139],v[0:3],a[4:7]
RD a[220:223],v227 offset:4096
CV v71,v102,v103
MM a[32:35],a[144:147],v[0:3],a[32:35]
EX v104,v104
MM a[32:35],a[148:151],v[4:7],a[32:35]
buffer_load_dwordx4 v231, s[8:11], s64 offen offset:3072 lds
MM a[40:43],a[148:151],v[12:15],a[40:43]
s_add_u32 m0, s65, 98304
EX v105,v105
MM a[40:43],a[144:147],v[8:11],a[40:43]
RD a[224:227],v224 offset:8192
CV v76,v104,v105
MM a[48:51],a[144:147],v[16:19],a[48:51]
EX v106,v106
MM a[48:51],a[148:151],v[20:23],a[48:51]
EX v107,v107
MM a[56:59],a[148:151],v[28:31],a[56:59]
RD a[228:231],v225 offset:8192
CV v77,v106,v107
MM a[56:59],a[144:147],v[24:27],a[56:59]
EX v108,v108
MM a[60:63],a[152:155],v[24:27],a[60:63]
buffer_load_dwordx4 v235, s[12:15], s81 offen offset:0 lds
)ASM"
R"ASM(
MM a[60:63],a[156:159],v[28:31],a[60:63]
EX v109,v109
MM a[52:55],a[156:159],v[20:23],a[52:55]
RD a[232:235],v226 offset:8192
CV v78,v108,v109
MM a[52:55],a[152:155],v[16:19],a[52:55]
EX v110,v110
MM a[44:47],a[152:155],v[8:11],a[44:47]
EX v111,v111
MM a[44:47],a[156:159],v[12:15],a[44:47]
RD a[236:239],v227 offset:8192
CV v79,v110,v111
MM a[36:39],a[156:159],v[4:7],a[36:39]
buffer_load_dwordx4 v235, s[12:15], s64 offen offset:1024 lds
MM a[36:39],a[152:155],v[0:3],a[36:39]
EX v112,v112
MM a[64:67],a[160:163],v[0:3],a[64:67]
EX v113,v113
MM a[64:67],a[164:167],v[4:7],a[64:67]
RD a[240:243],v224 offset:12288
CV v84,v112,v113
MM a[72:75],a[164:167],v[12:15],a[72:75]
EX v114,v114
MM a[72:75],a[160:163],v[8:11],a[72:75]
RD a[244:247],v225 offset:12288
RD a[248:251],v226 offset:12288
MM a[80:83],a[160:163],v[16:19],a[80:83]
s_waitcnt vmcnt(10)
v_mov_b32 v250, s69
s_mov_b64 exec, 1
buffer_store_dword v250, off, s[20:23], s97 offset:0 sc1
s_mov_b64 exec, -1
MM a[80:83],a[164:167],v[20:23],a[80:83]
s_add_u32 m0, s91, 139264
s_add_u32 s77, s90, 0
buffer_load_dwordx4 v253, s[28:31], s77 offen offset:0 lds
MM a[88:91],a[164:167],v[28:31],a[88:91]
EX v115,v115
MM a[88:91],a[160:163],v[24:27],a[88:91]
RD a[252:255],v227 offset:12288
CV v85,v114,v115
MM a[92:95],a[168:171],v[24:27],a[92:95]
EX v116,v116
MM a[92:95],a[172:175],v[28:31],a[92:95]
EX v117,v117
MM a[84:87],a[172:175],v[20:23],a[84:87]
s_add_u32 m0, s65, 98304
s_nop 0
buffer_load_dwordx4 v235, s[12:15], s81 offen offset:2048 lds
MM a[84:87],a[168:171],v[16:19],a[84:87]
EX v118,v118
MM a[76:79],a[168:171],v[8:11],a[76:79]
EX v119,v119
MM a[76:79],a[172:175],v[12:15],a[76:79]
EX v120,v120
MM a[68:71],a[172:175],v[4:7],a[68:71]
buffer_load_dwordx4 v235, s[12:15], s64 offen offset:3072 lds
MM a[68:71],a[168:171],v[0:3],a[68:71]
EX v121,v121
MM a[96:99],a[176:179],v[0:3],a[96:99]
EX v122,v122
MM a[96:99],a[180:183],v[4:7],a[96:99]
EX v123,v123
MM a[104:107],a[180:183],v[12:15],a[104:107]
EX v124,v124
MM a[104:107],a[176:179],v[8:11],a[104:107]
EX v125,v125
MM a[112:115],a[176:179],v[16:19],a[112:115]
EX v126,v126
MM a[112:115],a[180:183],v[20:23],a[112:115]
EX v127,v127
MM a[120:123],a[180:183],v[28:31],a[120:123]
CV v86,v116,v117
CV v87,v118,v119
MM a[120:123],a[176:179],v[24:27],a[120:123]
CV v92,v120,v121
CV v93,v122,v123
MM a[124:127],a[184:187],v[24:27],a[124:127]
v_add_f32 v244, v144, v145
v_add_f32 v245, v148, v149
MM a[124:127],a[188:191],v[28:31],a[124:127]
v_add_f32 v246, v152, v153
v_add_f32 v247, v156, v157
MM a[116:119],a[188:191],v[20:23],a[116:119]
CV v94,v124,v125
CV v95,v126,v127
MM a[116:119],a[184:187],v[16:19],a[116:119]
MM a[108:111],a[184:187],v[8:11],a[108:111]
MM a[108:111],a[188:191],v[12:15],a[108:111]
MM a[100:103],a[188:191],v[4:7],a[100:103]
MM a[100:103],a[184:187],v[0:3],a[100:103]
v_smfmac_f32_16x16x64_bf16 v[144:147], v[236:239], v[64:71], v234
v_smfmac_f32_16x16x64_bf16 v[148:151], v[236:239], v[72:79], v234
v_smfmac_f32_16x16x64_bf16 v[152:155], v[236:239], v[80:87], v234
v_smfmac_f32_16x16x64_bf16 v[156:159], v[236:239], v[88:95], v234
s_waitcnt lgkmcnt(0)
MM v[0:3],a[192:195],v[160:163],v[128:131]
s_add_u32 s64, s64, 16384
TR a[128:129],v232 offset:49152
s_add_u32 s81, s64, 896
MM v[0:3],a[196:199],v[176:179],v[0:3]
TR a[130:131],v232 offset:53248
v_max3_f32 v249, v144, v145, v148
MM v[0:3],a[200:203],v[192:195],v[0:3]
TR a[132:133],v232 offset:57344
v_max3_f32 v249, v249, v149, v152
MM v[0:3],a[204:207],v[208:211],v[0:3]
TR a[134:135],v232 offset:61440
v_max3_f32 v249, v249, v153, v156
MM v[4:7],a[220:223],v[208:211],v[128:131]
TR a[136:137],v233 offset:49152
v_max_f32 v249, v249, v157
MM v[4:7],a[216:219],v[192:195],v[4:7]
TR a[138:139],v233 offset:53248
v_cmp_lt_f32 vcc, s71, v249
MM v[4:7],a[212:215],v[176:179],v[4:7]
TR a[140:141],v233 offset:57344
TR a[142:143],v233 offset:61440
MM v[4:7],a[208:211],v[160:163],v[4:7]
s_cbranch_vccnz LSITE19_%=
LBACK19_%=:
MM v[12:15],a[208:211],v[164:167],v[132:135]
s_add_u32 m0, s91, 140288
s_add_u32 s77, s90, 10240
buffer_load_dwordx4 v253, s[28:31], s77 offen offset:0 lds
MM v[12:15],a[212:215],v[180:183],v[12:15]
EX v0,v0
MM v[12:15],a[216:219],v[196:199],v[12:15]
EX v1,v1
MM v[12:15],a[220:223],v[212:215],v[12:15]
TR a[144:145],v232 offset:49664
CV v0,v0,v1
MM v[8:11],a[204:207],v[212:215],v[132:135]
EX v2,v2
MM v[8:11],a[200:203],v[196:199],v[8:11]
EX v3,v3
MM v[8:11],a[196:199],v[180:183],v[8:11]
TR a[146:147],v232 offset:53760
CV v1,v2,v3
MM v[8:11],a[192:195],v[164:167],v[8:11]
EX v4,v4
MM v[16:19],a[192:195],v[168:171],v[136:139]
TR a[148:149],v232 offset:57856
TR a[150:151],v232 offset:61952
MM v[16:19],a[196:199],v[184:187],v[16:19]
EX v5,v5
MM v[16:19],a[200:203],v[200:203],v[16:19]
TR a[152:153],v233 offset:49664
CV v2,v4,v5
MM v[16:19],a[204:207],v[216:219],v[16:19]
EX v6,v6
MM v[20:23],a[220:223],v[216:219],v[136:139]
EX v7,v7
MM v[20:23],a[216:219],v[200:203],v[20:23]
TR a[154:155],v233 offset:53760
CV v3,v6,v7
MM v[20:23],a[212:215],v[184:187],v[20:23]
EX v8,v8
MM v[20:23],a[208:211],v[168:171],v[20:23]
TR a[156:157],v233 offset:57856
TR a[158:159],v233 offset:61952
MM v[28:31],a[208:211],v[172:175],v[140:143]
EX v9,v9
MM v[28:31],a[212:215],v[188:191],v[28:31]
TR a[160:161],v232 offset:50176
CV v8,v8,v9
MM v[28:31],a[216:219],v[204:207],v[28:31]
EX v10,v10
MM v[28:31],a[220:223],v[220:223],v[28:31]
EX v11,v11
MM v[24:27],a[204:207],v[220:223],v[140:143]
TR a[162:163],v232 offset:54272
CV v9,v10,v11
MM v[24:27],a[200:203],v[204:207],v[24:27]
TR a[164:165],v232 offset:58368
TR a[166:167],v232 offset:62464
MM v[24:27],a[196:199],v[188:191],v[24:27]
EX v12,v12
MM v[24:27],a[192:195],v[172:175],v[24:27]
EX v13,v13
MM v[32:35],a[224:227],v[160:163],v[128:131]
TR a[168:169],v233 offset:50176
CV v10,v12,v13
MM v[32:35],a[228:231],v[176:179],v[32:35]
EX v14,v14
MM v[32:35],a[232:235],v[192:195],v[32:35]
TR a[170:171],v233 offset:54272
TR a[172:173],v233 offset:58368
MM v[32:35],a[236:239],v[208:211],v[32:35]
EX v15,v15
MM v[36:39],a[252:255],v[208:211],v[128:131]
TR a[174:175],v233 offset:62464
CV v11,v14,v15
MM v[36:39],a[248:251],v[192:195],v[36:39]
EX v16,v16
MM v[36:39],a[244:247],v[176:179],v[36:39]
EX v17,v17
MM v[36:39],a[240:243],v[160:163],v[36:39]
TR a[176:177],v232 offset:50688
CV v16,v16,v17
MM v[44:47],a[240:243],v[164:167],v[132:135]
s_add_u32 m0, s91, 141312
s_add_u32 s77, s90, 20480
buffer_load_dwordx4 v253, s[28:31], s77 offen offset:0 lds
MM v[44:47],a[244:247],v[180:183],v[44:47]
EX v18,v18
MM v[44:47],a[248:251],v[196:199],v[44:47]
TR a[178:179],v232 offset:54784
TR a[180:181],v232 offset:58880
MM v[44:47],a[252:255],v[212:215],v[44:47]
EX v19,v19
MM v[40:43],a[236:239],v[212:215],v[132:135]
TR a[182:183],v232 offset:62976
CV v17,v18,v19
MM v[40:43],a[232:235],v[196:199],v[40:43]
EX v20,v20
MM v[40:43],a[228:231],v[180:183],v[40:43]
EX v21,v21
MM v[40:43],a[224:227],v[164:167],v[40:43]
TR a[184:185],v233 offset:50688
CV v18,v20,v21
MM v[48:51],a[224:227],v[168:171],v[136:139]
EX v22,v22
MM v[48:51],a[228:231],v[184:187],v[48:51]
TR a[186:187],v233 offset:54784
TR a[188:189],v233 offset:58880
MM v[48:51],a[232:235],v[200:203],v[48:51]
EX v23,v23
MM v[48:51],a[236:239],v[216:219],v[48:51]
TR a[190:191],v233 offset:62976
CV v19,v22,v23
MM v[52:55],a[252:255],v[216:219],v[136:139]
EX v24,v24
MM v[52:55],a[248:251],v[200:203],v[52:55]
EX v25,v25
MM v[52:55],a[244:247],v[184:187],v[52:55]
EX v26,v26
MM v[52:55],a[240:243],v[168:171],v[52:55]
EX v27,v27
MM v[60:63],a[240:243],v[172:175],v[140:143]
EX v28,v28
MM v[60:63],a[244:247],v[188:191],v[60:63]
EX v29,v29
MM v[60:63],a[248:251],v[204:207],v[60:63]
EX v30,v30
MM v[60:63],a[252:255],v[220:223],v[60:63]
EX v31,v31
MM v[56:59],a[236:239],v[220:223],v[140:143]
EX v32,v32
MM v[56:59],a[232:235],v[204:207],v[56:59]
EX v33,v33
MM v[56:59],a[228:231],v[188:191],v[56:59]
EX v34,v34
MM v[56:59],a[224:227],v[172:175],v[56:59]
s_waitcnt lgkmcnt(0)
s_barrier
MM a[0:3],a[128:131],v[64:67],a[0:3]
s_add_u32 m0, s65, 49152
CV v24,v24,v25
CV v25,v26,v27
MM a[0:3],a[132:135],v[68:71],a[0:3]
RD a[192:195],v224 offset:16384
CV v26,v28,v29
MM a[8:11],a[132:135],v[76:79],a[8:11]
RD a[196:199],v225 offset:16384
CV v27,v30,v31
MM a[8:11],a[128:131],v[72:75],a[8:11]
buffer_load_dwordx4 v228, s[8:11], s64 offen offset:0 lds
MM a[16:19],a[128:131],v[80:83],a[16:19]
s_add_u32 m0, s91, 142336
s_add_u32 s77, s90, 30720
buffer_load_dwordx4 v253, s[28:31], s77 offen offset:0 lds
MM a[16:19],a[132:135],v[84:87],a[16:19]
RD a[200:203],v226 offset:16384
CV v4,v32,v33
MM a[24:27],a[132:135],v[92:95],a[24:27]
RD a[204:207],v227 offset:16384
RD a[208:211],v224 offset:20480
MM a[24:27],a[128:131],v[88:91],a[24:27]
s_add_u32 m0, s65, 49152
s_nop 0
buffer_load_dwordx4 v229, s[8:11], s64 offen offset:1024 lds
MM a[28:31],a[136:139],v[88:91],a[28:31]
EX v35,v35
MM a[28:31],a[140:143],v[92:95],a[28:31]
RD a[212:215],v225 offset:20480
CV v5,v34,v35
MM a[20:23],a[140:143],v[84:87],a[20:23]
EX v36,v36
MM a[20:23],a[136:139],v[80:83],a[20:23]
EX v37,v37
MM a[12:15],a[136:139],v[72:75],a[12:15]
RD a[216:219],v226 offset:20480
CV v6,v36,v37
MM a[12:15],a[140:143],v[76:79],a[12:15]
buffer_load_dwordx4 v230, s[8:11], s64 offen offset:2048 lds
MM a[4:7],a[140:143],v[68:71],a[4:7]
EX v38,v38
MM a[4:7],a[136:139],v[64:67],a[4:7]
EX v39,v39
MM a[32:35],a[144:147],v[64:67],a[32:35]
RD a[220:223],v227 offset:20480
CV v7,v38,v39
MM a[32:35],a[148:151],v[68:71],a[32:35]
EX v40,v40
MM a[40:43],a[148:151],v[76:79],a[40:43]
EX v41,v41
MM a[40:43],a[144:147],v[72:75],a[40:43]
RD a[224:227],v224 offset:24576
CV v12,v40,v41
MM a[48:51],a[144:147],v[80:83],a[48:51]
buffer_load_dwordx4 v231, s[8:11], s64 offen offset:3072 lds
MM a[48:51],a[148:151],v[84:87],a[48:51]
s_add_u32 m0, s65, 114688
EX v42,v42
MM a[56:59],a[148:151],v[92:95],a[56:59]
EX v43,v43
MM a[56:59],a[144:147],v[88:91],a[56:59]
RD a[228:231],v225 offset:24576
CV v13,v42,v43
MM a[60:63],a[152:155],v[88:91],a[60:63]
EX v44,v44
MM a[60:63],a[156:159],v[92:95],a[60:63]
buffer_load_dwordx4 v235, s[12:15], s81 offen offset:0 lds
MM a[52:55],a[156:159],v[84:87],a[52:55]
EX v45,v45
MM a[52:55],a[152:155],v[80:83],a[52:55]
RD a[232:235],v226 offset:24576
CV v14,v44,v45
MM a[44:47],a[152:155],v[72:75],a[44:47]
EX v46,v46
MM a[44:47],a[156:159],v[76:79],a[44:47]
EX v47,v47
MM a[36:39],a[156:159],v[68:71],a[36:39]
RD a[236:239],v227 offset:24576
CV v15,v46,v47
MM a[36:39],a[152:155],v[64:67],a[36:39]
EX v48,v48
MM a[64:67],a[160:163],v[64:67],a[64:67]
buffer_load_dwordx4 v235, s[12:15], s64 offen offset:1024 lds
MM a[64:67],a[164:167],v[68:71],a[64:67]
EX v49,v49
MM a[72:75],a[164:167],v[76:79],a[72:75]
RD a[240:243],v224 offset:28672
CV v20,v48,v49
MM a[72:75],a[160:163],v[72:75],a[72:75]
EX v50,v50
MM a[80:83],a[160:163],v[80:83],a[80:83]
s_add_u32 m0, s91, 143360
s_add_u32 s77, s90, 40960
buffer_load_dwordx4 v253, s[28:31], s77 offen offset:0 lds
MM a[80:83],a[164:167],v[84:87],a[80:83]
EX v51,v51
MM a[88:91],a[164:167],v[92:95],a[88:91]
RD a[244:247],v225 offset:28672
CV v21,v50,v51
MM a[88:91],a[160:163],v[88:91],a[88:91]
EX v52,v52
MM a[92:95],a[168:171],v[88:91],a[92:95]
EX v53,v53
MM a[92:95],a[172:175],v[92:95],a[92:95]
RD a[248:251],v226 offset:28672
CV v22,v52,v53
MM a[84:87],a[172:175],v[84:87],a[84:87]
s_add_u32 m0, s65, 114688
s_nop 0
buffer_load_dwordx4 v235, s[12:15], s81 offen offset:2048 lds
MM a[84:87],a[168:171],v[80:83],a[84:87]
EX v54,v54
MM a[76:79],a[168:171],v[72:75],a[76:79]
EX v55,v55
MM a[76:79],a[172:175],v[76:79],a[76:79]
RD a[252:255],v227 offset:28672
CV v23,v54,v55
MM a[68:71],a[172:175],v[68:71],a[68:71]
EX v56,v56
MM a[68:71],a[168:171],v[64:67],a[68:71]
EX v57,v57
MM a[96:99],a[176:179],v[64:67],a[96:99]
buffer_load_dwordx4 v235, s[12:15], s64 offen offset:3072 lds
MM a[96:99],a[180:183],v[68:71],a[96:99]
EX v58,v58
MM a[104:107],a[180:183],v[76:79],a[104:107]
EX v59,v59
MM a[104:107],a[176:179],v[72:75],a[104:107]
EX v60,v60
MM a[112:115],a[176:179],v[80:83],a[112:115]
EX v61,v61
MM a[112:115],a[180:183],v[84:87],a[112:115]
EX v62,v62
MM a[120:123],a[180:183],v[92:95],a[120:123]
)ASM"
R"ASM(
s_add_u32 m0, s91, 144384
s_add_u32 s77, s90, 51200
buffer_load_dwordx4 v253, s[28:31], s77 offen offset:0 lds
MM a[120:123],a[176:179],v[88:91],a[120:123]
EX v63,v63
MM a[124:127],a[184:187],v[88:91],a[124:127]
v_add_f32 v244, v144, v145
v_add_f32 v245, v148, v149
MM a[124:127],a[188:191],v[92:95],a[124:127]
v_add_f32 v246, v152, v153
v_add_f32 v247, v156, v157
MM a[116:119],a[188:191],v[84:87],a[116:119]
CV v28,v56,v57
CV v29,v58,v59
MM a[116:119],a[184:187],v[80:83],a[116:119]
CV v30,v60,v61
CV v31,v62,v63
MM a[108:111],a[184:187],v[72:75],a[108:111]
MM a[108:111],a[188:191],v[76:79],a[108:111]
MM a[100:103],a[188:191],v[68:71],a[100:103]
MM a[100:103],a[184:187],v[64:67],a[100:103]
v_smfmac_f32_16x16x64_bf16 v[144:147], v[236:239], v[0:7], v234
v_smfmac_f32_16x16x64_bf16 v[148:151], v[236:239], v[8:15], v234
v_smfmac_f32_16x16x64_bf16 v[152:155], v[236:239], v[16:23], v234
v_smfmac_f32_16x16x64_bf16 v[156:159], v[236:239], v[24:31], v234
s_waitcnt lgkmcnt(0)
MM v[64:67],a[192:195],v[160:163],v[128:131]
s_add_u32 s64, s64, 16384
TR a[128:129],v232 offset:0
s_add_u32 s81, s64, 896
MM v[64:67],a[196:199],v[176:179],v[64:67]
TR a[130:131],v232 offset:4096
v_max3_f32 v249, v144, v145, v148
MM v[64:67],a[200:203],v[192:195],v[64:67]
TR a[132:133],v232 offset:8192
v_max3_f32 v249, v249, v149, v152
MM v[64:67],a[204:207],v[208:211],v[64:67]
TR a[134:135],v232 offset:12288
v_max3_f32 v249, v249, v153, v156
MM v[68:71],a[220:223],v[208:211],v[128:131]
TR a[136:137],v233 offset:0
v_max_f32 v249, v249, v157
MM v[68:71],a[216:219],v[192:195],v[68:71]
TR a[138:139],v233 offset:4096
v_cmp_lt_f32 vcc, s71, v249
MM v[68:71],a[212:215],v[176:179],v[68:71]
TR a[140:141],v233 offset:8192
TR a[142:143],v233 offset:12288
MM v[68:71],a[208:211],v[160:163],v[68:71]
s_cbranch_vccnz LSITE20_%=
LBACK20_%=:
MM v[76:79],a[208:211],v[164:167],v[132:135]
v_bfe_u32 v253, v248, 4, 4
v_lshlrev_b32 v253, 2, v253
MM v[76:79],a[212:215],v[180:183],v[76:79]
s_and_b32 s76, s18, 0xffffffc0
EX v64,v64
MM v[76:79],a[216:219],v[196:199],v[76:79]
s_add_u32 s76, s76, 77824
buffer_load_dword v252, v253, s[20:23], s76 offen offset:0 sc1
MM v[76:79],a[220:223],v[212:215],v[76:79]
EX v65,v65
MM v[72:75],a[204:207],v[212:215],v[132:135]
EX v66,v66
MM v[72:75],a[200:203],v[196:199],v[72:75]
TR a[144:145],v232 offset:512
CV v64,v64,v65
MM v[72:75],a[196:199],v[180:183],v[72:75]
EX v67,v67
MM v[72:75],a[192:195],v[164:167],v[72:75]
TR a[146:147],v232 offset:4608
CV v65,v66,v67
MM v[80:83],a[192:195],v[168:171],v[136:139]
EX v68,v68
MM v[80:83],a[196:199],v[184:187],v[80:83]
TR a[148:149],v232 offset:8704
TR a[150:151],v232 offset:12800
MM v[80:83],a[200:203],v[200:203],v[80:83]
EX v69,v69
MM v[80:83],a[204:207],v[216:219],v[80:83]
TR a[152:153],v233 offset:512
CV v66,v68,v69
MM v[84:87],a[220:223],v[216:219],v[136:139]
EX v70,v70
MM v[84:87],a[216:219],v[200:203],v[84:87]
EX v71,v71
MM v[84:87],a[212:215],v[184:187],v[84:87]
TR a[154:155],v233 offset:4608
CV v67,v70,v71
MM v[84:87],a[208:211],v[168:171],v[84:87]
EX v72,v72
MM v[92:95],a[208:211],v[172:175],v[140:143]
TR a[156:157],v233 offset:8704
TR a[158:159],v233 offset:12800
MM v[92:95],a[212:215],v[188:191],v[92:95]
EX v73,v73
MM v[92:95],a[216:219],v[204:207],v[92:95]
TR a[160:161],v232 offset:1024
CV v72,v72,v73
MM v[92:95],a[220:223],v[220:223],v[92:95]
EX v74,v74
MM v[88:91],a[204:207],v[220:223],v[140:143]
EX v75,v75
MM v[88:91],a[200:203],v[204:207],v[88:91]
TR a[162:163],v232 offset:5120
CV v73,v74,v75
MM v[88:91],a[196:199],v[188:191],v[88:91]
EX v76,v76
MM v[88:91],a[192:195],v[172:175],v[88:91]
TR a[164:165],v232 offset:9216
TR a[166:167],v232 offset:13312
MM v[96:99],a[224:227],v[160:163],v[128:131]
EX v77,v77
MM v[96:99],a[228:231],v[176:179],v[96:99]
TR a[168:169],v233 offset:1024
CV v74,v76,v77
MM v[96:99],a[232:235],v[192:195],v[96:99]
EX v78,v78
MM v[96:99],a[236:239],v[208:211],v[96:99]
EX v79,v79
MM v[100:103],a[252:255],v[208:211],v[128:131]
TR a[170:171],v233 offset:5120
CV v75,v78,v79
MM v[100:103],a[248:251],v[192:195],v[100:103]
EX v80,v80
MM v[100:103],a[244:247],v[176:179],v[100:103]
TR a[172:173],v233 offset:9216
TR a[174:175],v233 offset:13312
MM v[100:103],a[240:243],v[160:163],v[100:103]
EX v81,v81
s_nop 0
LPG16_%=:
s_cbranch_execz LTR2_%=
MM v[108:111],a[240:243],v[164:167],v[132:135]
TR a[176:177],v232 offset:1536
CV v80,v80,v81
MM v[108:111],a[244:247],v[180:183],v[108:111]
EX v82,v82
MM v[108:111],a[248:251],v[196:199],v[108:111]
TR a[178:179],v232 offset:5632
TR a[180:181],v232 offset:9728
MM v[108:111],a[252:255],v[212:215],v[108:111]
EX v83,v83
MM v[104:107],a[236:239],v[212:215],v[132:135]
TR a[182:183],v232 offset:13824
CV v81,v82,v83
MM v[104:107],a[232:235],v[196:199],v[104:107]
EX v84,v84
MM v[104:107],a[228:231],v[180:183],v[104:107]
EX v85,v85
MM v[104:107],a[224:227],v[164:167],v[104:107]
TR a[184:185],v233 offset:1536
CV v82,v84,v85
MM v[112:115],a[224:227],v[168:171],v[136:139]
EX v86,v86
MM v[112:115],a[228:231],v[184:187],v[112:115]
TR a[186:187],v233 offset:5632
TR a[188:189],v233 offset:9728
MM v[112:115],a[232:235],v[200:203],v[112:115]
EX v87,v87
MM v[112:115],a[236:239],v[216:219],v[112:115]
TR a[190:191],v233 offset:13824
CV v83,v86,v87
MM v[116:119],a[252:255],v[216:219],v[136:139]
EX v88,v88
MM v[116:119],a[248:251],v[200:203],v[116:119]
EX v89,v89
MM v[116:119],a[244:247],v[184:187],v[116:119]
EX v90,v90
MM v[116:119],a[240:243],v[168:171],v[116:119]
EX v91,v91
MM v[124:127],a[240:243],v[172:175],v[140:143]
EX v92,v92
MM v[124:127],a[244:247],v[188:191],v[124:127]
EX v93,v93
MM v[124:127],a[248:251],v[204:207],v[124:127]
EX v94,v94
MM v[124:127],a[252:255],v[220:223],v[124:127]
EX v95,v95
MM v[120:123],a[236:239],v[220:223],v[140:143]
EX v96,v96
MM v[120:123],a[232:235],v[204:207],v[120:123]
EX v97,v97
MM v[120:123],a[228:231],v[188:191],v[120:123]
s_getpc_b64 s[98:99]
LTA3_%=:
s_min_u32 s73, %[swave], 3
s_lshl_b32 s73, s73, 12
s_add_u32 s73, s73, LPG17_%=-LTA3_%=
s_add_u32 s98, s98, s73
s_addc_u32 s99, s99, 0
s_mov_b64 exec, 0
s_setpc_b64 s[98:99]
LTR3_%=:
s_mov_b64 exec, -1
CV v88,v88,v89
MM v[120:123],a[224:227],v[172:175],v[120:123]
s_waitcnt vmcnt(20) lgkmcnt(0)
s_barrier
MM a[0:3],a[128:131],v[0:3],a[0:3]
s_add_u32 m0, s65, 0
CV v89,v90,v91
CV v90,v92,v93
MM a[0:3],a[132:135],v[4:7],a[0:3]
RD a[192:195],v224 offset:32768
CV v91,v94,v95
MM a[8:11],a[132:135],v[12:15],a[8:11]
buffer_load_dwordx4 v228, s[8:11], s64 offen offset:0 lds
MM a[8:11],a[128:131],v[8:11],a[8:11]
RD a[196:199],v225 offset:32768
CV v68,v96,v97
MM a[16:19],a[128:131],v[16:19],a[16:19]
RD a[200:203],v226 offset:32768
RD a[204:207],v227 offset:32768
MM a[16:19],a[132:135],v[20:23],a[16:19]
buffer_load_dwordx4 v229, s[8:11], s64 offen offset:1024 lds
MM a[24:27],a[132:135],v[28:31],a[24:27]
EX v98,v98
MM a[24:27],a[128:131],v[24:27],a[24:27]
RD a[208:211],v224 offset:36864
RD a[212:215],v225 offset:36864
MM a[28:31],a[136:139],v[24:27],a[28:31]
EX v99,v99
MM a[28:31],a[140:143],v[28:31],a[28:31]
RD a[216:219],v226 offset:36864
CV v69,v98,v99
MM a[20:23],a[140:143],v[20:23],a[20:23]
EX v100,v100
MM a[20:23],a[136:139],v[16:19],a[20:23]
EX v101,v101
MM a[12:15],a[136:139],v[8:11],a[12:15]
RD a[220:223],v227 offset:36864
CV v70,v100,v101
MM a[12:15],a[140:143],v[12:15],a[12:15]
buffer_load_dwordx4 v230, s[8:11], s64 offen offset:2048 lds
MM a[4:7],a[140:143],v[4:7],a[4:7]
EX v102,v102
MM a[4:7],a[136:139],v[0:3],a[4:7]
EX v103,v103
MM a[32:35],a[144:147],v[0:3],a[32:35]
RD a[224:227],v224 offset:40960
CV v71,v102,v103
MM a[32:35],a[148:151],v[4:7],a[32:35]
EX v104,v104
MM a[40:43],a[148:151],v[12:15],a[40:43]
EX v105,v105
MM a[40:43],a[144:147],v[8:11],a[40:43]
RD a[228:231],v225 offset:40960
CV v76,v104,v105
MM a[48:51],a[144:147],v[16:19],a[48:51]
buffer_load_dwordx4 v231, s[8:11], s64 offen offset:3072 lds
MM a[48:51],a[148:151],v[20:23],a[48:51]
s_add_u32 m0, s65, 65536
EX v106,v106
MM a[56:59],a[148:151],v[28:31],a[56:59]
EX v107,v107
MM a[56:59],a[144:147],v[24:27],a[56:59]
RD a[232:235],v226 offset:40960
CV v77,v106,v107
MM a[60:63],a[152:155],v[24:27],a[60:63]
EX v108,v108
MM a[60:63],a[156:159],v[28:31],a[60:63]
buffer_load_dwordx4 v235, s[12:15], s81 offen offset:0 lds
MM a[52:55],a[156:159],v[20:23],a[52:55]
EX v109,v109
MM a[52:55],a[152:155],v[16:19],a[52:55]
RD a[236:239],v227 offset:40960
CV v78,v108,v109
MM a[44:47],a[152:155],v[8:11],a[44:47]
EX v110,v110
MM a[44:47],a[156:159],v[12:15],a[44:47]
EX v111,v111
MM a[36:39],a[156:159],v[4:7],a[36:39]
RD a[240:243],v224 offset:45056
CV v79,v110,v111
MM a[36:39],a[152:155],v[0:3],a[36:39]
EX v112,v112
MM a[64:67],a[160:163],v[0:3],a[64:67]
buffer_load_dwordx4 v235, s[12:15], s64 offen offset:1024 lds
MM a[64:67],a[164:167],v[4:7],a[64:67]
EX v113,v113
MM a[72:75],a[164:167],v[12:15],a[72:75]
RD a[244:247],v225 offset:45056
CV v84,v112,v113
MM a[72:75],a[160:163],v[8:11],a[72:75]
EX v114,v114
MM a[80:83],a[160:163],v[16:19],a[80:83]
EX v115,v115
MM a[80:83],a[164:167],v[20:23],a[80:83]
RD a[248:251],v226 offset:45056
CV v85,v114,v115
MM a[88:91],a[164:167],v[28:31],a[88:91]
EX v116,v116
MM a[88:91],a[160:163],v[24:27],a[88:91]
buffer_load_dwordx4 v235, s[12:15], s81 offen offset:2048 lds
MM a[92:95],a[168:171],v[24:27],a[92:95]
EX v117,v117
MM a[92:95],a[172:175],v[28:31],a[92:95]
RD a[252:255],v227 offset:45056
CV v86,v116,v117
MM a[84:87],a[172:175],v[20:23],a[84:87]
EX v118,v118
MM a[84:87],a[168:171],v[16:19],a[84:87]
EX v119,v119
MM a[76:79],a[168:171],v[8:11],a[76:79]
CV v87,v118,v119
MM a[76:79],a[172:175],v[12:15],a[76:79]
EX v120,v120
MM a[68:71],a[172:175],v[4:7],a[68:71]
buffer_load_dwordx4 v235, s[12:15], s64 offen offset:3072 lds
MM a[68:71],a[168:171],v[0:3],a[68:71]
EX v121,v121
MM a[96:99],a[176:179],v[0:3],a[96:99]
EX v122,v122
MM a[96:99],a[180:183],v[4:7],a[96:99]
EX v123,v123
MM a[104:107],a[180:183],v[12:15],a[104:107]
EX v124,v124
MM a[104:107],a[176:179],v[8:11],a[104:107]
EX v125,v125
MM a[112:115],a[176:179],v[16:19],a[112:115]
EX v126,v126
MM a[112:115],a[180:183],v[20:23],a[112:115]
EX v127,v127
MM a[120:123],a[180:183],v[28:31],a[120:123]
s_waitcnt vmcnt(8)
v_cmp_ne_u32 vcc, s69, v252
s_cbranch_vccnz LRFG_%=
LRFGb_%=:
MM a[120:123],a[176:179],v[24:27],a[120:123]
v_lshrrev_b32 v253, 8, v248
v_lshlrev_b32 v253, 10, v253
MM a[124:127],a[184:187],v[24:27],a[124:127]
v_add_f32 v244, v144, v145
v_add_f32 v245, v148, v149
MM a[124:127],a[188:191],v[28:31],a[124:127]
v_add_f32 v246, v152, v153
v_add_f32 v247, v156, v157
v_and_b32 v252, 240, v248
MM a[116:119],a[188:191],v[20:23],a[116:119]
v_add_u32 v253, v253, v252
CV v92,v120,v121
MM a[116:119],a[184:187],v[16:19],a[116:119]
s_lshr_b32 s76, s18, 6
CV v93,v122,v123
CV v94,v124,v125
MM a[108:111],a[184:187],v[8:11],a[108:111]
s_lshl_b32 s76, s76, 13
s_bfe_u32 s90, s18, 131076
CV v95,v126,v127
MM a[108:111],a[188:191],v[12:15],a[108:111]
s_lshl_b32 s90, s90, 8
s_add_u32 s76, s76, s90
s_add_u32 s76, s76, 22183936
MM a[100:103],a[188:191],v[4:7],a[100:103]
s_lshl_b32 s90, %[swave], 11
s_add_u32 s90, s90, %[slds]
MM a[100:103],a[184:187],v[0:3],a[100:103]
s_add_u32 m0, s90, 131072
s_nop 0
buffer_load_dwordx4 v253, s[20:23], s76 offen offset:0 sc1 lds
v_smfmac_f32_16x16x64_bf16 v[144:147], v[236:239], v[64:71], v234
v_smfmac_f32_16x16x64_bf16 v[148:151], v[236:239], v[72:79], v234
v_smfmac_f32_16x16x64_bf16 v[152:155], v[236:239], v[80:87], v234
s_add_u32 m0, s90, 132096
s_add_u32 s76, s76, 4096
buffer_load_dwordx4 v253, s[20:23], s76 offen offset:0 sc1 lds
v_smfmac_f32_16x16x64_bf16 v[156:159], v[236:239], v[88:95], v234
s_waitcnt lgkmcnt(0)
s_add_u32 s67, s95, 3072
s_mov_b32 s70, 3
s_cmp_ge_u32 s16, 90
s_cbranch_scc1 LLAP_%=
MM v[0:3],a[192:195],v[160:163],v[128:131]
s_add_u32 s64, s64, 16384
TR a[128:129],v232 offset:16384
s_add_u32 s81, s64, 896
MM v[0:3],a[196:199],v[176:179],v[0:3]
TR a[130:131],v232 offset:20480
v_max3_f32 v249, v144, v145, v148
MM v[0:3],a[200:203],v[192:195],v[0:3]
TR a[132:133],v232 offset:24576
v_max3_f32 v249, v249, v149, v152
MM v[0:3],a[204:207],v[208:211],v[0:3]
TR a[134:135],v232 offset:28672
)ASM"
R"ASM(
v_max3_f32 v249, v249, v153, v156
MM v[4:7],a[220:223],v[208:211],v[128:131]
TR a[136:137],v233 offset:16384
v_max_f32 v249, v249, v157
MM v[4:7],a[216:219],v[192:195],v[4:7]
TR a[138:139],v233 offset:20480
v_cmp_lt_f32 vcc, s71, v249
MM v[4:7],a[212:215],v[176:179],v[4:7]
TR a[140:141],v233 offset:24576
TR a[142:143],v233 offset:28672
MM v[4:7],a[208:211],v[160:163],v[4:7]
s_cbranch_vccnz LSITE21_%=
LBACK21_%=:
MM v[12:15],a[208:211],v[164:167],v[132:135]
EX v0,v0
MM v[12:15],a[212:215],v[180:183],v[12:15]
EX v1,v1
MM v[12:15],a[216:219],v[196:199],v[12:15]
TR a[144:145],v232 offset:16896
CV v0,v0,v1
MM v[12:15],a[220:223],v[212:215],v[12:15]
EX v2,v2
MM v[8:11],a[204:207],v[212:215],v[132:135]
EX v3,v3
MM v[8:11],a[200:203],v[196:199],v[8:11]
TR a[146:147],v232 offset:20992
CV v1,v2,v3
MM v[8:11],a[196:199],v[180:183],v[8:11]
EX v4,v4
MM v[8:11],a[192:195],v[164:167],v[8:11]
TR a[148:149],v232 offset:25088
TR a[150:151],v232 offset:29184
MM v[16:19],a[192:195],v[168:171],v[136:139]
EX v5,v5
MM v[16:19],a[196:199],v[184:187],v[16:19]
TR a[152:153],v233 offset:16896
CV v2,v4,v5
MM v[16:19],a[200:203],v[200:203],v[16:19]
EX v6,v6
MM v[16:19],a[204:207],v[216:219],v[16:19]
TR a[154:155],v233 offset:20992
TR a[156:157],v233 offset:25088
MM v[20:23],a[220:223],v[216:219],v[136:139]
s_add_u32 s87, s95, 3072
buffer_store_dword v240, v248, s[20:23], s87 offen offset:12 sc1
MM v[20:23],a[216:219],v[200:203],v[20:23]
EX v7,v7
MM v[20:23],a[212:215],v[184:187],v[20:23]
TR a[158:159],v233 offset:29184
CV v3,v6,v7
MM v[20:23],a[208:211],v[168:171],v[20:23]
EX v8,v8
MM v[28:31],a[208:211],v[172:175],v[140:143]
EX v9,v9
MM v[28:31],a[212:215],v[188:191],v[28:31]
TR a[160:161],v232 offset:17408
CV v8,v8,v9
MM v[28:31],a[216:219],v[204:207],v[28:31]
EX v10,v10
MM v[28:31],a[220:223],v[220:223],v[28:31]
EX v11,v11
MM v[24:27],a[204:207],v[220:223],v[140:143]
TR a[162:163],v232 offset:21504
CV v9,v10,v11
MM v[24:27],a[200:203],v[204:207],v[24:27]
TR a[164:165],v232 offset:25600
TR a[166:167],v232 offset:29696
MM v[24:27],a[196:199],v[188:191],v[24:27]
EX v12,v12
MM v[24:27],a[192:195],v[172:175],v[24:27]
EX v13,v13
MM v[32:35],a[224:227],v[160:163],v[128:131]
TR a[168:169],v233 offset:17408
CV v10,v12,v13
MM v[32:35],a[228:231],v[176:179],v[32:35]
EX v14,v14
MM v[32:35],a[232:235],v[192:195],v[32:35]
TR a[170:171],v233 offset:21504
TR a[172:173],v233 offset:25600
MM v[32:35],a[236:239],v[208:211],v[32:35]
EX v15,v15
MM v[36:39],a[252:255],v[208:211],v[128:131]
TR a[174:175],v233 offset:29696
CV v11,v14,v15
MM v[36:39],a[248:251],v[192:195],v[36:39]
EX v16,v16
MM v[36:39],a[244:247],v[176:179],v[36:39]
EX v17,v17
MM v[36:39],a[240:243],v[160:163],v[36:39]
TR a[176:177],v232 offset:17920
CV v16,v16,v17
MM v[44:47],a[240:243],v[164:167],v[132:135]
EX v18,v18
MM v[44:47],a[244:247],v[180:183],v[44:47]
TR a[178:179],v232 offset:22016
TR a[180:181],v232 offset:26112
MM v[44:47],a[248:251],v[196:199],v[44:47]
EX v19,v19
MM v[44:47],a[252:255],v[212:215],v[44:47]
TR a[182:183],v232 offset:30208
CV v17,v18,v19
MM v[40:43],a[236:239],v[212:215],v[132:135]
EX v20,v20
MM v[40:43],a[232:235],v[196:199],v[40:43]
EX v21,v21
MM v[40:43],a[228:231],v[180:183],v[40:43]
TR a[184:185],v233 offset:17920
CV v18,v20,v21
MM v[40:43],a[224:227],v[164:167],v[40:43]
EX v22,v22
MM v[48:51],a[224:227],v[168:171],v[136:139]
TR a[186:187],v233 offset:22016
TR a[188:189],v233 offset:26112
MM v[48:51],a[228:231],v[184:187],v[48:51]
EX v23,v23
MM v[48:51],a[232:235],v[200:203],v[48:51]
TR a[190:191],v233 offset:30208
CV v19,v22,v23
MM v[48:51],a[236:239],v[216:219],v[48:51]
EX v24,v24
MM v[52:55],a[252:255],v[216:219],v[136:139]
EX v25,v25
MM v[52:55],a[248:251],v[200:203],v[52:55]
EX v26,v26
MM v[52:55],a[244:247],v[184:187],v[52:55]
EX v27,v27
MM v[52:55],a[240:243],v[168:171],v[52:55]
EX v28,v28
MM v[60:63],a[240:243],v[172:175],v[140:143]
EX v29,v29
MM v[60:63],a[244:247],v[188:191],v[60:63]
EX v30,v30
MM v[60:63],a[248:251],v[204:207],v[60:63]
EX v31,v31
MM v[60:63],a[252:255],v[220:223],v[60:63]
EX v32,v32
MM v[56:59],a[236:239],v[220:223],v[140:143]
EX v33,v33
MM v[56:59],a[232:235],v[204:207],v[56:59]
EX v34,v34
MM v[56:59],a[228:231],v[188:191],v[56:59]
EX v35,v35
MM v[56:59],a[224:227],v[172:175],v[56:59]
s_waitcnt lgkmcnt(0)
s_barrier
MM a[0:3],a[128:131],v[64:67],a[0:3]
s_add_u32 m0, s65, 16384
CV v24,v24,v25
CV v25,v26,v27
MM a[0:3],a[132:135],v[68:71],a[0:3]
RD a[192:195],v224 offset:49152
CV v26,v28,v29
MM a[8:11],a[132:135],v[76:79],a[8:11]
RD a[196:199],v225 offset:49152
CV v27,v30,v31
MM a[8:11],a[128:131],v[72:75],a[8:11]
buffer_load_dwordx4 v228, s[8:11], s64 offen offset:0 lds
MM a[16:19],a[128:131],v[80:83],a[16:19]
RD a[200:203],v226 offset:49152
CV v4,v32,v33
MM a[16:19],a[132:135],v[84:87],a[16:19]
RD a[204:207],v227 offset:49152
CV v5,v34,v35
MM a[24:27],a[132:135],v[92:95],a[24:27]
buffer_load_dwordx4 v229, s[8:11], s64 offen offset:1024 lds
MM a[24:27],a[128:131],v[88:91],a[24:27]
RD a[208:211],v224 offset:53248
RD a[212:215],v225 offset:53248
MM a[28:31],a[136:139],v[88:91],a[28:31]
buffer_load_dwordx4 v230, s[8:11], s64 offen offset:2048 lds
MM a[28:31],a[140:143],v[92:95],a[28:31]
EX v36,v36
MM a[20:23],a[140:143],v[84:87],a[20:23]
EX v37,v37
MM a[20:23],a[136:139],v[80:83],a[20:23]
RD a[216:219],v226 offset:53248
CV v6,v36,v37
MM a[12:15],a[136:139],v[72:75],a[12:15]
EX v38,v38
MM a[12:15],a[140:143],v[76:79],a[12:15]
RD a[220:223],v227 offset:53248
RD a[224:227],v224 offset:57344
MM a[4:7],a[140:143],v[68:71],a[4:7]
EX v39,v39
MM a[4:7],a[136:139],v[64:67],a[4:7]
RD a[228:231],v225 offset:57344
CV v7,v38,v39
MM a[32:35],a[144:147],v[64:67],a[32:35]
EX v40,v40
MM a[32:35],a[148:151],v[68:71],a[32:35]
buffer_load_dwordx4 v231, s[8:11], s64 offen offset:3072 lds
MM a[40:43],a[148:151],v[76:79],a[40:43]
s_add_u32 m0, s65, 81920
EX v41,v41
MM a[40:43],a[144:147],v[72:75],a[40:43]
RD a[232:235],v226 offset:57344
CV v12,v40,v41
MM a[48:51],a[144:147],v[80:83],a[48:51]
EX v42,v42
MM a[48:51],a[148:151],v[84:87],a[48:51]
EX v43,v43
MM a[56:59],a[148:151],v[92:95],a[56:59]
RD a[236:239],v227 offset:57344
CV v13,v42,v43
MM a[56:59],a[144:147],v[88:91],a[56:59]
EX v44,v44
MM a[60:63],a[152:155],v[88:91],a[60:63]
buffer_load_dwordx4 v235, s[12:15], s81 offen offset:0 lds
MM a[60:63],a[156:159],v[92:95],a[60:63]
EX v45,v45
MM a[52:55],a[156:159],v[84:87],a[52:55]
RD a[240:243],v224 offset:61440
CV v14,v44,v45
MM a[52:55],a[152:155],v[80:83],a[52:55]
EX v46,v46
MM a[44:47],a[152:155],v[72:75],a[44:47]
EX v47,v47
MM a[44:47],a[156:159],v[76:79],a[44:47]
RD a[244:247],v225 offset:61440
CV v15,v46,v47
MM a[36:39],a[156:159],v[68:71],a[36:39]
buffer_load_dwordx4 v235, s[12:15], s64 offen offset:1024 lds
MM a[36:39],a[152:155],v[64:67],a[36:39]
EX v48,v48
MM a[64:67],a[160:163],v[64:67],a[64:67]
EX v49,v49
MM a[64:67],a[164:167],v[68:71],a[64:67]
RD a[248:251],v226 offset:61440
CV v20,v48,v49
MM a[72:75],a[164:167],v[76:79],a[72:75]
EX v50,v50
MM a[72:75],a[160:163],v[72:75],a[72:75]
EX v51,v51
MM a[80:83],a[160:163],v[80:83],a[80:83]
RD a[252:255],v227 offset:61440
CV v21,v50,v51
MM a[80:83],a[164:167],v[84:87],a[80:83]
buffer_load_dwordx4 v235, s[12:15], s81 offen offset:2048 lds
MM a[88:91],a[164:167],v[92:95],a[88:91]
EX v52,v52
MM a[88:91],a[160:163],v[88:91],a[88:91]
EX v53,v53
MM a[92:95],a[168:171],v[88:91],a[92:95]
EX v54,v54
MM a[92:95],a[172:175],v[92:95],a[92:95]
buffer_load_dwordx4 v235, s[12:15], s64 offen offset:3072 lds
MM a[84:87],a[172:175],v[84:87],a[84:87]
EX v55,v55
MM a[84:87],a[168:171],v[80:83],a[84:87]
EX v56,v56
MM a[76:79],a[168:171],v[72:75],a[76:79]
EX v57,v57
MM a[76:79],a[172:175],v[76:79],a[76:79]
EX v58,v58
MM a[68:71],a[172:175],v[68:71],a[68:71]
EX v59,v59
MM a[68:71],a[168:171],v[64:67],a[68:71]
EX v60,v60
MM a[96:99],a[176:179],v[64:67],a[96:99]
EX v61,v61
MM a[96:99],a[180:183],v[68:71],a[96:99]
EX v62,v62
MM a[104:107],a[180:183],v[76:79],a[104:107]
EX v63,v63
MM a[104:107],a[176:179],v[72:75],a[104:107]
CV v22,v52,v53
CV v23,v54,v55
MM a[112:115],a[176:179],v[80:83],a[112:115]
CV v28,v56,v57
CV v29,v58,v59
MM a[112:115],a[180:183],v[84:87],a[112:115]
CV v30,v60,v61
CV v31,v62,v63
MM a[120:123],a[180:183],v[92:95],a[120:123]
MM a[120:123],a[176:179],v[88:91],a[120:123]
MM a[124:127],a[184:187],v[88:91],a[124:127]
v_add_f32 v244, v144, v145
v_add_f32 v245, v148, v149
MM a[124:127],a[188:191],v[92:95],a[124:127]
v_add_f32 v246, v152, v153
v_add_f32 v247, v156, v157
MM a[116:119],a[188:191],v[84:87],a[116:119]
MM a[116:119],a[184:187],v[80:83],a[116:119]
MM a[108:111],a[184:187],v[72:75],a[108:111]
MM a[108:111],a[188:191],v[76:79],a[108:111]
MM a[100:103],a[188:191],v[68:71],a[100:103]
MM a[100:103],a[184:187],v[64:67],a[100:103]
v_smfmac_f32_16x16x64_bf16 v[144:147], v[236:239], v[0:7], v234
v_smfmac_f32_16x16x64_bf16 v[148:151], v[236:239], v[8:15], v234
v_smfmac_f32_16x16x64_bf16 v[152:155], v[236:239], v[16:23], v234
v_smfmac_f32_16x16x64_bf16 v[156:159], v[236:239], v[24:31], v234
s_waitcnt lgkmcnt(0)
MM v[64:67],a[192:195],v[160:163],v[128:131]
s_add_u32 s64, s64, 16384
TR a[128:129],v232 offset:32768
s_add_u32 s81, s64, 896
MM v[64:67],a[196:199],v[176:179],v[64:67]
TR a[130:131],v232 offset:36864
v_max3_f32 v249, v144, v145, v148
MM v[64:67],a[200:203],v[192:195],v[64:67]
TR a[132:133],v232 offset:40960
v_max3_f32 v249, v249, v149, v152
MM v[64:67],a[204:207],v[208:211],v[64:67]
TR a[134:135],v232 offset:45056
v_max3_f32 v249, v249, v153, v156
MM v[68:71],a[220:223],v[208:211],v[128:131]
TR a[136:137],v233 offset:32768
v_max_f32 v249, v249, v157
MM v[68:71],a[216:219],v[192:195],v[68:71]
TR a[138:139],v233 offset:36864
v_cmp_lt_f32 vcc, s71, v249
MM v[68:71],a[212:215],v[176:179],v[68:71]
TR a[140:141],v233 offset:40960
TR a[142:143],v233 offset:45056
MM v[68:71],a[208:211],v[160:163],v[68:71]
s_cbranch_vccnz LSITE22_%=
LBACK22_%=:
MM v[76:79],a[208:211],v[164:167],v[132:135]
v_lshrrev_b32 v43, 7, v248
v_bfe_u32 v42, v248, 6, 1
MM v[76:79],a[212:215],v[180:183],v[76:79]
v_lshl_add_u32 v40, v42, 3, v43
v_lshlrev_b32 v40, 4, v40
MM v[76:79],a[216:219],v[196:199],v[76:79]
v_bfe_u32 v42, v248, 4, 2
v_lshl_add_u32 v40, v42, 2, v40
MM v[76:79],a[220:223],v[212:215],v[76:79]
s_lshl_b32 s76, %[swave], 11
EX v64,v64
MM v[72:75],a[204:207],v[212:215],v[132:135]
s_add_u32 s76, s76, %[slds]
EX v65,v65
MM v[72:75],a[200:203],v[196:199],v[72:75]
s_add_u32 s76, s76, 131072
v_add_u32 v40, s76, v40
CV v64,v64,v65
MM v[72:75],a[196:199],v[180:183],v[72:75]
s_waitcnt vmcnt(9)
ds_read_b32 v32, v40 offset:0
ds_read_b32 v36, v40 offset:256
MM v[72:75],a[192:195],v[164:167],v[72:75]
ds_read_b32 v33, v40 offset:512
ds_read_b32 v37, v40 offset:768
MM v[80:83],a[192:195],v[168:171],v[136:139]
ds_read_b32 v34, v40 offset:1024
s_nop 0
LPG17_%=:
s_cbranch_execz LTR3_%=
ds_read_b32 v38, v40 offset:1280
EX v66,v66
MM v[80:83],a[196:199],v[184:187],v[80:83]
ds_read_b32 v35, v40 offset:1536
ds_read_b32 v39, v40 offset:1792
EX v67,v67
MM v[80:83],a[200:203],v[200:203],v[80:83]
v_mul_u32_u24 v41, 3456, v43
v_bfe_u32 v42, v248, 4, 3
MM v[80:83],a[204:207],v[216:219],v[80:83]
TR a[144:145],v232 offset:33280
v_lshl_add_u32 v41, v42, 1, v41
EX v68,v68
MM v[84:87],a[220:223],v[216:219],v[136:139]
s_add_u32 s76, s18, 21626880
EX v69,v69
MM v[84:87],a[216:219],v[200:203],v[84:87]
TR a[146:147],v232 offset:37376
TR a[148:149],v232 offset:41472
MM v[84:87],a[212:215],v[184:187],v[84:87]
EX v70,v70
MM v[84:87],a[208:211],v[168:171],v[84:87]
TR a[150:151],v232 offset:45568
CV v65,v66,v67
MM v[92:95],a[208:211],v[172:175],v[140:143]
EX v71,v71
MM v[92:95],a[212:215],v[188:191],v[92:95]
EX v72,v72
MM v[92:95],a[216:219],v[204:207],v[92:95]
TR a[152:153],v233 offset:33280
CV v66,v68,v69
MM v[92:95],a[220:223],v[220:223],v[92:95]
EX v73,v73
MM v[88:91],a[204:207],v[220:223],v[140:143]
s_waitcnt lgkmcnt(5)
v_add_f32 v32, v32, v33
v_add_f32 v36, v36, v37
MM v[88:91],a[200:203],v[204:207],v[88:91]
v_add_f32 v34, v34, v35
v_add_f32 v38, v38, v39
MM v[88:91],a[196:199],v[188:191],v[88:91]
TR a[154:155],v233 offset:37376
)ASM"
R"ASM(
v_add_f32 v32, v32, v34
v_add_f32 v36, v36, v38
MM v[88:91],a[192:195],v[172:175],v[88:91]
CV v42,v32,v36
v_lshlrev_b32 v32, 16, v42
EX v74,v74
MM v[96:99],a[224:227],v[160:163],v[128:131]
TR a[156:157],v233 offset:41472
v_and_b32 v36, 0xffff0000, v42
v_mul_f32 v42, 0xbfb8aa3b, v32
EX v75,v75
MM v[96:99],a[228:231],v[176:179],v[96:99]
EX v42,v42
MM v[96:99],a[232:235],v[192:195],v[96:99]
TR a[158:159],v233 offset:45568
v_add_f32 v42, 1.0, v42
EX v76,v76
MM v[96:99],a[236:239],v[208:211],v[96:99]
v_rcp_f32 v42, v42
MM v[100:103],a[252:255],v[208:211],v[128:131]
TR a[160:161],v232 offset:33792
v_mul_f32 v42, v32, v42
CV v42,v42,v42
EX v77,v77
CV v67,v70,v71
MM v[100:103],a[248:251],v[192:195],v[100:103]
v_lshlrev_b32 v42, 16, v42
v_mul_f32 v32, v42, v36
MM v[100:103],a[244:247],v[176:179],v[100:103]
TR a[162:163],v232 offset:37888
CV v43,v32,v32
EX v78,v78
MM v[100:103],a[240:243],v[160:163],v[100:103]
buffer_store_short v43, v41, s[20:23], s76 offen offset:0 sc1
s_nop 1
TR a[164:165],v232 offset:41984
MM v[108:111],a[240:243],v[164:167],v[132:135]
EX v79,v79
CV v72,v72,v73
MM v[108:111],a[244:247],v[180:183],v[108:111]
TR a[166:167],v232 offset:46080
CV v73,v74,v75
MM v[108:111],a[248:251],v[196:199],v[108:111]
EX v80,v80
MM v[108:111],a[252:255],v[212:215],v[108:111]
TR a[168:169],v233 offset:33792
TR a[170:171],v233 offset:37888
EX v81,v81
MM v[104:107],a[236:239],v[212:215],v[132:135]
TR a[172:173],v233 offset:41984
CV v74,v76,v77
MM v[104:107],a[232:235],v[196:199],v[104:107]
EX v82,v82
MM v[104:107],a[228:231],v[180:183],v[104:107]
EX v83,v83
MM v[104:107],a[224:227],v[164:167],v[104:107]
TR a[174:175],v233 offset:46080
CV v75,v78,v79
MM v[112:115],a[224:227],v[168:171],v[136:139]
EX v84,v84
MM v[112:115],a[228:231],v[184:187],v[112:115]
TR a[176:177],v232 offset:34304
TR a[178:179],v232 offset:38400
MM v[112:115],a[232:235],v[200:203],v[112:115]
EX v85,v85
MM v[112:115],a[236:239],v[216:219],v[112:115]
TR a[180:181],v232 offset:42496
CV v80,v80,v81
MM v[116:119],a[252:255],v[216:219],v[136:139]
EX v86,v86
MM v[116:119],a[248:251],v[200:203],v[116:119]
TR a[182:183],v232 offset:46592
CV v81,v82,v83
MM v[116:119],a[244:247],v[184:187],v[116:119]
EX v87,v87
MM v[116:119],a[240:243],v[168:171],v[116:119]
TR a[184:185],v233 offset:34304
CV v82,v84,v85
MM v[124:127],a[240:243],v[172:175],v[140:143]
EX v88,v88
MM v[124:127],a[244:247],v[188:191],v[124:127]
TR a[186:187],v233 offset:38400
TR a[188:189],v233 offset:42496
MM v[124:127],a[248:251],v[204:207],v[124:127]
TR a[190:191],v233 offset:46592
CV v83,v86,v87
EX v89,v89
MM v[124:127],a[252:255],v[220:223],v[124:127]
EX v90,v90
MM v[120:123],a[236:239],v[220:223],v[140:143]
EX v91,v91
MM v[120:123],a[232:235],v[204:207],v[120:123]
EX v92,v92
MM v[120:123],a[228:231],v[188:191],v[120:123]
EX v93,v93
MM v[120:123],a[224:227],v[172:175],v[120:123]
s_waitcnt lgkmcnt(0)
s_barrier
MM a[0:3],a[128:131],v[0:3],a[0:3]
s_add_u32 m0, s65, 32768
CV v88,v88,v89
CV v89,v90,v91
MM a[0:3],a[132:135],v[4:7],a[0:3]
EX v94,v94
MM a[8:11],a[132:135],v[12:15],a[8:11]
RD a[192:195],v224 offset:0
CV v90,v92,v93
MM a[8:11],a[128:131],v[8:11],a[8:11]
EX v95,v95
MM a[16:19],a[128:131],v[16:19],a[16:19]
RD a[196:199],v225 offset:0
CV v91,v94,v95
MM a[16:19],a[132:135],v[20:23],a[16:19]
EX v96,v96
MM a[24:27],a[132:135],v[28:31],a[24:27]
buffer_load_dwordx4 v228, s[8:11], s64 offen offset:0 lds
MM a[24:27],a[128:131],v[24:27],a[24:27]
EX v97,v97
MM a[28:31],a[136:139],v[24:27],a[28:31]
EX v98,v98
MM a[28:31],a[140:143],v[28:31],a[28:31]
RD a[200:203],v226 offset:0
CV v68,v96,v97
MM a[20:23],a[140:143],v[20:23],a[20:23]
EX v99,v99
MM a[20:23],a[136:139],v[16:19],a[20:23]
RD a[204:207],v227 offset:0
CV v69,v98,v99
MM a[12:15],a[136:139],v[8:11],a[12:15]
EX v100,v100
MM a[12:15],a[140:143],v[12:15],a[12:15]
buffer_load_dwordx4 v229, s[8:11], s64 offen offset:1024 lds
MM a[4:7],a[140:143],v[4:7],a[4:7]
EX v101,v101
MM a[4:7],a[136:139],v[0:3],a[4:7]
RD a[208:211],v224 offset:4096
CV v70,v100,v101
MM a[32:35],a[144:147],v[0:3],a[32:35]
EX v102,v102
MM a[32:35],a[148:151],v[4:7],a[32:35]
EX v103,v103
MM a[40:43],a[148:151],v[12:15],a[40:43]
RD a[212:215],v225 offset:4096
CV v71,v102,v103
MM a[40:43],a[144:147],v[8:11],a[40:43]
EX v104,v104
MM a[48:51],a[144:147],v[16:19],a[48:51]
buffer_load_dwordx4 v230, s[8:11], s64 offen offset:2048 lds
MM a[48:51],a[148:151],v[20:23],a[48:51]
EX v105,v105
MM a[56:59],a[148:151],v[28:31],a[56:59]
RD a[216:219],v226 offset:4096
CV v76,v104,v105
MM a[56:59],a[144:147],v[24:27],a[56:59]
EX v106,v106
MM a[60:63],a[152:155],v[24:27],a[60:63]
EX v107,v107
MM a[60:63],a[156:159],v[28:31],a[60:63]
RD a[220:223],v227 offset:4096
CV v77,v106,v107
MM a[52:55],a[156:159],v[20:23],a[52:55]
EX v108,v108
MM a[52:55],a[152:155],v[16:19],a[52:55]
buffer_load_dwordx4 v231, s[8:11], s64 offen offset:3072 lds
MM a[44:47],a[152:155],v[8:11],a[44:47]
s_add_u32 m0, s65, 98304
EX v109,v109
MM a[44:47],a[156:159],v[12:15],a[44:47]
RD a[224:227],v224 offset:8192
CV v78,v108,v109
MM a[36:39],a[156:159],v[4:7],a[36:39]
EX v110,v110
MM a[36:39],a[152:155],v[0:3],a[36:39]
EX v111,v111
MM a[64:67],a[160:163],v[0:3],a[64:67]
RD a[228:231],v225 offset:8192
CV v79,v110,v111
MM a[64:67],a[164:167],v[4:7],a[64:67]
EX v112,v112
MM a[72:75],a[164:167],v[12:15],a[72:75]
buffer_load_dwordx4 v235, s[12:15], s81 offen offset:0 lds
MM a[72:75],a[160:163],v[8:11],a[72:75]
EX v113,v113
MM a[80:83],a[160:163],v[16:19],a[80:83]
RD a[232:235],v226 offset:8192
CV v84,v112,v113
MM a[80:83],a[164:167],v[20:23],a[80:83]
EX v114,v114
MM a[88:91],a[164:167],v[28:31],a[88:91]
EX v115,v115
MM a[88:91],a[160:163],v[24:27],a[88:91]
RD a[236:239],v227 offset:8192
CV v85,v114,v115
MM a[92:95],a[168:171],v[24:27],a[92:95]
EX v116,v116
MM a[92:95],a[172:175],v[28:31],a[92:95]
buffer_load_dwordx4 v235, s[12:15], s64 offen offset:1024 lds
MM a[84:87],a[172:175],v[20:23],a[84:87]
EX v117,v117
MM a[84:87],a[168:171],v[16:19],a[84:87]
RD a[240:243],v224 offset:12288
CV v86,v116,v117
MM a[76:79],a[168:171],v[8:11],a[76:79]
EX v118,v118
MM a[76:79],a[172:175],v[12:15],a[76:79]
EX v119,v119
MM a[68:71],a[172:175],v[4:7],a[68:71]
RD a[244:247],v225 offset:12288
CV v87,v118,v119
MM a[68:71],a[168:171],v[0:3],a[68:71]
buffer_load_dwordx4 v235, s[12:15], s81 offen offset:2048 lds
MM a[96:99],a[176:179],v[0:3],a[96:99]
EX v120,v120
MM a[96:99],a[180:183],v[4:7],a[96:99]
EX v121,v121
MM a[104:107],a[180:183],v[12:15],a[104:107]
RD a[248:251],v226 offset:12288
CV v92,v120,v121
MM a[104:107],a[176:179],v[8:11],a[104:107]
EX v122,v122
MM a[112:115],a[176:179],v[16:19],a[112:115]
EX v123,v123
MM a[112:115],a[180:183],v[20:23],a[112:115]
RD a[252:255],v227 offset:12288
CV v93,v122,v123
MM a[120:123],a[180:183],v[28:31],a[120:123]
buffer_load_dwordx4 v235, s[12:15], s64 offen offset:3072 lds
MM a[120:123],a[176:179],v[24:27],a[120:123]
EX v124,v124
MM a[124:127],a[184:187],v[24:27],a[124:127]
v_add_f32 v244, v144, v145
v_add_f32 v245, v148, v149
MM a[124:127],a[188:191],v[28:31],a[124:127]
v_add_f32 v246, v152, v153
v_add_f32 v247, v156, v157
MM a[116:119],a[188:191],v[20:23],a[116:119]
EX v125,v125
MM a[116:119],a[184:187],v[16:19],a[116:119]
EX v126,v126
MM a[108:111],a[184:187],v[8:11],a[108:111]
EX v127,v127
MM a[108:111],a[188:191],v[12:15],a[108:111]
CV v94,v124,v125
CV v95,v126,v127
MM a[100:103],a[188:191],v[4:7],a[100:103]
MM a[100:103],a[184:187],v[0:3],a[100:103]
v_smfmac_f32_16x16x64_bf16 v[144:147], v[236:239], v[64:71], v234
v_smfmac_f32_16x16x64_bf16 v[148:151], v[236:239], v[72:79], v234
v_smfmac_f32_16x16x64_bf16 v[152:155], v[236:239], v[80:87], v234
v_smfmac_f32_16x16x64_bf16 v[156:159], v[236:239], v[88:95], v234
s_waitcnt lgkmcnt(0)
MM v[0:3],a[192:195],v[160:163],v[128:131]
s_add_u32 s64, s64, 16384
TR a[128:129],v232 offset:49152
s_add_u32 s81, s64, 896
MM v[0:3],a[196:199],v[176:179],v[0:3]
TR a[130:131],v232 offset:53248
v_max3_f32 v249, v144, v145, v148
MM v[0:3],a[200:203],v[192:195],v[0:3]
TR a[132:133],v232 offset:57344
v_max3_f32 v249, v249, v149, v152
MM v[0:3],a[204:207],v[208:211],v[0:3]
TR a[134:135],v232 offset:61440
v_max3_f32 v249, v249, v153, v156
MM v[4:7],a[220:223],v[208:211],v[128:131]
TR a[136:137],v233 offset:49152
v_max_f32 v249, v249, v157
MM v[4:7],a[216:219],v[192:195],v[4:7]
TR a[138:139],v233 offset:53248
v_cmp_lt_f32 vcc, s71, v249
MM v[4:7],a[212:215],v[176:179],v[4:7]
TR a[140:141],v233 offset:57344
TR a[142:143],v233 offset:61440
MM v[4:7],a[208:211],v[160:163],v[4:7]
s_cbranch_vccnz LSITE23_%=
LBACK23_%=:
MM v[12:15],a[208:211],v[164:167],v[132:135]
EX v0,v0
MM v[12:15],a[212:215],v[180:183],v[12:15]
EX v1,v1
MM v[12:15],a[216:219],v[196:199],v[12:15]
TR a[144:145],v232 offset:49664
CV v0,v0,v1
MM v[12:15],a[220:223],v[212:215],v[12:15]
EX v2,v2
MM v[8:11],a[204:207],v[212:215],v[132:135]
EX v3,v3
MM v[8:11],a[200:203],v[196:199],v[8:11]
TR a[146:147],v232 offset:53760
CV v1,v2,v3
MM v[8:11],a[196:199],v[180:183],v[8:11]
EX v4,v4
MM v[8:11],a[192:195],v[164:167],v[8:11]
TR a[148:149],v232 offset:57856
TR a[150:151],v232 offset:61952
MM v[16:19],a[192:195],v[168:171],v[136:139]
EX v5,v5
MM v[16:19],a[196:199],v[184:187],v[16:19]
TR a[152:153],v233 offset:49664
CV v2,v4,v5
MM v[16:19],a[200:203],v[200:203],v[16:19]
EX v6,v6
MM v[16:19],a[204:207],v[216:219],v[16:19]
TR a[154:155],v233 offset:53760
TR a[156:157],v233 offset:57856
MM v[20:23],a[220:223],v[216:219],v[136:139]
EX v7,v7
MM v[20:23],a[216:219],v[200:203],v[20:23]
TR a[158:159],v233 offset:61952
CV v3,v6,v7
MM v[20:23],a[212:215],v[184:187],v[20:23]
EX v8,v8
MM v[20:23],a[208:211],v[168:171],v[20:23]
EX v9,v9
MM v[28:31],a[208:211],v[172:175],v[140:143]
TR a[160:161],v232 offset:50176
CV v8,v8,v9
MM v[28:31],a[212:215],v[188:191],v[28:31]
EX v10,v10
MM v[28:31],a[216:219],v[204:207],v[28:31]
TR a[162:163],v232 offset:54272
TR a[164:165],v232 offset:58368
MM v[28:31],a[220:223],v[220:223],v[28:31]
EX v11,v11
MM v[24:27],a[204:207],v[220:223],v[140:143]
TR a[166:167],v232 offset:62464
CV v9,v10,v11
MM v[24:27],a[200:203],v[204:207],v[24:27]
EX v12,v12
MM v[24:27],a[196:199],v[188:191],v[24:27]
EX v13,v13
MM v[24:27],a[192:195],v[172:175],v[24:27]
TR a[168:169],v233 offset:50176
CV v10,v12,v13
MM v[32:35],a[224:227],v[160:163],v[128:131]
EX v14,v14
MM v[32:35],a[228:231],v[176:179],v[32:35]
TR a[170:171],v233 offset:54272
TR a[172:173],v233 offset:58368
MM v[32:35],a[232:235],v[192:195],v[32:35]
EX v15,v15
MM v[32:35],a[236:239],v[208:211],v[32:35]
TR a[174:175],v233 offset:62464
CV v11,v14,v15
MM v[36:39],a[252:255],v[208:211],v[128:131]
EX v16,v16
MM v[36:39],a[248:251],v[192:195],v[36:39]
EX v17,v17
MM v[36:39],a[244:247],v[176:179],v[36:39]
TR a[176:177],v232 offset:50688
CV v16,v16,v17
MM v[36:39],a[240:243],v[160:163],v[36:39]
EX v18,v18
MM v[44:47],a[240:243],v[164:167],v[132:135]
TR a[178:179],v232 offset:54784
TR a[180:181],v232 offset:58880
MM v[44:47],a[244:247],v[180:183],v[44:47]
EX v19,v19
MM v[44:47],a[248:251],v[196:199],v[44:47]
TR a[182:183],v232 offset:62976
CV v17,v18,v19
MM v[44:47],a[252:255],v[212:215],v[44:47]
EX v20,v20
MM v[40:43],a[236:239],v[212:215],v[132:135]
TR a[184:185],v233 offset:50688
TR a[186:187],v233 offset:54784
MM v[40:43],a[232:235],v[196:199],v[40:43]
EX v21,v21
MM v[40:43],a[228:231],v[180:183],v[40:43]
TR a[188:189],v233 offset:58880
CV v18,v20,v21
MM v[40:43],a[224:227],v[164:167],v[40:43]
EX v22,v22
MM v[48:51],a[224:227],v[168:171],v[136:139]
EX v23,v23
MM v[48:51],a[228:231],v[184:187],v[48:51]
TR a[190:191],v233 offset:62976
CV v19,v22,v23
MM v[48:51],a[232:235],v[200:203],v[48:51]
EX v24,v24
MM v[48:51],a[236:239],v[216:219],v[48:51]
EX v25,v25
MM v[52:55],a[252:255],v[216:219],v[136:139]
EX v26,v26
MM v[52:55],a[248:251],v[200:203],v[52:55]
EX v27,v27
MM v[52:55],a[244:247],v[184:187],v[52:55]
EX v28,v28
MM v[52:55],a[240:243],v[168:171],v[52:55]
EX v29,v29
MM v[60:63],a[240:243],v[172:175],v[140:143]
EX v30,v30
MM v[60:63],a[244:247],v[188:191],v[60:63]
EX v31,v31
MM v[60:63],a[248:251],v[204:207],v[60:63]
EX v32,v32
MM v[60:63],a[252:255],v[220:223],v[60:63]
)ASM"
R"ASM(
EX v33,v33
MM v[56:59],a[236:239],v[220:223],v[140:143]
EX v34,v34
MM v[56:59],a[232:235],v[204:207],v[56:59]
EX v35,v35
MM v[56:59],a[228:231],v[188:191],v[56:59]
EX v36,v36
MM v[56:59],a[224:227],v[172:175],v[56:59]
s_waitcnt vmcnt(13) lgkmcnt(0)
s_barrier
MM a[0:3],a[128:131],v[64:67],a[0:3]
s_add_u32 m0, s65, 49152
CV v24,v24,v25
CV v25,v26,v27
MM a[0:3],a[132:135],v[68:71],a[0:3]
RD a[192:195],v224 offset:16384
CV v26,v28,v29
MM a[8:11],a[132:135],v[76:79],a[8:11]
RD a[196:199],v225 offset:16384
CV v27,v30,v31
MM a[8:11],a[128:131],v[72:75],a[8:11]
buffer_load_dwordx4 v228, s[8:11], s64 offen offset:0 lds
MM a[16:19],a[128:131],v[80:83],a[16:19]
RD a[200:203],v226 offset:16384
CV v4,v32,v33
MM a[16:19],a[132:135],v[84:87],a[16:19]
RD a[204:207],v227 offset:16384
CV v5,v34,v35
MM a[24:27],a[132:135],v[92:95],a[24:27]
buffer_load_dwordx4 v229, s[8:11], s64 offen offset:1024 lds
MM a[24:27],a[128:131],v[88:91],a[24:27]
RD a[208:211],v224 offset:20480
RD a[212:215],v225 offset:20480
MM a[28:31],a[136:139],v[88:91],a[28:31]
buffer_load_dwordx4 v230, s[8:11], s64 offen offset:2048 lds
MM a[28:31],a[140:143],v[92:95],a[28:31]
RD a[216:219],v226 offset:20480
RD a[220:223],v227 offset:20480
MM a[20:23],a[140:143],v[84:87],a[20:23]
EX v37,v37
MM a[20:23],a[136:139],v[80:83],a[20:23]
RD a[224:227],v224 offset:24576
CV v6,v36,v37
MM a[12:15],a[136:139],v[72:75],a[12:15]
EX v38,v38
MM a[12:15],a[140:143],v[76:79],a[12:15]
buffer_load_dwordx4 v231, s[8:11], s64 offen offset:3072 lds
MM a[4:7],a[140:143],v[68:71],a[4:7]
s_add_u32 m0, s65, 114688
EX v39,v39
MM a[4:7],a[136:139],v[64:67],a[4:7]
RD a[228:231],v225 offset:24576
CV v7,v38,v39
MM a[32:35],a[144:147],v[64:67],a[32:35]
EX v40,v40
MM a[32:35],a[148:151],v[68:71],a[32:35]
EX v41,v41
MM a[40:43],a[148:151],v[76:79],a[40:43]
RD a[232:235],v226 offset:24576
CV v12,v40,v41
MM a[40:43],a[144:147],v[72:75],a[40:43]
EX v42,v42
MM a[48:51],a[144:147],v[80:83],a[48:51]
buffer_load_dwordx4 v235, s[12:15], s81 offen offset:0 lds
MM a[48:51],a[148:151],v[84:87],a[48:51]
EX v43,v43
MM a[56:59],a[148:151],v[92:95],a[56:59]
RD a[236:239],v227 offset:24576
CV v13,v42,v43
MM a[56:59],a[144:147],v[88:91],a[56:59]
EX v44,v44
MM a[60:63],a[152:155],v[88:91],a[60:63]
EX v45,v45
MM a[60:63],a[156:159],v[92:95],a[60:63]
RD a[240:243],v224 offset:28672
CV v14,v44,v45
MM a[52:55],a[156:159],v[84:87],a[52:55]
EX v46,v46
MM a[52:55],a[152:155],v[80:83],a[52:55]
buffer_load_dwordx4 v235, s[12:15], s64 offen offset:1024 lds
MM a[44:47],a[152:155],v[72:75],a[44:47]
EX v47,v47
MM a[44:47],a[156:159],v[76:79],a[44:47]
RD a[244:247],v225 offset:28672
CV v15,v46,v47
MM a[36:39],a[156:159],v[68:71],a[36:39]
EX v48,v48
MM a[36:39],a[152:155],v[64:67],a[36:39]
EX v49,v49
MM a[64:67],a[160:163],v[64:67],a[64:67]
RD a[248:251],v226 offset:28672
CV v20,v48,v49
MM a[64:67],a[164:167],v[68:71],a[64:67]
EX v50,v50
MM a[72:75],a[164:167],v[76:79],a[72:75]
buffer_load_dwordx4 v235, s[12:15], s81 offen offset:2048 lds
MM a[72:75],a[160:163],v[72:75],a[72:75]
EX v51,v51
MM a[80:83],a[160:163],v[80:83],a[80:83]
RD a[252:255],v227 offset:28672
CV v21,v50,v51
MM a[80:83],a[164:167],v[84:87],a[80:83]
EX v52,v52
MM a[88:91],a[164:167],v[92:95],a[88:91]
EX v53,v53
MM a[88:91],a[160:163],v[88:91],a[88:91]
buffer_load_dwordx4 v235, s[12:15], s64 offen offset:3072 lds
MM a[92:95],a[168:171],v[88:91],a[92:95]
EX v54,v54
MM a[92:95],a[172:175],v[92:95],a[92:95]
EX v55,v55
MM a[84:87],a[172:175],v[84:87],a[84:87]
EX v56,v56
MM a[84:87],a[168:171],v[80:83],a[84:87]
EX v57,v57
MM a[76:79],a[168:171],v[72:75],a[76:79]
EX v58,v58
MM a[76:79],a[172:175],v[76:79],a[76:79]
EX v59,v59
MM a[68:71],a[172:175],v[68:71],a[68:71]
EX v60,v60
MM a[68:71],a[168:171],v[64:67],a[68:71]
EX v61,v61
MM a[96:99],a[176:179],v[64:67],a[96:99]
EX v62,v62
MM a[96:99],a[180:183],v[68:71],a[96:99]
EX v63,v63
MM a[104:107],a[180:183],v[76:79],a[104:107]
CV v22,v52,v53
CV v23,v54,v55
MM a[104:107],a[176:179],v[72:75],a[104:107]
CV v28,v56,v57
CV v29,v58,v59
MM a[112:115],a[176:179],v[80:83],a[112:115]
CV v30,v60,v61
CV v31,v62,v63
MM a[112:115],a[180:183],v[84:87],a[112:115]
MM a[120:123],a[180:183],v[92:95],a[120:123]
s_lshr_b32 s91, s18, 2
s_add_u32 s91, s91, 69632
s_waitcnt vmcnt(16)
MM a[120:123],a[176:179],v[88:91],a[120:123]
v_mov_b32 v250, s69
s_mov_b64 exec, 1
buffer_store_dword v250, off, s[20:23], s91 offset:0 sc1
s_mov_b64 exec, -1
MM a[124:127],a[184:187],v[88:91],a[124:127]
v_add_f32 v244, v144, v145
v_add_f32 v245, v148, v149
MM a[124:127],a[188:191],v[92:95],a[124:127]
v_add_f32 v246, v152, v153
v_add_f32 v247, v156, v157
v_lshlrev_b32 v96, 3, v248
MM a[116:119],a[188:191],v[84:87],a[116:119]
v_min_u32 v96, 4992, v96
LPG18_%=:
s_cbranch_execz LTR3_%=
s_sub_u32 s91, 5242880, s26
MM a[116:119],a[184:187],v[80:83],a[116:119]
s_sub_u32 s60, s24, s91
s_subb_u32 s61, s25, 0
s_mov_b32 s62, 5242880
MM a[108:111],a[184:187],v[72:75],a[108:111]
s_mov_b32 s63, 131072
s_lshl_b32 s90, s16, 2
s_add_u32 s90, s90, %[swave]
MM a[108:111],a[188:191],v[76:79],a[108:111]
s_mul_i32 s90, s90, 10240
s_add_u32 s90, s90, 0
s_add_u32 s91, %[slds], 135168
MM a[100:103],a[188:191],v[68:71],a[100:103]
s_mov_b32 m0, s91
s_nop 0
buffer_load_dword v96, s[60:63], s90 offen offset:0 lds
MM a[100:103],a[184:187],v[64:67],a[100:103]
s_lshr_b32 s77, %[sb], 3
s_lshl_b32 s77, s77, 2
s_add_u32 s77, s77, %[swave]
v_smfmac_f32_16x16x64_bf16 v[144:147], v[236:239], v[0:7], v234
s_lshl_b32 s77, s77, 9
s_add_u32 s77, s77, 0
v_smfmac_f32_16x16x64_bf16 v[148:151], v[236:239], v[8:15], v234
v_lshrrev_b32 v253, 1, v248
v_smfmac_f32_16x16x64_bf16 v[152:155], v[236:239], v[16:23], v234
v_add_u32 v253, s77, v253
v_smfmac_f32_16x16x64_bf16 v[156:159], v[236:239], v[24:31], v234
s_waitcnt lgkmcnt(0)
s_mov_b32 s77, LKEND_ka-LPD1_%=-16
v_min_u32 v253, s77, v253
s_getpc_b64 s[98:99]
LPD1_%=:
global_load_dword v253, v253, s[98:99]
MM v[64:67],a[192:195],v[160:163],v[128:131]
s_add_u32 s64, s64, 16384
TR a[128:129],v232 offset:0
s_add_u32 s81, s64, 896
MM v[64:67],a[196:199],v[176:179],v[64:67]
TR a[130:131],v232 offset:4096
v_max3_f32 v249, v144, v145, v148
MM v[64:67],a[200:203],v[192:195],v[64:67]
TR a[132:133],v232 offset:8192
v_max3_f32 v249, v249, v149, v152
MM v[64:67],a[204:207],v[208:211],v[64:67]
TR a[134:135],v232 offset:12288
v_max3_f32 v249, v249, v153, v156
MM v[68:71],a[220:223],v[208:211],v[128:131]
TR a[136:137],v233 offset:0
v_max_f32 v249, v249, v157
MM v[68:71],a[216:219],v[192:195],v[68:71]
TR a[138:139],v233 offset:4096
v_cmp_lt_f32 vcc, s71, v249
MM v[68:71],a[212:215],v[176:179],v[68:71]
TR a[140:141],v233 offset:8192
TR a[142:143],v233 offset:12288
MM v[68:71],a[208:211],v[160:163],v[68:71]
s_cbranch_vccnz LSITE24_%=
LBACK24_%=:
MM v[76:79],a[208:211],v[164:167],v[132:135]
EX v64,v64
MM v[76:79],a[212:215],v[180:183],v[76:79]
EX v65,v65
MM v[76:79],a[216:219],v[196:199],v[76:79]
TR a[144:145],v232 offset:512
CV v64,v64,v65
MM v[76:79],a[220:223],v[212:215],v[76:79]
EX v66,v66
MM v[72:75],a[204:207],v[212:215],v[132:135]
EX v67,v67
MM v[72:75],a[200:203],v[196:199],v[72:75]
TR a[146:147],v232 offset:4608
CV v65,v66,v67
MM v[72:75],a[196:199],v[180:183],v[72:75]
EX v68,v68
MM v[72:75],a[192:195],v[164:167],v[72:75]
TR a[148:149],v232 offset:8704
TR a[150:151],v232 offset:12800
MM v[80:83],a[192:195],v[168:171],v[136:139]
EX v69,v69
MM v[80:83],a[196:199],v[184:187],v[80:83]
TR a[152:153],v233 offset:512
CV v66,v68,v69
MM v[80:83],a[200:203],v[200:203],v[80:83]
EX v70,v70
MM v[80:83],a[204:207],v[216:219],v[80:83]
TR a[154:155],v233 offset:4608
TR a[156:157],v233 offset:8704
MM v[84:87],a[220:223],v[216:219],v[136:139]
s_add_u32 s87, s95, 4096
buffer_store_dword v240, v248, s[20:23], s87 offen offset:12 sc1
MM v[84:87],a[216:219],v[200:203],v[84:87]
EX v71,v71
MM v[84:87],a[212:215],v[184:187],v[84:87]
TR a[158:159],v233 offset:12800
CV v67,v70,v71
MM v[84:87],a[208:211],v[168:171],v[84:87]
EX v72,v72
MM v[92:95],a[208:211],v[172:175],v[140:143]
EX v73,v73
MM v[92:95],a[212:215],v[188:191],v[92:95]
TR a[160:161],v232 offset:1024
CV v72,v72,v73
MM v[92:95],a[216:219],v[204:207],v[92:95]
EX v74,v74
MM v[92:95],a[220:223],v[220:223],v[92:95]
EX v75,v75
MM v[88:91],a[204:207],v[220:223],v[140:143]
TR a[162:163],v232 offset:5120
CV v73,v74,v75
MM v[88:91],a[200:203],v[204:207],v[88:91]
TR a[164:165],v232 offset:9216
TR a[166:167],v232 offset:13312
MM v[88:91],a[196:199],v[188:191],v[88:91]
EX v76,v76
MM v[88:91],a[192:195],v[172:175],v[88:91]
EX v77,v77
MM v[96:99],a[224:227],v[160:163],v[128:131]
TR a[168:169],v233 offset:1024
CV v74,v76,v77
MM v[96:99],a[228:231],v[176:179],v[96:99]
EX v78,v78
MM v[96:99],a[232:235],v[192:195],v[96:99]
TR a[170:171],v233 offset:5120
TR a[172:173],v233 offset:9216
MM v[96:99],a[236:239],v[208:211],v[96:99]
EX v79,v79
MM v[100:103],a[252:255],v[208:211],v[128:131]
TR a[174:175],v233 offset:13312
CV v75,v78,v79
MM v[100:103],a[248:251],v[192:195],v[100:103]
EX v80,v80
MM v[100:103],a[244:247],v[176:179],v[100:103]
EX v81,v81
MM v[100:103],a[240:243],v[160:163],v[100:103]
TR a[176:177],v232 offset:1536
CV v80,v80,v81
MM v[108:111],a[240:243],v[164:167],v[132:135]
EX v82,v82
MM v[108:111],a[244:247],v[180:183],v[108:111]
TR a[178:179],v232 offset:5632
TR a[180:181],v232 offset:9728
MM v[108:111],a[248:251],v[196:199],v[108:111]
EX v83,v83
MM v[108:111],a[252:255],v[212:215],v[108:111]
TR a[182:183],v232 offset:13824
CV v81,v82,v83
MM v[104:107],a[236:239],v[212:215],v[132:135]
EX v84,v84
MM v[104:107],a[232:235],v[196:199],v[104:107]
EX v85,v85
MM v[104:107],a[228:231],v[180:183],v[104:107]
TR a[184:185],v233 offset:1536
CV v82,v84,v85
MM v[104:107],a[224:227],v[164:167],v[104:107]
EX v86,v86
MM v[112:115],a[224:227],v[168:171],v[136:139]
TR a[186:187],v233 offset:5632
TR a[188:189],v233 offset:9728
MM v[112:115],a[228:231],v[184:187],v[112:115]
EX v87,v87
MM v[112:115],a[232:235],v[200:203],v[112:115]
TR a[190:191],v233 offset:13824
CV v83,v86,v87
MM v[112:115],a[236:239],v[216:219],v[112:115]
EX v88,v88
MM v[116:119],a[252:255],v[216:219],v[136:139]
EX v89,v89
MM v[116:119],a[248:251],v[200:203],v[116:119]
EX v90,v90
MM v[116:119],a[244:247],v[184:187],v[116:119]
EX v91,v91
MM v[116:119],a[240:243],v[168:171],v[116:119]
EX v92,v92
MM v[124:127],a[240:243],v[172:175],v[140:143]
EX v93,v93
MM v[124:127],a[244:247],v[188:191],v[124:127]
EX v94,v94
MM v[124:127],a[248:251],v[204:207],v[124:127]
EX v95,v95
MM v[124:127],a[252:255],v[220:223],v[124:127]
EX v96,v96
MM v[120:123],a[236:239],v[220:223],v[140:143]
EX v97,v97
MM v[120:123],a[232:235],v[204:207],v[120:123]
EX v98,v98
MM v[120:123],a[228:231],v[188:191],v[120:123]
EX v99,v99
MM v[120:123],a[224:227],v[172:175],v[120:123]
s_waitcnt vmcnt(16) lgkmcnt(0)
s_barrier
MM a[0:3],a[128:131],v[0:3],a[0:3]
s_add_u32 m0, s65, 0
CV v88,v88,v89
CV v89,v90,v91
MM a[0:3],a[132:135],v[4:7],a[0:3]
RD a[192:195],v224 offset:32768
CV v90,v92,v93
MM a[8:11],a[132:135],v[12:15],a[8:11]
RD a[196:199],v225 offset:32768
CV v91,v94,v95
MM a[8:11],a[128:131],v[8:11],a[8:11]
buffer_load_dwordx4 v228, s[8:11], s64 offen offset:0 lds
MM a[16:19],a[128:131],v[16:19],a[16:19]
RD a[200:203],v226 offset:32768
CV v68,v96,v97
MM a[16:19],a[132:135],v[20:23],a[16:19]
RD a[204:207],v227 offset:32768
CV v69,v98,v99
MM a[24:27],a[132:135],v[28:31],a[24:27]
buffer_load_dwordx4 v229, s[8:11], s64 offen offset:1024 lds
MM a[24:27],a[128:131],v[24:27],a[24:27]
RD a[208:211],v224 offset:36864
RD a[212:215],v225 offset:36864
MM a[28:31],a[136:139],v[24:27],a[28:31]
buffer_load_dwordx4 v230, s[8:11], s64 offen offset:2048 lds
MM a[28:31],a[140:143],v[28:31],a[28:31]
EX v100,v100
MM a[20:23],a[140:143],v[20:23],a[20:23]
EX v101,v101
MM a[20:23],a[136:139],v[16:19],a[20:23]
RD a[216:219],v226 offset:36864
CV v70,v100,v101
MM a[12:15],a[136:139],v[8:11],a[12:15]
EX v102,v102
MM a[12:15],a[140:143],v[12:15],a[12:15]
RD a[220:223],v227 offset:36864
RD a[224:227],v224 offset:40960
MM a[4:7],a[140:143],v[4:7],a[4:7]
EX v103,v103
MM a[4:7],a[136:139],v[0:3],a[4:7]
RD a[228:231],v225 offset:40960
CV v71,v102,v103
)ASM"
R"ASM(
MM a[32:35],a[144:147],v[0:3],a[32:35]
EX v104,v104
MM a[32:35],a[148:151],v[4:7],a[32:35]
buffer_load_dwordx4 v231, s[8:11], s64 offen offset:3072 lds
MM a[40:43],a[148:151],v[12:15],a[40:43]
s_add_u32 m0, s65, 65536
EX v105,v105
MM a[40:43],a[144:147],v[8:11],a[40:43]
RD a[232:235],v226 offset:40960
CV v76,v104,v105
MM a[48:51],a[144:147],v[16:19],a[48:51]
EX v106,v106
MM a[48:51],a[148:151],v[20:23],a[48:51]
EX v107,v107
MM a[56:59],a[148:151],v[28:31],a[56:59]
RD a[236:239],v227 offset:40960
CV v77,v106,v107
MM a[56:59],a[144:147],v[24:27],a[56:59]
EX v108,v108
MM a[60:63],a[152:155],v[24:27],a[60:63]
buffer_load_dwordx4 v235, s[12:15], s81 offen offset:0 lds
MM a[60:63],a[156:159],v[28:31],a[60:63]
EX v109,v109
MM a[52:55],a[156:159],v[20:23],a[52:55]
RD a[240:243],v224 offset:45056
CV v78,v108,v109
MM a[52:55],a[152:155],v[16:19],a[52:55]
EX v110,v110
MM a[44:47],a[152:155],v[8:11],a[44:47]
EX v111,v111
MM a[44:47],a[156:159],v[12:15],a[44:47]
RD a[244:247],v225 offset:45056
CV v79,v110,v111
MM a[36:39],a[156:159],v[4:7],a[36:39]
buffer_load_dwordx4 v235, s[12:15], s64 offen offset:1024 lds
MM a[36:39],a[152:155],v[0:3],a[36:39]
EX v112,v112
MM a[64:67],a[160:163],v[0:3],a[64:67]
EX v113,v113
MM a[64:67],a[164:167],v[4:7],a[64:67]
RD a[248:251],v226 offset:45056
CV v84,v112,v113
MM a[72:75],a[164:167],v[12:15],a[72:75]
EX v114,v114
MM a[72:75],a[160:163],v[8:11],a[72:75]
EX v115,v115
MM a[80:83],a[160:163],v[16:19],a[80:83]
RD a[252:255],v227 offset:45056
CV v85,v114,v115
MM a[80:83],a[164:167],v[20:23],a[80:83]
buffer_load_dwordx4 v235, s[12:15], s81 offen offset:2048 lds
MM a[88:91],a[164:167],v[28:31],a[88:91]
EX v116,v116
MM a[88:91],a[160:163],v[24:27],a[88:91]
EX v117,v117
MM a[92:95],a[168:171],v[24:27],a[92:95]
EX v118,v118
MM a[92:95],a[172:175],v[28:31],a[92:95]
buffer_load_dwordx4 v235, s[12:15], s64 offen offset:3072 lds
MM a[84:87],a[172:175],v[20:23],a[84:87]
EX v119,v119
MM a[84:87],a[168:171],v[16:19],a[84:87]
EX v120,v120
MM a[76:79],a[168:171],v[8:11],a[76:79]
EX v121,v121
MM a[76:79],a[172:175],v[12:15],a[76:79]
EX v122,v122
MM a[68:71],a[172:175],v[4:7],a[68:71]
EX v123,v123
MM a[68:71],a[168:171],v[0:3],a[68:71]
EX v124,v124
MM a[96:99],a[176:179],v[0:3],a[96:99]
EX v125,v125
MM a[96:99],a[180:183],v[4:7],a[96:99]
EX v126,v126
MM a[104:107],a[180:183],v[12:15],a[104:107]
EX v127,v127
MM a[104:107],a[176:179],v[8:11],a[104:107]
CV v86,v116,v117
CV v87,v118,v119
MM a[112:115],a[176:179],v[16:19],a[112:115]
CV v92,v120,v121
CV v93,v122,v123
MM a[112:115],a[180:183],v[20:23],a[112:115]
CV v94,v124,v125
CV v95,v126,v127
MM a[120:123],a[180:183],v[28:31],a[120:123]
MM a[120:123],a[176:179],v[24:27],a[120:123]
MM a[124:127],a[184:187],v[24:27],a[124:127]
v_add_f32 v244, v144, v145
v_add_f32 v245, v148, v149
MM a[124:127],a[188:191],v[28:31],a[124:127]
v_add_f32 v246, v152, v153
v_add_f32 v247, v156, v157
MM a[116:119],a[188:191],v[20:23],a[116:119]
MM a[116:119],a[184:187],v[16:19],a[116:119]
MM a[108:111],a[184:187],v[8:11],a[108:111]
MM a[108:111],a[188:191],v[12:15],a[108:111]
MM a[100:103],a[188:191],v[4:7],a[100:103]
MM a[100:103],a[184:187],v[0:3],a[100:103]
v_smfmac_f32_16x16x64_bf16 v[144:147], v[236:239], v[64:71], v234
v_smfmac_f32_16x16x64_bf16 v[148:151], v[236:239], v[72:79], v234
v_smfmac_f32_16x16x64_bf16 v[152:155], v[236:239], v[80:87], v234
v_smfmac_f32_16x16x64_bf16 v[156:159], v[236:239], v[88:95], v234
s_waitcnt lgkmcnt(0)
MM v[0:3],a[192:195],v[160:163],v[128:131]
s_add_u32 s64, s64, 16384
TR a[128:129],v232 offset:16384
s_add_u32 s81, s64, 896
MM v[0:3],a[196:199],v[176:179],v[0:3]
TR a[130:131],v232 offset:20480
v_max3_f32 v249, v144, v145, v148
MM v[0:3],a[200:203],v[192:195],v[0:3]
TR a[132:133],v232 offset:24576
v_max3_f32 v249, v249, v149, v152
MM v[0:3],a[204:207],v[208:211],v[0:3]
TR a[134:135],v232 offset:28672
v_max3_f32 v249, v249, v153, v156
MM v[4:7],a[220:223],v[208:211],v[128:131]
TR a[136:137],v233 offset:16384
v_max_f32 v249, v249, v157
MM v[4:7],a[216:219],v[192:195],v[4:7]
TR a[138:139],v233 offset:20480
v_cmp_lt_f32 vcc, s71, v249
MM v[4:7],a[212:215],v[176:179],v[4:7]
TR a[140:141],v233 offset:24576
TR a[142:143],v233 offset:28672
MM v[4:7],a[208:211],v[160:163],v[4:7]
s_cbranch_vccnz LSITE25_%=
LBACK25_%=:
MM v[12:15],a[208:211],v[164:167],v[132:135]
s_waitcnt vmcnt(9)
EX v0,v0
MM v[12:15],a[212:215],v[180:183],v[12:15]
s_mul_i32 s76, s46, 96
EX v1,v1
MM v[12:15],a[216:219],v[196:199],v[12:15]
s_add_u32 s76, s76, 69632
v_lshrrev_b32 v253, 2, v248
v_min_u32 v253, 92, v253
MM v[12:15],a[220:223],v[212:215],v[12:15]
buffer_load_dword v252, v253, s[20:23], s76 offen offset:0 sc1
MM v[8:11],a[204:207],v[212:215],v[132:135]
EX v2,v2
MM v[8:11],a[200:203],v[196:199],v[8:11]
TR a[144:145],v232 offset:16896
CV v0,v0,v1
MM v[8:11],a[196:199],v[180:183],v[8:11]
EX v3,v3
MM v[8:11],a[192:195],v[164:167],v[8:11]
TR a[146:147],v232 offset:20992
CV v1,v2,v3
MM v[16:19],a[192:195],v[168:171],v[136:139]
EX v4,v4
MM v[16:19],a[196:199],v[184:187],v[16:19]
TR a[148:149],v232 offset:25088
TR a[150:151],v232 offset:29184
MM v[16:19],a[200:203],v[200:203],v[16:19]
EX v5,v5
MM v[16:19],a[204:207],v[216:219],v[16:19]
TR a[152:153],v233 offset:16896
CV v2,v4,v5
MM v[20:23],a[220:223],v[216:219],v[136:139]
EX v6,v6
MM v[20:23],a[216:219],v[200:203],v[20:23]
EX v7,v7
MM v[20:23],a[212:215],v[184:187],v[20:23]
TR a[154:155],v233 offset:20992
CV v3,v6,v7
MM v[20:23],a[208:211],v[168:171],v[20:23]
EX v8,v8
MM v[28:31],a[208:211],v[172:175],v[140:143]
TR a[156:157],v233 offset:25088
TR a[158:159],v233 offset:29184
MM v[28:31],a[212:215],v[188:191],v[28:31]
EX v9,v9
MM v[28:31],a[216:219],v[204:207],v[28:31]
TR a[160:161],v232 offset:17408
CV v8,v8,v9
MM v[28:31],a[220:223],v[220:223],v[28:31]
EX v10,v10
MM v[24:27],a[204:207],v[220:223],v[140:143]
EX v11,v11
MM v[24:27],a[200:203],v[204:207],v[24:27]
TR a[162:163],v232 offset:21504
CV v9,v10,v11
MM v[24:27],a[196:199],v[188:191],v[24:27]
EX v12,v12
MM v[24:27],a[192:195],v[172:175],v[24:27]
TR a[164:165],v232 offset:25600
TR a[166:167],v232 offset:29696
MM v[32:35],a[224:227],v[160:163],v[128:131]
EX v13,v13
MM v[32:35],a[228:231],v[176:179],v[32:35]
TR a[168:169],v233 offset:17408
CV v10,v12,v13
MM v[32:35],a[232:235],v[192:195],v[32:35]
EX v14,v14
MM v[32:35],a[236:239],v[208:211],v[32:35]
EX v15,v15
MM v[36:39],a[252:255],v[208:211],v[128:131]
TR a[170:171],v233 offset:21504
CV v11,v14,v15
MM v[36:39],a[248:251],v[192:195],v[36:39]
EX v16,v16
MM v[36:39],a[244:247],v[176:179],v[36:39]
TR a[172:173],v233 offset:25600
TR a[174:175],v233 offset:29696
MM v[36:39],a[240:243],v[160:163],v[36:39]
EX v17,v17
MM v[44:47],a[240:243],v[164:167],v[132:135]
TR a[176:177],v232 offset:17920
CV v16,v16,v17
MM v[44:47],a[244:247],v[180:183],v[44:47]
EX v18,v18
MM v[44:47],a[248:251],v[196:199],v[44:47]
TR a[178:179],v232 offset:22016
TR a[180:181],v232 offset:26112
MM v[44:47],a[252:255],v[212:215],v[44:47]
EX v19,v19
MM v[40:43],a[236:239],v[212:215],v[132:135]
TR a[182:183],v232 offset:30208
CV v17,v18,v19
MM v[40:43],a[232:235],v[196:199],v[40:43]
EX v20,v20
MM v[40:43],a[228:231],v[180:183],v[40:43]
EX v21,v21
MM v[40:43],a[224:227],v[164:167],v[40:43]
TR a[184:185],v233 offset:17920
CV v18,v20,v21
MM v[48:51],a[224:227],v[168:171],v[136:139]
EX v22,v22
MM v[48:51],a[228:231],v[184:187],v[48:51]
TR a[186:187],v233 offset:22016
TR a[188:189],v233 offset:26112
MM v[48:51],a[232:235],v[200:203],v[48:51]
EX v23,v23
MM v[48:51],a[236:239],v[216:219],v[48:51]
TR a[190:191],v233 offset:30208
CV v19,v22,v23
MM v[52:55],a[252:255],v[216:219],v[136:139]
EX v24,v24
MM v[52:55],a[248:251],v[200:203],v[52:55]
EX v25,v25
MM v[52:55],a[244:247],v[184:187],v[52:55]
EX v26,v26
MM v[52:55],a[240:243],v[168:171],v[52:55]
EX v27,v27
MM v[60:63],a[240:243],v[172:175],v[140:143]
EX v28,v28
MM v[60:63],a[244:247],v[188:191],v[60:63]
EX v29,v29
MM v[60:63],a[248:251],v[204:207],v[60:63]
EX v30,v30
MM v[60:63],a[252:255],v[220:223],v[60:63]
EX v31,v31
MM v[56:59],a[236:239],v[220:223],v[140:143]
EX v32,v32
MM v[56:59],a[232:235],v[204:207],v[56:59]
EX v33,v33
MM v[56:59],a[228:231],v[188:191],v[56:59]
EX v34,v34
MM v[56:59],a[224:227],v[172:175],v[56:59]
s_waitcnt lgkmcnt(0)
s_barrier
MM a[0:3],a[128:131],v[64:67],a[0:3]
s_add_u32 m0, s65, 16384
CV v24,v24,v25
CV v25,v26,v27
MM a[0:3],a[132:135],v[68:71],a[0:3]
RD a[192:195],v224 offset:49152
CV v26,v28,v29
MM a[8:11],a[132:135],v[76:79],a[8:11]
RD a[196:199],v225 offset:49152
CV v27,v30,v31
MM a[8:11],a[128:131],v[72:75],a[8:11]
buffer_load_dwordx4 v228, s[8:11], s64 offen offset:0 lds
MM a[16:19],a[128:131],v[80:83],a[16:19]
RD a[200:203],v226 offset:49152
CV v4,v32,v33
MM a[16:19],a[132:135],v[84:87],a[16:19]
buffer_load_dwordx4 v229, s[8:11], s64 offen offset:1024 lds
MM a[24:27],a[132:135],v[92:95],a[24:27]
RD a[204:207],v227 offset:49152
RD a[208:211],v224 offset:53248
MM a[24:27],a[128:131],v[88:91],a[24:27]
EX v35,v35
MM a[28:31],a[136:139],v[88:91],a[28:31]
RD a[212:215],v225 offset:53248
CV v5,v34,v35
MM a[28:31],a[140:143],v[92:95],a[28:31]
EX v36,v36
MM a[20:23],a[140:143],v[84:87],a[20:23]
buffer_load_dwordx4 v230, s[8:11], s64 offen offset:2048 lds
MM a[20:23],a[136:139],v[80:83],a[20:23]
EX v37,v37
MM a[12:15],a[136:139],v[72:75],a[12:15]
RD a[216:219],v226 offset:53248
CV v6,v36,v37
MM a[12:15],a[140:143],v[76:79],a[12:15]
EX v38,v38
MM a[4:7],a[140:143],v[68:71],a[4:7]
EX v39,v39
MM a[4:7],a[136:139],v[64:67],a[4:7]
RD a[220:223],v227 offset:53248
CV v7,v38,v39
MM a[32:35],a[144:147],v[64:67],a[32:35]
EX v40,v40
MM a[32:35],a[148:151],v[68:71],a[32:35]
buffer_load_dwordx4 v231, s[8:11], s64 offen offset:3072 lds
MM a[40:43],a[148:151],v[76:79],a[40:43]
s_add_u32 m0, s65, 81920
EX v41,v41
MM a[40:43],a[144:147],v[72:75],a[40:43]
RD a[224:227],v224 offset:57344
CV v12,v40,v41
MM a[48:51],a[144:147],v[80:83],a[48:51]
EX v42,v42
MM a[48:51],a[148:151],v[84:87],a[48:51]
EX v43,v43
MM a[56:59],a[148:151],v[92:95],a[56:59]
RD a[228:231],v225 offset:57344
CV v13,v42,v43
MM a[56:59],a[144:147],v[88:91],a[56:59]
EX v44,v44
MM a[60:63],a[152:155],v[88:91],a[60:63]
buffer_load_dwordx4 v235, s[12:15], s81 offen offset:0 lds
MM a[60:63],a[156:159],v[92:95],a[60:63]
EX v45,v45
MM a[52:55],a[156:159],v[84:87],a[52:55]
RD a[232:235],v226 offset:57344
CV v14,v44,v45
MM a[52:55],a[152:155],v[80:83],a[52:55]
EX v46,v46
MM a[44:47],a[152:155],v[72:75],a[44:47]
EX v47,v47
MM a[44:47],a[156:159],v[76:79],a[44:47]
RD a[236:239],v227 offset:57344
CV v15,v46,v47
MM a[36:39],a[156:159],v[68:71],a[36:39]
buffer_load_dwordx4 v235, s[12:15], s64 offen offset:1024 lds
MM a[36:39],a[152:155],v[64:67],a[36:39]
EX v48,v48
MM a[64:67],a[160:163],v[64:67],a[64:67]
EX v49,v49
MM a[64:67],a[164:167],v[68:71],a[64:67]
RD a[240:243],v224 offset:61440
CV v20,v48,v49
MM a[72:75],a[164:167],v[76:79],a[72:75]
EX v50,v50
MM a[72:75],a[160:163],v[72:75],a[72:75]
RD a[244:247],v225 offset:61440
RD a[248:251],v226 offset:61440
MM a[80:83],a[160:163],v[80:83],a[80:83]
EX v51,v51
MM a[80:83],a[164:167],v[84:87],a[80:83]
LPG19_%=:
s_cbranch_execz LTR3_%=
RD a[252:255],v227 offset:61440
CV v21,v50,v51
MM a[88:91],a[164:167],v[92:95],a[88:91]
EX v52,v52
MM a[88:91],a[160:163],v[88:91],a[88:91]
buffer_load_dwordx4 v235, s[12:15], s81 offen offset:2048 lds
MM a[92:95],a[168:171],v[88:91],a[92:95]
EX v53,v53
MM a[92:95],a[172:175],v[92:95],a[92:95]
EX v54,v54
MM a[84:87],a[172:175],v[84:87],a[84:87]
buffer_load_dwordx4 v235, s[12:15], s64 offen offset:3072 lds
MM a[84:87],a[168:171],v[80:83],a[84:87]
EX v55,v55
MM a[76:79],a[168:171],v[72:75],a[76:79]
EX v56,v56
MM a[76:79],a[172:175],v[76:79],a[76:79]
EX v57,v57
MM a[68:71],a[172:175],v[68:71],a[68:71]
EX v58,v58
MM a[68:71],a[168:171],v[64:67],a[68:71]
EX v59,v59
MM a[96:99],a[176:179],v[64:67],a[96:99]
EX v60,v60
MM a[96:99],a[180:183],v[68:71],a[96:99]
EX v61,v61
MM a[104:107],a[180:183],v[76:79],a[104:107]
EX v62,v62
MM a[104:107],a[176:179],v[72:75],a[104:107]
EX v63,v63
MM a[112:115],a[176:179],v[80:83],a[112:115]
CV v22,v52,v53
CV v23,v54,v55
MM a[112:115],a[180:183],v[84:87],a[112:115]
)ASM"
R"ASM(
CV v28,v56,v57
CV v29,v58,v59
MM a[120:123],a[180:183],v[92:95],a[120:123]
s_waitcnt vmcnt(8)
v_cmp_ne_u32 vcc, s69, v252
s_cbranch_vccnz LRFH_%=
LRFHb_%=:
MM a[120:123],a[176:179],v[88:91],a[120:123]
v_bfe_u32 v253, v248, 4, 3
v_mul_u32_u24 v253, 3456, v253
MM a[124:127],a[184:187],v[88:91],a[124:127]
v_add_f32 v244, v144, v145
v_add_f32 v245, v148, v149
MM a[124:127],a[188:191],v[92:95],a[124:127]
v_add_f32 v246, v152, v153
v_add_f32 v247, v156, v157
v_lshrrev_b32 v252, 7, v248
MM a[116:119],a[188:191],v[84:87],a[116:119]
v_lshl_add_u32 v253, v252, 4, v253
CV v30,v60,v61
MM a[116:119],a[184:187],v[80:83],a[116:119]
s_min_u32 s90, %[swave], 2
s_mul_i32 s76, s46, 384
CV v31,v62,v63
MM a[108:111],a[184:187],v[72:75],a[108:111]
s_lshl_b32 s91, s90, 7
s_add_u32 s76, s76, s91
s_add_u32 s76, s76, 21626880
MM a[108:111],a[188:191],v[76:79],a[108:111]
s_lshl_b32 s90, s90, 10
s_add_u32 s90, s90, %[slds]
MM a[100:103],a[188:191],v[68:71],a[100:103]
s_add_u32 m0, s90, 131072
s_nop 0
buffer_load_dwordx4 v253, s[20:23], s76 offen offset:0 sc1 lds
MM a[100:103],a[184:187],v[64:67],a[100:103]
v_smfmac_f32_16x16x64_bf16 v[144:147], v[236:239], v[0:7], v234
v_smfmac_f32_16x16x64_bf16 v[148:151], v[236:239], v[8:15], v234
v_smfmac_f32_16x16x64_bf16 v[152:155], v[236:239], v[16:23], v234
v_smfmac_f32_16x16x64_bf16 v[156:159], v[236:239], v[24:31], v234
s_waitcnt lgkmcnt(0)
MM v[64:67],a[192:195],v[160:163],v[128:131]
s_add_u32 s64, s64, 16384
TR a[128:129],v232 offset:32768
s_add_u32 s81, s64, 896
MM v[64:67],a[196:199],v[176:179],v[64:67]
TR a[130:131],v232 offset:36864
v_max3_f32 v249, v144, v145, v148
MM v[64:67],a[200:203],v[192:195],v[64:67]
TR a[132:133],v232 offset:40960
v_max3_f32 v249, v249, v149, v152
MM v[64:67],a[204:207],v[208:211],v[64:67]
TR a[134:135],v232 offset:45056
v_max3_f32 v249, v249, v153, v156
MM v[68:71],a[220:223],v[208:211],v[128:131]
TR a[136:137],v233 offset:32768
v_max_f32 v249, v249, v157
MM v[68:71],a[216:219],v[192:195],v[68:71]
TR a[138:139],v233 offset:36864
v_cmp_lt_f32 vcc, s71, v249
MM v[68:71],a[212:215],v[176:179],v[68:71]
TR a[140:141],v233 offset:40960
TR a[142:143],v233 offset:45056
MM v[68:71],a[208:211],v[160:163],v[68:71]
s_cbranch_vccnz LSITE26_%=
LBACK26_%=:
MM v[76:79],a[208:211],v[164:167],v[132:135]
EX v64,v64
MM v[76:79],a[212:215],v[180:183],v[76:79]
EX v65,v65
MM v[76:79],a[216:219],v[196:199],v[76:79]
TR a[144:145],v232 offset:33280
CV v64,v64,v65
MM v[76:79],a[220:223],v[212:215],v[76:79]
EX v66,v66
MM v[72:75],a[204:207],v[212:215],v[132:135]
EX v67,v67
MM v[72:75],a[200:203],v[196:199],v[72:75]
TR a[146:147],v232 offset:37376
CV v65,v66,v67
MM v[72:75],a[196:199],v[180:183],v[72:75]
EX v68,v68
MM v[72:75],a[192:195],v[164:167],v[72:75]
TR a[148:149],v232 offset:41472
TR a[150:151],v232 offset:45568
MM v[80:83],a[192:195],v[168:171],v[136:139]
EX v69,v69
MM v[80:83],a[196:199],v[184:187],v[80:83]
TR a[152:153],v233 offset:33280
CV v66,v68,v69
MM v[80:83],a[200:203],v[200:203],v[80:83]
EX v70,v70
MM v[80:83],a[204:207],v[216:219],v[80:83]
TR a[154:155],v233 offset:37376
TR a[156:157],v233 offset:41472
MM v[84:87],a[220:223],v[216:219],v[136:139]
EX v71,v71
MM v[84:87],a[216:219],v[200:203],v[84:87]
TR a[158:159],v233 offset:45568
CV v67,v70,v71
MM v[84:87],a[212:215],v[184:187],v[84:87]
EX v72,v72
MM v[84:87],a[208:211],v[168:171],v[84:87]
EX v73,v73
MM v[92:95],a[208:211],v[172:175],v[140:143]
TR a[160:161],v232 offset:33792
CV v72,v72,v73
MM v[92:95],a[212:215],v[188:191],v[92:95]
EX v74,v74
MM v[92:95],a[216:219],v[204:207],v[92:95]
TR a[162:163],v232 offset:37888
TR a[164:165],v232 offset:41984
MM v[92:95],a[220:223],v[220:223],v[92:95]
EX v75,v75
MM v[88:91],a[204:207],v[220:223],v[140:143]
TR a[166:167],v232 offset:46080
CV v73,v74,v75
MM v[88:91],a[200:203],v[204:207],v[88:91]
EX v76,v76
MM v[88:91],a[196:199],v[188:191],v[88:91]
EX v77,v77
MM v[88:91],a[192:195],v[172:175],v[88:91]
TR a[168:169],v233 offset:33792
CV v74,v76,v77
MM v[96:99],a[224:227],v[160:163],v[128:131]
EX v78,v78
MM v[96:99],a[228:231],v[176:179],v[96:99]
TR a[170:171],v233 offset:37888
TR a[172:173],v233 offset:41984
MM v[96:99],a[232:235],v[192:195],v[96:99]
EX v79,v79
MM v[96:99],a[236:239],v[208:211],v[96:99]
TR a[174:175],v233 offset:46080
CV v75,v78,v79
MM v[100:103],a[252:255],v[208:211],v[128:131]
EX v80,v80
MM v[100:103],a[248:251],v[192:195],v[100:103]
EX v81,v81
MM v[100:103],a[244:247],v[176:179],v[100:103]
TR a[176:177],v232 offset:34304
CV v80,v80,v81
MM v[100:103],a[240:243],v[160:163],v[100:103]
EX v82,v82
MM v[108:111],a[240:243],v[164:167],v[132:135]
TR a[178:179],v232 offset:38400
TR a[180:181],v232 offset:42496
MM v[108:111],a[244:247],v[180:183],v[108:111]
EX v83,v83
MM v[108:111],a[248:251],v[196:199],v[108:111]
TR a[182:183],v232 offset:46592
CV v81,v82,v83
MM v[108:111],a[252:255],v[212:215],v[108:111]
EX v84,v84
MM v[104:107],a[236:239],v[212:215],v[132:135]
TR a[184:185],v233 offset:34304
TR a[186:187],v233 offset:38400
MM v[104:107],a[232:235],v[196:199],v[104:107]
EX v85,v85
MM v[104:107],a[228:231],v[180:183],v[104:107]
TR a[188:189],v233 offset:42496
CV v82,v84,v85
MM v[104:107],a[224:227],v[164:167],v[104:107]
EX v86,v86
MM v[112:115],a[224:227],v[168:171],v[136:139]
EX v87,v87
MM v[112:115],a[228:231],v[184:187],v[112:115]
TR a[190:191],v233 offset:46592
CV v83,v86,v87
MM v[112:115],a[232:235],v[200:203],v[112:115]
EX v88,v88
MM v[112:115],a[236:239],v[216:219],v[112:115]
EX v89,v89
MM v[116:119],a[252:255],v[216:219],v[136:139]
EX v90,v90
MM v[116:119],a[248:251],v[200:203],v[116:119]
EX v91,v91
MM v[116:119],a[244:247],v[184:187],v[116:119]
EX v92,v92
MM v[116:119],a[240:243],v[168:171],v[116:119]
EX v93,v93
MM v[124:127],a[240:243],v[172:175],v[140:143]
EX v94,v94
MM v[124:127],a[244:247],v[188:191],v[124:127]
EX v95,v95
MM v[124:127],a[248:251],v[204:207],v[124:127]
EX v96,v96
MM v[124:127],a[252:255],v[220:223],v[124:127]
EX v97,v97
MM v[120:123],a[236:239],v[220:223],v[140:143]
EX v98,v98
MM v[120:123],a[232:235],v[204:207],v[120:123]
EX v99,v99
MM v[120:123],a[228:231],v[188:191],v[120:123]
s_getpc_b64 s[98:99]
LTA4_%=:
s_min_u32 s73, %[swave], 1
s_lshl_b32 s73, s73, 12
s_add_u32 s73, s73, LPG21_%=-LTA4_%=
s_add_u32 s98, s98, s73
s_addc_u32 s99, s99, 0
s_mov_b64 exec, 0
s_setpc_b64 s[98:99]
LTR4_%=:
s_mov_b64 exec, -1
CV v88,v88,v89
MM v[120:123],a[224:227],v[172:175],v[120:123]
s_waitcnt lgkmcnt(0)
s_barrier
MM a[0:3],a[128:131],v[0:3],a[0:3]
s_add_u32 m0, s65, 32768
CV v89,v90,v91
CV v90,v92,v93
MM a[0:3],a[132:135],v[4:7],a[0:3]
RD a[192:195],v224 offset:0
CV v91,v94,v95
MM a[8:11],a[132:135],v[12:15],a[8:11]
buffer_load_dwordx4 v228, s[8:11], s64 offen offset:0 lds
MM a[8:11],a[128:131],v[8:11],a[8:11]
RD a[196:199],v225 offset:0
CV v68,v96,v97
MM a[16:19],a[128:131],v[16:19],a[16:19]
RD a[200:203],v226 offset:0
CV v69,v98,v99
MM a[16:19],a[132:135],v[20:23],a[16:19]
buffer_load_dwordx4 v229, s[8:11], s64 offen offset:1024 lds
MM a[24:27],a[132:135],v[28:31],a[24:27]
RD a[204:207],v227 offset:0
RD a[208:211],v224 offset:4096
MM a[24:27],a[128:131],v[24:27],a[24:27]
RD a[212:215],v225 offset:4096
RD a[216:219],v226 offset:4096
MM a[28:31],a[136:139],v[24:27],a[28:31]
buffer_load_dwordx4 v230, s[8:11], s64 offen offset:2048 lds
MM a[28:31],a[140:143],v[28:31],a[28:31]
EX v100,v100
MM a[20:23],a[140:143],v[20:23],a[20:23]
EX v101,v101
MM a[20:23],a[136:139],v[16:19],a[20:23]
RD a[220:223],v227 offset:4096
CV v70,v100,v101
MM a[12:15],a[136:139],v[8:11],a[12:15]
EX v102,v102
MM a[12:15],a[140:143],v[12:15],a[12:15]
buffer_load_dwordx4 v231, s[8:11], s64 offen offset:3072 lds
MM a[4:7],a[140:143],v[4:7],a[4:7]
s_add_u32 m0, s65, 98304
EX v103,v103
MM a[4:7],a[136:139],v[0:3],a[4:7]
RD a[224:227],v224 offset:8192
CV v71,v102,v103
MM a[32:35],a[144:147],v[0:3],a[32:35]
EX v104,v104
MM a[32:35],a[148:151],v[4:7],a[32:35]
EX v105,v105
MM a[40:43],a[148:151],v[12:15],a[40:43]
RD a[228:231],v225 offset:8192
CV v76,v104,v105
MM a[40:43],a[144:147],v[8:11],a[40:43]
EX v106,v106
MM a[48:51],a[144:147],v[16:19],a[48:51]
buffer_load_dwordx4 v235, s[12:15], s81 offen offset:0 lds
MM a[48:51],a[148:151],v[20:23],a[48:51]
EX v107,v107
MM a[56:59],a[148:151],v[28:31],a[56:59]
RD a[232:235],v226 offset:8192
CV v77,v106,v107
MM a[56:59],a[144:147],v[24:27],a[56:59]
EX v108,v108
MM a[60:63],a[152:155],v[24:27],a[60:63]
EX v109,v109
MM a[60:63],a[156:159],v[28:31],a[60:63]
RD a[236:239],v227 offset:8192
CV v78,v108,v109
MM a[52:55],a[156:159],v[20:23],a[52:55]
EX v110,v110
MM a[52:55],a[152:155],v[16:19],a[52:55]
buffer_load_dwordx4 v235, s[12:15], s64 offen offset:1024 lds
MM a[44:47],a[152:155],v[8:11],a[44:47]
EX v111,v111
MM a[44:47],a[156:159],v[12:15],a[44:47]
RD a[240:243],v224 offset:12288
CV v79,v110,v111
MM a[36:39],a[156:159],v[4:7],a[36:39]
EX v112,v112
MM a[36:39],a[152:155],v[0:3],a[36:39]
EX v113,v113
MM a[64:67],a[160:163],v[0:3],a[64:67]
RD a[244:247],v225 offset:12288
CV v84,v112,v113
MM a[64:67],a[164:167],v[4:7],a[64:67]
EX v114,v114
MM a[72:75],a[164:167],v[12:15],a[72:75]
buffer_load_dwordx4 v235, s[12:15], s81 offen offset:2048 lds
MM a[72:75],a[160:163],v[8:11],a[72:75]
EX v115,v115
MM a[80:83],a[160:163],v[16:19],a[80:83]
RD a[248:251],v226 offset:12288
CV v85,v114,v115
MM a[80:83],a[164:167],v[20:23],a[80:83]
EX v116,v116
MM a[88:91],a[164:167],v[28:31],a[88:91]
EX v117,v117
MM a[88:91],a[160:163],v[24:27],a[88:91]
RD a[252:255],v227 offset:12288
CV v86,v116,v117
MM a[92:95],a[168:171],v[24:27],a[92:95]
EX v118,v118
MM a[92:95],a[172:175],v[28:31],a[92:95]
buffer_load_dwordx4 v235, s[12:15], s64 offen offset:3072 lds
MM a[84:87],a[172:175],v[20:23],a[84:87]
EX v119,v119
MM a[84:87],a[168:171],v[16:19],a[84:87]
CV v87,v118,v119
MM a[76:79],a[168:171],v[8:11],a[76:79]
EX v120,v120
MM a[76:79],a[172:175],v[12:15],a[76:79]
EX v121,v121
MM a[68:71],a[172:175],v[4:7],a[68:71]
EX v122,v122
MM a[68:71],a[168:171],v[0:3],a[68:71]
EX v123,v123
MM a[96:99],a[176:179],v[0:3],a[96:99]
EX v124,v124
MM a[96:99],a[180:183],v[4:7],a[96:99]
EX v125,v125
MM a[104:107],a[180:183],v[12:15],a[104:107]
EX v126,v126
MM a[104:107],a[176:179],v[8:11],a[104:107]
EX v127,v127
MM a[112:115],a[176:179],v[16:19],a[112:115]
CV v92,v120,v121
CV v93,v122,v123
MM a[112:115],a[180:183],v[20:23],a[112:115]
CV v94,v124,v125
CV v95,v126,v127
MM a[120:123],a[180:183],v[28:31],a[120:123]
MM a[120:123],a[176:179],v[24:27],a[120:123]
MM a[124:127],a[184:187],v[24:27],a[124:127]
v_add_f32 v244, v144, v145
v_add_f32 v245, v148, v149
MM a[124:127],a[188:191],v[28:31],a[124:127]
v_add_f32 v246, v152, v153
v_add_f32 v247, v156, v157
MM a[116:119],a[188:191],v[20:23],a[116:119]
MM a[116:119],a[184:187],v[16:19],a[116:119]
MM a[108:111],a[184:187],v[8:11],a[108:111]
MM a[108:111],a[188:191],v[12:15],a[108:111]
MM a[100:103],a[188:191],v[4:7],a[100:103]
MM a[100:103],a[184:187],v[0:3],a[100:103]
v_smfmac_f32_16x16x64_bf16 v[144:147], v[236:239], v[64:71], v234
v_smfmac_f32_16x16x64_bf16 v[148:151], v[236:239], v[72:79], v234
v_smfmac_f32_16x16x64_bf16 v[152:155], v[236:239], v[80:87], v234
v_smfmac_f32_16x16x64_bf16 v[156:159], v[236:239], v[88:95], v234
s_waitcnt lgkmcnt(0)
MM v[0:3],a[192:195],v[160:163],v[128:131]
s_add_u32 s64, s64, 16384
TR a[128:129],v232 offset:49152
s_add_u32 s81, s64, 896
MM v[0:3],a[196:199],v[176:179],v[0:3]
TR a[130:131],v232 offset:53248
v_max3_f32 v249, v144, v145, v148
MM v[0:3],a[200:203],v[192:195],v[0:3]
TR a[132:133],v232 offset:57344
v_max3_f32 v249, v249, v149, v152
MM v[0:3],a[204:207],v[208:211],v[0:3]
TR a[134:135],v232 offset:61440
v_max3_f32 v249, v249, v153, v156
MM v[4:7],a[220:223],v[208:211],v[128:131]
TR a[136:137],v233 offset:49152
v_max_f32 v249, v249, v157
MM v[4:7],a[216:219],v[192:195],v[4:7]
TR a[138:139],v233 offset:53248
v_cmp_lt_f32 vcc, s71, v249
MM v[4:7],a[212:215],v[176:179],v[4:7]
TR a[140:141],v233 offset:57344
TR a[142:143],v233 offset:61440
MM v[4:7],a[208:211],v[160:163],v[4:7]
s_cbranch_vccnz LSITE27_%=
LBACK27_%=:
MM v[12:15],a[208:211],v[164:167],v[132:135]
v_lshrrev_b32 v125, 8, v248
v_lshlrev_b32 v124, 10, v125
)ASM"
R"ASM(
MM v[12:15],a[212:215],v[180:183],v[12:15]
v_bfe_u32 v126, v248, 6, 2
v_lshl_add_u32 v124, v126, 7, v124
MM v[12:15],a[216:219],v[196:199],v[12:15]
v_bfe_u32 v126, v248, 4, 2
v_lshl_add_u32 v124, v126, 3, v124
MM v[12:15],a[220:223],v[212:215],v[12:15]
s_lshl_b32 s91, %[swave], 5
EX v0,v0
MM v[8:11],a[204:207],v[212:215],v[132:135]
s_add_u32 s91, s91, %[slds]
EX v1,v1
MM v[8:11],a[200:203],v[196:199],v[8:11]
s_add_u32 s91, s91, 139264
v_add_u32 v124, s91, v124
v_and_b32 v126, 112, v248
MM v[8:11],a[196:199],v[180:183],v[8:11]
s_add_u32 s91, %[slds], 131072
v_lshl_add_u32 v126, v125, 7, v126
v_add_u32 v126, s91, v126
MM v[8:11],a[192:195],v[164:167],v[8:11]
v_bfe_u32 v127, v248, 4, 3
v_mul_u32_u24 v127, 2560, v127
MM v[16:19],a[192:195],v[168:171],v[136:139]
v_lshl_add_u32 v127, v125, 4, v127
CV v0,v0,v1
EX v2,v2
MM v[16:19],a[196:199],v[184:187],v[16:19]
s_lshl_b32 s90, %[swave], 6
EX v3,v3
MM v[16:19],a[200:203],v[200:203],v[16:19]
s_lshl_b32 s91, s47, 8
EX v4,v4
MM v[16:19],a[204:207],v[216:219],v[16:19]
s_add_u32 s90, s90, s91
TR a[144:145],v232 offset:49664
TR a[146:147],v232 offset:53760
MM v[20:23],a[220:223],v[216:219],v[136:139]
s_mul_i32 s91, s46, 17920
EX v5,v5
MM v[20:23],a[216:219],v[200:203],v[20:23]
s_add_u32 s90, s90, s91
TR a[148:149],v232 offset:57856
CV v1,v2,v3
MM v[20:23],a[212:215],v[184:187],v[20:23]
s_add_u32 s90, s90, 21659648
EX v6,v6
MM v[20:23],a[208:211],v[168:171],v[20:23]
EX v7,v7
MM v[28:31],a[208:211],v[172:175],v[140:143]
TR a[150:151],v232 offset:61952
TR a[152:153],v233 offset:49664
MM v[28:31],a[212:215],v[188:191],v[28:31]
EX v8,v8
MM v[28:31],a[216:219],v[204:207],v[28:31]
TR a[154:155],v233 offset:53760
CV v2,v4,v5
MM v[28:31],a[220:223],v[220:223],v[28:31]
EX v9,v9
MM v[24:27],a[204:207],v[220:223],v[140:143]
TR a[156:157],v233 offset:57856
CV v3,v6,v7
MM v[24:27],a[200:203],v[204:207],v[24:27]
EX v10,v10
MM v[24:27],a[196:199],v[188:191],v[24:27]
TR a[158:159],v233 offset:61952
CV v8,v8,v9
MM v[24:27],a[192:195],v[172:175],v[24:27]
EX v11,v11
MM v[32:35],a[224:227],v[160:163],v[128:131]
TR a[160:161],v232 offset:50176
TR a[162:163],v232 offset:54272
MM v[32:35],a[228:231],v[176:179],v[32:35]
EX v12,v12
MM v[32:35],a[232:235],v[192:195],v[32:35]
EX v13,v13
MM v[32:35],a[236:239],v[208:211],v[32:35]
TR a[164:165],v232 offset:58368
CV v9,v10,v11
MM v[36:39],a[252:255],v[208:211],v[128:131]
EX v14,v14
MM v[36:39],a[248:251],v[192:195],v[36:39]
TR a[166:167],v232 offset:62464
CV v10,v12,v13
MM v[36:39],a[244:247],v[176:179],v[36:39]
EX v15,v15
MM v[36:39],a[240:243],v[160:163],v[36:39]
TR a[168:169],v233 offset:50176
TR a[170:171],v233 offset:54272
MM v[44:47],a[240:243],v[164:167],v[132:135]
EX v16,v16
MM v[44:47],a[244:247],v[180:183],v[44:47]
EX v17,v17
MM v[44:47],a[248:251],v[196:199],v[44:47]
TR a[172:173],v233 offset:58368
CV v11,v14,v15
MM v[44:47],a[252:255],v[212:215],v[44:47]
EX v18,v18
MM v[40:43],a[236:239],v[212:215],v[132:135]
TR a[174:175],v233 offset:62464
CV v16,v16,v17
MM v[40:43],a[232:235],v[196:199],v[40:43]
EX v19,v19
MM v[40:43],a[228:231],v[180:183],v[40:43]
TR a[176:177],v232 offset:50688
TR a[178:179],v232 offset:54784
MM v[40:43],a[224:227],v[164:167],v[40:43]
EX v20,v20
MM v[48:51],a[224:227],v[168:171],v[136:139]
EX v21,v21
MM v[48:51],a[228:231],v[184:187],v[48:51]
TR a[180:181],v232 offset:58880
CV v17,v18,v19
MM v[48:51],a[232:235],v[200:203],v[48:51]
EX v22,v22
MM v[48:51],a[236:239],v[216:219],v[48:51]
TR a[182:183],v232 offset:62976
CV v18,v20,v21
MM v[52:55],a[252:255],v[216:219],v[136:139]
EX v23,v23
MM v[52:55],a[248:251],v[200:203],v[52:55]
TR a[184:185],v233 offset:50688
TR a[186:187],v233 offset:54784
MM v[52:55],a[244:247],v[184:187],v[52:55]
EX v24,v24
MM v[52:55],a[240:243],v[168:171],v[52:55]
TR a[188:189],v233 offset:58880
CV v19,v22,v23
MM v[60:63],a[240:243],v[172:175],v[140:143]
EX v25,v25
MM v[60:63],a[244:247],v[188:191],v[60:63]
TR a[190:191],v233 offset:62976
CV v24,v24,v25
MM v[60:63],a[248:251],v[204:207],v[60:63]
EX v26,v26
MM v[60:63],a[252:255],v[220:223],v[60:63]
EX v27,v27
MM v[56:59],a[236:239],v[220:223],v[140:143]
EX v28,v28
MM v[56:59],a[232:235],v[204:207],v[56:59]
EX v29,v29
MM v[56:59],a[228:231],v[188:191],v[56:59]
EX v30,v30
MM v[56:59],a[224:227],v[172:175],v[56:59]
s_waitcnt vmcnt(8) lgkmcnt(0)
s_barrier
MM a[0:3],a[128:131],v[64:67],a[0:3]
s_add_u32 m0, s65, 49152
CV v25,v26,v27
CV v26,v28,v29
MM a[0:3],a[132:135],v[68:71],a[0:3]
RD a[192:195],v224 offset:16384
RD a[196:199],v225 offset:16384
MM a[8:11],a[132:135],v[76:79],a[8:11]
EX v31,v31
MM a[8:11],a[128:131],v[72:75],a[8:11]
RD a[200:203],v226 offset:16384
CV v27,v30,v31
MM a[16:19],a[128:131],v[80:83],a[16:19]
TR v[96:97],v124 offset:0
TR v[98:99],v124 offset:512
MM a[16:19],a[132:135],v[84:87],a[16:19]
RD v[108:111],v126 offset:0
TR v[100:101],v124 offset:4096
MM a[24:27],a[132:135],v[92:95],a[24:27]
TR v[102:103],v124 offset:4608
RD v[112:115],v126 offset:512
MM a[24:27],a[128:131],v[88:91],a[24:27]
TR v[104:105],v124 offset:8192
TR v[106:107],v124 offset:8704
MM a[28:31],a[136:139],v[88:91],a[28:31]
RD v[116:119],v126 offset:1024
RD a[204:207],v227 offset:16384
EX v32,v32
MM a[28:31],a[140:143],v[92:95],a[28:31]
EX v33,v33
MM a[20:23],a[140:143],v[84:87],a[20:23]
EX v34,v34
MM a[20:23],a[136:139],v[80:83],a[20:23]
buffer_load_dwordx4 v228, s[8:11], s64 offen offset:0 lds
LPG20_%=:
s_cbranch_execz LTR3_%=
MM a[12:15],a[136:139],v[72:75],a[12:15]
EX v35,v35
MM a[12:15],a[140:143],v[76:79],a[12:15]
EX v36,v36
MM a[4:7],a[140:143],v[68:71],a[4:7]
CV v4,v32,v33
CV v5,v34,v35
MM a[4:7],a[136:139],v[64:67],a[4:7]
EX v37,v37
MM a[32:35],a[144:147],v[64:67],a[32:35]
EX v38,v38
MM a[32:35],a[148:151],v[68:71],a[32:35]
buffer_load_dwordx4 v229, s[8:11], s64 offen offset:1024 lds
MM a[40:43],a[148:151],v[76:79],a[40:43]
EX v39,v39
MM a[40:43],a[144:147],v[72:75],a[40:43]
RD a[208:211],v224 offset:20480
CV v6,v36,v37
MM a[48:51],a[144:147],v[80:83],a[48:51]
s_waitcnt lgkmcnt(2)
EX v40,v40
MM a[48:51],a[148:151],v[84:87],a[48:51]
RD a[212:215],v225 offset:20480
CV v7,v38,v39
MM v[120:123],v[96:99],v[108:111],0
MM v[120:123],v[100:103],v[112:115],v[120:123]
MM v[120:123],v[104:107],v[116:119],v[120:123]
MM a[56:59],a[148:151],v[92:95],a[56:59]
TR v[96:97],v124 offset:12288
TR v[98:99],v124 offset:12800
MM a[56:59],a[144:147],v[88:91],a[56:59]
RD v[108:111],v126 offset:1536
TR v[100:101],v124 offset:16384
MM a[60:63],a[152:155],v[88:91],a[60:63]
TR v[102:103],v124 offset:16896
RD v[112:115],v126 offset:2048
EX v41,v41
MM a[60:63],a[156:159],v[92:95],a[60:63]
TR v[104:105],v124 offset:20480
TR v[106:107],v124 offset:20992
MM a[52:55],a[156:159],v[84:87],a[52:55]
RD v[116:119],v126 offset:2560
CV v12,v40,v41
EX v42,v42
MM a[52:55],a[152:155],v[80:83],a[52:55]
EX v43,v43
MM a[44:47],a[152:155],v[72:75],a[44:47]
buffer_load_dwordx4 v230, s[8:11], s64 offen offset:2048 lds
MM a[44:47],a[156:159],v[76:79],a[44:47]
EX v44,v44
MM a[36:39],a[156:159],v[68:71],a[36:39]
EX v45,v45
MM a[36:39],a[152:155],v[64:67],a[36:39]
RD a[216:219],v226 offset:20480
CV v13,v42,v43
MM a[64:67],a[160:163],v[64:67],a[64:67]
EX v46,v46
MM a[64:67],a[164:167],v[68:71],a[64:67]
EX v47,v47
MM a[72:75],a[164:167],v[76:79],a[72:75]
buffer_load_dwordx4 v231, s[8:11], s64 offen offset:3072 lds
MM a[72:75],a[160:163],v[72:75],a[72:75]
s_add_u32 m0, s65, 114688
RD a[220:223],v227 offset:20480
CV v14,v44,v45
MM a[80:83],a[160:163],v[80:83],a[80:83]
EX v48,v48
MM a[80:83],a[164:167],v[84:87],a[80:83]
EX v49,v49
MM a[88:91],a[164:167],v[92:95],a[88:91]
s_waitcnt lgkmcnt(2)
RD a[224:227],v224 offset:24576
CV v15,v46,v47
MM a[88:91],a[160:163],v[88:91],a[88:91]
EX v50,v50
MM v[120:123],v[96:99],v[108:111],v[120:123]
MM v[120:123],v[100:103],v[112:115],v[120:123]
MM v[120:123],v[104:107],v[116:119],v[120:123]
MM a[92:95],a[168:171],v[88:91],a[92:95]
EX v51,v51
MM a[92:95],a[172:175],v[92:95],a[92:95]
buffer_load_dwordx4 v235, s[12:15], s81 offen offset:0 lds
MM a[84:87],a[172:175],v[84:87],a[84:87]
RD a[228:231],v225 offset:24576
CV v20,v48,v49
MM a[84:87],a[168:171],v[80:83],a[84:87]
EX v52,v52
MM a[76:79],a[168:171],v[72:75],a[76:79]
EX v53,v53
MM a[76:79],a[172:175],v[76:79],a[76:79]
RD a[232:235],v226 offset:24576
CV v21,v50,v51
MM a[68:71],a[172:175],v[68:71],a[68:71]
s_mov_b32 exec_lo, 0x00ff00ff
s_mov_b32 exec_hi, 0x00ff00ff
buffer_store_dwordx4 v[120:123], v127, s[20:23], s90 offen offset:0 sc1
s_mov_b64 exec, -1
s_nop 0
MM a[68:71],a[168:171],v[64:67],a[68:71]
EX v54,v54
MM a[96:99],a[176:179],v[64:67],a[96:99]
EX v55,v55
MM a[96:99],a[180:183],v[68:71],a[96:99]
RD a[236:239],v227 offset:24576
CV v22,v52,v53
MM a[104:107],a[180:183],v[76:79],a[104:107]
EX v56,v56
MM a[104:107],a[176:179],v[72:75],a[104:107]
buffer_load_dwordx4 v235, s[12:15], s64 offen offset:1024 lds
MM a[112:115],a[176:179],v[80:83],a[112:115]
EX v57,v57
MM a[112:115],a[180:183],v[84:87],a[112:115]
RD a[240:243],v224 offset:28672
CV v23,v54,v55
MM a[120:123],a[180:183],v[92:95],a[120:123]
EX v58,v58
MM a[120:123],a[176:179],v[88:91],a[120:123]
EX v59,v59
MM a[124:127],a[184:187],v[88:91],a[124:127]
v_add_f32 v244, v144, v145
v_add_f32 v245, v148, v149
MM a[124:127],a[188:191],v[92:95],a[124:127]
v_add_f32 v246, v152, v153
v_add_f32 v247, v156, v157
MM a[116:119],a[188:191],v[84:87],a[116:119]
EX v60,v60
MM a[116:119],a[184:187],v[80:83],a[116:119]
RD a[244:247],v225 offset:28672
CV v28,v56,v57
MM a[108:111],a[184:187],v[72:75],a[108:111]
EX v61,v61
MM a[108:111],a[188:191],v[76:79],a[108:111]
buffer_load_dwordx4 v235, s[12:15], s81 offen offset:2048 lds
MM a[100:103],a[188:191],v[68:71],a[100:103]
EX v62,v62
MM a[100:103],a[184:187],v[64:67],a[100:103]
RD a[248:251],v226 offset:28672
CV v29,v58,v59
v_smfmac_f32_16x16x64_bf16 v[144:147], v[236:239], v[0:7], v234
RD a[252:255],v227 offset:28672
EX v63,v63
v_smfmac_f32_16x16x64_bf16 v[148:151], v[236:239], v[8:15], v234
CV v30,v60,v61
v_smfmac_f32_16x16x64_bf16 v[152:155], v[236:239], v[16:23], v234
CV v31,v62,v63
s_nop 1
v_smfmac_f32_16x16x64_bf16 v[156:159], v[236:239], v[24:31], v234
s_waitcnt lgkmcnt(0)
buffer_load_dwordx4 v235, s[12:15], s64 offen offset:3072 lds
MM v[64:67],a[192:195],v[160:163],v[128:131]
s_add_u32 s64, s64, 16384
TR a[128:129],v232 offset:0
s_add_u32 s81, s64, 896
MM v[64:67],a[196:199],v[176:179],v[64:67]
TR a[130:131],v232 offset:4096
v_max3_f32 v249, v144, v145, v148
MM v[64:67],a[200:203],v[192:195],v[64:67]
TR a[132:133],v232 offset:8192
v_max3_f32 v249, v249, v149, v152
MM v[64:67],a[204:207],v[208:211],v[64:67]
TR a[134:135],v232 offset:12288
v_max3_f32 v249, v249, v153, v156
MM v[68:71],a[220:223],v[208:211],v[128:131]
TR a[136:137],v233 offset:0
v_max_f32 v249, v249, v157
MM v[68:71],a[216:219],v[192:195],v[68:71]
TR a[138:139],v233 offset:4096
v_cmp_lt_f32 vcc, s71, v249
MM v[68:71],a[212:215],v[176:179],v[68:71]
TR a[140:141],v233 offset:8192
TR a[142:143],v233 offset:12288
MM v[68:71],a[208:211],v[160:163],v[68:71]
s_cbranch_vccnz LSITE28_%=
LBACK28_%=:
MM v[76:79],a[208:211],v[164:167],v[132:135]
EX v64,v64
MM v[76:79],a[212:215],v[180:183],v[76:79]
EX v65,v65
MM v[76:79],a[216:219],v[196:199],v[76:79]
TR a[144:145],v232 offset:512
CV v64,v64,v65
MM v[76:79],a[220:223],v[212:215],v[76:79]
EX v66,v66
MM v[72:75],a[204:207],v[212:215],v[132:135]
EX v67,v67
MM v[72:75],a[200:203],v[196:199],v[72:75]
TR a[146:147],v232 offset:4608
CV v65,v66,v67
MM v[72:75],a[196:199],v[180:183],v[72:75]
EX v68,v68
MM v[72:75],a[192:195],v[164:167],v[72:75]
TR a[148:149],v232 offset:8704
TR a[150:151],v232 offset:12800
MM v[80:83],a[192:195],v[168:171],v[136:139]
EX v69,v69
MM v[80:83],a[196:199],v[184:187],v[80:83]
TR a[152:153],v233 offset:512
CV v66,v68,v69
MM v[80:83],a[200:203],v[200:203],v[80:83]
EX v70,v70
MM v[80:83],a[204:207],v[216:219],v[80:83]
TR a[154:155],v233 offset:4608
TR a[156:157],v233 offset:8704
MM v[84:87],a[220:223],v[216:219],v[136:139]
v_lshlrev_b32 v32, 3, v248
v_min_u32 v32, 4992, v32
MM v[84:87],a[216:219],v[200:203],v[84:87]
s_sub_u32 s91, 5242880, s26
s_sub_u32 s60, s24, s91
s_subb_u32 s61, s25, 0
)ASM"
R"ASM(
MM v[84:87],a[212:215],v[184:187],v[84:87]
s_mov_b32 s62, 5242880
EX v71,v71
MM v[84:87],a[208:211],v[168:171],v[84:87]
s_mov_b32 s63, 131072
TR a[158:159],v233 offset:12800
CV v67,v70,v71
MM v[92:95],a[208:211],v[172:175],v[140:143]
s_lshl_b32 s90, s16, 2
EX v72,v72
MM v[92:95],a[212:215],v[188:191],v[92:95]
s_add_u32 s90, s90, %[swave]
EX v73,v73
MM v[92:95],a[216:219],v[204:207],v[92:95]
s_mul_i32 s90, s90, 10240
TR a[160:161],v232 offset:1024
CV v72,v72,v73
MM v[92:95],a[220:223],v[220:223],v[92:95]
s_add_u32 s90, s90, 5120
EX v74,v74
MM v[88:91],a[204:207],v[220:223],v[140:143]
s_add_u32 s91, %[slds], 135168
s_mov_b32 m0, s91
s_nop 0
buffer_load_dword v32, s[60:63], s90 offen offset:0 lds
MM v[88:91],a[200:203],v[204:207],v[88:91]
EX v75,v75
MM v[88:91],a[196:199],v[188:191],v[88:91]
TR a[162:163],v232 offset:5120
CV v73,v74,v75
MM v[88:91],a[192:195],v[172:175],v[88:91]
EX v76,v76
MM v[96:99],a[224:227],v[160:163],v[128:131]
TR a[164:165],v232 offset:9216
TR a[166:167],v232 offset:13312
MM v[96:99],a[228:231],v[176:179],v[96:99]
EX v77,v77
MM v[96:99],a[232:235],v[192:195],v[96:99]
TR a[168:169],v233 offset:1024
CV v74,v76,v77
MM v[96:99],a[236:239],v[208:211],v[96:99]
EX v78,v78
MM v[100:103],a[252:255],v[208:211],v[128:131]
EX v79,v79
MM v[100:103],a[248:251],v[192:195],v[100:103]
TR a[170:171],v233 offset:5120
CV v75,v78,v79
MM v[100:103],a[244:247],v[176:179],v[100:103]
EX v80,v80
MM v[100:103],a[240:243],v[160:163],v[100:103]
TR a[172:173],v233 offset:9216
TR a[174:175],v233 offset:13312
MM v[108:111],a[240:243],v[164:167],v[132:135]
s_add_u32 s67, s95, 5120
EX v81,v81
MM v[108:111],a[244:247],v[180:183],v[108:111]
s_mov_b32 s70, 1
TR a[176:177],v232 offset:1536
CV v80,v80,v81
MM v[108:111],a[248:251],v[196:199],v[108:111]
EX v82,v82
MM v[108:111],a[252:255],v[212:215],v[108:111]
EX v83,v83
MM v[104:107],a[236:239],v[212:215],v[132:135]
TR a[178:179],v232 offset:5632
CV v81,v82,v83
MM v[104:107],a[232:235],v[196:199],v[104:107]
EX v84,v84
MM v[104:107],a[228:231],v[180:183],v[104:107]
TR a[180:181],v232 offset:9728
TR a[182:183],v232 offset:13824
MM v[104:107],a[224:227],v[164:167],v[104:107]
EX v85,v85
MM v[112:115],a[224:227],v[168:171],v[136:139]
TR a[184:185],v233 offset:1536
CV v82,v84,v85
MM v[112:115],a[228:231],v[184:187],v[112:115]
EX v86,v86
MM v[112:115],a[232:235],v[200:203],v[112:115]
EX v87,v87
MM v[112:115],a[236:239],v[216:219],v[112:115]
TR a[186:187],v233 offset:5632
CV v83,v86,v87
MM v[116:119],a[252:255],v[216:219],v[136:139]
EX v88,v88
MM v[116:119],a[248:251],v[200:203],v[116:119]
TR a[188:189],v233 offset:9728
TR a[190:191],v233 offset:13824
MM v[116:119],a[244:247],v[184:187],v[116:119]
EX v89,v89
MM v[116:119],a[240:243],v[168:171],v[116:119]
EX v90,v90
MM v[124:127],a[240:243],v[172:175],v[140:143]
EX v91,v91
MM v[124:127],a[244:247],v[188:191],v[124:127]
EX v92,v92
MM v[124:127],a[248:251],v[204:207],v[124:127]
EX v93,v93
MM v[124:127],a[252:255],v[220:223],v[124:127]
EX v94,v94
MM v[120:123],a[236:239],v[220:223],v[140:143]
EX v95,v95
MM v[120:123],a[232:235],v[204:207],v[120:123]
EX v96,v96
MM v[120:123],a[228:231],v[188:191],v[120:123]
EX v97,v97
MM v[120:123],a[224:227],v[172:175],v[120:123]
s_waitcnt vmcnt(14) lgkmcnt(0)
s_barrier
MM a[0:3],a[128:131],v[0:3],a[0:3]
s_add_u32 m0, s65, 0
CV v88,v88,v89
CV v89,v90,v91
MM a[0:3],a[132:135],v[4:7],a[0:3]
RD a[192:195],v224 offset:32768
CV v90,v92,v93
MM a[8:11],a[132:135],v[12:15],a[8:11]
RD a[196:199],v225 offset:32768
CV v91,v94,v95
MM a[8:11],a[128:131],v[8:11],a[8:11]
buffer_load_dwordx4 v228, s[8:11], s64 offen offset:0 lds
MM a[16:19],a[128:131],v[16:19],a[16:19]
RD a[200:203],v226 offset:32768
CV v68,v96,v97
MM a[16:19],a[132:135],v[20:23],a[16:19]
buffer_load_dwordx4 v229, s[8:11], s64 offen offset:1024 lds
MM a[24:27],a[132:135],v[28:31],a[24:27]
EX v98,v98
MM a[24:27],a[128:131],v[24:27],a[24:27]
RD a[204:207],v227 offset:32768
RD a[208:211],v224 offset:36864
MM a[28:31],a[136:139],v[24:27],a[28:31]
EX v99,v99
MM a[28:31],a[140:143],v[28:31],a[28:31]
RD a[212:215],v225 offset:36864
CV v69,v98,v99
MM a[20:23],a[140:143],v[20:23],a[20:23]
EX v100,v100
MM a[20:23],a[136:139],v[16:19],a[20:23]
EX v101,v101
MM a[12:15],a[136:139],v[8:11],a[12:15]
RD a[216:219],v226 offset:36864
CV v70,v100,v101
MM a[12:15],a[140:143],v[12:15],a[12:15]
buffer_load_dwordx4 v230, s[8:11], s64 offen offset:2048 lds
MM a[4:7],a[140:143],v[4:7],a[4:7]
EX v102,v102
MM a[4:7],a[136:139],v[0:3],a[4:7]
EX v103,v103
MM a[32:35],a[144:147],v[0:3],a[32:35]
RD a[220:223],v227 offset:36864
CV v71,v102,v103
MM a[32:35],a[148:151],v[4:7],a[32:35]
EX v104,v104
MM a[40:43],a[148:151],v[12:15],a[40:43]
EX v105,v105
MM a[40:43],a[144:147],v[8:11],a[40:43]
RD a[224:227],v224 offset:40960
CV v76,v104,v105
MM a[48:51],a[144:147],v[16:19],a[48:51]
buffer_load_dwordx4 v231, s[8:11], s64 offen offset:3072 lds
MM a[48:51],a[148:151],v[20:23],a[48:51]
s_add_u32 m0, s65, 65536
EX v106,v106
MM a[56:59],a[148:151],v[28:31],a[56:59]
EX v107,v107
MM a[56:59],a[144:147],v[24:27],a[56:59]
RD a[228:231],v225 offset:40960
CV v77,v106,v107
MM a[60:63],a[152:155],v[24:27],a[60:63]
EX v108,v108
MM a[60:63],a[156:159],v[28:31],a[60:63]
buffer_load_dwordx4 v235, s[12:15], s81 offen offset:0 lds
MM a[52:55],a[156:159],v[20:23],a[52:55]
EX v109,v109
MM a[52:55],a[152:155],v[16:19],a[52:55]
RD a[232:235],v226 offset:40960
CV v78,v108,v109
MM a[44:47],a[152:155],v[8:11],a[44:47]
EX v110,v110
MM a[44:47],a[156:159],v[12:15],a[44:47]
EX v111,v111
MM a[36:39],a[156:159],v[4:7],a[36:39]
RD a[236:239],v227 offset:40960
CV v79,v110,v111
MM a[36:39],a[152:155],v[0:3],a[36:39]
EX v112,v112
MM a[64:67],a[160:163],v[0:3],a[64:67]
buffer_load_dwordx4 v235, s[12:15], s64 offen offset:1024 lds
MM a[64:67],a[164:167],v[4:7],a[64:67]
EX v113,v113
MM a[72:75],a[164:167],v[12:15],a[72:75]
RD a[240:243],v224 offset:45056
CV v84,v112,v113
MM a[72:75],a[160:163],v[8:11],a[72:75]
EX v114,v114
MM a[80:83],a[160:163],v[16:19],a[80:83]
EX v115,v115
MM a[80:83],a[164:167],v[20:23],a[80:83]
RD a[244:247],v225 offset:45056
CV v85,v114,v115
MM a[88:91],a[164:167],v[28:31],a[88:91]
EX v116,v116
MM a[88:91],a[160:163],v[24:27],a[88:91]
buffer_load_dwordx4 v235, s[12:15], s81 offen offset:2048 lds
MM a[92:95],a[168:171],v[24:27],a[92:95]
EX v117,v117
MM a[92:95],a[172:175],v[28:31],a[92:95]
RD a[248:251],v226 offset:45056
CV v86,v116,v117
MM a[84:87],a[172:175],v[20:23],a[84:87]
EX v118,v118
MM a[84:87],a[168:171],v[16:19],a[84:87]
EX v119,v119
MM a[76:79],a[168:171],v[8:11],a[76:79]
RD a[252:255],v227 offset:45056
CV v87,v118,v119
MM a[76:79],a[172:175],v[12:15],a[76:79]
EX v120,v120
MM a[68:71],a[172:175],v[4:7],a[68:71]
buffer_load_dwordx4 v235, s[12:15], s64 offen offset:3072 lds
MM a[68:71],a[168:171],v[0:3],a[68:71]
EX v121,v121
MM a[96:99],a[176:179],v[0:3],a[96:99]
EX v122,v122
MM a[96:99],a[180:183],v[4:7],a[96:99]
EX v123,v123
MM a[104:107],a[180:183],v[12:15],a[104:107]
EX v124,v124
MM a[104:107],a[176:179],v[8:11],a[104:107]
EX v125,v125
MM a[112:115],a[176:179],v[16:19],a[112:115]
EX v126,v126
MM a[112:115],a[180:183],v[20:23],a[112:115]
EX v127,v127
MM a[120:123],a[180:183],v[28:31],a[120:123]
s_lshl_b32 s91, s47, 2
CV v92,v120,v121
CV v93,v122,v123
MM a[120:123],a[176:179],v[24:27],a[120:123]
s_add_u32 s91, s91, %[swave]
CV v94,v124,v125
CV v95,v126,v127
MM a[124:127],a[184:187],v[24:27],a[124:127]
s_lshl_b32 s91, s91, 2
v_add_f32 v244, v144, v145
v_add_f32 v245, v148, v149
MM a[124:127],a[188:191],v[28:31],a[124:127]
s_add_u32 s91, s91, 73728
v_add_f32 v246, v152, v153
v_add_f32 v247, v156, v157
MM a[116:119],a[188:191],v[20:23],a[116:119]
s_waitcnt vmcnt(12)
v_mov_b32 v250, s69
s_mov_b64 exec, 1
buffer_store_dword v250, off, s[20:23], s91 offset:0 sc1
s_mov_b64 exec, -1
MM a[116:119],a[184:187],v[16:19],a[116:119]
MM a[108:111],a[184:187],v[8:11],a[108:111]
MM a[108:111],a[188:191],v[12:15],a[108:111]
MM a[100:103],a[188:191],v[4:7],a[100:103]
MM a[100:103],a[184:187],v[0:3],a[100:103]
v_smfmac_f32_16x16x64_bf16 v[144:147], v[236:239], v[64:71], v234
v_smfmac_f32_16x16x64_bf16 v[148:151], v[236:239], v[72:79], v234
v_smfmac_f32_16x16x64_bf16 v[152:155], v[236:239], v[80:87], v234
v_smfmac_f32_16x16x64_bf16 v[156:159], v[236:239], v[88:95], v234
s_waitcnt lgkmcnt(0)
s_cmp_ge_u32 s16, 20
s_cbranch_scc1 LLAP_%=
MM v[0:3],a[192:195],v[160:163],v[128:131]
s_add_u32 s64, s64, 16384
TR a[128:129],v232 offset:16384
s_add_u32 s81, s64, 896
MM v[0:3],a[196:199],v[176:179],v[0:3]
TR a[130:131],v232 offset:20480
v_max3_f32 v249, v144, v145, v148
MM v[0:3],a[200:203],v[192:195],v[0:3]
TR a[132:133],v232 offset:24576
v_max3_f32 v249, v249, v149, v152
MM v[0:3],a[204:207],v[208:211],v[0:3]
TR a[134:135],v232 offset:28672
v_max3_f32 v249, v249, v153, v156
MM v[4:7],a[220:223],v[208:211],v[128:131]
TR a[136:137],v233 offset:16384
v_max_f32 v249, v249, v157
MM v[4:7],a[216:219],v[192:195],v[4:7]
TR a[138:139],v233 offset:20480
v_cmp_lt_f32 vcc, s71, v249
MM v[4:7],a[212:215],v[176:179],v[4:7]
TR a[140:141],v233 offset:24576
TR a[142:143],v233 offset:28672
MM v[4:7],a[208:211],v[160:163],v[4:7]
s_cbranch_vccnz LSITE29_%=
LBACK29_%=:
MM v[12:15],a[208:211],v[164:167],v[132:135]
EX v0,v0
MM v[12:15],a[212:215],v[180:183],v[12:15]
EX v1,v1
MM v[12:15],a[216:219],v[196:199],v[12:15]
TR a[144:145],v232 offset:16896
CV v0,v0,v1
MM v[12:15],a[220:223],v[212:215],v[12:15]
EX v2,v2
MM v[8:11],a[204:207],v[212:215],v[132:135]
EX v3,v3
MM v[8:11],a[200:203],v[196:199],v[8:11]
TR a[146:147],v232 offset:20992
CV v1,v2,v3
MM v[8:11],a[196:199],v[180:183],v[8:11]
EX v4,v4
MM v[8:11],a[192:195],v[164:167],v[8:11]
TR a[148:149],v232 offset:25088
TR a[150:151],v232 offset:29184
MM v[16:19],a[192:195],v[168:171],v[136:139]
EX v5,v5
MM v[16:19],a[196:199],v[184:187],v[16:19]
TR a[152:153],v233 offset:16896
CV v2,v4,v5
MM v[16:19],a[200:203],v[200:203],v[16:19]
EX v6,v6
MM v[16:19],a[204:207],v[216:219],v[16:19]
TR a[154:155],v233 offset:20992
TR a[156:157],v233 offset:25088
MM v[20:23],a[220:223],v[216:219],v[136:139]
s_add_u32 s87, s95, 5120
buffer_store_dword v240, v248, s[20:23], s87 offen offset:12 sc1
MM v[20:23],a[216:219],v[200:203],v[20:23]
EX v7,v7
MM v[20:23],a[212:215],v[184:187],v[20:23]
TR a[158:159],v233 offset:29184
CV v3,v6,v7
MM v[20:23],a[208:211],v[168:171],v[20:23]
EX v8,v8
MM v[28:31],a[208:211],v[172:175],v[140:143]
EX v9,v9
MM v[28:31],a[212:215],v[188:191],v[28:31]
TR a[160:161],v232 offset:17408
CV v8,v8,v9
MM v[28:31],a[216:219],v[204:207],v[28:31]
EX v10,v10
MM v[28:31],a[220:223],v[220:223],v[28:31]
EX v11,v11
MM v[24:27],a[204:207],v[220:223],v[140:143]
TR a[162:163],v232 offset:21504
CV v9,v10,v11
MM v[24:27],a[200:203],v[204:207],v[24:27]
TR a[164:165],v232 offset:25600
TR a[166:167],v232 offset:29696
MM v[24:27],a[196:199],v[188:191],v[24:27]
EX v12,v12
MM v[24:27],a[192:195],v[172:175],v[24:27]
EX v13,v13
MM v[32:35],a[224:227],v[160:163],v[128:131]
TR a[168:169],v233 offset:17408
CV v10,v12,v13
MM v[32:35],a[228:231],v[176:179],v[32:35]
EX v14,v14
MM v[32:35],a[232:235],v[192:195],v[32:35]
TR a[170:171],v233 offset:21504
TR a[172:173],v233 offset:25600
MM v[32:35],a[236:239],v[208:211],v[32:35]
EX v15,v15
MM v[36:39],a[252:255],v[208:211],v[128:131]
TR a[174:175],v233 offset:29696
CV v11,v14,v15
MM v[36:39],a[248:251],v[192:195],v[36:39]
EX v16,v16
MM v[36:39],a[244:247],v[176:179],v[36:39]
EX v17,v17
MM v[36:39],a[240:243],v[160:163],v[36:39]
TR a[176:177],v232 offset:17920
CV v16,v16,v17
MM v[44:47],a[240:243],v[164:167],v[132:135]
EX v18,v18
MM v[44:47],a[244:247],v[180:183],v[44:47]
TR a[178:179],v232 offset:22016
TR a[180:181],v232 offset:26112
MM v[44:47],a[248:251],v[196:199],v[44:47]
EX v19,v19
MM v[44:47],a[252:255],v[212:215],v[44:47]
s_nop 0
LPG21_%=:
s_cbranch_execz LTR4_%=
TR a[182:183],v232 offset:30208
CV v17,v18,v19
MM v[40:43],a[236:239],v[212:215],v[132:135]
EX v20,v20
MM v[40:43],a[232:235],v[196:199],v[40:43]
EX v21,v21
MM v[40:43],a[228:231],v[180:183],v[40:43]
TR a[184:185],v233 offset:17920
CV v18,v20,v21
MM v[40:43],a[224:227],v[164:167],v[40:43]
)ASM"
R"ASM(
EX v22,v22
MM v[48:51],a[224:227],v[168:171],v[136:139]
TR a[186:187],v233 offset:22016
TR a[188:189],v233 offset:26112
MM v[48:51],a[228:231],v[184:187],v[48:51]
EX v23,v23
MM v[48:51],a[232:235],v[200:203],v[48:51]
TR a[190:191],v233 offset:30208
CV v19,v22,v23
MM v[48:51],a[236:239],v[216:219],v[48:51]
EX v24,v24
MM v[52:55],a[252:255],v[216:219],v[136:139]
EX v25,v25
MM v[52:55],a[248:251],v[200:203],v[52:55]
EX v26,v26
MM v[52:55],a[244:247],v[184:187],v[52:55]
EX v27,v27
MM v[52:55],a[240:243],v[168:171],v[52:55]
EX v28,v28
MM v[60:63],a[240:243],v[172:175],v[140:143]
EX v29,v29
MM v[60:63],a[244:247],v[188:191],v[60:63]
EX v30,v30
MM v[60:63],a[248:251],v[204:207],v[60:63]
EX v31,v31
MM v[60:63],a[252:255],v[220:223],v[60:63]
EX v32,v32
MM v[56:59],a[236:239],v[220:223],v[140:143]
EX v33,v33
MM v[56:59],a[232:235],v[204:207],v[56:59]
EX v34,v34
MM v[56:59],a[228:231],v[188:191],v[56:59]
EX v35,v35
MM v[56:59],a[224:227],v[172:175],v[56:59]
s_waitcnt lgkmcnt(0)
s_barrier
MM a[0:3],a[128:131],v[64:67],a[0:3]
s_add_u32 m0, s65, 16384
CV v24,v24,v25
CV v25,v26,v27
MM a[0:3],a[132:135],v[68:71],a[0:3]
RD a[192:195],v224 offset:49152
CV v26,v28,v29
MM a[8:11],a[132:135],v[76:79],a[8:11]
RD a[196:199],v225 offset:49152
CV v27,v30,v31
MM a[8:11],a[128:131],v[72:75],a[8:11]
buffer_load_dwordx4 v228, s[8:11], s64 offen offset:0 lds
MM a[16:19],a[128:131],v[80:83],a[16:19]
RD a[200:203],v226 offset:49152
CV v4,v32,v33
MM a[16:19],a[132:135],v[84:87],a[16:19]
RD a[204:207],v227 offset:49152
CV v5,v34,v35
MM a[24:27],a[132:135],v[92:95],a[24:27]
buffer_load_dwordx4 v229, s[8:11], s64 offen offset:1024 lds
MM a[24:27],a[128:131],v[88:91],a[24:27]
RD a[208:211],v224 offset:53248
RD a[212:215],v225 offset:53248
MM a[28:31],a[136:139],v[88:91],a[28:31]
buffer_load_dwordx4 v230, s[8:11], s64 offen offset:2048 lds
MM a[28:31],a[140:143],v[92:95],a[28:31]
EX v36,v36
MM a[20:23],a[140:143],v[84:87],a[20:23]
EX v37,v37
MM a[20:23],a[136:139],v[80:83],a[20:23]
RD a[216:219],v226 offset:53248
CV v6,v36,v37
MM a[12:15],a[136:139],v[72:75],a[12:15]
EX v38,v38
MM a[12:15],a[140:143],v[76:79],a[12:15]
RD a[220:223],v227 offset:53248
RD a[224:227],v224 offset:57344
MM a[4:7],a[140:143],v[68:71],a[4:7]
EX v39,v39
MM a[4:7],a[136:139],v[64:67],a[4:7]
RD a[228:231],v225 offset:57344
CV v7,v38,v39
MM a[32:35],a[144:147],v[64:67],a[32:35]
EX v40,v40
MM a[32:35],a[148:151],v[68:71],a[32:35]
buffer_load_dwordx4 v231, s[8:11], s64 offen offset:3072 lds
MM a[40:43],a[148:151],v[76:79],a[40:43]
s_add_u32 m0, s65, 81920
EX v41,v41
MM a[40:43],a[144:147],v[72:75],a[40:43]
RD a[232:235],v226 offset:57344
CV v12,v40,v41
MM a[48:51],a[144:147],v[80:83],a[48:51]
EX v42,v42
MM a[48:51],a[148:151],v[84:87],a[48:51]
EX v43,v43
MM a[56:59],a[148:151],v[92:95],a[56:59]
RD a[236:239],v227 offset:57344
CV v13,v42,v43
MM a[56:59],a[144:147],v[88:91],a[56:59]
EX v44,v44
MM a[60:63],a[152:155],v[88:91],a[60:63]
buffer_load_dwordx4 v235, s[12:15], s81 offen offset:0 lds
MM a[60:63],a[156:159],v[92:95],a[60:63]
EX v45,v45
MM a[52:55],a[156:159],v[84:87],a[52:55]
RD a[240:243],v224 offset:61440
CV v14,v44,v45
MM a[52:55],a[152:155],v[80:83],a[52:55]
EX v46,v46
MM a[44:47],a[152:155],v[72:75],a[44:47]
EX v47,v47
MM a[44:47],a[156:159],v[76:79],a[44:47]
RD a[244:247],v225 offset:61440
CV v15,v46,v47
MM a[36:39],a[156:159],v[68:71],a[36:39]
buffer_load_dwordx4 v235, s[12:15], s64 offen offset:1024 lds
MM a[36:39],a[152:155],v[64:67],a[36:39]
EX v48,v48
MM a[64:67],a[160:163],v[64:67],a[64:67]
EX v49,v49
MM a[64:67],a[164:167],v[68:71],a[64:67]
RD a[248:251],v226 offset:61440
CV v20,v48,v49
MM a[72:75],a[164:167],v[76:79],a[72:75]
EX v50,v50
MM a[72:75],a[160:163],v[72:75],a[72:75]
EX v51,v51
MM a[80:83],a[160:163],v[80:83],a[80:83]
RD a[252:255],v227 offset:61440
CV v21,v50,v51
MM a[80:83],a[164:167],v[84:87],a[80:83]
buffer_load_dwordx4 v235, s[12:15], s81 offen offset:2048 lds
MM a[88:91],a[164:167],v[92:95],a[88:91]
EX v52,v52
MM a[88:91],a[160:163],v[88:91],a[88:91]
EX v53,v53
MM a[92:95],a[168:171],v[88:91],a[92:95]
EX v54,v54
MM a[92:95],a[172:175],v[92:95],a[92:95]
buffer_load_dwordx4 v235, s[12:15], s64 offen offset:3072 lds
MM a[84:87],a[172:175],v[84:87],a[84:87]
EX v55,v55
MM a[84:87],a[168:171],v[80:83],a[84:87]
EX v56,v56
MM a[76:79],a[168:171],v[72:75],a[76:79]
EX v57,v57
MM a[76:79],a[172:175],v[76:79],a[76:79]
EX v58,v58
MM a[68:71],a[172:175],v[68:71],a[68:71]
EX v59,v59
MM a[68:71],a[168:171],v[64:67],a[68:71]
EX v60,v60
MM a[96:99],a[176:179],v[64:67],a[96:99]
EX v61,v61
MM a[96:99],a[180:183],v[68:71],a[96:99]
EX v62,v62
MM a[104:107],a[180:183],v[76:79],a[104:107]
EX v63,v63
MM a[104:107],a[176:179],v[72:75],a[104:107]
CV v22,v52,v53
CV v23,v54,v55
MM a[112:115],a[176:179],v[80:83],a[112:115]
CV v28,v56,v57
CV v29,v58,v59
MM a[112:115],a[180:183],v[84:87],a[112:115]
CV v30,v60,v61
CV v31,v62,v63
MM a[120:123],a[180:183],v[92:95],a[120:123]
MM a[120:123],a[176:179],v[88:91],a[120:123]
MM a[124:127],a[184:187],v[88:91],a[124:127]
v_add_f32 v244, v144, v145
v_add_f32 v245, v148, v149
MM a[124:127],a[188:191],v[92:95],a[124:127]
v_add_f32 v246, v152, v153
v_add_f32 v247, v156, v157
MM a[116:119],a[188:191],v[84:87],a[116:119]
MM a[116:119],a[184:187],v[80:83],a[116:119]
MM a[108:111],a[184:187],v[72:75],a[108:111]
MM a[108:111],a[188:191],v[76:79],a[108:111]
MM a[100:103],a[188:191],v[68:71],a[100:103]
MM a[100:103],a[184:187],v[64:67],a[100:103]
v_smfmac_f32_16x16x64_bf16 v[144:147], v[236:239], v[0:7], v234
v_smfmac_f32_16x16x64_bf16 v[148:151], v[236:239], v[8:15], v234
v_smfmac_f32_16x16x64_bf16 v[152:155], v[236:239], v[16:23], v234
v_smfmac_f32_16x16x64_bf16 v[156:159], v[236:239], v[24:31], v234
s_waitcnt lgkmcnt(0)
MM v[64:67],a[192:195],v[160:163],v[128:131]
s_add_u32 s64, s64, 16384
TR a[128:129],v232 offset:32768
s_add_u32 s81, s64, 896
MM v[64:67],a[196:199],v[176:179],v[64:67]
TR a[130:131],v232 offset:36864
v_max3_f32 v249, v144, v145, v148
MM v[64:67],a[200:203],v[192:195],v[64:67]
TR a[132:133],v232 offset:40960
v_max3_f32 v249, v249, v149, v152
MM v[64:67],a[204:207],v[208:211],v[64:67]
TR a[134:135],v232 offset:45056
v_max3_f32 v249, v249, v153, v156
MM v[68:71],a[220:223],v[208:211],v[128:131]
TR a[136:137],v233 offset:32768
v_max_f32 v249, v249, v157
MM v[68:71],a[216:219],v[192:195],v[68:71]
TR a[138:139],v233 offset:36864
v_cmp_lt_f32 vcc, s71, v249
MM v[68:71],a[212:215],v[176:179],v[68:71]
TR a[140:141],v233 offset:40960
TR a[142:143],v233 offset:45056
MM v[68:71],a[208:211],v[160:163],v[68:71]
s_cbranch_vccnz LSITE30_%=
LBACK30_%=:
MM v[76:79],a[208:211],v[164:167],v[132:135]
s_and_b32 s90, s17, 63
s_cmp_lt_u32 s17, 80
s_cselect_b32 s17, s17, s90
s_cselect_b64 s[44:45], -1, 0
MM v[76:79],a[212:215],v[180:183],v[76:79]
v_lshrrev_b32 v253, 4, v248
v_min_u32 v253, 35, v253
MM v[76:79],a[216:219],v[196:199],v[76:79]
v_lshrrev_b32 v252, 2, v253
v_mul_u32_u24 v252, 144, v252
MM v[76:79],a[220:223],v[212:215],v[76:79]
TR a[144:145],v232 offset:33280
v_lshl_add_u32 v253, v253, 2, v252
MM v[72:75],a[204:207],v[212:215],v[132:135]
s_lshr_b32 s90, s17, 3
EX v64,v64
MM v[72:75],a[200:203],v[196:199],v[72:75]
s_lshl_b32 s76, s90, 4
EX v65,v65
MM v[72:75],a[196:199],v[180:183],v[72:75]
s_add_u32 s76, s76, 73728
buffer_load_dword v252, v253, s[20:23], s76 offen offset:0 sc1
MM v[72:75],a[192:195],v[164:167],v[72:75]
EX v66,v66
MM v[80:83],a[192:195],v[168:171],v[136:139]
EX v67,v67
MM v[80:83],a[196:199],v[184:187],v[80:83]
EX v68,v68
MM v[80:83],a[200:203],v[200:203],v[80:83]
TR a[146:147],v232 offset:37376
CV v64,v64,v65
MM v[80:83],a[204:207],v[216:219],v[80:83]
EX v69,v69
MM v[84:87],a[220:223],v[216:219],v[136:139]
TR a[148:149],v232 offset:41472
CV v65,v66,v67
MM v[84:87],a[216:219],v[200:203],v[84:87]
EX v70,v70
MM v[84:87],a[212:215],v[184:187],v[84:87]
TR a[150:151],v232 offset:45568
CV v66,v68,v69
MM v[84:87],a[208:211],v[168:171],v[84:87]
EX v71,v71
MM v[92:95],a[208:211],v[172:175],v[140:143]
TR a[152:153],v233 offset:33280
TR a[154:155],v233 offset:37376
MM v[92:95],a[212:215],v[188:191],v[92:95]
EX v72,v72
MM v[92:95],a[216:219],v[204:207],v[92:95]
EX v73,v73
MM v[92:95],a[220:223],v[220:223],v[92:95]
TR a[156:157],v233 offset:41472
CV v67,v70,v71
MM v[88:91],a[204:207],v[220:223],v[140:143]
EX v74,v74
MM v[88:91],a[200:203],v[204:207],v[88:91]
TR a[158:159],v233 offset:45568
CV v72,v72,v73
MM v[88:91],a[196:199],v[188:191],v[88:91]
EX v75,v75
MM v[88:91],a[192:195],v[172:175],v[88:91]
TR a[160:161],v232 offset:33792
TR a[162:163],v232 offset:37888
MM v[96:99],a[224:227],v[160:163],v[128:131]
EX v76,v76
MM v[96:99],a[228:231],v[176:179],v[96:99]
TR a[164:165],v232 offset:41984
CV v73,v74,v75
MM v[96:99],a[232:235],v[192:195],v[96:99]
EX v77,v77
MM v[96:99],a[236:239],v[208:211],v[96:99]
EX v78,v78
MM v[100:103],a[252:255],v[208:211],v[128:131]
TR a[166:167],v232 offset:46080
CV v74,v76,v77
MM v[100:103],a[248:251],v[192:195],v[100:103]
EX v79,v79
MM v[100:103],a[244:247],v[176:179],v[100:103]
TR a[168:169],v233 offset:33792
CV v75,v78,v79
MM v[100:103],a[240:243],v[160:163],v[100:103]
EX v80,v80
MM v[108:111],a[240:243],v[164:167],v[132:135]
TR a[170:171],v233 offset:37888
TR a[172:173],v233 offset:41984
MM v[108:111],a[244:247],v[180:183],v[108:111]
EX v81,v81
MM v[108:111],a[248:251],v[196:199],v[108:111]
TR a[174:175],v233 offset:46080
CV v80,v80,v81
MM v[108:111],a[252:255],v[212:215],v[108:111]
EX v82,v82
MM v[104:107],a[236:239],v[212:215],v[132:135]
EX v83,v83
MM v[104:107],a[232:235],v[196:199],v[104:107]
TR a[176:177],v232 offset:34304
TR a[178:179],v232 offset:38400
MM v[104:107],a[228:231],v[180:183],v[104:107]
EX v84,v84
MM v[104:107],a[224:227],v[164:167],v[104:107]
TR a[180:181],v232 offset:42496
CV v81,v82,v83
MM v[112:115],a[224:227],v[168:171],v[136:139]
EX v85,v85
MM v[112:115],a[228:231],v[184:187],v[112:115]
TR a[182:183],v232 offset:46592
CV v82,v84,v85
MM v[112:115],a[232:235],v[200:203],v[112:115]
EX v86,v86
MM v[112:115],a[236:239],v[216:219],v[112:115]
TR a[184:185],v233 offset:34304
TR a[186:187],v233 offset:38400
MM v[116:119],a[252:255],v[216:219],v[136:139]
EX v87,v87
MM v[116:119],a[248:251],v[200:203],v[116:119]
TR a[188:189],v233 offset:42496
CV v83,v86,v87
MM v[116:119],a[244:247],v[184:187],v[116:119]
EX v88,v88
MM v[116:119],a[240:243],v[168:171],v[116:119]
EX v89,v89
MM v[124:127],a[240:243],v[172:175],v[140:143]
TR a[190:191],v233 offset:46592
CV v88,v88,v89
MM v[124:127],a[244:247],v[188:191],v[124:127]
EX v90,v90
MM v[124:127],a[248:251],v[204:207],v[124:127]
EX v91,v91
MM v[124:127],a[252:255],v[220:223],v[124:127]
EX v92,v92
MM v[120:123],a[236:239],v[220:223],v[140:143]
EX v93,v93
MM v[120:123],a[232:235],v[204:207],v[120:123]
EX v94,v94
MM v[120:123],a[228:231],v[188:191],v[120:123]
EX v95,v95
MM v[120:123],a[224:227],v[172:175],v[120:123]
s_waitcnt vmcnt(15) lgkmcnt(0)
s_barrier
MM a[0:3],a[128:131],v[0:3],a[0:3]
s_add_u32 m0, s65, 32768
CV v89,v90,v91
CV v90,v92,v93
MM a[0:3],a[132:135],v[4:7],a[0:3]
RD a[192:195],v224 offset:0
CV v91,v94,v95
MM a[8:11],a[132:135],v[12:15],a[8:11]
buffer_load_dwordx4 v228, s[8:11], s64 offen offset:0 lds
MM a[8:11],a[128:131],v[8:11],a[8:11]
RD a[196:199],v225 offset:0
RD a[200:203],v226 offset:0
MM a[16:19],a[128:131],v[16:19],a[16:19]
EX v96,v96
MM a[16:19],a[132:135],v[20:23],a[16:19]
EX v97,v97
MM a[24:27],a[132:135],v[28:31],a[24:27]
RD a[204:207],v227 offset:0
CV v68,v96,v97
MM a[24:27],a[128:131],v[24:27],a[24:27]
EX v98,v98
MM a[28:31],a[136:139],v[24:27],a[28:31]
buffer_load_dwordx4 v229, s[8:11], s64 offen offset:1024 lds
MM a[28:31],a[140:143],v[28:31],a[28:31]
EX v99,v99
MM a[20:23],a[140:143],v[20:23],a[20:23]
RD a[208:211],v224 offset:4096
CV v69,v98,v99
MM a[20:23],a[136:139],v[16:19],a[20:23]
EX v100,v100
MM a[12:15],a[136:139],v[8:11],a[12:15]
EX v101,v101
MM a[12:15],a[140:143],v[12:15],a[12:15]
RD a[212:215],v225 offset:4096
CV v70,v100,v101
MM a[4:7],a[140:143],v[4:7],a[4:7]
EX v102,v102
MM a[4:7],a[136:139],v[0:3],a[4:7]
buffer_load_dwordx4 v230, s[8:11], s64 offen offset:2048 lds
MM a[32:35],a[144:147],v[0:3],a[32:35]
)ASM"
R"ASM(
EX v103,v103
MM a[32:35],a[148:151],v[4:7],a[32:35]
RD a[216:219],v226 offset:4096
CV v71,v102,v103
MM a[40:43],a[148:151],v[12:15],a[40:43]
EX v104,v104
MM a[40:43],a[144:147],v[8:11],a[40:43]
EX v105,v105
MM a[48:51],a[144:147],v[16:19],a[48:51]
RD a[220:223],v227 offset:4096
CV v76,v104,v105
MM a[48:51],a[148:151],v[20:23],a[48:51]
EX v106,v106
MM a[56:59],a[148:151],v[28:31],a[56:59]
buffer_load_dwordx4 v231, s[8:11], s64 offen offset:3072 lds
MM a[56:59],a[144:147],v[24:27],a[56:59]
s_add_u32 m0, s65, 98304
EX v107,v107
MM a[60:63],a[152:155],v[24:27],a[60:63]
RD a[224:227],v224 offset:8192
CV v77,v106,v107
MM a[60:63],a[156:159],v[28:31],a[60:63]
EX v108,v108
MM a[52:55],a[156:159],v[20:23],a[52:55]
EX v109,v109
MM a[52:55],a[152:155],v[16:19],a[52:55]
RD a[228:231],v225 offset:8192
CV v78,v108,v109
MM a[44:47],a[152:155],v[8:11],a[44:47]
EX v110,v110
MM a[44:47],a[156:159],v[12:15],a[44:47]
buffer_load_dwordx4 v235, s[12:15], s81 offen offset:0 lds
MM a[36:39],a[156:159],v[4:7],a[36:39]
EX v111,v111
MM a[36:39],a[152:155],v[0:3],a[36:39]
RD a[232:235],v226 offset:8192
CV v79,v110,v111
MM a[64:67],a[160:163],v[0:3],a[64:67]
EX v112,v112
MM a[64:67],a[164:167],v[4:7],a[64:67]
EX v113,v113
MM a[72:75],a[164:167],v[12:15],a[72:75]
RD a[236:239],v227 offset:8192
CV v84,v112,v113
MM a[72:75],a[160:163],v[8:11],a[72:75]
buffer_load_dwordx4 v235, s[12:15], s64 offen offset:1024 lds
MM a[80:83],a[160:163],v[16:19],a[80:83]
EX v114,v114
MM a[80:83],a[164:167],v[20:23],a[80:83]
EX v115,v115
MM a[88:91],a[164:167],v[28:31],a[88:91]
RD a[240:243],v224 offset:12288
CV v85,v114,v115
MM a[88:91],a[160:163],v[24:27],a[88:91]
EX v116,v116
MM a[92:95],a[168:171],v[24:27],a[92:95]
EX v117,v117
MM a[92:95],a[172:175],v[28:31],a[92:95]
RD a[244:247],v225 offset:12288
CV v86,v116,v117
MM a[84:87],a[172:175],v[20:23],a[84:87]
buffer_load_dwordx4 v235, s[12:15], s81 offen offset:2048 lds
MM a[84:87],a[168:171],v[16:19],a[84:87]
EX v118,v118
MM a[76:79],a[168:171],v[8:11],a[76:79]
EX v119,v119
MM a[76:79],a[172:175],v[12:15],a[76:79]
RD a[248:251],v226 offset:12288
CV v87,v118,v119
MM a[68:71],a[172:175],v[4:7],a[68:71]
EX v120,v120
MM a[68:71],a[168:171],v[0:3],a[68:71]
EX v121,v121
MM a[96:99],a[176:179],v[0:3],a[96:99]
RD a[252:255],v227 offset:12288
CV v92,v120,v121
MM a[96:99],a[180:183],v[4:7],a[96:99]
buffer_load_dwordx4 v235, s[12:15], s64 offen offset:3072 lds
MM a[104:107],a[180:183],v[12:15],a[104:107]
EX v122,v122
MM a[104:107],a[176:179],v[8:11],a[104:107]
EX v123,v123
MM a[112:115],a[176:179],v[16:19],a[112:115]
EX v124,v124
MM a[112:115],a[180:183],v[20:23],a[112:115]
EX v125,v125
MM a[120:123],a[180:183],v[28:31],a[120:123]
EX v126,v126
MM a[120:123],a[176:179],v[24:27],a[120:123]
EX v127,v127
MM a[124:127],a[184:187],v[24:27],a[124:127]
v_add_f32 v244, v144, v145
v_add_f32 v245, v148, v149
MM a[124:127],a[188:191],v[28:31],a[124:127]
v_add_f32 v246, v152, v153
v_add_f32 v247, v156, v157
MM a[116:119],a[188:191],v[20:23],a[116:119]
CV v93,v122,v123
CV v94,v124,v125
MM a[116:119],a[184:187],v[16:19],a[116:119]
CV v95,v126,v127
MM a[108:111],a[184:187],v[8:11],a[108:111]
MM a[108:111],a[188:191],v[12:15],a[108:111]
MM a[100:103],a[188:191],v[4:7],a[100:103]
MM a[100:103],a[184:187],v[0:3],a[100:103]
v_smfmac_f32_16x16x64_bf16 v[144:147], v[236:239], v[64:71], v234
v_smfmac_f32_16x16x64_bf16 v[148:151], v[236:239], v[72:79], v234
v_smfmac_f32_16x16x64_bf16 v[152:155], v[236:239], v[80:87], v234
v_smfmac_f32_16x16x64_bf16 v[156:159], v[236:239], v[88:95], v234
s_waitcnt lgkmcnt(0)
MM v[0:3],a[192:195],v[160:163],v[128:131]
s_add_u32 s64, s64, 16384
TR a[128:129],v232 offset:49152
s_add_u32 s81, s64, 896
MM v[0:3],a[196:199],v[176:179],v[0:3]
TR a[130:131],v232 offset:53248
v_max3_f32 v249, v144, v145, v148
MM v[0:3],a[200:203],v[192:195],v[0:3]
TR a[132:133],v232 offset:57344
v_max3_f32 v249, v249, v149, v152
MM v[0:3],a[204:207],v[208:211],v[0:3]
TR a[134:135],v232 offset:61440
v_max3_f32 v249, v249, v153, v156
MM v[4:7],a[220:223],v[208:211],v[128:131]
TR a[136:137],v233 offset:49152
v_max_f32 v249, v249, v157
MM v[4:7],a[216:219],v[192:195],v[4:7]
TR a[138:139],v233 offset:53248
v_cmp_lt_f32 vcc, s71, v249
MM v[4:7],a[212:215],v[176:179],v[4:7]
TR a[140:141],v233 offset:57344
TR a[142:143],v233 offset:61440
MM v[4:7],a[208:211],v[160:163],v[4:7]
s_cbranch_vccnz LSITE31_%=
LBACK31_%=:
MM v[12:15],a[208:211],v[164:167],v[132:135]
s_waitcnt vmcnt(8)
v_cmp_ne_u32 vcc, s69, v252
s_cbranch_vccnz LRFY_%=
LRFYb_%=:
MM v[12:15],a[212:215],v[180:183],v[12:15]
v_lshrrev_b32 v109, 2, v248
v_lshrrev_b32 v110, 3, v248
MM v[12:15],a[216:219],v[196:199],v[12:15]
s_and_b32 s76, s17, 7
EX v0,v0
MM v[12:15],a[220:223],v[212:215],v[12:15]
s_mul_i32 s76, s76, 2560
EX v1,v1
MM v[8:11],a[204:207],v[212:215],v[132:135]
s_lshl_b32 s91, s90, 8
EX v2,v2
MM v[8:11],a[200:203],v[196:199],v[8:11]
s_add_u32 s76, s76, s91
TR a[144:145],v232 offset:49664
CV v0,v0,v1
MM v[8:11],a[196:199],v[180:183],v[8:11]
s_add_u32 s76, s76, 21659648
buffer_load_dword v96, v109, s[20:23], s76 offen offset:0 sc1
MM v[8:11],a[192:195],v[164:167],v[8:11]
s_add_u32 s76, s76, 20480
EX v3,v3
MM v[16:19],a[192:195],v[168:171],v[136:139]
buffer_load_dword v97, v109, s[20:23], s76 offen offset:0 sc1
MM v[16:19],a[196:199],v[184:187],v[16:19]
s_add_u32 s76, s76, 20480
EX v4,v4
MM v[16:19],a[200:203],v[200:203],v[16:19]
buffer_load_dword v98, v109, s[20:23], s76 offen offset:0 sc1
MM v[16:19],a[204:207],v[216:219],v[16:19]
s_add_u32 s76, s76, 20480
EX v5,v5
MM v[20:23],a[220:223],v[216:219],v[136:139]
buffer_load_dword v99, v109, s[20:23], s76 offen offset:0 sc1
MM v[20:23],a[216:219],v[200:203],v[20:23]
s_add_u32 s76, s76, 20480
TR a[146:147],v232 offset:53760
TR a[148:149],v232 offset:57856
MM v[20:23],a[212:215],v[184:187],v[20:23]
buffer_load_dword v100, v109, s[20:23], s76 offen offset:0 sc1
MM v[20:23],a[208:211],v[168:171],v[20:23]
s_add_u32 s76, s76, 20480
EX v6,v6
MM v[28:31],a[208:211],v[172:175],v[140:143]
buffer_load_dword v101, v109, s[20:23], s76 offen offset:0 sc1
MM v[28:31],a[212:215],v[188:191],v[28:31]
s_add_u32 s76, s76, 20480
EX v7,v7
MM v[28:31],a[216:219],v[204:207],v[28:31]
buffer_load_dword v102, v109, s[20:23], s76 offen offset:0 sc1
MM v[28:31],a[220:223],v[220:223],v[28:31]
s_add_u32 s76, s76, 20480
s_nop 0
LPG22_%=:
s_cbranch_execz LTR4_%=
TR a[150:151],v232 offset:61952
EX v8,v8
MM v[24:27],a[204:207],v[220:223],v[140:143]
buffer_load_dword v103, v109, s[20:23], s76 offen offset:0 sc1
MM v[24:27],a[200:203],v[204:207],v[24:27]
s_add_u32 s76, s76, 20480
TR a[152:153],v233 offset:49664
EX v9,v9
CV v1,v2,v3
MM v[24:27],a[196:199],v[188:191],v[24:27]
buffer_load_dword v104, v109, s[20:23], s76 offen offset:0 sc1
TR a[154:155],v233 offset:53760
MM v[24:27],a[192:195],v[172:175],v[24:27]
s_and_b32 s76, s19, 65535
EX v10,v10
MM v[32:35],a[224:227],v[160:163],v[128:131]
s_sub_u32 s76, 65535, s76
TR a[156:157],v233 offset:57856
CV v2,v4,v5
EX v11,v11
MM v[32:35],a[228:231],v[176:179],v[32:35]
s_mul_i32 s76, s76, 1280
TR a[158:159],v233 offset:61952
CV v3,v6,v7
MM v[32:35],a[232:235],v[192:195],v[32:35]
s_lshl_b32 s91, s90, 7
EX v12,v12
MM v[32:35],a[236:239],v[208:211],v[32:35]
s_add_u32 s76, s76, s91
TR a[160:161],v232 offset:50176
TR a[162:163],v232 offset:54272
MM v[36:39],a[252:255],v[208:211],v[128:131]
s_add_u32 s76, s76, 131072
buffer_load_ushort v108, v110, s[20:23], s76 offen sc1
EX v13,v13
MM v[36:39],a[248:251],v[192:195],v[36:39]
s_ashr_i32 s91, s19, 31
EX v14,v14
MM v[36:39],a[244:247],v[176:179],v[36:39]
s_not_b32 s91, s91
TR a[164:165],v232 offset:58368
CV v8,v8,v9
MM v[36:39],a[240:243],v[160:163],v[36:39]
s_or_b32 s91, s91, 0x80000000
EX v15,v15
MM v[44:47],a[240:243],v[164:167],v[132:135]
s_xor_b32 s19, s19, s91
TR a[166:167],v232 offset:62464
CV v9,v10,v11
MM v[44:47],a[244:247],v[180:183],v[44:47]
s_and_b32 s19, s19, 0xffff0000
v_mov_b32 v111, 0xbfb8aa3b
v_mul_f32 v111, s19, v111
MM v[44:47],a[248:251],v[196:199],v[44:47]
EX v111,v111
EX v16,v16
MM v[44:47],a[252:255],v[212:215],v[44:47]
TR a[168:169],v233 offset:50176
v_add_f32 v111, 1.0, v111
EX v17,v17
MM v[40:43],a[236:239],v[212:215],v[132:135]
v_rcp_f32 v111, v111
MM v[40:43],a[232:235],v[196:199],v[40:43]
TR a[170:171],v233 offset:54272
CV v111,v111,v111
v_and_b32 v111, 0xffff0000, v111
EX v18,v18
MM v[40:43],a[228:231],v[180:183],v[40:43]
TR a[172:173],v233 offset:58368
CV v10,v12,v13
MM v[40:43],a[224:227],v[164:167],v[40:43]
EX v19,v19
MM v[48:51],a[224:227],v[168:171],v[136:139]
TR a[174:175],v233 offset:62464
CV v11,v14,v15
MM v[48:51],a[228:231],v[184:187],v[48:51]
EX v20,v20
MM v[48:51],a[232:235],v[200:203],v[48:51]
TR a[176:177],v232 offset:50688
TR a[178:179],v232 offset:54784
MM v[48:51],a[236:239],v[216:219],v[48:51]
EX v21,v21
MM v[52:55],a[252:255],v[216:219],v[136:139]
EX v22,v22
MM v[52:55],a[248:251],v[200:203],v[52:55]
TR a[180:181],v232 offset:58880
CV v16,v16,v17
MM v[52:55],a[244:247],v[184:187],v[52:55]
EX v23,v23
MM v[52:55],a[240:243],v[168:171],v[52:55]
TR a[182:183],v232 offset:62976
CV v17,v18,v19
MM v[60:63],a[240:243],v[172:175],v[140:143]
EX v24,v24
MM v[60:63],a[244:247],v[188:191],v[60:63]
TR a[184:185],v233 offset:50688
TR a[186:187],v233 offset:54784
MM v[60:63],a[248:251],v[204:207],v[60:63]
TR a[188:189],v233 offset:58880
TR a[190:191],v233 offset:62976
EX v25,v25
MM v[60:63],a[252:255],v[220:223],v[60:63]
EX v26,v26
MM v[56:59],a[236:239],v[220:223],v[140:143]
EX v27,v27
MM v[56:59],a[232:235],v[204:207],v[56:59]
EX v28,v28
MM v[56:59],a[228:231],v[188:191],v[56:59]
EX v29,v29
CV v18,v20,v21
MM v[56:59],a[224:227],v[172:175],v[56:59]
s_waitcnt lgkmcnt(0)
s_barrier
MM a[0:3],a[128:131],v[64:67],a[0:3]
s_add_u32 m0, s65, 49152
CV v19,v22,v23
CV v24,v24,v25
MM a[0:3],a[132:135],v[68:71],a[0:3]
CV v25,v26,v27
CV v26,v28,v29
MM a[8:11],a[132:135],v[76:79],a[8:11]
EX v30,v30
MM a[8:11],a[128:131],v[72:75],a[8:11]
EX v31,v31
MM a[16:19],a[128:131],v[80:83],a[16:19]
RD a[192:195],v224 offset:16384
CV v27,v30,v31
MM a[16:19],a[132:135],v[84:87],a[16:19]
EX v32,v32
MM a[24:27],a[132:135],v[92:95],a[24:27]
buffer_load_dwordx4 v228, s[8:11], s64 offen offset:0 lds
MM a[24:27],a[128:131],v[88:91],a[24:27]
EX v33,v33
MM a[28:31],a[136:139],v[88:91],a[28:31]
RD a[196:199],v225 offset:16384
CV v4,v32,v33
MM a[28:31],a[140:143],v[92:95],a[28:31]
EX v34,v34
MM a[20:23],a[140:143],v[84:87],a[20:23]
EX v35,v35
MM a[20:23],a[136:139],v[80:83],a[20:23]
RD a[200:203],v226 offset:16384
CV v5,v34,v35
MM a[12:15],a[136:139],v[72:75],a[12:15]
EX v36,v36
MM a[12:15],a[140:143],v[76:79],a[12:15]
buffer_load_dwordx4 v229, s[8:11], s64 offen offset:1024 lds
MM a[4:7],a[140:143],v[68:71],a[4:7]
EX v37,v37
MM a[4:7],a[136:139],v[64:67],a[4:7]
RD a[204:207],v227 offset:16384
CV v6,v36,v37
MM a[32:35],a[144:147],v[64:67],a[32:35]
EX v38,v38
MM a[32:35],a[148:151],v[68:71],a[32:35]
EX v39,v39
MM a[40:43],a[148:151],v[76:79],a[40:43]
RD a[208:211],v224 offset:20480
CV v7,v38,v39
MM a[40:43],a[144:147],v[72:75],a[40:43]
EX v40,v40
MM a[48:51],a[144:147],v[80:83],a[48:51]
buffer_load_dwordx4 v230, s[8:11], s64 offen offset:2048 lds
MM a[48:51],a[148:151],v[84:87],a[48:51]
EX v41,v41
MM a[56:59],a[148:151],v[92:95],a[56:59]
RD a[212:215],v225 offset:20480
CV v12,v40,v41
MM a[56:59],a[144:147],v[88:91],a[56:59]
EX v42,v42
MM a[60:63],a[152:155],v[88:91],a[60:63]
EX v43,v43
MM a[60:63],a[156:159],v[92:95],a[60:63]
RD a[216:219],v226 offset:20480
CV v13,v42,v43
MM a[52:55],a[156:159],v[84:87],a[52:55]
EX v44,v44
MM a[52:55],a[152:155],v[80:83],a[52:55]
buffer_load_dwordx4 v231, s[8:11], s64 offen offset:3072 lds
MM a[44:47],a[152:155],v[72:75],a[44:47]
s_add_u32 m0, s65, 114688
EX v45,v45
MM a[44:47],a[156:159],v[76:79],a[44:47]
RD a[220:223],v227 offset:20480
CV v14,v44,v45
MM a[36:39],a[156:159],v[68:71],a[36:39]
EX v46,v46
MM a[36:39],a[152:155],v[64:67],a[36:39]
RD a[224:227],v224 offset:24576
RD a[228:231],v225 offset:24576
MM a[64:67],a[160:163],v[64:67],a[64:67]
EX v47,v47
MM a[64:67],a[164:167],v[68:71],a[64:67]
RD a[232:235],v226 offset:24576
)ASM"
R"ASM(
CV v15,v46,v47
MM a[72:75],a[164:167],v[76:79],a[72:75]
EX v48,v48
MM a[72:75],a[160:163],v[72:75],a[72:75]
buffer_load_dwordx4 v235, s[12:15], s81 offen offset:0 lds
MM a[80:83],a[160:163],v[80:83],a[80:83]
EX v49,v49
MM a[80:83],a[164:167],v[84:87],a[80:83]
RD a[236:239],v227 offset:24576
CV v20,v48,v49
MM a[88:91],a[164:167],v[92:95],a[88:91]
EX v50,v50
MM a[88:91],a[160:163],v[88:91],a[88:91]
EX v51,v51
MM a[92:95],a[168:171],v[88:91],a[92:95]
RD a[240:243],v224 offset:28672
CV v21,v50,v51
MM a[92:95],a[172:175],v[92:95],a[92:95]
EX v52,v52
MM a[84:87],a[172:175],v[84:87],a[84:87]
buffer_load_dwordx4 v235, s[12:15], s64 offen offset:1024 lds
MM a[84:87],a[168:171],v[80:83],a[84:87]
EX v53,v53
MM a[76:79],a[168:171],v[72:75],a[76:79]
RD a[244:247],v225 offset:28672
CV v22,v52,v53
MM a[76:79],a[172:175],v[76:79],a[76:79]
EX v54,v54
MM a[68:71],a[172:175],v[68:71],a[68:71]
s_waitcnt vmcnt(6)
v_add_f32 v96, v96, v100
v_add_f32 v97, v97, v101
MM a[68:71],a[168:171],v[64:67],a[68:71]
v_add_f32 v98, v98, v102
v_add_f32 v99, v99, v103
MM a[96:99],a[176:179],v[64:67],a[96:99]
v_add_f32 v96, v96, v98
v_add_f32 v97, v97, v99
MM a[96:99],a[180:183],v[68:71],a[96:99]
v_add_f32 v96, v96, v104
v_lshlrev_b32 v108, 16, v108
MM a[104:107],a[180:183],v[76:79],a[104:107]
v_add_f32 v96, v96, v97
CV v96,v96,v96
EX v55,v55
MM a[104:107],a[176:179],v[72:75],a[104:107]
v_and_b32 v96, 0xffff0000, v96
v_mul_f32 v96, v96, v111
MM a[112:115],a[176:179],v[80:83],a[112:115]
CV v96,v96,v96
v_and_b32 v96, 0xffff0000, v96
EX v56,v56
MM a[112:115],a[180:183],v[84:87],a[112:115]
v_add_f32 v96, v96, v108
CV v96,v96,v96
MM a[120:123],a[180:183],v[92:95],a[120:123]
s_mov_b64 exec, s[44:45]
buffer_store_short v96, v110, s[20:23], s76 offen offset:0 sc1
s_mov_b64 exec, -1
s_nop 0
EX v57,v57
MM a[120:123],a[176:179],v[88:91],a[120:123]
EX v58,v58
MM a[124:127],a[184:187],v[88:91],a[124:127]
v_add_f32 v244, v144, v145
v_add_f32 v245, v148, v149
MM a[124:127],a[188:191],v[92:95],a[124:127]
v_add_f32 v246, v152, v153
v_add_f32 v247, v156, v157
MM a[116:119],a[188:191],v[84:87],a[116:119]
EX v59,v59
MM a[116:119],a[184:187],v[80:83],a[116:119]
EX v60,v60
MM a[108:111],a[184:187],v[72:75],a[108:111]
CV v23,v54,v55
CV v28,v56,v57
MM a[108:111],a[188:191],v[76:79],a[108:111]
EX v61,v61
MM a[100:103],a[188:191],v[68:71],a[100:103]
buffer_load_dwordx4 v235, s[12:15], s81 offen offset:2048 lds
MM a[100:103],a[184:187],v[64:67],a[100:103]
EX v62,v62
v_smfmac_f32_16x16x64_bf16 v[144:147], v[236:239], v[0:7], v234
RD a[248:251],v226 offset:28672
RD a[252:255],v227 offset:28672
EX v63,v63
v_smfmac_f32_16x16x64_bf16 v[148:151], v[236:239], v[8:15], v234
CV v29,v58,v59
v_smfmac_f32_16x16x64_bf16 v[152:155], v[236:239], v[16:23], v234
CV v30,v60,v61
CV v31,v62,v63
s_nop 1
v_smfmac_f32_16x16x64_bf16 v[156:159], v[236:239], v[24:31], v234
s_waitcnt lgkmcnt(0)
buffer_load_dwordx4 v235, s[12:15], s64 offen offset:3072 lds
MM v[64:67],a[192:195],v[160:163],v[128:131]
s_sub_u32 s64, s64, 507904
TR a[128:129],v232 offset:0
s_add_u32 s81, s64, 896
MM v[64:67],a[196:199],v[176:179],v[64:67]
TR a[130:131],v232 offset:4096
v_max3_f32 v249, v144, v145, v148
MM v[64:67],a[200:203],v[192:195],v[64:67]
TR a[132:133],v232 offset:8192
v_max3_f32 v249, v249, v149, v152
MM v[64:67],a[204:207],v[208:211],v[64:67]
TR a[134:135],v232 offset:12288
v_max3_f32 v249, v249, v153, v156
MM v[68:71],a[220:223],v[208:211],v[128:131]
TR a[136:137],v233 offset:0
v_max_f32 v249, v249, v157
MM v[68:71],a[216:219],v[192:195],v[68:71]
TR a[138:139],v233 offset:4096
v_cmp_lt_f32 vcc, s71, v249
MM v[68:71],a[212:215],v[176:179],v[68:71]
TR a[140:141],v233 offset:8192
TR a[142:143],v233 offset:12288
MM v[68:71],a[208:211],v[160:163],v[68:71]
s_cbranch_vccnz LSITE32_%=
LBACK32_%=:
MM v[76:79],a[208:211],v[164:167],v[132:135]
EX v64,v64
MM v[76:79],a[212:215],v[180:183],v[76:79]
EX v65,v65
MM v[76:79],a[216:219],v[196:199],v[76:79]
TR a[144:145],v232 offset:512
CV v64,v64,v65
MM v[76:79],a[220:223],v[212:215],v[76:79]
EX v66,v66
MM v[72:75],a[204:207],v[212:215],v[132:135]
EX v67,v67
MM v[72:75],a[200:203],v[196:199],v[72:75]
TR a[146:147],v232 offset:4608
CV v65,v66,v67
MM v[72:75],a[196:199],v[180:183],v[72:75]
EX v68,v68
MM v[72:75],a[192:195],v[164:167],v[72:75]
TR a[148:149],v232 offset:8704
TR a[150:151],v232 offset:12800
MM v[80:83],a[192:195],v[168:171],v[136:139]
EX v69,v69
MM v[80:83],a[196:199],v[184:187],v[80:83]
TR a[152:153],v233 offset:512
CV v66,v68,v69
MM v[80:83],a[200:203],v[200:203],v[80:83]
EX v70,v70
MM v[80:83],a[204:207],v[216:219],v[80:83]
TR a[154:155],v233 offset:4608
TR a[156:157],v233 offset:8704
MM v[84:87],a[220:223],v[216:219],v[136:139]
EX v71,v71
MM v[84:87],a[216:219],v[200:203],v[84:87]
TR a[158:159],v233 offset:12800
CV v67,v70,v71
MM v[84:87],a[212:215],v[184:187],v[84:87]
EX v72,v72
MM v[84:87],a[208:211],v[168:171],v[84:87]
EX v73,v73
MM v[92:95],a[208:211],v[172:175],v[140:143]
TR a[160:161],v232 offset:1024
CV v72,v72,v73
MM v[92:95],a[212:215],v[188:191],v[92:95]
EX v74,v74
MM v[92:95],a[216:219],v[204:207],v[92:95]
TR a[162:163],v232 offset:5120
TR a[164:165],v232 offset:9216
MM v[92:95],a[220:223],v[220:223],v[92:95]
EX v75,v75
MM v[88:91],a[204:207],v[220:223],v[140:143]
TR a[166:167],v232 offset:13312
CV v73,v74,v75
MM v[88:91],a[200:203],v[204:207],v[88:91]
EX v76,v76
MM v[88:91],a[196:199],v[188:191],v[88:91]
EX v77,v77
MM v[88:91],a[192:195],v[172:175],v[88:91]
TR a[168:169],v233 offset:1024
CV v74,v76,v77
MM v[96:99],a[224:227],v[160:163],v[128:131]
EX v78,v78
MM v[96:99],a[228:231],v[176:179],v[96:99]
TR a[170:171],v233 offset:5120
TR a[172:173],v233 offset:9216
MM v[96:99],a[232:235],v[192:195],v[96:99]
EX v79,v79
MM v[96:99],a[236:239],v[208:211],v[96:99]
TR a[174:175],v233 offset:13312
CV v75,v78,v79
MM v[100:103],a[252:255],v[208:211],v[128:131]
EX v80,v80
MM v[100:103],a[248:251],v[192:195],v[100:103]
EX v81,v81
MM v[100:103],a[244:247],v[176:179],v[100:103]
TR a[176:177],v232 offset:1536
CV v80,v80,v81
MM v[100:103],a[240:243],v[160:163],v[100:103]
EX v82,v82
MM v[108:111],a[240:243],v[164:167],v[132:135]
TR a[178:179],v232 offset:5632
TR a[180:181],v232 offset:9728
MM v[108:111],a[244:247],v[180:183],v[108:111]
EX v83,v83
MM v[108:111],a[248:251],v[196:199],v[108:111]
TR a[182:183],v232 offset:13824
CV v81,v82,v83
MM v[108:111],a[252:255],v[212:215],v[108:111]
EX v84,v84
MM v[104:107],a[236:239],v[212:215],v[132:135]
TR a[184:185],v233 offset:1536
TR a[186:187],v233 offset:5632
MM v[104:107],a[232:235],v[196:199],v[104:107]
EX v85,v85
MM v[104:107],a[228:231],v[180:183],v[104:107]
TR a[188:189],v233 offset:9728
CV v82,v84,v85
MM v[104:107],a[224:227],v[164:167],v[104:107]
EX v86,v86
MM v[112:115],a[224:227],v[168:171],v[136:139]
EX v87,v87
MM v[112:115],a[228:231],v[184:187],v[112:115]
TR a[190:191],v233 offset:13824
CV v83,v86,v87
MM v[112:115],a[232:235],v[200:203],v[112:115]
EX v88,v88
MM v[112:115],a[236:239],v[216:219],v[112:115]
EX v89,v89
MM v[116:119],a[252:255],v[216:219],v[136:139]
EX v90,v90
MM v[116:119],a[248:251],v[200:203],v[116:119]
EX v91,v91
MM v[116:119],a[244:247],v[184:187],v[116:119]
EX v92,v92
MM v[116:119],a[240:243],v[168:171],v[116:119]
EX v93,v93
MM v[124:127],a[240:243],v[172:175],v[140:143]
EX v94,v94
MM v[124:127],a[244:247],v[188:191],v[124:127]
EX v95,v95
MM v[124:127],a[248:251],v[204:207],v[124:127]
EX v96,v96
MM v[124:127],a[252:255],v[220:223],v[124:127]
EX v97,v97
MM v[120:123],a[236:239],v[220:223],v[140:143]
EX v98,v98
MM v[120:123],a[232:235],v[204:207],v[120:123]
EX v99,v99
MM v[120:123],a[228:231],v[188:191],v[120:123]
EX v100,v100
MM v[120:123],a[224:227],v[172:175],v[120:123]
s_waitcnt lgkmcnt(0)
s_barrier
MM a[0:3],a[128:131],v[0:3],a[0:3]
s_add_u32 m0, s65, 0
CV v88,v88,v89
CV v89,v90,v91
MM a[0:3],a[132:135],v[4:7],a[0:3]
RD a[192:195],v224 offset:32768
CV v90,v92,v93
MM a[8:11],a[132:135],v[12:15],a[8:11]
RD a[196:199],v225 offset:32768
CV v91,v94,v95
MM a[8:11],a[128:131],v[8:11],a[8:11]
buffer_load_dwordx4 v228, s[8:11], s64 offen offset:0 lds
MM a[16:19],a[128:131],v[16:19],a[16:19]
RD a[200:203],v226 offset:32768
CV v68,v96,v97
MM a[16:19],a[132:135],v[20:23],a[16:19]
RD a[204:207],v227 offset:32768
CV v69,v98,v99
MM a[24:27],a[132:135],v[28:31],a[24:27]
buffer_load_dwordx4 v229, s[8:11], s64 offen offset:1024 lds
MM a[24:27],a[128:131],v[24:27],a[24:27]
RD a[208:211],v224 offset:36864
RD a[212:215],v225 offset:36864
MM a[28:31],a[136:139],v[24:27],a[28:31]
buffer_load_dwordx4 v230, s[8:11], s64 offen offset:2048 lds
MM a[28:31],a[140:143],v[28:31],a[28:31]
RD a[216:219],v226 offset:36864
RD a[220:223],v227 offset:36864
MM a[20:23],a[140:143],v[20:23],a[20:23]
EX v101,v101
MM a[20:23],a[136:139],v[16:19],a[20:23]
RD a[224:227],v224 offset:40960
CV v70,v100,v101
MM a[12:15],a[136:139],v[8:11],a[12:15]
EX v102,v102
MM a[12:15],a[140:143],v[12:15],a[12:15]
buffer_load_dwordx4 v231, s[8:11], s64 offen offset:3072 lds
MM a[4:7],a[140:143],v[4:7],a[4:7]
s_add_u32 m0, s65, 65536
EX v103,v103
MM a[4:7],a[136:139],v[0:3],a[4:7]
RD a[228:231],v225 offset:40960
CV v71,v102,v103
MM a[32:35],a[144:147],v[0:3],a[32:35]
EX v104,v104
MM a[32:35],a[148:151],v[4:7],a[32:35]
EX v105,v105
MM a[40:43],a[148:151],v[12:15],a[40:43]
RD a[232:235],v226 offset:40960
CV v76,v104,v105
MM a[40:43],a[144:147],v[8:11],a[40:43]
EX v106,v106
MM a[48:51],a[144:147],v[16:19],a[48:51]
buffer_load_dwordx4 v235, s[12:15], s81 offen offset:0 lds
MM a[48:51],a[148:151],v[20:23],a[48:51]
EX v107,v107
MM a[56:59],a[148:151],v[28:31],a[56:59]
RD a[236:239],v227 offset:40960
CV v77,v106,v107
MM a[56:59],a[144:147],v[24:27],a[56:59]
EX v108,v108
MM a[60:63],a[152:155],v[24:27],a[60:63]
EX v109,v109
MM a[60:63],a[156:159],v[28:31],a[60:63]
RD a[240:243],v224 offset:45056
CV v78,v108,v109
MM a[52:55],a[156:159],v[20:23],a[52:55]
EX v110,v110
MM a[52:55],a[152:155],v[16:19],a[52:55]
buffer_load_dwordx4 v235, s[12:15], s64 offen offset:1024 lds
MM a[44:47],a[152:155],v[8:11],a[44:47]
EX v111,v111
MM a[44:47],a[156:159],v[12:15],a[44:47]
RD a[244:247],v225 offset:45056
CV v79,v110,v111
MM a[36:39],a[156:159],v[4:7],a[36:39]
EX v112,v112
MM a[36:39],a[152:155],v[0:3],a[36:39]
EX v113,v113
MM a[64:67],a[160:163],v[0:3],a[64:67]
RD a[248:251],v226 offset:45056
CV v84,v112,v113
MM a[64:67],a[164:167],v[4:7],a[64:67]
EX v114,v114
MM a[72:75],a[164:167],v[12:15],a[72:75]
buffer_load_dwordx4 v235, s[12:15], s81 offen offset:2048 lds
MM a[72:75],a[160:163],v[8:11],a[72:75]
EX v115,v115
MM a[80:83],a[160:163],v[16:19],a[80:83]
RD a[252:255],v227 offset:45056
CV v85,v114,v115
MM a[80:83],a[164:167],v[20:23],a[80:83]
EX v116,v116
MM a[88:91],a[164:167],v[28:31],a[88:91]
EX v117,v117
MM a[88:91],a[160:163],v[24:27],a[88:91]
buffer_load_dwordx4 v235, s[12:15], s64 offen offset:3072 lds
MM a[92:95],a[168:171],v[24:27],a[92:95]
EX v118,v118
MM a[92:95],a[172:175],v[28:31],a[92:95]
EX v119,v119
MM a[84:87],a[172:175],v[20:23],a[84:87]
EX v120,v120
MM a[84:87],a[168:171],v[16:19],a[84:87]
EX v121,v121
MM a[76:79],a[168:171],v[8:11],a[76:79]
EX v122,v122
MM a[76:79],a[172:175],v[12:15],a[76:79]
EX v123,v123
MM a[68:71],a[172:175],v[4:7],a[68:71]
EX v124,v124
MM a[68:71],a[168:171],v[0:3],a[68:71]
EX v125,v125
MM a[96:99],a[176:179],v[0:3],a[96:99]
EX v126,v126
MM a[96:99],a[180:183],v[4:7],a[96:99]
EX v127,v127
MM a[104:107],a[180:183],v[12:15],a[104:107]
CV v86,v116,v117
CV v87,v118,v119
MM a[104:107],a[176:179],v[8:11],a[104:107]
CV v92,v120,v121
CV v93,v122,v123
MM a[112:115],a[176:179],v[16:19],a[112:115]
CV v94,v124,v125
CV v95,v126,v127
MM a[112:115],a[180:183],v[20:23],a[112:115]
MM a[120:123],a[180:183],v[28:31],a[120:123]
s_lshl_b32 s91, s17, 2
s_add_u32 s91, s91, 94464
s_and_b64 s[44:45], s[44:45], 1
MM a[120:123],a[176:179],v[24:27],a[120:123]
s_waitcnt vmcnt(10)
v_mov_b32 v250, s69
s_mov_b64 exec, s[44:45]
buffer_store_dword v250, off, s[20:23], s91 sc1
s_mov_b64 exec, -1
MM a[124:127],a[184:187],v[24:27],a[124:127]
v_add_f32 v244, v144, v145
v_add_f32 v245, v148, v149
MM a[124:127],a[188:191],v[28:31],a[124:127]
)ASM"
R"ASM(
v_add_f32 v246, v152, v153
v_add_f32 v247, v156, v157
MM a[116:119],a[188:191],v[20:23],a[116:119]
MM a[116:119],a[184:187],v[16:19],a[116:119]
MM a[108:111],a[184:187],v[8:11],a[108:111]
MM a[108:111],a[188:191],v[12:15],a[108:111]
MM a[100:103],a[188:191],v[4:7],a[100:103]
MM a[100:103],a[184:187],v[0:3],a[100:103]
v_smfmac_f32_16x16x64_bf16 v[144:147], v[236:239], v[64:71], v234
v_smfmac_f32_16x16x64_bf16 v[148:151], v[236:239], v[72:79], v234
s_nop 0
LPG23_%=:
s_cbranch_execz LPG27_%=
v_smfmac_f32_16x16x64_bf16 v[152:155], v[236:239], v[80:87], v234
v_smfmac_f32_16x16x64_bf16 v[156:159], v[236:239], v[88:95], v234
s_waitcnt lgkmcnt(0)
LLAJ_%=:
MM v[0:3],a[192:195],v[160:163],v[128:131]
s_add_u32 s64, s64, 16384
TR a[128:129],v232 offset:16384
s_add_u32 s81, s64, 896
MM v[0:3],a[196:199],v[176:179],v[0:3]
TR a[130:131],v232 offset:20480
v_max3_f32 v249, v144, v145, v148
MM v[0:3],a[200:203],v[192:195],v[0:3]
TR a[132:133],v232 offset:24576
v_max3_f32 v249, v249, v149, v152
MM v[0:3],a[204:207],v[208:211],v[0:3]
TR a[134:135],v232 offset:28672
v_max3_f32 v249, v249, v153, v156
MM v[4:7],a[220:223],v[208:211],v[128:131]
TR a[136:137],v233 offset:16384
v_max_f32 v249, v249, v157
MM v[4:7],a[216:219],v[192:195],v[4:7]
TR a[138:139],v233 offset:20480
v_cmp_lt_f32 vcc, s71, v249
MM v[4:7],a[212:215],v[176:179],v[4:7]
TR a[140:141],v233 offset:24576
TR a[142:143],v233 offset:28672
MM v[4:7],a[208:211],v[160:163],v[4:7]
s_cbranch_vccnz LSITE33_%=
LBACK33_%=:
MM v[12:15],a[208:211],v[164:167],v[132:135]
v_bfe_u32 v242, v248, 6, 4
v_mul_u32_u24 v242, 1280, v242
MM v[12:15],a[212:215],v[180:183],v[12:15]
v_and_b32 v252, 48, v248
v_bfe_u32 v243, v248, 8, 1
MM v[12:15],a[216:219],v[196:199],v[12:15]
v_lshlrev_b32 v243, 5, v243
v_xor_b32 v252, v252, v243
MM v[12:15],a[220:223],v[212:215],v[12:15]
v_add_u32 v252, v252, v242
v_lshrrev_b32 v242, 3, v248
MM v[8:11],a[204:207],v[212:215],v[132:135]
v_xor_b32 v243, v242, v248
v_and_b32 v243, 96, v243
MM v[8:11],a[200:203],v[196:199],v[8:11]
TR a[144:145],v232 offset:16896
v_and_or_b32 v243, v248, 16, v243
MM v[8:11],a[196:199],v[180:183],v[8:11]
s_and_b32 s66, s49, 8
EX v0,v0
MM v[8:11],a[192:195],v[164:167],v[8:11]
s_lshl_b32 s66, s66, 3
v_lshrrev_b32 v242, 7, v248
v_xor_b32 v243, s66, v243
EX v1,v1
MM v[16:19],a[192:195],v[168:171],v[136:139]
v_mul_u32_u24 v242, 1280, v242
v_add_u32 v240, v242, v243
EX v2,v2
MM v[16:19],a[196:199],v[184:187],v[16:19]
s_mul_i32 s88, %[swave], 40960
EX v3,v3
MM v[16:19],a[200:203],v[200:203],v[16:19]
s_and_b32 s66, s84, 1
EX v4,v4
MM v[16:19],a[204:207],v[216:219],v[16:19]
s_lshl_b32 s86, s66, 6
TR a[146:147],v232 offset:20992
CV v0,v0,v1
MM v[20:23],a[220:223],v[216:219],v[136:139]
s_add_u32 s86, s86, s88
EX v5,v5
MM v[20:23],a[216:219],v[200:203],v[20:23]
s_xor_b32 s66, s66, 1
TR a[148:149],v232 offset:25088
CV v1,v2,v3
MM v[20:23],a[212:215],v[184:187],v[20:23]
s_lshl_b32 s66, s66, 8
EX v6,v6
MM v[20:23],a[208:211],v[168:171],v[20:23]
s_add_u32 s88, s88, s66
EX v7,v7
MM v[28:31],a[208:211],v[172:175],v[140:143]
s_and_b32 s66, s49, 16
TR a[150:151],v232 offset:29184
TR a[152:153],v233 offset:16896
MM v[28:31],a[212:215],v[188:191],v[28:31]
s_lshl_b32 s66, s66, 3
EX v8,v8
MM v[28:31],a[216:219],v[204:207],v[28:31]
s_sub_u32 s87, s86, s66
TR a[154:155],v233 offset:20992
CV v2,v4,v5
MM v[28:31],a[220:223],v[220:223],v[28:31]
s_add_u32 s87, s87, 128
EX v9,v9
MM v[24:27],a[204:207],v[220:223],v[140:143]
s_add_u32 s86, s86, s66
TR a[156:157],v233 offset:25088
CV v3,v6,v7
MM v[24:27],a[200:203],v[204:207],v[24:27]
s_and_b32 s94, s49, 7
EX v10,v10
MM v[24:27],a[196:199],v[188:191],v[24:27]
s_lshl_b32 s89, %[swave], 11
TR a[158:159],v233 offset:29184
CV v8,v8,v9
MM v[24:27],a[192:195],v[172:175],v[24:27]
s_add_u32 s89, s89, %[slds]
EX v11,v11
MM v[32:35],a[224:227],v[160:163],v[128:131]
s_xor_b32 s66, s94, 4
TR a[160:161],v232 offset:17408
TR a[162:163],v232 offset:21504
MM v[32:35],a[228:231],v[176:179],v[32:35]
s_mul_i32 s66, s66, 163840
EX v12,v12
MM v[32:35],a[232:235],v[192:195],v[32:35]
s_add_u32 s66, s66, s88
s_add_u32 m0, s89, 131072
s_nop 0
buffer_load_dwordx4 v252, s[24:27], s66 offen offset:0 lds
MM v[32:35],a[236:239],v[208:211],v[32:35]
s_add_u32 s66, s66, 19456
EX v13,v13
MM v[36:39],a[252:255],v[208:211],v[128:131]
TR a[164:165],v232 offset:25600
CV v9,v10,v11
MM v[36:39],a[248:251],v[192:195],v[36:39]
EX v14,v14
MM v[36:39],a[244:247],v[176:179],v[36:39]
EX v15,v15
MM v[36:39],a[240:243],v[160:163],v[36:39]
TR a[166:167],v232 offset:29696
TR a[168:169],v233 offset:17408
MM v[44:47],a[240:243],v[164:167],v[132:135]
EX v16,v16
MM v[44:47],a[244:247],v[180:183],v[44:47]
TR a[170:171],v233 offset:21504
CV v10,v12,v13
MM v[44:47],a[248:251],v[196:199],v[44:47]
EX v17,v17
MM v[44:47],a[252:255],v[212:215],v[44:47]
s_add_u32 m0, s89, 131072
s_nop 0
buffer_load_dwordx4 v252, s[24:27], s66 offen offset:1024 lds
MM v[40:43],a[236:239],v[212:215],v[132:135]
EX v18,v18
MM v[40:43],a[232:235],v[196:199],v[40:43]
TR a[172:173],v233 offset:25600
CV v11,v14,v15
MM v[40:43],a[228:231],v[180:183],v[40:43]
EX v19,v19
MM v[40:43],a[224:227],v[164:167],v[40:43]
TR a[174:175],v233 offset:29696
TR a[176:177],v232 offset:17920
MM v[48:51],a[224:227],v[168:171],v[136:139]
EX v20,v20
MM v[48:51],a[228:231],v[184:187],v[48:51]
TR a[178:179],v232 offset:22016
CV v16,v16,v17
MM v[48:51],a[232:235],v[200:203],v[48:51]
EX v21,v21
MM v[48:51],a[236:239],v[216:219],v[48:51]
s_lshl_b32 s60, s50, 2
TR a[180:181],v232 offset:26112
CV v17,v18,v19
MM v[52:55],a[252:255],v[216:219],v[136:139]
s_add_u32 s60, s60, %[swave]
EX v22,v22
MM v[52:55],a[248:251],v[200:203],v[52:55]
s_lshl_b32 s60, s60, 1
TR a[182:183],v232 offset:30208
TR a[184:185],v233 offset:17920
MM v[52:55],a[244:247],v[184:187],v[52:55]
s_bfe_u32 s61, s49, 65538
EX v23,v23
MM v[52:55],a[240:243],v[168:171],v[52:55]
s_xor_b32 s61, s61, 1
EX v24,v24
MM v[60:63],a[240:243],v[172:175],v[140:143]
s_add_u32 s60, s60, s61
TR a[186:187],v233 offset:22016
CV v18,v20,v21
MM v[60:63],a[244:247],v[188:191],v[60:63]
s_mul_i32 s18, s60, 40960
EX v25,v25
MM v[60:63],a[248:251],v[204:207],v[60:63]
s_mul_i32 s60, s84, 320
TR a[188:189],v233 offset:26112
TR a[190:191],v233 offset:30208
MM v[60:63],a[252:255],v[220:223],v[60:63]
s_add_u32 s18, s18, s60
EX v26,v26
MM v[56:59],a[236:239],v[220:223],v[140:143]
s_lshr_b32 s62, s49, 3
CV v19,v22,v23
CV v24,v24,v25
MM v[56:59],a[232:235],v[204:207],v[56:59]
s_mul_i32 s63, s62, 10240
EX v27,v27
MM v[56:59],a[228:231],v[188:191],v[56:59]
s_add_u32 s63, s63, s18
EX v28,v28
MM v[56:59],a[224:227],v[172:175],v[56:59]
s_waitcnt vmcnt(14) lgkmcnt(0)
s_barrier
MM a[0:3],a[128:131],v[64:67],a[0:3]
s_and_b32 s60, s84, 1
EX v29,v29
MM a[0:3],a[132:135],v[68:71],a[0:3]
s_add_u32 s61, s60, s62
CV v25,v26,v27
CV v26,v28,v29
MM a[8:11],a[132:135],v[76:79],a[8:11]
s_lshl_b32 s61, s61, 6
EX v30,v30
MM a[8:11],a[128:131],v[72:75],a[8:11]
s_add_u32 s18, s18, s61
EX v31,v31
MM a[16:19],a[128:131],v[80:83],a[16:19]
s_xor_b32 s60, s60, 1
RD a[192:195],v224 offset:49152
CV v27,v30,v31
MM a[16:19],a[132:135],v[84:87],a[16:19]
s_lshl_b32 s60, s60, 8
EX v32,v32
MM a[24:27],a[132:135],v[92:95],a[24:27]
s_add_u32 s63, s63, s60
s_add_u32 m0, s65, 16384
RD a[196:199],v225 offset:49152
MM a[24:27],a[128:131],v[88:91],a[24:27]
s_mul_i32 s60, s62, 33792
EX v33,v33
MM a[28:31],a[136:139],v[88:91],a[28:31]
s_sub_u32 s60, s95, s60
buffer_load_dwordx4 v228, s[8:11], s64 offen offset:0 lds
MM a[28:31],a[140:143],v[92:95],a[28:31]
s_xor_b32 s61, s62, 1
EX v34,v34
MM a[20:23],a[140:143],v[84:87],a[20:23]
s_mul_i32 s61, s61, 33792
EX v35,v35
MM a[20:23],a[136:139],v[80:83],a[20:23]
s_add_u32 s61, s61, s60
RD a[200:203],v226 offset:49152
CV v4,v32,v33
MM a[12:15],a[136:139],v[72:75],a[12:15]
s_add_u32 s36, s61, 2048
EX v36,v36
MM a[12:15],a[140:143],v[76:79],a[12:15]
s_xor_b32 s61, s62, 2
CV v5,v34,v35
MM a[4:7],a[140:143],v[68:71],a[4:7]
s_mul_i32 s61, s61, 33792
EX v37,v37
MM a[4:7],a[136:139],v[64:67],a[4:7]
s_add_u32 s61, s61, s60
buffer_load_dwordx4 v229, s[8:11], s64 offen offset:1024 lds
MM a[32:35],a[144:147],v[64:67],a[32:35]
s_add_u32 s37, s61, 4096
EX v38,v38
MM a[32:35],a[148:151],v[68:71],a[32:35]
s_xor_b32 s61, s62, 3
RD a[204:207],v227 offset:49152
CV v6,v36,v37
MM a[40:43],a[148:151],v[76:79],a[40:43]
s_mul_i32 s61, s61, 33792
EX v39,v39
MM a[40:43],a[144:147],v[72:75],a[40:43]
s_add_u32 s61, s61, s60
RD a[208:211],v224 offset:53248
CV v7,v38,v39
MM a[48:51],a[144:147],v[80:83],a[48:51]
s_add_u32 s38, s61, 6144
EX v40,v40
MM a[48:51],a[148:151],v[84:87],a[48:51]
s_lshl_b32 s61, s62, 9
EX v41,v41
MM a[56:59],a[148:151],v[92:95],a[56:59]
s_add_u32 s39, s60, s61
RD a[212:215],v225 offset:53248
MM a[56:59],a[144:147],v[88:91],a[56:59]
s_mov_b32 s17, 15
buffer_load_dwordx4 v230, s[8:11], s64 offen offset:2048 lds
MM a[60:63],a[152:155],v[88:91],a[60:63]
s_mov_b32 s58, 0
EX v42,v42
MM a[60:63],a[156:159],v[92:95],a[60:63]
s_lshl_b32 s60, s50, 1
EX v43,v43
MM a[52:55],a[156:159],v[84:87],a[52:55]
s_bfe_u32 s61, s49, 65538
RD a[216:219],v226 offset:53248
CV v12,v40,v41
MM a[52:55],a[152:155],v[80:83],a[52:55]
s_add_u32 s60, s60, s61
EX v44,v44
MM a[44:47],a[152:155],v[72:75],a[44:47]
s_lshl_b32 s60, s60, 2
CV v13,v42,v43
MM a[44:47],a[156:159],v[76:79],a[44:47]
s_add_u32 s60, s60, s84
EX v45,v45
MM a[36:39],a[156:159],v[68:71],a[36:39]
s_lshl_b32 s60, s60, 2
buffer_load_dwordx4 v231, s[8:11], s64 offen offset:3072 lds
MM a[36:39],a[152:155],v[64:67],a[36:39]
s_add_u32 s68, s60, %[swave]
EX v46,v46
MM a[64:67],a[160:163],v[64:67],a[64:67]
s_lshl_b32 s68, s68, 6
RD a[220:223],v227 offset:53248
CV v14,v44,v45
MM a[64:67],a[164:167],v[68:71],a[64:67]
s_add_u32 s68, s68, 16384
EX v47,v47
MM a[72:75],a[164:167],v[76:79],a[72:75]
s_lshr_b32 s61, s49, 3
EX v48,v48
MM a[72:75],a[160:163],v[72:75],a[72:75]
s_lshl_b32 s19, s61, 4
RD a[224:227],v224 offset:57344
CV v15,v46,v47
MM a[80:83],a[160:163],v[80:83],a[80:83]
s_add_u32 s19, s19, s68
EX v49,v49
MM a[80:83],a[164:167],v[84:87],a[80:83]
s_lshr_b32 s96, s49, 2
s_add_u32 m0, s65, 81920
CV v20,v48,v49
MM a[88:91],a[164:167],v[92:95],a[88:91]
s_lshl_b32 s97, s50, 3
EX v50,v50
MM a[88:91],a[160:163],v[88:91],a[88:91]
s_add_u32 s96, s96, s97
buffer_load_dwordx4 v235, s[12:15], s81 offen offset:0 lds
MM a[92:95],a[168:171],v[88:91],a[92:95]
s_lshl_b32 s96, s96, 2
EX v51,v51
MM a[92:95],a[172:175],v[92:95],a[92:95]
s_add_u32 s96, s96, %[swave]
RD a[228:231],v225 offset:57344
CV v21,v50,v51
MM a[84:87],a[172:175],v[84:87],a[84:87]
s_lshl_b32 s96, s96, 6
EX v52,v52
MM a[84:87],a[168:171],v[80:83],a[84:87]
s_add_u32 s96, s96, 0
RD a[232:235],v226 offset:57344
MM a[76:79],a[168:171],v[72:75],a[76:79]
s_xor_b32 s44, s96, 256
EX v53,v53
MM a[76:79],a[172:175],v[76:79],a[76:79]
s_xor_b32 s66, s94, 5
RD a[236:239],v227 offset:57344
CV v22,v52,v53
MM a[68:71],a[172:175],v[68:71],a[68:71]
s_mul_i32 s66, s66, 163840
EX v54,v54
MM a[68:71],a[168:171],v[64:67],a[68:71]
s_add_u32 s66, s66, s88
s_add_u32 m0, s89, 139264
s_nop 0
buffer_load_dwordx4 v252, s[24:27], s66 offen offset:0 lds
MM a[96:99],a[176:179],v[64:67],a[96:99]
s_add_u32 s66, s66, 19456
EX v55,v55
MM a[96:99],a[180:183],v[68:71],a[96:99]
s_add_u32 m0, s65, 81920
s_nop 0
buffer_load_dwordx4 v235, s[12:15], s64 offen offset:1024 lds
MM a[104:107],a[180:183],v[76:79],a[104:107]
EX v56,v56
MM a[104:107],a[176:179],v[72:75],a[104:107]
RD a[240:243],v224 offset:61440
CV v23,v54,v55
MM a[112:115],a[176:179],v[80:83],a[112:115]
EX v57,v57
MM a[112:115],a[180:183],v[84:87],a[112:115]
EX v58,v58
MM a[120:123],a[180:183],v[92:95],a[120:123]
RD a[244:247],v225 offset:61440
CV v28,v56,v57
MM a[120:123],a[176:179],v[88:91],a[120:123]
EX v59,v59
MM a[124:127],a[184:187],v[88:91],a[124:127]
v_add_f32 v244, v144, v145
v_add_f32 v245, v148, v149
MM a[124:127],a[188:191],v[92:95],a[124:127]
v_add_f32 v246, v152, v153
v_add_f32 v247, v156, v157
MM a[116:119],a[188:191],v[84:87],a[116:119]
s_add_u32 m0, s89, 139264
s_nop 0
buffer_load_dwordx4 v252, s[24:27], s66 offen offset:1024 lds
MM a[116:119],a[184:187],v[80:83],a[116:119]
)ASM"
R"ASM(
EX v60,v60
MM a[108:111],a[184:187],v[72:75],a[108:111]
EX v61,v61
MM a[108:111],a[188:191],v[76:79],a[108:111]
s_add_u32 m0, s65, 81920
s_nop 0
buffer_load_dwordx4 v235, s[12:15], s81 offen offset:2048 lds
MM a[100:103],a[188:191],v[68:71],a[100:103]
EX v62,v62
MM a[100:103],a[184:187],v[64:67],a[100:103]
RD a[248:251],v226 offset:61440
CV v29,v58,v59
v_smfmac_f32_16x16x64_bf16 v[144:147], v[236:239], v[0:7], v234
RD a[252:255],v227 offset:61440
EX v63,v63
v_smfmac_f32_16x16x64_bf16 v[148:151], v[236:239], v[8:15], v234
CV v30,v60,v61
v_smfmac_f32_16x16x64_bf16 v[152:155], v[236:239], v[16:23], v234
CV v31,v62,v63
s_nop 1
v_smfmac_f32_16x16x64_bf16 v[156:159], v[236:239], v[24:31], v234
s_waitcnt lgkmcnt(0)
buffer_load_dwordx4 v235, s[12:15], s64 offen offset:3072 lds
MM v[64:67],a[192:195],v[160:163],v[128:131]
s_add_u32 s64, s64, 16384
TR a[128:129],v232 offset:32768
s_add_u32 s81, s64, 896
MM v[64:67],a[196:199],v[176:179],v[64:67]
TR a[130:131],v232 offset:36864
v_max3_f32 v249, v144, v145, v148
MM v[64:67],a[200:203],v[192:195],v[64:67]
TR a[132:133],v232 offset:40960
v_max3_f32 v249, v249, v149, v152
MM v[64:67],a[204:207],v[208:211],v[64:67]
TR a[134:135],v232 offset:45056
v_max3_f32 v249, v249, v153, v156
MM v[68:71],a[220:223],v[208:211],v[128:131]
TR a[136:137],v233 offset:32768
v_max_f32 v249, v249, v157
MM v[68:71],a[216:219],v[192:195],v[68:71]
TR a[138:139],v233 offset:36864
v_cmp_lt_f32 vcc, s71, v249
MM v[68:71],a[212:215],v[176:179],v[68:71]
TR a[140:141],v233 offset:40960
TR a[142:143],v233 offset:45056
MM v[68:71],a[208:211],v[160:163],v[68:71]
s_cbranch_vccnz LSITE34_%=
LBACK34_%=:
MM v[76:79],a[208:211],v[164:167],v[132:135]
buffer_load_dword v242, off, s[20:23], s96 sc1
MM v[76:79],a[212:215],v[180:183],v[76:79]
buffer_load_dword v243, off, s[20:23], s19 sc1
MM v[76:79],a[216:219],v[196:199],v[76:79]
s_xor_b32 s66, s94, 6
v_and_b32 v32, 496, v248
v_mul_u32_u24 v32, 80, v32
MM v[76:79],a[220:223],v[212:215],v[76:79]
s_mul_i32 s66, s66, 163840
EX v64,v64
MM v[72:75],a[204:207],v[212:215],v[132:135]
s_add_u32 s66, s66, s88
s_add_u32 m0, s89, 147456
s_nop 0
buffer_load_dwordx4 v252, s[24:27], s66 offen offset:0 lds
MM v[72:75],a[200:203],v[196:199],v[72:75]
s_add_u32 s66, s66, 19456
EX v65,v65
MM v[72:75],a[196:199],v[180:183],v[72:75]
s_sub_u32 s60, s63, s18
v_add_u32 v32, s18, v32
CV v64,v64,v65
MM v[72:75],a[192:195],v[164:167],v[72:75]
s_mov_b32 s61, 131072
s_mov_b32 exec_lo, 0
v_add_u32 v32, s60, v32
s_mov_b64 exec, -1
MM v[80:83],a[192:195],v[168:171],v[136:139]
s_mov_b32 exec_hi, 0xff
buffer_load_dword v36, v32, s[32:35], 0 offen offset:0
s_mov_b64 exec, -1
EX v66,v66
MM v[80:83],a[196:199],v[184:187],v[80:83]
EX v67,v67
MM v[80:83],a[200:203],v[200:203],v[80:83]
EX v68,v68
MM v[80:83],a[204:207],v[216:219],v[80:83]
s_mov_b32 exec_hi, 0xff
buffer_load_dword v37, v32, s[20:23], s61 offen offset:0 sc1
s_mov_b64 exec, -1
MM v[84:87],a[220:223],v[216:219],v[136:139]
TR a[144:145],v232 offset:33280
TR a[146:147],v232 offset:37376
MM v[84:87],a[216:219],v[200:203],v[84:87]
s_add_u32 m0, s89, 147456
s_nop 0
buffer_load_dwordx4 v252, s[24:27], s66 offen offset:1024 lds
EX v69,v69
MM v[84:87],a[212:215],v[184:187],v[84:87]
EX v70,v70
MM v[84:87],a[208:211],v[168:171],v[84:87]
TR a[148:149],v232 offset:41472
CV v65,v66,v67
MM v[92:95],a[208:211],v[172:175],v[140:143]
EX v71,v71
MM v[92:95],a[212:215],v[188:191],v[92:95]
TR a[150:151],v232 offset:45568
TR a[152:153],v233 offset:33280
MM v[92:95],a[216:219],v[204:207],v[92:95]
EX v72,v72
MM v[92:95],a[220:223],v[220:223],v[92:95]
EX v73,v73
MM v[88:91],a[204:207],v[220:223],v[140:143]
TR a[154:155],v233 offset:37376
CV v66,v68,v69
MM v[88:91],a[200:203],v[204:207],v[88:91]
s_xor_b32 s66, s94, 7
EX v74,v74
MM v[88:91],a[196:199],v[188:191],v[88:91]
s_mul_i32 s66, s66, 163840
TR a[156:157],v233 offset:41472
CV v67,v70,v71
MM v[88:91],a[192:195],v[172:175],v[88:91]
s_add_u32 s66, s66, s88
s_add_u32 m0, s89, 155648
s_nop 0
buffer_load_dwordx4 v252, s[24:27], s66 offen offset:0 lds
MM v[96:99],a[224:227],v[160:163],v[128:131]
s_add_u32 s66, s66, 19456
EX v75,v75
MM v[96:99],a[228:231],v[176:179],v[96:99]
TR a[158:159],v233 offset:45568
TR a[160:161],v232 offset:33792
MM v[96:99],a[232:235],v[192:195],v[96:99]
EX v76,v76
MM v[96:99],a[236:239],v[208:211],v[96:99]
EX v77,v77
MM v[100:103],a[252:255],v[208:211],v[128:131]
TR a[162:163],v232 offset:37888
CV v72,v72,v73
MM v[100:103],a[248:251],v[192:195],v[100:103]
EX v78,v78
MM v[100:103],a[244:247],v[176:179],v[100:103]
TR a[164:165],v232 offset:41984
CV v73,v74,v75
MM v[100:103],a[240:243],v[160:163],v[100:103]
EX v79,v79
MM v[108:111],a[240:243],v[164:167],v[132:135]
s_add_u32 m0, s89, 155648
s_nop 0
buffer_load_dwordx4 v252, s[24:27], s66 offen offset:1024 lds
MM v[108:111],a[244:247],v[180:183],v[108:111]
TR a[166:167],v232 offset:46080
TR a[168:169],v233 offset:33792
MM v[108:111],a[248:251],v[196:199],v[108:111]
EX v80,v80
MM v[108:111],a[252:255],v[212:215],v[108:111]
EX v81,v81
MM v[104:107],a[236:239],v[212:215],v[132:135]
TR a[170:171],v233 offset:37888
CV v74,v76,v77
MM v[104:107],a[232:235],v[196:199],v[104:107]
EX v82,v82
MM v[104:107],a[228:231],v[180:183],v[104:107]
TR a[172:173],v233 offset:41984
TR a[174:175],v233 offset:46080
MM v[104:107],a[224:227],v[164:167],v[104:107]
EX v83,v83
MM v[112:115],a[224:227],v[168:171],v[136:139]
TR a[176:177],v232 offset:34304
CV v75,v78,v79
MM v[112:115],a[228:231],v[184:187],v[112:115]
EX v84,v84
MM v[112:115],a[232:235],v[200:203],v[112:115]
EX v85,v85
MM v[112:115],a[236:239],v[216:219],v[112:115]
TR a[178:179],v232 offset:38400
CV v80,v80,v81
MM v[116:119],a[252:255],v[216:219],v[136:139]
EX v86,v86
MM v[116:119],a[248:251],v[200:203],v[116:119]
TR a[180:181],v232 offset:42496
TR a[182:183],v232 offset:46592
MM v[116:119],a[244:247],v[184:187],v[116:119]
EX v87,v87
MM v[116:119],a[240:243],v[168:171],v[116:119]
TR a[184:185],v233 offset:34304
CV v81,v82,v83
MM v[124:127],a[240:243],v[172:175],v[140:143]
EX v88,v88
MM v[124:127],a[244:247],v[188:191],v[124:127]
TR a[186:187],v233 offset:38400
CV v82,v84,v85
MM v[124:127],a[248:251],v[204:207],v[124:127]
TR a[188:189],v233 offset:42496
TR a[190:191],v233 offset:46592
EX v89,v89
MM v[124:127],a[252:255],v[220:223],v[124:127]
EX v90,v90
MM v[120:123],a[236:239],v[220:223],v[140:143]
CV v83,v86,v87
CV v88,v88,v89
MM v[120:123],a[232:235],v[204:207],v[120:123]
EX v91,v91
MM v[120:123],a[228:231],v[188:191],v[120:123]
EX v92,v92
MM v[120:123],a[224:227],v[172:175],v[120:123]
s_waitcnt vmcnt(24) lgkmcnt(0)
s_barrier
MM a[0:3],a[128:131],v[0:3],a[0:3]
s_add_u32 m0, s65, 32768
EX v93,v93
MM a[0:3],a[132:135],v[4:7],a[0:3]
CV v89,v90,v91
CV v90,v92,v93
MM a[8:11],a[132:135],v[12:15],a[8:11]
EX v94,v94
MM a[8:11],a[128:131],v[8:11],a[8:11]
EX v95,v95
MM a[16:19],a[128:131],v[16:19],a[16:19]
RD a[192:195],v224 offset:0
LPG24_%=:
s_cbranch_execz LPG28_%=
CV v91,v94,v95
MM a[16:19],a[132:135],v[20:23],a[16:19]
EX v96,v96
MM a[24:27],a[132:135],v[28:31],a[24:27]
buffer_load_dwordx4 v228, s[8:11], s64 offen offset:0 lds
MM a[24:27],a[128:131],v[24:27],a[24:27]
EX v97,v97
MM a[28:31],a[136:139],v[24:27],a[28:31]
RD a[196:199],v225 offset:0
CV v68,v96,v97
MM a[28:31],a[140:143],v[28:31],a[28:31]
EX v98,v98
MM a[20:23],a[140:143],v[20:23],a[20:23]
EX v99,v99
MM a[20:23],a[136:139],v[16:19],a[20:23]
TR a[128:129],v232 offset:49152
RD a[200:203],v226 offset:0
MM a[12:15],a[136:139],v[8:11],a[12:15]
EX v100,v100
MM a[12:15],a[140:143],v[12:15],a[12:15]
TR a[130:131],v232 offset:53248
CV v69,v98,v99
MM a[4:7],a[140:143],v[4:7],a[4:7]
EX v101,v101
MM a[4:7],a[136:139],v[0:3],a[4:7]
TR a[132:133],v232 offset:57344
RD a[204:207],v227 offset:0
MM a[32:35],a[144:147],v[0:3],a[32:35]
EX v102,v102
MM a[32:35],a[148:151],v[4:7],a[32:35]
buffer_load_dwordx4 v229, s[8:11], s64 offen offset:1024 lds
MM a[40:43],a[148:151],v[12:15],a[40:43]
TR a[134:135],v232 offset:61440
CV v70,v100,v101
MM a[40:43],a[144:147],v[8:11],a[40:43]
EX v103,v103
MM a[48:51],a[144:147],v[16:19],a[48:51]
TR a[136:137],v233 offset:49152
TR a[138:139],v233 offset:53248
MM a[48:51],a[148:151],v[20:23],a[48:51]
EX v104,v104
MM a[56:59],a[148:151],v[28:31],a[56:59]
EX v105,v105
MM a[56:59],a[144:147],v[24:27],a[56:59]
TR a[140:141],v233 offset:57344
CV v71,v102,v103
MM a[60:63],a[152:155],v[24:27],a[60:63]
EX v106,v106
MM a[60:63],a[156:159],v[28:31],a[60:63]
RD a[208:211],v224 offset:4096
TR a[142:143],v233 offset:61440
MM a[52:55],a[156:159],v[20:23],a[52:55]
EX v107,v107
MM a[52:55],a[152:155],v[16:19],a[52:55]
buffer_load_dwordx4 v230, s[8:11], s64 offen offset:2048 lds
MM a[44:47],a[152:155],v[8:11],a[44:47]
TR a[144:145],v232 offset:49664
RD a[212:215],v225 offset:4096
MM a[44:47],a[156:159],v[12:15],a[44:47]
EX v108,v108
MM a[36:39],a[156:159],v[4:7],a[36:39]
TR a[146:147],v232 offset:53760
CV v76,v104,v105
MM a[36:39],a[152:155],v[0:3],a[36:39]
EX v109,v109
MM a[64:67],a[160:163],v[0:3],a[64:67]
TR a[148:149],v232 offset:57856
CV v77,v106,v107
MM a[64:67],a[164:167],v[4:7],a[64:67]
EX v110,v110
MM a[72:75],a[164:167],v[12:15],a[72:75]
EX v111,v111
MM a[72:75],a[160:163],v[8:11],a[72:75]
TR a[150:151],v232 offset:61952
RD a[216:219],v226 offset:4096
MM a[80:83],a[160:163],v[16:19],a[80:83]
TR a[152:153],v233 offset:49664
CV v78,v108,v109
MM a[80:83],a[164:167],v[20:23],a[80:83]
EX v112,v112
MM a[88:91],a[164:167],v[28:31],a[88:91]
EX v113,v113
MM a[88:91],a[160:163],v[24:27],a[88:91]
TR a[154:155],v233 offset:53760
RD a[220:223],v227 offset:4096
MM a[92:95],a[168:171],v[24:27],a[92:95]
buffer_load_dwordx4 v231, s[8:11], s64 offen offset:3072 lds
MM a[92:95],a[172:175],v[28:31],a[92:95]
s_add_u32 m0, s65, 98304
EX v114,v114
MM a[84:87],a[172:175],v[20:23],a[84:87]
TR a[156:157],v233 offset:57856
TR a[158:159],v233 offset:61952
MM a[84:87],a[168:171],v[16:19],a[84:87]
EX v115,v115
MM a[76:79],a[168:171],v[8:11],a[76:79]
EX v116,v116
MM a[76:79],a[172:175],v[12:15],a[76:79]
TR a[160:161],v232 offset:50176
CV v79,v110,v111
MM a[68:71],a[172:175],v[4:7],a[68:71]
EX v117,v117
MM a[68:71],a[168:171],v[0:3],a[68:71]
TR a[162:163],v232 offset:54272
RD a[224:227],v224 offset:8192
MM a[96:99],a[176:179],v[0:3],a[96:99]
EX v118,v118
MM a[96:99],a[180:183],v[4:7],a[96:99]
TR a[164:165],v232 offset:58368
CV v84,v112,v113
MM a[104:107],a[180:183],v[12:15],a[104:107]
buffer_load_dwordx4 v235, s[12:15], s81 offen offset:0 lds
TR a[166:167],v232 offset:62464
EX v119,v119
MM a[104:107],a[176:179],v[8:11],a[104:107]
RD a[228:231],v225 offset:8192
TR a[168:169],v233 offset:50176
MM a[112:115],a[176:179],v[16:19],a[112:115]
EX v120,v120
MM a[112:115],a[180:183],v[20:23],a[112:115]
TR a[170:171],v233 offset:54272
CV v85,v114,v115
MM a[120:123],a[180:183],v[28:31],a[120:123]
s_waitcnt vmcnt(8)
EX v121,v121
MM a[120:123],a[176:179],v[24:27],a[120:123]
s_lshr_b32 s77, %[sb], 3
RD a[232:235],v226 offset:8192
TR a[172:173],v233 offset:58368
MM a[124:127],a[184:187],v[24:27],a[124:127]
s_lshl_b32 s77, s77, 2
v_add_f32 v244, v144, v145
v_add_f32 v245, v148, v149
EX v122,v122
MM a[124:127],a[188:191],v[28:31],a[124:127]
s_add_u32 s77, s77, %[swave]
v_add_f32 v246, v152, v153
v_add_f32 v247, v156, v157
CV v86,v116,v117
MM a[116:119],a[188:191],v[20:23],a[116:119]
s_lshl_b32 s77, s77, 9
TR a[174:175],v233 offset:62464
buffer_load_dwordx4 v235, s[12:15], s64 offen offset:1024 lds
EX v123,v123
MM a[116:119],a[184:187],v[16:19],a[116:119]
s_add_u32 s77, s77, 0
TR a[176:177],v232 offset:50688
v_lshrrev_b32 v253, 1, v248
v_add_u32 v253, s77, v253
MM a[108:111],a[184:187],v[8:11],a[108:111]
s_mov_b32 s77, LKEND_ka-LPD2_%=-16
RD a[236:239],v227 offset:8192
v_min_u32 v253, s77, v253
EX v124,v124
CV v87,v118,v119
MM a[108:111],a[188:191],v[12:15],a[108:111]
s_getpc_b64 s[98:99]
LPD2_%=:
global_load_dword v253, v253, s[98:99]
TR a[178:179],v232 offset:54784
EX v125,v125
MM a[100:103],a[188:191],v[4:7],a[100:103]
TR a[180:181],v232 offset:58880
RD a[240:243],v224 offset:12288
MM a[100:103],a[184:187],v[0:3],a[100:103]
EX v126,v126
v_smfmac_f32_16x16x64_bf16 v[144:147], v[236:239], v[64:71], v234
)ASM"
R"ASM(
RD a[244:247],v225 offset:12288
RD a[248:251],v226 offset:12288
RD a[252:255],v227 offset:12288
EX v127,v127
v_smfmac_f32_16x16x64_bf16 v[148:151], v[236:239], v[72:79], v234
TR a[182:183],v232 offset:62976
CV v92,v120,v121
v_smfmac_f32_16x16x64_bf16 v[152:155], v[236:239], v[80:87], v234
buffer_load_dwordx4 v235, s[12:15], s81 offen offset:2048 lds
CV v93,v122,v123
CV v94,v124,v125
CV v95,v126,v127
s_nop 1
v_smfmac_f32_16x16x64_bf16 v[156:159], v[236:239], v[88:95], v234
TR a[184:185],v233 offset:50688
TR a[186:187],v233 offset:54784
TR a[188:189],v233 offset:58880
TR a[190:191],v233 offset:62976
s_waitcnt lgkmcnt(5)
buffer_load_dwordx4 v235, s[12:15], s64 offen offset:3072 lds
MM v[16:19],a[192:195],v[168:171],v[136:139]
MM v[16:19],a[196:199],v[184:187],v[16:19]
MM v[16:19],a[200:203],v[200:203],v[16:19]
v_max3_f32 v249, v144, v145, v148
v_max3_f32 v249, v249, v149, v152
MM v[16:19],a[204:207],v[216:219],v[16:19]
v_max3_f32 v249, v249, v153, v156
v_max_f32 v249, v249, v157
MM v[20:23],a[220:223],v[216:219],v[136:139]
v_cmp_lt_f32 vcc, s71, v249
MM v[20:23],a[216:219],v[200:203],v[20:23]
s_cbranch_vccnz LSITE35_%=
LBACK35_%=:
v_add_f32 v244, v144, v145
MM v[20:23],a[212:215],v[184:187],v[20:23]
v_add_f32 v245, v148, v149
v_add_f32 v246, v152, v153
MM v[20:23],a[208:211],v[168:171],v[20:23]
s_waitcnt lgkmcnt(0)
v_add_f32 v247, v156, v157
MM a[0:3],a[128:131],v[64:67],a[0:3]
EX v16,v16
MM a[0:3],a[132:135],v[68:71],a[0:3]
v_rcp_f32 v145, v244
MM a[8:11],a[132:135],v[76:79],a[8:11]
EX v17,v17
MM a[8:11],a[128:131],v[72:75],a[8:11]
EX v18,v18
MM a[12:15],a[136:139],v[72:75],a[12:15]
v_rcp_f32 v149, v245
MM a[12:15],a[140:143],v[76:79],a[12:15]
CV v16,v16,v17
v_bfe_u32 v144, v241, 0, 8
MM a[4:7],a[140:143],v[68:71],a[4:7]
EX v19,v19
MM a[4:7],a[136:139],v[64:67],a[4:7]
CV v17,v18,v19
v_lshl_or_b32 v144, v144, 1, 1
MM a[32:35],a[144:147],v[64:67],a[32:35]
EX v20,v20
MM a[32:35],a[148:151],v[68:71],a[32:35]
v_lshlrev_b32 v144, 22, v144
v_mul_f32 v144, -v144, v145
MM a[40:43],a[148:151],v[76:79],a[40:43]
v_bfe_u32 v148, v241, 8, 8
v_lshl_or_b32 v148, v148, 1, 1
MM a[40:43],a[144:147],v[72:75],a[40:43]
v_lshlrev_b32 v148, 22, v148
v_mul_f32 v148, -v148, v149
MM a[44:47],a[152:155],v[72:75],a[44:47]
s_mul_i32 s78, s49, 17408
s_add_u32 s78, s83, s78
s_add_u32 s78, s78, 0
s_add_u32 s79, s78, 4096
AR v0,a0
AR v1,a1
AR v2,a2
MM a[44:47],a[156:159],v[76:79],a[44:47]
AR v3,a3
AR v4,a4
AR v5,a5
MM a[36:39],a[156:159],v[68:71],a[36:39]
AR v6,a6
AR v7,a7
v_fma_f32 v0, v0, v145, v144
v_fma_f32 v1, v1, v145, v144
MM a[36:39],a[152:155],v[64:67],a[36:39]
v_fma_f32 v2, v2, v145, v144
v_fma_f32 v3, v3, v145, v144
v_fma_f32 v4, v4, v145, v144
v_fma_f32 v5, v5, v145, v144
MM a[64:67],a[160:163],v[64:67],a[64:67]
v_fma_f32 v6, v6, v145, v144
v_fma_f32 v7, v7, v145, v144
CV v160,v0,v1
CV v161,v2,v3
MM a[64:67],a[164:167],v[68:71],a[64:67]
CV v162,v4,v5
CV v163,v6,v7
MM a[72:75],a[164:167],v[76:79],a[72:75]
buffer_store_dwordx4 v[160:163], v248, s[20:23], s78 offen offset:0 sc1
AR v8,a8
AR v9,a9
AR v10,a10
MM a[72:75],a[160:163],v[72:75],a[72:75]
AR v11,a11
AR v12,a12
AR v13,a13
EX v21,v21
AR v14,a14
MM a[76:79],a[168:171],v[72:75],a[76:79]
AR v15,a15
v_fma_f32 v8, v8, v149, v148
v_fma_f32 v9, v9, v149, v148
v_fma_f32 v10, v10, v149, v148
MM a[76:79],a[172:175],v[76:79],a[76:79]
v_fma_f32 v11, v11, v149, v148
v_fma_f32 v12, v12, v149, v148
v_fma_f32 v13, v13, v149, v148
v_fma_f32 v14, v14, v149, v148
EX v22,v22
MM a[68:71],a[172:175],v[68:71],a[68:71]
v_fma_f32 v15, v15, v149, v148
CV v164,v8,v9
CV v165,v10,v11
CV v166,v12,v13
MM a[68:71],a[168:171],v[64:67],a[68:71]
CV v167,v14,v15
CV v18,v20,v21
MM a[96:99],a[176:179],v[64:67],a[96:99]
buffer_store_dwordx4 v[164:167], v248, s[20:23], s79 offen offset:0 sc1
AR v32,a32
AR v33,a33
AR v34,a34
EX v23,v23
AR v35,a35
MM a[96:99],a[180:183],v[68:71],a[96:99]
AR v36,a36
AR v37,a37
AR v38,a38
AR v39,a39
MM a[104:107],a[180:183],v[76:79],a[104:107]
v_fma_f32 v32, v32, v145, v144
v_fma_f32 v33, v33, v145, v144
v_fma_f32 v34, v34, v145, v144
v_fma_f32 v35, v35, v145, v144
MM a[104:107],a[176:179],v[72:75],a[104:107]
v_fma_f32 v36, v36, v145, v144
v_fma_f32 v37, v37, v145, v144
v_fma_f32 v38, v38, v145, v144
MM a[108:111],a[184:187],v[72:75],a[108:111]
v_fma_f32 v39, v39, v145, v144
CV v176,v32,v33
CV v177,v34,v35
CV v178,v36,v37
MM a[108:111],a[188:191],v[76:79],a[108:111]
CV v179,v38,v39
CV v19,v22,v23
MM a[100:103],a[188:191],v[68:71],a[100:103]
buffer_store_dwordx4 v[176:179], v248, s[20:23], s78 offen offset:1024 sc1
AR v40,a40
AR v41,a41
AR v42,a42
AR v43,a43
AR v44,a44
MM a[100:103],a[184:187],v[64:67],a[100:103]
AR v45,a45
AR v46,a46
AR v47,a47
v_fma_f32 v40, v40, v149, v148
MM v[28:31],a[208:211],v[172:175],v[140:143]
v_fma_f32 v41, v41, v149, v148
v_fma_f32 v42, v42, v149, v148
v_fma_f32 v43, v43, v149, v148
MM v[28:31],a[212:215],v[188:191],v[28:31]
v_fma_f32 v44, v44, v149, v148
v_fma_f32 v45, v45, v149, v148
v_fma_f32 v46, v46, v149, v148
v_fma_f32 v47, v47, v149, v148
MM v[28:31],a[216:219],v[204:207],v[28:31]
CV v180,v40,v41
CV v181,v42,v43
CV v182,v44,v45
CV v183,v46,v47
MM v[28:31],a[220:223],v[220:223],v[28:31]
buffer_store_dwordx4 v[180:183], v248, s[20:23], s79 offen offset:1024 sc1
AR v0,a64
AR v1,a65
MM v[24:27],a[204:207],v[220:223],v[140:143]
AR v2,a66
AR v3,a67
AR v4,a68
MM v[24:27],a[200:203],v[204:207],v[24:27]
AR v5,a69
AR v6,a70
AR v7,a71
v_fma_f32 v0, v0, v145, v144
MM v[24:27],a[196:199],v[188:191],v[24:27]
v_fma_f32 v1, v1, v145, v144
v_fma_f32 v2, v2, v145, v144
v_fma_f32 v3, v3, v145, v144
v_fma_f32 v4, v4, v145, v144
MM v[24:27],a[192:195],v[172:175],v[24:27]
v_fma_f32 v5, v5, v145, v144
v_fma_f32 v6, v6, v145, v144
v_fma_f32 v7, v7, v145, v144
CV v192,v0,v1
MM v[48:51],a[224:227],v[168:171],v[136:139]
CV v193,v2,v3
CV v194,v4,v5
CV v195,v6,v7
MM v[48:51],a[228:231],v[184:187],v[48:51]
buffer_store_dwordx4 v[192:195], v248, s[20:23], s78 offen offset:2048 sc1
AR v8,a72
AR v9,a73
MM v[48:51],a[232:235],v[200:203],v[48:51]
AR v10,a74
AR v11,a75
AR v12,a76
AR v13,a77
MM v[48:51],a[236:239],v[216:219],v[48:51]
AR v14,a78
AR v15,a79
v_fma_f32 v8, v8, v149, v148
v_fma_f32 v9, v9, v149, v148
MM v[52:55],a[252:255],v[216:219],v[136:139]
EX v24,v24
EX v25,v25
EX v26,v26
EX v27,v27
EX v28,v28
EX v29,v29
v_fma_f32 v10, v10, v149, v148
v_fma_f32 v11, v11, v149, v148
EX v30,v30
v_fma_f32 v12, v12, v149, v148
v_fma_f32 v13, v13, v149, v148
MM v[52:55],a[248:251],v[200:203],v[52:55]
v_fma_f32 v14, v14, v149, v148
v_fma_f32 v15, v15, v149, v148
CV v196,v8,v9
EX v31,v31
CV v24,v24,v25
CV v197,v10,v11
MM v[52:55],a[244:247],v[184:187],v[52:55]
CV v198,v12,v13
CV v199,v14,v15
MM v[52:55],a[240:243],v[168:171],v[52:55]
buffer_store_dwordx4 v[196:199], v248, s[20:23], s79 offen offset:2048 sc1
AR v32,a96
AR v33,a97
AR v34,a98
MM v[60:63],a[240:243],v[172:175],v[140:143]
AR v35,a99
AR v36,a100
AR v37,a101
EX v48,v48
AR v38,a102
MM v[60:63],a[244:247],v[188:191],v[60:63]
AR v39,a103
v_fma_f32 v32, v32, v145, v144
v_fma_f32 v33, v33, v145, v144
v_fma_f32 v34, v34, v145, v144
CV v25,v26,v27
MM v[60:63],a[248:251],v[204:207],v[60:63]
v_fma_f32 v35, v35, v145, v144
v_fma_f32 v36, v36, v145, v144
v_fma_f32 v37, v37, v145, v144
MM v[60:63],a[252:255],v[220:223],v[60:63]
v_fma_f32 v38, v38, v145, v144
v_fma_f32 v39, v39, v145, v144
CV v208,v32,v33
EX v49,v49
CV v209,v34,v35
MM v[56:59],a[236:239],v[220:223],v[140:143]
CV v210,v36,v37
CV v211,v38,v39
MM v[56:59],a[232:235],v[204:207],v[56:59]
buffer_store_dwordx4 v[208:211], v248, s[20:23], s78 offen offset:3072 sc1
AR v40,a104
AR v41,a105
AR v42,a106
AR v43,a107
EX v50,v50
MM v[56:59],a[228:231],v[188:191],v[56:59]
AR v44,a108
AR v45,a109
AR v46,a110
AR v47,a111
CV v26,v28,v29
MM v[56:59],a[224:227],v[172:175],v[56:59]
s_waitcnt lgkmcnt(0)
s_barrier
v_fma_f32 v40, v40, v149, v148
v_fma_f32 v41, v41, v149, v148
v_fma_f32 v42, v42, v149, v148
MM a[16:19],a[128:131],v[80:83],a[16:19]
s_xor_b32 s66, s94, 4
v_fma_f32 v43, v43, v149, v148
v_fma_f32 v44, v44, v149, v148
v_fma_f32 v45, v45, v149, v148
v_fma_f32 v46, v46, v149, v148
EX v51,v51
MM a[16:19],a[132:135],v[84:87],a[16:19]
s_mul_i32 s66, s66, 163840
v_fma_f32 v47, v47, v149, v148
CV v212,v40,v41
CV v213,v42,v43
CV v214,v44,v45
MM a[24:27],a[132:135],v[92:95],a[24:27]
s_add_u32 s66, s66, s87
CV v215,v46,v47
CV v27,v30,v31
MM a[24:27],a[128:131],v[88:91],a[24:27]
s_add_u32 m0, s65, 114688
buffer_store_dwordx4 v[212:215], v248, s[20:23], s79 offen offset:3072 sc1
EX v52,v52
MM a[28:31],a[136:139],v[88:91],a[28:31]
EX v53,v53
MM a[28:31],a[140:143],v[92:95],a[28:31]
RD a[192:195],v224 offset:16384
CV v20,v48,v49
MM a[20:23],a[140:143],v[84:87],a[20:23]
buffer_load_dwordx4 v240, s[24:27], s66 offen offset:0 lds
MM a[20:23],a[136:139],v[80:83],a[20:23]
s_add_u32 s66, s66, 9216
EX v54,v54
MM a[48:51],a[144:147],v[80:83],a[48:51]
RD a[196:199],v225 offset:16384
CV v21,v50,v51
MM a[48:51],a[148:151],v[84:87],a[48:51]
EX v55,v55
MM a[56:59],a[148:151],v[92:95],a[56:59]
RD a[200:203],v226 offset:16384
RD a[204:207],v227 offset:16384
MM a[56:59],a[144:147],v[88:91],a[56:59]
buffer_load_dwordx4 v240, s[24:27], s66 offen offset:1024 lds
MM a[60:63],a[152:155],v[88:91],a[60:63]
s_add_u32 s66, s66, 9216
EX v56,v56
MM a[60:63],a[156:159],v[92:95],a[60:63]
RD a[208:211],v224 offset:20480
CV v22,v52,v53
MM a[52:55],a[156:159],v[84:87],a[52:55]
buffer_load_dwordx4 v240, s[24:27], s66 offen offset:2048 lds
MM a[52:55],a[152:155],v[80:83],a[52:55]
s_add_u32 s66, s66, 9216
EX v57,v57
MM a[80:83],a[160:163],v[80:83],a[80:83]
RD a[212:215],v225 offset:20480
RD a[216:219],v226 offset:20480
MM a[80:83],a[164:167],v[84:87],a[80:83]
buffer_load_dwordx4 v240, s[24:27], s66 offen offset:3072 lds
MM a[88:91],a[164:167],v[92:95],a[88:91]
s_xor_b32 s66, s94, 4
EX v58,v58
MM a[88:91],a[160:163],v[88:91],a[88:91]
s_mul_i32 s66, s66, 163840
RD a[220:223],v227 offset:20480
CV v23,v54,v55
MM a[92:95],a[168:171],v[88:91],a[92:95]
s_add_u32 s66, s66, s86
EX v59,v59
MM a[92:95],a[172:175],v[92:95],a[92:95]
s_add_u32 m0, s65, 49152
RD a[224:227],v224 offset:24576
RD a[228:231],v225 offset:24576
MM a[84:87],a[172:175],v[84:87],a[84:87]
buffer_load_dwordx4 v240, s[24:27], s66 offen offset:0 lds
MM a[84:87],a[168:171],v[80:83],a[84:87]
s_add_u32 s66, s66, 9216
EX v60,v60
MM a[112:115],a[176:179],v[80:83],a[112:115]
RD a[232:235],v226 offset:24576
CV v28,v56,v57
MM a[112:115],a[180:183],v[84:87],a[112:115]
EX v61,v61
MM a[120:123],a[180:183],v[92:95],a[120:123]
buffer_load_dwordx4 v240, s[24:27], s66 offen offset:1024 lds
MM a[120:123],a[176:179],v[88:91],a[120:123]
s_add_u32 s66, s66, 9216
RD a[236:239],v227 offset:24576
RD a[240:243],v224 offset:28672
MM a[124:127],a[184:187],v[88:91],a[124:127]
EX v62,v62
MM a[124:127],a[188:191],v[92:95],a[124:127]
buffer_load_dwordx4 v240, s[24:27], s66 offen offset:2048 lds
MM a[116:119],a[188:191],v[84:87],a[116:119]
s_add_u32 s66, s66, 9216
RD a[244:247],v225 offset:28672
RD a[248:251],v226 offset:28672
RD a[252:255],v227 offset:28672
EX v63,v63
MM a[116:119],a[184:187],v[80:83],a[116:119]
buffer_load_dwordx4 v240, s[24:27], s66 offen offset:3072 lds
v_smfmac_f32_16x16x64_bf16 v[152:155], v[236:239], v[16:23], v234
s_nop 0
LPG25_%=:
s_cbranch_execz LPG29_%=
CV v29,v58,v59
CV v30,v60,v61
CV v31,v62,v63
s_nop 1
v_smfmac_f32_16x16x64_bf16 v[156:159], v[236:239], v[24:31], v234
s_waitcnt lgkmcnt(0)
MM v[80:83],a[192:195],v[168:171],v[136:139]
TR a[128:129],v232 offset:0
TR a[130:131],v232 offset:4096
MM v[80:83],a[196:199],v[184:187],v[80:83]
TR a[132:133],v232 offset:8192
TR a[134:135],v232 offset:12288
MM v[80:83],a[200:203],v[200:203],v[80:83]
TR a[136:137],v233 offset:0
TR a[138:139],v233 offset:4096
MM v[80:83],a[204:207],v[216:219],v[80:83]
v_max3_f32 v249, v152, v153, v156
v_max_f32 v249, v249, v157
MM v[84:87],a[220:223],v[216:219],v[136:139]
TR a[140:141],v233 offset:8192
v_cmp_lt_f32 vcc, s71, v249
MM v[84:87],a[216:219],v[200:203],v[84:87]
s_cbranch_vccnz LSITE36_%=
LBACK36_%=:
TR a[142:143],v233 offset:12288
v_add_f32 v246, v152, v153
MM v[84:87],a[212:215],v[184:187],v[84:87]
)ASM"
R"ASM(
TR a[144:145],v232 offset:512
v_add_f32 v247, v156, v157
MM v[84:87],a[208:211],v[168:171],v[84:87]
TR a[146:147],v232 offset:4608
TR a[148:149],v232 offset:8704
MM v[92:95],a[208:211],v[172:175],v[140:143]
TR a[150:151],v232 offset:12800
TR a[152:153],v233 offset:512
MM v[92:95],a[212:215],v[188:191],v[92:95]
EX v80,v80
MM v[92:95],a[216:219],v[204:207],v[92:95]
v_lshrrev_b32 v98, 1, v248
v_min_u32 v98, 312, v98
MM v[92:95],a[220:223],v[220:223],v[92:95]
s_mov_b32 s62, 94464
buffer_load_dwordx2 v[96:97], v98, s[20:23], s62 offen offset:0 sc1
MM v[88:91],a[204:207],v[220:223],v[140:143]
EX v81,v81
MM v[88:91],a[200:203],v[204:207],v[88:91]
TR a[154:155],v233 offset:4608
TR a[156:157],v233 offset:8704
MM v[88:91],a[196:199],v[188:191],v[88:91]
s_lshl_b32 s97, s84, 2
s_add_u32 s97, s97, s96
s_add_u32 s97, s97, 0
MM v[88:91],a[192:195],v[172:175],v[88:91]
s_waitcnt vmcnt(9)
v_mov_b32 v242, s69
s_mov_b64 exec, 1
buffer_store_dword v242, off, s[20:23], s97 offset:0 sc1
s_mov_b64 exec, -1
TR a[158:159],v233 offset:12800
EX v82,v82
MM v[112:115],a[224:227],v[168:171],v[136:139]
TR a[160:161],v232 offset:1024
TR a[162:163],v232 offset:5120
EX v83,v83
MM v[112:115],a[228:231],v[184:187],v[112:115]
TR a[164:165],v232 offset:9216
TR a[166:167],v232 offset:13312
MM v[112:115],a[232:235],v[200:203],v[112:115]
EX v84,v84
MM v[112:115],a[236:239],v[216:219],v[112:115]
TR a[168:169],v233 offset:1024
TR a[170:171],v233 offset:5120
MM v[116:119],a[252:255],v[216:219],v[136:139]
EX v85,v85
MM v[116:119],a[248:251],v[200:203],v[116:119]
TR a[172:173],v233 offset:9216
TR a[174:175],v233 offset:13312
MM v[116:119],a[244:247],v[184:187],v[116:119]
TR a[176:177],v232 offset:1536
EX v86,v86
MM v[116:119],a[240:243],v[168:171],v[116:119]
TR a[178:179],v232 offset:5632
CV v80,v80,v81
EX v87,v87
MM v[124:127],a[240:243],v[172:175],v[140:143]
TR a[180:181],v232 offset:9728
TR a[182:183],v232 offset:13824
MM v[124:127],a[244:247],v[188:191],v[124:127]
EX v88,v88
MM v[124:127],a[248:251],v[204:207],v[124:127]
TR a[184:185],v233 offset:1536
TR a[186:187],v233 offset:5632
TR a[188:189],v233 offset:9728
TR a[190:191],v233 offset:13824
EX v89,v89
MM v[124:127],a[252:255],v[220:223],v[124:127]
CV v81,v82,v83
CV v82,v84,v85
MM v[120:123],a[236:239],v[220:223],v[140:143]
EX v90,v90
MM v[120:123],a[232:235],v[204:207],v[120:123]
EX v91,v91
MM v[120:123],a[228:231],v[188:191],v[120:123]
CV v83,v86,v87
CV v88,v88,v89
MM v[120:123],a[224:227],v[172:175],v[120:123]
s_waitcnt lgkmcnt(0)
s_barrier
CV v89,v90,v91
EX v92,v92
MM a[16:19],a[128:131],v[16:19],a[16:19]
s_xor_b32 s66, s94, 5
EX v93,v93
MM a[16:19],a[132:135],v[20:23],a[16:19]
s_mul_i32 s66, s66, 163840
EX v94,v94
MM a[24:27],a[132:135],v[28:31],a[24:27]
s_add_u32 s66, s66, s87
EX v95,v95
MM a[24:27],a[128:131],v[24:27],a[24:27]
s_add_u32 m0, s65, 65536
RD a[192:195],v224 offset:32768
CV v90,v92,v93
MM a[28:31],a[136:139],v[24:27],a[28:31]
EX v112,v112
MM a[28:31],a[140:143],v[28:31],a[28:31]
buffer_load_dwordx4 v240, s[24:27], s66 offen offset:0 lds
MM a[20:23],a[140:143],v[20:23],a[20:23]
s_add_u32 s66, s66, 9216
EX v113,v113
MM a[20:23],a[136:139],v[16:19],a[20:23]
RD a[196:199],v225 offset:32768
CV v91,v94,v95
MM a[48:51],a[144:147],v[16:19],a[48:51]
EX v114,v114
MM a[48:51],a[148:151],v[20:23],a[48:51]
RD a[200:203],v226 offset:32768
RD a[204:207],v227 offset:32768
MM a[56:59],a[148:151],v[28:31],a[56:59]
EX v115,v115
MM a[56:59],a[144:147],v[24:27],a[56:59]
buffer_load_dwordx4 v240, s[24:27], s66 offen offset:1024 lds
MM a[60:63],a[152:155],v[24:27],a[60:63]
s_add_u32 s66, s66, 9216
EX v116,v116
MM a[60:63],a[156:159],v[28:31],a[60:63]
RD a[208:211],v224 offset:36864
CV v84,v112,v113
MM a[52:55],a[156:159],v[20:23],a[52:55]
EX v117,v117
MM a[52:55],a[152:155],v[16:19],a[52:55]
buffer_load_dwordx4 v240, s[24:27], s66 offen offset:2048 lds
MM a[80:83],a[160:163],v[16:19],a[80:83]
s_add_u32 s66, s66, 9216
EX v118,v118
MM a[80:83],a[164:167],v[20:23],a[80:83]
RD a[212:215],v225 offset:36864
RD a[216:219],v226 offset:36864
MM a[88:91],a[164:167],v[28:31],a[88:91]
EX v119,v119
MM a[88:91],a[160:163],v[24:27],a[88:91]
buffer_load_dwordx4 v240, s[24:27], s66 offen offset:3072 lds
EX v120,v120
MM a[92:95],a[168:171],v[24:27],a[92:95]
s_xor_b32 s66, s94, 5
RD a[220:223],v227 offset:36864
CV v85,v114,v115
MM a[92:95],a[172:175],v[28:31],a[92:95]
s_mul_i32 s66, s66, 163840
EX v121,v121
MM a[84:87],a[172:175],v[20:23],a[84:87]
s_add_u32 s66, s66, s86
RD a[224:227],v224 offset:40960
v_lshrrev_b32 v243, 2, v248
v_min_u32 v243, 12, v243
MM a[84:87],a[168:171],v[16:19],a[84:87]
s_add_u32 m0, s65, 0
buffer_load_dword v242, v243, s[20:23], s44 offen offset:0 sc1
EX v122,v122
MM a[112:115],a[176:179],v[16:19],a[112:115]
buffer_load_dwordx4 v240, s[24:27], s66 offen offset:0 lds
RD a[228:231],v225 offset:40960
EX v123,v123
CV v86,v116,v117
MM a[112:115],a[180:183],v[20:23],a[112:115]
s_add_u32 s66, s66, 9216
RD a[232:235],v226 offset:40960
CV v87,v118,v119
MM a[120:123],a[180:183],v[28:31],a[120:123]
EX v124,v124
MM a[120:123],a[176:179],v[24:27],a[120:123]
buffer_load_dwordx4 v240, s[24:27], s66 offen offset:1024 lds
MM a[124:127],a[184:187],v[24:27],a[124:127]
s_add_u32 s66, s66, 9216
RD a[236:239],v227 offset:40960
EX v125,v125
MM a[124:127],a[188:191],v[28:31],a[124:127]
EX v126,v126
MM a[116:119],a[188:191],v[20:23],a[116:119]
RD a[240:243],v224 offset:45056
RD a[244:247],v225 offset:45056
RD a[248:251],v226 offset:45056
RD a[252:255],v227 offset:45056
EX v127,v127
MM a[116:119],a[184:187],v[16:19],a[116:119]
buffer_load_dwordx4 v240, s[24:27], s66 offen offset:2048 lds
CV v92,v120,v121
v_smfmac_f32_16x16x64_bf16 v[152:155], v[236:239], v[80:87], v234
s_add_u32 s66, s66, 9216
CV v93,v122,v123
CV v94,v124,v125
CV v95,v126,v127
s_nop 1
v_smfmac_f32_16x16x64_bf16 v[156:159], v[236:239], v[88:95], v234
s_waitcnt lgkmcnt(0)
buffer_load_dwordx4 v240, s[24:27], s66 offen offset:3072 lds
v_lshrrev_b32 v145, 9, v248
v_mul_u32_u24 v145, 1536, v145
v_sub_u32 v244, v232, v145
v_sub_u32 v235, v233, v145
v_add_u32 v244, 65536, v244
v_add_u32 v235, 65536, v235
v_and_b32 v145, 960, v248
v_lshlrev_b32 v145, 1, v145
v_bfe_u32 v146, v248, 7, 2
v_lshl_add_u32 v145, v146, 5, v145
v_bfe_u32 v146, v248, 4, 2
v_lshl_add_u32 v145, v146, 3, v145
v_add_u32 v228, %[slds], v145
v_xor_b32 v229, 32, v228
v_xor_b32 v230, 64, v228
v_xor_b32 v231, 96, v228
v_add_u32 v144, 65536, v228
v_add_u32 v145, 65536, v229
v_add_u32 v146, 65536, v230
v_add_u32 v147, 65536, v231
MM v[16:19],a[192:195],v[168:171],v[136:139]
TR a[128:129],v232 offset:16384
TR a[130:131],v232 offset:20480
MM v[16:19],a[196:199],v[184:187],v[16:19]
TR a[132:133],v232 offset:24576
TR a[134:135],v232 offset:28672
MM v[16:19],a[200:203],v[200:203],v[16:19]
TR a[136:137],v233 offset:16384
TR a[138:139],v233 offset:20480
MM v[16:19],a[204:207],v[216:219],v[16:19]
v_max3_f32 v249, v152, v153, v156
v_max_f32 v249, v249, v157
MM v[20:23],a[220:223],v[216:219],v[136:139]
TR a[140:141],v233 offset:24576
v_cmp_lt_f32 vcc, s71, v249
MM v[20:23],a[216:219],v[200:203],v[20:23]
s_cbranch_vccnz LSITE37_%=
LBACK37_%=:
TR a[142:143],v233 offset:28672
v_add_f32 v246, v152, v153
MM v[20:23],a[212:215],v[184:187],v[20:23]
TR a[144:145],v232 offset:16896
v_add_f32 v247, v156, v157
MM v[20:23],a[208:211],v[168:171],v[20:23]
TR a[146:147],v232 offset:20992
TR a[148:149],v232 offset:25088
MM v[28:31],a[208:211],v[172:175],v[140:143]
TR a[150:151],v232 offset:29184
TR a[152:153],v233 offset:16896
MM v[28:31],a[212:215],v[188:191],v[28:31]
EX v16,v16
MM v[28:31],a[216:219],v[204:207],v[28:31]
s_waitcnt vmcnt(10)
v_xor_b32 v96, s69, v96
v_xor_b32 v97, s69, v97
MM v[28:31],a[220:223],v[220:223],v[28:31]
TR a[154:155],v233 offset:20992
v_or_b32 v99, v96, v97
MM v[24:27],a[204:207],v[220:223],v[140:143]
v_cmp_ne_u32 vcc, 0, v99
s_cbranch_vccnz LRPW_%=
LRPWb_%=:
MM v[24:27],a[200:203],v[204:207],v[24:27]
EX v17,v17
MM v[24:27],a[196:199],v[188:191],v[24:27]
TR a[156:157],v233 offset:25088
TR a[158:159],v233 offset:29184
MM v[24:27],a[192:195],v[172:175],v[24:27]
EX v18,v18
MM v[48:51],a[224:227],v[168:171],v[136:139]
TR a[160:161],v232 offset:17408
TR a[162:163],v232 offset:21504
EX v19,v19
MM v[48:51],a[228:231],v[184:187],v[48:51]
TR a[164:165],v232 offset:25600
TR a[166:167],v232 offset:29696
MM v[48:51],a[232:235],v[200:203],v[48:51]
EX v20,v20
MM v[48:51],a[236:239],v[216:219],v[48:51]
TR a[168:169],v233 offset:17408
TR a[170:171],v233 offset:21504
MM v[52:55],a[252:255],v[216:219],v[136:139]
EX v21,v21
MM v[52:55],a[248:251],v[200:203],v[52:55]
TR a[172:173],v233 offset:25600
TR a[174:175],v233 offset:29696
MM v[52:55],a[244:247],v[184:187],v[52:55]
s_waitcnt vmcnt(4)
TR a[176:177],v232 offset:17920
v_cmp_ne_u32 vcc, s69, v242
s_cbranch_vccnz LRPO0_%=
LRPO0b_%=:
EX v22,v22
MM v[52:55],a[240:243],v[168:171],v[52:55]
s_xor_b32 s92, s49, 4
s_mul_i32 s92, s92, 17408
s_add_u32 s92, s83, s92
TR a[178:179],v232 offset:22016
EX v23,v23
MM v[60:63],a[240:243],v[172:175],v[140:143]
buffer_load_dwordx4 a[0:3], v248, s[20:23], s92 offen offset:0 sc1
TR a[180:181],v232 offset:26112
CV v16,v16,v17
MM v[60:63],a[244:247],v[188:191],v[60:63]
buffer_load_dwordx4 a[32:35], v248, s[20:23], s92 offen offset:1024 sc1
TR a[182:183],v232 offset:30208
EX v24,v24
MM v[60:63],a[248:251],v[204:207],v[60:63]
TR a[184:185],v233 offset:17920
TR a[186:187],v233 offset:22016
TR a[188:189],v233 offset:26112
TR a[190:191],v233 offset:30208
EX v25,v25
MM v[60:63],a[252:255],v[220:223],v[60:63]
EX v26,v26
CV v17,v18,v19
MM v[56:59],a[236:239],v[220:223],v[140:143]
buffer_load_dwordx4 a[64:67], v248, s[20:23], s92 offen offset:2048 sc1
MM v[56:59],a[232:235],v[204:207],v[56:59]
EX v27,v27
MM v[56:59],a[228:231],v[188:191],v[56:59]
EX v28,v28
CV v18,v20,v21
MM v[56:59],a[224:227],v[172:175],v[56:59]
s_waitcnt lgkmcnt(0)
s_barrier
CV v19,v22,v23
MM a[16:19],a[128:131],v[80:83],a[16:19]
s_xor_b32 s66, s94, 6
buffer_load_dwordx4 a[96:99], v248, s[20:23], s92 offen offset:3072 sc1
MM a[16:19],a[132:135],v[84:87],a[16:19]
s_add_u32 s92, s92, 4096
EX v29,v29
MM a[24:27],a[132:135],v[92:95],a[24:27]
s_mul_i32 s66, s66, 163840
buffer_load_dwordx4 a[4:7], v248, s[20:23], s92 offen offset:0 sc1
MM a[24:27],a[128:131],v[88:91],a[24:27]
s_add_u32 s66, s66, s87
EX v30,v30
MM a[28:31],a[136:139],v[88:91],a[28:31]
s_add_u32 m0, s65, 81920
EX v31,v31
MM a[28:31],a[140:143],v[92:95],a[28:31]
buffer_load_dwordx4 a[36:39], v248, s[20:23], s92 offen offset:1024 sc1
CV v24,v24,v25
MM a[20:23],a[140:143],v[84:87],a[20:23]
EX v48,v48
MM a[20:23],a[136:139],v[80:83],a[20:23]
CV v25,v26,v27
CV v26,v28,v29
MM a[48:51],a[144:147],v[80:83],a[48:51]
buffer_load_dwordx4 a[68:71], v248, s[20:23], s92 offen offset:2048 sc1
EX v49,v49
MM a[48:51],a[148:151],v[84:87],a[48:51]
EX v50,v50
MM a[56:59],a[148:151],v[92:95],a[56:59]
EX v51,v51
MM a[56:59],a[144:147],v[88:91],a[56:59]
buffer_load_dwordx4 a[100:103], v248, s[20:23], s92 offen offset:3072 sc1
MM a[60:63],a[152:155],v[88:91],a[60:63]
TR a[128:129],v232 offset:32768
CV v27,v30,v31
MM a[60:63],a[156:159],v[92:95],a[60:63]
TR a[130:131],v232 offset:36864
EX v52,v52
MM a[52:55],a[156:159],v[84:87],a[52:55]
buffer_load_dwordx4 v240, s[24:27], s66 offen offset:0 lds
s_add_u32 s66, s66, 9216
TR a[132:133],v232 offset:40960
EX v53,v53
MM a[52:55],a[152:155],v[80:83],a[52:55]
TR a[134:135],v232 offset:45056
TR a[136:137],v233 offset:32768
MM a[80:83],a[160:163],v[80:83],a[80:83]
TR a[138:139],v233 offset:36864
TR a[140:141],v233 offset:40960
EX v54,v54
MM a[80:83],a[164:167],v[84:87],a[80:83]
buffer_load_dwordx4 v240, s[24:27], s66 offen offset:1024 lds
s_add_u32 s66, s66, 9216
TR a[142:143],v233 offset:45056
MM a[88:91],a[164:167],v[92:95],a[88:91]
TR a[144:145],v232 offset:33280
EX v55,v55
CV v20,v48,v49
MM a[88:91],a[160:163],v[88:91],a[88:91]
TR a[146:147],v232 offset:37376
TR a[148:149],v232 offset:41472
EX v56,v56
MM a[92:95],a[168:171],v[88:91],a[92:95]
buffer_load_dwordx4 v240, s[24:27], s66 offen offset:2048 lds
s_add_u32 s66, s66, 9216
TR a[150:151],v232 offset:45568
MM a[92:95],a[172:175],v[92:95],a[92:95]
)ASM"
R"ASM(
TR a[152:153],v233 offset:33280
EX v57,v57
CV v21,v50,v51
MM a[84:87],a[172:175],v[84:87],a[84:87]
TR a[154:155],v233 offset:37376
TR a[156:157],v233 offset:41472
MM a[84:87],a[168:171],v[80:83],a[84:87]
buffer_load_dwordx4 v240, s[24:27], s66 offen offset:3072 lds
TR a[158:159],v233 offset:45568
EX v58,v58
MM a[112:115],a[176:179],v[80:83],a[112:115]
s_xor_b32 s66, s94, 6
s_mul_i32 s66, s66, 163840
s_add_u32 s66, s66, s86
TR a[160:161],v232 offset:33792
s_add_u32 m0, s65, 16384
EX v59,v59
CV v22,v52,v53
MM a[112:115],a[180:183],v[84:87],a[112:115]
TR a[162:163],v232 offset:37888
TR a[164:165],v232 offset:41984
MM a[120:123],a[180:183],v[92:95],a[120:123]
buffer_load_dwordx4 v240, s[24:27], s66 offen offset:0 lds
TR a[166:167],v232 offset:46080
EX v60,v60
MM a[120:123],a[176:179],v[88:91],a[120:123]
s_add_u32 s66, s66, 9216
TR a[168:169],v233 offset:33792
TR a[170:171],v233 offset:37888
CV v23,v54,v55
MM a[124:127],a[184:187],v[88:91],a[124:127]
TR a[172:173],v233 offset:41984
EX v61,v61
MM a[124:127],a[188:191],v[92:95],a[124:127]
buffer_load_dwordx4 v240, s[24:27], s66 offen offset:1024 lds
TR a[174:175],v233 offset:46080
EX v62,v62
MM a[116:119],a[188:191],v[84:87],a[116:119]
s_add_u32 s66, s66, 9216
TR a[176:177],v232 offset:34304
TR a[178:179],v232 offset:38400
EX v63,v63
MM a[116:119],a[184:187],v[80:83],a[116:119]
TR a[180:181],v232 offset:42496
CV v28,v56,v57
v_smfmac_f32_16x16x64_bf16 v[152:155], v[236:239], v[16:23], v234
s_xor_b32 s92, s49, 5
s_mul_i32 s92, s92, 17408
buffer_load_dwordx4 v240, s[24:27], s66 offen offset:2048 lds
s_add_u32 s66, s66, 9216
TR a[182:183],v232 offset:46592
CV v29,v58,v59
CV v30,v60,v61
CV v31,v62,v63
s_nop 1
v_smfmac_f32_16x16x64_bf16 v[156:159], v[236:239], v[24:31], v234
TR a[184:185],v233 offset:34304
TR a[186:187],v233 offset:38400
TR a[188:189],v233 offset:42496
TR a[190:191],v233 offset:46592
s_waitcnt lgkmcnt(0)
buffer_load_dwordx4 v240, s[24:27], s66 offen offset:3072 lds
s_nop 6
v_max3_f32 v249, v152, v153, v156
v_max_f32 v249, v249, v157
v_cmp_lt_f32 vcc, s71, v249
s_cbranch_vccnz LSITE38_%=
LBACK38_%=:
v_add_f32 v246, v152, v153
v_add_f32 v247, v156, v157
s_waitcnt lgkmcnt(0)
MM a[16:19],a[128:131],v[16:19],a[16:19]
s_add_u32 s92, s83, s92
s_barrier
MM a[16:19],a[132:135],v[20:23],a[16:19]
buffer_load_dwordx4 a[8:11], v248, s[20:23], s92 offen offset:0 sc1
MM a[24:27],a[132:135],v[28:31],a[24:27]
v_rcp_f32 v153, v246
MM a[24:27],a[128:131],v[24:27],a[24:27]
v_rcp_f32 v157, v247
MM a[28:31],a[136:139],v[24:27],a[28:31]
v_bfe_u32 v152, v241, 16, 8
v_lshl_or_b32 v152, v152, 1, 1
MM a[28:31],a[140:143],v[28:31],a[28:31]
buffer_load_dwordx4 a[40:43], v248, s[20:23], s92 offen offset:1024 sc1
MM a[20:23],a[140:143],v[20:23],a[20:23]
v_lshlrev_b32 v152, 22, v152
v_mul_f32 v152, -v152, v153
MM a[20:23],a[136:139],v[16:19],a[20:23]
v_bfe_u32 v156, v241, 24, 8
v_lshl_or_b32 v156, v156, 1, 1
MM a[48:51],a[144:147],v[16:19],a[48:51]
TR a[128:129],v244 offset:0
TR a[130:131],v244 offset:1024
MM a[48:51],a[148:151],v[20:23],a[48:51]
buffer_load_dwordx4 a[72:75], v248, s[20:23], s92 offen offset:2048 sc1
MM a[56:59],a[148:151],v[28:31],a[56:59]
TR a[132:133],v244 offset:2048
TR a[134:135],v244 offset:3072
MM a[56:59],a[144:147],v[24:27],a[56:59]
v_lshlrev_b32 v156, 22, v156
v_mul_f32 v156, -v156, v157
MM a[60:63],a[152:155],v[24:27],a[60:63]
s_mul_i32 s78, s49, 17408
TR a[136:137],v244 offset:4096
TR a[138:139],v244 offset:5120
MM a[60:63],a[156:159],v[28:31],a[60:63]
s_add_u32 s78, s83, s78
buffer_load_dwordx4 a[104:107], v248, s[20:23], s92 offen offset:3072 sc1
MM a[52:55],a[156:159],v[20:23],a[52:55]
s_add_u32 s78, s78, 8192
TR a[140:141],v244 offset:6144
TR a[142:143],v244 offset:7168
MM a[52:55],a[152:155],v[16:19],a[52:55]
s_add_u32 s92, s92, 4096
s_add_u32 s79, s78, 4096
AR v80,a16
MM a[80:83],a[160:163],v[16:19],a[80:83]
TR a[144:145],v235 offset:0
TR a[146:147],v235 offset:1024
MM a[80:83],a[164:167],v[20:23],a[80:83]
buffer_load_dwordx4 a[12:15], v248, s[20:23], s92 offen offset:0 sc1
MM a[88:91],a[164:167],v[28:31],a[88:91]
TR a[148:149],v235 offset:2048
TR a[150:151],v235 offset:3072
AR v81,a17
MM a[88:91],a[160:163],v[24:27],a[88:91]
AR v82,a18
AR v83,a19
MM a[92:95],a[168:171],v[24:27],a[92:95]
TR a[152:153],v235 offset:4096
TR a[154:155],v235 offset:5120
AR v84,a20
AR v85,a21
MM a[92:95],a[172:175],v[28:31],a[92:95]
s_xor_b32 s66, s94, 7
buffer_load_dwordx4 a[44:47], v248, s[20:23], s92 offen offset:1024 sc1
AR v86,a22
MM a[84:87],a[172:175],v[20:23],a[84:87]
s_mul_i32 s66, s66, 163840
TR a[156:157],v235 offset:6144
TR a[158:159],v235 offset:7168
AR v87,a23
v_fma_f32 v80, v80, v153, v152
MM a[84:87],a[168:171],v[16:19],a[84:87]
s_add_u32 s66, s66, s87
v_fma_f32 v81, v81, v153, v152
v_fma_f32 v82, v82, v153, v152
MM a[112:115],a[176:179],v[16:19],a[112:115]
s_add_u32 m0, s65, 98304
TR a[160:161],v144 offset:49152
TR a[162:163],v144 offset:51200
v_fma_f32 v83, v83, v153, v152
MM a[112:115],a[180:183],v[20:23],a[112:115]
buffer_load_dwordx4 a[76:79], v248, s[20:23], s92 offen offset:2048 sc1
v_fma_f32 v84, v84, v153, v152
v_fma_f32 v85, v85, v153, v152
MM a[120:123],a[180:183],v[28:31],a[120:123]
TR a[164:165],v144 offset:53248
TR a[166:167],v144 offset:55296
v_fma_f32 v86, v86, v153, v152
v_fma_f32 v87, v87, v153, v152
MM a[120:123],a[176:179],v[24:27],a[120:123]
CV v168,v80,v81
CV v169,v82,v83
MM a[124:127],a[184:187],v[24:27],a[124:127]
TR a[168:169],v144 offset:57344
TR a[170:171],v144 offset:59392
CV v170,v84,v85
MM a[124:127],a[188:191],v[28:31],a[124:127]
buffer_load_dwordx4 a[108:111], v248, s[20:23], s92 offen offset:3072 sc1
CV v171,v86,v87
MM a[116:119],a[188:191],v[20:23],a[116:119]
TR a[172:173],v144 offset:61440
TR a[174:175],v144 offset:63488
s_nop 0
LPG26_%=:
s_cbranch_execz LPG30_%=
MM a[116:119],a[184:187],v[16:19],a[116:119]
s_waitcnt vmcnt(20)
s_waitcnt lgkmcnt(15)
MM v[128:131],a[128:131],a[0:3],0
TR a[176:177],v145 offset:49152
TR a[178:179],v145 offset:51200
MM v[128:131],a[132:135],a[32:35],v[128:131]
buffer_store_dwordx4 v[168:171], v248, s[20:23], s78 offen offset:0 sc1
AR v88,a24
AR v89,a25
AR v90,a26
AR v91,a27
AR v92,a28
AR v93,a29
MM v[128:131],a[136:139],a[64:67],v[128:131]
TR a[180:181],v145 offset:53248
TR a[182:183],v145 offset:55296
AR v94,a30
v_bfe_u32 v249, v248, 6, 2
MM v[128:131],a[140:143],a[96:99],v[128:131]
s_waitcnt vmcnt(17)
v_lshlrev_b32 v249, 8, v249
v_lshrrev_b32 v250, 8, v248
AR v95,a31
v_fma_f32 v88, v88, v157, v156
MM v[136:139],a[140:143],a[100:103],0
TR a[184:185],v145 offset:57344
TR a[186:187],v145 offset:59392
v_fma_f32 v89, v89, v157, v156
v_fma_f32 v90, v90, v157, v156
MM v[136:139],a[136:139],a[68:71],v[136:139]
v_lshl_add_u32 v249, v250, 6, v249
v_and_b32 v251, 48, v248
v_fma_f32 v91, v91, v157, v156
MM v[136:139],a[132:135],a[36:39],v[136:139]
TR a[188:189],v145 offset:61440
TR a[190:191],v145 offset:63488
v_fma_f32 v92, v92, v157, v156
v_fma_f32 v93, v93, v157, v156
MM v[136:139],a[128:131],a[4:7],v[136:139]
s_waitcnt lgkmcnt(15)
buffer_load_dwordx4 v240, s[24:27], s66 offen offset:0 lds
v_add_u32 v249, v249, v251
v_fma_f32 v94, v94, v157, v156
v_fma_f32 v95, v95, v157, v156
MM v[140:143],a[144:147],a[4:7],0
s_add_u32 s66, s66, 9216
TR a[128:129],v146 offset:49152
TR a[130:131],v146 offset:51200
CV v172,v88,v89
CV v173,v90,v91
MM v[140:143],a[148:151],a[36:39],v[140:143]
s_lshr_b32 s60, s49, 3
CV v174,v92,v93
CV v175,v94,v95
MM v[140:143],a[152:155],a[68:71],v[140:143]
buffer_store_dwordx4 v[172:175], v248, s[20:23], s79 offen offset:0 sc1
TR a[132:133],v146 offset:53248
TR a[134:135],v146 offset:55296
MM v[140:143],a[156:159],a[100:103],v[140:143]
AR v112,a48
AR v113,a49
MM v[132:135],a[156:159],a[96:99],0
TR a[136:137],v146 offset:57344
TR a[138:139],v146 offset:59392
MM v[132:135],a[152:155],a[64:67],v[132:135]
buffer_load_dwordx4 v240, s[24:27], s66 offen offset:1024 lds
AR v114,a50
AR v115,a51
MM v[132:135],a[148:151],a[32:35],v[132:135]
s_add_u32 s66, s66, 9216
TR a[140:141],v146 offset:61440
TR a[142:143],v146 offset:63488
AR v116,a52
MM v[132:135],a[144:147],a[0:3],v[132:135]
s_waitcnt lgkmcnt(15)
AR v117,a53
AR v118,a54
MM v[64:67],a[160:163],a[0:3],0
s_xor_b32 s92, s49, 6
TR a[144:145],v147 offset:49152
TR a[146:147],v147 offset:51200
AR v119,a55
v_fma_f32 v112, v112, v153, v152
MM v[64:67],a[164:167],a[32:35],v[64:67]
s_mul_i32 s92, s92, 17408
s_add_u32 s92, s83, s92
v_fma_f32 v113, v113, v153, v152
v_fma_f32 v114, v114, v153, v152
MM v[64:67],a[168:171],a[64:67],v[64:67]
TR a[148:149],v147 offset:53248
TR a[150:151],v147 offset:55296
v_fma_f32 v115, v115, v153, v152
MM v[64:67],a[172:175],a[96:99],v[64:67]
buffer_load_dwordx4 v[160:163], v248, s[20:23], s92 offen offset:0 sc1
v_fma_f32 v116, v116, v153, v152
v_fma_f32 v117, v117, v153, v152
MM v[72:75],a[172:175],a[100:103],0
TR a[152:153],v147 offset:57344
TR a[154:155],v147 offset:59392
v_fma_f32 v118, v118, v153, v152
v_fma_f32 v119, v119, v153, v152
MM v[72:75],a[168:171],a[68:71],v[72:75]
buffer_load_dwordx4 v[176:179], v248, s[20:23], s92 offen offset:1024 sc1
CV v184,v112,v113
MM v[72:75],a[164:167],a[36:39],v[72:75]
TR a[156:157],v147 offset:61440
TR a[158:159],v147 offset:63488
CV v185,v114,v115
CV v186,v116,v117
MM v[72:75],a[160:163],a[4:7],v[72:75]
s_waitcnt lgkmcnt(15)
CV v187,v118,v119
v_xor_b32 v253, s60, v250
MM v[76:79],a[176:179],a[4:7],0
TR a[160:161],v228 offset:49152
TR a[162:163],v228 offset:51200
MM v[76:79],a[180:183],a[36:39],v[76:79]
buffer_store_dwordx4 v[184:187], v248, s[20:23], s78 offen offset:1024 sc1
AR v120,a56
AR v121,a57
MM v[76:79],a[184:187],a[68:71],v[76:79]
TR a[164:165],v228 offset:53248
TR a[166:167],v228 offset:55296
AR v122,a58
AR v123,a59
MM v[76:79],a[188:191],a[100:103],v[76:79]
AR v124,a60
AR v125,a61
MM v[68:71],a[188:191],a[96:99],0
TR a[168:169],v228 offset:57344
TR a[170:171],v228 offset:59392
AR v126,a62
MM v[68:71],a[184:187],a[64:67],v[68:71]
buffer_load_dwordx4 v[192:195], v248, s[20:23], s92 offen offset:2048 sc1
AR v127,a63
v_fma_f32 v120, v120, v157, v156
MM v[68:71],a[180:183],a[32:35],v[68:71]
TR a[172:173],v228 offset:61440
TR a[174:175],v228 offset:63488
v_fma_f32 v121, v121, v157, v156
v_fma_f32 v122, v122, v157, v156
MM v[68:71],a[176:179],a[0:3],v[68:71]
s_waitcnt lgkmcnt(15)
v_fma_f32 v123, v123, v157, v156
v_fma_f32 v124, v124, v157, v156
MM v[96:99],a[128:131],a[0:3],0
TR a[176:177],v229 offset:49152
TR a[178:179],v229 offset:51200
v_fma_f32 v125, v125, v157, v156
MM v[96:99],a[132:135],a[32:35],v[96:99]
buffer_load_dwordx4 v[208:211], v248, s[20:23], s92 offen offset:3072 sc1
v_fma_f32 v126, v126, v157, v156
v_fma_f32 v127, v127, v157, v156
MM v[96:99],a[136:139],a[64:67],v[96:99]
TR a[180:181],v229 offset:53248
TR a[182:183],v229 offset:55296
CV v188,v120,v121
MM v[96:99],a[140:143],a[96:99],v[96:99]
s_add_u32 s92, s92, 4096
CV v189,v122,v123
CV v190,v124,v125
MM v[104:107],a[140:143],a[100:103],0
TR a[184:185],v229 offset:57344
TR a[186:187],v229 offset:59392
CV v191,v126,v127
MM v[104:107],a[136:139],a[68:71],v[104:107]
buffer_store_dwordx4 v[188:191], v248, s[20:23], s79 offen offset:1024 sc1
MM v[104:107],a[132:135],a[36:39],v[104:107]
TR a[188:189],v229 offset:61440
TR a[190:191],v229 offset:63488
AR v80,a80
AR v81,a81
MM v[104:107],a[128:131],a[4:7],v[104:107]
s_waitcnt lgkmcnt(15)
AR v82,a82
AR v83,a83
MM v[108:111],a[144:147],a[4:7],0
TR a[128:129],v230 offset:49152
TR a[130:131],v230 offset:51200
AR v84,a84
MM v[108:111],a[148:151],a[36:39],v[108:111]
buffer_load_dwordx4 v[164:167], v248, s[20:23], s92 offen offset:0 sc1
AR v85,a85
AR v86,a86
MM v[108:111],a[152:155],a[68:71],v[108:111]
TR a[132:133],v230 offset:53248
TR a[134:135],v230 offset:55296
AR v87,a87
v_fma_f32 v80, v80, v153, v152
MM v[108:111],a[156:159],a[100:103],v[108:111]
v_fma_f32 v81, v81, v153, v152
v_fma_f32 v82, v82, v153, v152
MM v[100:103],a[156:159],a[96:99],0
TR a[136:137],v230 offset:57344
TR a[138:139],v230 offset:59392
v_fma_f32 v83, v83, v153, v152
MM v[100:103],a[152:155],a[64:67],v[100:103]
buffer_load_dwordx4 v[180:183], v248, s[20:23], s92 offen offset:1024 sc1
)ASM"
R"ASM(
v_fma_f32 v84, v84, v153, v152
v_fma_f32 v85, v85, v153, v152
MM v[100:103],a[148:151],a[32:35],v[100:103]
TR a[140:141],v230 offset:61440
TR a[142:143],v230 offset:63488
v_fma_f32 v86, v86, v153, v152
v_fma_f32 v87, v87, v153, v152
MM v[100:103],a[144:147],a[0:3],v[100:103]
s_waitcnt lgkmcnt(15)
CV v200,v80,v81
CV v201,v82,v83
MM v[0:3],a[160:163],a[0:3],0
TR a[144:145],v231 offset:49152
TR a[146:147],v231 offset:51200
CV v202,v84,v85
MM v[0:3],a[164:167],a[32:35],v[0:3]
buffer_load_dwordx4 v[196:199], v248, s[20:23], s92 offen offset:2048 sc1
CV v203,v86,v87
MM v[0:3],a[168:171],a[64:67],v[0:3]
TR a[148:149],v231 offset:53248
TR a[150:151],v231 offset:55296
MM v[0:3],a[172:175],a[96:99],v[0:3]
v_and_b32 v238, 240, v248
v_lshl_add_u32 v232, v250, 2, v238
MM v[8:11],a[172:175],a[100:103],0
s_add_u32 s61, s65, 131072
TR a[152:153],v231 offset:57344
TR a[154:155],v231 offset:59392
MM v[8:11],a[168:171],a[68:71],v[8:11]
buffer_store_dwordx4 v[200:203], v248, s[20:23], s78 offen offset:2048 sc1
AR v88,a88
AR v89,a89
AR v90,a90
AR v91,a91
AR v92,a92
MM v[8:11],a[164:167],a[36:39],v[8:11]
TR a[156:157],v231 offset:61440
TR a[158:159],v231 offset:63488
AR v93,a93
AR v94,a94
MM v[8:11],a[160:163],a[4:7],v[8:11]
s_waitcnt lgkmcnt(15)
AR v95,a95
v_fma_f32 v88, v88, v157, v156
s_barrier
MM v[12:15],a[176:179],a[4:7],0
TR a[160:161],v244 offset:8192
TR a[162:163],v244 offset:9216
v_fma_f32 v89, v89, v157, v156
v_fma_f32 v90, v90, v157, v156
MM v[12:15],a[180:183],a[36:39],v[12:15]
buffer_load_dwordx4 v[212:215], v248, s[20:23], s92 offen offset:3072 sc1
v_fma_f32 v91, v91, v157, v156
MM v[12:15],a[184:187],a[68:71],v[12:15]
TR a[164:165],v244 offset:10240
TR a[166:167],v244 offset:11264
v_fma_f32 v92, v92, v157, v156
v_fma_f32 v93, v93, v157, v156
MM v[12:15],a[188:191],a[100:103],v[12:15]
v_fma_f32 v94, v94, v157, v156
v_fma_f32 v95, v95, v157, v156
MM v[4:7],a[188:191],a[96:99],0
TR a[168:169],v244 offset:12288
TR a[170:171],v244 offset:13312
CV v204,v88,v89
MM v[4:7],a[184:187],a[64:67],v[4:7]
buffer_load_dwordx4 v240, s[24:27], s66 offen offset:2048 lds
CV v205,v90,v91
CV v206,v92,v93
MM v[4:7],a[180:183],a[32:35],v[4:7]
s_add_u32 s66, s66, 9216
TR a[172:173],v244 offset:14336
TR a[174:175],v244 offset:15360
CV v207,v94,v95
MM v[4:7],a[176:179],a[0:3],v[4:7]
s_waitcnt lgkmcnt(15)
v_mul_u32_u24 v224, 5, v238
v_lshl_add_u32 v224, v250, 3, v224
MM v[32:35],a[128:131],a[0:3],0
TR a[176:177],v235 offset:8192
TR a[178:179],v235 offset:9216
MM v[32:35],a[132:135],a[32:35],v[32:35]
buffer_store_dwordx4 v[204:207], v248, s[20:23], s79 offen offset:2048 sc1
AR v112,a112
AR v113,a113
AR v114,a114
AR v115,a115
MM v[32:35],a[136:139],a[64:67],v[32:35]
TR a[180:181],v235 offset:10240
TR a[182:183],v235 offset:11264
AR v116,a116
MM v[32:35],a[140:143],a[96:99],v[32:35]
AR v117,a117
AR v118,a118
MM v[40:43],a[140:143],a[100:103],0
TR a[184:185],v235 offset:12288
TR a[186:187],v235 offset:13312
AR v119,a119
v_fma_f32 v112, v112, v153, v152
MM v[40:43],a[136:139],a[68:71],v[40:43]
buffer_load_dwordx4 v240, s[24:27], s66 offen offset:3072 lds
v_fma_f32 v113, v113, v153, v152
MM v[40:43],a[132:135],a[36:39],v[40:43]
TR a[188:189],v235 offset:14336
TR a[190:191],v235 offset:15360
v_fma_f32 v114, v114, v153, v152
v_fma_f32 v115, v115, v153, v152
MM v[40:43],a[128:131],a[4:7],v[40:43]
s_waitcnt lgkmcnt(15)
v_fma_f32 v116, v116, v153, v152
v_fma_f32 v117, v117, v153, v152
MM v[44:47],a[144:147],a[4:7],0
s_xor_b32 s66, s94, 7
TR a[128:129],v144 offset:0
TR a[130:131],v144 offset:2048
v_fma_f32 v118, v118, v153, v152
v_fma_f32 v119, v119, v153, v152
MM v[44:47],a[148:151],a[36:39],v[44:47]
s_mul_i32 s66, s66, 163840
CV v216,v112,v113
CV v217,v114,v115
MM v[44:47],a[152:155],a[68:71],v[44:47]
s_add_u32 s66, s66, s86
TR a[132:133],v144 offset:4096
TR a[134:135],v144 offset:6144
CV v218,v116,v117
MM v[44:47],a[156:159],a[100:103],v[44:47]
s_add_u32 m0, s65, 32768
CV v219,v118,v119
v_add_u32 v224, s61, v224
MM v[36:39],a[156:159],a[96:99],0
buffer_store_dwordx4 v[216:219], v248, s[20:23], s78 offen offset:3072 sc1
TR a[136:137],v144 offset:8192
TR a[138:139],v144 offset:10240
MM v[36:39],a[152:155],a[64:67],v[36:39]
AR v120,a120
AR v121,a121
MM v[36:39],a[148:151],a[32:35],v[36:39]
TR a[140:141],v144 offset:12288
TR a[142:143],v144 offset:14336
AR v122,a122
AR v123,a123
MM v[36:39],a[144:147],a[0:3],v[36:39]
s_waitcnt lgkmcnt(15)
buffer_load_dwordx4 v240, s[24:27], s66 offen offset:0 lds
AR v124,a124
s_waitcnt vmcnt(24)
MM v[128:131],a[160:163],a[8:11],v[128:131]
s_add_u32 s66, s66, 9216
TR a[144:145],v145 offset:0
TR a[146:147],v145 offset:2048
AR v125,a125
AR v126,a126
MM v[128:131],a[164:167],a[40:43],v[128:131]
AR v127,a127
v_fma_f32 v120, v120, v157, v156
MM v[128:131],a[168:171],a[72:75],v[128:131]
TR a[148:149],v145 offset:4096
TR a[150:151],v145 offset:6144
v_fma_f32 v121, v121, v157, v156
v_fma_f32 v122, v122, v157, v156
MM v[128:131],a[172:175],a[104:107],v[128:131]
s_waitcnt vmcnt(20)
buffer_load_dwordx4 v240, s[24:27], s66 offen offset:1024 lds
v_fma_f32 v123, v123, v157, v156
MM v[136:139],a[172:175],a[108:111],v[136:139]
s_add_u32 s66, s66, 9216
TR a[152:153],v145 offset:8192
TR a[154:155],v145 offset:10240
v_fma_f32 v124, v124, v157, v156
v_fma_f32 v125, v125, v157, v156
MM v[136:139],a[168:171],a[76:79],v[136:139]
v_fma_f32 v126, v126, v157, v156
v_fma_f32 v127, v127, v157, v156
MM v[136:139],a[164:167],a[44:47],v[136:139]
TR a[156:157],v145 offset:12288
TR a[158:159],v145 offset:14336
CV v220,v120,v121
MM v[136:139],a[160:163],a[12:15],v[136:139]
s_waitcnt lgkmcnt(15)
buffer_load_dwordx4 v240, s[24:27], s66 offen offset:2048 lds
CV v221,v122,v123
CV v222,v124,v125
MM v[140:143],a[176:179],a[12:15],v[140:143]
s_add_u32 s66, s66, 9216
TR a[160:161],v146 offset:0
TR a[162:163],v146 offset:2048
CV v223,v126,v127
MM v[140:143],a[180:183],a[44:47],v[140:143]
v_mul_u32_u24 v254, 1280, v250
v_mul_u32_u24 v250, 33792, v253
MM v[140:143],a[184:187],a[76:79],v[140:143]
buffer_store_dwordx4 v[220:223], v248, s[20:23], s79 offen offset:3072 sc1
TR a[164:165],v146 offset:4096
TR a[166:167],v146 offset:6144
MM v[140:143],a[188:191],a[108:111],v[140:143]
v_add_u32 v250, v250, v238
v_lshlrev_b32 v253, 4, v253
MM v[132:135],a[188:191],a[104:107],v[132:135]
TR a[168:169],v146 offset:8192
TR a[170:171],v146 offset:10240
MM v[132:135],a[184:187],a[72:75],v[132:135]
v_bfe_u32 v238, v248, 4, 3
v_lshl_add_u32 v254, v238, 3, v254
MM v[132:135],a[180:183],a[40:43],v[132:135]
TR a[172:173],v146 offset:12288
TR a[174:175],v146 offset:14336
MM v[132:135],a[176:179],a[8:11],v[132:135]
s_waitcnt lgkmcnt(15)
buffer_load_dwordx4 v240, s[24:27], s66 offen offset:3072 lds
MM v[64:67],a[128:131],a[8:11],v[64:67]
s_xor_b32 s92, s49, 7
TR a[176:177],v147 offset:0
TR a[178:179],v147 offset:2048
MM v[64:67],a[132:135],a[40:43],v[64:67]
s_mul_i32 s92, s92, 17408
s_add_u32 s92, s83, s92
v_bfe_u32 v238, v248, 7, 1
MM v[64:67],a[136:139],a[72:75],v[64:67]
s_xor_b32 s66, s94, 0
TR a[180:181],v147 offset:4096
TR a[182:183],v147 offset:6144
MM v[64:67],a[140:143],a[104:107],v[64:67]
s_mul_i32 s66, s66, 163840
buffer_load_dwordx4 a[192:195], v248, s[20:23], s92 offen offset:0 sc1
MM v[72:75],a[140:143],a[108:111],v[72:75]
s_add_u32 s66, s66, s88
TR a[184:185],v147 offset:8192
TR a[186:187],v147 offset:10240
MM v[72:75],a[136:139],a[76:79],v[72:75]
s_add_u32 m0, s89, 131072
v_mul_u32_u24 v238, 5120, v238
v_add_u32 v254, v254, v238
MM v[72:75],a[132:135],a[44:47],v[72:75]
TR a[188:189],v147 offset:12288
TR a[190:191],v147 offset:14336
MM v[72:75],a[128:131],a[12:15],v[72:75]
s_waitcnt lgkmcnt(15)
buffer_load_dwordx4 a[208:211], v248, s[20:23], s92 offen offset:1024 sc1
MM v[76:79],a[144:147],a[12:15],v[76:79]
TR a[128:129],v228 offset:0
TR a[130:131],v228 offset:2048
MM v[76:79],a[148:151],a[44:47],v[76:79]
v_lshrrev_b32 v238, 6, v248
v_mul_u32_u24 v225, 80, v238
MM v[76:79],a[152:155],a[76:79],v[76:79]
TR a[132:133],v228 offset:4096
TR a[134:135],v228 offset:6144
MM v[76:79],a[156:159],a[108:111],v[76:79]
buffer_load_dwordx4 a[224:227], v248, s[20:23], s92 offen offset:2048 sc1
MM v[68:71],a[156:159],a[104:107],v[68:71]
TR a[136:137],v228 offset:8192
TR a[138:139],v228 offset:10240
MM v[68:71],a[152:155],a[72:75],v[68:71]
v_add3_u32 v225, v225, v251, s61
v_mul_u32_u24 v238, 1280, v238
MM v[68:71],a[148:151],a[40:43],v[68:71]
buffer_load_dwordx4 v252, s[24:27], s66 offen offset:0 lds
TR a[140:141],v228 offset:12288
TR a[142:143],v228 offset:14336
MM v[68:71],a[144:147],a[8:11],v[68:71]
s_waitcnt lgkmcnt(15)
s_add_u32 s66, s66, 19456
v_add_u32 v251, v251, v238
MM v[96:99],a[160:163],a[8:11],v[96:99]
TR a[144:145],v229 offset:0
TR a[146:147],v229 offset:2048
MM v[96:99],a[164:167],a[40:43],v[96:99]
MM v[96:99],a[168:171],a[72:75],v[96:99]
TR a[148:149],v229 offset:4096
TR a[150:151],v229 offset:6144
MM v[96:99],a[172:175],a[104:107],v[96:99]
buffer_load_dwordx4 a[240:243], v248, s[20:23], s92 offen offset:3072 sc1
MM v[104:107],a[172:175],a[108:111],v[104:107]
TR a[152:153],v229 offset:8192
TR a[154:155],v229 offset:10240
MM v[104:107],a[168:171],a[76:79],v[104:107]
s_add_u32 s92, s92, 4096
MM v[104:107],a[164:167],a[44:47],v[104:107]
TR a[156:157],v229 offset:12288
TR a[158:159],v229 offset:14336
MM v[104:107],a[160:163],a[12:15],v[104:107]
s_waitcnt lgkmcnt(15)
buffer_load_dwordx4 a[196:199], v248, s[20:23], s92 offen offset:0 sc1
MM v[108:111],a[176:179],a[12:15],v[108:111]
TR a[160:161],v230 offset:0
TR a[162:163],v230 offset:2048
MM v[108:111],a[180:183],a[44:47],v[108:111]
MM v[108:111],a[184:187],a[76:79],v[108:111]
buffer_load_dwordx4 v252, s[24:27], s66 offen offset:1024 lds
TR a[164:165],v230 offset:4096
TR a[166:167],v230 offset:6144
MM v[108:111],a[188:191],a[108:111],v[108:111]
MM v[100:103],a[188:191],a[104:107],v[100:103]
s_xor_b32 s66, s94, 0
TR a[168:169],v230 offset:8192
TR a[170:171],v230 offset:10240
MM v[100:103],a[184:187],a[72:75],v[100:103]
s_mul_i32 s66, s66, 163840
s_add_u32 s66, s66, s87
s_add_u32 m0, s65, 114688
MM v[100:103],a[180:183],a[40:43],v[100:103]
TR a[172:173],v230 offset:12288
TR a[174:175],v230 offset:14336
MM v[100:103],a[176:179],a[8:11],v[100:103]
s_waitcnt lgkmcnt(15)
buffer_load_dwordx4 a[212:215], v248, s[20:23], s92 offen offset:1024 sc1
MM v[0:3],a[128:131],a[8:11],v[0:3]
TR a[176:177],v231 offset:0
TR a[178:179],v231 offset:2048
MM v[0:3],a[132:135],a[40:43],v[0:3]
MM v[0:3],a[136:139],a[72:75],v[0:3]
TR a[180:181],v231 offset:4096
TR a[182:183],v231 offset:6144
MM v[0:3],a[140:143],a[104:107],v[0:3]
buffer_load_dwordx4 a[228:231], v248, s[20:23], s92 offen offset:2048 sc1
MM v[8:11],a[140:143],a[108:111],v[8:11]
TR a[184:185],v231 offset:8192
TR a[186:187],v231 offset:10240
MM v[8:11],a[136:139],a[76:79],v[8:11]
MM v[8:11],a[132:135],a[44:47],v[8:11]
buffer_load_dwordx4 v240, s[24:27], s66 offen offset:0 lds
TR a[188:189],v231 offset:12288
TR a[190:191],v231 offset:14336
MM v[8:11],a[128:131],a[12:15],v[8:11]
s_waitcnt lgkmcnt(15)
s_add_u32 s66, s66, 9216
s_barrier
MM v[12:15],a[144:147],a[12:15],v[12:15]
TR a[128:129],v244 offset:16384
TR a[130:131],v244 offset:17408
MM v[12:15],a[148:151],a[44:47],v[12:15]
MM v[12:15],a[152:155],a[76:79],v[12:15]
TR a[132:133],v244 offset:18432
TR a[134:135],v244 offset:19456
MM v[12:15],a[156:159],a[108:111],v[12:15]
LPG27_%=:
s_cbranch_execz LTR1_%=
buffer_load_dwordx4 a[244:247], v248, s[20:23], s92 offen offset:3072 sc1
MM v[4:7],a[156:159],a[104:107],v[4:7]
TR a[136:137],v244 offset:20480
TR a[138:139],v244 offset:21504
MM v[4:7],a[152:155],a[72:75],v[4:7]
MM v[4:7],a[148:151],a[40:43],v[4:7]
TR a[140:141],v244 offset:22528
TR a[142:143],v244 offset:23552
MM v[4:7],a[144:147],a[8:11],v[4:7]
s_waitcnt lgkmcnt(15)
buffer_load_dwordx4 v240, s[24:27], s66 offen offset:1024 lds
MM v[32:35],a[160:163],a[8:11],v[32:35]
s_add_u32 s66, s66, 9216
TR a[144:145],v235 offset:16384
TR a[146:147],v235 offset:17408
MM v[32:35],a[164:167],a[40:43],v[32:35]
MM v[32:35],a[168:171],a[72:75],v[32:35]
TR a[148:149],v235 offset:18432
TR a[150:151],v235 offset:19456
MM v[32:35],a[172:175],a[104:107],v[32:35]
)ASM"
R"ASM(
buffer_load_dwordx4 v240, s[24:27], s66 offen offset:2048 lds
MM v[40:43],a[172:175],a[108:111],v[40:43]
s_add_u32 s66, s66, 9216
TR a[152:153],v235 offset:20480
TR a[154:155],v235 offset:21504
MM v[40:43],a[168:171],a[76:79],v[40:43]
MM v[40:43],a[164:167],a[44:47],v[40:43]
TR a[156:157],v235 offset:22528
TR a[158:159],v235 offset:23552
MM v[40:43],a[160:163],a[12:15],v[40:43]
s_waitcnt lgkmcnt(15)
buffer_load_dwordx4 v240, s[24:27], s66 offen offset:3072 lds
MM v[44:47],a[176:179],a[12:15],v[44:47]
s_lshl_b32 s97, s84, 2
TR a[160:161],v144 offset:16384
TR a[162:163],v144 offset:18432
MM v[44:47],a[180:183],a[44:47],v[44:47]
s_add_u32 s97, s97, s96
s_add_u32 s97, s97, 16
s_waitcnt vmcnt(15)
MM v[44:47],a[184:187],a[76:79],v[44:47]
s_xor_b32 s66, s94, 0
TR a[164:165],v144 offset:20480
TR a[166:167],v144 offset:22528
MM v[44:47],a[188:191],a[108:111],v[44:47]
s_mul_i32 s66, s66, 163840
v_mov_b32 v242, s69
s_mov_b64 exec, 1
buffer_store_dword v242, off, s[20:23], s97 offset:0 sc1
s_mov_b64 exec, -1
MM v[36:39],a[188:191],a[104:107],v[36:39]
s_add_u32 s66, s66, s86
TR a[168:169],v144 offset:24576
TR a[170:171],v144 offset:26624
MM v[36:39],a[184:187],a[72:75],v[36:39]
s_add_u32 m0, s65, 49152
MM v[36:39],a[180:183],a[40:43],v[36:39]
TR a[172:173],v144 offset:28672
TR a[174:175],v144 offset:30720
MM v[36:39],a[176:179],a[8:11],v[36:39]
s_waitcnt lgkmcnt(15)
buffer_load_dwordx4 v240, s[24:27], s66 offen offset:0 lds
MM v[128:131],a[128:131],v[160:163],v[128:131]
s_add_u32 s66, s66, 9216
TR a[176:177],v145 offset:16384
TR a[178:179],v145 offset:18432
MM v[128:131],a[132:135],v[176:179],v[128:131]
MM v[128:131],a[136:139],v[192:195],v[128:131]
TR a[180:181],v145 offset:20480
TR a[182:183],v145 offset:22528
MM v[128:131],a[140:143],v[208:211],v[128:131]
buffer_load_dwordx4 v240, s[24:27], s66 offen offset:1024 lds
MM v[136:139],a[140:143],v[212:215],v[136:139]
s_add_u32 s66, s66, 9216
TR a[184:185],v145 offset:24576
TR a[186:187],v145 offset:26624
MM v[136:139],a[136:139],v[196:199],v[136:139]
MM v[136:139],a[132:135],v[180:183],v[136:139]
TR a[188:189],v145 offset:28672
TR a[190:191],v145 offset:30720
MM v[136:139],a[128:131],v[164:167],v[136:139]
s_waitcnt lgkmcnt(15)
buffer_load_dwordx4 v240, s[24:27], s66 offen offset:2048 lds
MM v[140:143],a[144:147],v[164:167],v[140:143]
s_add_u32 s66, s66, 9216
TR a[128:129],v146 offset:16384
TR a[130:131],v146 offset:18432
MM v[140:143],a[148:151],v[180:183],v[140:143]
MM v[140:143],a[152:155],v[196:199],v[140:143]
TR a[132:133],v146 offset:20480
TR a[134:135],v146 offset:22528
MM v[140:143],a[156:159],v[212:215],v[140:143]
buffer_load_dwordx4 v240, s[24:27], s66 offen offset:3072 lds
MM v[132:135],a[156:159],v[208:211],v[132:135]
TR a[136:137],v146 offset:24576
TR a[138:139],v146 offset:26624
MM v[132:135],a[152:155],v[192:195],v[132:135]
s_xor_b32 s66, s94, 1
s_mul_i32 s66, s66, 163840
s_add_u32 s66, s66, s88
MM v[132:135],a[148:151],v[176:179],v[132:135]
s_add_u32 m0, s89, 139264
TR a[140:141],v146 offset:28672
TR a[142:143],v146 offset:30720
MM v[132:135],a[144:147],v[160:163],v[132:135]
s_waitcnt lgkmcnt(15)
buffer_load_dwordx4 v252, s[24:27], s66 offen offset:0 lds
MM v[64:67],a[160:163],v[160:163],v[64:67]
s_add_u32 s66, s66, 19456
TR a[144:145],v147 offset:16384
TR a[146:147],v147 offset:18432
MM v[64:67],a[164:167],v[176:179],v[64:67]
MM v[64:67],a[168:171],v[192:195],v[64:67]
TR a[148:149],v147 offset:20480
TR a[150:151],v147 offset:22528
MM v[64:67],a[172:175],v[208:211],v[64:67]
buffer_load_dwordx4 v252, s[24:27], s66 offen offset:1024 lds
MM v[72:75],a[172:175],v[212:215],v[72:75]
TR a[152:153],v147 offset:24576
TR a[154:155],v147 offset:26624
MM v[72:75],a[168:171],v[196:199],v[72:75]
s_xor_b32 s66, s94, 1
s_mul_i32 s66, s66, 163840
s_add_u32 s66, s66, s87
MM v[72:75],a[164:167],v[180:183],v[72:75]
s_add_u32 m0, s65, 65536
TR a[156:157],v147 offset:28672
TR a[158:159],v147 offset:30720
MM v[72:75],a[160:163],v[164:167],v[72:75]
s_waitcnt lgkmcnt(15)
buffer_load_dwordx4 v240, s[24:27], s66 offen offset:0 lds
MM v[76:79],a[176:179],v[164:167],v[76:79]
s_add_u32 s66, s66, 9216
TR a[160:161],v228 offset:16384
TR a[162:163],v228 offset:18432
MM v[76:79],a[180:183],v[180:183],v[76:79]
MM v[76:79],a[184:187],v[196:199],v[76:79]
TR a[164:165],v228 offset:20480
TR a[166:167],v228 offset:22528
MM v[76:79],a[188:191],v[212:215],v[76:79]
buffer_load_dwordx4 v240, s[24:27], s66 offen offset:1024 lds
MM v[68:71],a[188:191],v[208:211],v[68:71]
s_add_u32 s66, s66, 9216
TR a[168:169],v228 offset:24576
TR a[170:171],v228 offset:26624
MM v[68:71],a[184:187],v[192:195],v[68:71]
MM v[68:71],a[180:183],v[176:179],v[68:71]
TR a[172:173],v228 offset:28672
TR a[174:175],v228 offset:30720
MM v[68:71],a[176:179],v[160:163],v[68:71]
s_waitcnt lgkmcnt(15)
buffer_load_dwordx4 v240, s[24:27], s66 offen offset:2048 lds
MM v[96:99],a[128:131],v[160:163],v[96:99]
s_add_u32 s80, s18, 131072
TR a[176:177],v229 offset:16384
TR a[178:179],v229 offset:18432
MM v[96:99],a[132:135],v[176:179],v[96:99]
s_add_u32 s66, s66, 9216
buffer_load_dwordx4 v[16:19], v251, s[20:23], s80 offen offset:0 sc1
MM v[96:99],a[136:139],v[192:195],v[96:99]
TR a[180:181],v229 offset:20480
TR a[182:183],v229 offset:22528
MM v[96:99],a[140:143],v[208:211],v[96:99]
v_mov_b32 v237, 0x3f80
v_and_b32 v236, 16, v248
MM v[104:107],a[140:143],v[212:215],v[104:107]
TR a[184:185],v229 offset:24576
TR a[186:187],v229 offset:26624
MM v[104:107],a[136:139],v[196:199],v[104:107]
s_add_u32 s80, s18, 151552
buffer_load_dwordx4 v[20:23], v251, s[20:23], s80 offen offset:0 sc1
MM v[104:107],a[132:135],v[180:183],v[104:107]
TR a[188:189],v229 offset:28672
TR a[190:191],v229 offset:30720
MM v[104:107],a[128:131],v[164:167],v[104:107]
s_waitcnt lgkmcnt(15)
v_lshlrev_b32 v236, v236, v237
s_mov_b32 s28, 0x30003
MM v[108:111],a[144:147],v[164:167],v[108:111]
s_mov_b32 s29, s28
TR a[128:129],v230 offset:16384
TR a[130:131],v230 offset:18432
MM v[108:111],a[148:151],v[180:183],v[108:111]
s_add_u32 s80, s63, 131072
v_lshrrev_b32 v243, 2, v248
v_min_u32 v243, 12, v243
MM v[108:111],a[152:155],v[196:199],v[108:111]
TR a[132:133],v230 offset:20480
TR a[134:135],v230 offset:22528
MM v[108:111],a[156:159],v[212:215],v[108:111]
buffer_load_dword v242, v243, s[20:23], s96 offen offset:16 sc1
buffer_load_dwordx4 v240, s[24:27], s66 offen offset:3072 lds
buffer_load_dwordx2 v[150:151], v254, s[20:23], s80 offen offset:0 sc1
MM v[100:103],a[156:159],v[208:211],v[100:103]
TR a[136:137],v230 offset:24576
TR a[138:139],v230 offset:26624
MM v[100:103],a[152:155],v[192:195],v[100:103]
v_cndmask_b32 v48, 0, v236, s[28:29]
s_lshl_b64 s[28:29], s[28:29], 2
MM v[100:103],a[148:151],v[176:179],v[100:103]
TR a[140:141],v230 offset:28672
TR a[142:143],v230 offset:30720
MM v[100:103],a[144:147],v[160:163],v[100:103]
s_waitcnt lgkmcnt(15)
v_cndmask_b32 v49, 0, v236, s[28:29]
s_lshl_b64 s[28:29], s[28:29], 2
MM v[0:3],a[160:163],v[160:163],v[0:3]
TR a[144:145],v231 offset:16384
TR a[146:147],v231 offset:18432
MM v[0:3],a[164:167],v[176:179],v[0:3]
v_cndmask_b32 v50, 0, v236, s[28:29]
s_lshl_b64 s[28:29], s[28:29], 2
MM v[0:3],a[168:171],v[192:195],v[0:3]
TR a[148:149],v231 offset:20480
TR a[150:151],v231 offset:22528
MM v[0:3],a[172:175],v[208:211],v[0:3]
v_cndmask_b32 v51, 0, v236, s[28:29]
s_lshl_b64 s[28:29], s[28:29], 2
MM v[8:11],a[172:175],v[212:215],v[8:11]
TR a[152:153],v231 offset:24576
TR a[154:155],v231 offset:26624
MM v[8:11],a[168:171],v[196:199],v[8:11]
v_cndmask_b32 v52, 0, v236, s[28:29]
s_lshl_b64 s[28:29], s[28:29], 2
MM v[8:11],a[164:167],v[180:183],v[8:11]
TR a[156:157],v231 offset:28672
TR a[158:159],v231 offset:30720
MM v[8:11],a[160:163],v[164:167],v[8:11]
s_waitcnt lgkmcnt(15)
v_cndmask_b32 v53, 0, v236, s[28:29]
s_lshl_b64 s[28:29], s[28:29], 2
s_waitcnt vmcnt(29)
s_barrier
MM v[12:15],a[176:179],v[164:167],v[12:15]
s_xor_b32 s66, s94, 2
TR a[160:161],v244 offset:24576
TR a[162:163],v244 offset:25600
MM v[12:15],a[180:183],v[180:183],v[12:15]
s_mul_i32 s66, s66, 163840
v_cndmask_b32 v54, 0, v236, s[28:29]
s_lshl_b64 s[28:29], s[28:29], 2
MM v[12:15],a[184:187],v[196:199],v[12:15]
s_add_u32 s66, s66, s88
TR a[164:165],v244 offset:26624
TR a[166:167],v244 offset:27648
MM v[12:15],a[188:191],v[212:215],v[12:15]
s_add_u32 m0, s89, 147456
v_cndmask_b32 v55, 0, v236, s[28:29]
s_mov_b32 s28, 0x300003
MM v[4:7],a[188:191],v[208:211],v[4:7]
s_mov_b32 s29, 0x30000300
TR a[168:169],v244 offset:28672
TR a[170:171],v244 offset:29696
MM v[4:7],a[184:187],v[192:195],v[4:7]
buffer_load_dwordx4 v252, s[24:27], s66 offen offset:0 lds
MM v[4:7],a[180:183],v[176:179],v[4:7]
s_add_u32 s66, s66, 19456
TR a[172:173],v244 offset:30720
TR a[174:175],v244 offset:31744
MM v[4:7],a[176:179],v[160:163],v[4:7]
s_waitcnt lgkmcnt(15)
v_mov_b32 v56, 0
v_mov_b32 v57, 0
MM v[32:35],a[128:131],v[160:163],v[32:35]
TR a[176:177],v235 offset:24576
TR a[178:179],v235 offset:25600
MM v[32:35],a[132:135],v[176:179],v[32:35]
buffer_load_dwordx4 v252, s[24:27], s66 offen offset:1024 lds
MM v[32:35],a[136:139],v[192:195],v[32:35]
TR a[180:181],v235 offset:26624
TR a[182:183],v235 offset:27648
MM v[32:35],a[140:143],v[208:211],v[32:35]
s_xor_b32 s66, s94, 2
v_mov_b32 v60, 0
v_mov_b32 v61, 0
MM v[40:43],a[140:143],v[212:215],v[40:43]
s_mul_i32 s66, s66, 163840
TR a[184:185],v235 offset:28672
TR a[186:187],v235 offset:29696
MM v[40:43],a[136:139],v[196:199],v[40:43]
s_add_u32 s66, s66, s87
v_cndmask_b32 v58, 0, v236, s[28:29]
s_lshl_b64 s[28:29], s[28:29], 2
MM v[40:43],a[132:135],v[180:183],v[40:43]
s_add_u32 m0, s65, 81920
TR a[188:189],v235 offset:30720
TR a[190:191],v235 offset:31744
MM v[40:43],a[128:131],v[164:167],v[40:43]
s_waitcnt lgkmcnt(15)
buffer_load_dwordx4 v240, s[24:27], s66 offen offset:0 lds
MM v[44:47],a[144:147],v[164:167],v[44:47]
s_add_u32 s66, s66, 9216
TR a[128:129],v144 offset:32768
TR a[130:131],v144 offset:34816
MM v[44:47],a[148:151],v[180:183],v[44:47]
v_cndmask_b32 v59, 0, v236, s[28:29]
MM v[44:47],a[152:155],v[196:199],v[44:47]
TR a[132:133],v144 offset:36864
TR a[134:135],v144 offset:38912
MM v[44:47],a[156:159],v[212:215],v[44:47]
buffer_load_dwordx4 v240, s[24:27], s66 offen offset:1024 lds
MM v[36:39],a[156:159],v[208:211],v[36:39]
s_add_u32 s66, s66, 9216
TR a[136:137],v144 offset:40960
TR a[138:139],v144 offset:43008
MM v[36:39],a[152:155],v[192:195],v[36:39]
MM v[36:39],a[148:151],v[176:179],v[36:39]
TR a[140:141],v144 offset:45056
TR a[142:143],v144 offset:47104
MM v[36:39],a[144:147],v[160:163],v[36:39]
s_waitcnt lgkmcnt(15)
buffer_load_dwordx4 v240, s[24:27], s66 offen offset:2048 lds
s_waitcnt vmcnt(29)
MM v[128:131],a[160:163],a[192:195],v[128:131]
s_add_u32 s80, s18, 0
TR a[144:145],v145 offset:32768
TR a[146:147],v145 offset:34816
MM v[128:131],a[164:167],a[208:211],v[128:131]
s_add_u32 s66, s66, 9216
buffer_load_dwordx4 v[24:27], v251, s[32:35], s80 offen offset:0
MM v[128:131],a[168:171],a[224:227],v[128:131]
TR a[148:149],v145 offset:36864
TR a[150:151],v145 offset:38912
MM v[128:131],a[172:175],a[240:243],v[128:131]
s_waitcnt vmcnt(24)
MM v[136:139],a[172:175],a[244:247],v[136:139]
TR a[152:153],v145 offset:40960
TR a[154:155],v145 offset:43008
MM v[136:139],a[168:171],a[228:231],v[136:139]
s_add_u32 s80, s18, 20480
buffer_load_dwordx4 v[28:31], v251, s[32:35], s80 offen offset:0
MM v[136:139],a[164:167],a[212:215],v[136:139]
TR a[156:157],v145 offset:45056
TR a[158:159],v145 offset:47104
MM v[136:139],a[160:163],a[196:199],v[136:139]
s_waitcnt lgkmcnt(15)
MM v[140:143],a[176:179],a[196:199],v[140:143]
TR a[160:161],v146 offset:32768
TR a[162:163],v146 offset:34816
MM v[140:143],a[180:183],a[212:215],v[140:143]
s_add_u32 s80, s63, 0
buffer_load_dwordx2 v[62:63], v254, s[32:35], s80 offen offset:0
MM v[140:143],a[184:187],a[228:231],v[140:143]
TR a[164:165],v146 offset:36864
TR a[166:167],v146 offset:38912
MM v[140:143],a[188:191],a[244:247],v[140:143]
buffer_load_dwordx4 v240, s[24:27], s66 offen offset:3072 lds
MM v[132:135],a[188:191],a[240:243],v[132:135]
TR a[168:169],v146 offset:40960
TR a[170:171],v146 offset:43008
)ASM"
R"ASM(
MM v[132:135],a[184:187],a[224:227],v[132:135]
MM v[132:135],a[180:183],a[208:211],v[132:135]
TR a[172:173],v146 offset:45056
TR a[174:175],v146 offset:47104
MM v[132:135],a[176:179],a[192:195],v[132:135]
s_waitcnt lgkmcnt(15)
MM v[64:67],a[128:131],a[192:195],v[64:67]
s_waitcnt vmcnt(11)
TR a[176:177],v147 offset:32768
TR a[178:179],v147 offset:34816
MM v[64:67],a[132:135],a[208:211],v[64:67]
v_cmp_ne_u32 vcc, s69, v242
s_cbranch_vccnz LRPO1_%=
LRPO1b_%=:
MM v[64:67],a[136:139],a[224:227],v[64:67]
s_xor_b32 s92, s49, 1
TR a[180:181],v147 offset:36864
TR a[182:183],v147 offset:38912
MM v[64:67],a[140:143],a[240:243],v[64:67]
s_mul_i32 s92, s92, 17408
s_add_u32 s92, s83, s92
s_add_u32 s92, s92, 8192
MM v[72:75],a[140:143],a[244:247],v[72:75]
TR a[184:185],v147 offset:40960
TR a[186:187],v147 offset:43008
MM v[72:75],a[136:139],a[228:231],v[72:75]
buffer_load_dwordx4 a[16:19], v248, s[20:23], s92 offen offset:0 sc1
MM v[72:75],a[132:135],a[212:215],v[72:75]
TR a[188:189],v147 offset:45056
TR a[190:191],v147 offset:47104
MM v[72:75],a[128:131],a[196:199],v[72:75]
s_waitcnt lgkmcnt(15)
buffer_load_dwordx4 a[48:51], v248, s[20:23], s92 offen offset:1024 sc1
MM v[76:79],a[144:147],a[196:199],v[76:79]
TR a[128:129],v228 offset:32768
TR a[130:131],v228 offset:34816
MM v[76:79],a[148:151],a[212:215],v[76:79]
MM v[76:79],a[152:155],a[228:231],v[76:79]
TR a[132:133],v228 offset:36864
TR a[134:135],v228 offset:38912
MM v[76:79],a[156:159],a[244:247],v[76:79]
buffer_load_dwordx4 a[80:83], v248, s[20:23], s92 offen offset:2048 sc1
MM v[68:71],a[156:159],a[240:243],v[68:71]
TR a[136:137],v228 offset:40960
TR a[138:139],v228 offset:43008
MM v[68:71],a[152:155],a[224:227],v[68:71]
MM v[68:71],a[148:151],a[208:211],v[68:71]
TR a[140:141],v228 offset:45056
TR a[142:143],v228 offset:47104
MM v[68:71],a[144:147],a[192:195],v[68:71]
s_waitcnt lgkmcnt(15)
buffer_load_dwordx4 a[112:115], v248, s[20:23], s92 offen offset:3072 sc1
MM v[96:99],a[160:163],a[192:195],v[96:99]
s_add_u32 s92, s92, 4096
TR a[144:145],v229 offset:32768
TR a[146:147],v229 offset:34816
MM v[96:99],a[164:167],a[208:211],v[96:99]
MM v[96:99],a[168:171],a[224:227],v[96:99]
TR a[148:149],v229 offset:36864
TR a[150:151],v229 offset:38912
MM v[96:99],a[172:175],a[240:243],v[96:99]
buffer_load_dwordx4 a[20:23], v248, s[20:23], s92 offen offset:0 sc1
MM v[104:107],a[172:175],a[244:247],v[104:107]
TR a[152:153],v229 offset:40960
TR a[154:155],v229 offset:43008
MM v[104:107],a[168:171],a[228:231],v[104:107]
MM v[104:107],a[164:167],a[212:215],v[104:107]
TR a[156:157],v229 offset:45056
TR a[158:159],v229 offset:47104
MM v[104:107],a[160:163],a[196:199],v[104:107]
s_waitcnt lgkmcnt(15)
buffer_load_dwordx4 a[52:55], v248, s[20:23], s92 offen offset:1024 sc1
MM v[108:111],a[176:179],a[196:199],v[108:111]
TR a[160:161],v230 offset:32768
TR a[162:163],v230 offset:34816
MM v[108:111],a[180:183],a[212:215],v[108:111]
MM v[108:111],a[184:187],a[228:231],v[108:111]
TR a[164:165],v230 offset:36864
TR a[166:167],v230 offset:38912
MM v[108:111],a[188:191],a[244:247],v[108:111]
buffer_load_dwordx4 a[84:87], v248, s[20:23], s92 offen offset:2048 sc1
MM v[100:103],a[188:191],a[240:243],v[100:103]
TR a[168:169],v230 offset:40960
TR a[170:171],v230 offset:43008
MM v[100:103],a[184:187],a[224:227],v[100:103]
MM v[100:103],a[180:183],a[208:211],v[100:103]
TR a[172:173],v230 offset:45056
TR a[174:175],v230 offset:47104
MM v[100:103],a[176:179],a[192:195],v[100:103]
s_waitcnt lgkmcnt(15)
buffer_load_dwordx4 a[116:119], v248, s[20:23], s92 offen offset:3072 sc1
MM v[0:3],a[128:131],a[192:195],v[0:3]
TR a[176:177],v231 offset:32768
TR a[178:179],v231 offset:34816
MM v[0:3],a[132:135],a[208:211],v[0:3]
MM v[0:3],a[136:139],a[224:227],v[0:3]
TR a[180:181],v231 offset:36864
TR a[182:183],v231 offset:38912
MM v[0:3],a[140:143],a[240:243],v[0:3]
MM v[8:11],a[140:143],a[244:247],v[8:11]
TR a[184:185],v231 offset:40960
TR a[186:187],v231 offset:43008
MM v[8:11],a[136:139],a[228:231],v[8:11]
MM v[8:11],a[132:135],a[212:215],v[8:11]
TR a[188:189],v231 offset:45056
TR a[190:191],v231 offset:47104
MM v[8:11],a[128:131],a[196:199],v[8:11]
s_waitcnt lgkmcnt(15)
s_barrier
MM v[12:15],a[144:147],a[196:199],v[12:15]
s_xor_b32 s66, s94, 3
TR a[128:129],v244 offset:0
TR a[130:131],v244 offset:1024
MM v[12:15],a[148:151],a[212:215],v[12:15]
s_mul_i32 s66, s66, 163840
s_add_u32 s66, s66, s88
s_add_u32 m0, s89, 155648
MM v[12:15],a[152:155],a[228:231],v[12:15]
TR a[132:133],v244 offset:2048
TR a[134:135],v244 offset:3072
MM v[12:15],a[156:159],a[244:247],v[12:15]
buffer_load_dwordx4 v252, s[24:27], s66 offen offset:0 lds
MM v[4:7],a[156:159],a[240:243],v[4:7]
s_add_u32 s66, s66, 19456
TR a[136:137],v244 offset:4096
TR a[138:139],v244 offset:5120
MM v[4:7],a[152:155],a[224:227],v[4:7]
MM v[4:7],a[148:151],a[208:211],v[4:7]
TR a[140:141],v244 offset:6144
TR a[142:143],v244 offset:7168
MM v[4:7],a[144:147],a[192:195],v[4:7]
s_waitcnt lgkmcnt(15)
buffer_load_dwordx4 v252, s[24:27], s66 offen offset:1024 lds
MM v[32:35],a[160:163],a[192:195],v[32:35]
TR a[144:145],v235 offset:0
TR a[146:147],v235 offset:1024
MM v[32:35],a[164:167],a[208:211],v[32:35]
s_xor_b32 s66, s94, 3
s_mul_i32 s66, s66, 163840
s_add_u32 s66, s66, s87
MM v[32:35],a[168:171],a[224:227],v[32:35]
s_add_u32 m0, s65, 98304
TR a[148:149],v235 offset:2048
TR a[150:151],v235 offset:3072
MM v[32:35],a[172:175],a[240:243],v[32:35]
buffer_load_dwordx4 v240, s[24:27], s66 offen offset:0 lds
MM v[40:43],a[172:175],a[244:247],v[40:43]
s_add_u32 s66, s66, 9216
TR a[152:153],v235 offset:4096
TR a[154:155],v235 offset:5120
MM v[40:43],a[168:171],a[228:231],v[40:43]
MM v[40:43],a[164:167],a[212:215],v[40:43]
TR a[156:157],v235 offset:6144
TR a[158:159],v235 offset:7168
MM v[40:43],a[160:163],a[196:199],v[40:43]
s_waitcnt lgkmcnt(15)
buffer_load_dwordx4 v240, s[24:27], s66 offen offset:1024 lds
MM v[44:47],a[176:179],a[196:199],v[44:47]
s_add_u32 s66, s66, 9216
TR a[160:161],v144 offset:49152
TR a[162:163],v144 offset:51200
MM v[44:47],a[180:183],a[212:215],v[44:47]
MM v[44:47],a[184:187],a[228:231],v[44:47]
TR a[164:165],v144 offset:53248
TR a[166:167],v144 offset:55296
MM v[44:47],a[188:191],a[244:247],v[44:47]
buffer_load_dwordx4 v240, s[24:27], s66 offen offset:2048 lds
MM v[36:39],a[188:191],a[240:243],v[36:39]
s_add_u32 s66, s66, 9216
TR a[168:169],v144 offset:57344
TR a[170:171],v144 offset:59392
MM v[36:39],a[184:187],a[224:227],v[36:39]
MM v[36:39],a[180:183],a[208:211],v[36:39]
TR a[172:173],v144 offset:61440
TR a[174:175],v144 offset:63488
MM v[36:39],a[176:179],a[192:195],v[36:39]
s_waitcnt lgkmcnt(15)
buffer_load_dwordx4 v240, s[24:27], s66 offen offset:3072 lds
MM v[128:131],a[128:131],v[168:171],v[128:131]
TR a[176:177],v145 offset:49152
TR a[178:179],v145 offset:51200
MM v[128:131],a[132:135],v[184:187],v[128:131]
MM v[128:131],a[136:139],v[200:203],v[128:131]
TR a[180:181],v145 offset:53248
TR a[182:183],v145 offset:55296
MM v[128:131],a[140:143],v[216:219],v[128:131]
MM v[136:139],a[140:143],v[220:223],v[136:139]
TR a[184:185],v145 offset:57344
TR a[186:187],v145 offset:59392
MM v[136:139],a[136:139],v[204:207],v[136:139]
MM v[136:139],a[132:135],v[188:191],v[136:139]
TR a[188:189],v145 offset:61440
TR a[190:191],v145 offset:63488
MM v[136:139],a[128:131],v[172:175],v[136:139]
s_waitcnt lgkmcnt(15)
MM v[140:143],a[144:147],v[172:175],v[140:143]
s_xor_b32 s92, s49, 2
TR a[128:129],v146 offset:49152
TR a[130:131],v146 offset:51200
MM v[140:143],a[148:151],v[188:191],v[140:143]
LPG28_%=:
s_cbranch_execz LTR1_%=
s_mul_i32 s92, s92, 17408
s_add_u32 s92, s83, s92
s_add_u32 s92, s92, 8192
MM v[140:143],a[152:155],v[204:207],v[140:143]
TR a[132:133],v146 offset:53248
TR a[134:135],v146 offset:55296
MM v[140:143],a[156:159],v[220:223],v[140:143]
buffer_load_dwordx4 a[24:27], v248, s[20:23], s92 offen offset:0 sc1
MM v[132:135],a[156:159],v[216:219],v[132:135]
TR a[136:137],v146 offset:57344
TR a[138:139],v146 offset:59392
MM v[132:135],a[152:155],v[200:203],v[132:135]
buffer_load_dwordx4 a[56:59], v248, s[20:23], s92 offen offset:1024 sc1
MM v[132:135],a[148:151],v[184:187],v[132:135]
TR a[140:141],v146 offset:61440
TR a[142:143],v146 offset:63488
MM v[132:135],a[144:147],v[168:171],v[132:135]
s_waitcnt lgkmcnt(15)
MM v[64:67],a[160:163],v[168:171],v[64:67]
TR a[144:145],v147 offset:49152
TR a[146:147],v147 offset:51200
MM v[64:67],a[164:167],v[184:187],v[64:67]
buffer_load_dwordx4 a[88:91], v248, s[20:23], s92 offen offset:2048 sc1
MM v[64:67],a[168:171],v[200:203],v[64:67]
TR a[148:149],v147 offset:53248
TR a[150:151],v147 offset:55296
MM v[64:67],a[172:175],v[216:219],v[64:67]
MM v[72:75],a[172:175],v[220:223],v[72:75]
TR a[152:153],v147 offset:57344
TR a[154:155],v147 offset:59392
MM v[72:75],a[168:171],v[204:207],v[72:75]
buffer_load_dwordx4 a[120:123], v248, s[20:23], s92 offen offset:3072 sc1
MM v[72:75],a[164:167],v[188:191],v[72:75]
s_add_u32 s92, s92, 4096
TR a[156:157],v147 offset:61440
TR a[158:159],v147 offset:63488
MM v[72:75],a[160:163],v[172:175],v[72:75]
s_waitcnt lgkmcnt(15)
MM v[76:79],a[176:179],v[172:175],v[76:79]
TR a[160:161],v230 offset:49152
TR a[162:163],v230 offset:51200
MM v[76:79],a[180:183],v[188:191],v[76:79]
buffer_load_dwordx4 a[28:31], v248, s[20:23], s92 offen offset:0 sc1
MM v[76:79],a[184:187],v[204:207],v[76:79]
TR a[164:165],v230 offset:53248
TR a[166:167],v230 offset:55296
MM v[76:79],a[188:191],v[220:223],v[76:79]
MM v[68:71],a[188:191],v[216:219],v[68:71]
TR a[168:169],v230 offset:57344
TR a[170:171],v230 offset:59392
MM v[68:71],a[184:187],v[200:203],v[68:71]
buffer_load_dwordx4 a[60:63], v248, s[20:23], s92 offen offset:1024 sc1
MM v[68:71],a[180:183],v[184:187],v[68:71]
TR a[172:173],v230 offset:61440
TR a[174:175],v230 offset:63488
MM v[68:71],a[176:179],v[168:171],v[68:71]
s_waitcnt lgkmcnt(15)
MM v[96:99],a[128:131],v[168:171],v[96:99]
TR a[176:177],v231 offset:49152
TR a[178:179],v231 offset:51200
MM v[96:99],a[132:135],v[184:187],v[96:99]
buffer_load_dwordx4 a[92:95], v248, s[20:23], s92 offen offset:2048 sc1
MM v[96:99],a[136:139],v[200:203],v[96:99]
TR a[180:181],v231 offset:53248
TR a[182:183],v231 offset:55296
MM v[96:99],a[140:143],v[216:219],v[96:99]
MM v[104:107],a[140:143],v[220:223],v[104:107]
TR a[184:185],v231 offset:57344
TR a[186:187],v231 offset:59392
MM v[104:107],a[136:139],v[204:207],v[104:107]
buffer_load_dwordx4 a[124:127], v248, s[20:23], s92 offen offset:3072 sc1
MM v[104:107],a[132:135],v[188:191],v[104:107]
s_xor_b32 s92, s49, 3
TR a[188:189],v231 offset:61440
TR a[190:191],v231 offset:63488
MM v[104:107],a[128:131],v[172:175],v[104:107]
s_waitcnt lgkmcnt(15)
s_mul_i32 s92, s92, 17408
s_add_u32 s92, s83, s92
s_waitcnt vmcnt(32)
s_barrier
MM v[108:111],a[144:147],v[172:175],v[108:111]
s_add_u32 s92, s92, 8192
TR a[128:129],v244 offset:8192
TR a[130:131],v244 offset:9216
MM v[108:111],a[148:151],v[188:191],v[108:111]
buffer_load_dwordx4 a[200:203], v248, s[20:23], s92 offen offset:0 sc1
MM v[108:111],a[152:155],v[204:207],v[108:111]
TR a[132:133],v244 offset:10240
TR a[134:135],v244 offset:11264
MM v[108:111],a[156:159],v[220:223],v[108:111]
MM v[100:103],a[156:159],v[216:219],v[100:103]
TR a[136:137],v244 offset:12288
TR a[138:139],v244 offset:13312
MM v[100:103],a[152:155],v[200:203],v[100:103]
buffer_load_dwordx4 a[216:219], v248, s[20:23], s92 offen offset:1024 sc1
MM v[100:103],a[148:151],v[184:187],v[100:103]
TR a[140:141],v244 offset:14336
TR a[142:143],v244 offset:15360
MM v[100:103],a[144:147],v[168:171],v[100:103]
s_waitcnt lgkmcnt(15)
MM v[32:35],a[160:163],v[168:171],v[32:35]
TR a[144:145],v235 offset:8192
TR a[146:147],v235 offset:9216
MM v[32:35],a[164:167],v[184:187],v[32:35]
buffer_load_dwordx4 a[232:235], v248, s[20:23], s92 offen offset:2048 sc1
MM v[32:35],a[168:171],v[200:203],v[32:35]
TR a[148:149],v235 offset:10240
TR a[150:151],v235 offset:11264
MM v[32:35],a[172:175],v[216:219],v[32:35]
MM v[40:43],a[172:175],v[220:223],v[40:43]
TR a[152:153],v235 offset:12288
TR a[154:155],v235 offset:13312
MM v[40:43],a[168:171],v[204:207],v[40:43]
)ASM"
R"ASM(
buffer_load_dwordx4 a[248:251], v248, s[20:23], s92 offen offset:3072 sc1
MM v[40:43],a[164:167],v[188:191],v[40:43]
s_add_u32 s92, s92, 4096
TR a[156:157],v235 offset:14336
TR a[158:159],v235 offset:15360
MM v[40:43],a[160:163],v[172:175],v[40:43]
s_waitcnt lgkmcnt(15)
MM v[44:47],a[176:179],v[172:175],v[44:47]
TR a[160:161],v144 offset:0
TR a[162:163],v144 offset:2048
MM v[44:47],a[180:183],v[188:191],v[44:47]
buffer_load_dwordx4 a[204:207], v248, s[20:23], s92 offen offset:0 sc1
MM v[44:47],a[184:187],v[204:207],v[44:47]
TR a[164:165],v144 offset:4096
TR a[166:167],v144 offset:6144
MM v[44:47],a[188:191],v[220:223],v[44:47]
MM v[36:39],a[188:191],v[216:219],v[36:39]
TR a[168:169],v144 offset:8192
TR a[170:171],v144 offset:10240
MM v[36:39],a[184:187],v[200:203],v[36:39]
buffer_load_dwordx4 a[220:223], v248, s[20:23], s92 offen offset:1024 sc1
MM v[36:39],a[180:183],v[184:187],v[36:39]
TR a[172:173],v144 offset:12288
TR a[174:175],v144 offset:14336
MM v[36:39],a[176:179],v[168:171],v[36:39]
s_waitcnt lgkmcnt(15)
s_waitcnt vmcnt(24)
MM v[128:131],a[128:131],a[16:19],v[128:131]
TR a[176:177],v145 offset:0
TR a[178:179],v145 offset:2048
MM v[128:131],a[132:135],a[48:51],v[128:131]
buffer_load_dwordx4 a[236:239], v248, s[20:23], s92 offen offset:2048 sc1
MM v[128:131],a[136:139],a[80:83],v[128:131]
TR a[180:181],v145 offset:4096
TR a[182:183],v145 offset:6144
MM v[128:131],a[140:143],a[112:115],v[128:131]
s_waitcnt vmcnt(21)
MM v[136:139],a[140:143],a[116:119],v[136:139]
TR a[184:185],v145 offset:8192
TR a[186:187],v145 offset:10240
MM v[136:139],a[136:139],a[84:87],v[136:139]
buffer_load_dwordx4 a[252:255], v248, s[20:23], s92 offen offset:3072 sc1
MM v[136:139],a[132:135],a[52:55],v[136:139]
TR a[188:189],v145 offset:12288
TR a[190:191],v145 offset:14336
MM v[136:139],a[128:131],a[20:23],v[136:139]
s_waitcnt lgkmcnt(15)
MM v[140:143],a[144:147],a[20:23],v[140:143]
TR a[128:129],v146 offset:0
TR a[130:131],v146 offset:2048
MM v[140:143],a[148:151],a[52:55],v[140:143]
MM v[140:143],a[152:155],a[84:87],v[140:143]
TR a[132:133],v146 offset:4096
TR a[134:135],v146 offset:6144
MM v[140:143],a[156:159],a[116:119],v[140:143]
MM v[132:135],a[156:159],a[112:115],v[132:135]
TR a[136:137],v146 offset:8192
TR a[138:139],v146 offset:10240
MM v[132:135],a[152:155],a[80:83],v[132:135]
MM v[132:135],a[148:151],a[48:51],v[132:135]
TR a[140:141],v146 offset:12288
TR a[142:143],v146 offset:14336
MM v[132:135],a[144:147],a[16:19],v[132:135]
s_waitcnt lgkmcnt(15)
MM v[64:67],a[160:163],a[16:19],v[64:67]
TR a[144:145],v147 offset:0
TR a[146:147],v147 offset:2048
MM v[64:67],a[164:167],a[48:51],v[64:67]
MM v[64:67],a[168:171],a[80:83],v[64:67]
TR a[148:149],v147 offset:4096
TR a[150:151],v147 offset:6144
MM v[64:67],a[172:175],a[112:115],v[64:67]
MM v[72:75],a[172:175],a[116:119],v[72:75]
TR a[152:153],v147 offset:8192
TR a[154:155],v147 offset:10240
MM v[72:75],a[168:171],a[84:87],v[72:75]
MM v[72:75],a[164:167],a[52:55],v[72:75]
TR a[156:157],v147 offset:12288
TR a[158:159],v147 offset:14336
MM v[72:75],a[160:163],a[20:23],v[72:75]
s_waitcnt lgkmcnt(15)
s_waitcnt vmcnt(16)
s_barrier
MM v[76:79],a[176:179],a[20:23],v[76:79]
s_xor_b32 s66, s94, 1
TR a[160:161],v244 offset:16384
TR a[162:163],v244 offset:17408
MM v[76:79],a[180:183],a[52:55],v[76:79]
s_mul_i32 s66, s66, 163840
s_add_u32 s66, s66, s86
s_add_u32 m0, s65, 0
MM v[76:79],a[184:187],a[84:87],v[76:79]
TR a[164:165],v244 offset:18432
TR a[166:167],v244 offset:19456
MM v[76:79],a[188:191],a[116:119],v[76:79]
buffer_load_dwordx4 v240, s[24:27], s66 offen offset:0 lds
MM v[68:71],a[188:191],a[112:115],v[68:71]
s_add_u32 s66, s66, 9216
TR a[168:169],v244 offset:20480
TR a[170:171],v244 offset:21504
MM v[68:71],a[184:187],a[80:83],v[68:71]
MM v[68:71],a[180:183],a[48:51],v[68:71]
TR a[172:173],v244 offset:22528
TR a[174:175],v244 offset:23552
MM v[68:71],a[176:179],a[16:19],v[68:71]
s_waitcnt lgkmcnt(15)
buffer_load_dwordx4 v240, s[24:27], s66 offen offset:1024 lds
MM v[96:99],a[128:131],a[16:19],v[96:99]
s_add_u32 s66, s66, 9216
TR a[176:177],v244 offset:24576
TR a[178:179],v244 offset:25600
MM v[96:99],a[132:135],a[48:51],v[96:99]
MM v[96:99],a[136:139],a[80:83],v[96:99]
TR a[180:181],v244 offset:26624
TR a[182:183],v244 offset:27648
MM v[96:99],a[140:143],a[112:115],v[96:99]
buffer_load_dwordx4 v240, s[24:27], s66 offen offset:2048 lds
MM v[104:107],a[140:143],a[116:119],v[104:107]
s_add_u32 s66, s66, 9216
TR a[184:185],v244 offset:28672
TR a[186:187],v244 offset:29696
MM v[104:107],a[136:139],a[84:87],v[104:107]
MM v[104:107],a[132:135],a[52:55],v[104:107]
TR a[188:189],v244 offset:30720
TR a[190:191],v244 offset:31744
MM v[104:107],a[128:131],a[20:23],v[104:107]
s_waitcnt lgkmcnt(15)
buffer_load_dwordx4 v240, s[24:27], s66 offen offset:3072 lds
MM v[108:111],a[144:147],a[20:23],v[108:111]
TR a[128:129],v235 offset:16384
TR a[130:131],v235 offset:17408
MM v[108:111],a[148:151],a[52:55],v[108:111]
s_xor_b32 s66, s94, 2
s_mul_i32 s66, s66, 163840
s_add_u32 s66, s66, s86
MM v[108:111],a[152:155],a[84:87],v[108:111]
s_add_u32 m0, s65, 16384
TR a[132:133],v235 offset:18432
TR a[134:135],v235 offset:19456
MM v[108:111],a[156:159],a[116:119],v[108:111]
buffer_load_dwordx4 v240, s[24:27], s66 offen offset:0 lds
MM v[100:103],a[156:159],a[112:115],v[100:103]
s_add_u32 s66, s66, 9216
TR a[136:137],v235 offset:20480
TR a[138:139],v235 offset:21504
MM v[100:103],a[152:155],a[80:83],v[100:103]
MM v[100:103],a[148:151],a[48:51],v[100:103]
TR a[140:141],v235 offset:22528
TR a[142:143],v235 offset:23552
MM v[100:103],a[144:147],a[16:19],v[100:103]
s_waitcnt lgkmcnt(15)
buffer_load_dwordx4 v240, s[24:27], s66 offen offset:1024 lds
s_waitcnt vmcnt(18)
MM v[128:131],a[160:163],a[24:27],v[128:131]
s_add_u32 s66, s66, 9216
TR a[144:145],v235 offset:24576
TR a[146:147],v235 offset:25600
MM v[128:131],a[164:167],a[56:59],v[128:131]
MM v[128:131],a[168:171],a[88:91],v[128:131]
TR a[148:149],v235 offset:26624
TR a[150:151],v235 offset:27648
MM v[128:131],a[172:175],a[120:123],v[128:131]
s_waitcnt vmcnt(14)
buffer_load_dwordx4 v240, s[24:27], s66 offen offset:2048 lds
MM v[136:139],a[172:175],a[124:127],v[136:139]
s_add_u32 s66, s66, 9216
TR a[152:153],v235 offset:28672
TR a[154:155],v235 offset:29696
MM v[136:139],a[168:171],a[92:95],v[136:139]
MM v[136:139],a[164:167],a[60:63],v[136:139]
TR a[156:157],v235 offset:30720
TR a[158:159],v235 offset:31744
MM v[136:139],a[160:163],a[28:31],v[136:139]
s_waitcnt lgkmcnt(15)
buffer_load_dwordx4 v240, s[24:27], s66 offen offset:3072 lds
s_waitcnt vmcnt(12)
MM v[128:131],a[176:179],a[200:203],v[128:131]
s_memrealtime s[56:57]
TR a[160:161],v144 offset:16384
TR a[162:163],v144 offset:18432
MM v[128:131],a[180:183],a[216:219],v[128:131]
s_xor_b32 s66, s94, 3
s_mul_i32 s66, s66, 163840
s_add_u32 s66, s66, s86
MM v[128:131],a[184:187],a[232:235],v[128:131]
s_add_u32 m0, s65, 32768
TR a[164:165],v144 offset:20480
TR a[166:167],v144 offset:22528
MM v[128:131],a[188:191],a[248:251],v[128:131]
s_waitcnt vmcnt(8)
buffer_load_dwordx4 v240, s[24:27], s66 offen offset:0 lds
MM v[136:139],a[188:191],a[252:255],v[136:139]
s_add_u32 s66, s66, 9216
TR a[168:169],v144 offset:24576
TR a[170:171],v144 offset:26624
MM v[136:139],a[184:187],a[236:239],v[136:139]
MM v[136:139],a[180:183],a[220:223],v[136:139]
TR a[172:173],v144 offset:28672
TR a[174:175],v144 offset:30720
MM v[136:139],a[176:179],a[204:207],v[136:139]
s_waitcnt lgkmcnt(0)
buffer_load_dwordx4 v240, s[24:27], s66 offen offset:1024 lds
MM v[140:143],a[128:131],a[28:31],v[140:143]
s_add_u32 s66, s66, 9216
TR a[176:177],v144 offset:32768
TR a[178:179],v144 offset:34816
MM v[140:143],a[132:135],a[60:63],v[140:143]
v_mov_b32 v158, s56
v_mov_b32 v159, s69
MM v[140:143],a[136:139],a[92:95],v[140:143]
TR a[180:181],v144 offset:36864
TR a[182:183],v144 offset:38912
MM v[140:143],a[140:143],a[124:127],v[140:143]
s_add_u32 s67, s95, 0
CV v128,v128,v129
CV v129,v130,v131
MM v[132:135],a[140:143],a[120:123],v[132:135]
buffer_load_dwordx4 v240, s[24:27], s66 offen offset:2048 lds
s_mov_b64 exec, 1
buffer_store_dwordx2 v[158:159], off, s[20:23], s19 offset:8 sc1
s_mov_b64 exec, -1
s_nop 0
TR a[184:185],v144 offset:40960
TR a[186:187],v144 offset:43008
MM v[132:135],a[136:139],a[88:91],v[132:135]
s_add_u32 s66, s66, 9216
CV v136,v136,v137
CV v137,v138,v139
MM v[132:135],a[132:135],a[56:59],v[132:135]
TR a[188:189],v144 offset:45056
TR a[190:191],v144 offset:47104
MM v[132:135],a[128:131],a[24:27],v[132:135]
buffer_load_dwordx4 v240, s[24:27], s66 offen offset:3072 lds
MM v[132:135],a[144:147],a[200:203],v[132:135]
TR a[128:129],v145 offset:16384
TR a[130:131],v145 offset:18432
MM v[132:135],a[148:151],a[216:219],v[132:135]
MM v[132:135],a[152:155],a[232:235],v[132:135]
TR a[132:133],v145 offset:20480
TR a[134:135],v145 offset:22528
MM v[132:135],a[156:159],a[248:251],v[132:135]
MM v[140:143],a[156:159],a[252:255],v[140:143]
TR a[136:137],v145 offset:24576
TR a[138:139],v145 offset:26624
MM v[140:143],a[152:155],a[236:239],v[140:143]
MM v[140:143],a[148:151],a[220:223],v[140:143]
TR a[140:141],v145 offset:28672
TR a[142:143],v145 offset:30720
MM v[140:143],a[144:147],a[204:207],v[140:143]
CV v130,v132,v133
CV v131,v134,v135
MM v[72:75],a[160:163],a[28:31],v[72:75]
TR a[144:145],v145 offset:32768
TR a[146:147],v145 offset:34816
MM v[72:75],a[164:167],a[60:63],v[72:75]
v_min_u32 v131, -2, v131
MM v[72:75],a[168:171],a[92:95],v[72:75]
TR a[148:149],v145 offset:36864
TR a[150:151],v145 offset:38912
MM v[72:75],a[172:175],a[124:127],v[72:75]
buffer_store_dwordx4 v[128:131], v249, s[20:23], s67 offen offset:0 sc1
MM v[64:67],a[172:175],a[120:123],v[64:67]
TR a[152:153],v145 offset:40960
TR a[154:155],v145 offset:43008
MM v[64:67],a[168:171],a[88:91],v[64:67]
CV v138,v140,v141
CV v139,v142,v143
MM v[64:67],a[164:167],a[56:59],v[64:67]
TR a[156:157],v145 offset:45056
TR a[158:159],v145 offset:47104
MM v[64:67],a[160:163],a[24:27],v[64:67]
s_waitcnt lgkmcnt(15)
v_min_u32 v139, -2, v139
MM v[64:67],a[176:179],a[200:203],v[64:67]
TR a[160:161],v146 offset:16384
TR a[162:163],v146 offset:18432
MM v[64:67],a[180:183],a[216:219],v[64:67]
buffer_store_dwordx4 v[136:139], v249, s[20:23], s67 offen offset:1024 sc1
MM v[64:67],a[184:187],a[232:235],v[64:67]
TR a[164:165],v146 offset:20480
TR a[166:167],v146 offset:22528
MM v[64:67],a[188:191],a[248:251],v[64:67]
MM v[72:75],a[188:191],a[252:255],v[72:75]
TR a[168:169],v146 offset:24576
TR a[170:171],v146 offset:26624
MM v[72:75],a[184:187],a[236:239],v[72:75]
MM v[72:75],a[180:183],a[220:223],v[72:75]
TR a[172:173],v146 offset:28672
TR a[174:175],v146 offset:30720
MM v[72:75],a[176:179],a[204:207],v[72:75]
s_waitcnt lgkmcnt(15)
MM v[76:79],a[128:131],a[28:31],v[76:79]
TR a[176:177],v146 offset:32768
TR a[178:179],v146 offset:34816
MM v[76:79],a[132:135],a[60:63],v[76:79]
v_lshlrev_b32 v136, 16, v150
v_and_b32 v137, 0xffff0000, v150
MM v[76:79],a[136:139],a[92:95],v[76:79]
TR a[180:181],v146 offset:36864
TR a[182:183],v146 offset:38912
MM v[76:79],a[140:143],a[124:127],v[76:79]
s_add_u32 s67, s95, 4096
CV v64,v64,v65
CV v65,v66,v67
MM v[68:71],a[140:143],a[120:123],v[68:71]
TR a[184:185],v146 offset:40960
TR a[186:187],v146 offset:43008
MM v[68:71],a[136:139],a[88:91],v[68:71]
CV v72,v72,v73
CV v73,v74,v75
MM v[68:71],a[132:135],a[56:59],v[68:71]
TR a[188:189],v146 offset:45056
TR a[190:191],v146 offset:47104
MM v[68:71],a[128:131],a[24:27],v[68:71]
s_waitcnt lgkmcnt(15)
v_lshlrev_b32 v138, 16, v151
v_and_b32 v139, 0xffff0000, v151
MM v[68:71],a[144:147],a[200:203],v[68:71]
TR a[128:129],v147 offset:16384
TR a[130:131],v147 offset:18432
MM v[68:71],a[148:151],a[216:219],v[68:71]
buffer_load_dwordx2 v[148:149], v253, s[20:23], s68 offen offset:8 sc1
MM v[68:71],a[152:155],a[232:235],v[68:71]
TR a[132:133],v147 offset:20480
TR a[134:135],v147 offset:22528
MM v[68:71],a[156:159],a[248:251],v[68:71]
v_mul_f32 v140, 0xbfb8aa3b, v136
v_mul_f32 v141, 0xbfb8aa3b, v137
MM v[76:79],a[156:159],a[252:255],v[76:79]
TR a[136:137],v147 offset:24576
TR a[138:139],v147 offset:26624
)ASM"
R"ASM(
MM v[76:79],a[152:155],a[236:239],v[76:79]
v_mul_f32 v142, 0xbfb8aa3b, v138
v_mul_f32 v143, 0xbfb8aa3b, v139
MM v[76:79],a[148:151],a[220:223],v[76:79]
TR a[140:141],v147 offset:28672
TR a[142:143],v147 offset:30720
MM v[76:79],a[144:147],a[204:207],v[76:79]
s_waitcnt lgkmcnt(15)
CV v66,v68,v69
CV v67,v70,v71
s_waitcnt vmcnt(3)
s_barrier
MM v[104:107],a[160:163],a[28:31],v[104:107]
TR a[144:145],v147 offset:32768
TR a[146:147],v147 offset:34816
MM v[104:107],a[164:167],a[60:63],v[104:107]
EX v140,v140
v_min_u32 v67, -2, v67
MM v[104:107],a[168:171],a[92:95],v[104:107]
TR a[148:149],v147 offset:36864
TR a[150:151],v147 offset:38912
MM v[104:107],a[172:175],a[124:127],v[104:107]
buffer_store_dwordx4 v[64:67], v248, s[20:23], s67 offen offset:0 sc1
MM v[96:99],a[172:175],a[120:123],v[96:99]
TR a[152:153],v147 offset:40960
TR a[154:155],v147 offset:43008
MM v[96:99],a[168:171],a[88:91],v[96:99]
CV v74,v76,v77
CV v75,v78,v79
MM v[96:99],a[164:167],a[56:59],v[96:99]
TR a[156:157],v147 offset:45056
TR a[158:159],v147 offset:47104
MM v[96:99],a[160:163],a[24:27],v[96:99]
s_waitcnt lgkmcnt(15)
EX v141,v141
v_min_u32 v75, -2, v75
MM v[96:99],a[176:179],a[200:203],v[96:99]
TR a[160:161],v230 offset:0
TR a[162:163],v230 offset:2048
MM v[96:99],a[180:183],a[216:219],v[96:99]
buffer_store_dwordx4 v[72:75], v248, s[20:23], s67 offen offset:1024 sc1
MM v[96:99],a[184:187],a[232:235],v[96:99]
TR a[164:165],v230 offset:4096
TR a[166:167],v230 offset:6144
MM v[96:99],a[188:191],a[248:251],v[96:99]
EX v142,v142
MM v[104:107],a[188:191],a[252:255],v[104:107]
TR a[168:169],v230 offset:8192
TR a[170:171],v230 offset:10240
MM v[104:107],a[184:187],a[236:239],v[104:107]
EX v143,v143
MM v[104:107],a[180:183],a[220:223],v[104:107]
TR a[172:173],v230 offset:12288
TR a[174:175],v230 offset:14336
MM v[104:107],a[176:179],a[204:207],v[104:107]
s_waitcnt lgkmcnt(15)
v_add_f32 v140, 1.0, v140
v_add_f32 v141, 1.0, v141
MM v[108:111],a[128:131],a[28:31],v[108:111]
TR a[176:177],v230 offset:16384
TR a[178:179],v230 offset:18432
MM v[108:111],a[132:135],a[60:63],v[108:111]
v_add_f32 v142, 1.0, v142
v_add_f32 v143, 1.0, v143
MM v[108:111],a[136:139],a[92:95],v[108:111]
TR a[180:181],v230 offset:20480
TR a[182:183],v230 offset:22528
MM v[108:111],a[140:143],a[124:127],v[108:111]
s_add_u32 s67, s95, 4096
CV v96,v96,v97
CV v97,v98,v99
MM v[100:103],a[140:143],a[120:123],v[100:103]
TR a[184:185],v230 offset:24576
TR a[186:187],v230 offset:26624
MM v[100:103],a[136:139],a[88:91],v[100:103]
CV v104,v104,v105
CV v105,v106,v107
MM v[100:103],a[132:135],a[56:59],v[100:103]
TR a[188:189],v230 offset:28672
TR a[190:191],v230 offset:30720
MM v[100:103],a[128:131],a[24:27],v[100:103]
s_waitcnt lgkmcnt(15)
v_rcp_f32 v140, v140
MM v[100:103],a[144:147],a[200:203],v[100:103]
TR a[128:129],v230 offset:32768
TR a[130:131],v230 offset:34816
MM v[100:103],a[148:151],a[216:219],v[100:103]
v_rcp_f32 v141, v141
MM v[100:103],a[152:155],a[232:235],v[100:103]
TR a[132:133],v230 offset:36864
TR a[134:135],v230 offset:38912
MM v[100:103],a[156:159],a[248:251],v[100:103]
v_rcp_f32 v142, v142
MM v[108:111],a[156:159],a[252:255],v[108:111]
TR a[136:137],v230 offset:40960
TR a[138:139],v230 offset:43008
MM v[108:111],a[152:155],a[236:239],v[108:111]
v_rcp_f32 v143, v143
MM v[108:111],a[148:151],a[220:223],v[108:111]
TR a[140:141],v230 offset:45056
s_nop 0
LPG29_%=:
s_cbranch_execz LTR1_%=
TR a[142:143],v230 offset:47104
MM v[108:111],a[144:147],a[204:207],v[108:111]
s_waitcnt lgkmcnt(15)
CV v98,v100,v101
CV v99,v102,v103
MM v[40:43],a[160:163],a[20:23],v[40:43]
TR a[144:145],v231 offset:0
TR a[146:147],v231 offset:2048
MM v[40:43],a[164:167],a[52:55],v[40:43]
v_min_u32 v99, -2, v99
v_mul_f32 v140, v136, v140
MM v[40:43],a[168:171],a[84:87],v[40:43]
TR a[148:149],v231 offset:4096
TR a[150:151],v231 offset:6144
MM v[40:43],a[172:175],a[116:119],v[40:43]
buffer_store_dwordx4 v[96:99], v248, s[20:23], s67 offen offset:2048 sc1
MM v[32:35],a[172:175],a[112:115],v[32:35]
TR a[152:153],v231 offset:8192
TR a[154:155],v231 offset:10240
MM v[32:35],a[168:171],a[80:83],v[32:35]
CV v106,v108,v109
CV v107,v110,v111
MM v[32:35],a[164:167],a[48:51],v[32:35]
TR a[156:157],v231 offset:12288
TR a[158:159],v231 offset:14336
MM v[32:35],a[160:163],a[16:19],v[32:35]
s_waitcnt lgkmcnt(15)
v_min_u32 v107, -2, v107
v_mul_f32 v141, v137, v141
MM v[32:35],a[176:179],a[24:27],v[32:35]
TR a[160:161],v231 offset:16384
TR a[162:163],v231 offset:18432
MM v[32:35],a[180:183],a[56:59],v[32:35]
buffer_store_dwordx4 v[104:107], v248, s[20:23], s67 offen offset:3072 sc1
MM v[32:35],a[184:187],a[88:91],v[32:35]
TR a[164:165],v231 offset:20480
TR a[166:167],v231 offset:22528
MM v[32:35],a[188:191],a[120:123],v[32:35]
v_mul_f32 v142, v138, v142
v_mul_f32 v143, v139, v143
MM v[40:43],a[188:191],a[124:127],v[40:43]
TR a[168:169],v231 offset:24576
TR a[170:171],v231 offset:26624
MM v[40:43],a[184:187],a[92:95],v[40:43]
CV v150,v140,v141
CV v151,v142,v143
MM v[40:43],a[180:183],a[60:63],v[40:43]
TR a[172:173],v231 offset:28672
TR a[174:175],v231 offset:30720
MM v[40:43],a[176:179],a[28:31],v[40:43]
s_waitcnt lgkmcnt(15)
v_lshlrev_b32 v160, 16, v16
v_and_b32 v161, 0xffff0000, v16
MM v[40:43],a[128:131],a[204:207],v[40:43]
TR a[176:177],v231 offset:32768
TR a[178:179],v231 offset:34816
MM v[40:43],a[132:135],a[220:223],v[40:43]
v_lshlrev_b32 v162, 16, v17
v_and_b32 v163, 0xffff0000, v17
MM v[40:43],a[136:139],a[236:239],v[40:43]
TR a[180:181],v231 offset:36864
TR a[182:183],v231 offset:38912
MM v[40:43],a[140:143],a[252:255],v[40:43]
v_lshlrev_b32 v164, 16, v18
v_and_b32 v165, 0xffff0000, v18
MM v[32:35],a[140:143],a[248:251],v[32:35]
TR a[184:185],v231 offset:40960
TR a[186:187],v231 offset:43008
MM v[32:35],a[136:139],a[232:235],v[32:35]
v_lshlrev_b32 v166, 16, v19
v_and_b32 v167, 0xffff0000, v19
MM v[32:35],a[132:135],a[216:219],v[32:35]
TR a[188:189],v231 offset:45056
TR a[190:191],v231 offset:47104
MM v[32:35],a[128:131],a[200:203],v[32:35]
s_waitcnt lgkmcnt(15)
v_mul_f32 v136, 0xbfb8aa3b, v160
v_mul_f32 v137, 0xbfb8aa3b, v161
MM v[36:39],a[144:147],a[16:19],v[36:39]
TR a[128:129],v228 offset:49152
TR a[130:131],v228 offset:51200
MM v[36:39],a[148:151],a[48:51],v[36:39]
v_mul_f32 v138, 0xbfb8aa3b, v162
v_mul_f32 v139, 0xbfb8aa3b, v163
MM v[36:39],a[152:155],a[80:83],v[36:39]
TR a[132:133],v228 offset:53248
TR a[134:135],v228 offset:55296
MM v[36:39],a[156:159],a[112:115],v[36:39]
s_add_u32 s67, s95, 0
CV v32,v32,v33
CV v33,v34,v35
MM v[44:47],a[156:159],a[116:119],v[44:47]
TR a[136:137],v228 offset:57344
TR a[138:139],v228 offset:59392
MM v[44:47],a[152:155],a[84:87],v[44:47]
CV v40,v40,v41
CV v41,v42,v43
MM v[44:47],a[148:151],a[52:55],v[44:47]
TR a[140:141],v228 offset:61440
TR a[142:143],v228 offset:63488
MM v[44:47],a[144:147],a[20:23],v[44:47]
s_waitcnt lgkmcnt(15)
v_mul_f32 v140, 0xbfb8aa3b, v164
v_mul_f32 v141, 0xbfb8aa3b, v165
MM v[44:47],a[160:163],a[28:31],v[44:47]
s_waitcnt vmcnt(4)
TR a[144:145],v228 offset:0
TR a[146:147],v228 offset:2048
MM v[44:47],a[164:167],a[60:63],v[44:47]
v_cmp_eq_u32 s[8:9], s69, v149
v_subrev_u32 v226, s56, v148
MM v[44:47],a[168:171],a[92:95],v[44:47]
TR a[148:149],v228 offset:4096
TR a[150:151],v228 offset:6144
MM v[44:47],a[172:175],a[124:127],v[44:47]
v_cndmask_b32 v226, 4.0, v226, s[8:9]
v_mul_f32 v142, 0xbfb8aa3b, v166
MM v[36:39],a[172:175],a[120:123],v[36:39]
TR a[152:153],v228 offset:8192
TR a[154:155],v228 offset:10240
MM v[36:39],a[168:171],a[88:91],v[36:39]
v_readlane_b32 s13, v226, 16
v_readlane_b32 s14, v226, 32
MM v[36:39],a[164:167],a[56:59],v[36:39]
TR a[156:157],v228 offset:12288
TR a[158:159],v228 offset:14336
MM v[36:39],a[160:163],a[24:27],v[36:39]
s_waitcnt lgkmcnt(15)
v_readlane_b32 s15, v226, 48
v_mul_f32 v143, 0xbfb8aa3b, v167
MM v[36:39],a[176:179],a[200:203],v[36:39]
s_max_i32 s12, s13, s14
TR a[160:161],v228 offset:16384
TR a[162:163],v228 offset:18432
MM v[36:39],a[180:183],a[216:219],v[36:39]
s_max_i32 s12, s12, s15
EX v136,v136
MM v[36:39],a[184:187],a[232:235],v[36:39]
s_max_i32 s12, s12, 0
TR a[164:165],v228 offset:20480
TR a[166:167],v228 offset:22528
MM v[36:39],a[188:191],a[248:251],v[36:39]
buffer_load_dwordx2 v[148:149], v253, s[20:23], s68 offen offset:8 sc1
MM v[44:47],a[188:191],a[252:255],v[44:47]
TR a[168:169],v228 offset:24576
TR a[170:171],v228 offset:26624
MM v[44:47],a[184:187],a[236:239],v[44:47]
EX v137,v137
MM v[44:47],a[180:183],a[220:223],v[44:47]
TR a[172:173],v228 offset:28672
TR a[174:175],v228 offset:30720
MM v[44:47],a[176:179],a[204:207],v[44:47]
s_waitcnt lgkmcnt(15)
CV v34,v36,v37
CV v35,v38,v39
v_min_u32 v35, -2, v35
buffer_store_dwordx4 v[32:35], v248, s[20:23], s67 offen offset:2048 sc1
MM v[8:11],a[128:131],v[172:175],v[8:11]
TR a[176:177],v228 offset:32768
TR a[178:179],v228 offset:34816
MM v[8:11],a[132:135],v[188:191],v[8:11]
EX v138,v138
MM v[8:11],a[136:139],v[204:207],v[8:11]
TR a[180:181],v228 offset:36864
TR a[182:183],v228 offset:38912
MM v[8:11],a[140:143],v[220:223],v[8:11]
CV v42,v44,v45
CV v43,v46,v47
v_min_u32 v43, -2, v43
buffer_store_dwordx4 v[40:43], v248, s[20:23], s67 offen offset:3072 sc1
MM v[0:3],a[140:143],v[216:219],v[0:3]
TR a[184:185],v228 offset:40960
TR a[186:187],v228 offset:43008
MM v[0:3],a[136:139],v[200:203],v[0:3]
EX v139,v139
MM v[0:3],a[132:135],v[184:187],v[0:3]
TR a[188:189],v228 offset:45056
TR a[190:191],v228 offset:47104
MM v[0:3],a[128:131],v[168:171],v[0:3]
s_waitcnt lgkmcnt(15)
EX v140,v140
MM v[0:3],a[144:147],a[16:19],v[0:3]
TR a[128:129],v229 offset:49152
TR a[130:131],v229 offset:51200
MM v[0:3],a[148:151],a[48:51],v[0:3]
s_cmp_le_i32 s13, -42
s_cselect_b32 s60, 2, 0
s_cselect_b64 s[8:9], -1, 0
MM v[0:3],a[152:155],a[80:83],v[0:3]
s_or_b32 s58, s58, s60
TR a[132:133],v229 offset:53248
TR a[134:135],v229 offset:55296
MM v[0:3],a[156:159],a[112:115],v[0:3]
s_mov_b64 exec, s[8:9]
buffer_load_dwordx4 v[80:83], v248, s[20:23], s36 offen offset:0 sc1
s_mov_b64 exec, -1
MM v[8:11],a[156:159],a[116:119],v[8:11]
TR a[136:137],v229 offset:57344
TR a[138:139],v229 offset:59392
MM v[8:11],a[152:155],a[84:87],v[8:11]
s_mov_b64 exec, s[8:9]
buffer_load_dwordx4 v[84:87], v248, s[20:23], s36 offen offset:1024 sc1
s_mov_b64 exec, -1
MM v[8:11],a[148:151],a[52:55],v[8:11]
TR a[140:141],v229 offset:61440
TR a[142:143],v229 offset:63488
MM v[8:11],a[144:147],a[20:23],v[8:11]
s_waitcnt lgkmcnt(15)
EX v141,v141
MM v[8:11],a[160:163],a[28:31],v[8:11]
TR a[144:145],v229 offset:0
TR a[146:147],v229 offset:2048
MM v[8:11],a[164:167],a[60:63],v[8:11]
s_cmp_le_i32 s15, 17
s_cselect_b32 s60, 8, 0
s_cselect_b64 s[8:9], -1, 0
MM v[8:11],a[168:171],a[92:95],v[8:11]
s_or_b32 s58, s58, s60
TR a[148:149],v229 offset:4096
TR a[150:151],v229 offset:6144
MM v[8:11],a[172:175],a[124:127],v[8:11]
s_mov_b64 exec, s[8:9]
buffer_load_dwordx4 v[112:115], v248, s[20:23], s38 offen offset:0 sc1
s_mov_b64 exec, -1
MM v[0:3],a[172:175],a[120:123],v[0:3]
TR a[152:153],v229 offset:8192
TR a[154:155],v229 offset:10240
MM v[0:3],a[168:171],a[88:91],v[0:3]
s_mov_b64 exec, s[8:9]
buffer_load_dwordx4 v[116:119], v248, s[20:23], s38 offen offset:1024 sc1
s_mov_b64 exec, -1
MM v[0:3],a[164:167],a[56:59],v[0:3]
TR a[156:157],v229 offset:12288
TR a[158:159],v229 offset:14336
MM v[0:3],a[160:163],a[24:27],v[0:3]
s_waitcnt lgkmcnt(15)
EX v142,v142
MM v[0:3],a[176:179],a[200:203],v[0:3]
TR a[160:161],v229 offset:16384
TR a[162:163],v229 offset:18432
MM v[0:3],a[180:183],a[216:219],v[0:3]
s_cmp_le_i32 s14, 59
s_cselect_b32 s60, 4, 0
s_cselect_b64 s[8:9], -1, 0
MM v[0:3],a[184:187],a[232:235],v[0:3]
s_or_b32 s58, s58, s60
TR a[164:165],v229 offset:20480
TR a[166:167],v229 offset:22528
MM v[0:3],a[188:191],a[248:251],v[0:3]
s_mov_b64 exec, s[8:9]
buffer_load_dwordx4 v[88:91], v248, s[20:23], s37 offen offset:0 sc1
s_mov_b64 exec, -1
MM v[8:11],a[188:191],a[252:255],v[8:11]
TR a[168:169],v229 offset:24576
TR a[170:171],v229 offset:26624
MM v[8:11],a[184:187],a[236:239],v[8:11]
s_mov_b64 exec, s[8:9]
buffer_load_dwordx4 v[92:95], v248, s[20:23], s37 offen offset:1024 sc1
s_mov_b64 exec, -1
MM v[8:11],a[180:183],a[220:223],v[8:11]
)ASM"
R"ASM(
TR a[172:173],v229 offset:28672
TR a[174:175],v229 offset:30720
MM v[8:11],a[176:179],a[204:207],v[8:11]
s_waitcnt lgkmcnt(15)
EX v143,v143
MM v[12:15],a[128:131],v[172:175],v[12:15]
TR a[176:177],v229 offset:32768
TR a[178:179],v229 offset:34816
MM v[12:15],a[132:135],v[188:191],v[12:15]
s_cmp_le_i32 s12, 102
s_cselect_b32 s60, 1, 0
s_cselect_b64 s[8:9], -1, 0
MM v[12:15],a[136:139],v[204:207],v[12:15]
s_or_b32 s58, s58, s60
TR a[180:181],v229 offset:36864
TR a[182:183],v229 offset:38912
MM v[12:15],a[140:143],v[220:223],v[12:15]
s_mov_b64 exec, s[8:9]
buffer_load_dwordx4 v[120:123], v250, s[20:23], s39 offen offset:0 sc1
s_mov_b64 exec, -1
MM v[4:7],a[140:143],v[216:219],v[4:7]
TR a[184:185],v229 offset:40960
TR a[186:187],v229 offset:43008
MM v[4:7],a[136:139],v[200:203],v[4:7]
s_mov_b64 exec, s[8:9]
buffer_load_dwordx4 v[124:127], v250, s[20:23], s39 offen offset:256 sc1
s_mov_b64 exec, -1
MM v[4:7],a[132:135],v[184:187],v[4:7]
TR a[188:189],v229 offset:45056
TR a[190:191],v229 offset:47104
MM v[4:7],a[128:131],v[168:171],v[4:7]
s_waitcnt lgkmcnt(15)
v_add_f32 v136, 1.0, v136
v_add_f32 v137, 1.0, v137
MM v[4:7],a[144:147],a[16:19],v[4:7]
v_add_f32 v138, 1.0, v138
v_add_f32 v139, 1.0, v139
MM v[4:7],a[148:151],a[48:51],v[4:7]
v_add_f32 v140, 1.0, v140
v_add_f32 v141, 1.0, v141
MM v[4:7],a[152:155],a[80:83],v[4:7]
v_add_f32 v142, 1.0, v142
v_add_f32 v143, 1.0, v143
MM v[4:7],a[156:159],a[112:115],v[4:7]
v_rcp_f32 v136, v136
MM v[12:15],a[156:159],a[116:119],v[12:15]
v_rcp_f32 v137, v137
MM v[12:15],a[152:155],a[84:87],v[12:15]
v_rcp_f32 v138, v138
MM v[12:15],a[148:151],a[52:55],v[12:15]
v_rcp_f32 v139, v139
MM v[12:15],a[144:147],a[20:23],v[12:15]
s_waitcnt lgkmcnt(8)
v_rcp_f32 v140, v140
MM v[12:15],a[160:163],a[28:31],v[12:15]
s_cmp_le_i32 s13, 0
s_cselect_b32 s60, 2, 0
s_andn2_b32 s60, s60, s58
s_cselect_b64 s[8:9], -1, 0
MM v[12:15],a[164:167],a[60:63],v[12:15]
s_or_b32 s58, s58, s60
s_mov_b64 exec, s[8:9]
buffer_load_dwordx4 v[80:83], v248, s[20:23], s36 offen offset:0 sc1
s_mov_b64 exec, -1
MM v[12:15],a[168:171],a[92:95],v[12:15]
v_rcp_f32 v141, v141
MM v[12:15],a[172:175],a[124:127],v[12:15]
s_mov_b64 exec, s[8:9]
buffer_load_dwordx4 v[84:87], v248, s[20:23], s36 offen offset:1024 sc1
s_mov_b64 exec, -1
MM v[4:7],a[172:175],a[120:123],v[4:7]
v_rcp_f32 v142, v142
MM v[4:7],a[168:171],a[88:91],v[4:7]
v_rcp_f32 v143, v143
MM v[4:7],a[164:167],a[56:59],v[4:7]
v_mul_f32 v136, v160, v136
v_mul_f32 v137, v161, v137
MM v[4:7],a[160:163],a[24:27],v[4:7]
s_waitcnt lgkmcnt(0)
v_mul_f32 v138, v162, v138
v_mul_f32 v139, v163, v139
MM v[4:7],a[176:179],a[200:203],v[4:7]
v_mul_f32 v140, v164, v140
v_mul_f32 v141, v165, v141
MM v[4:7],a[180:183],a[216:219],v[4:7]
v_mul_f32 v142, v166, v142
v_mul_f32 v143, v167, v143
MM v[4:7],a[184:187],a[232:235],v[4:7]
CV v16,v136,v137
CV v17,v138,v139
MM v[4:7],a[188:191],a[248:251],v[4:7]
CV v18,v140,v141
CV v19,v142,v143
MM v[12:15],a[188:191],a[252:255],v[12:15]
v_lshlrev_b32 v160, 16, v16
v_and_b32 v161, 0xffff0000, v16
MM v[12:15],a[184:187],a[236:239],v[12:15]
v_lshlrev_b32 v162, 16, v17
v_and_b32 v163, 0xffff0000, v17
MM v[12:15],a[180:183],a[220:223],v[12:15]
v_lshlrev_b32 v164, 16, v18
v_and_b32 v165, 0xffff0000, v18
MM v[12:15],a[176:179],a[204:207],v[12:15]
v_lshlrev_b32 v166, 16, v19
v_and_b32 v167, 0xffff0000, v19
s_memrealtime s[54:55]
s_cmp_eq_u32 s58, 15
s_cbranch_scc1 LSK1_%=
s_memrealtime s[98:99]
s_andn2_b32 s91, s17, s58
s_mov_b32 s77, 0x7fffffff
s_waitcnt lgkmcnt(0)
s_sub_u32 s76, s98, s54
s_bitcmp0_b32 s91, 1
s_cbranch_scc1 LSM11_%=
s_sub_u32 s90, s13, s76
s_sub_u32 s90, s90, 12
s_cmp_gt_i32 s90, 0
s_cbranch_scc1 LSM11_%=
buffer_load_dwordx4 v[80:83], v248, s[20:23], s36 offen offset:0 sc1
buffer_load_dwordx4 v[84:87], v248, s[20:23], s36 offen offset:1024 sc1
s_bitset1_b32 s58, 1
LSM11_%=:
s_bitcmp0_b32 s91, 3
s_cbranch_scc1 LSM13_%=
s_sub_u32 s90, s15, s76
s_sub_u32 s90, s90, 63
s_cmp_gt_i32 s90, 0
s_cbranch_scc1 LSM13_%=
buffer_load_dwordx4 v[112:115], v248, s[20:23], s38 offen offset:0 sc1
buffer_load_dwordx4 v[116:119], v248, s[20:23], s38 offen offset:1024 sc1
s_bitset1_b32 s58, 3
LSM13_%=:
s_bitcmp0_b32 s91, 2
s_cbranch_scc1 LSM12_%=
s_sub_u32 s90, s14, s76
s_sub_u32 s90, s90, 97
s_cmp_gt_i32 s90, 0
s_cbranch_scc1 LSM12_%=
buffer_load_dwordx4 v[88:91], v248, s[20:23], s37 offen offset:0 sc1
buffer_load_dwordx4 v[92:95], v248, s[20:23], s37 offen offset:1024 sc1
s_bitset1_b32 s58, 2
LSM12_%=:
s_bitcmp0_b32 s91, 0
s_cbranch_scc1 LSM10_%=
s_sub_u32 s90, s12, s76
s_sub_u32 s90, s90, 131
s_cmp_gt_i32 s90, 0
s_cbranch_scc1 LSM10_%=
buffer_load_dwordx4 v[120:123], v250, s[20:23], s39 offen offset:0 sc1
buffer_load_dwordx4 v[124:127], v250, s[20:23], s39 offen offset:256 sc1
s_bitset1_b32 s58, 0
LSM10_%=:
LSK1_%=:
v_lshlrev_b32 v176, 16, v20
v_and_b32 v177, 0xffff0000, v20
v_lshlrev_b32 v178, 16, v21
v_and_b32 v179, 0xffff0000, v21
v_lshlrev_b32 v180, 16, v22
v_and_b32 v181, 0xffff0000, v22
v_lshlrev_b32 v182, 16, v23
v_and_b32 v183, 0xffff0000, v23
v_mul_f32 v136, 0xbfb8aa3b, v176
v_mul_f32 v137, 0xbfb8aa3b, v177
v_mul_f32 v138, 0xbfb8aa3b, v178
v_mul_f32 v139, 0xbfb8aa3b, v179
v_mul_f32 v140, 0xbfb8aa3b, v180
v_mul_f32 v141, 0xbfb8aa3b, v181
v_mul_f32 v142, 0xbfb8aa3b, v182
v_mul_f32 v143, 0xbfb8aa3b, v183
EX v136,v136
EX v137,v137
EX v138,v138
EX v139,v139
EX v140,v140
EX v141,v141
EX v142,v142
EX v143,v143
v_add_f32 v136, 1.0, v136
v_add_f32 v137, 1.0, v137
v_add_f32 v138, 1.0, v138
v_add_f32 v139, 1.0, v139
v_add_f32 v140, 1.0, v140
v_add_f32 v141, 1.0, v141
v_add_f32 v142, 1.0, v142
v_add_f32 v143, 1.0, v143
v_rcp_f32 v136, v136
v_rcp_f32 v137, v137
v_rcp_f32 v138, v138
v_rcp_f32 v139, v139
s_waitcnt vmcnt(12)
v_cmp_eq_u32 s[8:9], s69, v149
v_subrev_u32 v226, s56, v148
s_nop 0
v_cndmask_b32 v226, 4.0, v226, s[8:9]
s_nop 0
v_readlane_b32 s13, v226, 16
v_readlane_b32 s14, v226, 32
v_readlane_b32 s15, v226, 48
s_max_i32 s12, s13, s14
s_max_i32 s12, s12, s15
s_max_i32 s12, s12, 0
s_cmp_eq_u32 s58, 15
s_cbranch_scc1 LSK2_%=
s_memrealtime s[98:99]
s_andn2_b32 s91, s17, s58
s_mov_b32 s77, 0x7fffffff
s_waitcnt lgkmcnt(0)
s_sub_u32 s76, s98, s54
s_bitcmp0_b32 s91, 1
s_cbranch_scc1 LSM21_%=
s_sub_u32 s90, s13, s76
s_sub_u32 s90, s90, 12
s_cmp_gt_i32 s90, 0
s_cbranch_scc1 LSM21_%=
buffer_load_dwordx4 v[80:83], v248, s[20:23], s36 offen offset:0 sc1
buffer_load_dwordx4 v[84:87], v248, s[20:23], s36 offen offset:1024 sc1
s_bitset1_b32 s58, 1
LSM21_%=:
s_bitcmp0_b32 s91, 3
s_cbranch_scc1 LSM23_%=
s_sub_u32 s90, s15, s76
s_sub_u32 s90, s90, 63
s_cmp_gt_i32 s90, 0
s_cbranch_scc1 LSM23_%=
buffer_load_dwordx4 v[112:115], v248, s[20:23], s38 offen offset:0 sc1
buffer_load_dwordx4 v[116:119], v248, s[20:23], s38 offen offset:1024 sc1
s_bitset1_b32 s58, 3
LSM23_%=:
s_bitcmp0_b32 s91, 2
s_cbranch_scc1 LSM22_%=
s_sub_u32 s90, s14, s76
s_sub_u32 s90, s90, 97
s_cmp_gt_i32 s90, 0
s_cbranch_scc1 LSM22_%=
buffer_load_dwordx4 v[88:91], v248, s[20:23], s37 offen offset:0 sc1
buffer_load_dwordx4 v[92:95], v248, s[20:23], s37 offen offset:1024 sc1
s_bitset1_b32 s58, 2
LSM22_%=:
s_bitcmp0_b32 s91, 0
s_cbranch_scc1 LSM20_%=
s_sub_u32 s90, s12, s76
s_sub_u32 s90, s90, 131
s_cmp_gt_i32 s90, 0
s_cbranch_scc1 LSM20_%=
buffer_load_dwordx4 v[120:123], v250, s[20:23], s39 offen offset:0 sc1
buffer_load_dwordx4 v[124:127], v250, s[20:23], s39 offen offset:256 sc1
s_bitset1_b32 s58, 0
LSM20_%=:
LSK2_%=:
v_rcp_f32 v140, v140
v_rcp_f32 v141, v141
v_rcp_f32 v142, v142
v_rcp_f32 v143, v143
v_mul_f32 v136, v176, v136
v_mul_f32 v137, v177, v137
v_mul_f32 v138, v178, v138
v_mul_f32 v139, v179, v139
v_mul_f32 v140, v180, v140
v_mul_f32 v141, v181, v141
v_mul_f32 v142, v182, v142
v_mul_f32 v143, v183, v143
CV v20,v136,v137
CV v21,v138,v139
CV v22,v140,v141
CV v23,v142,v143
v_lshlrev_b32 v176, 16, v20
v_and_b32 v177, 0xffff0000, v20
v_lshlrev_b32 v178, 16, v21
v_and_b32 v179, 0xffff0000, v21
v_lshlrev_b32 v180, 16, v22
v_and_b32 v181, 0xffff0000, v22
v_lshlrev_b32 v182, 16, v23
v_and_b32 v183, 0xffff0000, v23
v_lshlrev_b32 v192, 16, v24
v_and_b32 v193, 0xffff0000, v24
v_lshlrev_b32 v194, 16, v25
v_and_b32 v195, 0xffff0000, v25
v_lshlrev_b32 v196, 16, v26
v_and_b32 v197, 0xffff0000, v26
v_lshlrev_b32 v198, 16, v27
v_and_b32 v199, 0xffff0000, v27
v_lshlrev_b32 v208, 16, v28
v_and_b32 v209, 0xffff0000, v28
v_lshlrev_b32 v210, 16, v29
v_and_b32 v211, 0xffff0000, v29
v_lshlrev_b32 v212, 16, v30
v_and_b32 v213, 0xffff0000, v30
v_lshlrev_b32 v214, 16, v31
v_and_b32 v215, 0xffff0000, v31
s_mov_b32 s73, 30000
s_cmp_lg_u32 s58, 15
s_cbranch_scc1 LSI_%=
s_waitcnt vmcnt(0)
v_max3_u32 v236, v123, v127, v83
v_max3_u32 v237, v87, v91, v95
v_max3_u32 v236, v236, v115, v119
v_max_u32 v236, v236, v237
v_cmp_eq_u32 vcc, -1, v236
s_cbranch_vccnz LSI_%=
LSF_%=:
s_waitcnt lgkmcnt(0)
MM v[152:155],v[48:51],v[120:123],0
MM v[152:155],v[52:55],v[124:127],v[152:155]
MM v[0:3],v[58:61],v[88:91],v[0:3]
v_lshlrev_b32 v128, 16, v150
v_and_b32 v129, 0xffff0000, v150
MM v[0:3],v[58:61],v[112:115],v[0:3]
v_lshlrev_b32 v130, 16, v151
v_and_b32 v131, 0xffff0000, v151
MM v[0:3],v[58:61],v[80:83],v[0:3]
v_lshlrev_b32 v132, 16, v62
v_and_b32 v133, 0xffff0000, v62
MM v[4:7],v[56:59],v[88:91],v[4:7]
v_lshlrev_b32 v134, 16, v63
v_and_b32 v135, 0xffff0000, v63
MM v[4:7],v[56:59],v[112:115],v[4:7]
CV v236,v152,v153
CV v237,v154,v155
MM v[4:7],v[56:59],v[80:83],v[4:7]
ds_bpermute_b32 v236, v232, v236
ds_bpermute_b32 v237, v232, v237
MM v[8:11],v[58:61],v[92:95],v[8:11]
MM v[8:11],v[58:61],v[116:119],v[8:11]
MM v[8:11],v[58:61],v[84:87],v[8:11]
s_add_u32 s80, s18, 20480
MM v[12:15],v[56:59],v[92:95],v[12:15]
MM v[12:15],v[56:59],v[116:119],v[12:15]
CV v32,v0,v1
CV v33,v2,v3
MM v[12:15],v[56:59],v[84:87],v[12:15]
CV v34,v4,v5
CV v35,v6,v7
ds_write2_b64 v224, v[32:33], v[34:35] offset0:0 offset1:4
RD v[40:43],v225 offset:0
CV v36,v8,v9
CV v37,v10,v11
s_nop 1
CV v38,v12,v13
CV v39,v14,v15
ds_write2_b64 v224, v[36:37], v[38:39] offset0:160 offset1:164
RD v[44:47],v225 offset:1280
s_waitcnt lgkmcnt(4)
v_lshlrev_b32 v152, 16, v236
v_and_b32 v153, 0xffff0000, v236
v_lshlrev_b32 v154, 16, v237
v_and_b32 v155, 0xffff0000, v237
v_pk_mul_f32 v[152:153], v[152:153], v[128:129]
v_pk_mul_f32 v[154:155], v[154:155], v[130:131]
CV v236,v152,v153
CV v237,v154,v155
v_lshlrev_b32 v152, 16, v236
v_and_b32 v153, 0xffff0000, v236
v_lshlrev_b32 v154, 16, v237
v_and_b32 v155, 0xffff0000, v237
v_pk_add_f32 v[152:153], v[152:153], v[132:133]
v_pk_add_f32 v[154:155], v[154:155], v[134:135]
CV v238,v152,v153
CV v239,v154,v155
buffer_store_dwordx2 v[238:239], v254, s[40:43], s63 offen offset:0 nt
s_nop 0
s_waitcnt lgkmcnt(2)
v_lshlrev_b32 v0, 16, v40
v_and_b32 v1, 0xffff0000, v40
v_lshlrev_b32 v2, 16, v41
v_and_b32 v3, 0xffff0000, v41
v_lshlrev_b32 v4, 16, v42
v_and_b32 v5, 0xffff0000, v42
v_lshlrev_b32 v6, 16, v43
v_and_b32 v7, 0xffff0000, v43
v_pk_mul_f32 v[0:1], v[0:1], v[160:161]
v_pk_mul_f32 v[2:3], v[2:3], v[162:163]
v_pk_mul_f32 v[4:5], v[4:5], v[164:165]
v_pk_mul_f32 v[6:7], v[6:7], v[166:167]
CV v40,v0,v1
CV v41,v2,v3
CV v42,v4,v5
CV v43,v6,v7
v_lshlrev_b32 v0, 16, v40
v_and_b32 v1, 0xffff0000, v40
v_lshlrev_b32 v2, 16, v41
v_and_b32 v3, 0xffff0000, v41
v_lshlrev_b32 v4, 16, v42
v_and_b32 v5, 0xffff0000, v42
v_lshlrev_b32 v6, 16, v43
v_and_b32 v7, 0xffff0000, v43
v_pk_add_f32 v[0:1], v[0:1], v[192:193]
v_pk_add_f32 v[2:3], v[2:3], v[194:195]
LPG30_%=:
s_cbranch_execz LTR1_%=
v_pk_add_f32 v[4:5], v[4:5], v[196:197]
v_pk_add_f32 v[6:7], v[6:7], v[198:199]
CV v40,v0,v1
CV v41,v2,v3
CV v42,v4,v5
CV v43,v6,v7
buffer_store_dwordx4 v[40:43], v251, s[40:43], s18 offen offset:0 nt
s_waitcnt lgkmcnt(0)
v_lshlrev_b32 v8, 16, v44
v_and_b32 v9, 0xffff0000, v44
v_lshlrev_b32 v10, 16, v45
v_and_b32 v11, 0xffff0000, v45
v_lshlrev_b32 v12, 16, v46
v_and_b32 v13, 0xffff0000, v46
v_lshlrev_b32 v14, 16, v47
v_and_b32 v15, 0xffff0000, v47
v_pk_mul_f32 v[8:9], v[8:9], v[176:177]
v_pk_mul_f32 v[10:11], v[10:11], v[178:179]
v_pk_mul_f32 v[12:13], v[12:13], v[180:181]
v_pk_mul_f32 v[14:15], v[14:15], v[182:183]
CV v44,v8,v9
CV v45,v10,v11
CV v46,v12,v13
)ASM"
R"ASM(
CV v47,v14,v15
v_lshlrev_b32 v8, 16, v44
v_and_b32 v9, 0xffff0000, v44
v_lshlrev_b32 v10, 16, v45
v_and_b32 v11, 0xffff0000, v45
v_lshlrev_b32 v12, 16, v46
v_and_b32 v13, 0xffff0000, v46
v_lshlrev_b32 v14, 16, v47
v_and_b32 v15, 0xffff0000, v47
v_pk_add_f32 v[8:9], v[8:9], v[208:209]
v_pk_add_f32 v[10:11], v[10:11], v[210:211]
v_pk_add_f32 v[12:13], v[12:13], v[212:213]
v_pk_add_f32 v[14:15], v[14:15], v[214:215]
CV v44,v8,v9
CV v45,v10,v11
CV v46,v12,v13
CV v47,v14,v15
buffer_store_dwordx4 v[44:47], v251, s[40:43], s80 offen offset:0 nt
s_waitcnt vmcnt(0)
s_endpgm
LSI_%=:
s_memrealtime s[98:99]
s_andn2_b32 s91, s17, s58
s_mov_b32 s77, 0x7fffffff
s_waitcnt lgkmcnt(0)
s_sub_u32 s76, s98, s54
s_bitcmp0_b32 s91, 1
s_cbranch_scc1 LSM1_%=
s_sub_u32 s90, s13, s76
s_sub_u32 s90, s90, 12
s_cmp_gt_i32 s90, 0
s_cbranch_scc1 LSN1_%=
buffer_load_dwordx4 v[80:83], v248, s[20:23], s36 offen offset:0 sc1
buffer_load_dwordx4 v[84:87], v248, s[20:23], s36 offen offset:1024 sc1
s_bitset1_b32 s58, 1
s_branch LSM1_%=
LSN1_%=:
s_min_i32 s77, s77, s90
LSM1_%=:
s_bitcmp0_b32 s91, 3
s_cbranch_scc1 LSM3_%=
s_sub_u32 s90, s15, s76
s_sub_u32 s90, s90, 63
s_cmp_gt_i32 s90, 0
s_cbranch_scc1 LSN3_%=
buffer_load_dwordx4 v[112:115], v248, s[20:23], s38 offen offset:0 sc1
buffer_load_dwordx4 v[116:119], v248, s[20:23], s38 offen offset:1024 sc1
s_bitset1_b32 s58, 3
s_branch LSM3_%=
LSN3_%=:
s_min_i32 s77, s77, s90
LSM3_%=:
s_bitcmp0_b32 s91, 2
s_cbranch_scc1 LSM2_%=
s_sub_u32 s90, s14, s76
s_sub_u32 s90, s90, 97
s_cmp_gt_i32 s90, 0
s_cbranch_scc1 LSN2_%=
buffer_load_dwordx4 v[88:91], v248, s[20:23], s37 offen offset:0 sc1
buffer_load_dwordx4 v[92:95], v248, s[20:23], s37 offen offset:1024 sc1
s_bitset1_b32 s58, 2
s_branch LSM2_%=
LSN2_%=:
s_min_i32 s77, s77, s90
LSM2_%=:
s_bitcmp0_b32 s91, 0
s_cbranch_scc1 LSM0_%=
s_sub_u32 s90, s12, s76
s_sub_u32 s90, s90, 131
s_cmp_gt_i32 s90, 0
s_cbranch_scc1 LSN0_%=
buffer_load_dwordx4 v[120:123], v250, s[20:23], s39 offen offset:0 sc1
buffer_load_dwordx4 v[124:127], v250, s[20:23], s39 offen offset:256 sc1
s_bitset1_b32 s58, 0
s_branch LSM0_%=
LSN0_%=:
s_min_i32 s77, s77, s90
LSM0_%=:
s_cmp_eq_u32 s12, 4.0
s_cselect_b32 s90, 30, 45
s_cmp_lt_i32 s77, s90
s_cbranch_scc1 LSI_%=
buffer_load_dwordx2 v[148:149], v253, s[20:23], s68 offen offset:8 sc1
s_waitcnt vmcnt(1)
s_bitcmp0_b32 s58, 1
s_cbranch_scc1 LSB1_%=
v_max_u32 v237, v83, v87
v_cmp_eq_u32 vcc, -1, v237
s_cbranch_vccnz LSB1_%=
s_bitset0_b32 s17, 1
LSB1_%=:
s_bitcmp0_b32 s58, 3
s_cbranch_scc1 LSB3_%=
v_max_u32 v239, v115, v119
v_cmp_eq_u32 vcc, -1, v239
s_cbranch_vccnz LSB3_%=
s_bitset0_b32 s17, 3
LSB3_%=:
s_bitcmp0_b32 s58, 2
s_cbranch_scc1 LSB2_%=
v_max_u32 v238, v91, v95
v_cmp_eq_u32 vcc, -1, v238
s_cbranch_vccnz LSB2_%=
s_bitset0_b32 s17, 2
LSB2_%=:
s_bitcmp0_b32 s58, 0
s_cbranch_scc1 LSB0_%=
v_max_u32 v236, v123, v127
v_cmp_eq_u32 vcc, -1, v236
s_cbranch_vccnz LSB0_%=
s_bitset0_b32 s17, 0
LSB0_%=:
s_cmp_eq_u32 s17, 0
s_cbranch_scc1 LSF_%=
s_mov_b32 s58, 0
s_waitcnt vmcnt(0)
v_cmp_eq_u32 s[8:9], s69, v149
v_subrev_u32 v226, s56, v148
s_nop 0
v_cndmask_b32 v226, 4.0, v226, s[8:9]
s_nop 0
v_readlane_b32 s13, v226, 16
v_readlane_b32 s14, v226, 32
v_readlane_b32 s15, v226, 48
s_max_i32 s12, s13, s14
s_max_i32 s12, s12, s15
s_max_i32 s12, s12, 0
s_sub_u32 s73, s73, 1
s_cbranch_scc0 LSI_%=
LSE_%=:
s_waitcnt vmcnt(0)
s_endpgm
LRG1o_%=:
s_mov_b32 s70, 30000
LRG1ol_%=:
s_sleep 2
buffer_load_dwordx4 v[200:203], v208, s[20:23], s62 offen offset:0 sc1
buffer_load_dwordx4 v[204:207], v208, s[20:23], s93 offen offset:0 sc1
s_waitcnt vmcnt(0)
v_xor_b32 v201, s69, v201
v_xor_b32 v203, s69, v203
v_xor_b32 v205, s69, v205
v_xor_b32 v207, s69, v207
v_or3_b32 v209, v201, v203, v205
v_or_b32 v209, v209, v207
v_cmp_ne_u32 vcc, 0, v209
s_cbranch_vccz LRG1ob_%=
s_sub_u32 s70, s70, 1
s_cbranch_scc0 LRG1ol_%=
s_branch LRG1ob_%=
LRFR_%=:
s_mov_b32 s91, 30000
LRFRl_%=:
s_sleep 2
buffer_load_dwordx4 v[48:51], v248, s[20:23], s90 offen offset:0 sc1
s_waitcnt vmcnt(0)
v_xor_b32 v48, s69, v48
v_xor_b32 v49, s69, v49
v_xor_b32 v50, s69, v50
v_xor_b32 v51, s69, v51
v_or3_b32 v252, v48, v49, v50
v_or_b32 v252, v252, v51
v_cmp_ne_u32 vcc, 0, v252
s_cbranch_vccz LRFRb_%=
s_sub_u32 s91, s91, 1
s_cbranch_scc0 LRFRl_%=
s_branch LRFRb_%=
LRRK_%=:
s_mov_b32 s91, 30000
LRRKl_%=:
s_sleep 2
buffer_load_dwordx2 v[32:33], v34, s[20:23], s90 offen offset:0 sc1
s_waitcnt vmcnt(0)
v_cmp_ne_u32 vcc, s69, v33
s_cbranch_vccz LRRKb_%=
s_sub_u32 s91, s91, 1
s_cbranch_scc0 LRRKl_%=
s_branch LRRKb_%=
LRK_%=:
s_cmp_ge_u32 s60, 8
s_cbranch_scc1 LRK0_%=
v_mov_b32 v253, 0
v_cmp_lt_u32 vcc, s52, v96
v_cmp_lt_u32 s[98:99], s52, v97
s_nop 0
v_addc_co_u32 v253, vcc, 0, v253, vcc
v_addc_co_u32 v253, s[98:99], 0, v253, s[98:99]
v_cmp_lt_u32 vcc, s52, v98
v_cmp_lt_u32 s[98:99], s52, v99
s_nop 0
v_addc_co_u32 v253, vcc, 0, v253, vcc
v_addc_co_u32 v253, s[98:99], 0, v253, s[98:99]
v_cmp_lt_u32 vcc, s52, v100
v_cmp_lt_u32 s[98:99], s52, v101
s_nop 0
v_addc_co_u32 v253, vcc, 0, v253, vcc
v_addc_co_u32 v253, s[98:99], 0, v253, s[98:99]
v_cmp_lt_u32 vcc, s52, v102
v_cmp_lt_u32 s[98:99], s52, v103
s_nop 0
v_addc_co_u32 v253, vcc, 0, v253, vcc
v_addc_co_u32 v253, s[98:99], 0, v253, s[98:99]
v_cmp_lt_u32 vcc, s52, v104
v_cmp_lt_u32 s[98:99], s52, v105
s_nop 0
v_addc_co_u32 v253, vcc, 0, v253, vcc
v_addc_co_u32 v253, s[98:99], 0, v253, s[98:99]
v_cmp_lt_u32 vcc, s52, v106
v_cmp_lt_u32 s[98:99], s52, v107
s_nop 0
v_addc_co_u32 v253, vcc, 0, v253, vcc
v_addc_co_u32 v253, s[98:99], 0, v253, s[98:99]
v_cmp_lt_u32 vcc, s52, v108
v_cmp_lt_u32 s[98:99], s52, v109
s_nop 0
v_addc_co_u32 v253, vcc, 0, v253, vcc
v_addc_co_u32 v253, s[98:99], 0, v253, s[98:99]
v_cmp_lt_u32 vcc, s52, v110
v_cmp_lt_u32 s[98:99], s52, v111
s_nop 0
v_addc_co_u32 v253, vcc, 0, v253, vcc
v_addc_co_u32 v253, s[98:99], 0, v253, s[98:99]
v_cmp_lt_u32 vcc, s52, v112
v_cmp_lt_u32 s[98:99], s52, v113
s_nop 0
v_addc_co_u32 v253, vcc, 0, v253, vcc
v_addc_co_u32 v253, s[98:99], 0, v253, s[98:99]
v_cmp_lt_u32 vcc, s52, v114
v_cmp_lt_u32 s[98:99], s52, v115
s_nop 0
v_addc_co_u32 v253, vcc, 0, v253, vcc
v_addc_co_u32 v253, s[98:99], 0, v253, s[98:99]
v_cmp_lt_u32 vcc, s52, v116
v_cmp_lt_u32 s[98:99], s52, v117
s_nop 0
v_addc_co_u32 v253, vcc, 0, v253, vcc
v_addc_co_u32 v253, s[98:99], 0, v253, s[98:99]
v_cmp_lt_u32 vcc, s52, v118
v_cmp_lt_u32 s[98:99], s52, v119
s_nop 0
v_addc_co_u32 v253, vcc, 0, v253, vcc
v_addc_co_u32 v253, s[98:99], 0, v253, s[98:99]
v_cmp_lt_u32 vcc, s52, v120
v_cmp_lt_u32 s[98:99], s52, v121
s_nop 0
v_addc_co_u32 v253, vcc, 0, v253, vcc
v_addc_co_u32 v253, s[98:99], 0, v253, s[98:99]
v_cmp_lt_u32 vcc, s52, v122
v_cmp_lt_u32 s[98:99], s52, v123
s_nop 0
v_addc_co_u32 v253, vcc, 0, v253, vcc
v_addc_co_u32 v253, s[98:99], 0, v253, s[98:99]
v_cmp_lt_u32 vcc, s52, v124
v_cmp_lt_u32 s[98:99], s52, v125
s_nop 0
v_addc_co_u32 v253, vcc, 0, v253, vcc
v_addc_co_u32 v253, s[98:99], 0, v253, s[98:99]
v_cmp_lt_u32 vcc, s52, v126
v_cmp_lt_u32 s[98:99], s52, v127
s_nop 0
v_addc_co_u32 v253, vcc, 0, v253, vcc
v_addc_co_u32 v253, s[98:99], 0, v253, s[98:99]
s_nop 1
v_add_u32_dpp v253, v253, v253 row_ror:8 row_mask:0xf bank_mask:0xf
s_nop 1
v_add_u32_dpp v253, v253, v253 row_ror:4 row_mask:0xf bank_mask:0xf
s_nop 1
v_add_u32_dpp v253, v253, v253 row_ror:2 row_mask:0xf bank_mask:0xf
s_nop 1
v_add_u32_dpp v253, v253, v253 row_ror:1 row_mask:0xf bank_mask:0xf
v_mov_b32 v250, v253
s_nop 1
v_permlane16_swap_b32 v253, v250
v_add_u32 v253, v253, v250
v_mov_b32 v250, v253
s_nop 1
v_permlane32_swap_b32 v253, v250
v_add_u32 v253, v253, v250
v_cmp_gt_u32 vcc, 8, v253
v_mov_b32 v250, s52
v_mov_b32 v251, s69
v_lshlrev_b32 v253, 3, v253
s_mov_b32 s76, 94208
s_cbranch_vccz LRK0_%=
s_mov_b64 exec, 1
buffer_store_dwordx2 v[250:251], v253, s[20:23], s76 offen offset:0 sc1
s_mov_b64 exec, -1
s_nop 1
LRK0_%=:
s_cmp_ge_u32 s62, 8
s_cbranch_scc1 LRK1_%=
v_mov_b32 v253, 0
v_cmp_lt_u32 vcc, s53, v96
v_cmp_lt_u32 s[98:99], s53, v97
s_nop 0
v_addc_co_u32 v253, vcc, 0, v253, vcc
v_addc_co_u32 v253, s[98:99], 0, v253, s[98:99]
v_cmp_lt_u32 vcc, s53, v98
v_cmp_lt_u32 s[98:99], s53, v99
s_nop 0
v_addc_co_u32 v253, vcc, 0, v253, vcc
v_addc_co_u32 v253, s[98:99], 0, v253, s[98:99]
v_cmp_lt_u32 vcc, s53, v100
v_cmp_lt_u32 s[98:99], s53, v101
s_nop 0
v_addc_co_u32 v253, vcc, 0, v253, vcc
v_addc_co_u32 v253, s[98:99], 0, v253, s[98:99]
v_cmp_lt_u32 vcc, s53, v102
v_cmp_lt_u32 s[98:99], s53, v103
s_nop 0
v_addc_co_u32 v253, vcc, 0, v253, vcc
v_addc_co_u32 v253, s[98:99], 0, v253, s[98:99]
v_cmp_lt_u32 vcc, s53, v104
v_cmp_lt_u32 s[98:99], s53, v105
s_nop 0
v_addc_co_u32 v253, vcc, 0, v253, vcc
v_addc_co_u32 v253, s[98:99], 0, v253, s[98:99]
v_cmp_lt_u32 vcc, s53, v106
v_cmp_lt_u32 s[98:99], s53, v107
s_nop 0
v_addc_co_u32 v253, vcc, 0, v253, vcc
v_addc_co_u32 v253, s[98:99], 0, v253, s[98:99]
v_cmp_lt_u32 vcc, s53, v108
v_cmp_lt_u32 s[98:99], s53, v109
s_nop 0
v_addc_co_u32 v253, vcc, 0, v253, vcc
v_addc_co_u32 v253, s[98:99], 0, v253, s[98:99]
v_cmp_lt_u32 vcc, s53, v110
v_cmp_lt_u32 s[98:99], s53, v111
s_nop 0
v_addc_co_u32 v253, vcc, 0, v253, vcc
v_addc_co_u32 v253, s[98:99], 0, v253, s[98:99]
v_cmp_lt_u32 vcc, s53, v112
v_cmp_lt_u32 s[98:99], s53, v113
s_nop 0
v_addc_co_u32 v253, vcc, 0, v253, vcc
v_addc_co_u32 v253, s[98:99], 0, v253, s[98:99]
v_cmp_lt_u32 vcc, s53, v114
v_cmp_lt_u32 s[98:99], s53, v115
s_nop 0
v_addc_co_u32 v253, vcc, 0, v253, vcc
v_addc_co_u32 v253, s[98:99], 0, v253, s[98:99]
v_cmp_lt_u32 vcc, s53, v116
v_cmp_lt_u32 s[98:99], s53, v117
s_nop 0
v_addc_co_u32 v253, vcc, 0, v253, vcc
v_addc_co_u32 v253, s[98:99], 0, v253, s[98:99]
v_cmp_lt_u32 vcc, s53, v118
v_cmp_lt_u32 s[98:99], s53, v119
s_nop 0
v_addc_co_u32 v253, vcc, 0, v253, vcc
v_addc_co_u32 v253, s[98:99], 0, v253, s[98:99]
v_cmp_lt_u32 vcc, s53, v120
v_cmp_lt_u32 s[98:99], s53, v121
s_nop 0
v_addc_co_u32 v253, vcc, 0, v253, vcc
v_addc_co_u32 v253, s[98:99], 0, v253, s[98:99]
v_cmp_lt_u32 vcc, s53, v122
v_cmp_lt_u32 s[98:99], s53, v123
s_nop 0
v_addc_co_u32 v253, vcc, 0, v253, vcc
v_addc_co_u32 v253, s[98:99], 0, v253, s[98:99]
v_cmp_lt_u32 vcc, s53, v124
v_cmp_lt_u32 s[98:99], s53, v125
s_nop 0
v_addc_co_u32 v253, vcc, 0, v253, vcc
v_addc_co_u32 v253, s[98:99], 0, v253, s[98:99]
v_cmp_lt_u32 vcc, s53, v126
v_cmp_lt_u32 s[98:99], s53, v127
s_nop 0
v_addc_co_u32 v253, vcc, 0, v253, vcc
v_addc_co_u32 v253, s[98:99], 0, v253, s[98:99]
s_nop 1
v_add_u32_dpp v253, v253, v253 row_ror:8 row_mask:0xf bank_mask:0xf
s_nop 1
v_add_u32_dpp v253, v253, v253 row_ror:4 row_mask:0xf bank_mask:0xf
s_nop 1
v_add_u32_dpp v253, v253, v253 row_ror:2 row_mask:0xf bank_mask:0xf
s_nop 1
v_add_u32_dpp v253, v253, v253 row_ror:1 row_mask:0xf bank_mask:0xf
v_mov_b32 v250, v253
s_nop 1
v_permlane16_swap_b32 v253, v250
v_add_u32 v253, v253, v250
v_mov_b32 v250, v253
s_nop 1
v_permlane32_swap_b32 v253, v250
v_add_u32 v253, v253, v250
v_cmp_gt_u32 vcc, 8, v253
v_mov_b32 v250, s53
v_mov_b32 v251, s69
v_lshlrev_b32 v253, 3, v253
s_mov_b32 s76, 94208
s_cbranch_vccz LRK1_%=
s_mov_b64 exec, 1
buffer_store_dwordx2 v[250:251], v253, s[20:23], s76 offen offset:0 sc1
s_mov_b64 exec, -1
s_nop 1
LRK1_%=:
s_cmp_ge_u32 s56, 8
s_cbranch_scc1 LRK2_%=
v_mov_b32 v253, 0
v_cmp_lt_u32 vcc, s54, v96
v_cmp_lt_u32 s[98:99], s54, v97
s_nop 0
v_addc_co_u32 v253, vcc, 0, v253, vcc
v_addc_co_u32 v253, s[98:99], 0, v253, s[98:99]
v_cmp_lt_u32 vcc, s54, v98
v_cmp_lt_u32 s[98:99], s54, v99
s_nop 0
v_addc_co_u32 v253, vcc, 0, v253, vcc
v_addc_co_u32 v253, s[98:99], 0, v253, s[98:99]
v_cmp_lt_u32 vcc, s54, v100
v_cmp_lt_u32 s[98:99], s54, v101
s_nop 0
v_addc_co_u32 v253, vcc, 0, v253, vcc
v_addc_co_u32 v253, s[98:99], 0, v253, s[98:99]
v_cmp_lt_u32 vcc, s54, v102
v_cmp_lt_u32 s[98:99], s54, v103
s_nop 0
v_addc_co_u32 v253, vcc, 0, v253, vcc
v_addc_co_u32 v253, s[98:99], 0, v253, s[98:99]
v_cmp_lt_u32 vcc, s54, v104
v_cmp_lt_u32 s[98:99], s54, v105
s_nop 0
v_addc_co_u32 v253, vcc, 0, v253, vcc
v_addc_co_u32 v253, s[98:99], 0, v253, s[98:99]
v_cmp_lt_u32 vcc, s54, v106
v_cmp_lt_u32 s[98:99], s54, v107
s_nop 0
v_addc_co_u32 v253, vcc, 0, v253, vcc
)ASM"
R"ASM(
v_addc_co_u32 v253, s[98:99], 0, v253, s[98:99]
v_cmp_lt_u32 vcc, s54, v108
v_cmp_lt_u32 s[98:99], s54, v109
s_nop 0
v_addc_co_u32 v253, vcc, 0, v253, vcc
v_addc_co_u32 v253, s[98:99], 0, v253, s[98:99]
v_cmp_lt_u32 vcc, s54, v110
v_cmp_lt_u32 s[98:99], s54, v111
s_nop 0
v_addc_co_u32 v253, vcc, 0, v253, vcc
v_addc_co_u32 v253, s[98:99], 0, v253, s[98:99]
v_cmp_lt_u32 vcc, s54, v112
v_cmp_lt_u32 s[98:99], s54, v113
s_nop 0
v_addc_co_u32 v253, vcc, 0, v253, vcc
v_addc_co_u32 v253, s[98:99], 0, v253, s[98:99]
v_cmp_lt_u32 vcc, s54, v114
v_cmp_lt_u32 s[98:99], s54, v115
s_nop 0
v_addc_co_u32 v253, vcc, 0, v253, vcc
v_addc_co_u32 v253, s[98:99], 0, v253, s[98:99]
v_cmp_lt_u32 vcc, s54, v116
v_cmp_lt_u32 s[98:99], s54, v117
s_nop 0
v_addc_co_u32 v253, vcc, 0, v253, vcc
v_addc_co_u32 v253, s[98:99], 0, v253, s[98:99]
v_cmp_lt_u32 vcc, s54, v118
v_cmp_lt_u32 s[98:99], s54, v119
s_nop 0
v_addc_co_u32 v253, vcc, 0, v253, vcc
v_addc_co_u32 v253, s[98:99], 0, v253, s[98:99]
v_cmp_lt_u32 vcc, s54, v120
v_cmp_lt_u32 s[98:99], s54, v121
s_nop 0
v_addc_co_u32 v253, vcc, 0, v253, vcc
v_addc_co_u32 v253, s[98:99], 0, v253, s[98:99]
v_cmp_lt_u32 vcc, s54, v122
v_cmp_lt_u32 s[98:99], s54, v123
s_nop 0
v_addc_co_u32 v253, vcc, 0, v253, vcc
v_addc_co_u32 v253, s[98:99], 0, v253, s[98:99]
v_cmp_lt_u32 vcc, s54, v124
v_cmp_lt_u32 s[98:99], s54, v125
s_nop 0
v_addc_co_u32 v253, vcc, 0, v253, vcc
v_addc_co_u32 v253, s[98:99], 0, v253, s[98:99]
v_cmp_lt_u32 vcc, s54, v126
v_cmp_lt_u32 s[98:99], s54, v127
s_nop 0
v_addc_co_u32 v253, vcc, 0, v253, vcc
v_addc_co_u32 v253, s[98:99], 0, v253, s[98:99]
s_nop 1
v_add_u32_dpp v253, v253, v253 row_ror:8 row_mask:0xf bank_mask:0xf
s_nop 1
v_add_u32_dpp v253, v253, v253 row_ror:4 row_mask:0xf bank_mask:0xf
s_nop 1
v_add_u32_dpp v253, v253, v253 row_ror:2 row_mask:0xf bank_mask:0xf
s_nop 1
v_add_u32_dpp v253, v253, v253 row_ror:1 row_mask:0xf bank_mask:0xf
v_mov_b32 v250, v253
s_nop 1
v_permlane16_swap_b32 v253, v250
v_add_u32 v253, v253, v250
v_mov_b32 v250, v253
s_nop 1
v_permlane32_swap_b32 v253, v250
v_add_u32 v253, v253, v250
v_cmp_gt_u32 vcc, 8, v253
v_mov_b32 v250, s54
v_mov_b32 v251, s69
v_lshlrev_b32 v253, 3, v253
s_mov_b32 s76, 94208
s_cbranch_vccz LRK2_%=
s_mov_b64 exec, 1
buffer_store_dwordx2 v[250:251], v253, s[20:23], s76 offen offset:0 sc1
s_mov_b64 exec, -1
s_nop 1
LRK2_%=:
s_cmp_ge_u32 s58, 8
s_cbranch_scc1 LRK3_%=
v_mov_b32 v253, 0
v_cmp_lt_u32 vcc, s55, v96
v_cmp_lt_u32 s[98:99], s55, v97
s_nop 0
v_addc_co_u32 v253, vcc, 0, v253, vcc
v_addc_co_u32 v253, s[98:99], 0, v253, s[98:99]
v_cmp_lt_u32 vcc, s55, v98
v_cmp_lt_u32 s[98:99], s55, v99
s_nop 0
v_addc_co_u32 v253, vcc, 0, v253, vcc
v_addc_co_u32 v253, s[98:99], 0, v253, s[98:99]
v_cmp_lt_u32 vcc, s55, v100
v_cmp_lt_u32 s[98:99], s55, v101
s_nop 0
v_addc_co_u32 v253, vcc, 0, v253, vcc
v_addc_co_u32 v253, s[98:99], 0, v253, s[98:99]
v_cmp_lt_u32 vcc, s55, v102
v_cmp_lt_u32 s[98:99], s55, v103
s_nop 0
v_addc_co_u32 v253, vcc, 0, v253, vcc
v_addc_co_u32 v253, s[98:99], 0, v253, s[98:99]
v_cmp_lt_u32 vcc, s55, v104
v_cmp_lt_u32 s[98:99], s55, v105
s_nop 0
v_addc_co_u32 v253, vcc, 0, v253, vcc
v_addc_co_u32 v253, s[98:99], 0, v253, s[98:99]
v_cmp_lt_u32 vcc, s55, v106
v_cmp_lt_u32 s[98:99], s55, v107
s_nop 0
v_addc_co_u32 v253, vcc, 0, v253, vcc
v_addc_co_u32 v253, s[98:99], 0, v253, s[98:99]
v_cmp_lt_u32 vcc, s55, v108
v_cmp_lt_u32 s[98:99], s55, v109
s_nop 0
v_addc_co_u32 v253, vcc, 0, v253, vcc
v_addc_co_u32 v253, s[98:99], 0, v253, s[98:99]
v_cmp_lt_u32 vcc, s55, v110
v_cmp_lt_u32 s[98:99], s55, v111
s_nop 0
v_addc_co_u32 v253, vcc, 0, v253, vcc
v_addc_co_u32 v253, s[98:99], 0, v253, s[98:99]
v_cmp_lt_u32 vcc, s55, v112
v_cmp_lt_u32 s[98:99], s55, v113
s_nop 0
v_addc_co_u32 v253, vcc, 0, v253, vcc
v_addc_co_u32 v253, s[98:99], 0, v253, s[98:99]
v_cmp_lt_u32 vcc, s55, v114
v_cmp_lt_u32 s[98:99], s55, v115
s_nop 0
v_addc_co_u32 v253, vcc, 0, v253, vcc
v_addc_co_u32 v253, s[98:99], 0, v253, s[98:99]
v_cmp_lt_u32 vcc, s55, v116
v_cmp_lt_u32 s[98:99], s55, v117
s_nop 0
v_addc_co_u32 v253, vcc, 0, v253, vcc
v_addc_co_u32 v253, s[98:99], 0, v253, s[98:99]
v_cmp_lt_u32 vcc, s55, v118
v_cmp_lt_u32 s[98:99], s55, v119
s_nop 0
v_addc_co_u32 v253, vcc, 0, v253, vcc
v_addc_co_u32 v253, s[98:99], 0, v253, s[98:99]
v_cmp_lt_u32 vcc, s55, v120
v_cmp_lt_u32 s[98:99], s55, v121
s_nop 0
v_addc_co_u32 v253, vcc, 0, v253, vcc
v_addc_co_u32 v253, s[98:99], 0, v253, s[98:99]
v_cmp_lt_u32 vcc, s55, v122
v_cmp_lt_u32 s[98:99], s55, v123
s_nop 0
v_addc_co_u32 v253, vcc, 0, v253, vcc
v_addc_co_u32 v253, s[98:99], 0, v253, s[98:99]
v_cmp_lt_u32 vcc, s55, v124
v_cmp_lt_u32 s[98:99], s55, v125
s_nop 0
v_addc_co_u32 v253, vcc, 0, v253, vcc
v_addc_co_u32 v253, s[98:99], 0, v253, s[98:99]
v_cmp_lt_u32 vcc, s55, v126
v_cmp_lt_u32 s[98:99], s55, v127
s_nop 0
v_addc_co_u32 v253, vcc, 0, v253, vcc
v_addc_co_u32 v253, s[98:99], 0, v253, s[98:99]
s_nop 1
v_add_u32_dpp v253, v253, v253 row_ror:8 row_mask:0xf bank_mask:0xf
s_nop 1
v_add_u32_dpp v253, v253, v253 row_ror:4 row_mask:0xf bank_mask:0xf
s_nop 1
v_add_u32_dpp v253, v253, v253 row_ror:2 row_mask:0xf bank_mask:0xf
s_nop 1
v_add_u32_dpp v253, v253, v253 row_ror:1 row_mask:0xf bank_mask:0xf
v_mov_b32 v250, v253
s_nop 1
v_permlane16_swap_b32 v253, v250
v_add_u32 v253, v253, v250
v_mov_b32 v250, v253
s_nop 1
v_permlane32_swap_b32 v253, v250
v_add_u32 v253, v253, v250
v_cmp_gt_u32 vcc, 8, v253
v_mov_b32 v250, s55
v_mov_b32 v251, s69
v_lshlrev_b32 v253, 3, v253
s_mov_b32 s76, 94208
s_cbranch_vccz LRK3_%=
s_mov_b64 exec, 1
buffer_store_dwordx2 v[250:251], v253, s[20:23], s76 offen offset:0 sc1
s_mov_b64 exec, -1
s_nop 1
LRK3_%=:
s_branch LRKb_%=
LRIX_%=:
s_mov_b32 s91, 30000
LRIXl_%=:
s_sleep 2
v_bfe_u32 v251, v248, 4, 3
v_lshlrev_b32 v251, 3, v251
buffer_load_dwordx2 v[252:253], v251, s[20:23], s86 offen offset:0 sc1
s_waitcnt vmcnt(0)
v_cmp_ne_u32 vcc, s69, v253
s_cbranch_vccz LRIXb_%=
s_sub_u32 s91, s91, 1
s_cbranch_scc0 LRIXl_%=
s_branch LRIXb_%=
LRFG_%=:
s_mov_b32 s91, 30000
LRFGl_%=:
s_sleep 2
buffer_load_dword v252, v253, s[20:23], s76 offen offset:0 sc1
s_waitcnt vmcnt(0)
v_cmp_ne_u32 vcc, s69, v252
s_cbranch_vccz LRFGb_%=
s_sub_u32 s91, s91, 1
s_cbranch_scc0 LRFGl_%=
s_branch LRFGb_%=
LRFH_%=:
s_mov_b32 s91, 30000
LRFHl_%=:
s_sleep 2
buffer_load_dword v252, v253, s[20:23], s76 offen offset:0 sc1
s_waitcnt vmcnt(0)
v_cmp_ne_u32 vcc, s69, v252
s_cbranch_vccz LRFHb_%=
s_sub_u32 s91, s91, 1
s_cbranch_scc0 LRFHl_%=
s_branch LRFHb_%=
LRFY_%=:
s_mov_b32 s91, 30000
LRFYl_%=:
s_sleep 2
buffer_load_dword v252, v253, s[20:23], s76 offen offset:0 sc1
s_waitcnt vmcnt(0)
v_cmp_ne_u32 vcc, s69, v252
s_cbranch_vccz LRFYb_%=
s_sub_u32 s91, s91, 1
s_cbranch_scc0 LRFYl_%=
s_branch LRFYb_%=
LRPO0_%=:
s_mov_b32 s97, 30000
LRPO0l_%=:
s_sleep 2
buffer_load_dword v242, v243, s[20:23], s44 offen offset:0 sc1
s_waitcnt vmcnt(0)
v_cmp_ne_u32 vcc, s69, v242
s_cbranch_vccz LRPO0b_%=
s_sub_u32 s97, s97, 1
s_cbranch_scc0 LRPO0l_%=
s_branch LRPO0b_%=
LRPW_%=:
s_mov_b32 s61, 30000
LRPWl_%=:
s_sleep 2
buffer_load_dwordx2 v[96:97], v98, s[20:23], s62 offen offset:0 sc1
s_waitcnt vmcnt(0)
v_xor_b32 v96, s69, v96
v_xor_b32 v97, s69, v97
v_or_b32 v99, v96, v97
v_cmp_ne_u32 vcc, 0, v99
s_cbranch_vccz LRPWb_%=
s_sub_u32 s61, s61, 1
s_cbranch_scc0 LRPWl_%=
s_branch LRPWb_%=
LRPO1_%=:
s_mov_b32 s97, 30000
LRPO1l_%=:
s_sleep 2
buffer_load_dword v242, v243, s[20:23], s96 offen offset:16 sc1
s_waitcnt vmcnt(0)
v_cmp_ne_u32 vcc, s69, v242
s_cbranch_vccz LRPO1b_%=
s_sub_u32 s97, s97, 1
s_cbranch_scc0 LRPO1l_%=
s_branch LRPO1b_%=
LKEND_ka:
LRET_%=:
s_cmp_eq_u32 s73, 0
s_cbranch_scc1 LBACK0_%=
s_cmp_eq_u32 s73, 2
s_cbranch_scc1 LBACK2_%=
s_cmp_eq_u32 s73, 1
s_cbranch_scc1 LBACK1_%=
s_cmp_eq_u32 s73, 3
s_cbranch_scc1 LBACK3_%=
s_cmp_eq_u32 s73, 5
s_cbranch_scc1 LBACK5_%=
s_cmp_eq_u32 s73, 7
s_cbranch_scc1 LBACK7_%=
s_cmp_eq_u32 s73, 9
s_cbranch_scc1 LBACK9_%=
s_cmp_eq_u32 s73, 11
s_cbranch_scc1 LBACK11_%=
s_cmp_eq_u32 s73, 13
s_cbranch_scc1 LBACK13_%=
s_cmp_eq_u32 s73, 15
s_cbranch_scc1 LBACK15_%=
s_cmp_eq_u32 s73, 17
s_cbranch_scc1 LBACK17_%=
s_cmp_eq_u32 s73, 19
s_cbranch_scc1 LBACK19_%=
s_cmp_eq_u32 s73, 21
s_cbranch_scc1 LBACK21_%=
s_cmp_eq_u32 s73, 23
s_cbranch_scc1 LBACK23_%=
s_cmp_eq_u32 s73, 25
s_cbranch_scc1 LBACK25_%=
s_cmp_eq_u32 s73, 27
s_cbranch_scc1 LBACK27_%=
s_cmp_eq_u32 s73, 29
s_cbranch_scc1 LBACK29_%=
s_cmp_eq_u32 s73, 31
s_cbranch_scc1 LBACK31_%=
s_cmp_eq_u32 s73, 33
s_cbranch_scc1 LBACK33_%=
s_cmp_eq_u32 s73, 35
s_cbranch_scc1 LBACK35_%=
s_cmp_eq_u32 s73, 4
s_cbranch_scc1 LBACK4_%=
s_cmp_eq_u32 s73, 6
s_cbranch_scc1 LBACK6_%=
s_cmp_eq_u32 s73, 8
s_cbranch_scc1 LBACK8_%=
s_cmp_eq_u32 s73, 10
s_cbranch_scc1 LBACK10_%=
s_cmp_eq_u32 s73, 12
s_cbranch_scc1 LBACK12_%=
s_cmp_eq_u32 s73, 14
s_cbranch_scc1 LBACK14_%=
s_cmp_eq_u32 s73, 16
s_cbranch_scc1 LBACK16_%=
s_cmp_eq_u32 s73, 18
s_cbranch_scc1 LBACK18_%=
s_cmp_eq_u32 s73, 20
s_cbranch_scc1 LBACK20_%=
s_cmp_eq_u32 s73, 22
s_cbranch_scc1 LBACK22_%=
s_cmp_eq_u32 s73, 24
s_cbranch_scc1 LBACK24_%=
s_cmp_eq_u32 s73, 26
s_cbranch_scc1 LBACK26_%=
s_cmp_eq_u32 s73, 28
s_cbranch_scc1 LBACK28_%=
s_cmp_eq_u32 s73, 30
s_cbranch_scc1 LBACK30_%=
s_cmp_eq_u32 s73, 32
s_cbranch_scc1 LBACK32_%=
s_cmp_eq_u32 s73, 34
s_cbranch_scc1 LBACK34_%=
s_cmp_eq_u32 s73, 36
s_cbranch_scc1 LBACK36_%=
s_cmp_eq_u32 s73, 38
s_cbranch_scc1 LBACK38_%=
s_cmp_eq_u32 s73, 37
s_cbranch_scc1 LBACK37_%=
s_endpgm
LSITE0_%=:
s_mov_b32 s73, 0
s_mov_b32 s74, 0
s_mov_b32 s75, 16384
s_branch LRARE002_%=
LSITE2_%=:
s_mov_b32 s73, 2
s_mov_b32 s74, 32768
s_mov_b32 s75, 49152
s_branch LRARE002_%=
LSITE1_%=:
s_mov_b32 s73, 1
s_mov_b32 s74, 16384
s_mov_b32 s75, 32768
s_branch LRARE102_%=
LSITE3_%=:
s_mov_b32 s73, 3
s_mov_b32 s74, 49152
s_mov_b32 s75, 0
s_branch LRARE104_%=
LSITE5_%=:
s_mov_b32 s73, 5
s_mov_b32 s74, 16384
s_mov_b32 s75, 32768
s_branch LRARE104_%=
LSITE7_%=:
s_mov_b32 s73, 7
s_mov_b32 s74, 49152
s_mov_b32 s75, 0
s_branch LRARE104_%=
LSITE9_%=:
s_mov_b32 s73, 9
s_mov_b32 s74, 16384
s_mov_b32 s75, 32768
s_branch LRARE104_%=
LSITE11_%=:
s_mov_b32 s73, 11
s_mov_b32 s74, 49152
s_mov_b32 s75, 0
s_branch LRARE104_%=
LSITE13_%=:
s_mov_b32 s73, 13
s_mov_b32 s74, 16384
s_mov_b32 s75, 32768
s_branch LRARE104_%=
LSITE15_%=:
s_mov_b32 s73, 15
s_mov_b32 s74, 49152
s_mov_b32 s75, 0
s_branch LRARE104_%=
LSITE17_%=:
s_mov_b32 s73, 17
s_mov_b32 s74, 16384
s_mov_b32 s75, 32768
s_branch LRARE104_%=
LSITE19_%=:
s_mov_b32 s73, 19
s_mov_b32 s74, 49152
s_mov_b32 s75, 0
s_branch LRARE104_%=
LSITE21_%=:
s_mov_b32 s73, 21
s_mov_b32 s74, 16384
s_mov_b32 s75, 32768
s_branch LRARE104_%=
LSITE23_%=:
s_mov_b32 s73, 23
s_mov_b32 s74, 49152
s_mov_b32 s75, 0
s_branch LRARE104_%=
LSITE25_%=:
s_mov_b32 s73, 25
s_mov_b32 s74, 16384
s_mov_b32 s75, 32768
s_branch LRARE104_%=
LSITE27_%=:
s_mov_b32 s73, 27
s_mov_b32 s74, 49152
s_mov_b32 s75, 0
s_branch LRARE104_%=
LSITE29_%=:
s_mov_b32 s73, 29
s_mov_b32 s74, 16384
s_mov_b32 s75, 32768
s_branch LRARE104_%=
LSITE31_%=:
s_mov_b32 s73, 31
s_mov_b32 s74, 49152
s_mov_b32 s75, 0
s_branch LRARE104_%=
LSITE33_%=:
s_mov_b32 s73, 33
s_mov_b32 s74, 16384
s_mov_b32 s75, 32768
s_branch LRARE104_%=
LSITE35_%=:
s_mov_b32 s73, 35
s_mov_b32 s74, 49152
s_mov_b32 s75, 0
s_branch LRARE104_%=
LSITE4_%=:
s_mov_b32 s73, 4
s_mov_b32 s74, 0
s_mov_b32 s75, 16384
s_branch LRARE004_%=
LSITE6_%=:
s_mov_b32 s73, 6
s_mov_b32 s74, 32768
s_mov_b32 s75, 49152
s_branch LRARE004_%=
LSITE8_%=:
s_mov_b32 s73, 8
s_mov_b32 s74, 0
s_mov_b32 s75, 16384
s_branch LRARE004_%=
LSITE10_%=:
s_mov_b32 s73, 10
s_mov_b32 s74, 32768
s_mov_b32 s75, 49152
s_branch LRARE004_%=
LSITE12_%=:
s_mov_b32 s73, 12
s_mov_b32 s74, 0
s_mov_b32 s75, 16384
s_branch LRARE004_%=
LSITE14_%=:
s_mov_b32 s73, 14
s_mov_b32 s74, 32768
s_mov_b32 s75, 49152
s_branch LRARE004_%=
LSITE16_%=:
s_mov_b32 s73, 16
s_mov_b32 s74, 0
s_mov_b32 s75, 16384
s_branch LRARE004_%=
LSITE18_%=:
s_mov_b32 s73, 18
s_mov_b32 s74, 32768
s_mov_b32 s75, 49152
s_branch LRARE004_%=
LSITE20_%=:
s_mov_b32 s73, 20
s_mov_b32 s74, 0
s_mov_b32 s75, 16384
s_branch LRARE004_%=
LSITE22_%=:
s_mov_b32 s73, 22
)ASM"
R"ASM(
s_mov_b32 s74, 32768
s_mov_b32 s75, 49152
s_branch LRARE004_%=
LSITE24_%=:
s_mov_b32 s73, 24
s_mov_b32 s74, 0
s_mov_b32 s75, 16384
s_branch LRARE004_%=
LSITE26_%=:
s_mov_b32 s73, 26
s_mov_b32 s74, 32768
s_mov_b32 s75, 49152
s_branch LRARE004_%=
LSITE28_%=:
s_mov_b32 s73, 28
s_mov_b32 s74, 0
s_mov_b32 s75, 16384
s_branch LRARE004_%=
LSITE30_%=:
s_mov_b32 s73, 30
s_mov_b32 s74, 32768
s_mov_b32 s75, 49152
s_branch LRARE004_%=
LSITE32_%=:
s_mov_b32 s73, 32
s_mov_b32 s74, 0
s_mov_b32 s75, 16384
s_branch LRARE004_%=
LSITE34_%=:
s_mov_b32 s73, 34
s_mov_b32 s74, 32768
s_mov_b32 s75, 49152
s_branch LRARE004_%=
LSITE36_%=:
s_mov_b32 s73, 36
s_mov_b32 s74, 0
s_mov_b32 s75, 16384
s_branch LRARE022_%=
LSITE38_%=:
s_mov_b32 s73, 38
s_mov_b32 s74, 32768
s_mov_b32 s75, 49152
s_branch LRARE022_%=
LSITE37_%=:
s_mov_b32 s73, 37
s_mov_b32 s74, 16384
s_mov_b32 s75, 32768
s_branch LRARE122_%=
LRARE002_%=:
s_nop 15
s_waitcnt vmcnt(0) lgkmcnt(0)
v_add_u32 v249, s74, v224
RD a[192:195],v249 offset:0
RD a[208:211],v249 offset:4096
RD a[224:227],v249 offset:8192
RD a[240:243],v249 offset:12288
v_add_u32 v250, s74, v225
RD a[196:199],v250 offset:0
RD a[212:215],v250 offset:4096
RD a[228:231],v250 offset:8192
RD a[244:247],v250 offset:12288
v_add_u32 v251, s74, v226
RD a[200:203],v251 offset:0
RD a[216:219],v251 offset:4096
RD a[232:235],v251 offset:8192
RD a[248:251],v251 offset:12288
v_add_u32 v254, s74, v227
RD a[204:207],v254 offset:0
RD a[220:223],v254 offset:4096
RD a[236:239],v254 offset:8192
RD a[252:255],v254 offset:12288
s_waitcnt lgkmcnt(0)
MM v[0:3],a[192:195],v[160:163],v[128:131]
MM v[0:3],a[196:199],v[176:179],v[0:3]
MM v[0:3],a[200:203],v[192:195],v[0:3]
MM v[0:3],a[204:207],v[208:211],v[0:3]
MM v[4:7],a[220:223],v[208:211],v[128:131]
MM v[4:7],a[216:219],v[192:195],v[4:7]
MM v[4:7],a[212:215],v[176:179],v[4:7]
MM v[4:7],a[208:211],v[160:163],v[4:7]
MM v[12:15],a[208:211],v[164:167],v[132:135]
MM v[12:15],a[212:215],v[180:183],v[12:15]
MM v[12:15],a[216:219],v[196:199],v[12:15]
MM v[12:15],a[220:223],v[212:215],v[12:15]
MM v[8:11],a[204:207],v[212:215],v[132:135]
MM v[8:11],a[200:203],v[196:199],v[8:11]
MM v[8:11],a[196:199],v[180:183],v[8:11]
MM v[8:11],a[192:195],v[164:167],v[8:11]
MM v[32:35],a[224:227],v[160:163],v[128:131]
MM v[32:35],a[228:231],v[176:179],v[32:35]
MM v[32:35],a[232:235],v[192:195],v[32:35]
MM v[32:35],a[236:239],v[208:211],v[32:35]
MM v[36:39],a[252:255],v[208:211],v[128:131]
MM v[36:39],a[248:251],v[192:195],v[36:39]
MM v[36:39],a[244:247],v[176:179],v[36:39]
MM v[36:39],a[240:243],v[160:163],v[36:39]
MM v[44:47],a[240:243],v[164:167],v[132:135]
MM v[44:47],a[244:247],v[180:183],v[44:47]
MM v[44:47],a[248:251],v[196:199],v[44:47]
MM v[44:47],a[252:255],v[212:215],v[44:47]
MM v[40:43],a[236:239],v[212:215],v[132:135]
MM v[40:43],a[232:235],v[196:199],v[40:43]
MM v[40:43],a[228:231],v[180:183],v[40:43]
MM v[40:43],a[224:227],v[164:167],v[40:43]
s_nop 15
v_add_u32 v249, s75, v224
RD a[192:195],v249 offset:0
RD a[208:211],v249 offset:4096
RD a[224:227],v249 offset:8192
RD a[240:243],v249 offset:12288
v_add_u32 v250, s75, v225
RD a[196:199],v250 offset:0
RD a[212:215],v250 offset:4096
RD a[228:231],v250 offset:8192
RD a[244:247],v250 offset:12288
v_add_u32 v251, s75, v226
RD a[200:203],v251 offset:0
RD a[216:219],v251 offset:4096
RD a[232:235],v251 offset:8192
RD a[248:251],v251 offset:12288
v_add_u32 v254, s75, v227
RD a[204:207],v254 offset:0
RD a[220:223],v254 offset:4096
RD a[236:239],v254 offset:8192
RD a[252:255],v254 offset:12288
s_waitcnt lgkmcnt(0)
v_max3_f32 v249, v0, v1, v2
v_max3_f32 v251, v32, v33, v34
v_max3_f32 v249, v249, v3, v4
v_max3_f32 v251, v251, v35, v36
v_max3_f32 v249, v249, v5, v6
v_max3_f32 v251, v251, v37, v38
v_max3_f32 v249, v249, v7, v39
v_max_f32 v249, v249, v251
v_mov_b32 v251, v249
s_nop 1
v_permlane16_swap_b32 v249, v251
v_max_f32 v249, v249, v251
v_mov_b32 v251, v249
s_nop 1
v_permlane32_swap_b32 v249, v251
v_max_f32 v249, v249, v251
v_log_f32 v250, v244
s_nop 0
v_max3_f32 v249, 0, v249, v250
v_sub_f32 v250, 0, v249
EX v250,v250
v_sub_f32 v128, v128, v249
v_sub_f32 v129, v129, v249
v_sub_f32 v130, v130, v249
v_sub_f32 v131, v131, v249
v_sub_f32 v64, v64, v249
v_sub_f32 v0, v0, v249
v_sub_f32 v65, v65, v249
v_sub_f32 v1, v1, v249
v_sub_f32 v66, v66, v249
v_sub_f32 v2, v2, v249
v_sub_f32 v67, v67, v249
v_sub_f32 v3, v3, v249
v_sub_f32 v68, v68, v249
v_sub_f32 v4, v4, v249
v_sub_f32 v69, v69, v249
v_sub_f32 v5, v5, v249
v_sub_f32 v70, v70, v249
v_sub_f32 v6, v6, v249
v_sub_f32 v71, v71, v249
v_sub_f32 v7, v7, v249
v_sub_f32 v96, v96, v249
v_sub_f32 v32, v32, v249
v_sub_f32 v97, v97, v249
v_sub_f32 v33, v33, v249
v_sub_f32 v98, v98, v249
v_sub_f32 v34, v34, v249
v_sub_f32 v99, v99, v249
v_sub_f32 v35, v35, v249
v_sub_f32 v100, v100, v249
v_sub_f32 v36, v36, v249
v_sub_f32 v101, v101, v249
v_sub_f32 v37, v37, v249
v_sub_f32 v102, v102, v249
v_sub_f32 v38, v38, v249
v_sub_f32 v103, v103, v249
v_sub_f32 v39, v39, v249
v_mul_f32 v144, v244, v250
v_mov_b32 v145, 0
v_bfe_u32 v249, v241, 0, 8
v_lshl_or_b32 v249, v249, 1, 1
v_lshlrev_b32 v249, 22, v249
v_and_b32 v241, 0xffffff00, v241
AR v254,a0
v_sub_f32 v254, v254, v249
v_mul_f32 v254, v254, v250
AW a0,v254
AR v251,a1
v_sub_f32 v251, v251, v249
v_mul_f32 v251, v251, v250
AW a1,v251
AR v254,a2
v_sub_f32 v254, v254, v249
v_mul_f32 v254, v254, v250
AW a2,v254
AR v251,a3
v_sub_f32 v251, v251, v249
v_mul_f32 v251, v251, v250
AW a3,v251
AR v254,a4
v_sub_f32 v254, v254, v249
v_mul_f32 v254, v254, v250
AW a4,v254
AR v251,a5
v_sub_f32 v251, v251, v249
v_mul_f32 v251, v251, v250
AW a5,v251
AR v254,a6
v_sub_f32 v254, v254, v249
v_mul_f32 v254, v254, v250
AW a6,v254
AR v251,a7
v_sub_f32 v251, v251, v249
v_mul_f32 v251, v251, v250
AW a7,v251
AR v254,a32
v_sub_f32 v254, v254, v249
v_mul_f32 v254, v254, v250
AW a32,v254
AR v251,a33
v_sub_f32 v251, v251, v249
v_mul_f32 v251, v251, v250
AW a33,v251
AR v254,a34
v_sub_f32 v254, v254, v249
v_mul_f32 v254, v254, v250
AW a34,v254
AR v251,a35
v_sub_f32 v251, v251, v249
v_mul_f32 v251, v251, v250
AW a35,v251
AR v254,a36
v_sub_f32 v254, v254, v249
v_mul_f32 v254, v254, v250
AW a36,v254
AR v251,a37
v_sub_f32 v251, v251, v249
v_mul_f32 v251, v251, v250
AW a37,v251
AR v254,a38
v_sub_f32 v254, v254, v249
v_mul_f32 v254, v254, v250
AW a38,v254
AR v251,a39
v_sub_f32 v251, v251, v249
v_mul_f32 v251, v251, v250
AW a39,v251
AR v254,a64
v_sub_f32 v254, v254, v249
v_mul_f32 v254, v254, v250
AW a64,v254
AR v251,a65
v_sub_f32 v251, v251, v249
v_mul_f32 v251, v251, v250
AW a65,v251
AR v254,a66
v_sub_f32 v254, v254, v249
v_mul_f32 v254, v254, v250
AW a66,v254
AR v251,a67
v_sub_f32 v251, v251, v249
v_mul_f32 v251, v251, v250
AW a67,v251
AR v254,a68
v_sub_f32 v254, v254, v249
v_mul_f32 v254, v254, v250
AW a68,v254
AR v251,a69
v_sub_f32 v251, v251, v249
v_mul_f32 v251, v251, v250
AW a69,v251
AR v254,a70
v_sub_f32 v254, v254, v249
v_mul_f32 v254, v254, v250
AW a70,v254
AR v251,a71
v_sub_f32 v251, v251, v249
v_mul_f32 v251, v251, v250
AW a71,v251
AR v254,a96
v_sub_f32 v254, v254, v249
v_mul_f32 v254, v254, v250
AW a96,v254
AR v251,a97
v_sub_f32 v251, v251, v249
v_mul_f32 v251, v251, v250
AW a97,v251
AR v254,a98
v_sub_f32 v254, v254, v249
v_mul_f32 v254, v254, v250
AW a98,v254
AR v251,a99
v_sub_f32 v251, v251, v249
v_mul_f32 v251, v251, v250
AW a99,v251
AR v254,a100
v_sub_f32 v254, v254, v249
v_mul_f32 v254, v254, v250
AW a100,v254
AR v251,a101
v_sub_f32 v251, v251, v249
v_mul_f32 v251, v251, v250
AW a101,v251
AR v254,a102
v_sub_f32 v254, v254, v249
v_mul_f32 v254, v254, v250
AW a102,v254
AR v251,a103
v_sub_f32 v251, v251, v249
v_mul_f32 v251, v251, v250
AW a103,v251
v_max3_f32 v249, v8, v9, v10
v_max3_f32 v251, v40, v41, v42
v_max3_f32 v249, v249, v11, v12
v_max3_f32 v251, v251, v43, v44
v_max3_f32 v249, v249, v13, v14
v_max3_f32 v251, v251, v45, v46
v_max3_f32 v249, v249, v15, v47
v_max_f32 v249, v249, v251
v_mov_b32 v251, v249
s_nop 1
v_permlane16_swap_b32 v249, v251
v_max_f32 v249, v249, v251
v_mov_b32 v251, v249
s_nop 1
v_permlane32_swap_b32 v249, v251
v_max_f32 v249, v249, v251
v_log_f32 v250, v245
s_nop 0
v_max3_f32 v249, 0, v249, v250
v_sub_f32 v250, 0, v249
EX v250,v250
v_sub_f32 v132, v132, v249
v_sub_f32 v133, v133, v249
v_sub_f32 v134, v134, v249
v_sub_f32 v135, v135, v249
v_sub_f32 v72, v72, v249
v_sub_f32 v8, v8, v249
v_sub_f32 v73, v73, v249
v_sub_f32 v9, v9, v249
v_sub_f32 v74, v74, v249
v_sub_f32 v10, v10, v249
v_sub_f32 v75, v75, v249
v_sub_f32 v11, v11, v249
v_sub_f32 v76, v76, v249
v_sub_f32 v12, v12, v249
v_sub_f32 v77, v77, v249
v_sub_f32 v13, v13, v249
v_sub_f32 v78, v78, v249
v_sub_f32 v14, v14, v249
v_sub_f32 v79, v79, v249
v_sub_f32 v15, v15, v249
v_sub_f32 v104, v104, v249
v_sub_f32 v40, v40, v249
v_sub_f32 v105, v105, v249
v_sub_f32 v41, v41, v249
v_sub_f32 v106, v106, v249
v_sub_f32 v42, v42, v249
v_sub_f32 v107, v107, v249
v_sub_f32 v43, v43, v249
v_sub_f32 v108, v108, v249
v_sub_f32 v44, v44, v249
v_sub_f32 v109, v109, v249
v_sub_f32 v45, v45, v249
v_sub_f32 v110, v110, v249
v_sub_f32 v46, v46, v249
v_sub_f32 v111, v111, v249
v_sub_f32 v47, v47, v249
v_mul_f32 v148, v245, v250
v_mov_b32 v149, 0
v_bfe_u32 v249, v241, 8, 8
v_lshl_or_b32 v249, v249, 1, 1
v_lshlrev_b32 v249, 22, v249
v_and_b32 v241, 0xffff00ff, v241
AR v254,a8
v_sub_f32 v254, v254, v249
v_mul_f32 v254, v254, v250
AW a8,v254
AR v251,a9
v_sub_f32 v251, v251, v249
v_mul_f32 v251, v251, v250
AW a9,v251
AR v254,a10
v_sub_f32 v254, v254, v249
v_mul_f32 v254, v254, v250
AW a10,v254
AR v251,a11
v_sub_f32 v251, v251, v249
v_mul_f32 v251, v251, v250
AW a11,v251
AR v254,a12
v_sub_f32 v254, v254, v249
v_mul_f32 v254, v254, v250
AW a12,v254
AR v251,a13
v_sub_f32 v251, v251, v249
v_mul_f32 v251, v251, v250
AW a13,v251
AR v254,a14
v_sub_f32 v254, v254, v249
v_mul_f32 v254, v254, v250
AW a14,v254
AR v251,a15
v_sub_f32 v251, v251, v249
v_mul_f32 v251, v251, v250
AW a15,v251
AR v254,a40
v_sub_f32 v254, v254, v249
v_mul_f32 v254, v254, v250
AW a40,v254
AR v251,a41
v_sub_f32 v251, v251, v249
v_mul_f32 v251, v251, v250
AW a41,v251
AR v254,a42
v_sub_f32 v254, v254, v249
v_mul_f32 v254, v254, v250
AW a42,v254
AR v251,a43
v_sub_f32 v251, v251, v249
v_mul_f32 v251, v251, v250
AW a43,v251
AR v254,a44
v_sub_f32 v254, v254, v249
v_mul_f32 v254, v254, v250
AW a44,v254
AR v251,a45
v_sub_f32 v251, v251, v249
v_mul_f32 v251, v251, v250
AW a45,v251
AR v254,a46
v_sub_f32 v254, v254, v249
v_mul_f32 v254, v254, v250
AW a46,v254
AR v251,a47
v_sub_f32 v251, v251, v249
v_mul_f32 v251, v251, v250
AW a47,v251
AR v254,a72
v_sub_f32 v254, v254, v249
v_mul_f32 v254, v254, v250
AW a72,v254
AR v251,a73
v_sub_f32 v251, v251, v249
v_mul_f32 v251, v251, v250
AW a73,v251
AR v254,a74
v_sub_f32 v254, v254, v249
v_mul_f32 v254, v254, v250
AW a74,v254
AR v251,a75
v_sub_f32 v251, v251, v249
v_mul_f32 v251, v251, v250
AW a75,v251
AR v254,a76
v_sub_f32 v254, v254, v249
v_mul_f32 v254, v254, v250
AW a76,v254
AR v251,a77
v_sub_f32 v251, v251, v249
v_mul_f32 v251, v251, v250
AW a77,v251
AR v254,a78
v_sub_f32 v254, v254, v249
v_mul_f32 v254, v254, v250
AW a78,v254
AR v251,a79
v_sub_f32 v251, v251, v249
v_mul_f32 v251, v251, v250
AW a79,v251
AR v254,a104
v_sub_f32 v254, v254, v249
v_mul_f32 v254, v254, v250
AW a104,v254
AR v251,a105
v_sub_f32 v251, v251, v249
v_mul_f32 v251, v251, v250
AW a105,v251
AR v254,a106
v_sub_f32 v254, v254, v249
v_mul_f32 v254, v254, v250
AW a106,v254
AR v251,a107
v_sub_f32 v251, v251, v249
v_mul_f32 v251, v251, v250
AW a107,v251
AR v254,a108
v_sub_f32 v254, v254, v249
v_mul_f32 v254, v254, v250
AW a108,v254
AR v251,a109
v_sub_f32 v251, v251, v249
v_mul_f32 v251, v251, v250
AW a109,v251
AR v254,a110
v_sub_f32 v254, v254, v249
v_mul_f32 v254, v254, v250
AW a110,v254
AR v251,a111
v_sub_f32 v251, v251, v249
v_mul_f32 v251, v251, v250
AW a111,v251
EX v0,v0
EX v1,v1
EX v2,v2
EX v3,v3
EX v4,v4
EX v5,v5
EX v6,v6
EX v7,v7
EX v8,v8
EX v9,v9
EX v10,v10
EX v11,v11
EX v12,v12
EX v13,v13
EX v14,v14
EX v15,v15
EX v32,v32
EX v33,v33
EX v34,v34
EX v35,v35
EX v36,v36
EX v37,v37
EX v38,v38
EX v39,v39
EX v40,v40
EX v41,v41
EX v42,v42
EX v43,v43
EX v44,v44
)ASM"
R"ASM(
EX v45,v45
EX v46,v46
EX v47,v47
CV v0,v0,v1
CV v1,v2,v3
CV v2,v4,v5
CV v3,v6,v7
CV v8,v8,v9
CV v9,v10,v11
CV v10,v12,v13
CV v11,v14,v15
CV v4,v32,v33
CV v5,v34,v35
CV v6,v36,v37
CV v7,v38,v39
CV v12,v40,v41
CV v13,v42,v43
CV v14,v44,v45
CV v15,v46,v47
v_smfmac_f32_16x16x64_bf16 v[144:147], v[236:239], v[0:7], v234
s_nop 0
v_smfmac_f32_16x16x64_bf16 v[148:151], v[236:239], v[8:15], v234
s_nop 9
s_branch LRET_%=
LRARE102_%=:
s_nop 15
s_waitcnt vmcnt(0) lgkmcnt(0)
v_add_u32 v249, s74, v224
RD a[192:195],v249 offset:0
RD a[208:211],v249 offset:4096
RD a[224:227],v249 offset:8192
RD a[240:243],v249 offset:12288
v_add_u32 v250, s74, v225
RD a[196:199],v250 offset:0
RD a[212:215],v250 offset:4096
RD a[228:231],v250 offset:8192
RD a[244:247],v250 offset:12288
v_add_u32 v251, s74, v226
RD a[200:203],v251 offset:0
RD a[216:219],v251 offset:4096
RD a[232:235],v251 offset:8192
RD a[248:251],v251 offset:12288
v_add_u32 v254, s74, v227
RD a[204:207],v254 offset:0
RD a[220:223],v254 offset:4096
RD a[236:239],v254 offset:8192
RD a[252:255],v254 offset:12288
s_waitcnt lgkmcnt(0)
MM v[64:67],a[192:195],v[160:163],v[128:131]
MM v[64:67],a[196:199],v[176:179],v[64:67]
MM v[64:67],a[200:203],v[192:195],v[64:67]
MM v[64:67],a[204:207],v[208:211],v[64:67]
MM v[68:71],a[220:223],v[208:211],v[128:131]
MM v[68:71],a[216:219],v[192:195],v[68:71]
MM v[68:71],a[212:215],v[176:179],v[68:71]
MM v[68:71],a[208:211],v[160:163],v[68:71]
MM v[76:79],a[208:211],v[164:167],v[132:135]
MM v[76:79],a[212:215],v[180:183],v[76:79]
MM v[76:79],a[216:219],v[196:199],v[76:79]
MM v[76:79],a[220:223],v[212:215],v[76:79]
MM v[72:75],a[204:207],v[212:215],v[132:135]
MM v[72:75],a[200:203],v[196:199],v[72:75]
MM v[72:75],a[196:199],v[180:183],v[72:75]
MM v[72:75],a[192:195],v[164:167],v[72:75]
MM v[96:99],a[224:227],v[160:163],v[128:131]
MM v[96:99],a[228:231],v[176:179],v[96:99]
MM v[96:99],a[232:235],v[192:195],v[96:99]
MM v[96:99],a[236:239],v[208:211],v[96:99]
MM v[100:103],a[252:255],v[208:211],v[128:131]
MM v[100:103],a[248:251],v[192:195],v[100:103]
MM v[100:103],a[244:247],v[176:179],v[100:103]
MM v[100:103],a[240:243],v[160:163],v[100:103]
MM v[108:111],a[240:243],v[164:167],v[132:135]
MM v[108:111],a[244:247],v[180:183],v[108:111]
MM v[108:111],a[248:251],v[196:199],v[108:111]
MM v[108:111],a[252:255],v[212:215],v[108:111]
MM v[104:107],a[236:239],v[212:215],v[132:135]
MM v[104:107],a[232:235],v[196:199],v[104:107]
MM v[104:107],a[228:231],v[180:183],v[104:107]
MM v[104:107],a[224:227],v[164:167],v[104:107]
s_nop 15
v_add_u32 v249, s75, v224
RD a[192:195],v249 offset:0
RD a[208:211],v249 offset:4096
RD a[224:227],v249 offset:8192
RD a[240:243],v249 offset:12288
v_add_u32 v250, s75, v225
RD a[196:199],v250 offset:0
RD a[212:215],v250 offset:4096
RD a[228:231],v250 offset:8192
RD a[244:247],v250 offset:12288
v_add_u32 v251, s75, v226
RD a[200:203],v251 offset:0
RD a[216:219],v251 offset:4096
RD a[232:235],v251 offset:8192
RD a[248:251],v251 offset:12288
v_add_u32 v254, s75, v227
RD a[204:207],v254 offset:0
RD a[220:223],v254 offset:4096
RD a[236:239],v254 offset:8192
RD a[252:255],v254 offset:12288
s_waitcnt lgkmcnt(0)
v_max3_f32 v249, v64, v65, v66
v_max3_f32 v251, v96, v97, v98
v_max3_f32 v249, v249, v67, v68
v_max3_f32 v251, v251, v99, v100
v_max3_f32 v249, v249, v69, v70
v_max3_f32 v251, v251, v101, v102
v_max3_f32 v249, v249, v71, v103
v_max_f32 v249, v249, v251
v_mov_b32 v251, v249
s_nop 1
v_permlane16_swap_b32 v249, v251
v_max_f32 v249, v249, v251
v_mov_b32 v251, v249
s_nop 1
v_permlane32_swap_b32 v249, v251
v_max_f32 v249, v249, v251
v_log_f32 v250, v244
s_nop 0
v_max3_f32 v249, 0, v249, v250
v_sub_f32 v250, 0, v249
EX v250,v250
v_sub_f32 v128, v128, v249
v_sub_f32 v129, v129, v249
v_sub_f32 v130, v130, v249
v_sub_f32 v131, v131, v249
v_sub_f32 v0, v0, v249
v_sub_f32 v64, v64, v249
v_sub_f32 v1, v1, v249
v_sub_f32 v65, v65, v249
v_sub_f32 v2, v2, v249
v_sub_f32 v66, v66, v249
v_sub_f32 v3, v3, v249
v_sub_f32 v67, v67, v249
v_sub_f32 v4, v4, v249
v_sub_f32 v68, v68, v249
v_sub_f32 v5, v5, v249
v_sub_f32 v69, v69, v249
v_sub_f32 v6, v6, v249
v_sub_f32 v70, v70, v249
v_sub_f32 v7, v7, v249
v_sub_f32 v71, v71, v249
v_sub_f32 v32, v32, v249
v_sub_f32 v96, v96, v249
v_sub_f32 v33, v33, v249
v_sub_f32 v97, v97, v249
v_sub_f32 v34, v34, v249
v_sub_f32 v98, v98, v249
v_sub_f32 v35, v35, v249
v_sub_f32 v99, v99, v249
v_sub_f32 v36, v36, v249
v_sub_f32 v100, v100, v249
v_sub_f32 v37, v37, v249
v_sub_f32 v101, v101, v249
v_sub_f32 v38, v38, v249
v_sub_f32 v102, v102, v249
v_sub_f32 v39, v39, v249
v_sub_f32 v103, v103, v249
v_mul_f32 v144, v244, v250
v_mov_b32 v145, 0
v_bfe_u32 v249, v241, 0, 8
v_lshl_or_b32 v249, v249, 1, 1
v_lshlrev_b32 v249, 22, v249
v_and_b32 v241, 0xffffff00, v241
AR v254,a0
v_sub_f32 v254, v254, v249
v_mul_f32 v254, v254, v250
AW a0,v254
AR v251,a1
v_sub_f32 v251, v251, v249
v_mul_f32 v251, v251, v250
AW a1,v251
AR v254,a2
v_sub_f32 v254, v254, v249
v_mul_f32 v254, v254, v250
AW a2,v254
AR v251,a3
v_sub_f32 v251, v251, v249
v_mul_f32 v251, v251, v250
AW a3,v251
AR v254,a4
v_sub_f32 v254, v254, v249
v_mul_f32 v254, v254, v250
AW a4,v254
AR v251,a5
v_sub_f32 v251, v251, v249
v_mul_f32 v251, v251, v250
AW a5,v251
AR v254,a6
v_sub_f32 v254, v254, v249
v_mul_f32 v254, v254, v250
AW a6,v254
AR v251,a7
v_sub_f32 v251, v251, v249
v_mul_f32 v251, v251, v250
AW a7,v251
AR v254,a32
v_sub_f32 v254, v254, v249
v_mul_f32 v254, v254, v250
AW a32,v254
AR v251,a33
v_sub_f32 v251, v251, v249
v_mul_f32 v251, v251, v250
AW a33,v251
AR v254,a34
v_sub_f32 v254, v254, v249
v_mul_f32 v254, v254, v250
AW a34,v254
AR v251,a35
v_sub_f32 v251, v251, v249
v_mul_f32 v251, v251, v250
AW a35,v251
AR v254,a36
v_sub_f32 v254, v254, v249
v_mul_f32 v254, v254, v250
AW a36,v254
AR v251,a37
v_sub_f32 v251, v251, v249
v_mul_f32 v251, v251, v250
AW a37,v251
AR v254,a38
v_sub_f32 v254, v254, v249
v_mul_f32 v254, v254, v250
AW a38,v254
AR v251,a39
v_sub_f32 v251, v251, v249
v_mul_f32 v251, v251, v250
AW a39,v251
AR v254,a64
v_sub_f32 v254, v254, v249
v_mul_f32 v254, v254, v250
AW a64,v254
AR v251,a65
v_sub_f32 v251, v251, v249
v_mul_f32 v251, v251, v250
AW a65,v251
AR v254,a66
v_sub_f32 v254, v254, v249
v_mul_f32 v254, v254, v250
AW a66,v254
AR v251,a67
v_sub_f32 v251, v251, v249
v_mul_f32 v251, v251, v250
AW a67,v251
AR v254,a68
v_sub_f32 v254, v254, v249
v_mul_f32 v254, v254, v250
AW a68,v254
AR v251,a69
v_sub_f32 v251, v251, v249
v_mul_f32 v251, v251, v250
AW a69,v251
AR v254,a70
v_sub_f32 v254, v254, v249
v_mul_f32 v254, v254, v250
AW a70,v254
AR v251,a71
v_sub_f32 v251, v251, v249
v_mul_f32 v251, v251, v250
AW a71,v251
AR v254,a96
v_sub_f32 v254, v254, v249
v_mul_f32 v254, v254, v250
AW a96,v254
AR v251,a97
v_sub_f32 v251, v251, v249
v_mul_f32 v251, v251, v250
AW a97,v251
AR v254,a98
v_sub_f32 v254, v254, v249
v_mul_f32 v254, v254, v250
AW a98,v254
AR v251,a99
v_sub_f32 v251, v251, v249
v_mul_f32 v251, v251, v250
AW a99,v251
AR v254,a100
v_sub_f32 v254, v254, v249
v_mul_f32 v254, v254, v250
AW a100,v254
AR v251,a101
v_sub_f32 v251, v251, v249
v_mul_f32 v251, v251, v250
AW a101,v251
AR v254,a102
v_sub_f32 v254, v254, v249
v_mul_f32 v254, v254, v250
AW a102,v254
AR v251,a103
v_sub_f32 v251, v251, v249
v_mul_f32 v251, v251, v250
AW a103,v251
v_max3_f32 v249, v72, v73, v74
v_max3_f32 v251, v104, v105, v106
v_max3_f32 v249, v249, v75, v76
v_max3_f32 v251, v251, v107, v108
v_max3_f32 v249, v249, v77, v78
v_max3_f32 v251, v251, v109, v110
v_max3_f32 v249, v249, v79, v111
v_max_f32 v249, v249, v251
v_mov_b32 v251, v249
s_nop 1
v_permlane16_swap_b32 v249, v251
v_max_f32 v249, v249, v251
v_mov_b32 v251, v249
s_nop 1
v_permlane32_swap_b32 v249, v251
v_max_f32 v249, v249, v251
v_log_f32 v250, v245
s_nop 0
v_max3_f32 v249, 0, v249, v250
v_sub_f32 v250, 0, v249
EX v250,v250
v_sub_f32 v132, v132, v249
v_sub_f32 v133, v133, v249
v_sub_f32 v134, v134, v249
v_sub_f32 v135, v135, v249
v_sub_f32 v8, v8, v249
v_sub_f32 v72, v72, v249
v_sub_f32 v9, v9, v249
v_sub_f32 v73, v73, v249
v_sub_f32 v10, v10, v249
v_sub_f32 v74, v74, v249
v_sub_f32 v11, v11, v249
v_sub_f32 v75, v75, v249
v_sub_f32 v12, v12, v249
v_sub_f32 v76, v76, v249
v_sub_f32 v13, v13, v249
v_sub_f32 v77, v77, v249
v_sub_f32 v14, v14, v249
v_sub_f32 v78, v78, v249
v_sub_f32 v15, v15, v249
v_sub_f32 v79, v79, v249
v_sub_f32 v40, v40, v249
v_sub_f32 v104, v104, v249
v_sub_f32 v41, v41, v249
v_sub_f32 v105, v105, v249
v_sub_f32 v42, v42, v249
v_sub_f32 v106, v106, v249
v_sub_f32 v43, v43, v249
v_sub_f32 v107, v107, v249
v_sub_f32 v44, v44, v249
v_sub_f32 v108, v108, v249
v_sub_f32 v45, v45, v249
v_sub_f32 v109, v109, v249
v_sub_f32 v46, v46, v249
v_sub_f32 v110, v110, v249
v_sub_f32 v47, v47, v249
v_sub_f32 v111, v111, v249
v_mul_f32 v148, v245, v250
v_mov_b32 v149, 0
v_bfe_u32 v249, v241, 8, 8
v_lshl_or_b32 v249, v249, 1, 1
v_lshlrev_b32 v249, 22, v249
v_and_b32 v241, 0xffff00ff, v241
AR v254,a8
v_sub_f32 v254, v254, v249
v_mul_f32 v254, v254, v250
AW a8,v254
AR v251,a9
v_sub_f32 v251, v251, v249
v_mul_f32 v251, v251, v250
AW a9,v251
AR v254,a10
v_sub_f32 v254, v254, v249
v_mul_f32 v254, v254, v250
AW a10,v254
AR v251,a11
v_sub_f32 v251, v251, v249
v_mul_f32 v251, v251, v250
AW a11,v251
AR v254,a12
v_sub_f32 v254, v254, v249
v_mul_f32 v254, v254, v250
AW a12,v254
AR v251,a13
v_sub_f32 v251, v251, v249
v_mul_f32 v251, v251, v250
AW a13,v251
AR v254,a14
v_sub_f32 v254, v254, v249
v_mul_f32 v254, v254, v250
AW a14,v254
AR v251,a15
v_sub_f32 v251, v251, v249
v_mul_f32 v251, v251, v250
AW a15,v251
AR v254,a40
v_sub_f32 v254, v254, v249
v_mul_f32 v254, v254, v250
AW a40,v254
AR v251,a41
v_sub_f32 v251, v251, v249
v_mul_f32 v251, v251, v250
AW a41,v251
AR v254,a42
v_sub_f32 v254, v254, v249
v_mul_f32 v254, v254, v250
AW a42,v254
AR v251,a43
v_sub_f32 v251, v251, v249
v_mul_f32 v251, v251, v250
AW a43,v251
AR v254,a44
v_sub_f32 v254, v254, v249
v_mul_f32 v254, v254, v250
AW a44,v254
AR v251,a45
v_sub_f32 v251, v251, v249
v_mul_f32 v251, v251, v250
AW a45,v251
AR v254,a46
v_sub_f32 v254, v254, v249
v_mul_f32 v254, v254, v250
AW a46,v254
AR v251,a47
v_sub_f32 v251, v251, v249
v_mul_f32 v251, v251, v250
AW a47,v251
AR v254,a72
v_sub_f32 v254, v254, v249
v_mul_f32 v254, v254, v250
AW a72,v254
AR v251,a73
v_sub_f32 v251, v251, v249
v_mul_f32 v251, v251, v250
AW a73,v251
AR v254,a74
v_sub_f32 v254, v254, v249
v_mul_f32 v254, v254, v250
AW a74,v254
AR v251,a75
v_sub_f32 v251, v251, v249
v_mul_f32 v251, v251, v250
AW a75,v251
AR v254,a76
v_sub_f32 v254, v254, v249
v_mul_f32 v254, v254, v250
AW a76,v254
AR v251,a77
v_sub_f32 v251, v251, v249
v_mul_f32 v251, v251, v250
AW a77,v251
AR v254,a78
v_sub_f32 v254, v254, v249
v_mul_f32 v254, v254, v250
AW a78,v254
AR v251,a79
v_sub_f32 v251, v251, v249
v_mul_f32 v251, v251, v250
AW a79,v251
AR v254,a104
v_sub_f32 v254, v254, v249
v_mul_f32 v254, v254, v250
AW a104,v254
AR v251,a105
v_sub_f32 v251, v251, v249
v_mul_f32 v251, v251, v250
AW a105,v251
AR v254,a106
v_sub_f32 v254, v254, v249
v_mul_f32 v254, v254, v250
AW a106,v254
AR v251,a107
v_sub_f32 v251, v251, v249
v_mul_f32 v251, v251, v250
AW a107,v251
AR v254,a108
v_sub_f32 v254, v254, v249
v_mul_f32 v254, v254, v250
AW a108,v254
AR v251,a109
v_sub_f32 v251, v251, v249
v_mul_f32 v251, v251, v250
AW a109,v251
AR v254,a110
v_sub_f32 v254, v254, v249
v_mul_f32 v254, v254, v250
AW a110,v254
AR v251,a111
v_sub_f32 v251, v251, v249
v_mul_f32 v251, v251, v250
AW a111,v251
EX v64,v64
EX v65,v65
EX v66,v66
EX v67,v67
EX v68,v68
EX v69,v69
EX v70,v70
EX v71,v71
EX v72,v72
EX v73,v73
EX v74,v74
EX v75,v75
EX v76,v76
EX v77,v77
EX v78,v78
EX v79,v79
EX v96,v96
EX v97,v97
EX v98,v98
EX v99,v99
EX v100,v100
EX v101,v101
EX v102,v102
EX v103,v103
EX v104,v104
EX v105,v105
EX v106,v106
EX v107,v107
EX v108,v108
EX v109,v109
EX v110,v110
EX v111,v111
CV v64,v64,v65
CV v65,v66,v67
CV v66,v68,v69
CV v67,v70,v71
CV v72,v72,v73
CV v73,v74,v75
CV v74,v76,v77
CV v75,v78,v79
CV v68,v96,v97
CV v69,v98,v99
CV v70,v100,v101
CV v71,v102,v103
CV v76,v104,v105
CV v77,v106,v107
CV v78,v108,v109
CV v79,v110,v111
v_smfmac_f32_16x16x64_bf16 v[144:147], v[236:239], v[64:71], v234
)ASM"
R"ASM(
s_nop 0
v_smfmac_f32_16x16x64_bf16 v[148:151], v[236:239], v[72:79], v234
s_nop 9
s_branch LRET_%=
LRARE104_%=:
s_nop 15
s_waitcnt vmcnt(0) lgkmcnt(0)
v_add_u32 v249, s74, v224
RD a[192:195],v249 offset:0
RD a[208:211],v249 offset:4096
RD a[224:227],v249 offset:8192
RD a[240:243],v249 offset:12288
v_add_u32 v250, s74, v225
RD a[196:199],v250 offset:0
RD a[212:215],v250 offset:4096
RD a[228:231],v250 offset:8192
RD a[244:247],v250 offset:12288
v_add_u32 v251, s74, v226
RD a[200:203],v251 offset:0
RD a[216:219],v251 offset:4096
RD a[232:235],v251 offset:8192
RD a[248:251],v251 offset:12288
v_add_u32 v254, s74, v227
RD a[204:207],v254 offset:0
RD a[220:223],v254 offset:4096
RD a[236:239],v254 offset:8192
RD a[252:255],v254 offset:12288
s_waitcnt lgkmcnt(0)
MM v[64:67],a[192:195],v[160:163],v[128:131]
MM v[64:67],a[196:199],v[176:179],v[64:67]
MM v[64:67],a[200:203],v[192:195],v[64:67]
MM v[64:67],a[204:207],v[208:211],v[64:67]
MM v[68:71],a[220:223],v[208:211],v[128:131]
MM v[68:71],a[216:219],v[192:195],v[68:71]
MM v[68:71],a[212:215],v[176:179],v[68:71]
MM v[68:71],a[208:211],v[160:163],v[68:71]
MM v[76:79],a[208:211],v[164:167],v[132:135]
MM v[76:79],a[212:215],v[180:183],v[76:79]
MM v[76:79],a[216:219],v[196:199],v[76:79]
MM v[76:79],a[220:223],v[212:215],v[76:79]
MM v[72:75],a[204:207],v[212:215],v[132:135]
MM v[72:75],a[200:203],v[196:199],v[72:75]
MM v[72:75],a[196:199],v[180:183],v[72:75]
MM v[72:75],a[192:195],v[164:167],v[72:75]
MM v[80:83],a[192:195],v[168:171],v[136:139]
MM v[80:83],a[196:199],v[184:187],v[80:83]
MM v[80:83],a[200:203],v[200:203],v[80:83]
MM v[80:83],a[204:207],v[216:219],v[80:83]
MM v[84:87],a[220:223],v[216:219],v[136:139]
MM v[84:87],a[216:219],v[200:203],v[84:87]
MM v[84:87],a[212:215],v[184:187],v[84:87]
MM v[84:87],a[208:211],v[168:171],v[84:87]
MM v[92:95],a[208:211],v[172:175],v[140:143]
MM v[92:95],a[212:215],v[188:191],v[92:95]
MM v[92:95],a[216:219],v[204:207],v[92:95]
MM v[92:95],a[220:223],v[220:223],v[92:95]
MM v[88:91],a[204:207],v[220:223],v[140:143]
MM v[88:91],a[200:203],v[204:207],v[88:91]
MM v[88:91],a[196:199],v[188:191],v[88:91]
MM v[88:91],a[192:195],v[172:175],v[88:91]
MM v[96:99],a[224:227],v[160:163],v[128:131]
MM v[96:99],a[228:231],v[176:179],v[96:99]
MM v[96:99],a[232:235],v[192:195],v[96:99]
MM v[96:99],a[236:239],v[208:211],v[96:99]
MM v[100:103],a[252:255],v[208:211],v[128:131]
MM v[100:103],a[248:251],v[192:195],v[100:103]
MM v[100:103],a[244:247],v[176:179],v[100:103]
MM v[100:103],a[240:243],v[160:163],v[100:103]
MM v[108:111],a[240:243],v[164:167],v[132:135]
MM v[108:111],a[244:247],v[180:183],v[108:111]
MM v[108:111],a[248:251],v[196:199],v[108:111]
MM v[108:111],a[252:255],v[212:215],v[108:111]
MM v[104:107],a[236:239],v[212:215],v[132:135]
MM v[104:107],a[232:235],v[196:199],v[104:107]
MM v[104:107],a[228:231],v[180:183],v[104:107]
MM v[104:107],a[224:227],v[164:167],v[104:107]
MM v[112:115],a[224:227],v[168:171],v[136:139]
MM v[112:115],a[228:231],v[184:187],v[112:115]
MM v[112:115],a[232:235],v[200:203],v[112:115]
MM v[112:115],a[236:239],v[216:219],v[112:115]
MM v[116:119],a[252:255],v[216:219],v[136:139]
MM v[116:119],a[248:251],v[200:203],v[116:119]
MM v[116:119],a[244:247],v[184:187],v[116:119]
MM v[116:119],a[240:243],v[168:171],v[116:119]
MM v[124:127],a[240:243],v[172:175],v[140:143]
MM v[124:127],a[244:247],v[188:191],v[124:127]
MM v[124:127],a[248:251],v[204:207],v[124:127]
MM v[124:127],a[252:255],v[220:223],v[124:127]
MM v[120:123],a[236:239],v[220:223],v[140:143]
MM v[120:123],a[232:235],v[204:207],v[120:123]
MM v[120:123],a[228:231],v[188:191],v[120:123]
MM v[120:123],a[224:227],v[172:175],v[120:123]
s_nop 15
v_add_u32 v249, s75, v224
RD a[192:195],v249 offset:0
RD a[208:211],v249 offset:4096
RD a[224:227],v249 offset:8192
RD a[240:243],v249 offset:12288
v_add_u32 v250, s75, v225
RD a[196:199],v250 offset:0
RD a[212:215],v250 offset:4096
RD a[228:231],v250 offset:8192
RD a[244:247],v250 offset:12288
v_add_u32 v251, s75, v226
RD a[200:203],v251 offset:0
RD a[216:219],v251 offset:4096
RD a[232:235],v251 offset:8192
RD a[248:251],v251 offset:12288
v_add_u32 v254, s75, v227
RD a[204:207],v254 offset:0
RD a[220:223],v254 offset:4096
RD a[236:239],v254 offset:8192
RD a[252:255],v254 offset:12288
s_waitcnt lgkmcnt(0)
v_max3_f32 v249, v64, v65, v66
v_max3_f32 v251, v96, v97, v98
v_max3_f32 v249, v249, v67, v68
v_max3_f32 v251, v251, v99, v100
v_max3_f32 v249, v249, v69, v70
v_max3_f32 v251, v251, v101, v102
v_max3_f32 v249, v249, v71, v103
v_max_f32 v249, v249, v251
v_mov_b32 v251, v249
s_nop 1
v_permlane16_swap_b32 v249, v251
v_max_f32 v249, v249, v251
v_mov_b32 v251, v249
s_nop 1
v_permlane32_swap_b32 v249, v251
v_max_f32 v249, v249, v251
v_log_f32 v250, v244
s_nop 0
v_max3_f32 v249, 0, v249, v250
v_sub_f32 v250, 0, v249
EX v250,v250
v_sub_f32 v128, v128, v249
v_sub_f32 v129, v129, v249
v_sub_f32 v130, v130, v249
v_sub_f32 v131, v131, v249
v_sub_f32 v0, v0, v249
v_sub_f32 v64, v64, v249
v_sub_f32 v1, v1, v249
v_sub_f32 v65, v65, v249
v_sub_f32 v2, v2, v249
v_sub_f32 v66, v66, v249
v_sub_f32 v3, v3, v249
v_sub_f32 v67, v67, v249
v_sub_f32 v4, v4, v249
v_sub_f32 v68, v68, v249
v_sub_f32 v5, v5, v249
v_sub_f32 v69, v69, v249
v_sub_f32 v6, v6, v249
v_sub_f32 v70, v70, v249
v_sub_f32 v7, v7, v249
v_sub_f32 v71, v71, v249
v_sub_f32 v32, v32, v249
v_sub_f32 v96, v96, v249
v_sub_f32 v33, v33, v249
v_sub_f32 v97, v97, v249
v_sub_f32 v34, v34, v249
v_sub_f32 v98, v98, v249
v_sub_f32 v35, v35, v249
v_sub_f32 v99, v99, v249
v_sub_f32 v36, v36, v249
v_sub_f32 v100, v100, v249
v_sub_f32 v37, v37, v249
v_sub_f32 v101, v101, v249
v_sub_f32 v38, v38, v249
v_sub_f32 v102, v102, v249
v_sub_f32 v39, v39, v249
v_sub_f32 v103, v103, v249
v_mul_f32 v144, v244, v250
v_mov_b32 v145, 0
v_bfe_u32 v249, v241, 0, 8
v_lshl_or_b32 v249, v249, 1, 1
v_lshlrev_b32 v249, 22, v249
v_and_b32 v241, 0xffffff00, v241
AR v254,a0
v_sub_f32 v254, v254, v249
v_mul_f32 v254, v254, v250
AW a0,v254
AR v251,a1
v_sub_f32 v251, v251, v249
v_mul_f32 v251, v251, v250
AW a1,v251
AR v254,a2
v_sub_f32 v254, v254, v249
v_mul_f32 v254, v254, v250
AW a2,v254
AR v251,a3
v_sub_f32 v251, v251, v249
v_mul_f32 v251, v251, v250
AW a3,v251
AR v254,a4
v_sub_f32 v254, v254, v249
v_mul_f32 v254, v254, v250
AW a4,v254
AR v251,a5
v_sub_f32 v251, v251, v249
v_mul_f32 v251, v251, v250
AW a5,v251
AR v254,a6
v_sub_f32 v254, v254, v249
v_mul_f32 v254, v254, v250
AW a6,v254
AR v251,a7
v_sub_f32 v251, v251, v249
v_mul_f32 v251, v251, v250
AW a7,v251
AR v254,a32
v_sub_f32 v254, v254, v249
v_mul_f32 v254, v254, v250
AW a32,v254
AR v251,a33
v_sub_f32 v251, v251, v249
v_mul_f32 v251, v251, v250
AW a33,v251
AR v254,a34
v_sub_f32 v254, v254, v249
v_mul_f32 v254, v254, v250
AW a34,v254
AR v251,a35
v_sub_f32 v251, v251, v249
v_mul_f32 v251, v251, v250
AW a35,v251
AR v254,a36
v_sub_f32 v254, v254, v249
v_mul_f32 v254, v254, v250
AW a36,v254
AR v251,a37
v_sub_f32 v251, v251, v249
v_mul_f32 v251, v251, v250
AW a37,v251
AR v254,a38
v_sub_f32 v254, v254, v249
v_mul_f32 v254, v254, v250
AW a38,v254
AR v251,a39
v_sub_f32 v251, v251, v249
v_mul_f32 v251, v251, v250
AW a39,v251
AR v254,a64
v_sub_f32 v254, v254, v249
v_mul_f32 v254, v254, v250
AW a64,v254
AR v251,a65
v_sub_f32 v251, v251, v249
v_mul_f32 v251, v251, v250
AW a65,v251
AR v254,a66
v_sub_f32 v254, v254, v249
v_mul_f32 v254, v254, v250
AW a66,v254
AR v251,a67
v_sub_f32 v251, v251, v249
v_mul_f32 v251, v251, v250
AW a67,v251
AR v254,a68
v_sub_f32 v254, v254, v249
v_mul_f32 v254, v254, v250
AW a68,v254
AR v251,a69
v_sub_f32 v251, v251, v249
v_mul_f32 v251, v251, v250
AW a69,v251
AR v254,a70
v_sub_f32 v254, v254, v249
v_mul_f32 v254, v254, v250
AW a70,v254
AR v251,a71
v_sub_f32 v251, v251, v249
v_mul_f32 v251, v251, v250
AW a71,v251
AR v254,a96
v_sub_f32 v254, v254, v249
v_mul_f32 v254, v254, v250
AW a96,v254
AR v251,a97
v_sub_f32 v251, v251, v249
v_mul_f32 v251, v251, v250
AW a97,v251
AR v254,a98
v_sub_f32 v254, v254, v249
v_mul_f32 v254, v254, v250
AW a98,v254
AR v251,a99
v_sub_f32 v251, v251, v249
v_mul_f32 v251, v251, v250
AW a99,v251
AR v254,a100
v_sub_f32 v254, v254, v249
v_mul_f32 v254, v254, v250
AW a100,v254
AR v251,a101
v_sub_f32 v251, v251, v249
v_mul_f32 v251, v251, v250
AW a101,v251
AR v254,a102
v_sub_f32 v254, v254, v249
v_mul_f32 v254, v254, v250
AW a102,v254
AR v251,a103
v_sub_f32 v251, v251, v249
v_mul_f32 v251, v251, v250
AW a103,v251
v_max3_f32 v249, v72, v73, v74
v_max3_f32 v251, v104, v105, v106
v_max3_f32 v249, v249, v75, v76
v_max3_f32 v251, v251, v107, v108
v_max3_f32 v249, v249, v77, v78
v_max3_f32 v251, v251, v109, v110
v_max3_f32 v249, v249, v79, v111
v_max_f32 v249, v249, v251
v_mov_b32 v251, v249
s_nop 1
v_permlane16_swap_b32 v249, v251
v_max_f32 v249, v249, v251
v_mov_b32 v251, v249
s_nop 1
v_permlane32_swap_b32 v249, v251
v_max_f32 v249, v249, v251
v_log_f32 v250, v245
s_nop 0
v_max3_f32 v249, 0, v249, v250
v_sub_f32 v250, 0, v249
EX v250,v250
v_sub_f32 v132, v132, v249
v_sub_f32 v133, v133, v249
v_sub_f32 v134, v134, v249
v_sub_f32 v135, v135, v249
v_sub_f32 v8, v8, v249
v_sub_f32 v72, v72, v249
v_sub_f32 v9, v9, v249
v_sub_f32 v73, v73, v249
v_sub_f32 v10, v10, v249
v_sub_f32 v74, v74, v249
v_sub_f32 v11, v11, v249
v_sub_f32 v75, v75, v249
v_sub_f32 v12, v12, v249
v_sub_f32 v76, v76, v249
v_sub_f32 v13, v13, v249
v_sub_f32 v77, v77, v249
v_sub_f32 v14, v14, v249
v_sub_f32 v78, v78, v249
v_sub_f32 v15, v15, v249
v_sub_f32 v79, v79, v249
v_sub_f32 v40, v40, v249
v_sub_f32 v104, v104, v249
v_sub_f32 v41, v41, v249
v_sub_f32 v105, v105, v249
v_sub_f32 v42, v42, v249
v_sub_f32 v106, v106, v249
v_sub_f32 v43, v43, v249
v_sub_f32 v107, v107, v249
v_sub_f32 v44, v44, v249
v_sub_f32 v108, v108, v249
v_sub_f32 v45, v45, v249
v_sub_f32 v109, v109, v249
v_sub_f32 v46, v46, v249
v_sub_f32 v110, v110, v249
v_sub_f32 v47, v47, v249
v_sub_f32 v111, v111, v249
v_mul_f32 v148, v245, v250
v_mov_b32 v149, 0
v_bfe_u32 v249, v241, 8, 8
v_lshl_or_b32 v249, v249, 1, 1
v_lshlrev_b32 v249, 22, v249
v_and_b32 v241, 0xffff00ff, v241
AR v254,a8
v_sub_f32 v254, v254, v249
v_mul_f32 v254, v254, v250
AW a8,v254
AR v251,a9
v_sub_f32 v251, v251, v249
v_mul_f32 v251, v251, v250
AW a9,v251
AR v254,a10
v_sub_f32 v254, v254, v249
v_mul_f32 v254, v254, v250
AW a10,v254
AR v251,a11
v_sub_f32 v251, v251, v249
v_mul_f32 v251, v251, v250
AW a11,v251
AR v254,a12
v_sub_f32 v254, v254, v249
v_mul_f32 v254, v254, v250
AW a12,v254
AR v251,a13
v_sub_f32 v251, v251, v249
v_mul_f32 v251, v251, v250
AW a13,v251
AR v254,a14
v_sub_f32 v254, v254, v249
v_mul_f32 v254, v254, v250
AW a14,v254
AR v251,a15
v_sub_f32 v251, v251, v249
v_mul_f32 v251, v251, v250
AW a15,v251
AR v254,a40
v_sub_f32 v254, v254, v249
v_mul_f32 v254, v254, v250
AW a40,v254
AR v251,a41
v_sub_f32 v251, v251, v249
v_mul_f32 v251, v251, v250
AW a41,v251
AR v254,a42
v_sub_f32 v254, v254, v249
v_mul_f32 v254, v254, v250
AW a42,v254
AR v251,a43
v_sub_f32 v251, v251, v249
v_mul_f32 v251, v251, v250
AW a43,v251
AR v254,a44
v_sub_f32 v254, v254, v249
v_mul_f32 v254, v254, v250
AW a44,v254
AR v251,a45
v_sub_f32 v251, v251, v249
v_mul_f32 v251, v251, v250
AW a45,v251
AR v254,a46
v_sub_f32 v254, v254, v249
v_mul_f32 v254, v254, v250
AW a46,v254
AR v251,a47
v_sub_f32 v251, v251, v249
v_mul_f32 v251, v251, v250
AW a47,v251
AR v254,a72
v_sub_f32 v254, v254, v249
v_mul_f32 v254, v254, v250
AW a72,v254
AR v251,a73
v_sub_f32 v251, v251, v249
v_mul_f32 v251, v251, v250
AW a73,v251
AR v254,a74
v_sub_f32 v254, v254, v249
v_mul_f32 v254, v254, v250
AW a74,v254
AR v251,a75
v_sub_f32 v251, v251, v249
v_mul_f32 v251, v251, v250
AW a75,v251
AR v254,a76
v_sub_f32 v254, v254, v249
v_mul_f32 v254, v254, v250
AW a76,v254
AR v251,a77
v_sub_f32 v251, v251, v249
v_mul_f32 v251, v251, v250
AW a77,v251
AR v254,a78
v_sub_f32 v254, v254, v249
v_mul_f32 v254, v254, v250
AW a78,v254
AR v251,a79
v_sub_f32 v251, v251, v249
v_mul_f32 v251, v251, v250
AW a79,v251
AR v254,a104
v_sub_f32 v254, v254, v249
v_mul_f32 v254, v254, v250
AW a104,v254
AR v251,a105
v_sub_f32 v251, v251, v249
v_mul_f32 v251, v251, v250
AW a105,v251
AR v254,a106
)ASM"
R"ASM(
v_sub_f32 v254, v254, v249
v_mul_f32 v254, v254, v250
AW a106,v254
AR v251,a107
v_sub_f32 v251, v251, v249
v_mul_f32 v251, v251, v250
AW a107,v251
AR v254,a108
v_sub_f32 v254, v254, v249
v_mul_f32 v254, v254, v250
AW a108,v254
AR v251,a109
v_sub_f32 v251, v251, v249
v_mul_f32 v251, v251, v250
AW a109,v251
AR v254,a110
v_sub_f32 v254, v254, v249
v_mul_f32 v254, v254, v250
AW a110,v254
AR v251,a111
v_sub_f32 v251, v251, v249
v_mul_f32 v251, v251, v250
AW a111,v251
v_max3_f32 v249, v80, v81, v82
v_max3_f32 v251, v112, v113, v114
v_max3_f32 v249, v249, v83, v84
v_max3_f32 v251, v251, v115, v116
v_max3_f32 v249, v249, v85, v86
v_max3_f32 v251, v251, v117, v118
v_max3_f32 v249, v249, v87, v119
v_max_f32 v249, v249, v251
v_mov_b32 v251, v249
s_nop 1
v_permlane16_swap_b32 v249, v251
v_max_f32 v249, v249, v251
v_mov_b32 v251, v249
s_nop 1
v_permlane32_swap_b32 v249, v251
v_max_f32 v249, v249, v251
v_log_f32 v250, v246
s_nop 0
v_max3_f32 v249, 0, v249, v250
v_sub_f32 v250, 0, v249
EX v250,v250
v_sub_f32 v136, v136, v249
v_sub_f32 v137, v137, v249
v_sub_f32 v138, v138, v249
v_sub_f32 v139, v139, v249
v_sub_f32 v16, v16, v249
v_sub_f32 v80, v80, v249
v_sub_f32 v17, v17, v249
v_sub_f32 v81, v81, v249
v_sub_f32 v18, v18, v249
v_sub_f32 v82, v82, v249
v_sub_f32 v19, v19, v249
v_sub_f32 v83, v83, v249
v_sub_f32 v20, v20, v249
v_sub_f32 v84, v84, v249
v_sub_f32 v21, v21, v249
v_sub_f32 v85, v85, v249
v_sub_f32 v22, v22, v249
v_sub_f32 v86, v86, v249
v_sub_f32 v23, v23, v249
v_sub_f32 v87, v87, v249
v_sub_f32 v48, v48, v249
v_sub_f32 v112, v112, v249
v_sub_f32 v49, v49, v249
v_sub_f32 v113, v113, v249
v_sub_f32 v50, v50, v249
v_sub_f32 v114, v114, v249
v_sub_f32 v51, v51, v249
v_sub_f32 v115, v115, v249
v_sub_f32 v52, v52, v249
v_sub_f32 v116, v116, v249
v_sub_f32 v53, v53, v249
v_sub_f32 v117, v117, v249
v_sub_f32 v54, v54, v249
v_sub_f32 v118, v118, v249
v_sub_f32 v55, v55, v249
v_sub_f32 v119, v119, v249
v_mul_f32 v152, v246, v250
v_mov_b32 v153, 0
v_bfe_u32 v249, v241, 16, 8
v_lshl_or_b32 v249, v249, 1, 1
v_lshlrev_b32 v249, 22, v249
v_and_b32 v241, 0xff00ffff, v241
AR v254,a16
v_sub_f32 v254, v254, v249
v_mul_f32 v254, v254, v250
AW a16,v254
AR v251,a17
v_sub_f32 v251, v251, v249
v_mul_f32 v251, v251, v250
AW a17,v251
AR v254,a18
v_sub_f32 v254, v254, v249
v_mul_f32 v254, v254, v250
AW a18,v254
AR v251,a19
v_sub_f32 v251, v251, v249
v_mul_f32 v251, v251, v250
AW a19,v251
AR v254,a20
v_sub_f32 v254, v254, v249
v_mul_f32 v254, v254, v250
AW a20,v254
AR v251,a21
v_sub_f32 v251, v251, v249
v_mul_f32 v251, v251, v250
AW a21,v251
AR v254,a22
v_sub_f32 v254, v254, v249
v_mul_f32 v254, v254, v250
AW a22,v254
AR v251,a23
v_sub_f32 v251, v251, v249
v_mul_f32 v251, v251, v250
AW a23,v251
AR v254,a48
v_sub_f32 v254, v254, v249
v_mul_f32 v254, v254, v250
AW a48,v254
AR v251,a49
v_sub_f32 v251, v251, v249
v_mul_f32 v251, v251, v250
AW a49,v251
AR v254,a50
v_sub_f32 v254, v254, v249
v_mul_f32 v254, v254, v250
AW a50,v254
AR v251,a51
v_sub_f32 v251, v251, v249
v_mul_f32 v251, v251, v250
AW a51,v251
AR v254,a52
v_sub_f32 v254, v254, v249
v_mul_f32 v254, v254, v250
AW a52,v254
AR v251,a53
v_sub_f32 v251, v251, v249
v_mul_f32 v251, v251, v250
AW a53,v251
AR v254,a54
v_sub_f32 v254, v254, v249
v_mul_f32 v254, v254, v250
AW a54,v254
AR v251,a55
v_sub_f32 v251, v251, v249
v_mul_f32 v251, v251, v250
AW a55,v251
AR v254,a80
v_sub_f32 v254, v254, v249
v_mul_f32 v254, v254, v250
AW a80,v254
AR v251,a81
v_sub_f32 v251, v251, v249
v_mul_f32 v251, v251, v250
AW a81,v251
AR v254,a82
v_sub_f32 v254, v254, v249
v_mul_f32 v254, v254, v250
AW a82,v254
AR v251,a83
v_sub_f32 v251, v251, v249
v_mul_f32 v251, v251, v250
AW a83,v251
AR v254,a84
v_sub_f32 v254, v254, v249
v_mul_f32 v254, v254, v250
AW a84,v254
AR v251,a85
v_sub_f32 v251, v251, v249
v_mul_f32 v251, v251, v250
AW a85,v251
AR v254,a86
v_sub_f32 v254, v254, v249
v_mul_f32 v254, v254, v250
AW a86,v254
AR v251,a87
v_sub_f32 v251, v251, v249
v_mul_f32 v251, v251, v250
AW a87,v251
AR v254,a112
v_sub_f32 v254, v254, v249
v_mul_f32 v254, v254, v250
AW a112,v254
AR v251,a113
v_sub_f32 v251, v251, v249
v_mul_f32 v251, v251, v250
AW a113,v251
AR v254,a114
v_sub_f32 v254, v254, v249
v_mul_f32 v254, v254, v250
AW a114,v254
AR v251,a115
v_sub_f32 v251, v251, v249
v_mul_f32 v251, v251, v250
AW a115,v251
AR v254,a116
v_sub_f32 v254, v254, v249
v_mul_f32 v254, v254, v250
AW a116,v254
AR v251,a117
v_sub_f32 v251, v251, v249
v_mul_f32 v251, v251, v250
AW a117,v251
AR v254,a118
v_sub_f32 v254, v254, v249
v_mul_f32 v254, v254, v250
AW a118,v254
AR v251,a119
v_sub_f32 v251, v251, v249
v_mul_f32 v251, v251, v250
AW a119,v251
v_max3_f32 v249, v88, v89, v90
v_max3_f32 v251, v120, v121, v122
v_max3_f32 v249, v249, v91, v92
v_max3_f32 v251, v251, v123, v124
v_max3_f32 v249, v249, v93, v94
v_max3_f32 v251, v251, v125, v126
v_max3_f32 v249, v249, v95, v127
v_max_f32 v249, v249, v251
v_mov_b32 v251, v249
s_nop 1
v_permlane16_swap_b32 v249, v251
v_max_f32 v249, v249, v251
v_mov_b32 v251, v249
s_nop 1
v_permlane32_swap_b32 v249, v251
v_max_f32 v249, v249, v251
v_log_f32 v250, v247
s_nop 0
v_max3_f32 v249, 0, v249, v250
v_sub_f32 v250, 0, v249
EX v250,v250
v_sub_f32 v140, v140, v249
v_sub_f32 v141, v141, v249
v_sub_f32 v142, v142, v249
v_sub_f32 v143, v143, v249
v_sub_f32 v24, v24, v249
v_sub_f32 v88, v88, v249
v_sub_f32 v25, v25, v249
v_sub_f32 v89, v89, v249
v_sub_f32 v26, v26, v249
v_sub_f32 v90, v90, v249
v_sub_f32 v27, v27, v249
v_sub_f32 v91, v91, v249
v_sub_f32 v28, v28, v249
v_sub_f32 v92, v92, v249
v_sub_f32 v29, v29, v249
v_sub_f32 v93, v93, v249
v_sub_f32 v30, v30, v249
v_sub_f32 v94, v94, v249
v_sub_f32 v31, v31, v249
v_sub_f32 v95, v95, v249
v_sub_f32 v56, v56, v249
v_sub_f32 v120, v120, v249
v_sub_f32 v57, v57, v249
v_sub_f32 v121, v121, v249
v_sub_f32 v58, v58, v249
v_sub_f32 v122, v122, v249
v_sub_f32 v59, v59, v249
v_sub_f32 v123, v123, v249
v_sub_f32 v60, v60, v249
v_sub_f32 v124, v124, v249
v_sub_f32 v61, v61, v249
v_sub_f32 v125, v125, v249
v_sub_f32 v62, v62, v249
v_sub_f32 v126, v126, v249
v_sub_f32 v63, v63, v249
v_sub_f32 v127, v127, v249
v_mul_f32 v156, v247, v250
v_mov_b32 v157, 0
v_bfe_u32 v249, v241, 24, 8
v_lshl_or_b32 v249, v249, 1, 1
v_lshlrev_b32 v249, 22, v249
v_and_b32 v241, 0xffffff, v241
AR v254,a24
v_sub_f32 v254, v254, v249
v_mul_f32 v254, v254, v250
AW a24,v254
AR v251,a25
v_sub_f32 v251, v251, v249
v_mul_f32 v251, v251, v250
AW a25,v251
AR v254,a26
v_sub_f32 v254, v254, v249
v_mul_f32 v254, v254, v250
AW a26,v254
AR v251,a27
v_sub_f32 v251, v251, v249
v_mul_f32 v251, v251, v250
AW a27,v251
AR v254,a28
v_sub_f32 v254, v254, v249
v_mul_f32 v254, v254, v250
AW a28,v254
AR v251,a29
v_sub_f32 v251, v251, v249
v_mul_f32 v251, v251, v250
AW a29,v251
AR v254,a30
v_sub_f32 v254, v254, v249
v_mul_f32 v254, v254, v250
AW a30,v254
AR v251,a31
v_sub_f32 v251, v251, v249
v_mul_f32 v251, v251, v250
AW a31,v251
AR v254,a56
v_sub_f32 v254, v254, v249
v_mul_f32 v254, v254, v250
AW a56,v254
AR v251,a57
v_sub_f32 v251, v251, v249
v_mul_f32 v251, v251, v250
AW a57,v251
AR v254,a58
v_sub_f32 v254, v254, v249
v_mul_f32 v254, v254, v250
AW a58,v254
AR v251,a59
v_sub_f32 v251, v251, v249
v_mul_f32 v251, v251, v250
AW a59,v251
AR v254,a60
v_sub_f32 v254, v254, v249
v_mul_f32 v254, v254, v250
AW a60,v254
AR v251,a61
v_sub_f32 v251, v251, v249
v_mul_f32 v251, v251, v250
AW a61,v251
AR v254,a62
v_sub_f32 v254, v254, v249
v_mul_f32 v254, v254, v250
AW a62,v254
AR v251,a63
v_sub_f32 v251, v251, v249
v_mul_f32 v251, v251, v250
AW a63,v251
AR v254,a88
v_sub_f32 v254, v254, v249
v_mul_f32 v254, v254, v250
AW a88,v254
AR v251,a89
v_sub_f32 v251, v251, v249
v_mul_f32 v251, v251, v250
AW a89,v251
AR v254,a90
v_sub_f32 v254, v254, v249
v_mul_f32 v254, v254, v250
AW a90,v254
AR v251,a91
v_sub_f32 v251, v251, v249
v_mul_f32 v251, v251, v250
AW a91,v251
AR v254,a92
v_sub_f32 v254, v254, v249
v_mul_f32 v254, v254, v250
AW a92,v254
AR v251,a93
v_sub_f32 v251, v251, v249
v_mul_f32 v251, v251, v250
AW a93,v251
AR v254,a94
v_sub_f32 v254, v254, v249
v_mul_f32 v254, v254, v250
AW a94,v254
AR v251,a95
v_sub_f32 v251, v251, v249
v_mul_f32 v251, v251, v250
AW a95,v251
AR v254,a120
v_sub_f32 v254, v254, v249
v_mul_f32 v254, v254, v250
AW a120,v254
AR v251,a121
v_sub_f32 v251, v251, v249
v_mul_f32 v251, v251, v250
AW a121,v251
AR v254,a122
v_sub_f32 v254, v254, v249
v_mul_f32 v254, v254, v250
AW a122,v254
AR v251,a123
v_sub_f32 v251, v251, v249
v_mul_f32 v251, v251, v250
AW a123,v251
AR v254,a124
v_sub_f32 v254, v254, v249
v_mul_f32 v254, v254, v250
AW a124,v254
AR v251,a125
v_sub_f32 v251, v251, v249
v_mul_f32 v251, v251, v250
AW a125,v251
AR v254,a126
v_sub_f32 v254, v254, v249
v_mul_f32 v254, v254, v250
AW a126,v254
AR v251,a127
v_sub_f32 v251, v251, v249
v_mul_f32 v251, v251, v250
AW a127,v251
EX v64,v64
EX v65,v65
EX v66,v66
EX v67,v67
EX v68,v68
EX v69,v69
EX v70,v70
EX v71,v71
EX v72,v72
EX v73,v73
EX v74,v74
EX v75,v75
EX v76,v76
EX v77,v77
EX v78,v78
EX v79,v79
EX v80,v80
EX v81,v81
EX v82,v82
EX v83,v83
EX v84,v84
EX v85,v85
EX v86,v86
EX v87,v87
EX v88,v88
EX v89,v89
EX v90,v90
EX v91,v91
EX v92,v92
EX v93,v93
EX v94,v94
EX v95,v95
EX v96,v96
EX v97,v97
EX v98,v98
EX v99,v99
EX v100,v100
EX v101,v101
EX v102,v102
EX v103,v103
EX v104,v104
EX v105,v105
EX v106,v106
EX v107,v107
EX v108,v108
EX v109,v109
EX v110,v110
EX v111,v111
EX v112,v112
EX v113,v113
EX v114,v114
EX v115,v115
EX v116,v116
EX v117,v117
EX v118,v118
EX v119,v119
EX v120,v120
EX v121,v121
EX v122,v122
EX v123,v123
EX v124,v124
EX v125,v125
EX v126,v126
EX v127,v127
CV v64,v64,v65
CV v65,v66,v67
CV v66,v68,v69
CV v67,v70,v71
CV v72,v72,v73
CV v73,v74,v75
CV v74,v76,v77
CV v75,v78,v79
CV v80,v80,v81
CV v81,v82,v83
CV v82,v84,v85
CV v83,v86,v87
CV v88,v88,v89
CV v89,v90,v91
CV v90,v92,v93
CV v91,v94,v95
CV v68,v96,v97
CV v69,v98,v99
CV v70,v100,v101
CV v71,v102,v103
CV v76,v104,v105
CV v77,v106,v107
CV v78,v108,v109
CV v79,v110,v111
CV v84,v112,v113
CV v85,v114,v115
CV v86,v116,v117
CV v87,v118,v119
CV v92,v120,v121
CV v93,v122,v123
CV v94,v124,v125
CV v95,v126,v127
v_smfmac_f32_16x16x64_bf16 v[144:147], v[236:239], v[64:71], v234
v_smfmac_f32_16x16x64_bf16 v[148:151], v[236:239], v[72:79], v234
v_smfmac_f32_16x16x64_bf16 v[152:155], v[236:239], v[80:87], v234
v_smfmac_f32_16x16x64_bf16 v[156:159], v[236:239], v[88:95], v234
s_nop 9
s_branch LRET_%=
LRARE004_%=:
s_nop 15
s_waitcnt vmcnt(0) lgkmcnt(0)
v_add_u32 v249, s74, v224
RD a[192:195],v249 offset:0
RD a[208:211],v249 offset:4096
RD a[224:227],v249 offset:8192
RD a[240:243],v249 offset:12288
v_add_u32 v250, s74, v225
RD a[196:199],v250 offset:0
RD a[212:215],v250 offset:4096
RD a[228:231],v250 offset:8192
RD a[244:247],v250 offset:12288
v_add_u32 v251, s74, v226
RD a[200:203],v251 offset:0
RD a[216:219],v251 offset:4096
RD a[232:235],v251 offset:8192
RD a[248:251],v251 offset:12288
v_add_u32 v254, s74, v227
RD a[204:207],v254 offset:0
RD a[220:223],v254 offset:4096
RD a[236:239],v254 offset:8192
RD a[252:255],v254 offset:12288
s_waitcnt lgkmcnt(0)
MM v[0:3],a[192:195],v[160:163],v[128:131]
MM v[0:3],a[196:199],v[176:179],v[0:3]
MM v[0:3],a[200:203],v[192:195],v[0:3]
MM v[0:3],a[204:207],v[208:211],v[0:3]
MM v[4:7],a[220:223],v[208:211],v[128:131]
MM v[4:7],a[216:219],v[192:195],v[4:7]
MM v[4:7],a[212:215],v[176:179],v[4:7]
MM v[4:7],a[208:211],v[160:163],v[4:7]
MM v[12:15],a[208:211],v[164:167],v[132:135]
MM v[12:15],a[212:215],v[180:183],v[12:15]
MM v[12:15],a[216:219],v[196:199],v[12:15]
MM v[12:15],a[220:223],v[212:215],v[12:15]
MM v[8:11],a[204:207],v[212:215],v[132:135]
MM v[8:11],a[200:203],v[196:199],v[8:11]
MM v[8:11],a[196:199],v[180:183],v[8:11]
MM v[8:11],a[192:195],v[164:167],v[8:11]
MM v[16:19],a[192:195],v[168:171],v[136:139]
MM v[16:19],a[196:199],v[184:187],v[16:19]
MM v[16:19],a[200:203],v[200:203],v[16:19]
MM v[16:19],a[204:207],v[216:219],v[16:19]
MM v[20:23],a[220:223],v[216:219],v[136:139]
MM v[20:23],a[216:219],v[200:203],v[20:23]
MM v[20:23],a[212:215],v[184:187],v[20:23]
MM v[20:23],a[208:211],v[168:171],v[20:23]
MM v[28:31],a[208:211],v[172:175],v[140:143]
)ASM"
R"ASM(
MM v[28:31],a[212:215],v[188:191],v[28:31]
MM v[28:31],a[216:219],v[204:207],v[28:31]
MM v[28:31],a[220:223],v[220:223],v[28:31]
MM v[24:27],a[204:207],v[220:223],v[140:143]
MM v[24:27],a[200:203],v[204:207],v[24:27]
MM v[24:27],a[196:199],v[188:191],v[24:27]
MM v[24:27],a[192:195],v[172:175],v[24:27]
MM v[32:35],a[224:227],v[160:163],v[128:131]
MM v[32:35],a[228:231],v[176:179],v[32:35]
MM v[32:35],a[232:235],v[192:195],v[32:35]
MM v[32:35],a[236:239],v[208:211],v[32:35]
MM v[36:39],a[252:255],v[208:211],v[128:131]
MM v[36:39],a[248:251],v[192:195],v[36:39]
MM v[36:39],a[244:247],v[176:179],v[36:39]
MM v[36:39],a[240:243],v[160:163],v[36:39]
MM v[44:47],a[240:243],v[164:167],v[132:135]
MM v[44:47],a[244:247],v[180:183],v[44:47]
MM v[44:47],a[248:251],v[196:199],v[44:47]
MM v[44:47],a[252:255],v[212:215],v[44:47]
MM v[40:43],a[236:239],v[212:215],v[132:135]
MM v[40:43],a[232:235],v[196:199],v[40:43]
MM v[40:43],a[228:231],v[180:183],v[40:43]
MM v[40:43],a[224:227],v[164:167],v[40:43]
MM v[48:51],a[224:227],v[168:171],v[136:139]
MM v[48:51],a[228:231],v[184:187],v[48:51]
MM v[48:51],a[232:235],v[200:203],v[48:51]
MM v[48:51],a[236:239],v[216:219],v[48:51]
MM v[52:55],a[252:255],v[216:219],v[136:139]
MM v[52:55],a[248:251],v[200:203],v[52:55]
MM v[52:55],a[244:247],v[184:187],v[52:55]
MM v[52:55],a[240:243],v[168:171],v[52:55]
MM v[60:63],a[240:243],v[172:175],v[140:143]
MM v[60:63],a[244:247],v[188:191],v[60:63]
MM v[60:63],a[248:251],v[204:207],v[60:63]
MM v[60:63],a[252:255],v[220:223],v[60:63]
MM v[56:59],a[236:239],v[220:223],v[140:143]
MM v[56:59],a[232:235],v[204:207],v[56:59]
MM v[56:59],a[228:231],v[188:191],v[56:59]
MM v[56:59],a[224:227],v[172:175],v[56:59]
s_nop 15
v_add_u32 v249, s75, v224
RD a[192:195],v249 offset:0
RD a[208:211],v249 offset:4096
RD a[224:227],v249 offset:8192
RD a[240:243],v249 offset:12288
v_add_u32 v250, s75, v225
RD a[196:199],v250 offset:0
RD a[212:215],v250 offset:4096
RD a[228:231],v250 offset:8192
RD a[244:247],v250 offset:12288
v_add_u32 v251, s75, v226
RD a[200:203],v251 offset:0
RD a[216:219],v251 offset:4096
RD a[232:235],v251 offset:8192
RD a[248:251],v251 offset:12288
v_add_u32 v254, s75, v227
RD a[204:207],v254 offset:0
RD a[220:223],v254 offset:4096
RD a[236:239],v254 offset:8192
RD a[252:255],v254 offset:12288
s_waitcnt lgkmcnt(0)
v_max3_f32 v249, v0, v1, v2
v_max3_f32 v251, v32, v33, v34
v_max3_f32 v249, v249, v3, v4
v_max3_f32 v251, v251, v35, v36
v_max3_f32 v249, v249, v5, v6
v_max3_f32 v251, v251, v37, v38
v_max3_f32 v249, v249, v7, v39
v_max_f32 v249, v249, v251
v_mov_b32 v251, v249
s_nop 1
v_permlane16_swap_b32 v249, v251
v_max_f32 v249, v249, v251
v_mov_b32 v251, v249
s_nop 1
v_permlane32_swap_b32 v249, v251
v_max_f32 v249, v249, v251
v_log_f32 v250, v244
s_nop 0
v_max3_f32 v249, 0, v249, v250
v_sub_f32 v250, 0, v249
EX v250,v250
v_sub_f32 v128, v128, v249
v_sub_f32 v129, v129, v249
v_sub_f32 v130, v130, v249
v_sub_f32 v131, v131, v249
v_sub_f32 v64, v64, v249
v_sub_f32 v0, v0, v249
v_sub_f32 v65, v65, v249
v_sub_f32 v1, v1, v249
v_sub_f32 v66, v66, v249
v_sub_f32 v2, v2, v249
v_sub_f32 v67, v67, v249
v_sub_f32 v3, v3, v249
v_sub_f32 v68, v68, v249
v_sub_f32 v4, v4, v249
v_sub_f32 v69, v69, v249
v_sub_f32 v5, v5, v249
v_sub_f32 v70, v70, v249
v_sub_f32 v6, v6, v249
v_sub_f32 v71, v71, v249
v_sub_f32 v7, v7, v249
v_sub_f32 v96, v96, v249
v_sub_f32 v32, v32, v249
v_sub_f32 v97, v97, v249
v_sub_f32 v33, v33, v249
v_sub_f32 v98, v98, v249
v_sub_f32 v34, v34, v249
v_sub_f32 v99, v99, v249
v_sub_f32 v35, v35, v249
v_sub_f32 v100, v100, v249
v_sub_f32 v36, v36, v249
v_sub_f32 v101, v101, v249
v_sub_f32 v37, v37, v249
v_sub_f32 v102, v102, v249
v_sub_f32 v38, v38, v249
v_sub_f32 v103, v103, v249
v_sub_f32 v39, v39, v249
v_mul_f32 v144, v244, v250
v_mov_b32 v145, 0
v_bfe_u32 v249, v241, 0, 8
v_lshl_or_b32 v249, v249, 1, 1
v_lshlrev_b32 v249, 22, v249
v_and_b32 v241, 0xffffff00, v241
AR v254,a0
v_sub_f32 v254, v254, v249
v_mul_f32 v254, v254, v250
AW a0,v254
AR v251,a1
v_sub_f32 v251, v251, v249
v_mul_f32 v251, v251, v250
AW a1,v251
AR v254,a2
v_sub_f32 v254, v254, v249
v_mul_f32 v254, v254, v250
AW a2,v254
AR v251,a3
v_sub_f32 v251, v251, v249
v_mul_f32 v251, v251, v250
AW a3,v251
AR v254,a4
v_sub_f32 v254, v254, v249
v_mul_f32 v254, v254, v250
AW a4,v254
AR v251,a5
v_sub_f32 v251, v251, v249
v_mul_f32 v251, v251, v250
AW a5,v251
AR v254,a6
v_sub_f32 v254, v254, v249
v_mul_f32 v254, v254, v250
AW a6,v254
AR v251,a7
v_sub_f32 v251, v251, v249
v_mul_f32 v251, v251, v250
AW a7,v251
AR v254,a32
v_sub_f32 v254, v254, v249
v_mul_f32 v254, v254, v250
AW a32,v254
AR v251,a33
v_sub_f32 v251, v251, v249
v_mul_f32 v251, v251, v250
AW a33,v251
AR v254,a34
v_sub_f32 v254, v254, v249
v_mul_f32 v254, v254, v250
AW a34,v254
AR v251,a35
v_sub_f32 v251, v251, v249
v_mul_f32 v251, v251, v250
AW a35,v251
AR v254,a36
v_sub_f32 v254, v254, v249
v_mul_f32 v254, v254, v250
AW a36,v254
AR v251,a37
v_sub_f32 v251, v251, v249
v_mul_f32 v251, v251, v250
AW a37,v251
AR v254,a38
v_sub_f32 v254, v254, v249
v_mul_f32 v254, v254, v250
AW a38,v254
AR v251,a39
v_sub_f32 v251, v251, v249
v_mul_f32 v251, v251, v250
AW a39,v251
AR v254,a64
v_sub_f32 v254, v254, v249
v_mul_f32 v254, v254, v250
AW a64,v254
AR v251,a65
v_sub_f32 v251, v251, v249
v_mul_f32 v251, v251, v250
AW a65,v251
AR v254,a66
v_sub_f32 v254, v254, v249
v_mul_f32 v254, v254, v250
AW a66,v254
AR v251,a67
v_sub_f32 v251, v251, v249
v_mul_f32 v251, v251, v250
AW a67,v251
AR v254,a68
v_sub_f32 v254, v254, v249
v_mul_f32 v254, v254, v250
AW a68,v254
AR v251,a69
v_sub_f32 v251, v251, v249
v_mul_f32 v251, v251, v250
AW a69,v251
AR v254,a70
v_sub_f32 v254, v254, v249
v_mul_f32 v254, v254, v250
AW a70,v254
AR v251,a71
v_sub_f32 v251, v251, v249
v_mul_f32 v251, v251, v250
AW a71,v251
AR v254,a96
v_sub_f32 v254, v254, v249
v_mul_f32 v254, v254, v250
AW a96,v254
AR v251,a97
v_sub_f32 v251, v251, v249
v_mul_f32 v251, v251, v250
AW a97,v251
AR v254,a98
v_sub_f32 v254, v254, v249
v_mul_f32 v254, v254, v250
AW a98,v254
AR v251,a99
v_sub_f32 v251, v251, v249
v_mul_f32 v251, v251, v250
AW a99,v251
AR v254,a100
v_sub_f32 v254, v254, v249
v_mul_f32 v254, v254, v250
AW a100,v254
AR v251,a101
v_sub_f32 v251, v251, v249
v_mul_f32 v251, v251, v250
AW a101,v251
AR v254,a102
v_sub_f32 v254, v254, v249
v_mul_f32 v254, v254, v250
AW a102,v254
AR v251,a103
v_sub_f32 v251, v251, v249
v_mul_f32 v251, v251, v250
AW a103,v251
v_max3_f32 v249, v8, v9, v10
v_max3_f32 v251, v40, v41, v42
v_max3_f32 v249, v249, v11, v12
v_max3_f32 v251, v251, v43, v44
v_max3_f32 v249, v249, v13, v14
v_max3_f32 v251, v251, v45, v46
v_max3_f32 v249, v249, v15, v47
v_max_f32 v249, v249, v251
v_mov_b32 v251, v249
s_nop 1
v_permlane16_swap_b32 v249, v251
v_max_f32 v249, v249, v251
v_mov_b32 v251, v249
s_nop 1
v_permlane32_swap_b32 v249, v251
v_max_f32 v249, v249, v251
v_log_f32 v250, v245
s_nop 0
v_max3_f32 v249, 0, v249, v250
v_sub_f32 v250, 0, v249
EX v250,v250
v_sub_f32 v132, v132, v249
v_sub_f32 v133, v133, v249
v_sub_f32 v134, v134, v249
v_sub_f32 v135, v135, v249
v_sub_f32 v72, v72, v249
v_sub_f32 v8, v8, v249
v_sub_f32 v73, v73, v249
v_sub_f32 v9, v9, v249
v_sub_f32 v74, v74, v249
v_sub_f32 v10, v10, v249
v_sub_f32 v75, v75, v249
v_sub_f32 v11, v11, v249
v_sub_f32 v76, v76, v249
v_sub_f32 v12, v12, v249
v_sub_f32 v77, v77, v249
v_sub_f32 v13, v13, v249
v_sub_f32 v78, v78, v249
v_sub_f32 v14, v14, v249
v_sub_f32 v79, v79, v249
v_sub_f32 v15, v15, v249
v_sub_f32 v104, v104, v249
v_sub_f32 v40, v40, v249
v_sub_f32 v105, v105, v249
v_sub_f32 v41, v41, v249
v_sub_f32 v106, v106, v249
v_sub_f32 v42, v42, v249
v_sub_f32 v107, v107, v249
v_sub_f32 v43, v43, v249
v_sub_f32 v108, v108, v249
v_sub_f32 v44, v44, v249
v_sub_f32 v109, v109, v249
v_sub_f32 v45, v45, v249
v_sub_f32 v110, v110, v249
v_sub_f32 v46, v46, v249
v_sub_f32 v111, v111, v249
v_sub_f32 v47, v47, v249
v_mul_f32 v148, v245, v250
v_mov_b32 v149, 0
v_bfe_u32 v249, v241, 8, 8
v_lshl_or_b32 v249, v249, 1, 1
v_lshlrev_b32 v249, 22, v249
v_and_b32 v241, 0xffff00ff, v241
AR v254,a8
v_sub_f32 v254, v254, v249
v_mul_f32 v254, v254, v250
AW a8,v254
AR v251,a9
v_sub_f32 v251, v251, v249
v_mul_f32 v251, v251, v250
AW a9,v251
AR v254,a10
v_sub_f32 v254, v254, v249
v_mul_f32 v254, v254, v250
AW a10,v254
AR v251,a11
v_sub_f32 v251, v251, v249
v_mul_f32 v251, v251, v250
AW a11,v251
AR v254,a12
v_sub_f32 v254, v254, v249
v_mul_f32 v254, v254, v250
AW a12,v254
AR v251,a13
v_sub_f32 v251, v251, v249
v_mul_f32 v251, v251, v250
AW a13,v251
AR v254,a14
v_sub_f32 v254, v254, v249
v_mul_f32 v254, v254, v250
AW a14,v254
AR v251,a15
v_sub_f32 v251, v251, v249
v_mul_f32 v251, v251, v250
AW a15,v251
AR v254,a40
v_sub_f32 v254, v254, v249
v_mul_f32 v254, v254, v250
AW a40,v254
AR v251,a41
v_sub_f32 v251, v251, v249
v_mul_f32 v251, v251, v250
AW a41,v251
AR v254,a42
v_sub_f32 v254, v254, v249
v_mul_f32 v254, v254, v250
AW a42,v254
AR v251,a43
v_sub_f32 v251, v251, v249
v_mul_f32 v251, v251, v250
AW a43,v251
AR v254,a44
v_sub_f32 v254, v254, v249
v_mul_f32 v254, v254, v250
AW a44,v254
AR v251,a45
v_sub_f32 v251, v251, v249
v_mul_f32 v251, v251, v250
AW a45,v251
AR v254,a46
v_sub_f32 v254, v254, v249
v_mul_f32 v254, v254, v250
AW a46,v254
AR v251,a47
v_sub_f32 v251, v251, v249
v_mul_f32 v251, v251, v250
AW a47,v251
AR v254,a72
v_sub_f32 v254, v254, v249
v_mul_f32 v254, v254, v250
AW a72,v254
AR v251,a73
v_sub_f32 v251, v251, v249
v_mul_f32 v251, v251, v250
AW a73,v251
AR v254,a74
v_sub_f32 v254, v254, v249
v_mul_f32 v254, v254, v250
AW a74,v254
AR v251,a75
v_sub_f32 v251, v251, v249
v_mul_f32 v251, v251, v250
AW a75,v251
AR v254,a76
v_sub_f32 v254, v254, v249
v_mul_f32 v254, v254, v250
AW a76,v254
AR v251,a77
v_sub_f32 v251, v251, v249
v_mul_f32 v251, v251, v250
AW a77,v251
AR v254,a78
v_sub_f32 v254, v254, v249
v_mul_f32 v254, v254, v250
AW a78,v254
AR v251,a79
v_sub_f32 v251, v251, v249
v_mul_f32 v251, v251, v250
AW a79,v251
AR v254,a104
v_sub_f32 v254, v254, v249
v_mul_f32 v254, v254, v250
AW a104,v254
AR v251,a105
v_sub_f32 v251, v251, v249
v_mul_f32 v251, v251, v250
AW a105,v251
AR v254,a106
v_sub_f32 v254, v254, v249
v_mul_f32 v254, v254, v250
AW a106,v254
AR v251,a107
v_sub_f32 v251, v251, v249
v_mul_f32 v251, v251, v250
AW a107,v251
AR v254,a108
v_sub_f32 v254, v254, v249
v_mul_f32 v254, v254, v250
AW a108,v254
AR v251,a109
v_sub_f32 v251, v251, v249
v_mul_f32 v251, v251, v250
AW a109,v251
AR v254,a110
v_sub_f32 v254, v254, v249
v_mul_f32 v254, v254, v250
AW a110,v254
AR v251,a111
v_sub_f32 v251, v251, v249
v_mul_f32 v251, v251, v250
AW a111,v251
v_max3_f32 v249, v16, v17, v18
v_max3_f32 v251, v48, v49, v50
v_max3_f32 v249, v249, v19, v20
v_max3_f32 v251, v251, v51, v52
v_max3_f32 v249, v249, v21, v22
v_max3_f32 v251, v251, v53, v54
v_max3_f32 v249, v249, v23, v55
v_max_f32 v249, v249, v251
v_mov_b32 v251, v249
s_nop 1
v_permlane16_swap_b32 v249, v251
v_max_f32 v249, v249, v251
v_mov_b32 v251, v249
s_nop 1
v_permlane32_swap_b32 v249, v251
v_max_f32 v249, v249, v251
v_log_f32 v250, v246
s_nop 0
v_max3_f32 v249, 0, v249, v250
v_sub_f32 v250, 0, v249
EX v250,v250
v_sub_f32 v136, v136, v249
v_sub_f32 v137, v137, v249
v_sub_f32 v138, v138, v249
v_sub_f32 v139, v139, v249
v_sub_f32 v80, v80, v249
v_sub_f32 v16, v16, v249
v_sub_f32 v81, v81, v249
v_sub_f32 v17, v17, v249
v_sub_f32 v82, v82, v249
v_sub_f32 v18, v18, v249
v_sub_f32 v83, v83, v249
v_sub_f32 v19, v19, v249
v_sub_f32 v84, v84, v249
v_sub_f32 v20, v20, v249
v_sub_f32 v85, v85, v249
v_sub_f32 v21, v21, v249
v_sub_f32 v86, v86, v249
v_sub_f32 v22, v22, v249
v_sub_f32 v87, v87, v249
v_sub_f32 v23, v23, v249
v_sub_f32 v112, v112, v249
v_sub_f32 v48, v48, v249
v_sub_f32 v113, v113, v249
v_sub_f32 v49, v49, v249
v_sub_f32 v114, v114, v249
v_sub_f32 v50, v50, v249
v_sub_f32 v115, v115, v249
v_sub_f32 v51, v51, v249
v_sub_f32 v116, v116, v249
v_sub_f32 v52, v52, v249
v_sub_f32 v117, v117, v249
v_sub_f32 v53, v53, v249
v_sub_f32 v118, v118, v249
v_sub_f32 v54, v54, v249
v_sub_f32 v119, v119, v249
v_sub_f32 v55, v55, v249
v_mul_f32 v152, v246, v250
v_mov_b32 v153, 0
v_bfe_u32 v249, v241, 16, 8
)ASM"
R"ASM(
v_lshl_or_b32 v249, v249, 1, 1
v_lshlrev_b32 v249, 22, v249
v_and_b32 v241, 0xff00ffff, v241
AR v254,a16
v_sub_f32 v254, v254, v249
v_mul_f32 v254, v254, v250
AW a16,v254
AR v251,a17
v_sub_f32 v251, v251, v249
v_mul_f32 v251, v251, v250
AW a17,v251
AR v254,a18
v_sub_f32 v254, v254, v249
v_mul_f32 v254, v254, v250
AW a18,v254
AR v251,a19
v_sub_f32 v251, v251, v249
v_mul_f32 v251, v251, v250
AW a19,v251
AR v254,a20
v_sub_f32 v254, v254, v249
v_mul_f32 v254, v254, v250
AW a20,v254
AR v251,a21
v_sub_f32 v251, v251, v249
v_mul_f32 v251, v251, v250
AW a21,v251
AR v254,a22
v_sub_f32 v254, v254, v249
v_mul_f32 v254, v254, v250
AW a22,v254
AR v251,a23
v_sub_f32 v251, v251, v249
v_mul_f32 v251, v251, v250
AW a23,v251
AR v254,a48
v_sub_f32 v254, v254, v249
v_mul_f32 v254, v254, v250
AW a48,v254
AR v251,a49
v_sub_f32 v251, v251, v249
v_mul_f32 v251, v251, v250
AW a49,v251
AR v254,a50
v_sub_f32 v254, v254, v249
v_mul_f32 v254, v254, v250
AW a50,v254
AR v251,a51
v_sub_f32 v251, v251, v249
v_mul_f32 v251, v251, v250
AW a51,v251
AR v254,a52
v_sub_f32 v254, v254, v249
v_mul_f32 v254, v254, v250
AW a52,v254
AR v251,a53
v_sub_f32 v251, v251, v249
v_mul_f32 v251, v251, v250
AW a53,v251
AR v254,a54
v_sub_f32 v254, v254, v249
v_mul_f32 v254, v254, v250
AW a54,v254
AR v251,a55
v_sub_f32 v251, v251, v249
v_mul_f32 v251, v251, v250
AW a55,v251
AR v254,a80
v_sub_f32 v254, v254, v249
v_mul_f32 v254, v254, v250
AW a80,v254
AR v251,a81
v_sub_f32 v251, v251, v249
v_mul_f32 v251, v251, v250
AW a81,v251
AR v254,a82
v_sub_f32 v254, v254, v249
v_mul_f32 v254, v254, v250
AW a82,v254
AR v251,a83
v_sub_f32 v251, v251, v249
v_mul_f32 v251, v251, v250
AW a83,v251
AR v254,a84
v_sub_f32 v254, v254, v249
v_mul_f32 v254, v254, v250
AW a84,v254
AR v251,a85
v_sub_f32 v251, v251, v249
v_mul_f32 v251, v251, v250
AW a85,v251
AR v254,a86
v_sub_f32 v254, v254, v249
v_mul_f32 v254, v254, v250
AW a86,v254
AR v251,a87
v_sub_f32 v251, v251, v249
v_mul_f32 v251, v251, v250
AW a87,v251
AR v254,a112
v_sub_f32 v254, v254, v249
v_mul_f32 v254, v254, v250
AW a112,v254
AR v251,a113
v_sub_f32 v251, v251, v249
v_mul_f32 v251, v251, v250
AW a113,v251
AR v254,a114
v_sub_f32 v254, v254, v249
v_mul_f32 v254, v254, v250
AW a114,v254
AR v251,a115
v_sub_f32 v251, v251, v249
v_mul_f32 v251, v251, v250
AW a115,v251
AR v254,a116
v_sub_f32 v254, v254, v249
v_mul_f32 v254, v254, v250
AW a116,v254
AR v251,a117
v_sub_f32 v251, v251, v249
v_mul_f32 v251, v251, v250
AW a117,v251
AR v254,a118
v_sub_f32 v254, v254, v249
v_mul_f32 v254, v254, v250
AW a118,v254
AR v251,a119
v_sub_f32 v251, v251, v249
v_mul_f32 v251, v251, v250
AW a119,v251
v_max3_f32 v249, v24, v25, v26
v_max3_f32 v251, v56, v57, v58
v_max3_f32 v249, v249, v27, v28
v_max3_f32 v251, v251, v59, v60
v_max3_f32 v249, v249, v29, v30
v_max3_f32 v251, v251, v61, v62
v_max3_f32 v249, v249, v31, v63
v_max_f32 v249, v249, v251
v_mov_b32 v251, v249
s_nop 1
v_permlane16_swap_b32 v249, v251
v_max_f32 v249, v249, v251
v_mov_b32 v251, v249
s_nop 1
v_permlane32_swap_b32 v249, v251
v_max_f32 v249, v249, v251
v_log_f32 v250, v247
s_nop 0
v_max3_f32 v249, 0, v249, v250
v_sub_f32 v250, 0, v249
EX v250,v250
v_sub_f32 v140, v140, v249
v_sub_f32 v141, v141, v249
v_sub_f32 v142, v142, v249
v_sub_f32 v143, v143, v249
v_sub_f32 v88, v88, v249
v_sub_f32 v24, v24, v249
v_sub_f32 v89, v89, v249
v_sub_f32 v25, v25, v249
v_sub_f32 v90, v90, v249
v_sub_f32 v26, v26, v249
v_sub_f32 v91, v91, v249
v_sub_f32 v27, v27, v249
v_sub_f32 v92, v92, v249
v_sub_f32 v28, v28, v249
v_sub_f32 v93, v93, v249
v_sub_f32 v29, v29, v249
v_sub_f32 v94, v94, v249
v_sub_f32 v30, v30, v249
v_sub_f32 v95, v95, v249
v_sub_f32 v31, v31, v249
v_sub_f32 v120, v120, v249
v_sub_f32 v56, v56, v249
v_sub_f32 v121, v121, v249
v_sub_f32 v57, v57, v249
v_sub_f32 v122, v122, v249
v_sub_f32 v58, v58, v249
v_sub_f32 v123, v123, v249
v_sub_f32 v59, v59, v249
v_sub_f32 v124, v124, v249
v_sub_f32 v60, v60, v249
v_sub_f32 v125, v125, v249
v_sub_f32 v61, v61, v249
v_sub_f32 v126, v126, v249
v_sub_f32 v62, v62, v249
v_sub_f32 v127, v127, v249
v_sub_f32 v63, v63, v249
v_mul_f32 v156, v247, v250
v_mov_b32 v157, 0
v_bfe_u32 v249, v241, 24, 8
v_lshl_or_b32 v249, v249, 1, 1
v_lshlrev_b32 v249, 22, v249
v_and_b32 v241, 0xffffff, v241
AR v254,a24
v_sub_f32 v254, v254, v249
v_mul_f32 v254, v254, v250
AW a24,v254
AR v251,a25
v_sub_f32 v251, v251, v249
v_mul_f32 v251, v251, v250
AW a25,v251
AR v254,a26
v_sub_f32 v254, v254, v249
v_mul_f32 v254, v254, v250
AW a26,v254
AR v251,a27
v_sub_f32 v251, v251, v249
v_mul_f32 v251, v251, v250
AW a27,v251
AR v254,a28
v_sub_f32 v254, v254, v249
v_mul_f32 v254, v254, v250
AW a28,v254
AR v251,a29
v_sub_f32 v251, v251, v249
v_mul_f32 v251, v251, v250
AW a29,v251
AR v254,a30
v_sub_f32 v254, v254, v249
v_mul_f32 v254, v254, v250
AW a30,v254
AR v251,a31
v_sub_f32 v251, v251, v249
v_mul_f32 v251, v251, v250
AW a31,v251
AR v254,a56
v_sub_f32 v254, v254, v249
v_mul_f32 v254, v254, v250
AW a56,v254
AR v251,a57
v_sub_f32 v251, v251, v249
v_mul_f32 v251, v251, v250
AW a57,v251
AR v254,a58
v_sub_f32 v254, v254, v249
v_mul_f32 v254, v254, v250
AW a58,v254
AR v251,a59
v_sub_f32 v251, v251, v249
v_mul_f32 v251, v251, v250
AW a59,v251
AR v254,a60
v_sub_f32 v254, v254, v249
v_mul_f32 v254, v254, v250
AW a60,v254
AR v251,a61
v_sub_f32 v251, v251, v249
v_mul_f32 v251, v251, v250
AW a61,v251
AR v254,a62
v_sub_f32 v254, v254, v249
v_mul_f32 v254, v254, v250
AW a62,v254
AR v251,a63
v_sub_f32 v251, v251, v249
v_mul_f32 v251, v251, v250
AW a63,v251
AR v254,a88
v_sub_f32 v254, v254, v249
v_mul_f32 v254, v254, v250
AW a88,v254
AR v251,a89
v_sub_f32 v251, v251, v249
v_mul_f32 v251, v251, v250
AW a89,v251
AR v254,a90
v_sub_f32 v254, v254, v249
v_mul_f32 v254, v254, v250
AW a90,v254
AR v251,a91
v_sub_f32 v251, v251, v249
v_mul_f32 v251, v251, v250
AW a91,v251
AR v254,a92
v_sub_f32 v254, v254, v249
v_mul_f32 v254, v254, v250
AW a92,v254
AR v251,a93
v_sub_f32 v251, v251, v249
v_mul_f32 v251, v251, v250
AW a93,v251
AR v254,a94
v_sub_f32 v254, v254, v249
v_mul_f32 v254, v254, v250
AW a94,v254
AR v251,a95
v_sub_f32 v251, v251, v249
v_mul_f32 v251, v251, v250
AW a95,v251
AR v254,a120
v_sub_f32 v254, v254, v249
v_mul_f32 v254, v254, v250
AW a120,v254
AR v251,a121
v_sub_f32 v251, v251, v249
v_mul_f32 v251, v251, v250
AW a121,v251
AR v254,a122
v_sub_f32 v254, v254, v249
v_mul_f32 v254, v254, v250
AW a122,v254
AR v251,a123
v_sub_f32 v251, v251, v249
v_mul_f32 v251, v251, v250
AW a123,v251
AR v254,a124
v_sub_f32 v254, v254, v249
v_mul_f32 v254, v254, v250
AW a124,v254
AR v251,a125
v_sub_f32 v251, v251, v249
v_mul_f32 v251, v251, v250
AW a125,v251
AR v254,a126
v_sub_f32 v254, v254, v249
v_mul_f32 v254, v254, v250
AW a126,v254
AR v251,a127
v_sub_f32 v251, v251, v249
v_mul_f32 v251, v251, v250
AW a127,v251
EX v0,v0
EX v1,v1
EX v2,v2
EX v3,v3
EX v4,v4
EX v5,v5
EX v6,v6
EX v7,v7
EX v8,v8
EX v9,v9
EX v10,v10
EX v11,v11
EX v12,v12
EX v13,v13
EX v14,v14
EX v15,v15
EX v16,v16
EX v17,v17
EX v18,v18
EX v19,v19
EX v20,v20
EX v21,v21
EX v22,v22
EX v23,v23
EX v24,v24
EX v25,v25
EX v26,v26
EX v27,v27
EX v28,v28
EX v29,v29
EX v30,v30
EX v31,v31
EX v32,v32
EX v33,v33
EX v34,v34
EX v35,v35
EX v36,v36
EX v37,v37
EX v38,v38
EX v39,v39
EX v40,v40
EX v41,v41
EX v42,v42
EX v43,v43
EX v44,v44
EX v45,v45
EX v46,v46
EX v47,v47
EX v48,v48
EX v49,v49
EX v50,v50
EX v51,v51
EX v52,v52
EX v53,v53
EX v54,v54
EX v55,v55
EX v56,v56
EX v57,v57
EX v58,v58
EX v59,v59
EX v60,v60
EX v61,v61
EX v62,v62
EX v63,v63
CV v0,v0,v1
CV v1,v2,v3
CV v2,v4,v5
CV v3,v6,v7
CV v8,v8,v9
CV v9,v10,v11
CV v10,v12,v13
CV v11,v14,v15
CV v16,v16,v17
CV v17,v18,v19
CV v18,v20,v21
CV v19,v22,v23
CV v24,v24,v25
CV v25,v26,v27
CV v26,v28,v29
CV v27,v30,v31
CV v4,v32,v33
CV v5,v34,v35
CV v6,v36,v37
CV v7,v38,v39
CV v12,v40,v41
CV v13,v42,v43
CV v14,v44,v45
CV v15,v46,v47
CV v20,v48,v49
CV v21,v50,v51
CV v22,v52,v53
CV v23,v54,v55
CV v28,v56,v57
CV v29,v58,v59
CV v30,v60,v61
CV v31,v62,v63
v_smfmac_f32_16x16x64_bf16 v[144:147], v[236:239], v[0:7], v234
v_smfmac_f32_16x16x64_bf16 v[148:151], v[236:239], v[8:15], v234
v_smfmac_f32_16x16x64_bf16 v[152:155], v[236:239], v[16:23], v234
v_smfmac_f32_16x16x64_bf16 v[156:159], v[236:239], v[24:31], v234
s_nop 9
s_branch LRET_%=
LRARE022_%=:
s_nop 15
s_waitcnt vmcnt(0) lgkmcnt(0)
v_add_u32 v249, s74, v224
RD a[192:195],v249 offset:0
RD a[208:211],v249 offset:4096
RD a[224:227],v249 offset:8192
RD a[240:243],v249 offset:12288
v_add_u32 v250, s74, v225
RD a[196:199],v250 offset:0
RD a[212:215],v250 offset:4096
RD a[228:231],v250 offset:8192
RD a[244:247],v250 offset:12288
v_add_u32 v251, s74, v226
RD a[200:203],v251 offset:0
RD a[216:219],v251 offset:4096
RD a[232:235],v251 offset:8192
RD a[248:251],v251 offset:12288
v_add_u32 v254, s74, v227
RD a[204:207],v254 offset:0
RD a[220:223],v254 offset:4096
RD a[236:239],v254 offset:8192
RD a[252:255],v254 offset:12288
s_waitcnt lgkmcnt(0)
MM v[16:19],a[192:195],v[168:171],v[136:139]
MM v[16:19],a[196:199],v[184:187],v[16:19]
MM v[16:19],a[200:203],v[200:203],v[16:19]
MM v[16:19],a[204:207],v[216:219],v[16:19]
MM v[20:23],a[220:223],v[216:219],v[136:139]
MM v[20:23],a[216:219],v[200:203],v[20:23]
MM v[20:23],a[212:215],v[184:187],v[20:23]
MM v[20:23],a[208:211],v[168:171],v[20:23]
MM v[28:31],a[208:211],v[172:175],v[140:143]
MM v[28:31],a[212:215],v[188:191],v[28:31]
MM v[28:31],a[216:219],v[204:207],v[28:31]
MM v[28:31],a[220:223],v[220:223],v[28:31]
MM v[24:27],a[204:207],v[220:223],v[140:143]
MM v[24:27],a[200:203],v[204:207],v[24:27]
MM v[24:27],a[196:199],v[188:191],v[24:27]
MM v[24:27],a[192:195],v[172:175],v[24:27]
MM v[48:51],a[224:227],v[168:171],v[136:139]
MM v[48:51],a[228:231],v[184:187],v[48:51]
MM v[48:51],a[232:235],v[200:203],v[48:51]
MM v[48:51],a[236:239],v[216:219],v[48:51]
MM v[52:55],a[252:255],v[216:219],v[136:139]
MM v[52:55],a[248:251],v[200:203],v[52:55]
MM v[52:55],a[244:247],v[184:187],v[52:55]
MM v[52:55],a[240:243],v[168:171],v[52:55]
MM v[60:63],a[240:243],v[172:175],v[140:143]
MM v[60:63],a[244:247],v[188:191],v[60:63]
MM v[60:63],a[248:251],v[204:207],v[60:63]
MM v[60:63],a[252:255],v[220:223],v[60:63]
MM v[56:59],a[236:239],v[220:223],v[140:143]
MM v[56:59],a[232:235],v[204:207],v[56:59]
MM v[56:59],a[228:231],v[188:191],v[56:59]
MM v[56:59],a[224:227],v[172:175],v[56:59]
s_nop 15
v_add_u32 v249, s75, v224
RD a[192:195],v249 offset:0
RD a[208:211],v249 offset:4096
RD a[224:227],v249 offset:8192
RD a[240:243],v249 offset:12288
v_add_u32 v250, s75, v225
RD a[196:199],v250 offset:0
RD a[212:215],v250 offset:4096
RD a[228:231],v250 offset:8192
RD a[244:247],v250 offset:12288
v_add_u32 v251, s75, v226
RD a[200:203],v251 offset:0
RD a[216:219],v251 offset:4096
RD a[232:235],v251 offset:8192
RD a[248:251],v251 offset:12288
v_add_u32 v254, s75, v227
RD a[204:207],v254 offset:0
RD a[220:223],v254 offset:4096
RD a[236:239],v254 offset:8192
RD a[252:255],v254 offset:12288
s_waitcnt lgkmcnt(0)
v_max3_f32 v249, v16, v17, v18
v_max3_f32 v251, v48, v49, v50
v_max3_f32 v249, v249, v19, v20
v_max3_f32 v251, v251, v51, v52
v_max3_f32 v249, v249, v21, v22
v_max3_f32 v251, v251, v53, v54
v_max3_f32 v249, v249, v23, v55
v_max_f32 v249, v249, v251
v_mov_b32 v251, v249
s_nop 1
v_permlane16_swap_b32 v249, v251
v_max_f32 v249, v249, v251
v_mov_b32 v251, v249
s_nop 1
v_permlane32_swap_b32 v249, v251
v_max_f32 v249, v249, v251
v_log_f32 v250, v246
s_nop 0
v_max3_f32 v249, 0, v249, v250
v_sub_f32 v250, 0, v249
EX v250,v250
v_sub_f32 v136, v136, v249
v_sub_f32 v137, v137, v249
v_sub_f32 v138, v138, v249
v_sub_f32 v139, v139, v249
v_sub_f32 v80, v80, v249
v_sub_f32 v16, v16, v249
v_sub_f32 v81, v81, v249
v_sub_f32 v17, v17, v249
v_sub_f32 v82, v82, v249
v_sub_f32 v18, v18, v249
v_sub_f32 v83, v83, v249
v_sub_f32 v19, v19, v249
v_sub_f32 v84, v84, v249
v_sub_f32 v20, v20, v249
v_sub_f32 v85, v85, v249
v_sub_f32 v21, v21, v249
v_sub_f32 v86, v86, v249
v_sub_f32 v22, v22, v249
v_sub_f32 v87, v87, v249
v_sub_f32 v23, v23, v249
v_sub_f32 v112, v112, v249
v_sub_f32 v48, v48, v249
v_sub_f32 v113, v113, v249
v_sub_f32 v49, v49, v249
v_sub_f32 v114, v114, v249
)ASM"
R"ASM(
v_sub_f32 v50, v50, v249
v_sub_f32 v115, v115, v249
v_sub_f32 v51, v51, v249
v_sub_f32 v116, v116, v249
v_sub_f32 v52, v52, v249
v_sub_f32 v117, v117, v249
v_sub_f32 v53, v53, v249
v_sub_f32 v118, v118, v249
v_sub_f32 v54, v54, v249
v_sub_f32 v119, v119, v249
v_sub_f32 v55, v55, v249
v_mul_f32 v152, v246, v250
v_mov_b32 v153, 0
v_bfe_u32 v249, v241, 16, 8
v_lshl_or_b32 v249, v249, 1, 1
v_lshlrev_b32 v249, 22, v249
v_and_b32 v241, 0xff00ffff, v241
AR v254,a16
v_sub_f32 v254, v254, v249
v_mul_f32 v254, v254, v250
AW a16,v254
AR v251,a17
v_sub_f32 v251, v251, v249
v_mul_f32 v251, v251, v250
AW a17,v251
AR v254,a18
v_sub_f32 v254, v254, v249
v_mul_f32 v254, v254, v250
AW a18,v254
AR v251,a19
v_sub_f32 v251, v251, v249
v_mul_f32 v251, v251, v250
AW a19,v251
AR v254,a20
v_sub_f32 v254, v254, v249
v_mul_f32 v254, v254, v250
AW a20,v254
AR v251,a21
v_sub_f32 v251, v251, v249
v_mul_f32 v251, v251, v250
AW a21,v251
AR v254,a22
v_sub_f32 v254, v254, v249
v_mul_f32 v254, v254, v250
AW a22,v254
AR v251,a23
v_sub_f32 v251, v251, v249
v_mul_f32 v251, v251, v250
AW a23,v251
AR v254,a48
v_sub_f32 v254, v254, v249
v_mul_f32 v254, v254, v250
AW a48,v254
AR v251,a49
v_sub_f32 v251, v251, v249
v_mul_f32 v251, v251, v250
AW a49,v251
AR v254,a50
v_sub_f32 v254, v254, v249
v_mul_f32 v254, v254, v250
AW a50,v254
AR v251,a51
v_sub_f32 v251, v251, v249
v_mul_f32 v251, v251, v250
AW a51,v251
AR v254,a52
v_sub_f32 v254, v254, v249
v_mul_f32 v254, v254, v250
AW a52,v254
AR v251,a53
v_sub_f32 v251, v251, v249
v_mul_f32 v251, v251, v250
AW a53,v251
AR v254,a54
v_sub_f32 v254, v254, v249
v_mul_f32 v254, v254, v250
AW a54,v254
AR v251,a55
v_sub_f32 v251, v251, v249
v_mul_f32 v251, v251, v250
AW a55,v251
AR v254,a80
v_sub_f32 v254, v254, v249
v_mul_f32 v254, v254, v250
AW a80,v254
AR v251,a81
v_sub_f32 v251, v251, v249
v_mul_f32 v251, v251, v250
AW a81,v251
AR v254,a82
v_sub_f32 v254, v254, v249
v_mul_f32 v254, v254, v250
AW a82,v254
AR v251,a83
v_sub_f32 v251, v251, v249
v_mul_f32 v251, v251, v250
AW a83,v251
AR v254,a84
v_sub_f32 v254, v254, v249
v_mul_f32 v254, v254, v250
AW a84,v254
AR v251,a85
v_sub_f32 v251, v251, v249
v_mul_f32 v251, v251, v250
AW a85,v251
AR v254,a86
v_sub_f32 v254, v254, v249
v_mul_f32 v254, v254, v250
AW a86,v254
AR v251,a87
v_sub_f32 v251, v251, v249
v_mul_f32 v251, v251, v250
AW a87,v251
AR v254,a112
v_sub_f32 v254, v254, v249
v_mul_f32 v254, v254, v250
AW a112,v254
AR v251,a113
v_sub_f32 v251, v251, v249
v_mul_f32 v251, v251, v250
AW a113,v251
AR v254,a114
v_sub_f32 v254, v254, v249
v_mul_f32 v254, v254, v250
AW a114,v254
AR v251,a115
v_sub_f32 v251, v251, v249
v_mul_f32 v251, v251, v250
AW a115,v251
AR v254,a116
v_sub_f32 v254, v254, v249
v_mul_f32 v254, v254, v250
AW a116,v254
AR v251,a117
v_sub_f32 v251, v251, v249
v_mul_f32 v251, v251, v250
AW a117,v251
AR v254,a118
v_sub_f32 v254, v254, v249
v_mul_f32 v254, v254, v250
AW a118,v254
AR v251,a119
v_sub_f32 v251, v251, v249
v_mul_f32 v251, v251, v250
AW a119,v251
v_max3_f32 v249, v24, v25, v26
v_max3_f32 v251, v56, v57, v58
v_max3_f32 v249, v249, v27, v28
v_max3_f32 v251, v251, v59, v60
v_max3_f32 v249, v249, v29, v30
v_max3_f32 v251, v251, v61, v62
v_max3_f32 v249, v249, v31, v63
v_max_f32 v249, v249, v251
v_mov_b32 v251, v249
s_nop 1
v_permlane16_swap_b32 v249, v251
v_max_f32 v249, v249, v251
v_mov_b32 v251, v249
s_nop 1
v_permlane32_swap_b32 v249, v251
v_max_f32 v249, v249, v251
v_log_f32 v250, v247
s_nop 0
v_max3_f32 v249, 0, v249, v250
v_sub_f32 v250, 0, v249
EX v250,v250
v_sub_f32 v140, v140, v249
v_sub_f32 v141, v141, v249
v_sub_f32 v142, v142, v249
v_sub_f32 v143, v143, v249
v_sub_f32 v88, v88, v249
v_sub_f32 v24, v24, v249
v_sub_f32 v89, v89, v249
v_sub_f32 v25, v25, v249
v_sub_f32 v90, v90, v249
v_sub_f32 v26, v26, v249
v_sub_f32 v91, v91, v249
v_sub_f32 v27, v27, v249
v_sub_f32 v92, v92, v249
v_sub_f32 v28, v28, v249
v_sub_f32 v93, v93, v249
v_sub_f32 v29, v29, v249
v_sub_f32 v94, v94, v249
v_sub_f32 v30, v30, v249
v_sub_f32 v95, v95, v249
v_sub_f32 v31, v31, v249
v_sub_f32 v120, v120, v249
v_sub_f32 v56, v56, v249
v_sub_f32 v121, v121, v249
v_sub_f32 v57, v57, v249
v_sub_f32 v122, v122, v249
v_sub_f32 v58, v58, v249
v_sub_f32 v123, v123, v249
v_sub_f32 v59, v59, v249
v_sub_f32 v124, v124, v249
v_sub_f32 v60, v60, v249
v_sub_f32 v125, v125, v249
v_sub_f32 v61, v61, v249
v_sub_f32 v126, v126, v249
v_sub_f32 v62, v62, v249
v_sub_f32 v127, v127, v249
v_sub_f32 v63, v63, v249
v_mul_f32 v156, v247, v250
v_mov_b32 v157, 0
v_bfe_u32 v249, v241, 24, 8
v_lshl_or_b32 v249, v249, 1, 1
v_lshlrev_b32 v249, 22, v249
v_and_b32 v241, 0xffffff, v241
AR v254,a24
v_sub_f32 v254, v254, v249
v_mul_f32 v254, v254, v250
AW a24,v254
AR v251,a25
v_sub_f32 v251, v251, v249
v_mul_f32 v251, v251, v250
AW a25,v251
AR v254,a26
v_sub_f32 v254, v254, v249
v_mul_f32 v254, v254, v250
AW a26,v254
AR v251,a27
v_sub_f32 v251, v251, v249
v_mul_f32 v251, v251, v250
AW a27,v251
AR v254,a28
v_sub_f32 v254, v254, v249
v_mul_f32 v254, v254, v250
AW a28,v254
AR v251,a29
v_sub_f32 v251, v251, v249
v_mul_f32 v251, v251, v250
AW a29,v251
AR v254,a30
v_sub_f32 v254, v254, v249
v_mul_f32 v254, v254, v250
AW a30,v254
AR v251,a31
v_sub_f32 v251, v251, v249
v_mul_f32 v251, v251, v250
AW a31,v251
AR v254,a56
v_sub_f32 v254, v254, v249
v_mul_f32 v254, v254, v250
AW a56,v254
AR v251,a57
v_sub_f32 v251, v251, v249
v_mul_f32 v251, v251, v250
AW a57,v251
AR v254,a58
v_sub_f32 v254, v254, v249
v_mul_f32 v254, v254, v250
AW a58,v254
AR v251,a59
v_sub_f32 v251, v251, v249
v_mul_f32 v251, v251, v250
AW a59,v251
AR v254,a60
v_sub_f32 v254, v254, v249
v_mul_f32 v254, v254, v250
AW a60,v254
AR v251,a61
v_sub_f32 v251, v251, v249
v_mul_f32 v251, v251, v250
AW a61,v251
AR v254,a62
v_sub_f32 v254, v254, v249
v_mul_f32 v254, v254, v250
AW a62,v254
AR v251,a63
v_sub_f32 v251, v251, v249
v_mul_f32 v251, v251, v250
AW a63,v251
AR v254,a88
v_sub_f32 v254, v254, v249
v_mul_f32 v254, v254, v250
AW a88,v254
AR v251,a89
v_sub_f32 v251, v251, v249
v_mul_f32 v251, v251, v250
AW a89,v251
AR v254,a90
v_sub_f32 v254, v254, v249
v_mul_f32 v254, v254, v250
AW a90,v254
AR v251,a91
v_sub_f32 v251, v251, v249
v_mul_f32 v251, v251, v250
AW a91,v251
AR v254,a92
v_sub_f32 v254, v254, v249
v_mul_f32 v254, v254, v250
AW a92,v254
AR v251,a93
v_sub_f32 v251, v251, v249
v_mul_f32 v251, v251, v250
AW a93,v251
AR v254,a94
v_sub_f32 v254, v254, v249
v_mul_f32 v254, v254, v250
AW a94,v254
AR v251,a95
v_sub_f32 v251, v251, v249
v_mul_f32 v251, v251, v250
AW a95,v251
AR v254,a120
v_sub_f32 v254, v254, v249
v_mul_f32 v254, v254, v250
AW a120,v254
AR v251,a121
v_sub_f32 v251, v251, v249
v_mul_f32 v251, v251, v250
AW a121,v251
AR v254,a122
v_sub_f32 v254, v254, v249
v_mul_f32 v254, v254, v250
AW a122,v254
AR v251,a123
v_sub_f32 v251, v251, v249
v_mul_f32 v251, v251, v250
AW a123,v251
AR v254,a124
v_sub_f32 v254, v254, v249
v_mul_f32 v254, v254, v250
AW a124,v254
AR v251,a125
v_sub_f32 v251, v251, v249
v_mul_f32 v251, v251, v250
AW a125,v251
AR v254,a126
v_sub_f32 v254, v254, v249
v_mul_f32 v254, v254, v250
AW a126,v254
AR v251,a127
v_sub_f32 v251, v251, v249
v_mul_f32 v251, v251, v250
AW a127,v251
EX v16,v16
EX v17,v17
EX v18,v18
EX v19,v19
EX v20,v20
EX v21,v21
EX v22,v22
EX v23,v23
EX v24,v24
EX v25,v25
EX v26,v26
EX v27,v27
EX v28,v28
EX v29,v29
EX v30,v30
EX v31,v31
EX v48,v48
EX v49,v49
EX v50,v50
EX v51,v51
EX v52,v52
EX v53,v53
EX v54,v54
EX v55,v55
EX v56,v56
EX v57,v57
EX v58,v58
EX v59,v59
EX v60,v60
EX v61,v61
EX v62,v62
EX v63,v63
CV v16,v16,v17
CV v17,v18,v19
CV v18,v20,v21
CV v19,v22,v23
CV v24,v24,v25
CV v25,v26,v27
CV v26,v28,v29
CV v27,v30,v31
CV v20,v48,v49
CV v21,v50,v51
CV v22,v52,v53
CV v23,v54,v55
CV v28,v56,v57
CV v29,v58,v59
CV v30,v60,v61
CV v31,v62,v63
v_smfmac_f32_16x16x64_bf16 v[152:155], v[236:239], v[16:23], v234
s_nop 0
v_smfmac_f32_16x16x64_bf16 v[156:159], v[236:239], v[24:31], v234
s_nop 9
s_branch LRET_%=
LRARE122_%=:
s_nop 15
s_waitcnt vmcnt(0) lgkmcnt(0)
v_add_u32 v249, s74, v224
RD a[192:195],v249 offset:0
RD a[208:211],v249 offset:4096
RD a[224:227],v249 offset:8192
RD a[240:243],v249 offset:12288
v_add_u32 v250, s74, v225
RD a[196:199],v250 offset:0
RD a[212:215],v250 offset:4096
RD a[228:231],v250 offset:8192
RD a[244:247],v250 offset:12288
v_add_u32 v251, s74, v226
RD a[200:203],v251 offset:0
RD a[216:219],v251 offset:4096
RD a[232:235],v251 offset:8192
RD a[248:251],v251 offset:12288
v_add_u32 v254, s74, v227
RD a[204:207],v254 offset:0
RD a[220:223],v254 offset:4096
RD a[236:239],v254 offset:8192
RD a[252:255],v254 offset:12288
s_waitcnt lgkmcnt(0)
MM v[80:83],a[192:195],v[168:171],v[136:139]
MM v[80:83],a[196:199],v[184:187],v[80:83]
MM v[80:83],a[200:203],v[200:203],v[80:83]
MM v[80:83],a[204:207],v[216:219],v[80:83]
MM v[84:87],a[220:223],v[216:219],v[136:139]
MM v[84:87],a[216:219],v[200:203],v[84:87]
MM v[84:87],a[212:215],v[184:187],v[84:87]
MM v[84:87],a[208:211],v[168:171],v[84:87]
MM v[92:95],a[208:211],v[172:175],v[140:143]
MM v[92:95],a[212:215],v[188:191],v[92:95]
MM v[92:95],a[216:219],v[204:207],v[92:95]
MM v[92:95],a[220:223],v[220:223],v[92:95]
MM v[88:91],a[204:207],v[220:223],v[140:143]
MM v[88:91],a[200:203],v[204:207],v[88:91]
MM v[88:91],a[196:199],v[188:191],v[88:91]
MM v[88:91],a[192:195],v[172:175],v[88:91]
MM v[112:115],a[224:227],v[168:171],v[136:139]
MM v[112:115],a[228:231],v[184:187],v[112:115]
MM v[112:115],a[232:235],v[200:203],v[112:115]
MM v[112:115],a[236:239],v[216:219],v[112:115]
MM v[116:119],a[252:255],v[216:219],v[136:139]
MM v[116:119],a[248:251],v[200:203],v[116:119]
MM v[116:119],a[244:247],v[184:187],v[116:119]
MM v[116:119],a[240:243],v[168:171],v[116:119]
MM v[124:127],a[240:243],v[172:175],v[140:143]
MM v[124:127],a[244:247],v[188:191],v[124:127]
MM v[124:127],a[248:251],v[204:207],v[124:127]
MM v[124:127],a[252:255],v[220:223],v[124:127]
MM v[120:123],a[236:239],v[220:223],v[140:143]
MM v[120:123],a[232:235],v[204:207],v[120:123]
MM v[120:123],a[228:231],v[188:191],v[120:123]
MM v[120:123],a[224:227],v[172:175],v[120:123]
s_nop 15
v_add_u32 v249, s75, v224
RD a[192:195],v249 offset:0
RD a[208:211],v249 offset:4096
RD a[224:227],v249 offset:8192
RD a[240:243],v249 offset:12288
v_add_u32 v250, s75, v225
RD a[196:199],v250 offset:0
RD a[212:215],v250 offset:4096
RD a[228:231],v250 offset:8192
RD a[244:247],v250 offset:12288
v_add_u32 v251, s75, v226
RD a[200:203],v251 offset:0
RD a[216:219],v251 offset:4096
RD a[232:235],v251 offset:8192
RD a[248:251],v251 offset:12288
v_add_u32 v254, s75, v227
RD a[204:207],v254 offset:0
RD a[220:223],v254 offset:4096
RD a[236:239],v254 offset:8192
RD a[252:255],v254 offset:12288
s_waitcnt lgkmcnt(0)
v_max3_f32 v249, v80, v81, v82
v_max3_f32 v251, v112, v113, v114
v_max3_f32 v249, v249, v83, v84
v_max3_f32 v251, v251, v115, v116
v_max3_f32 v249, v249, v85, v86
v_max3_f32 v251, v251, v117, v118
v_max3_f32 v249, v249, v87, v119
v_max_f32 v249, v249, v251
v_mov_b32 v251, v249
s_nop 1
v_permlane16_swap_b32 v249, v251
v_max_f32 v249, v249, v251
v_mov_b32 v251, v249
s_nop 1
v_permlane32_swap_b32 v249, v251
v_max_f32 v249, v249, v251
v_log_f32 v250, v246
s_nop 0
v_max3_f32 v249, 0, v249, v250
v_sub_f32 v250, 0, v249
EX v250,v250
v_sub_f32 v136, v136, v249
v_sub_f32 v137, v137, v249
v_sub_f32 v138, v138, v249
v_sub_f32 v139, v139, v249
v_sub_f32 v16, v16, v249
v_sub_f32 v80, v80, v249
v_sub_f32 v17, v17, v249
v_sub_f32 v81, v81, v249
v_sub_f32 v18, v18, v249
v_sub_f32 v82, v82, v249
v_sub_f32 v19, v19, v249
v_sub_f32 v83, v83, v249
v_sub_f32 v20, v20, v249
v_sub_f32 v84, v84, v249
v_sub_f32 v21, v21, v249
v_sub_f32 v85, v85, v249
v_sub_f32 v22, v22, v249
v_sub_f32 v86, v86, v249
v_sub_f32 v23, v23, v249
v_sub_f32 v87, v87, v249
v_sub_f32 v48, v48, v249
v_sub_f32 v112, v112, v249
v_sub_f32 v49, v49, v249
v_sub_f32 v113, v113, v249
v_sub_f32 v50, v50, v249
v_sub_f32 v114, v114, v249
v_sub_f32 v51, v51, v249
v_sub_f32 v115, v115, v249
v_sub_f32 v52, v52, v249
v_sub_f32 v116, v116, v249
v_sub_f32 v53, v53, v249
v_sub_f32 v117, v117, v249
v_sub_f32 v54, v54, v249
v_sub_f32 v118, v118, v249
v_sub_f32 v55, v55, v249
)ASM"
R"ASM(
v_sub_f32 v119, v119, v249
v_mul_f32 v152, v246, v250
v_mov_b32 v153, 0
v_bfe_u32 v249, v241, 16, 8
v_lshl_or_b32 v249, v249, 1, 1
v_lshlrev_b32 v249, 22, v249
v_and_b32 v241, 0xff00ffff, v241
AR v254,a16
v_sub_f32 v254, v254, v249
v_mul_f32 v254, v254, v250
AW a16,v254
AR v251,a17
v_sub_f32 v251, v251, v249
v_mul_f32 v251, v251, v250
AW a17,v251
AR v254,a18
v_sub_f32 v254, v254, v249
v_mul_f32 v254, v254, v250
AW a18,v254
AR v251,a19
v_sub_f32 v251, v251, v249
v_mul_f32 v251, v251, v250
AW a19,v251
AR v254,a20
v_sub_f32 v254, v254, v249
v_mul_f32 v254, v254, v250
AW a20,v254
AR v251,a21
v_sub_f32 v251, v251, v249
v_mul_f32 v251, v251, v250
AW a21,v251
AR v254,a22
v_sub_f32 v254, v254, v249
v_mul_f32 v254, v254, v250
AW a22,v254
AR v251,a23
v_sub_f32 v251, v251, v249
v_mul_f32 v251, v251, v250
AW a23,v251
AR v254,a48
v_sub_f32 v254, v254, v249
v_mul_f32 v254, v254, v250
AW a48,v254
AR v251,a49
v_sub_f32 v251, v251, v249
v_mul_f32 v251, v251, v250
AW a49,v251
AR v254,a50
v_sub_f32 v254, v254, v249
v_mul_f32 v254, v254, v250
AW a50,v254
AR v251,a51
v_sub_f32 v251, v251, v249
v_mul_f32 v251, v251, v250
AW a51,v251
AR v254,a52
v_sub_f32 v254, v254, v249
v_mul_f32 v254, v254, v250
AW a52,v254
AR v251,a53
v_sub_f32 v251, v251, v249
v_mul_f32 v251, v251, v250
AW a53,v251
AR v254,a54
v_sub_f32 v254, v254, v249
v_mul_f32 v254, v254, v250
AW a54,v254
AR v251,a55
v_sub_f32 v251, v251, v249
v_mul_f32 v251, v251, v250
AW a55,v251
AR v254,a80
v_sub_f32 v254, v254, v249
v_mul_f32 v254, v254, v250
AW a80,v254
AR v251,a81
v_sub_f32 v251, v251, v249
v_mul_f32 v251, v251, v250
AW a81,v251
AR v254,a82
v_sub_f32 v254, v254, v249
v_mul_f32 v254, v254, v250
AW a82,v254
AR v251,a83
v_sub_f32 v251, v251, v249
v_mul_f32 v251, v251, v250
AW a83,v251
AR v254,a84
v_sub_f32 v254, v254, v249
v_mul_f32 v254, v254, v250
AW a84,v254
AR v251,a85
v_sub_f32 v251, v251, v249
v_mul_f32 v251, v251, v250
AW a85,v251
AR v254,a86
v_sub_f32 v254, v254, v249
v_mul_f32 v254, v254, v250
AW a86,v254
AR v251,a87
v_sub_f32 v251, v251, v249
v_mul_f32 v251, v251, v250
AW a87,v251
AR v254,a112
v_sub_f32 v254, v254, v249
v_mul_f32 v254, v254, v250
AW a112,v254
AR v251,a113
v_sub_f32 v251, v251, v249
v_mul_f32 v251, v251, v250
AW a113,v251
AR v254,a114
v_sub_f32 v254, v254, v249
v_mul_f32 v254, v254, v250
AW a114,v254
AR v251,a115
v_sub_f32 v251, v251, v249
v_mul_f32 v251, v251, v250
AW a115,v251
AR v254,a116
v_sub_f32 v254, v254, v249
v_mul_f32 v254, v254, v250
AW a116,v254
AR v251,a117
v_sub_f32 v251, v251, v249
v_mul_f32 v251, v251, v250
AW a117,v251
AR v254,a118
v_sub_f32 v254, v254, v249
v_mul_f32 v254, v254, v250
AW a118,v254
AR v251,a119
v_sub_f32 v251, v251, v249
v_mul_f32 v251, v251, v250
AW a119,v251
v_max3_f32 v249, v88, v89, v90
v_max3_f32 v251, v120, v121, v122
v_max3_f32 v249, v249, v91, v92
v_max3_f32 v251, v251, v123, v124
v_max3_f32 v249, v249, v93, v94
v_max3_f32 v251, v251, v125, v126
v_max3_f32 v249, v249, v95, v127
v_max_f32 v249, v249, v251
v_mov_b32 v251, v249
s_nop 1
v_permlane16_swap_b32 v249, v251
v_max_f32 v249, v249, v251
v_mov_b32 v251, v249
s_nop 1
v_permlane32_swap_b32 v249, v251
v_max_f32 v249, v249, v251
v_log_f32 v250, v247
s_nop 0
v_max3_f32 v249, 0, v249, v250
v_sub_f32 v250, 0, v249
EX v250,v250
v_sub_f32 v140, v140, v249
v_sub_f32 v141, v141, v249
v_sub_f32 v142, v142, v249
v_sub_f32 v143, v143, v249
v_sub_f32 v24, v24, v249
v_sub_f32 v88, v88, v249
v_sub_f32 v25, v25, v249
v_sub_f32 v89, v89, v249
v_sub_f32 v26, v26, v249
v_sub_f32 v90, v90, v249
v_sub_f32 v27, v27, v249
v_sub_f32 v91, v91, v249
v_sub_f32 v28, v28, v249
v_sub_f32 v92, v92, v249
v_sub_f32 v29, v29, v249
v_sub_f32 v93, v93, v249
v_sub_f32 v30, v30, v249
v_sub_f32 v94, v94, v249
v_sub_f32 v31, v31, v249
v_sub_f32 v95, v95, v249
v_sub_f32 v56, v56, v249
v_sub_f32 v120, v120, v249
v_sub_f32 v57, v57, v249
v_sub_f32 v121, v121, v249
v_sub_f32 v58, v58, v249
v_sub_f32 v122, v122, v249
v_sub_f32 v59, v59, v249
v_sub_f32 v123, v123, v249
v_sub_f32 v60, v60, v249
v_sub_f32 v124, v124, v249
v_sub_f32 v61, v61, v249
v_sub_f32 v125, v125, v249
v_sub_f32 v62, v62, v249
v_sub_f32 v126, v126, v249
v_sub_f32 v63, v63, v249
v_sub_f32 v127, v127, v249
v_mul_f32 v156, v247, v250
v_mov_b32 v157, 0
v_bfe_u32 v249, v241, 24, 8
v_lshl_or_b32 v249, v249, 1, 1
v_lshlrev_b32 v249, 22, v249
v_and_b32 v241, 0xffffff, v241
AR v254,a24
v_sub_f32 v254, v254, v249
v_mul_f32 v254, v254, v250
AW a24,v254
AR v251,a25
v_sub_f32 v251, v251, v249
v_mul_f32 v251, v251, v250
AW a25,v251
AR v254,a26
v_sub_f32 v254, v254, v249
v_mul_f32 v254, v254, v250
AW a26,v254
AR v251,a27
v_sub_f32 v251, v251, v249
v_mul_f32 v251, v251, v250
AW a27,v251
AR v254,a28
v_sub_f32 v254, v254, v249
v_mul_f32 v254, v254, v250
AW a28,v254
AR v251,a29
v_sub_f32 v251, v251, v249
v_mul_f32 v251, v251, v250
AW a29,v251
AR v254,a30
v_sub_f32 v254, v254, v249
v_mul_f32 v254, v254, v250
AW a30,v254
AR v251,a31
v_sub_f32 v251, v251, v249
v_mul_f32 v251, v251, v250
AW a31,v251
AR v254,a56
v_sub_f32 v254, v254, v249
v_mul_f32 v254, v254, v250
AW a56,v254
AR v251,a57
v_sub_f32 v251, v251, v249
v_mul_f32 v251, v251, v250
AW a57,v251
AR v254,a58
v_sub_f32 v254, v254, v249
v_mul_f32 v254, v254, v250
AW a58,v254
AR v251,a59
v_sub_f32 v251, v251, v249
v_mul_f32 v251, v251, v250
AW a59,v251
AR v254,a60
v_sub_f32 v254, v254, v249
v_mul_f32 v254, v254, v250
AW a60,v254
AR v251,a61
v_sub_f32 v251, v251, v249
v_mul_f32 v251, v251, v250
AW a61,v251
AR v254,a62
v_sub_f32 v254, v254, v249
v_mul_f32 v254, v254, v250
AW a62,v254
AR v251,a63
v_sub_f32 v251, v251, v249
v_mul_f32 v251, v251, v250
AW a63,v251
AR v254,a88
v_sub_f32 v254, v254, v249
v_mul_f32 v254, v254, v250
AW a88,v254
AR v251,a89
v_sub_f32 v251, v251, v249
v_mul_f32 v251, v251, v250
AW a89,v251
AR v254,a90
v_sub_f32 v254, v254, v249
v_mul_f32 v254, v254, v250
AW a90,v254
AR v251,a91
v_sub_f32 v251, v251, v249
v_mul_f32 v251, v251, v250
AW a91,v251
AR v254,a92
v_sub_f32 v254, v254, v249
v_mul_f32 v254, v254, v250
AW a92,v254
AR v251,a93
v_sub_f32 v251, v251, v249
v_mul_f32 v251, v251, v250
AW a93,v251
AR v254,a94
v_sub_f32 v254, v254, v249
v_mul_f32 v254, v254, v250
AW a94,v254
AR v251,a95
v_sub_f32 v251, v251, v249
v_mul_f32 v251, v251, v250
AW a95,v251
AR v254,a120
v_sub_f32 v254, v254, v249
v_mul_f32 v254, v254, v250
AW a120,v254
AR v251,a121
v_sub_f32 v251, v251, v249
v_mul_f32 v251, v251, v250
AW a121,v251
AR v254,a122
v_sub_f32 v254, v254, v249
v_mul_f32 v254, v254, v250
AW a122,v254
AR v251,a123
v_sub_f32 v251, v251, v249
v_mul_f32 v251, v251, v250
AW a123,v251
AR v254,a124
v_sub_f32 v254, v254, v249
v_mul_f32 v254, v254, v250
AW a124,v254
AR v251,a125
v_sub_f32 v251, v251, v249
v_mul_f32 v251, v251, v250
AW a125,v251
AR v254,a126
v_sub_f32 v254, v254, v249
v_mul_f32 v254, v254, v250
AW a126,v254
AR v251,a127
v_sub_f32 v251, v251, v249
v_mul_f32 v251, v251, v250
AW a127,v251
EX v80,v80
EX v81,v81
EX v82,v82
EX v83,v83
EX v84,v84
EX v85,v85
EX v86,v86
EX v87,v87
EX v88,v88
EX v89,v89
EX v90,v90
EX v91,v91
EX v92,v92
EX v93,v93
EX v94,v94
EX v95,v95
EX v112,v112
EX v113,v113
EX v114,v114
EX v115,v115
EX v116,v116
EX v117,v117
EX v118,v118
EX v119,v119
EX v120,v120
EX v121,v121
EX v122,v122
EX v123,v123
EX v124,v124
EX v125,v125
EX v126,v126
EX v127,v127
CV v80,v80,v81
CV v81,v82,v83
CV v82,v84,v85
CV v83,v86,v87
CV v88,v88,v89
CV v89,v90,v91
CV v90,v92,v93
CV v91,v94,v95
CV v84,v112,v113
CV v85,v114,v115
CV v86,v116,v117
CV v87,v118,v119
CV v92,v120,v121
CV v93,v122,v123
CV v94,v124,v125
CV v95,v126,v127
v_smfmac_f32_16x16x64_bf16 v[152:155], v[236:239], v[80:87], v234
s_nop 0
v_smfmac_f32_16x16x64_bf16 v[156:159], v[236:239], v[88:95], v234
s_nop 9
s_branch LRET_%=
LEND_%=:
)ASM"

    :: [ka] "s"(__builtin_amdgcn_kernarg_segment_ptr()), [slds] "s"(slds), [swave] "s"(w), [sb] "s"(blockIdx.x)
    : "memory", "vcc", "scc", CLOB_V, CLOB_A, CLOB_S);
}
constexpr int DYN_LDS = 163840;

extern "C" void kernel_launch(void* const* d_in, const int* in_sizes, int n_in, void* d_out, int out_size, void* d_ws, size_t ws_size, hipStream_t stream) {
#if !defined(__HIP_DEVICE_COMPILE__)
  if (DYN_LDS) hipFuncSetAttribute((const void*)ka_fused, hipFuncAttributeMaxDynamicSharedMemorySize, DYN_LDS);
  ka_fused<<<256, 256, DYN_LDS, stream>>>(d_in[3], d_in[0], d_ws, d_in[1], d_in[6], d_in[2], d_in[4], d_in[7], d_in[8], d_in[9], d_out);
#endif
}
```

```cpp
#include <hip/hip_runtime.h>
#include <stdint.h>

using f32x4  = __attribute__((ext_vector_type(4))) float;
using u32x4  = __attribute__((ext_vector_type(4))) unsigned;
using u32x2  = __attribute__((ext_vector_type(2))) unsigned;

constexpr int SEQ = 2048, DM = 640, NH = 32, DH = 128, HD = NH * DH, FF = 1728, TOPC = 8;

constexpr unsigned WS_FO  = 0;
constexpr unsigned WS_EP  = 32768;
constexpr unsigned WS_G1  = 36864;
constexpr unsigned WS_G2  = 53248;
constexpr unsigned WS_FH  = 69632;
constexpr unsigned WS_FY  = 73728;
constexpr unsigned WS_FG  = 77824;
constexpr unsigned WS_FR  = 81920;
constexpr unsigned WS_RK  = 86016;
constexpr unsigned WS_XN  = 131072;
constexpr unsigned WS_OF  = WS_XN + SEQ * DM * 2;
constexpr unsigned WS_H   = WS_OF + (18u << 20);
constexpr unsigned WS_Y   = WS_H + 32768;
constexpr unsigned WS_P   = WS_Y + 1048576;
constexpr unsigned WS_SPAN = 64u << 20;

#define CLOB_V "v0","v1","v2","v3","v4","v5","v6","v7","v8","v9","v10","v11","v12","v13","v14","v15","v16","v17","v18","v19","v20","v21","v22","v23","v24","v25","v26","v27","v28","v29","v30","v31","v32","v33","v34","v35","v36","v37","v38","v39","v40","v41","v42","v43","v44","v45","v46","v47","v48","v49","v50","v51","v52","v53","v54","v55","v56","v57","v58","v59","v60","v61","v62","v63","v64","v65","v66","v67","v68","v69","v70","v71","v72","v73","v74","v75","v76","v77","v78","v79","v80","v81","v82","v83","v84","v85","v86","v87","v88","v89","v90","v91","v92","v93","v94","v95","v96","v97","v98","v99","v100","v101","v102","v103","v104","v105","v106","v107","v108","v109","v110","v111","v112","v113","v114","v115","v116","v117","v118","v119","v120","v121","v122","v123","v124","v125","v126","v127","v128","v129","v130","v131","v132","v133","v134","v135","v136","v137","v138","v139","v140","v141","v142","v143","v144","v145","v146","v147","v148","v149","v150","v151","v152","v153","v154","v155","v156","v157","v158","v159","v160","v161","v162","v163","v164","v165","v166","v167","v168","v169","v170","v171","v172","v173","v174","v175","v176","v177","v178","v179","v180","v181","v182","v183","v184","v185","v186","v187","v188","v189","v190","v191","v192","v193","v194","v195","v196","v197","v198","v199","v200","v201","v202","v203","v204","v205","v206","v207","v208","v209","v210","v211","v212","v213","v214","v215","v216","v217","v218","v219","v220","v221","v222","v223","v224","v225","v226","v227","v228","v229","v230","v231","v232","v233","v234","v235","v236","v237","v238","v239","v240","v241","v242","v243","v244","v245","v246","v247","v248","v249","v250","v251","v252","v253","v254","v255"
#define CLOB_A "a0","a1","a2","a3","a4","a5","a6","a7","a8","a9","a10","a11","a12","a13","a14","a15","a16","a17","a18","a19","a20","a21","a22","a23","a24","a25","a26","a27","a28","a29","a30","a31","a32","a33","a34","a35","a36","a37","a38","a39","a40","a41","a42","a43","a44","a45","a46","a47","a48","a49","a50","a51","a52","a53","a54","a55","a56","a57","a58","a59","a60","a61","a62","a63","a64","a65","a66","a67","a68","a69","a70","a71","a72","a73","a74","a75","a76","a77","a78","a79","a80","a81","a82","a83","a84","a85","a86","a87","a88","a89","a90","a91","a92","a93","a94","a95","a96","a97","a98","a99","a100","a101","a102","a103","a104","a105","a106","a107","a108","a109","a110","a111","a112","a113","a114","a115","a116","a117","a118","a119","a120","a121","a122","a123","a124","a125","a126","a127","a128","a129","a130","a131","a132","a133","a134","a135","a136","a137","a138","a139","a140","a141","a142","a143","a144","a145","a146","a147","a148","a149","a150","a151","a152","a153","a154","a155","a156","a157","a158","a159","a160","a161","a162","a163","a164","a165","a166","a167","a168","a169","a170","a171","a172","a173","a174","a175","a176","a177","a178","a179","a180","a181","a182","a183","a184","a185","a186","a187","a188","a189","a190","a191","a192","a193","a194","a195","a196","a197","a198","a199","a200","a201","a202","a203","a204","a205","a206","a207","a208","a209","a210","a211","a212","a213","a214","a215","a216","a217","a218","a219","a220","a221","a222","a223","a224","a225","a226","a227","a228","a229","a230","a231","a232","a233","a234","a235","a236","a237","a238","a239","a240","a241","a242","a243","a244","a245","a246","a247","a248","a249","a250","a251","a252","a253","a254","a255"
#define CLOB_S "s8","s9","s10","s11","s12","s13","s14","s15","s16","s17","s18","s19","s20","s21","s22","s23","s24","s25","s26","s27","s28","s29","s30","s31","s32","s33","s34","s35","s36","s37","s38","s39","s40","s41","s42","s43","s44","s45","s46","s47","s48","s49","s50","s51","s52","s53","s54","s55","s56","s57","s58","s59","s60","s61","s62","s63","s64","s65","s66","s67","s68","s69","s70","s71","s72","s73","s74","s75","s76","s77","s78","s79","s80","s81","s82","s83","s84","s85","s86","s87","s88","s89","s90","s91","s92","s93","s94","s95","s96","s97","s98","s99"
__global__ __launch_bounds__(256, 1) __attribute__((aligned(4096))) void ka_fused(const void* wq, const void* x, void* ws, const void* Kc, const void* wr, const void* Vc, const void* wo, const void* wg, const void* wu, const void* wd, void* out) {
  extern __shared__ __attribute__((aligned(16))) char lds[];
  const unsigned w = __builtin_amdgcn_readfirstlane(threadIdx.x >> 6);
  const unsigned slds = __builtin_amdgcn_readfirstlane((unsigned)(size_t)lds);
  asm volatile(
R"ASM(
.macro MM a, b, c, d:vararg
v_mfma_f32_16x16x32_bf16 \a, \b, \c, \d
.endm
.macro TR a, b:vararg
ds_read_b64_tr_b16 \a, \b
.endm
.macro CV a, b, c:vararg
v_cvt_pk_bf16_f32 \a, \b, \c
.endm
.macro AR a, b:vararg
v_accvgpr_read_b32 \a, \b
.endm
.macro AW a, b:vararg
v_accvgpr_write_b32 \a, \b
.endm
.macro EX a, b:vararg
v_exp_f32 \a, \b
.endm
.macro RD a, b:vararg
ds_read_b128 \a, \b
.endm
s_load_dwordx2 s[16:17], %[ka], 0
s_load_dwordx2 s[32:33], %[ka], 8
s_load_dwordx2 s[20:21], %[ka], 16
s_branch LPG0_%=
.p2align 6
LPG0_%=:
v_mbcnt_lo_u32_b32 v127, -1, 0
v_mbcnt_hi_u32_b32 v127, -1, v127
v_lshlrev_b32 v248, 4, v127
s_lshr_b32 s77, %[sb], 3
s_lshl_b32 s77, s77, 2
s_add_u32 s77, s77, %[swave]
s_lshl_b32 s77, s77, 10
v_add_u32 v118, s77, v248
s_mov_b32 s77, LKEND_ka-LPC_%=-16
v_min_u32 v118, s77, v118
s_getpc_b64 s[98:99]
LPC_%=:
global_load_dwordx4 a[252:255], v118, s[98:99]
s_and_b32 s77, %[sb], 7
s_lshl_b32 s77, s77, 2
s_bfe_u32 s84, %[sb], 131075
s_add_u32 s49, s77, s84
s_lshr_b32 s50, %[sb], 5
s_lshl_b32 s76, %[sb], 2
s_add_u32 s76, s76, %[swave]
v_lshrrev_b32 v180, 5, v127
s_mul_i32 s82, s76, 2560
v_and_b32 v182, 496, v248
v_mul_u32_u24 v181, 1280, v180
v_add_u32 v168, v181, v182
v_lshrrev_b32 v210, 1, v182
v_add_u32 v181, v181, v210
s_lshl_b32 s77, %[swave], 12
s_add_u32 s65, s77, %[slds]
s_lshl_b32 s89, %[swave], 17
v_bfe_u32 v210, v127, 2, 3
v_and_b32 v212, 3, v127
v_lshlrev_b32 v213, 4, v212
v_lshl_add_u32 v213, v180, 6, v213
v_lshrrev_b32 v253, 3, v127
v_lshlrev_b32 v253, 13, v253
v_and_b32 v254, 112, v248
v_or_b32 v253, v253, v254
v_and_b32 v254, 16, v127
v_lshlrev_b32 v254, 2, v254
v_xor_b32 v253, v253, v254
v_add_u32 v254, 128, v253
s_mul_i32 s96, %[swave], 4112
s_add_u32 s96, s96, %[slds]
s_lshl_b32 s77, s50, 2
s_add_u32 s77, s77, %[swave]
s_mul_i32 s68, s77, 81920
s_and_b32 s62, %[sb], 1
s_mul_i32 s63, s62, 40960
s_add_u32 s68, s68, s63
s_lshl_b32 s63, s63, 1
s_mul_i32 s83, s77, 560128
s_add_u32 s83, s83, 2752512
s_lshl_b32 s93, s77, 9
s_lshl_b32 s94, %[swave], 13
s_add_u32 s94, s94, %[slds]
v_lshrrev_b32 v211, 3, v127
v_mul_u32_u24 v220, 1280, v211
v_lshrrev_b32 v212, 4, v127
v_and_b32 v211, 7, v127
v_xor_b32 v211, v211, v212
v_lshl_add_u32 v220, v211, 4, v220
v_xor_b32 v221, 64, v220
s_bitcmp1_b32 s84, 0
s_mov_b32 s77, 0x77552200
s_cselect_b32 s77, 0x77552200, s77
s_lshl_b32 s91, s50, 2
s_lshr_b32 s77, s77, s91
s_and_b32 s77, s77, 15
s_lshl_b32 s86, s77, 7
s_lshl_b32 s85, s77, 19
s_lshl_b32 s90, s49, 8
s_sub_u32 s18, 5242880, s90
s_mov_b32 s19, 131072
s_mov_b32 s34, 2621440
s_mov_b32 s35, 131072
s_mov_b32 s22, 67108864
s_mov_b32 s23, 131072
s_add_u32 s87, s89, s85
s_add_u32 s85, s85, 524288
s_cmp_ge_u32 s85, 5242880
s_cselect_b32 s85, 0, s85
s_add_u32 m0, s96, 65536
s_waitcnt lgkmcnt(0)
s_add_u32 s16, s16, s90
s_addc_u32 s17, s17, 0
buffer_load_dwordx4 v253, s[16:19], s87 offen offset:0 lds
s_add_u32 m0, s96, 66560
s_nop 0
buffer_load_dwordx4 v254, s[16:19], s87 offen offset:0 lds
s_add_u32 s87, s87, 65536
s_add_u32 m0, s96, 67592
s_nop 0
buffer_load_dwordx4 v253, s[16:19], s87 offen offset:0 lds
s_add_u32 m0, s96, 68616
s_nop 0
buffer_load_dwordx4 v254, s[16:19], s87 offen offset:0 lds
s_add_u32 s88, s68, s86
s_add_u32 s86, s86, 128
s_cmp_ge_u32 s86, 1280
s_cselect_b32 s86, 0, s86
s_add_u32 m0, s94, 0
s_add_u32 s95, s88, 0
buffer_load_dwordx4 v220, s[32:35], s95 offen offset:0 lds
s_add_u32 s95, s88, 9216
buffer_load_dwordx4 v221, s[32:35], s95 offen offset:1024 lds
s_add_u32 s95, s88, 18432
buffer_load_dwordx4 v220, s[32:35], s95 offen offset:2048 lds
s_add_u32 s95, s88, 27648
buffer_load_dwordx4 v221, s[32:35], s95 offen offset:3072 lds
s_load_dwordx2 s[8:9], %[ka], 24
s_load_dwordx2 s[12:13], %[ka], 40
s_load_dwordx2 s[24:25], %[ka], 48
s_load_dwordx2 s[44:45], %[ka], 32
s_load_dwordx2 s[28:29], %[ka], 72
s_load_dwordx2 s[40:41], %[ka], 80
s_lshl_b32 s77, %[swave], 2
s_and_b32 s77, s77, 8
s_add_u32 s77, s77, 56
s_load_dwordx2 s[36:37], %[ka], s77
s_mov_b32 s66, 32768
buffer_load_dword v169, off, s[20:23], s66 sc1
buffer_load_dwordx4 v[128:131], v168, s[32:35], s82 offen offset:0
buffer_load_dwordx4 v[132:135], v168, s[32:35], s82 offen offset:512
buffer_load_dwordx2 v[136:137], v181, s[32:35], s82 offen offset:1024
s_add_u32 s82, s82, 131072
s_sub_u32 s88, s88, s63
s_add_u32 m0, s94, 4096
s_add_u32 s95, s88, 40960
buffer_load_dwordx4 v220, s[32:35], s95 offen offset:0 lds
s_add_u32 s95, s88, 50176
buffer_load_dwordx4 v221, s[32:35], s95 offen offset:1024 lds
s_add_u32 s95, s88, 59392
buffer_load_dwordx4 v220, s[32:35], s95 offen offset:2048 lds
s_add_u32 s95, s88, 68608
buffer_load_dwordx4 v221, s[32:35], s95 offen offset:3072 lds
v_and_b32 v211, 15, v127
v_bfe_u32 v222, v127, 1, 3
v_xor_b32 v222, v222, v212
v_lshlrev_b32 v222, 4, v222
v_lshl_add_u32 v222, v211, 7, v222
v_xor_b32 v223, 64, v222
v_add_u32 v222, s94, v222
v_add_u32 v223, s94, v223
v_and_b32 v214, 1, v212
v_lshlrev_b32 v215, 5, v214
v_xor_b32 v215, v215, v213
v_lshl_add_u32 v235, v210, 8, v215
v_add_u32 v235, 128, v235
v_and_b32 v234, 1, v127
v_mul_u32_u24 v234, 43690, v234
v_add_u32 v234, 17476, v234
v_bfe_u32 v215, v127, 2, 2
v_and_b32 v216, 3, v127
v_lshlrev_b32 v252, 7, v215
v_lshl_add_u32 v252, v216, 4, v252
v_and_b32 v251, 8, v127
v_lshl_add_u32 v252, v251, 3, v252
v_mul_u32_u24 v251, 2056, v212
s_xor_b32 s77, %[swave], 0
s_and_b32 s91, s77, 1
s_lshl_b32 s91, s91, 6
s_lshr_b32 s77, s77, 1
s_lshl_b32 s77, s77, 10
s_add_u32 s77, s77, %[slds]
s_add_u32 s77, s77, 65536
v_xor_b32 v192, s91, v252
v_add3_u32 v192, s77, v192, v251
v_add_u32 v196, 49344, v192
s_xor_b32 s77, %[swave], 1
s_and_b32 s91, s77, 1
s_lshl_b32 s91, s91, 6
s_lshr_b32 s77, s77, 1
s_lshl_b32 s77, s77, 10
s_add_u32 s77, s77, %[slds]
s_add_u32 s77, s77, 65536
v_xor_b32 v193, s91, v252
v_add3_u32 v193, s77, v193, v251
v_add_u32 v197, 49344, v193
s_xor_b32 s77, %[swave], 2
s_and_b32 s91, s77, 1
s_lshl_b32 s91, s91, 6
s_lshr_b32 s77, s77, 1
s_lshl_b32 s77, s77, 10
s_add_u32 s77, s77, %[slds]
s_add_u32 s77, s77, 65536
v_xor_b32 v194, s91, v252
v_add3_u32 v194, s77, v194, v251
v_add_u32 v198, 49344, v194
s_xor_b32 s77, %[swave], 3
s_and_b32 s91, s77, 1
s_lshl_b32 s91, s91, 6
s_lshr_b32 s77, s77, 1
s_lshl_b32 s77, s77, 10
s_add_u32 s77, s77, %[slds]
s_add_u32 s77, s77, 65536
v_xor_b32 v195, s91, v252
v_add3_u32 v195, s77, v195, v251
v_add_u32 v199, 49344, v195
v_mov_b32 v236, 0x3f803f80
v_mov_b32 v184, 0
v_mov_b32 v237, 0x3f803f80
v_mov_b32 v185, 0
v_mov_b32 v238, 0x3f803f80
v_mov_b32 v188, 0
v_mov_b32 v239, 0x3f803f80
v_mov_b32 v189, 0
s_add_u32 s87, s89, s85
s_add_u32 s85, s85, 524288
s_cmp_ge_u32 s85, 5242880
s_cselect_b32 s85, 0, s85
s_add_u32 m0, s96, 81984
s_nop 0
buffer_load_dwordx4 v253, s[16:19], s87 offen offset:0 lds
s_add_u32 m0, s96, 83008
s_nop 0
buffer_load_dwordx4 v254, s[16:19], s87 offen offset:0 lds
s_add_u32 s87, s87, 65536
s_add_u32 m0, s96, 84040
s_nop 0
buffer_load_dwordx4 v253, s[16:19], s87 offen offset:0 lds
s_add_u32 m0, s96, 85064
s_nop 0
buffer_load_dwordx4 v254, s[16:19], s87 offen offset:0 lds
s_getpc_b64 s[98:99]
LTA0_%=:
s_min_u32 s73, %[swave], 3
s_lshl_b32 s73, s73, 12
s_add_u32 s73, s73, LPG1_%=-LTA0_%=
s_add_u32 s98, s98, s73
s_addc_u32 s99, s99, 0
s_mov_b64 exec, 0
s_setpc_b64 s[98:99]
LTR0_%=:
s_mov_b64 exec, -1
s_waitcnt lgkmcnt(0)
s_add_u32 s88, s68, s86
s_add_u32 s86, s86, 128
s_cmp_ge_u32 s86, 1280
s_cselect_b32 s86, 0, s86
s_add_u32 m0, s94, 32768
s_add_u32 s95, s88, 0
buffer_load_dwordx4 v220, s[32:35], s95 offen offset:0 lds
s_add_u32 s95, s88, 9216
buffer_load_dwordx4 v221, s[32:35], s95 offen offset:1024 lds
s_add_u32 s95, s88, 18432
buffer_load_dwordx4 v220, s[32:35], s95 offen offset:2048 lds
s_add_u32 s95, s88, 27648
buffer_load_dwordx4 v221, s[32:35], s95 offen offset:3072 lds
s_sub_u32 s88, s88, s63
s_add_u32 m0, s94, 36864
s_add_u32 s95, s88, 40960
buffer_load_dwordx4 v220, s[32:35], s95 offen offset:0 lds
s_add_u32 s95, s88, 50176
buffer_load_dwordx4 v221, s[32:35], s95 offen offset:1024 lds
s_add_u32 s95, s88, 59392
buffer_load_dwordx4 v220, s[32:35], s95 offen offset:2048 lds
s_add_u32 s95, s88, 68608
buffer_load_dwordx4 v221, s[32:35], s95 offen offset:3072 lds
s_waitcnt vmcnt(20)
RD a[128:131],v222 offset:0
RD a[132:135],v223 offset:0
RD a[136:139],v222 offset:2048
RD a[140:143],v223 offset:2048
s_barrier
TR a[0:1],v192 offset:0
TR a[2:3],v192 offset:512
TR a[4:5],v192 offset:8224
TR a[6:7],v192 offset:8736
TR a[8:9],v192 offset:8
TR a[10:11],v192 offset:520
TR a[12:13],v192 offset:8232
TR a[14:15],v192 offset:8744
TR a[16:17],v193 offset:0
TR a[18:19],v193 offset:512
TR a[20:21],v193 offset:8224
TR a[22:23],v193 offset:8736
TR a[24:25],v193 offset:8
TR a[26:27],v193 offset:520
TR a[28:29],v193 offset:8232
TR a[30:31],v193 offset:8744
s_waitcnt lgkmcnt(15)
s_waitcnt lgkmcnt(12)
MM v[0:3],a[0:3],a[128:131],0
s_add_u32 s87, s89, s85
TR a[32:33],v194 offset:0
TR a[34:35],v194 offset:512
MM v[0:3],a[4:7],a[132:135],v[0:3]
s_add_u32 s85, s85, 524288
TR a[36:37],v194 offset:8224
TR a[38:39],v194 offset:8736
MM v[8:11],a[4:7],a[140:143],0
s_waitcnt vmcnt(16)
TR a[40:41],v194 offset:8
TR a[42:43],v194 offset:520
MM v[8:11],a[0:3],a[136:139],v[8:11]
s_waitcnt lgkmcnt(14)
TR a[44:45],v194 offset:8232
TR a[46:47],v194 offset:8744
s_cmp_ge_u32 s85, 5242880
s_cselect_b32 s85, 0, s85
MM v[4:7],a[8:11],a[128:131],0
s_add_u32 m0, s96, 98432
v_max3_f32 v200, |v0|, |v1|, |v2|
v_max_f32 v200, v200, |v3|
v_readfirstlane_b32 s69, v169
MM v[4:7],a[12:15],a[132:135],v[4:7]
s_lshl_b32 s90, s76, 1
TR a[48:49],v195 offset:0
TR a[50:51],v195 offset:512
MM v[12:15],a[12:15],a[140:143],0
s_and_b32 s77, s90, 15
buffer_load_dwordx4 v253, s[16:19], s87 offen offset:0 lds
v_max3_f32 v201, |v8|, |v9|, |v10|
v_max_f32 v201, v201, |v11|
MM v[12:15],a[8:11],a[136:139],v[12:15]
s_add_u32 m0, s96, 99456
TR a[52:53],v195 offset:8224
v_min_f32 v200, v200, v201
v_and_b32 v200, 0x7f800000, v200
v_lshlrev_b32 v148, 16, v128
v_smfmac_f32_16x16x64_bf16 v[184:187], v[236:239], a[0:7], v234
s_lshl_b32 s77, s77, 1
v_add_u32 v200, 0x2c00000, v200
v_smfmac_f32_16x16x64_bf16 v[188:191], v[236:239], a[8:15], v234
s_waitcnt lgkmcnt(15)
v_min_u32 v200, 0x78400000, v200
v_mov_b32 v201, v200
v_mov_b32 v202, v200
v_mov_b32 v203, v200
s_nop 1
MM v[32:35],a[16:19],a[128:131],v[200:203]
s_bfe_u32 s91, s90, 65540
TR a[54:55],v195 offset:8736
buffer_load_dwordx4 v254, s[16:19], s87 offen offset:0 lds
v_mov_b32 v250, v200
v_and_b32 v149, 0xffff0000, v128
MM v[32:35],a[20:23],a[132:135],v[32:35]
s_add_u32 s87, s87, 65536
TR a[56:57],v195 offset:8
TR a[58:59],v195 offset:520
MM v[40:43],a[20:23],a[140:143],v[200:203]
s_add_u32 m0, s96, 100488
TR a[60:61],v195 offset:8232
TR a[62:63],v195 offset:8744
MM v[40:43],a[16:19],a[136:139],v[40:43]
s_waitcnt lgkmcnt(15)
buffer_load_dwordx4 v253, s[16:19], s87 offen offset:0 lds
v_lshlrev_b32 v150, 16, v129
MM v[36:39],a[24:27],a[128:131],v[200:203]
s_add_u32 m0, s96, 101512
v_and_b32 v151, 0xffff0000, v129
v_lshlrev_b32 v152, 16, v130
MM v[36:39],a[28:31],a[132:135],v[36:39]
s_add_u32 s77, s77, s91
v_and_b32 v153, 0xffff0000, v130
v_lshlrev_b32 v154, 16, v131
MM v[44:47],a[28:31],a[140:143],v[200:203]
s_and_b32 s91, s90, 0xffffffe0
buffer_load_dwordx4 v254, s[16:19], s87 offen offset:0 lds
MM v[44:47],a[24:27],a[136:139],v[44:47]
s_waitcnt lgkmcnt(12)
s_waitcnt vmcnt(16)
RD a[144:147],v222 offset:4096
v_and_b32 v155, 0xffff0000, v131
MM v[64:67],a[32:35],a[128:131],v[200:203]
s_add_u32 s77, s77, s91
RD a[148:151],v223 offset:4096
RD a[152:155],v222 offset:6144
MM v[64:67],a[36:39],a[132:135],v[64:67]
s_lshl_b32 s77, s77, 3
RD a[156:159],v223 offset:6144
v_lshlrev_b32 v156, 16, v132
MM v[72:75],a[36:39],a[140:143],v[200:203]
s_add_u32 s97, s77, 36864
s_waitcnt vmcnt(12)
s_barrier
v_and_b32 v157, 0xffff0000, v132
MM v[72:75],a[32:35],a[136:139],v[72:75]
s_waitcnt lgkmcnt(12)
global_load_dwordx4 v[138:141], v182, s[44:45] offset:0
MM v[68:71],a[40:43],a[128:131],v[200:203]
s_sub_u32 s79, 65535, s90
v_lshlrev_b32 v158, 16, v133
v_and_b32 v159, 0xffff0000, v133
MM v[68:71],a[44:47],a[132:135],v[68:71]
s_lshl_b32 s78, s90, 2
v_lshlrev_b32 v160, 16, v134
v_and_b32 v161, 0xffff0000, v134
MM v[76:79],a[44:47],a[140:143],v[200:203]
s_add_u32 s78, s78, 53248
global_load_dwordx4 v[142:145], v182, s[44:45] offset:512
MM v[76:79],a[40:43],a[136:139],v[76:79]
s_waitcnt lgkmcnt(8)
v_lshrrev_b32 v182, 1, v182
v_lshlrev_b32 v162, 16, v135
MM v[96:99],a[48:51],a[128:131],v[200:203]
s_lshl_b32 s80, s76, 2
)ASM"
R"ASM(
global_load_dwordx2 v[146:147], v182, s[44:45] offset:1024
MM v[96:99],a[52:55],a[132:135],v[96:99]
s_waitcnt lgkmcnt(0)
s_add_u32 s88, s68, s86
s_add_u32 s86, s86, 128
v_and_b32 v163, 0xffff0000, v135
MM v[104:107],a[52:55],a[140:143],v[200:203]
s_add_u32 s80, s80, 81920
s_cmp_ge_u32 s86, 1280
s_cselect_b32 s86, 0, s86
MM v[104:107],a[48:51],a[136:139],v[104:107]
s_add_u32 m0, s94, 0
s_add_u32 s95, s88, 0
v_lshlrev_b32 v164, 16, v136
MM v[100:103],a[56:59],a[128:131],v[200:203]
s_mov_b32 s42, 2621440
buffer_load_dwordx4 v220, s[32:35], s95 offen offset:0 lds
MM v[100:103],a[60:63],a[132:135],v[100:103]
s_add_u32 s95, s88, 9216
v_and_b32 v165, 0xffff0000, v136
v_lshlrev_b32 v166, 16, v137
MM v[108:111],a[60:63],a[140:143],v[200:203]
s_mov_b32 s43, 131072
buffer_load_dwordx4 v221, s[32:35], s95 offen offset:1024 lds
MM v[108:111],a[56:59],a[136:139],v[108:111]
s_add_u32 s95, s88, 18432
v_and_b32 v167, 0xffff0000, v137
s_add_u32 s69, s69, 1
MM v[16:19],a[0:3],a[144:147],0
v_add_f32 v170, v148, v149
v_add_f32 v171, v150, v151
MM v[16:19],a[4:7],a[148:151],v[16:19]
v_add_f32 v170, v170, v152
v_add_f32 v171, v171, v153
MM v[24:27],a[4:7],a[156:159],0
buffer_load_dwordx4 v220, s[32:35], s95 offen offset:2048 lds
MM v[24:27],a[0:3],a[152:155],v[24:27]
s_add_u32 s95, s88, 27648
v_add_f32 v170, v170, v154
v_add_f32 v171, v171, v155
MM v[20:23],a[8:11],a[144:147],0
v_max3_f32 v204, |v16|, |v17|, |v18|
v_max_f32 v204, v204, |v19|
MM v[20:23],a[12:15],a[148:151],v[20:23]
TR a[0:1],v192 offset:16448
TR a[2:3],v192 offset:16960
v_add_f32 v170, v170, v156
MM v[28:31],a[12:15],a[156:159],0
v_max3_f32 v205, |v24|, |v25|, |v26|
v_max_f32 v205, v205, |v27|
MM v[28:31],a[8:11],a[152:155],v[28:31]
TR a[4:5],v192 offset:24672
v_min_f32 v204, v204, v205
v_and_b32 v204, 0x7f800000, v204
v_add_u32 v204, 0x2c00000, v204
v_min_u32 v204, 0x78400000, v204
v_mov_b32 v205, v204
v_mov_b32 v206, v204
v_mov_b32 v207, v204
s_nop 1
MM v[48:51],a[16:19],a[144:147],v[204:207]
TR a[6:7],v192 offset:25184
buffer_load_dwordx4 v221, s[32:35], s95 offen offset:3072 lds
v_mov_b32 v252, v204
v_add_f32 v171, v171, v157
MM v[48:51],a[20:23],a[148:151],v[48:51]
s_sub_u32 s88, s88, s63
TR a[8:9],v192 offset:16456
TR a[10:11],v192 offset:16968
MM v[56:59],a[20:23],a[156:159],v[204:207]
s_add_u32 m0, s94, 4096
TR a[12:13],v192 offset:24680
TR a[14:15],v192 offset:25192
MM v[56:59],a[16:19],a[152:155],v[56:59]
s_add_u32 s95, s88, 40960
buffer_load_dwordx4 v220, s[32:35], s95 offen offset:0 lds
v_add_f32 v170, v170, v158
MM v[52:55],a[24:27],a[144:147],v[204:207]
s_add_u32 s95, s88, 50176
TR a[16:17],v193 offset:16448
TR a[18:19],v193 offset:16960
MM v[52:55],a[28:31],a[148:151],v[52:55]
TR a[20:21],v193 offset:24672
TR a[22:23],v193 offset:25184
MM v[60:63],a[28:31],a[156:159],v[204:207]
buffer_load_dwordx4 v221, s[32:35], s95 offen offset:1024 lds
v_add_f32 v171, v171, v159
MM v[60:63],a[24:27],a[152:155],v[60:63]
s_add_u32 s95, s88, 59392
v_add_f32 v170, v170, v160
v_add_f32 v171, v171, v161
MM v[80:83],a[32:35],a[144:147],v[204:207]
TR a[24:25],v193 offset:16456
TR a[26:27],v193 offset:16968
MM v[80:83],a[36:39],a[148:151],v[80:83]
TR a[28:29],v193 offset:24680
TR a[30:31],v193 offset:25192
v_add_f32 v170, v170, v162
MM v[88:91],a[36:39],a[156:159],v[204:207]
buffer_load_dwordx4 v220, s[32:35], s95 offen offset:2048 lds
MM v[88:91],a[32:35],a[152:155],v[88:91]
s_add_u32 s95, s88, 68608
v_add_f32 v171, v171, v163
v_add_f32 v170, v170, v164
MM v[84:87],a[40:43],a[144:147],v[204:207]
TR a[32:33],v194 offset:16448
TR a[34:35],v194 offset:16960
MM v[84:87],a[44:47],a[148:151],v[84:87]
TR a[36:37],v194 offset:24672
TR a[38:39],v194 offset:25184
v_add_f32 v171, v171, v165
MM v[92:95],a[44:47],a[156:159],v[204:207]
buffer_load_dwordx4 v221, s[32:35], s95 offen offset:3072 lds
MM v[92:95],a[40:43],a[152:155],v[92:95]
s_add_u32 s87, s89, s85
s_add_u32 s85, s85, 524288
v_add_f32 v170, v170, v166
MM v[112:115],a[48:51],a[144:147],v[204:207]
TR a[40:41],v194 offset:16456
TR a[42:43],v194 offset:16968
MM v[112:115],a[52:55],a[148:151],v[112:115]
TR a[44:45],v194 offset:24680
TR a[46:47],v194 offset:25192
s_cmp_ge_u32 s85, 5242880
s_cselect_b32 s85, 0, s85
v_add_f32 v171, v171, v167
MM v[120:123],a[52:55],a[156:159],v[204:207]
s_add_u32 m0, s96, 114880
s_nop 0
buffer_load_dwordx4 v253, s[16:19], s87 offen offset:0 lds
MM v[120:123],a[48:51],a[152:155],v[120:123]
s_add_u32 m0, s96, 115904
v_add_f32 v172, v170, v171
v_lshlrev_b32 v232, 11, v180
MM v[116:119],a[56:59],a[144:147],v[204:207]
TR a[48:49],v195 offset:16448
TR a[50:51],v195 offset:16960
MM v[116:119],a[60:63],a[148:151],v[116:119]
TR a[52:53],v195 offset:24672
TR a[54:55],v195 offset:25184
MM v[124:127],a[60:63],a[156:159],v[204:207]
buffer_load_dwordx4 v254, s[16:19], s87 offen offset:0 lds
v_add_f32_dpp v172, v172, v172 row_ror:8 row_mask:0xf bank_mask:0xf
MM v[124:127],a[56:59],a[152:155],v[124:127]
s_add_u32 s87, s87, 65536
s_add_u32 m0, s96, 116936
v_add_f32_dpp v172, v172, v172 row_ror:4 row_mask:0xf bank_mask:0xf
buffer_load_dwordx4 v253, s[16:19], s87 offen offset:0 lds
s_add_u32 m0, s96, 117960
s_nop 0
buffer_load_dwordx4 v254, s[16:19], s87 offen offset:0 lds
s_waitcnt vmcnt(19)
RD a[160:163],v222 offset:32768
RD a[164:167],v223 offset:32768
RD a[168:171],v222 offset:34816
RD a[172:175],v223 offset:34816
RD a[176:179],v222 offset:36864
RD a[180:183],v223 offset:36864
RD a[184:187],v222 offset:38912
RD a[188:191],v223 offset:38912
s_waitcnt vmcnt(15) lgkmcnt(0)
s_barrier
MM v[0:3],a[0:3],a[160:163],v[0:3]
TR a[56:57],v195 offset:16456
TR a[58:59],v195 offset:16968
MM v[0:3],a[4:7],a[164:167],v[0:3]
TR a[60:61],v195 offset:24680
TR a[62:63],v195 offset:25192
MM v[8:11],a[4:7],a[172:175],v[8:11]
v_add_f32_dpp v172, v172, v172 row_ror:2 row_mask:0xf bank_mask:0xf
v_lshl_add_u32 v232, v214, 8, v232
MM v[8:11],a[0:3],a[168:171],v[8:11]
s_add_u32 s88, s68, s86
s_add_u32 s86, s86, 128
MM v[16:19],a[0:3],a[176:179],v[16:19]
s_cmp_ge_u32 s86, 1280
s_cselect_b32 s86, 0, s86
s_add_u32 m0, s94, 32768
MM v[16:19],a[4:7],a[180:183],v[16:19]
s_add_u32 s95, s88, 0
buffer_load_dwordx4 v220, s[32:35], s95 offen offset:0 lds
v_add_f32_dpp v172, v172, v172 row_ror:1 row_mask:0xf bank_mask:0xf
MM v[24:27],a[4:7],a[188:191],v[24:27]
s_add_u32 s95, s88, 9216
v_mov_b32 v173, v172
v_lshl_add_u32 v232, v215, 6, v232
MM v[24:27],a[0:3],a[184:187],v[24:27]
buffer_load_dwordx4 v221, s[32:35], s95 offen offset:1024 lds
MM v[4:7],a[8:11],a[160:163],v[4:7]
s_add_u32 s95, s88, 18432
v_permlane16_swap_b32 v172, v173
v_add_f32 v172, v172, v173
MM v[4:7],a[12:15],a[164:167],v[4:7]
v_mul_f32 v172, 0x3acccccd, v172
v_sub_f32 v176, v148, v172
MM v[12:15],a[12:15],a[172:175],v[12:15]
v_sub_f32 v177, v149, v172
v_mul_f32 v170, v176, v176
MM v[12:15],a[8:11],a[168:171],v[12:15]
buffer_load_dwordx4 v220, s[32:35], s95 offen offset:2048 lds
MM v[20:23],a[8:11],a[176:179],v[20:23]
s_add_u32 s95, s88, 27648
v_mul_f32 v171, v177, v177
v_sub_f32 v176, v150, v172
MM v[20:23],a[12:15],a[180:183],v[20:23]
v_sub_f32 v177, v151, v172
v_fma_f32 v170, v176, v176, v170
MM v[28:31],a[12:15],a[188:191],v[28:31]
v_fma_f32 v171, v177, v177, v171
v_sub_f32 v176, v152, v172
MM v[28:31],a[8:11],a[184:187],v[28:31]
buffer_load_dwordx4 v221, s[32:35], s95 offen offset:3072 lds
v_smfmac_f32_16x16x64_bf16 v[184:187], v[236:239], a[0:7], v234
s_sub_u32 s88, s88, s63
s_add_u32 m0, s94, 36864
v_sub_f32 v177, v153, v172
v_smfmac_f32_16x16x64_bf16 v[188:191], v[236:239], a[8:15], v234
s_add_u32 s95, s88, 40960
v_fma_f32 v170, v176, v176, v170
MM v[32:35],a[16:19],a[160:163],v[32:35]
TR a[0:1],v192 offset:32896
TR a[2:3],v192 offset:33408
s_nop 0
LPG1_%=:
s_cbranch_execz LPG5_%=
MM v[32:35],a[20:23],a[164:167],v[32:35]
TR a[4:5],v192 offset:41120
TR a[6:7],v192 offset:41632
v_fma_f32 v171, v177, v177, v171
MM v[40:43],a[20:23],a[172:175],v[40:43]
TR a[8:9],v192 offset:32904
TR a[10:11],v192 offset:33416
MM v[40:43],a[16:19],a[168:171],v[40:43]
TR a[12:13],v192 offset:41128
TR a[14:15],v192 offset:41640
buffer_load_dwordx4 v220, s[32:35], s95 offen offset:0 lds
MM v[48:51],a[16:19],a[176:179],v[48:51]
s_add_u32 s95, s88, 50176
v_sub_f32 v176, v154, v172
v_sub_f32 v177, v155, v172
MM v[48:51],a[20:23],a[180:183],v[48:51]
buffer_load_dwordx4 v221, s[32:35], s95 offen offset:1024 lds
MM v[56:59],a[20:23],a[188:191],v[56:59]
s_add_u32 s95, s88, 59392
v_fma_f32 v170, v176, v176, v170
v_fma_f32 v171, v177, v177, v171
MM v[56:59],a[16:19],a[184:187],v[56:59]
v_sub_f32 v176, v156, v172
v_sub_f32 v177, v157, v172
MM v[36:39],a[24:27],a[160:163],v[36:39]
TR a[16:17],v193 offset:32896
TR a[18:19],v193 offset:33408
MM v[36:39],a[28:31],a[164:167],v[36:39]
TR a[20:21],v193 offset:41120
TR a[22:23],v193 offset:41632
v_fma_f32 v170, v176, v176, v170
MM v[44:47],a[28:31],a[172:175],v[44:47]
buffer_load_dwordx4 v220, s[32:35], s95 offen offset:2048 lds
MM v[44:47],a[24:27],a[168:171],v[44:47]
s_add_u32 s95, s88, 68608
v_fma_f32 v171, v177, v177, v171
v_sub_f32 v176, v158, v172
MM v[52:55],a[24:27],a[176:179],v[52:55]
v_sub_f32 v177, v159, v172
v_fma_f32 v170, v176, v176, v170
MM v[52:55],a[28:31],a[180:183],v[52:55]
buffer_load_dwordx4 v221, s[32:35], s95 offen offset:3072 lds
MM v[60:63],a[28:31],a[188:191],v[60:63]
s_add_u32 s87, s89, s85
s_add_u32 s85, s85, 524288
v_fma_f32 v171, v177, v177, v171
MM v[60:63],a[24:27],a[184:187],v[60:63]
s_cmp_ge_u32 s85, 5242880
s_cselect_b32 s85, 0, s85
s_add_u32 m0, s96, 131328
MM v[64:67],a[32:35],a[160:163],v[64:67]
TR a[24:25],v193 offset:32904
TR a[26:27],v193 offset:33416
v_sub_f32 v176, v160, v172
MM v[64:67],a[36:39],a[164:167],v[64:67]
TR a[28:29],v193 offset:41128
TR a[30:31],v193 offset:41640
MM v[72:75],a[36:39],a[172:175],v[72:75]
buffer_load_dwordx4 v253, s[16:19], s87 offen offset:0 lds
MM v[72:75],a[32:35],a[168:171],v[72:75]
s_add_u32 m0, s96, 132352
v_sub_f32 v177, v161, v172
v_fma_f32 v170, v176, v176, v170
MM v[80:83],a[32:35],a[176:179],v[80:83]
v_fma_f32 v171, v177, v177, v171
v_sub_f32 v176, v162, v172
MM v[80:83],a[36:39],a[180:183],v[80:83]
buffer_load_dwordx4 v254, s[16:19], s87 offen offset:0 lds
MM v[88:91],a[36:39],a[188:191],v[88:91]
s_add_u32 s87, s87, 65536
s_add_u32 m0, s96, 133384
v_sub_f32 v177, v163, v172
MM v[88:91],a[32:35],a[184:187],v[88:91]
v_fma_f32 v170, v176, v176, v170
v_fma_f32 v171, v177, v177, v171
MM v[68:71],a[40:43],a[160:163],v[68:71]
TR a[32:33],v194 offset:32896
TR a[34:35],v194 offset:33408
MM v[68:71],a[44:47],a[164:167],v[68:71]
TR a[36:37],v194 offset:41120
TR a[38:39],v194 offset:41632
v_sub_f32 v176, v164, v172
MM v[76:79],a[44:47],a[172:175],v[76:79]
buffer_load_dwordx4 v253, s[16:19], s87 offen offset:0 lds
MM v[76:79],a[40:43],a[168:171],v[76:79]
s_add_u32 m0, s96, 134408
v_sub_f32 v177, v165, v172
v_fma_f32 v170, v176, v176, v170
MM v[84:87],a[40:43],a[176:179],v[84:87]
v_fma_f32 v171, v177, v177, v171
v_sub_f32 v176, v166, v172
MM v[84:87],a[44:47],a[180:183],v[84:87]
buffer_load_dwordx4 v254, s[16:19], s87 offen offset:0 lds
MM v[92:95],a[44:47],a[188:191],v[92:95]
s_waitcnt vmcnt(16)
RD a[192:195],v222 offset:0
v_sub_f32 v177, v167, v172
MM v[92:95],a[40:43],a[184:187],v[92:95]
RD a[196:199],v223 offset:0
v_fma_f32 v170, v176, v176, v170
MM v[96:99],a[48:51],a[160:163],v[96:99]
TR a[40:41],v194 offset:32904
TR a[42:43],v194 offset:33416
MM v[96:99],a[52:55],a[164:167],v[96:99]
TR a[44:45],v194 offset:41128
TR a[46:47],v194 offset:41640
v_fma_f32 v171, v177, v177, v171
MM v[104:107],a[52:55],a[172:175],v[104:107]
RD a[200:203],v222 offset:2048
v_add_f32 v170, v170, v171
MM v[104:107],a[48:51],a[168:171],v[104:107]
RD a[204:207],v223 offset:2048
v_add_f32_dpp v170, v170, v170 row_ror:8 row_mask:0xf bank_mask:0xf
MM v[112:115],a[48:51],a[176:179],v[112:115]
RD a[208:211],v222 offset:4096
v_add_f32_dpp v170, v170, v170 row_ror:4 row_mask:0xf bank_mask:0xf
MM v[112:115],a[52:55],a[180:183],v[112:115]
RD a[212:215],v223 offset:4096
v_add_f32_dpp v170, v170, v170 row_ror:2 row_mask:0xf bank_mask:0xf
MM v[120:123],a[52:55],a[188:191],v[120:123]
RD a[216:219],v222 offset:6144
)ASM"
R"ASM(
v_add_f32_dpp v170, v170, v170 row_ror:1 row_mask:0xf bank_mask:0xf
MM v[120:123],a[48:51],a[184:187],v[120:123]
s_waitcnt lgkmcnt(15)
RD a[220:223],v223 offset:6144
v_mov_b32 v173, v170
MM v[100:103],a[56:59],a[160:163],v[100:103]
TR a[48:49],v195 offset:32896
TR a[50:51],v195 offset:33408
MM v[100:103],a[60:63],a[164:167],v[100:103]
TR a[52:53],v195 offset:41120
TR a[54:55],v195 offset:41632
MM v[108:111],a[60:63],a[172:175],v[108:111]
v_permlane16_swap_b32 v170, v173
v_add_f32 v170, v170, v173
MM v[108:111],a[56:59],a[168:171],v[108:111]
s_waitcnt lgkmcnt(4)
s_add_u32 s88, s68, s86
s_add_u32 s86, s86, 128
MM v[116:119],a[56:59],a[176:179],v[116:119]
s_cmp_ge_u32 s86, 1280
s_cselect_b32 s86, 0, s86
s_add_u32 m0, s94, 0
v_mul_f32 v170, 0x3acccccd, v170
MM v[116:119],a[60:63],a[180:183],v[116:119]
s_add_u32 s95, s88, 0
buffer_load_dwordx4 v220, s[32:35], s95 offen offset:0 lds
MM v[124:127],a[60:63],a[188:191],v[124:127]
s_add_u32 s95, s88, 9216
CV v178,v172,v170
v_lshlrev_b32 v174, 16, v178
MM v[124:127],a[56:59],a[184:187],v[124:127]
buffer_load_dwordx4 v221, s[32:35], s95 offen offset:1024 lds
s_waitcnt vmcnt(14)
s_barrier
MM v[0:3],a[0:3],a[192:195],v[0:3]
s_add_u32 s95, s88, 18432
TR a[56:57],v195 offset:32904
TR a[58:59],v195 offset:33416
v_and_b32 v170, 0xffff0000, v178
MM v[0:3],a[4:7],a[196:199],v[0:3]
TR a[60:61],v195 offset:41128
TR a[62:63],v195 offset:41640
MM v[8:11],a[4:7],a[204:207],v[8:11]
v_add_f32 v170, 0x3727c5ac, v170
CV v170,v170,v170
MM v[8:11],a[0:3],a[200:203],v[8:11]
buffer_load_dwordx4 v220, s[32:35], s95 offen offset:2048 lds
MM v[16:19],a[0:3],a[208:211],v[16:19]
s_add_u32 s95, s88, 27648
v_and_b32 v170, 0xffff0000, v170
v_lshl_add_u32 v232, v216, 3, v232
MM v[16:19],a[4:7],a[212:215],v[16:19]
v_sqrt_f32 v170, v170
MM v[24:27],a[4:7],a[220:223],v[24:27]
v_mov_b32 v179, s69
CV v178,v174,v170
MM v[24:27],a[0:3],a[216:219],v[24:27]
buffer_load_dwordx4 v221, s[32:35], s95 offen offset:3072 lds
MM v[4:7],a[8:11],a[192:195],v[4:7]
s_sub_u32 s88, s88, s63
s_add_u32 m0, s94, 4096
s_add_u32 s95, s88, 40960
v_and_b32 v170, 0xffff0000, v178
MM v[4:7],a[12:15],a[196:199],v[4:7]
v_rcp_f32 v175, v170
MM v[12:15],a[12:15],a[204:207],v[12:15]
v_lshlrev_b32 v173, 4, v180
v_lshl_add_u32 v232, v214, 5, v232
MM v[12:15],a[8:11],a[200:203],v[12:15]
buffer_load_dwordx4 v220, s[32:35], s95 offen offset:0 lds
MM v[20:23],a[8:11],a[208:211],v[20:23]
s_add_u32 s95, s88, 50176
s_mov_b32 exec_lo, 1
s_mov_b32 exec_hi, 1
buffer_store_dwordx2 v[178:179], v173, s[20:23], s97 offen offset:0 sc1
s_mov_b64 exec, -1
MM v[20:23],a[12:15],a[212:215],v[20:23]
v_sub_f32 v148, v148, v174
v_sub_f32 v149, v149, v174
MM v[28:31],a[12:15],a[220:223],v[28:31]
v_sub_f32 v150, v150, v174
v_sub_f32 v151, v151, v174
MM v[28:31],a[8:11],a[216:219],v[28:31]
buffer_load_dwordx4 v221, s[32:35], s95 offen offset:1024 lds
v_smfmac_f32_16x16x64_bf16 v[184:187], v[236:239], a[0:7], v234
s_add_u32 s95, s88, 59392
v_sub_f32 v152, v152, v174
v_smfmac_f32_16x16x64_bf16 v[188:191], v[236:239], a[8:15], v234
v_sub_f32 v153, v153, v174
MM v[32:35],a[16:19],a[192:195],v[32:35]
TR a[0:1],v196 offset:0
TR a[2:3],v196 offset:512
MM v[32:35],a[20:23],a[196:199],v[32:35]
TR a[4:5],v196 offset:8224
TR a[6:7],v196 offset:8736
v_sub_f32 v154, v154, v174
MM v[40:43],a[20:23],a[204:207],v[40:43]
TR a[8:9],v196 offset:8
TR a[10:11],v196 offset:520
MM v[40:43],a[16:19],a[200:203],v[40:43]
TR a[12:13],v196 offset:8232
TR a[14:15],v196 offset:8744
buffer_load_dwordx4 v220, s[32:35], s95 offen offset:2048 lds
MM v[48:51],a[16:19],a[208:211],v[48:51]
s_add_u32 s95, s88, 68608
v_sub_f32 v155, v155, v174
v_sub_f32 v156, v156, v174
MM v[48:51],a[20:23],a[212:215],v[48:51]
buffer_load_dwordx4 v221, s[32:35], s95 offen offset:3072 lds
MM v[56:59],a[20:23],a[220:223],v[56:59]
s_add_u32 s87, s89, s85
s_add_u32 s85, s85, 524288
v_sub_f32 v157, v157, v174
MM v[56:59],a[16:19],a[216:219],v[56:59]
s_cmp_ge_u32 s85, 5242880
s_cselect_b32 s85, 0, s85
s_add_u32 m0, s96, 65536
MM v[36:39],a[24:27],a[192:195],v[36:39]
TR a[16:17],v197 offset:0
TR a[18:19],v197 offset:512
v_sub_f32 v158, v158, v174
MM v[36:39],a[28:31],a[196:199],v[36:39]
TR a[20:21],v197 offset:8224
TR a[22:23],v197 offset:8736
MM v[44:47],a[28:31],a[204:207],v[44:47]
buffer_load_dwordx4 v253, s[16:19], s87 offen offset:0 lds
MM v[44:47],a[24:27],a[200:203],v[44:47]
s_add_u32 m0, s96, 66560
v_sub_f32 v159, v159, v174
v_sub_f32 v160, v160, v174
MM v[52:55],a[24:27],a[208:211],v[52:55]
v_sub_f32 v161, v161, v174
v_sub_f32 v162, v162, v174
MM v[52:55],a[28:31],a[212:215],v[52:55]
buffer_load_dwordx4 v254, s[16:19], s87 offen offset:0 lds
MM v[60:63],a[28:31],a[220:223],v[60:63]
s_add_u32 s87, s87, 65536
s_add_u32 m0, s96, 67592
v_sub_f32 v163, v163, v174
MM v[60:63],a[24:27],a[216:219],v[60:63]
v_sub_f32 v164, v164, v174
v_sub_f32 v165, v165, v174
MM v[64:67],a[32:35],a[192:195],v[64:67]
TR a[24:25],v197 offset:8
TR a[26:27],v197 offset:520
MM v[64:67],a[36:39],a[196:199],v[64:67]
TR a[28:29],v197 offset:8232
TR a[30:31],v197 offset:8744
v_sub_f32 v166, v166, v174
MM v[72:75],a[36:39],a[204:207],v[72:75]
buffer_load_dwordx4 v253, s[16:19], s87 offen offset:0 lds
MM v[72:75],a[32:35],a[200:203],v[72:75]
s_add_u32 m0, s96, 68616
v_sub_f32 v167, v167, v174
CV v128,v148,v149
MM v[80:83],a[32:35],a[208:211],v[80:83]
CV v129,v150,v151
CV v130,v152,v153
MM v[80:83],a[36:39],a[212:215],v[80:83]
buffer_load_dwordx4 v254, s[16:19], s87 offen offset:0 lds
MM v[88:91],a[36:39],a[220:223],v[88:91]
s_waitcnt vmcnt(17)
RD a[224:227],v222 offset:32768
CV v131,v154,v155
MM v[88:91],a[32:35],a[216:219],v[88:91]
RD a[228:231],v223 offset:32768
CV v132,v156,v157
MM v[68:71],a[40:43],a[192:195],v[68:71]
TR a[32:33],v198 offset:0
TR a[34:35],v198 offset:512
MM v[68:71],a[44:47],a[196:199],v[68:71]
TR a[36:37],v198 offset:8224
TR a[38:39],v198 offset:8736
CV v133,v158,v159
MM v[76:79],a[44:47],a[204:207],v[76:79]
RD a[232:235],v222 offset:34816
CV v134,v160,v161
MM v[76:79],a[40:43],a[200:203],v[76:79]
RD a[236:239],v223 offset:34816
CV v135,v162,v163
MM v[84:87],a[40:43],a[208:211],v[84:87]
RD a[240:243],v222 offset:36864
CV v136,v164,v165
MM v[84:87],a[44:47],a[212:215],v[84:87]
RD a[244:247],v223 offset:36864
CV v137,v166,v167
MM v[92:95],a[44:47],a[220:223],v[92:95]
RD a[248:251],v222 offset:38912
v_lshlrev_b32 v148, 16, v128
MM v[92:95],a[40:43],a[216:219],v[92:95]
s_waitcnt lgkmcnt(15)
RD a[252:255],v223 offset:38912
v_and_b32 v149, 0xffff0000, v128
MM v[96:99],a[48:51],a[192:195],v[96:99]
TR a[40:41],v198 offset:8
TR a[42:43],v198 offset:520
MM v[96:99],a[52:55],a[196:199],v[96:99]
TR a[44:45],v198 offset:8232
TR a[46:47],v198 offset:8744
v_lshlrev_b32 v150, 16, v129
MM v[104:107],a[52:55],a[204:207],v[104:107]
v_and_b32 v151, 0xffff0000, v129
v_lshlrev_b32 v152, 16, v130
MM v[104:107],a[48:51],a[200:203],v[104:107]
s_waitcnt lgkmcnt(4)
s_add_u32 s88, s68, s86
s_add_u32 s86, s86, 128
MM v[112:115],a[48:51],a[208:211],v[112:115]
s_cmp_ge_u32 s86, 1280
s_cselect_b32 s86, 0, s86
s_add_u32 m0, s94, 32768
v_and_b32 v153, 0xffff0000, v130
MM v[112:115],a[52:55],a[212:215],v[112:115]
s_add_u32 s95, s88, 0
buffer_load_dwordx4 v220, s[32:35], s95 offen offset:0 lds
MM v[120:123],a[52:55],a[220:223],v[120:123]
s_add_u32 s95, s88, 9216
v_lshlrev_b32 v154, 16, v131
v_and_b32 v155, 0xffff0000, v131
MM v[120:123],a[48:51],a[216:219],v[120:123]
buffer_load_dwordx4 v221, s[32:35], s95 offen offset:1024 lds
MM v[100:103],a[56:59],a[192:195],v[100:103]
s_add_u32 s95, s88, 18432
TR a[48:49],v199 offset:0
TR a[50:51],v199 offset:512
v_lshlrev_b32 v156, 16, v132
MM v[100:103],a[60:63],a[196:199],v[100:103]
TR a[52:53],v199 offset:8224
TR a[54:55],v199 offset:8736
MM v[108:111],a[60:63],a[204:207],v[108:111]
v_and_b32 v157, 0xffff0000, v132
v_lshlrev_b32 v158, 16, v133
MM v[108:111],a[56:59],a[200:203],v[108:111]
buffer_load_dwordx4 v220, s[32:35], s95 offen offset:2048 lds
MM v[116:119],a[56:59],a[208:211],v[116:119]
s_add_u32 s95, s88, 27648
v_and_b32 v159, 0xffff0000, v133
v_lshlrev_b32 v160, 16, v134
MM v[116:119],a[60:63],a[212:215],v[116:119]
v_and_b32 v161, 0xffff0000, v134
v_lshlrev_b32 v162, 16, v135
MM v[124:127],a[60:63],a[220:223],v[124:127]
v_and_b32 v163, 0xffff0000, v135
v_lshlrev_b32 v164, 16, v136
MM v[124:127],a[56:59],a[216:219],v[124:127]
buffer_load_dwordx4 v221, s[32:35], s95 offen offset:3072 lds
s_waitcnt vmcnt(17)
s_barrier
MM v[0:3],a[0:3],a[224:227],v[0:3]
s_sub_u32 s88, s88, s63
TR a[56:57],v199 offset:8
TR a[58:59],v199 offset:520
v_and_b32 v165, 0xffff0000, v136
MM v[0:3],a[4:7],a[228:231],v[0:3]
s_add_u32 m0, s94, 36864
TR a[60:61],v199 offset:8232
TR a[62:63],v199 offset:8744
MM v[8:11],a[4:7],a[236:239],v[8:11]
s_add_u32 s95, s88, 40960
v_lshlrev_b32 v166, 16, v137
v_and_b32 v167, 0xffff0000, v137
MM v[8:11],a[0:3],a[232:235],v[8:11]
buffer_load_dwordx4 v220, s[32:35], s95 offen offset:0 lds
MM v[16:19],a[0:3],a[240:243],v[16:19]
s_add_u32 s95, s88, 50176
v_mul_f32 v148, v148, v175
v_mul_f32 v149, v149, v175
MM v[16:19],a[4:7],a[244:247],v[16:19]
v_mul_f32 v150, v150, v175
v_mul_f32 v151, v151, v175
MM v[24:27],a[4:7],a[252:255],v[24:27]
v_mul_f32 v152, v152, v175
v_mul_f32 v153, v153, v175
MM v[24:27],a[0:3],a[248:251],v[24:27]
buffer_load_dwordx4 v221, s[32:35], s95 offen offset:1024 lds
MM v[4:7],a[8:11],a[224:227],v[4:7]
s_add_u32 s95, s88, 59392
v_mul_f32 v154, v154, v175
v_mul_f32 v155, v155, v175
MM v[4:7],a[12:15],a[228:231],v[4:7]
v_mul_f32 v156, v156, v175
v_mul_f32 v157, v157, v175
MM v[12:15],a[12:15],a[236:239],v[12:15]
v_mul_f32 v158, v158, v175
v_mul_f32 v159, v159, v175
MM v[12:15],a[8:11],a[232:235],v[12:15]
buffer_load_dwordx4 v220, s[32:35], s95 offen offset:2048 lds
MM v[20:23],a[8:11],a[240:243],v[20:23]
s_add_u32 s95, s88, 68608
v_mul_f32 v160, v160, v175
v_mul_f32 v161, v161, v175
MM v[20:23],a[12:15],a[244:247],v[20:23]
v_mul_f32 v162, v162, v175
v_mul_f32 v163, v163, v175
MM v[28:31],a[12:15],a[252:255],v[28:31]
v_mul_f32 v164, v164, v175
v_mul_f32 v165, v165, v175
MM v[28:31],a[8:11],a[248:251],v[28:31]
buffer_load_dwordx4 v221, s[32:35], s95 offen offset:3072 lds
v_smfmac_f32_16x16x64_bf16 v[184:187], v[236:239], a[0:7], v234
s_add_u32 s87, s89, s85
s_add_u32 s85, s85, 524288
v_mul_f32 v166, v166, v175
v_smfmac_f32_16x16x64_bf16 v[188:191], v[236:239], a[8:15], v234
s_cmp_ge_u32 s85, 5242880
s_cselect_b32 s85, 0, s85
MM v[32:35],a[16:19],a[224:227],v[32:35]
s_add_u32 m0, s96, 81984
TR a[0:1],v196 offset:16448
TR a[2:3],v196 offset:16960
MM v[32:35],a[20:23],a[228:231],v[32:35]
TR a[4:5],v196 offset:24672
TR a[6:7],v196 offset:25184
v_mul_f32 v167, v167, v175
MM v[40:43],a[20:23],a[236:239],v[40:43]
TR a[8:9],v196 offset:16456
TR a[10:11],v196 offset:16968
MM v[40:43],a[16:19],a[232:235],v[40:43]
TR a[12:13],v196 offset:24680
TR a[14:15],v196 offset:25192
buffer_load_dwordx4 v253, s[16:19], s87 offen offset:0 lds
MM v[48:51],a[16:19],a[240:243],v[48:51]
s_add_u32 m0, s96, 83008
CV v128,v148,v149
CV v129,v150,v151
MM v[48:51],a[20:23],a[244:247],v[48:51]
buffer_load_dwordx4 v254, s[16:19], s87 offen offset:0 lds
MM v[56:59],a[20:23],a[252:255],v[56:59]
s_add_u32 s87, s87, 65536
s_add_u32 m0, s96, 84040
CV v130,v152,v153
MM v[56:59],a[16:19],a[248:251],v[56:59]
CV v131,v154,v155
CV v132,v156,v157
MM v[36:39],a[24:27],a[224:227],v[36:39]
TR a[16:17],v197 offset:16448
TR a[18:19],v197 offset:16960
MM v[36:39],a[28:31],a[228:231],v[36:39]
TR a[20:21],v197 offset:24672
TR a[22:23],v197 offset:25184
CV v133,v158,v159
MM v[44:47],a[28:31],a[236:239],v[44:47]
buffer_load_dwordx4 v253, s[16:19], s87 offen offset:0 lds
MM v[44:47],a[24:27],a[232:235],v[44:47]
s_add_u32 m0, s96, 85064
CV v134,v160,v161
CV v135,v162,v163
MM v[52:55],a[24:27],a[240:243],v[52:55]
CV v136,v164,v165
CV v137,v166,v167
MM v[52:55],a[28:31],a[244:247],v[52:55]
buffer_load_dwordx4 v254, s[16:19], s87 offen offset:0 lds
MM v[60:63],a[28:31],a[252:255],v[60:63]
s_waitcnt vmcnt(16)
RD a[128:131],v222 offset:0
v_xor_b32 v233, 32, v232
MM v[60:63],a[24:27],a[248:251],v[60:63]
s_add_u32 s77, %[slds], 65536
)ASM"
R"ASM(
RD a[132:135],v223 offset:0
v_add_u32 v232, s77, v232
MM v[64:67],a[32:35],a[224:227],v[64:67]
TR a[24:25],v197 offset:16456
TR a[26:27],v197 offset:16968
MM v[64:67],a[36:39],a[228:231],v[64:67]
TR a[28:29],v197 offset:24680
TR a[30:31],v197 offset:25192
MM v[72:75],a[36:39],a[236:239],v[72:75]
RD a[136:139],v222 offset:2048
v_add_u32 v233, s77, v233
MM v[72:75],a[32:35],a[232:235],v[72:75]
RD a[140:143],v223 offset:2048
buffer_store_dwordx4 v[128:131], v168, s[20:23], s82 offen offset:0 sc1
v_lshlrev_b32 v214, 8, v211
MM v[80:83],a[32:35],a[240:243],v[80:83]
RD a[144:147],v222 offset:4096
v_add_u32 v214, %[slds], v214
MM v[80:83],a[36:39],a[244:247],v[80:83]
RD a[148:151],v223 offset:4096
v_xor_b32 v215, v211, v212
MM v[88:91],a[36:39],a[252:255],v[88:91]
s_lshl_b32 s77, %[swave], 2
RD a[152:155],v222 offset:6144
v_xor_b32 v213, s77, v215
MM v[88:91],a[32:35],a[248:251],v[88:91]
s_waitcnt lgkmcnt(15)
RD a[156:159],v223 offset:6144
buffer_store_dwordx4 v[132:135], v168, s[20:23], s82 offen offset:512 sc1
v_xor_b32 v216, 0, v213
MM v[68:71],a[40:43],a[224:227],v[68:71]
TR a[32:33],v198 offset:16448
TR a[34:35],v198 offset:16960
MM v[68:71],a[44:47],a[228:231],v[68:71]
TR a[36:37],v198 offset:24672
TR a[38:39],v198 offset:25184
MM v[76:79],a[44:47],a[236:239],v[76:79]
buffer_store_dwordx2 v[136:137], v181, s[20:23], s82 offen offset:1024 sc1
MM v[76:79],a[40:43],a[232:235],v[76:79]
s_waitcnt lgkmcnt(4)
s_add_u32 s88, s68, s86
s_add_u32 s86, s86, 128
MM v[84:87],a[40:43],a[240:243],v[84:87]
s_cmp_ge_u32 s86, 1280
s_cselect_b32 s86, 0, s86
MM v[84:87],a[44:47],a[244:247],v[84:87]
s_add_u32 m0, s94, 0
s_add_u32 s95, s88, 0
v_lshlrev_b32 v148, 16, v128
MM v[92:95],a[44:47],a[252:255],v[92:95]
buffer_load_dwordx4 v220, s[32:35], s95 offen offset:0 lds
MM v[92:95],a[40:43],a[248:251],v[92:95]
s_add_u32 s95, s88, 9216
v_and_b32 v149, 0xffff0000, v128
v_lshlrev_b32 v176, 16, v138
MM v[96:99],a[48:51],a[224:227],v[96:99]
TR a[40:41],v198 offset:16456
TR a[42:43],v198 offset:16968
MM v[96:99],a[52:55],a[228:231],v[96:99]
TR a[44:45],v198 offset:24680
TR a[46:47],v198 offset:25192
v_and_b32 v177, 0xffff0000, v138
MM v[104:107],a[52:55],a[236:239],v[104:107]
buffer_load_dwordx4 v221, s[32:35], s95 offen offset:1024 lds
MM v[104:107],a[48:51],a[232:235],v[104:107]
s_add_u32 s95, s88, 18432
v_mul_f32 v170, v148, v176
v_mul_f32 v171, v149, v177
MM v[112:115],a[48:51],a[240:243],v[112:115]
buffer_load_dwordx4 v220, s[32:35], s95 offen offset:2048 lds
MM v[112:115],a[52:55],a[244:247],v[112:115]
s_add_u32 s95, s88, 27648
v_lshlrev_b32 v148, 16, v129
v_and_b32 v149, 0xffff0000, v129
MM v[120:123],a[52:55],a[252:255],v[120:123]
v_lshlrev_b32 v176, 16, v139
v_and_b32 v177, 0xffff0000, v139
MM v[120:123],a[48:51],a[248:251],v[120:123]
v_fma_f32 v170, v148, v176, v170
v_fma_f32 v171, v149, v177, v171
MM v[100:103],a[56:59],a[224:227],v[100:103]
TR a[48:49],v199 offset:16448
TR a[50:51],v199 offset:16960
MM v[100:103],a[60:63],a[228:231],v[100:103]
TR a[52:53],v199 offset:24672
TR a[54:55],v199 offset:25184
v_lshlrev_b32 v148, 16, v130
MM v[108:111],a[60:63],a[236:239],v[108:111]
buffer_load_dwordx4 v221, s[32:35], s95 offen offset:3072 lds
MM v[108:111],a[56:59],a[232:235],v[108:111]
s_sub_u32 s88, s88, s63
s_add_u32 m0, s94, 4096
s_add_u32 s95, s88, 40960
MM v[116:119],a[56:59],a[240:243],v[116:119]
buffer_load_dwordx4 v220, s[32:35], s95 offen offset:0 lds
LPG2_%=:
s_cbranch_execz LPG6_%=
v_and_b32 v149, 0xffff0000, v130
MM v[116:119],a[60:63],a[244:247],v[116:119]
s_add_u32 s95, s88, 50176
v_lshlrev_b32 v176, 16, v140
v_and_b32 v177, 0xffff0000, v140
MM v[124:127],a[60:63],a[252:255],v[124:127]
v_fma_f32 v170, v148, v176, v170
v_fma_f32 v171, v149, v177, v171
MM v[124:127],a[56:59],a[248:251],v[124:127]
v_lshlrev_b32 v148, 16, v131
v_and_b32 v149, 0xffff0000, v131
s_waitcnt vmcnt(20)
s_barrier
MM v[0:3],a[0:3],a[128:131],v[0:3]
TR a[56:57],v199 offset:16456
TR a[58:59],v199 offset:16968
MM v[0:3],a[4:7],a[132:135],v[0:3]
TR a[60:61],v199 offset:24680
TR a[62:63],v199 offset:25192
v_lshlrev_b32 v176, 16, v141
MM v[8:11],a[4:7],a[140:143],v[8:11]
buffer_load_dwordx4 v221, s[32:35], s95 offen offset:1024 lds
MM v[8:11],a[0:3],a[136:139],v[8:11]
s_add_u32 s95, s88, 59392
v_and_b32 v177, 0xffff0000, v141
v_fma_f32 v170, v148, v176, v170
MM v[16:19],a[0:3],a[144:147],v[16:19]
buffer_load_dwordx4 v220, s[32:35], s95 offen offset:2048 lds
MM v[16:19],a[4:7],a[148:151],v[16:19]
s_add_u32 s95, s88, 68608
v_fma_f32 v171, v149, v177, v171
v_lshlrev_b32 v148, 16, v132
MM v[24:27],a[4:7],a[156:159],v[24:27]
v_and_b32 v149, 0xffff0000, v132
v_lshlrev_b32 v176, 16, v142
MM v[24:27],a[0:3],a[152:155],v[24:27]
v_and_b32 v177, 0xffff0000, v142
v_fma_f32 v170, v148, v176, v170
MM v[4:7],a[8:11],a[128:131],v[4:7]
buffer_load_dwordx4 v221, s[32:35], s95 offen offset:3072 lds
MM v[4:7],a[12:15],a[132:135],v[4:7]
s_add_u32 s87, s89, s85
s_add_u32 s85, s85, 524288
v_fma_f32 v171, v149, v177, v171
MM v[12:15],a[12:15],a[140:143],v[12:15]
s_cmp_ge_u32 s85, 5242880
s_cselect_b32 s85, 0, s85
s_add_u32 m0, s96, 98432
MM v[12:15],a[8:11],a[136:139],v[12:15]
v_lshlrev_b32 v148, 16, v133
v_and_b32 v149, 0xffff0000, v133
MM v[20:23],a[8:11],a[144:147],v[20:23]
buffer_load_dwordx4 v253, s[16:19], s87 offen offset:0 lds
MM v[20:23],a[12:15],a[148:151],v[20:23]
s_add_u32 m0, s96, 99456
v_lshlrev_b32 v176, 16, v143
v_and_b32 v177, 0xffff0000, v143
MM v[28:31],a[12:15],a[156:159],v[28:31]
v_fma_f32 v170, v148, v176, v170
v_fma_f32 v171, v149, v177, v171
MM v[28:31],a[8:11],a[152:155],v[28:31]
v_lshlrev_b32 v148, 16, v134
v_and_b32 v149, 0xffff0000, v134
v_smfmac_f32_16x16x64_bf16 v[184:187], v[236:239], a[0:7], v234
v_lshlrev_b32 v176, 16, v144
v_smfmac_f32_16x16x64_bf16 v[188:191], v[236:239], a[8:15], v234
v_and_b32 v177, 0xffff0000, v144
MM v[32:35],a[16:19],a[128:131],v[32:35]
TR a[0:1],v192 offset:0
TR a[2:3],v192 offset:512
buffer_load_dwordx4 v254, s[16:19], s87 offen offset:0 lds
MM v[32:35],a[20:23],a[132:135],v[32:35]
s_add_u32 s87, s87, 65536
TR a[4:5],v192 offset:8224
TR a[6:7],v192 offset:8736
v_fma_f32 v170, v148, v176, v170
MM v[40:43],a[20:23],a[140:143],v[40:43]
s_add_u32 m0, s96, 100488
TR a[8:9],v192 offset:8
TR a[10:11],v192 offset:520
MM v[40:43],a[16:19],a[136:139],v[40:43]
TR a[12:13],v192 offset:8232
TR a[14:15],v192 offset:8744
MM v[48:51],a[16:19],a[144:147],v[48:51]
buffer_load_dwordx4 v253, s[16:19], s87 offen offset:0 lds
v_fma_f32 v171, v149, v177, v171
MM v[48:51],a[20:23],a[148:151],v[48:51]
s_add_u32 m0, s96, 101512
v_lshlrev_b32 v148, 16, v135
v_and_b32 v149, 0xffff0000, v135
MM v[56:59],a[20:23],a[156:159],v[56:59]
buffer_load_dwordx4 v254, s[16:19], s87 offen offset:0 lds
MM v[56:59],a[16:19],a[152:155],v[56:59]
s_waitcnt vmcnt(19)
RD a[160:163],v222 offset:32768
v_lshlrev_b32 v176, 16, v145
MM v[36:39],a[24:27],a[128:131],v[36:39]
TR a[16:17],v193 offset:0
TR a[18:19],v193 offset:512
MM v[36:39],a[28:31],a[132:135],v[36:39]
TR a[20:21],v193 offset:8224
TR a[22:23],v193 offset:8736
v_and_b32 v177, 0xffff0000, v145
MM v[44:47],a[28:31],a[140:143],v[44:47]
RD a[164:167],v223 offset:32768
v_fma_f32 v170, v148, v176, v170
MM v[44:47],a[24:27],a[136:139],v[44:47]
RD a[168:171],v222 offset:34816
v_fma_f32 v171, v149, v177, v171
MM v[52:55],a[24:27],a[144:147],v[52:55]
RD a[172:175],v223 offset:34816
v_lshlrev_b32 v148, 16, v136
MM v[52:55],a[28:31],a[148:151],v[52:55]
RD a[176:179],v222 offset:36864
v_and_b32 v149, 0xffff0000, v136
MM v[60:63],a[28:31],a[156:159],v[60:63]
RD a[180:183],v223 offset:36864
v_lshlrev_b32 v176, 16, v146
MM v[60:63],a[24:27],a[152:155],v[60:63]
s_waitcnt lgkmcnt(15)
RD a[184:187],v222 offset:38912
v_and_b32 v177, 0xffff0000, v146
MM v[64:67],a[32:35],a[128:131],v[64:67]
TR a[24:25],v193 offset:8
TR a[26:27],v193 offset:520
MM v[64:67],a[36:39],a[132:135],v[64:67]
TR a[28:29],v193 offset:8232
TR a[30:31],v193 offset:8744
v_fma_f32 v170, v148, v176, v170
MM v[72:75],a[36:39],a[140:143],v[72:75]
RD a[188:191],v223 offset:38912
v_fma_f32 v171, v149, v177, v171
MM v[72:75],a[32:35],a[136:139],v[72:75]
v_lshlrev_b32 v148, 16, v137
v_and_b32 v149, 0xffff0000, v137
MM v[80:83],a[32:35],a[144:147],v[80:83]
v_lshlrev_b32 v176, 16, v147
v_and_b32 v177, 0xffff0000, v147
MM v[80:83],a[36:39],a[148:151],v[80:83]
v_fma_f32 v170, v148, v176, v170
v_fma_f32 v171, v149, v177, v171
MM v[88:91],a[36:39],a[156:159],v[88:91]
s_waitcnt lgkmcnt(0)
s_add_u32 s88, s68, s86
s_add_u32 s86, s86, 128
MM v[88:91],a[32:35],a[152:155],v[88:91]
s_cmp_ge_u32 s86, 1280
s_cselect_b32 s86, 0, s86
s_add_u32 m0, s94, 32768
v_add_f32 v170, v170, v171
MM v[68:71],a[40:43],a[128:131],v[68:71]
s_add_u32 s95, s88, 0
TR a[32:33],v194 offset:0
TR a[34:35],v194 offset:512
MM v[68:71],a[44:47],a[132:135],v[68:71]
TR a[36:37],v194 offset:8224
TR a[38:39],v194 offset:8736
MM v[76:79],a[44:47],a[140:143],v[76:79]
buffer_load_dwordx4 v220, s[32:35], s95 offen offset:0 lds
v_add_f32_dpp v170, v170, v170 row_ror:8 row_mask:0xf bank_mask:0xf
MM v[76:79],a[40:43],a[136:139],v[76:79]
s_add_u32 s95, s88, 9216
v_add_f32_dpp v170, v170, v170 row_ror:4 row_mask:0xf bank_mask:0xf
v_lshl_add_u32 v224, v216, 4, v214
MM v[84:87],a[40:43],a[144:147],v[84:87]
buffer_load_dwordx4 v221, s[32:35], s95 offen offset:1024 lds
MM v[84:87],a[44:47],a[148:151],v[84:87]
s_add_u32 s95, s88, 18432
v_add_f32_dpp v170, v170, v170 row_ror:2 row_mask:0xf bank_mask:0xf
v_xor_b32 v216, 4, v213
MM v[92:95],a[44:47],a[156:159],v[92:95]
v_add_f32_dpp v170, v170, v170 row_ror:1 row_mask:0xf bank_mask:0xf
v_mov_b32 v173, v170
MM v[92:95],a[40:43],a[152:155],v[92:95]
s_nop 0
v_permlane16_swap_b32 v170, v173
v_add_f32 v170, v170, v173
MM v[96:99],a[48:51],a[128:131],v[96:99]
TR a[40:41],v194 offset:8
TR a[42:43],v194 offset:520
MM v[96:99],a[52:55],a[132:135],v[96:99]
TR a[44:45],v194 offset:8232
TR a[46:47],v194 offset:8744
CV v170,v170,v170
MM v[104:107],a[52:55],a[140:143],v[104:107]
buffer_load_dwordx4 v220, s[32:35], s95 offen offset:2048 lds
MM v[104:107],a[48:51],a[136:139],v[104:107]
s_add_u32 s95, s88, 27648
v_ashrrev_i32 v171, 31, v170
v_or_b32 v171, 0x80000000, v171
MM v[112:115],a[48:51],a[144:147],v[112:115]
buffer_load_dwordx4 v221, s[32:35], s95 offen offset:3072 lds
MM v[112:115],a[52:55],a[148:151],v[112:115]
s_sub_u32 s88, s88, s63
s_add_u32 m0, s94, 36864
s_add_u32 s95, s88, 40960
v_xor_b32 v170, v170, v171
MM v[120:123],a[52:55],a[156:159],v[120:123]
v_and_b32 v170, 0xffff0000, v170
v_sub_u32 v171, s79, v180
MM v[120:123],a[48:51],a[152:155],v[120:123]
v_or_b32 v170, v170, v171
v_lshlrev_b32 v173, 2, v180
MM v[100:103],a[56:59],a[128:131],v[100:103]
TR a[48:49],v195 offset:0
TR a[50:51],v195 offset:512
MM v[100:103],a[60:63],a[132:135],v[100:103]
TR a[52:53],v195 offset:8224
TR a[54:55],v195 offset:8736
MM v[108:111],a[60:63],a[140:143],v[108:111]
buffer_load_dwordx4 v220, s[32:35], s95 offen offset:0 lds
s_mov_b32 exec_lo, 1
s_mov_b32 exec_hi, 1
buffer_store_dword v170, v173, s[20:23], s78 offen offset:0 sc1
s_mov_b64 exec, -1
MM v[108:111],a[56:59],a[136:139],v[108:111]
s_add_u32 s95, s88, 50176
v_mov_b32 v171, s69
v_lshl_add_u32 v225, v216, 4, v214
MM v[116:119],a[56:59],a[144:147],v[116:119]
buffer_load_dwordx4 v221, s[32:35], s95 offen offset:1024 lds
MM v[116:119],a[60:63],a[148:151],v[116:119]
s_add_u32 s95, s88, 59392
v_xor_b32 v216, 8, v213
v_lshl_add_u32 v226, v216, 4, v214
MM v[124:127],a[60:63],a[156:159],v[124:127]
v_xor_b32 v216, 12, v213
v_lshl_add_u32 v227, v216, 4, v214
MM v[124:127],a[56:59],a[152:155],v[124:127]
v_lshlrev_b32 v214, 8, v212
v_xor_b32 v216, 0, v215
s_waitcnt vmcnt(22)
s_barrier
MM v[0:3],a[0:3],a[160:163],v[0:3]
TR a[56:57],v195 offset:8
TR a[58:59],v195 offset:520
MM v[0:3],a[4:7],a[164:167],v[0:3]
TR a[60:61],v195 offset:8232
TR a[62:63],v195 offset:8744
MM v[8:11],a[4:7],a[172:175],v[8:11]
buffer_load_dwordx4 v220, s[32:35], s95 offen offset:2048 lds
MM v[8:11],a[0:3],a[168:171],v[8:11]
s_add_u32 s95, s88, 68608
v_lshl_add_u32 v228, v216, 4, v214
v_xor_b32 v216, 4, v215
MM v[16:19],a[0:3],a[176:179],v[16:19]
)ASM"
R"ASM(
buffer_load_dwordx4 v221, s[32:35], s95 offen offset:3072 lds
MM v[16:19],a[4:7],a[180:183],v[16:19]
s_add_u32 s87, s89, s85
s_add_u32 s85, s85, 524288
v_lshl_add_u32 v229, v216, 4, v214
MM v[24:27],a[4:7],a[188:191],v[24:27]
s_cmp_ge_u32 s85, 5242880
s_cselect_b32 s85, 0, s85
s_add_u32 m0, s96, 114880
MM v[24:27],a[0:3],a[184:187],v[24:27]
v_xor_b32 v216, 8, v215
v_lshl_add_u32 v230, v216, 4, v214
MM v[4:7],a[8:11],a[160:163],v[4:7]
buffer_load_dwordx4 v253, s[16:19], s87 offen offset:0 lds
MM v[4:7],a[12:15],a[164:167],v[4:7]
s_add_u32 m0, s96, 115904
v_xor_b32 v216, 12, v215
v_lshl_add_u32 v231, v216, 4, v214
MM v[12:15],a[12:15],a[172:175],v[12:15]
s_lshl_b32 s90, s49, 19
s_add_u32 s8, s8, s90
s_addc_u32 s9, s9, 0
MM v[12:15],a[8:11],a[168:171],v[12:15]
s_mov_b32 s10, 524288
s_mov_b32 s11, 131072
MM v[20:23],a[8:11],a[176:179],v[20:23]
buffer_load_dwordx4 v254, s[16:19], s87 offen offset:0 lds
MM v[20:23],a[12:15],a[180:183],v[20:23]
s_add_u32 s87, s87, 65536
s_add_u32 s12, s12, s90
s_addc_u32 s13, s13, 0
MM v[28:31],a[12:15],a[188:191],v[28:31]
s_add_u32 m0, s96, 116936
s_mov_b32 s14, 525312
s_mov_b32 s15, 131072
MM v[28:31],a[8:11],a[184:187],v[28:31]
s_sub_u32 s12, s12, 1024
s_subb_u32 s13, s13, 0
s_lshr_b32 s90, s49, 3
v_smfmac_f32_16x16x64_bf16 v[184:187], v[236:239], a[0:7], v234
s_mul_i32 s90, s90, 1310720
s_mul_i32 s77, s84, 320
v_smfmac_f32_16x16x64_bf16 v[188:191], v[236:239], a[8:15], v234
s_add_u32 s90, s90, s77
MM v[32:35],a[16:19],a[160:163],v[32:35]
TR a[0:1],v192 offset:16448
TR a[2:3],v192 offset:16960
buffer_load_dwordx4 v253, s[16:19], s87 offen offset:0 lds
MM v[32:35],a[20:23],a[164:167],v[32:35]
s_add_u32 m0, s96, 117960
TR a[4:5],v192 offset:24672
TR a[6:7],v192 offset:25184
MM v[40:43],a[20:23],a[172:175],v[40:43]
TR a[8:9],v192 offset:16456
TR a[10:11],v192 offset:16968
MM v[40:43],a[16:19],a[168:171],v[40:43]
TR a[12:13],v192 offset:24680
TR a[14:15],v192 offset:25192
MM v[48:51],a[16:19],a[176:179],v[48:51]
buffer_load_dwordx4 v254, s[16:19], s87 offen offset:0 lds
MM v[48:51],a[20:23],a[180:183],v[48:51]
s_waitcnt vmcnt(17)
s_add_u32 s24, s24, s90
s_addc_u32 s25, s25, 0
MM v[56:59],a[20:23],a[188:191],v[56:59]
s_sub_u32 s26, 5242880, s90
RD a[192:195],v222 offset:0
s_mov_b32 s27, 131072
MM v[56:59],a[16:19],a[184:187],v[56:59]
s_mov_b32 s38, 2211840
RD a[196:199],v223 offset:0
s_mov_b32 s39, 131072
MM v[36:39],a[24:27],a[160:163],v[36:39]
s_mov_b32 s71, 0x4e800000
TR a[16:17],v193 offset:16448
TR a[18:19],v193 offset:16960
MM v[36:39],a[28:31],a[164:167],v[36:39]
s_mov_b32 s72, 0x3e0293ee
TR a[20:21],v193 offset:24672
TR a[22:23],v193 offset:25184
MM v[44:47],a[28:31],a[172:175],v[44:47]
s_lshl_b32 s64, %[swave], 12
RD a[200:203],v222 offset:2048
MM v[44:47],a[24:27],a[168:171],v[44:47]
RD a[204:207],v223 offset:2048
MM v[52:55],a[24:27],a[176:179],v[52:55]
RD a[208:211],v222 offset:4096
MM v[52:55],a[28:31],a[180:183],v[52:55]
RD a[212:215],v223 offset:4096
MM v[60:63],a[28:31],a[188:191],v[60:63]
RD a[216:219],v222 offset:6144
MM v[60:63],a[24:27],a[184:187],v[60:63]
s_waitcnt lgkmcnt(15)
RD a[220:223],v223 offset:6144
MM v[64:67],a[32:35],a[160:163],v[64:67]
TR a[24:25],v193 offset:16456
TR a[26:27],v193 offset:16968
MM v[64:67],a[36:39],a[164:167],v[64:67]
TR a[28:29],v193 offset:24680
TR a[30:31],v193 offset:25192
MM v[72:75],a[36:39],a[172:175],v[72:75]
MM v[72:75],a[32:35],a[168:171],v[72:75]
s_waitcnt lgkmcnt(4)
s_add_u32 s88, s68, s86
s_add_u32 s86, s86, 128
MM v[80:83],a[32:35],a[176:179],v[80:83]
s_cmp_ge_u32 s86, 1280
s_cselect_b32 s86, 0, s86
s_add_u32 m0, s94, 0
MM v[80:83],a[36:39],a[180:183],v[80:83]
s_add_u32 s95, s88, 0
buffer_load_dwordx4 v220, s[32:35], s95 offen offset:0 lds
MM v[88:91],a[36:39],a[188:191],v[88:91]
s_add_u32 s95, s88, 9216
MM v[88:91],a[32:35],a[184:187],v[88:91]
buffer_load_dwordx4 v221, s[32:35], s95 offen offset:1024 lds
MM v[68:71],a[40:43],a[160:163],v[68:71]
s_add_u32 s95, s88, 18432
TR a[32:33],v194 offset:16448
TR a[34:35],v194 offset:16960
MM v[68:71],a[44:47],a[164:167],v[68:71]
TR a[36:37],v194 offset:24672
TR a[38:39],v194 offset:25184
MM v[76:79],a[44:47],a[172:175],v[76:79]
MM v[76:79],a[40:43],a[168:171],v[76:79]
buffer_load_dwordx4 v220, s[32:35], s95 offen offset:2048 lds
MM v[84:87],a[40:43],a[176:179],v[84:87]
s_add_u32 s95, s88, 27648
MM v[84:87],a[44:47],a[180:183],v[84:87]
MM v[92:95],a[44:47],a[188:191],v[92:95]
MM v[92:95],a[40:43],a[184:187],v[92:95]
buffer_load_dwordx4 v221, s[32:35], s95 offen offset:3072 lds
MM v[96:99],a[48:51],a[160:163],v[96:99]
s_sub_u32 s88, s88, s63
TR a[40:41],v194 offset:16456
TR a[42:43],v194 offset:16968
MM v[96:99],a[52:55],a[164:167],v[96:99]
s_add_u32 m0, s94, 4096
TR a[44:45],v194 offset:24680
TR a[46:47],v194 offset:25192
MM v[104:107],a[52:55],a[172:175],v[104:107]
s_add_u32 s95, s88, 40960
MM v[104:107],a[48:51],a[168:171],v[104:107]
buffer_load_dwordx4 v220, s[32:35], s95 offen offset:0 lds
MM v[112:115],a[48:51],a[176:179],v[112:115]
s_add_u32 s95, s88, 50176
MM v[112:115],a[52:55],a[180:183],v[112:115]
MM v[120:123],a[52:55],a[188:191],v[120:123]
MM v[120:123],a[48:51],a[184:187],v[120:123]
buffer_load_dwordx4 v221, s[32:35], s95 offen offset:1024 lds
MM v[100:103],a[56:59],a[160:163],v[100:103]
s_add_u32 s95, s88, 59392
TR a[48:49],v195 offset:16448
TR a[50:51],v195 offset:16960
MM v[100:103],a[60:63],a[164:167],v[100:103]
TR a[52:53],v195 offset:24672
TR a[54:55],v195 offset:25184
MM v[108:111],a[60:63],a[172:175],v[108:111]
MM v[108:111],a[56:59],a[168:171],v[108:111]
buffer_load_dwordx4 v220, s[32:35], s95 offen offset:2048 lds
MM v[116:119],a[56:59],a[176:179],v[116:119]
s_add_u32 s95, s88, 68608
MM v[116:119],a[60:63],a[180:183],v[116:119]
MM v[124:127],a[60:63],a[188:191],v[124:127]
MM v[124:127],a[56:59],a[184:187],v[124:127]
buffer_load_dwordx4 v221, s[32:35], s95 offen offset:3072 lds
s_waitcnt vmcnt(21)
s_barrier
MM v[0:3],a[0:3],a[192:195],v[0:3]
s_add_u32 s87, s89, s85
TR a[56:57],v195 offset:16456
TR a[58:59],v195 offset:16968
MM v[0:3],a[4:7],a[196:199],v[0:3]
s_add_u32 s85, s85, 524288
TR a[60:61],v195 offset:24680
TR a[62:63],v195 offset:25192
MM v[8:11],a[4:7],a[204:207],v[8:11]
s_cmp_ge_u32 s85, 5242880
s_cselect_b32 s85, 0, s85
s_add_u32 m0, s96, 131328
v_and_b32 v208, 240, v248
MM v[8:11],a[0:3],a[200:203],v[8:11]
s_lshl_b32 s62, s62, 8
buffer_load_dwordx4 v253, s[16:19], s87 offen offset:0 lds
MM v[16:19],a[0:3],a[208:211],v[16:19]
s_add_u32 m0, s96, 132352
s_add_u32 s93, s93, 36864
s_add_u32 s62, s62, s93
MM v[16:19],a[4:7],a[212:215],v[16:19]
s_xor_b32 s93, s62, 256
MM v[24:27],a[4:7],a[220:223],v[24:27]
MM v[24:27],a[0:3],a[216:219],v[24:27]
buffer_load_dwordx4 v254, s[16:19], s87 offen offset:0 lds
MM v[4:7],a[8:11],a[192:195],v[4:7]
s_add_u32 s87, s87, 65536
s_add_u32 m0, s96, 133384
MM v[4:7],a[12:15],a[196:199],v[4:7]
MM v[12:15],a[12:15],a[204:207],v[12:15]
MM v[12:15],a[8:11],a[200:203],v[12:15]
buffer_load_dwordx4 v253, s[16:19], s87 offen offset:0 lds
MM v[20:23],a[8:11],a[208:211],v[20:23]
s_add_u32 m0, s96, 134408
MM v[20:23],a[12:15],a[212:215],v[20:23]
MM v[28:31],a[12:15],a[220:223],v[28:31]
MM v[28:31],a[8:11],a[216:219],v[28:31]
buffer_load_dwordx4 v254, s[16:19], s87 offen offset:0 lds
v_smfmac_f32_16x16x64_bf16 v[184:187], v[236:239], a[0:7], v234
s_waitcnt vmcnt(16)
RD a[224:227],v222 offset:32768
v_smfmac_f32_16x16x64_bf16 v[188:191], v[236:239], a[8:15], v234
RD a[228:231],v223 offset:32768
MM v[32:35],a[16:19],a[192:195],v[32:35]
TR a[0:1],v192 offset:32896
TR a[2:3],v192 offset:33408
MM v[32:35],a[20:23],a[196:199],v[32:35]
TR a[4:5],v192 offset:41120
TR a[6:7],v192 offset:41632
MM v[40:43],a[20:23],a[204:207],v[40:43]
TR a[8:9],v192 offset:32904
TR a[10:11],v192 offset:33416
RD a[232:235],v222 offset:34816
MM v[40:43],a[16:19],a[200:203],v[40:43]
TR a[12:13],v192 offset:41128
TR a[14:15],v192 offset:41640
buffer_load_dwordx4 v[200:203], v208, s[20:23], s62 offen offset:0 sc1
MM v[48:51],a[16:19],a[208:211],v[48:51]
RD a[236:239],v223 offset:34816
MM v[48:51],a[20:23],a[212:215],v[48:51]
buffer_load_dwordx4 v[204:207], v208, s[20:23], s93 offen offset:0 sc1
MM v[56:59],a[20:23],a[220:223],v[56:59]
RD a[240:243],v222 offset:36864
MM v[56:59],a[16:19],a[216:219],v[56:59]
s_waitcnt lgkmcnt(15)
RD a[244:247],v223 offset:36864
MM v[36:39],a[24:27],a[192:195],v[36:39]
TR a[16:17],v193 offset:32896
TR a[18:19],v193 offset:33408
MM v[36:39],a[28:31],a[196:199],v[36:39]
TR a[20:21],v193 offset:41120
TR a[22:23],v193 offset:41632
MM v[44:47],a[28:31],a[204:207],v[44:47]
RD a[248:251],v222 offset:38912
MM v[44:47],a[24:27],a[200:203],v[44:47]
RD a[252:255],v223 offset:38912
MM v[52:55],a[24:27],a[208:211],v[52:55]
MM v[52:55],a[28:31],a[212:215],v[52:55]
MM v[60:63],a[28:31],a[220:223],v[60:63]
MM v[60:63],a[24:27],a[216:219],v[60:63]
s_waitcnt lgkmcnt(0)
s_add_u32 s88, s68, s86
s_add_u32 s86, s86, 128
MM v[64:67],a[32:35],a[192:195],v[64:67]
TR a[24:25],v193 offset:32904
TR a[26:27],v193 offset:33416
MM v[64:67],a[36:39],a[196:199],v[64:67]
TR a[28:29],v193 offset:41128
TR a[30:31],v193 offset:41640
s_cmp_ge_u32 s86, 1280
s_cselect_b32 s86, 0, s86
MM v[72:75],a[36:39],a[204:207],v[72:75]
s_add_u32 m0, s94, 32768
s_add_u32 s95, s88, 0
MM v[72:75],a[32:35],a[200:203],v[72:75]
buffer_load_dwordx4 v220, s[32:35], s95 offen offset:0 lds
MM v[80:83],a[32:35],a[208:211],v[80:83]
s_add_u32 s95, s88, 9216
MM v[80:83],a[36:39],a[212:215],v[80:83]
buffer_load_dwordx4 v221, s[32:35], s95 offen offset:1024 lds
MM v[88:91],a[36:39],a[220:223],v[88:91]
s_add_u32 s95, s88, 18432
MM v[88:91],a[32:35],a[216:219],v[88:91]
MM v[68:71],a[40:43],a[192:195],v[68:71]
TR a[32:33],v194 offset:32896
TR a[34:35],v194 offset:33408
MM v[68:71],a[44:47],a[196:199],v[68:71]
TR a[36:37],v194 offset:41120
TR a[38:39],v194 offset:41632
MM v[76:79],a[44:47],a[204:207],v[76:79]
buffer_load_dwordx4 v220, s[32:35], s95 offen offset:2048 lds
MM v[76:79],a[40:43],a[200:203],v[76:79]
s_add_u32 s95, s88, 27648
MM v[84:87],a[40:43],a[208:211],v[84:87]
MM v[84:87],a[44:47],a[212:215],v[84:87]
buffer_load_dwordx4 v221, s[32:35], s95 offen offset:3072 lds
MM v[92:95],a[44:47],a[220:223],v[92:95]
s_waitcnt vmcnt(10)
RD a[128:131],v222 offset:0
MM v[92:95],a[40:43],a[216:219],v[92:95]
RD a[132:135],v223 offset:0
MM v[96:99],a[48:51],a[192:195],v[96:99]
TR a[40:41],v194 offset:32904
TR a[42:43],v194 offset:33416
MM v[96:99],a[52:55],a[196:199],v[96:99]
TR a[44:45],v194 offset:41128
TR a[46:47],v194 offset:41640
MM v[104:107],a[52:55],a[204:207],v[104:107]
RD a[136:139],v222 offset:2048
MM v[104:107],a[48:51],a[200:203],v[104:107]
RD a[140:143],v223 offset:2048
MM v[112:115],a[48:51],a[208:211],v[112:115]
RD a[144:147],v222 offset:4096
MM v[112:115],a[52:55],a[212:215],v[112:115]
RD a[148:151],v223 offset:4096
MM v[120:123],a[52:55],a[220:223],v[120:123]
RD a[152:155],v222 offset:6144
MM v[120:123],a[48:51],a[216:219],v[120:123]
RD a[156:159],v223 offset:6144
s_nop 0
LPG3_%=:
s_cbranch_execz LPG7_%=
MM v[100:103],a[56:59],a[192:195],v[100:103]
s_sub_u32 s88, s88, s63
TR a[48:49],v195 offset:32896
TR a[50:51],v195 offset:33408
MM v[100:103],a[60:63],a[196:199],v[100:103]
s_add_u32 m0, s94, 36864
TR a[52:53],v195 offset:41120
TR a[54:55],v195 offset:41632
MM v[108:111],a[60:63],a[204:207],v[108:111]
s_add_u32 s95, s88, 40960
buffer_load_dwordx4 v220, s[32:35], s95 offen offset:0 lds
MM v[108:111],a[56:59],a[200:203],v[108:111]
s_add_u32 s95, s88, 50176
MM v[116:119],a[56:59],a[208:211],v[116:119]
buffer_load_dwordx4 v221, s[32:35], s95 offen offset:1024 lds
MM v[116:119],a[60:63],a[212:215],v[116:119]
s_add_u32 s95, s88, 59392
MM v[124:127],a[60:63],a[220:223],v[124:127]
MM v[124:127],a[56:59],a[216:219],v[124:127]
buffer_load_dwordx4 v220, s[32:35], s95 offen offset:2048 lds
s_add_u32 s95, s88, 68608
buffer_load_dwordx4 v221, s[32:35], s95 offen offset:3072 lds
s_waitcnt lgkmcnt(4)
s_waitcnt vmcnt(10)
s_barrier
MM v[0:3],a[0:3],a[224:227],v[0:3]
s_add_u32 s77, s64, 0
TR a[56:57],v195 offset:32904
TR a[58:59],v195 offset:33416
MM v[0:3],a[4:7],a[228:231],v[0:3]
)ASM"
R"ASM(
s_add_u32 m0, s65, 0
TR a[60:61],v195 offset:41128
TR a[62:63],v195 offset:41640
MM v[8:11],a[4:7],a[236:239],v[8:11]
s_waitcnt vmcnt(8)
TR a[64:65],v196 offset:0
TR a[66:67],v196 offset:512
MM v[8:11],a[0:3],a[232:235],v[8:11]
s_lshl_b32 s76, %[swave], 7
buffer_load_dwordx4 v228, s[8:11], s77 offen offset:0 lds
v_lshrrev_b32 v253, 8, v248
v_lshlrev_b32 v253, 5, v253
MM v[16:19],a[0:3],a[240:243],v[16:19]
s_add_u32 s76, s76, %[slds]
TR a[68:69],v196 offset:8224
TR a[70:71],v196 offset:8736
v_xor_b32 v201, s69, v201
MM v[16:19],a[4:7],a[244:247],v[16:19]
s_mov_b64 exec, 1
buffer_store_dword v171, off, s[20:23], s80 sc1
s_mov_b64 exec, -1
v_add_u32 v253, s76, v253
v_add_u32 v253, 151552, v253
MM v[24:27],a[4:7],a[252:255],v[24:27]
TR a[72:73],v196 offset:16448
TR a[74:75],v196 offset:16960
MM v[24:27],a[0:3],a[248:251],v[24:27]
TR a[76:77],v196 offset:24672
TR a[78:79],v196 offset:25184
buffer_load_dwordx4 v229, s[8:11], s77 offen offset:1024 lds
v_xor_b32 v203, s69, v203
MM v[4:7],a[8:11],a[224:227],v[4:7]
TR a[80:81],v196 offset:8
TR a[82:83],v196 offset:520
MM v[4:7],a[12:15],a[228:231],v[4:7]
TR a[84:85],v196 offset:8232
TR a[86:87],v196 offset:8744
MM v[12:15],a[12:15],a[236:239],v[12:15]
TR a[88:89],v196 offset:16456
TR a[90:91],v196 offset:16968
v_xor_b32 v205, s69, v205
MM v[12:15],a[8:11],a[232:235],v[12:15]
TR a[92:93],v196 offset:24680
TR a[94:95],v196 offset:25192
buffer_load_dwordx4 v230, s[8:11], s77 offen offset:2048 lds
MM v[20:23],a[8:11],a[240:243],v[20:23]
TR a[96:97],v197 offset:0
TR a[98:99],v197 offset:512
MM v[20:23],a[12:15],a[244:247],v[20:23]
TR a[100:101],v197 offset:8224
TR a[102:103],v197 offset:8736
v_xor_b32 v207, s69, v207
MM v[28:31],a[12:15],a[252:255],v[28:31]
TR a[104:105],v197 offset:16448
TR a[106:107],v197 offset:16960
MM v[28:31],a[8:11],a[248:251],v[28:31]
TR a[108:109],v197 offset:24672
TR a[110:111],v197 offset:25184
buffer_load_dwordx4 v231, s[8:11], s77 offen offset:3072 lds
v_smfmac_f32_16x16x64_bf16 v[184:187], v[236:239], a[0:7], v234
s_add_u32 s77, s64, 16384
TR a[112:113],v197 offset:8
v_or3_b32 v209, v201, v203, v205
v_smfmac_f32_16x16x64_bf16 v[188:191], v[236:239], a[8:15], v234
s_add_u32 m0, s65, 16384
TR a[114:115],v197 offset:520
MM v[32:35],a[16:19],a[224:227],v[32:35]
TR a[116:117],v197 offset:8232
TR a[118:119],v197 offset:8744
MM v[32:35],a[20:23],a[228:231],v[32:35]
TR a[120:121],v197 offset:16456
TR a[122:123],v197 offset:16968
v_or_b32 v209, v209, v207
MM v[40:43],a[20:23],a[236:239],v[40:43]
TR a[124:125],v197 offset:24680
TR a[126:127],v197 offset:25192
MM v[40:43],a[16:19],a[232:235],v[40:43]
TR a[0:1],v198 offset:0
TR a[2:3],v198 offset:512
buffer_load_dwordx4 v228, s[8:11], s77 offen offset:0 lds
MM v[48:51],a[16:19],a[240:243],v[48:51]
TR a[4:5],v198 offset:8224
TR a[6:7],v198 offset:8736
v_cmp_ne_u32 vcc, 0, v209
s_cbranch_vccnz LRG1o_%=
LRG1ob_%=:
MM v[48:51],a[20:23],a[244:247],v[48:51]
TR a[8:9],v198 offset:16448
TR a[10:11],v198 offset:16960
MM v[56:59],a[20:23],a[252:255],v[56:59]
TR a[12:13],v198 offset:24672
TR a[14:15],v198 offset:25184
MM v[56:59],a[16:19],a[248:251],v[56:59]
v_lshlrev_b32 v240, 16, v200
v_and_b32 v200, 0xffff0000, v200
MM v[36:39],a[24:27],a[224:227],v[36:39]
v_rcp_f32 v244, v200
MM v[36:39],a[28:31],a[228:231],v[36:39]
v_xor_b32 v240, 0x80000000, v240
v_mul_f32 v244, s72, v244
MM v[44:47],a[28:31],a[236:239],v[44:47]
v_lshlrev_b32 v241, 16, v202
v_and_b32 v202, 0xffff0000, v202
MM v[44:47],a[24:27],a[232:235],v[44:47]
buffer_load_dwordx4 v229, s[8:11], s77 offen offset:1024 lds
MM v[52:55],a[24:27],a[240:243],v[52:55]
v_rcp_f32 v245, v202
MM v[52:55],a[28:31],a[244:247],v[52:55]
v_xor_b32 v241, 0x80000000, v241
v_mul_f32 v245, s72, v245
MM v[60:63],a[28:31],a[252:255],v[60:63]
v_lshlrev_b32 v242, 16, v204
v_and_b32 v204, 0xffff0000, v204
MM v[60:63],a[24:27],a[248:251],v[60:63]
v_rcp_f32 v246, v204
MM v[64:67],a[32:35],a[224:227],v[64:67]
TR a[16:17],v198 offset:8
TR a[18:19],v198 offset:520
MM v[64:67],a[36:39],a[228:231],v[64:67]
TR a[20:21],v198 offset:8232
TR a[22:23],v198 offset:8744
v_xor_b32 v242, 0x80000000, v242
MM v[72:75],a[36:39],a[236:239],v[72:75]
TR a[24:25],v198 offset:16456
TR a[26:27],v198 offset:16968
MM v[72:75],a[32:35],a[232:235],v[72:75]
TR a[28:29],v198 offset:24680
TR a[30:31],v198 offset:25192
MM v[80:83],a[32:35],a[240:243],v[80:83]
v_mul_f32 v246, s72, v246
v_lshlrev_b32 v243, 16, v206
MM v[80:83],a[36:39],a[244:247],v[80:83]
buffer_load_dwordx4 v230, s[8:11], s77 offen offset:2048 lds
MM v[88:91],a[36:39],a[252:255],v[88:91]
v_and_b32 v206, 0xffff0000, v206
MM v[88:91],a[32:35],a[248:251],v[88:91]
s_waitcnt lgkmcnt(15)
v_rcp_f32 v247, v206
v_smfmac_f32_16x16x64_bf16 v[184:187], v[236:239], a[64:71], v234
v_xor_b32 v243, 0x80000000, v243
v_smfmac_f32_16x16x64_bf16 v[184:187], v[236:239], a[72:79], v234
v_mul_f32 v247, s72, v247
v_smfmac_f32_16x16x64_bf16 v[188:191], v[236:239], a[80:87], v234
v_smfmac_f32_16x16x64_bf16 v[188:191], v[236:239], a[88:95], v234
MM v[68:71],a[40:43],a[224:227],v[68:71]
v_bfe_u32 v183, v248, 6, 2
v_lshlrev_b32 v183, 5, v183
MM v[68:71],a[44:47],a[228:231],v[68:71]
v_bfe_u32 v182, v248, 4, 2
v_lshl_add_u32 v183, v182, 2, v183
MM v[76:79],a[44:47],a[236:239],v[76:79]
v_add_u32 v183, s76, v183
v_add_u32 v183, 151552, v183
MM v[76:79],a[40:43],a[232:235],v[76:79]
buffer_load_dwordx4 v231, s[8:11], s77 offen offset:3072 lds
MM v[84:87],a[40:43],a[240:243],v[84:87]
s_waitcnt vmcnt(9)
RD a[160:163],v222 offset:32768
MM v[84:87],a[44:47],a[244:247],v[84:87]
RD a[164:167],v223 offset:32768
MM v[92:95],a[44:47],a[252:255],v[92:95]
RD a[168:171],v222 offset:34816
MM v[92:95],a[40:43],a[248:251],v[92:95]
v_add_f32 v184, v184, v185
v_add_f32 v188, v188, v189
MM v[96:99],a[48:51],a[224:227],v[96:99]
TR a[32:33],v199 offset:0
TR a[34:35],v199 offset:512
MM v[96:99],a[52:55],a[228:231],v[96:99]
RD a[172:175],v223 offset:34816
ds_write_b32 v183, v184 offset:0
ds_write_b32 v183, v188 offset:16
MM v[104:107],a[52:55],a[236:239],v[104:107]
TR a[36:37],v199 offset:8224
TR a[38:39],v199 offset:8736
MM v[104:107],a[48:51],a[232:235],v[104:107]
TR a[40:41],v199 offset:16448
TR a[42:43],v199 offset:16960
MM v[112:115],a[48:51],a[240:243],v[112:115]
TR a[44:45],v199 offset:24672
TR a[46:47],v199 offset:25184
RD a[176:179],v222 offset:36864
MM v[112:115],a[52:55],a[244:247],v[112:115]
RD a[180:183],v223 offset:36864
MM v[120:123],a[52:55],a[252:255],v[120:123]
RD a[184:187],v222 offset:38912
MM v[120:123],a[48:51],a[248:251],v[120:123]
RD a[188:191],v223 offset:38912
MM v[100:103],a[56:59],a[224:227],v[100:103]
s_add_u32 s77, s64, 0
s_add_u32 s81, s77, 896
s_add_u32 m0, s65, 65536
MM v[100:103],a[60:63],a[228:231],v[100:103]
buffer_load_dwordx4 v235, s[12:15], s81 offen offset:0 lds
MM v[108:111],a[60:63],a[236:239],v[108:111]
MM v[108:111],a[56:59],a[232:235],v[108:111]
MM v[116:119],a[56:59],a[240:243],v[116:119]
MM v[116:119],a[60:63],a[244:247],v[116:119]
MM v[124:127],a[60:63],a[252:255],v[124:127]
MM v[124:127],a[56:59],a[248:251],v[124:127]
buffer_load_dwordx4 v235, s[12:15], s77 offen offset:1024 lds
s_waitcnt lgkmcnt(0)
s_barrier
MM v[0:3],a[64:67],a[128:131],v[0:3]
s_memrealtime s[52:53]
TR a[48:49],v199 offset:8
TR a[50:51],v199 offset:520
MM v[0:3],a[68:71],a[132:135],v[0:3]
s_memtime s[54:55]
TR a[52:53],v199 offset:8232
TR a[54:55],v199 offset:8744
MM v[0:3],a[72:75],a[160:163],v[0:3]
TR a[56:57],v199 offset:16456
TR a[58:59],v199 offset:16968
MM v[0:3],a[76:79],a[164:167],v[0:3]
TR a[60:61],v199 offset:24680
TR a[62:63],v199 offset:25192
MM v[8:11],a[76:79],a[172:175],v[8:11]
v_xor_b32 v151, 0, v253
v_xor_b32 v143, 0, v253
MM v[8:11],a[72:75],a[168:171],v[8:11]
RD v[144:147],v151 offset:0
RD v[148:151],v151 offset:16
MM v[8:11],a[68:71],a[140:143],v[8:11]
RD v[136:139],v143 offset:0
RD v[140:143],v143 offset:16
MM v[8:11],a[64:67],a[136:139],v[8:11]
MM v[4:7],a[80:83],a[128:131],v[4:7]
MM v[4:7],a[84:87],a[132:135],v[4:7]
s_waitcnt lgkmcnt(2)
buffer_load_dwordx4 v235, s[12:15], s81 offen offset:2048 lds
MM v[4:7],a[88:91],a[160:163],v[4:7]
s_waitcnt lgkmcnt(0)
MM v[4:7],a[92:95],a[164:167],v[4:7]
MM v[12:15],a[92:95],a[172:175],v[12:15]
MM v[12:15],a[88:91],a[168:171],v[12:15]
MM v[12:15],a[84:87],a[140:143],v[12:15]
MM v[12:15],a[80:83],a[136:139],v[12:15]
MM v[32:35],a[96:99],a[128:131],v[32:35]
MM v[32:35],a[100:103],a[132:135],v[32:35]
MM v[32:35],a[104:107],a[160:163],v[32:35]
MM v[32:35],a[108:111],a[164:167],v[32:35]
buffer_load_dwordx4 v235, s[12:15], s77 offen offset:3072 lds
v_fmac_f32 v0, v240, v144
v_fmac_f32 v1, v240, v145
v_fmac_f32 v2, v240, v146
MM v[40:43],a[108:111],a[172:175],v[40:43]
s_add_u32 s77, s64, 32768
s_add_u32 m0, s65, 32768
v_fmac_f32 v3, v240, v147
v_fmac_f32 v4, v240, v148
MM v[40:43],a[104:107],a[168:171],v[40:43]
v_fmac_f32 v5, v240, v149
v_fmac_f32 v6, v240, v150
MM v[40:43],a[100:103],a[140:143],v[40:43]
v_fmac_f32 v7, v240, v151
v_mul_f32 v0, v244, v0
v_mul_f32 v1, v244, v1
MM v[40:43],a[96:99],a[136:139],v[40:43]
v_mul_f32 v2, v244, v2
v_mul_f32 v3, v244, v3
MM v[36:39],a[112:115],a[128:131],v[36:39]
v_mul_f32 v4, v244, v4
v_mul_f32 v5, v244, v5
MM v[36:39],a[116:119],a[132:135],v[36:39]
v_mul_f32 v6, v244, v6
v_mul_f32 v7, v244, v7
CV v160,v0,v1
MM v[36:39],a[120:123],a[160:163],v[36:39]
CV v161,v2,v3
CV v162,v4,v5
MM v[36:39],a[124:127],a[164:167],v[36:39]
CV v163,v6,v7
v_fmac_f32 v8, v241, v144
MM v[44:47],a[124:127],a[172:175],v[44:47]
v_fmac_f32 v9, v241, v145
v_fmac_f32 v10, v241, v146
v_fmac_f32 v11, v241, v147
MM v[44:47],a[120:123],a[168:171],v[44:47]
buffer_load_dwordx4 v228, s[8:11], s77 offen offset:0 lds
v_fmac_f32 v12, v241, v148
v_fmac_f32 v13, v241, v149
MM v[44:47],a[116:119],a[140:143],v[44:47]
v_fmac_f32 v14, v241, v150
v_fmac_f32 v15, v241, v151
v_mul_f32 v8, v245, v8
MM v[44:47],a[112:115],a[136:139],v[44:47]
v_mul_f32 v9, v245, v9
v_mul_f32 v10, v245, v10
MM v[64:67],a[0:3],a[128:131],v[64:67]
v_mul_f32 v11, v245, v11
v_mul_f32 v12, v245, v12
MM v[64:67],a[4:7],a[132:135],v[64:67]
v_mul_f32 v13, v245, v13
v_mul_f32 v14, v245, v14
v_mul_f32 v15, v245, v15
MM v[64:67],a[8:11],a[160:163],v[64:67]
CV v164,v8,v9
CV v165,v10,v11
MM v[64:67],a[12:15],a[164:167],v[64:67]
CV v166,v12,v13
CV v167,v14,v15
MM v[72:75],a[12:15],a[172:175],v[72:75]
v_xor_b32 v159, 128, v253
MM v[72:75],a[8:11],a[168:171],v[72:75]
RD v[152:155],v159 offset:0
RD v[156:159],v159 offset:16
MM v[72:75],a[4:7],a[140:143],v[72:75]
MM v[72:75],a[0:3],a[136:139],v[72:75]
buffer_load_dwordx4 v229, s[8:11], s77 offen offset:1024 lds
MM v[68:71],a[16:19],a[128:131],v[68:71]
MM v[68:71],a[20:23],a[132:135],v[68:71]
s_waitcnt lgkmcnt(0)
v_fmac_f32 v32, v240, v152
v_fmac_f32 v33, v240, v153
v_fmac_f32 v34, v240, v154
v_fmac_f32 v35, v240, v155
v_fmac_f32 v36, v240, v156
v_fmac_f32 v37, v240, v157
v_fmac_f32 v38, v240, v158
v_fmac_f32 v39, v240, v159
v_mul_f32 v179, -v250, v244
v_fma_f32 v32, v244, v32, v179
v_fma_f32 v33, v244, v33, v179
v_fma_f32 v34, v244, v34, v179
MM v[68:71],a[24:27],a[160:163],v[68:71]
v_fma_f32 v35, v244, v35, v179
v_fma_f32 v36, v244, v36, v179
MM v[68:71],a[28:31],a[164:167],v[68:71]
v_fma_f32 v37, v244, v37, v179
v_fma_f32 v38, v244, v38, v179
v_fma_f32 v39, v244, v39, v179
MM v[76:79],a[28:31],a[172:175],v[76:79]
CV v176,v32,v33
CV v177,v34,v35
MM v[76:79],a[24:27],a[168:171],v[76:79]
CV v178,v36,v37
CV v179,v38,v39
v_fmac_f32 v40, v241, v152
MM v[76:79],a[20:23],a[140:143],v[76:79]
v_fmac_f32 v41, v241, v153
v_fmac_f32 v42, v241, v154
v_fmac_f32 v43, v241, v155
MM v[76:79],a[16:19],a[136:139],v[76:79]
v_fmac_f32 v44, v241, v156
v_fmac_f32 v45, v241, v157
MM v[96:99],a[32:35],a[128:131],v[96:99]
v_fmac_f32 v46, v241, v158
v_fmac_f32 v47, v241, v159
MM v[96:99],a[36:39],a[132:135],v[96:99]
buffer_load_dwordx4 v230, s[8:11], s77 offen offset:2048 lds
v_mul_f32 v183, -v250, v245
v_fma_f32 v40, v245, v40, v183
v_fma_f32 v41, v245, v41, v183
MM v[96:99],a[40:43],a[160:163],v[96:99]
v_fma_f32 v42, v245, v42, v183
v_fma_f32 v43, v245, v43, v183
MM v[96:99],a[44:47],a[164:167],v[96:99]
v_fma_f32 v44, v245, v44, v183
)ASM"
R"ASM(
v_fma_f32 v45, v245, v45, v183
MM v[104:107],a[44:47],a[172:175],v[104:107]
v_fma_f32 v46, v245, v46, v183
v_fma_f32 v47, v245, v47, v183
CV v180,v40,v41
MM v[104:107],a[40:43],a[168:171],v[104:107]
CV v181,v42,v43
CV v182,v44,v45
MM v[104:107],a[36:39],a[140:143],v[104:107]
CV v183,v46,v47
v_xor_b32 v151, 256, v253
MM v[104:107],a[32:35],a[136:139],v[104:107]
RD v[144:147],v151 offset:0
RD v[148:151],v151 offset:16
MM v[100:103],a[48:51],a[128:131],v[100:103]
MM v[100:103],a[52:55],a[132:135],v[100:103]
MM v[100:103],a[56:59],a[160:163],v[100:103]
MM v[100:103],a[60:63],a[164:167],v[100:103]
s_waitcnt lgkmcnt(0)
buffer_load_dwordx4 v231, s[8:11], s77 offen offset:3072 lds
v_fmac_f32 v64, v240, v144
v_fmac_f32 v65, v240, v145
v_fmac_f32 v66, v240, v146
v_fmac_f32 v67, v240, v147
v_fmac_f32 v68, v240, v148
v_fmac_f32 v69, v240, v149
v_fmac_f32 v70, v240, v150
v_fmac_f32 v71, v240, v151
v_mul_f32 v195, -v250, v244
v_fma_f32 v64, v244, v64, v195
v_fma_f32 v65, v244, v65, v195
MM v[108:111],a[60:63],a[172:175],v[108:111]
s_add_u32 s77, s64, 16384
s_add_u32 s81, s77, 896
s_add_u32 m0, s65, 81920
v_fma_f32 v66, v244, v66, v195
v_fma_f32 v67, v244, v67, v195
MM v[108:111],a[56:59],a[168:171],v[108:111]
v_fma_f32 v68, v244, v68, v195
v_fma_f32 v69, v244, v69, v195
MM v[108:111],a[52:55],a[140:143],v[108:111]
v_fma_f32 v70, v244, v70, v195
v_fma_f32 v71, v244, v71, v195
CV v192,v64,v65
MM v[108:111],a[48:51],a[136:139],v[108:111]
CV v193,v66,v67
CV v194,v68,v69
MM v[16:19],a[64:67],a[144:147],v[16:19]
CV v195,v70,v71
v_fmac_f32 v72, v241, v144
v_fmac_f32 v73, v241, v145
MM v[16:19],a[68:71],a[148:151],v[16:19]
v_fmac_f32 v74, v241, v146
v_fmac_f32 v75, v241, v147
v_fmac_f32 v76, v241, v148
MM v[16:19],a[72:75],a[176:179],v[16:19]
v_fmac_f32 v77, v241, v149
v_fmac_f32 v78, v241, v150
MM v[16:19],a[76:79],a[180:183],v[16:19]
v_fmac_f32 v79, v241, v151
v_mul_f32 v199, -v250, v245
v_fma_f32 v72, v245, v72, v199
MM v[24:27],a[76:79],a[188:191],v[24:27]
v_fma_f32 v73, v245, v73, v199
v_fma_f32 v74, v245, v74, v199
MM v[24:27],a[72:75],a[184:187],v[24:27]
buffer_load_dwordx4 v235, s[12:15], s81 offen offset:0 lds
v_fma_f32 v75, v245, v75, v199
v_fma_f32 v76, v245, v76, v199
MM v[24:27],a[68:71],a[156:159],v[24:27]
s_waitcnt vmcnt(13)
s_barrier
RD a[192:195],v224 offset:0
v_fma_f32 v77, v245, v77, v199
v_fma_f32 v78, v245, v78, v199
v_fma_f32 v79, v245, v79, v199
MM v[24:27],a[64:67],a[152:155],v[24:27]
RD a[196:199],v225 offset:0
RD a[200:203],v226 offset:0
CV v196,v72,v73
CV v197,v74,v75
MM v[20:23],a[80:83],a[144:147],v[20:23]
RD a[204:207],v227 offset:0
RD a[208:211],v224 offset:4096
CV v198,v76,v77
CV v199,v78,v79
MM v[20:23],a[84:87],a[148:151],v[20:23]
RD a[212:215],v225 offset:4096
RD a[216:219],v226 offset:4096
v_xor_b32 v159, 384, v253
MM v[20:23],a[88:91],a[176:179],v[20:23]
RD a[220:223],v227 offset:4096
RD a[224:227],v224 offset:8192
RD v[152:155],v159 offset:0
RD v[156:159],v159 offset:16
MM v[20:23],a[92:95],a[180:183],v[20:23]
RD a[228:231],v225 offset:8192
RD a[232:235],v226 offset:8192
MM v[28:31],a[92:95],a[188:191],v[28:31]
RD a[236:239],v227 offset:8192
RD a[240:243],v224 offset:12288
MM v[28:31],a[88:91],a[184:187],v[28:31]
RD a[244:247],v225 offset:12288
RD a[248:251],v226 offset:12288
MM v[28:31],a[84:87],a[156:159],v[28:31]
s_waitcnt lgkmcnt(6)
RD a[252:255],v227 offset:12288
v_fmac_f32 v96, v240, v152
v_fmac_f32 v97, v240, v153
v_fmac_f32 v98, v240, v154
v_fmac_f32 v99, v240, v155
v_fmac_f32 v100, v240, v156
v_fmac_f32 v101, v240, v157
v_fmac_f32 v102, v240, v158
v_fmac_f32 v103, v240, v159
v_mul_f32 v211, -v250, v244
v_fma_f32 v96, v244, v96, v211
v_fma_f32 v97, v244, v97, v211
v_fma_f32 v98, v244, v98, v211
MM v[28:31],a[80:83],a[152:155],v[28:31]
buffer_load_dwordx4 v235, s[12:15], s77 offen offset:1024 lds
v_fma_f32 v99, v244, v99, v211
v_fma_f32 v100, v244, v100, v211
MM v[48:51],a[96:99],a[144:147],v[48:51]
v_fma_f32 v101, v244, v101, v211
v_fma_f32 v102, v244, v102, v211
v_fma_f32 v103, v244, v103, v211
MM v[48:51],a[100:103],a[148:151],v[48:51]
CV v208,v96,v97
CV v209,v98,v99
MM v[48:51],a[104:107],a[176:179],v[48:51]
CV v210,v100,v101
CV v211,v102,v103
v_fmac_f32 v104, v241, v152
MM v[48:51],a[108:111],a[180:183],v[48:51]
v_fmac_f32 v105, v241, v153
v_fmac_f32 v106, v241, v154
v_fmac_f32 v107, v241, v155
MM v[56:59],a[108:111],a[188:191],v[56:59]
v_fmac_f32 v108, v241, v156
v_fmac_f32 v109, v241, v157
MM v[56:59],a[104:107],a[184:187],v[56:59]
v_fmac_f32 v110, v241, v158
v_fmac_f32 v111, v241, v159
MM v[56:59],a[100:103],a[156:159],v[56:59]
v_mul_f32 v215, -v250, v245
v_fma_f32 v104, v245, v104, v215
v_fma_f32 v105, v245, v105, v215
MM v[56:59],a[96:99],a[152:155],v[56:59]
v_fma_f32 v106, v245, v106, v215
v_fma_f32 v107, v245, v107, v215
MM v[52:55],a[112:115],a[144:147],v[52:55]
v_fma_f32 v108, v245, v108, v215
v_fma_f32 v109, v245, v109, v215
MM v[52:55],a[116:119],a[148:151],v[52:55]
buffer_load_dwordx4 v235, s[12:15], s81 offen offset:2048 lds
v_fma_f32 v110, v245, v110, v215
v_fma_f32 v111, v245, v111, v215
CV v212,v104,v105
MM v[52:55],a[120:123],a[176:179],v[52:55]
CV v213,v106,v107
CV v214,v108,v109
CV v215,v110,v111
MM v[52:55],a[124:127],a[180:183],v[52:55]
v_mov_b32 v241, 0
v_mov_b32 v244, 0
s_waitcnt lgkmcnt(0)
MM v[0:3],a[192:195],v[160:163],0
v_mov_b32 v144, 0
v_mov_b32 v145, 0
MM v[0:3],a[196:199],v[176:179],v[0:3]
v_mov_b32 v245, 0
v_mov_b32 v148, 0
MM v[0:3],a[200:203],v[192:195],v[0:3]
v_mov_b32 v149, 0
v_fmac_f32 v16, v242, v136
MM v[0:3],a[204:207],v[208:211],v[0:3]
v_fmac_f32 v17, v242, v137
v_fmac_f32 v18, v242, v138
MM v[8:11],a[192:195],v[164:167],0
v_fmac_f32 v19, v242, v139
v_fmac_f32 v20, v242, v140
MM v[8:11],a[196:199],v[180:183],v[8:11]
v_fmac_f32 v21, v242, v141
v_fmac_f32 v22, v242, v142
MM v[8:11],a[200:203],v[196:199],v[8:11]
v_fmac_f32 v23, v242, v143
v_mul_f32 v16, v246, v16
MM v[8:11],a[204:207],v[212:215],v[8:11]
buffer_load_dwordx4 v235, s[12:15], s77 offen offset:3072 lds
MM v[4:7],a[208:211],v[160:163],0
s_add_u32 s77, s64, 32768
s_add_u32 s81, s77, 896
s_add_u32 m0, s65, 98304
MM v[4:7],a[212:215],v[176:179],v[4:7]
v_max3_f32 v146, v0, v1, v2
v_max_f32 v146, v146, v3
MM v[4:7],a[216:219],v[192:195],v[4:7]
LPG4_%=:
s_cbranch_execz LPG8_%=
v_mov_b32 v147, v146
v_mul_f32 v17, v246, v17
MM v[4:7],a[220:223],v[208:211],v[4:7]
v_permlane16_swap_b32 v146, v147
v_max_f32 v146, v146, v147
MM v[12:15],a[208:211],v[164:167],0
s_waitcnt vmcnt(8)
s_barrier
RD a[192:195],v224 offset:16384
MM v[12:15],a[212:215],v[180:183],v[12:15]
RD a[196:199],v225 offset:16384
RD a[200:203],v226 offset:16384
MM v[12:15],a[216:219],v[196:199],v[12:15]
RD a[204:207],v227 offset:16384
v_mov_b32 v147, v146
MM v[12:15],a[220:223],v[212:215],v[12:15]
s_nop 0
v_permlane32_swap_b32 v146, v147
v_max_f32 v146, v146, v147
v_sub_f32 v128, 0xc1600000, v146
v_sub_f32 v129, 0xc1600000, v146
MM v[32:35],a[224:227],v[160:163],0
RD a[208:211],v224 offset:20480
RD a[212:215],v225 offset:20480
v_sub_f32 v130, 0xc1600000, v146
v_sub_f32 v131, 0xc1600000, v146
v_max3_f32 v150, v8, v9, v10
MM v[32:35],a[228:231],v[176:179],v[32:35]
buffer_load_dwordx4 v235, s[12:15], s81 offen offset:0 lds
v_max_f32 v150, v150, v11
v_mov_b32 v151, v150
MM v[32:35],a[232:235],v[192:195],v[32:35]
RD a[216:219],v226 offset:20480
RD a[220:223],v227 offset:20480
v_permlane16_swap_b32 v150, v151
v_max_f32 v150, v150, v151
v_mov_b32 v151, v150
MM v[32:35],a[236:239],v[208:211],v[32:35]
s_nop 0
v_permlane32_swap_b32 v150, v151
v_max_f32 v150, v150, v151
v_add_f32 v0, v0, v128
v_sub_f32 v132, 0xc1600000, v150
MM v[40:43],a[224:227],v[164:167],0
v_sub_f32 v133, 0xc1600000, v150
v_sub_f32 v134, 0xc1600000, v150
v_sub_f32 v135, 0xc1600000, v150
v_add_f32 v1, v1, v128
EX v0,v0
MM v[40:43],a[228:231],v[180:183],v[40:43]
EX v1,v1
MM v[40:43],a[232:235],v[196:199],v[40:43]
v_add_f32 v2, v2, v128
v_add_f32 v3, v3, v128
MM v[40:43],a[236:239],v[212:215],v[40:43]
EX v2,v2
MM v[36:39],a[240:243],v[160:163],v[128:131]
RD a[224:227],v224 offset:24576
RD a[228:231],v225 offset:24576
EX v3,v3
MM v[36:39],a[244:247],v[176:179],v[36:39]
RD a[232:235],v226 offset:24576
RD a[236:239],v227 offset:24576
CV v0,v0,v1
v_add_f32 v4, v4, v128
MM v[36:39],a[248:251],v[192:195],v[36:39]
v_add_f32 v5, v5, v128
v_mul_f32 v18, v246, v18
EX v4,v4
MM v[36:39],a[252:255],v[208:211],v[36:39]
buffer_load_dwordx4 v235, s[12:15], s77 offen offset:1024 lds
EX v5,v5
MM v[44:47],a[240:243],v[164:167],v[132:135]
CV v1,v2,v3
v_add_f32 v6, v6, v128
MM v[44:47],a[244:247],v[180:183],v[44:47]
v_add_f32 v7, v7, v128
v_mul_f32 v19, v246, v19
MM v[44:47],a[248:251],v[196:199],v[44:47]
EX v6,v6
EX v7,v7
MM v[44:47],a[252:255],v[212:215],v[44:47]
CV v2,v4,v5
v_add_f32 v8, v8, v132
MM v[60:63],a[124:127],a[188:191],v[60:63]
RD a[240:243],v224 offset:28672
RD a[244:247],v225 offset:28672
v_add_f32 v9, v9, v132
MM v[60:63],a[120:123],a[184:187],v[60:63]
RD a[248:251],v226 offset:28672
RD a[252:255],v227 offset:28672
EX v8,v8
MM v[60:63],a[116:119],a[156:159],v[60:63]
EX v9,v9
MM v[60:63],a[112:115],a[152:155],v[60:63]
CV v3,v6,v7
v_add_f32 v10, v10, v132
MM v[80:83],a[0:3],a[144:147],v[80:83]
v_add_f32 v11, v11, v132
v_mul_f32 v20, v246, v20
EX v10,v10
MM v[80:83],a[4:7],a[148:151],v[80:83]
buffer_load_dwordx4 v235, s[12:15], s81 offen offset:2048 lds
EX v11,v11
MM v[80:83],a[8:11],a[176:179],v[80:83]
CV v8,v8,v9
v_add_f32 v12, v12, v132
MM v[80:83],a[12:15],a[180:183],v[80:83]
v_add_f32 v13, v13, v132
v_mul_f32 v21, v246, v21
EX v12,v12
MM v[88:91],a[12:15],a[188:191],v[88:91]
EX v13,v13
MM v[88:91],a[8:11],a[184:187],v[88:91]
CV v9,v10,v11
v_add_f32 v14, v14, v132
MM v[88:91],a[4:7],a[156:159],v[88:91]
v_add_f32 v15, v15, v132
v_mul_f32 v22, v246, v22
EX v14,v14
MM v[88:91],a[0:3],a[152:155],v[88:91]
EX v15,v15
MM v[84:87],a[16:19],a[144:147],v[84:87]
CV v10,v12,v13
v_add_f32 v32, v32, v128
MM v[84:87],a[20:23],a[148:151],v[84:87]
v_add_f32 v33, v33, v128
v_mul_f32 v23, v246, v23
MM v[84:87],a[24:27],a[176:179],v[84:87]
EX v32,v32
MM v[84:87],a[28:31],a[180:183],v[84:87]
buffer_load_dwordx4 v235, s[12:15], s77 offen offset:3072 lds
EX v33,v33
MM v[92:95],a[28:31],a[188:191],v[92:95]
s_add_u32 s77, s64, 49152
s_add_u32 m0, s65, 49152
CV v11,v14,v15
v_add_f32 v34, v34, v128
v_add_f32 v35, v35, v128
MM v[92:95],a[24:27],a[184:187],v[92:95]
buffer_load_dwordx4 v228, s[8:11], s77 offen offset:0 lds
EX v34,v34
MM v[92:95],a[20:23],a[156:159],v[92:95]
EX v35,v35
MM v[92:95],a[16:19],a[152:155],v[92:95]
CV v4,v32,v33
CV v168,v16,v17
EX v36,v36
MM v[112:115],a[32:35],a[144:147],v[112:115]
EX v37,v37
MM v[112:115],a[36:39],a[148:151],v[112:115]
buffer_load_dwordx4 v229, s[8:11], s77 offen offset:1024 lds
CV v5,v34,v35
MM v[112:115],a[40:43],a[176:179],v[112:115]
EX v38,v38
EX v39,v39
MM v[112:115],a[44:47],a[180:183],v[112:115]
CV v6,v36,v37
v_add_f32 v40, v40, v132
MM v[120:123],a[44:47],a[188:191],v[120:123]
v_add_f32 v41, v41, v132
CV v169,v18,v19
MM v[120:123],a[40:43],a[184:187],v[120:123]
buffer_load_dwordx4 v230, s[8:11], s77 offen offset:2048 lds
EX v40,v40
MM v[120:123],a[36:39],a[156:159],v[120:123]
EX v41,v41
MM v[120:123],a[32:35],a[152:155],v[120:123]
CV v7,v38,v39
v_add_f32 v42, v42, v132
MM v[116:119],a[48:51],a[144:147],v[116:119]
v_add_f32 v43, v43, v132
CV v170,v20,v21
EX v42,v42
MM v[116:119],a[52:55],a[148:151],v[116:119]
buffer_load_dwordx4 v231, s[8:11], s77 offen offset:3072 lds
EX v43,v43
MM v[116:119],a[56:59],a[176:179],v[116:119]
s_add_u32 s77, s64, 49152
s_add_u32 s81, s77, 896
s_add_u32 m0, s65, 114688
CV v12,v40,v41
EX v44,v44
MM v[116:119],a[60:63],a[180:183],v[116:119]
EX v45,v45
MM v[124:127],a[60:63],a[188:191],v[124:127]
CV v13,v42,v43
CV v171,v22,v23
MM v[124:127],a[56:59],a[184:187],v[124:127]
buffer_load_dwordx4 v235, s[12:15], s81 offen offset:0 lds
EX v46,v46
MM v[124:127],a[52:55],a[156:159],v[124:127]
EX v47,v47
CV v14,v44,v45
CV v15,v46,v47
MM v[124:127],a[48:51],a[152:155],v[124:127]
v_fmac_f32 v24, v243, v136
v_fmac_f32 v25, v243, v137
v_smfmac_f32_16x16x64_bf16 v[144:147], v[236:239], v[0:7], v234
v_fmac_f32 v26, v243, v138
)ASM"
R"ASM(
v_smfmac_f32_16x16x64_bf16 v[148:151], v[236:239], v[8:15], v234
s_waitcnt lgkmcnt(0)
v_fmac_f32 v27, v243, v139
buffer_load_dwordx4 v235, s[12:15], s77 offen offset:1024 lds
buffer_load_dwordx4 v235, s[12:15], s81 offen offset:2048 lds
buffer_load_dwordx4 v235, s[12:15], s77 offen offset:3072 lds
s_add_u32 s64, s64, 49152
MM v[64:67],a[192:195],v[160:163],v[128:131]
s_add_u32 s64, s64, 16384
v_fmac_f32 v28, v243, v140
v_fmac_f32 v29, v243, v141
MM v[64:67],a[196:199],v[176:179],v[64:67]
s_add_u32 s81, s64, 896
TR a[128:129],v232 offset:0
v_fmac_f32 v30, v243, v142
MM v[64:67],a[200:203],v[192:195],v[64:67]
v_fmac_f32 v31, v243, v143
v_mul_f32 v24, v247, v24
MM v[64:67],a[204:207],v[208:211],v[64:67]
v_max3_f32 v249, v144, v145, v148
v_max_f32 v249, v249, v149
v_mul_f32 v25, v247, v25
MM v[68:71],a[220:223],v[208:211],v[128:131]
TR a[130:131],v232 offset:4096
TR a[132:133],v232 offset:8192
v_cmp_lt_f32 vcc, s71, v249
v_mul_f32 v26, v247, v26
v_mul_f32 v27, v247, v27
MM v[68:71],a[216:219],v[192:195],v[68:71]
s_cbranch_vccnz LSITE0_%=
LBACK0_%=:
TR a[134:135],v232 offset:12288
v_add_f32 v244, v144, v145
v_mul_f32 v28, v247, v28
v_mul_f32 v29, v247, v29
v_mul_f32 v30, v247, v30
MM v[68:71],a[212:215],v[176:179],v[68:71]
TR a[136:137],v233 offset:0
v_add_f32 v245, v148, v149
v_mul_f32 v31, v247, v31
CV v172,v24,v25
MM v[68:71],a[208:211],v[160:163],v[68:71]
TR a[138:139],v233 offset:4096
CV v173,v26,v27
CV v174,v28,v29
MM v[76:79],a[208:211],v[164:167],v[132:135]
TR a[140:141],v233 offset:8192
TR a[142:143],v233 offset:12288
CV v175,v30,v31
v_xor_b32 v143, 128, v253
MM v[76:79],a[212:215],v[180:183],v[76:79]
RD v[136:139],v143 offset:0
RD v[140:143],v143 offset:16
TR a[144:145],v232 offset:512
MM v[76:79],a[216:219],v[196:199],v[76:79]
s_waitcnt lgkmcnt(9)
TR a[146:147],v232 offset:4608
TR a[148:149],v232 offset:8704
MM v[76:79],a[220:223],v[212:215],v[76:79]
EX v64,v64
MM v[32:35],a[128:131],v[0:3],0
MM v[36:39],a[128:131],v[8:11],0
MM v[72:75],a[204:207],v[212:215],v[132:135]
TR a[150:151],v232 offset:12800
TR a[152:153],v233 offset:512
MM v[72:75],a[200:203],v[196:199],v[72:75]
s_waitcnt lgkmcnt(5)
TR a[154:155],v233 offset:4608
v_fmac_f32 v48, v242, v136
v_fmac_f32 v49, v242, v137
v_fmac_f32 v50, v242, v138
v_fmac_f32 v51, v242, v139
v_fmac_f32 v52, v242, v140
v_fmac_f32 v53, v242, v141
v_fmac_f32 v54, v242, v142
v_fmac_f32 v55, v242, v143
v_mul_f32 v187, -v252, v246
v_fma_f32 v48, v246, v48, v187
v_fma_f32 v49, v246, v49, v187
EX v65,v65
MM v[72:75],a[196:199],v[180:183],v[72:75]
TR a[156:157],v233 offset:8704
v_fma_f32 v50, v246, v50, v187
v_fma_f32 v51, v246, v51, v187
v_fma_f32 v52, v246, v52, v187
MM v[72:75],a[192:195],v[164:167],v[72:75]
TR a[158:159],v233 offset:12800
v_fma_f32 v53, v246, v53, v187
EX v66,v66
v_fma_f32 v54, v246, v54, v187
MM v[96:99],a[224:227],v[160:163],v[128:131]
TR a[160:161],v232 offset:1024
v_max3_f32 v32, |v32|, |v33|, |v34|
v_max_f32 v32, v32, |v35|
v_fma_f32 v55, v246, v55, v187
CV v184,v48,v49
EX v67,v67
MM v[96:99],a[228:231],v[176:179],v[96:99]
TR a[162:163],v232 offset:5120
TR a[164:165],v232 offset:9216
v_bfe_u32 v32, v32, 23, 8
v_add_u32 v32, 7, v32
CV v185,v50,v51
CV v186,v52,v53
CV v187,v54,v55
v_fmac_f32 v56, v243, v136
MM v[96:99],a[232:235],v[192:195],v[96:99]
TR a[166:167],v232 offset:13312
v_min_u32 v32, 240, v32
v_lshl_or_b32 v241, v32, 0, v241
v_fmac_f32 v57, v243, v137
v_fmac_f32 v58, v243, v138
EX v68,v68
MM v[96:99],a[236:239],v[208:211],v[96:99]
TR a[168:169],v233 offset:1024
v_lshl_or_b32 v32, v32, 1, 1
v_lshlrev_b32 v32, 22, v32
v_fmac_f32 v59, v243, v139
v_fmac_f32 v60, v243, v140
MM v[100:103],a[252:255],v[208:211],v[128:131]
TR a[170:171],v233 offset:5120
AW a100,v32
AW a101,v32
v_fmac_f32 v61, v243, v141
v_fmac_f32 v62, v243, v142
EX v69,v69
v_fmac_f32 v63, v243, v143
MM v[100:103],a[248:251],v[192:195],v[100:103]
TR a[172:173],v233 offset:9216
TR a[174:175],v233 offset:13312
AW a102,v32
AW a103,v32
v_mul_f32 v191, -v252, v247
v_fma_f32 v56, v247, v56, v191
MM v[100:103],a[244:247],v[176:179],v[100:103]
TR a[176:177],v232 offset:1536
v_max3_f32 v36, |v36|, |v37|, |v38|
v_max_f32 v36, v36, |v39|
v_fma_f32 v57, v247, v57, v191
EX v70,v70
v_fma_f32 v58, v247, v58, v191
MM v[100:103],a[240:243],v[160:163],v[100:103]
TR a[178:179],v232 offset:5632
v_bfe_u32 v36, v36, 23, 8
v_add_u32 v36, 7, v36
v_fma_f32 v59, v247, v59, v191
v_fma_f32 v60, v247, v60, v191
EX v71,v71
v_fma_f32 v61, v247, v61, v191
MM v[108:111],a[240:243],v[164:167],v[132:135]
TR a[180:181],v232 offset:9728
v_min_u32 v36, 240, v36
v_lshl_or_b32 v241, v36, 8, v241
v_fma_f32 v62, v247, v62, v191
v_fma_f32 v63, v247, v63, v191
CV v64,v64,v65
MM v[108:111],a[244:247],v[180:183],v[108:111]
TR a[182:183],v232 offset:13824
v_lshl_or_b32 v36, v36, 1, 1
v_lshlrev_b32 v36, 22, v36
CV v188,v56,v57
CV v189,v58,v59
EX v72,v72
CV v190,v60,v61
MM v[108:111],a[248:251],v[196:199],v[108:111]
TR a[184:185],v233 offset:1536
TR a[186:187],v233 offset:5632
TR a[188:189],v233 offset:9728
TR a[190:191],v233 offset:13824
AW a108,v36
AW a109,v36
CV v191,v62,v63
v_xor_b32 v143, 256, v253
EX v73,v73
MM v[108:111],a[252:255],v[212:215],v[108:111]
RD v[136:139],v143 offset:0
RD v[140:143],v143 offset:16
AW a110,v36
AW a111,v36
CV v65,v66,v67
MM v[104:107],a[236:239],v[212:215],v[132:135]
EX v74,v74
MM v[104:107],a[232:235],v[196:199],v[104:107]
EX v75,v75
MM v[104:107],a[228:231],v[180:183],v[104:107]
EX v76,v76
CV v66,v68,v69
MM v[104:107],a[224:227],v[164:167],v[104:107]
s_waitcnt vmcnt(12) lgkmcnt(0)
s_barrier
v_fmac_f32 v80, v242, v136
v_fmac_f32 v81, v242, v137
v_fmac_f32 v82, v242, v138
v_fmac_f32 v83, v242, v139
v_fmac_f32 v84, v242, v140
v_fmac_f32 v85, v242, v141
v_fmac_f32 v86, v242, v142
v_fmac_f32 v87, v242, v143
v_mul_f32 v203, -v252, v246
v_fma_f32 v80, v246, v80, v203
MM a[0:3],a[128:131],v[0:3],a[100:103]
s_add_u32 m0, s65, 0
v_fma_f32 v81, v246, v81, v203
v_fma_f32 v82, v246, v82, v203
v_fma_f32 v83, v246, v83, v203
MM a[0:3],a[132:135],v[4:7],a[0:3]
v_fma_f32 v84, v246, v84, v203
v_fma_f32 v85, v246, v85, v203
EX v77,v77
CV v67,v70,v71
MM a[8:11],a[132:135],v[12:15],a[108:111]
v_fma_f32 v86, v246, v86, v203
v_fma_f32 v87, v246, v87, v203
MM a[8:11],a[128:131],v[8:11],a[8:11]
CV v200,v80,v81
CV v201,v82,v83
EX v78,v78
CV v202,v84,v85
MM a[12:15],a[136:139],v[8:11],a[108:111]
CV v203,v86,v87
v_fmac_f32 v88, v243, v136
v_fmac_f32 v89, v243, v137
EX v79,v79
MM a[12:15],a[140:143],v[12:15],a[12:15]
v_fmac_f32 v90, v243, v138
v_fmac_f32 v91, v243, v139
CV v72,v72,v73
MM a[4:7],a[140:143],v[4:7],a[100:103]
v_fmac_f32 v92, v243, v140
v_fmac_f32 v93, v243, v141
EX v96,v96
v_fmac_f32 v94, v243, v142
MM a[4:7],a[136:139],v[0:3],a[4:7]
v_fmac_f32 v95, v243, v143
v_mul_f32 v207, -v252, v247
MM a[32:35],a[144:147],v[0:3],a[100:103]
RD a[192:195],v224 offset:32768
v_fma_f32 v88, v247, v88, v207
v_fma_f32 v89, v247, v89, v207
EX v97,v97
CV v73,v74,v75
v_fma_f32 v90, v247, v90, v207
MM a[32:35],a[148:151],v[4:7],a[32:35]
buffer_load_dwordx4 v228, s[8:11], s64 offen offset:0 lds
v_fma_f32 v91, v247, v91, v207
v_fma_f32 v92, v247, v92, v207
EX v98,v98
MM a[40:43],a[148:151],v[12:15],a[108:111]
RD a[196:199],v225 offset:32768
v_fma_f32 v93, v247, v93, v207
v_fma_f32 v94, v247, v94, v207
MM a[40:43],a[144:147],v[8:11],a[40:43]
RD a[200:203],v226 offset:32768
v_fma_f32 v95, v247, v95, v207
CV v204,v88,v89
EX v99,v99
CV v74,v76,v77
CV v205,v90,v91
MM a[44:47],a[152:155],v[8:11],a[108:111]
CV v206,v92,v93
CV v207,v94,v95
MM a[44:47],a[156:159],v[12:15],a[44:47]
RD a[204:207],v227 offset:32768
buffer_load_dwordx4 v229, s[8:11], s64 offen offset:1024 lds
v_xor_b32 v143, 384, v253
EX v100,v100
MM a[36:39],a[156:159],v[4:7],a[100:103]
RD v[136:139],v143 offset:0
RD v[140:143],v143 offset:16
EX v101,v101
CV v75,v78,v79
MM a[36:39],a[152:155],v[0:3],a[36:39]
RD a[208:211],v224 offset:36864
RD a[212:215],v225 offset:36864
MM a[64:67],a[160:163],v[0:3],a[100:103]
EX v102,v102
MM a[64:67],a[164:167],v[4:7],a[64:67]
buffer_load_dwordx4 v230, s[8:11], s64 offen offset:2048 lds
MM a[72:75],a[164:167],v[12:15],a[108:111]
s_waitcnt lgkmcnt(2)
v_fmac_f32 v112, v242, v136
v_fmac_f32 v113, v242, v137
v_fmac_f32 v114, v242, v138
v_fmac_f32 v115, v242, v139
v_fmac_f32 v116, v242, v140
v_fmac_f32 v117, v242, v141
v_fmac_f32 v118, v242, v142
v_fmac_f32 v119, v242, v143
v_mul_f32 v219, -v252, v246
v_fma_f32 v112, v246, v112, v219
v_fma_f32 v113, v246, v113, v219
EX v103,v103
CV v68,v96,v97
v_fma_f32 v114, v246, v114, v219
MM a[72:75],a[160:163],v[8:11],a[72:75]
RD a[216:219],v226 offset:36864
v_fma_f32 v115, v246, v115, v219
v_fma_f32 v116, v246, v116, v219
EX v104,v104
MM a[76:79],a[168:171],v[8:11],a[108:111]
buffer_load_dwordx4 v231, s[8:11], s64 offen offset:3072 lds
RD a[220:223],v227 offset:36864
v_fma_f32 v117, v246, v117, v219
v_fma_f32 v118, v246, v118, v219
MM a[76:79],a[172:175],v[12:15],a[76:79]
s_add_u32 m0, s65, 65536
v_fma_f32 v119, v246, v119, v219
CV v216,v112,v113
EX v105,v105
CV v217,v114,v115
CV v69,v98,v99
MM a[68:71],a[172:175],v[4:7],a[100:103]
RD a[224:227],v224 offset:40960
CV v218,v116,v117
CV v219,v118,v119
v_fmac_f32 v120, v243, v136
MM a[68:71],a[168:171],v[0:3],a[68:71]
v_fmac_f32 v121, v243, v137
v_fmac_f32 v122, v243, v138
EX v106,v106
v_fmac_f32 v123, v243, v139
MM a[96:99],a[176:179],v[0:3],a[100:103]
RD a[228:231],v225 offset:40960
buffer_load_dwordx4 v235, s[12:15], s81 offen offset:0 lds
v_fmac_f32 v124, v243, v140
v_fmac_f32 v125, v243, v141
EX v107,v107
CV v70,v100,v101
MM a[96:99],a[180:183],v[4:7],a[96:99]
v_fmac_f32 v126, v243, v142
v_fmac_f32 v127, v243, v143
MM a[104:107],a[180:183],v[12:15],a[108:111]
RD a[232:235],v226 offset:40960
v_mul_f32 v223, -v252, v247
v_fma_f32 v120, v247, v120, v223
EX v108,v108
v_fma_f32 v121, v247, v121, v223
MM a[104:107],a[176:179],v[8:11],a[104:107]
v_fma_f32 v122, v247, v122, v223
v_fma_f32 v123, v247, v123, v223
CV v71,v102,v103
MM a[108:111],a[184:187],v[8:11],a[108:111]
RD a[236:239],v227 offset:40960
buffer_load_dwordx4 v235, s[12:15], s64 offen offset:1024 lds
v_fma_f32 v124, v247, v124, v223
v_fma_f32 v125, v247, v125, v223
EX v109,v109
MM a[108:111],a[188:191],v[12:15],a[108:111]
v_fma_f32 v126, v247, v126, v223
v_fma_f32 v127, v247, v127, v223
EX v110,v110
CV v220,v120,v121
MM a[100:103],a[188:191],v[4:7],a[100:103]
RD a[240:243],v224 offset:45056
RD a[244:247],v225 offset:45056
RD a[248:251],v226 offset:45056
RD a[252:255],v227 offset:45056
CV v221,v122,v123
EX v111,v111
CV v222,v124,v125
CV v223,v126,v127
MM a[100:103],a[184:187],v[0:3],a[100:103]
buffer_load_dwordx4 v235, s[12:15], s81 offen offset:2048 lds
CV v76,v104,v105
v_smfmac_f32_16x16x64_bf16 v[144:147], v[236:239], v[64:71], v234
CV v77,v106,v107
CV v78,v108,v109
CV v79,v110,v111
s_nop 1
v_smfmac_f32_16x16x64_bf16 v[148:151], v[236:239], v[72:79], v234
s_waitcnt lgkmcnt(0)
buffer_load_dwordx4 v235, s[12:15], s64 offen offset:3072 lds
MM v[0:3],a[192:195],v[160:163],v[128:131]
s_add_u32 s64, s64, 16384
TR a[128:129],v232 offset:16384
TR a[130:131],v232 offset:20480
MM v[0:3],a[196:199],v[176:179],v[0:3]
s_add_u32 s81, s64, 896
TR a[132:133],v232 offset:24576
TR a[134:135],v232 offset:28672
MM v[0:3],a[200:203],v[192:195],v[0:3]
TR a[136:137],v233 offset:16384
TR a[138:139],v233 offset:20480
MM v[0:3],a[204:207],v[208:211],v[0:3]
v_max3_f32 v249, v144, v145, v148
v_max_f32 v249, v249, v149
MM v[4:7],a[220:223],v[208:211],v[128:131]
TR a[140:141],v233 offset:24576
v_cmp_lt_f32 vcc, s71, v249
MM v[4:7],a[216:219],v[192:195],v[4:7]
s_cbranch_vccnz LSITE1_%=
LBACK1_%=:
TR a[142:143],v233 offset:28672
v_add_f32 v244, v144, v145
MM v[4:7],a[212:215],v[176:179],v[4:7]
TR a[144:145],v232 offset:16896
v_add_f32 v245, v148, v149
MM v[4:7],a[208:211],v[160:163],v[4:7]
TR a[146:147],v232 offset:20992
TR a[148:149],v232 offset:25088
MM v[12:15],a[208:211],v[164:167],v[132:135]
EX v0,v0
MM v[12:15],a[212:215],v[180:183],v[12:15]
EX v1,v1
MM v[12:15],a[216:219],v[196:199],v[12:15]
TR a[150:151],v232 offset:29184
CV v0,v0,v1
MM v[12:15],a[220:223],v[212:215],v[12:15]
TR a[152:153],v233 offset:16896
)ASM"
R"ASM(
TR a[154:155],v233 offset:20992
MM v[8:11],a[204:207],v[212:215],v[132:135]
EX v2,v2
MM v[8:11],a[200:203],v[196:199],v[8:11]
EX v3,v3
MM v[8:11],a[196:199],v[180:183],v[8:11]
LPG5_%=:
s_cbranch_execz LPG9_%=
TR a[156:157],v233 offset:25088
CV v1,v2,v3
MM v[8:11],a[192:195],v[164:167],v[8:11]
TR a[158:159],v233 offset:29184
TR a[160:161],v232 offset:17408
MM v[32:35],a[224:227],v[160:163],v[128:131]
EX v4,v4
MM v[32:35],a[228:231],v[176:179],v[32:35]
TR a[162:163],v232 offset:21504
TR a[164:165],v232 offset:25600
MM v[32:35],a[232:235],v[192:195],v[32:35]
EX v5,v5
MM v[32:35],a[236:239],v[208:211],v[32:35]
TR a[166:167],v232 offset:29696
CV v2,v4,v5
MM v[36:39],a[252:255],v[208:211],v[128:131]
EX v6,v6
MM v[36:39],a[248:251],v[192:195],v[36:39]
TR a[168:169],v233 offset:17408
TR a[170:171],v233 offset:21504
MM v[36:39],a[244:247],v[176:179],v[36:39]
EX v7,v7
MM v[36:39],a[240:243],v[160:163],v[36:39]
TR a[172:173],v233 offset:25600
CV v3,v6,v7
MM v[44:47],a[240:243],v[164:167],v[132:135]
EX v8,v8
MM v[44:47],a[244:247],v[180:183],v[44:47]
TR a[174:175],v233 offset:29696
TR a[176:177],v232 offset:17920
MM v[44:47],a[248:251],v[196:199],v[44:47]
EX v9,v9
MM v[44:47],a[252:255],v[212:215],v[44:47]
TR a[178:179],v232 offset:22016
TR a[180:181],v232 offset:26112
MM v[40:43],a[236:239],v[212:215],v[132:135]
EX v10,v10
MM v[40:43],a[232:235],v[196:199],v[40:43]
TR a[182:183],v232 offset:30208
CV v8,v8,v9
MM v[40:43],a[228:231],v[180:183],v[40:43]
EX v11,v11
MM v[40:43],a[224:227],v[164:167],v[40:43]
TR a[184:185],v233 offset:17920
TR a[186:187],v233 offset:22016
MM v[16:19],a[192:195],v[168:171],0
EX v12,v12
MM v[16:19],a[196:199],v[184:187],v[16:19]
TR a[188:189],v233 offset:26112
TR a[190:191],v233 offset:30208
MM v[16:19],a[200:203],v[200:203],v[16:19]
EX v13,v13
MM v[16:19],a[204:207],v[216:219],v[16:19]
EX v14,v14
MM v[24:27],a[192:195],v[172:175],0
EX v15,v15
MM v[24:27],a[196:199],v[188:191],v[24:27]
EX v32,v32
MM v[24:27],a[200:203],v[204:207],v[24:27]
EX v33,v33
MM v[24:27],a[204:207],v[220:223],v[24:27]
s_waitcnt vmcnt(12) lgkmcnt(0)
s_barrier
CV v9,v10,v11
MM a[0:3],a[128:131],v[64:67],a[0:3]
s_add_u32 m0, s65, 16384
CV v10,v12,v13
CV v11,v14,v15
MM a[0:3],a[132:135],v[68:71],a[0:3]
RD a[192:195],v224 offset:49152
CV v4,v32,v33
MM a[8:11],a[132:135],v[76:79],a[8:11]
v_max3_f32 v154, v16, v17, v18
v_max_f32 v154, v154, v19
MM a[8:11],a[128:131],v[72:75],a[8:11]
buffer_load_dwordx4 v228, s[8:11], s64 offen offset:0 lds
MM a[12:15],a[136:139],v[72:75],a[12:15]
EX v34,v34
MM a[12:15],a[140:143],v[76:79],a[12:15]
RD a[196:199],v225 offset:49152
v_mov_b32 v155, v154
MM a[4:7],a[140:143],v[68:71],a[4:7]
s_nop 0
v_permlane16_swap_b32 v154, v155
v_max_f32 v154, v154, v155
MM a[4:7],a[136:139],v[64:67],a[4:7]
EX v35,v35
MM a[32:35],a[144:147],v[64:67],a[32:35]
RD a[200:203],v226 offset:49152
v_mov_b32 v155, v154
MM a[32:35],a[148:151],v[68:71],a[32:35]
s_nop 0
v_permlane32_swap_b32 v154, v155
v_max_f32 v154, v154, v155
MM a[40:43],a[148:151],v[76:79],a[40:43]
EX v36,v36
MM a[40:43],a[144:147],v[72:75],a[40:43]
RD a[204:207],v227 offset:49152
v_sub_f32 v136, 0xc1600000, v154
MM a[44:47],a[152:155],v[72:75],a[44:47]
EX v37,v37
MM a[44:47],a[156:159],v[76:79],a[44:47]
buffer_load_dwordx4 v229, s[8:11], s64 offen offset:1024 lds
v_sub_f32 v137, 0xc1600000, v154
v_sub_f32 v138, 0xc1600000, v154
MM a[36:39],a[156:159],v[68:71],a[36:39]
EX v38,v38
MM a[36:39],a[152:155],v[64:67],a[36:39]
RD a[208:211],v224 offset:53248
v_sub_f32 v139, 0xc1600000, v154
v_mov_b32 v246, 0
MM a[64:67],a[160:163],v[64:67],a[64:67]
EX v39,v39
v_mov_b32 v152, 0
MM a[64:67],a[164:167],v[68:71],a[64:67]
buffer_load_dwordx4 v230, s[8:11], s64 offen offset:2048 lds
RD a[212:215],v225 offset:53248
v_mov_b32 v153, 0
MM a[72:75],a[164:167],v[76:79],a[72:75]
EX v40,v40
v_max3_f32 v158, v24, v25, v26
MM a[72:75],a[160:163],v[72:75],a[72:75]
RD a[216:219],v226 offset:53248
v_max_f32 v158, v158, v27
CV v5,v34,v35
MM a[76:79],a[168:171],v[72:75],a[76:79]
buffer_load_dwordx4 v231, s[8:11], s64 offen offset:3072 lds
RD a[220:223],v227 offset:53248
EX v41,v41
v_mov_b32 v159, v158
MM a[76:79],a[172:175],v[76:79],a[76:79]
s_add_u32 m0, s65, 81920
v_permlane16_swap_b32 v158, v159
v_max_f32 v158, v158, v159
MM a[68:71],a[172:175],v[68:71],a[68:71]
RD a[224:227],v224 offset:57344
EX v42,v42
v_mov_b32 v159, v158
MM a[68:71],a[168:171],v[64:67],a[68:71]
s_nop 0
v_permlane32_swap_b32 v158, v159
CV v6,v36,v37
EX v43,v43
MM a[96:99],a[176:179],v[64:67],a[96:99]
RD a[228:231],v225 offset:57344
buffer_load_dwordx4 v235, s[12:15], s81 offen offset:0 lds
v_max_f32 v158, v158, v159
MM a[96:99],a[180:183],v[68:71],a[96:99]
v_sub_f32 v140, 0xc1600000, v158
CV v7,v38,v39
EX v44,v44
MM a[104:107],a[180:183],v[76:79],a[104:107]
RD a[232:235],v226 offset:57344
v_sub_f32 v141, 0xc1600000, v158
MM a[104:107],a[176:179],v[72:75],a[104:107]
v_sub_f32 v142, 0xc1600000, v158
v_sub_f32 v143, 0xc1600000, v158
EX v45,v45
MM a[108:111],a[184:187],v[72:75],a[108:111]
RD a[236:239],v227 offset:57344
buffer_load_dwordx4 v235, s[12:15], s64 offen offset:1024 lds
v_mov_b32 v247, 0
v_mov_b32 v156, 0
v_mov_b32 v157, 0
MM a[108:111],a[188:191],v[76:79],a[108:111]
EX v46,v46
MM a[100:103],a[188:191],v[68:71],a[100:103]
RD a[240:243],v224 offset:61440
RD a[244:247],v225 offset:61440
RD a[248:251],v226 offset:61440
RD a[252:255],v227 offset:61440
EX v47,v47
MM a[100:103],a[184:187],v[64:67],a[100:103]
buffer_load_dwordx4 v235, s[12:15], s81 offen offset:2048 lds
CV v12,v40,v41
v_smfmac_f32_16x16x64_bf16 v[144:147], v[236:239], v[0:7], v234
CV v13,v42,v43
CV v14,v44,v45
CV v15,v46,v47
s_nop 1
v_smfmac_f32_16x16x64_bf16 v[148:151], v[236:239], v[8:15], v234
s_waitcnt lgkmcnt(0)
buffer_load_dwordx4 v235, s[12:15], s64 offen offset:3072 lds
MM v[64:67],a[192:195],v[160:163],v[128:131]
s_add_u32 s64, s64, 16384
TR a[128:129],v232 offset:32768
s_add_u32 s81, s64, 896
MM v[64:67],a[196:199],v[176:179],v[64:67]
TR a[130:131],v232 offset:36864
TR a[132:133],v232 offset:40960
MM v[64:67],a[200:203],v[192:195],v[64:67]
TR a[134:135],v232 offset:45056
TR a[136:137],v233 offset:32768
MM v[64:67],a[204:207],v[208:211],v[64:67]
v_max3_f32 v249, v144, v145, v148
v_max_f32 v249, v249, v149
MM v[68:71],a[220:223],v[208:211],v[128:131]
TR a[138:139],v233 offset:36864
v_cmp_lt_f32 vcc, s71, v249
MM v[68:71],a[216:219],v[192:195],v[68:71]
s_cbranch_vccnz LSITE2_%=
LBACK2_%=:
TR a[140:141],v233 offset:40960
v_add_f32 v244, v144, v145
MM v[68:71],a[212:215],v[176:179],v[68:71]
TR a[142:143],v233 offset:45056
v_add_f32 v245, v148, v149
MM v[68:71],a[208:211],v[160:163],v[68:71]
TR a[144:145],v232 offset:33280
TR a[146:147],v232 offset:37376
MM v[76:79],a[208:211],v[164:167],v[132:135]
s_lshl_b32 s90, %[swave], 10
s_add_u32 s90, s90, 81920
TR a[148:149],v232 offset:41472
MM v[76:79],a[212:215],v[180:183],v[76:79]
buffer_load_dwordx4 v[56:59], v248, s[20:23], s90 offen offset:0 sc1
MM v[76:79],a[216:219],v[196:199],v[76:79]
EX v64,v64
MM v[76:79],a[220:223],v[212:215],v[76:79]
EX v65,v65
MM v[72:75],a[204:207],v[212:215],v[132:135]
EX v66,v66
MM v[72:75],a[200:203],v[196:199],v[72:75]
EX v67,v67
MM v[72:75],a[196:199],v[180:183],v[72:75]
CV v64,v64,v65
CV v65,v66,v67
MM v[72:75],a[192:195],v[164:167],v[72:75]
EX v68,v68
MM v[80:83],a[192:195],v[168:171],v[136:139]
EX v69,v69
MM v[80:83],a[196:199],v[184:187],v[80:83]
EX v70,v70
MM v[80:83],a[200:203],v[200:203],v[80:83]
EX v71,v71
MM v[80:83],a[204:207],v[216:219],v[80:83]
CV v66,v68,v69
CV v67,v70,v71
MM v[84:87],a[220:223],v[216:219],v[136:139]
EX v72,v72
MM v[84:87],a[216:219],v[200:203],v[84:87]
EX v73,v73
MM v[84:87],a[212:215],v[184:187],v[84:87]
TR a[150:151],v232 offset:45568
TR a[152:153],v233 offset:33280
MM v[84:87],a[208:211],v[168:171],v[84:87]
EX v74,v74
MM v[92:95],a[208:211],v[172:175],v[140:143]
EX v75,v75
MM v[92:95],a[212:215],v[188:191],v[92:95]
TR a[154:155],v233 offset:37376
CV v72,v72,v73
MM v[92:95],a[216:219],v[204:207],v[92:95]
EX v76,v76
MM v[92:95],a[220:223],v[220:223],v[92:95]
EX v77,v77
MM v[88:91],a[204:207],v[220:223],v[140:143]
TR a[156:157],v233 offset:41472
CV v73,v74,v75
MM v[88:91],a[200:203],v[204:207],v[88:91]
EX v78,v78
MM v[88:91],a[196:199],v[188:191],v[88:91]
EX v79,v79
MM v[88:91],a[192:195],v[172:175],v[88:91]
TR a[158:159],v233 offset:45568
CV v74,v76,v77
MM v[96:99],a[224:227],v[160:163],v[128:131]
EX v80,v80
MM v[96:99],a[228:231],v[176:179],v[96:99]
EX v81,v81
MM v[96:99],a[232:235],v[192:195],v[96:99]
TR a[160:161],v232 offset:33792
TR a[162:163],v232 offset:37888
MM v[96:99],a[236:239],v[208:211],v[96:99]
EX v82,v82
MM v[100:103],a[252:255],v[208:211],v[128:131]
EX v83,v83
MM v[100:103],a[248:251],v[192:195],v[100:103]
TR a[164:165],v232 offset:41984
CV v75,v78,v79
MM v[100:103],a[244:247],v[176:179],v[100:103]
EX v84,v84
MM v[100:103],a[240:243],v[160:163],v[100:103]
EX v85,v85
MM v[108:111],a[240:243],v[164:167],v[132:135]
TR a[166:167],v232 offset:46080
CV v80,v80,v81
MM v[108:111],a[244:247],v[180:183],v[108:111]
EX v86,v86
MM v[108:111],a[248:251],v[196:199],v[108:111]
EX v87,v87
MM v[108:111],a[252:255],v[212:215],v[108:111]
TR a[168:169],v233 offset:33792
TR a[170:171],v233 offset:37888
MM v[104:107],a[236:239],v[212:215],v[132:135]
EX v88,v88
MM v[104:107],a[232:235],v[196:199],v[104:107]
EX v89,v89
MM v[104:107],a[228:231],v[180:183],v[104:107]
TR a[172:173],v233 offset:41984
CV v81,v82,v83
EX v90,v90
MM v[104:107],a[224:227],v[164:167],v[104:107]
TR a[174:175],v233 offset:46080
CV v82,v84,v85
MM v[112:115],a[224:227],v[168:171],v[136:139]
EX v91,v91
MM v[112:115],a[228:231],v[184:187],v[112:115]
EX v92,v92
MM v[112:115],a[232:235],v[200:203],v[112:115]
TR a[176:177],v232 offset:34304
EX v93,v93
MM v[112:115],a[236:239],v[216:219],v[112:115]
TR a[178:179],v232 offset:38400
CV v83,v86,v87
MM v[116:119],a[252:255],v[216:219],v[136:139]
EX v94,v94
MM v[116:119],a[248:251],v[200:203],v[116:119]
EX v95,v95
MM v[116:119],a[244:247],v[184:187],v[116:119]
TR a[180:181],v232 offset:42496
CV v88,v88,v89
MM v[116:119],a[240:243],v[168:171],v[116:119]
s_memrealtime s[56:57]
EX v96,v96
MM v[124:127],a[240:243],v[172:175],v[140:143]
s_memtime s[58:59]
TR a[182:183],v232 offset:46592
EX v97,v97
CV v89,v90,v91
MM v[124:127],a[244:247],v[188:191],v[124:127]
EX v98,v98
MM v[124:127],a[248:251],v[204:207],v[124:127]
TR a[184:185],v233 offset:34304
TR a[186:187],v233 offset:38400
TR a[188:189],v233 offset:42496
TR a[190:191],v233 offset:46592
MM v[124:127],a[252:255],v[220:223],v[124:127]
EX v99,v99
CV v90,v92,v93
MM v[120:123],a[236:239],v[220:223],v[140:143]
EX v100,v100
MM v[120:123],a[232:235],v[204:207],v[120:123]
EX v101,v101
CV v91,v94,v95
MM v[120:123],a[228:231],v[188:191],v[120:123]
EX v102,v102
MM v[120:123],a[224:227],v[172:175],v[120:123]
s_waitcnt vmcnt(13) lgkmcnt(0)
s_barrier
CV v68,v96,v97
MM a[0:3],a[128:131],v[0:3],a[0:3]
s_sub_u32 s52, s56, s52
s_sub_u32 s54, s58, s54
v_cvt_f32_u32 v250, s52
EX v103,v103
MM a[0:3],a[132:135],v[4:7],a[0:3]
s_add_u32 m0, s65, 32768
v_cvt_f32_u32 v251, s54
CV v69,v98,v99
EX v104,v104
MM a[8:11],a[132:135],v[12:15],a[8:11]
v_rcp_f32 v251, v251
MM a[8:11],a[128:131],v[8:11],a[8:11]
v_mul_f32 v250, 0x47abe000, v250
s_lshl_b32 s52, %[swave], 11
EX v105,v105
MM a[12:15],a[136:139],v[8:11],a[12:15]
v_mul_f32 v250, v250, v251
s_lshl_b32 s54, %[sb], 3
EX v106,v106
MM a[12:15],a[140:143],v[12:15],a[12:15]
v_cvt_u32_f32 v250, v250
s_add_u32 s52, s52, s54
EX v107,v107
MM a[4:7],a[140:143],v[4:7],a[4:7]
s_waitcnt vmcnt(0)
v_add_u32 v250, s56, v250
v_mov_b32 v251, s69
CV v70,v100,v101
MM a[4:7],a[136:139],v[0:3],a[4:7]
s_add_u32 s52, s52, 86016
s_mov_b64 exec, 1
buffer_store_dwordx2 v[250:251], off, s[20:23], s52 sc1
s_mov_b64 exec, -1
s_nop 0
EX v108,v108
MM a[32:35],a[144:147],v[0:3],a[32:35]
RD a[192:195],v224 offset:0
v_xor_b32 v56, s69, v56
v_xor_b32 v57, s69, v57
EX v109,v109
CV v71,v102,v103
MM a[32:35],a[148:151],v[4:7],a[32:35]
buffer_load_dwordx4 v228, s[8:11], s64 offen offset:0 lds
v_xor_b32 v58, s69, v58
v_xor_b32 v59, s69, v59
MM a[40:43],a[148:151],v[12:15],a[40:43]
)ASM"
R"ASM(
RD a[196:199],v225 offset:0
v_or3_b32 v252, v56, v57, v58
v_or_b32 v252, v252, v59
EX v110,v110
MM a[40:43],a[144:147],v[8:11],a[40:43]
v_cmp_ne_u32 vcc, 0, v252
s_cbranch_vccnz LRFR_%=
LRFRb_%=:
EX v111,v111
CV v76,v104,v105
MM a[44:47],a[152:155],v[8:11],a[44:47]
s_lshl_b32 s90, %[swave], 11
s_add_u32 s78, s90, %[slds]
s_add_u32 s78, s78, 155648
RD a[200:203],v226 offset:0
EX v112,v112
MM a[44:47],a[156:159],v[12:15],a[44:47]
s_add_u32 s90, s90, 53248
buffer_load_dwordx4 v229, s[8:11], s64 offen offset:1024 lds
MM a[36:39],a[156:159],v[4:7],a[36:39]
RD a[204:207],v227 offset:0
EX v113,v113
CV v77,v106,v107
MM a[36:39],a[152:155],v[0:3],a[36:39]
EX v114,v114
MM a[64:67],a[160:163],v[0:3],a[64:67]
RD a[208:211],v224 offset:4096
EX v115,v115
CV v78,v108,v109
MM a[64:67],a[164:167],v[4:7],a[64:67]
buffer_load_dwordx4 v230, s[8:11], s64 offen offset:2048 lds
MM a[72:75],a[164:167],v[12:15],a[72:75]
RD a[212:215],v225 offset:4096
EX v116,v116
MM a[72:75],a[160:163],v[8:11],a[72:75]
EX v117,v117
CV v79,v110,v111
MM a[76:79],a[168:171],v[8:11],a[76:79]
RD a[216:219],v226 offset:4096
CV v84,v112,v113
EX v118,v118
MM a[76:79],a[172:175],v[12:15],a[76:79]
buffer_load_dwordx4 v231, s[8:11], s64 offen offset:3072 lds
MM a[68:71],a[172:175],v[4:7],a[68:71]
s_add_u32 m0, s65, 98304
RD a[220:223],v227 offset:4096
EX v119,v119
MM a[68:71],a[168:171],v[0:3],a[68:71]
EX v120,v120
MM a[96:99],a[176:179],v[0:3],a[96:99]
RD a[224:227],v224 offset:8192
CV v85,v114,v115
EX v121,v121
MM a[96:99],a[180:183],v[4:7],a[96:99]
RD a[228:231],v225 offset:8192
CV v86,v116,v117
MM a[104:107],a[180:183],v[12:15],a[104:107]
buffer_load_dwordx4 v235, s[12:15], s81 offen offset:0 lds
EX v122,v122
MM a[104:107],a[176:179],v[8:11],a[104:107]
EX v123,v123
MM a[108:111],a[184:187],v[8:11],a[108:111]
RD a[232:235],v226 offset:8192
CV v87,v118,v119
MM a[108:111],a[188:191],v[12:15],a[108:111]
EX v124,v124
MM a[100:103],a[188:191],v[4:7],a[100:103]
RD a[236:239],v227 offset:8192
buffer_load_dwordx4 v235, s[12:15], s64 offen offset:1024 lds
EX v125,v125
MM a[100:103],a[184:187],v[0:3],a[100:103]
EX v126,v126
v_smfmac_f32_16x16x64_bf16 v[144:147], v[236:239], v[64:71], v234
RD a[240:243],v224 offset:12288
RD a[244:247],v225 offset:12288
RD a[248:251],v226 offset:12288
RD a[252:255],v227 offset:12288
EX v127,v127
v_smfmac_f32_16x16x64_bf16 v[148:151], v[236:239], v[72:79], v234
CV v92,v120,v121
v_smfmac_f32_16x16x64_bf16 v[152:155], v[236:239], v[80:87], v234
buffer_load_dwordx4 v235, s[12:15], s81 offen offset:2048 lds
CV v93,v122,v123
CV v94,v124,v125
CV v95,v126,v127
s_nop 1
v_smfmac_f32_16x16x64_bf16 v[156:159], v[236:239], v[88:95], v234
s_waitcnt lgkmcnt(0)
buffer_load_dwordx4 v235, s[12:15], s64 offen offset:3072 lds
MM v[0:3],a[192:195],v[160:163],v[128:131]
s_add_u32 s64, s64, 16384
TR a[128:129],v232 offset:49152
s_add_u32 s81, s64, 896
MM v[0:3],a[196:199],v[176:179],v[0:3]
TR a[130:131],v232 offset:53248
v_max3_f32 v249, v144, v145, v148
MM v[0:3],a[200:203],v[192:195],v[0:3]
TR a[132:133],v232 offset:57344
v_max3_f32 v249, v249, v149, v152
MM v[0:3],a[204:207],v[208:211],v[0:3]
TR a[134:135],v232 offset:61440
v_max3_f32 v249, v249, v153, v156
MM v[4:7],a[220:223],v[208:211],v[128:131]
TR a[136:137],v233 offset:49152
v_max_f32 v249, v249, v157
MM v[4:7],a[216:219],v[192:195],v[4:7]
TR a[138:139],v233 offset:53248
v_cmp_lt_f32 vcc, s71, v249
MM v[4:7],a[212:215],v[176:179],v[4:7]
TR a[140:141],v233 offset:57344
TR a[142:143],v233 offset:61440
MM v[4:7],a[208:211],v[160:163],v[4:7]
s_cbranch_vccnz LSITE3_%=
LBACK3_%=:
MM v[12:15],a[208:211],v[164:167],v[132:135]
s_mov_b32 m0, s78
EX v0,v0
MM v[12:15],a[212:215],v[180:183],v[12:15]
buffer_load_dwordx4 v248, s[20:23], s90 offen offset:0 sc1 lds
MM v[12:15],a[216:219],v[196:199],v[12:15]
s_waitcnt lgkmcnt(6)
buffer_load_dwordx4 v248, s[20:23], s90 offen offset:1024 sc1 lds
MM v[12:15],a[220:223],v[212:215],v[12:15]
EX v1,v1
MM v[112:115],a[128:131],v[80:83],0
MM v[116:119],a[128:131],v[88:91],0
MM v[8:11],a[204:207],v[212:215],v[132:135]
EX v2,v2
MM v[8:11],a[200:203],v[196:199],v[8:11]
TR a[144:145],v232 offset:49664
CV v0,v0,v1
MM v[8:11],a[196:199],v[180:183],v[8:11]
EX v3,v3
MM v[8:11],a[192:195],v[164:167],v[8:11]
TR a[146:147],v232 offset:53760
CV v1,v2,v3
MM v[16:19],a[192:195],v[168:171],v[136:139]
v_max3_f32 v112, |v112|, |v113|, |v114|
v_max_f32 v112, v112, |v115|
MM v[16:19],a[196:199],v[184:187],v[16:19]
v_bfe_u32 v112, v112, 23, 8
v_add_u32 v112, 7, v112
MM v[16:19],a[200:203],v[200:203],v[16:19]
v_min_u32 v112, 240, v112
v_lshl_or_b32 v241, v112, 16, v241
MM v[16:19],a[204:207],v[216:219],v[16:19]
v_lshl_or_b32 v112, v112, 1, 1
v_lshlrev_b32 v112, 22, v112
EX v4,v4
MM v[20:23],a[220:223],v[216:219],v[136:139]
AW a116,v112
AW a117,v112
MM v[20:23],a[216:219],v[200:203],v[20:23]
AW a118,v112
AW a119,v112
EX v5,v5
MM v[20:23],a[212:215],v[184:187],v[20:23]
v_max3_f32 v116, |v116|, |v117|, |v118|
v_max_f32 v116, v116, |v119|
MM v[20:23],a[208:211],v[168:171],v[20:23]
v_bfe_u32 v116, v116, 23, 8
v_add_u32 v116, 7, v116
EX v6,v6
MM v[28:31],a[208:211],v[172:175],v[140:143]
v_min_u32 v116, 240, v116
v_lshl_or_b32 v241, v116, 24, v241
MM v[28:31],a[212:215],v[188:191],v[28:31]
TR a[148:149],v232 offset:57856
v_lshl_or_b32 v116, v116, 1, 1
v_lshlrev_b32 v116, 22, v116
EX v7,v7
MM v[28:31],a[216:219],v[204:207],v[28:31]
AW a124,v116
AW a125,v116
MM v[28:31],a[220:223],v[220:223],v[28:31]
TR a[150:151],v232 offset:61952
AW a126,v116
AW a127,v116
EX v8,v8
MM v[24:27],a[204:207],v[220:223],v[140:143]
TR a[152:153],v233 offset:49664
TR a[154:155],v233 offset:53760
MM v[24:27],a[200:203],v[204:207],v[24:27]
EX v9,v9
MM v[24:27],a[196:199],v[188:191],v[24:27]
EX v10,v10
MM v[24:27],a[192:195],v[172:175],v[24:27]
TR a[156:157],v233 offset:57856
CV v2,v4,v5
MM v[32:35],a[224:227],v[160:163],v[128:131]
EX v11,v11
MM v[32:35],a[228:231],v[176:179],v[32:35]
TR a[158:159],v233 offset:61952
CV v3,v6,v7
MM v[32:35],a[232:235],v[192:195],v[32:35]
EX v12,v12
MM v[32:35],a[236:239],v[208:211],v[32:35]
TR a[160:161],v232 offset:50176
TR a[162:163],v232 offset:54272
MM v[36:39],a[252:255],v[208:211],v[128:131]
EX v13,v13
MM v[36:39],a[248:251],v[192:195],v[36:39]
EX v14,v14
MM v[36:39],a[244:247],v[176:179],v[36:39]
TR a[164:165],v232 offset:58368
s_nop 0
LPG6_%=:
s_cbranch_execz LPG10_%=
CV v8,v8,v9
MM v[36:39],a[240:243],v[160:163],v[36:39]
EX v15,v15
MM v[44:47],a[240:243],v[164:167],v[132:135]
s_lshl_b32 s76, s50, 1
TR a[166:167],v232 offset:62464
CV v9,v10,v11
MM v[44:47],a[244:247],v[180:183],v[44:47]
s_bfe_u32 s77, s49, 65538
EX v16,v16
MM v[44:47],a[248:251],v[196:199],v[44:47]
s_add_u32 s76, s76, s77
TR a[168:169],v233 offset:50176
TR a[170:171],v233 offset:54272
MM v[44:47],a[252:255],v[212:215],v[44:47]
s_lshl_b32 s76, s76, 2
EX v17,v17
MM v[40:43],a[236:239],v[212:215],v[132:135]
s_add_u32 s76, s76, s84
TR a[172:173],v233 offset:58368
CV v10,v12,v13
MM v[40:43],a[232:235],v[196:199],v[40:43]
s_lshl_b32 s76, s76, 2
EX v18,v18
MM v[40:43],a[228:231],v[180:183],v[40:43]
s_lshr_b32 s77, s49, 3
EX v19,v19
MM v[40:43],a[224:227],v[164:167],v[40:43]
s_add_u32 s76, s76, s77
TR a[174:175],v233 offset:62464
CV v11,v14,v15
MM v[48:51],a[224:227],v[168:171],v[136:139]
s_mul_i32 s95, s76, 33792
EX v20,v20
MM v[48:51],a[228:231],v[184:187],v[48:51]
s_lshl_b32 s76, %[swave], 13
TR a[176:177],v232 offset:50688
TR a[178:179],v232 offset:54784
MM v[48:51],a[232:235],v[200:203],v[48:51]
s_add_u32 s95, s95, s76
EX v21,v21
MM v[48:51],a[236:239],v[216:219],v[48:51]
s_add_u32 s95, s95, 22708224
v_mov_b32 v243, -1
CV v16,v16,v17
MM v[52:55],a[252:255],v[216:219],v[136:139]
s_mov_b32 s67, s95
EX v22,v22
MM v[52:55],a[248:251],v[200:203],v[52:55]
s_add_u32 s87, s95, 6144
buffer_store_dword v243, v248, s[20:23], s87 offen offset:12 sc1
MM v[52:55],a[244:247],v[184:187],v[52:55]
TR a[180:181],v232 offset:58880
TR a[182:183],v232 offset:62976
MM v[52:55],a[240:243],v[168:171],v[52:55]
EX v23,v23
MM v[60:63],a[240:243],v[172:175],v[140:143]
EX v24,v24
MM v[60:63],a[244:247],v[188:191],v[60:63]
TR a[184:185],v233 offset:50688
CV v17,v18,v19
MM v[60:63],a[248:251],v[204:207],v[60:63]
TR a[186:187],v233 offset:54784
TR a[188:189],v233 offset:58880
TR a[190:191],v233 offset:62976
EX v25,v25
MM v[60:63],a[252:255],v[220:223],v[60:63]
s_add_u32 s87, s95, 7168
buffer_store_dword v243, v248, s[20:23], s87 offen offset:12 sc1
MM v[56:59],a[236:239],v[220:223],v[140:143]
EX v26,v26
MM v[56:59],a[232:235],v[204:207],v[56:59]
EX v27,v27
MM v[56:59],a[228:231],v[188:191],v[56:59]
EX v28,v28
CV v18,v20,v21
MM v[56:59],a[224:227],v[172:175],v[56:59]
s_waitcnt lgkmcnt(0)
s_barrier
MM a[0:3],a[128:131],v[64:67],a[0:3]
s_add_u32 m0, s65, 49152
CV v19,v22,v23
CV v24,v24,v25
MM a[0:3],a[132:135],v[68:71],a[0:3]
EX v29,v29
MM a[8:11],a[132:135],v[76:79],a[8:11]
EX v30,v30
MM a[8:11],a[128:131],v[72:75],a[8:11]
CV v25,v26,v27
CV v26,v28,v29
MM a[16:19],a[128:131],v[80:83],a[116:119]
EX v31,v31
MM a[16:19],a[132:135],v[84:87],a[16:19]
EX v32,v32
MM a[24:27],a[132:135],v[92:95],a[124:127]
RD a[192:195],v224 offset:16384
CV v27,v30,v31
MM a[24:27],a[128:131],v[88:91],a[24:27]
EX v33,v33
MM a[28:31],a[136:139],v[88:91],a[124:127]
buffer_load_dwordx4 v228, s[8:11], s64 offen offset:0 lds
MM a[28:31],a[140:143],v[92:95],a[28:31]
EX v34,v34
MM a[20:23],a[140:143],v[84:87],a[116:119]
RD a[196:199],v225 offset:16384
CV v4,v32,v33
MM a[20:23],a[136:139],v[80:83],a[20:23]
EX v35,v35
MM a[12:15],a[136:139],v[72:75],a[12:15]
EX v36,v36
MM a[12:15],a[140:143],v[76:79],a[12:15]
RD a[200:203],v226 offset:16384
CV v5,v34,v35
MM a[4:7],a[140:143],v[68:71],a[4:7]
EX v37,v37
MM a[4:7],a[136:139],v[64:67],a[4:7]
buffer_load_dwordx4 v229, s[8:11], s64 offen offset:1024 lds
MM a[32:35],a[144:147],v[64:67],a[32:35]
EX v38,v38
MM a[32:35],a[148:151],v[68:71],a[32:35]
RD a[204:207],v227 offset:16384
CV v6,v36,v37
MM a[40:43],a[148:151],v[76:79],a[40:43]
EX v39,v39
MM a[40:43],a[144:147],v[72:75],a[40:43]
RD a[208:211],v224 offset:20480
CV v7,v38,v39
MM a[48:51],a[144:147],v[80:83],a[116:119]
EX v40,v40
MM a[48:51],a[148:151],v[84:87],a[48:51]
EX v41,v41
MM a[56:59],a[148:151],v[92:95],a[124:127]
buffer_load_dwordx4 v230, s[8:11], s64 offen offset:2048 lds
MM a[56:59],a[144:147],v[88:91],a[56:59]
EX v42,v42
MM a[60:63],a[152:155],v[88:91],a[124:127]
RD a[212:215],v225 offset:20480
CV v12,v40,v41
MM a[60:63],a[156:159],v[92:95],a[60:63]
EX v43,v43
MM a[52:55],a[156:159],v[84:87],a[116:119]
RD a[216:219],v226 offset:20480
CV v13,v42,v43
MM a[52:55],a[152:155],v[80:83],a[52:55]
EX v44,v44
MM a[44:47],a[152:155],v[72:75],a[44:47]
EX v45,v45
MM a[44:47],a[156:159],v[76:79],a[44:47]
buffer_load_dwordx4 v231, s[8:11], s64 offen offset:3072 lds
MM a[36:39],a[156:159],v[68:71],a[36:39]
s_add_u32 m0, s65, 114688
RD a[220:223],v227 offset:20480
CV v14,v44,v45
MM a[36:39],a[152:155],v[64:67],a[36:39]
EX v46,v46
MM a[64:67],a[160:163],v[64:67],a[64:67]
EX v47,v47
MM a[64:67],a[164:167],v[68:71],a[64:67]
RD a[224:227],v224 offset:24576
CV v15,v46,v47
MM a[72:75],a[164:167],v[76:79],a[72:75]
EX v48,v48
MM a[72:75],a[160:163],v[72:75],a[72:75]
buffer_load_dwordx4 v235, s[12:15], s81 offen offset:0 lds
MM a[80:83],a[160:163],v[80:83],a[116:119]
EX v49,v49
MM a[80:83],a[164:167],v[84:87],a[80:83]
RD a[228:231],v225 offset:24576
CV v20,v48,v49
MM a[88:91],a[164:167],v[92:95],a[124:127]
EX v50,v50
MM a[88:91],a[160:163],v[88:91],a[88:91]
EX v51,v51
MM a[92:95],a[168:171],v[88:91],a[124:127]
RD a[232:235],v226 offset:24576
CV v21,v50,v51
MM a[92:95],a[172:175],v[92:95],a[92:95]
EX v52,v52
MM a[84:87],a[172:175],v[84:87],a[116:119]
buffer_load_dwordx4 v235, s[12:15], s64 offen offset:1024 lds
MM a[84:87],a[168:171],v[80:83],a[84:87]
EX v53,v53
MM a[76:79],a[168:171],v[72:75],a[76:79]
RD a[236:239],v227 offset:24576
CV v22,v52,v53
MM a[76:79],a[172:175],v[76:79],a[76:79]
EX v54,v54
MM a[68:71],a[172:175],v[68:71],a[68:71]
EX v55,v55
MM a[68:71],a[168:171],v[64:67],a[68:71]
RD a[240:243],v224 offset:28672
CV v23,v54,v55
MM a[96:99],a[176:179],v[64:67],a[96:99]
EX v56,v56
)ASM"
R"ASM(
MM a[96:99],a[180:183],v[68:71],a[96:99]
buffer_load_dwordx4 v235, s[12:15], s81 offen offset:2048 lds
MM a[104:107],a[180:183],v[76:79],a[104:107]
EX v57,v57
MM a[104:107],a[176:179],v[72:75],a[104:107]
RD a[244:247],v225 offset:28672
CV v28,v56,v57
MM a[112:115],a[176:179],v[80:83],a[116:119]
EX v58,v58
MM a[112:115],a[180:183],v[84:87],a[112:115]
RD a[248:251],v226 offset:28672
RD a[252:255],v227 offset:28672
MM a[120:123],a[180:183],v[92:95],a[124:127]
EX v59,v59
MM a[120:123],a[176:179],v[88:91],a[120:123]
EX v60,v60
MM a[124:127],a[184:187],v[88:91],a[124:127]
v_add_f32 v244, v144, v145
v_add_f32 v245, v148, v149
MM a[124:127],a[188:191],v[92:95],a[124:127]
v_add_f32 v246, v152, v153
v_add_f32 v247, v156, v157
MM a[116:119],a[188:191],v[84:87],a[116:119]
EX v61,v61
MM a[116:119],a[184:187],v[80:83],a[116:119]
buffer_load_dwordx4 v235, s[12:15], s64 offen offset:3072 lds
MM a[108:111],a[184:187],v[72:75],a[108:111]
EX v62,v62
MM a[108:111],a[188:191],v[76:79],a[108:111]
EX v63,v63
MM a[100:103],a[188:191],v[68:71],a[100:103]
CV v29,v58,v59
CV v30,v60,v61
MM a[100:103],a[184:187],v[64:67],a[100:103]
CV v31,v62,v63
v_smfmac_f32_16x16x64_bf16 v[144:147], v[236:239], v[0:7], v234
v_smfmac_f32_16x16x64_bf16 v[148:151], v[236:239], v[8:15], v234
v_smfmac_f32_16x16x64_bf16 v[152:155], v[236:239], v[16:23], v234
v_smfmac_f32_16x16x64_bf16 v[156:159], v[236:239], v[24:31], v234
s_waitcnt lgkmcnt(0)
MM v[64:67],a[192:195],v[160:163],v[128:131]
s_add_u32 s64, s64, 16384
TR a[128:129],v232 offset:0
s_add_u32 s81, s64, 896
MM v[64:67],a[196:199],v[176:179],v[64:67]
TR a[130:131],v232 offset:4096
v_max3_f32 v249, v144, v145, v148
MM v[64:67],a[200:203],v[192:195],v[64:67]
TR a[132:133],v232 offset:8192
v_max3_f32 v249, v249, v149, v152
MM v[64:67],a[204:207],v[208:211],v[64:67]
TR a[134:135],v232 offset:12288
v_max3_f32 v249, v249, v153, v156
MM v[68:71],a[220:223],v[208:211],v[128:131]
TR a[136:137],v233 offset:0
v_max_f32 v249, v249, v157
MM v[68:71],a[216:219],v[192:195],v[68:71]
TR a[138:139],v233 offset:4096
v_cmp_lt_f32 vcc, s71, v249
MM v[68:71],a[212:215],v[176:179],v[68:71]
TR a[140:141],v233 offset:8192
TR a[142:143],v233 offset:12288
MM v[68:71],a[208:211],v[160:163],v[68:71]
s_cbranch_vccnz LSITE4_%=
LBACK4_%=:
MM v[76:79],a[208:211],v[164:167],v[132:135]
s_cmp_lg_u32 %[sb], 0
s_cbranch_scc1 LEP_%=
v_mov_b32 v250, s69
s_mov_b32 s76, 32768
s_mov_b64 exec, 1
buffer_store_dword v250, off, s[20:23], s76 sc1
s_mov_b64 exec, -1
s_nop 1
LEP_%=:
MM v[76:79],a[212:215],v[180:183],v[76:79]
EX v64,v64
MM v[76:79],a[216:219],v[196:199],v[76:79]
EX v65,v65
MM v[76:79],a[220:223],v[212:215],v[76:79]
TR a[144:145],v232 offset:512
CV v64,v64,v65
MM v[72:75],a[204:207],v[212:215],v[132:135]
TR a[146:147],v232 offset:4608
v_lshrrev_b32 v34, 1, v248
MM v[72:75],a[200:203],v[196:199],v[72:75]
s_mov_b32 s90, 86016
buffer_load_dwordx2 v[32:33], v34, s[20:23], s90 offen offset:0 sc1
MM v[72:75],a[196:199],v[180:183],v[72:75]
EX v66,v66
MM v[72:75],a[192:195],v[164:167],v[72:75]
EX v67,v67
MM v[80:83],a[192:195],v[168:171],v[136:139]
EX v68,v68
MM v[80:83],a[196:199],v[184:187],v[80:83]
TR a[148:149],v232 offset:8704
CV v65,v66,v67
MM v[80:83],a[200:203],v[200:203],v[80:83]
EX v69,v69
MM v[80:83],a[204:207],v[216:219],v[80:83]
TR a[150:151],v232 offset:12800
CV v66,v68,v69
MM v[84:87],a[220:223],v[216:219],v[136:139]
EX v70,v70
MM v[84:87],a[216:219],v[200:203],v[84:87]
TR a[152:153],v233 offset:512
TR a[154:155],v233 offset:4608
MM v[84:87],a[212:215],v[184:187],v[84:87]
EX v71,v71
MM v[84:87],a[208:211],v[168:171],v[84:87]
TR a[156:157],v233 offset:8704
CV v67,v70,v71
MM v[92:95],a[208:211],v[172:175],v[140:143]
EX v72,v72
MM v[92:95],a[212:215],v[188:191],v[92:95]
EX v73,v73
MM v[92:95],a[216:219],v[204:207],v[92:95]
TR a[158:159],v233 offset:12800
CV v72,v72,v73
MM v[92:95],a[220:223],v[220:223],v[92:95]
EX v74,v74
MM v[88:91],a[204:207],v[220:223],v[140:143]
TR a[160:161],v232 offset:1024
TR a[162:163],v232 offset:5120
MM v[88:91],a[200:203],v[204:207],v[88:91]
EX v75,v75
MM v[88:91],a[196:199],v[188:191],v[88:91]
TR a[164:165],v232 offset:9216
CV v73,v74,v75
MM v[88:91],a[192:195],v[172:175],v[88:91]
EX v76,v76
MM v[96:99],a[224:227],v[160:163],v[128:131]
EX v77,v77
MM v[96:99],a[228:231],v[176:179],v[96:99]
TR a[166:167],v232 offset:13312
CV v74,v76,v77
MM v[96:99],a[232:235],v[192:195],v[96:99]
EX v78,v78
MM v[96:99],a[236:239],v[208:211],v[96:99]
TR a[168:169],v233 offset:1024
TR a[170:171],v233 offset:5120
MM v[100:103],a[252:255],v[208:211],v[128:131]
EX v79,v79
MM v[100:103],a[248:251],v[192:195],v[100:103]
TR a[172:173],v233 offset:9216
CV v75,v78,v79
MM v[100:103],a[244:247],v[176:179],v[100:103]
EX v80,v80
MM v[100:103],a[240:243],v[160:163],v[100:103]
EX v81,v81
MM v[108:111],a[240:243],v[164:167],v[132:135]
TR a[174:175],v233 offset:13312
CV v80,v80,v81
MM v[108:111],a[244:247],v[180:183],v[108:111]
EX v82,v82
MM v[108:111],a[248:251],v[196:199],v[108:111]
TR a[176:177],v232 offset:1536
TR a[178:179],v232 offset:5632
MM v[108:111],a[252:255],v[212:215],v[108:111]
EX v83,v83
MM v[104:107],a[236:239],v[212:215],v[132:135]
TR a[180:181],v232 offset:9728
CV v81,v82,v83
MM v[104:107],a[232:235],v[196:199],v[104:107]
EX v84,v84
MM v[104:107],a[228:231],v[180:183],v[104:107]
TR a[182:183],v232 offset:13824
TR a[184:185],v233 offset:1536
MM v[104:107],a[224:227],v[164:167],v[104:107]
EX v85,v85
MM v[112:115],a[224:227],v[168:171],v[136:139]
TR a[186:187],v233 offset:5632
CV v82,v84,v85
MM v[112:115],a[228:231],v[184:187],v[112:115]
EX v86,v86
MM v[112:115],a[232:235],v[200:203],v[112:115]
EX v87,v87
MM v[112:115],a[236:239],v[216:219],v[112:115]
TR a[188:189],v233 offset:9728
CV v83,v86,v87
MM v[116:119],a[252:255],v[216:219],v[136:139]
EX v88,v88
MM v[116:119],a[248:251],v[200:203],v[116:119]
EX v89,v89
MM v[116:119],a[244:247],v[184:187],v[116:119]
TR a[190:191],v233 offset:13824
CV v88,v88,v89
MM v[116:119],a[240:243],v[168:171],v[116:119]
EX v90,v90
MM v[124:127],a[240:243],v[172:175],v[140:143]
EX v91,v91
MM v[124:127],a[244:247],v[188:191],v[124:127]
EX v92,v92
MM v[124:127],a[248:251],v[204:207],v[124:127]
EX v93,v93
MM v[124:127],a[252:255],v[220:223],v[124:127]
EX v94,v94
MM v[120:123],a[236:239],v[220:223],v[140:143]
EX v95,v95
MM v[120:123],a[232:235],v[204:207],v[120:123]
EX v96,v96
MM v[120:123],a[228:231],v[188:191],v[120:123]
EX v97,v97
MM v[120:123],a[224:227],v[172:175],v[120:123]
s_waitcnt vmcnt(11) lgkmcnt(0)
s_barrier
MM a[0:3],a[128:131],v[0:3],a[0:3]
s_add_u32 m0, s65, 0
CV v89,v90,v91
CV v90,v92,v93
MM a[0:3],a[132:135],v[4:7],a[0:3]
RD a[192:195],v224 offset:32768
CV v91,v94,v95
MM a[8:11],a[132:135],v[12:15],a[8:11]
buffer_load_dwordx4 v228, s[8:11], s64 offen offset:0 lds
MM a[8:11],a[128:131],v[8:11],a[8:11]
RD a[196:199],v225 offset:32768
CV v68,v96,v97
MM a[16:19],a[128:131],v[16:19],a[16:19]
RD a[200:203],v226 offset:32768
RD a[204:207],v227 offset:32768
MM a[16:19],a[132:135],v[20:23],a[16:19]
buffer_load_dwordx4 v229, s[8:11], s64 offen offset:1024 lds
MM a[24:27],a[132:135],v[28:31],a[24:27]
EX v98,v98
MM a[24:27],a[128:131],v[24:27],a[24:27]
RD a[208:211],v224 offset:36864
RD a[212:215],v225 offset:36864
MM a[28:31],a[136:139],v[24:27],a[28:31]
EX v99,v99
MM a[28:31],a[140:143],v[28:31],a[28:31]
RD a[216:219],v226 offset:36864
CV v69,v98,v99
MM a[20:23],a[140:143],v[20:23],a[20:23]
EX v100,v100
MM a[20:23],a[136:139],v[16:19],a[20:23]
EX v101,v101
MM a[12:15],a[136:139],v[8:11],a[12:15]
s_waitcnt vmcnt(2)
v_cmp_ne_u32 vcc, s69, v33
s_cbranch_vccnz LRRK_%=
LRRKb_%=:
MM a[12:15],a[140:143],v[12:15],a[12:15]
v_add_u32_dpp v32, v32, v32 row_ror:8 row_mask:0xf bank_mask:0xf
CV v70,v100,v101
MM a[4:7],a[140:143],v[4:7],a[4:7]
s_and_b32 s76, %[sb], 7
RD a[220:223],v227 offset:36864
v_mov_b32 v35, v32
MM a[4:7],a[136:139],v[0:3],a[4:7]
s_nop 0
v_permlane16_swap_b32 v32, v35
v_add_u32 v32, v32, v35
MM a[32:35],a[144:147],v[0:3],a[32:35]
RD a[224:227],v224 offset:40960
v_mov_b32 v35, v32
MM a[32:35],a[148:151],v[4:7],a[32:35]
s_nop 0
v_permlane32_swap_b32 v32, v35
v_add_u32 v32, v32, v35
MM a[40:43],a[148:151],v[12:15],a[40:43]
RD a[228:231],v225 offset:40960
v_readlane_b32 s77, v32, s76
MM a[40:43],a[144:147],v[8:11],a[40:43]
s_bfm_b32 s76, s76, 0
EX v102,v102
MM a[48:51],a[144:147],v[16:19],a[48:51]
v_cmp_gt_u32 s[60:61], s77, v32
v_cmp_eq_u32 s[62:63], s77, v32
MM a[48:51],a[148:151],v[20:23],a[48:51]
s_and_b32 s62, s62, s76
EX v103,v103
MM a[56:59],a[148:151],v[28:31],a[56:59]
s_or_b32 s60, s60, s62
EX v104,v104
MM a[56:59],a[144:147],v[24:27],a[56:59]
s_and_b32 s60, s60, 255
EX v105,v105
MM a[60:63],a[152:155],v[24:27],a[60:63]
s_bcnt1_i32_b32 s51, s60
s_cmp_lt_u32 s51, 4
s_cselect_b32 s82, 3, 99
MM a[60:63],a[156:159],v[28:31],a[60:63]
s_lshr_b32 s16, %[sb], 3
EX v106,v106
MM a[52:55],a[156:159],v[20:23],a[52:55]
s_lshl_b32 s76, s51, 5
CV v71,v102,v103
CV v76,v104,v105
MM a[52:55],a[152:155],v[16:19],a[52:55]
s_add_u32 s16, s16, s76
EX v107,v107
MM a[44:47],a[152:155],v[8:11],a[44:47]
buffer_load_dwordx4 v230, s[8:11], s64 offen offset:2048 lds
MM a[44:47],a[156:159],v[12:15],a[44:47]
EX v108,v108
MM a[36:39],a[156:159],v[4:7],a[36:39]
EX v109,v109
MM a[36:39],a[152:155],v[0:3],a[36:39]
CV v77,v106,v107
CV v78,v108,v109
MM a[64:67],a[160:163],v[0:3],a[64:67]
EX v110,v110
MM a[64:67],a[164:167],v[4:7],a[64:67]
EX v111,v111
MM a[72:75],a[164:167],v[12:15],a[72:75]
buffer_load_dwordx4 v231, s[8:11], s64 offen offset:3072 lds
MM a[72:75],a[160:163],v[8:11],a[72:75]
s_add_u32 m0, s65, 65536
EX v112,v112
MM a[80:83],a[160:163],v[16:19],a[80:83]
RD a[232:235],v226 offset:40960
CV v79,v110,v111
MM a[80:83],a[164:167],v[20:23],a[80:83]
EX v113,v113
MM a[88:91],a[164:167],v[28:31],a[88:91]
EX v114,v114
MM a[88:91],a[160:163],v[24:27],a[88:91]
buffer_load_dwordx4 v235, s[12:15], s81 offen offset:0 lds
MM a[92:95],a[168:171],v[24:27],a[92:95]
EX v115,v115
MM a[92:95],a[172:175],v[28:31],a[92:95]
CV v84,v112,v113
CV v85,v114,v115
MM a[84:87],a[172:175],v[20:23],a[84:87]
EX v116,v116
MM a[84:87],a[168:171],v[16:19],a[84:87]
EX v117,v117
MM a[76:79],a[168:171],v[8:11],a[76:79]
RD a[236:239],v227 offset:40960
CV v86,v116,v117
MM a[76:79],a[172:175],v[12:15],a[76:79]
EX v118,v118
MM a[68:71],a[172:175],v[4:7],a[68:71]
buffer_load_dwordx4 v235, s[12:15], s64 offen offset:1024 lds
MM a[68:71],a[168:171],v[0:3],a[68:71]
EX v119,v119
MM a[96:99],a[176:179],v[0:3],a[96:99]
RD a[240:243],v224 offset:45056
CV v87,v118,v119
MM a[96:99],a[180:183],v[4:7],a[96:99]
EX v120,v120
MM a[104:107],a[180:183],v[12:15],a[104:107]
EX v121,v121
MM a[104:107],a[176:179],v[8:11],a[104:107]
RD a[244:247],v225 offset:45056
CV v92,v120,v121
MM a[112:115],a[176:179],v[16:19],a[112:115]
buffer_load_dwordx4 v235, s[12:15], s81 offen offset:2048 lds
MM a[112:115],a[180:183],v[20:23],a[112:115]
EX v122,v122
MM a[120:123],a[180:183],v[28:31],a[120:123]
EX v123,v123
MM a[120:123],a[176:179],v[24:27],a[120:123]
RD a[248:251],v226 offset:45056
CV v93,v122,v123
MM a[124:127],a[184:187],v[24:27],a[124:127]
v_add_f32 v244, v144, v145
v_add_f32 v245, v148, v149
MM a[124:127],a[188:191],v[28:31],a[124:127]
v_add_f32 v246, v152, v153
v_add_f32 v247, v156, v157
MM a[116:119],a[188:191],v[20:23],a[116:119]
EX v124,v124
MM a[116:119],a[184:187],v[16:19],a[116:119]
EX v125,v125
MM a[108:111],a[184:187],v[8:11],a[108:111]
EX v126,v126
MM a[108:111],a[188:191],v[12:15],a[108:111]
RD a[252:255],v227 offset:45056
CV v94,v124,v125
MM a[100:103],a[188:191],v[4:7],a[100:103]
EX v127,v127
MM a[100:103],a[184:187],v[0:3],a[100:103]
buffer_load_dwordx4 v235, s[12:15], s64 offen offset:3072 lds
v_smfmac_f32_16x16x64_bf16 v[144:147], v[236:239], v[64:71], v234
CV v95,v126,v127
v_smfmac_f32_16x16x64_bf16 v[148:151], v[236:239], v[72:79], v234
v_smfmac_f32_16x16x64_bf16 v[152:155], v[236:239], v[80:87], v234
v_smfmac_f32_16x16x64_bf16 v[156:159], v[236:239], v[88:95], v234
s_waitcnt lgkmcnt(0)
s_cmp_lt_u32 s51, 4
s_cbranch_scc1 LLAR_%=
s_mov_b32 s70, 6
s_branch LLAP_%=
s_nop 0
s_nop 0
s_nop 0
s_nop 0
s_nop 0
s_nop 0
s_nop 0
s_nop 0
)ASM"
R"ASM(
s_nop 0
s_nop 0
s_nop 0
s_nop 0
s_nop 0
s_nop 0
s_nop 0
s_nop 0
s_nop 0
s_nop 0
s_nop 0
s_nop 0
s_nop 0
s_nop 0
s_nop 0
s_nop 0
s_nop 0
s_nop 0
s_nop 0
s_nop 0
s_nop 0
s_nop 0
s_nop 0
s_nop 0
s_nop 0
s_nop 0
s_nop 0
s_nop 0
s_nop 0
s_nop 0
s_nop 0
s_nop 0
s_nop 0
s_nop 0
s_nop 0
s_nop 0
LPG7_%=:
s_cbranch_execz LPG11_%=
s_nop 0
s_nop 0
s_nop 0
s_nop 0
s_nop 0
s_nop 0
s_nop 0
s_nop 0
s_nop 0
s_nop 0
s_nop 0
s_nop 0
s_nop 0
s_nop 0
s_nop 0
s_nop 0
s_nop 0
s_nop 0
s_nop 0
s_nop 0
s_nop 0
s_nop 0
s_nop 0
s_nop 0
s_nop 0
s_nop 0
s_nop 0
s_nop 0
s_nop 0
s_nop 0
s_nop 0
LLAP_%=:
MM v[0:3],a[192:195],v[160:163],v[128:131]
s_add_u32 s64, s64, 16384
TR a[128:129],v232 offset:16384
s_add_u32 s81, s64, 896
MM v[0:3],a[196:199],v[176:179],v[0:3]
TR a[130:131],v232 offset:20480
v_max3_f32 v249, v144, v145, v148
MM v[0:3],a[200:203],v[192:195],v[0:3]
TR a[132:133],v232 offset:24576
v_max3_f32 v249, v249, v149, v152
MM v[0:3],a[204:207],v[208:211],v[0:3]
TR a[134:135],v232 offset:28672
v_max3_f32 v249, v249, v153, v156
MM v[4:7],a[220:223],v[208:211],v[128:131]
TR a[136:137],v233 offset:16384
v_max_f32 v249, v249, v157
MM v[4:7],a[216:219],v[192:195],v[4:7]
TR a[138:139],v233 offset:20480
v_cmp_lt_f32 vcc, s71, v249
MM v[4:7],a[212:215],v[176:179],v[4:7]
TR a[140:141],v233 offset:24576
TR a[142:143],v233 offset:28672
MM v[4:7],a[208:211],v[160:163],v[4:7]
s_cbranch_vccnz LSITE5_%=
LBACK5_%=:
MM v[12:15],a[208:211],v[164:167],v[132:135]
EX v0,v0
MM v[12:15],a[212:215],v[180:183],v[12:15]
EX v1,v1
MM v[12:15],a[216:219],v[196:199],v[12:15]
TR a[144:145],v232 offset:16896
CV v0,v0,v1
MM v[12:15],a[220:223],v[212:215],v[12:15]
EX v2,v2
MM v[8:11],a[204:207],v[212:215],v[132:135]
EX v3,v3
MM v[8:11],a[200:203],v[196:199],v[8:11]
TR a[146:147],v232 offset:20992
CV v1,v2,v3
MM v[8:11],a[196:199],v[180:183],v[8:11]
EX v4,v4
MM v[8:11],a[192:195],v[164:167],v[8:11]
TR a[148:149],v232 offset:25088
TR a[150:151],v232 offset:29184
MM v[16:19],a[192:195],v[168:171],v[136:139]
EX v5,v5
MM v[16:19],a[196:199],v[184:187],v[16:19]
TR a[152:153],v233 offset:16896
CV v2,v4,v5
MM v[16:19],a[200:203],v[200:203],v[16:19]
EX v6,v6
MM v[16:19],a[204:207],v[216:219],v[16:19]
TR a[154:155],v233 offset:20992
TR a[156:157],v233 offset:25088
MM v[20:23],a[220:223],v[216:219],v[136:139]
EX v7,v7
MM v[20:23],a[216:219],v[200:203],v[20:23]
TR a[158:159],v233 offset:29184
CV v3,v6,v7
MM v[20:23],a[212:215],v[184:187],v[20:23]
EX v8,v8
MM v[20:23],a[208:211],v[168:171],v[20:23]
EX v9,v9
MM v[28:31],a[208:211],v[172:175],v[140:143]
TR a[160:161],v232 offset:17408
CV v8,v8,v9
MM v[28:31],a[212:215],v[188:191],v[28:31]
EX v10,v10
MM v[28:31],a[216:219],v[204:207],v[28:31]
TR a[162:163],v232 offset:21504
TR a[164:165],v232 offset:25600
MM v[28:31],a[220:223],v[220:223],v[28:31]
EX v11,v11
MM v[24:27],a[204:207],v[220:223],v[140:143]
TR a[166:167],v232 offset:29696
CV v9,v10,v11
MM v[24:27],a[200:203],v[204:207],v[24:27]
EX v12,v12
MM v[24:27],a[196:199],v[188:191],v[24:27]
EX v13,v13
MM v[24:27],a[192:195],v[172:175],v[24:27]
TR a[168:169],v233 offset:17408
CV v10,v12,v13
MM v[32:35],a[224:227],v[160:163],v[128:131]
EX v14,v14
MM v[32:35],a[228:231],v[176:179],v[32:35]
TR a[170:171],v233 offset:21504
TR a[172:173],v233 offset:25600
MM v[32:35],a[232:235],v[192:195],v[32:35]
EX v15,v15
MM v[32:35],a[236:239],v[208:211],v[32:35]
TR a[174:175],v233 offset:29696
CV v11,v14,v15
MM v[36:39],a[252:255],v[208:211],v[128:131]
EX v16,v16
MM v[36:39],a[248:251],v[192:195],v[36:39]
EX v17,v17
MM v[36:39],a[244:247],v[176:179],v[36:39]
TR a[176:177],v232 offset:17920
CV v16,v16,v17
MM v[36:39],a[240:243],v[160:163],v[36:39]
EX v18,v18
MM v[44:47],a[240:243],v[164:167],v[132:135]
TR a[178:179],v232 offset:22016
TR a[180:181],v232 offset:26112
MM v[44:47],a[244:247],v[180:183],v[44:47]
EX v19,v19
MM v[44:47],a[248:251],v[196:199],v[44:47]
TR a[182:183],v232 offset:30208
CV v17,v18,v19
MM v[44:47],a[252:255],v[212:215],v[44:47]
EX v20,v20
MM v[40:43],a[236:239],v[212:215],v[132:135]
TR a[184:185],v233 offset:17920
TR a[186:187],v233 offset:22016
MM v[40:43],a[232:235],v[196:199],v[40:43]
EX v21,v21
MM v[40:43],a[228:231],v[180:183],v[40:43]
TR a[188:189],v233 offset:26112
CV v18,v20,v21
MM v[40:43],a[224:227],v[164:167],v[40:43]
EX v22,v22
MM v[48:51],a[224:227],v[168:171],v[136:139]
EX v23,v23
MM v[48:51],a[228:231],v[184:187],v[48:51]
TR a[190:191],v233 offset:30208
CV v19,v22,v23
MM v[48:51],a[232:235],v[200:203],v[48:51]
EX v24,v24
MM v[48:51],a[236:239],v[216:219],v[48:51]
EX v25,v25
MM v[52:55],a[252:255],v[216:219],v[136:139]
EX v26,v26
MM v[52:55],a[248:251],v[200:203],v[52:55]
EX v27,v27
MM v[52:55],a[244:247],v[184:187],v[52:55]
EX v28,v28
MM v[52:55],a[240:243],v[168:171],v[52:55]
EX v29,v29
MM v[60:63],a[240:243],v[172:175],v[140:143]
EX v30,v30
MM v[60:63],a[244:247],v[188:191],v[60:63]
EX v31,v31
MM v[60:63],a[248:251],v[204:207],v[60:63]
EX v32,v32
MM v[60:63],a[252:255],v[220:223],v[60:63]
EX v33,v33
MM v[56:59],a[236:239],v[220:223],v[140:143]
EX v34,v34
MM v[56:59],a[232:235],v[204:207],v[56:59]
EX v35,v35
MM v[56:59],a[228:231],v[188:191],v[56:59]
EX v36,v36
MM v[56:59],a[224:227],v[172:175],v[56:59]
s_waitcnt vmcnt(12) lgkmcnt(0)
s_barrier
MM a[0:3],a[128:131],v[64:67],a[0:3]
s_add_u32 m0, s65, 16384
CV v24,v24,v25
CV v25,v26,v27
MM a[0:3],a[132:135],v[68:71],a[0:3]
RD a[192:195],v224 offset:49152
CV v26,v28,v29
MM a[8:11],a[132:135],v[76:79],a[8:11]
RD a[196:199],v225 offset:49152
CV v27,v30,v31
MM a[8:11],a[128:131],v[72:75],a[8:11]
buffer_load_dwordx4 v228, s[8:11], s64 offen offset:0 lds
MM a[16:19],a[128:131],v[80:83],a[16:19]
RD a[200:203],v226 offset:49152
CV v4,v32,v33
MM a[16:19],a[132:135],v[84:87],a[16:19]
RD a[204:207],v227 offset:49152
CV v5,v34,v35
MM a[24:27],a[132:135],v[92:95],a[24:27]
buffer_load_dwordx4 v229, s[8:11], s64 offen offset:1024 lds
MM a[24:27],a[128:131],v[88:91],a[24:27]
RD a[208:211],v224 offset:53248
RD a[212:215],v225 offset:53248
MM a[28:31],a[136:139],v[88:91],a[28:31]
buffer_load_dwordx4 v230, s[8:11], s64 offen offset:2048 lds
MM a[28:31],a[140:143],v[92:95],a[28:31]
RD a[216:219],v226 offset:53248
RD a[220:223],v227 offset:53248
MM a[20:23],a[140:143],v[84:87],a[20:23]
EX v37,v37
MM a[20:23],a[136:139],v[80:83],a[20:23]
RD a[224:227],v224 offset:57344
CV v6,v36,v37
MM a[12:15],a[136:139],v[72:75],a[12:15]
EX v38,v38
MM a[12:15],a[140:143],v[76:79],a[12:15]
buffer_load_dwordx4 v231, s[8:11], s64 offen offset:3072 lds
MM a[4:7],a[140:143],v[68:71],a[4:7]
s_add_u32 m0, s65, 81920
EX v39,v39
MM a[4:7],a[136:139],v[64:67],a[4:7]
RD a[228:231],v225 offset:57344
CV v7,v38,v39
MM a[32:35],a[144:147],v[64:67],a[32:35]
EX v40,v40
MM a[32:35],a[148:151],v[68:71],a[32:35]
EX v41,v41
MM a[40:43],a[148:151],v[76:79],a[40:43]
RD a[232:235],v226 offset:57344
CV v12,v40,v41
MM a[40:43],a[144:147],v[72:75],a[40:43]
EX v42,v42
MM a[48:51],a[144:147],v[80:83],a[48:51]
buffer_load_dwordx4 v235, s[12:15], s81 offen offset:0 lds
MM a[48:51],a[148:151],v[84:87],a[48:51]
EX v43,v43
MM a[56:59],a[148:151],v[92:95],a[56:59]
RD a[236:239],v227 offset:57344
CV v13,v42,v43
MM a[56:59],a[144:147],v[88:91],a[56:59]
EX v44,v44
MM a[60:63],a[152:155],v[88:91],a[60:63]
EX v45,v45
MM a[60:63],a[156:159],v[92:95],a[60:63]
RD a[240:243],v224 offset:61440
CV v14,v44,v45
MM a[52:55],a[156:159],v[84:87],a[52:55]
EX v46,v46
MM a[52:55],a[152:155],v[80:83],a[52:55]
buffer_load_dwordx4 v235, s[12:15], s64 offen offset:1024 lds
MM a[44:47],a[152:155],v[72:75],a[44:47]
EX v47,v47
MM a[44:47],a[156:159],v[76:79],a[44:47]
RD a[244:247],v225 offset:61440
CV v15,v46,v47
MM a[36:39],a[156:159],v[68:71],a[36:39]
EX v48,v48
MM a[36:39],a[152:155],v[64:67],a[36:39]
EX v49,v49
MM a[64:67],a[160:163],v[64:67],a[64:67]
RD a[248:251],v226 offset:61440
CV v20,v48,v49
MM a[64:67],a[164:167],v[68:71],a[64:67]
EX v50,v50
MM a[72:75],a[164:167],v[76:79],a[72:75]
buffer_load_dwordx4 v235, s[12:15], s81 offen offset:2048 lds
MM a[72:75],a[160:163],v[72:75],a[72:75]
EX v51,v51
MM a[80:83],a[160:163],v[80:83],a[80:83]
RD a[252:255],v227 offset:61440
CV v21,v50,v51
MM a[80:83],a[164:167],v[84:87],a[80:83]
EX v52,v52
MM a[88:91],a[164:167],v[92:95],a[88:91]
EX v53,v53
MM a[88:91],a[160:163],v[88:91],a[88:91]
buffer_load_dwordx4 v235, s[12:15], s64 offen offset:3072 lds
MM a[92:95],a[168:171],v[88:91],a[92:95]
EX v54,v54
MM a[92:95],a[172:175],v[92:95],a[92:95]
EX v55,v55
MM a[84:87],a[172:175],v[84:87],a[84:87]
EX v56,v56
MM a[84:87],a[168:171],v[80:83],a[84:87]
EX v57,v57
MM a[76:79],a[168:171],v[72:75],a[76:79]
EX v58,v58
MM a[76:79],a[172:175],v[76:79],a[76:79]
EX v59,v59
MM a[68:71],a[172:175],v[68:71],a[68:71]
EX v60,v60
MM a[68:71],a[168:171],v[64:67],a[68:71]
EX v61,v61
MM a[96:99],a[176:179],v[64:67],a[96:99]
EX v62,v62
MM a[96:99],a[180:183],v[68:71],a[96:99]
EX v63,v63
MM a[104:107],a[180:183],v[76:79],a[104:107]
CV v22,v52,v53
CV v23,v54,v55
MM a[104:107],a[176:179],v[72:75],a[104:107]
CV v28,v56,v57
CV v29,v58,v59
MM a[112:115],a[176:179],v[80:83],a[112:115]
CV v30,v60,v61
CV v31,v62,v63
MM a[112:115],a[180:183],v[84:87],a[112:115]
MM a[120:123],a[180:183],v[92:95],a[120:123]
MM a[120:123],a[176:179],v[88:91],a[120:123]
MM a[124:127],a[184:187],v[88:91],a[124:127]
v_add_f32 v244, v144, v145
v_add_f32 v245, v148, v149
MM a[124:127],a[188:191],v[92:95],a[124:127]
v_add_f32 v246, v152, v153
v_add_f32 v247, v156, v157
MM a[116:119],a[188:191],v[84:87],a[116:119]
s_cmp_eq_u32 s70, s82
s_cbranch_scc1 LWW_%=
LLALWW_%=:
MM a[116:119],a[184:187],v[80:83],a[116:119]
MM a[108:111],a[184:187],v[72:75],a[108:111]
MM a[108:111],a[188:191],v[76:79],a[108:111]
MM a[100:103],a[188:191],v[68:71],a[100:103]
MM a[100:103],a[184:187],v[64:67],a[100:103]
v_smfmac_f32_16x16x64_bf16 v[144:147], v[236:239], v[0:7], v234
v_smfmac_f32_16x16x64_bf16 v[148:151], v[236:239], v[8:15], v234
v_smfmac_f32_16x16x64_bf16 v[152:155], v[236:239], v[16:23], v234
v_smfmac_f32_16x16x64_bf16 v[156:159], v[236:239], v[24:31], v234
s_waitcnt lgkmcnt(0)
MM v[64:67],a[192:195],v[160:163],v[128:131]
s_add_u32 s64, s64, 16384
TR a[128:129],v232 offset:32768
s_add_u32 s81, s64, 896
MM v[64:67],a[196:199],v[176:179],v[64:67]
TR a[130:131],v232 offset:36864
v_max3_f32 v249, v144, v145, v148
MM v[64:67],a[200:203],v[192:195],v[64:67]
TR a[132:133],v232 offset:40960
v_max3_f32 v249, v249, v149, v152
MM v[64:67],a[204:207],v[208:211],v[64:67]
TR a[134:135],v232 offset:45056
v_max3_f32 v249, v249, v153, v156
MM v[68:71],a[220:223],v[208:211],v[128:131]
TR a[136:137],v233 offset:32768
v_max_f32 v249, v249, v157
MM v[68:71],a[216:219],v[192:195],v[68:71]
TR a[138:139],v233 offset:36864
v_cmp_lt_f32 vcc, s71, v249
MM v[68:71],a[212:215],v[176:179],v[68:71]
TR a[140:141],v233 offset:40960
TR a[142:143],v233 offset:45056
MM v[68:71],a[208:211],v[160:163],v[68:71]
s_cbranch_vccnz LSITE6_%=
LBACK6_%=:
MM v[76:79],a[208:211],v[164:167],v[132:135]
EX v64,v64
MM v[76:79],a[212:215],v[180:183],v[76:79]
EX v65,v65
MM v[76:79],a[216:219],v[196:199],v[76:79]
TR a[144:145],v232 offset:33280
CV v64,v64,v65
MM v[76:79],a[220:223],v[212:215],v[76:79]
EX v66,v66
MM v[72:75],a[204:207],v[212:215],v[132:135]
EX v67,v67
MM v[72:75],a[200:203],v[196:199],v[72:75]
TR a[146:147],v232 offset:37376
CV v65,v66,v67
MM v[72:75],a[196:199],v[180:183],v[72:75]
EX v68,v68
MM v[72:75],a[192:195],v[164:167],v[72:75]
TR a[148:149],v232 offset:41472
TR a[150:151],v232 offset:45568
MM v[80:83],a[192:195],v[168:171],v[136:139]
EX v69,v69
MM v[80:83],a[196:199],v[184:187],v[80:83]
TR a[152:153],v233 offset:33280
CV v66,v68,v69
MM v[80:83],a[200:203],v[200:203],v[80:83]
EX v70,v70
MM v[80:83],a[204:207],v[216:219],v[80:83]
TR a[154:155],v233 offset:37376
TR a[156:157],v233 offset:41472
MM v[84:87],a[220:223],v[216:219],v[136:139]
EX v71,v71
MM v[84:87],a[216:219],v[200:203],v[84:87]
TR a[158:159],v233 offset:45568
CV v67,v70,v71
MM v[84:87],a[212:215],v[184:187],v[84:87]
EX v72,v72
MM v[84:87],a[208:211],v[168:171],v[84:87]
EX v73,v73
MM v[92:95],a[208:211],v[172:175],v[140:143]
TR a[160:161],v232 offset:33792
CV v72,v72,v73
MM v[92:95],a[212:215],v[188:191],v[92:95]
)ASM"
R"ASM(
EX v74,v74
MM v[92:95],a[216:219],v[204:207],v[92:95]
TR a[162:163],v232 offset:37888
TR a[164:165],v232 offset:41984
MM v[92:95],a[220:223],v[220:223],v[92:95]
EX v75,v75
MM v[88:91],a[204:207],v[220:223],v[140:143]
TR a[166:167],v232 offset:46080
CV v73,v74,v75
MM v[88:91],a[200:203],v[204:207],v[88:91]
EX v76,v76
MM v[88:91],a[196:199],v[188:191],v[88:91]
EX v77,v77
MM v[88:91],a[192:195],v[172:175],v[88:91]
TR a[168:169],v233 offset:33792
CV v74,v76,v77
MM v[96:99],a[224:227],v[160:163],v[128:131]
EX v78,v78
MM v[96:99],a[228:231],v[176:179],v[96:99]
TR a[170:171],v233 offset:37888
TR a[172:173],v233 offset:41984
MM v[96:99],a[232:235],v[192:195],v[96:99]
EX v79,v79
MM v[96:99],a[236:239],v[208:211],v[96:99]
TR a[174:175],v233 offset:46080
CV v75,v78,v79
MM v[100:103],a[252:255],v[208:211],v[128:131]
EX v80,v80
MM v[100:103],a[248:251],v[192:195],v[100:103]
EX v81,v81
MM v[100:103],a[244:247],v[176:179],v[100:103]
TR a[176:177],v232 offset:34304
CV v80,v80,v81
MM v[100:103],a[240:243],v[160:163],v[100:103]
EX v82,v82
MM v[108:111],a[240:243],v[164:167],v[132:135]
buffer_store_dword v243, v248, s[20:23], s67 offen offset:12 sc1
MM v[108:111],a[244:247],v[180:183],v[108:111]
s_add_u32 s67, s67, 1024
TR a[178:179],v232 offset:38400
TR a[180:181],v232 offset:42496
MM v[108:111],a[248:251],v[196:199],v[108:111]
EX v83,v83
MM v[108:111],a[252:255],v[212:215],v[108:111]
TR a[182:183],v232 offset:46592
CV v81,v82,v83
MM v[104:107],a[236:239],v[212:215],v[132:135]
EX v84,v84
MM v[104:107],a[232:235],v[196:199],v[104:107]
EX v85,v85
MM v[104:107],a[228:231],v[180:183],v[104:107]
TR a[184:185],v233 offset:34304
CV v82,v84,v85
MM v[104:107],a[224:227],v[164:167],v[104:107]
EX v86,v86
MM v[112:115],a[224:227],v[168:171],v[136:139]
TR a[186:187],v233 offset:38400
TR a[188:189],v233 offset:42496
MM v[112:115],a[228:231],v[184:187],v[112:115]
EX v87,v87
MM v[112:115],a[232:235],v[200:203],v[112:115]
TR a[190:191],v233 offset:46592
CV v83,v86,v87
MM v[112:115],a[236:239],v[216:219],v[112:115]
EX v88,v88
MM v[116:119],a[252:255],v[216:219],v[136:139]
EX v89,v89
MM v[116:119],a[248:251],v[200:203],v[116:119]
EX v90,v90
MM v[116:119],a[244:247],v[184:187],v[116:119]
EX v91,v91
MM v[116:119],a[240:243],v[168:171],v[116:119]
EX v92,v92
MM v[124:127],a[240:243],v[172:175],v[140:143]
EX v93,v93
MM v[124:127],a[244:247],v[188:191],v[124:127]
EX v94,v94
MM v[124:127],a[248:251],v[204:207],v[124:127]
EX v95,v95
MM v[124:127],a[252:255],v[220:223],v[124:127]
EX v96,v96
MM v[120:123],a[236:239],v[220:223],v[140:143]
EX v97,v97
MM v[120:123],a[232:235],v[204:207],v[120:123]
EX v98,v98
MM v[120:123],a[228:231],v[188:191],v[120:123]
EX v99,v99
MM v[120:123],a[224:227],v[172:175],v[120:123]
s_waitcnt vmcnt(12) lgkmcnt(0)
s_barrier
MM a[0:3],a[128:131],v[0:3],a[0:3]
s_add_u32 m0, s65, 32768
CV v88,v88,v89
CV v89,v90,v91
MM a[0:3],a[132:135],v[4:7],a[0:3]
RD a[192:195],v224 offset:0
CV v90,v92,v93
MM a[8:11],a[132:135],v[12:15],a[8:11]
RD a[196:199],v225 offset:0
CV v91,v94,v95
MM a[8:11],a[128:131],v[8:11],a[8:11]
buffer_load_dwordx4 v228, s[8:11], s64 offen offset:0 lds
MM a[16:19],a[128:131],v[16:19],a[16:19]
RD a[200:203],v226 offset:0
CV v68,v96,v97
MM a[16:19],a[132:135],v[20:23],a[16:19]
RD a[204:207],v227 offset:0
CV v69,v98,v99
MM a[24:27],a[132:135],v[28:31],a[24:27]
buffer_load_dwordx4 v229, s[8:11], s64 offen offset:1024 lds
MM a[24:27],a[128:131],v[24:27],a[24:27]
RD a[208:211],v224 offset:4096
RD a[212:215],v225 offset:4096
MM a[28:31],a[136:139],v[24:27],a[28:31]
buffer_load_dwordx4 v230, s[8:11], s64 offen offset:2048 lds
MM a[28:31],a[140:143],v[28:31],a[28:31]
EX v100,v100
MM a[20:23],a[140:143],v[20:23],a[20:23]
EX v101,v101
MM a[20:23],a[136:139],v[16:19],a[20:23]
RD a[216:219],v226 offset:4096
CV v70,v100,v101
MM a[12:15],a[136:139],v[8:11],a[12:15]
EX v102,v102
MM a[12:15],a[140:143],v[12:15],a[12:15]
RD a[220:223],v227 offset:4096
RD a[224:227],v224 offset:8192
MM a[4:7],a[140:143],v[4:7],a[4:7]
EX v103,v103
MM a[4:7],a[136:139],v[0:3],a[4:7]
RD a[228:231],v225 offset:8192
CV v71,v102,v103
MM a[32:35],a[144:147],v[0:3],a[32:35]
EX v104,v104
MM a[32:35],a[148:151],v[4:7],a[32:35]
buffer_load_dwordx4 v231, s[8:11], s64 offen offset:3072 lds
MM a[40:43],a[148:151],v[12:15],a[40:43]
s_add_u32 m0, s65, 98304
EX v105,v105
MM a[40:43],a[144:147],v[8:11],a[40:43]
RD a[232:235],v226 offset:8192
CV v76,v104,v105
MM a[48:51],a[144:147],v[16:19],a[48:51]
EX v106,v106
MM a[48:51],a[148:151],v[20:23],a[48:51]
EX v107,v107
MM a[56:59],a[148:151],v[28:31],a[56:59]
RD a[236:239],v227 offset:8192
CV v77,v106,v107
MM a[56:59],a[144:147],v[24:27],a[56:59]
EX v108,v108
MM a[60:63],a[152:155],v[24:27],a[60:63]
buffer_load_dwordx4 v235, s[12:15], s81 offen offset:0 lds
MM a[60:63],a[156:159],v[28:31],a[60:63]
EX v109,v109
MM a[52:55],a[156:159],v[20:23],a[52:55]
RD a[240:243],v224 offset:12288
CV v78,v108,v109
MM a[52:55],a[152:155],v[16:19],a[52:55]
EX v110,v110
MM a[44:47],a[152:155],v[8:11],a[44:47]
EX v111,v111
MM a[44:47],a[156:159],v[12:15],a[44:47]
RD a[244:247],v225 offset:12288
CV v79,v110,v111
MM a[36:39],a[156:159],v[4:7],a[36:39]
buffer_load_dwordx4 v235, s[12:15], s64 offen offset:1024 lds
MM a[36:39],a[152:155],v[0:3],a[36:39]
EX v112,v112
MM a[64:67],a[160:163],v[0:3],a[64:67]
EX v113,v113
MM a[64:67],a[164:167],v[4:7],a[64:67]
RD a[248:251],v226 offset:12288
CV v84,v112,v113
MM a[72:75],a[164:167],v[12:15],a[72:75]
EX v114,v114
MM a[72:75],a[160:163],v[8:11],a[72:75]
EX v115,v115
MM a[80:83],a[160:163],v[16:19],a[80:83]
RD a[252:255],v227 offset:12288
CV v85,v114,v115
MM a[80:83],a[164:167],v[20:23],a[80:83]
buffer_load_dwordx4 v235, s[12:15], s81 offen offset:2048 lds
MM a[88:91],a[164:167],v[28:31],a[88:91]
EX v116,v116
MM a[88:91],a[160:163],v[24:27],a[88:91]
EX v117,v117
MM a[92:95],a[168:171],v[24:27],a[92:95]
EX v118,v118
MM a[92:95],a[172:175],v[28:31],a[92:95]
buffer_load_dwordx4 v235, s[12:15], s64 offen offset:3072 lds
MM a[84:87],a[172:175],v[20:23],a[84:87]
EX v119,v119
MM a[84:87],a[168:171],v[16:19],a[84:87]
EX v120,v120
MM a[76:79],a[168:171],v[8:11],a[76:79]
LPG8_%=:
s_cbranch_execz LPG12_%=
EX v121,v121
MM a[76:79],a[172:175],v[12:15],a[76:79]
EX v122,v122
MM a[68:71],a[172:175],v[4:7],a[68:71]
EX v123,v123
MM a[68:71],a[168:171],v[0:3],a[68:71]
EX v124,v124
MM a[96:99],a[176:179],v[0:3],a[96:99]
EX v125,v125
MM a[96:99],a[180:183],v[4:7],a[96:99]
EX v126,v126
MM a[104:107],a[180:183],v[12:15],a[104:107]
EX v127,v127
MM a[104:107],a[176:179],v[8:11],a[104:107]
CV v86,v116,v117
CV v87,v118,v119
MM a[112:115],a[176:179],v[16:19],a[112:115]
CV v92,v120,v121
CV v93,v122,v123
MM a[112:115],a[180:183],v[20:23],a[112:115]
CV v94,v124,v125
CV v95,v126,v127
MM a[120:123],a[180:183],v[28:31],a[120:123]
MM a[120:123],a[176:179],v[24:27],a[120:123]
MM a[124:127],a[184:187],v[24:27],a[124:127]
v_add_f32 v244, v144, v145
v_add_f32 v245, v148, v149
MM a[124:127],a[188:191],v[28:31],a[124:127]
v_add_f32 v246, v152, v153
v_add_f32 v247, v156, v157
MM a[116:119],a[188:191],v[20:23],a[116:119]
MM a[116:119],a[184:187],v[16:19],a[116:119]
MM a[108:111],a[184:187],v[8:11],a[108:111]
MM a[108:111],a[188:191],v[12:15],a[108:111]
MM a[100:103],a[188:191],v[4:7],a[100:103]
MM a[100:103],a[184:187],v[0:3],a[100:103]
v_smfmac_f32_16x16x64_bf16 v[144:147], v[236:239], v[64:71], v234
v_smfmac_f32_16x16x64_bf16 v[148:151], v[236:239], v[72:79], v234
v_smfmac_f32_16x16x64_bf16 v[152:155], v[236:239], v[80:87], v234
v_smfmac_f32_16x16x64_bf16 v[156:159], v[236:239], v[88:95], v234
s_waitcnt lgkmcnt(0)
MM v[0:3],a[192:195],v[160:163],v[128:131]
s_add_u32 s64, s64, 16384
TR a[128:129],v232 offset:49152
s_add_u32 s81, s64, 896
MM v[0:3],a[196:199],v[176:179],v[0:3]
TR a[130:131],v232 offset:53248
v_max3_f32 v249, v144, v145, v148
MM v[0:3],a[200:203],v[192:195],v[0:3]
TR a[132:133],v232 offset:57344
v_max3_f32 v249, v249, v149, v152
MM v[0:3],a[204:207],v[208:211],v[0:3]
TR a[134:135],v232 offset:61440
v_max3_f32 v249, v249, v153, v156
MM v[4:7],a[220:223],v[208:211],v[128:131]
TR a[136:137],v233 offset:49152
v_max_f32 v249, v249, v157
MM v[4:7],a[216:219],v[192:195],v[4:7]
TR a[138:139],v233 offset:53248
v_cmp_lt_f32 vcc, s71, v249
MM v[4:7],a[212:215],v[176:179],v[4:7]
TR a[140:141],v233 offset:57344
TR a[142:143],v233 offset:61440
MM v[4:7],a[208:211],v[160:163],v[4:7]
s_cbranch_vccnz LSITE7_%=
LBACK7_%=:
MM v[12:15],a[208:211],v[164:167],v[132:135]
EX v0,v0
MM v[12:15],a[212:215],v[180:183],v[12:15]
EX v1,v1
MM v[12:15],a[216:219],v[196:199],v[12:15]
TR a[144:145],v232 offset:49664
CV v0,v0,v1
MM v[12:15],a[220:223],v[212:215],v[12:15]
EX v2,v2
MM v[8:11],a[204:207],v[212:215],v[132:135]
EX v3,v3
MM v[8:11],a[200:203],v[196:199],v[8:11]
TR a[146:147],v232 offset:53760
CV v1,v2,v3
MM v[8:11],a[196:199],v[180:183],v[8:11]
EX v4,v4
MM v[8:11],a[192:195],v[164:167],v[8:11]
TR a[148:149],v232 offset:57856
TR a[150:151],v232 offset:61952
MM v[16:19],a[192:195],v[168:171],v[136:139]
EX v5,v5
MM v[16:19],a[196:199],v[184:187],v[16:19]
TR a[152:153],v233 offset:49664
CV v2,v4,v5
MM v[16:19],a[200:203],v[200:203],v[16:19]
EX v6,v6
MM v[16:19],a[204:207],v[216:219],v[16:19]
TR a[154:155],v233 offset:53760
TR a[156:157],v233 offset:57856
MM v[20:23],a[220:223],v[216:219],v[136:139]
EX v7,v7
MM v[20:23],a[216:219],v[200:203],v[20:23]
TR a[158:159],v233 offset:61952
CV v3,v6,v7
MM v[20:23],a[212:215],v[184:187],v[20:23]
EX v8,v8
MM v[20:23],a[208:211],v[168:171],v[20:23]
EX v9,v9
MM v[28:31],a[208:211],v[172:175],v[140:143]
TR a[160:161],v232 offset:50176
CV v8,v8,v9
MM v[28:31],a[212:215],v[188:191],v[28:31]
EX v10,v10
MM v[28:31],a[216:219],v[204:207],v[28:31]
TR a[162:163],v232 offset:54272
TR a[164:165],v232 offset:58368
MM v[28:31],a[220:223],v[220:223],v[28:31]
EX v11,v11
MM v[24:27],a[204:207],v[220:223],v[140:143]
TR a[166:167],v232 offset:62464
CV v9,v10,v11
MM v[24:27],a[200:203],v[204:207],v[24:27]
EX v12,v12
MM v[24:27],a[196:199],v[188:191],v[24:27]
EX v13,v13
MM v[24:27],a[192:195],v[172:175],v[24:27]
TR a[168:169],v233 offset:50176
CV v10,v12,v13
MM v[32:35],a[224:227],v[160:163],v[128:131]
EX v14,v14
MM v[32:35],a[228:231],v[176:179],v[32:35]
TR a[170:171],v233 offset:54272
TR a[172:173],v233 offset:58368
MM v[32:35],a[232:235],v[192:195],v[32:35]
EX v15,v15
MM v[32:35],a[236:239],v[208:211],v[32:35]
TR a[174:175],v233 offset:62464
CV v11,v14,v15
MM v[36:39],a[252:255],v[208:211],v[128:131]
EX v16,v16
MM v[36:39],a[248:251],v[192:195],v[36:39]
EX v17,v17
MM v[36:39],a[244:247],v[176:179],v[36:39]
TR a[176:177],v232 offset:50688
CV v16,v16,v17
MM v[36:39],a[240:243],v[160:163],v[36:39]
EX v18,v18
MM v[44:47],a[240:243],v[164:167],v[132:135]
TR a[178:179],v232 offset:54784
TR a[180:181],v232 offset:58880
MM v[44:47],a[244:247],v[180:183],v[44:47]
EX v19,v19
MM v[44:47],a[248:251],v[196:199],v[44:47]
TR a[182:183],v232 offset:62976
CV v17,v18,v19
MM v[44:47],a[252:255],v[212:215],v[44:47]
EX v20,v20
MM v[40:43],a[236:239],v[212:215],v[132:135]
TR a[184:185],v233 offset:50688
TR a[186:187],v233 offset:54784
MM v[40:43],a[232:235],v[196:199],v[40:43]
EX v21,v21
MM v[40:43],a[228:231],v[180:183],v[40:43]
TR a[188:189],v233 offset:58880
CV v18,v20,v21
MM v[40:43],a[224:227],v[164:167],v[40:43]
EX v22,v22
MM v[48:51],a[224:227],v[168:171],v[136:139]
EX v23,v23
MM v[48:51],a[228:231],v[184:187],v[48:51]
TR a[190:191],v233 offset:62976
CV v19,v22,v23
MM v[48:51],a[232:235],v[200:203],v[48:51]
EX v24,v24
MM v[48:51],a[236:239],v[216:219],v[48:51]
EX v25,v25
MM v[52:55],a[252:255],v[216:219],v[136:139]
EX v26,v26
MM v[52:55],a[248:251],v[200:203],v[52:55]
EX v27,v27
MM v[52:55],a[244:247],v[184:187],v[52:55]
EX v28,v28
MM v[52:55],a[240:243],v[168:171],v[52:55]
EX v29,v29
MM v[60:63],a[240:243],v[172:175],v[140:143]
EX v30,v30
MM v[60:63],a[244:247],v[188:191],v[60:63]
EX v31,v31
MM v[60:63],a[248:251],v[204:207],v[60:63]
EX v32,v32
MM v[60:63],a[252:255],v[220:223],v[60:63]
EX v33,v33
MM v[56:59],a[236:239],v[220:223],v[140:143]
EX v34,v34
MM v[56:59],a[232:235],v[204:207],v[56:59]
EX v35,v35
MM v[56:59],a[228:231],v[188:191],v[56:59]
)ASM"
R"ASM(
EX v36,v36
MM v[56:59],a[224:227],v[172:175],v[56:59]
s_waitcnt vmcnt(12) lgkmcnt(0)
s_barrier
MM a[0:3],a[128:131],v[64:67],a[0:3]
s_add_u32 m0, s65, 49152
CV v24,v24,v25
CV v25,v26,v27
MM a[0:3],a[132:135],v[68:71],a[0:3]
RD a[192:195],v224 offset:16384
CV v26,v28,v29
MM a[8:11],a[132:135],v[76:79],a[8:11]
RD a[196:199],v225 offset:16384
CV v27,v30,v31
MM a[8:11],a[128:131],v[72:75],a[8:11]
buffer_load_dwordx4 v228, s[8:11], s64 offen offset:0 lds
MM a[16:19],a[128:131],v[80:83],a[16:19]
RD a[200:203],v226 offset:16384
CV v4,v32,v33
MM a[16:19],a[132:135],v[84:87],a[16:19]
RD a[204:207],v227 offset:16384
CV v5,v34,v35
MM a[24:27],a[132:135],v[92:95],a[24:27]
buffer_load_dwordx4 v229, s[8:11], s64 offen offset:1024 lds
MM a[24:27],a[128:131],v[88:91],a[24:27]
RD a[208:211],v224 offset:20480
RD a[212:215],v225 offset:20480
MM a[28:31],a[136:139],v[88:91],a[28:31]
buffer_load_dwordx4 v230, s[8:11], s64 offen offset:2048 lds
MM a[28:31],a[140:143],v[92:95],a[28:31]
RD a[216:219],v226 offset:20480
RD a[220:223],v227 offset:20480
MM a[20:23],a[140:143],v[84:87],a[20:23]
EX v37,v37
MM a[20:23],a[136:139],v[80:83],a[20:23]
RD a[224:227],v224 offset:24576
CV v6,v36,v37
MM a[12:15],a[136:139],v[72:75],a[12:15]
EX v38,v38
MM a[12:15],a[140:143],v[76:79],a[12:15]
buffer_load_dwordx4 v231, s[8:11], s64 offen offset:3072 lds
MM a[4:7],a[140:143],v[68:71],a[4:7]
s_add_u32 m0, s65, 114688
EX v39,v39
MM a[4:7],a[136:139],v[64:67],a[4:7]
RD a[228:231],v225 offset:24576
CV v7,v38,v39
MM a[32:35],a[144:147],v[64:67],a[32:35]
EX v40,v40
MM a[32:35],a[148:151],v[68:71],a[32:35]
EX v41,v41
MM a[40:43],a[148:151],v[76:79],a[40:43]
RD a[232:235],v226 offset:24576
CV v12,v40,v41
MM a[40:43],a[144:147],v[72:75],a[40:43]
EX v42,v42
MM a[48:51],a[144:147],v[80:83],a[48:51]
buffer_load_dwordx4 v235, s[12:15], s81 offen offset:0 lds
MM a[48:51],a[148:151],v[84:87],a[48:51]
EX v43,v43
MM a[56:59],a[148:151],v[92:95],a[56:59]
RD a[236:239],v227 offset:24576
CV v13,v42,v43
MM a[56:59],a[144:147],v[88:91],a[56:59]
EX v44,v44
MM a[60:63],a[152:155],v[88:91],a[60:63]
EX v45,v45
MM a[60:63],a[156:159],v[92:95],a[60:63]
RD a[240:243],v224 offset:28672
CV v14,v44,v45
MM a[52:55],a[156:159],v[84:87],a[52:55]
EX v46,v46
MM a[52:55],a[152:155],v[80:83],a[52:55]
buffer_load_dwordx4 v235, s[12:15], s64 offen offset:1024 lds
MM a[44:47],a[152:155],v[72:75],a[44:47]
EX v47,v47
MM a[44:47],a[156:159],v[76:79],a[44:47]
RD a[244:247],v225 offset:28672
CV v15,v46,v47
MM a[36:39],a[156:159],v[68:71],a[36:39]
EX v48,v48
MM a[36:39],a[152:155],v[64:67],a[36:39]
EX v49,v49
MM a[64:67],a[160:163],v[64:67],a[64:67]
RD a[248:251],v226 offset:28672
CV v20,v48,v49
MM a[64:67],a[164:167],v[68:71],a[64:67]
EX v50,v50
MM a[72:75],a[164:167],v[76:79],a[72:75]
buffer_load_dwordx4 v235, s[12:15], s81 offen offset:2048 lds
MM a[72:75],a[160:163],v[72:75],a[72:75]
EX v51,v51
MM a[80:83],a[160:163],v[80:83],a[80:83]
RD a[252:255],v227 offset:28672
CV v21,v50,v51
MM a[80:83],a[164:167],v[84:87],a[80:83]
EX v52,v52
MM a[88:91],a[164:167],v[92:95],a[88:91]
EX v53,v53
MM a[88:91],a[160:163],v[88:91],a[88:91]
buffer_load_dwordx4 v235, s[12:15], s64 offen offset:3072 lds
MM a[92:95],a[168:171],v[88:91],a[92:95]
EX v54,v54
MM a[92:95],a[172:175],v[92:95],a[92:95]
EX v55,v55
MM a[84:87],a[172:175],v[84:87],a[84:87]
EX v56,v56
MM a[84:87],a[168:171],v[80:83],a[84:87]
EX v57,v57
MM a[76:79],a[168:171],v[72:75],a[76:79]
EX v58,v58
MM a[76:79],a[172:175],v[76:79],a[76:79]
EX v59,v59
MM a[68:71],a[172:175],v[68:71],a[68:71]
EX v60,v60
MM a[68:71],a[168:171],v[64:67],a[68:71]
EX v61,v61
MM a[96:99],a[176:179],v[64:67],a[96:99]
EX v62,v62
MM a[96:99],a[180:183],v[68:71],a[96:99]
EX v63,v63
MM a[104:107],a[180:183],v[76:79],a[104:107]
CV v22,v52,v53
CV v23,v54,v55
MM a[104:107],a[176:179],v[72:75],a[104:107]
CV v28,v56,v57
CV v29,v58,v59
MM a[112:115],a[176:179],v[80:83],a[112:115]
CV v30,v60,v61
CV v31,v62,v63
MM a[112:115],a[180:183],v[84:87],a[112:115]
MM a[120:123],a[180:183],v[92:95],a[120:123]
MM a[120:123],a[176:179],v[88:91],a[120:123]
MM a[124:127],a[184:187],v[88:91],a[124:127]
v_add_f32 v244, v144, v145
v_add_f32 v245, v148, v149
MM a[124:127],a[188:191],v[92:95],a[124:127]
v_add_f32 v246, v152, v153
v_add_f32 v247, v156, v157
MM a[116:119],a[188:191],v[84:87],a[116:119]
s_cmp_eq_u32 s70, 2
s_cbranch_scc1 LIP_%=
LLALIP_%=:
MM a[116:119],a[184:187],v[80:83],a[116:119]
MM a[108:111],a[184:187],v[72:75],a[108:111]
MM a[108:111],a[188:191],v[76:79],a[108:111]
MM a[100:103],a[188:191],v[68:71],a[100:103]
MM a[100:103],a[184:187],v[64:67],a[100:103]
v_smfmac_f32_16x16x64_bf16 v[144:147], v[236:239], v[0:7], v234
v_smfmac_f32_16x16x64_bf16 v[148:151], v[236:239], v[8:15], v234
v_smfmac_f32_16x16x64_bf16 v[152:155], v[236:239], v[16:23], v234
v_smfmac_f32_16x16x64_bf16 v[156:159], v[236:239], v[24:31], v234
s_waitcnt lgkmcnt(0)
MM v[64:67],a[192:195],v[160:163],v[128:131]
s_add_u32 s64, s64, 16384
TR a[128:129],v232 offset:0
s_and_b32 s64, s64, 524287
MM v[64:67],a[196:199],v[176:179],v[64:67]
s_add_u32 s81, s64, 896
TR a[130:131],v232 offset:4096
v_max3_f32 v249, v144, v145, v148
MM v[64:67],a[200:203],v[192:195],v[64:67]
TR a[132:133],v232 offset:8192
v_max3_f32 v249, v249, v149, v152
MM v[64:67],a[204:207],v[208:211],v[64:67]
TR a[134:135],v232 offset:12288
v_max3_f32 v249, v249, v153, v156
MM v[68:71],a[220:223],v[208:211],v[128:131]
TR a[136:137],v233 offset:0
v_max_f32 v249, v249, v157
MM v[68:71],a[216:219],v[192:195],v[68:71]
TR a[138:139],v233 offset:4096
v_cmp_lt_f32 vcc, s71, v249
MM v[68:71],a[212:215],v[176:179],v[68:71]
TR a[140:141],v233 offset:8192
TR a[142:143],v233 offset:12288
MM v[68:71],a[208:211],v[160:163],v[68:71]
s_cbranch_vccnz LSITE8_%=
LBACK8_%=:
MM v[76:79],a[208:211],v[164:167],v[132:135]
EX v64,v64
MM v[76:79],a[212:215],v[180:183],v[76:79]
EX v65,v65
MM v[76:79],a[216:219],v[196:199],v[76:79]
TR a[144:145],v232 offset:512
CV v64,v64,v65
MM v[76:79],a[220:223],v[212:215],v[76:79]
EX v66,v66
MM v[72:75],a[204:207],v[212:215],v[132:135]
EX v67,v67
MM v[72:75],a[200:203],v[196:199],v[72:75]
TR a[146:147],v232 offset:4608
CV v65,v66,v67
MM v[72:75],a[196:199],v[180:183],v[72:75]
EX v68,v68
MM v[72:75],a[192:195],v[164:167],v[72:75]
TR a[148:149],v232 offset:8704
TR a[150:151],v232 offset:12800
MM v[80:83],a[192:195],v[168:171],v[136:139]
EX v69,v69
MM v[80:83],a[196:199],v[184:187],v[80:83]
TR a[152:153],v233 offset:512
CV v66,v68,v69
MM v[80:83],a[200:203],v[200:203],v[80:83]
EX v70,v70
MM v[80:83],a[204:207],v[216:219],v[80:83]
TR a[154:155],v233 offset:4608
TR a[156:157],v233 offset:8704
MM v[84:87],a[220:223],v[216:219],v[136:139]
EX v71,v71
MM v[84:87],a[216:219],v[200:203],v[84:87]
TR a[158:159],v233 offset:12800
CV v67,v70,v71
MM v[84:87],a[212:215],v[184:187],v[84:87]
EX v72,v72
MM v[84:87],a[208:211],v[168:171],v[84:87]
EX v73,v73
MM v[92:95],a[208:211],v[172:175],v[140:143]
TR a[160:161],v232 offset:1024
CV v72,v72,v73
MM v[92:95],a[212:215],v[188:191],v[92:95]
EX v74,v74
MM v[92:95],a[216:219],v[204:207],v[92:95]
TR a[162:163],v232 offset:5120
TR a[164:165],v232 offset:9216
MM v[92:95],a[220:223],v[220:223],v[92:95]
EX v75,v75
MM v[88:91],a[204:207],v[220:223],v[140:143]
TR a[166:167],v232 offset:13312
CV v73,v74,v75
MM v[88:91],a[200:203],v[204:207],v[88:91]
EX v76,v76
MM v[88:91],a[196:199],v[188:191],v[88:91]
EX v77,v77
MM v[88:91],a[192:195],v[172:175],v[88:91]
TR a[168:169],v233 offset:1024
CV v74,v76,v77
MM v[96:99],a[224:227],v[160:163],v[128:131]
EX v78,v78
MM v[96:99],a[228:231],v[176:179],v[96:99]
TR a[170:171],v233 offset:5120
TR a[172:173],v233 offset:9216
MM v[96:99],a[232:235],v[192:195],v[96:99]
EX v79,v79
MM v[96:99],a[236:239],v[208:211],v[96:99]
TR a[174:175],v233 offset:13312
CV v75,v78,v79
MM v[100:103],a[252:255],v[208:211],v[128:131]
EX v80,v80
MM v[100:103],a[248:251],v[192:195],v[100:103]
EX v81,v81
MM v[100:103],a[244:247],v[176:179],v[100:103]
TR a[176:177],v232 offset:1536
CV v80,v80,v81
MM v[100:103],a[240:243],v[160:163],v[100:103]
EX v82,v82
MM v[108:111],a[240:243],v[164:167],v[132:135]
TR a[178:179],v232 offset:5632
TR a[180:181],v232 offset:9728
MM v[108:111],a[244:247],v[180:183],v[108:111]
EX v83,v83
MM v[108:111],a[248:251],v[196:199],v[108:111]
TR a[182:183],v232 offset:13824
CV v81,v82,v83
MM v[108:111],a[252:255],v[212:215],v[108:111]
EX v84,v84
MM v[104:107],a[236:239],v[212:215],v[132:135]
TR a[184:185],v233 offset:1536
TR a[186:187],v233 offset:5632
MM v[104:107],a[232:235],v[196:199],v[104:107]
EX v85,v85
MM v[104:107],a[228:231],v[180:183],v[104:107]
TR a[188:189],v233 offset:9728
CV v82,v84,v85
MM v[104:107],a[224:227],v[164:167],v[104:107]
EX v86,v86
MM v[112:115],a[224:227],v[168:171],v[136:139]
EX v87,v87
MM v[112:115],a[228:231],v[184:187],v[112:115]
TR a[190:191],v233 offset:13824
CV v83,v86,v87
MM v[112:115],a[232:235],v[200:203],v[112:115]
EX v88,v88
MM v[112:115],a[236:239],v[216:219],v[112:115]
EX v89,v89
MM v[116:119],a[252:255],v[216:219],v[136:139]
EX v90,v90
MM v[116:119],a[248:251],v[200:203],v[116:119]
EX v91,v91
MM v[116:119],a[244:247],v[184:187],v[116:119]
EX v92,v92
MM v[116:119],a[240:243],v[168:171],v[116:119]
EX v93,v93
MM v[124:127],a[240:243],v[172:175],v[140:143]
EX v94,v94
MM v[124:127],a[244:247],v[188:191],v[124:127]
EX v95,v95
MM v[124:127],a[248:251],v[204:207],v[124:127]
EX v96,v96
MM v[124:127],a[252:255],v[220:223],v[124:127]
EX v97,v97
MM v[120:123],a[236:239],v[220:223],v[140:143]
EX v98,v98
MM v[120:123],a[232:235],v[204:207],v[120:123]
EX v99,v99
MM v[120:123],a[228:231],v[188:191],v[120:123]
s_cmp_eq_u32 s70, 1
s_cbranch_scc1 LTP_%=
LLALTP_%=:
MM v[120:123],a[224:227],v[172:175],v[120:123]
s_waitcnt vmcnt(12) lgkmcnt(0)
s_barrier
MM a[0:3],a[128:131],v[0:3],a[0:3]
s_add_u32 m0, s65, 0
CV v88,v88,v89
CV v89,v90,v91
MM a[0:3],a[132:135],v[4:7],a[0:3]
RD a[192:195],v224 offset:32768
CV v90,v92,v93
MM a[8:11],a[132:135],v[12:15],a[8:11]
RD a[196:199],v225 offset:32768
CV v91,v94,v95
MM a[8:11],a[128:131],v[8:11],a[8:11]
buffer_load_dwordx4 v228, s[8:11], s64 offen offset:0 lds
MM a[16:19],a[128:131],v[16:19],a[16:19]
RD a[200:203],v226 offset:32768
CV v68,v96,v97
MM a[16:19],a[132:135],v[20:23],a[16:19]
RD a[204:207],v227 offset:32768
CV v69,v98,v99
MM a[24:27],a[132:135],v[28:31],a[24:27]
buffer_load_dwordx4 v229, s[8:11], s64 offen offset:1024 lds
MM a[24:27],a[128:131],v[24:27],a[24:27]
RD a[208:211],v224 offset:36864
RD a[212:215],v225 offset:36864
MM a[28:31],a[136:139],v[24:27],a[28:31]
buffer_load_dwordx4 v230, s[8:11], s64 offen offset:2048 lds
MM a[28:31],a[140:143],v[28:31],a[28:31]
EX v100,v100
MM a[20:23],a[140:143],v[20:23],a[20:23]
EX v101,v101
MM a[20:23],a[136:139],v[16:19],a[20:23]
RD a[216:219],v226 offset:36864
CV v70,v100,v101
MM a[12:15],a[136:139],v[8:11],a[12:15]
EX v102,v102
MM a[12:15],a[140:143],v[12:15],a[12:15]
RD a[220:223],v227 offset:36864
RD a[224:227],v224 offset:40960
MM a[4:7],a[140:143],v[4:7],a[4:7]
EX v103,v103
MM a[4:7],a[136:139],v[0:3],a[4:7]
RD a[228:231],v225 offset:40960
CV v71,v102,v103
MM a[32:35],a[144:147],v[0:3],a[32:35]
EX v104,v104
MM a[32:35],a[148:151],v[4:7],a[32:35]
buffer_load_dwordx4 v231, s[8:11], s64 offen offset:3072 lds
MM a[40:43],a[148:151],v[12:15],a[40:43]
s_add_u32 m0, s65, 65536
EX v105,v105
MM a[40:43],a[144:147],v[8:11],a[40:43]
RD a[232:235],v226 offset:40960
CV v76,v104,v105
MM a[48:51],a[144:147],v[16:19],a[48:51]
EX v106,v106
MM a[48:51],a[148:151],v[20:23],a[48:51]
EX v107,v107
MM a[56:59],a[148:151],v[28:31],a[56:59]
RD a[236:239],v227 offset:40960
CV v77,v106,v107
MM a[56:59],a[144:147],v[24:27],a[56:59]
EX v108,v108
MM a[60:63],a[152:155],v[24:27],a[60:63]
buffer_load_dwordx4 v235, s[12:15], s81 offen offset:0 lds
MM a[60:63],a[156:159],v[28:31],a[60:63]
EX v109,v109
MM a[52:55],a[156:159],v[20:23],a[52:55]
RD a[240:243],v224 offset:45056
CV v78,v108,v109
MM a[52:55],a[152:155],v[16:19],a[52:55]
EX v110,v110
MM a[44:47],a[152:155],v[8:11],a[44:47]
EX v111,v111
MM a[44:47],a[156:159],v[12:15],a[44:47]
RD a[244:247],v225 offset:45056
CV v79,v110,v111
MM a[36:39],a[156:159],v[4:7],a[36:39]
)ASM"
R"ASM(
EX v112,v112
MM a[36:39],a[152:155],v[0:3],a[36:39]
buffer_load_dwordx4 v235, s[12:15], s64 offen offset:1024 lds
MM a[64:67],a[160:163],v[0:3],a[64:67]
EX v113,v113
MM a[64:67],a[164:167],v[4:7],a[64:67]
RD a[248:251],v226 offset:45056
s_nop 0
LPG9_%=:
s_cbranch_execz LTR0_%=
CV v84,v112,v113
MM a[72:75],a[164:167],v[12:15],a[72:75]
EX v114,v114
MM a[72:75],a[160:163],v[8:11],a[72:75]
EX v115,v115
MM a[80:83],a[160:163],v[16:19],a[80:83]
RD a[252:255],v227 offset:45056
CV v85,v114,v115
MM a[80:83],a[164:167],v[20:23],a[80:83]
buffer_load_dwordx4 v235, s[12:15], s81 offen offset:2048 lds
MM a[88:91],a[164:167],v[28:31],a[88:91]
EX v116,v116
MM a[88:91],a[160:163],v[24:27],a[88:91]
EX v117,v117
MM a[92:95],a[168:171],v[24:27],a[92:95]
EX v118,v118
MM a[92:95],a[172:175],v[28:31],a[92:95]
buffer_load_dwordx4 v235, s[12:15], s64 offen offset:3072 lds
MM a[84:87],a[172:175],v[20:23],a[84:87]
EX v119,v119
MM a[84:87],a[168:171],v[16:19],a[84:87]
EX v120,v120
MM a[76:79],a[168:171],v[8:11],a[76:79]
EX v121,v121
MM a[76:79],a[172:175],v[12:15],a[76:79]
EX v122,v122
MM a[68:71],a[172:175],v[4:7],a[68:71]
EX v123,v123
MM a[68:71],a[168:171],v[0:3],a[68:71]
EX v124,v124
MM a[96:99],a[176:179],v[0:3],a[96:99]
EX v125,v125
MM a[96:99],a[180:183],v[4:7],a[96:99]
EX v126,v126
MM a[104:107],a[180:183],v[12:15],a[104:107]
EX v127,v127
MM a[104:107],a[176:179],v[8:11],a[104:107]
CV v86,v116,v117
CV v87,v118,v119
MM a[112:115],a[176:179],v[16:19],a[112:115]
CV v92,v120,v121
CV v93,v122,v123
MM a[112:115],a[180:183],v[20:23],a[112:115]
CV v94,v124,v125
CV v95,v126,v127
MM a[120:123],a[180:183],v[28:31],a[120:123]
MM a[120:123],a[176:179],v[24:27],a[120:123]
MM a[124:127],a[184:187],v[24:27],a[124:127]
v_add_f32 v244, v144, v145
v_add_f32 v245, v148, v149
MM a[124:127],a[188:191],v[28:31],a[124:127]
v_add_f32 v246, v152, v153
v_add_f32 v247, v156, v157
MM a[116:119],a[188:191],v[20:23],a[116:119]
s_cmp_eq_u32 s70, s82
s_cbranch_scc1 LWV_%=
LLALWV_%=:
MM a[116:119],a[184:187],v[16:19],a[116:119]
MM a[108:111],a[184:187],v[8:11],a[108:111]
MM a[108:111],a[188:191],v[12:15],a[108:111]
MM a[100:103],a[188:191],v[4:7],a[100:103]
MM a[100:103],a[184:187],v[0:3],a[100:103]
v_smfmac_f32_16x16x64_bf16 v[144:147], v[236:239], v[64:71], v234
v_smfmac_f32_16x16x64_bf16 v[148:151], v[236:239], v[72:79], v234
v_smfmac_f32_16x16x64_bf16 v[152:155], v[236:239], v[80:87], v234
v_smfmac_f32_16x16x64_bf16 v[156:159], v[236:239], v[88:95], v234
s_waitcnt lgkmcnt(0)
s_sub_u32 s70, s70, 1
s_cmp_lg_u32 s70, 0
s_cbranch_scc1 LLAP_%=
s_branch LLAJ_%=
LWW_%=:
v_lshlrev_b32 v250, 3, v248
v_min_u32 v250, 4992, v250
s_sub_u32 s91, 5242880, s26
s_sub_u32 s60, s24, s91
s_subb_u32 s61, s25, 0
s_mov_b32 s62, 5242880
s_mov_b32 s63, 131072
s_lshl_b32 s90, s16, 2
s_add_u32 s90, s90, %[swave]
s_mul_i32 s90, s90, 10240
s_add_u32 s90, s90, 0
s_add_u32 s91, %[slds], 135168
s_mov_b32 m0, s91
s_nop 0
buffer_load_dword v250, s[60:63], s90 offen offset:0 lds
s_branch LLALWW_%=
LIP_%=:
s_lshr_b32 s77, %[sb], 3
s_lshl_b32 s77, s77, 2
s_add_u32 s77, s77, %[swave]
s_lshl_b32 s77, s77, 9
s_add_u32 s77, s77, LLAJ_%=-LPDP_%=
v_lshrrev_b32 v253, 1, v248
v_add_u32 v253, s77, v253
s_mov_b32 s77, LKEND_ka-LPDP_%=-16
v_min_u32 v253, s77, v253
s_getpc_b64 s[98:99]
LPDP_%=:
global_load_dword v253, v253, s[98:99]
s_branch LLALIP_%=
LTP_%=:
s_getpc_b64 s[98:99]
LTA1_%=:
s_min_u32 s73, %[swave], 3
s_lshl_b32 s73, s73, 12
s_add_u32 s73, s73, LPG23_%=-LTA1_%=
s_add_u32 s98, s98, s73
s_addc_u32 s99, s99, 0
s_mov_b64 exec, 0
s_setpc_b64 s[98:99]
LTR1_%=:
s_mov_b64 exec, -1
s_branch LLALTP_%=
LWV_%=:
v_lshlrev_b32 v250, 3, v248
v_min_u32 v250, 4992, v250
s_sub_u32 s91, 5242880, s26
s_sub_u32 s60, s24, s91
s_subb_u32 s61, s25, 0
s_mov_b32 s62, 5242880
s_mov_b32 s63, 131072
s_lshl_b32 s90, s16, 2
s_add_u32 s90, s90, %[swave]
s_mul_i32 s90, s90, 10240
s_add_u32 s90, s90, 5120
s_add_u32 s91, %[slds], 135168
s_mov_b32 m0, s91
s_nop 0
buffer_load_dword v250, s[60:63], s90 offen offset:0 lds
s_branch LLALWV_%=
s_nop 0
s_nop 0
s_nop 0
s_nop 0
s_nop 0
s_nop 0
s_nop 0
s_nop 0
s_nop 0
s_nop 0
s_nop 0
s_nop 0
s_nop 0
s_nop 0
s_nop 0
s_nop 0
s_nop 0
s_nop 0
s_nop 0
s_nop 0
s_nop 0
s_nop 0
s_nop 0
s_nop 0
s_nop 0
s_nop 0
s_nop 0
s_nop 0
s_nop 0
s_nop 0
s_nop 0
s_nop 0
s_nop 0
s_nop 0
s_nop 0
s_nop 0
s_nop 0
s_nop 0
s_nop 0
s_nop 0
s_nop 0
s_nop 0
s_nop 0
s_nop 0
s_nop 0
s_nop 0
s_nop 0
s_nop 0
s_nop 0
s_nop 0
s_nop 0
s_nop 0
s_nop 0
s_nop 0
s_nop 0
s_nop 0
s_nop 0
s_nop 0
s_nop 0
s_nop 0
s_nop 0
s_nop 0
s_nop 0
s_nop 0
s_nop 0
s_nop 0
s_nop 0
s_nop 0
s_nop 0
s_nop 0
s_nop 0
s_nop 0
s_nop 0
s_nop 0
s_nop 0
s_nop 0
s_nop 0
s_nop 0
s_nop 0
s_nop 0
s_nop 0
s_nop 0
s_nop 0
s_nop 0
s_nop 0
s_nop 0
s_nop 0
s_nop 0
s_nop 0
s_nop 0
s_nop 0
s_nop 0
s_nop 0
s_nop 0
s_nop 0
s_nop 0
s_nop 0
s_nop 0
s_nop 0
s_nop 0
s_nop 0
s_nop 0
s_nop 0
s_nop 0
s_nop 0
s_nop 0
s_nop 0
s_nop 0
s_nop 0
s_nop 0
s_nop 0
s_nop 0
s_nop 0
s_nop 0
s_nop 0
s_nop 0
s_nop 0
s_nop 0
s_nop 0
s_nop 0
s_nop 0
s_nop 0
s_nop 0
s_nop 0
s_nop 0
s_nop 0
s_nop 0
s_nop 0
s_nop 0
s_nop 0
s_nop 0
s_nop 0
s_nop 0
s_nop 0
s_nop 0
s_nop 0
s_nop 0
s_nop 0
s_nop 0
s_nop 0
s_nop 0
s_nop 0
s_nop 0
s_nop 0
s_nop 0
s_nop 0
s_nop 0
s_nop 0
s_nop 0
s_nop 0
s_nop 0
s_nop 0
s_nop 0
s_nop 0
s_nop 0
s_nop 0
s_nop 0
s_nop 0
s_nop 0
s_nop 0
s_nop 0
s_nop 0
s_nop 0
s_nop 0
s_nop 0
s_nop 0
s_nop 0
s_nop 0
s_nop 0
s_nop 0
s_nop 0
s_nop 0
s_nop 0
s_nop 0
s_nop 0
s_nop 0
s_nop 0
s_nop 0
s_nop 0
s_nop 0
s_nop 0
s_nop 0
s_nop 0
s_nop 0
s_nop 0
s_nop 0
s_nop 0
s_nop 0
s_nop 0
s_nop 0
s_nop 0
s_nop 0
s_nop 0
s_nop 0
s_nop 0
s_nop 0
s_nop 0
s_nop 0
s_nop 0
s_nop 0
s_nop 0
s_nop 0
s_nop 0
s_nop 0
s_nop 0
s_nop 0
s_nop 0
s_nop 0
s_nop 0
s_nop 0
s_nop 0
s_nop 0
s_nop 0
s_nop 0
s_nop 0
s_nop 0
s_nop 0
s_nop 0
s_nop 0
s_nop 0
s_nop 0
s_nop 0
s_nop 0
s_nop 0
s_nop 0
s_nop 0
s_nop 0
s_nop 0
s_nop 0
s_nop 0
s_nop 0
s_nop 0
s_nop 0
s_nop 0
s_nop 0
s_nop 0
s_nop 0
s_nop 0
s_nop 0
s_nop 0
s_nop 0
s_nop 0
s_nop 0
s_nop 0
s_nop 0
s_nop 0
s_nop 0
s_nop 0
s_nop 0
s_nop 0
s_nop 0
s_nop 0
s_nop 0
s_nop 0
s_nop 0
s_nop 0
s_nop 0
s_nop 0
s_nop 0
s_nop 0
s_nop 0
s_nop 0
s_nop 0
s_nop 0
s_nop 0
s_nop 0
s_nop 0
s_nop 0
s_nop 0
s_nop 0
s_nop 0
s_nop 0
s_nop 0
s_nop 0
s_nop 0
s_nop 0
s_nop 0
s_nop 0
s_nop 0
s_nop 0
s_nop 0
s_nop 0
s_nop 0
s_nop 0
s_nop 0
s_nop 0
s_nop 0
s_nop 0
s_nop 0
s_nop 0
s_nop 0
s_nop 0
s_nop 0
s_nop 0
s_nop 0
s_nop 0
s_nop 0
s_nop 0
s_nop 0
s_nop 0
s_nop 0
s_nop 0
s_nop 0
s_nop 0
s_nop 0
s_nop 0
s_nop 0
s_nop 0
s_nop 0
s_nop 0
s_nop 0
s_nop 0
s_nop 0
s_nop 0
s_nop 0
s_nop 0
s_nop 0
s_nop 0
s_nop 0
s_nop 0
s_nop 0
s_nop 0
s_nop 0
s_nop 0
s_nop 0
s_nop 0
s_nop 0
s_nop 0
s_nop 0
s_nop 0
s_nop 0
s_nop 0
s_nop 0
s_nop 0
s_nop 0
s_nop 0
s_nop 0
s_nop 0
s_nop 0
s_nop 0
s_nop 0
s_nop 0
s_nop 0
s_nop 0
s_nop 0
s_nop 0
s_nop 0
s_nop 0
s_nop 0
s_nop 0
s_nop 0
s_nop 0
s_nop 0
s_nop 0
s_nop 0
s_nop 0
s_nop 0
s_nop 0
s_nop 0
s_nop 0
s_nop 0
s_nop 0
s_nop 0
s_nop 0
s_nop 0
s_nop 0
s_nop 0
s_nop 0
s_nop 0
s_nop 0
s_nop 0
s_nop 0
s_nop 0
s_nop 0
s_nop 0
s_nop 0
s_nop 0
s_nop 0
s_nop 0
s_nop 0
s_nop 0
s_nop 0
s_nop 0
s_nop 0
s_nop 0
s_nop 0
s_nop 0
s_nop 0
s_nop 0
s_nop 0
s_nop 0
s_nop 0
s_nop 0
s_nop 0
s_nop 0
s_nop 0
s_nop 0
s_nop 0
s_nop 0
s_nop 0
s_nop 0
s_nop 0
s_nop 0
s_nop 0
s_nop 0
s_nop 0
s_nop 0
LLAR_%=:
MM v[0:3],a[192:195],v[160:163],v[128:131]
s_add_u32 s64, s64, 16384
TR a[128:129],v232 offset:16384
s_add_u32 s81, s64, 896
MM v[0:3],a[196:199],v[176:179],v[0:3]
TR a[130:131],v232 offset:20480
v_max3_f32 v249, v144, v145, v148
MM v[0:3],a[200:203],v[192:195],v[0:3]
TR a[132:133],v232 offset:24576
v_max3_f32 v249, v249, v149, v152
MM v[0:3],a[204:207],v[208:211],v[0:3]
TR a[134:135],v232 offset:28672
v_max3_f32 v249, v249, v153, v156
MM v[4:7],a[220:223],v[208:211],v[128:131]
TR a[136:137],v233 offset:16384
v_max_f32 v249, v249, v157
MM v[4:7],a[216:219],v[192:195],v[4:7]
TR a[138:139],v233 offset:20480
v_cmp_lt_f32 vcc, s71, v249
MM v[4:7],a[212:215],v[176:179],v[4:7]
TR a[140:141],v233 offset:24576
TR a[142:143],v233 offset:28672
MM v[4:7],a[208:211],v[160:163],v[4:7]
s_cbranch_vccnz LSITE9_%=
LBACK9_%=:
MM v[12:15],a[208:211],v[164:167],v[132:135]
s_lshl_b32 s17, s16, 2
EX v0,v0
MM v[12:15],a[212:215],v[180:183],v[12:15]
s_add_u32 s17, s17, %[swave]
EX v1,v1
MM v[12:15],a[216:219],v[196:199],v[12:15]
s_sub_u32 s90, s16, 108
TR a[144:145],v232 offset:16896
CV v0,v0,v1
MM v[12:15],a[220:223],v[212:215],v[12:15]
s_min_u32 s90, s90, s16
EX v2,v2
MM v[8:11],a[204:207],v[212:215],v[132:135]
s_lshl_b32 s48, s90, 5
EX v3,v3
MM v[8:11],a[200:203],v[196:199],v[8:11]
s_add_u32 s76, %[slds], 155648
v_add_u32 v250, s76, v248
CV v1,v2,v3
MM v[8:11],a[196:199],v[180:183],v[8:11]
s_lshl_b32 s77, s17, 4
TR a[146:147],v232 offset:20992
TR a[148:149],v232 offset:25088
MM v[8:11],a[192:195],v[164:167],v[8:11]
s_add_u32 s76, s76, s77
TR a[150:151],v232 offset:29184
v_mov_b32 v251, s76
MM v[16:19],a[192:195],v[168:171],v[136:139]
RD v[124:127],v251 offset:0
RD v[96:99],v250 offset:0
MM v[16:19],a[196:199],v[184:187],v[16:19]
RD v[100:103],v250 offset:1024
RD v[104:107],v250 offset:2048
MM v[16:19],a[200:203],v[200:203],v[16:19]
RD v[108:111],v250 offset:3072
RD v[112:115],v250 offset:4096
MM v[16:19],a[204:207],v[216:219],v[16:19]
RD v[116:119],v250 offset:5120
RD v[120:123],v250 offset:6144
MM v[20:23],a[220:223],v[216:219],v[136:139]
EX v4,v4
MM v[20:23],a[216:219],v[200:203],v[20:23]
EX v5,v5
MM v[20:23],a[212:215],v[184:187],v[20:23]
s_waitcnt lgkmcnt(7)
v_readfirstlane_b32 s52, v124
v_readfirstlane_b32 s53, v125
MM v[20:23],a[208:211],v[168:171],v[20:23]
v_readfirstlane_b32 s54, v126
v_readfirstlane_b32 s55, v127
EX v6,v6
MM v[28:31],a[208:211],v[172:175],v[140:143]
RD v[124:127],v250 offset:7168
TR a[152:153],v233 offset:16896
MM v[28:31],a[212:215],v[188:191],v[28:31]
s_waitcnt lgkmcnt(2)
v_max3_u32 v253, v96, v97, v98
v_max3_u32 v251, v99, v100, v101
EX v7,v7
MM v[28:31],a[216:219],v[204:207],v[28:31]
v_max3_u32 v253, v253, v102, v103
v_max3_u32 v251, v251, v104, v105
MM v[28:31],a[220:223],v[220:223],v[28:31]
v_max3_u32 v253, v253, v106, v107
v_max3_u32 v251, v251, v108, v109
EX v8,v8
MM v[24:27],a[204:207],v[220:223],v[140:143]
v_max3_u32 v253, v253, v110, v111
v_max3_u32 v251, v251, v112, v113
MM v[24:27],a[200:203],v[204:207],v[24:27]
v_max3_u32 v253, v253, v114, v115
v_max3_u32 v251, v251, v116, v117
EX v9,v9
MM v[24:27],a[196:199],v[188:191],v[24:27]
TR a[154:155],v233 offset:20992
v_max3_u32 v253, v253, v118, v119
v_max3_u32 v251, v251, v120, v121
MM v[24:27],a[192:195],v[172:175],v[24:27]
v_max3_u32 v253, v253, v122, v123
CV v2,v4,v5
EX v10,v10
MM v[32:35],a[224:227],v[160:163],v[128:131]
s_waitcnt lgkmcnt(2)
TR a[156:157],v233 offset:25088
v_max3_u32 v251, v251, v124, v125
v_max3_u32 v253, v253, v126, v127
EX v11,v11
MM v[32:35],a[228:231],v[176:179],v[32:35]
v_max_u32 v253, v253, v251
v_cmp_lt_u32 s[60:61], s52, v253
MM v[32:35],a[232:235],v[192:195],v[32:35]
TR a[158:159],v233 offset:29184
v_cmp_lt_u32 s[62:63], s53, v253
v_cmp_lt_u32 s[56:57], s54, v253
EX v12,v12
MM v[32:35],a[236:239],v[208:211],v[32:35]
v_cmp_lt_u32 s[58:59], s55, v253
CV v3,v6,v7
MM v[36:39],a[252:255],v[208:211],v[128:131]
TR a[160:161],v232 offset:17408
EX v13,v13
MM v[36:39],a[248:251],v[192:195],v[36:39]
s_bcnt1_i32_b64 s60, s[60:61]
TR a[162:163],v232 offset:21504
CV v8,v8,v9
MM v[36:39],a[244:247],v[176:179],v[36:39]
s_bcnt1_i32_b64 s62, s[62:63]
EX v14,v14
MM v[36:39],a[240:243],v[160:163],v[36:39]
s_bcnt1_i32_b64 s56, s[56:57]
TR a[164:165],v232 offset:25600
TR a[166:167],v232 offset:29696
MM v[44:47],a[240:243],v[164:167],v[132:135]
s_bcnt1_i32_b64 s58, s[58:59]
EX v15,v15
MM v[44:47],a[244:247],v[180:183],v[44:47]
s_min_u32 s76, s60, s62
EX v16,v16
MM v[44:47],a[248:251],v[196:199],v[44:47]
s_min_u32 s77, s56, s58
TR a[168:169],v233 offset:17408
CV v9,v10,v11
MM v[44:47],a[252:255],v[212:215],v[44:47]
s_min_u32 s76, s76, s77
s_cmp_lt_u32 s76, 8
s_cbranch_scc1 LRK_%=
LRKb_%=:
EX v17,v17
MM v[40:43],a[236:239],v[212:215],v[132:135]
v_lshrrev_b32 v252, 6, v248
v_mul_u32_u24 v252, 3456, v252
)ASM"
R"ASM(
MM v[40:43],a[232:235],v[196:199],v[40:43]
TR a[170:171],v233 offset:21504
v_and_b32 v251, 48, v248
v_add_u32 v252, v252, v251
EX v18,v18
MM v[40:43],a[228:231],v[180:183],v[40:43]
s_and_b32 s90, %[swave], 1
TR a[172:173],v233 offset:25600
CV v10,v12,v13
MM v[40:43],a[224:227],v[164:167],v[40:43]
s_mul_i32 s90, s90, 276480
EX v19,v19
MM v[48:51],a[224:227],v[168:171],v[136:139]
s_bfe_u32 s91, s48, 131077
TR a[174:175],v233 offset:29696
CV v11,v14,v15
MM v[48:51],a[228:231],v[184:187],v[48:51]
s_mul_i32 s91, s91, 552960
EX v20,v20
MM v[48:51],a[232:235],v[200:203],v[48:51]
s_add_u32 s90, s90, s91
TR a[176:177],v232 offset:17920
TR a[178:179],v232 offset:22016
MM v[48:51],a[236:239],v[216:219],v[48:51]
s_and_b32 s91, s48, 0xffffff80
EX v21,v21
MM v[52:55],a[252:255],v[216:219],v[136:139]
s_add_u32 s90, s90, s91
EX v22,v22
MM v[52:55],a[248:251],v[200:203],v[52:55]
s_and_b32 s91, s48, 16
TR a[180:181],v232 offset:26112
CV v16,v16,v17
MM v[52:55],a[244:247],v[184:187],v[52:55]
s_lshl_b32 s91, s91, 2
EX v23,v23
MM v[52:55],a[240:243],v[168:171],v[52:55]
s_add_u32 s66, s90, s91
TR a[182:183],v232 offset:30208
CV v17,v18,v19
MM v[60:63],a[240:243],v[172:175],v[140:143]
s_mul_i32 s91, %[swave], 5120
EX v24,v24
MM v[60:63],a[244:247],v[188:191],v[60:63]
s_add_u32 s91, s91, %[slds]
TR a[184:185],v233 offset:17920
TR a[186:187],v233 offset:22016
MM v[60:63],a[248:251],v[204:207],v[60:63]
s_add_u32 s67, s91, 131072
TR a[188:189],v233 offset:26112
TR a[190:191],v233 offset:30208
EX v25,v25
MM v[60:63],a[252:255],v[220:223],v[60:63]
EX v26,v26
MM v[56:59],a[236:239],v[220:223],v[140:143]
EX v27,v27
MM v[56:59],a[232:235],v[204:207],v[56:59]
EX v28,v28
MM v[56:59],a[228:231],v[188:191],v[56:59]
EX v29,v29
CV v18,v20,v21
MM v[56:59],a[224:227],v[172:175],v[56:59]
s_waitcnt lgkmcnt(0)
s_barrier
MM a[0:3],a[128:131],v[64:67],a[0:3]
s_add_u32 m0, s65, 16384
CV v19,v22,v23
CV v24,v24,v25
MM a[0:3],a[132:135],v[68:71],a[0:3]
CV v25,v26,v27
CV v26,v28,v29
MM a[8:11],a[132:135],v[76:79],a[8:11]
EX v30,v30
MM a[8:11],a[128:131],v[72:75],a[8:11]
EX v31,v31
MM a[16:19],a[128:131],v[80:83],a[16:19]
RD a[192:195],v224 offset:49152
CV v27,v30,v31
MM a[16:19],a[132:135],v[84:87],a[16:19]
EX v32,v32
MM a[24:27],a[132:135],v[92:95],a[24:27]
buffer_load_dwordx4 v228, s[8:11], s64 offen offset:0 lds
MM a[24:27],a[128:131],v[88:91],a[24:27]
EX v33,v33
MM a[28:31],a[136:139],v[88:91],a[28:31]
RD a[196:199],v225 offset:49152
CV v4,v32,v33
MM a[28:31],a[140:143],v[92:95],a[28:31]
EX v34,v34
MM a[20:23],a[140:143],v[84:87],a[20:23]
EX v35,v35
MM a[20:23],a[136:139],v[80:83],a[20:23]
RD a[200:203],v226 offset:49152
CV v5,v34,v35
MM a[12:15],a[136:139],v[72:75],a[12:15]
EX v36,v36
MM a[12:15],a[140:143],v[76:79],a[12:15]
s_nop 0
LPG10_%=:
s_cbranch_execz LTR0_%=
buffer_load_dwordx4 v229, s[8:11], s64 offen offset:1024 lds
MM a[4:7],a[140:143],v[68:71],a[4:7]
EX v37,v37
MM a[4:7],a[136:139],v[64:67],a[4:7]
RD a[204:207],v227 offset:49152
CV v6,v36,v37
MM a[32:35],a[144:147],v[64:67],a[32:35]
EX v38,v38
MM a[32:35],a[148:151],v[68:71],a[32:35]
EX v39,v39
MM a[40:43],a[148:151],v[76:79],a[40:43]
RD a[208:211],v224 offset:53248
CV v7,v38,v39
MM a[40:43],a[144:147],v[72:75],a[40:43]
EX v40,v40
MM a[48:51],a[144:147],v[80:83],a[48:51]
buffer_load_dwordx4 v230, s[8:11], s64 offen offset:2048 lds
MM a[48:51],a[148:151],v[84:87],a[48:51]
EX v41,v41
MM a[56:59],a[148:151],v[92:95],a[56:59]
RD a[212:215],v225 offset:53248
CV v12,v40,v41
MM a[56:59],a[144:147],v[88:91],a[56:59]
EX v42,v42
MM a[60:63],a[152:155],v[88:91],a[60:63]
EX v43,v43
MM a[60:63],a[156:159],v[92:95],a[60:63]
RD a[216:219],v226 offset:53248
CV v13,v42,v43
MM a[52:55],a[156:159],v[84:87],a[52:55]
EX v44,v44
MM a[52:55],a[152:155],v[80:83],a[52:55]
buffer_load_dwordx4 v231, s[8:11], s64 offen offset:3072 lds
MM a[44:47],a[152:155],v[72:75],a[44:47]
s_add_u32 m0, s65, 81920
EX v45,v45
MM a[44:47],a[156:159],v[76:79],a[44:47]
RD a[220:223],v227 offset:53248
CV v14,v44,v45
MM a[36:39],a[156:159],v[68:71],a[36:39]
EX v46,v46
MM a[36:39],a[152:155],v[64:67],a[36:39]
RD a[224:227],v224 offset:57344
RD a[228:231],v225 offset:57344
MM a[64:67],a[160:163],v[64:67],a[64:67]
EX v47,v47
MM a[64:67],a[164:167],v[68:71],a[64:67]
RD a[232:235],v226 offset:57344
CV v15,v46,v47
MM a[72:75],a[164:167],v[76:79],a[72:75]
EX v48,v48
MM a[72:75],a[160:163],v[72:75],a[72:75]
buffer_load_dwordx4 v235, s[12:15], s81 offen offset:0 lds
MM a[80:83],a[160:163],v[80:83],a[80:83]
s_add_u32 m0, s67, 0
s_add_u32 s85, s66, 0
buffer_load_dwordx4 v252, s[36:39], s85 offen offset:0 lds
MM a[80:83],a[164:167],v[84:87],a[80:83]
EX v49,v49
MM a[88:91],a[164:167],v[92:95],a[88:91]
EX v50,v50
MM a[88:91],a[160:163],v[88:91],a[88:91]
RD a[236:239],v227 offset:57344
CV v20,v48,v49
MM a[92:95],a[168:171],v[88:91],a[92:95]
EX v51,v51
MM a[92:95],a[172:175],v[92:95],a[92:95]
RD a[240:243],v224 offset:61440
CV v21,v50,v51
MM a[84:87],a[172:175],v[84:87],a[84:87]
EX v52,v52
MM a[84:87],a[168:171],v[80:83],a[84:87]
EX v53,v53
MM a[76:79],a[168:171],v[72:75],a[76:79]
RD a[244:247],v225 offset:61440
CV v22,v52,v53
MM a[76:79],a[172:175],v[76:79],a[76:79]
EX v54,v54
MM a[68:71],a[172:175],v[68:71],a[68:71]
s_add_u32 m0, s65, 81920
s_nop 0
buffer_load_dwordx4 v235, s[12:15], s64 offen offset:1024 lds
MM a[68:71],a[168:171],v[64:67],a[68:71]
EX v55,v55
MM a[96:99],a[176:179],v[64:67],a[96:99]
RD a[248:251],v226 offset:61440
CV v23,v54,v55
MM a[96:99],a[180:183],v[68:71],a[96:99]
EX v56,v56
MM a[104:107],a[180:183],v[76:79],a[104:107]
EX v57,v57
MM a[104:107],a[176:179],v[72:75],a[104:107]
RD a[252:255],v227 offset:61440
CV v28,v56,v57
MM a[112:115],a[176:179],v[80:83],a[112:115]
buffer_load_dwordx4 v235, s[12:15], s81 offen offset:2048 lds
MM a[112:115],a[180:183],v[84:87],a[112:115]
EX v58,v58
MM a[120:123],a[180:183],v[92:95],a[120:123]
EX v59,v59
MM a[120:123],a[176:179],v[88:91],a[120:123]
EX v60,v60
MM a[124:127],a[184:187],v[88:91],a[124:127]
v_add_f32 v244, v144, v145
v_add_f32 v245, v148, v149
MM a[124:127],a[188:191],v[92:95],a[124:127]
v_add_f32 v246, v152, v153
v_add_f32 v247, v156, v157
MM a[116:119],a[188:191],v[84:87],a[116:119]
EX v61,v61
MM a[116:119],a[184:187],v[80:83],a[116:119]
buffer_load_dwordx4 v235, s[12:15], s64 offen offset:3072 lds
MM a[108:111],a[184:187],v[72:75],a[108:111]
EX v62,v62
MM a[108:111],a[188:191],v[76:79],a[108:111]
EX v63,v63
MM a[100:103],a[188:191],v[68:71],a[100:103]
CV v29,v58,v59
CV v30,v60,v61
MM a[100:103],a[184:187],v[64:67],a[100:103]
CV v31,v62,v63
v_smfmac_f32_16x16x64_bf16 v[144:147], v[236:239], v[0:7], v234
v_smfmac_f32_16x16x64_bf16 v[148:151], v[236:239], v[8:15], v234
v_smfmac_f32_16x16x64_bf16 v[152:155], v[236:239], v[16:23], v234
v_smfmac_f32_16x16x64_bf16 v[156:159], v[236:239], v[24:31], v234
s_waitcnt lgkmcnt(0)
MM v[64:67],a[192:195],v[160:163],v[128:131]
s_add_u32 s64, s64, 16384
TR a[128:129],v232 offset:32768
s_add_u32 s81, s64, 896
MM v[64:67],a[196:199],v[176:179],v[64:67]
TR a[130:131],v232 offset:36864
v_max3_f32 v249, v144, v145, v148
MM v[64:67],a[200:203],v[192:195],v[64:67]
TR a[132:133],v232 offset:40960
v_max3_f32 v249, v249, v149, v152
MM v[64:67],a[204:207],v[208:211],v[64:67]
TR a[134:135],v232 offset:45056
v_max3_f32 v249, v249, v153, v156
MM v[68:71],a[220:223],v[208:211],v[128:131]
TR a[136:137],v233 offset:32768
v_max_f32 v249, v249, v157
MM v[68:71],a[216:219],v[192:195],v[68:71]
TR a[138:139],v233 offset:36864
v_cmp_lt_f32 vcc, s71, v249
MM v[68:71],a[212:215],v[176:179],v[68:71]
TR a[140:141],v233 offset:40960
TR a[142:143],v233 offset:45056
MM v[68:71],a[208:211],v[160:163],v[68:71]
s_cbranch_vccnz LSITE10_%=
LBACK10_%=:
MM v[76:79],a[208:211],v[164:167],v[132:135]
s_add_u32 m0, s67, 1024
s_add_u32 s85, s66, 55296
buffer_load_dwordx4 v252, s[36:39], s85 offen offset:0 lds
MM v[76:79],a[212:215],v[180:183],v[76:79]
EX v64,v64
MM v[76:79],a[216:219],v[196:199],v[76:79]
EX v65,v65
MM v[76:79],a[220:223],v[212:215],v[76:79]
TR a[144:145],v232 offset:33280
CV v64,v64,v65
MM v[72:75],a[204:207],v[212:215],v[132:135]
EX v66,v66
MM v[72:75],a[200:203],v[196:199],v[72:75]
EX v67,v67
MM v[72:75],a[196:199],v[180:183],v[72:75]
TR a[146:147],v232 offset:37376
CV v65,v66,v67
MM v[72:75],a[192:195],v[164:167],v[72:75]
EX v68,v68
MM v[80:83],a[192:195],v[168:171],v[136:139]
TR a[148:149],v232 offset:41472
TR a[150:151],v232 offset:45568
MM v[80:83],a[196:199],v[184:187],v[80:83]
EX v69,v69
MM v[80:83],a[200:203],v[200:203],v[80:83]
TR a[152:153],v233 offset:33280
CV v66,v68,v69
MM v[80:83],a[204:207],v[216:219],v[80:83]
EX v70,v70
MM v[84:87],a[220:223],v[216:219],v[136:139]
EX v71,v71
MM v[84:87],a[216:219],v[200:203],v[84:87]
TR a[154:155],v233 offset:37376
CV v67,v70,v71
MM v[84:87],a[212:215],v[184:187],v[84:87]
EX v72,v72
MM v[84:87],a[208:211],v[168:171],v[84:87]
TR a[156:157],v233 offset:41472
TR a[158:159],v233 offset:45568
MM v[92:95],a[208:211],v[172:175],v[140:143]
EX v73,v73
MM v[92:95],a[212:215],v[188:191],v[92:95]
TR a[160:161],v232 offset:33792
CV v72,v72,v73
MM v[92:95],a[216:219],v[204:207],v[92:95]
EX v74,v74
MM v[92:95],a[220:223],v[220:223],v[92:95]
EX v75,v75
MM v[88:91],a[204:207],v[220:223],v[140:143]
TR a[162:163],v232 offset:37888
CV v73,v74,v75
MM v[88:91],a[200:203],v[204:207],v[88:91]
TR a[164:165],v232 offset:41984
TR a[166:167],v232 offset:46080
MM v[88:91],a[196:199],v[188:191],v[88:91]
EX v76,v76
MM v[88:91],a[192:195],v[172:175],v[88:91]
EX v77,v77
MM v[96:99],a[224:227],v[160:163],v[128:131]
TR a[168:169],v233 offset:33792
CV v74,v76,v77
MM v[96:99],a[228:231],v[176:179],v[96:99]
EX v78,v78
MM v[96:99],a[232:235],v[192:195],v[96:99]
TR a[170:171],v233 offset:37888
TR a[172:173],v233 offset:41984
MM v[96:99],a[236:239],v[208:211],v[96:99]
EX v79,v79
MM v[100:103],a[252:255],v[208:211],v[128:131]
TR a[174:175],v233 offset:46080
CV v75,v78,v79
MM v[100:103],a[248:251],v[192:195],v[100:103]
EX v80,v80
MM v[100:103],a[244:247],v[176:179],v[100:103]
EX v81,v81
MM v[100:103],a[240:243],v[160:163],v[100:103]
TR a[176:177],v232 offset:34304
CV v80,v80,v81
MM v[108:111],a[240:243],v[164:167],v[132:135]
s_add_u32 m0, s67, 2048
s_add_u32 s85, s66, 110592
buffer_load_dwordx4 v252, s[36:39], s85 offen offset:0 lds
MM v[108:111],a[244:247],v[180:183],v[108:111]
EX v82,v82
MM v[108:111],a[248:251],v[196:199],v[108:111]
TR a[178:179],v232 offset:38400
TR a[180:181],v232 offset:42496
MM v[108:111],a[252:255],v[212:215],v[108:111]
EX v83,v83
MM v[104:107],a[236:239],v[212:215],v[132:135]
TR a[182:183],v232 offset:46592
CV v81,v82,v83
MM v[104:107],a[232:235],v[196:199],v[104:107]
EX v84,v84
MM v[104:107],a[228:231],v[180:183],v[104:107]
EX v85,v85
MM v[104:107],a[224:227],v[164:167],v[104:107]
TR a[184:185],v233 offset:34304
CV v82,v84,v85
MM v[112:115],a[224:227],v[168:171],v[136:139]
EX v86,v86
MM v[112:115],a[228:231],v[184:187],v[112:115]
TR a[186:187],v233 offset:38400
TR a[188:189],v233 offset:42496
MM v[112:115],a[232:235],v[200:203],v[112:115]
EX v87,v87
MM v[112:115],a[236:239],v[216:219],v[112:115]
TR a[190:191],v233 offset:46592
CV v83,v86,v87
MM v[116:119],a[252:255],v[216:219],v[136:139]
EX v88,v88
MM v[116:119],a[248:251],v[200:203],v[116:119]
EX v89,v89
MM v[116:119],a[244:247],v[184:187],v[116:119]
EX v90,v90
MM v[116:119],a[240:243],v[168:171],v[116:119]
EX v91,v91
MM v[124:127],a[240:243],v[172:175],v[140:143]
EX v92,v92
MM v[124:127],a[244:247],v[188:191],v[124:127]
EX v93,v93
MM v[124:127],a[248:251],v[204:207],v[124:127]
EX v94,v94
MM v[124:127],a[252:255],v[220:223],v[124:127]
EX v95,v95
MM v[120:123],a[236:239],v[220:223],v[140:143]
EX v96,v96
MM v[120:123],a[232:235],v[204:207],v[120:123]
EX v97,v97
MM v[120:123],a[228:231],v[188:191],v[120:123]
EX v98,v98
MM v[120:123],a[224:227],v[172:175],v[120:123]
s_waitcnt vmcnt(15) lgkmcnt(0)
s_barrier
MM a[0:3],a[128:131],v[0:3],a[0:3]
s_add_u32 m0, s65, 32768
CV v88,v88,v89
CV v89,v90,v91
MM a[0:3],a[132:135],v[4:7],a[0:3]
RD a[192:195],v224 offset:0
CV v90,v92,v93
MM a[8:11],a[132:135],v[12:15],a[8:11]
RD a[196:199],v225 offset:0
CV v91,v94,v95
MM a[8:11],a[128:131],v[8:11],a[8:11]
)ASM"
R"ASM(
buffer_load_dwordx4 v228, s[8:11], s64 offen offset:0 lds
MM a[16:19],a[128:131],v[16:19],a[16:19]
RD a[200:203],v226 offset:0
CV v68,v96,v97
MM a[16:19],a[132:135],v[20:23],a[16:19]
buffer_load_dwordx4 v229, s[8:11], s64 offen offset:1024 lds
MM a[24:27],a[132:135],v[28:31],a[24:27]
RD a[204:207],v227 offset:0
RD a[208:211],v224 offset:4096
MM a[24:27],a[128:131],v[24:27],a[24:27]
EX v99,v99
MM a[28:31],a[136:139],v[24:27],a[28:31]
RD a[212:215],v225 offset:4096
CV v69,v98,v99
MM a[28:31],a[140:143],v[28:31],a[28:31]
EX v100,v100
MM a[20:23],a[140:143],v[20:23],a[20:23]
buffer_load_dwordx4 v230, s[8:11], s64 offen offset:2048 lds
MM a[20:23],a[136:139],v[16:19],a[20:23]
EX v101,v101
MM a[12:15],a[136:139],v[8:11],a[12:15]
RD a[216:219],v226 offset:4096
CV v70,v100,v101
MM a[12:15],a[140:143],v[12:15],a[12:15]
EX v102,v102
MM a[4:7],a[140:143],v[4:7],a[4:7]
EX v103,v103
MM a[4:7],a[136:139],v[0:3],a[4:7]
RD a[220:223],v227 offset:4096
CV v71,v102,v103
MM a[32:35],a[144:147],v[0:3],a[32:35]
EX v104,v104
MM a[32:35],a[148:151],v[4:7],a[32:35]
buffer_load_dwordx4 v231, s[8:11], s64 offen offset:3072 lds
MM a[40:43],a[148:151],v[12:15],a[40:43]
s_add_u32 m0, s65, 98304
EX v105,v105
MM a[40:43],a[144:147],v[8:11],a[40:43]
RD a[224:227],v224 offset:8192
CV v76,v104,v105
MM a[48:51],a[144:147],v[16:19],a[48:51]
EX v106,v106
MM a[48:51],a[148:151],v[20:23],a[48:51]
EX v107,v107
MM a[56:59],a[148:151],v[28:31],a[56:59]
RD a[228:231],v225 offset:8192
CV v77,v106,v107
MM a[56:59],a[144:147],v[24:27],a[56:59]
EX v108,v108
MM a[60:63],a[152:155],v[24:27],a[60:63]
buffer_load_dwordx4 v235, s[12:15], s81 offen offset:0 lds
MM a[60:63],a[156:159],v[28:31],a[60:63]
EX v109,v109
MM a[52:55],a[156:159],v[20:23],a[52:55]
RD a[232:235],v226 offset:8192
CV v78,v108,v109
MM a[52:55],a[152:155],v[16:19],a[52:55]
EX v110,v110
MM a[44:47],a[152:155],v[8:11],a[44:47]
EX v111,v111
MM a[44:47],a[156:159],v[12:15],a[44:47]
RD a[236:239],v227 offset:8192
CV v79,v110,v111
MM a[36:39],a[156:159],v[4:7],a[36:39]
buffer_load_dwordx4 v235, s[12:15], s64 offen offset:1024 lds
MM a[36:39],a[152:155],v[0:3],a[36:39]
EX v112,v112
MM a[64:67],a[160:163],v[0:3],a[64:67]
EX v113,v113
MM a[64:67],a[164:167],v[4:7],a[64:67]
RD a[240:243],v224 offset:12288
CV v84,v112,v113
MM a[72:75],a[164:167],v[12:15],a[72:75]
EX v114,v114
MM a[72:75],a[160:163],v[8:11],a[72:75]
RD a[244:247],v225 offset:12288
RD a[248:251],v226 offset:12288
MM a[80:83],a[160:163],v[16:19],a[80:83]
s_add_u32 m0, s67, 3072
s_add_u32 s85, s66, 165888
buffer_load_dwordx4 v252, s[36:39], s85 offen offset:0 lds
MM a[80:83],a[164:167],v[20:23],a[80:83]
EX v115,v115
MM a[88:91],a[164:167],v[28:31],a[88:91]
RD a[252:255],v227 offset:12288
CV v85,v114,v115
MM a[88:91],a[160:163],v[24:27],a[88:91]
EX v116,v116
MM a[92:95],a[168:171],v[24:27],a[92:95]
EX v117,v117
MM a[92:95],a[172:175],v[28:31],a[92:95]
s_add_u32 m0, s65, 98304
s_nop 0
buffer_load_dwordx4 v235, s[12:15], s81 offen offset:2048 lds
MM a[84:87],a[172:175],v[20:23],a[84:87]
EX v118,v118
MM a[84:87],a[168:171],v[16:19],a[84:87]
EX v119,v119
MM a[76:79],a[168:171],v[8:11],a[76:79]
buffer_load_dwordx4 v235, s[12:15], s64 offen offset:3072 lds
MM a[76:79],a[172:175],v[12:15],a[76:79]
EX v120,v120
MM a[68:71],a[172:175],v[4:7],a[68:71]
EX v121,v121
MM a[68:71],a[168:171],v[0:3],a[68:71]
EX v122,v122
MM a[96:99],a[176:179],v[0:3],a[96:99]
EX v123,v123
MM a[96:99],a[180:183],v[4:7],a[96:99]
EX v124,v124
MM a[104:107],a[180:183],v[12:15],a[104:107]
EX v125,v125
MM a[104:107],a[176:179],v[8:11],a[104:107]
EX v126,v126
MM a[112:115],a[176:179],v[16:19],a[112:115]
EX v127,v127
MM a[112:115],a[180:183],v[20:23],a[112:115]
CV v86,v116,v117
CV v87,v118,v119
MM a[120:123],a[180:183],v[28:31],a[120:123]
CV v92,v120,v121
CV v93,v122,v123
MM a[120:123],a[176:179],v[24:27],a[120:123]
CV v94,v124,v125
CV v95,v126,v127
MM a[124:127],a[184:187],v[24:27],a[124:127]
v_add_f32 v244, v144, v145
v_add_f32 v245, v148, v149
MM a[124:127],a[188:191],v[28:31],a[124:127]
v_add_f32 v246, v152, v153
v_add_f32 v247, v156, v157
MM a[116:119],a[188:191],v[20:23],a[116:119]
MM a[116:119],a[184:187],v[16:19],a[116:119]
MM a[108:111],a[184:187],v[8:11],a[108:111]
MM a[108:111],a[188:191],v[12:15],a[108:111]
MM a[100:103],a[188:191],v[4:7],a[100:103]
MM a[100:103],a[184:187],v[0:3],a[100:103]
v_smfmac_f32_16x16x64_bf16 v[144:147], v[236:239], v[64:71], v234
v_smfmac_f32_16x16x64_bf16 v[148:151], v[236:239], v[72:79], v234
v_smfmac_f32_16x16x64_bf16 v[152:155], v[236:239], v[80:87], v234
v_smfmac_f32_16x16x64_bf16 v[156:159], v[236:239], v[88:95], v234
s_waitcnt lgkmcnt(0)
MM v[0:3],a[192:195],v[160:163],v[128:131]
s_add_u32 s64, s64, 16384
TR a[128:129],v232 offset:49152
s_add_u32 s81, s64, 896
MM v[0:3],a[196:199],v[176:179],v[0:3]
TR a[130:131],v232 offset:53248
v_max3_f32 v249, v144, v145, v148
MM v[0:3],a[200:203],v[192:195],v[0:3]
TR a[132:133],v232 offset:57344
v_max3_f32 v249, v249, v149, v152
MM v[0:3],a[204:207],v[208:211],v[0:3]
TR a[134:135],v232 offset:61440
v_max3_f32 v249, v249, v153, v156
MM v[4:7],a[220:223],v[208:211],v[128:131]
TR a[136:137],v233 offset:49152
v_max_f32 v249, v249, v157
MM v[4:7],a[216:219],v[192:195],v[4:7]
TR a[138:139],v233 offset:53248
v_cmp_lt_f32 vcc, s71, v249
MM v[4:7],a[212:215],v[176:179],v[4:7]
TR a[140:141],v233 offset:57344
TR a[142:143],v233 offset:61440
MM v[4:7],a[208:211],v[160:163],v[4:7]
s_cbranch_vccnz LSITE11_%=
LBACK11_%=:
MM v[12:15],a[208:211],v[164:167],v[132:135]
s_add_u32 m0, s67, 4096
s_add_u32 s85, s66, 221184
buffer_load_dwordx4 v252, s[36:39], s85 offen offset:0 lds
MM v[12:15],a[212:215],v[180:183],v[12:15]
EX v0,v0
MM v[12:15],a[216:219],v[196:199],v[12:15]
EX v1,v1
MM v[12:15],a[220:223],v[212:215],v[12:15]
TR a[144:145],v232 offset:49664
CV v0,v0,v1
MM v[8:11],a[204:207],v[212:215],v[132:135]
EX v2,v2
MM v[8:11],a[200:203],v[196:199],v[8:11]
EX v3,v3
MM v[8:11],a[196:199],v[180:183],v[8:11]
TR a[146:147],v232 offset:53760
CV v1,v2,v3
MM v[8:11],a[192:195],v[164:167],v[8:11]
EX v4,v4
MM v[16:19],a[192:195],v[168:171],v[136:139]
TR a[148:149],v232 offset:57856
TR a[150:151],v232 offset:61952
MM v[16:19],a[196:199],v[184:187],v[16:19]
EX v5,v5
MM v[16:19],a[200:203],v[200:203],v[16:19]
TR a[152:153],v233 offset:49664
CV v2,v4,v5
MM v[16:19],a[204:207],v[216:219],v[16:19]
EX v6,v6
MM v[20:23],a[220:223],v[216:219],v[136:139]
EX v7,v7
MM v[20:23],a[216:219],v[200:203],v[20:23]
TR a[154:155],v233 offset:53760
CV v3,v6,v7
MM v[20:23],a[212:215],v[184:187],v[20:23]
EX v8,v8
MM v[20:23],a[208:211],v[168:171],v[20:23]
TR a[156:157],v233 offset:57856
TR a[158:159],v233 offset:61952
MM v[28:31],a[208:211],v[172:175],v[140:143]
EX v9,v9
MM v[28:31],a[212:215],v[188:191],v[28:31]
TR a[160:161],v232 offset:50176
CV v8,v8,v9
MM v[28:31],a[216:219],v[204:207],v[28:31]
EX v10,v10
MM v[28:31],a[220:223],v[220:223],v[28:31]
EX v11,v11
MM v[24:27],a[204:207],v[220:223],v[140:143]
TR a[162:163],v232 offset:54272
CV v9,v10,v11
MM v[24:27],a[200:203],v[204:207],v[24:27]
TR a[164:165],v232 offset:58368
TR a[166:167],v232 offset:62464
MM v[24:27],a[196:199],v[188:191],v[24:27]
EX v12,v12
MM v[24:27],a[192:195],v[172:175],v[24:27]
EX v13,v13
MM v[32:35],a[224:227],v[160:163],v[128:131]
TR a[168:169],v233 offset:50176
CV v10,v12,v13
MM v[32:35],a[228:231],v[176:179],v[32:35]
EX v14,v14
MM v[32:35],a[232:235],v[192:195],v[32:35]
TR a[170:171],v233 offset:54272
TR a[172:173],v233 offset:58368
MM v[32:35],a[236:239],v[208:211],v[32:35]
EX v15,v15
MM v[36:39],a[252:255],v[208:211],v[128:131]
TR a[174:175],v233 offset:62464
CV v11,v14,v15
MM v[36:39],a[248:251],v[192:195],v[36:39]
EX v16,v16
MM v[36:39],a[244:247],v[176:179],v[36:39]
EX v17,v17
MM v[36:39],a[240:243],v[160:163],v[36:39]
TR a[176:177],v232 offset:50688
CV v16,v16,v17
MM v[44:47],a[240:243],v[164:167],v[132:135]
EX v18,v18
MM v[44:47],a[244:247],v[180:183],v[44:47]
TR a[178:179],v232 offset:54784
TR a[180:181],v232 offset:58880
MM v[44:47],a[248:251],v[196:199],v[44:47]
EX v19,v19
MM v[44:47],a[252:255],v[212:215],v[44:47]
TR a[182:183],v232 offset:62976
CV v17,v18,v19
MM v[40:43],a[236:239],v[212:215],v[132:135]
EX v20,v20
MM v[40:43],a[232:235],v[196:199],v[40:43]
EX v21,v21
MM v[40:43],a[228:231],v[180:183],v[40:43]
TR a[184:185],v233 offset:50688
CV v18,v20,v21
MM v[40:43],a[224:227],v[164:167],v[40:43]
EX v22,v22
MM v[48:51],a[224:227],v[168:171],v[136:139]
TR a[186:187],v233 offset:54784
TR a[188:189],v233 offset:58880
MM v[48:51],a[228:231],v[184:187],v[48:51]
EX v23,v23
MM v[48:51],a[232:235],v[200:203],v[48:51]
TR a[190:191],v233 offset:62976
CV v19,v22,v23
MM v[48:51],a[236:239],v[216:219],v[48:51]
EX v24,v24
MM v[52:55],a[252:255],v[216:219],v[136:139]
EX v25,v25
MM v[52:55],a[248:251],v[200:203],v[52:55]
EX v26,v26
MM v[52:55],a[244:247],v[184:187],v[52:55]
EX v27,v27
MM v[52:55],a[240:243],v[168:171],v[52:55]
EX v28,v28
MM v[60:63],a[240:243],v[172:175],v[140:143]
EX v29,v29
MM v[60:63],a[244:247],v[188:191],v[60:63]
EX v30,v30
MM v[60:63],a[248:251],v[204:207],v[60:63]
EX v31,v31
LPG11_%=:
s_cbranch_execz LTR0_%=
MM v[60:63],a[252:255],v[220:223],v[60:63]
EX v32,v32
MM v[56:59],a[236:239],v[220:223],v[140:143]
EX v33,v33
MM v[56:59],a[232:235],v[204:207],v[56:59]
EX v34,v34
MM v[56:59],a[228:231],v[188:191],v[56:59]
EX v35,v35
MM v[56:59],a[224:227],v[172:175],v[56:59]
s_waitcnt vmcnt(17) lgkmcnt(0)
s_barrier
MM a[0:3],a[128:131],v[64:67],a[0:3]
s_mov_b32 s86, 94208
v_bfe_u32 v251, v248, 4, 3
v_lshlrev_b32 v251, 3, v251
buffer_load_dwordx2 v[252:253], v251, s[20:23], s86 offen offset:0 sc1
MM a[0:3],a[132:135],v[68:71],a[0:3]
s_add_u32 m0, s65, 49152
CV v24,v24,v25
CV v25,v26,v27
MM a[8:11],a[132:135],v[76:79],a[8:11]
RD a[192:195],v224 offset:16384
CV v26,v28,v29
MM a[8:11],a[128:131],v[72:75],a[8:11]
RD a[196:199],v225 offset:16384
CV v27,v30,v31
MM a[16:19],a[128:131],v[80:83],a[16:19]
buffer_load_dwordx4 v228, s[8:11], s64 offen offset:0 lds
MM a[16:19],a[132:135],v[84:87],a[16:19]
RD a[200:203],v226 offset:16384
CV v4,v32,v33
MM a[24:27],a[132:135],v[92:95],a[24:27]
RD a[204:207],v227 offset:16384
CV v5,v34,v35
MM a[24:27],a[128:131],v[88:91],a[24:27]
buffer_load_dwordx4 v229, s[8:11], s64 offen offset:1024 lds
MM a[28:31],a[136:139],v[88:91],a[28:31]
RD a[208:211],v224 offset:20480
RD a[212:215],v225 offset:20480
MM a[28:31],a[140:143],v[92:95],a[28:31]
EX v36,v36
MM a[20:23],a[140:143],v[84:87],a[20:23]
buffer_load_dwordx4 v230, s[8:11], s64 offen offset:2048 lds
MM a[20:23],a[136:139],v[80:83],a[20:23]
EX v37,v37
MM a[12:15],a[136:139],v[72:75],a[12:15]
RD a[216:219],v226 offset:20480
CV v6,v36,v37
MM a[12:15],a[140:143],v[76:79],a[12:15]
EX v38,v38
MM a[4:7],a[140:143],v[68:71],a[4:7]
EX v39,v39
MM a[4:7],a[136:139],v[64:67],a[4:7]
RD a[220:223],v227 offset:20480
CV v7,v38,v39
MM a[32:35],a[144:147],v[64:67],a[32:35]
EX v40,v40
MM a[32:35],a[148:151],v[68:71],a[32:35]
buffer_load_dwordx4 v231, s[8:11], s64 offen offset:3072 lds
MM a[40:43],a[148:151],v[76:79],a[40:43]
s_add_u32 m0, s65, 114688
EX v41,v41
MM a[40:43],a[144:147],v[72:75],a[40:43]
RD a[224:227],v224 offset:24576
CV v12,v40,v41
MM a[48:51],a[144:147],v[80:83],a[48:51]
EX v42,v42
MM a[48:51],a[148:151],v[84:87],a[48:51]
EX v43,v43
MM a[56:59],a[148:151],v[92:95],a[56:59]
RD a[228:231],v225 offset:24576
CV v13,v42,v43
MM a[56:59],a[144:147],v[88:91],a[56:59]
EX v44,v44
MM a[60:63],a[152:155],v[88:91],a[60:63]
buffer_load_dwordx4 v235, s[12:15], s81 offen offset:0 lds
MM a[60:63],a[156:159],v[92:95],a[60:63]
EX v45,v45
MM a[52:55],a[156:159],v[84:87],a[52:55]
RD a[232:235],v226 offset:24576
CV v14,v44,v45
MM a[52:55],a[152:155],v[80:83],a[52:55]
EX v46,v46
MM a[44:47],a[152:155],v[72:75],a[44:47]
EX v47,v47
MM a[44:47],a[156:159],v[76:79],a[44:47]
RD a[236:239],v227 offset:24576
CV v15,v46,v47
MM a[36:39],a[156:159],v[68:71],a[36:39]
buffer_load_dwordx4 v235, s[12:15], s64 offen offset:1024 lds
MM a[36:39],a[152:155],v[64:67],a[36:39]
EX v48,v48
MM a[64:67],a[160:163],v[64:67],a[64:67]
EX v49,v49
MM a[64:67],a[164:167],v[68:71],a[64:67]
RD a[240:243],v224 offset:28672
)ASM"
R"ASM(
CV v20,v48,v49
MM a[72:75],a[164:167],v[76:79],a[72:75]
EX v50,v50
MM a[72:75],a[160:163],v[72:75],a[72:75]
RD a[244:247],v225 offset:28672
RD a[248:251],v226 offset:28672
MM a[80:83],a[160:163],v[80:83],a[80:83]
EX v51,v51
MM a[80:83],a[164:167],v[84:87],a[80:83]
RD a[252:255],v227 offset:28672
CV v21,v50,v51
MM a[88:91],a[164:167],v[92:95],a[88:91]
EX v52,v52
MM a[88:91],a[160:163],v[88:91],a[88:91]
buffer_load_dwordx4 v235, s[12:15], s81 offen offset:2048 lds
MM a[92:95],a[168:171],v[88:91],a[92:95]
EX v53,v53
MM a[92:95],a[172:175],v[92:95],a[92:95]
EX v54,v54
MM a[84:87],a[172:175],v[84:87],a[84:87]
buffer_load_dwordx4 v235, s[12:15], s64 offen offset:3072 lds
MM a[84:87],a[168:171],v[80:83],a[84:87]
EX v55,v55
MM a[76:79],a[168:171],v[72:75],a[76:79]
EX v56,v56
MM a[76:79],a[172:175],v[76:79],a[76:79]
EX v57,v57
MM a[68:71],a[172:175],v[68:71],a[68:71]
EX v58,v58
MM a[68:71],a[168:171],v[64:67],a[68:71]
EX v59,v59
MM a[96:99],a[176:179],v[64:67],a[96:99]
EX v60,v60
MM a[96:99],a[180:183],v[68:71],a[96:99]
EX v61,v61
MM a[104:107],a[180:183],v[76:79],a[104:107]
EX v62,v62
MM a[104:107],a[176:179],v[72:75],a[104:107]
EX v63,v63
MM a[112:115],a[176:179],v[80:83],a[112:115]
CV v22,v52,v53
CV v23,v54,v55
MM a[112:115],a[180:183],v[84:87],a[112:115]
CV v28,v56,v57
CV v29,v58,v59
MM a[120:123],a[180:183],v[92:95],a[120:123]
CV v30,v60,v61
CV v31,v62,v63
MM a[120:123],a[176:179],v[88:91],a[120:123]
MM a[124:127],a[184:187],v[88:91],a[124:127]
v_add_f32 v244, v144, v145
v_add_f32 v245, v148, v149
MM a[124:127],a[188:191],v[92:95],a[124:127]
v_add_f32 v246, v152, v153
v_add_f32 v247, v156, v157
MM a[116:119],a[188:191],v[84:87],a[116:119]
MM a[116:119],a[184:187],v[80:83],a[116:119]
MM a[108:111],a[184:187],v[72:75],a[108:111]
MM a[108:111],a[188:191],v[76:79],a[108:111]
MM a[100:103],a[188:191],v[68:71],a[100:103]
MM a[100:103],a[184:187],v[64:67],a[100:103]
v_smfmac_f32_16x16x64_bf16 v[144:147], v[236:239], v[0:7], v234
v_smfmac_f32_16x16x64_bf16 v[148:151], v[236:239], v[8:15], v234
v_smfmac_f32_16x16x64_bf16 v[152:155], v[236:239], v[16:23], v234
v_smfmac_f32_16x16x64_bf16 v[156:159], v[236:239], v[24:31], v234
s_waitcnt lgkmcnt(0)
MM v[64:67],a[192:195],v[160:163],v[128:131]
s_add_u32 s64, s64, 16384
TR a[128:129],v232 offset:0
s_add_u32 s81, s64, 896
MM v[64:67],a[196:199],v[176:179],v[64:67]
TR a[130:131],v232 offset:4096
v_max3_f32 v249, v144, v145, v148
MM v[64:67],a[200:203],v[192:195],v[64:67]
TR a[132:133],v232 offset:8192
v_max3_f32 v249, v249, v149, v152
MM v[64:67],a[204:207],v[208:211],v[64:67]
TR a[134:135],v232 offset:12288
v_max3_f32 v249, v249, v153, v156
MM v[68:71],a[220:223],v[208:211],v[128:131]
TR a[136:137],v233 offset:0
v_max_f32 v249, v249, v157
MM v[68:71],a[216:219],v[192:195],v[68:71]
TR a[138:139],v233 offset:4096
v_cmp_lt_f32 vcc, s71, v249
MM v[68:71],a[212:215],v[176:179],v[68:71]
TR a[140:141],v233 offset:8192
TR a[142:143],v233 offset:12288
MM v[68:71],a[208:211],v[160:163],v[68:71]
s_cbranch_vccnz LSITE12_%=
LBACK12_%=:
MM v[76:79],a[208:211],v[164:167],v[132:135]
s_waitcnt vmcnt(8)
v_cmp_ne_u32 vcc, s69, v253
s_cbranch_vccnz LRIX_%=
LRIXb_%=:
MM v[76:79],a[212:215],v[180:183],v[76:79]
s_and_b32 s90, s17, 7
v_and_b32 v253, 65535, v252
v_readlane_b32 s19, v252, s90
MM v[76:79],a[216:219],v[196:199],v[76:79]
v_sub_u32 v253, 65535, v253
v_mul_u32_u24 v253, 1280, v253
MM v[76:79],a[220:223],v[212:215],v[76:79]
v_lshrrev_b32 v252, 7, v248
v_lshl_add_u32 v253, v252, 4, v253
MM v[72:75],a[204:207],v[212:215],v[132:135]
s_mul_i32 s91, %[swave], 3072
EX v64,v64
MM v[72:75],a[200:203],v[196:199],v[72:75]
s_add_u32 s91, s91, %[slds]
EX v65,v65
MM v[72:75],a[196:199],v[180:183],v[72:75]
s_bfe_u32 s90, s48, 131077
EX v66,v66
MM v[72:75],a[192:195],v[164:167],v[72:75]
s_mul_i32 s90, s90, 320
EX v67,v67
MM v[80:83],a[192:195],v[168:171],v[136:139]
s_add_u32 s90, s90, 131072
s_add_u32 m0, s91, 151552
s_nop 0
buffer_load_dwordx4 v253, s[20:23], s90 offen offset:0 sc1 lds
MM v[80:83],a[196:199],v[184:187],v[80:83]
TR a[144:145],v232 offset:512
CV v64,v64,v65
MM v[80:83],a[200:203],v[200:203],v[80:83]
s_add_u32 m0, s91, 152448
s_nop 0
buffer_load_dwordx4 v253, s[20:23], s90 offen offset:128 sc1 lds
MM v[80:83],a[204:207],v[216:219],v[80:83]
EX v68,v68
MM v[84:87],a[220:223],v[216:219],v[136:139]
s_add_u32 m0, s91, 153344
s_nop 0
buffer_load_dwordx4 v253, s[20:23], s90 offen offset:256 sc1 lds
MM v[84:87],a[216:219],v[200:203],v[84:87]
EX v69,v69
MM v[84:87],a[212:215],v[184:187],v[84:87]
s_lshr_b32 s77, %[sb], 3
TR a[146:147],v232 offset:4608
TR a[148:149],v232 offset:8704
MM v[84:87],a[208:211],v[168:171],v[84:87]
s_lshl_b32 s77, s77, 2
EX v70,v70
MM v[92:95],a[208:211],v[172:175],v[140:143]
s_add_u32 s77, s77, %[swave]
EX v71,v71
MM v[92:95],a[212:215],v[188:191],v[92:95]
s_lshl_b32 s77, s77, 9
TR a[150:151],v232 offset:12800
CV v65,v66,v67
MM v[92:95],a[216:219],v[204:207],v[92:95]
s_add_u32 s77, s77, 0
v_lshrrev_b32 v253, 1, v248
v_add_u32 v253, s77, v253
MM v[92:95],a[220:223],v[220:223],v[92:95]
s_mov_b32 s77, LKEND_ka-LPD0_%=-16
TR a[152:153],v233 offset:512
v_min_u32 v253, s77, v253
EX v72,v72
MM v[88:91],a[204:207],v[220:223],v[140:143]
s_getpc_b64 s[98:99]
LPD0_%=:
global_load_dword v253, v253, s[98:99]
MM v[88:91],a[200:203],v[204:207],v[88:91]
s_add_u32 s67, s95, 1024
EX v73,v73
MM v[88:91],a[196:199],v[188:191],v[88:91]
s_mov_b32 s70, 5
TR a[154:155],v233 offset:4608
CV v66,v68,v69
MM v[88:91],a[192:195],v[172:175],v[88:91]
EX v74,v74
MM v[96:99],a[224:227],v[160:163],v[128:131]
TR a[156:157],v233 offset:8704
TR a[158:159],v233 offset:12800
EX v75,v75
MM v[96:99],a[228:231],v[176:179],v[96:99]
TR a[160:161],v232 offset:1024
CV v67,v70,v71
MM v[96:99],a[232:235],v[192:195],v[96:99]
EX v76,v76
MM v[96:99],a[236:239],v[208:211],v[96:99]
EX v77,v77
MM v[100:103],a[252:255],v[208:211],v[128:131]
TR a[162:163],v232 offset:5120
CV v72,v72,v73
MM v[100:103],a[248:251],v[192:195],v[100:103]
EX v78,v78
MM v[100:103],a[244:247],v[176:179],v[100:103]
TR a[164:165],v232 offset:9216
CV v73,v74,v75
MM v[100:103],a[240:243],v[160:163],v[100:103]
EX v79,v79
MM v[108:111],a[240:243],v[164:167],v[132:135]
TR a[166:167],v232 offset:13312
TR a[168:169],v233 offset:1024
MM v[108:111],a[244:247],v[180:183],v[108:111]
EX v80,v80
MM v[108:111],a[248:251],v[196:199],v[108:111]
EX v81,v81
MM v[108:111],a[252:255],v[212:215],v[108:111]
TR a[170:171],v233 offset:5120
CV v74,v76,v77
MM v[104:107],a[236:239],v[212:215],v[132:135]
EX v82,v82
MM v[104:107],a[232:235],v[196:199],v[104:107]
TR a[172:173],v233 offset:9216
CV v75,v78,v79
MM v[104:107],a[228:231],v[180:183],v[104:107]
EX v83,v83
MM v[104:107],a[224:227],v[164:167],v[104:107]
TR a[174:175],v233 offset:13312
TR a[176:177],v232 offset:1536
MM v[112:115],a[224:227],v[168:171],v[136:139]
EX v84,v84
MM v[112:115],a[228:231],v[184:187],v[112:115]
TR a[178:179],v232 offset:5632
CV v80,v80,v81
MM v[112:115],a[232:235],v[200:203],v[112:115]
EX v85,v85
MM v[112:115],a[236:239],v[216:219],v[112:115]
TR a[180:181],v232 offset:9728
CV v81,v82,v83
MM v[116:119],a[252:255],v[216:219],v[136:139]
EX v86,v86
MM v[116:119],a[248:251],v[200:203],v[116:119]
TR a[182:183],v232 offset:13824
TR a[184:185],v233 offset:1536
MM v[116:119],a[244:247],v[184:187],v[116:119]
EX v87,v87
MM v[116:119],a[240:243],v[168:171],v[116:119]
EX v88,v88
MM v[124:127],a[240:243],v[172:175],v[140:143]
TR a[186:187],v233 offset:5632
CV v82,v84,v85
MM v[124:127],a[244:247],v[188:191],v[124:127]
EX v89,v89
MM v[124:127],a[248:251],v[204:207],v[124:127]
TR a[188:189],v233 offset:9728
TR a[190:191],v233 offset:13824
MM v[124:127],a[252:255],v[220:223],v[124:127]
EX v90,v90
MM v[120:123],a[236:239],v[220:223],v[140:143]
CV v83,v86,v87
CV v88,v88,v89
MM v[120:123],a[232:235],v[204:207],v[120:123]
EX v91,v91
MM v[120:123],a[228:231],v[188:191],v[120:123]
EX v92,v92
MM v[120:123],a[224:227],v[172:175],v[120:123]
s_waitcnt lgkmcnt(0)
s_barrier
MM a[0:3],a[128:131],v[0:3],a[0:3]
s_add_u32 m0, s65, 0
EX v93,v93
MM a[0:3],a[132:135],v[4:7],a[0:3]
CV v89,v90,v91
CV v90,v92,v93
MM a[8:11],a[132:135],v[12:15],a[8:11]
EX v94,v94
MM a[8:11],a[128:131],v[8:11],a[8:11]
EX v95,v95
MM a[16:19],a[128:131],v[16:19],a[16:19]
RD a[192:195],v224 offset:32768
CV v91,v94,v95
MM a[16:19],a[132:135],v[20:23],a[16:19]
EX v96,v96
MM a[24:27],a[132:135],v[28:31],a[24:27]
buffer_load_dwordx4 v228, s[8:11], s64 offen offset:0 lds
MM a[24:27],a[128:131],v[24:27],a[24:27]
EX v97,v97
MM a[28:31],a[136:139],v[24:27],a[28:31]
RD a[196:199],v225 offset:32768
CV v68,v96,v97
MM a[28:31],a[140:143],v[28:31],a[28:31]
EX v98,v98
MM a[20:23],a[140:143],v[20:23],a[20:23]
EX v99,v99
MM a[20:23],a[136:139],v[16:19],a[20:23]
RD a[200:203],v226 offset:32768
CV v69,v98,v99
MM a[12:15],a[136:139],v[8:11],a[12:15]
EX v100,v100
MM a[12:15],a[140:143],v[12:15],a[12:15]
buffer_load_dwordx4 v229, s[8:11], s64 offen offset:1024 lds
MM a[4:7],a[140:143],v[4:7],a[4:7]
EX v101,v101
MM a[4:7],a[136:139],v[0:3],a[4:7]
RD a[204:207],v227 offset:32768
CV v70,v100,v101
MM a[32:35],a[144:147],v[0:3],a[32:35]
EX v102,v102
MM a[32:35],a[148:151],v[4:7],a[32:35]
EX v103,v103
MM a[40:43],a[148:151],v[12:15],a[40:43]
RD a[208:211],v224 offset:36864
CV v71,v102,v103
MM a[40:43],a[144:147],v[8:11],a[40:43]
EX v104,v104
MM a[48:51],a[144:147],v[16:19],a[48:51]
buffer_load_dwordx4 v230, s[8:11], s64 offen offset:2048 lds
MM a[48:51],a[148:151],v[20:23],a[48:51]
EX v105,v105
MM a[56:59],a[148:151],v[28:31],a[56:59]
RD a[212:215],v225 offset:36864
CV v76,v104,v105
MM a[56:59],a[144:147],v[24:27],a[56:59]
EX v106,v106
MM a[60:63],a[152:155],v[24:27],a[60:63]
EX v107,v107
MM a[60:63],a[156:159],v[28:31],a[60:63]
RD a[216:219],v226 offset:36864
CV v77,v106,v107
MM a[52:55],a[156:159],v[20:23],a[52:55]
EX v108,v108
MM a[52:55],a[152:155],v[16:19],a[52:55]
buffer_load_dwordx4 v231, s[8:11], s64 offen offset:3072 lds
MM a[44:47],a[152:155],v[8:11],a[44:47]
s_add_u32 m0, s65, 65536
EX v109,v109
MM a[44:47],a[156:159],v[12:15],a[44:47]
RD a[220:223],v227 offset:36864
CV v78,v108,v109
MM a[36:39],a[156:159],v[4:7],a[36:39]
EX v110,v110
MM a[36:39],a[152:155],v[0:3],a[36:39]
RD a[224:227],v224 offset:40960
RD a[228:231],v225 offset:40960
MM a[64:67],a[160:163],v[0:3],a[64:67]
EX v111,v111
MM a[64:67],a[164:167],v[4:7],a[64:67]
RD a[232:235],v226 offset:40960
CV v79,v110,v111
MM a[72:75],a[164:167],v[12:15],a[72:75]
EX v112,v112
MM a[72:75],a[160:163],v[8:11],a[72:75]
buffer_load_dwordx4 v235, s[12:15], s81 offen offset:0 lds
MM a[80:83],a[160:163],v[16:19],a[80:83]
EX v113,v113
MM a[80:83],a[164:167],v[20:23],a[80:83]
RD a[236:239],v227 offset:40960
CV v84,v112,v113
MM a[88:91],a[164:167],v[28:31],a[88:91]
EX v114,v114
MM a[88:91],a[160:163],v[24:27],a[88:91]
EX v115,v115
MM a[92:95],a[168:171],v[24:27],a[92:95]
RD a[240:243],v224 offset:45056
CV v85,v114,v115
MM a[92:95],a[172:175],v[28:31],a[92:95]
EX v116,v116
MM a[84:87],a[172:175],v[20:23],a[84:87]
buffer_load_dwordx4 v235, s[12:15], s64 offen offset:1024 lds
MM a[84:87],a[168:171],v[16:19],a[84:87]
EX v117,v117
MM a[76:79],a[168:171],v[8:11],a[76:79]
RD a[244:247],v225 offset:45056
CV v86,v116,v117
MM a[76:79],a[172:175],v[12:15],a[76:79]
EX v118,v118
MM a[68:71],a[172:175],v[4:7],a[68:71]
EX v119,v119
MM a[68:71],a[168:171],v[0:3],a[68:71]
RD a[248:251],v226 offset:45056
CV v87,v118,v119
MM a[96:99],a[176:179],v[0:3],a[96:99]
EX v120,v120
MM a[96:99],a[180:183],v[4:7],a[96:99]
buffer_load_dwordx4 v235, s[12:15], s81 offen offset:2048 lds
MM a[104:107],a[180:183],v[12:15],a[104:107]
EX v121,v121
MM a[104:107],a[176:179],v[8:11],a[104:107]
RD a[252:255],v227 offset:45056
CV v92,v120,v121
MM a[112:115],a[176:179],v[16:19],a[112:115]
EX v122,v122
MM a[112:115],a[180:183],v[20:23],a[112:115]
EX v123,v123
MM a[120:123],a[180:183],v[28:31],a[120:123]
s_add_u32 s87, s95, 0
buffer_store_dword v243, v248, s[20:23], s87 offen offset:12 sc1
MM a[120:123],a[176:179],v[24:27],a[120:123]
EX v124,v124
MM a[124:127],a[184:187],v[24:27],a[124:127]
v_add_f32 v244, v144, v145
v_add_f32 v245, v148, v149
MM a[124:127],a[188:191],v[28:31],a[124:127]
)ASM"
R"ASM(
v_add_f32 v246, v152, v153
v_add_f32 v247, v156, v157
MM a[116:119],a[188:191],v[20:23],a[116:119]
EX v125,v125
MM a[116:119],a[184:187],v[16:19],a[116:119]
buffer_load_dwordx4 v235, s[12:15], s64 offen offset:3072 lds
MM a[108:111],a[184:187],v[8:11],a[108:111]
EX v126,v126
MM a[108:111],a[188:191],v[12:15],a[108:111]
EX v127,v127
MM a[100:103],a[188:191],v[4:7],a[100:103]
CV v93,v122,v123
CV v94,v124,v125
MM a[100:103],a[184:187],v[0:3],a[100:103]
CV v95,v126,v127
v_smfmac_f32_16x16x64_bf16 v[144:147], v[236:239], v[64:71], v234
v_smfmac_f32_16x16x64_bf16 v[148:151], v[236:239], v[72:79], v234
v_smfmac_f32_16x16x64_bf16 v[152:155], v[236:239], v[80:87], v234
v_smfmac_f32_16x16x64_bf16 v[156:159], v[236:239], v[88:95], v234
s_waitcnt lgkmcnt(0)
s_cmp_ge_u32 s16, 108
s_cbranch_scc1 LLAP_%=
MM v[0:3],a[192:195],v[160:163],v[128:131]
s_add_u32 s64, s64, 16384
TR a[128:129],v232 offset:16384
s_add_u32 s81, s64, 896
MM v[0:3],a[196:199],v[176:179],v[0:3]
TR a[130:131],v232 offset:20480
v_max3_f32 v249, v144, v145, v148
MM v[0:3],a[200:203],v[192:195],v[0:3]
TR a[132:133],v232 offset:24576
v_max3_f32 v249, v249, v149, v152
MM v[0:3],a[204:207],v[208:211],v[0:3]
TR a[134:135],v232 offset:28672
v_max3_f32 v249, v249, v153, v156
MM v[4:7],a[220:223],v[208:211],v[128:131]
TR a[136:137],v233 offset:16384
v_max_f32 v249, v249, v157
MM v[4:7],a[216:219],v[192:195],v[4:7]
TR a[138:139],v233 offset:20480
v_cmp_lt_f32 vcc, s71, v249
MM v[4:7],a[212:215],v[176:179],v[4:7]
TR a[140:141],v233 offset:24576
TR a[142:143],v233 offset:28672
MM v[4:7],a[208:211],v[160:163],v[4:7]
s_cbranch_vccnz LSITE13_%=
LBACK13_%=:
MM v[12:15],a[208:211],v[164:167],v[132:135]
v_lshrrev_b32 v127, 8, v248
v_lshlrev_b32 v124, 9, v127
MM v[12:15],a[212:215],v[180:183],v[12:15]
v_and_b32 v126, 192, v248
v_add_u32 v124, v124, v126
MM v[12:15],a[216:219],v[196:199],v[12:15]
v_bfe_u32 v126, v248, 4, 2
v_lshl_add_u32 v124, v126, 3, v124
MM v[12:15],a[220:223],v[212:215],v[12:15]
s_lshr_b32 s76, %[swave], 1
EX v0,v0
MM v[8:11],a[204:207],v[212:215],v[132:135]
s_mul_i32 s76, s76, 10240
EX v1,v1
MM v[8:11],a[200:203],v[196:199],v[8:11]
s_and_b32 s77, %[swave], 1
EX v2,v2
MM v[8:11],a[196:199],v[180:183],v[8:11]
s_lshl_b32 s77, s77, 5
TR a[144:145],v232 offset:16896
CV v0,v0,v1
MM v[8:11],a[192:195],v[164:167],v[8:11]
s_add_u32 s76, s76, s77
EX v3,v3
MM v[16:19],a[192:195],v[168:171],v[136:139]
s_add_u32 s76, s76, %[slds]
EX v4,v4
MM v[16:19],a[196:199],v[184:187],v[16:19]
s_add_u32 s76, s76, 131072
v_add_u32 v124, s76, v124
v_bfe_u32 v126, v248, 4, 3
MM v[16:19],a[200:203],v[200:203],v[16:19]
v_lshl_add_u32 v126, v127, 3, v126
v_lshlrev_b32 v126, 4, v126
MM v[16:19],a[204:207],v[216:219],v[16:19]
s_mul_i32 s76, %[swave], 3072
EX v5,v5
MM v[20:23],a[220:223],v[216:219],v[136:139]
s_add_u32 s76, s76, %[slds]
TR a[146:147],v232 offset:20992
CV v1,v2,v3
MM v[20:23],a[216:219],v[200:203],v[20:23]
s_add_u32 s76, s76, 151552
TR a[148:149],v232 offset:25088
v_add_u32 v125, s76, v126
MM v[20:23],a[212:215],v[184:187],v[20:23]
s_lshr_b32 s76, s48, 7
EX v6,v6
MM v[20:23],a[208:211],v[168:171],v[20:23]
s_lshl_b32 s76, s76, 5
EX v7,v7
MM v[28:31],a[208:211],v[172:175],v[140:143]
s_and_b32 s77, s48, 16
TR a[150:151],v232 offset:29184
TR a[152:153],v233 offset:16896
MM v[28:31],a[212:215],v[188:191],v[28:31]
s_add_u32 s76, s76, s77
EX v8,v8
MM v[28:31],a[216:219],v[204:207],v[28:31]
s_bfe_u32 s77, s48, 131077
TR a[154:155],v233 offset:20992
CV v2,v4,v5
MM v[28:31],a[220:223],v[220:223],v[28:31]
s_lshl_b32 s77, s77, 2
EX v9,v9
s_nop 0
LPG12_%=:
s_cbranch_execz LTR0_%=
MM v[24:27],a[204:207],v[220:223],v[140:143]
s_add_u32 s76, s76, s77
TR a[156:157],v233 offset:25088
CV v3,v6,v7
MM v[24:27],a[200:203],v[204:207],v[24:27]
s_add_u32 s76, s76, %[swave]
EX v10,v10
MM v[24:27],a[196:199],v[188:191],v[24:27]
s_lshl_b32 s97, s76, 2
TR a[158:159],v233 offset:29184
CV v8,v8,v9
MM v[24:27],a[192:195],v[172:175],v[24:27]
s_add_u32 s97, s97, 77824
EX v11,v11
MM v[32:35],a[224:227],v[160:163],v[128:131]
s_lshl_b32 s76, s76, 9
TR a[160:161],v232 offset:17408
TR a[162:163],v232 offset:21504
MM v[32:35],a[228:231],v[176:179],v[32:35]
s_add_u32 s76, s76, 22183936
EX v12,v12
MM v[32:35],a[232:235],v[192:195],v[32:35]
EX v13,v13
MM v[32:35],a[236:239],v[208:211],v[32:35]
TR a[164:165],v232 offset:25600
CV v9,v10,v11
MM v[36:39],a[252:255],v[208:211],v[128:131]
EX v14,v14
MM v[36:39],a[248:251],v[192:195],v[36:39]
TR a[166:167],v232 offset:29696
CV v10,v12,v13
MM v[36:39],a[244:247],v[176:179],v[36:39]
EX v15,v15
MM v[36:39],a[240:243],v[160:163],v[36:39]
TR a[168:169],v233 offset:17408
TR a[170:171],v233 offset:21504
MM v[44:47],a[240:243],v[164:167],v[132:135]
EX v16,v16
MM v[44:47],a[244:247],v[180:183],v[44:47]
EX v17,v17
MM v[44:47],a[248:251],v[196:199],v[44:47]
TR a[172:173],v233 offset:25600
CV v11,v14,v15
MM v[44:47],a[252:255],v[212:215],v[44:47]
EX v18,v18
MM v[40:43],a[236:239],v[212:215],v[132:135]
TR a[174:175],v233 offset:29696
CV v16,v16,v17
MM v[40:43],a[232:235],v[196:199],v[40:43]
EX v19,v19
MM v[40:43],a[228:231],v[180:183],v[40:43]
TR a[176:177],v232 offset:17920
TR a[178:179],v232 offset:22016
MM v[40:43],a[224:227],v[164:167],v[40:43]
EX v20,v20
MM v[48:51],a[224:227],v[168:171],v[136:139]
EX v21,v21
MM v[48:51],a[228:231],v[184:187],v[48:51]
TR a[180:181],v232 offset:26112
CV v17,v18,v19
MM v[48:51],a[232:235],v[200:203],v[48:51]
EX v22,v22
MM v[48:51],a[236:239],v[216:219],v[48:51]
TR a[182:183],v232 offset:30208
CV v18,v20,v21
MM v[52:55],a[252:255],v[216:219],v[136:139]
EX v23,v23
MM v[52:55],a[248:251],v[200:203],v[52:55]
TR a[184:185],v233 offset:17920
TR a[186:187],v233 offset:22016
MM v[52:55],a[244:247],v[184:187],v[52:55]
EX v24,v24
MM v[52:55],a[240:243],v[168:171],v[52:55]
TR a[188:189],v233 offset:26112
CV v19,v22,v23
MM v[60:63],a[240:243],v[172:175],v[140:143]
EX v25,v25
MM v[60:63],a[244:247],v[188:191],v[60:63]
TR a[190:191],v233 offset:30208
CV v24,v24,v25
MM v[60:63],a[248:251],v[204:207],v[60:63]
EX v26,v26
MM v[60:63],a[252:255],v[220:223],v[60:63]
EX v27,v27
MM v[56:59],a[236:239],v[220:223],v[140:143]
EX v28,v28
MM v[56:59],a[232:235],v[204:207],v[56:59]
EX v29,v29
MM v[56:59],a[228:231],v[188:191],v[56:59]
s_getpc_b64 s[98:99]
LTA2_%=:
s_min_u32 s73, %[swave], 3
s_lshl_b32 s73, s73, 12
s_add_u32 s73, s73, LPG13_%=-LTA2_%=
s_add_u32 s98, s98, s73
s_addc_u32 s99, s99, 0
s_mov_b64 exec, 0
s_setpc_b64 s[98:99]
LTR2_%=:
s_mov_b64 exec, -1
CV v25,v26,v27
MM v[56:59],a[224:227],v[172:175],v[56:59]
s_waitcnt vmcnt(17) lgkmcnt(0)
s_barrier
MM a[0:3],a[128:131],v[64:67],a[0:3]
s_add_u32 m0, s65, 16384
RD a[192:195],v224 offset:49152
CV v26,v28,v29
MM a[0:3],a[132:135],v[68:71],a[0:3]
EX v30,v30
MM a[8:11],a[132:135],v[76:79],a[8:11]
EX v31,v31
MM a[8:11],a[128:131],v[72:75],a[8:11]
RD a[196:199],v225 offset:49152
CV v27,v30,v31
MM a[16:19],a[128:131],v[80:83],a[16:19]
s_waitcnt vmcnt(10)
TR v[96:97],v124 offset:0
TR v[98:99],v124 offset:256
MM a[16:19],a[132:135],v[84:87],a[16:19]
RD v[108:111],v125 offset:0
TR v[100:101],v124 offset:2048
MM a[24:27],a[132:135],v[92:95],a[24:27]
TR v[102:103],v124 offset:2304
RD v[112:115],v125 offset:512
MM a[24:27],a[128:131],v[88:91],a[24:27]
TR v[104:105],v124 offset:4096
TR v[106:107],v124 offset:4352
MM a[28:31],a[136:139],v[88:91],a[28:31]
RD v[116:119],v125 offset:1024
RD a[200:203],v226 offset:49152
EX v32,v32
MM a[28:31],a[140:143],v[92:95],a[28:31]
EX v33,v33
MM a[20:23],a[140:143],v[84:87],a[20:23]
EX v34,v34
MM a[20:23],a[136:139],v[80:83],a[20:23]
buffer_load_dwordx4 v228, s[8:11], s64 offen offset:0 lds
MM a[12:15],a[136:139],v[72:75],a[12:15]
EX v35,v35
MM a[12:15],a[140:143],v[76:79],a[12:15]
EX v36,v36
MM a[4:7],a[140:143],v[68:71],a[4:7]
CV v4,v32,v33
CV v5,v34,v35
MM a[4:7],a[136:139],v[64:67],a[4:7]
EX v37,v37
MM a[32:35],a[144:147],v[64:67],a[32:35]
EX v38,v38
MM a[32:35],a[148:151],v[68:71],a[32:35]
s_waitcnt lgkmcnt(1)
buffer_load_dwordx4 v229, s[8:11], s64 offen offset:1024 lds
MM a[40:43],a[148:151],v[76:79],a[40:43]
EX v39,v39
MM v[120:123],v[96:99],v[108:111],0
MM v[120:123],v[100:103],v[112:115],v[120:123]
MM v[120:123],v[104:107],v[116:119],v[120:123]
MM a[40:43],a[144:147],v[72:75],a[40:43]
TR v[96:97],v124 offset:6144
TR v[98:99],v124 offset:6400
MM a[48:51],a[144:147],v[80:83],a[48:51]
RD v[108:111],v125 offset:1536
TR v[100:101],v124 offset:8192
MM a[48:51],a[148:151],v[84:87],a[48:51]
TR v[102:103],v124 offset:8448
RD v[112:115],v125 offset:2048
MM a[56:59],a[148:151],v[92:95],a[56:59]
EX v40,v40
MM a[56:59],a[144:147],v[88:91],a[56:59]
EX v41,v41
MM a[60:63],a[152:155],v[88:91],a[60:63]
RD a[204:207],v227 offset:49152
CV v6,v36,v37
MM a[60:63],a[156:159],v[92:95],a[60:63]
EX v42,v42
MM a[52:55],a[156:159],v[84:87],a[52:55]
RD a[208:211],v224 offset:53248
CV v7,v38,v39
MM a[52:55],a[152:155],v[80:83],a[52:55]
EX v43,v43
MM a[44:47],a[152:155],v[72:75],a[44:47]
buffer_load_dwordx4 v230, s[8:11], s64 offen offset:2048 lds
MM a[44:47],a[156:159],v[76:79],a[44:47]
EX v44,v44
MM a[36:39],a[156:159],v[68:71],a[36:39]
s_waitcnt lgkmcnt(2)
EX v45,v45
MM a[36:39],a[152:155],v[64:67],a[36:39]
RD a[212:215],v225 offset:53248
CV v12,v40,v41
MM v[120:123],v[96:99],v[108:111],v[120:123]
MM v[120:123],v[100:103],v[112:115],v[120:123]
MM a[64:67],a[160:163],v[64:67],a[64:67]
EX v46,v46
MM a[64:67],a[164:167],v[68:71],a[64:67]
RD a[216:219],v226 offset:53248
CV v13,v42,v43
MM a[72:75],a[164:167],v[76:79],a[72:75]
EX v47,v47
MM a[72:75],a[160:163],v[72:75],a[72:75]
buffer_load_dwordx4 v231, s[8:11], s64 offen offset:3072 lds
MM a[80:83],a[160:163],v[80:83],a[80:83]
s_add_u32 m0, s65, 81920
EX v48,v48
MM a[80:83],a[164:167],v[84:87],a[80:83]
RD a[220:223],v227 offset:53248
CV v14,v44,v45
MM a[88:91],a[164:167],v[92:95],a[88:91]
s_mov_b32 exec_lo, 0x00ff00ff
s_mov_b32 exec_hi, 0x00ff00ff
buffer_store_dwordx4 v[120:123], v126, s[20:23], s76 offen offset:0 sc1
s_mov_b64 exec, -1
s_nop 0
MM a[88:91],a[160:163],v[88:91],a[88:91]
EX v49,v49
MM a[92:95],a[168:171],v[88:91],a[92:95]
EX v50,v50
MM a[92:95],a[172:175],v[92:95],a[92:95]
RD a[224:227],v224 offset:57344
CV v15,v46,v47
MM a[84:87],a[172:175],v[84:87],a[84:87]
EX v51,v51
MM a[84:87],a[168:171],v[80:83],a[84:87]
EX v52,v52
MM a[76:79],a[168:171],v[72:75],a[76:79]
buffer_load_dwordx4 v235, s[12:15], s81 offen offset:0 lds
MM a[76:79],a[172:175],v[76:79],a[76:79]
EX v53,v53
MM a[68:71],a[172:175],v[68:71],a[68:71]
RD a[228:231],v225 offset:57344
CV v20,v48,v49
MM a[68:71],a[168:171],v[64:67],a[68:71]
EX v54,v54
MM a[96:99],a[176:179],v[64:67],a[96:99]
RD a[232:235],v226 offset:57344
CV v21,v50,v51
MM a[96:99],a[180:183],v[68:71],a[96:99]
EX v55,v55
MM a[104:107],a[180:183],v[76:79],a[104:107]
RD a[236:239],v227 offset:57344
CV v22,v52,v53
MM a[104:107],a[176:179],v[72:75],a[104:107]
EX v56,v56
MM a[112:115],a[176:179],v[80:83],a[112:115]
buffer_load_dwordx4 v235, s[12:15], s64 offen offset:1024 lds
MM a[112:115],a[180:183],v[84:87],a[112:115]
EX v57,v57
MM a[120:123],a[180:183],v[92:95],a[120:123]
EX v58,v58
MM a[120:123],a[176:179],v[88:91],a[120:123]
RD a[240:243],v224 offset:61440
CV v23,v54,v55
MM a[124:127],a[184:187],v[88:91],a[124:127]
v_add_f32 v244, v144, v145
v_add_f32 v245, v148, v149
MM a[124:127],a[188:191],v[92:95],a[124:127]
v_add_f32 v246, v152, v153
v_add_f32 v247, v156, v157
MM a[116:119],a[188:191],v[84:87],a[116:119]
EX v59,v59
MM a[116:119],a[184:187],v[80:83],a[116:119]
EX v60,v60
MM a[108:111],a[184:187],v[72:75],a[108:111]
EX v61,v61
MM a[108:111],a[188:191],v[76:79],a[108:111]
RD a[244:247],v225 offset:61440
CV v28,v56,v57
MM a[100:103],a[188:191],v[68:71],a[100:103]
buffer_load_dwordx4 v235, s[12:15], s81 offen offset:2048 lds
MM a[100:103],a[184:187],v[64:67],a[100:103]
EX v62,v62
v_smfmac_f32_16x16x64_bf16 v[144:147], v[236:239], v[0:7], v234
RD a[248:251],v226 offset:61440
RD a[252:255],v227 offset:61440
EX v63,v63
v_smfmac_f32_16x16x64_bf16 v[148:151], v[236:239], v[8:15], v234
CV v29,v58,v59
v_smfmac_f32_16x16x64_bf16 v[152:155], v[236:239], v[16:23], v234
CV v30,v60,v61
CV v31,v62,v63
s_nop 1
)ASM"
R"ASM(
v_smfmac_f32_16x16x64_bf16 v[156:159], v[236:239], v[24:31], v234
s_waitcnt lgkmcnt(0)
buffer_load_dwordx4 v235, s[12:15], s64 offen offset:3072 lds
MM v[64:67],a[192:195],v[160:163],v[128:131]
s_add_u32 s64, s64, 16384
TR a[128:129],v232 offset:32768
s_add_u32 s81, s64, 896
MM v[64:67],a[196:199],v[176:179],v[64:67]
TR a[130:131],v232 offset:36864
v_max3_f32 v249, v144, v145, v148
MM v[64:67],a[200:203],v[192:195],v[64:67]
TR a[132:133],v232 offset:40960
v_max3_f32 v249, v249, v149, v152
MM v[64:67],a[204:207],v[208:211],v[64:67]
TR a[134:135],v232 offset:45056
v_max3_f32 v249, v249, v153, v156
MM v[68:71],a[220:223],v[208:211],v[128:131]
TR a[136:137],v233 offset:32768
v_max_f32 v249, v249, v157
MM v[68:71],a[216:219],v[192:195],v[68:71]
TR a[138:139],v233 offset:36864
v_cmp_lt_f32 vcc, s71, v249
MM v[68:71],a[212:215],v[176:179],v[68:71]
TR a[140:141],v233 offset:40960
TR a[142:143],v233 offset:45056
MM v[68:71],a[208:211],v[160:163],v[68:71]
s_cbranch_vccnz LSITE14_%=
LBACK14_%=:
MM v[76:79],a[208:211],v[164:167],v[132:135]
s_add_u32 s48, s48, 16
v_lshrrev_b32 v252, 6, v248
v_mul_u32_u24 v252, 3456, v252
MM v[76:79],a[212:215],v[180:183],v[76:79]
v_and_b32 v251, 48, v248
v_add_u32 v252, v252, v251
MM v[76:79],a[216:219],v[196:199],v[76:79]
s_and_b32 s90, %[swave], 1
EX v64,v64
MM v[76:79],a[220:223],v[212:215],v[76:79]
s_mul_i32 s90, s90, 276480
EX v65,v65
MM v[72:75],a[204:207],v[212:215],v[132:135]
s_bfe_u32 s91, s48, 131077
EX v66,v66
MM v[72:75],a[200:203],v[196:199],v[72:75]
s_mul_i32 s91, s91, 552960
TR a[144:145],v232 offset:33280
CV v64,v64,v65
MM v[72:75],a[196:199],v[180:183],v[72:75]
s_add_u32 s90, s90, s91
EX v67,v67
MM v[72:75],a[192:195],v[164:167],v[72:75]
s_and_b32 s91, s48, 0xffffff80
TR a[146:147],v232 offset:37376
CV v65,v66,v67
MM v[80:83],a[192:195],v[168:171],v[136:139]
s_add_u32 s90, s90, s91
EX v68,v68
MM v[80:83],a[196:199],v[184:187],v[80:83]
s_and_b32 s91, s48, 16
TR a[148:149],v232 offset:41472
TR a[150:151],v232 offset:45568
MM v[80:83],a[200:203],v[200:203],v[80:83]
s_lshl_b32 s91, s91, 2
EX v69,v69
MM v[80:83],a[204:207],v[216:219],v[80:83]
s_add_u32 s66, s90, s91
TR a[152:153],v233 offset:33280
CV v66,v68,v69
MM v[84:87],a[220:223],v[216:219],v[136:139]
s_mul_i32 s91, %[swave], 5120
EX v70,v70
MM v[84:87],a[216:219],v[200:203],v[84:87]
s_add_u32 s91, s91, %[slds]
EX v71,v71
MM v[84:87],a[212:215],v[184:187],v[84:87]
s_add_u32 s67, s91, 131072
TR a[154:155],v233 offset:37376
CV v67,v70,v71
MM v[84:87],a[208:211],v[168:171],v[84:87]
s_and_b32 s18, s17, 255
EX v72,v72
MM v[92:95],a[208:211],v[172:175],v[140:143]
s_sub_u32 s90, s18, 40
s_cmp_ge_u32 s18, 216
s_cselect_b32 s18, s90, s18
MM v[92:95],a[212:215],v[188:191],v[92:95]
s_lshl_b32 s18, s18, 4
EX v73,v73
MM v[92:95],a[216:219],v[204:207],v[92:95]
s_sub_u32 s90, s16, 90
TR a[156:157],v233 offset:41472
CV v72,v72,v73
MM v[92:95],a[220:223],v[220:223],v[92:95]
s_min_u32 s47, s90, s16
EX v74,v74
MM v[88:91],a[204:207],v[220:223],v[140:143]
s_mul_i32 s90, s47, 205
TR a[158:159],v233 offset:45568
TR a[160:161],v232 offset:33792
MM v[88:91],a[200:203],v[204:207],v[88:91]
s_lshr_b32 s46, s90, 11
EX v75,v75
MM v[88:91],a[196:199],v[188:191],v[88:91]
s_mul_i32 s90, s46, 244480
TR a[162:163],v232 offset:37888
CV v73,v74,v75
MM v[88:91],a[192:195],v[172:175],v[88:91]
s_lshl_b32 s91, s47, 7
EX v76,v76
MM v[96:99],a[224:227],v[160:163],v[128:131]
s_add_u32 s90, s90, s91
s_add_u32 s28, s28, s90
s_addc_u32 s29, s29, 0
MM v[96:99],a[228:231],v[176:179],v[96:99]
s_sub_u32 s30, 2211840, s90
EX v77,v77
MM v[96:99],a[232:235],v[192:195],v[96:99]
s_mov_b32 s31, 131072
TR a[164:165],v232 offset:41984
CV v74,v76,v77
MM v[96:99],a[236:239],v[208:211],v[96:99]
EX v78,v78
MM v[100:103],a[252:255],v[208:211],v[128:131]
TR a[166:167],v232 offset:46080
TR a[168:169],v233 offset:33792
MM v[100:103],a[248:251],v[192:195],v[100:103]
EX v79,v79
MM v[100:103],a[244:247],v[176:179],v[100:103]
TR a[170:171],v233 offset:37888
CV v75,v78,v79
MM v[100:103],a[240:243],v[160:163],v[100:103]
EX v80,v80
MM v[108:111],a[240:243],v[164:167],v[132:135]
TR a[172:173],v233 offset:41984
TR a[174:175],v233 offset:46080
MM v[108:111],a[244:247],v[180:183],v[108:111]
EX v81,v81
MM v[108:111],a[248:251],v[196:199],v[108:111]
EX v82,v82
MM v[108:111],a[252:255],v[212:215],v[108:111]
TR a[176:177],v232 offset:34304
CV v80,v80,v81
MM v[104:107],a[236:239],v[212:215],v[132:135]
EX v83,v83
MM v[104:107],a[232:235],v[196:199],v[104:107]
TR a[178:179],v232 offset:38400
CV v81,v82,v83
MM v[104:107],a[228:231],v[180:183],v[104:107]
EX v84,v84
MM v[104:107],a[224:227],v[164:167],v[104:107]
TR a[180:181],v232 offset:42496
TR a[182:183],v232 offset:46592
MM v[112:115],a[224:227],v[168:171],v[136:139]
EX v85,v85
MM v[112:115],a[228:231],v[184:187],v[112:115]
TR a[184:185],v233 offset:34304
CV v82,v84,v85
MM v[112:115],a[232:235],v[200:203],v[112:115]
EX v86,v86
MM v[112:115],a[236:239],v[216:219],v[112:115]
EX v87,v87
MM v[116:119],a[252:255],v[216:219],v[136:139]
TR a[186:187],v233 offset:38400
CV v83,v86,v87
MM v[116:119],a[248:251],v[200:203],v[116:119]
EX v88,v88
MM v[116:119],a[244:247],v[184:187],v[116:119]
TR a[188:189],v233 offset:42496
TR a[190:191],v233 offset:46592
MM v[116:119],a[240:243],v[168:171],v[116:119]
EX v89,v89
MM v[124:127],a[240:243],v[172:175],v[140:143]
EX v90,v90
MM v[124:127],a[244:247],v[188:191],v[124:127]
EX v91,v91
MM v[124:127],a[248:251],v[204:207],v[124:127]
EX v92,v92
MM v[124:127],a[252:255],v[220:223],v[124:127]
EX v93,v93
MM v[120:123],a[236:239],v[220:223],v[140:143]
EX v94,v94
MM v[120:123],a[232:235],v[204:207],v[120:123]
EX v95,v95
MM v[120:123],a[228:231],v[188:191],v[120:123]
EX v96,v96
MM v[120:123],a[224:227],v[172:175],v[120:123]
s_waitcnt vmcnt(14) lgkmcnt(0)
s_barrier
MM a[0:3],a[128:131],v[0:3],a[0:3]
s_add_u32 m0, s65, 32768
CV v88,v88,v89
CV v89,v90,v91
MM a[0:3],a[132:135],v[4:7],a[0:3]
RD a[192:195],v224 offset:0
CV v90,v92,v93
MM a[8:11],a[132:135],v[12:15],a[8:11]
RD a[196:199],v225 offset:0
CV v91,v94,v95
MM a[8:11],a[128:131],v[8:11],a[8:11]
buffer_load_dwordx4 v228, s[8:11], s64 offen offset:0 lds
MM a[16:19],a[128:131],v[16:19],a[16:19]
RD a[200:203],v226 offset:0
RD a[204:207],v227 offset:0
MM a[16:19],a[132:135],v[20:23],a[16:19]
EX v97,v97
MM a[24:27],a[132:135],v[28:31],a[24:27]
RD a[208:211],v224 offset:4096
CV v68,v96,v97
MM a[24:27],a[128:131],v[24:27],a[24:27]
EX v98,v98
MM a[28:31],a[136:139],v[24:27],a[28:31]
buffer_load_dwordx4 v229, s[8:11], s64 offen offset:1024 lds
MM a[28:31],a[140:143],v[28:31],a[28:31]
EX v99,v99
MM a[20:23],a[140:143],v[20:23],a[20:23]
RD a[212:215],v225 offset:4096
CV v69,v98,v99
MM a[20:23],a[136:139],v[16:19],a[20:23]
EX v100,v100
MM a[12:15],a[136:139],v[8:11],a[12:15]
EX v101,v101
MM a[12:15],a[140:143],v[12:15],a[12:15]
RD a[216:219],v226 offset:4096
CV v70,v100,v101
MM a[4:7],a[140:143],v[4:7],a[4:7]
EX v102,v102
MM a[4:7],a[136:139],v[0:3],a[4:7]
buffer_load_dwordx4 v230, s[8:11], s64 offen offset:2048 lds
MM a[32:35],a[144:147],v[0:3],a[32:35]
EX v103,v103
MM a[32:35],a[148:151],v[4:7],a[32:35]
RD a[220:223],v227 offset:4096
CV v71,v102,v103
MM a[40:43],a[148:151],v[12:15],a[40:43]
EX v104,v104
MM a[40:43],a[144:147],v[8:11],a[40:43]
EX v105,v105
MM a[48:51],a[144:147],v[16:19],a[48:51]
RD a[224:227],v224 offset:8192
CV v76,v104,v105
MM a[48:51],a[148:151],v[20:23],a[48:51]
EX v106,v106
MM a[56:59],a[148:151],v[28:31],a[56:59]
buffer_load_dwordx4 v231, s[8:11], s64 offen offset:3072 lds
MM a[56:59],a[144:147],v[24:27],a[56:59]
s_add_u32 m0, s65, 98304
EX v107,v107
MM a[60:63],a[152:155],v[24:27],a[60:63]
RD a[228:231],v225 offset:8192
CV v77,v106,v107
MM a[60:63],a[156:159],v[28:31],a[60:63]
EX v108,v108
MM a[52:55],a[156:159],v[20:23],a[52:55]
EX v109,v109
MM a[52:55],a[152:155],v[16:19],a[52:55]
RD a[232:235],v226 offset:8192
CV v78,v108,v109
MM a[44:47],a[152:155],v[8:11],a[44:47]
EX v110,v110
MM a[44:47],a[156:159],v[12:15],a[44:47]
buffer_load_dwordx4 v235, s[12:15], s81 offen offset:0 lds
MM a[36:39],a[156:159],v[4:7],a[36:39]
EX v111,v111
MM a[36:39],a[152:155],v[0:3],a[36:39]
RD a[236:239],v227 offset:8192
CV v79,v110,v111
MM a[64:67],a[160:163],v[0:3],a[64:67]
EX v112,v112
MM a[64:67],a[164:167],v[4:7],a[64:67]
EX v113,v113
MM a[72:75],a[164:167],v[12:15],a[72:75]
RD a[240:243],v224 offset:12288
CV v84,v112,v113
MM a[72:75],a[160:163],v[8:11],a[72:75]
buffer_load_dwordx4 v235, s[12:15], s64 offen offset:1024 lds
MM a[80:83],a[160:163],v[16:19],a[80:83]
s_waitcnt vmcnt(10)
v_mov_b32 v250, s69
s_mov_b64 exec, 1
buffer_store_dword v250, off, s[20:23], s97 offset:0 sc1
s_mov_b64 exec, -1
MM a[80:83],a[164:167],v[20:23],a[80:83]
s_add_u32 m0, s67, 0
s_add_u32 s85, s66, 0
buffer_load_dwordx4 v252, s[36:39], s85 offen offset:0 lds
MM a[88:91],a[164:167],v[28:31],a[88:91]
EX v114,v114
MM a[88:91],a[160:163],v[24:27],a[88:91]
EX v115,v115
MM a[92:95],a[168:171],v[24:27],a[92:95]
RD a[244:247],v225 offset:12288
CV v85,v114,v115
MM a[92:95],a[172:175],v[28:31],a[92:95]
EX v116,v116
MM a[84:87],a[172:175],v[20:23],a[84:87]
EX v117,v117
MM a[84:87],a[168:171],v[16:19],a[84:87]
RD a[248:251],v226 offset:12288
CV v86,v116,v117
MM a[76:79],a[168:171],v[8:11],a[76:79]
EX v118,v118
MM a[76:79],a[172:175],v[12:15],a[76:79]
EX v119,v119
MM a[68:71],a[172:175],v[4:7],a[68:71]
RD a[252:255],v227 offset:12288
CV v87,v118,v119
MM a[68:71],a[168:171],v[0:3],a[68:71]
s_add_u32 m0, s65, 98304
s_nop 0
buffer_load_dwordx4 v235, s[12:15], s81 offen offset:2048 lds
MM a[96:99],a[176:179],v[0:3],a[96:99]
EX v120,v120
MM a[96:99],a[180:183],v[4:7],a[96:99]
EX v121,v121
MM a[104:107],a[180:183],v[12:15],a[104:107]
EX v122,v122
MM a[104:107],a[176:179],v[8:11],a[104:107]
s_nop 0
LPG13_%=:
s_cbranch_execz LTR2_%=
buffer_load_dwordx4 v235, s[12:15], s64 offen offset:3072 lds
MM a[112:115],a[176:179],v[16:19],a[112:115]
EX v123,v123
MM a[112:115],a[180:183],v[20:23],a[112:115]
EX v124,v124
MM a[120:123],a[180:183],v[28:31],a[120:123]
EX v125,v125
MM a[120:123],a[176:179],v[24:27],a[120:123]
EX v126,v126
MM a[124:127],a[184:187],v[24:27],a[124:127]
v_add_f32 v244, v144, v145
v_add_f32 v245, v148, v149
MM a[124:127],a[188:191],v[28:31],a[124:127]
v_add_f32 v246, v152, v153
v_add_f32 v247, v156, v157
MM a[116:119],a[188:191],v[20:23],a[116:119]
EX v127,v127
MM a[116:119],a[184:187],v[16:19],a[116:119]
CV v92,v120,v121
CV v93,v122,v123
MM a[108:111],a[184:187],v[8:11],a[108:111]
CV v94,v124,v125
CV v95,v126,v127
MM a[108:111],a[188:191],v[12:15],a[108:111]
MM a[100:103],a[188:191],v[4:7],a[100:103]
MM a[100:103],a[184:187],v[0:3],a[100:103]
v_smfmac_f32_16x16x64_bf16 v[144:147], v[236:239], v[64:71], v234
v_smfmac_f32_16x16x64_bf16 v[148:151], v[236:239], v[72:79], v234
v_smfmac_f32_16x16x64_bf16 v[152:155], v[236:239], v[80:87], v234
v_smfmac_f32_16x16x64_bf16 v[156:159], v[236:239], v[88:95], v234
s_waitcnt lgkmcnt(0)
MM v[0:3],a[192:195],v[160:163],v[128:131]
s_add_u32 s64, s64, 16384
TR a[128:129],v232 offset:49152
s_add_u32 s81, s64, 896
MM v[0:3],a[196:199],v[176:179],v[0:3]
TR a[130:131],v232 offset:53248
v_max3_f32 v249, v144, v145, v148
MM v[0:3],a[200:203],v[192:195],v[0:3]
TR a[132:133],v232 offset:57344
v_max3_f32 v249, v249, v149, v152
MM v[0:3],a[204:207],v[208:211],v[0:3]
TR a[134:135],v232 offset:61440
v_max3_f32 v249, v249, v153, v156
MM v[4:7],a[220:223],v[208:211],v[128:131]
TR a[136:137],v233 offset:49152
v_max_f32 v249, v249, v157
MM v[4:7],a[216:219],v[192:195],v[4:7]
TR a[138:139],v233 offset:53248
v_cmp_lt_f32 vcc, s71, v249
MM v[4:7],a[212:215],v[176:179],v[4:7]
TR a[140:141],v233 offset:57344
TR a[142:143],v233 offset:61440
MM v[4:7],a[208:211],v[160:163],v[4:7]
s_cbranch_vccnz LSITE15_%=
LBACK15_%=:
MM v[12:15],a[208:211],v[164:167],v[132:135]
s_add_u32 m0, s67, 1024
s_add_u32 s85, s66, 55296
buffer_load_dwordx4 v252, s[36:39], s85 offen offset:0 lds
MM v[12:15],a[212:215],v[180:183],v[12:15]
EX v0,v0
MM v[12:15],a[216:219],v[196:199],v[12:15]
EX v1,v1
MM v[12:15],a[220:223],v[212:215],v[12:15]
TR a[144:145],v232 offset:49664
CV v0,v0,v1
MM v[8:11],a[204:207],v[212:215],v[132:135]
)ASM"
R"ASM(
EX v2,v2
MM v[8:11],a[200:203],v[196:199],v[8:11]
EX v3,v3
MM v[8:11],a[196:199],v[180:183],v[8:11]
TR a[146:147],v232 offset:53760
CV v1,v2,v3
MM v[8:11],a[192:195],v[164:167],v[8:11]
EX v4,v4
MM v[16:19],a[192:195],v[168:171],v[136:139]
TR a[148:149],v232 offset:57856
TR a[150:151],v232 offset:61952
MM v[16:19],a[196:199],v[184:187],v[16:19]
EX v5,v5
MM v[16:19],a[200:203],v[200:203],v[16:19]
TR a[152:153],v233 offset:49664
CV v2,v4,v5
MM v[16:19],a[204:207],v[216:219],v[16:19]
EX v6,v6
MM v[20:23],a[220:223],v[216:219],v[136:139]
EX v7,v7
MM v[20:23],a[216:219],v[200:203],v[20:23]
TR a[154:155],v233 offset:53760
CV v3,v6,v7
MM v[20:23],a[212:215],v[184:187],v[20:23]
EX v8,v8
MM v[20:23],a[208:211],v[168:171],v[20:23]
TR a[156:157],v233 offset:57856
TR a[158:159],v233 offset:61952
MM v[28:31],a[208:211],v[172:175],v[140:143]
EX v9,v9
MM v[28:31],a[212:215],v[188:191],v[28:31]
TR a[160:161],v232 offset:50176
CV v8,v8,v9
MM v[28:31],a[216:219],v[204:207],v[28:31]
EX v10,v10
MM v[28:31],a[220:223],v[220:223],v[28:31]
EX v11,v11
MM v[24:27],a[204:207],v[220:223],v[140:143]
TR a[162:163],v232 offset:54272
CV v9,v10,v11
MM v[24:27],a[200:203],v[204:207],v[24:27]
TR a[164:165],v232 offset:58368
TR a[166:167],v232 offset:62464
MM v[24:27],a[196:199],v[188:191],v[24:27]
EX v12,v12
MM v[24:27],a[192:195],v[172:175],v[24:27]
EX v13,v13
MM v[32:35],a[224:227],v[160:163],v[128:131]
TR a[168:169],v233 offset:50176
CV v10,v12,v13
MM v[32:35],a[228:231],v[176:179],v[32:35]
EX v14,v14
MM v[32:35],a[232:235],v[192:195],v[32:35]
TR a[170:171],v233 offset:54272
TR a[172:173],v233 offset:58368
MM v[32:35],a[236:239],v[208:211],v[32:35]
EX v15,v15
MM v[36:39],a[252:255],v[208:211],v[128:131]
TR a[174:175],v233 offset:62464
CV v11,v14,v15
MM v[36:39],a[248:251],v[192:195],v[36:39]
EX v16,v16
MM v[36:39],a[244:247],v[176:179],v[36:39]
EX v17,v17
MM v[36:39],a[240:243],v[160:163],v[36:39]
TR a[176:177],v232 offset:50688
CV v16,v16,v17
MM v[44:47],a[240:243],v[164:167],v[132:135]
s_add_u32 m0, s67, 2048
s_add_u32 s85, s66, 110592
buffer_load_dwordx4 v252, s[36:39], s85 offen offset:0 lds
MM v[44:47],a[244:247],v[180:183],v[44:47]
EX v18,v18
MM v[44:47],a[248:251],v[196:199],v[44:47]
TR a[178:179],v232 offset:54784
TR a[180:181],v232 offset:58880
MM v[44:47],a[252:255],v[212:215],v[44:47]
EX v19,v19
MM v[40:43],a[236:239],v[212:215],v[132:135]
TR a[182:183],v232 offset:62976
CV v17,v18,v19
MM v[40:43],a[232:235],v[196:199],v[40:43]
EX v20,v20
MM v[40:43],a[228:231],v[180:183],v[40:43]
EX v21,v21
MM v[40:43],a[224:227],v[164:167],v[40:43]
TR a[184:185],v233 offset:50688
CV v18,v20,v21
MM v[48:51],a[224:227],v[168:171],v[136:139]
EX v22,v22
MM v[48:51],a[228:231],v[184:187],v[48:51]
TR a[186:187],v233 offset:54784
TR a[188:189],v233 offset:58880
MM v[48:51],a[232:235],v[200:203],v[48:51]
EX v23,v23
MM v[48:51],a[236:239],v[216:219],v[48:51]
TR a[190:191],v233 offset:62976
CV v19,v22,v23
MM v[52:55],a[252:255],v[216:219],v[136:139]
EX v24,v24
MM v[52:55],a[248:251],v[200:203],v[52:55]
EX v25,v25
MM v[52:55],a[244:247],v[184:187],v[52:55]
EX v26,v26
MM v[52:55],a[240:243],v[168:171],v[52:55]
EX v27,v27
MM v[60:63],a[240:243],v[172:175],v[140:143]
EX v28,v28
MM v[60:63],a[244:247],v[188:191],v[60:63]
EX v29,v29
MM v[60:63],a[248:251],v[204:207],v[60:63]
EX v30,v30
MM v[60:63],a[252:255],v[220:223],v[60:63]
EX v31,v31
MM v[56:59],a[236:239],v[220:223],v[140:143]
EX v32,v32
MM v[56:59],a[232:235],v[204:207],v[56:59]
EX v33,v33
MM v[56:59],a[228:231],v[188:191],v[56:59]
EX v34,v34
MM v[56:59],a[224:227],v[172:175],v[56:59]
s_waitcnt lgkmcnt(0)
s_barrier
MM a[0:3],a[128:131],v[64:67],a[0:3]
s_add_u32 m0, s65, 49152
CV v24,v24,v25
CV v25,v26,v27
MM a[0:3],a[132:135],v[68:71],a[0:3]
RD a[192:195],v224 offset:16384
CV v26,v28,v29
MM a[8:11],a[132:135],v[76:79],a[8:11]
RD a[196:199],v225 offset:16384
CV v27,v30,v31
MM a[8:11],a[128:131],v[72:75],a[8:11]
buffer_load_dwordx4 v228, s[8:11], s64 offen offset:0 lds
MM a[16:19],a[128:131],v[80:83],a[16:19]
s_add_u32 m0, s67, 3072
s_add_u32 s85, s66, 165888
buffer_load_dwordx4 v252, s[36:39], s85 offen offset:0 lds
MM a[16:19],a[132:135],v[84:87],a[16:19]
RD a[200:203],v226 offset:16384
CV v4,v32,v33
MM a[24:27],a[132:135],v[92:95],a[24:27]
RD a[204:207],v227 offset:16384
RD a[208:211],v224 offset:20480
MM a[24:27],a[128:131],v[88:91],a[24:27]
s_add_u32 m0, s65, 49152
s_nop 0
buffer_load_dwordx4 v229, s[8:11], s64 offen offset:1024 lds
MM a[28:31],a[136:139],v[88:91],a[28:31]
EX v35,v35
MM a[28:31],a[140:143],v[92:95],a[28:31]
RD a[212:215],v225 offset:20480
CV v5,v34,v35
MM a[20:23],a[140:143],v[84:87],a[20:23]
EX v36,v36
MM a[20:23],a[136:139],v[80:83],a[20:23]
EX v37,v37
MM a[12:15],a[136:139],v[72:75],a[12:15]
RD a[216:219],v226 offset:20480
CV v6,v36,v37
MM a[12:15],a[140:143],v[76:79],a[12:15]
buffer_load_dwordx4 v230, s[8:11], s64 offen offset:2048 lds
MM a[4:7],a[140:143],v[68:71],a[4:7]
EX v38,v38
MM a[4:7],a[136:139],v[64:67],a[4:7]
EX v39,v39
MM a[32:35],a[144:147],v[64:67],a[32:35]
RD a[220:223],v227 offset:20480
CV v7,v38,v39
MM a[32:35],a[148:151],v[68:71],a[32:35]
EX v40,v40
MM a[40:43],a[148:151],v[76:79],a[40:43]
EX v41,v41
MM a[40:43],a[144:147],v[72:75],a[40:43]
RD a[224:227],v224 offset:24576
CV v12,v40,v41
MM a[48:51],a[144:147],v[80:83],a[48:51]
buffer_load_dwordx4 v231, s[8:11], s64 offen offset:3072 lds
MM a[48:51],a[148:151],v[84:87],a[48:51]
s_add_u32 m0, s65, 114688
EX v42,v42
MM a[56:59],a[148:151],v[92:95],a[56:59]
EX v43,v43
MM a[56:59],a[144:147],v[88:91],a[56:59]
RD a[228:231],v225 offset:24576
CV v13,v42,v43
MM a[60:63],a[152:155],v[88:91],a[60:63]
EX v44,v44
MM a[60:63],a[156:159],v[92:95],a[60:63]
buffer_load_dwordx4 v235, s[12:15], s81 offen offset:0 lds
MM a[52:55],a[156:159],v[84:87],a[52:55]
EX v45,v45
MM a[52:55],a[152:155],v[80:83],a[52:55]
RD a[232:235],v226 offset:24576
CV v14,v44,v45
MM a[44:47],a[152:155],v[72:75],a[44:47]
EX v46,v46
MM a[44:47],a[156:159],v[76:79],a[44:47]
EX v47,v47
MM a[36:39],a[156:159],v[68:71],a[36:39]
RD a[236:239],v227 offset:24576
CV v15,v46,v47
MM a[36:39],a[152:155],v[64:67],a[36:39]
EX v48,v48
MM a[64:67],a[160:163],v[64:67],a[64:67]
buffer_load_dwordx4 v235, s[12:15], s64 offen offset:1024 lds
MM a[64:67],a[164:167],v[68:71],a[64:67]
EX v49,v49
MM a[72:75],a[164:167],v[76:79],a[72:75]
RD a[240:243],v224 offset:28672
CV v20,v48,v49
MM a[72:75],a[160:163],v[72:75],a[72:75]
EX v50,v50
MM a[80:83],a[160:163],v[80:83],a[80:83]
EX v51,v51
MM a[80:83],a[164:167],v[84:87],a[80:83]
RD a[244:247],v225 offset:28672
CV v21,v50,v51
MM a[88:91],a[164:167],v[92:95],a[88:91]
EX v52,v52
MM a[88:91],a[160:163],v[88:91],a[88:91]
buffer_load_dwordx4 v235, s[12:15], s81 offen offset:2048 lds
MM a[92:95],a[168:171],v[88:91],a[92:95]
EX v53,v53
MM a[92:95],a[172:175],v[92:95],a[92:95]
RD a[248:251],v226 offset:28672
CV v22,v52,v53
MM a[84:87],a[172:175],v[84:87],a[84:87]
EX v54,v54
MM a[84:87],a[168:171],v[80:83],a[84:87]
EX v55,v55
MM a[76:79],a[168:171],v[72:75],a[76:79]
RD a[252:255],v227 offset:28672
CV v23,v54,v55
MM a[76:79],a[172:175],v[76:79],a[76:79]
EX v56,v56
MM a[68:71],a[172:175],v[68:71],a[68:71]
buffer_load_dwordx4 v235, s[12:15], s64 offen offset:3072 lds
MM a[68:71],a[168:171],v[64:67],a[68:71]
EX v57,v57
MM a[96:99],a[176:179],v[64:67],a[96:99]
EX v58,v58
MM a[96:99],a[180:183],v[68:71],a[96:99]
EX v59,v59
MM a[104:107],a[180:183],v[76:79],a[104:107]
EX v60,v60
MM a[104:107],a[176:179],v[72:75],a[104:107]
EX v61,v61
MM a[112:115],a[176:179],v[80:83],a[112:115]
EX v62,v62
MM a[112:115],a[180:183],v[84:87],a[112:115]
EX v63,v63
MM a[120:123],a[180:183],v[92:95],a[120:123]
s_add_u32 m0, s67, 4096
s_add_u32 s85, s66, 221184
buffer_load_dwordx4 v252, s[36:39], s85 offen offset:0 lds
MM a[120:123],a[176:179],v[88:91],a[120:123]
CV v28,v56,v57
CV v29,v58,v59
MM a[124:127],a[184:187],v[88:91],a[124:127]
v_add_f32 v244, v144, v145
v_add_f32 v245, v148, v149
MM a[124:127],a[188:191],v[92:95],a[124:127]
v_add_f32 v246, v152, v153
v_add_f32 v247, v156, v157
MM a[116:119],a[188:191],v[84:87],a[116:119]
CV v30,v60,v61
CV v31,v62,v63
MM a[116:119],a[184:187],v[80:83],a[116:119]
MM a[108:111],a[184:187],v[72:75],a[108:111]
MM a[108:111],a[188:191],v[76:79],a[108:111]
MM a[100:103],a[188:191],v[68:71],a[100:103]
MM a[100:103],a[184:187],v[64:67],a[100:103]
v_smfmac_f32_16x16x64_bf16 v[144:147], v[236:239], v[0:7], v234
v_smfmac_f32_16x16x64_bf16 v[148:151], v[236:239], v[8:15], v234
v_smfmac_f32_16x16x64_bf16 v[152:155], v[236:239], v[16:23], v234
v_smfmac_f32_16x16x64_bf16 v[156:159], v[236:239], v[24:31], v234
s_waitcnt lgkmcnt(0)
MM v[64:67],a[192:195],v[160:163],v[128:131]
s_add_u32 s64, s64, 16384
TR a[128:129],v232 offset:0
s_add_u32 s81, s64, 896
MM v[64:67],a[196:199],v[176:179],v[64:67]
TR a[130:131],v232 offset:4096
v_max3_f32 v249, v144, v145, v148
MM v[64:67],a[200:203],v[192:195],v[64:67]
TR a[132:133],v232 offset:8192
v_max3_f32 v249, v249, v149, v152
MM v[64:67],a[204:207],v[208:211],v[64:67]
TR a[134:135],v232 offset:12288
v_max3_f32 v249, v249, v153, v156
MM v[68:71],a[220:223],v[208:211],v[128:131]
TR a[136:137],v233 offset:0
v_max_f32 v249, v249, v157
MM v[68:71],a[216:219],v[192:195],v[68:71]
TR a[138:139],v233 offset:4096
v_cmp_lt_f32 vcc, s71, v249
MM v[68:71],a[212:215],v[176:179],v[68:71]
TR a[140:141],v233 offset:8192
TR a[142:143],v233 offset:12288
MM v[68:71],a[208:211],v[160:163],v[68:71]
s_cbranch_vccnz LSITE16_%=
LBACK16_%=:
MM v[76:79],a[208:211],v[164:167],v[132:135]
EX v64,v64
MM v[76:79],a[212:215],v[180:183],v[76:79]
EX v65,v65
MM v[76:79],a[216:219],v[196:199],v[76:79]
TR a[144:145],v232 offset:512
CV v64,v64,v65
MM v[76:79],a[220:223],v[212:215],v[76:79]
EX v66,v66
MM v[72:75],a[204:207],v[212:215],v[132:135]
EX v67,v67
MM v[72:75],a[200:203],v[196:199],v[72:75]
TR a[146:147],v232 offset:4608
CV v65,v66,v67
MM v[72:75],a[196:199],v[180:183],v[72:75]
EX v68,v68
MM v[72:75],a[192:195],v[164:167],v[72:75]
TR a[148:149],v232 offset:8704
TR a[150:151],v232 offset:12800
MM v[80:83],a[192:195],v[168:171],v[136:139]
EX v69,v69
MM v[80:83],a[196:199],v[184:187],v[80:83]
TR a[152:153],v233 offset:512
CV v66,v68,v69
MM v[80:83],a[200:203],v[200:203],v[80:83]
EX v70,v70
MM v[80:83],a[204:207],v[216:219],v[80:83]
TR a[154:155],v233 offset:4608
TR a[156:157],v233 offset:8704
MM v[84:87],a[220:223],v[216:219],v[136:139]
s_add_u32 s87, s95, 1024
buffer_store_dword v243, v248, s[20:23], s87 offen offset:12 sc1
MM v[84:87],a[216:219],v[200:203],v[84:87]
EX v71,v71
MM v[84:87],a[212:215],v[184:187],v[84:87]
TR a[158:159],v233 offset:12800
CV v67,v70,v71
MM v[84:87],a[208:211],v[168:171],v[84:87]
EX v72,v72
MM v[92:95],a[208:211],v[172:175],v[140:143]
EX v73,v73
MM v[92:95],a[212:215],v[188:191],v[92:95]
TR a[160:161],v232 offset:1024
CV v72,v72,v73
MM v[92:95],a[216:219],v[204:207],v[92:95]
EX v74,v74
MM v[92:95],a[220:223],v[220:223],v[92:95]
EX v75,v75
MM v[88:91],a[204:207],v[220:223],v[140:143]
TR a[162:163],v232 offset:5120
CV v73,v74,v75
MM v[88:91],a[200:203],v[204:207],v[88:91]
TR a[164:165],v232 offset:9216
TR a[166:167],v232 offset:13312
MM v[88:91],a[196:199],v[188:191],v[88:91]
EX v76,v76
MM v[88:91],a[192:195],v[172:175],v[88:91]
EX v77,v77
MM v[96:99],a[224:227],v[160:163],v[128:131]
TR a[168:169],v233 offset:1024
CV v74,v76,v77
MM v[96:99],a[228:231],v[176:179],v[96:99]
EX v78,v78
MM v[96:99],a[232:235],v[192:195],v[96:99]
TR a[170:171],v233 offset:5120
TR a[172:173],v233 offset:9216
MM v[96:99],a[236:239],v[208:211],v[96:99]
EX v79,v79
MM v[100:103],a[252:255],v[208:211],v[128:131]
TR a[174:175],v233 offset:13312
CV v75,v78,v79
MM v[100:103],a[248:251],v[192:195],v[100:103]
EX v80,v80
MM v[100:103],a[244:247],v[176:179],v[100:103]
EX v81,v81
MM v[100:103],a[240:243],v[160:163],v[100:103]
TR a[176:177],v232 offset:1536
CV v80,v80,v81
MM v[108:111],a[240:243],v[164:167],v[132:135]
EX v82,v82
MM v[108:111],a[244:247],v[180:183],v[108:111]
TR a[178:179],v232 offset:5632
TR a[180:181],v232 offset:9728
MM v[108:111],a[248:251],v[196:199],v[108:111]
EX v83,v83
MM v[108:111],a[252:255],v[212:215],v[108:111]
)ASM"
R"ASM(
TR a[182:183],v232 offset:13824
CV v81,v82,v83
MM v[104:107],a[236:239],v[212:215],v[132:135]
EX v84,v84
MM v[104:107],a[232:235],v[196:199],v[104:107]
EX v85,v85
MM v[104:107],a[228:231],v[180:183],v[104:107]
TR a[184:185],v233 offset:1536
CV v82,v84,v85
MM v[104:107],a[224:227],v[164:167],v[104:107]
EX v86,v86
MM v[112:115],a[224:227],v[168:171],v[136:139]
TR a[186:187],v233 offset:5632
TR a[188:189],v233 offset:9728
MM v[112:115],a[228:231],v[184:187],v[112:115]
EX v87,v87
MM v[112:115],a[232:235],v[200:203],v[112:115]
TR a[190:191],v233 offset:13824
CV v83,v86,v87
MM v[112:115],a[236:239],v[216:219],v[112:115]
EX v88,v88
MM v[116:119],a[252:255],v[216:219],v[136:139]
EX v89,v89
MM v[116:119],a[248:251],v[200:203],v[116:119]
EX v90,v90
MM v[116:119],a[244:247],v[184:187],v[116:119]
EX v91,v91
MM v[116:119],a[240:243],v[168:171],v[116:119]
EX v92,v92
MM v[124:127],a[240:243],v[172:175],v[140:143]
EX v93,v93
MM v[124:127],a[244:247],v[188:191],v[124:127]
EX v94,v94
MM v[124:127],a[248:251],v[204:207],v[124:127]
EX v95,v95
MM v[124:127],a[252:255],v[220:223],v[124:127]
EX v96,v96
MM v[120:123],a[236:239],v[220:223],v[140:143]
EX v97,v97
MM v[120:123],a[232:235],v[204:207],v[120:123]
EX v98,v98
MM v[120:123],a[228:231],v[188:191],v[120:123]
EX v99,v99
MM v[120:123],a[224:227],v[172:175],v[120:123]
s_waitcnt vmcnt(19) lgkmcnt(0)
s_barrier
MM a[0:3],a[128:131],v[0:3],a[0:3]
s_add_u32 m0, s65, 0
CV v88,v88,v89
CV v89,v90,v91
MM a[0:3],a[132:135],v[4:7],a[0:3]
RD a[192:195],v224 offset:32768
CV v90,v92,v93
MM a[8:11],a[132:135],v[12:15],a[8:11]
RD a[196:199],v225 offset:32768
CV v91,v94,v95
MM a[8:11],a[128:131],v[8:11],a[8:11]
buffer_load_dwordx4 v228, s[8:11], s64 offen offset:0 lds
MM a[16:19],a[128:131],v[16:19],a[16:19]
RD a[200:203],v226 offset:32768
CV v68,v96,v97
MM a[16:19],a[132:135],v[20:23],a[16:19]
RD a[204:207],v227 offset:32768
CV v69,v98,v99
MM a[24:27],a[132:135],v[28:31],a[24:27]
buffer_load_dwordx4 v229, s[8:11], s64 offen offset:1024 lds
MM a[24:27],a[128:131],v[24:27],a[24:27]
RD a[208:211],v224 offset:36864
RD a[212:215],v225 offset:36864
MM a[28:31],a[136:139],v[24:27],a[28:31]
buffer_load_dwordx4 v230, s[8:11], s64 offen offset:2048 lds
MM a[28:31],a[140:143],v[28:31],a[28:31]
EX v100,v100
MM a[20:23],a[140:143],v[20:23],a[20:23]
EX v101,v101
MM a[20:23],a[136:139],v[16:19],a[20:23]
RD a[216:219],v226 offset:36864
CV v70,v100,v101
MM a[12:15],a[136:139],v[8:11],a[12:15]
EX v102,v102
MM a[12:15],a[140:143],v[12:15],a[12:15]
RD a[220:223],v227 offset:36864
RD a[224:227],v224 offset:40960
MM a[4:7],a[140:143],v[4:7],a[4:7]
EX v103,v103
MM a[4:7],a[136:139],v[0:3],a[4:7]
RD a[228:231],v225 offset:40960
CV v71,v102,v103
MM a[32:35],a[144:147],v[0:3],a[32:35]
EX v104,v104
MM a[32:35],a[148:151],v[4:7],a[32:35]
buffer_load_dwordx4 v231, s[8:11], s64 offen offset:3072 lds
MM a[40:43],a[148:151],v[12:15],a[40:43]
s_add_u32 m0, s65, 65536
EX v105,v105
MM a[40:43],a[144:147],v[8:11],a[40:43]
RD a[232:235],v226 offset:40960
CV v76,v104,v105
MM a[48:51],a[144:147],v[16:19],a[48:51]
EX v106,v106
MM a[48:51],a[148:151],v[20:23],a[48:51]
EX v107,v107
MM a[56:59],a[148:151],v[28:31],a[56:59]
RD a[236:239],v227 offset:40960
CV v77,v106,v107
MM a[56:59],a[144:147],v[24:27],a[56:59]
EX v108,v108
MM a[60:63],a[152:155],v[24:27],a[60:63]
buffer_load_dwordx4 v235, s[12:15], s81 offen offset:0 lds
MM a[60:63],a[156:159],v[28:31],a[60:63]
EX v109,v109
MM a[52:55],a[156:159],v[20:23],a[52:55]
s_add_u32 s87, s95, 2048
buffer_store_dword v243, v248, s[20:23], s87 offen offset:12 sc1
MM a[52:55],a[152:155],v[16:19],a[52:55]
RD a[240:243],v224 offset:45056
CV v78,v108,v109
MM a[44:47],a[152:155],v[8:11],a[44:47]
EX v110,v110
MM a[44:47],a[156:159],v[12:15],a[44:47]
EX v111,v111
MM a[36:39],a[156:159],v[4:7],a[36:39]
RD a[244:247],v225 offset:45056
CV v79,v110,v111
MM a[36:39],a[152:155],v[0:3],a[36:39]
LPG14_%=:
s_cbranch_execz LTR2_%=
EX v112,v112
MM a[64:67],a[160:163],v[0:3],a[64:67]
buffer_load_dwordx4 v235, s[12:15], s64 offen offset:1024 lds
MM a[64:67],a[164:167],v[4:7],a[64:67]
EX v113,v113
MM a[72:75],a[164:167],v[12:15],a[72:75]
RD a[248:251],v226 offset:45056
CV v84,v112,v113
MM a[72:75],a[160:163],v[8:11],a[72:75]
EX v114,v114
MM a[80:83],a[160:163],v[16:19],a[80:83]
EX v115,v115
MM a[80:83],a[164:167],v[20:23],a[80:83]
RD a[252:255],v227 offset:45056
CV v85,v114,v115
MM a[88:91],a[164:167],v[28:31],a[88:91]
EX v116,v116
MM a[88:91],a[160:163],v[24:27],a[88:91]
buffer_load_dwordx4 v235, s[12:15], s81 offen offset:2048 lds
MM a[92:95],a[168:171],v[24:27],a[92:95]
EX v117,v117
MM a[92:95],a[172:175],v[28:31],a[92:95]
EX v118,v118
MM a[84:87],a[172:175],v[20:23],a[84:87]
buffer_load_dwordx4 v235, s[12:15], s64 offen offset:3072 lds
MM a[84:87],a[168:171],v[16:19],a[84:87]
EX v119,v119
MM a[76:79],a[168:171],v[8:11],a[76:79]
EX v120,v120
MM a[76:79],a[172:175],v[12:15],a[76:79]
EX v121,v121
MM a[68:71],a[172:175],v[4:7],a[68:71]
EX v122,v122
MM a[68:71],a[168:171],v[0:3],a[68:71]
EX v123,v123
MM a[96:99],a[176:179],v[0:3],a[96:99]
EX v124,v124
MM a[96:99],a[180:183],v[4:7],a[96:99]
EX v125,v125
MM a[104:107],a[180:183],v[12:15],a[104:107]
EX v126,v126
MM a[104:107],a[176:179],v[8:11],a[104:107]
EX v127,v127
MM a[112:115],a[176:179],v[16:19],a[112:115]
CV v86,v116,v117
CV v87,v118,v119
MM a[112:115],a[180:183],v[20:23],a[112:115]
CV v92,v120,v121
CV v93,v122,v123
MM a[120:123],a[180:183],v[28:31],a[120:123]
CV v94,v124,v125
CV v95,v126,v127
MM a[120:123],a[176:179],v[24:27],a[120:123]
MM a[124:127],a[184:187],v[24:27],a[124:127]
v_add_f32 v244, v144, v145
v_add_f32 v245, v148, v149
MM a[124:127],a[188:191],v[28:31],a[124:127]
v_add_f32 v246, v152, v153
v_add_f32 v247, v156, v157
MM a[116:119],a[188:191],v[20:23],a[116:119]
MM a[116:119],a[184:187],v[16:19],a[116:119]
MM a[108:111],a[184:187],v[8:11],a[108:111]
MM a[108:111],a[188:191],v[12:15],a[108:111]
MM a[100:103],a[188:191],v[4:7],a[100:103]
MM a[100:103],a[184:187],v[0:3],a[100:103]
v_smfmac_f32_16x16x64_bf16 v[144:147], v[236:239], v[64:71], v234
v_smfmac_f32_16x16x64_bf16 v[148:151], v[236:239], v[72:79], v234
v_smfmac_f32_16x16x64_bf16 v[152:155], v[236:239], v[80:87], v234
v_smfmac_f32_16x16x64_bf16 v[156:159], v[236:239], v[88:95], v234
s_waitcnt lgkmcnt(0)
MM v[0:3],a[192:195],v[160:163],v[128:131]
s_add_u32 s64, s64, 16384
TR a[128:129],v232 offset:16384
s_add_u32 s81, s64, 896
MM v[0:3],a[196:199],v[176:179],v[0:3]
TR a[130:131],v232 offset:20480
v_max3_f32 v249, v144, v145, v148
MM v[0:3],a[200:203],v[192:195],v[0:3]
TR a[132:133],v232 offset:24576
v_max3_f32 v249, v249, v149, v152
MM v[0:3],a[204:207],v[208:211],v[0:3]
TR a[134:135],v232 offset:28672
v_max3_f32 v249, v249, v153, v156
MM v[4:7],a[220:223],v[208:211],v[128:131]
TR a[136:137],v233 offset:16384
v_max_f32 v249, v249, v157
MM v[4:7],a[216:219],v[192:195],v[4:7]
TR a[138:139],v233 offset:20480
v_cmp_lt_f32 vcc, s71, v249
MM v[4:7],a[212:215],v[176:179],v[4:7]
TR a[140:141],v233 offset:24576
TR a[142:143],v233 offset:28672
MM v[4:7],a[208:211],v[160:163],v[4:7]
s_cbranch_vccnz LSITE17_%=
LBACK17_%=:
MM v[12:15],a[208:211],v[164:167],v[132:135]
v_lshrrev_b32 v127, 8, v248
v_lshlrev_b32 v124, 9, v127
MM v[12:15],a[212:215],v[180:183],v[12:15]
v_and_b32 v126, 192, v248
v_add_u32 v124, v124, v126
MM v[12:15],a[216:219],v[196:199],v[12:15]
v_bfe_u32 v126, v248, 4, 2
v_lshl_add_u32 v124, v126, 3, v124
MM v[12:15],a[220:223],v[212:215],v[12:15]
s_lshr_b32 s76, %[swave], 1
EX v0,v0
MM v[8:11],a[204:207],v[212:215],v[132:135]
s_mul_i32 s76, s76, 10240
EX v1,v1
MM v[8:11],a[200:203],v[196:199],v[8:11]
s_and_b32 s77, %[swave], 1
EX v2,v2
MM v[8:11],a[196:199],v[180:183],v[8:11]
s_lshl_b32 s77, s77, 5
TR a[144:145],v232 offset:16896
CV v0,v0,v1
MM v[8:11],a[192:195],v[164:167],v[8:11]
s_add_u32 s76, s76, s77
EX v3,v3
MM v[16:19],a[192:195],v[168:171],v[136:139]
s_add_u32 s76, s76, %[slds]
EX v4,v4
MM v[16:19],a[196:199],v[184:187],v[16:19]
s_add_u32 s76, s76, 131072
v_add_u32 v124, s76, v124
v_bfe_u32 v126, v248, 4, 3
MM v[16:19],a[200:203],v[200:203],v[16:19]
v_lshl_add_u32 v126, v127, 3, v126
v_lshlrev_b32 v126, 4, v126
MM v[16:19],a[204:207],v[216:219],v[16:19]
s_mul_i32 s76, %[swave], 3072
EX v5,v5
MM v[20:23],a[220:223],v[216:219],v[136:139]
s_add_u32 s76, s76, %[slds]
TR a[146:147],v232 offset:20992
CV v1,v2,v3
MM v[20:23],a[216:219],v[200:203],v[20:23]
s_add_u32 s76, s76, 151552
TR a[148:149],v232 offset:25088
v_add_u32 v125, s76, v126
MM v[20:23],a[212:215],v[184:187],v[20:23]
s_lshr_b32 s76, s48, 7
EX v6,v6
MM v[20:23],a[208:211],v[168:171],v[20:23]
s_lshl_b32 s76, s76, 5
EX v7,v7
MM v[28:31],a[208:211],v[172:175],v[140:143]
s_and_b32 s77, s48, 16
TR a[150:151],v232 offset:29184
TR a[152:153],v233 offset:16896
MM v[28:31],a[212:215],v[188:191],v[28:31]
s_add_u32 s76, s76, s77
EX v8,v8
MM v[28:31],a[216:219],v[204:207],v[28:31]
s_bfe_u32 s77, s48, 131077
TR a[154:155],v233 offset:20992
CV v2,v4,v5
MM v[28:31],a[220:223],v[220:223],v[28:31]
s_lshl_b32 s77, s77, 2
EX v9,v9
MM v[24:27],a[204:207],v[220:223],v[140:143]
s_add_u32 s76, s76, s77
TR a[156:157],v233 offset:25088
CV v3,v6,v7
MM v[24:27],a[200:203],v[204:207],v[24:27]
s_add_u32 s76, s76, %[swave]
EX v10,v10
MM v[24:27],a[196:199],v[188:191],v[24:27]
s_lshl_b32 s97, s76, 2
TR a[158:159],v233 offset:29184
CV v8,v8,v9
MM v[24:27],a[192:195],v[172:175],v[24:27]
s_add_u32 s97, s97, 77824
EX v11,v11
MM v[32:35],a[224:227],v[160:163],v[128:131]
s_lshl_b32 s76, s76, 9
TR a[160:161],v232 offset:17408
TR a[162:163],v232 offset:21504
MM v[32:35],a[228:231],v[176:179],v[32:35]
s_add_u32 s76, s76, 22183936
EX v12,v12
MM v[32:35],a[232:235],v[192:195],v[32:35]
EX v13,v13
MM v[32:35],a[236:239],v[208:211],v[32:35]
TR a[164:165],v232 offset:25600
CV v9,v10,v11
MM v[36:39],a[252:255],v[208:211],v[128:131]
EX v14,v14
MM v[36:39],a[248:251],v[192:195],v[36:39]
TR a[166:167],v232 offset:29696
CV v10,v12,v13
MM v[36:39],a[244:247],v[176:179],v[36:39]
EX v15,v15
MM v[36:39],a[240:243],v[160:163],v[36:39]
TR a[168:169],v233 offset:17408
TR a[170:171],v233 offset:21504
MM v[44:47],a[240:243],v[164:167],v[132:135]
EX v16,v16
MM v[44:47],a[244:247],v[180:183],v[44:47]
EX v17,v17
MM v[44:47],a[248:251],v[196:199],v[44:47]
TR a[172:173],v233 offset:25600
CV v11,v14,v15
MM v[44:47],a[252:255],v[212:215],v[44:47]
EX v18,v18
MM v[40:43],a[236:239],v[212:215],v[132:135]
TR a[174:175],v233 offset:29696
CV v16,v16,v17
MM v[40:43],a[232:235],v[196:199],v[40:43]
EX v19,v19
MM v[40:43],a[228:231],v[180:183],v[40:43]
TR a[176:177],v232 offset:17920
TR a[178:179],v232 offset:22016
MM v[40:43],a[224:227],v[164:167],v[40:43]
EX v20,v20
MM v[48:51],a[224:227],v[168:171],v[136:139]
EX v21,v21
MM v[48:51],a[228:231],v[184:187],v[48:51]
TR a[180:181],v232 offset:26112
CV v17,v18,v19
MM v[48:51],a[232:235],v[200:203],v[48:51]
EX v22,v22
MM v[48:51],a[236:239],v[216:219],v[48:51]
TR a[182:183],v232 offset:30208
CV v18,v20,v21
MM v[52:55],a[252:255],v[216:219],v[136:139]
EX v23,v23
MM v[52:55],a[248:251],v[200:203],v[52:55]
TR a[184:185],v233 offset:17920
TR a[186:187],v233 offset:22016
MM v[52:55],a[244:247],v[184:187],v[52:55]
EX v24,v24
MM v[52:55],a[240:243],v[168:171],v[52:55]
TR a[188:189],v233 offset:26112
CV v19,v22,v23
MM v[60:63],a[240:243],v[172:175],v[140:143]
EX v25,v25
MM v[60:63],a[244:247],v[188:191],v[60:63]
TR a[190:191],v233 offset:30208
CV v24,v24,v25
MM v[60:63],a[248:251],v[204:207],v[60:63]
EX v26,v26
MM v[60:63],a[252:255],v[220:223],v[60:63]
EX v27,v27
MM v[56:59],a[236:239],v[220:223],v[140:143]
EX v28,v28
MM v[56:59],a[232:235],v[204:207],v[56:59]
EX v29,v29
MM v[56:59],a[228:231],v[188:191],v[56:59]
EX v30,v30
MM v[56:59],a[224:227],v[172:175],v[56:59]
s_waitcnt vmcnt(10) lgkmcnt(0)
s_barrier
MM a[0:3],a[128:131],v[64:67],a[0:3]
s_add_u32 m0, s65, 16384
CV v25,v26,v27
CV v26,v28,v29
MM a[0:3],a[132:135],v[68:71],a[0:3]
RD a[192:195],v224 offset:49152
RD a[196:199],v225 offset:49152
MM a[8:11],a[132:135],v[76:79],a[8:11]
EX v31,v31
MM a[8:11],a[128:131],v[72:75],a[8:11]
RD a[200:203],v226 offset:49152
CV v27,v30,v31
MM a[16:19],a[128:131],v[80:83],a[16:19]
)ASM"
R"ASM(
TR v[96:97],v124 offset:0
TR v[98:99],v124 offset:256
MM a[16:19],a[132:135],v[84:87],a[16:19]
RD v[108:111],v125 offset:0
TR v[100:101],v124 offset:2048
MM a[24:27],a[132:135],v[92:95],a[24:27]
TR v[102:103],v124 offset:2304
RD v[112:115],v125 offset:512
MM a[24:27],a[128:131],v[88:91],a[24:27]
TR v[104:105],v124 offset:4096
TR v[106:107],v124 offset:4352
MM a[28:31],a[136:139],v[88:91],a[28:31]
RD v[116:119],v125 offset:1024
RD a[204:207],v227 offset:49152
EX v32,v32
MM a[28:31],a[140:143],v[92:95],a[28:31]
EX v33,v33
MM a[20:23],a[140:143],v[84:87],a[20:23]
EX v34,v34
MM a[20:23],a[136:139],v[80:83],a[20:23]
buffer_load_dwordx4 v228, s[8:11], s64 offen offset:0 lds
MM a[12:15],a[136:139],v[72:75],a[12:15]
EX v35,v35
MM a[12:15],a[140:143],v[76:79],a[12:15]
EX v36,v36
MM a[4:7],a[140:143],v[68:71],a[4:7]
CV v4,v32,v33
CV v5,v34,v35
MM a[4:7],a[136:139],v[64:67],a[4:7]
EX v37,v37
MM a[32:35],a[144:147],v[64:67],a[32:35]
EX v38,v38
MM a[32:35],a[148:151],v[68:71],a[32:35]
s_waitcnt lgkmcnt(1)
buffer_load_dwordx4 v229, s[8:11], s64 offen offset:1024 lds
MM a[40:43],a[148:151],v[76:79],a[40:43]
EX v39,v39
MM v[120:123],v[96:99],v[108:111],0
MM v[120:123],v[100:103],v[112:115],v[120:123]
MM v[120:123],v[104:107],v[116:119],v[120:123]
MM a[40:43],a[144:147],v[72:75],a[40:43]
TR v[96:97],v124 offset:6144
TR v[98:99],v124 offset:6400
MM a[48:51],a[144:147],v[80:83],a[48:51]
RD v[108:111],v125 offset:1536
TR v[100:101],v124 offset:8192
MM a[48:51],a[148:151],v[84:87],a[48:51]
TR v[102:103],v124 offset:8448
RD v[112:115],v125 offset:2048
MM a[56:59],a[148:151],v[92:95],a[56:59]
EX v40,v40
MM a[56:59],a[144:147],v[88:91],a[56:59]
EX v41,v41
MM a[60:63],a[152:155],v[88:91],a[60:63]
CV v6,v36,v37
CV v7,v38,v39
MM a[60:63],a[156:159],v[92:95],a[60:63]
EX v42,v42
MM a[52:55],a[156:159],v[84:87],a[52:55]
RD a[208:211],v224 offset:53248
RD a[212:215],v225 offset:53248
MM a[52:55],a[152:155],v[80:83],a[52:55]
EX v43,v43
MM a[44:47],a[152:155],v[72:75],a[44:47]
buffer_load_dwordx4 v230, s[8:11], s64 offen offset:2048 lds
MM a[44:47],a[156:159],v[76:79],a[44:47]
EX v44,v44
MM a[36:39],a[156:159],v[68:71],a[36:39]
s_waitcnt lgkmcnt(2)
EX v45,v45
MM a[36:39],a[152:155],v[64:67],a[36:39]
CV v12,v40,v41
CV v13,v42,v43
MM v[120:123],v[96:99],v[108:111],v[120:123]
MM v[120:123],v[100:103],v[112:115],v[120:123]
MM a[64:67],a[160:163],v[64:67],a[64:67]
EX v46,v46
MM a[64:67],a[164:167],v[68:71],a[64:67]
RD a[216:219],v226 offset:53248
CV v14,v44,v45
MM a[72:75],a[164:167],v[76:79],a[72:75]
EX v47,v47
MM a[72:75],a[160:163],v[72:75],a[72:75]
buffer_load_dwordx4 v231, s[8:11], s64 offen offset:3072 lds
MM a[80:83],a[160:163],v[80:83],a[80:83]
s_add_u32 m0, s65, 81920
EX v48,v48
MM a[80:83],a[164:167],v[84:87],a[80:83]
RD a[220:223],v227 offset:53248
CV v15,v46,v47
MM a[88:91],a[164:167],v[92:95],a[88:91]
s_mov_b32 exec_lo, 0x00ff00ff
s_mov_b32 exec_hi, 0x00ff00ff
buffer_store_dwordx4 v[120:123], v126, s[20:23], s76 offen offset:0 sc1
s_mov_b64 exec, -1
s_nop 0
MM a[88:91],a[160:163],v[88:91],a[88:91]
EX v49,v49
MM a[92:95],a[168:171],v[88:91],a[92:95]
EX v50,v50
MM a[92:95],a[172:175],v[92:95],a[92:95]
EX v51,v51
MM a[84:87],a[172:175],v[84:87],a[84:87]
RD a[224:227],v224 offset:57344
CV v20,v48,v49
MM a[84:87],a[168:171],v[80:83],a[84:87]
EX v52,v52
MM a[76:79],a[168:171],v[72:75],a[76:79]
buffer_load_dwordx4 v235, s[12:15], s81 offen offset:0 lds
MM a[76:79],a[172:175],v[76:79],a[76:79]
EX v53,v53
MM a[68:71],a[172:175],v[68:71],a[68:71]
RD a[228:231],v225 offset:57344
CV v21,v50,v51
MM a[68:71],a[168:171],v[64:67],a[68:71]
EX v54,v54
MM a[96:99],a[176:179],v[64:67],a[96:99]
RD a[232:235],v226 offset:57344
CV v22,v52,v53
MM a[96:99],a[180:183],v[68:71],a[96:99]
EX v55,v55
MM a[104:107],a[180:183],v[76:79],a[104:107]
EX v56,v56
MM a[104:107],a[176:179],v[72:75],a[104:107]
buffer_load_dwordx4 v235, s[12:15], s64 offen offset:1024 lds
MM a[112:115],a[176:179],v[80:83],a[112:115]
EX v57,v57
MM a[112:115],a[180:183],v[84:87],a[112:115]
RD a[236:239],v227 offset:57344
CV v23,v54,v55
MM a[120:123],a[180:183],v[92:95],a[120:123]
EX v58,v58
MM a[120:123],a[176:179],v[88:91],a[120:123]
RD a[240:243],v224 offset:61440
CV v28,v56,v57
MM a[124:127],a[184:187],v[88:91],a[124:127]
v_add_f32 v244, v144, v145
v_add_f32 v245, v148, v149
MM a[124:127],a[188:191],v[92:95],a[124:127]
v_add_f32 v246, v152, v153
v_add_f32 v247, v156, v157
MM a[116:119],a[188:191],v[84:87],a[116:119]
EX v59,v59
MM a[116:119],a[184:187],v[80:83],a[116:119]
EX v60,v60
MM a[108:111],a[184:187],v[72:75],a[108:111]
EX v61,v61
MM a[108:111],a[188:191],v[76:79],a[108:111]
buffer_load_dwordx4 v235, s[12:15], s81 offen offset:2048 lds
MM a[100:103],a[188:191],v[68:71],a[100:103]
EX v62,v62
MM a[100:103],a[184:187],v[64:67],a[100:103]
RD a[244:247],v225 offset:61440
CV v29,v58,v59
v_smfmac_f32_16x16x64_bf16 v[144:147], v[236:239], v[0:7], v234
RD a[248:251],v226 offset:61440
RD a[252:255],v227 offset:61440
EX v63,v63
v_smfmac_f32_16x16x64_bf16 v[148:151], v[236:239], v[8:15], v234
CV v30,v60,v61
v_smfmac_f32_16x16x64_bf16 v[152:155], v[236:239], v[16:23], v234
CV v31,v62,v63
s_nop 1
v_smfmac_f32_16x16x64_bf16 v[156:159], v[236:239], v[24:31], v234
s_waitcnt lgkmcnt(0)
buffer_load_dwordx4 v235, s[12:15], s64 offen offset:3072 lds
MM v[64:67],a[192:195],v[160:163],v[128:131]
s_add_u32 s64, s64, 16384
TR a[128:129],v232 offset:32768
s_add_u32 s81, s64, 896
MM v[64:67],a[196:199],v[176:179],v[64:67]
TR a[130:131],v232 offset:36864
v_max3_f32 v249, v144, v145, v148
MM v[64:67],a[200:203],v[192:195],v[64:67]
TR a[132:133],v232 offset:40960
v_max3_f32 v249, v249, v149, v152
MM v[64:67],a[204:207],v[208:211],v[64:67]
TR a[134:135],v232 offset:45056
v_max3_f32 v249, v249, v153, v156
MM v[68:71],a[220:223],v[208:211],v[128:131]
TR a[136:137],v233 offset:32768
v_max_f32 v249, v249, v157
MM v[68:71],a[216:219],v[192:195],v[68:71]
TR a[138:139],v233 offset:36864
v_cmp_lt_f32 vcc, s71, v249
MM v[68:71],a[212:215],v[176:179],v[68:71]
TR a[140:141],v233 offset:40960
TR a[142:143],v233 offset:45056
MM v[68:71],a[208:211],v[160:163],v[68:71]
s_cbranch_vccnz LSITE18_%=
LBACK18_%=:
MM v[76:79],a[208:211],v[164:167],v[132:135]
EX v64,v64
MM v[76:79],a[212:215],v[180:183],v[76:79]
EX v65,v65
MM v[76:79],a[216:219],v[196:199],v[76:79]
TR a[144:145],v232 offset:33280
CV v64,v64,v65
MM v[76:79],a[220:223],v[212:215],v[76:79]
EX v66,v66
MM v[72:75],a[204:207],v[212:215],v[132:135]
EX v67,v67
MM v[72:75],a[200:203],v[196:199],v[72:75]
TR a[146:147],v232 offset:37376
CV v65,v66,v67
MM v[72:75],a[196:199],v[180:183],v[72:75]
EX v68,v68
MM v[72:75],a[192:195],v[164:167],v[72:75]
TR a[148:149],v232 offset:41472
TR a[150:151],v232 offset:45568
MM v[80:83],a[192:195],v[168:171],v[136:139]
EX v69,v69
MM v[80:83],a[196:199],v[184:187],v[80:83]
TR a[152:153],v233 offset:33280
CV v66,v68,v69
MM v[80:83],a[200:203],v[200:203],v[80:83]
EX v70,v70
MM v[80:83],a[204:207],v[216:219],v[80:83]
TR a[154:155],v233 offset:37376
TR a[156:157],v233 offset:41472
MM v[84:87],a[220:223],v[216:219],v[136:139]
EX v71,v71
MM v[84:87],a[216:219],v[200:203],v[84:87]
TR a[158:159],v233 offset:45568
CV v67,v70,v71
MM v[84:87],a[212:215],v[184:187],v[84:87]
EX v72,v72
MM v[84:87],a[208:211],v[168:171],v[84:87]
EX v73,v73
MM v[92:95],a[208:211],v[172:175],v[140:143]
TR a[160:161],v232 offset:33792
CV v72,v72,v73
MM v[92:95],a[212:215],v[188:191],v[92:95]
EX v74,v74
MM v[92:95],a[216:219],v[204:207],v[92:95]
TR a[162:163],v232 offset:37888
TR a[164:165],v232 offset:41984
MM v[92:95],a[220:223],v[220:223],v[92:95]
EX v75,v75
MM v[88:91],a[204:207],v[220:223],v[140:143]
TR a[166:167],v232 offset:46080
CV v73,v74,v75
MM v[88:91],a[200:203],v[204:207],v[88:91]
EX v76,v76
MM v[88:91],a[196:199],v[188:191],v[88:91]
EX v77,v77
MM v[88:91],a[192:195],v[172:175],v[88:91]
TR a[168:169],v233 offset:33792
CV v74,v76,v77
MM v[96:99],a[224:227],v[160:163],v[128:131]
EX v78,v78
MM v[96:99],a[228:231],v[176:179],v[96:99]
TR a[170:171],v233 offset:37888
TR a[172:173],v233 offset:41984
MM v[96:99],a[232:235],v[192:195],v[96:99]
EX v79,v79
MM v[96:99],a[236:239],v[208:211],v[96:99]
TR a[174:175],v233 offset:46080
CV v75,v78,v79
MM v[100:103],a[252:255],v[208:211],v[128:131]
EX v80,v80
MM v[100:103],a[248:251],v[192:195],v[100:103]
EX v81,v81
MM v[100:103],a[244:247],v[176:179],v[100:103]
TR a[176:177],v232 offset:34304
CV v80,v80,v81
MM v[100:103],a[240:243],v[160:163],v[100:103]
EX v82,v82
MM v[108:111],a[240:243],v[164:167],v[132:135]
v_lshrrev_b32 v253, 7, v248
v_mul_u32_u24 v253, 1280, v253
MM v[108:111],a[244:247],v[180:183],v[108:111]
v_and_b32 v252, 112, v248
v_add_u32 v253, v253, v252
MM v[108:111],a[248:251],v[196:199],v[108:111]
s_mul_i32 s90, %[swave], 61440
TR a[178:179],v232 offset:38400
TR a[180:181],v232 offset:42496
MM v[108:111],a[252:255],v[212:215],v[108:111]
s_mul_i32 s91, %[swave], 6144
EX v83,v83
MM v[104:107],a[236:239],v[212:215],v[132:135]
s_add_u32 s91, s91, %[slds]
TR a[182:183],v232 offset:46592
CV v81,v82,v83
MM v[104:107],a[232:235],v[196:199],v[104:107]
EX v84,v84
MM v[104:107],a[228:231],v[180:183],v[104:107]
EX v85,v85
MM v[104:107],a[224:227],v[164:167],v[104:107]
TR a[184:185],v233 offset:34304
CV v82,v84,v85
MM v[112:115],a[224:227],v[168:171],v[136:139]
EX v86,v86
MM v[112:115],a[228:231],v[184:187],v[112:115]
TR a[186:187],v233 offset:38400
TR a[188:189],v233 offset:42496
MM v[112:115],a[232:235],v[200:203],v[112:115]
EX v87,v87
MM v[112:115],a[236:239],v[216:219],v[112:115]
TR a[190:191],v233 offset:46592
CV v83,v86,v87
MM v[116:119],a[252:255],v[216:219],v[136:139]
EX v88,v88
MM v[116:119],a[248:251],v[200:203],v[116:119]
EX v89,v89
MM v[116:119],a[244:247],v[184:187],v[116:119]
EX v90,v90
MM v[116:119],a[240:243],v[168:171],v[116:119]
EX v91,v91
MM v[124:127],a[240:243],v[172:175],v[140:143]
EX v92,v92
MM v[124:127],a[244:247],v[188:191],v[124:127]
EX v93,v93
MM v[124:127],a[248:251],v[204:207],v[124:127]
EX v94,v94
MM v[124:127],a[252:255],v[220:223],v[124:127]
EX v95,v95
MM v[120:123],a[236:239],v[220:223],v[140:143]
EX v96,v96
MM v[120:123],a[232:235],v[204:207],v[120:123]
EX v97,v97
MM v[120:123],a[228:231],v[188:191],v[120:123]
EX v98,v98
MM v[120:123],a[224:227],v[172:175],v[120:123]
s_waitcnt vmcnt(14) lgkmcnt(0)
s_barrier
MM a[0:3],a[128:131],v[0:3],a[0:3]
s_nop 0
LPG15_%=:
s_cbranch_execz LTR2_%=
s_add_u32 m0, s65, 32768
CV v88,v88,v89
CV v89,v90,v91
MM a[0:3],a[132:135],v[4:7],a[0:3]
RD a[192:195],v224 offset:0
CV v90,v92,v93
MM a[8:11],a[132:135],v[12:15],a[8:11]
RD a[196:199],v225 offset:0
CV v91,v94,v95
MM a[8:11],a[128:131],v[8:11],a[8:11]
buffer_load_dwordx4 v228, s[8:11], s64 offen offset:0 lds
MM a[16:19],a[128:131],v[16:19],a[16:19]
RD a[200:203],v226 offset:0
CV v68,v96,v97
MM a[16:19],a[132:135],v[20:23],a[16:19]
buffer_load_dwordx4 v229, s[8:11], s64 offen offset:1024 lds
MM a[24:27],a[132:135],v[28:31],a[24:27]
RD a[204:207],v227 offset:0
RD a[208:211],v224 offset:4096
MM a[24:27],a[128:131],v[24:27],a[24:27]
EX v99,v99
MM a[28:31],a[136:139],v[24:27],a[28:31]
RD a[212:215],v225 offset:4096
CV v69,v98,v99
MM a[28:31],a[140:143],v[28:31],a[28:31]
EX v100,v100
MM a[20:23],a[140:143],v[20:23],a[20:23]
buffer_load_dwordx4 v230, s[8:11], s64 offen offset:2048 lds
MM a[20:23],a[136:139],v[16:19],a[20:23]
EX v101,v101
MM a[12:15],a[136:139],v[8:11],a[12:15]
RD a[216:219],v226 offset:4096
CV v70,v100,v101
MM a[12:15],a[140:143],v[12:15],a[12:15]
EX v102,v102
MM a[4:7],a[140:143],v[4:7],a[4:7]
EX v103,v103
MM a[4:7],a[136:139],v[0:3],a[4:7]
RD a[220:223],v227 offset:4096
CV v71,v102,v103
MM a[32:35],a[144:147],v[0:3],a[32:35]
EX v104,v104
MM a[32:35],a[148:151],v[4:7],a[32:35]
buffer_load_dwordx4 v231, s[8:11], s64 offen offset:3072 lds
MM a[40:43],a[148:151],v[12:15],a[40:43]
s_add_u32 m0, s65, 98304
EX v105,v105
MM a[40:43],a[144:147],v[8:11],a[40:43]
RD a[224:227],v224 offset:8192
CV v76,v104,v105
MM a[48:51],a[144:147],v[16:19],a[48:51]
EX v106,v106
MM a[48:51],a[148:151],v[20:23],a[48:51]
EX v107,v107
MM a[56:59],a[148:151],v[28:31],a[56:59]
RD a[228:231],v225 offset:8192
)ASM"
R"ASM(
CV v77,v106,v107
MM a[56:59],a[144:147],v[24:27],a[56:59]
EX v108,v108
MM a[60:63],a[152:155],v[24:27],a[60:63]
buffer_load_dwordx4 v235, s[12:15], s81 offen offset:0 lds
MM a[60:63],a[156:159],v[28:31],a[60:63]
EX v109,v109
MM a[52:55],a[156:159],v[20:23],a[52:55]
RD a[232:235],v226 offset:8192
CV v78,v108,v109
MM a[52:55],a[152:155],v[16:19],a[52:55]
EX v110,v110
MM a[44:47],a[152:155],v[8:11],a[44:47]
EX v111,v111
MM a[44:47],a[156:159],v[12:15],a[44:47]
RD a[236:239],v227 offset:8192
CV v79,v110,v111
MM a[36:39],a[156:159],v[4:7],a[36:39]
buffer_load_dwordx4 v235, s[12:15], s64 offen offset:1024 lds
MM a[36:39],a[152:155],v[0:3],a[36:39]
EX v112,v112
MM a[64:67],a[160:163],v[0:3],a[64:67]
EX v113,v113
MM a[64:67],a[164:167],v[4:7],a[64:67]
RD a[240:243],v224 offset:12288
CV v84,v112,v113
MM a[72:75],a[164:167],v[12:15],a[72:75]
EX v114,v114
MM a[72:75],a[160:163],v[8:11],a[72:75]
RD a[244:247],v225 offset:12288
RD a[248:251],v226 offset:12288
MM a[80:83],a[160:163],v[16:19],a[80:83]
s_waitcnt vmcnt(10)
v_mov_b32 v250, s69
s_mov_b64 exec, 1
buffer_store_dword v250, off, s[20:23], s97 offset:0 sc1
s_mov_b64 exec, -1
MM a[80:83],a[164:167],v[20:23],a[80:83]
s_add_u32 m0, s91, 139264
s_add_u32 s77, s90, 0
buffer_load_dwordx4 v253, s[28:31], s77 offen offset:0 lds
MM a[88:91],a[164:167],v[28:31],a[88:91]
EX v115,v115
MM a[88:91],a[160:163],v[24:27],a[88:91]
RD a[252:255],v227 offset:12288
CV v85,v114,v115
MM a[92:95],a[168:171],v[24:27],a[92:95]
EX v116,v116
MM a[92:95],a[172:175],v[28:31],a[92:95]
EX v117,v117
MM a[84:87],a[172:175],v[20:23],a[84:87]
s_add_u32 m0, s65, 98304
s_nop 0
buffer_load_dwordx4 v235, s[12:15], s81 offen offset:2048 lds
MM a[84:87],a[168:171],v[16:19],a[84:87]
EX v118,v118
MM a[76:79],a[168:171],v[8:11],a[76:79]
EX v119,v119
MM a[76:79],a[172:175],v[12:15],a[76:79]
EX v120,v120
MM a[68:71],a[172:175],v[4:7],a[68:71]
buffer_load_dwordx4 v235, s[12:15], s64 offen offset:3072 lds
MM a[68:71],a[168:171],v[0:3],a[68:71]
EX v121,v121
MM a[96:99],a[176:179],v[0:3],a[96:99]
EX v122,v122
MM a[96:99],a[180:183],v[4:7],a[96:99]
EX v123,v123
MM a[104:107],a[180:183],v[12:15],a[104:107]
EX v124,v124
MM a[104:107],a[176:179],v[8:11],a[104:107]
EX v125,v125
MM a[112:115],a[176:179],v[16:19],a[112:115]
EX v126,v126
MM a[112:115],a[180:183],v[20:23],a[112:115]
EX v127,v127
MM a[120:123],a[180:183],v[28:31],a[120:123]
CV v86,v116,v117
CV v87,v118,v119
MM a[120:123],a[176:179],v[24:27],a[120:123]
CV v92,v120,v121
CV v93,v122,v123
MM a[124:127],a[184:187],v[24:27],a[124:127]
v_add_f32 v244, v144, v145
v_add_f32 v245, v148, v149
MM a[124:127],a[188:191],v[28:31],a[124:127]
v_add_f32 v246, v152, v153
v_add_f32 v247, v156, v157
MM a[116:119],a[188:191],v[20:23],a[116:119]
CV v94,v124,v125
CV v95,v126,v127
MM a[116:119],a[184:187],v[16:19],a[116:119]
MM a[108:111],a[184:187],v[8:11],a[108:111]
MM a[108:111],a[188:191],v[12:15],a[108:111]
MM a[100:103],a[188:191],v[4:7],a[100:103]
MM a[100:103],a[184:187],v[0:3],a[100:103]
v_smfmac_f32_16x16x64_bf16 v[144:147], v[236:239], v[64:71], v234
v_smfmac_f32_16x16x64_bf16 v[148:151], v[236:239], v[72:79], v234
v_smfmac_f32_16x16x64_bf16 v[152:155], v[236:239], v[80:87], v234
v_smfmac_f32_16x16x64_bf16 v[156:159], v[236:239], v[88:95], v234
s_waitcnt lgkmcnt(0)
MM v[0:3],a[192:195],v[160:163],v[128:131]
s_add_u32 s64, s64, 16384
TR a[128:129],v232 offset:49152
s_add_u32 s81, s64, 896
MM v[0:3],a[196:199],v[176:179],v[0:3]
TR a[130:131],v232 offset:53248
v_max3_f32 v249, v144, v145, v148
MM v[0:3],a[200:203],v[192:195],v[0:3]
TR a[132:133],v232 offset:57344
v_max3_f32 v249, v249, v149, v152
MM v[0:3],a[204:207],v[208:211],v[0:3]
TR a[134:135],v232 offset:61440
v_max3_f32 v249, v249, v153, v156
MM v[4:7],a[220:223],v[208:211],v[128:131]
TR a[136:137],v233 offset:49152
v_max_f32 v249, v249, v157
MM v[4:7],a[216:219],v[192:195],v[4:7]
TR a[138:139],v233 offset:53248
v_cmp_lt_f32 vcc, s71, v249
MM v[4:7],a[212:215],v[176:179],v[4:7]
TR a[140:141],v233 offset:57344
TR a[142:143],v233 offset:61440
MM v[4:7],a[208:211],v[160:163],v[4:7]
s_cbranch_vccnz LSITE19_%=
LBACK19_%=:
MM v[12:15],a[208:211],v[164:167],v[132:135]
s_add_u32 m0, s91, 140288
s_add_u32 s77, s90, 10240
buffer_load_dwordx4 v253, s[28:31], s77 offen offset:0 lds
MM v[12:15],a[212:215],v[180:183],v[12:15]
EX v0,v0
MM v[12:15],a[216:219],v[196:199],v[12:15]
EX v1,v1
MM v[12:15],a[220:223],v[212:215],v[12:15]
TR a[144:145],v232 offset:49664
CV v0,v0,v1
MM v[8:11],a[204:207],v[212:215],v[132:135]
EX v2,v2
MM v[8:11],a[200:203],v[196:199],v[8:11]
EX v3,v3
MM v[8:11],a[196:199],v[180:183],v[8:11]
TR a[146:147],v232 offset:53760
CV v1,v2,v3
MM v[8:11],a[192:195],v[164:167],v[8:11]
EX v4,v4
MM v[16:19],a[192:195],v[168:171],v[136:139]
TR a[148:149],v232 offset:57856
TR a[150:151],v232 offset:61952
MM v[16:19],a[196:199],v[184:187],v[16:19]
EX v5,v5
MM v[16:19],a[200:203],v[200:203],v[16:19]
TR a[152:153],v233 offset:49664
CV v2,v4,v5
MM v[16:19],a[204:207],v[216:219],v[16:19]
EX v6,v6
MM v[20:23],a[220:223],v[216:219],v[136:139]
EX v7,v7
MM v[20:23],a[216:219],v[200:203],v[20:23]
TR a[154:155],v233 offset:53760
CV v3,v6,v7
MM v[20:23],a[212:215],v[184:187],v[20:23]
EX v8,v8
MM v[20:23],a[208:211],v[168:171],v[20:23]
TR a[156:157],v233 offset:57856
TR a[158:159],v233 offset:61952
MM v[28:31],a[208:211],v[172:175],v[140:143]
EX v9,v9
MM v[28:31],a[212:215],v[188:191],v[28:31]
TR a[160:161],v232 offset:50176
CV v8,v8,v9
MM v[28:31],a[216:219],v[204:207],v[28:31]
EX v10,v10
MM v[28:31],a[220:223],v[220:223],v[28:31]
EX v11,v11
MM v[24:27],a[204:207],v[220:223],v[140:143]
TR a[162:163],v232 offset:54272
CV v9,v10,v11
MM v[24:27],a[200:203],v[204:207],v[24:27]
TR a[164:165],v232 offset:58368
TR a[166:167],v232 offset:62464
MM v[24:27],a[196:199],v[188:191],v[24:27]
EX v12,v12
MM v[24:27],a[192:195],v[172:175],v[24:27]
EX v13,v13
MM v[32:35],a[224:227],v[160:163],v[128:131]
TR a[168:169],v233 offset:50176
CV v10,v12,v13
MM v[32:35],a[228:231],v[176:179],v[32:35]
EX v14,v14
MM v[32:35],a[232:235],v[192:195],v[32:35]
TR a[170:171],v233 offset:54272
TR a[172:173],v233 offset:58368
MM v[32:35],a[236:239],v[208:211],v[32:35]
EX v15,v15
MM v[36:39],a[252:255],v[208:211],v[128:131]
TR a[174:175],v233 offset:62464
CV v11,v14,v15
MM v[36:39],a[248:251],v[192:195],v[36:39]
EX v16,v16
MM v[36:39],a[244:247],v[176:179],v[36:39]
EX v17,v17
MM v[36:39],a[240:243],v[160:163],v[36:39]
TR a[176:177],v232 offset:50688
CV v16,v16,v17
MM v[44:47],a[240:243],v[164:167],v[132:135]
s_add_u32 m0, s91, 141312
s_add_u32 s77, s90, 20480
buffer_load_dwordx4 v253, s[28:31], s77 offen offset:0 lds
MM v[44:47],a[244:247],v[180:183],v[44:47]
EX v18,v18
MM v[44:47],a[248:251],v[196:199],v[44:47]
TR a[178:179],v232 offset:54784
TR a[180:181],v232 offset:58880
MM v[44:47],a[252:255],v[212:215],v[44:47]
EX v19,v19
MM v[40:43],a[236:239],v[212:215],v[132:135]
TR a[182:183],v232 offset:62976
CV v17,v18,v19
MM v[40:43],a[232:235],v[196:199],v[40:43]
EX v20,v20
MM v[40:43],a[228:231],v[180:183],v[40:43]
EX v21,v21
MM v[40:43],a[224:227],v[164:167],v[40:43]
TR a[184:185],v233 offset:50688
CV v18,v20,v21
MM v[48:51],a[224:227],v[168:171],v[136:139]
EX v22,v22
MM v[48:51],a[228:231],v[184:187],v[48:51]
TR a[186:187],v233 offset:54784
TR a[188:189],v233 offset:58880
MM v[48:51],a[232:235],v[200:203],v[48:51]
EX v23,v23
MM v[48:51],a[236:239],v[216:219],v[48:51]
TR a[190:191],v233 offset:62976
CV v19,v22,v23
MM v[52:55],a[252:255],v[216:219],v[136:139]
EX v24,v24
MM v[52:55],a[248:251],v[200:203],v[52:55]
EX v25,v25
MM v[52:55],a[244:247],v[184:187],v[52:55]
EX v26,v26
MM v[52:55],a[240:243],v[168:171],v[52:55]
EX v27,v27
MM v[60:63],a[240:243],v[172:175],v[140:143]
EX v28,v28
MM v[60:63],a[244:247],v[188:191],v[60:63]
EX v29,v29
MM v[60:63],a[248:251],v[204:207],v[60:63]
EX v30,v30
MM v[60:63],a[252:255],v[220:223],v[60:63]
EX v31,v31
MM v[56:59],a[236:239],v[220:223],v[140:143]
EX v32,v32
MM v[56:59],a[232:235],v[204:207],v[56:59]
EX v33,v33
MM v[56:59],a[228:231],v[188:191],v[56:59]
EX v34,v34
MM v[56:59],a[224:227],v[172:175],v[56:59]
s_waitcnt lgkmcnt(0)
s_barrier
MM a[0:3],a[128:131],v[64:67],a[0:3]
s_add_u32 m0, s65, 49152
CV v24,v24,v25
CV v25,v26,v27
MM a[0:3],a[132:135],v[68:71],a[0:3]
RD a[192:195],v224 offset:16384
CV v26,v28,v29
MM a[8:11],a[132:135],v[76:79],a[8:11]
RD a[196:199],v225 offset:16384
CV v27,v30,v31
MM a[8:11],a[128:131],v[72:75],a[8:11]
buffer_load_dwordx4 v228, s[8:11], s64 offen offset:0 lds
MM a[16:19],a[128:131],v[80:83],a[16:19]
s_add_u32 m0, s91, 142336
s_add_u32 s77, s90, 30720
buffer_load_dwordx4 v253, s[28:31], s77 offen offset:0 lds
MM a[16:19],a[132:135],v[84:87],a[16:19]
RD a[200:203],v226 offset:16384
CV v4,v32,v33
MM a[24:27],a[132:135],v[92:95],a[24:27]
RD a[204:207],v227 offset:16384
RD a[208:211],v224 offset:20480
MM a[24:27],a[128:131],v[88:91],a[24:27]
s_add_u32 m0, s65, 49152
s_nop 0
buffer_load_dwordx4 v229, s[8:11], s64 offen offset:1024 lds
MM a[28:31],a[136:139],v[88:91],a[28:31]
EX v35,v35
MM a[28:31],a[140:143],v[92:95],a[28:31]
RD a[212:215],v225 offset:20480
CV v5,v34,v35
MM a[20:23],a[140:143],v[84:87],a[20:23]
EX v36,v36
MM a[20:23],a[136:139],v[80:83],a[20:23]
EX v37,v37
MM a[12:15],a[136:139],v[72:75],a[12:15]
RD a[216:219],v226 offset:20480
CV v6,v36,v37
MM a[12:15],a[140:143],v[76:79],a[12:15]
buffer_load_dwordx4 v230, s[8:11], s64 offen offset:2048 lds
MM a[4:7],a[140:143],v[68:71],a[4:7]
EX v38,v38
MM a[4:7],a[136:139],v[64:67],a[4:7]
EX v39,v39
MM a[32:35],a[144:147],v[64:67],a[32:35]
RD a[220:223],v227 offset:20480
CV v7,v38,v39
MM a[32:35],a[148:151],v[68:71],a[32:35]
EX v40,v40
MM a[40:43],a[148:151],v[76:79],a[40:43]
EX v41,v41
MM a[40:43],a[144:147],v[72:75],a[40:43]
RD a[224:227],v224 offset:24576
CV v12,v40,v41
MM a[48:51],a[144:147],v[80:83],a[48:51]
buffer_load_dwordx4 v231, s[8:11], s64 offen offset:3072 lds
MM a[48:51],a[148:151],v[84:87],a[48:51]
s_add_u32 m0, s65, 114688
EX v42,v42
MM a[56:59],a[148:151],v[92:95],a[56:59]
EX v43,v43
MM a[56:59],a[144:147],v[88:91],a[56:59]
RD a[228:231],v225 offset:24576
CV v13,v42,v43
MM a[60:63],a[152:155],v[88:91],a[60:63]
EX v44,v44
MM a[60:63],a[156:159],v[92:95],a[60:63]
buffer_load_dwordx4 v235, s[12:15], s81 offen offset:0 lds
MM a[52:55],a[156:159],v[84:87],a[52:55]
EX v45,v45
MM a[52:55],a[152:155],v[80:83],a[52:55]
RD a[232:235],v226 offset:24576
CV v14,v44,v45
MM a[44:47],a[152:155],v[72:75],a[44:47]
EX v46,v46
MM a[44:47],a[156:159],v[76:79],a[44:47]
EX v47,v47
MM a[36:39],a[156:159],v[68:71],a[36:39]
RD a[236:239],v227 offset:24576
CV v15,v46,v47
MM a[36:39],a[152:155],v[64:67],a[36:39]
EX v48,v48
MM a[64:67],a[160:163],v[64:67],a[64:67]
buffer_load_dwordx4 v235, s[12:15], s64 offen offset:1024 lds
MM a[64:67],a[164:167],v[68:71],a[64:67]
EX v49,v49
MM a[72:75],a[164:167],v[76:79],a[72:75]
RD a[240:243],v224 offset:28672
CV v20,v48,v49
MM a[72:75],a[160:163],v[72:75],a[72:75]
EX v50,v50
MM a[80:83],a[160:163],v[80:83],a[80:83]
s_add_u32 m0, s91, 143360
s_add_u32 s77, s90, 40960
buffer_load_dwordx4 v253, s[28:31], s77 offen offset:0 lds
MM a[80:83],a[164:167],v[84:87],a[80:83]
EX v51,v51
MM a[88:91],a[164:167],v[92:95],a[88:91]
RD a[244:247],v225 offset:28672
CV v21,v50,v51
MM a[88:91],a[160:163],v[88:91],a[88:91]
EX v52,v52
MM a[92:95],a[168:171],v[88:91],a[92:95]
EX v53,v53
MM a[92:95],a[172:175],v[92:95],a[92:95]
RD a[248:251],v226 offset:28672
CV v22,v52,v53
MM a[84:87],a[172:175],v[84:87],a[84:87]
s_add_u32 m0, s65, 114688
s_nop 0
buffer_load_dwordx4 v235, s[12:15], s81 offen offset:2048 lds
MM a[84:87],a[168:171],v[80:83],a[84:87]
EX v54,v54
MM a[76:79],a[168:171],v[72:75],a[76:79]
EX v55,v55
MM a[76:79],a[172:175],v[76:79],a[76:79]
RD a[252:255],v227 offset:28672
CV v23,v54,v55
MM a[68:71],a[172:175],v[68:71],a[68:71]
EX v56,v56
MM a[68:71],a[168:171],v[64:67],a[68:71]
EX v57,v57
MM a[96:99],a[176:179],v[64:67],a[96:99]
buffer_load_dwordx4 v235, s[12:15], s64 offen offset:3072 lds
MM a[96:99],a[180:183],v[68:71],a[96:99]
EX v58,v58
MM a[104:107],a[180:183],v[76:79],a[104:107]
EX v59,v59
MM a[104:107],a[176:179],v[72:75],a[104:107]
)ASM"
R"ASM(
EX v60,v60
MM a[112:115],a[176:179],v[80:83],a[112:115]
EX v61,v61
MM a[112:115],a[180:183],v[84:87],a[112:115]
EX v62,v62
MM a[120:123],a[180:183],v[92:95],a[120:123]
s_add_u32 m0, s91, 144384
s_add_u32 s77, s90, 51200
buffer_load_dwordx4 v253, s[28:31], s77 offen offset:0 lds
MM a[120:123],a[176:179],v[88:91],a[120:123]
EX v63,v63
MM a[124:127],a[184:187],v[88:91],a[124:127]
v_add_f32 v244, v144, v145
v_add_f32 v245, v148, v149
MM a[124:127],a[188:191],v[92:95],a[124:127]
v_add_f32 v246, v152, v153
v_add_f32 v247, v156, v157
MM a[116:119],a[188:191],v[84:87],a[116:119]
CV v28,v56,v57
CV v29,v58,v59
MM a[116:119],a[184:187],v[80:83],a[116:119]
CV v30,v60,v61
CV v31,v62,v63
MM a[108:111],a[184:187],v[72:75],a[108:111]
MM a[108:111],a[188:191],v[76:79],a[108:111]
MM a[100:103],a[188:191],v[68:71],a[100:103]
MM a[100:103],a[184:187],v[64:67],a[100:103]
v_smfmac_f32_16x16x64_bf16 v[144:147], v[236:239], v[0:7], v234
v_smfmac_f32_16x16x64_bf16 v[148:151], v[236:239], v[8:15], v234
v_smfmac_f32_16x16x64_bf16 v[152:155], v[236:239], v[16:23], v234
v_smfmac_f32_16x16x64_bf16 v[156:159], v[236:239], v[24:31], v234
s_waitcnt lgkmcnt(0)
MM v[64:67],a[192:195],v[160:163],v[128:131]
s_add_u32 s64, s64, 16384
TR a[128:129],v232 offset:0
s_add_u32 s81, s64, 896
MM v[64:67],a[196:199],v[176:179],v[64:67]
TR a[130:131],v232 offset:4096
v_max3_f32 v249, v144, v145, v148
MM v[64:67],a[200:203],v[192:195],v[64:67]
TR a[132:133],v232 offset:8192
v_max3_f32 v249, v249, v149, v152
MM v[64:67],a[204:207],v[208:211],v[64:67]
TR a[134:135],v232 offset:12288
v_max3_f32 v249, v249, v153, v156
MM v[68:71],a[220:223],v[208:211],v[128:131]
TR a[136:137],v233 offset:0
v_max_f32 v249, v249, v157
MM v[68:71],a[216:219],v[192:195],v[68:71]
TR a[138:139],v233 offset:4096
v_cmp_lt_f32 vcc, s71, v249
MM v[68:71],a[212:215],v[176:179],v[68:71]
TR a[140:141],v233 offset:8192
TR a[142:143],v233 offset:12288
MM v[68:71],a[208:211],v[160:163],v[68:71]
s_cbranch_vccnz LSITE20_%=
LBACK20_%=:
MM v[76:79],a[208:211],v[164:167],v[132:135]
v_bfe_u32 v253, v248, 4, 4
v_lshlrev_b32 v253, 2, v253
MM v[76:79],a[212:215],v[180:183],v[76:79]
s_and_b32 s76, s18, 0xffffffc0
EX v64,v64
MM v[76:79],a[216:219],v[196:199],v[76:79]
s_add_u32 s76, s76, 77824
buffer_load_dword v252, v253, s[20:23], s76 offen offset:0 sc1
MM v[76:79],a[220:223],v[212:215],v[76:79]
EX v65,v65
MM v[72:75],a[204:207],v[212:215],v[132:135]
EX v66,v66
MM v[72:75],a[200:203],v[196:199],v[72:75]
TR a[144:145],v232 offset:512
CV v64,v64,v65
MM v[72:75],a[196:199],v[180:183],v[72:75]
EX v67,v67
MM v[72:75],a[192:195],v[164:167],v[72:75]
TR a[146:147],v232 offset:4608
CV v65,v66,v67
MM v[80:83],a[192:195],v[168:171],v[136:139]
EX v68,v68
MM v[80:83],a[196:199],v[184:187],v[80:83]
TR a[148:149],v232 offset:8704
TR a[150:151],v232 offset:12800
MM v[80:83],a[200:203],v[200:203],v[80:83]
EX v69,v69
MM v[80:83],a[204:207],v[216:219],v[80:83]
TR a[152:153],v233 offset:512
CV v66,v68,v69
MM v[84:87],a[220:223],v[216:219],v[136:139]
EX v70,v70
MM v[84:87],a[216:219],v[200:203],v[84:87]
EX v71,v71
MM v[84:87],a[212:215],v[184:187],v[84:87]
TR a[154:155],v233 offset:4608
CV v67,v70,v71
MM v[84:87],a[208:211],v[168:171],v[84:87]
EX v72,v72
MM v[92:95],a[208:211],v[172:175],v[140:143]
TR a[156:157],v233 offset:8704
TR a[158:159],v233 offset:12800
MM v[92:95],a[212:215],v[188:191],v[92:95]
EX v73,v73
MM v[92:95],a[216:219],v[204:207],v[92:95]
TR a[160:161],v232 offset:1024
CV v72,v72,v73
MM v[92:95],a[220:223],v[220:223],v[92:95]
EX v74,v74
MM v[88:91],a[204:207],v[220:223],v[140:143]
EX v75,v75
MM v[88:91],a[200:203],v[204:207],v[88:91]
TR a[162:163],v232 offset:5120
CV v73,v74,v75
MM v[88:91],a[196:199],v[188:191],v[88:91]
EX v76,v76
MM v[88:91],a[192:195],v[172:175],v[88:91]
TR a[164:165],v232 offset:9216
TR a[166:167],v232 offset:13312
MM v[96:99],a[224:227],v[160:163],v[128:131]
EX v77,v77
MM v[96:99],a[228:231],v[176:179],v[96:99]
TR a[168:169],v233 offset:1024
CV v74,v76,v77
MM v[96:99],a[232:235],v[192:195],v[96:99]
EX v78,v78
MM v[96:99],a[236:239],v[208:211],v[96:99]
EX v79,v79
MM v[100:103],a[252:255],v[208:211],v[128:131]
TR a[170:171],v233 offset:5120
CV v75,v78,v79
MM v[100:103],a[248:251],v[192:195],v[100:103]
EX v80,v80
MM v[100:103],a[244:247],v[176:179],v[100:103]
TR a[172:173],v233 offset:9216
TR a[174:175],v233 offset:13312
MM v[100:103],a[240:243],v[160:163],v[100:103]
EX v81,v81
s_nop 0
LPG16_%=:
s_cbranch_execz LTR2_%=
MM v[108:111],a[240:243],v[164:167],v[132:135]
TR a[176:177],v232 offset:1536
CV v80,v80,v81
MM v[108:111],a[244:247],v[180:183],v[108:111]
EX v82,v82
MM v[108:111],a[248:251],v[196:199],v[108:111]
TR a[178:179],v232 offset:5632
TR a[180:181],v232 offset:9728
MM v[108:111],a[252:255],v[212:215],v[108:111]
EX v83,v83
MM v[104:107],a[236:239],v[212:215],v[132:135]
TR a[182:183],v232 offset:13824
CV v81,v82,v83
MM v[104:107],a[232:235],v[196:199],v[104:107]
EX v84,v84
MM v[104:107],a[228:231],v[180:183],v[104:107]
EX v85,v85
MM v[104:107],a[224:227],v[164:167],v[104:107]
TR a[184:185],v233 offset:1536
CV v82,v84,v85
MM v[112:115],a[224:227],v[168:171],v[136:139]
EX v86,v86
MM v[112:115],a[228:231],v[184:187],v[112:115]
TR a[186:187],v233 offset:5632
TR a[188:189],v233 offset:9728
MM v[112:115],a[232:235],v[200:203],v[112:115]
EX v87,v87
MM v[112:115],a[236:239],v[216:219],v[112:115]
TR a[190:191],v233 offset:13824
CV v83,v86,v87
MM v[116:119],a[252:255],v[216:219],v[136:139]
EX v88,v88
MM v[116:119],a[248:251],v[200:203],v[116:119]
EX v89,v89
MM v[116:119],a[244:247],v[184:187],v[116:119]
EX v90,v90
MM v[116:119],a[240:243],v[168:171],v[116:119]
EX v91,v91
MM v[124:127],a[240:243],v[172:175],v[140:143]
EX v92,v92
MM v[124:127],a[244:247],v[188:191],v[124:127]
EX v93,v93
MM v[124:127],a[248:251],v[204:207],v[124:127]
EX v94,v94
MM v[124:127],a[252:255],v[220:223],v[124:127]
EX v95,v95
MM v[120:123],a[236:239],v[220:223],v[140:143]
EX v96,v96
MM v[120:123],a[232:235],v[204:207],v[120:123]
EX v97,v97
MM v[120:123],a[228:231],v[188:191],v[120:123]
s_getpc_b64 s[98:99]
LTA3_%=:
s_min_u32 s73, %[swave], 3
s_lshl_b32 s73, s73, 12
s_add_u32 s73, s73, LPG17_%=-LTA3_%=
s_add_u32 s98, s98, s73
s_addc_u32 s99, s99, 0
s_mov_b64 exec, 0
s_setpc_b64 s[98:99]
LTR3_%=:
s_mov_b64 exec, -1
CV v88,v88,v89
MM v[120:123],a[224:227],v[172:175],v[120:123]
s_waitcnt vmcnt(20) lgkmcnt(0)
s_barrier
MM a[0:3],a[128:131],v[0:3],a[0:3]
s_add_u32 m0, s65, 0
CV v89,v90,v91
CV v90,v92,v93
MM a[0:3],a[132:135],v[4:7],a[0:3]
RD a[192:195],v224 offset:32768
CV v91,v94,v95
MM a[8:11],a[132:135],v[12:15],a[8:11]
buffer_load_dwordx4 v228, s[8:11], s64 offen offset:0 lds
MM a[8:11],a[128:131],v[8:11],a[8:11]
RD a[196:199],v225 offset:32768
CV v68,v96,v97
MM a[16:19],a[128:131],v[16:19],a[16:19]
RD a[200:203],v226 offset:32768
RD a[204:207],v227 offset:32768
MM a[16:19],a[132:135],v[20:23],a[16:19]
buffer_load_dwordx4 v229, s[8:11], s64 offen offset:1024 lds
MM a[24:27],a[132:135],v[28:31],a[24:27]
EX v98,v98
MM a[24:27],a[128:131],v[24:27],a[24:27]
RD a[208:211],v224 offset:36864
RD a[212:215],v225 offset:36864
MM a[28:31],a[136:139],v[24:27],a[28:31]
EX v99,v99
MM a[28:31],a[140:143],v[28:31],a[28:31]
RD a[216:219],v226 offset:36864
CV v69,v98,v99
MM a[20:23],a[140:143],v[20:23],a[20:23]
EX v100,v100
MM a[20:23],a[136:139],v[16:19],a[20:23]
EX v101,v101
MM a[12:15],a[136:139],v[8:11],a[12:15]
RD a[220:223],v227 offset:36864
CV v70,v100,v101
MM a[12:15],a[140:143],v[12:15],a[12:15]
buffer_load_dwordx4 v230, s[8:11], s64 offen offset:2048 lds
MM a[4:7],a[140:143],v[4:7],a[4:7]
EX v102,v102
MM a[4:7],a[136:139],v[0:3],a[4:7]
EX v103,v103
MM a[32:35],a[144:147],v[0:3],a[32:35]
RD a[224:227],v224 offset:40960
CV v71,v102,v103
MM a[32:35],a[148:151],v[4:7],a[32:35]
EX v104,v104
MM a[40:43],a[148:151],v[12:15],a[40:43]
EX v105,v105
MM a[40:43],a[144:147],v[8:11],a[40:43]
RD a[228:231],v225 offset:40960
CV v76,v104,v105
MM a[48:51],a[144:147],v[16:19],a[48:51]
buffer_load_dwordx4 v231, s[8:11], s64 offen offset:3072 lds
MM a[48:51],a[148:151],v[20:23],a[48:51]
s_add_u32 m0, s65, 65536
EX v106,v106
MM a[56:59],a[148:151],v[28:31],a[56:59]
EX v107,v107
MM a[56:59],a[144:147],v[24:27],a[56:59]
RD a[232:235],v226 offset:40960
CV v77,v106,v107
MM a[60:63],a[152:155],v[24:27],a[60:63]
EX v108,v108
MM a[60:63],a[156:159],v[28:31],a[60:63]
buffer_load_dwordx4 v235, s[12:15], s81 offen offset:0 lds
MM a[52:55],a[156:159],v[20:23],a[52:55]
EX v109,v109
MM a[52:55],a[152:155],v[16:19],a[52:55]
RD a[236:239],v227 offset:40960
CV v78,v108,v109
MM a[44:47],a[152:155],v[8:11],a[44:47]
EX v110,v110
MM a[44:47],a[156:159],v[12:15],a[44:47]
EX v111,v111
MM a[36:39],a[156:159],v[4:7],a[36:39]
RD a[240:243],v224 offset:45056
CV v79,v110,v111
MM a[36:39],a[152:155],v[0:3],a[36:39]
EX v112,v112
MM a[64:67],a[160:163],v[0:3],a[64:67]
buffer_load_dwordx4 v235, s[12:15], s64 offen offset:1024 lds
MM a[64:67],a[164:167],v[4:7],a[64:67]
EX v113,v113
MM a[72:75],a[164:167],v[12:15],a[72:75]
RD a[244:247],v225 offset:45056
CV v84,v112,v113
MM a[72:75],a[160:163],v[8:11],a[72:75]
EX v114,v114
MM a[80:83],a[160:163],v[16:19],a[80:83]
EX v115,v115
MM a[80:83],a[164:167],v[20:23],a[80:83]
RD a[248:251],v226 offset:45056
CV v85,v114,v115
MM a[88:91],a[164:167],v[28:31],a[88:91]
EX v116,v116
MM a[88:91],a[160:163],v[24:27],a[88:91]
buffer_load_dwordx4 v235, s[12:15], s81 offen offset:2048 lds
MM a[92:95],a[168:171],v[24:27],a[92:95]
EX v117,v117
MM a[92:95],a[172:175],v[28:31],a[92:95]
RD a[252:255],v227 offset:45056
CV v86,v116,v117
MM a[84:87],a[172:175],v[20:23],a[84:87]
EX v118,v118
MM a[84:87],a[168:171],v[16:19],a[84:87]
EX v119,v119
MM a[76:79],a[168:171],v[8:11],a[76:79]
CV v87,v118,v119
MM a[76:79],a[172:175],v[12:15],a[76:79]
EX v120,v120
MM a[68:71],a[172:175],v[4:7],a[68:71]
buffer_load_dwordx4 v235, s[12:15], s64 offen offset:3072 lds
MM a[68:71],a[168:171],v[0:3],a[68:71]
EX v121,v121
MM a[96:99],a[176:179],v[0:3],a[96:99]
EX v122,v122
MM a[96:99],a[180:183],v[4:7],a[96:99]
EX v123,v123
MM a[104:107],a[180:183],v[12:15],a[104:107]
EX v124,v124
MM a[104:107],a[176:179],v[8:11],a[104:107]
EX v125,v125
MM a[112:115],a[176:179],v[16:19],a[112:115]
EX v126,v126
MM a[112:115],a[180:183],v[20:23],a[112:115]
EX v127,v127
MM a[120:123],a[180:183],v[28:31],a[120:123]
s_waitcnt vmcnt(8)
v_cmp_ne_u32 vcc, s69, v252
s_cbranch_vccnz LRFG_%=
LRFGb_%=:
MM a[120:123],a[176:179],v[24:27],a[120:123]
v_lshrrev_b32 v253, 8, v248
v_lshlrev_b32 v253, 10, v253
MM a[124:127],a[184:187],v[24:27],a[124:127]
v_add_f32 v244, v144, v145
v_add_f32 v245, v148, v149
MM a[124:127],a[188:191],v[28:31],a[124:127]
v_add_f32 v246, v152, v153
v_add_f32 v247, v156, v157
v_and_b32 v252, 240, v248
MM a[116:119],a[188:191],v[20:23],a[116:119]
v_add_u32 v253, v253, v252
CV v92,v120,v121
MM a[116:119],a[184:187],v[16:19],a[116:119]
s_lshr_b32 s76, s18, 6
CV v93,v122,v123
CV v94,v124,v125
MM a[108:111],a[184:187],v[8:11],a[108:111]
s_lshl_b32 s76, s76, 13
s_bfe_u32 s90, s18, 131076
CV v95,v126,v127
MM a[108:111],a[188:191],v[12:15],a[108:111]
s_lshl_b32 s90, s90, 8
s_add_u32 s76, s76, s90
s_add_u32 s76, s76, 22183936
MM a[100:103],a[188:191],v[4:7],a[100:103]
s_lshl_b32 s90, %[swave], 11
s_add_u32 s90, s90, %[slds]
MM a[100:103],a[184:187],v[0:3],a[100:103]
s_add_u32 m0, s90, 131072
s_nop 0
buffer_load_dwordx4 v253, s[20:23], s76 offen offset:0 sc1 lds
v_smfmac_f32_16x16x64_bf16 v[144:147], v[236:239], v[64:71], v234
v_smfmac_f32_16x16x64_bf16 v[148:151], v[236:239], v[72:79], v234
v_smfmac_f32_16x16x64_bf16 v[152:155], v[236:239], v[80:87], v234
s_add_u32 m0, s90, 132096
s_add_u32 s76, s76, 4096
buffer_load_dwordx4 v253, s[20:23], s76 offen offset:0 sc1 lds
v_smfmac_f32_16x16x64_bf16 v[156:159], v[236:239], v[88:95], v234
s_waitcnt lgkmcnt(0)
s_add_u32 s67, s95, 3072
s_mov_b32 s70, 3
s_cmp_ge_u32 s16, 90
s_cbranch_scc1 LLAP_%=
MM v[0:3],a[192:195],v[160:163],v[128:131]
s_add_u32 s64, s64, 16384
TR a[128:129],v232 offset:16384
s_add_u32 s81, s64, 896
MM v[0:3],a[196:199],v[176:179],v[0:3]
TR a[130:131],v232 offset:20480
v_max3_f32 v249, v144, v145, v148
)ASM"
R"ASM(
MM v[0:3],a[200:203],v[192:195],v[0:3]
TR a[132:133],v232 offset:24576
v_max3_f32 v249, v249, v149, v152
MM v[0:3],a[204:207],v[208:211],v[0:3]
TR a[134:135],v232 offset:28672
v_max3_f32 v249, v249, v153, v156
MM v[4:7],a[220:223],v[208:211],v[128:131]
TR a[136:137],v233 offset:16384
v_max_f32 v249, v249, v157
MM v[4:7],a[216:219],v[192:195],v[4:7]
TR a[138:139],v233 offset:20480
v_cmp_lt_f32 vcc, s71, v249
MM v[4:7],a[212:215],v[176:179],v[4:7]
TR a[140:141],v233 offset:24576
TR a[142:143],v233 offset:28672
MM v[4:7],a[208:211],v[160:163],v[4:7]
s_cbranch_vccnz LSITE21_%=
LBACK21_%=:
MM v[12:15],a[208:211],v[164:167],v[132:135]
EX v0,v0
MM v[12:15],a[212:215],v[180:183],v[12:15]
EX v1,v1
MM v[12:15],a[216:219],v[196:199],v[12:15]
TR a[144:145],v232 offset:16896
CV v0,v0,v1
MM v[12:15],a[220:223],v[212:215],v[12:15]
EX v2,v2
MM v[8:11],a[204:207],v[212:215],v[132:135]
EX v3,v3
MM v[8:11],a[200:203],v[196:199],v[8:11]
TR a[146:147],v232 offset:20992
CV v1,v2,v3
MM v[8:11],a[196:199],v[180:183],v[8:11]
EX v4,v4
MM v[8:11],a[192:195],v[164:167],v[8:11]
TR a[148:149],v232 offset:25088
TR a[150:151],v232 offset:29184
MM v[16:19],a[192:195],v[168:171],v[136:139]
EX v5,v5
MM v[16:19],a[196:199],v[184:187],v[16:19]
TR a[152:153],v233 offset:16896
CV v2,v4,v5
MM v[16:19],a[200:203],v[200:203],v[16:19]
EX v6,v6
MM v[16:19],a[204:207],v[216:219],v[16:19]
TR a[154:155],v233 offset:20992
TR a[156:157],v233 offset:25088
MM v[20:23],a[220:223],v[216:219],v[136:139]
s_add_u32 s87, s95, 3072
buffer_store_dword v243, v248, s[20:23], s87 offen offset:12 sc1
MM v[20:23],a[216:219],v[200:203],v[20:23]
EX v7,v7
MM v[20:23],a[212:215],v[184:187],v[20:23]
TR a[158:159],v233 offset:29184
CV v3,v6,v7
MM v[20:23],a[208:211],v[168:171],v[20:23]
EX v8,v8
MM v[28:31],a[208:211],v[172:175],v[140:143]
EX v9,v9
MM v[28:31],a[212:215],v[188:191],v[28:31]
TR a[160:161],v232 offset:17408
CV v8,v8,v9
MM v[28:31],a[216:219],v[204:207],v[28:31]
EX v10,v10
MM v[28:31],a[220:223],v[220:223],v[28:31]
EX v11,v11
MM v[24:27],a[204:207],v[220:223],v[140:143]
TR a[162:163],v232 offset:21504
CV v9,v10,v11
MM v[24:27],a[200:203],v[204:207],v[24:27]
TR a[164:165],v232 offset:25600
TR a[166:167],v232 offset:29696
MM v[24:27],a[196:199],v[188:191],v[24:27]
EX v12,v12
MM v[24:27],a[192:195],v[172:175],v[24:27]
EX v13,v13
MM v[32:35],a[224:227],v[160:163],v[128:131]
TR a[168:169],v233 offset:17408
CV v10,v12,v13
MM v[32:35],a[228:231],v[176:179],v[32:35]
EX v14,v14
MM v[32:35],a[232:235],v[192:195],v[32:35]
TR a[170:171],v233 offset:21504
TR a[172:173],v233 offset:25600
MM v[32:35],a[236:239],v[208:211],v[32:35]
EX v15,v15
MM v[36:39],a[252:255],v[208:211],v[128:131]
TR a[174:175],v233 offset:29696
CV v11,v14,v15
MM v[36:39],a[248:251],v[192:195],v[36:39]
EX v16,v16
MM v[36:39],a[244:247],v[176:179],v[36:39]
EX v17,v17
MM v[36:39],a[240:243],v[160:163],v[36:39]
TR a[176:177],v232 offset:17920
CV v16,v16,v17
MM v[44:47],a[240:243],v[164:167],v[132:135]
EX v18,v18
MM v[44:47],a[244:247],v[180:183],v[44:47]
TR a[178:179],v232 offset:22016
TR a[180:181],v232 offset:26112
MM v[44:47],a[248:251],v[196:199],v[44:47]
EX v19,v19
MM v[44:47],a[252:255],v[212:215],v[44:47]
TR a[182:183],v232 offset:30208
CV v17,v18,v19
MM v[40:43],a[236:239],v[212:215],v[132:135]
EX v20,v20
MM v[40:43],a[232:235],v[196:199],v[40:43]
EX v21,v21
MM v[40:43],a[228:231],v[180:183],v[40:43]
TR a[184:185],v233 offset:17920
CV v18,v20,v21
MM v[40:43],a[224:227],v[164:167],v[40:43]
EX v22,v22
MM v[48:51],a[224:227],v[168:171],v[136:139]
TR a[186:187],v233 offset:22016
TR a[188:189],v233 offset:26112
MM v[48:51],a[228:231],v[184:187],v[48:51]
EX v23,v23
MM v[48:51],a[232:235],v[200:203],v[48:51]
TR a[190:191],v233 offset:30208
CV v19,v22,v23
MM v[48:51],a[236:239],v[216:219],v[48:51]
EX v24,v24
MM v[52:55],a[252:255],v[216:219],v[136:139]
EX v25,v25
MM v[52:55],a[248:251],v[200:203],v[52:55]
EX v26,v26
MM v[52:55],a[244:247],v[184:187],v[52:55]
EX v27,v27
MM v[52:55],a[240:243],v[168:171],v[52:55]
EX v28,v28
MM v[60:63],a[240:243],v[172:175],v[140:143]
EX v29,v29
MM v[60:63],a[244:247],v[188:191],v[60:63]
EX v30,v30
MM v[60:63],a[248:251],v[204:207],v[60:63]
EX v31,v31
MM v[60:63],a[252:255],v[220:223],v[60:63]
EX v32,v32
MM v[56:59],a[236:239],v[220:223],v[140:143]
EX v33,v33
MM v[56:59],a[232:235],v[204:207],v[56:59]
EX v34,v34
MM v[56:59],a[228:231],v[188:191],v[56:59]
EX v35,v35
MM v[56:59],a[224:227],v[172:175],v[56:59]
s_waitcnt lgkmcnt(0)
s_barrier
MM a[0:3],a[128:131],v[64:67],a[0:3]
s_add_u32 m0, s65, 16384
CV v24,v24,v25
CV v25,v26,v27
MM a[0:3],a[132:135],v[68:71],a[0:3]
RD a[192:195],v224 offset:49152
CV v26,v28,v29
MM a[8:11],a[132:135],v[76:79],a[8:11]
RD a[196:199],v225 offset:49152
CV v27,v30,v31
MM a[8:11],a[128:131],v[72:75],a[8:11]
buffer_load_dwordx4 v228, s[8:11], s64 offen offset:0 lds
MM a[16:19],a[128:131],v[80:83],a[16:19]
RD a[200:203],v226 offset:49152
CV v4,v32,v33
MM a[16:19],a[132:135],v[84:87],a[16:19]
RD a[204:207],v227 offset:49152
CV v5,v34,v35
MM a[24:27],a[132:135],v[92:95],a[24:27]
buffer_load_dwordx4 v229, s[8:11], s64 offen offset:1024 lds
MM a[24:27],a[128:131],v[88:91],a[24:27]
RD a[208:211],v224 offset:53248
RD a[212:215],v225 offset:53248
MM a[28:31],a[136:139],v[88:91],a[28:31]
buffer_load_dwordx4 v230, s[8:11], s64 offen offset:2048 lds
MM a[28:31],a[140:143],v[92:95],a[28:31]
EX v36,v36
MM a[20:23],a[140:143],v[84:87],a[20:23]
EX v37,v37
MM a[20:23],a[136:139],v[80:83],a[20:23]
RD a[216:219],v226 offset:53248
CV v6,v36,v37
MM a[12:15],a[136:139],v[72:75],a[12:15]
EX v38,v38
MM a[12:15],a[140:143],v[76:79],a[12:15]
RD a[220:223],v227 offset:53248
RD a[224:227],v224 offset:57344
MM a[4:7],a[140:143],v[68:71],a[4:7]
EX v39,v39
MM a[4:7],a[136:139],v[64:67],a[4:7]
RD a[228:231],v225 offset:57344
CV v7,v38,v39
MM a[32:35],a[144:147],v[64:67],a[32:35]
EX v40,v40
MM a[32:35],a[148:151],v[68:71],a[32:35]
buffer_load_dwordx4 v231, s[8:11], s64 offen offset:3072 lds
MM a[40:43],a[148:151],v[76:79],a[40:43]
s_add_u32 m0, s65, 81920
EX v41,v41
MM a[40:43],a[144:147],v[72:75],a[40:43]
RD a[232:235],v226 offset:57344
CV v12,v40,v41
MM a[48:51],a[144:147],v[80:83],a[48:51]
EX v42,v42
MM a[48:51],a[148:151],v[84:87],a[48:51]
EX v43,v43
MM a[56:59],a[148:151],v[92:95],a[56:59]
RD a[236:239],v227 offset:57344
CV v13,v42,v43
MM a[56:59],a[144:147],v[88:91],a[56:59]
EX v44,v44
MM a[60:63],a[152:155],v[88:91],a[60:63]
buffer_load_dwordx4 v235, s[12:15], s81 offen offset:0 lds
MM a[60:63],a[156:159],v[92:95],a[60:63]
EX v45,v45
MM a[52:55],a[156:159],v[84:87],a[52:55]
RD a[240:243],v224 offset:61440
CV v14,v44,v45
MM a[52:55],a[152:155],v[80:83],a[52:55]
EX v46,v46
MM a[44:47],a[152:155],v[72:75],a[44:47]
EX v47,v47
MM a[44:47],a[156:159],v[76:79],a[44:47]
RD a[244:247],v225 offset:61440
CV v15,v46,v47
MM a[36:39],a[156:159],v[68:71],a[36:39]
buffer_load_dwordx4 v235, s[12:15], s64 offen offset:1024 lds
MM a[36:39],a[152:155],v[64:67],a[36:39]
EX v48,v48
MM a[64:67],a[160:163],v[64:67],a[64:67]
EX v49,v49
MM a[64:67],a[164:167],v[68:71],a[64:67]
RD a[248:251],v226 offset:61440
CV v20,v48,v49
MM a[72:75],a[164:167],v[76:79],a[72:75]
EX v50,v50
MM a[72:75],a[160:163],v[72:75],a[72:75]
EX v51,v51
MM a[80:83],a[160:163],v[80:83],a[80:83]
RD a[252:255],v227 offset:61440
CV v21,v50,v51
MM a[80:83],a[164:167],v[84:87],a[80:83]
buffer_load_dwordx4 v235, s[12:15], s81 offen offset:2048 lds
MM a[88:91],a[164:167],v[92:95],a[88:91]
EX v52,v52
MM a[88:91],a[160:163],v[88:91],a[88:91]
EX v53,v53
MM a[92:95],a[168:171],v[88:91],a[92:95]
EX v54,v54
MM a[92:95],a[172:175],v[92:95],a[92:95]
buffer_load_dwordx4 v235, s[12:15], s64 offen offset:3072 lds
MM a[84:87],a[172:175],v[84:87],a[84:87]
EX v55,v55
MM a[84:87],a[168:171],v[80:83],a[84:87]
EX v56,v56
MM a[76:79],a[168:171],v[72:75],a[76:79]
EX v57,v57
MM a[76:79],a[172:175],v[76:79],a[76:79]
EX v58,v58
MM a[68:71],a[172:175],v[68:71],a[68:71]
EX v59,v59
MM a[68:71],a[168:171],v[64:67],a[68:71]
EX v60,v60
MM a[96:99],a[176:179],v[64:67],a[96:99]
EX v61,v61
MM a[96:99],a[180:183],v[68:71],a[96:99]
EX v62,v62
MM a[104:107],a[180:183],v[76:79],a[104:107]
EX v63,v63
MM a[104:107],a[176:179],v[72:75],a[104:107]
CV v22,v52,v53
CV v23,v54,v55
MM a[112:115],a[176:179],v[80:83],a[112:115]
CV v28,v56,v57
CV v29,v58,v59
MM a[112:115],a[180:183],v[84:87],a[112:115]
CV v30,v60,v61
CV v31,v62,v63
MM a[120:123],a[180:183],v[92:95],a[120:123]
MM a[120:123],a[176:179],v[88:91],a[120:123]
MM a[124:127],a[184:187],v[88:91],a[124:127]
v_add_f32 v244, v144, v145
v_add_f32 v245, v148, v149
MM a[124:127],a[188:191],v[92:95],a[124:127]
v_add_f32 v246, v152, v153
v_add_f32 v247, v156, v157
MM a[116:119],a[188:191],v[84:87],a[116:119]
MM a[116:119],a[184:187],v[80:83],a[116:119]
MM a[108:111],a[184:187],v[72:75],a[108:111]
MM a[108:111],a[188:191],v[76:79],a[108:111]
MM a[100:103],a[188:191],v[68:71],a[100:103]
MM a[100:103],a[184:187],v[64:67],a[100:103]
v_smfmac_f32_16x16x64_bf16 v[144:147], v[236:239], v[0:7], v234
v_smfmac_f32_16x16x64_bf16 v[148:151], v[236:239], v[8:15], v234
v_smfmac_f32_16x16x64_bf16 v[152:155], v[236:239], v[16:23], v234
v_smfmac_f32_16x16x64_bf16 v[156:159], v[236:239], v[24:31], v234
s_waitcnt lgkmcnt(0)
MM v[64:67],a[192:195],v[160:163],v[128:131]
s_add_u32 s64, s64, 16384
TR a[128:129],v232 offset:32768
s_add_u32 s81, s64, 896
MM v[64:67],a[196:199],v[176:179],v[64:67]
TR a[130:131],v232 offset:36864
v_max3_f32 v249, v144, v145, v148
MM v[64:67],a[200:203],v[192:195],v[64:67]
TR a[132:133],v232 offset:40960
v_max3_f32 v249, v249, v149, v152
MM v[64:67],a[204:207],v[208:211],v[64:67]
TR a[134:135],v232 offset:45056
v_max3_f32 v249, v249, v153, v156
MM v[68:71],a[220:223],v[208:211],v[128:131]
TR a[136:137],v233 offset:32768
v_max_f32 v249, v249, v157
MM v[68:71],a[216:219],v[192:195],v[68:71]
TR a[138:139],v233 offset:36864
v_cmp_lt_f32 vcc, s71, v249
MM v[68:71],a[212:215],v[176:179],v[68:71]
TR a[140:141],v233 offset:40960
TR a[142:143],v233 offset:45056
MM v[68:71],a[208:211],v[160:163],v[68:71]
s_cbranch_vccnz LSITE22_%=
LBACK22_%=:
MM v[76:79],a[208:211],v[164:167],v[132:135]
v_lshrrev_b32 v43, 7, v248
v_bfe_u32 v42, v248, 6, 1
MM v[76:79],a[212:215],v[180:183],v[76:79]
v_lshl_add_u32 v40, v42, 3, v43
v_lshlrev_b32 v40, 4, v40
MM v[76:79],a[216:219],v[196:199],v[76:79]
v_bfe_u32 v42, v248, 4, 2
v_lshl_add_u32 v40, v42, 2, v40
MM v[76:79],a[220:223],v[212:215],v[76:79]
s_lshl_b32 s76, %[swave], 11
EX v64,v64
MM v[72:75],a[204:207],v[212:215],v[132:135]
s_add_u32 s76, s76, %[slds]
EX v65,v65
MM v[72:75],a[200:203],v[196:199],v[72:75]
s_add_u32 s76, s76, 131072
v_add_u32 v40, s76, v40
CV v64,v64,v65
MM v[72:75],a[196:199],v[180:183],v[72:75]
s_waitcnt vmcnt(9)
ds_read_b32 v32, v40 offset:0
ds_read_b32 v36, v40 offset:256
MM v[72:75],a[192:195],v[164:167],v[72:75]
ds_read_b32 v33, v40 offset:512
ds_read_b32 v37, v40 offset:768
MM v[80:83],a[192:195],v[168:171],v[136:139]
ds_read_b32 v34, v40 offset:1024
s_nop 0
LPG17_%=:
s_cbranch_execz LTR3_%=
ds_read_b32 v38, v40 offset:1280
EX v66,v66
MM v[80:83],a[196:199],v[184:187],v[80:83]
ds_read_b32 v35, v40 offset:1536
ds_read_b32 v39, v40 offset:1792
EX v67,v67
MM v[80:83],a[200:203],v[200:203],v[80:83]
v_mul_u32_u24 v41, 3456, v43
v_bfe_u32 v42, v248, 4, 3
MM v[80:83],a[204:207],v[216:219],v[80:83]
TR a[144:145],v232 offset:33280
v_lshl_add_u32 v41, v42, 1, v41
EX v68,v68
MM v[84:87],a[220:223],v[216:219],v[136:139]
s_add_u32 s76, s18, 21626880
EX v69,v69
MM v[84:87],a[216:219],v[200:203],v[84:87]
TR a[146:147],v232 offset:37376
TR a[148:149],v232 offset:41472
MM v[84:87],a[212:215],v[184:187],v[84:87]
EX v70,v70
MM v[84:87],a[208:211],v[168:171],v[84:87]
TR a[150:151],v232 offset:45568
CV v65,v66,v67
MM v[92:95],a[208:211],v[172:175],v[140:143]
EX v71,v71
MM v[92:95],a[212:215],v[188:191],v[92:95]
EX v72,v72
MM v[92:95],a[216:219],v[204:207],v[92:95]
TR a[152:153],v233 offset:33280
CV v66,v68,v69
MM v[92:95],a[220:223],v[220:223],v[92:95]
EX v73,v73
MM v[88:91],a[204:207],v[220:223],v[140:143]
s_waitcnt lgkmcnt(5)
v_add_f32 v32, v32, v33
v_add_f32 v36, v36, v37
)ASM"
R"ASM(
MM v[88:91],a[200:203],v[204:207],v[88:91]
v_add_f32 v34, v34, v35
v_add_f32 v38, v38, v39
MM v[88:91],a[196:199],v[188:191],v[88:91]
TR a[154:155],v233 offset:37376
v_add_f32 v32, v32, v34
v_add_f32 v36, v36, v38
MM v[88:91],a[192:195],v[172:175],v[88:91]
CV v42,v32,v36
v_lshlrev_b32 v32, 16, v42
EX v74,v74
MM v[96:99],a[224:227],v[160:163],v[128:131]
TR a[156:157],v233 offset:41472
v_and_b32 v36, 0xffff0000, v42
v_mul_f32 v42, 0xbfb8aa3b, v32
EX v75,v75
MM v[96:99],a[228:231],v[176:179],v[96:99]
EX v42,v42
MM v[96:99],a[232:235],v[192:195],v[96:99]
TR a[158:159],v233 offset:45568
v_add_f32 v42, 1.0, v42
EX v76,v76
MM v[96:99],a[236:239],v[208:211],v[96:99]
v_rcp_f32 v42, v42
MM v[100:103],a[252:255],v[208:211],v[128:131]
TR a[160:161],v232 offset:33792
v_mul_f32 v42, v32, v42
CV v42,v42,v42
EX v77,v77
CV v67,v70,v71
MM v[100:103],a[248:251],v[192:195],v[100:103]
v_lshlrev_b32 v42, 16, v42
v_mul_f32 v32, v42, v36
MM v[100:103],a[244:247],v[176:179],v[100:103]
TR a[162:163],v232 offset:37888
CV v43,v32,v32
EX v78,v78
MM v[100:103],a[240:243],v[160:163],v[100:103]
buffer_store_short v43, v41, s[20:23], s76 offen offset:0 sc1
s_nop 1
TR a[164:165],v232 offset:41984
MM v[108:111],a[240:243],v[164:167],v[132:135]
EX v79,v79
CV v72,v72,v73
MM v[108:111],a[244:247],v[180:183],v[108:111]
TR a[166:167],v232 offset:46080
CV v73,v74,v75
MM v[108:111],a[248:251],v[196:199],v[108:111]
EX v80,v80
MM v[108:111],a[252:255],v[212:215],v[108:111]
TR a[168:169],v233 offset:33792
TR a[170:171],v233 offset:37888
EX v81,v81
MM v[104:107],a[236:239],v[212:215],v[132:135]
TR a[172:173],v233 offset:41984
CV v74,v76,v77
MM v[104:107],a[232:235],v[196:199],v[104:107]
EX v82,v82
MM v[104:107],a[228:231],v[180:183],v[104:107]
EX v83,v83
MM v[104:107],a[224:227],v[164:167],v[104:107]
TR a[174:175],v233 offset:46080
CV v75,v78,v79
MM v[112:115],a[224:227],v[168:171],v[136:139]
EX v84,v84
MM v[112:115],a[228:231],v[184:187],v[112:115]
TR a[176:177],v232 offset:34304
TR a[178:179],v232 offset:38400
MM v[112:115],a[232:235],v[200:203],v[112:115]
EX v85,v85
MM v[112:115],a[236:239],v[216:219],v[112:115]
TR a[180:181],v232 offset:42496
CV v80,v80,v81
MM v[116:119],a[252:255],v[216:219],v[136:139]
EX v86,v86
MM v[116:119],a[248:251],v[200:203],v[116:119]
TR a[182:183],v232 offset:46592
CV v81,v82,v83
MM v[116:119],a[244:247],v[184:187],v[116:119]
EX v87,v87
MM v[116:119],a[240:243],v[168:171],v[116:119]
TR a[184:185],v233 offset:34304
CV v82,v84,v85
MM v[124:127],a[240:243],v[172:175],v[140:143]
EX v88,v88
MM v[124:127],a[244:247],v[188:191],v[124:127]
TR a[186:187],v233 offset:38400
TR a[188:189],v233 offset:42496
MM v[124:127],a[248:251],v[204:207],v[124:127]
TR a[190:191],v233 offset:46592
CV v83,v86,v87
EX v89,v89
MM v[124:127],a[252:255],v[220:223],v[124:127]
EX v90,v90
MM v[120:123],a[236:239],v[220:223],v[140:143]
EX v91,v91
MM v[120:123],a[232:235],v[204:207],v[120:123]
EX v92,v92
MM v[120:123],a[228:231],v[188:191],v[120:123]
EX v93,v93
MM v[120:123],a[224:227],v[172:175],v[120:123]
s_waitcnt lgkmcnt(0)
s_barrier
MM a[0:3],a[128:131],v[0:3],a[0:3]
s_add_u32 m0, s65, 32768
CV v88,v88,v89
CV v89,v90,v91
MM a[0:3],a[132:135],v[4:7],a[0:3]
EX v94,v94
MM a[8:11],a[132:135],v[12:15],a[8:11]
RD a[192:195],v224 offset:0
CV v90,v92,v93
MM a[8:11],a[128:131],v[8:11],a[8:11]
EX v95,v95
MM a[16:19],a[128:131],v[16:19],a[16:19]
RD a[196:199],v225 offset:0
CV v91,v94,v95
MM a[16:19],a[132:135],v[20:23],a[16:19]
EX v96,v96
MM a[24:27],a[132:135],v[28:31],a[24:27]
buffer_load_dwordx4 v228, s[8:11], s64 offen offset:0 lds
MM a[24:27],a[128:131],v[24:27],a[24:27]
EX v97,v97
MM a[28:31],a[136:139],v[24:27],a[28:31]
EX v98,v98
MM a[28:31],a[140:143],v[28:31],a[28:31]
RD a[200:203],v226 offset:0
CV v68,v96,v97
MM a[20:23],a[140:143],v[20:23],a[20:23]
EX v99,v99
MM a[20:23],a[136:139],v[16:19],a[20:23]
RD a[204:207],v227 offset:0
CV v69,v98,v99
MM a[12:15],a[136:139],v[8:11],a[12:15]
EX v100,v100
MM a[12:15],a[140:143],v[12:15],a[12:15]
buffer_load_dwordx4 v229, s[8:11], s64 offen offset:1024 lds
MM a[4:7],a[140:143],v[4:7],a[4:7]
EX v101,v101
MM a[4:7],a[136:139],v[0:3],a[4:7]
RD a[208:211],v224 offset:4096
CV v70,v100,v101
MM a[32:35],a[144:147],v[0:3],a[32:35]
EX v102,v102
MM a[32:35],a[148:151],v[4:7],a[32:35]
EX v103,v103
MM a[40:43],a[148:151],v[12:15],a[40:43]
RD a[212:215],v225 offset:4096
CV v71,v102,v103
MM a[40:43],a[144:147],v[8:11],a[40:43]
EX v104,v104
MM a[48:51],a[144:147],v[16:19],a[48:51]
buffer_load_dwordx4 v230, s[8:11], s64 offen offset:2048 lds
MM a[48:51],a[148:151],v[20:23],a[48:51]
EX v105,v105
MM a[56:59],a[148:151],v[28:31],a[56:59]
RD a[216:219],v226 offset:4096
CV v76,v104,v105
MM a[56:59],a[144:147],v[24:27],a[56:59]
EX v106,v106
MM a[60:63],a[152:155],v[24:27],a[60:63]
EX v107,v107
MM a[60:63],a[156:159],v[28:31],a[60:63]
RD a[220:223],v227 offset:4096
CV v77,v106,v107
MM a[52:55],a[156:159],v[20:23],a[52:55]
EX v108,v108
MM a[52:55],a[152:155],v[16:19],a[52:55]
buffer_load_dwordx4 v231, s[8:11], s64 offen offset:3072 lds
MM a[44:47],a[152:155],v[8:11],a[44:47]
s_add_u32 m0, s65, 98304
EX v109,v109
MM a[44:47],a[156:159],v[12:15],a[44:47]
RD a[224:227],v224 offset:8192
CV v78,v108,v109
MM a[36:39],a[156:159],v[4:7],a[36:39]
EX v110,v110
MM a[36:39],a[152:155],v[0:3],a[36:39]
EX v111,v111
MM a[64:67],a[160:163],v[0:3],a[64:67]
RD a[228:231],v225 offset:8192
CV v79,v110,v111
MM a[64:67],a[164:167],v[4:7],a[64:67]
EX v112,v112
MM a[72:75],a[164:167],v[12:15],a[72:75]
buffer_load_dwordx4 v235, s[12:15], s81 offen offset:0 lds
MM a[72:75],a[160:163],v[8:11],a[72:75]
EX v113,v113
MM a[80:83],a[160:163],v[16:19],a[80:83]
RD a[232:235],v226 offset:8192
CV v84,v112,v113
MM a[80:83],a[164:167],v[20:23],a[80:83]
EX v114,v114
MM a[88:91],a[164:167],v[28:31],a[88:91]
EX v115,v115
MM a[88:91],a[160:163],v[24:27],a[88:91]
RD a[236:239],v227 offset:8192
CV v85,v114,v115
MM a[92:95],a[168:171],v[24:27],a[92:95]
EX v116,v116
MM a[92:95],a[172:175],v[28:31],a[92:95]
buffer_load_dwordx4 v235, s[12:15], s64 offen offset:1024 lds
MM a[84:87],a[172:175],v[20:23],a[84:87]
EX v117,v117
MM a[84:87],a[168:171],v[16:19],a[84:87]
RD a[240:243],v224 offset:12288
CV v86,v116,v117
MM a[76:79],a[168:171],v[8:11],a[76:79]
EX v118,v118
MM a[76:79],a[172:175],v[12:15],a[76:79]
EX v119,v119
MM a[68:71],a[172:175],v[4:7],a[68:71]
RD a[244:247],v225 offset:12288
CV v87,v118,v119
MM a[68:71],a[168:171],v[0:3],a[68:71]
buffer_load_dwordx4 v235, s[12:15], s81 offen offset:2048 lds
MM a[96:99],a[176:179],v[0:3],a[96:99]
EX v120,v120
MM a[96:99],a[180:183],v[4:7],a[96:99]
EX v121,v121
MM a[104:107],a[180:183],v[12:15],a[104:107]
RD a[248:251],v226 offset:12288
CV v92,v120,v121
MM a[104:107],a[176:179],v[8:11],a[104:107]
EX v122,v122
MM a[112:115],a[176:179],v[16:19],a[112:115]
EX v123,v123
MM a[112:115],a[180:183],v[20:23],a[112:115]
RD a[252:255],v227 offset:12288
CV v93,v122,v123
MM a[120:123],a[180:183],v[28:31],a[120:123]
buffer_load_dwordx4 v235, s[12:15], s64 offen offset:3072 lds
MM a[120:123],a[176:179],v[24:27],a[120:123]
EX v124,v124
MM a[124:127],a[184:187],v[24:27],a[124:127]
v_add_f32 v244, v144, v145
v_add_f32 v245, v148, v149
MM a[124:127],a[188:191],v[28:31],a[124:127]
v_add_f32 v246, v152, v153
v_add_f32 v247, v156, v157
MM a[116:119],a[188:191],v[20:23],a[116:119]
EX v125,v125
MM a[116:119],a[184:187],v[16:19],a[116:119]
EX v126,v126
MM a[108:111],a[184:187],v[8:11],a[108:111]
EX v127,v127
MM a[108:111],a[188:191],v[12:15],a[108:111]
CV v94,v124,v125
CV v95,v126,v127
MM a[100:103],a[188:191],v[4:7],a[100:103]
MM a[100:103],a[184:187],v[0:3],a[100:103]
v_smfmac_f32_16x16x64_bf16 v[144:147], v[236:239], v[64:71], v234
v_smfmac_f32_16x16x64_bf16 v[148:151], v[236:239], v[72:79], v234
v_smfmac_f32_16x16x64_bf16 v[152:155], v[236:239], v[80:87], v234
v_smfmac_f32_16x16x64_bf16 v[156:159], v[236:239], v[88:95], v234
s_waitcnt lgkmcnt(0)
MM v[0:3],a[192:195],v[160:163],v[128:131]
s_add_u32 s64, s64, 16384
TR a[128:129],v232 offset:49152
s_add_u32 s81, s64, 896
MM v[0:3],a[196:199],v[176:179],v[0:3]
TR a[130:131],v232 offset:53248
v_max3_f32 v249, v144, v145, v148
MM v[0:3],a[200:203],v[192:195],v[0:3]
TR a[132:133],v232 offset:57344
v_max3_f32 v249, v249, v149, v152
MM v[0:3],a[204:207],v[208:211],v[0:3]
TR a[134:135],v232 offset:61440
v_max3_f32 v249, v249, v153, v156
MM v[4:7],a[220:223],v[208:211],v[128:131]
TR a[136:137],v233 offset:49152
v_max_f32 v249, v249, v157
MM v[4:7],a[216:219],v[192:195],v[4:7]
TR a[138:139],v233 offset:53248
v_cmp_lt_f32 vcc, s71, v249
MM v[4:7],a[212:215],v[176:179],v[4:7]
TR a[140:141],v233 offset:57344
TR a[142:143],v233 offset:61440
MM v[4:7],a[208:211],v[160:163],v[4:7]
s_cbranch_vccnz LSITE23_%=
LBACK23_%=:
MM v[12:15],a[208:211],v[164:167],v[132:135]
EX v0,v0
MM v[12:15],a[212:215],v[180:183],v[12:15]
EX v1,v1
MM v[12:15],a[216:219],v[196:199],v[12:15]
TR a[144:145],v232 offset:49664
CV v0,v0,v1
MM v[12:15],a[220:223],v[212:215],v[12:15]
EX v2,v2
MM v[8:11],a[204:207],v[212:215],v[132:135]
EX v3,v3
MM v[8:11],a[200:203],v[196:199],v[8:11]
TR a[146:147],v232 offset:53760
CV v1,v2,v3
MM v[8:11],a[196:199],v[180:183],v[8:11]
EX v4,v4
MM v[8:11],a[192:195],v[164:167],v[8:11]
TR a[148:149],v232 offset:57856
TR a[150:151],v232 offset:61952
MM v[16:19],a[192:195],v[168:171],v[136:139]
EX v5,v5
MM v[16:19],a[196:199],v[184:187],v[16:19]
TR a[152:153],v233 offset:49664
CV v2,v4,v5
MM v[16:19],a[200:203],v[200:203],v[16:19]
EX v6,v6
MM v[16:19],a[204:207],v[216:219],v[16:19]
TR a[154:155],v233 offset:53760
TR a[156:157],v233 offset:57856
MM v[20:23],a[220:223],v[216:219],v[136:139]
EX v7,v7
MM v[20:23],a[216:219],v[200:203],v[20:23]
TR a[158:159],v233 offset:61952
CV v3,v6,v7
MM v[20:23],a[212:215],v[184:187],v[20:23]
EX v8,v8
MM v[20:23],a[208:211],v[168:171],v[20:23]
EX v9,v9
MM v[28:31],a[208:211],v[172:175],v[140:143]
TR a[160:161],v232 offset:50176
CV v8,v8,v9
MM v[28:31],a[212:215],v[188:191],v[28:31]
EX v10,v10
MM v[28:31],a[216:219],v[204:207],v[28:31]
TR a[162:163],v232 offset:54272
TR a[164:165],v232 offset:58368
MM v[28:31],a[220:223],v[220:223],v[28:31]
EX v11,v11
MM v[24:27],a[204:207],v[220:223],v[140:143]
TR a[166:167],v232 offset:62464
CV v9,v10,v11
MM v[24:27],a[200:203],v[204:207],v[24:27]
EX v12,v12
MM v[24:27],a[196:199],v[188:191],v[24:27]
EX v13,v13
MM v[24:27],a[192:195],v[172:175],v[24:27]
TR a[168:169],v233 offset:50176
CV v10,v12,v13
MM v[32:35],a[224:227],v[160:163],v[128:131]
EX v14,v14
MM v[32:35],a[228:231],v[176:179],v[32:35]
TR a[170:171],v233 offset:54272
TR a[172:173],v233 offset:58368
MM v[32:35],a[232:235],v[192:195],v[32:35]
EX v15,v15
MM v[32:35],a[236:239],v[208:211],v[32:35]
TR a[174:175],v233 offset:62464
CV v11,v14,v15
MM v[36:39],a[252:255],v[208:211],v[128:131]
EX v16,v16
MM v[36:39],a[248:251],v[192:195],v[36:39]
EX v17,v17
MM v[36:39],a[244:247],v[176:179],v[36:39]
TR a[176:177],v232 offset:50688
CV v16,v16,v17
MM v[36:39],a[240:243],v[160:163],v[36:39]
EX v18,v18
MM v[44:47],a[240:243],v[164:167],v[132:135]
TR a[178:179],v232 offset:54784
TR a[180:181],v232 offset:58880
MM v[44:47],a[244:247],v[180:183],v[44:47]
EX v19,v19
MM v[44:47],a[248:251],v[196:199],v[44:47]
TR a[182:183],v232 offset:62976
CV v17,v18,v19
MM v[44:47],a[252:255],v[212:215],v[44:47]
EX v20,v20
MM v[40:43],a[236:239],v[212:215],v[132:135]
TR a[184:185],v233 offset:50688
TR a[186:187],v233 offset:54784
MM v[40:43],a[232:235],v[196:199],v[40:43]
EX v21,v21
MM v[40:43],a[228:231],v[180:183],v[40:43]
TR a[188:189],v233 offset:58880
CV v18,v20,v21
MM v[40:43],a[224:227],v[164:167],v[40:43]
EX v22,v22
MM v[48:51],a[224:227],v[168:171],v[136:139]
EX v23,v23
MM v[48:51],a[228:231],v[184:187],v[48:51]
TR a[190:191],v233 offset:62976
CV v19,v22,v23
MM v[48:51],a[232:235],v[200:203],v[48:51]
EX v24,v24
MM v[48:51],a[236:239],v[216:219],v[48:51]
EX v25,v25
MM v[52:55],a[252:255],v[216:219],v[136:139]
EX v26,v26
MM v[52:55],a[248:251],v[200:203],v[52:55]
EX v27,v27
MM v[52:55],a[244:247],v[184:187],v[52:55]
EX v28,v28
MM v[52:55],a[240:243],v[168:171],v[52:55]
EX v29,v29
MM v[60:63],a[240:243],v[172:175],v[140:143]
)ASM"
R"ASM(
EX v30,v30
MM v[60:63],a[244:247],v[188:191],v[60:63]
EX v31,v31
MM v[60:63],a[248:251],v[204:207],v[60:63]
EX v32,v32
MM v[60:63],a[252:255],v[220:223],v[60:63]
EX v33,v33
MM v[56:59],a[236:239],v[220:223],v[140:143]
EX v34,v34
MM v[56:59],a[232:235],v[204:207],v[56:59]
EX v35,v35
MM v[56:59],a[228:231],v[188:191],v[56:59]
EX v36,v36
MM v[56:59],a[224:227],v[172:175],v[56:59]
s_waitcnt vmcnt(13) lgkmcnt(0)
s_barrier
MM a[0:3],a[128:131],v[64:67],a[0:3]
s_add_u32 m0, s65, 49152
CV v24,v24,v25
CV v25,v26,v27
MM a[0:3],a[132:135],v[68:71],a[0:3]
RD a[192:195],v224 offset:16384
CV v26,v28,v29
MM a[8:11],a[132:135],v[76:79],a[8:11]
RD a[196:199],v225 offset:16384
CV v27,v30,v31
MM a[8:11],a[128:131],v[72:75],a[8:11]
buffer_load_dwordx4 v228, s[8:11], s64 offen offset:0 lds
MM a[16:19],a[128:131],v[80:83],a[16:19]
RD a[200:203],v226 offset:16384
CV v4,v32,v33
MM a[16:19],a[132:135],v[84:87],a[16:19]
RD a[204:207],v227 offset:16384
CV v5,v34,v35
MM a[24:27],a[132:135],v[92:95],a[24:27]
buffer_load_dwordx4 v229, s[8:11], s64 offen offset:1024 lds
MM a[24:27],a[128:131],v[88:91],a[24:27]
RD a[208:211],v224 offset:20480
RD a[212:215],v225 offset:20480
MM a[28:31],a[136:139],v[88:91],a[28:31]
buffer_load_dwordx4 v230, s[8:11], s64 offen offset:2048 lds
MM a[28:31],a[140:143],v[92:95],a[28:31]
RD a[216:219],v226 offset:20480
RD a[220:223],v227 offset:20480
MM a[20:23],a[140:143],v[84:87],a[20:23]
EX v37,v37
MM a[20:23],a[136:139],v[80:83],a[20:23]
RD a[224:227],v224 offset:24576
CV v6,v36,v37
MM a[12:15],a[136:139],v[72:75],a[12:15]
EX v38,v38
MM a[12:15],a[140:143],v[76:79],a[12:15]
buffer_load_dwordx4 v231, s[8:11], s64 offen offset:3072 lds
MM a[4:7],a[140:143],v[68:71],a[4:7]
s_add_u32 m0, s65, 114688
EX v39,v39
MM a[4:7],a[136:139],v[64:67],a[4:7]
RD a[228:231],v225 offset:24576
CV v7,v38,v39
MM a[32:35],a[144:147],v[64:67],a[32:35]
EX v40,v40
MM a[32:35],a[148:151],v[68:71],a[32:35]
EX v41,v41
MM a[40:43],a[148:151],v[76:79],a[40:43]
RD a[232:235],v226 offset:24576
CV v12,v40,v41
MM a[40:43],a[144:147],v[72:75],a[40:43]
EX v42,v42
MM a[48:51],a[144:147],v[80:83],a[48:51]
buffer_load_dwordx4 v235, s[12:15], s81 offen offset:0 lds
MM a[48:51],a[148:151],v[84:87],a[48:51]
EX v43,v43
MM a[56:59],a[148:151],v[92:95],a[56:59]
RD a[236:239],v227 offset:24576
CV v13,v42,v43
MM a[56:59],a[144:147],v[88:91],a[56:59]
EX v44,v44
MM a[60:63],a[152:155],v[88:91],a[60:63]
EX v45,v45
MM a[60:63],a[156:159],v[92:95],a[60:63]
RD a[240:243],v224 offset:28672
CV v14,v44,v45
MM a[52:55],a[156:159],v[84:87],a[52:55]
EX v46,v46
MM a[52:55],a[152:155],v[80:83],a[52:55]
buffer_load_dwordx4 v235, s[12:15], s64 offen offset:1024 lds
MM a[44:47],a[152:155],v[72:75],a[44:47]
EX v47,v47
MM a[44:47],a[156:159],v[76:79],a[44:47]
RD a[244:247],v225 offset:28672
CV v15,v46,v47
MM a[36:39],a[156:159],v[68:71],a[36:39]
EX v48,v48
MM a[36:39],a[152:155],v[64:67],a[36:39]
EX v49,v49
MM a[64:67],a[160:163],v[64:67],a[64:67]
RD a[248:251],v226 offset:28672
CV v20,v48,v49
MM a[64:67],a[164:167],v[68:71],a[64:67]
EX v50,v50
MM a[72:75],a[164:167],v[76:79],a[72:75]
buffer_load_dwordx4 v235, s[12:15], s81 offen offset:2048 lds
MM a[72:75],a[160:163],v[72:75],a[72:75]
EX v51,v51
MM a[80:83],a[160:163],v[80:83],a[80:83]
RD a[252:255],v227 offset:28672
CV v21,v50,v51
MM a[80:83],a[164:167],v[84:87],a[80:83]
EX v52,v52
MM a[88:91],a[164:167],v[92:95],a[88:91]
EX v53,v53
MM a[88:91],a[160:163],v[88:91],a[88:91]
buffer_load_dwordx4 v235, s[12:15], s64 offen offset:3072 lds
MM a[92:95],a[168:171],v[88:91],a[92:95]
EX v54,v54
MM a[92:95],a[172:175],v[92:95],a[92:95]
EX v55,v55
MM a[84:87],a[172:175],v[84:87],a[84:87]
EX v56,v56
MM a[84:87],a[168:171],v[80:83],a[84:87]
EX v57,v57
MM a[76:79],a[168:171],v[72:75],a[76:79]
EX v58,v58
MM a[76:79],a[172:175],v[76:79],a[76:79]
EX v59,v59
MM a[68:71],a[172:175],v[68:71],a[68:71]
EX v60,v60
MM a[68:71],a[168:171],v[64:67],a[68:71]
EX v61,v61
MM a[96:99],a[176:179],v[64:67],a[96:99]
EX v62,v62
MM a[96:99],a[180:183],v[68:71],a[96:99]
EX v63,v63
MM a[104:107],a[180:183],v[76:79],a[104:107]
CV v22,v52,v53
CV v23,v54,v55
MM a[104:107],a[176:179],v[72:75],a[104:107]
CV v28,v56,v57
CV v29,v58,v59
MM a[112:115],a[176:179],v[80:83],a[112:115]
CV v30,v60,v61
CV v31,v62,v63
MM a[112:115],a[180:183],v[84:87],a[112:115]
MM a[120:123],a[180:183],v[92:95],a[120:123]
s_lshr_b32 s91, s18, 2
s_add_u32 s91, s91, 69632
s_waitcnt vmcnt(16)
MM a[120:123],a[176:179],v[88:91],a[120:123]
v_mov_b32 v250, s69
s_mov_b64 exec, 1
buffer_store_dword v250, off, s[20:23], s91 offset:0 sc1
s_mov_b64 exec, -1
MM a[124:127],a[184:187],v[88:91],a[124:127]
v_add_f32 v244, v144, v145
v_add_f32 v245, v148, v149
MM a[124:127],a[188:191],v[92:95],a[124:127]
v_add_f32 v246, v152, v153
v_add_f32 v247, v156, v157
v_lshlrev_b32 v96, 3, v248
MM a[116:119],a[188:191],v[84:87],a[116:119]
v_min_u32 v96, 4992, v96
LPG18_%=:
s_cbranch_execz LTR3_%=
s_sub_u32 s91, 5242880, s26
MM a[116:119],a[184:187],v[80:83],a[116:119]
s_sub_u32 s60, s24, s91
s_subb_u32 s61, s25, 0
s_mov_b32 s62, 5242880
MM a[108:111],a[184:187],v[72:75],a[108:111]
s_mov_b32 s63, 131072
s_lshl_b32 s90, s16, 2
s_add_u32 s90, s90, %[swave]
MM a[108:111],a[188:191],v[76:79],a[108:111]
s_mul_i32 s90, s90, 10240
s_add_u32 s90, s90, 0
s_add_u32 s91, %[slds], 135168
MM a[100:103],a[188:191],v[68:71],a[100:103]
s_mov_b32 m0, s91
s_nop 0
buffer_load_dword v96, s[60:63], s90 offen offset:0 lds
MM a[100:103],a[184:187],v[64:67],a[100:103]
s_lshr_b32 s77, %[sb], 3
s_lshl_b32 s77, s77, 2
s_add_u32 s77, s77, %[swave]
v_smfmac_f32_16x16x64_bf16 v[144:147], v[236:239], v[0:7], v234
s_lshl_b32 s77, s77, 9
s_add_u32 s77, s77, 0
v_smfmac_f32_16x16x64_bf16 v[148:151], v[236:239], v[8:15], v234
v_lshrrev_b32 v253, 1, v248
v_smfmac_f32_16x16x64_bf16 v[152:155], v[236:239], v[16:23], v234
v_add_u32 v253, s77, v253
v_smfmac_f32_16x16x64_bf16 v[156:159], v[236:239], v[24:31], v234
s_waitcnt lgkmcnt(0)
s_mov_b32 s77, LKEND_ka-LPD1_%=-16
v_min_u32 v253, s77, v253
s_getpc_b64 s[98:99]
LPD1_%=:
global_load_dword v253, v253, s[98:99]
MM v[64:67],a[192:195],v[160:163],v[128:131]
s_add_u32 s64, s64, 16384
TR a[128:129],v232 offset:0
s_add_u32 s81, s64, 896
MM v[64:67],a[196:199],v[176:179],v[64:67]
TR a[130:131],v232 offset:4096
v_max3_f32 v249, v144, v145, v148
MM v[64:67],a[200:203],v[192:195],v[64:67]
TR a[132:133],v232 offset:8192
v_max3_f32 v249, v249, v149, v152
MM v[64:67],a[204:207],v[208:211],v[64:67]
TR a[134:135],v232 offset:12288
v_max3_f32 v249, v249, v153, v156
MM v[68:71],a[220:223],v[208:211],v[128:131]
TR a[136:137],v233 offset:0
v_max_f32 v249, v249, v157
MM v[68:71],a[216:219],v[192:195],v[68:71]
TR a[138:139],v233 offset:4096
v_cmp_lt_f32 vcc, s71, v249
MM v[68:71],a[212:215],v[176:179],v[68:71]
TR a[140:141],v233 offset:8192
TR a[142:143],v233 offset:12288
MM v[68:71],a[208:211],v[160:163],v[68:71]
s_cbranch_vccnz LSITE24_%=
LBACK24_%=:
MM v[76:79],a[208:211],v[164:167],v[132:135]
EX v64,v64
MM v[76:79],a[212:215],v[180:183],v[76:79]
EX v65,v65
MM v[76:79],a[216:219],v[196:199],v[76:79]
TR a[144:145],v232 offset:512
CV v64,v64,v65
MM v[76:79],a[220:223],v[212:215],v[76:79]
EX v66,v66
MM v[72:75],a[204:207],v[212:215],v[132:135]
EX v67,v67
MM v[72:75],a[200:203],v[196:199],v[72:75]
TR a[146:147],v232 offset:4608
CV v65,v66,v67
MM v[72:75],a[196:199],v[180:183],v[72:75]
EX v68,v68
MM v[72:75],a[192:195],v[164:167],v[72:75]
TR a[148:149],v232 offset:8704
TR a[150:151],v232 offset:12800
MM v[80:83],a[192:195],v[168:171],v[136:139]
EX v69,v69
MM v[80:83],a[196:199],v[184:187],v[80:83]
TR a[152:153],v233 offset:512
CV v66,v68,v69
MM v[80:83],a[200:203],v[200:203],v[80:83]
EX v70,v70
MM v[80:83],a[204:207],v[216:219],v[80:83]
TR a[154:155],v233 offset:4608
TR a[156:157],v233 offset:8704
MM v[84:87],a[220:223],v[216:219],v[136:139]
s_add_u32 s87, s95, 4096
buffer_store_dword v243, v248, s[20:23], s87 offen offset:12 sc1
MM v[84:87],a[216:219],v[200:203],v[84:87]
EX v71,v71
MM v[84:87],a[212:215],v[184:187],v[84:87]
TR a[158:159],v233 offset:12800
CV v67,v70,v71
MM v[84:87],a[208:211],v[168:171],v[84:87]
EX v72,v72
MM v[92:95],a[208:211],v[172:175],v[140:143]
EX v73,v73
MM v[92:95],a[212:215],v[188:191],v[92:95]
TR a[160:161],v232 offset:1024
CV v72,v72,v73
MM v[92:95],a[216:219],v[204:207],v[92:95]
EX v74,v74
MM v[92:95],a[220:223],v[220:223],v[92:95]
EX v75,v75
MM v[88:91],a[204:207],v[220:223],v[140:143]
TR a[162:163],v232 offset:5120
CV v73,v74,v75
MM v[88:91],a[200:203],v[204:207],v[88:91]
TR a[164:165],v232 offset:9216
TR a[166:167],v232 offset:13312
MM v[88:91],a[196:199],v[188:191],v[88:91]
EX v76,v76
MM v[88:91],a[192:195],v[172:175],v[88:91]
EX v77,v77
MM v[96:99],a[224:227],v[160:163],v[128:131]
TR a[168:169],v233 offset:1024
CV v74,v76,v77
MM v[96:99],a[228:231],v[176:179],v[96:99]
EX v78,v78
MM v[96:99],a[232:235],v[192:195],v[96:99]
TR a[170:171],v233 offset:5120
TR a[172:173],v233 offset:9216
MM v[96:99],a[236:239],v[208:211],v[96:99]
EX v79,v79
MM v[100:103],a[252:255],v[208:211],v[128:131]
TR a[174:175],v233 offset:13312
CV v75,v78,v79
MM v[100:103],a[248:251],v[192:195],v[100:103]
EX v80,v80
MM v[100:103],a[244:247],v[176:179],v[100:103]
EX v81,v81
MM v[100:103],a[240:243],v[160:163],v[100:103]
TR a[176:177],v232 offset:1536
CV v80,v80,v81
MM v[108:111],a[240:243],v[164:167],v[132:135]
EX v82,v82
MM v[108:111],a[244:247],v[180:183],v[108:111]
TR a[178:179],v232 offset:5632
TR a[180:181],v232 offset:9728
MM v[108:111],a[248:251],v[196:199],v[108:111]
EX v83,v83
MM v[108:111],a[252:255],v[212:215],v[108:111]
TR a[182:183],v232 offset:13824
CV v81,v82,v83
MM v[104:107],a[236:239],v[212:215],v[132:135]
EX v84,v84
MM v[104:107],a[232:235],v[196:199],v[104:107]
EX v85,v85
MM v[104:107],a[228:231],v[180:183],v[104:107]
TR a[184:185],v233 offset:1536
CV v82,v84,v85
MM v[104:107],a[224:227],v[164:167],v[104:107]
EX v86,v86
MM v[112:115],a[224:227],v[168:171],v[136:139]
TR a[186:187],v233 offset:5632
TR a[188:189],v233 offset:9728
MM v[112:115],a[228:231],v[184:187],v[112:115]
EX v87,v87
MM v[112:115],a[232:235],v[200:203],v[112:115]
TR a[190:191],v233 offset:13824
CV v83,v86,v87
MM v[112:115],a[236:239],v[216:219],v[112:115]
EX v88,v88
MM v[116:119],a[252:255],v[216:219],v[136:139]
EX v89,v89
MM v[116:119],a[248:251],v[200:203],v[116:119]
EX v90,v90
MM v[116:119],a[244:247],v[184:187],v[116:119]
EX v91,v91
MM v[116:119],a[240:243],v[168:171],v[116:119]
EX v92,v92
MM v[124:127],a[240:243],v[172:175],v[140:143]
EX v93,v93
MM v[124:127],a[244:247],v[188:191],v[124:127]
EX v94,v94
MM v[124:127],a[248:251],v[204:207],v[124:127]
EX v95,v95
MM v[124:127],a[252:255],v[220:223],v[124:127]
EX v96,v96
MM v[120:123],a[236:239],v[220:223],v[140:143]
EX v97,v97
MM v[120:123],a[232:235],v[204:207],v[120:123]
EX v98,v98
MM v[120:123],a[228:231],v[188:191],v[120:123]
EX v99,v99
MM v[120:123],a[224:227],v[172:175],v[120:123]
s_waitcnt vmcnt(16) lgkmcnt(0)
s_barrier
MM a[0:3],a[128:131],v[0:3],a[0:3]
s_add_u32 m0, s65, 0
CV v88,v88,v89
CV v89,v90,v91
MM a[0:3],a[132:135],v[4:7],a[0:3]
RD a[192:195],v224 offset:32768
CV v90,v92,v93
MM a[8:11],a[132:135],v[12:15],a[8:11]
RD a[196:199],v225 offset:32768
CV v91,v94,v95
MM a[8:11],a[128:131],v[8:11],a[8:11]
buffer_load_dwordx4 v228, s[8:11], s64 offen offset:0 lds
MM a[16:19],a[128:131],v[16:19],a[16:19]
RD a[200:203],v226 offset:32768
CV v68,v96,v97
MM a[16:19],a[132:135],v[20:23],a[16:19]
RD a[204:207],v227 offset:32768
CV v69,v98,v99
MM a[24:27],a[132:135],v[28:31],a[24:27]
buffer_load_dwordx4 v229, s[8:11], s64 offen offset:1024 lds
MM a[24:27],a[128:131],v[24:27],a[24:27]
RD a[208:211],v224 offset:36864
RD a[212:215],v225 offset:36864
MM a[28:31],a[136:139],v[24:27],a[28:31]
buffer_load_dwordx4 v230, s[8:11], s64 offen offset:2048 lds
MM a[28:31],a[140:143],v[28:31],a[28:31]
EX v100,v100
MM a[20:23],a[140:143],v[20:23],a[20:23]
EX v101,v101
MM a[20:23],a[136:139],v[16:19],a[20:23]
RD a[216:219],v226 offset:36864
CV v70,v100,v101
MM a[12:15],a[136:139],v[8:11],a[12:15]
EX v102,v102
MM a[12:15],a[140:143],v[12:15],a[12:15]
RD a[220:223],v227 offset:36864
)ASM"
R"ASM(
RD a[224:227],v224 offset:40960
MM a[4:7],a[140:143],v[4:7],a[4:7]
EX v103,v103
MM a[4:7],a[136:139],v[0:3],a[4:7]
RD a[228:231],v225 offset:40960
CV v71,v102,v103
MM a[32:35],a[144:147],v[0:3],a[32:35]
EX v104,v104
MM a[32:35],a[148:151],v[4:7],a[32:35]
buffer_load_dwordx4 v231, s[8:11], s64 offen offset:3072 lds
MM a[40:43],a[148:151],v[12:15],a[40:43]
s_add_u32 m0, s65, 65536
EX v105,v105
MM a[40:43],a[144:147],v[8:11],a[40:43]
RD a[232:235],v226 offset:40960
CV v76,v104,v105
MM a[48:51],a[144:147],v[16:19],a[48:51]
EX v106,v106
MM a[48:51],a[148:151],v[20:23],a[48:51]
EX v107,v107
MM a[56:59],a[148:151],v[28:31],a[56:59]
RD a[236:239],v227 offset:40960
CV v77,v106,v107
MM a[56:59],a[144:147],v[24:27],a[56:59]
EX v108,v108
MM a[60:63],a[152:155],v[24:27],a[60:63]
buffer_load_dwordx4 v235, s[12:15], s81 offen offset:0 lds
MM a[60:63],a[156:159],v[28:31],a[60:63]
EX v109,v109
MM a[52:55],a[156:159],v[20:23],a[52:55]
RD a[240:243],v224 offset:45056
CV v78,v108,v109
MM a[52:55],a[152:155],v[16:19],a[52:55]
EX v110,v110
MM a[44:47],a[152:155],v[8:11],a[44:47]
EX v111,v111
MM a[44:47],a[156:159],v[12:15],a[44:47]
RD a[244:247],v225 offset:45056
CV v79,v110,v111
MM a[36:39],a[156:159],v[4:7],a[36:39]
buffer_load_dwordx4 v235, s[12:15], s64 offen offset:1024 lds
MM a[36:39],a[152:155],v[0:3],a[36:39]
EX v112,v112
MM a[64:67],a[160:163],v[0:3],a[64:67]
EX v113,v113
MM a[64:67],a[164:167],v[4:7],a[64:67]
RD a[248:251],v226 offset:45056
CV v84,v112,v113
MM a[72:75],a[164:167],v[12:15],a[72:75]
EX v114,v114
MM a[72:75],a[160:163],v[8:11],a[72:75]
EX v115,v115
MM a[80:83],a[160:163],v[16:19],a[80:83]
RD a[252:255],v227 offset:45056
CV v85,v114,v115
MM a[80:83],a[164:167],v[20:23],a[80:83]
buffer_load_dwordx4 v235, s[12:15], s81 offen offset:2048 lds
MM a[88:91],a[164:167],v[28:31],a[88:91]
EX v116,v116
MM a[88:91],a[160:163],v[24:27],a[88:91]
EX v117,v117
MM a[92:95],a[168:171],v[24:27],a[92:95]
EX v118,v118
MM a[92:95],a[172:175],v[28:31],a[92:95]
buffer_load_dwordx4 v235, s[12:15], s64 offen offset:3072 lds
MM a[84:87],a[172:175],v[20:23],a[84:87]
EX v119,v119
MM a[84:87],a[168:171],v[16:19],a[84:87]
EX v120,v120
MM a[76:79],a[168:171],v[8:11],a[76:79]
EX v121,v121
MM a[76:79],a[172:175],v[12:15],a[76:79]
EX v122,v122
MM a[68:71],a[172:175],v[4:7],a[68:71]
EX v123,v123
MM a[68:71],a[168:171],v[0:3],a[68:71]
EX v124,v124
MM a[96:99],a[176:179],v[0:3],a[96:99]
EX v125,v125
MM a[96:99],a[180:183],v[4:7],a[96:99]
EX v126,v126
MM a[104:107],a[180:183],v[12:15],a[104:107]
EX v127,v127
MM a[104:107],a[176:179],v[8:11],a[104:107]
CV v86,v116,v117
CV v87,v118,v119
MM a[112:115],a[176:179],v[16:19],a[112:115]
CV v92,v120,v121
CV v93,v122,v123
MM a[112:115],a[180:183],v[20:23],a[112:115]
CV v94,v124,v125
CV v95,v126,v127
MM a[120:123],a[180:183],v[28:31],a[120:123]
MM a[120:123],a[176:179],v[24:27],a[120:123]
MM a[124:127],a[184:187],v[24:27],a[124:127]
v_add_f32 v244, v144, v145
v_add_f32 v245, v148, v149
MM a[124:127],a[188:191],v[28:31],a[124:127]
v_add_f32 v246, v152, v153
v_add_f32 v247, v156, v157
MM a[116:119],a[188:191],v[20:23],a[116:119]
MM a[116:119],a[184:187],v[16:19],a[116:119]
MM a[108:111],a[184:187],v[8:11],a[108:111]
MM a[108:111],a[188:191],v[12:15],a[108:111]
MM a[100:103],a[188:191],v[4:7],a[100:103]
MM a[100:103],a[184:187],v[0:3],a[100:103]
v_smfmac_f32_16x16x64_bf16 v[144:147], v[236:239], v[64:71], v234
v_smfmac_f32_16x16x64_bf16 v[148:151], v[236:239], v[72:79], v234
v_smfmac_f32_16x16x64_bf16 v[152:155], v[236:239], v[80:87], v234
v_smfmac_f32_16x16x64_bf16 v[156:159], v[236:239], v[88:95], v234
s_waitcnt lgkmcnt(0)
MM v[0:3],a[192:195],v[160:163],v[128:131]
s_add_u32 s64, s64, 16384
TR a[128:129],v232 offset:16384
s_add_u32 s81, s64, 896
MM v[0:3],a[196:199],v[176:179],v[0:3]
TR a[130:131],v232 offset:20480
v_max3_f32 v249, v144, v145, v148
MM v[0:3],a[200:203],v[192:195],v[0:3]
TR a[132:133],v232 offset:24576
v_max3_f32 v249, v249, v149, v152
MM v[0:3],a[204:207],v[208:211],v[0:3]
TR a[134:135],v232 offset:28672
v_max3_f32 v249, v249, v153, v156
MM v[4:7],a[220:223],v[208:211],v[128:131]
TR a[136:137],v233 offset:16384
v_max_f32 v249, v249, v157
MM v[4:7],a[216:219],v[192:195],v[4:7]
TR a[138:139],v233 offset:20480
v_cmp_lt_f32 vcc, s71, v249
MM v[4:7],a[212:215],v[176:179],v[4:7]
TR a[140:141],v233 offset:24576
TR a[142:143],v233 offset:28672
MM v[4:7],a[208:211],v[160:163],v[4:7]
s_cbranch_vccnz LSITE25_%=
LBACK25_%=:
MM v[12:15],a[208:211],v[164:167],v[132:135]
s_waitcnt vmcnt(9)
EX v0,v0
MM v[12:15],a[212:215],v[180:183],v[12:15]
s_mul_i32 s76, s46, 96
EX v1,v1
MM v[12:15],a[216:219],v[196:199],v[12:15]
s_add_u32 s76, s76, 69632
v_lshrrev_b32 v253, 2, v248
v_min_u32 v253, 92, v253
MM v[12:15],a[220:223],v[212:215],v[12:15]
buffer_load_dword v252, v253, s[20:23], s76 offen offset:0 sc1
MM v[8:11],a[204:207],v[212:215],v[132:135]
EX v2,v2
MM v[8:11],a[200:203],v[196:199],v[8:11]
TR a[144:145],v232 offset:16896
CV v0,v0,v1
MM v[8:11],a[196:199],v[180:183],v[8:11]
EX v3,v3
MM v[8:11],a[192:195],v[164:167],v[8:11]
TR a[146:147],v232 offset:20992
CV v1,v2,v3
MM v[16:19],a[192:195],v[168:171],v[136:139]
EX v4,v4
MM v[16:19],a[196:199],v[184:187],v[16:19]
TR a[148:149],v232 offset:25088
TR a[150:151],v232 offset:29184
MM v[16:19],a[200:203],v[200:203],v[16:19]
EX v5,v5
MM v[16:19],a[204:207],v[216:219],v[16:19]
TR a[152:153],v233 offset:16896
CV v2,v4,v5
MM v[20:23],a[220:223],v[216:219],v[136:139]
EX v6,v6
MM v[20:23],a[216:219],v[200:203],v[20:23]
EX v7,v7
MM v[20:23],a[212:215],v[184:187],v[20:23]
TR a[154:155],v233 offset:20992
CV v3,v6,v7
MM v[20:23],a[208:211],v[168:171],v[20:23]
EX v8,v8
MM v[28:31],a[208:211],v[172:175],v[140:143]
TR a[156:157],v233 offset:25088
TR a[158:159],v233 offset:29184
MM v[28:31],a[212:215],v[188:191],v[28:31]
EX v9,v9
MM v[28:31],a[216:219],v[204:207],v[28:31]
TR a[160:161],v232 offset:17408
CV v8,v8,v9
MM v[28:31],a[220:223],v[220:223],v[28:31]
EX v10,v10
MM v[24:27],a[204:207],v[220:223],v[140:143]
EX v11,v11
MM v[24:27],a[200:203],v[204:207],v[24:27]
TR a[162:163],v232 offset:21504
CV v9,v10,v11
MM v[24:27],a[196:199],v[188:191],v[24:27]
EX v12,v12
MM v[24:27],a[192:195],v[172:175],v[24:27]
TR a[164:165],v232 offset:25600
TR a[166:167],v232 offset:29696
MM v[32:35],a[224:227],v[160:163],v[128:131]
EX v13,v13
MM v[32:35],a[228:231],v[176:179],v[32:35]
TR a[168:169],v233 offset:17408
CV v10,v12,v13
MM v[32:35],a[232:235],v[192:195],v[32:35]
EX v14,v14
MM v[32:35],a[236:239],v[208:211],v[32:35]
EX v15,v15
MM v[36:39],a[252:255],v[208:211],v[128:131]
TR a[170:171],v233 offset:21504
CV v11,v14,v15
MM v[36:39],a[248:251],v[192:195],v[36:39]
EX v16,v16
MM v[36:39],a[244:247],v[176:179],v[36:39]
TR a[172:173],v233 offset:25600
TR a[174:175],v233 offset:29696
MM v[36:39],a[240:243],v[160:163],v[36:39]
EX v17,v17
MM v[44:47],a[240:243],v[164:167],v[132:135]
TR a[176:177],v232 offset:17920
CV v16,v16,v17
MM v[44:47],a[244:247],v[180:183],v[44:47]
EX v18,v18
MM v[44:47],a[248:251],v[196:199],v[44:47]
TR a[178:179],v232 offset:22016
TR a[180:181],v232 offset:26112
MM v[44:47],a[252:255],v[212:215],v[44:47]
EX v19,v19
MM v[40:43],a[236:239],v[212:215],v[132:135]
TR a[182:183],v232 offset:30208
CV v17,v18,v19
MM v[40:43],a[232:235],v[196:199],v[40:43]
EX v20,v20
MM v[40:43],a[228:231],v[180:183],v[40:43]
EX v21,v21
MM v[40:43],a[224:227],v[164:167],v[40:43]
TR a[184:185],v233 offset:17920
CV v18,v20,v21
MM v[48:51],a[224:227],v[168:171],v[136:139]
EX v22,v22
MM v[48:51],a[228:231],v[184:187],v[48:51]
TR a[186:187],v233 offset:22016
TR a[188:189],v233 offset:26112
MM v[48:51],a[232:235],v[200:203],v[48:51]
EX v23,v23
MM v[48:51],a[236:239],v[216:219],v[48:51]
TR a[190:191],v233 offset:30208
CV v19,v22,v23
MM v[52:55],a[252:255],v[216:219],v[136:139]
EX v24,v24
MM v[52:55],a[248:251],v[200:203],v[52:55]
EX v25,v25
MM v[52:55],a[244:247],v[184:187],v[52:55]
EX v26,v26
MM v[52:55],a[240:243],v[168:171],v[52:55]
EX v27,v27
MM v[60:63],a[240:243],v[172:175],v[140:143]
EX v28,v28
MM v[60:63],a[244:247],v[188:191],v[60:63]
EX v29,v29
MM v[60:63],a[248:251],v[204:207],v[60:63]
EX v30,v30
MM v[60:63],a[252:255],v[220:223],v[60:63]
EX v31,v31
MM v[56:59],a[236:239],v[220:223],v[140:143]
EX v32,v32
MM v[56:59],a[232:235],v[204:207],v[56:59]
EX v33,v33
MM v[56:59],a[228:231],v[188:191],v[56:59]
EX v34,v34
MM v[56:59],a[224:227],v[172:175],v[56:59]
s_waitcnt lgkmcnt(0)
s_barrier
MM a[0:3],a[128:131],v[64:67],a[0:3]
s_add_u32 m0, s65, 16384
CV v24,v24,v25
CV v25,v26,v27
MM a[0:3],a[132:135],v[68:71],a[0:3]
RD a[192:195],v224 offset:49152
CV v26,v28,v29
MM a[8:11],a[132:135],v[76:79],a[8:11]
RD a[196:199],v225 offset:49152
CV v27,v30,v31
MM a[8:11],a[128:131],v[72:75],a[8:11]
buffer_load_dwordx4 v228, s[8:11], s64 offen offset:0 lds
MM a[16:19],a[128:131],v[80:83],a[16:19]
RD a[200:203],v226 offset:49152
CV v4,v32,v33
MM a[16:19],a[132:135],v[84:87],a[16:19]
buffer_load_dwordx4 v229, s[8:11], s64 offen offset:1024 lds
MM a[24:27],a[132:135],v[92:95],a[24:27]
RD a[204:207],v227 offset:49152
RD a[208:211],v224 offset:53248
MM a[24:27],a[128:131],v[88:91],a[24:27]
EX v35,v35
MM a[28:31],a[136:139],v[88:91],a[28:31]
RD a[212:215],v225 offset:53248
CV v5,v34,v35
MM a[28:31],a[140:143],v[92:95],a[28:31]
EX v36,v36
MM a[20:23],a[140:143],v[84:87],a[20:23]
buffer_load_dwordx4 v230, s[8:11], s64 offen offset:2048 lds
MM a[20:23],a[136:139],v[80:83],a[20:23]
EX v37,v37
MM a[12:15],a[136:139],v[72:75],a[12:15]
RD a[216:219],v226 offset:53248
CV v6,v36,v37
MM a[12:15],a[140:143],v[76:79],a[12:15]
EX v38,v38
MM a[4:7],a[140:143],v[68:71],a[4:7]
EX v39,v39
MM a[4:7],a[136:139],v[64:67],a[4:7]
RD a[220:223],v227 offset:53248
CV v7,v38,v39
MM a[32:35],a[144:147],v[64:67],a[32:35]
EX v40,v40
MM a[32:35],a[148:151],v[68:71],a[32:35]
buffer_load_dwordx4 v231, s[8:11], s64 offen offset:3072 lds
MM a[40:43],a[148:151],v[76:79],a[40:43]
s_add_u32 m0, s65, 81920
EX v41,v41
MM a[40:43],a[144:147],v[72:75],a[40:43]
RD a[224:227],v224 offset:57344
CV v12,v40,v41
MM a[48:51],a[144:147],v[80:83],a[48:51]
EX v42,v42
MM a[48:51],a[148:151],v[84:87],a[48:51]
EX v43,v43
MM a[56:59],a[148:151],v[92:95],a[56:59]
RD a[228:231],v225 offset:57344
CV v13,v42,v43
MM a[56:59],a[144:147],v[88:91],a[56:59]
EX v44,v44
MM a[60:63],a[152:155],v[88:91],a[60:63]
buffer_load_dwordx4 v235, s[12:15], s81 offen offset:0 lds
MM a[60:63],a[156:159],v[92:95],a[60:63]
EX v45,v45
MM a[52:55],a[156:159],v[84:87],a[52:55]
RD a[232:235],v226 offset:57344
CV v14,v44,v45
MM a[52:55],a[152:155],v[80:83],a[52:55]
EX v46,v46
MM a[44:47],a[152:155],v[72:75],a[44:47]
EX v47,v47
MM a[44:47],a[156:159],v[76:79],a[44:47]
RD a[236:239],v227 offset:57344
CV v15,v46,v47
MM a[36:39],a[156:159],v[68:71],a[36:39]
buffer_load_dwordx4 v235, s[12:15], s64 offen offset:1024 lds
MM a[36:39],a[152:155],v[64:67],a[36:39]
EX v48,v48
MM a[64:67],a[160:163],v[64:67],a[64:67]
EX v49,v49
MM a[64:67],a[164:167],v[68:71],a[64:67]
RD a[240:243],v224 offset:61440
CV v20,v48,v49
MM a[72:75],a[164:167],v[76:79],a[72:75]
EX v50,v50
MM a[72:75],a[160:163],v[72:75],a[72:75]
RD a[244:247],v225 offset:61440
RD a[248:251],v226 offset:61440
MM a[80:83],a[160:163],v[80:83],a[80:83]
EX v51,v51
MM a[80:83],a[164:167],v[84:87],a[80:83]
LPG19_%=:
s_cbranch_execz LTR3_%=
RD a[252:255],v227 offset:61440
CV v21,v50,v51
MM a[88:91],a[164:167],v[92:95],a[88:91]
EX v52,v52
MM a[88:91],a[160:163],v[88:91],a[88:91]
buffer_load_dwordx4 v235, s[12:15], s81 offen offset:2048 lds
MM a[92:95],a[168:171],v[88:91],a[92:95]
EX v53,v53
MM a[92:95],a[172:175],v[92:95],a[92:95]
EX v54,v54
MM a[84:87],a[172:175],v[84:87],a[84:87]
buffer_load_dwordx4 v235, s[12:15], s64 offen offset:3072 lds
MM a[84:87],a[168:171],v[80:83],a[84:87]
EX v55,v55
MM a[76:79],a[168:171],v[72:75],a[76:79]
EX v56,v56
MM a[76:79],a[172:175],v[76:79],a[76:79]
EX v57,v57
MM a[68:71],a[172:175],v[68:71],a[68:71]
EX v58,v58
MM a[68:71],a[168:171],v[64:67],a[68:71]
EX v59,v59
MM a[96:99],a[176:179],v[64:67],a[96:99]
EX v60,v60
MM a[96:99],a[180:183],v[68:71],a[96:99]
EX v61,v61
MM a[104:107],a[180:183],v[76:79],a[104:107]
)ASM"
R"ASM(
EX v62,v62
MM a[104:107],a[176:179],v[72:75],a[104:107]
EX v63,v63
MM a[112:115],a[176:179],v[80:83],a[112:115]
CV v22,v52,v53
CV v23,v54,v55
MM a[112:115],a[180:183],v[84:87],a[112:115]
CV v28,v56,v57
CV v29,v58,v59
MM a[120:123],a[180:183],v[92:95],a[120:123]
s_waitcnt vmcnt(8)
v_cmp_ne_u32 vcc, s69, v252
s_cbranch_vccnz LRFH_%=
LRFHb_%=:
MM a[120:123],a[176:179],v[88:91],a[120:123]
v_bfe_u32 v253, v248, 4, 3
v_mul_u32_u24 v253, 3456, v253
MM a[124:127],a[184:187],v[88:91],a[124:127]
v_add_f32 v244, v144, v145
v_add_f32 v245, v148, v149
MM a[124:127],a[188:191],v[92:95],a[124:127]
v_add_f32 v246, v152, v153
v_add_f32 v247, v156, v157
v_lshrrev_b32 v252, 7, v248
MM a[116:119],a[188:191],v[84:87],a[116:119]
v_lshl_add_u32 v253, v252, 4, v253
CV v30,v60,v61
MM a[116:119],a[184:187],v[80:83],a[116:119]
s_min_u32 s90, %[swave], 2
s_mul_i32 s76, s46, 384
CV v31,v62,v63
MM a[108:111],a[184:187],v[72:75],a[108:111]
s_lshl_b32 s91, s90, 7
s_add_u32 s76, s76, s91
s_add_u32 s76, s76, 21626880
MM a[108:111],a[188:191],v[76:79],a[108:111]
s_lshl_b32 s90, s90, 10
s_add_u32 s90, s90, %[slds]
MM a[100:103],a[188:191],v[68:71],a[100:103]
s_add_u32 m0, s90, 131072
s_nop 0
buffer_load_dwordx4 v253, s[20:23], s76 offen offset:0 sc1 lds
MM a[100:103],a[184:187],v[64:67],a[100:103]
v_smfmac_f32_16x16x64_bf16 v[144:147], v[236:239], v[0:7], v234
v_smfmac_f32_16x16x64_bf16 v[148:151], v[236:239], v[8:15], v234
v_smfmac_f32_16x16x64_bf16 v[152:155], v[236:239], v[16:23], v234
v_smfmac_f32_16x16x64_bf16 v[156:159], v[236:239], v[24:31], v234
s_waitcnt lgkmcnt(0)
MM v[64:67],a[192:195],v[160:163],v[128:131]
s_add_u32 s64, s64, 16384
TR a[128:129],v232 offset:32768
s_add_u32 s81, s64, 896
MM v[64:67],a[196:199],v[176:179],v[64:67]
TR a[130:131],v232 offset:36864
v_max3_f32 v249, v144, v145, v148
MM v[64:67],a[200:203],v[192:195],v[64:67]
TR a[132:133],v232 offset:40960
v_max3_f32 v249, v249, v149, v152
MM v[64:67],a[204:207],v[208:211],v[64:67]
TR a[134:135],v232 offset:45056
v_max3_f32 v249, v249, v153, v156
MM v[68:71],a[220:223],v[208:211],v[128:131]
TR a[136:137],v233 offset:32768
v_max_f32 v249, v249, v157
MM v[68:71],a[216:219],v[192:195],v[68:71]
TR a[138:139],v233 offset:36864
v_cmp_lt_f32 vcc, s71, v249
MM v[68:71],a[212:215],v[176:179],v[68:71]
TR a[140:141],v233 offset:40960
TR a[142:143],v233 offset:45056
MM v[68:71],a[208:211],v[160:163],v[68:71]
s_cbranch_vccnz LSITE26_%=
LBACK26_%=:
MM v[76:79],a[208:211],v[164:167],v[132:135]
EX v64,v64
MM v[76:79],a[212:215],v[180:183],v[76:79]
EX v65,v65
MM v[76:79],a[216:219],v[196:199],v[76:79]
TR a[144:145],v232 offset:33280
CV v64,v64,v65
MM v[76:79],a[220:223],v[212:215],v[76:79]
EX v66,v66
MM v[72:75],a[204:207],v[212:215],v[132:135]
EX v67,v67
MM v[72:75],a[200:203],v[196:199],v[72:75]
TR a[146:147],v232 offset:37376
CV v65,v66,v67
MM v[72:75],a[196:199],v[180:183],v[72:75]
EX v68,v68
MM v[72:75],a[192:195],v[164:167],v[72:75]
TR a[148:149],v232 offset:41472
TR a[150:151],v232 offset:45568
MM v[80:83],a[192:195],v[168:171],v[136:139]
EX v69,v69
MM v[80:83],a[196:199],v[184:187],v[80:83]
TR a[152:153],v233 offset:33280
CV v66,v68,v69
MM v[80:83],a[200:203],v[200:203],v[80:83]
EX v70,v70
MM v[80:83],a[204:207],v[216:219],v[80:83]
TR a[154:155],v233 offset:37376
TR a[156:157],v233 offset:41472
MM v[84:87],a[220:223],v[216:219],v[136:139]
EX v71,v71
MM v[84:87],a[216:219],v[200:203],v[84:87]
TR a[158:159],v233 offset:45568
CV v67,v70,v71
MM v[84:87],a[212:215],v[184:187],v[84:87]
EX v72,v72
MM v[84:87],a[208:211],v[168:171],v[84:87]
EX v73,v73
MM v[92:95],a[208:211],v[172:175],v[140:143]
TR a[160:161],v232 offset:33792
CV v72,v72,v73
MM v[92:95],a[212:215],v[188:191],v[92:95]
EX v74,v74
MM v[92:95],a[216:219],v[204:207],v[92:95]
TR a[162:163],v232 offset:37888
TR a[164:165],v232 offset:41984
MM v[92:95],a[220:223],v[220:223],v[92:95]
EX v75,v75
MM v[88:91],a[204:207],v[220:223],v[140:143]
TR a[166:167],v232 offset:46080
CV v73,v74,v75
MM v[88:91],a[200:203],v[204:207],v[88:91]
EX v76,v76
MM v[88:91],a[196:199],v[188:191],v[88:91]
EX v77,v77
MM v[88:91],a[192:195],v[172:175],v[88:91]
TR a[168:169],v233 offset:33792
CV v74,v76,v77
MM v[96:99],a[224:227],v[160:163],v[128:131]
EX v78,v78
MM v[96:99],a[228:231],v[176:179],v[96:99]
TR a[170:171],v233 offset:37888
TR a[172:173],v233 offset:41984
MM v[96:99],a[232:235],v[192:195],v[96:99]
EX v79,v79
MM v[96:99],a[236:239],v[208:211],v[96:99]
TR a[174:175],v233 offset:46080
CV v75,v78,v79
MM v[100:103],a[252:255],v[208:211],v[128:131]
EX v80,v80
MM v[100:103],a[248:251],v[192:195],v[100:103]
EX v81,v81
MM v[100:103],a[244:247],v[176:179],v[100:103]
TR a[176:177],v232 offset:34304
CV v80,v80,v81
MM v[100:103],a[240:243],v[160:163],v[100:103]
EX v82,v82
MM v[108:111],a[240:243],v[164:167],v[132:135]
TR a[178:179],v232 offset:38400
TR a[180:181],v232 offset:42496
MM v[108:111],a[244:247],v[180:183],v[108:111]
EX v83,v83
MM v[108:111],a[248:251],v[196:199],v[108:111]
TR a[182:183],v232 offset:46592
CV v81,v82,v83
MM v[108:111],a[252:255],v[212:215],v[108:111]
EX v84,v84
MM v[104:107],a[236:239],v[212:215],v[132:135]
TR a[184:185],v233 offset:34304
TR a[186:187],v233 offset:38400
MM v[104:107],a[232:235],v[196:199],v[104:107]
EX v85,v85
MM v[104:107],a[228:231],v[180:183],v[104:107]
TR a[188:189],v233 offset:42496
CV v82,v84,v85
MM v[104:107],a[224:227],v[164:167],v[104:107]
EX v86,v86
MM v[112:115],a[224:227],v[168:171],v[136:139]
EX v87,v87
MM v[112:115],a[228:231],v[184:187],v[112:115]
TR a[190:191],v233 offset:46592
CV v83,v86,v87
MM v[112:115],a[232:235],v[200:203],v[112:115]
EX v88,v88
MM v[112:115],a[236:239],v[216:219],v[112:115]
EX v89,v89
MM v[116:119],a[252:255],v[216:219],v[136:139]
EX v90,v90
MM v[116:119],a[248:251],v[200:203],v[116:119]
EX v91,v91
MM v[116:119],a[244:247],v[184:187],v[116:119]
EX v92,v92
MM v[116:119],a[240:243],v[168:171],v[116:119]
EX v93,v93
MM v[124:127],a[240:243],v[172:175],v[140:143]
EX v94,v94
MM v[124:127],a[244:247],v[188:191],v[124:127]
EX v95,v95
MM v[124:127],a[248:251],v[204:207],v[124:127]
EX v96,v96
MM v[124:127],a[252:255],v[220:223],v[124:127]
EX v97,v97
MM v[120:123],a[236:239],v[220:223],v[140:143]
EX v98,v98
MM v[120:123],a[232:235],v[204:207],v[120:123]
EX v99,v99
MM v[120:123],a[228:231],v[188:191],v[120:123]
s_getpc_b64 s[98:99]
LTA4_%=:
s_min_u32 s73, %[swave], 1
s_lshl_b32 s73, s73, 12
s_add_u32 s73, s73, LPG21_%=-LTA4_%=
s_add_u32 s98, s98, s73
s_addc_u32 s99, s99, 0
s_mov_b64 exec, 0
s_setpc_b64 s[98:99]
LTR4_%=:
s_mov_b64 exec, -1
CV v88,v88,v89
MM v[120:123],a[224:227],v[172:175],v[120:123]
s_waitcnt lgkmcnt(0)
s_barrier
MM a[0:3],a[128:131],v[0:3],a[0:3]
s_add_u32 m0, s65, 32768
CV v89,v90,v91
CV v90,v92,v93
MM a[0:3],a[132:135],v[4:7],a[0:3]
RD a[192:195],v224 offset:0
CV v91,v94,v95
MM a[8:11],a[132:135],v[12:15],a[8:11]
buffer_load_dwordx4 v228, s[8:11], s64 offen offset:0 lds
MM a[8:11],a[128:131],v[8:11],a[8:11]
RD a[196:199],v225 offset:0
CV v68,v96,v97
MM a[16:19],a[128:131],v[16:19],a[16:19]
RD a[200:203],v226 offset:0
CV v69,v98,v99
MM a[16:19],a[132:135],v[20:23],a[16:19]
buffer_load_dwordx4 v229, s[8:11], s64 offen offset:1024 lds
MM a[24:27],a[132:135],v[28:31],a[24:27]
RD a[204:207],v227 offset:0
RD a[208:211],v224 offset:4096
MM a[24:27],a[128:131],v[24:27],a[24:27]
RD a[212:215],v225 offset:4096
RD a[216:219],v226 offset:4096
MM a[28:31],a[136:139],v[24:27],a[28:31]
buffer_load_dwordx4 v230, s[8:11], s64 offen offset:2048 lds
MM a[28:31],a[140:143],v[28:31],a[28:31]
EX v100,v100
MM a[20:23],a[140:143],v[20:23],a[20:23]
EX v101,v101
MM a[20:23],a[136:139],v[16:19],a[20:23]
RD a[220:223],v227 offset:4096
CV v70,v100,v101
MM a[12:15],a[136:139],v[8:11],a[12:15]
EX v102,v102
MM a[12:15],a[140:143],v[12:15],a[12:15]
buffer_load_dwordx4 v231, s[8:11], s64 offen offset:3072 lds
MM a[4:7],a[140:143],v[4:7],a[4:7]
s_add_u32 m0, s65, 98304
EX v103,v103
MM a[4:7],a[136:139],v[0:3],a[4:7]
RD a[224:227],v224 offset:8192
CV v71,v102,v103
MM a[32:35],a[144:147],v[0:3],a[32:35]
EX v104,v104
MM a[32:35],a[148:151],v[4:7],a[32:35]
EX v105,v105
MM a[40:43],a[148:151],v[12:15],a[40:43]
RD a[228:231],v225 offset:8192
CV v76,v104,v105
MM a[40:43],a[144:147],v[8:11],a[40:43]
EX v106,v106
MM a[48:51],a[144:147],v[16:19],a[48:51]
buffer_load_dwordx4 v235, s[12:15], s81 offen offset:0 lds
MM a[48:51],a[148:151],v[20:23],a[48:51]
EX v107,v107
MM a[56:59],a[148:151],v[28:31],a[56:59]
RD a[232:235],v226 offset:8192
CV v77,v106,v107
MM a[56:59],a[144:147],v[24:27],a[56:59]
EX v108,v108
MM a[60:63],a[152:155],v[24:27],a[60:63]
EX v109,v109
MM a[60:63],a[156:159],v[28:31],a[60:63]
RD a[236:239],v227 offset:8192
CV v78,v108,v109
MM a[52:55],a[156:159],v[20:23],a[52:55]
EX v110,v110
MM a[52:55],a[152:155],v[16:19],a[52:55]
buffer_load_dwordx4 v235, s[12:15], s64 offen offset:1024 lds
MM a[44:47],a[152:155],v[8:11],a[44:47]
EX v111,v111
MM a[44:47],a[156:159],v[12:15],a[44:47]
RD a[240:243],v224 offset:12288
CV v79,v110,v111
MM a[36:39],a[156:159],v[4:7],a[36:39]
EX v112,v112
MM a[36:39],a[152:155],v[0:3],a[36:39]
EX v113,v113
MM a[64:67],a[160:163],v[0:3],a[64:67]
RD a[244:247],v225 offset:12288
CV v84,v112,v113
MM a[64:67],a[164:167],v[4:7],a[64:67]
EX v114,v114
MM a[72:75],a[164:167],v[12:15],a[72:75]
buffer_load_dwordx4 v235, s[12:15], s81 offen offset:2048 lds
MM a[72:75],a[160:163],v[8:11],a[72:75]
EX v115,v115
MM a[80:83],a[160:163],v[16:19],a[80:83]
RD a[248:251],v226 offset:12288
CV v85,v114,v115
MM a[80:83],a[164:167],v[20:23],a[80:83]
EX v116,v116
MM a[88:91],a[164:167],v[28:31],a[88:91]
EX v117,v117
MM a[88:91],a[160:163],v[24:27],a[88:91]
RD a[252:255],v227 offset:12288
CV v86,v116,v117
MM a[92:95],a[168:171],v[24:27],a[92:95]
EX v118,v118
MM a[92:95],a[172:175],v[28:31],a[92:95]
buffer_load_dwordx4 v235, s[12:15], s64 offen offset:3072 lds
MM a[84:87],a[172:175],v[20:23],a[84:87]
EX v119,v119
MM a[84:87],a[168:171],v[16:19],a[84:87]
CV v87,v118,v119
MM a[76:79],a[168:171],v[8:11],a[76:79]
EX v120,v120
MM a[76:79],a[172:175],v[12:15],a[76:79]
EX v121,v121
MM a[68:71],a[172:175],v[4:7],a[68:71]
EX v122,v122
MM a[68:71],a[168:171],v[0:3],a[68:71]
EX v123,v123
MM a[96:99],a[176:179],v[0:3],a[96:99]
EX v124,v124
MM a[96:99],a[180:183],v[4:7],a[96:99]
EX v125,v125
MM a[104:107],a[180:183],v[12:15],a[104:107]
EX v126,v126
MM a[104:107],a[176:179],v[8:11],a[104:107]
EX v127,v127
MM a[112:115],a[176:179],v[16:19],a[112:115]
CV v92,v120,v121
CV v93,v122,v123
MM a[112:115],a[180:183],v[20:23],a[112:115]
CV v94,v124,v125
CV v95,v126,v127
MM a[120:123],a[180:183],v[28:31],a[120:123]
MM a[120:123],a[176:179],v[24:27],a[120:123]
MM a[124:127],a[184:187],v[24:27],a[124:127]
v_add_f32 v244, v144, v145
v_add_f32 v245, v148, v149
MM a[124:127],a[188:191],v[28:31],a[124:127]
v_add_f32 v246, v152, v153
v_add_f32 v247, v156, v157
MM a[116:119],a[188:191],v[20:23],a[116:119]
MM a[116:119],a[184:187],v[16:19],a[116:119]
MM a[108:111],a[184:187],v[8:11],a[108:111]
MM a[108:111],a[188:191],v[12:15],a[108:111]
MM a[100:103],a[188:191],v[4:7],a[100:103]
MM a[100:103],a[184:187],v[0:3],a[100:103]
v_smfmac_f32_16x16x64_bf16 v[144:147], v[236:239], v[64:71], v234
v_smfmac_f32_16x16x64_bf16 v[148:151], v[236:239], v[72:79], v234
v_smfmac_f32_16x16x64_bf16 v[152:155], v[236:239], v[80:87], v234
v_smfmac_f32_16x16x64_bf16 v[156:159], v[236:239], v[88:95], v234
s_waitcnt lgkmcnt(0)
MM v[0:3],a[192:195],v[160:163],v[128:131]
s_add_u32 s64, s64, 16384
TR a[128:129],v232 offset:49152
s_add_u32 s81, s64, 896
MM v[0:3],a[196:199],v[176:179],v[0:3]
TR a[130:131],v232 offset:53248
v_max3_f32 v249, v144, v145, v148
MM v[0:3],a[200:203],v[192:195],v[0:3]
TR a[132:133],v232 offset:57344
v_max3_f32 v249, v249, v149, v152
MM v[0:3],a[204:207],v[208:211],v[0:3]
TR a[134:135],v232 offset:61440
v_max3_f32 v249, v249, v153, v156
MM v[4:7],a[220:223],v[208:211],v[128:131]
TR a[136:137],v233 offset:49152
v_max_f32 v249, v249, v157
MM v[4:7],a[216:219],v[192:195],v[4:7]
TR a[138:139],v233 offset:53248
v_cmp_lt_f32 vcc, s71, v249
MM v[4:7],a[212:215],v[176:179],v[4:7]
TR a[140:141],v233 offset:57344
TR a[142:143],v233 offset:61440
)ASM"
R"ASM(
MM v[4:7],a[208:211],v[160:163],v[4:7]
s_cbranch_vccnz LSITE27_%=
LBACK27_%=:
MM v[12:15],a[208:211],v[164:167],v[132:135]
v_lshrrev_b32 v125, 8, v248
v_lshlrev_b32 v124, 10, v125
MM v[12:15],a[212:215],v[180:183],v[12:15]
v_bfe_u32 v126, v248, 6, 2
v_lshl_add_u32 v124, v126, 7, v124
MM v[12:15],a[216:219],v[196:199],v[12:15]
v_bfe_u32 v126, v248, 4, 2
v_lshl_add_u32 v124, v126, 3, v124
MM v[12:15],a[220:223],v[212:215],v[12:15]
s_lshl_b32 s91, %[swave], 5
EX v0,v0
MM v[8:11],a[204:207],v[212:215],v[132:135]
s_add_u32 s91, s91, %[slds]
EX v1,v1
MM v[8:11],a[200:203],v[196:199],v[8:11]
s_add_u32 s91, s91, 139264
v_add_u32 v124, s91, v124
v_and_b32 v126, 112, v248
MM v[8:11],a[196:199],v[180:183],v[8:11]
s_add_u32 s91, %[slds], 131072
v_lshl_add_u32 v126, v125, 7, v126
v_add_u32 v126, s91, v126
MM v[8:11],a[192:195],v[164:167],v[8:11]
v_bfe_u32 v127, v248, 4, 3
v_mul_u32_u24 v127, 2560, v127
MM v[16:19],a[192:195],v[168:171],v[136:139]
v_lshl_add_u32 v127, v125, 4, v127
CV v0,v0,v1
EX v2,v2
MM v[16:19],a[196:199],v[184:187],v[16:19]
s_lshl_b32 s90, %[swave], 6
EX v3,v3
MM v[16:19],a[200:203],v[200:203],v[16:19]
s_lshl_b32 s91, s47, 8
EX v4,v4
MM v[16:19],a[204:207],v[216:219],v[16:19]
s_add_u32 s90, s90, s91
TR a[144:145],v232 offset:49664
TR a[146:147],v232 offset:53760
MM v[20:23],a[220:223],v[216:219],v[136:139]
s_mul_i32 s91, s46, 17920
EX v5,v5
MM v[20:23],a[216:219],v[200:203],v[20:23]
s_add_u32 s90, s90, s91
TR a[148:149],v232 offset:57856
CV v1,v2,v3
MM v[20:23],a[212:215],v[184:187],v[20:23]
s_add_u32 s90, s90, 21659648
EX v6,v6
MM v[20:23],a[208:211],v[168:171],v[20:23]
EX v7,v7
MM v[28:31],a[208:211],v[172:175],v[140:143]
TR a[150:151],v232 offset:61952
TR a[152:153],v233 offset:49664
MM v[28:31],a[212:215],v[188:191],v[28:31]
EX v8,v8
MM v[28:31],a[216:219],v[204:207],v[28:31]
TR a[154:155],v233 offset:53760
CV v2,v4,v5
MM v[28:31],a[220:223],v[220:223],v[28:31]
EX v9,v9
MM v[24:27],a[204:207],v[220:223],v[140:143]
TR a[156:157],v233 offset:57856
CV v3,v6,v7
MM v[24:27],a[200:203],v[204:207],v[24:27]
EX v10,v10
MM v[24:27],a[196:199],v[188:191],v[24:27]
TR a[158:159],v233 offset:61952
CV v8,v8,v9
MM v[24:27],a[192:195],v[172:175],v[24:27]
EX v11,v11
MM v[32:35],a[224:227],v[160:163],v[128:131]
TR a[160:161],v232 offset:50176
TR a[162:163],v232 offset:54272
MM v[32:35],a[228:231],v[176:179],v[32:35]
EX v12,v12
MM v[32:35],a[232:235],v[192:195],v[32:35]
EX v13,v13
MM v[32:35],a[236:239],v[208:211],v[32:35]
TR a[164:165],v232 offset:58368
CV v9,v10,v11
MM v[36:39],a[252:255],v[208:211],v[128:131]
EX v14,v14
MM v[36:39],a[248:251],v[192:195],v[36:39]
TR a[166:167],v232 offset:62464
CV v10,v12,v13
MM v[36:39],a[244:247],v[176:179],v[36:39]
EX v15,v15
MM v[36:39],a[240:243],v[160:163],v[36:39]
TR a[168:169],v233 offset:50176
TR a[170:171],v233 offset:54272
MM v[44:47],a[240:243],v[164:167],v[132:135]
EX v16,v16
MM v[44:47],a[244:247],v[180:183],v[44:47]
EX v17,v17
MM v[44:47],a[248:251],v[196:199],v[44:47]
TR a[172:173],v233 offset:58368
CV v11,v14,v15
MM v[44:47],a[252:255],v[212:215],v[44:47]
EX v18,v18
MM v[40:43],a[236:239],v[212:215],v[132:135]
TR a[174:175],v233 offset:62464
CV v16,v16,v17
MM v[40:43],a[232:235],v[196:199],v[40:43]
EX v19,v19
MM v[40:43],a[228:231],v[180:183],v[40:43]
TR a[176:177],v232 offset:50688
TR a[178:179],v232 offset:54784
MM v[40:43],a[224:227],v[164:167],v[40:43]
EX v20,v20
MM v[48:51],a[224:227],v[168:171],v[136:139]
EX v21,v21
MM v[48:51],a[228:231],v[184:187],v[48:51]
TR a[180:181],v232 offset:58880
CV v17,v18,v19
MM v[48:51],a[232:235],v[200:203],v[48:51]
EX v22,v22
MM v[48:51],a[236:239],v[216:219],v[48:51]
TR a[182:183],v232 offset:62976
CV v18,v20,v21
MM v[52:55],a[252:255],v[216:219],v[136:139]
EX v23,v23
MM v[52:55],a[248:251],v[200:203],v[52:55]
TR a[184:185],v233 offset:50688
TR a[186:187],v233 offset:54784
MM v[52:55],a[244:247],v[184:187],v[52:55]
EX v24,v24
MM v[52:55],a[240:243],v[168:171],v[52:55]
TR a[188:189],v233 offset:58880
CV v19,v22,v23
MM v[60:63],a[240:243],v[172:175],v[140:143]
EX v25,v25
MM v[60:63],a[244:247],v[188:191],v[60:63]
TR a[190:191],v233 offset:62976
CV v24,v24,v25
MM v[60:63],a[248:251],v[204:207],v[60:63]
EX v26,v26
MM v[60:63],a[252:255],v[220:223],v[60:63]
EX v27,v27
MM v[56:59],a[236:239],v[220:223],v[140:143]
EX v28,v28
MM v[56:59],a[232:235],v[204:207],v[56:59]
EX v29,v29
MM v[56:59],a[228:231],v[188:191],v[56:59]
EX v30,v30
MM v[56:59],a[224:227],v[172:175],v[56:59]
s_waitcnt vmcnt(8) lgkmcnt(0)
s_barrier
MM a[0:3],a[128:131],v[64:67],a[0:3]
s_add_u32 m0, s65, 49152
CV v25,v26,v27
CV v26,v28,v29
MM a[0:3],a[132:135],v[68:71],a[0:3]
RD a[192:195],v224 offset:16384
RD a[196:199],v225 offset:16384
MM a[8:11],a[132:135],v[76:79],a[8:11]
EX v31,v31
MM a[8:11],a[128:131],v[72:75],a[8:11]
RD a[200:203],v226 offset:16384
CV v27,v30,v31
MM a[16:19],a[128:131],v[80:83],a[16:19]
TR v[96:97],v124 offset:0
TR v[98:99],v124 offset:512
MM a[16:19],a[132:135],v[84:87],a[16:19]
RD v[108:111],v126 offset:0
TR v[100:101],v124 offset:4096
MM a[24:27],a[132:135],v[92:95],a[24:27]
TR v[102:103],v124 offset:4608
RD v[112:115],v126 offset:512
MM a[24:27],a[128:131],v[88:91],a[24:27]
TR v[104:105],v124 offset:8192
TR v[106:107],v124 offset:8704
MM a[28:31],a[136:139],v[88:91],a[28:31]
RD v[116:119],v126 offset:1024
RD a[204:207],v227 offset:16384
EX v32,v32
MM a[28:31],a[140:143],v[92:95],a[28:31]
EX v33,v33
MM a[20:23],a[140:143],v[84:87],a[20:23]
EX v34,v34
MM a[20:23],a[136:139],v[80:83],a[20:23]
buffer_load_dwordx4 v228, s[8:11], s64 offen offset:0 lds
LPG20_%=:
s_cbranch_execz LTR3_%=
MM a[12:15],a[136:139],v[72:75],a[12:15]
EX v35,v35
MM a[12:15],a[140:143],v[76:79],a[12:15]
EX v36,v36
MM a[4:7],a[140:143],v[68:71],a[4:7]
CV v4,v32,v33
CV v5,v34,v35
MM a[4:7],a[136:139],v[64:67],a[4:7]
EX v37,v37
MM a[32:35],a[144:147],v[64:67],a[32:35]
EX v38,v38
MM a[32:35],a[148:151],v[68:71],a[32:35]
buffer_load_dwordx4 v229, s[8:11], s64 offen offset:1024 lds
MM a[40:43],a[148:151],v[76:79],a[40:43]
EX v39,v39
MM a[40:43],a[144:147],v[72:75],a[40:43]
RD a[208:211],v224 offset:20480
CV v6,v36,v37
MM a[48:51],a[144:147],v[80:83],a[48:51]
s_waitcnt lgkmcnt(2)
EX v40,v40
MM a[48:51],a[148:151],v[84:87],a[48:51]
RD a[212:215],v225 offset:20480
CV v7,v38,v39
MM v[120:123],v[96:99],v[108:111],0
MM v[120:123],v[100:103],v[112:115],v[120:123]
MM v[120:123],v[104:107],v[116:119],v[120:123]
MM a[56:59],a[148:151],v[92:95],a[56:59]
TR v[96:97],v124 offset:12288
TR v[98:99],v124 offset:12800
MM a[56:59],a[144:147],v[88:91],a[56:59]
RD v[108:111],v126 offset:1536
TR v[100:101],v124 offset:16384
MM a[60:63],a[152:155],v[88:91],a[60:63]
TR v[102:103],v124 offset:16896
RD v[112:115],v126 offset:2048
EX v41,v41
MM a[60:63],a[156:159],v[92:95],a[60:63]
TR v[104:105],v124 offset:20480
TR v[106:107],v124 offset:20992
MM a[52:55],a[156:159],v[84:87],a[52:55]
RD v[116:119],v126 offset:2560
CV v12,v40,v41
EX v42,v42
MM a[52:55],a[152:155],v[80:83],a[52:55]
EX v43,v43
MM a[44:47],a[152:155],v[72:75],a[44:47]
buffer_load_dwordx4 v230, s[8:11], s64 offen offset:2048 lds
MM a[44:47],a[156:159],v[76:79],a[44:47]
EX v44,v44
MM a[36:39],a[156:159],v[68:71],a[36:39]
EX v45,v45
MM a[36:39],a[152:155],v[64:67],a[36:39]
RD a[216:219],v226 offset:20480
CV v13,v42,v43
MM a[64:67],a[160:163],v[64:67],a[64:67]
EX v46,v46
MM a[64:67],a[164:167],v[68:71],a[64:67]
EX v47,v47
MM a[72:75],a[164:167],v[76:79],a[72:75]
buffer_load_dwordx4 v231, s[8:11], s64 offen offset:3072 lds
MM a[72:75],a[160:163],v[72:75],a[72:75]
s_add_u32 m0, s65, 114688
RD a[220:223],v227 offset:20480
CV v14,v44,v45
MM a[80:83],a[160:163],v[80:83],a[80:83]
EX v48,v48
MM a[80:83],a[164:167],v[84:87],a[80:83]
EX v49,v49
MM a[88:91],a[164:167],v[92:95],a[88:91]
s_waitcnt lgkmcnt(2)
RD a[224:227],v224 offset:24576
CV v15,v46,v47
MM a[88:91],a[160:163],v[88:91],a[88:91]
EX v50,v50
MM v[120:123],v[96:99],v[108:111],v[120:123]
MM v[120:123],v[100:103],v[112:115],v[120:123]
MM v[120:123],v[104:107],v[116:119],v[120:123]
MM a[92:95],a[168:171],v[88:91],a[92:95]
EX v51,v51
MM a[92:95],a[172:175],v[92:95],a[92:95]
buffer_load_dwordx4 v235, s[12:15], s81 offen offset:0 lds
MM a[84:87],a[172:175],v[84:87],a[84:87]
RD a[228:231],v225 offset:24576
CV v20,v48,v49
MM a[84:87],a[168:171],v[80:83],a[84:87]
EX v52,v52
MM a[76:79],a[168:171],v[72:75],a[76:79]
EX v53,v53
MM a[76:79],a[172:175],v[76:79],a[76:79]
RD a[232:235],v226 offset:24576
CV v21,v50,v51
MM a[68:71],a[172:175],v[68:71],a[68:71]
s_mov_b32 exec_lo, 0x00ff00ff
s_mov_b32 exec_hi, 0x00ff00ff
buffer_store_dwordx4 v[120:123], v127, s[20:23], s90 offen offset:0 sc1
s_mov_b64 exec, -1
s_nop 0
MM a[68:71],a[168:171],v[64:67],a[68:71]
EX v54,v54
MM a[96:99],a[176:179],v[64:67],a[96:99]
EX v55,v55
MM a[96:99],a[180:183],v[68:71],a[96:99]
RD a[236:239],v227 offset:24576
CV v22,v52,v53
MM a[104:107],a[180:183],v[76:79],a[104:107]
EX v56,v56
MM a[104:107],a[176:179],v[72:75],a[104:107]
buffer_load_dwordx4 v235, s[12:15], s64 offen offset:1024 lds
MM a[112:115],a[176:179],v[80:83],a[112:115]
EX v57,v57
MM a[112:115],a[180:183],v[84:87],a[112:115]
RD a[240:243],v224 offset:28672
CV v23,v54,v55
MM a[120:123],a[180:183],v[92:95],a[120:123]
EX v58,v58
MM a[120:123],a[176:179],v[88:91],a[120:123]
EX v59,v59
MM a[124:127],a[184:187],v[88:91],a[124:127]
v_add_f32 v244, v144, v145
v_add_f32 v245, v148, v149
MM a[124:127],a[188:191],v[92:95],a[124:127]
v_add_f32 v246, v152, v153
v_add_f32 v247, v156, v157
MM a[116:119],a[188:191],v[84:87],a[116:119]
EX v60,v60
MM a[116:119],a[184:187],v[80:83],a[116:119]
RD a[244:247],v225 offset:28672
CV v28,v56,v57
MM a[108:111],a[184:187],v[72:75],a[108:111]
EX v61,v61
MM a[108:111],a[188:191],v[76:79],a[108:111]
buffer_load_dwordx4 v235, s[12:15], s81 offen offset:2048 lds
MM a[100:103],a[188:191],v[68:71],a[100:103]
EX v62,v62
MM a[100:103],a[184:187],v[64:67],a[100:103]
RD a[248:251],v226 offset:28672
CV v29,v58,v59
v_smfmac_f32_16x16x64_bf16 v[144:147], v[236:239], v[0:7], v234
RD a[252:255],v227 offset:28672
EX v63,v63
v_smfmac_f32_16x16x64_bf16 v[148:151], v[236:239], v[8:15], v234
CV v30,v60,v61
v_smfmac_f32_16x16x64_bf16 v[152:155], v[236:239], v[16:23], v234
CV v31,v62,v63
s_nop 1
v_smfmac_f32_16x16x64_bf16 v[156:159], v[236:239], v[24:31], v234
s_waitcnt lgkmcnt(0)
buffer_load_dwordx4 v235, s[12:15], s64 offen offset:3072 lds
MM v[64:67],a[192:195],v[160:163],v[128:131]
s_add_u32 s64, s64, 16384
TR a[128:129],v232 offset:0
s_add_u32 s81, s64, 896
MM v[64:67],a[196:199],v[176:179],v[64:67]
TR a[130:131],v232 offset:4096
v_max3_f32 v249, v144, v145, v148
MM v[64:67],a[200:203],v[192:195],v[64:67]
TR a[132:133],v232 offset:8192
v_max3_f32 v249, v249, v149, v152
MM v[64:67],a[204:207],v[208:211],v[64:67]
TR a[134:135],v232 offset:12288
v_max3_f32 v249, v249, v153, v156
MM v[68:71],a[220:223],v[208:211],v[128:131]
TR a[136:137],v233 offset:0
v_max_f32 v249, v249, v157
MM v[68:71],a[216:219],v[192:195],v[68:71]
TR a[138:139],v233 offset:4096
v_cmp_lt_f32 vcc, s71, v249
MM v[68:71],a[212:215],v[176:179],v[68:71]
TR a[140:141],v233 offset:8192
TR a[142:143],v233 offset:12288
MM v[68:71],a[208:211],v[160:163],v[68:71]
s_cbranch_vccnz LSITE28_%=
LBACK28_%=:
MM v[76:79],a[208:211],v[164:167],v[132:135]
EX v64,v64
MM v[76:79],a[212:215],v[180:183],v[76:79]
EX v65,v65
MM v[76:79],a[216:219],v[196:199],v[76:79]
TR a[144:145],v232 offset:512
CV v64,v64,v65
MM v[76:79],a[220:223],v[212:215],v[76:79]
EX v66,v66
MM v[72:75],a[204:207],v[212:215],v[132:135]
EX v67,v67
MM v[72:75],a[200:203],v[196:199],v[72:75]
TR a[146:147],v232 offset:4608
CV v65,v66,v67
MM v[72:75],a[196:199],v[180:183],v[72:75]
EX v68,v68
MM v[72:75],a[192:195],v[164:167],v[72:75]
TR a[148:149],v232 offset:8704
TR a[150:151],v232 offset:12800
MM v[80:83],a[192:195],v[168:171],v[136:139]
EX v69,v69
MM v[80:83],a[196:199],v[184:187],v[80:83]
TR a[152:153],v233 offset:512
CV v66,v68,v69
MM v[80:83],a[200:203],v[200:203],v[80:83]
EX v70,v70
MM v[80:83],a[204:207],v[216:219],v[80:83]
TR a[154:155],v233 offset:4608
TR a[156:157],v233 offset:8704
MM v[84:87],a[220:223],v[216:219],v[136:139]
)ASM"
R"ASM(
v_lshlrev_b32 v32, 3, v248
v_min_u32 v32, 4992, v32
MM v[84:87],a[216:219],v[200:203],v[84:87]
s_sub_u32 s91, 5242880, s26
s_sub_u32 s60, s24, s91
s_subb_u32 s61, s25, 0
MM v[84:87],a[212:215],v[184:187],v[84:87]
s_mov_b32 s62, 5242880
EX v71,v71
MM v[84:87],a[208:211],v[168:171],v[84:87]
s_mov_b32 s63, 131072
TR a[158:159],v233 offset:12800
CV v67,v70,v71
MM v[92:95],a[208:211],v[172:175],v[140:143]
s_lshl_b32 s90, s16, 2
EX v72,v72
MM v[92:95],a[212:215],v[188:191],v[92:95]
s_add_u32 s90, s90, %[swave]
EX v73,v73
MM v[92:95],a[216:219],v[204:207],v[92:95]
s_mul_i32 s90, s90, 10240
TR a[160:161],v232 offset:1024
CV v72,v72,v73
MM v[92:95],a[220:223],v[220:223],v[92:95]
s_add_u32 s90, s90, 5120
EX v74,v74
MM v[88:91],a[204:207],v[220:223],v[140:143]
s_add_u32 s91, %[slds], 135168
s_mov_b32 m0, s91
s_nop 0
buffer_load_dword v32, s[60:63], s90 offen offset:0 lds
MM v[88:91],a[200:203],v[204:207],v[88:91]
EX v75,v75
MM v[88:91],a[196:199],v[188:191],v[88:91]
TR a[162:163],v232 offset:5120
CV v73,v74,v75
MM v[88:91],a[192:195],v[172:175],v[88:91]
EX v76,v76
MM v[96:99],a[224:227],v[160:163],v[128:131]
TR a[164:165],v232 offset:9216
TR a[166:167],v232 offset:13312
MM v[96:99],a[228:231],v[176:179],v[96:99]
EX v77,v77
MM v[96:99],a[232:235],v[192:195],v[96:99]
TR a[168:169],v233 offset:1024
CV v74,v76,v77
MM v[96:99],a[236:239],v[208:211],v[96:99]
EX v78,v78
MM v[100:103],a[252:255],v[208:211],v[128:131]
EX v79,v79
MM v[100:103],a[248:251],v[192:195],v[100:103]
TR a[170:171],v233 offset:5120
CV v75,v78,v79
MM v[100:103],a[244:247],v[176:179],v[100:103]
EX v80,v80
MM v[100:103],a[240:243],v[160:163],v[100:103]
TR a[172:173],v233 offset:9216
TR a[174:175],v233 offset:13312
MM v[108:111],a[240:243],v[164:167],v[132:135]
s_add_u32 s67, s95, 5120
EX v81,v81
MM v[108:111],a[244:247],v[180:183],v[108:111]
s_mov_b32 s70, 1
TR a[176:177],v232 offset:1536
CV v80,v80,v81
MM v[108:111],a[248:251],v[196:199],v[108:111]
EX v82,v82
MM v[108:111],a[252:255],v[212:215],v[108:111]
EX v83,v83
MM v[104:107],a[236:239],v[212:215],v[132:135]
TR a[178:179],v232 offset:5632
CV v81,v82,v83
MM v[104:107],a[232:235],v[196:199],v[104:107]
EX v84,v84
MM v[104:107],a[228:231],v[180:183],v[104:107]
TR a[180:181],v232 offset:9728
TR a[182:183],v232 offset:13824
MM v[104:107],a[224:227],v[164:167],v[104:107]
EX v85,v85
MM v[112:115],a[224:227],v[168:171],v[136:139]
TR a[184:185],v233 offset:1536
CV v82,v84,v85
MM v[112:115],a[228:231],v[184:187],v[112:115]
EX v86,v86
MM v[112:115],a[232:235],v[200:203],v[112:115]
EX v87,v87
MM v[112:115],a[236:239],v[216:219],v[112:115]
TR a[186:187],v233 offset:5632
CV v83,v86,v87
MM v[116:119],a[252:255],v[216:219],v[136:139]
EX v88,v88
MM v[116:119],a[248:251],v[200:203],v[116:119]
TR a[188:189],v233 offset:9728
TR a[190:191],v233 offset:13824
MM v[116:119],a[244:247],v[184:187],v[116:119]
EX v89,v89
MM v[116:119],a[240:243],v[168:171],v[116:119]
EX v90,v90
MM v[124:127],a[240:243],v[172:175],v[140:143]
EX v91,v91
MM v[124:127],a[244:247],v[188:191],v[124:127]
EX v92,v92
MM v[124:127],a[248:251],v[204:207],v[124:127]
EX v93,v93
MM v[124:127],a[252:255],v[220:223],v[124:127]
EX v94,v94
MM v[120:123],a[236:239],v[220:223],v[140:143]
EX v95,v95
MM v[120:123],a[232:235],v[204:207],v[120:123]
EX v96,v96
MM v[120:123],a[228:231],v[188:191],v[120:123]
EX v97,v97
MM v[120:123],a[224:227],v[172:175],v[120:123]
s_waitcnt vmcnt(14) lgkmcnt(0)
s_barrier
MM a[0:3],a[128:131],v[0:3],a[0:3]
s_add_u32 m0, s65, 0
CV v88,v88,v89
CV v89,v90,v91
MM a[0:3],a[132:135],v[4:7],a[0:3]
RD a[192:195],v224 offset:32768
CV v90,v92,v93
MM a[8:11],a[132:135],v[12:15],a[8:11]
RD a[196:199],v225 offset:32768
CV v91,v94,v95
MM a[8:11],a[128:131],v[8:11],a[8:11]
buffer_load_dwordx4 v228, s[8:11], s64 offen offset:0 lds
MM a[16:19],a[128:131],v[16:19],a[16:19]
RD a[200:203],v226 offset:32768
CV v68,v96,v97
MM a[16:19],a[132:135],v[20:23],a[16:19]
buffer_load_dwordx4 v229, s[8:11], s64 offen offset:1024 lds
MM a[24:27],a[132:135],v[28:31],a[24:27]
EX v98,v98
MM a[24:27],a[128:131],v[24:27],a[24:27]
RD a[204:207],v227 offset:32768
RD a[208:211],v224 offset:36864
MM a[28:31],a[136:139],v[24:27],a[28:31]
EX v99,v99
MM a[28:31],a[140:143],v[28:31],a[28:31]
RD a[212:215],v225 offset:36864
CV v69,v98,v99
MM a[20:23],a[140:143],v[20:23],a[20:23]
EX v100,v100
MM a[20:23],a[136:139],v[16:19],a[20:23]
EX v101,v101
MM a[12:15],a[136:139],v[8:11],a[12:15]
RD a[216:219],v226 offset:36864
CV v70,v100,v101
MM a[12:15],a[140:143],v[12:15],a[12:15]
buffer_load_dwordx4 v230, s[8:11], s64 offen offset:2048 lds
MM a[4:7],a[140:143],v[4:7],a[4:7]
EX v102,v102
MM a[4:7],a[136:139],v[0:3],a[4:7]
EX v103,v103
MM a[32:35],a[144:147],v[0:3],a[32:35]
RD a[220:223],v227 offset:36864
CV v71,v102,v103
MM a[32:35],a[148:151],v[4:7],a[32:35]
EX v104,v104
MM a[40:43],a[148:151],v[12:15],a[40:43]
EX v105,v105
MM a[40:43],a[144:147],v[8:11],a[40:43]
RD a[224:227],v224 offset:40960
CV v76,v104,v105
MM a[48:51],a[144:147],v[16:19],a[48:51]
buffer_load_dwordx4 v231, s[8:11], s64 offen offset:3072 lds
MM a[48:51],a[148:151],v[20:23],a[48:51]
s_add_u32 m0, s65, 65536
EX v106,v106
MM a[56:59],a[148:151],v[28:31],a[56:59]
EX v107,v107
MM a[56:59],a[144:147],v[24:27],a[56:59]
RD a[228:231],v225 offset:40960
CV v77,v106,v107
MM a[60:63],a[152:155],v[24:27],a[60:63]
EX v108,v108
MM a[60:63],a[156:159],v[28:31],a[60:63]
buffer_load_dwordx4 v235, s[12:15], s81 offen offset:0 lds
MM a[52:55],a[156:159],v[20:23],a[52:55]
EX v109,v109
MM a[52:55],a[152:155],v[16:19],a[52:55]
RD a[232:235],v226 offset:40960
CV v78,v108,v109
MM a[44:47],a[152:155],v[8:11],a[44:47]
EX v110,v110
MM a[44:47],a[156:159],v[12:15],a[44:47]
EX v111,v111
MM a[36:39],a[156:159],v[4:7],a[36:39]
RD a[236:239],v227 offset:40960
CV v79,v110,v111
MM a[36:39],a[152:155],v[0:3],a[36:39]
EX v112,v112
MM a[64:67],a[160:163],v[0:3],a[64:67]
buffer_load_dwordx4 v235, s[12:15], s64 offen offset:1024 lds
MM a[64:67],a[164:167],v[4:7],a[64:67]
EX v113,v113
MM a[72:75],a[164:167],v[12:15],a[72:75]
RD a[240:243],v224 offset:45056
CV v84,v112,v113
MM a[72:75],a[160:163],v[8:11],a[72:75]
EX v114,v114
MM a[80:83],a[160:163],v[16:19],a[80:83]
EX v115,v115
MM a[80:83],a[164:167],v[20:23],a[80:83]
RD a[244:247],v225 offset:45056
CV v85,v114,v115
MM a[88:91],a[164:167],v[28:31],a[88:91]
EX v116,v116
MM a[88:91],a[160:163],v[24:27],a[88:91]
buffer_load_dwordx4 v235, s[12:15], s81 offen offset:2048 lds
MM a[92:95],a[168:171],v[24:27],a[92:95]
EX v117,v117
MM a[92:95],a[172:175],v[28:31],a[92:95]
RD a[248:251],v226 offset:45056
CV v86,v116,v117
MM a[84:87],a[172:175],v[20:23],a[84:87]
EX v118,v118
MM a[84:87],a[168:171],v[16:19],a[84:87]
EX v119,v119
MM a[76:79],a[168:171],v[8:11],a[76:79]
RD a[252:255],v227 offset:45056
CV v87,v118,v119
MM a[76:79],a[172:175],v[12:15],a[76:79]
EX v120,v120
MM a[68:71],a[172:175],v[4:7],a[68:71]
buffer_load_dwordx4 v235, s[12:15], s64 offen offset:3072 lds
MM a[68:71],a[168:171],v[0:3],a[68:71]
EX v121,v121
MM a[96:99],a[176:179],v[0:3],a[96:99]
EX v122,v122
MM a[96:99],a[180:183],v[4:7],a[96:99]
EX v123,v123
MM a[104:107],a[180:183],v[12:15],a[104:107]
EX v124,v124
MM a[104:107],a[176:179],v[8:11],a[104:107]
EX v125,v125
MM a[112:115],a[176:179],v[16:19],a[112:115]
EX v126,v126
MM a[112:115],a[180:183],v[20:23],a[112:115]
EX v127,v127
MM a[120:123],a[180:183],v[28:31],a[120:123]
s_lshl_b32 s91, s47, 2
CV v92,v120,v121
CV v93,v122,v123
MM a[120:123],a[176:179],v[24:27],a[120:123]
s_add_u32 s91, s91, %[swave]
CV v94,v124,v125
CV v95,v126,v127
MM a[124:127],a[184:187],v[24:27],a[124:127]
s_lshl_b32 s91, s91, 2
v_add_f32 v244, v144, v145
v_add_f32 v245, v148, v149
MM a[124:127],a[188:191],v[28:31],a[124:127]
s_add_u32 s91, s91, 73728
v_add_f32 v246, v152, v153
v_add_f32 v247, v156, v157
MM a[116:119],a[188:191],v[20:23],a[116:119]
s_waitcnt vmcnt(12)
v_mov_b32 v250, s69
s_mov_b64 exec, 1
buffer_store_dword v250, off, s[20:23], s91 offset:0 sc1
s_mov_b64 exec, -1
MM a[116:119],a[184:187],v[16:19],a[116:119]
MM a[108:111],a[184:187],v[8:11],a[108:111]
MM a[108:111],a[188:191],v[12:15],a[108:111]
MM a[100:103],a[188:191],v[4:7],a[100:103]
MM a[100:103],a[184:187],v[0:3],a[100:103]
v_smfmac_f32_16x16x64_bf16 v[144:147], v[236:239], v[64:71], v234
v_smfmac_f32_16x16x64_bf16 v[148:151], v[236:239], v[72:79], v234
v_smfmac_f32_16x16x64_bf16 v[152:155], v[236:239], v[80:87], v234
v_smfmac_f32_16x16x64_bf16 v[156:159], v[236:239], v[88:95], v234
s_waitcnt lgkmcnt(0)
s_cmp_ge_u32 s16, 20
s_cbranch_scc1 LLAP_%=
MM v[0:3],a[192:195],v[160:163],v[128:131]
s_add_u32 s64, s64, 16384
TR a[128:129],v232 offset:16384
s_add_u32 s81, s64, 896
MM v[0:3],a[196:199],v[176:179],v[0:3]
TR a[130:131],v232 offset:20480
v_max3_f32 v249, v144, v145, v148
MM v[0:3],a[200:203],v[192:195],v[0:3]
TR a[132:133],v232 offset:24576
v_max3_f32 v249, v249, v149, v152
MM v[0:3],a[204:207],v[208:211],v[0:3]
TR a[134:135],v232 offset:28672
v_max3_f32 v249, v249, v153, v156
MM v[4:7],a[220:223],v[208:211],v[128:131]
TR a[136:137],v233 offset:16384
v_max_f32 v249, v249, v157
MM v[4:7],a[216:219],v[192:195],v[4:7]
TR a[138:139],v233 offset:20480
v_cmp_lt_f32 vcc, s71, v249
MM v[4:7],a[212:215],v[176:179],v[4:7]
TR a[140:141],v233 offset:24576
TR a[142:143],v233 offset:28672
MM v[4:7],a[208:211],v[160:163],v[4:7]
s_cbranch_vccnz LSITE29_%=
LBACK29_%=:
MM v[12:15],a[208:211],v[164:167],v[132:135]
EX v0,v0
MM v[12:15],a[212:215],v[180:183],v[12:15]
EX v1,v1
MM v[12:15],a[216:219],v[196:199],v[12:15]
TR a[144:145],v232 offset:16896
CV v0,v0,v1
MM v[12:15],a[220:223],v[212:215],v[12:15]
EX v2,v2
MM v[8:11],a[204:207],v[212:215],v[132:135]
EX v3,v3
MM v[8:11],a[200:203],v[196:199],v[8:11]
TR a[146:147],v232 offset:20992
CV v1,v2,v3
MM v[8:11],a[196:199],v[180:183],v[8:11]
EX v4,v4
MM v[8:11],a[192:195],v[164:167],v[8:11]
TR a[148:149],v232 offset:25088
TR a[150:151],v232 offset:29184
MM v[16:19],a[192:195],v[168:171],v[136:139]
EX v5,v5
MM v[16:19],a[196:199],v[184:187],v[16:19]
TR a[152:153],v233 offset:16896
CV v2,v4,v5
MM v[16:19],a[200:203],v[200:203],v[16:19]
EX v6,v6
MM v[16:19],a[204:207],v[216:219],v[16:19]
TR a[154:155],v233 offset:20992
TR a[156:157],v233 offset:25088
MM v[20:23],a[220:223],v[216:219],v[136:139]
s_add_u32 s87, s95, 5120
buffer_store_dword v243, v248, s[20:23], s87 offen offset:12 sc1
MM v[20:23],a[216:219],v[200:203],v[20:23]
EX v7,v7
MM v[20:23],a[212:215],v[184:187],v[20:23]
TR a[158:159],v233 offset:29184
CV v3,v6,v7
MM v[20:23],a[208:211],v[168:171],v[20:23]
EX v8,v8
MM v[28:31],a[208:211],v[172:175],v[140:143]
EX v9,v9
MM v[28:31],a[212:215],v[188:191],v[28:31]
TR a[160:161],v232 offset:17408
CV v8,v8,v9
MM v[28:31],a[216:219],v[204:207],v[28:31]
EX v10,v10
MM v[28:31],a[220:223],v[220:223],v[28:31]
EX v11,v11
MM v[24:27],a[204:207],v[220:223],v[140:143]
TR a[162:163],v232 offset:21504
CV v9,v10,v11
MM v[24:27],a[200:203],v[204:207],v[24:27]
TR a[164:165],v232 offset:25600
TR a[166:167],v232 offset:29696
MM v[24:27],a[196:199],v[188:191],v[24:27]
EX v12,v12
MM v[24:27],a[192:195],v[172:175],v[24:27]
EX v13,v13
MM v[32:35],a[224:227],v[160:163],v[128:131]
TR a[168:169],v233 offset:17408
CV v10,v12,v13
MM v[32:35],a[228:231],v[176:179],v[32:35]
EX v14,v14
MM v[32:35],a[232:235],v[192:195],v[32:35]
TR a[170:171],v233 offset:21504
TR a[172:173],v233 offset:25600
MM v[32:35],a[236:239],v[208:211],v[32:35]
EX v15,v15
MM v[36:39],a[252:255],v[208:211],v[128:131]
TR a[174:175],v233 offset:29696
CV v11,v14,v15
MM v[36:39],a[248:251],v[192:195],v[36:39]
EX v16,v16
MM v[36:39],a[244:247],v[176:179],v[36:39]
EX v17,v17
MM v[36:39],a[240:243],v[160:163],v[36:39]
TR a[176:177],v232 offset:17920
CV v16,v16,v17
MM v[44:47],a[240:243],v[164:167],v[132:135]
EX v18,v18
MM v[44:47],a[244:247],v[180:183],v[44:47]
TR a[178:179],v232 offset:22016
TR a[180:181],v232 offset:26112
MM v[44:47],a[248:251],v[196:199],v[44:47]
EX v19,v19
MM v[44:47],a[252:255],v[212:215],v[44:47]
s_nop 0
LPG21_%=:
s_cbranch_execz LTR4_%=
TR a[182:183],v232 offset:30208
CV v17,v18,v19
MM v[40:43],a[236:239],v[212:215],v[132:135]
EX v20,v20
MM v[40:43],a[232:235],v[196:199],v[40:43]
)ASM"
R"ASM(
EX v21,v21
MM v[40:43],a[228:231],v[180:183],v[40:43]
TR a[184:185],v233 offset:17920
CV v18,v20,v21
MM v[40:43],a[224:227],v[164:167],v[40:43]
EX v22,v22
MM v[48:51],a[224:227],v[168:171],v[136:139]
TR a[186:187],v233 offset:22016
TR a[188:189],v233 offset:26112
MM v[48:51],a[228:231],v[184:187],v[48:51]
EX v23,v23
MM v[48:51],a[232:235],v[200:203],v[48:51]
TR a[190:191],v233 offset:30208
CV v19,v22,v23
MM v[48:51],a[236:239],v[216:219],v[48:51]
EX v24,v24
MM v[52:55],a[252:255],v[216:219],v[136:139]
EX v25,v25
MM v[52:55],a[248:251],v[200:203],v[52:55]
EX v26,v26
MM v[52:55],a[244:247],v[184:187],v[52:55]
EX v27,v27
MM v[52:55],a[240:243],v[168:171],v[52:55]
EX v28,v28
MM v[60:63],a[240:243],v[172:175],v[140:143]
EX v29,v29
MM v[60:63],a[244:247],v[188:191],v[60:63]
EX v30,v30
MM v[60:63],a[248:251],v[204:207],v[60:63]
EX v31,v31
MM v[60:63],a[252:255],v[220:223],v[60:63]
EX v32,v32
MM v[56:59],a[236:239],v[220:223],v[140:143]
EX v33,v33
MM v[56:59],a[232:235],v[204:207],v[56:59]
EX v34,v34
MM v[56:59],a[228:231],v[188:191],v[56:59]
EX v35,v35
MM v[56:59],a[224:227],v[172:175],v[56:59]
s_waitcnt lgkmcnt(0)
s_barrier
MM a[0:3],a[128:131],v[64:67],a[0:3]
s_add_u32 m0, s65, 16384
CV v24,v24,v25
CV v25,v26,v27
MM a[0:3],a[132:135],v[68:71],a[0:3]
RD a[192:195],v224 offset:49152
CV v26,v28,v29
MM a[8:11],a[132:135],v[76:79],a[8:11]
RD a[196:199],v225 offset:49152
CV v27,v30,v31
MM a[8:11],a[128:131],v[72:75],a[8:11]
buffer_load_dwordx4 v228, s[8:11], s64 offen offset:0 lds
MM a[16:19],a[128:131],v[80:83],a[16:19]
RD a[200:203],v226 offset:49152
CV v4,v32,v33
MM a[16:19],a[132:135],v[84:87],a[16:19]
RD a[204:207],v227 offset:49152
CV v5,v34,v35
MM a[24:27],a[132:135],v[92:95],a[24:27]
buffer_load_dwordx4 v229, s[8:11], s64 offen offset:1024 lds
MM a[24:27],a[128:131],v[88:91],a[24:27]
RD a[208:211],v224 offset:53248
RD a[212:215],v225 offset:53248
MM a[28:31],a[136:139],v[88:91],a[28:31]
buffer_load_dwordx4 v230, s[8:11], s64 offen offset:2048 lds
MM a[28:31],a[140:143],v[92:95],a[28:31]
EX v36,v36
MM a[20:23],a[140:143],v[84:87],a[20:23]
EX v37,v37
MM a[20:23],a[136:139],v[80:83],a[20:23]
RD a[216:219],v226 offset:53248
CV v6,v36,v37
MM a[12:15],a[136:139],v[72:75],a[12:15]
EX v38,v38
MM a[12:15],a[140:143],v[76:79],a[12:15]
RD a[220:223],v227 offset:53248
RD a[224:227],v224 offset:57344
MM a[4:7],a[140:143],v[68:71],a[4:7]
EX v39,v39
MM a[4:7],a[136:139],v[64:67],a[4:7]
RD a[228:231],v225 offset:57344
CV v7,v38,v39
MM a[32:35],a[144:147],v[64:67],a[32:35]
EX v40,v40
MM a[32:35],a[148:151],v[68:71],a[32:35]
buffer_load_dwordx4 v231, s[8:11], s64 offen offset:3072 lds
MM a[40:43],a[148:151],v[76:79],a[40:43]
s_add_u32 m0, s65, 81920
EX v41,v41
MM a[40:43],a[144:147],v[72:75],a[40:43]
RD a[232:235],v226 offset:57344
CV v12,v40,v41
MM a[48:51],a[144:147],v[80:83],a[48:51]
EX v42,v42
MM a[48:51],a[148:151],v[84:87],a[48:51]
EX v43,v43
MM a[56:59],a[148:151],v[92:95],a[56:59]
RD a[236:239],v227 offset:57344
CV v13,v42,v43
MM a[56:59],a[144:147],v[88:91],a[56:59]
EX v44,v44
MM a[60:63],a[152:155],v[88:91],a[60:63]
buffer_load_dwordx4 v235, s[12:15], s81 offen offset:0 lds
MM a[60:63],a[156:159],v[92:95],a[60:63]
EX v45,v45
MM a[52:55],a[156:159],v[84:87],a[52:55]
RD a[240:243],v224 offset:61440
CV v14,v44,v45
MM a[52:55],a[152:155],v[80:83],a[52:55]
EX v46,v46
MM a[44:47],a[152:155],v[72:75],a[44:47]
EX v47,v47
MM a[44:47],a[156:159],v[76:79],a[44:47]
RD a[244:247],v225 offset:61440
CV v15,v46,v47
MM a[36:39],a[156:159],v[68:71],a[36:39]
buffer_load_dwordx4 v235, s[12:15], s64 offen offset:1024 lds
MM a[36:39],a[152:155],v[64:67],a[36:39]
EX v48,v48
MM a[64:67],a[160:163],v[64:67],a[64:67]
EX v49,v49
MM a[64:67],a[164:167],v[68:71],a[64:67]
RD a[248:251],v226 offset:61440
CV v20,v48,v49
MM a[72:75],a[164:167],v[76:79],a[72:75]
EX v50,v50
MM a[72:75],a[160:163],v[72:75],a[72:75]
EX v51,v51
MM a[80:83],a[160:163],v[80:83],a[80:83]
RD a[252:255],v227 offset:61440
CV v21,v50,v51
MM a[80:83],a[164:167],v[84:87],a[80:83]
buffer_load_dwordx4 v235, s[12:15], s81 offen offset:2048 lds
MM a[88:91],a[164:167],v[92:95],a[88:91]
EX v52,v52
MM a[88:91],a[160:163],v[88:91],a[88:91]
EX v53,v53
MM a[92:95],a[168:171],v[88:91],a[92:95]
EX v54,v54
MM a[92:95],a[172:175],v[92:95],a[92:95]
buffer_load_dwordx4 v235, s[12:15], s64 offen offset:3072 lds
MM a[84:87],a[172:175],v[84:87],a[84:87]
EX v55,v55
MM a[84:87],a[168:171],v[80:83],a[84:87]
EX v56,v56
MM a[76:79],a[168:171],v[72:75],a[76:79]
EX v57,v57
MM a[76:79],a[172:175],v[76:79],a[76:79]
EX v58,v58
MM a[68:71],a[172:175],v[68:71],a[68:71]
EX v59,v59
MM a[68:71],a[168:171],v[64:67],a[68:71]
EX v60,v60
MM a[96:99],a[176:179],v[64:67],a[96:99]
EX v61,v61
MM a[96:99],a[180:183],v[68:71],a[96:99]
EX v62,v62
MM a[104:107],a[180:183],v[76:79],a[104:107]
EX v63,v63
MM a[104:107],a[176:179],v[72:75],a[104:107]
CV v22,v52,v53
CV v23,v54,v55
MM a[112:115],a[176:179],v[80:83],a[112:115]
CV v28,v56,v57
CV v29,v58,v59
MM a[112:115],a[180:183],v[84:87],a[112:115]
CV v30,v60,v61
CV v31,v62,v63
MM a[120:123],a[180:183],v[92:95],a[120:123]
MM a[120:123],a[176:179],v[88:91],a[120:123]
MM a[124:127],a[184:187],v[88:91],a[124:127]
v_add_f32 v244, v144, v145
v_add_f32 v245, v148, v149
MM a[124:127],a[188:191],v[92:95],a[124:127]
v_add_f32 v246, v152, v153
v_add_f32 v247, v156, v157
MM a[116:119],a[188:191],v[84:87],a[116:119]
MM a[116:119],a[184:187],v[80:83],a[116:119]
MM a[108:111],a[184:187],v[72:75],a[108:111]
MM a[108:111],a[188:191],v[76:79],a[108:111]
MM a[100:103],a[188:191],v[68:71],a[100:103]
MM a[100:103],a[184:187],v[64:67],a[100:103]
v_smfmac_f32_16x16x64_bf16 v[144:147], v[236:239], v[0:7], v234
v_smfmac_f32_16x16x64_bf16 v[148:151], v[236:239], v[8:15], v234
v_smfmac_f32_16x16x64_bf16 v[152:155], v[236:239], v[16:23], v234
v_smfmac_f32_16x16x64_bf16 v[156:159], v[236:239], v[24:31], v234
s_waitcnt lgkmcnt(0)
MM v[64:67],a[192:195],v[160:163],v[128:131]
s_add_u32 s64, s64, 16384
TR a[128:129],v232 offset:32768
s_add_u32 s81, s64, 896
MM v[64:67],a[196:199],v[176:179],v[64:67]
TR a[130:131],v232 offset:36864
v_max3_f32 v249, v144, v145, v148
MM v[64:67],a[200:203],v[192:195],v[64:67]
TR a[132:133],v232 offset:40960
v_max3_f32 v249, v249, v149, v152
MM v[64:67],a[204:207],v[208:211],v[64:67]
TR a[134:135],v232 offset:45056
v_max3_f32 v249, v249, v153, v156
MM v[68:71],a[220:223],v[208:211],v[128:131]
TR a[136:137],v233 offset:32768
v_max_f32 v249, v249, v157
MM v[68:71],a[216:219],v[192:195],v[68:71]
TR a[138:139],v233 offset:36864
v_cmp_lt_f32 vcc, s71, v249
MM v[68:71],a[212:215],v[176:179],v[68:71]
TR a[140:141],v233 offset:40960
TR a[142:143],v233 offset:45056
MM v[68:71],a[208:211],v[160:163],v[68:71]
s_cbranch_vccnz LSITE30_%=
LBACK30_%=:
MM v[76:79],a[208:211],v[164:167],v[132:135]
s_and_b32 s90, s17, 63
s_cmp_lt_u32 s17, 80
s_cselect_b32 s17, s17, s90
s_cselect_b64 s[44:45], -1, 0
MM v[76:79],a[212:215],v[180:183],v[76:79]
v_lshrrev_b32 v253, 4, v248
v_min_u32 v253, 35, v253
MM v[76:79],a[216:219],v[196:199],v[76:79]
v_lshrrev_b32 v252, 2, v253
v_mul_u32_u24 v252, 144, v252
MM v[76:79],a[220:223],v[212:215],v[76:79]
TR a[144:145],v232 offset:33280
v_lshl_add_u32 v253, v253, 2, v252
MM v[72:75],a[204:207],v[212:215],v[132:135]
s_lshr_b32 s90, s17, 3
EX v64,v64
MM v[72:75],a[200:203],v[196:199],v[72:75]
s_lshl_b32 s76, s90, 4
EX v65,v65
MM v[72:75],a[196:199],v[180:183],v[72:75]
s_add_u32 s76, s76, 73728
buffer_load_dword v252, v253, s[20:23], s76 offen offset:0 sc1
MM v[72:75],a[192:195],v[164:167],v[72:75]
EX v66,v66
MM v[80:83],a[192:195],v[168:171],v[136:139]
EX v67,v67
MM v[80:83],a[196:199],v[184:187],v[80:83]
EX v68,v68
MM v[80:83],a[200:203],v[200:203],v[80:83]
TR a[146:147],v232 offset:37376
CV v64,v64,v65
MM v[80:83],a[204:207],v[216:219],v[80:83]
EX v69,v69
MM v[84:87],a[220:223],v[216:219],v[136:139]
TR a[148:149],v232 offset:41472
CV v65,v66,v67
MM v[84:87],a[216:219],v[200:203],v[84:87]
EX v70,v70
MM v[84:87],a[212:215],v[184:187],v[84:87]
TR a[150:151],v232 offset:45568
CV v66,v68,v69
MM v[84:87],a[208:211],v[168:171],v[84:87]
EX v71,v71
MM v[92:95],a[208:211],v[172:175],v[140:143]
TR a[152:153],v233 offset:33280
TR a[154:155],v233 offset:37376
MM v[92:95],a[212:215],v[188:191],v[92:95]
EX v72,v72
MM v[92:95],a[216:219],v[204:207],v[92:95]
EX v73,v73
MM v[92:95],a[220:223],v[220:223],v[92:95]
TR a[156:157],v233 offset:41472
CV v67,v70,v71
MM v[88:91],a[204:207],v[220:223],v[140:143]
EX v74,v74
MM v[88:91],a[200:203],v[204:207],v[88:91]
TR a[158:159],v233 offset:45568
CV v72,v72,v73
MM v[88:91],a[196:199],v[188:191],v[88:91]
EX v75,v75
MM v[88:91],a[192:195],v[172:175],v[88:91]
TR a[160:161],v232 offset:33792
TR a[162:163],v232 offset:37888
MM v[96:99],a[224:227],v[160:163],v[128:131]
EX v76,v76
MM v[96:99],a[228:231],v[176:179],v[96:99]
TR a[164:165],v232 offset:41984
CV v73,v74,v75
MM v[96:99],a[232:235],v[192:195],v[96:99]
EX v77,v77
MM v[96:99],a[236:239],v[208:211],v[96:99]
EX v78,v78
MM v[100:103],a[252:255],v[208:211],v[128:131]
TR a[166:167],v232 offset:46080
CV v74,v76,v77
MM v[100:103],a[248:251],v[192:195],v[100:103]
EX v79,v79
MM v[100:103],a[244:247],v[176:179],v[100:103]
TR a[168:169],v233 offset:33792
CV v75,v78,v79
MM v[100:103],a[240:243],v[160:163],v[100:103]
EX v80,v80
MM v[108:111],a[240:243],v[164:167],v[132:135]
TR a[170:171],v233 offset:37888
TR a[172:173],v233 offset:41984
MM v[108:111],a[244:247],v[180:183],v[108:111]
EX v81,v81
MM v[108:111],a[248:251],v[196:199],v[108:111]
TR a[174:175],v233 offset:46080
CV v80,v80,v81
MM v[108:111],a[252:255],v[212:215],v[108:111]
EX v82,v82
MM v[104:107],a[236:239],v[212:215],v[132:135]
EX v83,v83
MM v[104:107],a[232:235],v[196:199],v[104:107]
TR a[176:177],v232 offset:34304
TR a[178:179],v232 offset:38400
MM v[104:107],a[228:231],v[180:183],v[104:107]
EX v84,v84
MM v[104:107],a[224:227],v[164:167],v[104:107]
TR a[180:181],v232 offset:42496
CV v81,v82,v83
MM v[112:115],a[224:227],v[168:171],v[136:139]
EX v85,v85
MM v[112:115],a[228:231],v[184:187],v[112:115]
TR a[182:183],v232 offset:46592
CV v82,v84,v85
MM v[112:115],a[232:235],v[200:203],v[112:115]
EX v86,v86
MM v[112:115],a[236:239],v[216:219],v[112:115]
TR a[184:185],v233 offset:34304
TR a[186:187],v233 offset:38400
MM v[116:119],a[252:255],v[216:219],v[136:139]
EX v87,v87
MM v[116:119],a[248:251],v[200:203],v[116:119]
TR a[188:189],v233 offset:42496
CV v83,v86,v87
MM v[116:119],a[244:247],v[184:187],v[116:119]
EX v88,v88
MM v[116:119],a[240:243],v[168:171],v[116:119]
EX v89,v89
MM v[124:127],a[240:243],v[172:175],v[140:143]
TR a[190:191],v233 offset:46592
CV v88,v88,v89
MM v[124:127],a[244:247],v[188:191],v[124:127]
EX v90,v90
MM v[124:127],a[248:251],v[204:207],v[124:127]
EX v91,v91
MM v[124:127],a[252:255],v[220:223],v[124:127]
EX v92,v92
MM v[120:123],a[236:239],v[220:223],v[140:143]
EX v93,v93
MM v[120:123],a[232:235],v[204:207],v[120:123]
EX v94,v94
MM v[120:123],a[228:231],v[188:191],v[120:123]
EX v95,v95
MM v[120:123],a[224:227],v[172:175],v[120:123]
s_waitcnt vmcnt(15) lgkmcnt(0)
s_barrier
MM a[0:3],a[128:131],v[0:3],a[0:3]
s_add_u32 m0, s65, 32768
CV v89,v90,v91
CV v90,v92,v93
MM a[0:3],a[132:135],v[4:7],a[0:3]
RD a[192:195],v224 offset:0
CV v91,v94,v95
MM a[8:11],a[132:135],v[12:15],a[8:11]
buffer_load_dwordx4 v228, s[8:11], s64 offen offset:0 lds
MM a[8:11],a[128:131],v[8:11],a[8:11]
RD a[196:199],v225 offset:0
RD a[200:203],v226 offset:0
MM a[16:19],a[128:131],v[16:19],a[16:19]
EX v96,v96
MM a[16:19],a[132:135],v[20:23],a[16:19]
EX v97,v97
MM a[24:27],a[132:135],v[28:31],a[24:27]
RD a[204:207],v227 offset:0
CV v68,v96,v97
MM a[24:27],a[128:131],v[24:27],a[24:27]
EX v98,v98
MM a[28:31],a[136:139],v[24:27],a[28:31]
buffer_load_dwordx4 v229, s[8:11], s64 offen offset:1024 lds
MM a[28:31],a[140:143],v[28:31],a[28:31]
EX v99,v99
MM a[20:23],a[140:143],v[20:23],a[20:23]
RD a[208:211],v224 offset:4096
CV v69,v98,v99
MM a[20:23],a[136:139],v[16:19],a[20:23]
EX v100,v100
MM a[12:15],a[136:139],v[8:11],a[12:15]
EX v101,v101
MM a[12:15],a[140:143],v[12:15],a[12:15]
RD a[212:215],v225 offset:4096
CV v70,v100,v101
MM a[4:7],a[140:143],v[4:7],a[4:7]
)ASM"
R"ASM(
EX v102,v102
MM a[4:7],a[136:139],v[0:3],a[4:7]
buffer_load_dwordx4 v230, s[8:11], s64 offen offset:2048 lds
MM a[32:35],a[144:147],v[0:3],a[32:35]
EX v103,v103
MM a[32:35],a[148:151],v[4:7],a[32:35]
RD a[216:219],v226 offset:4096
CV v71,v102,v103
MM a[40:43],a[148:151],v[12:15],a[40:43]
EX v104,v104
MM a[40:43],a[144:147],v[8:11],a[40:43]
EX v105,v105
MM a[48:51],a[144:147],v[16:19],a[48:51]
RD a[220:223],v227 offset:4096
CV v76,v104,v105
MM a[48:51],a[148:151],v[20:23],a[48:51]
EX v106,v106
MM a[56:59],a[148:151],v[28:31],a[56:59]
buffer_load_dwordx4 v231, s[8:11], s64 offen offset:3072 lds
MM a[56:59],a[144:147],v[24:27],a[56:59]
s_add_u32 m0, s65, 98304
EX v107,v107
MM a[60:63],a[152:155],v[24:27],a[60:63]
RD a[224:227],v224 offset:8192
CV v77,v106,v107
MM a[60:63],a[156:159],v[28:31],a[60:63]
EX v108,v108
MM a[52:55],a[156:159],v[20:23],a[52:55]
EX v109,v109
MM a[52:55],a[152:155],v[16:19],a[52:55]
RD a[228:231],v225 offset:8192
CV v78,v108,v109
MM a[44:47],a[152:155],v[8:11],a[44:47]
EX v110,v110
MM a[44:47],a[156:159],v[12:15],a[44:47]
buffer_load_dwordx4 v235, s[12:15], s81 offen offset:0 lds
MM a[36:39],a[156:159],v[4:7],a[36:39]
EX v111,v111
MM a[36:39],a[152:155],v[0:3],a[36:39]
RD a[232:235],v226 offset:8192
CV v79,v110,v111
MM a[64:67],a[160:163],v[0:3],a[64:67]
EX v112,v112
MM a[64:67],a[164:167],v[4:7],a[64:67]
EX v113,v113
MM a[72:75],a[164:167],v[12:15],a[72:75]
RD a[236:239],v227 offset:8192
CV v84,v112,v113
MM a[72:75],a[160:163],v[8:11],a[72:75]
buffer_load_dwordx4 v235, s[12:15], s64 offen offset:1024 lds
MM a[80:83],a[160:163],v[16:19],a[80:83]
EX v114,v114
MM a[80:83],a[164:167],v[20:23],a[80:83]
EX v115,v115
MM a[88:91],a[164:167],v[28:31],a[88:91]
RD a[240:243],v224 offset:12288
CV v85,v114,v115
MM a[88:91],a[160:163],v[24:27],a[88:91]
EX v116,v116
MM a[92:95],a[168:171],v[24:27],a[92:95]
EX v117,v117
MM a[92:95],a[172:175],v[28:31],a[92:95]
RD a[244:247],v225 offset:12288
CV v86,v116,v117
MM a[84:87],a[172:175],v[20:23],a[84:87]
buffer_load_dwordx4 v235, s[12:15], s81 offen offset:2048 lds
MM a[84:87],a[168:171],v[16:19],a[84:87]
EX v118,v118
MM a[76:79],a[168:171],v[8:11],a[76:79]
EX v119,v119
MM a[76:79],a[172:175],v[12:15],a[76:79]
RD a[248:251],v226 offset:12288
CV v87,v118,v119
MM a[68:71],a[172:175],v[4:7],a[68:71]
EX v120,v120
MM a[68:71],a[168:171],v[0:3],a[68:71]
EX v121,v121
MM a[96:99],a[176:179],v[0:3],a[96:99]
RD a[252:255],v227 offset:12288
CV v92,v120,v121
MM a[96:99],a[180:183],v[4:7],a[96:99]
buffer_load_dwordx4 v235, s[12:15], s64 offen offset:3072 lds
MM a[104:107],a[180:183],v[12:15],a[104:107]
EX v122,v122
MM a[104:107],a[176:179],v[8:11],a[104:107]
EX v123,v123
MM a[112:115],a[176:179],v[16:19],a[112:115]
EX v124,v124
MM a[112:115],a[180:183],v[20:23],a[112:115]
EX v125,v125
MM a[120:123],a[180:183],v[28:31],a[120:123]
EX v126,v126
MM a[120:123],a[176:179],v[24:27],a[120:123]
EX v127,v127
MM a[124:127],a[184:187],v[24:27],a[124:127]
v_add_f32 v244, v144, v145
v_add_f32 v245, v148, v149
MM a[124:127],a[188:191],v[28:31],a[124:127]
v_add_f32 v246, v152, v153
v_add_f32 v247, v156, v157
MM a[116:119],a[188:191],v[20:23],a[116:119]
CV v93,v122,v123
CV v94,v124,v125
MM a[116:119],a[184:187],v[16:19],a[116:119]
CV v95,v126,v127
MM a[108:111],a[184:187],v[8:11],a[108:111]
MM a[108:111],a[188:191],v[12:15],a[108:111]
MM a[100:103],a[188:191],v[4:7],a[100:103]
MM a[100:103],a[184:187],v[0:3],a[100:103]
v_smfmac_f32_16x16x64_bf16 v[144:147], v[236:239], v[64:71], v234
v_smfmac_f32_16x16x64_bf16 v[148:151], v[236:239], v[72:79], v234
v_smfmac_f32_16x16x64_bf16 v[152:155], v[236:239], v[80:87], v234
v_smfmac_f32_16x16x64_bf16 v[156:159], v[236:239], v[88:95], v234
s_waitcnt lgkmcnt(0)
MM v[0:3],a[192:195],v[160:163],v[128:131]
s_add_u32 s64, s64, 16384
TR a[128:129],v232 offset:49152
s_add_u32 s81, s64, 896
MM v[0:3],a[196:199],v[176:179],v[0:3]
TR a[130:131],v232 offset:53248
v_max3_f32 v249, v144, v145, v148
MM v[0:3],a[200:203],v[192:195],v[0:3]
TR a[132:133],v232 offset:57344
v_max3_f32 v249, v249, v149, v152
MM v[0:3],a[204:207],v[208:211],v[0:3]
TR a[134:135],v232 offset:61440
v_max3_f32 v249, v249, v153, v156
MM v[4:7],a[220:223],v[208:211],v[128:131]
TR a[136:137],v233 offset:49152
v_max_f32 v249, v249, v157
MM v[4:7],a[216:219],v[192:195],v[4:7]
TR a[138:139],v233 offset:53248
v_cmp_lt_f32 vcc, s71, v249
MM v[4:7],a[212:215],v[176:179],v[4:7]
TR a[140:141],v233 offset:57344
TR a[142:143],v233 offset:61440
MM v[4:7],a[208:211],v[160:163],v[4:7]
s_cbranch_vccnz LSITE31_%=
LBACK31_%=:
MM v[12:15],a[208:211],v[164:167],v[132:135]
s_waitcnt vmcnt(8)
v_cmp_ne_u32 vcc, s69, v252
s_cbranch_vccnz LRFY_%=
LRFYb_%=:
MM v[12:15],a[212:215],v[180:183],v[12:15]
v_lshrrev_b32 v109, 2, v248
v_lshrrev_b32 v110, 3, v248
MM v[12:15],a[216:219],v[196:199],v[12:15]
s_and_b32 s76, s17, 7
EX v0,v0
MM v[12:15],a[220:223],v[212:215],v[12:15]
s_mul_i32 s76, s76, 2560
EX v1,v1
MM v[8:11],a[204:207],v[212:215],v[132:135]
s_lshl_b32 s91, s90, 8
EX v2,v2
MM v[8:11],a[200:203],v[196:199],v[8:11]
s_add_u32 s76, s76, s91
TR a[144:145],v232 offset:49664
CV v0,v0,v1
MM v[8:11],a[196:199],v[180:183],v[8:11]
s_add_u32 s76, s76, 21659648
buffer_load_dword v96, v109, s[20:23], s76 offen offset:0 sc1
MM v[8:11],a[192:195],v[164:167],v[8:11]
s_add_u32 s76, s76, 20480
EX v3,v3
MM v[16:19],a[192:195],v[168:171],v[136:139]
buffer_load_dword v97, v109, s[20:23], s76 offen offset:0 sc1
MM v[16:19],a[196:199],v[184:187],v[16:19]
s_add_u32 s76, s76, 20480
EX v4,v4
MM v[16:19],a[200:203],v[200:203],v[16:19]
buffer_load_dword v98, v109, s[20:23], s76 offen offset:0 sc1
MM v[16:19],a[204:207],v[216:219],v[16:19]
s_add_u32 s76, s76, 20480
EX v5,v5
MM v[20:23],a[220:223],v[216:219],v[136:139]
buffer_load_dword v99, v109, s[20:23], s76 offen offset:0 sc1
MM v[20:23],a[216:219],v[200:203],v[20:23]
s_add_u32 s76, s76, 20480
TR a[146:147],v232 offset:53760
TR a[148:149],v232 offset:57856
MM v[20:23],a[212:215],v[184:187],v[20:23]
buffer_load_dword v100, v109, s[20:23], s76 offen offset:0 sc1
MM v[20:23],a[208:211],v[168:171],v[20:23]
s_add_u32 s76, s76, 20480
EX v6,v6
MM v[28:31],a[208:211],v[172:175],v[140:143]
buffer_load_dword v101, v109, s[20:23], s76 offen offset:0 sc1
MM v[28:31],a[212:215],v[188:191],v[28:31]
s_add_u32 s76, s76, 20480
EX v7,v7
MM v[28:31],a[216:219],v[204:207],v[28:31]
buffer_load_dword v102, v109, s[20:23], s76 offen offset:0 sc1
MM v[28:31],a[220:223],v[220:223],v[28:31]
s_add_u32 s76, s76, 20480
s_nop 0
LPG22_%=:
s_cbranch_execz LTR4_%=
TR a[150:151],v232 offset:61952
EX v8,v8
MM v[24:27],a[204:207],v[220:223],v[140:143]
buffer_load_dword v103, v109, s[20:23], s76 offen offset:0 sc1
MM v[24:27],a[200:203],v[204:207],v[24:27]
s_add_u32 s76, s76, 20480
TR a[152:153],v233 offset:49664
EX v9,v9
CV v1,v2,v3
MM v[24:27],a[196:199],v[188:191],v[24:27]
buffer_load_dword v104, v109, s[20:23], s76 offen offset:0 sc1
TR a[154:155],v233 offset:53760
MM v[24:27],a[192:195],v[172:175],v[24:27]
s_and_b32 s76, s19, 65535
EX v10,v10
MM v[32:35],a[224:227],v[160:163],v[128:131]
s_sub_u32 s76, 65535, s76
TR a[156:157],v233 offset:57856
CV v2,v4,v5
EX v11,v11
MM v[32:35],a[228:231],v[176:179],v[32:35]
s_mul_i32 s76, s76, 1280
TR a[158:159],v233 offset:61952
CV v3,v6,v7
MM v[32:35],a[232:235],v[192:195],v[32:35]
s_lshl_b32 s91, s90, 7
EX v12,v12
MM v[32:35],a[236:239],v[208:211],v[32:35]
s_add_u32 s76, s76, s91
TR a[160:161],v232 offset:50176
TR a[162:163],v232 offset:54272
MM v[36:39],a[252:255],v[208:211],v[128:131]
s_add_u32 s76, s76, 131072
buffer_load_ushort v108, v110, s[20:23], s76 offen sc1
EX v13,v13
MM v[36:39],a[248:251],v[192:195],v[36:39]
s_ashr_i32 s91, s19, 31
EX v14,v14
MM v[36:39],a[244:247],v[176:179],v[36:39]
s_not_b32 s91, s91
TR a[164:165],v232 offset:58368
CV v8,v8,v9
MM v[36:39],a[240:243],v[160:163],v[36:39]
s_or_b32 s91, s91, 0x80000000
EX v15,v15
MM v[44:47],a[240:243],v[164:167],v[132:135]
s_xor_b32 s19, s19, s91
TR a[166:167],v232 offset:62464
CV v9,v10,v11
MM v[44:47],a[244:247],v[180:183],v[44:47]
s_and_b32 s19, s19, 0xffff0000
v_mov_b32 v111, 0xbfb8aa3b
v_mul_f32 v111, s19, v111
MM v[44:47],a[248:251],v[196:199],v[44:47]
EX v111,v111
EX v16,v16
MM v[44:47],a[252:255],v[212:215],v[44:47]
TR a[168:169],v233 offset:50176
v_add_f32 v111, 1.0, v111
EX v17,v17
MM v[40:43],a[236:239],v[212:215],v[132:135]
v_rcp_f32 v111, v111
MM v[40:43],a[232:235],v[196:199],v[40:43]
TR a[170:171],v233 offset:54272
CV v111,v111,v111
v_and_b32 v111, 0xffff0000, v111
EX v18,v18
MM v[40:43],a[228:231],v[180:183],v[40:43]
TR a[172:173],v233 offset:58368
CV v10,v12,v13
MM v[40:43],a[224:227],v[164:167],v[40:43]
EX v19,v19
MM v[48:51],a[224:227],v[168:171],v[136:139]
TR a[174:175],v233 offset:62464
CV v11,v14,v15
MM v[48:51],a[228:231],v[184:187],v[48:51]
EX v20,v20
MM v[48:51],a[232:235],v[200:203],v[48:51]
TR a[176:177],v232 offset:50688
TR a[178:179],v232 offset:54784
MM v[48:51],a[236:239],v[216:219],v[48:51]
EX v21,v21
MM v[52:55],a[252:255],v[216:219],v[136:139]
EX v22,v22
MM v[52:55],a[248:251],v[200:203],v[52:55]
TR a[180:181],v232 offset:58880
CV v16,v16,v17
MM v[52:55],a[244:247],v[184:187],v[52:55]
EX v23,v23
MM v[52:55],a[240:243],v[168:171],v[52:55]
TR a[182:183],v232 offset:62976
CV v17,v18,v19
MM v[60:63],a[240:243],v[172:175],v[140:143]
EX v24,v24
MM v[60:63],a[244:247],v[188:191],v[60:63]
TR a[184:185],v233 offset:50688
TR a[186:187],v233 offset:54784
MM v[60:63],a[248:251],v[204:207],v[60:63]
TR a[188:189],v233 offset:58880
TR a[190:191],v233 offset:62976
EX v25,v25
MM v[60:63],a[252:255],v[220:223],v[60:63]
EX v26,v26
MM v[56:59],a[236:239],v[220:223],v[140:143]
EX v27,v27
MM v[56:59],a[232:235],v[204:207],v[56:59]
EX v28,v28
MM v[56:59],a[228:231],v[188:191],v[56:59]
EX v29,v29
CV v18,v20,v21
MM v[56:59],a[224:227],v[172:175],v[56:59]
s_waitcnt lgkmcnt(0)
s_barrier
MM a[0:3],a[128:131],v[64:67],a[0:3]
s_add_u32 m0, s65, 49152
CV v19,v22,v23
CV v24,v24,v25
MM a[0:3],a[132:135],v[68:71],a[0:3]
CV v25,v26,v27
CV v26,v28,v29
MM a[8:11],a[132:135],v[76:79],a[8:11]
EX v30,v30
MM a[8:11],a[128:131],v[72:75],a[8:11]
EX v31,v31
MM a[16:19],a[128:131],v[80:83],a[16:19]
RD a[192:195],v224 offset:16384
CV v27,v30,v31
MM a[16:19],a[132:135],v[84:87],a[16:19]
EX v32,v32
MM a[24:27],a[132:135],v[92:95],a[24:27]
buffer_load_dwordx4 v228, s[8:11], s64 offen offset:0 lds
MM a[24:27],a[128:131],v[88:91],a[24:27]
EX v33,v33
MM a[28:31],a[136:139],v[88:91],a[28:31]
RD a[196:199],v225 offset:16384
CV v4,v32,v33
MM a[28:31],a[140:143],v[92:95],a[28:31]
EX v34,v34
MM a[20:23],a[140:143],v[84:87],a[20:23]
EX v35,v35
MM a[20:23],a[136:139],v[80:83],a[20:23]
RD a[200:203],v226 offset:16384
CV v5,v34,v35
MM a[12:15],a[136:139],v[72:75],a[12:15]
EX v36,v36
MM a[12:15],a[140:143],v[76:79],a[12:15]
buffer_load_dwordx4 v229, s[8:11], s64 offen offset:1024 lds
MM a[4:7],a[140:143],v[68:71],a[4:7]
EX v37,v37
MM a[4:7],a[136:139],v[64:67],a[4:7]
RD a[204:207],v227 offset:16384
CV v6,v36,v37
MM a[32:35],a[144:147],v[64:67],a[32:35]
EX v38,v38
MM a[32:35],a[148:151],v[68:71],a[32:35]
EX v39,v39
MM a[40:43],a[148:151],v[76:79],a[40:43]
RD a[208:211],v224 offset:20480
CV v7,v38,v39
MM a[40:43],a[144:147],v[72:75],a[40:43]
EX v40,v40
MM a[48:51],a[144:147],v[80:83],a[48:51]
buffer_load_dwordx4 v230, s[8:11], s64 offen offset:2048 lds
MM a[48:51],a[148:151],v[84:87],a[48:51]
EX v41,v41
MM a[56:59],a[148:151],v[92:95],a[56:59]
RD a[212:215],v225 offset:20480
CV v12,v40,v41
MM a[56:59],a[144:147],v[88:91],a[56:59]
EX v42,v42
MM a[60:63],a[152:155],v[88:91],a[60:63]
EX v43,v43
MM a[60:63],a[156:159],v[92:95],a[60:63]
RD a[216:219],v226 offset:20480
CV v13,v42,v43
MM a[52:55],a[156:159],v[84:87],a[52:55]
EX v44,v44
MM a[52:55],a[152:155],v[80:83],a[52:55]
buffer_load_dwordx4 v231, s[8:11], s64 offen offset:3072 lds
MM a[44:47],a[152:155],v[72:75],a[44:47]
s_add_u32 m0, s65, 114688
EX v45,v45
MM a[44:47],a[156:159],v[76:79],a[44:47]
RD a[220:223],v227 offset:20480
CV v14,v44,v45
MM a[36:39],a[156:159],v[68:71],a[36:39]
EX v46,v46
MM a[36:39],a[152:155],v[64:67],a[36:39]
RD a[224:227],v224 offset:24576
)ASM"
R"ASM(
RD a[228:231],v225 offset:24576
MM a[64:67],a[160:163],v[64:67],a[64:67]
EX v47,v47
MM a[64:67],a[164:167],v[68:71],a[64:67]
RD a[232:235],v226 offset:24576
CV v15,v46,v47
MM a[72:75],a[164:167],v[76:79],a[72:75]
EX v48,v48
MM a[72:75],a[160:163],v[72:75],a[72:75]
buffer_load_dwordx4 v235, s[12:15], s81 offen offset:0 lds
MM a[80:83],a[160:163],v[80:83],a[80:83]
EX v49,v49
MM a[80:83],a[164:167],v[84:87],a[80:83]
RD a[236:239],v227 offset:24576
CV v20,v48,v49
MM a[88:91],a[164:167],v[92:95],a[88:91]
EX v50,v50
MM a[88:91],a[160:163],v[88:91],a[88:91]
EX v51,v51
MM a[92:95],a[168:171],v[88:91],a[92:95]
RD a[240:243],v224 offset:28672
CV v21,v50,v51
MM a[92:95],a[172:175],v[92:95],a[92:95]
EX v52,v52
MM a[84:87],a[172:175],v[84:87],a[84:87]
buffer_load_dwordx4 v235, s[12:15], s64 offen offset:1024 lds
MM a[84:87],a[168:171],v[80:83],a[84:87]
EX v53,v53
MM a[76:79],a[168:171],v[72:75],a[76:79]
RD a[244:247],v225 offset:28672
CV v22,v52,v53
MM a[76:79],a[172:175],v[76:79],a[76:79]
EX v54,v54
MM a[68:71],a[172:175],v[68:71],a[68:71]
s_waitcnt vmcnt(6)
v_add_f32 v96, v96, v100
v_add_f32 v97, v97, v101
MM a[68:71],a[168:171],v[64:67],a[68:71]
v_add_f32 v98, v98, v102
v_add_f32 v99, v99, v103
MM a[96:99],a[176:179],v[64:67],a[96:99]
v_add_f32 v96, v96, v98
v_add_f32 v97, v97, v99
MM a[96:99],a[180:183],v[68:71],a[96:99]
v_add_f32 v96, v96, v104
v_lshlrev_b32 v108, 16, v108
MM a[104:107],a[180:183],v[76:79],a[104:107]
v_add_f32 v96, v96, v97
CV v96,v96,v96
EX v55,v55
MM a[104:107],a[176:179],v[72:75],a[104:107]
v_and_b32 v96, 0xffff0000, v96
v_mul_f32 v96, v96, v111
MM a[112:115],a[176:179],v[80:83],a[112:115]
CV v96,v96,v96
v_and_b32 v96, 0xffff0000, v96
EX v56,v56
MM a[112:115],a[180:183],v[84:87],a[112:115]
v_add_f32 v96, v96, v108
CV v96,v96,v96
MM a[120:123],a[180:183],v[92:95],a[120:123]
s_mov_b64 exec, s[44:45]
buffer_store_short v96, v110, s[20:23], s76 offen offset:0 sc1
s_mov_b64 exec, -1
s_nop 0
EX v57,v57
MM a[120:123],a[176:179],v[88:91],a[120:123]
EX v58,v58
MM a[124:127],a[184:187],v[88:91],a[124:127]
v_add_f32 v244, v144, v145
v_add_f32 v245, v148, v149
MM a[124:127],a[188:191],v[92:95],a[124:127]
v_add_f32 v246, v152, v153
v_add_f32 v247, v156, v157
MM a[116:119],a[188:191],v[84:87],a[116:119]
EX v59,v59
MM a[116:119],a[184:187],v[80:83],a[116:119]
EX v60,v60
MM a[108:111],a[184:187],v[72:75],a[108:111]
CV v23,v54,v55
CV v28,v56,v57
MM a[108:111],a[188:191],v[76:79],a[108:111]
EX v61,v61
MM a[100:103],a[188:191],v[68:71],a[100:103]
buffer_load_dwordx4 v235, s[12:15], s81 offen offset:2048 lds
MM a[100:103],a[184:187],v[64:67],a[100:103]
EX v62,v62
v_smfmac_f32_16x16x64_bf16 v[144:147], v[236:239], v[0:7], v234
RD a[248:251],v226 offset:28672
RD a[252:255],v227 offset:28672
EX v63,v63
v_smfmac_f32_16x16x64_bf16 v[148:151], v[236:239], v[8:15], v234
CV v29,v58,v59
v_smfmac_f32_16x16x64_bf16 v[152:155], v[236:239], v[16:23], v234
CV v30,v60,v61
CV v31,v62,v63
s_nop 1
v_smfmac_f32_16x16x64_bf16 v[156:159], v[236:239], v[24:31], v234
s_waitcnt lgkmcnt(0)
buffer_load_dwordx4 v235, s[12:15], s64 offen offset:3072 lds
MM v[64:67],a[192:195],v[160:163],v[128:131]
s_sub_u32 s64, s64, 507904
TR a[128:129],v232 offset:0
s_add_u32 s81, s64, 896
MM v[64:67],a[196:199],v[176:179],v[64:67]
TR a[130:131],v232 offset:4096
v_max3_f32 v249, v144, v145, v148
MM v[64:67],a[200:203],v[192:195],v[64:67]
TR a[132:133],v232 offset:8192
v_max3_f32 v249, v249, v149, v152
MM v[64:67],a[204:207],v[208:211],v[64:67]
TR a[134:135],v232 offset:12288
v_max3_f32 v249, v249, v153, v156
MM v[68:71],a[220:223],v[208:211],v[128:131]
TR a[136:137],v233 offset:0
v_max_f32 v249, v249, v157
MM v[68:71],a[216:219],v[192:195],v[68:71]
TR a[138:139],v233 offset:4096
v_cmp_lt_f32 vcc, s71, v249
MM v[68:71],a[212:215],v[176:179],v[68:71]
TR a[140:141],v233 offset:8192
TR a[142:143],v233 offset:12288
MM v[68:71],a[208:211],v[160:163],v[68:71]
s_cbranch_vccnz LSITE32_%=
LBACK32_%=:
MM v[76:79],a[208:211],v[164:167],v[132:135]
EX v64,v64
MM v[76:79],a[212:215],v[180:183],v[76:79]
EX v65,v65
MM v[76:79],a[216:219],v[196:199],v[76:79]
TR a[144:145],v232 offset:512
CV v64,v64,v65
MM v[76:79],a[220:223],v[212:215],v[76:79]
EX v66,v66
MM v[72:75],a[204:207],v[212:215],v[132:135]
EX v67,v67
MM v[72:75],a[200:203],v[196:199],v[72:75]
TR a[146:147],v232 offset:4608
CV v65,v66,v67
MM v[72:75],a[196:199],v[180:183],v[72:75]
EX v68,v68
MM v[72:75],a[192:195],v[164:167],v[72:75]
TR a[148:149],v232 offset:8704
TR a[150:151],v232 offset:12800
MM v[80:83],a[192:195],v[168:171],v[136:139]
EX v69,v69
MM v[80:83],a[196:199],v[184:187],v[80:83]
TR a[152:153],v233 offset:512
CV v66,v68,v69
MM v[80:83],a[200:203],v[200:203],v[80:83]
EX v70,v70
MM v[80:83],a[204:207],v[216:219],v[80:83]
TR a[154:155],v233 offset:4608
TR a[156:157],v233 offset:8704
MM v[84:87],a[220:223],v[216:219],v[136:139]
EX v71,v71
MM v[84:87],a[216:219],v[200:203],v[84:87]
TR a[158:159],v233 offset:12800
CV v67,v70,v71
MM v[84:87],a[212:215],v[184:187],v[84:87]
EX v72,v72
MM v[84:87],a[208:211],v[168:171],v[84:87]
EX v73,v73
MM v[92:95],a[208:211],v[172:175],v[140:143]
TR a[160:161],v232 offset:1024
CV v72,v72,v73
MM v[92:95],a[212:215],v[188:191],v[92:95]
EX v74,v74
MM v[92:95],a[216:219],v[204:207],v[92:95]
TR a[162:163],v232 offset:5120
TR a[164:165],v232 offset:9216
MM v[92:95],a[220:223],v[220:223],v[92:95]
EX v75,v75
MM v[88:91],a[204:207],v[220:223],v[140:143]
TR a[166:167],v232 offset:13312
CV v73,v74,v75
MM v[88:91],a[200:203],v[204:207],v[88:91]
EX v76,v76
MM v[88:91],a[196:199],v[188:191],v[88:91]
EX v77,v77
MM v[88:91],a[192:195],v[172:175],v[88:91]
TR a[168:169],v233 offset:1024
CV v74,v76,v77
MM v[96:99],a[224:227],v[160:163],v[128:131]
EX v78,v78
MM v[96:99],a[228:231],v[176:179],v[96:99]
TR a[170:171],v233 offset:5120
TR a[172:173],v233 offset:9216
MM v[96:99],a[232:235],v[192:195],v[96:99]
EX v79,v79
MM v[96:99],a[236:239],v[208:211],v[96:99]
TR a[174:175],v233 offset:13312
CV v75,v78,v79
MM v[100:103],a[252:255],v[208:211],v[128:131]
EX v80,v80
MM v[100:103],a[248:251],v[192:195],v[100:103]
EX v81,v81
MM v[100:103],a[244:247],v[176:179],v[100:103]
TR a[176:177],v232 offset:1536
CV v80,v80,v81
MM v[100:103],a[240:243],v[160:163],v[100:103]
EX v82,v82
MM v[108:111],a[240:243],v[164:167],v[132:135]
TR a[178:179],v232 offset:5632
TR a[180:181],v232 offset:9728
MM v[108:111],a[244:247],v[180:183],v[108:111]
EX v83,v83
MM v[108:111],a[248:251],v[196:199],v[108:111]
TR a[182:183],v232 offset:13824
CV v81,v82,v83
MM v[108:111],a[252:255],v[212:215],v[108:111]
EX v84,v84
MM v[104:107],a[236:239],v[212:215],v[132:135]
TR a[184:185],v233 offset:1536
TR a[186:187],v233 offset:5632
MM v[104:107],a[232:235],v[196:199],v[104:107]
EX v85,v85
MM v[104:107],a[228:231],v[180:183],v[104:107]
TR a[188:189],v233 offset:9728
CV v82,v84,v85
MM v[104:107],a[224:227],v[164:167],v[104:107]
EX v86,v86
MM v[112:115],a[224:227],v[168:171],v[136:139]
EX v87,v87
MM v[112:115],a[228:231],v[184:187],v[112:115]
TR a[190:191],v233 offset:13824
CV v83,v86,v87
MM v[112:115],a[232:235],v[200:203],v[112:115]
EX v88,v88
MM v[112:115],a[236:239],v[216:219],v[112:115]
EX v89,v89
MM v[116:119],a[252:255],v[216:219],v[136:139]
EX v90,v90
MM v[116:119],a[248:251],v[200:203],v[116:119]
EX v91,v91
MM v[116:119],a[244:247],v[184:187],v[116:119]
EX v92,v92
MM v[116:119],a[240:243],v[168:171],v[116:119]
EX v93,v93
MM v[124:127],a[240:243],v[172:175],v[140:143]
EX v94,v94
MM v[124:127],a[244:247],v[188:191],v[124:127]
EX v95,v95
MM v[124:127],a[248:251],v[204:207],v[124:127]
EX v96,v96
MM v[124:127],a[252:255],v[220:223],v[124:127]
EX v97,v97
MM v[120:123],a[236:239],v[220:223],v[140:143]
EX v98,v98
MM v[120:123],a[232:235],v[204:207],v[120:123]
EX v99,v99
MM v[120:123],a[228:231],v[188:191],v[120:123]
EX v100,v100
MM v[120:123],a[224:227],v[172:175],v[120:123]
s_waitcnt lgkmcnt(0)
s_barrier
MM a[0:3],a[128:131],v[0:3],a[0:3]
s_add_u32 m0, s65, 0
CV v88,v88,v89
CV v89,v90,v91
MM a[0:3],a[132:135],v[4:7],a[0:3]
RD a[192:195],v224 offset:32768
CV v90,v92,v93
MM a[8:11],a[132:135],v[12:15],a[8:11]
RD a[196:199],v225 offset:32768
CV v91,v94,v95
MM a[8:11],a[128:131],v[8:11],a[8:11]
buffer_load_dwordx4 v228, s[8:11], s64 offen offset:0 lds
MM a[16:19],a[128:131],v[16:19],a[16:19]
RD a[200:203],v226 offset:32768
CV v68,v96,v97
MM a[16:19],a[132:135],v[20:23],a[16:19]
RD a[204:207],v227 offset:32768
CV v69,v98,v99
MM a[24:27],a[132:135],v[28:31],a[24:27]
buffer_load_dwordx4 v229, s[8:11], s64 offen offset:1024 lds
MM a[24:27],a[128:131],v[24:27],a[24:27]
RD a[208:211],v224 offset:36864
RD a[212:215],v225 offset:36864
MM a[28:31],a[136:139],v[24:27],a[28:31]
buffer_load_dwordx4 v230, s[8:11], s64 offen offset:2048 lds
MM a[28:31],a[140:143],v[28:31],a[28:31]
RD a[216:219],v226 offset:36864
RD a[220:223],v227 offset:36864
MM a[20:23],a[140:143],v[20:23],a[20:23]
EX v101,v101
MM a[20:23],a[136:139],v[16:19],a[20:23]
RD a[224:227],v224 offset:40960
CV v70,v100,v101
MM a[12:15],a[136:139],v[8:11],a[12:15]
EX v102,v102
MM a[12:15],a[140:143],v[12:15],a[12:15]
buffer_load_dwordx4 v231, s[8:11], s64 offen offset:3072 lds
MM a[4:7],a[140:143],v[4:7],a[4:7]
s_add_u32 m0, s65, 65536
EX v103,v103
MM a[4:7],a[136:139],v[0:3],a[4:7]
RD a[228:231],v225 offset:40960
CV v71,v102,v103
MM a[32:35],a[144:147],v[0:3],a[32:35]
EX v104,v104
MM a[32:35],a[148:151],v[4:7],a[32:35]
EX v105,v105
MM a[40:43],a[148:151],v[12:15],a[40:43]
RD a[232:235],v226 offset:40960
CV v76,v104,v105
MM a[40:43],a[144:147],v[8:11],a[40:43]
EX v106,v106
MM a[48:51],a[144:147],v[16:19],a[48:51]
buffer_load_dwordx4 v235, s[12:15], s81 offen offset:0 lds
MM a[48:51],a[148:151],v[20:23],a[48:51]
EX v107,v107
MM a[56:59],a[148:151],v[28:31],a[56:59]
RD a[236:239],v227 offset:40960
CV v77,v106,v107
MM a[56:59],a[144:147],v[24:27],a[56:59]
EX v108,v108
MM a[60:63],a[152:155],v[24:27],a[60:63]
EX v109,v109
MM a[60:63],a[156:159],v[28:31],a[60:63]
RD a[240:243],v224 offset:45056
CV v78,v108,v109
MM a[52:55],a[156:159],v[20:23],a[52:55]
EX v110,v110
MM a[52:55],a[152:155],v[16:19],a[52:55]
buffer_load_dwordx4 v235, s[12:15], s64 offen offset:1024 lds
MM a[44:47],a[152:155],v[8:11],a[44:47]
EX v111,v111
MM a[44:47],a[156:159],v[12:15],a[44:47]
RD a[244:247],v225 offset:45056
CV v79,v110,v111
MM a[36:39],a[156:159],v[4:7],a[36:39]
EX v112,v112
MM a[36:39],a[152:155],v[0:3],a[36:39]
EX v113,v113
MM a[64:67],a[160:163],v[0:3],a[64:67]
RD a[248:251],v226 offset:45056
CV v84,v112,v113
MM a[64:67],a[164:167],v[4:7],a[64:67]
EX v114,v114
MM a[72:75],a[164:167],v[12:15],a[72:75]
buffer_load_dwordx4 v235, s[12:15], s81 offen offset:2048 lds
MM a[72:75],a[160:163],v[8:11],a[72:75]
EX v115,v115
MM a[80:83],a[160:163],v[16:19],a[80:83]
RD a[252:255],v227 offset:45056
CV v85,v114,v115
MM a[80:83],a[164:167],v[20:23],a[80:83]
EX v116,v116
MM a[88:91],a[164:167],v[28:31],a[88:91]
EX v117,v117
MM a[88:91],a[160:163],v[24:27],a[88:91]
buffer_load_dwordx4 v235, s[12:15], s64 offen offset:3072 lds
MM a[92:95],a[168:171],v[24:27],a[92:95]
EX v118,v118
MM a[92:95],a[172:175],v[28:31],a[92:95]
EX v119,v119
MM a[84:87],a[172:175],v[20:23],a[84:87]
EX v120,v120
MM a[84:87],a[168:171],v[16:19],a[84:87]
EX v121,v121
MM a[76:79],a[168:171],v[8:11],a[76:79]
EX v122,v122
MM a[76:79],a[172:175],v[12:15],a[76:79]
EX v123,v123
MM a[68:71],a[172:175],v[4:7],a[68:71]
EX v124,v124
MM a[68:71],a[168:171],v[0:3],a[68:71]
EX v125,v125
MM a[96:99],a[176:179],v[0:3],a[96:99]
EX v126,v126
MM a[96:99],a[180:183],v[4:7],a[96:99]
EX v127,v127
MM a[104:107],a[180:183],v[12:15],a[104:107]
CV v86,v116,v117
CV v87,v118,v119
MM a[104:107],a[176:179],v[8:11],a[104:107]
CV v92,v120,v121
CV v93,v122,v123
MM a[112:115],a[176:179],v[16:19],a[112:115]
CV v94,v124,v125
CV v95,v126,v127
MM a[112:115],a[180:183],v[20:23],a[112:115]
MM a[120:123],a[180:183],v[28:31],a[120:123]
s_lshl_b32 s91, s17, 2
s_add_u32 s91, s91, 94464
s_and_b64 s[44:45], s[44:45], 1
MM a[120:123],a[176:179],v[24:27],a[120:123]
s_waitcnt vmcnt(10)
v_mov_b32 v250, s69
s_mov_b64 exec, s[44:45]
buffer_store_dword v250, off, s[20:23], s91 sc1
s_mov_b64 exec, -1
)ASM"
R"ASM(
MM a[124:127],a[184:187],v[24:27],a[124:127]
v_add_f32 v244, v144, v145
v_add_f32 v245, v148, v149
MM a[124:127],a[188:191],v[28:31],a[124:127]
v_add_f32 v246, v152, v153
v_add_f32 v247, v156, v157
MM a[116:119],a[188:191],v[20:23],a[116:119]
MM a[116:119],a[184:187],v[16:19],a[116:119]
MM a[108:111],a[184:187],v[8:11],a[108:111]
MM a[108:111],a[188:191],v[12:15],a[108:111]
MM a[100:103],a[188:191],v[4:7],a[100:103]
MM a[100:103],a[184:187],v[0:3],a[100:103]
v_smfmac_f32_16x16x64_bf16 v[144:147], v[236:239], v[64:71], v234
v_smfmac_f32_16x16x64_bf16 v[148:151], v[236:239], v[72:79], v234
s_nop 0
LPG23_%=:
s_cbranch_execz LPG27_%=
v_smfmac_f32_16x16x64_bf16 v[152:155], v[236:239], v[80:87], v234
v_smfmac_f32_16x16x64_bf16 v[156:159], v[236:239], v[88:95], v234
s_waitcnt lgkmcnt(0)
LLAJ_%=:
MM v[0:3],a[192:195],v[160:163],v[128:131]
s_add_u32 s64, s64, 16384
TR a[128:129],v232 offset:16384
s_add_u32 s81, s64, 896
MM v[0:3],a[196:199],v[176:179],v[0:3]
TR a[130:131],v232 offset:20480
v_max3_f32 v249, v144, v145, v148
MM v[0:3],a[200:203],v[192:195],v[0:3]
TR a[132:133],v232 offset:24576
v_max3_f32 v249, v249, v149, v152
MM v[0:3],a[204:207],v[208:211],v[0:3]
TR a[134:135],v232 offset:28672
v_max3_f32 v249, v249, v153, v156
MM v[4:7],a[220:223],v[208:211],v[128:131]
TR a[136:137],v233 offset:16384
v_max_f32 v249, v249, v157
MM v[4:7],a[216:219],v[192:195],v[4:7]
TR a[138:139],v233 offset:20480
v_cmp_lt_f32 vcc, s71, v249
MM v[4:7],a[212:215],v[176:179],v[4:7]
TR a[140:141],v233 offset:24576
TR a[142:143],v233 offset:28672
MM v[4:7],a[208:211],v[160:163],v[4:7]
s_cbranch_vccnz LSITE33_%=
LBACK33_%=:
MM v[12:15],a[208:211],v[164:167],v[132:135]
v_bfe_u32 v242, v248, 6, 4
v_mul_u32_u24 v242, 1280, v242
MM v[12:15],a[212:215],v[180:183],v[12:15]
v_and_b32 v252, 48, v248
v_bfe_u32 v243, v248, 8, 1
MM v[12:15],a[216:219],v[196:199],v[12:15]
v_lshlrev_b32 v243, 5, v243
v_xor_b32 v252, v252, v243
MM v[12:15],a[220:223],v[212:215],v[12:15]
v_add_u32 v252, v252, v242
v_lshrrev_b32 v242, 3, v248
MM v[8:11],a[204:207],v[212:215],v[132:135]
v_xor_b32 v243, v242, v248
v_and_b32 v243, 96, v243
MM v[8:11],a[200:203],v[196:199],v[8:11]
TR a[144:145],v232 offset:16896
v_and_or_b32 v243, v248, 16, v243
MM v[8:11],a[196:199],v[180:183],v[8:11]
s_and_b32 s66, s49, 8
EX v0,v0
MM v[8:11],a[192:195],v[164:167],v[8:11]
s_lshl_b32 s66, s66, 3
v_lshrrev_b32 v242, 7, v248
v_xor_b32 v243, s66, v243
EX v1,v1
MM v[16:19],a[192:195],v[168:171],v[136:139]
v_mul_u32_u24 v242, 1280, v242
v_add_u32 v240, v242, v243
EX v2,v2
MM v[16:19],a[196:199],v[184:187],v[16:19]
s_mul_i32 s88, %[swave], 40960
EX v3,v3
MM v[16:19],a[200:203],v[200:203],v[16:19]
s_and_b32 s66, s84, 1
EX v4,v4
MM v[16:19],a[204:207],v[216:219],v[16:19]
s_lshl_b32 s86, s66, 6
TR a[146:147],v232 offset:20992
CV v0,v0,v1
MM v[20:23],a[220:223],v[216:219],v[136:139]
s_add_u32 s86, s86, s88
EX v5,v5
MM v[20:23],a[216:219],v[200:203],v[20:23]
s_xor_b32 s66, s66, 1
TR a[148:149],v232 offset:25088
CV v1,v2,v3
MM v[20:23],a[212:215],v[184:187],v[20:23]
s_lshl_b32 s66, s66, 8
EX v6,v6
MM v[20:23],a[208:211],v[168:171],v[20:23]
s_add_u32 s88, s88, s66
EX v7,v7
MM v[28:31],a[208:211],v[172:175],v[140:143]
s_and_b32 s66, s49, 16
TR a[150:151],v232 offset:29184
TR a[152:153],v233 offset:16896
MM v[28:31],a[212:215],v[188:191],v[28:31]
s_lshl_b32 s66, s66, 3
EX v8,v8
MM v[28:31],a[216:219],v[204:207],v[28:31]
s_sub_u32 s87, s86, s66
TR a[154:155],v233 offset:20992
CV v2,v4,v5
MM v[28:31],a[220:223],v[220:223],v[28:31]
s_add_u32 s87, s87, 128
EX v9,v9
MM v[24:27],a[204:207],v[220:223],v[140:143]
s_add_u32 s86, s86, s66
TR a[156:157],v233 offset:25088
CV v3,v6,v7
MM v[24:27],a[200:203],v[204:207],v[24:27]
s_and_b32 s94, s49, 7
EX v10,v10
MM v[24:27],a[196:199],v[188:191],v[24:27]
s_lshl_b32 s89, %[swave], 11
TR a[158:159],v233 offset:29184
CV v8,v8,v9
MM v[24:27],a[192:195],v[172:175],v[24:27]
s_add_u32 s89, s89, %[slds]
EX v11,v11
MM v[32:35],a[224:227],v[160:163],v[128:131]
s_xor_b32 s66, s94, 4
TR a[160:161],v232 offset:17408
TR a[162:163],v232 offset:21504
MM v[32:35],a[228:231],v[176:179],v[32:35]
s_mul_i32 s66, s66, 163840
EX v12,v12
MM v[32:35],a[232:235],v[192:195],v[32:35]
s_add_u32 s66, s66, s88
s_add_u32 m0, s89, 131072
s_nop 0
buffer_load_dwordx4 v252, s[24:27], s66 offen offset:0 lds
MM v[32:35],a[236:239],v[208:211],v[32:35]
s_add_u32 s66, s66, 19456
EX v13,v13
MM v[36:39],a[252:255],v[208:211],v[128:131]
TR a[164:165],v232 offset:25600
CV v9,v10,v11
MM v[36:39],a[248:251],v[192:195],v[36:39]
EX v14,v14
MM v[36:39],a[244:247],v[176:179],v[36:39]
EX v15,v15
MM v[36:39],a[240:243],v[160:163],v[36:39]
TR a[166:167],v232 offset:29696
TR a[168:169],v233 offset:17408
MM v[44:47],a[240:243],v[164:167],v[132:135]
EX v16,v16
MM v[44:47],a[244:247],v[180:183],v[44:47]
TR a[170:171],v233 offset:21504
CV v10,v12,v13
MM v[44:47],a[248:251],v[196:199],v[44:47]
EX v17,v17
MM v[44:47],a[252:255],v[212:215],v[44:47]
s_add_u32 m0, s89, 131072
s_nop 0
buffer_load_dwordx4 v252, s[24:27], s66 offen offset:1024 lds
MM v[40:43],a[236:239],v[212:215],v[132:135]
EX v18,v18
MM v[40:43],a[232:235],v[196:199],v[40:43]
TR a[172:173],v233 offset:25600
CV v11,v14,v15
MM v[40:43],a[228:231],v[180:183],v[40:43]
EX v19,v19
MM v[40:43],a[224:227],v[164:167],v[40:43]
TR a[174:175],v233 offset:29696
TR a[176:177],v232 offset:17920
MM v[48:51],a[224:227],v[168:171],v[136:139]
EX v20,v20
MM v[48:51],a[228:231],v[184:187],v[48:51]
TR a[178:179],v232 offset:22016
CV v16,v16,v17
MM v[48:51],a[232:235],v[200:203],v[48:51]
EX v21,v21
MM v[48:51],a[236:239],v[216:219],v[48:51]
s_lshl_b32 s60, s50, 2
TR a[180:181],v232 offset:26112
CV v17,v18,v19
MM v[52:55],a[252:255],v[216:219],v[136:139]
s_add_u32 s60, s60, %[swave]
EX v22,v22
MM v[52:55],a[248:251],v[200:203],v[52:55]
s_lshl_b32 s60, s60, 1
TR a[182:183],v232 offset:30208
TR a[184:185],v233 offset:17920
MM v[52:55],a[244:247],v[184:187],v[52:55]
s_bfe_u32 s61, s49, 65538
EX v23,v23
MM v[52:55],a[240:243],v[168:171],v[52:55]
s_xor_b32 s61, s61, 1
EX v24,v24
MM v[60:63],a[240:243],v[172:175],v[140:143]
s_add_u32 s60, s60, s61
TR a[186:187],v233 offset:22016
CV v18,v20,v21
MM v[60:63],a[244:247],v[188:191],v[60:63]
s_mul_i32 s18, s60, 40960
EX v25,v25
MM v[60:63],a[248:251],v[204:207],v[60:63]
s_mul_i32 s60, s84, 320
TR a[188:189],v233 offset:26112
TR a[190:191],v233 offset:30208
MM v[60:63],a[252:255],v[220:223],v[60:63]
s_add_u32 s18, s18, s60
EX v26,v26
MM v[56:59],a[236:239],v[220:223],v[140:143]
s_lshr_b32 s62, s49, 3
CV v19,v22,v23
CV v24,v24,v25
MM v[56:59],a[232:235],v[204:207],v[56:59]
s_mul_i32 s63, s62, 10240
EX v27,v27
MM v[56:59],a[228:231],v[188:191],v[56:59]
s_add_u32 s63, s63, s18
EX v28,v28
MM v[56:59],a[224:227],v[172:175],v[56:59]
s_waitcnt vmcnt(14) lgkmcnt(0)
s_barrier
MM a[0:3],a[128:131],v[64:67],a[0:3]
s_and_b32 s60, s84, 1
EX v29,v29
MM a[0:3],a[132:135],v[68:71],a[0:3]
s_add_u32 s61, s60, s62
CV v25,v26,v27
CV v26,v28,v29
MM a[8:11],a[132:135],v[76:79],a[8:11]
s_lshl_b32 s61, s61, 6
EX v30,v30
MM a[8:11],a[128:131],v[72:75],a[8:11]
s_add_u32 s18, s18, s61
EX v31,v31
MM a[16:19],a[128:131],v[80:83],a[16:19]
s_xor_b32 s60, s60, 1
RD a[192:195],v224 offset:49152
CV v27,v30,v31
MM a[16:19],a[132:135],v[84:87],a[16:19]
s_lshl_b32 s60, s60, 8
EX v32,v32
MM a[24:27],a[132:135],v[92:95],a[24:27]
s_add_u32 s63, s63, s60
s_add_u32 m0, s65, 16384
RD a[196:199],v225 offset:49152
MM a[24:27],a[128:131],v[88:91],a[24:27]
s_mul_i32 s60, s62, 33792
EX v33,v33
MM a[28:31],a[136:139],v[88:91],a[28:31]
s_sub_u32 s60, s95, s60
buffer_load_dwordx4 v228, s[8:11], s64 offen offset:0 lds
MM a[28:31],a[140:143],v[92:95],a[28:31]
s_xor_b32 s61, s62, 1
EX v34,v34
MM a[20:23],a[140:143],v[84:87],a[20:23]
s_mul_i32 s61, s61, 33792
EX v35,v35
MM a[20:23],a[136:139],v[80:83],a[20:23]
s_add_u32 s61, s61, s60
RD a[200:203],v226 offset:49152
CV v4,v32,v33
MM a[12:15],a[136:139],v[72:75],a[12:15]
s_add_u32 s36, s61, 2048
EX v36,v36
MM a[12:15],a[140:143],v[76:79],a[12:15]
s_xor_b32 s61, s62, 2
CV v5,v34,v35
MM a[4:7],a[140:143],v[68:71],a[4:7]
s_mul_i32 s61, s61, 33792
EX v37,v37
MM a[4:7],a[136:139],v[64:67],a[4:7]
s_add_u32 s61, s61, s60
buffer_load_dwordx4 v229, s[8:11], s64 offen offset:1024 lds
MM a[32:35],a[144:147],v[64:67],a[32:35]
s_add_u32 s37, s61, 4096
EX v38,v38
MM a[32:35],a[148:151],v[68:71],a[32:35]
s_xor_b32 s61, s62, 3
RD a[204:207],v227 offset:49152
CV v6,v36,v37
MM a[40:43],a[148:151],v[76:79],a[40:43]
s_mul_i32 s61, s61, 33792
EX v39,v39
MM a[40:43],a[144:147],v[72:75],a[40:43]
s_add_u32 s61, s61, s60
RD a[208:211],v224 offset:53248
CV v7,v38,v39
MM a[48:51],a[144:147],v[80:83],a[48:51]
s_add_u32 s38, s61, 6144
EX v40,v40
MM a[48:51],a[148:151],v[84:87],a[48:51]
s_lshl_b32 s61, s62, 9
EX v41,v41
MM a[56:59],a[148:151],v[92:95],a[56:59]
s_add_u32 s39, s60, s61
RD a[212:215],v225 offset:53248
MM a[56:59],a[144:147],v[88:91],a[56:59]
s_mov_b32 s17, 15
buffer_load_dwordx4 v230, s[8:11], s64 offen offset:2048 lds
MM a[60:63],a[152:155],v[88:91],a[60:63]
s_mov_b32 s58, 0
EX v42,v42
MM a[60:63],a[156:159],v[92:95],a[60:63]
s_lshl_b32 s60, s50, 1
EX v43,v43
MM a[52:55],a[156:159],v[84:87],a[52:55]
s_bfe_u32 s61, s49, 65538
RD a[216:219],v226 offset:53248
CV v12,v40,v41
MM a[52:55],a[152:155],v[80:83],a[52:55]
s_add_u32 s60, s60, s61
EX v44,v44
MM a[44:47],a[152:155],v[72:75],a[44:47]
s_lshl_b32 s60, s60, 2
CV v13,v42,v43
MM a[44:47],a[156:159],v[76:79],a[44:47]
s_add_u32 s60, s60, s84
EX v45,v45
MM a[36:39],a[156:159],v[68:71],a[36:39]
s_lshl_b32 s60, s60, 2
buffer_load_dwordx4 v231, s[8:11], s64 offen offset:3072 lds
MM a[36:39],a[152:155],v[64:67],a[36:39]
s_add_u32 s68, s60, %[swave]
EX v46,v46
MM a[64:67],a[160:163],v[64:67],a[64:67]
s_lshl_b32 s68, s68, 6
RD a[220:223],v227 offset:53248
CV v14,v44,v45
MM a[64:67],a[164:167],v[68:71],a[64:67]
s_add_u32 s68, s68, 16384
EX v47,v47
MM a[72:75],a[164:167],v[76:79],a[72:75]
s_lshr_b32 s61, s49, 3
EX v48,v48
MM a[72:75],a[160:163],v[72:75],a[72:75]
s_lshl_b32 s19, s61, 4
RD a[224:227],v224 offset:57344
CV v15,v46,v47
MM a[80:83],a[160:163],v[80:83],a[80:83]
s_add_u32 s19, s19, s68
EX v49,v49
MM a[80:83],a[164:167],v[84:87],a[80:83]
s_lshr_b32 s96, s49, 2
s_add_u32 m0, s65, 81920
CV v20,v48,v49
MM a[88:91],a[164:167],v[92:95],a[88:91]
s_lshl_b32 s97, s50, 3
EX v50,v50
MM a[88:91],a[160:163],v[88:91],a[88:91]
s_add_u32 s96, s96, s97
buffer_load_dwordx4 v235, s[12:15], s81 offen offset:0 lds
MM a[92:95],a[168:171],v[88:91],a[92:95]
s_lshl_b32 s96, s96, 2
EX v51,v51
MM a[92:95],a[172:175],v[92:95],a[92:95]
s_add_u32 s96, s96, %[swave]
RD a[228:231],v225 offset:57344
CV v21,v50,v51
MM a[84:87],a[172:175],v[84:87],a[84:87]
s_lshl_b32 s96, s96, 6
EX v52,v52
MM a[84:87],a[168:171],v[80:83],a[84:87]
s_add_u32 s96, s96, 0
RD a[232:235],v226 offset:57344
MM a[76:79],a[168:171],v[72:75],a[76:79]
s_xor_b32 s44, s96, 256
EX v53,v53
MM a[76:79],a[172:175],v[76:79],a[76:79]
s_xor_b32 s66, s94, 5
RD a[236:239],v227 offset:57344
CV v22,v52,v53
MM a[68:71],a[172:175],v[68:71],a[68:71]
s_mul_i32 s66, s66, 163840
EX v54,v54
MM a[68:71],a[168:171],v[64:67],a[68:71]
s_add_u32 s66, s66, s88
s_add_u32 m0, s89, 139264
s_nop 0
buffer_load_dwordx4 v252, s[24:27], s66 offen offset:0 lds
MM a[96:99],a[176:179],v[64:67],a[96:99]
s_add_u32 s66, s66, 19456
EX v55,v55
MM a[96:99],a[180:183],v[68:71],a[96:99]
s_add_u32 m0, s65, 81920
s_nop 0
buffer_load_dwordx4 v235, s[12:15], s64 offen offset:1024 lds
MM a[104:107],a[180:183],v[76:79],a[104:107]
EX v56,v56
MM a[104:107],a[176:179],v[72:75],a[104:107]
RD a[240:243],v224 offset:61440
CV v23,v54,v55
MM a[112:115],a[176:179],v[80:83],a[112:115]
EX v57,v57
MM a[112:115],a[180:183],v[84:87],a[112:115]
EX v58,v58
MM a[120:123],a[180:183],v[92:95],a[120:123]
RD a[244:247],v225 offset:61440
CV v28,v56,v57
MM a[120:123],a[176:179],v[88:91],a[120:123]
EX v59,v59
MM a[124:127],a[184:187],v[88:91],a[124:127]
v_add_f32 v244, v144, v145
v_add_f32 v245, v148, v149
MM a[124:127],a[188:191],v[92:95],a[124:127]
v_add_f32 v246, v152, v153
v_add_f32 v247, v156, v157
MM a[116:119],a[188:191],v[84:87],a[116:119]
)ASM"
R"ASM(
s_add_u32 m0, s89, 139264
s_nop 0
buffer_load_dwordx4 v252, s[24:27], s66 offen offset:1024 lds
MM a[116:119],a[184:187],v[80:83],a[116:119]
EX v60,v60
MM a[108:111],a[184:187],v[72:75],a[108:111]
EX v61,v61
MM a[108:111],a[188:191],v[76:79],a[108:111]
s_add_u32 m0, s65, 81920
s_nop 0
buffer_load_dwordx4 v235, s[12:15], s81 offen offset:2048 lds
MM a[100:103],a[188:191],v[68:71],a[100:103]
EX v62,v62
MM a[100:103],a[184:187],v[64:67],a[100:103]
RD a[248:251],v226 offset:61440
CV v29,v58,v59
v_smfmac_f32_16x16x64_bf16 v[144:147], v[236:239], v[0:7], v234
RD a[252:255],v227 offset:61440
EX v63,v63
v_smfmac_f32_16x16x64_bf16 v[148:151], v[236:239], v[8:15], v234
CV v30,v60,v61
v_smfmac_f32_16x16x64_bf16 v[152:155], v[236:239], v[16:23], v234
CV v31,v62,v63
s_nop 1
v_smfmac_f32_16x16x64_bf16 v[156:159], v[236:239], v[24:31], v234
s_waitcnt lgkmcnt(0)
buffer_load_dwordx4 v235, s[12:15], s64 offen offset:3072 lds
MM v[64:67],a[192:195],v[160:163],v[128:131]
s_add_u32 s64, s64, 16384
TR a[128:129],v232 offset:32768
s_add_u32 s81, s64, 896
MM v[64:67],a[196:199],v[176:179],v[64:67]
TR a[130:131],v232 offset:36864
v_max3_f32 v249, v144, v145, v148
MM v[64:67],a[200:203],v[192:195],v[64:67]
TR a[132:133],v232 offset:40960
v_max3_f32 v249, v249, v149, v152
MM v[64:67],a[204:207],v[208:211],v[64:67]
TR a[134:135],v232 offset:45056
v_max3_f32 v249, v249, v153, v156
MM v[68:71],a[220:223],v[208:211],v[128:131]
TR a[136:137],v233 offset:32768
v_max_f32 v249, v249, v157
MM v[68:71],a[216:219],v[192:195],v[68:71]
TR a[138:139],v233 offset:36864
v_cmp_lt_f32 vcc, s71, v249
MM v[68:71],a[212:215],v[176:179],v[68:71]
TR a[140:141],v233 offset:40960
TR a[142:143],v233 offset:45056
MM v[68:71],a[208:211],v[160:163],v[68:71]
s_cbranch_vccnz LSITE34_%=
LBACK34_%=:
MM v[76:79],a[208:211],v[164:167],v[132:135]
buffer_load_dword v242, off, s[20:23], s96 sc1
MM v[76:79],a[212:215],v[180:183],v[76:79]
buffer_load_dword v243, off, s[20:23], s19 sc1
MM v[76:79],a[216:219],v[196:199],v[76:79]
s_xor_b32 s66, s94, 6
v_and_b32 v32, 496, v248
v_mul_u32_u24 v32, 80, v32
MM v[76:79],a[220:223],v[212:215],v[76:79]
s_mul_i32 s66, s66, 163840
EX v64,v64
MM v[72:75],a[204:207],v[212:215],v[132:135]
s_add_u32 s66, s66, s88
s_add_u32 m0, s89, 147456
s_nop 0
buffer_load_dwordx4 v252, s[24:27], s66 offen offset:0 lds
MM v[72:75],a[200:203],v[196:199],v[72:75]
s_add_u32 s66, s66, 19456
EX v65,v65
MM v[72:75],a[196:199],v[180:183],v[72:75]
s_sub_u32 s60, s63, s18
v_add_u32 v32, s18, v32
CV v64,v64,v65
MM v[72:75],a[192:195],v[164:167],v[72:75]
s_mov_b32 s61, 131072
s_mov_b32 exec_lo, 0
v_add_u32 v32, s60, v32
s_mov_b64 exec, -1
MM v[80:83],a[192:195],v[168:171],v[136:139]
s_mov_b32 exec_hi, 0xff
buffer_load_dword v36, v32, s[32:35], 0 offen offset:0
s_mov_b64 exec, -1
EX v66,v66
MM v[80:83],a[196:199],v[184:187],v[80:83]
EX v67,v67
MM v[80:83],a[200:203],v[200:203],v[80:83]
EX v68,v68
MM v[80:83],a[204:207],v[216:219],v[80:83]
s_mov_b32 exec_hi, 0xff
buffer_load_dword v37, v32, s[20:23], s61 offen offset:0 sc1
s_mov_b64 exec, -1
MM v[84:87],a[220:223],v[216:219],v[136:139]
TR a[144:145],v232 offset:33280
TR a[146:147],v232 offset:37376
MM v[84:87],a[216:219],v[200:203],v[84:87]
s_add_u32 m0, s89, 147456
s_nop 0
buffer_load_dwordx4 v252, s[24:27], s66 offen offset:1024 lds
EX v69,v69
MM v[84:87],a[212:215],v[184:187],v[84:87]
EX v70,v70
MM v[84:87],a[208:211],v[168:171],v[84:87]
TR a[148:149],v232 offset:41472
CV v65,v66,v67
MM v[92:95],a[208:211],v[172:175],v[140:143]
EX v71,v71
MM v[92:95],a[212:215],v[188:191],v[92:95]
TR a[150:151],v232 offset:45568
TR a[152:153],v233 offset:33280
MM v[92:95],a[216:219],v[204:207],v[92:95]
EX v72,v72
MM v[92:95],a[220:223],v[220:223],v[92:95]
EX v73,v73
MM v[88:91],a[204:207],v[220:223],v[140:143]
TR a[154:155],v233 offset:37376
CV v66,v68,v69
MM v[88:91],a[200:203],v[204:207],v[88:91]
s_xor_b32 s66, s94, 7
EX v74,v74
MM v[88:91],a[196:199],v[188:191],v[88:91]
s_mul_i32 s66, s66, 163840
TR a[156:157],v233 offset:41472
CV v67,v70,v71
MM v[88:91],a[192:195],v[172:175],v[88:91]
s_add_u32 s66, s66, s88
s_add_u32 m0, s89, 155648
s_nop 0
buffer_load_dwordx4 v252, s[24:27], s66 offen offset:0 lds
MM v[96:99],a[224:227],v[160:163],v[128:131]
s_add_u32 s66, s66, 19456
EX v75,v75
MM v[96:99],a[228:231],v[176:179],v[96:99]
TR a[158:159],v233 offset:45568
TR a[160:161],v232 offset:33792
MM v[96:99],a[232:235],v[192:195],v[96:99]
EX v76,v76
MM v[96:99],a[236:239],v[208:211],v[96:99]
EX v77,v77
MM v[100:103],a[252:255],v[208:211],v[128:131]
TR a[162:163],v232 offset:37888
CV v72,v72,v73
MM v[100:103],a[248:251],v[192:195],v[100:103]
EX v78,v78
MM v[100:103],a[244:247],v[176:179],v[100:103]
TR a[164:165],v232 offset:41984
CV v73,v74,v75
MM v[100:103],a[240:243],v[160:163],v[100:103]
EX v79,v79
MM v[108:111],a[240:243],v[164:167],v[132:135]
s_add_u32 m0, s89, 155648
s_nop 0
buffer_load_dwordx4 v252, s[24:27], s66 offen offset:1024 lds
MM v[108:111],a[244:247],v[180:183],v[108:111]
TR a[166:167],v232 offset:46080
TR a[168:169],v233 offset:33792
MM v[108:111],a[248:251],v[196:199],v[108:111]
EX v80,v80
MM v[108:111],a[252:255],v[212:215],v[108:111]
EX v81,v81
MM v[104:107],a[236:239],v[212:215],v[132:135]
TR a[170:171],v233 offset:37888
CV v74,v76,v77
MM v[104:107],a[232:235],v[196:199],v[104:107]
EX v82,v82
MM v[104:107],a[228:231],v[180:183],v[104:107]
TR a[172:173],v233 offset:41984
TR a[174:175],v233 offset:46080
MM v[104:107],a[224:227],v[164:167],v[104:107]
EX v83,v83
MM v[112:115],a[224:227],v[168:171],v[136:139]
TR a[176:177],v232 offset:34304
CV v75,v78,v79
MM v[112:115],a[228:231],v[184:187],v[112:115]
EX v84,v84
MM v[112:115],a[232:235],v[200:203],v[112:115]
EX v85,v85
MM v[112:115],a[236:239],v[216:219],v[112:115]
TR a[178:179],v232 offset:38400
CV v80,v80,v81
MM v[116:119],a[252:255],v[216:219],v[136:139]
EX v86,v86
MM v[116:119],a[248:251],v[200:203],v[116:119]
TR a[180:181],v232 offset:42496
TR a[182:183],v232 offset:46592
MM v[116:119],a[244:247],v[184:187],v[116:119]
EX v87,v87
MM v[116:119],a[240:243],v[168:171],v[116:119]
TR a[184:185],v233 offset:34304
CV v81,v82,v83
MM v[124:127],a[240:243],v[172:175],v[140:143]
EX v88,v88
MM v[124:127],a[244:247],v[188:191],v[124:127]
TR a[186:187],v233 offset:38400
CV v82,v84,v85
MM v[124:127],a[248:251],v[204:207],v[124:127]
TR a[188:189],v233 offset:42496
TR a[190:191],v233 offset:46592
EX v89,v89
MM v[124:127],a[252:255],v[220:223],v[124:127]
EX v90,v90
MM v[120:123],a[236:239],v[220:223],v[140:143]
CV v83,v86,v87
CV v88,v88,v89
MM v[120:123],a[232:235],v[204:207],v[120:123]
EX v91,v91
MM v[120:123],a[228:231],v[188:191],v[120:123]
EX v92,v92
MM v[120:123],a[224:227],v[172:175],v[120:123]
s_waitcnt vmcnt(24) lgkmcnt(0)
s_barrier
MM a[0:3],a[128:131],v[0:3],a[0:3]
s_add_u32 m0, s65, 32768
EX v93,v93
MM a[0:3],a[132:135],v[4:7],a[0:3]
CV v89,v90,v91
CV v90,v92,v93
MM a[8:11],a[132:135],v[12:15],a[8:11]
EX v94,v94
MM a[8:11],a[128:131],v[8:11],a[8:11]
EX v95,v95
MM a[16:19],a[128:131],v[16:19],a[16:19]
RD a[192:195],v224 offset:0
LPG24_%=:
s_cbranch_execz LPG28_%=
CV v91,v94,v95
MM a[16:19],a[132:135],v[20:23],a[16:19]
EX v96,v96
MM a[24:27],a[132:135],v[28:31],a[24:27]
buffer_load_dwordx4 v228, s[8:11], s64 offen offset:0 lds
MM a[24:27],a[128:131],v[24:27],a[24:27]
EX v97,v97
MM a[28:31],a[136:139],v[24:27],a[28:31]
RD a[196:199],v225 offset:0
CV v68,v96,v97
MM a[28:31],a[140:143],v[28:31],a[28:31]
EX v98,v98
MM a[20:23],a[140:143],v[20:23],a[20:23]
EX v99,v99
MM a[20:23],a[136:139],v[16:19],a[20:23]
TR a[128:129],v232 offset:49152
RD a[200:203],v226 offset:0
MM a[12:15],a[136:139],v[8:11],a[12:15]
EX v100,v100
MM a[12:15],a[140:143],v[12:15],a[12:15]
TR a[130:131],v232 offset:53248
CV v69,v98,v99
MM a[4:7],a[140:143],v[4:7],a[4:7]
EX v101,v101
MM a[4:7],a[136:139],v[0:3],a[4:7]
TR a[132:133],v232 offset:57344
RD a[204:207],v227 offset:0
MM a[32:35],a[144:147],v[0:3],a[32:35]
EX v102,v102
MM a[32:35],a[148:151],v[4:7],a[32:35]
buffer_load_dwordx4 v229, s[8:11], s64 offen offset:1024 lds
MM a[40:43],a[148:151],v[12:15],a[40:43]
TR a[134:135],v232 offset:61440
CV v70,v100,v101
MM a[40:43],a[144:147],v[8:11],a[40:43]
EX v103,v103
MM a[48:51],a[144:147],v[16:19],a[48:51]
TR a[136:137],v233 offset:49152
TR a[138:139],v233 offset:53248
MM a[48:51],a[148:151],v[20:23],a[48:51]
EX v104,v104
MM a[56:59],a[148:151],v[28:31],a[56:59]
EX v105,v105
MM a[56:59],a[144:147],v[24:27],a[56:59]
TR a[140:141],v233 offset:57344
CV v71,v102,v103
MM a[60:63],a[152:155],v[24:27],a[60:63]
EX v106,v106
MM a[60:63],a[156:159],v[28:31],a[60:63]
RD a[208:211],v224 offset:4096
TR a[142:143],v233 offset:61440
MM a[52:55],a[156:159],v[20:23],a[52:55]
EX v107,v107
MM a[52:55],a[152:155],v[16:19],a[52:55]
buffer_load_dwordx4 v230, s[8:11], s64 offen offset:2048 lds
MM a[44:47],a[152:155],v[8:11],a[44:47]
TR a[144:145],v232 offset:49664
RD a[212:215],v225 offset:4096
MM a[44:47],a[156:159],v[12:15],a[44:47]
EX v108,v108
MM a[36:39],a[156:159],v[4:7],a[36:39]
TR a[146:147],v232 offset:53760
CV v76,v104,v105
MM a[36:39],a[152:155],v[0:3],a[36:39]
EX v109,v109
MM a[64:67],a[160:163],v[0:3],a[64:67]
TR a[148:149],v232 offset:57856
CV v77,v106,v107
MM a[64:67],a[164:167],v[4:7],a[64:67]
EX v110,v110
MM a[72:75],a[164:167],v[12:15],a[72:75]
EX v111,v111
MM a[72:75],a[160:163],v[8:11],a[72:75]
TR a[150:151],v232 offset:61952
RD a[216:219],v226 offset:4096
MM a[80:83],a[160:163],v[16:19],a[80:83]
TR a[152:153],v233 offset:49664
CV v78,v108,v109
MM a[80:83],a[164:167],v[20:23],a[80:83]
EX v112,v112
MM a[88:91],a[164:167],v[28:31],a[88:91]
EX v113,v113
MM a[88:91],a[160:163],v[24:27],a[88:91]
TR a[154:155],v233 offset:53760
RD a[220:223],v227 offset:4096
MM a[92:95],a[168:171],v[24:27],a[92:95]
buffer_load_dwordx4 v231, s[8:11], s64 offen offset:3072 lds
MM a[92:95],a[172:175],v[28:31],a[92:95]
s_add_u32 m0, s65, 98304
EX v114,v114
MM a[84:87],a[172:175],v[20:23],a[84:87]
TR a[156:157],v233 offset:57856
TR a[158:159],v233 offset:61952
MM a[84:87],a[168:171],v[16:19],a[84:87]
EX v115,v115
MM a[76:79],a[168:171],v[8:11],a[76:79]
EX v116,v116
MM a[76:79],a[172:175],v[12:15],a[76:79]
TR a[160:161],v232 offset:50176
CV v79,v110,v111
MM a[68:71],a[172:175],v[4:7],a[68:71]
EX v117,v117
MM a[68:71],a[168:171],v[0:3],a[68:71]
TR a[162:163],v232 offset:54272
RD a[224:227],v224 offset:8192
MM a[96:99],a[176:179],v[0:3],a[96:99]
EX v118,v118
MM a[96:99],a[180:183],v[4:7],a[96:99]
TR a[164:165],v232 offset:58368
CV v84,v112,v113
MM a[104:107],a[180:183],v[12:15],a[104:107]
buffer_load_dwordx4 v235, s[12:15], s81 offen offset:0 lds
TR a[166:167],v232 offset:62464
EX v119,v119
MM a[104:107],a[176:179],v[8:11],a[104:107]
RD a[228:231],v225 offset:8192
TR a[168:169],v233 offset:50176
MM a[112:115],a[176:179],v[16:19],a[112:115]
EX v120,v120
MM a[112:115],a[180:183],v[20:23],a[112:115]
TR a[170:171],v233 offset:54272
CV v85,v114,v115
MM a[120:123],a[180:183],v[28:31],a[120:123]
s_waitcnt vmcnt(8)
EX v121,v121
MM a[120:123],a[176:179],v[24:27],a[120:123]
s_lshr_b32 s77, %[sb], 3
RD a[232:235],v226 offset:8192
TR a[172:173],v233 offset:58368
MM a[124:127],a[184:187],v[24:27],a[124:127]
s_lshl_b32 s77, s77, 2
v_add_f32 v244, v144, v145
v_add_f32 v245, v148, v149
EX v122,v122
MM a[124:127],a[188:191],v[28:31],a[124:127]
s_add_u32 s77, s77, %[swave]
v_add_f32 v246, v152, v153
v_add_f32 v247, v156, v157
CV v86,v116,v117
MM a[116:119],a[188:191],v[20:23],a[116:119]
s_lshl_b32 s77, s77, 9
TR a[174:175],v233 offset:62464
buffer_load_dwordx4 v235, s[12:15], s64 offen offset:1024 lds
EX v123,v123
MM a[116:119],a[184:187],v[16:19],a[116:119]
s_add_u32 s77, s77, 0
TR a[176:177],v232 offset:50688
v_lshrrev_b32 v253, 1, v248
v_add_u32 v253, s77, v253
MM a[108:111],a[184:187],v[8:11],a[108:111]
s_mov_b32 s77, LKEND_ka-LPD2_%=-16
RD a[236:239],v227 offset:8192
v_min_u32 v253, s77, v253
EX v124,v124
CV v87,v118,v119
MM a[108:111],a[188:191],v[12:15],a[108:111]
s_getpc_b64 s[98:99]
LPD2_%=:
global_load_dword v253, v253, s[98:99]
TR a[178:179],v232 offset:54784
EX v125,v125
MM a[100:103],a[188:191],v[4:7],a[100:103]
TR a[180:181],v232 offset:58880
)ASM"
R"ASM(
RD a[240:243],v224 offset:12288
MM a[100:103],a[184:187],v[0:3],a[100:103]
EX v126,v126
v_smfmac_f32_16x16x64_bf16 v[144:147], v[236:239], v[64:71], v234
RD a[244:247],v225 offset:12288
RD a[248:251],v226 offset:12288
RD a[252:255],v227 offset:12288
EX v127,v127
v_smfmac_f32_16x16x64_bf16 v[148:151], v[236:239], v[72:79], v234
TR a[182:183],v232 offset:62976
CV v92,v120,v121
v_smfmac_f32_16x16x64_bf16 v[152:155], v[236:239], v[80:87], v234
buffer_load_dwordx4 v235, s[12:15], s81 offen offset:2048 lds
CV v93,v122,v123
CV v94,v124,v125
CV v95,v126,v127
s_nop 1
v_smfmac_f32_16x16x64_bf16 v[156:159], v[236:239], v[88:95], v234
TR a[184:185],v233 offset:50688
TR a[186:187],v233 offset:54784
TR a[188:189],v233 offset:58880
TR a[190:191],v233 offset:62976
s_waitcnt lgkmcnt(5)
buffer_load_dwordx4 v235, s[12:15], s64 offen offset:3072 lds
MM v[16:19],a[192:195],v[168:171],v[136:139]
MM v[16:19],a[196:199],v[184:187],v[16:19]
MM v[16:19],a[200:203],v[200:203],v[16:19]
v_max3_f32 v249, v144, v145, v148
v_max3_f32 v249, v249, v149, v152
MM v[16:19],a[204:207],v[216:219],v[16:19]
v_max3_f32 v249, v249, v153, v156
v_max_f32 v249, v249, v157
MM v[20:23],a[220:223],v[216:219],v[136:139]
v_cmp_lt_f32 vcc, s71, v249
MM v[20:23],a[216:219],v[200:203],v[20:23]
s_cbranch_vccnz LSITE35_%=
LBACK35_%=:
v_add_f32 v244, v144, v145
MM v[20:23],a[212:215],v[184:187],v[20:23]
v_add_f32 v245, v148, v149
v_add_f32 v246, v152, v153
MM v[20:23],a[208:211],v[168:171],v[20:23]
s_waitcnt lgkmcnt(0)
v_add_f32 v247, v156, v157
MM a[0:3],a[128:131],v[64:67],a[0:3]
EX v16,v16
MM a[0:3],a[132:135],v[68:71],a[0:3]
v_rcp_f32 v145, v244
MM a[8:11],a[132:135],v[76:79],a[8:11]
EX v17,v17
MM a[8:11],a[128:131],v[72:75],a[8:11]
EX v18,v18
MM a[12:15],a[136:139],v[72:75],a[12:15]
v_rcp_f32 v149, v245
MM a[12:15],a[140:143],v[76:79],a[12:15]
CV v16,v16,v17
v_bfe_u32 v144, v241, 0, 8
MM a[4:7],a[140:143],v[68:71],a[4:7]
EX v19,v19
MM a[4:7],a[136:139],v[64:67],a[4:7]
CV v17,v18,v19
v_lshl_or_b32 v144, v144, 1, 1
MM a[32:35],a[144:147],v[64:67],a[32:35]
EX v20,v20
MM a[32:35],a[148:151],v[68:71],a[32:35]
v_lshlrev_b32 v144, 22, v144
v_mul_f32 v144, -v144, v145
MM a[40:43],a[148:151],v[76:79],a[40:43]
v_bfe_u32 v148, v241, 8, 8
v_lshl_or_b32 v148, v148, 1, 1
MM a[40:43],a[144:147],v[72:75],a[40:43]
v_lshlrev_b32 v148, 22, v148
v_mul_f32 v148, -v148, v149
MM a[44:47],a[152:155],v[72:75],a[44:47]
s_mul_i32 s78, s49, 17408
s_add_u32 s78, s83, s78
s_add_u32 s78, s78, 0
s_add_u32 s79, s78, 4096
AR v0,a0
AR v1,a1
AR v2,a2
MM a[44:47],a[156:159],v[76:79],a[44:47]
AR v3,a3
AR v4,a4
AR v5,a5
MM a[36:39],a[156:159],v[68:71],a[36:39]
AR v6,a6
AR v7,a7
v_fma_f32 v0, v0, v145, v144
v_fma_f32 v1, v1, v145, v144
MM a[36:39],a[152:155],v[64:67],a[36:39]
v_fma_f32 v2, v2, v145, v144
v_fma_f32 v3, v3, v145, v144
v_fma_f32 v4, v4, v145, v144
v_fma_f32 v5, v5, v145, v144
MM a[64:67],a[160:163],v[64:67],a[64:67]
v_fma_f32 v6, v6, v145, v144
v_fma_f32 v7, v7, v145, v144
CV v160,v0,v1
CV v161,v2,v3
MM a[64:67],a[164:167],v[68:71],a[64:67]
CV v162,v4,v5
CV v163,v6,v7
MM a[72:75],a[164:167],v[76:79],a[72:75]
buffer_store_dwordx4 v[160:163], v248, s[20:23], s78 offen offset:0 sc1
AR v8,a8
AR v9,a9
AR v10,a10
MM a[72:75],a[160:163],v[72:75],a[72:75]
AR v11,a11
AR v12,a12
AR v13,a13
EX v21,v21
AR v14,a14
MM a[76:79],a[168:171],v[72:75],a[76:79]
AR v15,a15
v_fma_f32 v8, v8, v149, v148
v_fma_f32 v9, v9, v149, v148
v_fma_f32 v10, v10, v149, v148
MM a[76:79],a[172:175],v[76:79],a[76:79]
v_fma_f32 v11, v11, v149, v148
v_fma_f32 v12, v12, v149, v148
v_fma_f32 v13, v13, v149, v148
v_fma_f32 v14, v14, v149, v148
EX v22,v22
MM a[68:71],a[172:175],v[68:71],a[68:71]
v_fma_f32 v15, v15, v149, v148
CV v164,v8,v9
CV v165,v10,v11
CV v166,v12,v13
MM a[68:71],a[168:171],v[64:67],a[68:71]
CV v167,v14,v15
CV v18,v20,v21
MM a[96:99],a[176:179],v[64:67],a[96:99]
buffer_store_dwordx4 v[164:167], v248, s[20:23], s79 offen offset:0 sc1
AR v32,a32
AR v33,a33
AR v34,a34
EX v23,v23
AR v35,a35
MM a[96:99],a[180:183],v[68:71],a[96:99]
AR v36,a36
AR v37,a37
AR v38,a38
AR v39,a39
MM a[104:107],a[180:183],v[76:79],a[104:107]
v_fma_f32 v32, v32, v145, v144
v_fma_f32 v33, v33, v145, v144
v_fma_f32 v34, v34, v145, v144
v_fma_f32 v35, v35, v145, v144
MM a[104:107],a[176:179],v[72:75],a[104:107]
v_fma_f32 v36, v36, v145, v144
v_fma_f32 v37, v37, v145, v144
v_fma_f32 v38, v38, v145, v144
MM a[108:111],a[184:187],v[72:75],a[108:111]
v_fma_f32 v39, v39, v145, v144
CV v176,v32,v33
CV v177,v34,v35
CV v178,v36,v37
MM a[108:111],a[188:191],v[76:79],a[108:111]
CV v179,v38,v39
CV v19,v22,v23
MM a[100:103],a[188:191],v[68:71],a[100:103]
buffer_store_dwordx4 v[176:179], v248, s[20:23], s78 offen offset:1024 sc1
AR v40,a40
AR v41,a41
AR v42,a42
AR v43,a43
AR v44,a44
MM a[100:103],a[184:187],v[64:67],a[100:103]
AR v45,a45
AR v46,a46
AR v47,a47
v_fma_f32 v40, v40, v149, v148
MM v[28:31],a[208:211],v[172:175],v[140:143]
v_fma_f32 v41, v41, v149, v148
v_fma_f32 v42, v42, v149, v148
v_fma_f32 v43, v43, v149, v148
MM v[28:31],a[212:215],v[188:191],v[28:31]
v_fma_f32 v44, v44, v149, v148
v_fma_f32 v45, v45, v149, v148
v_fma_f32 v46, v46, v149, v148
v_fma_f32 v47, v47, v149, v148
MM v[28:31],a[216:219],v[204:207],v[28:31]
CV v180,v40,v41
CV v181,v42,v43
CV v182,v44,v45
CV v183,v46,v47
MM v[28:31],a[220:223],v[220:223],v[28:31]
buffer_store_dwordx4 v[180:183], v248, s[20:23], s79 offen offset:1024 sc1
AR v0,a64
AR v1,a65
MM v[24:27],a[204:207],v[220:223],v[140:143]
AR v2,a66
AR v3,a67
AR v4,a68
MM v[24:27],a[200:203],v[204:207],v[24:27]
AR v5,a69
AR v6,a70
AR v7,a71
v_fma_f32 v0, v0, v145, v144
MM v[24:27],a[196:199],v[188:191],v[24:27]
v_fma_f32 v1, v1, v145, v144
v_fma_f32 v2, v2, v145, v144
v_fma_f32 v3, v3, v145, v144
v_fma_f32 v4, v4, v145, v144
MM v[24:27],a[192:195],v[172:175],v[24:27]
v_fma_f32 v5, v5, v145, v144
v_fma_f32 v6, v6, v145, v144
v_fma_f32 v7, v7, v145, v144
CV v192,v0,v1
MM v[48:51],a[224:227],v[168:171],v[136:139]
CV v193,v2,v3
CV v194,v4,v5
CV v195,v6,v7
MM v[48:51],a[228:231],v[184:187],v[48:51]
buffer_store_dwordx4 v[192:195], v248, s[20:23], s78 offen offset:2048 sc1
AR v8,a72
AR v9,a73
MM v[48:51],a[232:235],v[200:203],v[48:51]
AR v10,a74
AR v11,a75
AR v12,a76
AR v13,a77
MM v[48:51],a[236:239],v[216:219],v[48:51]
AR v14,a78
AR v15,a79
v_fma_f32 v8, v8, v149, v148
v_fma_f32 v9, v9, v149, v148
MM v[52:55],a[252:255],v[216:219],v[136:139]
EX v24,v24
EX v25,v25
EX v26,v26
EX v27,v27
EX v28,v28
EX v29,v29
v_fma_f32 v10, v10, v149, v148
v_fma_f32 v11, v11, v149, v148
EX v30,v30
v_fma_f32 v12, v12, v149, v148
v_fma_f32 v13, v13, v149, v148
MM v[52:55],a[248:251],v[200:203],v[52:55]
v_fma_f32 v14, v14, v149, v148
v_fma_f32 v15, v15, v149, v148
CV v196,v8,v9
EX v31,v31
CV v24,v24,v25
CV v197,v10,v11
MM v[52:55],a[244:247],v[184:187],v[52:55]
CV v198,v12,v13
CV v199,v14,v15
MM v[52:55],a[240:243],v[168:171],v[52:55]
buffer_store_dwordx4 v[196:199], v248, s[20:23], s79 offen offset:2048 sc1
AR v32,a96
AR v33,a97
AR v34,a98
MM v[60:63],a[240:243],v[172:175],v[140:143]
AR v35,a99
AR v36,a100
AR v37,a101
EX v48,v48
AR v38,a102
MM v[60:63],a[244:247],v[188:191],v[60:63]
AR v39,a103
v_fma_f32 v32, v32, v145, v144
v_fma_f32 v33, v33, v145, v144
v_fma_f32 v34, v34, v145, v144
CV v25,v26,v27
MM v[60:63],a[248:251],v[204:207],v[60:63]
v_fma_f32 v35, v35, v145, v144
v_fma_f32 v36, v36, v145, v144
v_fma_f32 v37, v37, v145, v144
MM v[60:63],a[252:255],v[220:223],v[60:63]
v_fma_f32 v38, v38, v145, v144
v_fma_f32 v39, v39, v145, v144
CV v208,v32,v33
EX v49,v49
CV v209,v34,v35
MM v[56:59],a[236:239],v[220:223],v[140:143]
CV v210,v36,v37
CV v211,v38,v39
MM v[56:59],a[232:235],v[204:207],v[56:59]
buffer_store_dwordx4 v[208:211], v248, s[20:23], s78 offen offset:3072 sc1
AR v40,a104
AR v41,a105
AR v42,a106
AR v43,a107
EX v50,v50
MM v[56:59],a[228:231],v[188:191],v[56:59]
AR v44,a108
AR v45,a109
AR v46,a110
AR v47,a111
CV v26,v28,v29
MM v[56:59],a[224:227],v[172:175],v[56:59]
s_waitcnt lgkmcnt(0)
s_barrier
v_fma_f32 v40, v40, v149, v148
v_fma_f32 v41, v41, v149, v148
v_fma_f32 v42, v42, v149, v148
MM a[16:19],a[128:131],v[80:83],a[16:19]
s_xor_b32 s66, s94, 4
v_fma_f32 v43, v43, v149, v148
v_fma_f32 v44, v44, v149, v148
v_fma_f32 v45, v45, v149, v148
v_fma_f32 v46, v46, v149, v148
EX v51,v51
MM a[16:19],a[132:135],v[84:87],a[16:19]
s_mul_i32 s66, s66, 163840
v_fma_f32 v47, v47, v149, v148
CV v212,v40,v41
CV v213,v42,v43
CV v214,v44,v45
MM a[24:27],a[132:135],v[92:95],a[24:27]
s_add_u32 s66, s66, s87
CV v215,v46,v47
CV v27,v30,v31
MM a[24:27],a[128:131],v[88:91],a[24:27]
s_add_u32 m0, s65, 114688
buffer_store_dwordx4 v[212:215], v248, s[20:23], s79 offen offset:3072 sc1
EX v52,v52
MM a[28:31],a[136:139],v[88:91],a[28:31]
EX v53,v53
MM a[28:31],a[140:143],v[92:95],a[28:31]
RD a[192:195],v224 offset:16384
CV v20,v48,v49
MM a[20:23],a[140:143],v[84:87],a[20:23]
buffer_load_dwordx4 v240, s[24:27], s66 offen offset:0 lds
MM a[20:23],a[136:139],v[80:83],a[20:23]
s_add_u32 s66, s66, 9216
EX v54,v54
MM a[48:51],a[144:147],v[80:83],a[48:51]
RD a[196:199],v225 offset:16384
CV v21,v50,v51
MM a[48:51],a[148:151],v[84:87],a[48:51]
EX v55,v55
MM a[56:59],a[148:151],v[92:95],a[56:59]
RD a[200:203],v226 offset:16384
RD a[204:207],v227 offset:16384
MM a[56:59],a[144:147],v[88:91],a[56:59]
buffer_load_dwordx4 v240, s[24:27], s66 offen offset:1024 lds
MM a[60:63],a[152:155],v[88:91],a[60:63]
s_add_u32 s66, s66, 9216
EX v56,v56
MM a[60:63],a[156:159],v[92:95],a[60:63]
RD a[208:211],v224 offset:20480
CV v22,v52,v53
MM a[52:55],a[156:159],v[84:87],a[52:55]
buffer_load_dwordx4 v240, s[24:27], s66 offen offset:2048 lds
MM a[52:55],a[152:155],v[80:83],a[52:55]
s_add_u32 s66, s66, 9216
EX v57,v57
MM a[80:83],a[160:163],v[80:83],a[80:83]
RD a[212:215],v225 offset:20480
RD a[216:219],v226 offset:20480
MM a[80:83],a[164:167],v[84:87],a[80:83]
buffer_load_dwordx4 v240, s[24:27], s66 offen offset:3072 lds
MM a[88:91],a[164:167],v[92:95],a[88:91]
s_xor_b32 s66, s94, 4
EX v58,v58
MM a[88:91],a[160:163],v[88:91],a[88:91]
s_mul_i32 s66, s66, 163840
RD a[220:223],v227 offset:20480
CV v23,v54,v55
MM a[92:95],a[168:171],v[88:91],a[92:95]
s_add_u32 s66, s66, s86
EX v59,v59
MM a[92:95],a[172:175],v[92:95],a[92:95]
s_add_u32 m0, s65, 49152
RD a[224:227],v224 offset:24576
RD a[228:231],v225 offset:24576
MM a[84:87],a[172:175],v[84:87],a[84:87]
buffer_load_dwordx4 v240, s[24:27], s66 offen offset:0 lds
MM a[84:87],a[168:171],v[80:83],a[84:87]
s_add_u32 s66, s66, 9216
EX v60,v60
MM a[112:115],a[176:179],v[80:83],a[112:115]
RD a[232:235],v226 offset:24576
CV v28,v56,v57
MM a[112:115],a[180:183],v[84:87],a[112:115]
EX v61,v61
MM a[120:123],a[180:183],v[92:95],a[120:123]
buffer_load_dwordx4 v240, s[24:27], s66 offen offset:1024 lds
MM a[120:123],a[176:179],v[88:91],a[120:123]
s_add_u32 s66, s66, 9216
RD a[236:239],v227 offset:24576
RD a[240:243],v224 offset:28672
MM a[124:127],a[184:187],v[88:91],a[124:127]
EX v62,v62
MM a[124:127],a[188:191],v[92:95],a[124:127]
buffer_load_dwordx4 v240, s[24:27], s66 offen offset:2048 lds
MM a[116:119],a[188:191],v[84:87],a[116:119]
s_add_u32 s66, s66, 9216
RD a[244:247],v225 offset:28672
RD a[248:251],v226 offset:28672
RD a[252:255],v227 offset:28672
EX v63,v63
MM a[116:119],a[184:187],v[80:83],a[116:119]
buffer_load_dwordx4 v240, s[24:27], s66 offen offset:3072 lds
v_smfmac_f32_16x16x64_bf16 v[152:155], v[236:239], v[16:23], v234
s_nop 0
LPG25_%=:
s_cbranch_execz LPG29_%=
CV v29,v58,v59
CV v30,v60,v61
CV v31,v62,v63
s_nop 1
v_smfmac_f32_16x16x64_bf16 v[156:159], v[236:239], v[24:31], v234
s_waitcnt lgkmcnt(0)
MM v[80:83],a[192:195],v[168:171],v[136:139]
TR a[128:129],v232 offset:0
TR a[130:131],v232 offset:4096
MM v[80:83],a[196:199],v[184:187],v[80:83]
TR a[132:133],v232 offset:8192
TR a[134:135],v232 offset:12288
MM v[80:83],a[200:203],v[200:203],v[80:83]
TR a[136:137],v233 offset:0
TR a[138:139],v233 offset:4096
MM v[80:83],a[204:207],v[216:219],v[80:83]
v_max3_f32 v249, v152, v153, v156
v_max_f32 v249, v249, v157
MM v[84:87],a[220:223],v[216:219],v[136:139]
TR a[140:141],v233 offset:8192
v_cmp_lt_f32 vcc, s71, v249
MM v[84:87],a[216:219],v[200:203],v[84:87]
)ASM"
R"ASM(
s_cbranch_vccnz LSITE36_%=
LBACK36_%=:
TR a[142:143],v233 offset:12288
v_add_f32 v246, v152, v153
MM v[84:87],a[212:215],v[184:187],v[84:87]
TR a[144:145],v232 offset:512
v_add_f32 v247, v156, v157
MM v[84:87],a[208:211],v[168:171],v[84:87]
TR a[146:147],v232 offset:4608
TR a[148:149],v232 offset:8704
MM v[92:95],a[208:211],v[172:175],v[140:143]
s_lshl_b32 s97, s84, 2
s_add_u32 s97, s97, s96
s_add_u32 s97, s97, 0
MM v[92:95],a[212:215],v[188:191],v[92:95]
s_waitcnt vmcnt(8)
v_mov_b32 v242, s69
s_mov_b64 exec, 1
buffer_store_dword v242, off, s[20:23], s97 offset:0 sc1
s_mov_b64 exec, -1
MM v[92:95],a[216:219],v[204:207],v[92:95]
v_lshrrev_b32 v98, 1, v248
v_min_u32 v98, 312, v98
MM v[92:95],a[220:223],v[220:223],v[92:95]
s_mov_b32 s62, 94464
buffer_load_dwordx2 v[96:97], v98, s[20:23], s62 offen offset:0 sc1
MM v[88:91],a[204:207],v[220:223],v[140:143]
TR a[150:151],v232 offset:12800
EX v80,v80
MM v[88:91],a[200:203],v[204:207],v[88:91]
TR a[152:153],v233 offset:512
TR a[154:155],v233 offset:4608
EX v81,v81
MM v[88:91],a[196:199],v[188:191],v[88:91]
TR a[156:157],v233 offset:8704
TR a[158:159],v233 offset:12800
MM v[88:91],a[192:195],v[172:175],v[88:91]
EX v82,v82
MM v[112:115],a[224:227],v[168:171],v[136:139]
TR a[160:161],v232 offset:1024
TR a[162:163],v232 offset:5120
EX v83,v83
MM v[112:115],a[228:231],v[184:187],v[112:115]
TR a[164:165],v232 offset:9216
TR a[166:167],v232 offset:13312
MM v[112:115],a[232:235],v[200:203],v[112:115]
EX v84,v84
MM v[112:115],a[236:239],v[216:219],v[112:115]
TR a[168:169],v233 offset:1024
TR a[170:171],v233 offset:5120
MM v[116:119],a[252:255],v[216:219],v[136:139]
EX v85,v85
MM v[116:119],a[248:251],v[200:203],v[116:119]
TR a[172:173],v233 offset:9216
TR a[174:175],v233 offset:13312
MM v[116:119],a[244:247],v[184:187],v[116:119]
TR a[176:177],v232 offset:1536
EX v86,v86
MM v[116:119],a[240:243],v[168:171],v[116:119]
TR a[178:179],v232 offset:5632
CV v80,v80,v81
EX v87,v87
MM v[124:127],a[240:243],v[172:175],v[140:143]
TR a[180:181],v232 offset:9728
TR a[182:183],v232 offset:13824
MM v[124:127],a[244:247],v[188:191],v[124:127]
EX v88,v88
MM v[124:127],a[248:251],v[204:207],v[124:127]
TR a[184:185],v233 offset:1536
TR a[186:187],v233 offset:5632
TR a[188:189],v233 offset:9728
TR a[190:191],v233 offset:13824
EX v89,v89
MM v[124:127],a[252:255],v[220:223],v[124:127]
CV v81,v82,v83
CV v82,v84,v85
MM v[120:123],a[236:239],v[220:223],v[140:143]
EX v90,v90
MM v[120:123],a[232:235],v[204:207],v[120:123]
EX v91,v91
MM v[120:123],a[228:231],v[188:191],v[120:123]
CV v83,v86,v87
CV v88,v88,v89
MM v[120:123],a[224:227],v[172:175],v[120:123]
s_waitcnt lgkmcnt(0)
s_barrier
CV v89,v90,v91
EX v92,v92
MM a[16:19],a[128:131],v[16:19],a[16:19]
s_xor_b32 s66, s94, 5
EX v93,v93
MM a[16:19],a[132:135],v[20:23],a[16:19]
s_mul_i32 s66, s66, 163840
EX v94,v94
MM a[24:27],a[132:135],v[28:31],a[24:27]
s_add_u32 s66, s66, s87
EX v95,v95
MM a[24:27],a[128:131],v[24:27],a[24:27]
s_add_u32 m0, s65, 65536
RD a[192:195],v224 offset:32768
CV v90,v92,v93
MM a[28:31],a[136:139],v[24:27],a[28:31]
EX v112,v112
MM a[28:31],a[140:143],v[28:31],a[28:31]
buffer_load_dwordx4 v240, s[24:27], s66 offen offset:0 lds
MM a[20:23],a[140:143],v[20:23],a[20:23]
s_add_u32 s66, s66, 9216
EX v113,v113
MM a[20:23],a[136:139],v[16:19],a[20:23]
RD a[196:199],v225 offset:32768
CV v91,v94,v95
MM a[48:51],a[144:147],v[16:19],a[48:51]
v_lshrrev_b32 v243, 2, v248
v_min_u32 v243, 12, v243
MM a[48:51],a[148:151],v[20:23],a[48:51]
buffer_load_dword v242, v243, s[20:23], s44 offen offset:0 sc1
EX v114,v114
MM a[56:59],a[148:151],v[28:31],a[56:59]
RD a[200:203],v226 offset:32768
RD a[204:207],v227 offset:32768
MM a[56:59],a[144:147],v[24:27],a[56:59]
EX v115,v115
MM a[60:63],a[152:155],v[24:27],a[60:63]
buffer_load_dwordx4 v240, s[24:27], s66 offen offset:1024 lds
MM a[60:63],a[156:159],v[28:31],a[60:63]
s_add_u32 s66, s66, 9216
EX v116,v116
MM a[52:55],a[156:159],v[20:23],a[52:55]
EX v117,v117
MM a[52:55],a[152:155],v[16:19],a[52:55]
RD a[208:211],v224 offset:36864
RD a[212:215],v225 offset:36864
buffer_load_dwordx4 v240, s[24:27], s66 offen offset:2048 lds
MM a[80:83],a[160:163],v[16:19],a[80:83]
s_add_u32 s66, s66, 9216
EX v118,v118
MM a[80:83],a[164:167],v[20:23],a[80:83]
RD a[216:219],v226 offset:36864
CV v84,v112,v113
MM a[88:91],a[164:167],v[28:31],a[88:91]
EX v119,v119
MM a[88:91],a[160:163],v[24:27],a[88:91]
buffer_load_dwordx4 v240, s[24:27], s66 offen offset:3072 lds
EX v120,v120
MM a[92:95],a[168:171],v[24:27],a[92:95]
s_xor_b32 s66, s94, 5
RD a[220:223],v227 offset:36864
CV v85,v114,v115
MM a[92:95],a[172:175],v[28:31],a[92:95]
s_mul_i32 s66, s66, 163840
EX v121,v121
MM a[84:87],a[172:175],v[20:23],a[84:87]
s_add_u32 s66, s66, s86
RD a[224:227],v224 offset:40960
CV v86,v116,v117
MM a[84:87],a[168:171],v[16:19],a[84:87]
s_add_u32 m0, s65, 0
EX v122,v122
MM a[112:115],a[176:179],v[16:19],a[112:115]
buffer_load_dwordx4 v240, s[24:27], s66 offen offset:0 lds
RD a[228:231],v225 offset:40960
EX v123,v123
MM a[112:115],a[180:183],v[20:23],a[112:115]
s_add_u32 s66, s66, 9216
RD a[232:235],v226 offset:40960
CV v87,v118,v119
MM a[120:123],a[180:183],v[28:31],a[120:123]
EX v124,v124
MM a[120:123],a[176:179],v[24:27],a[120:123]
buffer_load_dwordx4 v240, s[24:27], s66 offen offset:1024 lds
MM a[124:127],a[184:187],v[24:27],a[124:127]
s_add_u32 s66, s66, 9216
RD a[236:239],v227 offset:40960
EX v125,v125
MM a[124:127],a[188:191],v[28:31],a[124:127]
EX v126,v126
MM a[116:119],a[188:191],v[20:23],a[116:119]
RD a[240:243],v224 offset:45056
RD a[244:247],v225 offset:45056
RD a[248:251],v226 offset:45056
RD a[252:255],v227 offset:45056
EX v127,v127
MM a[116:119],a[184:187],v[16:19],a[116:119]
buffer_load_dwordx4 v240, s[24:27], s66 offen offset:2048 lds
CV v92,v120,v121
v_smfmac_f32_16x16x64_bf16 v[152:155], v[236:239], v[80:87], v234
s_add_u32 s66, s66, 9216
CV v93,v122,v123
CV v94,v124,v125
CV v95,v126,v127
s_nop 1
v_smfmac_f32_16x16x64_bf16 v[156:159], v[236:239], v[88:95], v234
s_waitcnt lgkmcnt(0)
buffer_load_dwordx4 v240, s[24:27], s66 offen offset:3072 lds
v_lshrrev_b32 v145, 9, v248
v_mul_u32_u24 v145, 1536, v145
v_sub_u32 v244, v232, v145
v_sub_u32 v235, v233, v145
v_add_u32 v244, 65536, v244
v_add_u32 v235, 65536, v235
v_and_b32 v145, 960, v248
v_lshlrev_b32 v145, 1, v145
v_bfe_u32 v146, v248, 7, 2
v_lshl_add_u32 v145, v146, 5, v145
v_bfe_u32 v146, v248, 4, 2
v_lshl_add_u32 v145, v146, 3, v145
v_add_u32 v228, %[slds], v145
v_xor_b32 v229, 32, v228
v_xor_b32 v230, 64, v228
v_xor_b32 v231, 96, v228
v_add_u32 v144, 65536, v228
v_add_u32 v145, 65536, v229
v_add_u32 v146, 65536, v230
v_add_u32 v147, 65536, v231
MM v[16:19],a[192:195],v[168:171],v[136:139]
TR a[128:129],v232 offset:16384
TR a[130:131],v232 offset:20480
MM v[16:19],a[196:199],v[184:187],v[16:19]
TR a[132:133],v232 offset:24576
TR a[134:135],v232 offset:28672
MM v[16:19],a[200:203],v[200:203],v[16:19]
TR a[136:137],v233 offset:16384
TR a[138:139],v233 offset:20480
MM v[16:19],a[204:207],v[216:219],v[16:19]
v_max3_f32 v249, v152, v153, v156
v_max_f32 v249, v249, v157
MM v[20:23],a[220:223],v[216:219],v[136:139]
TR a[140:141],v233 offset:24576
v_cmp_lt_f32 vcc, s71, v249
MM v[20:23],a[216:219],v[200:203],v[20:23]
s_cbranch_vccnz LSITE37_%=
LBACK37_%=:
TR a[142:143],v233 offset:28672
v_add_f32 v246, v152, v153
MM v[20:23],a[212:215],v[184:187],v[20:23]
TR a[144:145],v232 offset:16896
v_add_f32 v247, v156, v157
MM v[20:23],a[208:211],v[168:171],v[20:23]
TR a[146:147],v232 offset:20992
TR a[148:149],v232 offset:25088
MM v[28:31],a[208:211],v[172:175],v[140:143]
s_waitcnt vmcnt(7)
v_cmp_ne_u32 vcc, s69, v242
s_cbranch_vccnz LRPO0_%=
LRPO0b_%=:
MM v[28:31],a[212:215],v[188:191],v[28:31]
s_xor_b32 s92, s49, 4
s_mul_i32 s92, s92, 17408
s_add_u32 s92, s83, s92
MM v[28:31],a[216:219],v[204:207],v[28:31]
buffer_load_dwordx4 a[0:3], v248, s[20:23], s92 offen offset:0 sc1
MM v[28:31],a[220:223],v[220:223],v[28:31]
buffer_load_dwordx4 a[32:35], v248, s[20:23], s92 offen offset:1024 sc1
MM v[24:27],a[204:207],v[220:223],v[140:143]
TR a[150:151],v232 offset:29184
v_xor_b32 v96, s69, v96
v_xor_b32 v97, s69, v97
EX v16,v16
MM v[24:27],a[200:203],v[204:207],v[24:27]
TR a[152:153],v233 offset:16896
TR a[154:155],v233 offset:20992
v_or_b32 v99, v96, v97
EX v17,v17
MM v[24:27],a[196:199],v[188:191],v[24:27]
buffer_load_dwordx4 a[64:67], v248, s[20:23], s92 offen offset:2048 sc1
TR a[156:157],v233 offset:25088
MM v[24:27],a[192:195],v[172:175],v[24:27]
TR a[158:159],v233 offset:29184
v_cmp_ne_u32 vcc, 0, v99
s_cbranch_vccnz LRPW_%=
LRPWb_%=:
EX v18,v18
MM v[48:51],a[224:227],v[168:171],v[136:139]
TR a[160:161],v232 offset:17408
TR a[162:163],v232 offset:21504
EX v19,v19
MM v[48:51],a[228:231],v[184:187],v[48:51]
buffer_load_dwordx4 a[96:99], v248, s[20:23], s92 offen offset:3072 sc1
TR a[164:165],v232 offset:25600
MM v[48:51],a[232:235],v[200:203],v[48:51]
s_add_u32 s92, s92, 4096
TR a[166:167],v232 offset:29696
TR a[168:169],v233 offset:17408
EX v20,v20
MM v[48:51],a[236:239],v[216:219],v[48:51]
TR a[170:171],v233 offset:21504
TR a[172:173],v233 offset:25600
MM v[52:55],a[252:255],v[216:219],v[136:139]
buffer_load_dwordx4 a[4:7], v248, s[20:23], s92 offen offset:0 sc1
EX v21,v21
MM v[52:55],a[248:251],v[200:203],v[52:55]
TR a[174:175],v233 offset:29696
CV v16,v16,v17
MM v[52:55],a[244:247],v[184:187],v[52:55]
TR a[176:177],v232 offset:17920
EX v22,v22
MM v[52:55],a[240:243],v[168:171],v[52:55]
buffer_load_dwordx4 a[36:39], v248, s[20:23], s92 offen offset:1024 sc1
TR a[178:179],v232 offset:22016
EX v23,v23
MM v[60:63],a[240:243],v[172:175],v[140:143]
TR a[180:181],v232 offset:26112
TR a[182:183],v232 offset:30208
MM v[60:63],a[244:247],v[188:191],v[60:63]
EX v24,v24
MM v[60:63],a[248:251],v[204:207],v[60:63]
buffer_load_dwordx4 a[68:71], v248, s[20:23], s92 offen offset:2048 sc1
TR a[184:185],v233 offset:17920
TR a[186:187],v233 offset:22016
TR a[188:189],v233 offset:26112
TR a[190:191],v233 offset:30208
EX v25,v25
MM v[60:63],a[252:255],v[220:223],v[60:63]
EX v26,v26
CV v17,v18,v19
MM v[56:59],a[236:239],v[220:223],v[140:143]
EX v27,v27
MM v[56:59],a[232:235],v[204:207],v[56:59]
buffer_load_dwordx4 a[100:103], v248, s[20:23], s92 offen offset:3072 sc1
MM v[56:59],a[228:231],v[188:191],v[56:59]
EX v28,v28
CV v18,v20,v21
MM v[56:59],a[224:227],v[172:175],v[56:59]
s_waitcnt lgkmcnt(0)
s_barrier
CV v19,v22,v23
MM a[16:19],a[128:131],v[80:83],a[16:19]
s_xor_b32 s66, s94, 6
EX v29,v29
MM a[16:19],a[132:135],v[84:87],a[16:19]
s_mul_i32 s66, s66, 163840
CV v24,v24,v25
CV v25,v26,v27
MM a[24:27],a[132:135],v[92:95],a[24:27]
s_add_u32 s66, s66, s87
EX v30,v30
MM a[24:27],a[128:131],v[88:91],a[24:27]
s_add_u32 m0, s65, 81920
EX v31,v31
MM a[28:31],a[136:139],v[88:91],a[28:31]
EX v48,v48
MM a[28:31],a[140:143],v[92:95],a[28:31]
CV v26,v28,v29
CV v27,v30,v31
MM a[20:23],a[140:143],v[84:87],a[20:23]
buffer_load_dwordx4 v240, s[24:27], s66 offen offset:0 lds
MM a[20:23],a[136:139],v[80:83],a[20:23]
s_add_u32 s66, s66, 9216
EX v49,v49
MM a[48:51],a[144:147],v[80:83],a[48:51]
EX v50,v50
MM a[48:51],a[148:151],v[84:87],a[48:51]
buffer_load_dwordx4 v240, s[24:27], s66 offen offset:1024 lds
MM a[56:59],a[148:151],v[92:95],a[56:59]
s_add_u32 s66, s66, 9216
EX v51,v51
MM a[56:59],a[144:147],v[88:91],a[56:59]
TR a[128:129],v232 offset:32768
CV v20,v48,v49
MM a[60:63],a[152:155],v[88:91],a[60:63]
EX v52,v52
MM a[60:63],a[156:159],v[92:95],a[60:63]
TR a[130:131],v232 offset:36864
TR a[132:133],v232 offset:40960
MM a[52:55],a[156:159],v[84:87],a[52:55]
EX v53,v53
MM a[52:55],a[152:155],v[80:83],a[52:55]
TR a[134:135],v232 offset:45056
TR a[136:137],v233 offset:32768
buffer_load_dwordx4 v240, s[24:27], s66 offen offset:2048 lds
MM a[80:83],a[160:163],v[80:83],a[80:83]
s_add_u32 s66, s66, 9216
TR a[138:139],v233 offset:36864
TR a[140:141],v233 offset:40960
EX v54,v54
MM a[80:83],a[164:167],v[84:87],a[80:83]
TR a[142:143],v233 offset:45056
TR a[144:145],v232 offset:33280
MM a[88:91],a[164:167],v[92:95],a[88:91]
s_xor_b32 s92, s49, 5
s_mul_i32 s92, s92, 17408
s_add_u32 s92, s83, s92
EX v55,v55
)ASM"
R"ASM(
MM a[88:91],a[160:163],v[88:91],a[88:91]
buffer_load_dwordx4 a[8:11], v248, s[20:23], s92 offen offset:0 sc1
TR a[146:147],v232 offset:37376
EX v56,v56
MM a[92:95],a[168:171],v[88:91],a[92:95]
TR a[148:149],v232 offset:41472
TR a[150:151],v232 offset:45568
buffer_load_dwordx4 v240, s[24:27], s66 offen offset:3072 lds
MM a[92:95],a[172:175],v[92:95],a[92:95]
s_xor_b32 s66, s94, 6
TR a[152:153],v233 offset:33280
EX v57,v57
CV v21,v50,v51
MM a[84:87],a[172:175],v[84:87],a[84:87]
s_mul_i32 s66, s66, 163840
TR a[154:155],v233 offset:37376
TR a[156:157],v233 offset:41472
MM a[84:87],a[168:171],v[80:83],a[84:87]
s_add_u32 s66, s66, s86
buffer_load_dwordx4 a[40:43], v248, s[20:23], s92 offen offset:1024 sc1
TR a[158:159],v233 offset:45568
s_add_u32 m0, s65, 16384
EX v58,v58
MM a[112:115],a[176:179],v[80:83],a[112:115]
TR a[160:161],v232 offset:33792
CV v22,v52,v53
EX v59,v59
MM a[112:115],a[180:183],v[84:87],a[112:115]
TR a[162:163],v232 offset:37888
TR a[164:165],v232 offset:41984
MM a[120:123],a[180:183],v[92:95],a[120:123]
buffer_load_dwordx4 a[72:75], v248, s[20:23], s92 offen offset:2048 sc1
TR a[166:167],v232 offset:46080
EX v60,v60
MM a[120:123],a[176:179],v[88:91],a[120:123]
TR a[168:169],v233 offset:33792
TR a[170:171],v233 offset:37888
CV v23,v54,v55
MM a[124:127],a[184:187],v[88:91],a[124:127]
TR a[172:173],v233 offset:41984
EX v61,v61
MM a[124:127],a[188:191],v[92:95],a[124:127]
buffer_load_dwordx4 a[104:107], v248, s[20:23], s92 offen offset:3072 sc1
TR a[174:175],v233 offset:46080
EX v62,v62
MM a[116:119],a[188:191],v[84:87],a[116:119]
TR a[176:177],v232 offset:34304
TR a[178:179],v232 offset:38400
EX v63,v63
MM a[116:119],a[184:187],v[80:83],a[116:119]
s_add_u32 s92, s92, 4096
TR a[180:181],v232 offset:42496
CV v28,v56,v57
v_smfmac_f32_16x16x64_bf16 v[152:155], v[236:239], v[16:23], v234
buffer_load_dwordx4 v240, s[24:27], s66 offen offset:0 lds
s_add_u32 s66, s66, 9216
TR a[182:183],v232 offset:46592
CV v29,v58,v59
CV v30,v60,v61
CV v31,v62,v63
s_nop 1
v_smfmac_f32_16x16x64_bf16 v[156:159], v[236:239], v[24:31], v234
TR a[184:185],v233 offset:34304
TR a[186:187],v233 offset:38400
TR a[188:189],v233 offset:42496
TR a[190:191],v233 offset:46592
s_waitcnt lgkmcnt(0)
buffer_load_dwordx4 v240, s[24:27], s66 offen offset:1024 lds
s_add_u32 s66, s66, 9216
buffer_load_dwordx4 v240, s[24:27], s66 offen offset:2048 lds
s_add_u32 s66, s66, 9216
buffer_load_dwordx4 v240, s[24:27], s66 offen offset:3072 lds
s_nop 6
v_max3_f32 v249, v152, v153, v156
v_max_f32 v249, v249, v157
v_cmp_lt_f32 vcc, s71, v249
s_cbranch_vccnz LSITE38_%=
LBACK38_%=:
v_add_f32 v246, v152, v153
v_add_f32 v247, v156, v157
s_waitcnt lgkmcnt(0)
MM a[16:19],a[128:131],v[16:19],a[16:19]
s_barrier
MM a[16:19],a[132:135],v[20:23],a[16:19]
buffer_load_dwordx4 a[12:15], v248, s[20:23], s92 offen offset:0 sc1
MM a[24:27],a[132:135],v[28:31],a[24:27]
v_rcp_f32 v153, v246
MM a[24:27],a[128:131],v[24:27],a[24:27]
v_rcp_f32 v157, v247
MM a[28:31],a[136:139],v[24:27],a[28:31]
v_bfe_u32 v152, v241, 16, 8
v_lshl_or_b32 v152, v152, 1, 1
MM a[28:31],a[140:143],v[28:31],a[28:31]
buffer_load_dwordx4 a[44:47], v248, s[20:23], s92 offen offset:1024 sc1
MM a[20:23],a[140:143],v[20:23],a[20:23]
v_lshlrev_b32 v152, 22, v152
v_mul_f32 v152, -v152, v153
MM a[20:23],a[136:139],v[16:19],a[20:23]
v_bfe_u32 v156, v241, 24, 8
v_lshl_or_b32 v156, v156, 1, 1
MM a[48:51],a[144:147],v[16:19],a[48:51]
TR a[128:129],v244 offset:0
TR a[130:131],v244 offset:1024
MM a[48:51],a[148:151],v[20:23],a[48:51]
buffer_load_dwordx4 a[76:79], v248, s[20:23], s92 offen offset:2048 sc1
MM a[56:59],a[148:151],v[28:31],a[56:59]
TR a[132:133],v244 offset:2048
TR a[134:135],v244 offset:3072
MM a[56:59],a[144:147],v[24:27],a[56:59]
v_lshlrev_b32 v156, 22, v156
v_mul_f32 v156, -v156, v157
MM a[60:63],a[152:155],v[24:27],a[60:63]
s_mul_i32 s78, s49, 17408
TR a[136:137],v244 offset:4096
TR a[138:139],v244 offset:5120
MM a[60:63],a[156:159],v[28:31],a[60:63]
s_add_u32 s78, s83, s78
buffer_load_dwordx4 a[108:111], v248, s[20:23], s92 offen offset:3072 sc1
MM a[52:55],a[156:159],v[20:23],a[52:55]
s_add_u32 s78, s78, 8192
TR a[140:141],v244 offset:6144
TR a[142:143],v244 offset:7168
MM a[52:55],a[152:155],v[16:19],a[52:55]
s_add_u32 s79, s78, 4096
AR v80,a16
AR v81,a17
MM a[80:83],a[160:163],v[16:19],a[80:83]
TR a[144:145],v235 offset:0
TR a[146:147],v235 offset:1024
MM a[80:83],a[164:167],v[20:23],a[80:83]
AR v82,a18
AR v83,a19
MM a[88:91],a[164:167],v[28:31],a[88:91]
TR a[148:149],v235 offset:2048
TR a[150:151],v235 offset:3072
MM a[88:91],a[160:163],v[24:27],a[88:91]
AR v84,a20
AR v85,a21
MM a[92:95],a[168:171],v[24:27],a[92:95]
TR a[152:153],v235 offset:4096
TR a[154:155],v235 offset:5120
MM a[92:95],a[172:175],v[28:31],a[92:95]
s_xor_b32 s66, s94, 7
AR v86,a22
AR v87,a23
MM a[84:87],a[172:175],v[20:23],a[84:87]
s_mul_i32 s66, s66, 163840
TR a[156:157],v235 offset:6144
TR a[158:159],v235 offset:7168
v_fma_f32 v80, v80, v153, v152
MM a[84:87],a[168:171],v[16:19],a[84:87]
s_add_u32 s66, s66, s87
v_fma_f32 v81, v81, v153, v152
v_fma_f32 v82, v82, v153, v152
MM a[112:115],a[176:179],v[16:19],a[112:115]
s_add_u32 m0, s65, 98304
TR a[160:161],v144 offset:49152
TR a[162:163],v144 offset:51200
v_fma_f32 v83, v83, v153, v152
MM a[112:115],a[180:183],v[20:23],a[112:115]
buffer_load_dwordx4 v240, s[24:27], s66 offen offset:0 lds
v_fma_f32 v84, v84, v153, v152
v_fma_f32 v85, v85, v153, v152
MM a[120:123],a[180:183],v[28:31],a[120:123]
s_add_u32 s66, s66, 9216
TR a[164:165],v144 offset:53248
TR a[166:167],v144 offset:55296
v_fma_f32 v86, v86, v153, v152
v_fma_f32 v87, v87, v153, v152
MM a[120:123],a[176:179],v[24:27],a[120:123]
CV v168,v80,v81
CV v169,v82,v83
MM a[124:127],a[184:187],v[24:27],a[124:127]
TR a[168:169],v144 offset:57344
TR a[170:171],v144 offset:59392
CV v170,v84,v85
MM a[124:127],a[188:191],v[28:31],a[124:127]
buffer_load_dwordx4 v240, s[24:27], s66 offen offset:1024 lds
CV v171,v86,v87
MM a[116:119],a[188:191],v[20:23],a[116:119]
LPG26_%=:
s_cbranch_execz LPG30_%=
s_add_u32 s66, s66, 9216
TR a[172:173],v144 offset:61440
TR a[174:175],v144 offset:63488
MM a[116:119],a[184:187],v[16:19],a[116:119]
buffer_store_dwordx4 v[168:171], v248, s[20:23], s78 offen offset:0 sc1
AR v88,a24
AR v89,a25
s_waitcnt vmcnt(23)
s_waitcnt lgkmcnt(15)
MM v[128:131],a[128:131],a[0:3],0
TR a[176:177],v145 offset:49152
TR a[178:179],v145 offset:51200
AR v90,a26
AR v91,a27
MM v[128:131],a[132:135],a[32:35],v[128:131]
v_bfe_u32 v249, v248, 6, 2
v_lshlrev_b32 v249, 8, v249
AR v92,a28
AR v93,a29
MM v[128:131],a[136:139],a[64:67],v[128:131]
TR a[180:181],v145 offset:53248
TR a[182:183],v145 offset:55296
AR v94,a30
MM v[128:131],a[140:143],a[96:99],v[128:131]
s_waitcnt vmcnt(19)
v_lshrrev_b32 v250, 8, v248
v_lshl_add_u32 v249, v250, 6, v249
AR v95,a31
v_fma_f32 v88, v88, v157, v156
MM v[136:139],a[140:143],a[100:103],0
TR a[184:185],v145 offset:57344
TR a[186:187],v145 offset:59392
v_fma_f32 v89, v89, v157, v156
v_fma_f32 v90, v90, v157, v156
MM v[136:139],a[136:139],a[68:71],v[136:139]
buffer_load_dwordx4 v240, s[24:27], s66 offen offset:2048 lds
v_and_b32 v251, 48, v248
v_add_u32 v249, v249, v251
v_fma_f32 v91, v91, v157, v156
MM v[136:139],a[132:135],a[36:39],v[136:139]
s_add_u32 s66, s66, 9216
TR a[188:189],v145 offset:61440
TR a[190:191],v145 offset:63488
v_fma_f32 v92, v92, v157, v156
v_fma_f32 v93, v93, v157, v156
MM v[136:139],a[128:131],a[4:7],v[136:139]
s_waitcnt lgkmcnt(15)
v_fma_f32 v94, v94, v157, v156
v_fma_f32 v95, v95, v157, v156
MM v[140:143],a[144:147],a[4:7],0
s_lshr_b32 s60, s49, 3
TR a[128:129],v146 offset:49152
TR a[130:131],v146 offset:51200
CV v172,v88,v89
CV v173,v90,v91
MM v[140:143],a[148:151],a[36:39],v[140:143]
buffer_load_dwordx4 v240, s[24:27], s66 offen offset:3072 lds
CV v174,v92,v93
MM v[140:143],a[152:155],a[68:71],v[140:143]
TR a[132:133],v146 offset:53248
TR a[134:135],v146 offset:55296
CV v175,v94,v95
MM v[140:143],a[156:159],a[100:103],v[140:143]
s_xor_b32 s66, s94, 7
buffer_store_dwordx4 v[172:175], v248, s[20:23], s79 offen offset:0 sc1
MM v[132:135],a[156:159],a[96:99],0
s_mul_i32 s66, s66, 163840
TR a[136:137],v146 offset:57344
TR a[138:139],v146 offset:59392
AR v112,a48
AR v113,a49
MM v[132:135],a[152:155],a[64:67],v[132:135]
s_add_u32 s66, s66, s86
AR v114,a50
AR v115,a51
MM v[132:135],a[148:151],a[32:35],v[132:135]
s_add_u32 m0, s65, 32768
TR a[140:141],v146 offset:61440
TR a[142:143],v146 offset:63488
AR v116,a52
MM v[132:135],a[144:147],a[0:3],v[132:135]
s_waitcnt lgkmcnt(15)
buffer_load_dwordx4 v240, s[24:27], s66 offen offset:0 lds
AR v117,a53
AR v118,a54
MM v[64:67],a[160:163],a[0:3],0
s_xor_b32 s92, s49, 6
TR a[144:145],v147 offset:49152
TR a[146:147],v147 offset:51200
AR v119,a55
v_fma_f32 v112, v112, v153, v152
MM v[64:67],a[164:167],a[32:35],v[64:67]
s_mul_i32 s92, s92, 17408
s_add_u32 s92, s83, s92
v_fma_f32 v113, v113, v153, v152
v_fma_f32 v114, v114, v153, v152
MM v[64:67],a[168:171],a[64:67],v[64:67]
s_add_u32 s66, s66, 9216
TR a[148:149],v147 offset:53248
TR a[150:151],v147 offset:55296
v_fma_f32 v115, v115, v153, v152
MM v[64:67],a[172:175],a[96:99],v[64:67]
buffer_load_dwordx4 v[160:163], v248, s[20:23], s92 offen offset:0 sc1
v_fma_f32 v116, v116, v153, v152
v_fma_f32 v117, v117, v153, v152
MM v[72:75],a[172:175],a[100:103],0
TR a[152:153],v147 offset:57344
TR a[154:155],v147 offset:59392
v_fma_f32 v118, v118, v153, v152
v_fma_f32 v119, v119, v153, v152
MM v[72:75],a[168:171],a[68:71],v[72:75]
CV v184,v112,v113
CV v185,v114,v115
MM v[72:75],a[164:167],a[36:39],v[72:75]
TR a[156:157],v147 offset:61440
TR a[158:159],v147 offset:63488
CV v186,v116,v117
MM v[72:75],a[160:163],a[4:7],v[72:75]
s_waitcnt lgkmcnt(15)
buffer_load_dwordx4 v[176:179], v248, s[20:23], s92 offen offset:1024 sc1
CV v187,v118,v119
MM v[76:79],a[176:179],a[4:7],0
TR a[160:161],v228 offset:49152
TR a[162:163],v228 offset:51200
MM v[76:79],a[180:183],a[36:39],v[76:79]
v_xor_b32 v253, s60, v250
v_and_b32 v238, 240, v248
MM v[76:79],a[184:187],a[68:71],v[76:79]
TR a[164:165],v228 offset:53248
TR a[166:167],v228 offset:55296
MM v[76:79],a[188:191],a[100:103],v[76:79]
buffer_store_dwordx4 v[184:187], v248, s[20:23], s78 offen offset:1024 sc1
AR v120,a56
AR v121,a57
AR v122,a58
AR v123,a59
AR v124,a60
AR v125,a61
MM v[68:71],a[188:191],a[96:99],0
TR a[168:169],v228 offset:57344
TR a[170:171],v228 offset:59392
AR v126,a62
MM v[68:71],a[184:187],a[64:67],v[68:71]
AR v127,a63
v_fma_f32 v120, v120, v157, v156
MM v[68:71],a[180:183],a[32:35],v[68:71]
TR a[172:173],v228 offset:61440
TR a[174:175],v228 offset:63488
v_fma_f32 v121, v121, v157, v156
v_fma_f32 v122, v122, v157, v156
MM v[68:71],a[176:179],a[0:3],v[68:71]
s_waitcnt lgkmcnt(15)
buffer_load_dwordx4 v[192:195], v248, s[20:23], s92 offen offset:2048 sc1
v_fma_f32 v123, v123, v157, v156
MM v[96:99],a[128:131],a[0:3],0
TR a[176:177],v229 offset:49152
TR a[178:179],v229 offset:51200
v_fma_f32 v124, v124, v157, v156
v_fma_f32 v125, v125, v157, v156
MM v[96:99],a[132:135],a[32:35],v[96:99]
v_fma_f32 v126, v126, v157, v156
v_fma_f32 v127, v127, v157, v156
MM v[96:99],a[136:139],a[64:67],v[96:99]
TR a[180:181],v229 offset:53248
TR a[182:183],v229 offset:55296
CV v188,v120,v121
MM v[96:99],a[140:143],a[96:99],v[96:99]
buffer_load_dwordx4 v[208:211], v248, s[20:23], s92 offen offset:3072 sc1
CV v189,v122,v123
CV v190,v124,v125
MM v[104:107],a[140:143],a[100:103],0
TR a[184:185],v229 offset:57344
TR a[186:187],v229 offset:59392
CV v191,v126,v127
MM v[104:107],a[136:139],a[68:71],v[104:107]
s_add_u32 s92, s92, 4096
v_lshl_add_u32 v232, v250, 2, v238
s_add_u32 s61, s65, 131072
MM v[104:107],a[132:135],a[36:39],v[104:107]
TR a[188:189],v229 offset:61440
TR a[190:191],v229 offset:63488
MM v[104:107],a[128:131],a[4:7],v[104:107]
s_waitcnt lgkmcnt(15)
buffer_store_dwordx4 v[188:191], v248, s[20:23], s79 offen offset:1024 sc1
AR v80,a80
AR v81,a81
AR v82,a82
AR v83,a83
MM v[108:111],a[144:147],a[4:7],0
TR a[128:129],v230 offset:49152
TR a[130:131],v230 offset:51200
AR v84,a84
MM v[108:111],a[148:151],a[36:39],v[108:111]
AR v85,a85
AR v86,a86
)ASM"
R"ASM(
MM v[108:111],a[152:155],a[68:71],v[108:111]
TR a[132:133],v230 offset:53248
TR a[134:135],v230 offset:55296
AR v87,a87
v_fma_f32 v80, v80, v153, v152
MM v[108:111],a[156:159],a[100:103],v[108:111]
buffer_load_dwordx4 v[164:167], v248, s[20:23], s92 offen offset:0 sc1
v_fma_f32 v81, v81, v153, v152
v_fma_f32 v82, v82, v153, v152
MM v[100:103],a[156:159],a[96:99],0
TR a[136:137],v230 offset:57344
TR a[138:139],v230 offset:59392
v_fma_f32 v83, v83, v153, v152
MM v[100:103],a[152:155],a[64:67],v[100:103]
v_fma_f32 v84, v84, v153, v152
v_fma_f32 v85, v85, v153, v152
MM v[100:103],a[148:151],a[32:35],v[100:103]
TR a[140:141],v230 offset:61440
TR a[142:143],v230 offset:63488
v_fma_f32 v86, v86, v153, v152
v_fma_f32 v87, v87, v153, v152
MM v[100:103],a[144:147],a[0:3],v[100:103]
s_waitcnt lgkmcnt(15)
buffer_load_dwordx4 v[180:183], v248, s[20:23], s92 offen offset:1024 sc1
CV v200,v80,v81
MM v[0:3],a[160:163],a[0:3],0
TR a[144:145],v231 offset:49152
TR a[146:147],v231 offset:51200
CV v201,v82,v83
CV v202,v84,v85
MM v[0:3],a[164:167],a[32:35],v[0:3]
CV v203,v86,v87
v_mul_u32_u24 v224, 5, v238
MM v[0:3],a[168:171],a[64:67],v[0:3]
TR a[148:149],v231 offset:53248
TR a[150:151],v231 offset:55296
MM v[0:3],a[172:175],a[96:99],v[0:3]
buffer_store_dwordx4 v[200:203], v248, s[20:23], s78 offen offset:2048 sc1
AR v88,a88
AR v89,a89
MM v[8:11],a[172:175],a[100:103],0
TR a[152:153],v231 offset:57344
TR a[154:155],v231 offset:59392
AR v90,a90
AR v91,a91
MM v[8:11],a[168:171],a[68:71],v[8:11]
AR v92,a92
AR v93,a93
MM v[8:11],a[164:167],a[36:39],v[8:11]
TR a[156:157],v231 offset:61440
TR a[158:159],v231 offset:63488
AR v94,a94
MM v[8:11],a[160:163],a[4:7],v[8:11]
s_waitcnt lgkmcnt(15)
buffer_load_dwordx4 v[196:199], v248, s[20:23], s92 offen offset:2048 sc1
AR v95,a95
v_fma_f32 v88, v88, v157, v156
s_barrier
MM v[12:15],a[176:179],a[4:7],0
TR a[160:161],v244 offset:8192
TR a[162:163],v244 offset:9216
v_fma_f32 v89, v89, v157, v156
v_fma_f32 v90, v90, v157, v156
MM v[12:15],a[180:183],a[36:39],v[12:15]
v_fma_f32 v91, v91, v157, v156
v_fma_f32 v92, v92, v157, v156
MM v[12:15],a[184:187],a[68:71],v[12:15]
TR a[164:165],v244 offset:10240
TR a[166:167],v244 offset:11264
v_fma_f32 v93, v93, v157, v156
MM v[12:15],a[188:191],a[100:103],v[12:15]
buffer_load_dwordx4 v[212:215], v248, s[20:23], s92 offen offset:3072 sc1
v_fma_f32 v94, v94, v157, v156
v_fma_f32 v95, v95, v157, v156
MM v[4:7],a[188:191],a[96:99],0
TR a[168:169],v244 offset:12288
TR a[170:171],v244 offset:13312
CV v204,v88,v89
MM v[4:7],a[184:187],a[64:67],v[4:7]
CV v205,v90,v91
CV v206,v92,v93
MM v[4:7],a[180:183],a[32:35],v[4:7]
TR a[172:173],v244 offset:14336
TR a[174:175],v244 offset:15360
CV v207,v94,v95
MM v[4:7],a[176:179],a[0:3],v[4:7]
s_waitcnt lgkmcnt(15)
buffer_store_dwordx4 v[204:207], v248, s[20:23], s79 offen offset:2048 sc1
MM v[32:35],a[128:131],a[0:3],0
TR a[176:177],v235 offset:8192
TR a[178:179],v235 offset:9216
AR v112,a112
AR v113,a113
MM v[32:35],a[132:135],a[32:35],v[32:35]
AR v114,a114
AR v115,a115
MM v[32:35],a[136:139],a[64:67],v[32:35]
TR a[180:181],v235 offset:10240
TR a[182:183],v235 offset:11264
AR v116,a116
MM v[32:35],a[140:143],a[96:99],v[32:35]
buffer_load_dwordx4 v240, s[24:27], s66 offen offset:1024 lds
AR v117,a117
AR v118,a118
MM v[40:43],a[140:143],a[100:103],0
s_add_u32 s66, s66, 9216
TR a[184:185],v235 offset:12288
TR a[186:187],v235 offset:13312
AR v119,a119
v_fma_f32 v112, v112, v153, v152
MM v[40:43],a[136:139],a[68:71],v[40:43]
v_fma_f32 v113, v113, v153, v152
v_fma_f32 v114, v114, v153, v152
MM v[40:43],a[132:135],a[36:39],v[40:43]
TR a[188:189],v235 offset:14336
TR a[190:191],v235 offset:15360
v_fma_f32 v115, v115, v153, v152
MM v[40:43],a[128:131],a[4:7],v[40:43]
s_waitcnt lgkmcnt(15)
buffer_load_dwordx4 v240, s[24:27], s66 offen offset:2048 lds
v_fma_f32 v116, v116, v153, v152
v_fma_f32 v117, v117, v153, v152
MM v[44:47],a[144:147],a[4:7],0
s_add_u32 s66, s66, 9216
TR a[128:129],v144 offset:0
TR a[130:131],v144 offset:2048
v_fma_f32 v118, v118, v153, v152
v_fma_f32 v119, v119, v153, v152
MM v[44:47],a[148:151],a[36:39],v[44:47]
CV v216,v112,v113
CV v217,v114,v115
MM v[44:47],a[152:155],a[68:71],v[44:47]
TR a[132:133],v144 offset:4096
TR a[134:135],v144 offset:6144
CV v218,v116,v117
MM v[44:47],a[156:159],a[100:103],v[44:47]
buffer_load_dwordx4 v240, s[24:27], s66 offen offset:3072 lds
CV v219,v118,v119
MM v[36:39],a[156:159],a[96:99],0
TR a[136:137],v144 offset:8192
TR a[138:139],v144 offset:10240
MM v[36:39],a[152:155],a[64:67],v[36:39]
s_xor_b32 s66, s94, 0
v_lshl_add_u32 v224, v250, 3, v224
v_add_u32 v224, s61, v224
MM v[36:39],a[148:151],a[32:35],v[36:39]
s_mul_i32 s66, s66, 163840
TR a[140:141],v144 offset:12288
TR a[142:143],v144 offset:14336
MM v[36:39],a[144:147],a[0:3],v[36:39]
s_waitcnt lgkmcnt(15)
buffer_store_dwordx4 v[216:219], v248, s[20:23], s78 offen offset:3072 sc1
AR v120,a120
AR v121,a121
AR v122,a122
AR v123,a123
AR v124,a124
s_waitcnt vmcnt(31)
MM v[128:131],a[160:163],a[8:11],v[128:131]
s_add_u32 s66, s66, s88
TR a[144:145],v145 offset:0
TR a[146:147],v145 offset:2048
AR v125,a125
AR v126,a126
MM v[128:131],a[164:167],a[40:43],v[128:131]
s_add_u32 m0, s89, 131072
AR v127,a127
v_fma_f32 v120, v120, v157, v156
MM v[128:131],a[168:171],a[72:75],v[128:131]
TR a[148:149],v145 offset:4096
TR a[150:151],v145 offset:6144
v_fma_f32 v121, v121, v157, v156
v_fma_f32 v122, v122, v157, v156
MM v[128:131],a[172:175],a[104:107],v[128:131]
s_waitcnt vmcnt(23)
buffer_load_dwordx4 v252, s[24:27], s66 offen offset:0 lds
v_fma_f32 v123, v123, v157, v156
MM v[136:139],a[172:175],a[108:111],v[136:139]
s_add_u32 s66, s66, 19456
TR a[152:153],v145 offset:8192
TR a[154:155],v145 offset:10240
v_fma_f32 v124, v124, v157, v156
v_fma_f32 v125, v125, v157, v156
MM v[136:139],a[168:171],a[76:79],v[136:139]
v_fma_f32 v126, v126, v157, v156
v_fma_f32 v127, v127, v157, v156
MM v[136:139],a[164:167],a[44:47],v[136:139]
TR a[156:157],v145 offset:12288
TR a[158:159],v145 offset:14336
CV v220,v120,v121
MM v[136:139],a[160:163],a[12:15],v[136:139]
s_waitcnt lgkmcnt(15)
buffer_load_dwordx4 v252, s[24:27], s66 offen offset:1024 lds
CV v221,v122,v123
CV v222,v124,v125
MM v[140:143],a[176:179],a[12:15],v[140:143]
TR a[160:161],v146 offset:0
TR a[162:163],v146 offset:2048
CV v223,v126,v127
MM v[140:143],a[180:183],a[44:47],v[140:143]
s_xor_b32 s66, s94, 0
v_mul_u32_u24 v254, 1280, v250
v_mul_u32_u24 v250, 33792, v253
MM v[140:143],a[184:187],a[76:79],v[140:143]
s_mul_i32 s66, s66, 163840
TR a[164:165],v146 offset:4096
TR a[166:167],v146 offset:6144
MM v[140:143],a[188:191],a[108:111],v[140:143]
s_add_u32 s66, s66, s87
buffer_store_dwordx4 v[220:223], v248, s[20:23], s79 offen offset:3072 sc1
MM v[132:135],a[188:191],a[104:107],v[132:135]
s_add_u32 m0, s65, 114688
TR a[168:169],v146 offset:8192
TR a[170:171],v146 offset:10240
MM v[132:135],a[184:187],a[72:75],v[132:135]
v_add_u32 v250, v250, v238
v_lshlrev_b32 v253, 4, v253
MM v[132:135],a[180:183],a[40:43],v[132:135]
TR a[172:173],v146 offset:12288
TR a[174:175],v146 offset:14336
MM v[132:135],a[176:179],a[8:11],v[132:135]
s_waitcnt lgkmcnt(15)
buffer_load_dwordx4 v240, s[24:27], s66 offen offset:0 lds
MM v[64:67],a[128:131],a[8:11],v[64:67]
s_xor_b32 s92, s49, 7
TR a[176:177],v147 offset:0
TR a[178:179],v147 offset:2048
MM v[64:67],a[132:135],a[40:43],v[64:67]
s_mul_i32 s92, s92, 17408
s_add_u32 s92, s83, s92
v_bfe_u32 v238, v248, 4, 3
MM v[64:67],a[136:139],a[72:75],v[64:67]
s_add_u32 s66, s66, 9216
TR a[180:181],v147 offset:4096
TR a[182:183],v147 offset:6144
MM v[64:67],a[140:143],a[104:107],v[64:67]
buffer_load_dwordx4 a[192:195], v248, s[20:23], s92 offen offset:0 sc1
MM v[72:75],a[140:143],a[108:111],v[72:75]
TR a[184:185],v147 offset:8192
TR a[186:187],v147 offset:10240
MM v[72:75],a[136:139],a[76:79],v[72:75]
v_lshl_add_u32 v254, v238, 3, v254
v_bfe_u32 v238, v248, 7, 1
MM v[72:75],a[132:135],a[44:47],v[72:75]
TR a[188:189],v147 offset:12288
TR a[190:191],v147 offset:14336
MM v[72:75],a[128:131],a[12:15],v[72:75]
s_waitcnt lgkmcnt(15)
buffer_load_dwordx4 a[208:211], v248, s[20:23], s92 offen offset:1024 sc1
MM v[76:79],a[144:147],a[12:15],v[76:79]
TR a[128:129],v228 offset:0
TR a[130:131],v228 offset:2048
MM v[76:79],a[148:151],a[44:47],v[76:79]
v_mul_u32_u24 v238, 5120, v238
v_add_u32 v254, v254, v238
MM v[76:79],a[152:155],a[76:79],v[76:79]
TR a[132:133],v228 offset:4096
TR a[134:135],v228 offset:6144
MM v[76:79],a[156:159],a[108:111],v[76:79]
buffer_load_dwordx4 a[224:227], v248, s[20:23], s92 offen offset:2048 sc1
MM v[68:71],a[156:159],a[104:107],v[68:71]
TR a[136:137],v228 offset:8192
TR a[138:139],v228 offset:10240
MM v[68:71],a[152:155],a[72:75],v[68:71]
v_lshrrev_b32 v238, 6, v248
v_mul_u32_u24 v225, 80, v238
MM v[68:71],a[148:151],a[40:43],v[68:71]
TR a[140:141],v228 offset:12288
TR a[142:143],v228 offset:14336
MM v[68:71],a[144:147],a[8:11],v[68:71]
s_waitcnt lgkmcnt(15)
buffer_load_dwordx4 a[240:243], v248, s[20:23], s92 offen offset:3072 sc1
MM v[96:99],a[160:163],a[8:11],v[96:99]
TR a[144:145],v229 offset:0
TR a[146:147],v229 offset:2048
MM v[96:99],a[164:167],a[40:43],v[96:99]
s_add_u32 s92, s92, 4096
v_add3_u32 v225, v225, v251, s61
v_mul_u32_u24 v238, 1280, v238
MM v[96:99],a[168:171],a[72:75],v[96:99]
TR a[148:149],v229 offset:4096
TR a[150:151],v229 offset:6144
MM v[96:99],a[172:175],a[104:107],v[96:99]
buffer_load_dwordx4 a[196:199], v248, s[20:23], s92 offen offset:0 sc1
MM v[104:107],a[172:175],a[108:111],v[104:107]
TR a[152:153],v229 offset:8192
TR a[154:155],v229 offset:10240
MM v[104:107],a[168:171],a[76:79],v[104:107]
v_add_u32 v251, v251, v238
MM v[104:107],a[164:167],a[44:47],v[104:107]
TR a[156:157],v229 offset:12288
TR a[158:159],v229 offset:14336
MM v[104:107],a[160:163],a[12:15],v[104:107]
s_waitcnt lgkmcnt(15)
buffer_load_dwordx4 a[212:215], v248, s[20:23], s92 offen offset:1024 sc1
MM v[108:111],a[176:179],a[12:15],v[108:111]
TR a[160:161],v230 offset:0
TR a[162:163],v230 offset:2048
MM v[108:111],a[180:183],a[44:47],v[108:111]
MM v[108:111],a[184:187],a[76:79],v[108:111]
TR a[164:165],v230 offset:4096
TR a[166:167],v230 offset:6144
MM v[108:111],a[188:191],a[108:111],v[108:111]
buffer_load_dwordx4 a[228:231], v248, s[20:23], s92 offen offset:2048 sc1
MM v[100:103],a[188:191],a[104:107],v[100:103]
TR a[168:169],v230 offset:8192
TR a[170:171],v230 offset:10240
MM v[100:103],a[184:187],a[72:75],v[100:103]
MM v[100:103],a[180:183],a[40:43],v[100:103]
TR a[172:173],v230 offset:12288
TR a[174:175],v230 offset:14336
MM v[100:103],a[176:179],a[8:11],v[100:103]
s_waitcnt lgkmcnt(15)
buffer_load_dwordx4 a[244:247], v248, s[20:23], s92 offen offset:3072 sc1
MM v[0:3],a[128:131],a[8:11],v[0:3]
TR a[176:177],v231 offset:0
TR a[178:179],v231 offset:2048
MM v[0:3],a[132:135],a[40:43],v[0:3]
MM v[0:3],a[136:139],a[72:75],v[0:3]
TR a[180:181],v231 offset:4096
TR a[182:183],v231 offset:6144
MM v[0:3],a[140:143],a[104:107],v[0:3]
buffer_load_dwordx4 v240, s[24:27], s66 offen offset:1024 lds
MM v[8:11],a[140:143],a[108:111],v[8:11]
s_add_u32 s66, s66, 9216
TR a[184:185],v231 offset:8192
TR a[186:187],v231 offset:10240
MM v[8:11],a[136:139],a[76:79],v[8:11]
MM v[8:11],a[132:135],a[44:47],v[8:11]
TR a[188:189],v231 offset:12288
TR a[190:191],v231 offset:14336
MM v[8:11],a[128:131],a[12:15],v[8:11]
s_waitcnt lgkmcnt(15)
buffer_load_dwordx4 v240, s[24:27], s66 offen offset:2048 lds
s_barrier
MM v[12:15],a[144:147],a[12:15],v[12:15]
s_add_u32 s66, s66, 9216
TR a[128:129],v244 offset:16384
TR a[130:131],v244 offset:17408
MM v[12:15],a[148:151],a[44:47],v[12:15]
LPG27_%=:
s_cbranch_execz LTR1_%=
MM v[12:15],a[152:155],a[76:79],v[12:15]
TR a[132:133],v244 offset:18432
TR a[134:135],v244 offset:19456
MM v[12:15],a[156:159],a[108:111],v[12:15]
buffer_load_dwordx4 v240, s[24:27], s66 offen offset:3072 lds
MM v[4:7],a[156:159],a[104:107],v[4:7]
TR a[136:137],v244 offset:20480
TR a[138:139],v244 offset:21504
MM v[4:7],a[152:155],a[72:75],v[4:7]
s_xor_b32 s66, s94, 0
s_mul_i32 s66, s66, 163840
s_add_u32 s66, s66, s86
MM v[4:7],a[148:151],a[40:43],v[4:7]
)ASM"
R"ASM(
s_add_u32 m0, s65, 49152
TR a[140:141],v244 offset:22528
TR a[142:143],v244 offset:23552
MM v[4:7],a[144:147],a[8:11],v[4:7]
s_waitcnt lgkmcnt(15)
buffer_load_dwordx4 v240, s[24:27], s66 offen offset:0 lds
MM v[32:35],a[160:163],a[8:11],v[32:35]
s_add_u32 s66, s66, 9216
TR a[144:145],v235 offset:16384
TR a[146:147],v235 offset:17408
MM v[32:35],a[164:167],a[40:43],v[32:35]
MM v[32:35],a[168:171],a[72:75],v[32:35]
TR a[148:149],v235 offset:18432
TR a[150:151],v235 offset:19456
MM v[32:35],a[172:175],a[104:107],v[32:35]
buffer_load_dwordx4 v240, s[24:27], s66 offen offset:1024 lds
MM v[40:43],a[172:175],a[108:111],v[40:43]
s_add_u32 s66, s66, 9216
TR a[152:153],v235 offset:20480
TR a[154:155],v235 offset:21504
MM v[40:43],a[168:171],a[76:79],v[40:43]
MM v[40:43],a[164:167],a[44:47],v[40:43]
TR a[156:157],v235 offset:22528
TR a[158:159],v235 offset:23552
MM v[40:43],a[160:163],a[12:15],v[40:43]
s_waitcnt lgkmcnt(15)
buffer_load_dwordx4 v240, s[24:27], s66 offen offset:2048 lds
MM v[44:47],a[176:179],a[12:15],v[44:47]
s_lshl_b32 s97, s84, 2
TR a[160:161],v144 offset:16384
TR a[162:163],v144 offset:18432
MM v[44:47],a[180:183],a[44:47],v[44:47]
s_add_u32 s97, s97, s96
s_add_u32 s97, s97, 16
s_waitcnt vmcnt(15)
MM v[44:47],a[184:187],a[76:79],v[44:47]
s_add_u32 s66, s66, 9216
TR a[164:165],v144 offset:20480
TR a[166:167],v144 offset:22528
MM v[44:47],a[188:191],a[108:111],v[44:47]
v_mov_b32 v242, s69
s_mov_b64 exec, 1
buffer_store_dword v242, off, s[20:23], s97 offset:0 sc1
s_mov_b64 exec, -1
MM v[36:39],a[188:191],a[104:107],v[36:39]
TR a[168:169],v144 offset:24576
TR a[170:171],v144 offset:26624
MM v[36:39],a[184:187],a[72:75],v[36:39]
buffer_load_dwordx4 v240, s[24:27], s66 offen offset:3072 lds
MM v[36:39],a[180:183],a[40:43],v[36:39]
TR a[172:173],v144 offset:28672
TR a[174:175],v144 offset:30720
MM v[36:39],a[176:179],a[8:11],v[36:39]
s_waitcnt lgkmcnt(15)
s_xor_b32 s66, s94, 1
s_mul_i32 s66, s66, 163840
MM v[128:131],a[128:131],v[160:163],v[128:131]
s_add_u32 s66, s66, s88
TR a[176:177],v145 offset:16384
TR a[178:179],v145 offset:18432
MM v[128:131],a[132:135],v[176:179],v[128:131]
s_add_u32 m0, s89, 139264
MM v[128:131],a[136:139],v[192:195],v[128:131]
TR a[180:181],v145 offset:20480
TR a[182:183],v145 offset:22528
MM v[128:131],a[140:143],v[208:211],v[128:131]
buffer_load_dwordx4 v252, s[24:27], s66 offen offset:0 lds
MM v[136:139],a[140:143],v[212:215],v[136:139]
s_add_u32 s66, s66, 19456
TR a[184:185],v145 offset:24576
TR a[186:187],v145 offset:26624
MM v[136:139],a[136:139],v[196:199],v[136:139]
MM v[136:139],a[132:135],v[180:183],v[136:139]
TR a[188:189],v145 offset:28672
TR a[190:191],v145 offset:30720
MM v[136:139],a[128:131],v[164:167],v[136:139]
s_waitcnt lgkmcnt(15)
buffer_load_dwordx4 v252, s[24:27], s66 offen offset:1024 lds
MM v[140:143],a[144:147],v[164:167],v[140:143]
TR a[128:129],v146 offset:16384
TR a[130:131],v146 offset:18432
MM v[140:143],a[148:151],v[180:183],v[140:143]
s_xor_b32 s66, s94, 1
s_mul_i32 s66, s66, 163840
s_add_u32 s66, s66, s87
MM v[140:143],a[152:155],v[196:199],v[140:143]
s_add_u32 m0, s65, 65536
TR a[132:133],v146 offset:20480
TR a[134:135],v146 offset:22528
MM v[140:143],a[156:159],v[212:215],v[140:143]
buffer_load_dwordx4 v240, s[24:27], s66 offen offset:0 lds
MM v[132:135],a[156:159],v[208:211],v[132:135]
s_add_u32 s66, s66, 9216
TR a[136:137],v146 offset:24576
TR a[138:139],v146 offset:26624
MM v[132:135],a[152:155],v[192:195],v[132:135]
MM v[132:135],a[148:151],v[176:179],v[132:135]
TR a[140:141],v146 offset:28672
TR a[142:143],v146 offset:30720
MM v[132:135],a[144:147],v[160:163],v[132:135]
s_waitcnt lgkmcnt(15)
buffer_load_dwordx4 v240, s[24:27], s66 offen offset:1024 lds
MM v[64:67],a[160:163],v[160:163],v[64:67]
s_add_u32 s66, s66, 9216
TR a[144:145],v147 offset:16384
TR a[146:147],v147 offset:18432
MM v[64:67],a[164:167],v[176:179],v[64:67]
MM v[64:67],a[168:171],v[192:195],v[64:67]
TR a[148:149],v147 offset:20480
TR a[150:151],v147 offset:22528
MM v[64:67],a[172:175],v[208:211],v[64:67]
buffer_load_dwordx4 v240, s[24:27], s66 offen offset:2048 lds
MM v[72:75],a[172:175],v[212:215],v[72:75]
s_add_u32 s66, s66, 9216
TR a[152:153],v147 offset:24576
TR a[154:155],v147 offset:26624
MM v[72:75],a[168:171],v[196:199],v[72:75]
MM v[72:75],a[164:167],v[180:183],v[72:75]
TR a[156:157],v147 offset:28672
TR a[158:159],v147 offset:30720
MM v[72:75],a[160:163],v[164:167],v[72:75]
s_waitcnt lgkmcnt(15)
buffer_load_dwordx4 v240, s[24:27], s66 offen offset:3072 lds
MM v[76:79],a[176:179],v[164:167],v[76:79]
TR a[160:161],v228 offset:16384
TR a[162:163],v228 offset:18432
MM v[76:79],a[180:183],v[180:183],v[76:79]
MM v[76:79],a[184:187],v[196:199],v[76:79]
TR a[164:165],v228 offset:20480
TR a[166:167],v228 offset:22528
MM v[76:79],a[188:191],v[212:215],v[76:79]
MM v[68:71],a[188:191],v[208:211],v[68:71]
TR a[168:169],v228 offset:24576
TR a[170:171],v228 offset:26624
MM v[68:71],a[184:187],v[192:195],v[68:71]
MM v[68:71],a[180:183],v[176:179],v[68:71]
TR a[172:173],v228 offset:28672
TR a[174:175],v228 offset:30720
MM v[68:71],a[176:179],v[160:163],v[68:71]
s_waitcnt lgkmcnt(15)
MM v[96:99],a[128:131],v[160:163],v[96:99]
s_add_u32 s80, s18, 131072
TR a[176:177],v229 offset:16384
TR a[178:179],v229 offset:18432
MM v[96:99],a[132:135],v[176:179],v[96:99]
buffer_load_dwordx4 v[16:19], v251, s[20:23], s80 offen offset:0 sc1
MM v[96:99],a[136:139],v[192:195],v[96:99]
TR a[180:181],v229 offset:20480
TR a[182:183],v229 offset:22528
MM v[96:99],a[140:143],v[208:211],v[96:99]
v_mov_b32 v237, 0x3f80
v_and_b32 v236, 16, v248
MM v[104:107],a[140:143],v[212:215],v[104:107]
TR a[184:185],v229 offset:24576
TR a[186:187],v229 offset:26624
MM v[104:107],a[136:139],v[196:199],v[104:107]
s_add_u32 s80, s18, 151552
buffer_load_dwordx4 v[20:23], v251, s[20:23], s80 offen offset:0 sc1
MM v[104:107],a[132:135],v[180:183],v[104:107]
TR a[188:189],v229 offset:28672
TR a[190:191],v229 offset:30720
MM v[104:107],a[128:131],v[164:167],v[104:107]
s_waitcnt lgkmcnt(15)
v_lshlrev_b32 v236, v236, v237
s_mov_b32 s28, 0x30003
MM v[108:111],a[144:147],v[164:167],v[108:111]
s_mov_b32 s29, s28
TR a[128:129],v230 offset:16384
TR a[130:131],v230 offset:18432
MM v[108:111],a[148:151],v[180:183],v[108:111]
s_add_u32 s80, s63, 131072
v_lshrrev_b32 v243, 2, v248
v_min_u32 v243, 12, v243
MM v[108:111],a[152:155],v[196:199],v[108:111]
TR a[132:133],v230 offset:20480
TR a[134:135],v230 offset:22528
MM v[108:111],a[156:159],v[212:215],v[108:111]
buffer_load_dword v242, v243, s[20:23], s96 offen offset:16 sc1
buffer_load_dwordx2 v[150:151], v254, s[20:23], s80 offen offset:0 sc1
MM v[100:103],a[156:159],v[208:211],v[100:103]
TR a[136:137],v230 offset:24576
TR a[138:139],v230 offset:26624
MM v[100:103],a[152:155],v[192:195],v[100:103]
v_cndmask_b32 v48, 0, v236, s[28:29]
s_lshl_b64 s[28:29], s[28:29], 2
MM v[100:103],a[148:151],v[176:179],v[100:103]
TR a[140:141],v230 offset:28672
TR a[142:143],v230 offset:30720
MM v[100:103],a[144:147],v[160:163],v[100:103]
s_waitcnt lgkmcnt(15)
v_cndmask_b32 v49, 0, v236, s[28:29]
s_lshl_b64 s[28:29], s[28:29], 2
MM v[0:3],a[160:163],v[160:163],v[0:3]
TR a[144:145],v231 offset:16384
TR a[146:147],v231 offset:18432
MM v[0:3],a[164:167],v[176:179],v[0:3]
v_cndmask_b32 v50, 0, v236, s[28:29]
s_lshl_b64 s[28:29], s[28:29], 2
MM v[0:3],a[168:171],v[192:195],v[0:3]
TR a[148:149],v231 offset:20480
TR a[150:151],v231 offset:22528
MM v[0:3],a[172:175],v[208:211],v[0:3]
v_cndmask_b32 v51, 0, v236, s[28:29]
s_lshl_b64 s[28:29], s[28:29], 2
MM v[8:11],a[172:175],v[212:215],v[8:11]
TR a[152:153],v231 offset:24576
TR a[154:155],v231 offset:26624
MM v[8:11],a[168:171],v[196:199],v[8:11]
v_cndmask_b32 v52, 0, v236, s[28:29]
s_lshl_b64 s[28:29], s[28:29], 2
MM v[8:11],a[164:167],v[180:183],v[8:11]
TR a[156:157],v231 offset:28672
TR a[158:159],v231 offset:30720
MM v[8:11],a[160:163],v[164:167],v[8:11]
s_waitcnt lgkmcnt(15)
v_cndmask_b32 v53, 0, v236, s[28:29]
s_lshl_b64 s[28:29], s[28:29], 2
s_barrier
MM v[12:15],a[176:179],v[164:167],v[12:15]
s_xor_b32 s66, s94, 2
TR a[160:161],v244 offset:24576
TR a[162:163],v244 offset:25600
MM v[12:15],a[180:183],v[180:183],v[12:15]
s_mul_i32 s66, s66, 163840
v_cndmask_b32 v54, 0, v236, s[28:29]
s_lshl_b64 s[28:29], s[28:29], 2
MM v[12:15],a[184:187],v[196:199],v[12:15]
s_add_u32 s66, s66, s88
TR a[164:165],v244 offset:26624
TR a[166:167],v244 offset:27648
MM v[12:15],a[188:191],v[212:215],v[12:15]
s_add_u32 m0, s89, 147456
v_cndmask_b32 v55, 0, v236, s[28:29]
s_mov_b32 s28, 0x300003
MM v[4:7],a[188:191],v[208:211],v[4:7]
s_mov_b32 s29, 0x30000300
TR a[168:169],v244 offset:28672
TR a[170:171],v244 offset:29696
MM v[4:7],a[184:187],v[192:195],v[4:7]
buffer_load_dwordx4 v252, s[24:27], s66 offen offset:0 lds
MM v[4:7],a[180:183],v[176:179],v[4:7]
s_add_u32 s66, s66, 19456
TR a[172:173],v244 offset:30720
TR a[174:175],v244 offset:31744
MM v[4:7],a[176:179],v[160:163],v[4:7]
s_waitcnt lgkmcnt(15)
v_mov_b32 v56, 0
v_mov_b32 v57, 0
MM v[32:35],a[128:131],v[160:163],v[32:35]
TR a[176:177],v235 offset:24576
TR a[178:179],v235 offset:25600
MM v[32:35],a[132:135],v[176:179],v[32:35]
buffer_load_dwordx4 v252, s[24:27], s66 offen offset:1024 lds
MM v[32:35],a[136:139],v[192:195],v[32:35]
TR a[180:181],v235 offset:26624
TR a[182:183],v235 offset:27648
MM v[32:35],a[140:143],v[208:211],v[32:35]
s_xor_b32 s66, s94, 2
v_mov_b32 v60, 0
v_mov_b32 v61, 0
MM v[40:43],a[140:143],v[212:215],v[40:43]
s_mul_i32 s66, s66, 163840
TR a[184:185],v235 offset:28672
TR a[186:187],v235 offset:29696
MM v[40:43],a[136:139],v[196:199],v[40:43]
s_add_u32 s66, s66, s87
v_cndmask_b32 v58, 0, v236, s[28:29]
s_lshl_b64 s[28:29], s[28:29], 2
MM v[40:43],a[132:135],v[180:183],v[40:43]
s_add_u32 m0, s65, 81920
TR a[188:189],v235 offset:30720
TR a[190:191],v235 offset:31744
MM v[40:43],a[128:131],v[164:167],v[40:43]
s_waitcnt lgkmcnt(15)
buffer_load_dwordx4 v240, s[24:27], s66 offen offset:0 lds
MM v[44:47],a[144:147],v[164:167],v[44:47]
s_add_u32 s66, s66, 9216
TR a[128:129],v144 offset:32768
TR a[130:131],v144 offset:34816
MM v[44:47],a[148:151],v[180:183],v[44:47]
v_cndmask_b32 v59, 0, v236, s[28:29]
MM v[44:47],a[152:155],v[196:199],v[44:47]
TR a[132:133],v144 offset:36864
TR a[134:135],v144 offset:38912
MM v[44:47],a[156:159],v[212:215],v[44:47]
buffer_load_dwordx4 v240, s[24:27], s66 offen offset:1024 lds
MM v[36:39],a[156:159],v[208:211],v[36:39]
s_add_u32 s66, s66, 9216
TR a[136:137],v144 offset:40960
TR a[138:139],v144 offset:43008
MM v[36:39],a[152:155],v[192:195],v[36:39]
MM v[36:39],a[148:151],v[176:179],v[36:39]
TR a[140:141],v144 offset:45056
TR a[142:143],v144 offset:47104
MM v[36:39],a[144:147],v[160:163],v[36:39]
s_waitcnt lgkmcnt(15)
buffer_load_dwordx4 v240, s[24:27], s66 offen offset:2048 lds
s_waitcnt vmcnt(27)
MM v[128:131],a[160:163],a[192:195],v[128:131]
s_add_u32 s80, s18, 0
TR a[144:145],v145 offset:32768
TR a[146:147],v145 offset:34816
MM v[128:131],a[164:167],a[208:211],v[128:131]
s_add_u32 s66, s66, 9216
buffer_load_dwordx4 v[24:27], v251, s[32:35], s80 offen offset:0
MM v[128:131],a[168:171],a[224:227],v[128:131]
TR a[148:149],v145 offset:36864
TR a[150:151],v145 offset:38912
MM v[128:131],a[172:175],a[240:243],v[128:131]
s_waitcnt vmcnt(24)
MM v[136:139],a[172:175],a[244:247],v[136:139]
TR a[152:153],v145 offset:40960
TR a[154:155],v145 offset:43008
MM v[136:139],a[168:171],a[228:231],v[136:139]
s_add_u32 s80, s18, 20480
buffer_load_dwordx4 v[28:31], v251, s[32:35], s80 offen offset:0
MM v[136:139],a[164:167],a[212:215],v[136:139]
TR a[156:157],v145 offset:45056
TR a[158:159],v145 offset:47104
MM v[136:139],a[160:163],a[196:199],v[136:139]
s_waitcnt lgkmcnt(15)
MM v[140:143],a[176:179],a[196:199],v[140:143]
TR a[160:161],v146 offset:32768
TR a[162:163],v146 offset:34816
MM v[140:143],a[180:183],a[212:215],v[140:143]
s_add_u32 s80, s63, 0
buffer_load_dwordx2 v[62:63], v254, s[32:35], s80 offen offset:0
MM v[140:143],a[184:187],a[228:231],v[140:143]
TR a[164:165],v146 offset:36864
TR a[166:167],v146 offset:38912
MM v[140:143],a[188:191],a[244:247],v[140:143]
)ASM"
R"ASM(
buffer_load_dwordx4 v240, s[24:27], s66 offen offset:3072 lds
MM v[132:135],a[188:191],a[240:243],v[132:135]
TR a[168:169],v146 offset:40960
TR a[170:171],v146 offset:43008
MM v[132:135],a[184:187],a[224:227],v[132:135]
MM v[132:135],a[180:183],a[208:211],v[132:135]
TR a[172:173],v146 offset:45056
TR a[174:175],v146 offset:47104
MM v[132:135],a[176:179],a[192:195],v[132:135]
s_waitcnt lgkmcnt(15)
MM v[64:67],a[128:131],a[192:195],v[64:67]
s_waitcnt vmcnt(10)
TR a[176:177],v147 offset:32768
TR a[178:179],v147 offset:34816
MM v[64:67],a[132:135],a[208:211],v[64:67]
v_cmp_ne_u32 vcc, s69, v242
s_cbranch_vccnz LRPO1_%=
LRPO1b_%=:
MM v[64:67],a[136:139],a[224:227],v[64:67]
s_xor_b32 s92, s49, 1
TR a[180:181],v147 offset:36864
TR a[182:183],v147 offset:38912
MM v[64:67],a[140:143],a[240:243],v[64:67]
s_mul_i32 s92, s92, 17408
s_add_u32 s92, s83, s92
s_add_u32 s92, s92, 8192
MM v[72:75],a[140:143],a[244:247],v[72:75]
TR a[184:185],v147 offset:40960
TR a[186:187],v147 offset:43008
MM v[72:75],a[136:139],a[228:231],v[72:75]
buffer_load_dwordx4 a[16:19], v248, s[20:23], s92 offen offset:0 sc1
MM v[72:75],a[132:135],a[212:215],v[72:75]
TR a[188:189],v147 offset:45056
TR a[190:191],v147 offset:47104
MM v[72:75],a[128:131],a[196:199],v[72:75]
s_waitcnt lgkmcnt(15)
buffer_load_dwordx4 a[48:51], v248, s[20:23], s92 offen offset:1024 sc1
MM v[76:79],a[144:147],a[196:199],v[76:79]
TR a[128:129],v228 offset:32768
TR a[130:131],v228 offset:34816
MM v[76:79],a[148:151],a[212:215],v[76:79]
MM v[76:79],a[152:155],a[228:231],v[76:79]
TR a[132:133],v228 offset:36864
TR a[134:135],v228 offset:38912
MM v[76:79],a[156:159],a[244:247],v[76:79]
buffer_load_dwordx4 a[80:83], v248, s[20:23], s92 offen offset:2048 sc1
MM v[68:71],a[156:159],a[240:243],v[68:71]
TR a[136:137],v228 offset:40960
TR a[138:139],v228 offset:43008
MM v[68:71],a[152:155],a[224:227],v[68:71]
MM v[68:71],a[148:151],a[208:211],v[68:71]
TR a[140:141],v228 offset:45056
TR a[142:143],v228 offset:47104
MM v[68:71],a[144:147],a[192:195],v[68:71]
s_waitcnt lgkmcnt(15)
buffer_load_dwordx4 a[112:115], v248, s[20:23], s92 offen offset:3072 sc1
MM v[96:99],a[160:163],a[192:195],v[96:99]
s_add_u32 s92, s92, 4096
TR a[144:145],v229 offset:32768
TR a[146:147],v229 offset:34816
MM v[96:99],a[164:167],a[208:211],v[96:99]
MM v[96:99],a[168:171],a[224:227],v[96:99]
TR a[148:149],v229 offset:36864
TR a[150:151],v229 offset:38912
MM v[96:99],a[172:175],a[240:243],v[96:99]
buffer_load_dwordx4 a[20:23], v248, s[20:23], s92 offen offset:0 sc1
MM v[104:107],a[172:175],a[244:247],v[104:107]
TR a[152:153],v229 offset:40960
TR a[154:155],v229 offset:43008
MM v[104:107],a[168:171],a[228:231],v[104:107]
MM v[104:107],a[164:167],a[212:215],v[104:107]
TR a[156:157],v229 offset:45056
TR a[158:159],v229 offset:47104
MM v[104:107],a[160:163],a[196:199],v[104:107]
s_waitcnt lgkmcnt(15)
buffer_load_dwordx4 a[52:55], v248, s[20:23], s92 offen offset:1024 sc1
MM v[108:111],a[176:179],a[196:199],v[108:111]
TR a[160:161],v230 offset:32768
TR a[162:163],v230 offset:34816
MM v[108:111],a[180:183],a[212:215],v[108:111]
MM v[108:111],a[184:187],a[228:231],v[108:111]
TR a[164:165],v230 offset:36864
TR a[166:167],v230 offset:38912
MM v[108:111],a[188:191],a[244:247],v[108:111]
buffer_load_dwordx4 a[84:87], v248, s[20:23], s92 offen offset:2048 sc1
MM v[100:103],a[188:191],a[240:243],v[100:103]
TR a[168:169],v230 offset:40960
TR a[170:171],v230 offset:43008
MM v[100:103],a[184:187],a[224:227],v[100:103]
MM v[100:103],a[180:183],a[208:211],v[100:103]
TR a[172:173],v230 offset:45056
TR a[174:175],v230 offset:47104
MM v[100:103],a[176:179],a[192:195],v[100:103]
s_waitcnt lgkmcnt(15)
buffer_load_dwordx4 a[116:119], v248, s[20:23], s92 offen offset:3072 sc1
MM v[0:3],a[128:131],a[192:195],v[0:3]
TR a[176:177],v231 offset:32768
TR a[178:179],v231 offset:34816
MM v[0:3],a[132:135],a[208:211],v[0:3]
MM v[0:3],a[136:139],a[224:227],v[0:3]
TR a[180:181],v231 offset:36864
TR a[182:183],v231 offset:38912
MM v[0:3],a[140:143],a[240:243],v[0:3]
MM v[8:11],a[140:143],a[244:247],v[8:11]
TR a[184:185],v231 offset:40960
TR a[186:187],v231 offset:43008
MM v[8:11],a[136:139],a[228:231],v[8:11]
MM v[8:11],a[132:135],a[212:215],v[8:11]
TR a[188:189],v231 offset:45056
TR a[190:191],v231 offset:47104
MM v[8:11],a[128:131],a[196:199],v[8:11]
s_waitcnt lgkmcnt(15)
s_barrier
MM v[12:15],a[144:147],a[196:199],v[12:15]
s_xor_b32 s66, s94, 3
TR a[128:129],v244 offset:0
TR a[130:131],v244 offset:1024
MM v[12:15],a[148:151],a[212:215],v[12:15]
s_mul_i32 s66, s66, 163840
s_add_u32 s66, s66, s88
s_add_u32 m0, s89, 155648
MM v[12:15],a[152:155],a[228:231],v[12:15]
TR a[132:133],v244 offset:2048
TR a[134:135],v244 offset:3072
MM v[12:15],a[156:159],a[244:247],v[12:15]
buffer_load_dwordx4 v252, s[24:27], s66 offen offset:0 lds
MM v[4:7],a[156:159],a[240:243],v[4:7]
s_add_u32 s66, s66, 19456
TR a[136:137],v244 offset:4096
TR a[138:139],v244 offset:5120
MM v[4:7],a[152:155],a[224:227],v[4:7]
MM v[4:7],a[148:151],a[208:211],v[4:7]
TR a[140:141],v244 offset:6144
TR a[142:143],v244 offset:7168
MM v[4:7],a[144:147],a[192:195],v[4:7]
s_waitcnt lgkmcnt(15)
buffer_load_dwordx4 v252, s[24:27], s66 offen offset:1024 lds
MM v[32:35],a[160:163],a[192:195],v[32:35]
TR a[144:145],v235 offset:0
TR a[146:147],v235 offset:1024
MM v[32:35],a[164:167],a[208:211],v[32:35]
s_xor_b32 s66, s94, 3
s_mul_i32 s66, s66, 163840
s_add_u32 s66, s66, s87
MM v[32:35],a[168:171],a[224:227],v[32:35]
s_add_u32 m0, s65, 98304
TR a[148:149],v235 offset:2048
TR a[150:151],v235 offset:3072
MM v[32:35],a[172:175],a[240:243],v[32:35]
buffer_load_dwordx4 v240, s[24:27], s66 offen offset:0 lds
MM v[40:43],a[172:175],a[244:247],v[40:43]
s_add_u32 s66, s66, 9216
TR a[152:153],v235 offset:4096
TR a[154:155],v235 offset:5120
MM v[40:43],a[168:171],a[228:231],v[40:43]
MM v[40:43],a[164:167],a[212:215],v[40:43]
TR a[156:157],v235 offset:6144
TR a[158:159],v235 offset:7168
MM v[40:43],a[160:163],a[196:199],v[40:43]
s_waitcnt lgkmcnt(15)
buffer_load_dwordx4 v240, s[24:27], s66 offen offset:1024 lds
MM v[44:47],a[176:179],a[196:199],v[44:47]
s_add_u32 s66, s66, 9216
TR a[160:161],v144 offset:49152
TR a[162:163],v144 offset:51200
MM v[44:47],a[180:183],a[212:215],v[44:47]
MM v[44:47],a[184:187],a[228:231],v[44:47]
TR a[164:165],v144 offset:53248
TR a[166:167],v144 offset:55296
MM v[44:47],a[188:191],a[244:247],v[44:47]
buffer_load_dwordx4 v240, s[24:27], s66 offen offset:2048 lds
MM v[36:39],a[188:191],a[240:243],v[36:39]
s_add_u32 s66, s66, 9216
TR a[168:169],v144 offset:57344
TR a[170:171],v144 offset:59392
MM v[36:39],a[184:187],a[224:227],v[36:39]
MM v[36:39],a[180:183],a[208:211],v[36:39]
TR a[172:173],v144 offset:61440
TR a[174:175],v144 offset:63488
MM v[36:39],a[176:179],a[192:195],v[36:39]
s_waitcnt lgkmcnt(15)
buffer_load_dwordx4 v240, s[24:27], s66 offen offset:3072 lds
MM v[128:131],a[128:131],v[168:171],v[128:131]
TR a[176:177],v145 offset:49152
TR a[178:179],v145 offset:51200
MM v[128:131],a[132:135],v[184:187],v[128:131]
MM v[128:131],a[136:139],v[200:203],v[128:131]
TR a[180:181],v145 offset:53248
TR a[182:183],v145 offset:55296
MM v[128:131],a[140:143],v[216:219],v[128:131]
MM v[136:139],a[140:143],v[220:223],v[136:139]
TR a[184:185],v145 offset:57344
TR a[186:187],v145 offset:59392
MM v[136:139],a[136:139],v[204:207],v[136:139]
MM v[136:139],a[132:135],v[188:191],v[136:139]
TR a[188:189],v145 offset:61440
TR a[190:191],v145 offset:63488
MM v[136:139],a[128:131],v[172:175],v[136:139]
s_waitcnt lgkmcnt(15)
MM v[140:143],a[144:147],v[172:175],v[140:143]
s_xor_b32 s92, s49, 2
TR a[128:129],v146 offset:49152
TR a[130:131],v146 offset:51200
MM v[140:143],a[148:151],v[188:191],v[140:143]
s_mul_i32 s92, s92, 17408
s_add_u32 s92, s83, s92
LPG28_%=:
s_cbranch_execz LTR1_%=
s_add_u32 s92, s92, 8192
MM v[140:143],a[152:155],v[204:207],v[140:143]
TR a[132:133],v146 offset:53248
TR a[134:135],v146 offset:55296
MM v[140:143],a[156:159],v[220:223],v[140:143]
buffer_load_dwordx4 a[24:27], v248, s[20:23], s92 offen offset:0 sc1
MM v[132:135],a[156:159],v[216:219],v[132:135]
TR a[136:137],v146 offset:57344
TR a[138:139],v146 offset:59392
MM v[132:135],a[152:155],v[200:203],v[132:135]
buffer_load_dwordx4 a[56:59], v248, s[20:23], s92 offen offset:1024 sc1
MM v[132:135],a[148:151],v[184:187],v[132:135]
TR a[140:141],v146 offset:61440
TR a[142:143],v146 offset:63488
MM v[132:135],a[144:147],v[168:171],v[132:135]
s_waitcnt lgkmcnt(15)
MM v[64:67],a[160:163],v[168:171],v[64:67]
TR a[144:145],v147 offset:49152
TR a[146:147],v147 offset:51200
MM v[64:67],a[164:167],v[184:187],v[64:67]
buffer_load_dwordx4 a[88:91], v248, s[20:23], s92 offen offset:2048 sc1
MM v[64:67],a[168:171],v[200:203],v[64:67]
TR a[148:149],v147 offset:53248
TR a[150:151],v147 offset:55296
MM v[64:67],a[172:175],v[216:219],v[64:67]
MM v[72:75],a[172:175],v[220:223],v[72:75]
TR a[152:153],v147 offset:57344
TR a[154:155],v147 offset:59392
MM v[72:75],a[168:171],v[204:207],v[72:75]
buffer_load_dwordx4 a[120:123], v248, s[20:23], s92 offen offset:3072 sc1
MM v[72:75],a[164:167],v[188:191],v[72:75]
s_add_u32 s92, s92, 4096
TR a[156:157],v147 offset:61440
TR a[158:159],v147 offset:63488
MM v[72:75],a[160:163],v[172:175],v[72:75]
s_waitcnt lgkmcnt(15)
MM v[76:79],a[176:179],v[172:175],v[76:79]
TR a[160:161],v230 offset:49152
TR a[162:163],v230 offset:51200
MM v[76:79],a[180:183],v[188:191],v[76:79]
buffer_load_dwordx4 a[28:31], v248, s[20:23], s92 offen offset:0 sc1
MM v[76:79],a[184:187],v[204:207],v[76:79]
TR a[164:165],v230 offset:53248
TR a[166:167],v230 offset:55296
MM v[76:79],a[188:191],v[220:223],v[76:79]
MM v[68:71],a[188:191],v[216:219],v[68:71]
TR a[168:169],v230 offset:57344
TR a[170:171],v230 offset:59392
MM v[68:71],a[184:187],v[200:203],v[68:71]
buffer_load_dwordx4 a[60:63], v248, s[20:23], s92 offen offset:1024 sc1
MM v[68:71],a[180:183],v[184:187],v[68:71]
TR a[172:173],v230 offset:61440
TR a[174:175],v230 offset:63488
MM v[68:71],a[176:179],v[168:171],v[68:71]
s_waitcnt lgkmcnt(15)
MM v[96:99],a[128:131],v[168:171],v[96:99]
TR a[176:177],v231 offset:49152
TR a[178:179],v231 offset:51200
MM v[96:99],a[132:135],v[184:187],v[96:99]
buffer_load_dwordx4 a[92:95], v248, s[20:23], s92 offen offset:2048 sc1
MM v[96:99],a[136:139],v[200:203],v[96:99]
TR a[180:181],v231 offset:53248
TR a[182:183],v231 offset:55296
MM v[96:99],a[140:143],v[216:219],v[96:99]
MM v[104:107],a[140:143],v[220:223],v[104:107]
TR a[184:185],v231 offset:57344
TR a[186:187],v231 offset:59392
MM v[104:107],a[136:139],v[204:207],v[104:107]
buffer_load_dwordx4 a[124:127], v248, s[20:23], s92 offen offset:3072 sc1
MM v[104:107],a[132:135],v[188:191],v[104:107]
s_xor_b32 s92, s49, 3
TR a[188:189],v231 offset:61440
TR a[190:191],v231 offset:63488
MM v[104:107],a[128:131],v[172:175],v[104:107]
s_waitcnt lgkmcnt(15)
s_mul_i32 s92, s92, 17408
s_add_u32 s92, s83, s92
s_barrier
MM v[108:111],a[144:147],v[172:175],v[108:111]
s_add_u32 s92, s92, 8192
TR a[128:129],v244 offset:8192
TR a[130:131],v244 offset:9216
MM v[108:111],a[148:151],v[188:191],v[108:111]
buffer_load_dwordx4 a[200:203], v248, s[20:23], s92 offen offset:0 sc1
MM v[108:111],a[152:155],v[204:207],v[108:111]
TR a[132:133],v244 offset:10240
TR a[134:135],v244 offset:11264
MM v[108:111],a[156:159],v[220:223],v[108:111]
MM v[100:103],a[156:159],v[216:219],v[100:103]
TR a[136:137],v244 offset:12288
TR a[138:139],v244 offset:13312
MM v[100:103],a[152:155],v[200:203],v[100:103]
buffer_load_dwordx4 a[216:219], v248, s[20:23], s92 offen offset:1024 sc1
MM v[100:103],a[148:151],v[184:187],v[100:103]
TR a[140:141],v244 offset:14336
TR a[142:143],v244 offset:15360
MM v[100:103],a[144:147],v[168:171],v[100:103]
s_waitcnt lgkmcnt(15)
MM v[32:35],a[160:163],v[168:171],v[32:35]
TR a[144:145],v235 offset:8192
TR a[146:147],v235 offset:9216
MM v[32:35],a[164:167],v[184:187],v[32:35]
buffer_load_dwordx4 a[232:235], v248, s[20:23], s92 offen offset:2048 sc1
MM v[32:35],a[168:171],v[200:203],v[32:35]
TR a[148:149],v235 offset:10240
TR a[150:151],v235 offset:11264
MM v[32:35],a[172:175],v[216:219],v[32:35]
)ASM"
R"ASM(
MM v[40:43],a[172:175],v[220:223],v[40:43]
TR a[152:153],v235 offset:12288
TR a[154:155],v235 offset:13312
MM v[40:43],a[168:171],v[204:207],v[40:43]
buffer_load_dwordx4 a[248:251], v248, s[20:23], s92 offen offset:3072 sc1
MM v[40:43],a[164:167],v[188:191],v[40:43]
s_add_u32 s92, s92, 4096
TR a[156:157],v235 offset:14336
TR a[158:159],v235 offset:15360
MM v[40:43],a[160:163],v[172:175],v[40:43]
s_waitcnt lgkmcnt(15)
MM v[44:47],a[176:179],v[172:175],v[44:47]
TR a[160:161],v144 offset:0
TR a[162:163],v144 offset:2048
MM v[44:47],a[180:183],v[188:191],v[44:47]
buffer_load_dwordx4 a[204:207], v248, s[20:23], s92 offen offset:0 sc1
MM v[44:47],a[184:187],v[204:207],v[44:47]
TR a[164:165],v144 offset:4096
TR a[166:167],v144 offset:6144
MM v[44:47],a[188:191],v[220:223],v[44:47]
MM v[36:39],a[188:191],v[216:219],v[36:39]
TR a[168:169],v144 offset:8192
TR a[170:171],v144 offset:10240
MM v[36:39],a[184:187],v[200:203],v[36:39]
buffer_load_dwordx4 a[220:223], v248, s[20:23], s92 offen offset:1024 sc1
MM v[36:39],a[180:183],v[184:187],v[36:39]
TR a[172:173],v144 offset:12288
TR a[174:175],v144 offset:14336
MM v[36:39],a[176:179],v[168:171],v[36:39]
s_waitcnt lgkmcnt(15)
s_waitcnt vmcnt(24)
MM v[128:131],a[128:131],a[16:19],v[128:131]
TR a[176:177],v145 offset:0
TR a[178:179],v145 offset:2048
MM v[128:131],a[132:135],a[48:51],v[128:131]
buffer_load_dwordx4 a[236:239], v248, s[20:23], s92 offen offset:2048 sc1
MM v[128:131],a[136:139],a[80:83],v[128:131]
TR a[180:181],v145 offset:4096
TR a[182:183],v145 offset:6144
MM v[128:131],a[140:143],a[112:115],v[128:131]
s_waitcnt vmcnt(21)
MM v[136:139],a[140:143],a[116:119],v[136:139]
TR a[184:185],v145 offset:8192
TR a[186:187],v145 offset:10240
MM v[136:139],a[136:139],a[84:87],v[136:139]
buffer_load_dwordx4 a[252:255], v248, s[20:23], s92 offen offset:3072 sc1
MM v[136:139],a[132:135],a[52:55],v[136:139]
TR a[188:189],v145 offset:12288
TR a[190:191],v145 offset:14336
MM v[136:139],a[128:131],a[20:23],v[136:139]
s_waitcnt lgkmcnt(15)
MM v[140:143],a[144:147],a[20:23],v[140:143]
TR a[128:129],v146 offset:0
TR a[130:131],v146 offset:2048
MM v[140:143],a[148:151],a[52:55],v[140:143]
MM v[140:143],a[152:155],a[84:87],v[140:143]
TR a[132:133],v146 offset:4096
TR a[134:135],v146 offset:6144
MM v[140:143],a[156:159],a[116:119],v[140:143]
MM v[132:135],a[156:159],a[112:115],v[132:135]
TR a[136:137],v146 offset:8192
TR a[138:139],v146 offset:10240
MM v[132:135],a[152:155],a[80:83],v[132:135]
MM v[132:135],a[148:151],a[48:51],v[132:135]
TR a[140:141],v146 offset:12288
TR a[142:143],v146 offset:14336
MM v[132:135],a[144:147],a[16:19],v[132:135]
s_waitcnt lgkmcnt(15)
MM v[64:67],a[160:163],a[16:19],v[64:67]
TR a[144:145],v147 offset:0
TR a[146:147],v147 offset:2048
MM v[64:67],a[164:167],a[48:51],v[64:67]
MM v[64:67],a[168:171],a[80:83],v[64:67]
TR a[148:149],v147 offset:4096
TR a[150:151],v147 offset:6144
MM v[64:67],a[172:175],a[112:115],v[64:67]
MM v[72:75],a[172:175],a[116:119],v[72:75]
TR a[152:153],v147 offset:8192
TR a[154:155],v147 offset:10240
MM v[72:75],a[168:171],a[84:87],v[72:75]
MM v[72:75],a[164:167],a[52:55],v[72:75]
TR a[156:157],v147 offset:12288
TR a[158:159],v147 offset:14336
MM v[72:75],a[160:163],a[20:23],v[72:75]
s_waitcnt lgkmcnt(15)
s_waitcnt vmcnt(16)
s_barrier
MM v[76:79],a[176:179],a[20:23],v[76:79]
s_xor_b32 s66, s94, 1
TR a[160:161],v244 offset:16384
TR a[162:163],v244 offset:17408
MM v[76:79],a[180:183],a[52:55],v[76:79]
s_mul_i32 s66, s66, 163840
s_add_u32 s66, s66, s86
s_add_u32 m0, s65, 0
MM v[76:79],a[184:187],a[84:87],v[76:79]
TR a[164:165],v244 offset:18432
TR a[166:167],v244 offset:19456
MM v[76:79],a[188:191],a[116:119],v[76:79]
buffer_load_dwordx4 v240, s[24:27], s66 offen offset:0 lds
MM v[68:71],a[188:191],a[112:115],v[68:71]
s_add_u32 s66, s66, 9216
TR a[168:169],v244 offset:20480
TR a[170:171],v244 offset:21504
MM v[68:71],a[184:187],a[80:83],v[68:71]
MM v[68:71],a[180:183],a[48:51],v[68:71]
TR a[172:173],v244 offset:22528
TR a[174:175],v244 offset:23552
MM v[68:71],a[176:179],a[16:19],v[68:71]
s_waitcnt lgkmcnt(15)
buffer_load_dwordx4 v240, s[24:27], s66 offen offset:1024 lds
MM v[96:99],a[128:131],a[16:19],v[96:99]
s_add_u32 s66, s66, 9216
TR a[176:177],v244 offset:24576
TR a[178:179],v244 offset:25600
MM v[96:99],a[132:135],a[48:51],v[96:99]
MM v[96:99],a[136:139],a[80:83],v[96:99]
TR a[180:181],v244 offset:26624
TR a[182:183],v244 offset:27648
MM v[96:99],a[140:143],a[112:115],v[96:99]
buffer_load_dwordx4 v240, s[24:27], s66 offen offset:2048 lds
MM v[104:107],a[140:143],a[116:119],v[104:107]
s_add_u32 s66, s66, 9216
TR a[184:185],v244 offset:28672
TR a[186:187],v244 offset:29696
MM v[104:107],a[136:139],a[84:87],v[104:107]
MM v[104:107],a[132:135],a[52:55],v[104:107]
TR a[188:189],v244 offset:30720
TR a[190:191],v244 offset:31744
MM v[104:107],a[128:131],a[20:23],v[104:107]
s_waitcnt lgkmcnt(15)
buffer_load_dwordx4 v240, s[24:27], s66 offen offset:3072 lds
MM v[108:111],a[144:147],a[20:23],v[108:111]
TR a[128:129],v235 offset:16384
TR a[130:131],v235 offset:17408
MM v[108:111],a[148:151],a[52:55],v[108:111]
s_xor_b32 s66, s94, 2
s_mul_i32 s66, s66, 163840
s_add_u32 s66, s66, s86
MM v[108:111],a[152:155],a[84:87],v[108:111]
s_add_u32 m0, s65, 16384
TR a[132:133],v235 offset:18432
TR a[134:135],v235 offset:19456
MM v[108:111],a[156:159],a[116:119],v[108:111]
buffer_load_dwordx4 v240, s[24:27], s66 offen offset:0 lds
MM v[100:103],a[156:159],a[112:115],v[100:103]
s_add_u32 s66, s66, 9216
TR a[136:137],v235 offset:20480
TR a[138:139],v235 offset:21504
MM v[100:103],a[152:155],a[80:83],v[100:103]
MM v[100:103],a[148:151],a[48:51],v[100:103]
TR a[140:141],v235 offset:22528
TR a[142:143],v235 offset:23552
MM v[100:103],a[144:147],a[16:19],v[100:103]
s_waitcnt lgkmcnt(15)
buffer_load_dwordx4 v240, s[24:27], s66 offen offset:1024 lds
s_waitcnt vmcnt(18)
MM v[128:131],a[160:163],a[24:27],v[128:131]
s_add_u32 s66, s66, 9216
TR a[144:145],v235 offset:24576
TR a[146:147],v235 offset:25600
MM v[128:131],a[164:167],a[56:59],v[128:131]
MM v[128:131],a[168:171],a[88:91],v[128:131]
TR a[148:149],v235 offset:26624
TR a[150:151],v235 offset:27648
MM v[128:131],a[172:175],a[120:123],v[128:131]
s_waitcnt vmcnt(14)
buffer_load_dwordx4 v240, s[24:27], s66 offen offset:2048 lds
MM v[136:139],a[172:175],a[124:127],v[136:139]
s_add_u32 s66, s66, 9216
TR a[152:153],v235 offset:28672
TR a[154:155],v235 offset:29696
MM v[136:139],a[168:171],a[92:95],v[136:139]
MM v[136:139],a[164:167],a[60:63],v[136:139]
TR a[156:157],v235 offset:30720
TR a[158:159],v235 offset:31744
MM v[136:139],a[160:163],a[28:31],v[136:139]
s_waitcnt lgkmcnt(15)
buffer_load_dwordx4 v240, s[24:27], s66 offen offset:3072 lds
s_waitcnt vmcnt(12)
MM v[128:131],a[176:179],a[200:203],v[128:131]
s_memrealtime s[56:57]
TR a[160:161],v144 offset:16384
TR a[162:163],v144 offset:18432
MM v[128:131],a[180:183],a[216:219],v[128:131]
s_xor_b32 s66, s94, 3
s_mul_i32 s66, s66, 163840
s_add_u32 s66, s66, s86
MM v[128:131],a[184:187],a[232:235],v[128:131]
s_add_u32 m0, s65, 32768
TR a[164:165],v144 offset:20480
TR a[166:167],v144 offset:22528
MM v[128:131],a[188:191],a[248:251],v[128:131]
s_waitcnt vmcnt(8)
buffer_load_dwordx4 v240, s[24:27], s66 offen offset:0 lds
MM v[136:139],a[188:191],a[252:255],v[136:139]
s_add_u32 s66, s66, 9216
TR a[168:169],v144 offset:24576
TR a[170:171],v144 offset:26624
MM v[136:139],a[184:187],a[236:239],v[136:139]
MM v[136:139],a[180:183],a[220:223],v[136:139]
TR a[172:173],v144 offset:28672
TR a[174:175],v144 offset:30720
MM v[136:139],a[176:179],a[204:207],v[136:139]
s_waitcnt lgkmcnt(0)
buffer_load_dwordx4 v240, s[24:27], s66 offen offset:1024 lds
MM v[140:143],a[128:131],a[28:31],v[140:143]
s_add_u32 s66, s66, 9216
TR a[176:177],v144 offset:32768
TR a[178:179],v144 offset:34816
MM v[140:143],a[132:135],a[60:63],v[140:143]
v_mov_b32 v158, s56
v_mov_b32 v159, s69
MM v[140:143],a[136:139],a[92:95],v[140:143]
TR a[180:181],v144 offset:36864
TR a[182:183],v144 offset:38912
MM v[140:143],a[140:143],a[124:127],v[140:143]
s_add_u32 s67, s95, 0
CV v128,v128,v129
CV v129,v130,v131
MM v[132:135],a[140:143],a[120:123],v[132:135]
buffer_load_dwordx4 v240, s[24:27], s66 offen offset:2048 lds
s_mov_b64 exec, 1
buffer_store_dwordx2 v[158:159], off, s[20:23], s19 offset:8 sc1
s_mov_b64 exec, -1
s_nop 0
TR a[184:185],v144 offset:40960
TR a[186:187],v144 offset:43008
MM v[132:135],a[136:139],a[88:91],v[132:135]
s_add_u32 s66, s66, 9216
CV v136,v136,v137
CV v137,v138,v139
MM v[132:135],a[132:135],a[56:59],v[132:135]
TR a[188:189],v144 offset:45056
TR a[190:191],v144 offset:47104
MM v[132:135],a[128:131],a[24:27],v[132:135]
buffer_load_dwordx4 v240, s[24:27], s66 offen offset:3072 lds
MM v[132:135],a[144:147],a[200:203],v[132:135]
TR a[128:129],v145 offset:16384
TR a[130:131],v145 offset:18432
MM v[132:135],a[148:151],a[216:219],v[132:135]
MM v[132:135],a[152:155],a[232:235],v[132:135]
TR a[132:133],v145 offset:20480
TR a[134:135],v145 offset:22528
MM v[132:135],a[156:159],a[248:251],v[132:135]
MM v[140:143],a[156:159],a[252:255],v[140:143]
TR a[136:137],v145 offset:24576
TR a[138:139],v145 offset:26624
MM v[140:143],a[152:155],a[236:239],v[140:143]
MM v[140:143],a[148:151],a[220:223],v[140:143]
TR a[140:141],v145 offset:28672
TR a[142:143],v145 offset:30720
MM v[140:143],a[144:147],a[204:207],v[140:143]
CV v130,v132,v133
CV v131,v134,v135
MM v[72:75],a[160:163],a[28:31],v[72:75]
TR a[144:145],v145 offset:32768
TR a[146:147],v145 offset:34816
MM v[72:75],a[164:167],a[60:63],v[72:75]
v_min_u32 v131, -2, v131
MM v[72:75],a[168:171],a[92:95],v[72:75]
TR a[148:149],v145 offset:36864
TR a[150:151],v145 offset:38912
MM v[72:75],a[172:175],a[124:127],v[72:75]
buffer_store_dwordx4 v[128:131], v249, s[20:23], s67 offen offset:0 sc1
MM v[64:67],a[172:175],a[120:123],v[64:67]
TR a[152:153],v145 offset:40960
TR a[154:155],v145 offset:43008
MM v[64:67],a[168:171],a[88:91],v[64:67]
CV v138,v140,v141
CV v139,v142,v143
MM v[64:67],a[164:167],a[56:59],v[64:67]
TR a[156:157],v145 offset:45056
TR a[158:159],v145 offset:47104
MM v[64:67],a[160:163],a[24:27],v[64:67]
s_waitcnt lgkmcnt(15)
v_min_u32 v139, -2, v139
MM v[64:67],a[176:179],a[200:203],v[64:67]
TR a[160:161],v146 offset:16384
TR a[162:163],v146 offset:18432
MM v[64:67],a[180:183],a[216:219],v[64:67]
buffer_store_dwordx4 v[136:139], v249, s[20:23], s67 offen offset:1024 sc1
MM v[64:67],a[184:187],a[232:235],v[64:67]
TR a[164:165],v146 offset:20480
TR a[166:167],v146 offset:22528
MM v[64:67],a[188:191],a[248:251],v[64:67]
MM v[72:75],a[188:191],a[252:255],v[72:75]
TR a[168:169],v146 offset:24576
TR a[170:171],v146 offset:26624
MM v[72:75],a[184:187],a[236:239],v[72:75]
MM v[72:75],a[180:183],a[220:223],v[72:75]
TR a[172:173],v146 offset:28672
TR a[174:175],v146 offset:30720
MM v[72:75],a[176:179],a[204:207],v[72:75]
s_waitcnt lgkmcnt(15)
MM v[76:79],a[128:131],a[28:31],v[76:79]
TR a[176:177],v146 offset:32768
TR a[178:179],v146 offset:34816
MM v[76:79],a[132:135],a[60:63],v[76:79]
v_lshlrev_b32 v136, 16, v150
v_and_b32 v137, 0xffff0000, v150
MM v[76:79],a[136:139],a[92:95],v[76:79]
TR a[180:181],v146 offset:36864
TR a[182:183],v146 offset:38912
MM v[76:79],a[140:143],a[124:127],v[76:79]
s_add_u32 s67, s95, 4096
CV v64,v64,v65
CV v65,v66,v67
MM v[68:71],a[140:143],a[120:123],v[68:71]
TR a[184:185],v146 offset:40960
TR a[186:187],v146 offset:43008
MM v[68:71],a[136:139],a[88:91],v[68:71]
CV v72,v72,v73
CV v73,v74,v75
MM v[68:71],a[132:135],a[56:59],v[68:71]
TR a[188:189],v146 offset:45056
TR a[190:191],v146 offset:47104
MM v[68:71],a[128:131],a[24:27],v[68:71]
s_waitcnt lgkmcnt(15)
v_lshlrev_b32 v138, 16, v151
v_and_b32 v139, 0xffff0000, v151
MM v[68:71],a[144:147],a[200:203],v[68:71]
TR a[128:129],v147 offset:16384
TR a[130:131],v147 offset:18432
MM v[68:71],a[148:151],a[216:219],v[68:71]
buffer_load_dwordx2 v[148:149], v253, s[20:23], s68 offen offset:8 sc1
MM v[68:71],a[152:155],a[232:235],v[68:71]
TR a[132:133],v147 offset:20480
TR a[134:135],v147 offset:22528
MM v[68:71],a[156:159],a[248:251],v[68:71]
v_mul_f32 v140, 0xbfb8aa3b, v136
)ASM"
R"ASM(
v_mul_f32 v141, 0xbfb8aa3b, v137
MM v[76:79],a[156:159],a[252:255],v[76:79]
TR a[136:137],v147 offset:24576
TR a[138:139],v147 offset:26624
MM v[76:79],a[152:155],a[236:239],v[76:79]
v_mul_f32 v142, 0xbfb8aa3b, v138
v_mul_f32 v143, 0xbfb8aa3b, v139
MM v[76:79],a[148:151],a[220:223],v[76:79]
TR a[140:141],v147 offset:28672
TR a[142:143],v147 offset:30720
MM v[76:79],a[144:147],a[204:207],v[76:79]
s_waitcnt lgkmcnt(15)
CV v66,v68,v69
CV v67,v70,v71
s_waitcnt vmcnt(3)
s_barrier
MM v[104:107],a[160:163],a[28:31],v[104:107]
TR a[144:145],v147 offset:32768
TR a[146:147],v147 offset:34816
MM v[104:107],a[164:167],a[60:63],v[104:107]
EX v140,v140
v_min_u32 v67, -2, v67
MM v[104:107],a[168:171],a[92:95],v[104:107]
TR a[148:149],v147 offset:36864
TR a[150:151],v147 offset:38912
MM v[104:107],a[172:175],a[124:127],v[104:107]
buffer_store_dwordx4 v[64:67], v248, s[20:23], s67 offen offset:0 sc1
MM v[96:99],a[172:175],a[120:123],v[96:99]
TR a[152:153],v147 offset:40960
TR a[154:155],v147 offset:43008
MM v[96:99],a[168:171],a[88:91],v[96:99]
CV v74,v76,v77
CV v75,v78,v79
MM v[96:99],a[164:167],a[56:59],v[96:99]
TR a[156:157],v147 offset:45056
TR a[158:159],v147 offset:47104
MM v[96:99],a[160:163],a[24:27],v[96:99]
s_waitcnt lgkmcnt(15)
EX v141,v141
v_min_u32 v75, -2, v75
MM v[96:99],a[176:179],a[200:203],v[96:99]
TR a[160:161],v230 offset:0
TR a[162:163],v230 offset:2048
MM v[96:99],a[180:183],a[216:219],v[96:99]
buffer_store_dwordx4 v[72:75], v248, s[20:23], s67 offen offset:1024 sc1
MM v[96:99],a[184:187],a[232:235],v[96:99]
TR a[164:165],v230 offset:4096
TR a[166:167],v230 offset:6144
MM v[96:99],a[188:191],a[248:251],v[96:99]
EX v142,v142
MM v[104:107],a[188:191],a[252:255],v[104:107]
TR a[168:169],v230 offset:8192
TR a[170:171],v230 offset:10240
MM v[104:107],a[184:187],a[236:239],v[104:107]
EX v143,v143
MM v[104:107],a[180:183],a[220:223],v[104:107]
TR a[172:173],v230 offset:12288
TR a[174:175],v230 offset:14336
MM v[104:107],a[176:179],a[204:207],v[104:107]
s_waitcnt lgkmcnt(15)
v_add_f32 v140, 1.0, v140
v_add_f32 v141, 1.0, v141
MM v[108:111],a[128:131],a[28:31],v[108:111]
TR a[176:177],v230 offset:16384
TR a[178:179],v230 offset:18432
MM v[108:111],a[132:135],a[60:63],v[108:111]
v_add_f32 v142, 1.0, v142
v_add_f32 v143, 1.0, v143
MM v[108:111],a[136:139],a[92:95],v[108:111]
TR a[180:181],v230 offset:20480
TR a[182:183],v230 offset:22528
MM v[108:111],a[140:143],a[124:127],v[108:111]
s_add_u32 s67, s95, 4096
CV v96,v96,v97
CV v97,v98,v99
MM v[100:103],a[140:143],a[120:123],v[100:103]
TR a[184:185],v230 offset:24576
TR a[186:187],v230 offset:26624
MM v[100:103],a[136:139],a[88:91],v[100:103]
CV v104,v104,v105
CV v105,v106,v107
MM v[100:103],a[132:135],a[56:59],v[100:103]
TR a[188:189],v230 offset:28672
TR a[190:191],v230 offset:30720
MM v[100:103],a[128:131],a[24:27],v[100:103]
s_waitcnt lgkmcnt(15)
v_rcp_f32 v140, v140
MM v[100:103],a[144:147],a[200:203],v[100:103]
TR a[128:129],v230 offset:32768
TR a[130:131],v230 offset:34816
MM v[100:103],a[148:151],a[216:219],v[100:103]
v_rcp_f32 v141, v141
MM v[100:103],a[152:155],a[232:235],v[100:103]
TR a[132:133],v230 offset:36864
TR a[134:135],v230 offset:38912
MM v[100:103],a[156:159],a[248:251],v[100:103]
v_rcp_f32 v142, v142
MM v[108:111],a[156:159],a[252:255],v[108:111]
TR a[136:137],v230 offset:40960
TR a[138:139],v230 offset:43008
MM v[108:111],a[152:155],a[236:239],v[108:111]
v_rcp_f32 v143, v143
MM v[108:111],a[148:151],a[220:223],v[108:111]
TR a[140:141],v230 offset:45056
TR a[142:143],v230 offset:47104
MM v[108:111],a[144:147],a[204:207],v[108:111]
s_waitcnt lgkmcnt(15)
LPG29_%=:
s_cbranch_execz LTR1_%=
CV v98,v100,v101
CV v99,v102,v103
MM v[40:43],a[160:163],a[20:23],v[40:43]
TR a[144:145],v231 offset:0
TR a[146:147],v231 offset:2048
MM v[40:43],a[164:167],a[52:55],v[40:43]
v_min_u32 v99, -2, v99
v_mul_f32 v140, v136, v140
MM v[40:43],a[168:171],a[84:87],v[40:43]
TR a[148:149],v231 offset:4096
TR a[150:151],v231 offset:6144
MM v[40:43],a[172:175],a[116:119],v[40:43]
buffer_store_dwordx4 v[96:99], v248, s[20:23], s67 offen offset:2048 sc1
MM v[32:35],a[172:175],a[112:115],v[32:35]
TR a[152:153],v231 offset:8192
TR a[154:155],v231 offset:10240
MM v[32:35],a[168:171],a[80:83],v[32:35]
CV v106,v108,v109
CV v107,v110,v111
MM v[32:35],a[164:167],a[48:51],v[32:35]
TR a[156:157],v231 offset:12288
TR a[158:159],v231 offset:14336
MM v[32:35],a[160:163],a[16:19],v[32:35]
s_waitcnt lgkmcnt(15)
v_min_u32 v107, -2, v107
v_mul_f32 v141, v137, v141
MM v[32:35],a[176:179],a[24:27],v[32:35]
TR a[160:161],v231 offset:16384
TR a[162:163],v231 offset:18432
MM v[32:35],a[180:183],a[56:59],v[32:35]
buffer_store_dwordx4 v[104:107], v248, s[20:23], s67 offen offset:3072 sc1
MM v[32:35],a[184:187],a[88:91],v[32:35]
TR a[164:165],v231 offset:20480
TR a[166:167],v231 offset:22528
MM v[32:35],a[188:191],a[120:123],v[32:35]
v_mul_f32 v142, v138, v142
v_mul_f32 v143, v139, v143
MM v[40:43],a[188:191],a[124:127],v[40:43]
TR a[168:169],v231 offset:24576
TR a[170:171],v231 offset:26624
MM v[40:43],a[184:187],a[92:95],v[40:43]
CV v150,v140,v141
CV v151,v142,v143
MM v[40:43],a[180:183],a[60:63],v[40:43]
TR a[172:173],v231 offset:28672
TR a[174:175],v231 offset:30720
MM v[40:43],a[176:179],a[28:31],v[40:43]
s_waitcnt lgkmcnt(15)
v_lshlrev_b32 v160, 16, v16
v_and_b32 v161, 0xffff0000, v16
MM v[40:43],a[128:131],a[204:207],v[40:43]
TR a[176:177],v231 offset:32768
TR a[178:179],v231 offset:34816
MM v[40:43],a[132:135],a[220:223],v[40:43]
v_lshlrev_b32 v162, 16, v17
v_and_b32 v163, 0xffff0000, v17
MM v[40:43],a[136:139],a[236:239],v[40:43]
TR a[180:181],v231 offset:36864
TR a[182:183],v231 offset:38912
MM v[40:43],a[140:143],a[252:255],v[40:43]
v_lshlrev_b32 v164, 16, v18
v_and_b32 v165, 0xffff0000, v18
MM v[32:35],a[140:143],a[248:251],v[32:35]
TR a[184:185],v231 offset:40960
TR a[186:187],v231 offset:43008
MM v[32:35],a[136:139],a[232:235],v[32:35]
v_lshlrev_b32 v166, 16, v19
v_and_b32 v167, 0xffff0000, v19
MM v[32:35],a[132:135],a[216:219],v[32:35]
TR a[188:189],v231 offset:45056
TR a[190:191],v231 offset:47104
MM v[32:35],a[128:131],a[200:203],v[32:35]
s_waitcnt lgkmcnt(15)
v_mul_f32 v136, 0xbfb8aa3b, v160
v_mul_f32 v137, 0xbfb8aa3b, v161
MM v[36:39],a[144:147],a[16:19],v[36:39]
TR a[128:129],v228 offset:49152
TR a[130:131],v228 offset:51200
MM v[36:39],a[148:151],a[48:51],v[36:39]
v_mul_f32 v138, 0xbfb8aa3b, v162
v_mul_f32 v139, 0xbfb8aa3b, v163
MM v[36:39],a[152:155],a[80:83],v[36:39]
TR a[132:133],v228 offset:53248
TR a[134:135],v228 offset:55296
MM v[36:39],a[156:159],a[112:115],v[36:39]
s_add_u32 s67, s95, 0
CV v32,v32,v33
CV v33,v34,v35
MM v[44:47],a[156:159],a[116:119],v[44:47]
TR a[136:137],v228 offset:57344
TR a[138:139],v228 offset:59392
MM v[44:47],a[152:155],a[84:87],v[44:47]
CV v40,v40,v41
CV v41,v42,v43
MM v[44:47],a[148:151],a[52:55],v[44:47]
TR a[140:141],v228 offset:61440
TR a[142:143],v228 offset:63488
MM v[44:47],a[144:147],a[20:23],v[44:47]
s_waitcnt lgkmcnt(15)
v_mul_f32 v140, 0xbfb8aa3b, v164
v_mul_f32 v141, 0xbfb8aa3b, v165
MM v[44:47],a[160:163],a[28:31],v[44:47]
s_waitcnt vmcnt(4)
TR a[144:145],v228 offset:0
TR a[146:147],v228 offset:2048
MM v[44:47],a[164:167],a[60:63],v[44:47]
v_cmp_eq_u32 s[8:9], s69, v149
v_subrev_u32 v226, s56, v148
MM v[44:47],a[168:171],a[92:95],v[44:47]
TR a[148:149],v228 offset:4096
TR a[150:151],v228 offset:6144
MM v[44:47],a[172:175],a[124:127],v[44:47]
v_cndmask_b32 v226, 4.0, v226, s[8:9]
v_mul_f32 v142, 0xbfb8aa3b, v166
MM v[36:39],a[172:175],a[120:123],v[36:39]
TR a[152:153],v228 offset:8192
TR a[154:155],v228 offset:10240
MM v[36:39],a[168:171],a[88:91],v[36:39]
v_readlane_b32 s13, v226, 16
v_readlane_b32 s14, v226, 32
MM v[36:39],a[164:167],a[56:59],v[36:39]
TR a[156:157],v228 offset:12288
TR a[158:159],v228 offset:14336
MM v[36:39],a[160:163],a[24:27],v[36:39]
s_waitcnt lgkmcnt(15)
v_readlane_b32 s15, v226, 48
v_mul_f32 v143, 0xbfb8aa3b, v167
MM v[36:39],a[176:179],a[200:203],v[36:39]
s_max_i32 s12, s13, s14
TR a[160:161],v228 offset:16384
TR a[162:163],v228 offset:18432
MM v[36:39],a[180:183],a[216:219],v[36:39]
s_max_i32 s12, s12, s15
EX v136,v136
MM v[36:39],a[184:187],a[232:235],v[36:39]
s_max_i32 s12, s12, 0
TR a[164:165],v228 offset:20480
TR a[166:167],v228 offset:22528
MM v[36:39],a[188:191],a[248:251],v[36:39]
buffer_load_dwordx2 v[148:149], v253, s[20:23], s68 offen offset:8 sc1
MM v[44:47],a[188:191],a[252:255],v[44:47]
TR a[168:169],v228 offset:24576
TR a[170:171],v228 offset:26624
MM v[44:47],a[184:187],a[236:239],v[44:47]
EX v137,v137
MM v[44:47],a[180:183],a[220:223],v[44:47]
TR a[172:173],v228 offset:28672
TR a[174:175],v228 offset:30720
MM v[44:47],a[176:179],a[204:207],v[44:47]
s_waitcnt lgkmcnt(15)
CV v34,v36,v37
CV v35,v38,v39
v_min_u32 v35, -2, v35
buffer_store_dwordx4 v[32:35], v248, s[20:23], s67 offen offset:2048 sc1
MM v[8:11],a[128:131],v[172:175],v[8:11]
TR a[176:177],v228 offset:32768
TR a[178:179],v228 offset:34816
MM v[8:11],a[132:135],v[188:191],v[8:11]
EX v138,v138
MM v[8:11],a[136:139],v[204:207],v[8:11]
TR a[180:181],v228 offset:36864
TR a[182:183],v228 offset:38912
MM v[8:11],a[140:143],v[220:223],v[8:11]
CV v42,v44,v45
CV v43,v46,v47
v_min_u32 v43, -2, v43
buffer_store_dwordx4 v[40:43], v248, s[20:23], s67 offen offset:3072 sc1
MM v[0:3],a[140:143],v[216:219],v[0:3]
TR a[184:185],v228 offset:40960
TR a[186:187],v228 offset:43008
MM v[0:3],a[136:139],v[200:203],v[0:3]
EX v139,v139
MM v[0:3],a[132:135],v[184:187],v[0:3]
TR a[188:189],v228 offset:45056
TR a[190:191],v228 offset:47104
MM v[0:3],a[128:131],v[168:171],v[0:3]
s_waitcnt lgkmcnt(15)
EX v140,v140
MM v[0:3],a[144:147],a[16:19],v[0:3]
TR a[128:129],v229 offset:49152
TR a[130:131],v229 offset:51200
MM v[0:3],a[148:151],a[48:51],v[0:3]
s_cmp_le_i32 s13, -42
s_cselect_b32 s60, 2, 0
s_cselect_b64 s[8:9], -1, 0
MM v[0:3],a[152:155],a[80:83],v[0:3]
s_or_b32 s58, s58, s60
TR a[132:133],v229 offset:53248
TR a[134:135],v229 offset:55296
MM v[0:3],a[156:159],a[112:115],v[0:3]
s_mov_b64 exec, s[8:9]
buffer_load_dwordx4 v[80:83], v248, s[20:23], s36 offen offset:0 sc1
s_mov_b64 exec, -1
MM v[8:11],a[156:159],a[116:119],v[8:11]
TR a[136:137],v229 offset:57344
TR a[138:139],v229 offset:59392
MM v[8:11],a[152:155],a[84:87],v[8:11]
s_mov_b64 exec, s[8:9]
buffer_load_dwordx4 v[84:87], v248, s[20:23], s36 offen offset:1024 sc1
s_mov_b64 exec, -1
MM v[8:11],a[148:151],a[52:55],v[8:11]
TR a[140:141],v229 offset:61440
TR a[142:143],v229 offset:63488
MM v[8:11],a[144:147],a[20:23],v[8:11]
s_waitcnt lgkmcnt(15)
EX v141,v141
MM v[8:11],a[160:163],a[28:31],v[8:11]
TR a[144:145],v229 offset:0
TR a[146:147],v229 offset:2048
MM v[8:11],a[164:167],a[60:63],v[8:11]
s_cmp_le_i32 s15, 17
s_cselect_b32 s60, 8, 0
s_cselect_b64 s[8:9], -1, 0
MM v[8:11],a[168:171],a[92:95],v[8:11]
s_or_b32 s58, s58, s60
TR a[148:149],v229 offset:4096
TR a[150:151],v229 offset:6144
MM v[8:11],a[172:175],a[124:127],v[8:11]
s_mov_b64 exec, s[8:9]
buffer_load_dwordx4 v[112:115], v248, s[20:23], s38 offen offset:0 sc1
s_mov_b64 exec, -1
MM v[0:3],a[172:175],a[120:123],v[0:3]
TR a[152:153],v229 offset:8192
TR a[154:155],v229 offset:10240
MM v[0:3],a[168:171],a[88:91],v[0:3]
s_mov_b64 exec, s[8:9]
buffer_load_dwordx4 v[116:119], v248, s[20:23], s38 offen offset:1024 sc1
s_mov_b64 exec, -1
MM v[0:3],a[164:167],a[56:59],v[0:3]
TR a[156:157],v229 offset:12288
TR a[158:159],v229 offset:14336
MM v[0:3],a[160:163],a[24:27],v[0:3]
s_waitcnt lgkmcnt(15)
EX v142,v142
MM v[0:3],a[176:179],a[200:203],v[0:3]
TR a[160:161],v229 offset:16384
TR a[162:163],v229 offset:18432
MM v[0:3],a[180:183],a[216:219],v[0:3]
s_cmp_le_i32 s14, 59
s_cselect_b32 s60, 4, 0
s_cselect_b64 s[8:9], -1, 0
MM v[0:3],a[184:187],a[232:235],v[0:3]
s_or_b32 s58, s58, s60
TR a[164:165],v229 offset:20480
TR a[166:167],v229 offset:22528
MM v[0:3],a[188:191],a[248:251],v[0:3]
s_mov_b64 exec, s[8:9]
buffer_load_dwordx4 v[88:91], v248, s[20:23], s37 offen offset:0 sc1
s_mov_b64 exec, -1
MM v[8:11],a[188:191],a[252:255],v[8:11]
TR a[168:169],v229 offset:24576
TR a[170:171],v229 offset:26624
MM v[8:11],a[184:187],a[236:239],v[8:11]
)ASM"
R"ASM(
s_mov_b64 exec, s[8:9]
buffer_load_dwordx4 v[92:95], v248, s[20:23], s37 offen offset:1024 sc1
s_mov_b64 exec, -1
MM v[8:11],a[180:183],a[220:223],v[8:11]
TR a[172:173],v229 offset:28672
TR a[174:175],v229 offset:30720
MM v[8:11],a[176:179],a[204:207],v[8:11]
s_waitcnt lgkmcnt(15)
EX v143,v143
MM v[12:15],a[128:131],v[172:175],v[12:15]
TR a[176:177],v229 offset:32768
TR a[178:179],v229 offset:34816
MM v[12:15],a[132:135],v[188:191],v[12:15]
s_cmp_le_i32 s12, 102
s_cselect_b32 s60, 1, 0
s_cselect_b64 s[8:9], -1, 0
MM v[12:15],a[136:139],v[204:207],v[12:15]
s_or_b32 s58, s58, s60
TR a[180:181],v229 offset:36864
TR a[182:183],v229 offset:38912
MM v[12:15],a[140:143],v[220:223],v[12:15]
s_mov_b64 exec, s[8:9]
buffer_load_dwordx4 v[120:123], v250, s[20:23], s39 offen offset:0 sc1
s_mov_b64 exec, -1
MM v[4:7],a[140:143],v[216:219],v[4:7]
TR a[184:185],v229 offset:40960
TR a[186:187],v229 offset:43008
MM v[4:7],a[136:139],v[200:203],v[4:7]
s_mov_b64 exec, s[8:9]
buffer_load_dwordx4 v[124:127], v250, s[20:23], s39 offen offset:256 sc1
s_mov_b64 exec, -1
MM v[4:7],a[132:135],v[184:187],v[4:7]
TR a[188:189],v229 offset:45056
TR a[190:191],v229 offset:47104
MM v[4:7],a[128:131],v[168:171],v[4:7]
s_waitcnt lgkmcnt(15)
v_add_f32 v136, 1.0, v136
v_add_f32 v137, 1.0, v137
MM v[4:7],a[144:147],a[16:19],v[4:7]
v_add_f32 v138, 1.0, v138
v_add_f32 v139, 1.0, v139
MM v[4:7],a[148:151],a[48:51],v[4:7]
v_add_f32 v140, 1.0, v140
v_add_f32 v141, 1.0, v141
MM v[4:7],a[152:155],a[80:83],v[4:7]
v_add_f32 v142, 1.0, v142
v_add_f32 v143, 1.0, v143
MM v[4:7],a[156:159],a[112:115],v[4:7]
v_rcp_f32 v136, v136
MM v[12:15],a[156:159],a[116:119],v[12:15]
v_rcp_f32 v137, v137
MM v[12:15],a[152:155],a[84:87],v[12:15]
v_rcp_f32 v138, v138
MM v[12:15],a[148:151],a[52:55],v[12:15]
v_rcp_f32 v139, v139
MM v[12:15],a[144:147],a[20:23],v[12:15]
s_waitcnt lgkmcnt(8)
v_rcp_f32 v140, v140
MM v[12:15],a[160:163],a[28:31],v[12:15]
s_cmp_le_i32 s13, 0
s_cselect_b32 s60, 2, 0
s_andn2_b32 s60, s60, s58
s_cselect_b64 s[8:9], -1, 0
MM v[12:15],a[164:167],a[60:63],v[12:15]
s_or_b32 s58, s58, s60
s_mov_b64 exec, s[8:9]
buffer_load_dwordx4 v[80:83], v248, s[20:23], s36 offen offset:0 sc1
s_mov_b64 exec, -1
MM v[12:15],a[168:171],a[92:95],v[12:15]
v_rcp_f32 v141, v141
MM v[12:15],a[172:175],a[124:127],v[12:15]
s_mov_b64 exec, s[8:9]
buffer_load_dwordx4 v[84:87], v248, s[20:23], s36 offen offset:1024 sc1
s_mov_b64 exec, -1
MM v[4:7],a[172:175],a[120:123],v[4:7]
v_rcp_f32 v142, v142
MM v[4:7],a[168:171],a[88:91],v[4:7]
v_rcp_f32 v143, v143
MM v[4:7],a[164:167],a[56:59],v[4:7]
v_mul_f32 v136, v160, v136
v_mul_f32 v137, v161, v137
MM v[4:7],a[160:163],a[24:27],v[4:7]
s_waitcnt lgkmcnt(0)
v_mul_f32 v138, v162, v138
v_mul_f32 v139, v163, v139
MM v[4:7],a[176:179],a[200:203],v[4:7]
v_mul_f32 v140, v164, v140
v_mul_f32 v141, v165, v141
MM v[4:7],a[180:183],a[216:219],v[4:7]
v_mul_f32 v142, v166, v142
v_mul_f32 v143, v167, v143
MM v[4:7],a[184:187],a[232:235],v[4:7]
CV v16,v136,v137
CV v17,v138,v139
MM v[4:7],a[188:191],a[248:251],v[4:7]
CV v18,v140,v141
CV v19,v142,v143
MM v[12:15],a[188:191],a[252:255],v[12:15]
v_lshlrev_b32 v160, 16, v16
v_and_b32 v161, 0xffff0000, v16
MM v[12:15],a[184:187],a[236:239],v[12:15]
v_lshlrev_b32 v162, 16, v17
v_and_b32 v163, 0xffff0000, v17
MM v[12:15],a[180:183],a[220:223],v[12:15]
v_lshlrev_b32 v164, 16, v18
v_and_b32 v165, 0xffff0000, v18
MM v[12:15],a[176:179],a[204:207],v[12:15]
v_lshlrev_b32 v166, 16, v19
v_and_b32 v167, 0xffff0000, v19
s_memrealtime s[54:55]
s_cmp_eq_u32 s58, 15
s_cbranch_scc1 LSK1_%=
s_memrealtime s[98:99]
s_andn2_b32 s91, s17, s58
s_mov_b32 s77, 0x7fffffff
s_waitcnt lgkmcnt(0)
s_sub_u32 s76, s98, s54
s_bitcmp0_b32 s91, 1
s_cbranch_scc1 LSM11_%=
s_sub_u32 s90, s13, s76
s_sub_u32 s90, s90, 12
s_cmp_gt_i32 s90, 0
s_cbranch_scc1 LSM11_%=
buffer_load_dwordx4 v[80:83], v248, s[20:23], s36 offen offset:0 sc1
buffer_load_dwordx4 v[84:87], v248, s[20:23], s36 offen offset:1024 sc1
s_bitset1_b32 s58, 1
LSM11_%=:
s_bitcmp0_b32 s91, 3
s_cbranch_scc1 LSM13_%=
s_sub_u32 s90, s15, s76
s_sub_u32 s90, s90, 63
s_cmp_gt_i32 s90, 0
s_cbranch_scc1 LSM13_%=
buffer_load_dwordx4 v[112:115], v248, s[20:23], s38 offen offset:0 sc1
buffer_load_dwordx4 v[116:119], v248, s[20:23], s38 offen offset:1024 sc1
s_bitset1_b32 s58, 3
LSM13_%=:
s_bitcmp0_b32 s91, 2
s_cbranch_scc1 LSM12_%=
s_sub_u32 s90, s14, s76
s_sub_u32 s90, s90, 97
s_cmp_gt_i32 s90, 0
s_cbranch_scc1 LSM12_%=
buffer_load_dwordx4 v[88:91], v248, s[20:23], s37 offen offset:0 sc1
buffer_load_dwordx4 v[92:95], v248, s[20:23], s37 offen offset:1024 sc1
s_bitset1_b32 s58, 2
LSM12_%=:
s_bitcmp0_b32 s91, 0
s_cbranch_scc1 LSM10_%=
s_sub_u32 s90, s12, s76
s_sub_u32 s90, s90, 131
s_cmp_gt_i32 s90, 0
s_cbranch_scc1 LSM10_%=
buffer_load_dwordx4 v[120:123], v250, s[20:23], s39 offen offset:0 sc1
buffer_load_dwordx4 v[124:127], v250, s[20:23], s39 offen offset:256 sc1
s_bitset1_b32 s58, 0
LSM10_%=:
LSK1_%=:
v_lshlrev_b32 v176, 16, v20
v_and_b32 v177, 0xffff0000, v20
v_lshlrev_b32 v178, 16, v21
v_and_b32 v179, 0xffff0000, v21
v_lshlrev_b32 v180, 16, v22
v_and_b32 v181, 0xffff0000, v22
v_lshlrev_b32 v182, 16, v23
v_and_b32 v183, 0xffff0000, v23
v_mul_f32 v136, 0xbfb8aa3b, v176
v_mul_f32 v137, 0xbfb8aa3b, v177
v_mul_f32 v138, 0xbfb8aa3b, v178
v_mul_f32 v139, 0xbfb8aa3b, v179
v_mul_f32 v140, 0xbfb8aa3b, v180
v_mul_f32 v141, 0xbfb8aa3b, v181
v_mul_f32 v142, 0xbfb8aa3b, v182
v_mul_f32 v143, 0xbfb8aa3b, v183
EX v136,v136
EX v137,v137
EX v138,v138
EX v139,v139
EX v140,v140
EX v141,v141
EX v142,v142
EX v143,v143
v_add_f32 v136, 1.0, v136
v_add_f32 v137, 1.0, v137
v_add_f32 v138, 1.0, v138
v_add_f32 v139, 1.0, v139
v_add_f32 v140, 1.0, v140
v_add_f32 v141, 1.0, v141
v_add_f32 v142, 1.0, v142
v_add_f32 v143, 1.0, v143
v_rcp_f32 v136, v136
v_rcp_f32 v137, v137
v_rcp_f32 v138, v138
v_rcp_f32 v139, v139
s_waitcnt vmcnt(12)
v_cmp_eq_u32 s[8:9], s69, v149
v_subrev_u32 v226, s56, v148
s_nop 0
v_cndmask_b32 v226, 4.0, v226, s[8:9]
s_nop 0
v_readlane_b32 s13, v226, 16
v_readlane_b32 s14, v226, 32
v_readlane_b32 s15, v226, 48
s_max_i32 s12, s13, s14
s_max_i32 s12, s12, s15
s_max_i32 s12, s12, 0
s_cmp_eq_u32 s58, 15
s_cbranch_scc1 LSK2_%=
s_memrealtime s[98:99]
s_andn2_b32 s91, s17, s58
s_mov_b32 s77, 0x7fffffff
s_waitcnt lgkmcnt(0)
s_sub_u32 s76, s98, s54
s_bitcmp0_b32 s91, 1
s_cbranch_scc1 LSM21_%=
s_sub_u32 s90, s13, s76
s_sub_u32 s90, s90, 12
s_cmp_gt_i32 s90, 0
s_cbranch_scc1 LSM21_%=
buffer_load_dwordx4 v[80:83], v248, s[20:23], s36 offen offset:0 sc1
buffer_load_dwordx4 v[84:87], v248, s[20:23], s36 offen offset:1024 sc1
s_bitset1_b32 s58, 1
LSM21_%=:
s_bitcmp0_b32 s91, 3
s_cbranch_scc1 LSM23_%=
s_sub_u32 s90, s15, s76
s_sub_u32 s90, s90, 63
s_cmp_gt_i32 s90, 0
s_cbranch_scc1 LSM23_%=
buffer_load_dwordx4 v[112:115], v248, s[20:23], s38 offen offset:0 sc1
buffer_load_dwordx4 v[116:119], v248, s[20:23], s38 offen offset:1024 sc1
s_bitset1_b32 s58, 3
LSM23_%=:
s_bitcmp0_b32 s91, 2
s_cbranch_scc1 LSM22_%=
s_sub_u32 s90, s14, s76
s_sub_u32 s90, s90, 97
s_cmp_gt_i32 s90, 0
s_cbranch_scc1 LSM22_%=
buffer_load_dwordx4 v[88:91], v248, s[20:23], s37 offen offset:0 sc1
buffer_load_dwordx4 v[92:95], v248, s[20:23], s37 offen offset:1024 sc1
s_bitset1_b32 s58, 2
LSM22_%=:
s_bitcmp0_b32 s91, 0
s_cbranch_scc1 LSM20_%=
s_sub_u32 s90, s12, s76
s_sub_u32 s90, s90, 131
s_cmp_gt_i32 s90, 0
s_cbranch_scc1 LSM20_%=
buffer_load_dwordx4 v[120:123], v250, s[20:23], s39 offen offset:0 sc1
buffer_load_dwordx4 v[124:127], v250, s[20:23], s39 offen offset:256 sc1
s_bitset1_b32 s58, 0
LSM20_%=:
LSK2_%=:
v_rcp_f32 v140, v140
v_rcp_f32 v141, v141
v_rcp_f32 v142, v142
v_rcp_f32 v143, v143
v_mul_f32 v136, v176, v136
v_mul_f32 v137, v177, v137
v_mul_f32 v138, v178, v138
v_mul_f32 v139, v179, v139
v_mul_f32 v140, v180, v140
v_mul_f32 v141, v181, v141
v_mul_f32 v142, v182, v142
v_mul_f32 v143, v183, v143
CV v20,v136,v137
CV v21,v138,v139
CV v22,v140,v141
CV v23,v142,v143
v_lshlrev_b32 v176, 16, v20
v_and_b32 v177, 0xffff0000, v20
v_lshlrev_b32 v178, 16, v21
v_and_b32 v179, 0xffff0000, v21
v_lshlrev_b32 v180, 16, v22
v_and_b32 v181, 0xffff0000, v22
v_lshlrev_b32 v182, 16, v23
v_and_b32 v183, 0xffff0000, v23
v_lshlrev_b32 v192, 16, v24
v_and_b32 v193, 0xffff0000, v24
v_lshlrev_b32 v194, 16, v25
v_and_b32 v195, 0xffff0000, v25
v_lshlrev_b32 v196, 16, v26
v_and_b32 v197, 0xffff0000, v26
v_lshlrev_b32 v198, 16, v27
v_and_b32 v199, 0xffff0000, v27
v_lshlrev_b32 v208, 16, v28
v_and_b32 v209, 0xffff0000, v28
v_lshlrev_b32 v210, 16, v29
v_and_b32 v211, 0xffff0000, v29
v_lshlrev_b32 v212, 16, v30
v_and_b32 v213, 0xffff0000, v30
v_lshlrev_b32 v214, 16, v31
v_and_b32 v215, 0xffff0000, v31
s_mov_b32 s73, 30000
s_cmp_lg_u32 s58, 15
s_cbranch_scc1 LSI_%=
s_waitcnt vmcnt(0)
v_max3_u32 v236, v123, v127, v83
v_max3_u32 v237, v87, v91, v95
v_max3_u32 v236, v236, v115, v119
v_max_u32 v236, v236, v237
v_cmp_eq_u32 vcc, -1, v236
s_cbranch_vccnz LSI_%=
LSF_%=:
s_waitcnt lgkmcnt(0)
MM v[152:155],v[48:51],v[120:123],0
MM v[152:155],v[52:55],v[124:127],v[152:155]
MM v[0:3],v[58:61],v[88:91],v[0:3]
v_lshlrev_b32 v128, 16, v150
v_and_b32 v129, 0xffff0000, v150
MM v[0:3],v[58:61],v[112:115],v[0:3]
v_lshlrev_b32 v130, 16, v151
v_and_b32 v131, 0xffff0000, v151
MM v[0:3],v[58:61],v[80:83],v[0:3]
v_lshlrev_b32 v132, 16, v62
v_and_b32 v133, 0xffff0000, v62
MM v[4:7],v[56:59],v[88:91],v[4:7]
v_lshlrev_b32 v134, 16, v63
v_and_b32 v135, 0xffff0000, v63
MM v[4:7],v[56:59],v[112:115],v[4:7]
CV v236,v152,v153
CV v237,v154,v155
MM v[4:7],v[56:59],v[80:83],v[4:7]
ds_bpermute_b32 v236, v232, v236
ds_bpermute_b32 v237, v232, v237
MM v[8:11],v[58:61],v[92:95],v[8:11]
MM v[8:11],v[58:61],v[116:119],v[8:11]
MM v[8:11],v[58:61],v[84:87],v[8:11]
s_add_u32 s80, s18, 20480
MM v[12:15],v[56:59],v[92:95],v[12:15]
MM v[12:15],v[56:59],v[116:119],v[12:15]
CV v32,v0,v1
CV v33,v2,v3
MM v[12:15],v[56:59],v[84:87],v[12:15]
CV v34,v4,v5
CV v35,v6,v7
ds_write2_b64 v224, v[32:33], v[34:35] offset0:0 offset1:4
RD v[40:43],v225 offset:0
CV v36,v8,v9
CV v37,v10,v11
s_nop 1
CV v38,v12,v13
CV v39,v14,v15
ds_write2_b64 v224, v[36:37], v[38:39] offset0:160 offset1:164
RD v[44:47],v225 offset:1280
s_waitcnt lgkmcnt(4)
v_lshlrev_b32 v152, 16, v236
v_and_b32 v153, 0xffff0000, v236
v_lshlrev_b32 v154, 16, v237
v_and_b32 v155, 0xffff0000, v237
v_pk_mul_f32 v[152:153], v[152:153], v[128:129]
v_pk_mul_f32 v[154:155], v[154:155], v[130:131]
CV v236,v152,v153
CV v237,v154,v155
v_lshlrev_b32 v152, 16, v236
v_and_b32 v153, 0xffff0000, v236
v_lshlrev_b32 v154, 16, v237
v_and_b32 v155, 0xffff0000, v237
v_pk_add_f32 v[152:153], v[152:153], v[132:133]
v_pk_add_f32 v[154:155], v[154:155], v[134:135]
CV v238,v152,v153
CV v239,v154,v155
buffer_store_dwordx2 v[238:239], v254, s[40:43], s63 offen offset:0 nt
s_nop 0
s_waitcnt lgkmcnt(2)
v_lshlrev_b32 v0, 16, v40
v_and_b32 v1, 0xffff0000, v40
v_lshlrev_b32 v2, 16, v41
v_and_b32 v3, 0xffff0000, v41
v_lshlrev_b32 v4, 16, v42
v_and_b32 v5, 0xffff0000, v42
v_lshlrev_b32 v6, 16, v43
v_and_b32 v7, 0xffff0000, v43
v_pk_mul_f32 v[0:1], v[0:1], v[160:161]
v_pk_mul_f32 v[2:3], v[2:3], v[162:163]
v_pk_mul_f32 v[4:5], v[4:5], v[164:165]
v_pk_mul_f32 v[6:7], v[6:7], v[166:167]
CV v40,v0,v1
CV v41,v2,v3
CV v42,v4,v5
CV v43,v6,v7
v_lshlrev_b32 v0, 16, v40
v_and_b32 v1, 0xffff0000, v40
v_lshlrev_b32 v2, 16, v41
v_and_b32 v3, 0xffff0000, v41
v_lshlrev_b32 v4, 16, v42
v_and_b32 v5, 0xffff0000, v42
v_lshlrev_b32 v6, 16, v43
v_and_b32 v7, 0xffff0000, v43
v_pk_add_f32 v[0:1], v[0:1], v[192:193]
v_pk_add_f32 v[2:3], v[2:3], v[194:195]
v_pk_add_f32 v[4:5], v[4:5], v[196:197]
v_pk_add_f32 v[6:7], v[6:7], v[198:199]
s_nop 0
LPG30_%=:
s_cbranch_execz LTR1_%=
CV v40,v0,v1
CV v41,v2,v3
CV v42,v4,v5
CV v43,v6,v7
buffer_store_dwordx4 v[40:43], v251, s[40:43], s18 offen offset:0 nt
s_waitcnt lgkmcnt(0)
v_lshlrev_b32 v8, 16, v44
v_and_b32 v9, 0xffff0000, v44
v_lshlrev_b32 v10, 16, v45
v_and_b32 v11, 0xffff0000, v45
v_lshlrev_b32 v12, 16, v46
v_and_b32 v13, 0xffff0000, v46
v_lshlrev_b32 v14, 16, v47
v_and_b32 v15, 0xffff0000, v47
v_pk_mul_f32 v[8:9], v[8:9], v[176:177]
v_pk_mul_f32 v[10:11], v[10:11], v[178:179]
)ASM"
R"ASM(
v_pk_mul_f32 v[12:13], v[12:13], v[180:181]
v_pk_mul_f32 v[14:15], v[14:15], v[182:183]
CV v44,v8,v9
CV v45,v10,v11
CV v46,v12,v13
CV v47,v14,v15
v_lshlrev_b32 v8, 16, v44
v_and_b32 v9, 0xffff0000, v44
v_lshlrev_b32 v10, 16, v45
v_and_b32 v11, 0xffff0000, v45
v_lshlrev_b32 v12, 16, v46
v_and_b32 v13, 0xffff0000, v46
v_lshlrev_b32 v14, 16, v47
v_and_b32 v15, 0xffff0000, v47
v_pk_add_f32 v[8:9], v[8:9], v[208:209]
v_pk_add_f32 v[10:11], v[10:11], v[210:211]
v_pk_add_f32 v[12:13], v[12:13], v[212:213]
v_pk_add_f32 v[14:15], v[14:15], v[214:215]
CV v44,v8,v9
CV v45,v10,v11
CV v46,v12,v13
CV v47,v14,v15
buffer_store_dwordx4 v[44:47], v251, s[40:43], s80 offen offset:0 nt
s_waitcnt vmcnt(0)
s_endpgm
LSI_%=:
s_memrealtime s[98:99]
s_andn2_b32 s91, s17, s58
s_mov_b32 s77, 0x7fffffff
s_waitcnt lgkmcnt(0)
s_sub_u32 s76, s98, s54
s_bitcmp0_b32 s91, 1
s_cbranch_scc1 LSM1_%=
s_sub_u32 s90, s13, s76
s_sub_u32 s90, s90, 12
s_cmp_gt_i32 s90, 0
s_cbranch_scc1 LSN1_%=
buffer_load_dwordx4 v[80:83], v248, s[20:23], s36 offen offset:0 sc1
buffer_load_dwordx4 v[84:87], v248, s[20:23], s36 offen offset:1024 sc1
s_bitset1_b32 s58, 1
s_branch LSM1_%=
LSN1_%=:
s_min_i32 s77, s77, s90
LSM1_%=:
s_bitcmp0_b32 s91, 3
s_cbranch_scc1 LSM3_%=
s_sub_u32 s90, s15, s76
s_sub_u32 s90, s90, 63
s_cmp_gt_i32 s90, 0
s_cbranch_scc1 LSN3_%=
buffer_load_dwordx4 v[112:115], v248, s[20:23], s38 offen offset:0 sc1
buffer_load_dwordx4 v[116:119], v248, s[20:23], s38 offen offset:1024 sc1
s_bitset1_b32 s58, 3
s_branch LSM3_%=
LSN3_%=:
s_min_i32 s77, s77, s90
LSM3_%=:
s_bitcmp0_b32 s91, 2
s_cbranch_scc1 LSM2_%=
s_sub_u32 s90, s14, s76
s_sub_u32 s90, s90, 97
s_cmp_gt_i32 s90, 0
s_cbranch_scc1 LSN2_%=
buffer_load_dwordx4 v[88:91], v248, s[20:23], s37 offen offset:0 sc1
buffer_load_dwordx4 v[92:95], v248, s[20:23], s37 offen offset:1024 sc1
s_bitset1_b32 s58, 2
s_branch LSM2_%=
LSN2_%=:
s_min_i32 s77, s77, s90
LSM2_%=:
s_bitcmp0_b32 s91, 0
s_cbranch_scc1 LSM0_%=
s_sub_u32 s90, s12, s76
s_sub_u32 s90, s90, 131
s_cmp_gt_i32 s90, 0
s_cbranch_scc1 LSN0_%=
buffer_load_dwordx4 v[120:123], v250, s[20:23], s39 offen offset:0 sc1
buffer_load_dwordx4 v[124:127], v250, s[20:23], s39 offen offset:256 sc1
s_bitset1_b32 s58, 0
s_branch LSM0_%=
LSN0_%=:
s_min_i32 s77, s77, s90
LSM0_%=:
s_cmp_eq_u32 s12, 4.0
s_cselect_b32 s90, 30, 45
s_cmp_lt_i32 s77, s90
s_cbranch_scc1 LSI_%=
buffer_load_dwordx2 v[148:149], v253, s[20:23], s68 offen offset:8 sc1
s_waitcnt vmcnt(1)
s_bitcmp0_b32 s58, 1
s_cbranch_scc1 LSB1_%=
v_max_u32 v237, v83, v87
v_cmp_eq_u32 vcc, -1, v237
s_cbranch_vccnz LSB1_%=
s_bitset0_b32 s17, 1
LSB1_%=:
s_bitcmp0_b32 s58, 3
s_cbranch_scc1 LSB3_%=
v_max_u32 v239, v115, v119
v_cmp_eq_u32 vcc, -1, v239
s_cbranch_vccnz LSB3_%=
s_bitset0_b32 s17, 3
LSB3_%=:
s_bitcmp0_b32 s58, 2
s_cbranch_scc1 LSB2_%=
v_max_u32 v238, v91, v95
v_cmp_eq_u32 vcc, -1, v238
s_cbranch_vccnz LSB2_%=
s_bitset0_b32 s17, 2
LSB2_%=:
s_bitcmp0_b32 s58, 0
s_cbranch_scc1 LSB0_%=
v_max_u32 v236, v123, v127
v_cmp_eq_u32 vcc, -1, v236
s_cbranch_vccnz LSB0_%=
s_bitset0_b32 s17, 0
LSB0_%=:
s_cmp_eq_u32 s17, 0
s_cbranch_scc1 LSF_%=
s_mov_b32 s58, 0
s_waitcnt vmcnt(0)
v_cmp_eq_u32 s[8:9], s69, v149
v_subrev_u32 v226, s56, v148
s_nop 0
v_cndmask_b32 v226, 4.0, v226, s[8:9]
s_nop 0
v_readlane_b32 s13, v226, 16
v_readlane_b32 s14, v226, 32
v_readlane_b32 s15, v226, 48
s_max_i32 s12, s13, s14
s_max_i32 s12, s12, s15
s_max_i32 s12, s12, 0
s_sub_u32 s73, s73, 1
s_cbranch_scc0 LSI_%=
LSE_%=:
s_waitcnt vmcnt(0)
s_endpgm
LRG1o_%=:
s_mov_b32 s70, 30000
LRG1ol_%=:
s_sleep 2
buffer_load_dwordx4 v[200:203], v208, s[20:23], s62 offen offset:0 sc1
buffer_load_dwordx4 v[204:207], v208, s[20:23], s93 offen offset:0 sc1
s_waitcnt vmcnt(0)
v_xor_b32 v201, s69, v201
v_xor_b32 v203, s69, v203
v_xor_b32 v205, s69, v205
v_xor_b32 v207, s69, v207
v_or3_b32 v209, v201, v203, v205
v_or_b32 v209, v209, v207
v_cmp_ne_u32 vcc, 0, v209
s_cbranch_vccz LRG1ob_%=
s_sub_u32 s70, s70, 1
s_cbranch_scc0 LRG1ol_%=
s_branch LRG1ob_%=
LRFR_%=:
s_mov_b32 s91, 30000
LRFRl_%=:
s_sleep 2
buffer_load_dwordx4 v[56:59], v248, s[20:23], s90 offen offset:0 sc1
s_waitcnt vmcnt(0)
v_xor_b32 v56, s69, v56
v_xor_b32 v57, s69, v57
v_xor_b32 v58, s69, v58
v_xor_b32 v59, s69, v59
v_or3_b32 v252, v56, v57, v58
v_or_b32 v252, v252, v59
v_cmp_ne_u32 vcc, 0, v252
s_cbranch_vccz LRFRb_%=
s_sub_u32 s91, s91, 1
s_cbranch_scc0 LRFRl_%=
s_branch LRFRb_%=
LRRK_%=:
s_mov_b32 s91, 30000
LRRKl_%=:
s_sleep 2
buffer_load_dwordx2 v[32:33], v34, s[20:23], s90 offen offset:0 sc1
s_waitcnt vmcnt(0)
v_cmp_ne_u32 vcc, s69, v33
s_cbranch_vccz LRRKb_%=
s_sub_u32 s91, s91, 1
s_cbranch_scc0 LRRKl_%=
s_branch LRRKb_%=
LRK_%=:
s_cmp_ge_u32 s60, 8
s_cbranch_scc1 LRK0_%=
v_mov_b32 v253, 0
v_cmp_lt_u32 vcc, s52, v96
v_cmp_lt_u32 s[98:99], s52, v97
s_nop 0
v_addc_co_u32 v253, vcc, 0, v253, vcc
v_addc_co_u32 v253, s[98:99], 0, v253, s[98:99]
v_cmp_lt_u32 vcc, s52, v98
v_cmp_lt_u32 s[98:99], s52, v99
s_nop 0
v_addc_co_u32 v253, vcc, 0, v253, vcc
v_addc_co_u32 v253, s[98:99], 0, v253, s[98:99]
v_cmp_lt_u32 vcc, s52, v100
v_cmp_lt_u32 s[98:99], s52, v101
s_nop 0
v_addc_co_u32 v253, vcc, 0, v253, vcc
v_addc_co_u32 v253, s[98:99], 0, v253, s[98:99]
v_cmp_lt_u32 vcc, s52, v102
v_cmp_lt_u32 s[98:99], s52, v103
s_nop 0
v_addc_co_u32 v253, vcc, 0, v253, vcc
v_addc_co_u32 v253, s[98:99], 0, v253, s[98:99]
v_cmp_lt_u32 vcc, s52, v104
v_cmp_lt_u32 s[98:99], s52, v105
s_nop 0
v_addc_co_u32 v253, vcc, 0, v253, vcc
v_addc_co_u32 v253, s[98:99], 0, v253, s[98:99]
v_cmp_lt_u32 vcc, s52, v106
v_cmp_lt_u32 s[98:99], s52, v107
s_nop 0
v_addc_co_u32 v253, vcc, 0, v253, vcc
v_addc_co_u32 v253, s[98:99], 0, v253, s[98:99]
v_cmp_lt_u32 vcc, s52, v108
v_cmp_lt_u32 s[98:99], s52, v109
s_nop 0
v_addc_co_u32 v253, vcc, 0, v253, vcc
v_addc_co_u32 v253, s[98:99], 0, v253, s[98:99]
v_cmp_lt_u32 vcc, s52, v110
v_cmp_lt_u32 s[98:99], s52, v111
s_nop 0
v_addc_co_u32 v253, vcc, 0, v253, vcc
v_addc_co_u32 v253, s[98:99], 0, v253, s[98:99]
v_cmp_lt_u32 vcc, s52, v112
v_cmp_lt_u32 s[98:99], s52, v113
s_nop 0
v_addc_co_u32 v253, vcc, 0, v253, vcc
v_addc_co_u32 v253, s[98:99], 0, v253, s[98:99]
v_cmp_lt_u32 vcc, s52, v114
v_cmp_lt_u32 s[98:99], s52, v115
s_nop 0
v_addc_co_u32 v253, vcc, 0, v253, vcc
v_addc_co_u32 v253, s[98:99], 0, v253, s[98:99]
v_cmp_lt_u32 vcc, s52, v116
v_cmp_lt_u32 s[98:99], s52, v117
s_nop 0
v_addc_co_u32 v253, vcc, 0, v253, vcc
v_addc_co_u32 v253, s[98:99], 0, v253, s[98:99]
v_cmp_lt_u32 vcc, s52, v118
v_cmp_lt_u32 s[98:99], s52, v119
s_nop 0
v_addc_co_u32 v253, vcc, 0, v253, vcc
v_addc_co_u32 v253, s[98:99], 0, v253, s[98:99]
v_cmp_lt_u32 vcc, s52, v120
v_cmp_lt_u32 s[98:99], s52, v121
s_nop 0
v_addc_co_u32 v253, vcc, 0, v253, vcc
v_addc_co_u32 v253, s[98:99], 0, v253, s[98:99]
v_cmp_lt_u32 vcc, s52, v122
v_cmp_lt_u32 s[98:99], s52, v123
s_nop 0
v_addc_co_u32 v253, vcc, 0, v253, vcc
v_addc_co_u32 v253, s[98:99], 0, v253, s[98:99]
v_cmp_lt_u32 vcc, s52, v124
v_cmp_lt_u32 s[98:99], s52, v125
s_nop 0
v_addc_co_u32 v253, vcc, 0, v253, vcc
v_addc_co_u32 v253, s[98:99], 0, v253, s[98:99]
v_cmp_lt_u32 vcc, s52, v126
v_cmp_lt_u32 s[98:99], s52, v127
s_nop 0
v_addc_co_u32 v253, vcc, 0, v253, vcc
v_addc_co_u32 v253, s[98:99], 0, v253, s[98:99]
s_nop 1
v_add_u32_dpp v253, v253, v253 row_ror:8 row_mask:0xf bank_mask:0xf
s_nop 1
v_add_u32_dpp v253, v253, v253 row_ror:4 row_mask:0xf bank_mask:0xf
s_nop 1
v_add_u32_dpp v253, v253, v253 row_ror:2 row_mask:0xf bank_mask:0xf
s_nop 1
v_add_u32_dpp v253, v253, v253 row_ror:1 row_mask:0xf bank_mask:0xf
v_mov_b32 v250, v253
s_nop 1
v_permlane16_swap_b32 v253, v250
v_add_u32 v253, v253, v250
v_mov_b32 v250, v253
s_nop 1
v_permlane32_swap_b32 v253, v250
v_add_u32 v253, v253, v250
v_cmp_gt_u32 vcc, 8, v253
v_mov_b32 v250, s52
v_mov_b32 v251, s69
v_lshlrev_b32 v253, 3, v253
s_mov_b32 s76, 94208
s_cbranch_vccz LRK0_%=
s_mov_b64 exec, 1
buffer_store_dwordx2 v[250:251], v253, s[20:23], s76 offen offset:0 sc1
s_mov_b64 exec, -1
s_nop 1
LRK0_%=:
s_cmp_ge_u32 s62, 8
s_cbranch_scc1 LRK1_%=
v_mov_b32 v253, 0
v_cmp_lt_u32 vcc, s53, v96
v_cmp_lt_u32 s[98:99], s53, v97
s_nop 0
v_addc_co_u32 v253, vcc, 0, v253, vcc
v_addc_co_u32 v253, s[98:99], 0, v253, s[98:99]
v_cmp_lt_u32 vcc, s53, v98
v_cmp_lt_u32 s[98:99], s53, v99
s_nop 0
v_addc_co_u32 v253, vcc, 0, v253, vcc
v_addc_co_u32 v253, s[98:99], 0, v253, s[98:99]
v_cmp_lt_u32 vcc, s53, v100
v_cmp_lt_u32 s[98:99], s53, v101
s_nop 0
v_addc_co_u32 v253, vcc, 0, v253, vcc
v_addc_co_u32 v253, s[98:99], 0, v253, s[98:99]
v_cmp_lt_u32 vcc, s53, v102
v_cmp_lt_u32 s[98:99], s53, v103
s_nop 0
v_addc_co_u32 v253, vcc, 0, v253, vcc
v_addc_co_u32 v253, s[98:99], 0, v253, s[98:99]
v_cmp_lt_u32 vcc, s53, v104
v_cmp_lt_u32 s[98:99], s53, v105
s_nop 0
v_addc_co_u32 v253, vcc, 0, v253, vcc
v_addc_co_u32 v253, s[98:99], 0, v253, s[98:99]
v_cmp_lt_u32 vcc, s53, v106
v_cmp_lt_u32 s[98:99], s53, v107
s_nop 0
v_addc_co_u32 v253, vcc, 0, v253, vcc
v_addc_co_u32 v253, s[98:99], 0, v253, s[98:99]
v_cmp_lt_u32 vcc, s53, v108
v_cmp_lt_u32 s[98:99], s53, v109
s_nop 0
v_addc_co_u32 v253, vcc, 0, v253, vcc
v_addc_co_u32 v253, s[98:99], 0, v253, s[98:99]
v_cmp_lt_u32 vcc, s53, v110
v_cmp_lt_u32 s[98:99], s53, v111
s_nop 0
v_addc_co_u32 v253, vcc, 0, v253, vcc
v_addc_co_u32 v253, s[98:99], 0, v253, s[98:99]
v_cmp_lt_u32 vcc, s53, v112
v_cmp_lt_u32 s[98:99], s53, v113
s_nop 0
v_addc_co_u32 v253, vcc, 0, v253, vcc
v_addc_co_u32 v253, s[98:99], 0, v253, s[98:99]
v_cmp_lt_u32 vcc, s53, v114
v_cmp_lt_u32 s[98:99], s53, v115
s_nop 0
v_addc_co_u32 v253, vcc, 0, v253, vcc
v_addc_co_u32 v253, s[98:99], 0, v253, s[98:99]
v_cmp_lt_u32 vcc, s53, v116
v_cmp_lt_u32 s[98:99], s53, v117
s_nop 0
v_addc_co_u32 v253, vcc, 0, v253, vcc
v_addc_co_u32 v253, s[98:99], 0, v253, s[98:99]
v_cmp_lt_u32 vcc, s53, v118
v_cmp_lt_u32 s[98:99], s53, v119
s_nop 0
v_addc_co_u32 v253, vcc, 0, v253, vcc
v_addc_co_u32 v253, s[98:99], 0, v253, s[98:99]
v_cmp_lt_u32 vcc, s53, v120
v_cmp_lt_u32 s[98:99], s53, v121
s_nop 0
v_addc_co_u32 v253, vcc, 0, v253, vcc
v_addc_co_u32 v253, s[98:99], 0, v253, s[98:99]
v_cmp_lt_u32 vcc, s53, v122
v_cmp_lt_u32 s[98:99], s53, v123
s_nop 0
v_addc_co_u32 v253, vcc, 0, v253, vcc
v_addc_co_u32 v253, s[98:99], 0, v253, s[98:99]
v_cmp_lt_u32 vcc, s53, v124
v_cmp_lt_u32 s[98:99], s53, v125
s_nop 0
v_addc_co_u32 v253, vcc, 0, v253, vcc
v_addc_co_u32 v253, s[98:99], 0, v253, s[98:99]
v_cmp_lt_u32 vcc, s53, v126
v_cmp_lt_u32 s[98:99], s53, v127
s_nop 0
v_addc_co_u32 v253, vcc, 0, v253, vcc
v_addc_co_u32 v253, s[98:99], 0, v253, s[98:99]
s_nop 1
v_add_u32_dpp v253, v253, v253 row_ror:8 row_mask:0xf bank_mask:0xf
s_nop 1
v_add_u32_dpp v253, v253, v253 row_ror:4 row_mask:0xf bank_mask:0xf
s_nop 1
v_add_u32_dpp v253, v253, v253 row_ror:2 row_mask:0xf bank_mask:0xf
s_nop 1
v_add_u32_dpp v253, v253, v253 row_ror:1 row_mask:0xf bank_mask:0xf
v_mov_b32 v250, v253
s_nop 1
v_permlane16_swap_b32 v253, v250
v_add_u32 v253, v253, v250
v_mov_b32 v250, v253
s_nop 1
v_permlane32_swap_b32 v253, v250
v_add_u32 v253, v253, v250
v_cmp_gt_u32 vcc, 8, v253
v_mov_b32 v250, s53
v_mov_b32 v251, s69
v_lshlrev_b32 v253, 3, v253
s_mov_b32 s76, 94208
s_cbranch_vccz LRK1_%=
s_mov_b64 exec, 1
buffer_store_dwordx2 v[250:251], v253, s[20:23], s76 offen offset:0 sc1
s_mov_b64 exec, -1
s_nop 1
LRK1_%=:
s_cmp_ge_u32 s56, 8
s_cbranch_scc1 LRK2_%=
v_mov_b32 v253, 0
v_cmp_lt_u32 vcc, s54, v96
v_cmp_lt_u32 s[98:99], s54, v97
s_nop 0
v_addc_co_u32 v253, vcc, 0, v253, vcc
v_addc_co_u32 v253, s[98:99], 0, v253, s[98:99]
v_cmp_lt_u32 vcc, s54, v98
v_cmp_lt_u32 s[98:99], s54, v99
s_nop 0
v_addc_co_u32 v253, vcc, 0, v253, vcc
v_addc_co_u32 v253, s[98:99], 0, v253, s[98:99]
v_cmp_lt_u32 vcc, s54, v100
v_cmp_lt_u32 s[98:99], s54, v101
s_nop 0
v_addc_co_u32 v253, vcc, 0, v253, vcc
v_addc_co_u32 v253, s[98:99], 0, v253, s[98:99]
v_cmp_lt_u32 vcc, s54, v102
v_cmp_lt_u32 s[98:99], s54, v103
s_nop 0
v_addc_co_u32 v253, vcc, 0, v253, vcc
v_addc_co_u32 v253, s[98:99], 0, v253, s[98:99]
v_cmp_lt_u32 vcc, s54, v104
v_cmp_lt_u32 s[98:99], s54, v105
s_nop 0
v_addc_co_u32 v253, vcc, 0, v253, vcc
v_addc_co_u32 v253, s[98:99], 0, v253, s[98:99]
)ASM"
R"ASM(
v_cmp_lt_u32 vcc, s54, v106
v_cmp_lt_u32 s[98:99], s54, v107
s_nop 0
v_addc_co_u32 v253, vcc, 0, v253, vcc
v_addc_co_u32 v253, s[98:99], 0, v253, s[98:99]
v_cmp_lt_u32 vcc, s54, v108
v_cmp_lt_u32 s[98:99], s54, v109
s_nop 0
v_addc_co_u32 v253, vcc, 0, v253, vcc
v_addc_co_u32 v253, s[98:99], 0, v253, s[98:99]
v_cmp_lt_u32 vcc, s54, v110
v_cmp_lt_u32 s[98:99], s54, v111
s_nop 0
v_addc_co_u32 v253, vcc, 0, v253, vcc
v_addc_co_u32 v253, s[98:99], 0, v253, s[98:99]
v_cmp_lt_u32 vcc, s54, v112
v_cmp_lt_u32 s[98:99], s54, v113
s_nop 0
v_addc_co_u32 v253, vcc, 0, v253, vcc
v_addc_co_u32 v253, s[98:99], 0, v253, s[98:99]
v_cmp_lt_u32 vcc, s54, v114
v_cmp_lt_u32 s[98:99], s54, v115
s_nop 0
v_addc_co_u32 v253, vcc, 0, v253, vcc
v_addc_co_u32 v253, s[98:99], 0, v253, s[98:99]
v_cmp_lt_u32 vcc, s54, v116
v_cmp_lt_u32 s[98:99], s54, v117
s_nop 0
v_addc_co_u32 v253, vcc, 0, v253, vcc
v_addc_co_u32 v253, s[98:99], 0, v253, s[98:99]
v_cmp_lt_u32 vcc, s54, v118
v_cmp_lt_u32 s[98:99], s54, v119
s_nop 0
v_addc_co_u32 v253, vcc, 0, v253, vcc
v_addc_co_u32 v253, s[98:99], 0, v253, s[98:99]
v_cmp_lt_u32 vcc, s54, v120
v_cmp_lt_u32 s[98:99], s54, v121
s_nop 0
v_addc_co_u32 v253, vcc, 0, v253, vcc
v_addc_co_u32 v253, s[98:99], 0, v253, s[98:99]
v_cmp_lt_u32 vcc, s54, v122
v_cmp_lt_u32 s[98:99], s54, v123
s_nop 0
v_addc_co_u32 v253, vcc, 0, v253, vcc
v_addc_co_u32 v253, s[98:99], 0, v253, s[98:99]
v_cmp_lt_u32 vcc, s54, v124
v_cmp_lt_u32 s[98:99], s54, v125
s_nop 0
v_addc_co_u32 v253, vcc, 0, v253, vcc
v_addc_co_u32 v253, s[98:99], 0, v253, s[98:99]
v_cmp_lt_u32 vcc, s54, v126
v_cmp_lt_u32 s[98:99], s54, v127
s_nop 0
v_addc_co_u32 v253, vcc, 0, v253, vcc
v_addc_co_u32 v253, s[98:99], 0, v253, s[98:99]
s_nop 1
v_add_u32_dpp v253, v253, v253 row_ror:8 row_mask:0xf bank_mask:0xf
s_nop 1
v_add_u32_dpp v253, v253, v253 row_ror:4 row_mask:0xf bank_mask:0xf
s_nop 1
v_add_u32_dpp v253, v253, v253 row_ror:2 row_mask:0xf bank_mask:0xf
s_nop 1
v_add_u32_dpp v253, v253, v253 row_ror:1 row_mask:0xf bank_mask:0xf
v_mov_b32 v250, v253
s_nop 1
v_permlane16_swap_b32 v253, v250
v_add_u32 v253, v253, v250
v_mov_b32 v250, v253
s_nop 1
v_permlane32_swap_b32 v253, v250
v_add_u32 v253, v253, v250
v_cmp_gt_u32 vcc, 8, v253
v_mov_b32 v250, s54
v_mov_b32 v251, s69
v_lshlrev_b32 v253, 3, v253
s_mov_b32 s76, 94208
s_cbranch_vccz LRK2_%=
s_mov_b64 exec, 1
buffer_store_dwordx2 v[250:251], v253, s[20:23], s76 offen offset:0 sc1
s_mov_b64 exec, -1
s_nop 1
LRK2_%=:
s_cmp_ge_u32 s58, 8
s_cbranch_scc1 LRK3_%=
v_mov_b32 v253, 0
v_cmp_lt_u32 vcc, s55, v96
v_cmp_lt_u32 s[98:99], s55, v97
s_nop 0
v_addc_co_u32 v253, vcc, 0, v253, vcc
v_addc_co_u32 v253, s[98:99], 0, v253, s[98:99]
v_cmp_lt_u32 vcc, s55, v98
v_cmp_lt_u32 s[98:99], s55, v99
s_nop 0
v_addc_co_u32 v253, vcc, 0, v253, vcc
v_addc_co_u32 v253, s[98:99], 0, v253, s[98:99]
v_cmp_lt_u32 vcc, s55, v100
v_cmp_lt_u32 s[98:99], s55, v101
s_nop 0
v_addc_co_u32 v253, vcc, 0, v253, vcc
v_addc_co_u32 v253, s[98:99], 0, v253, s[98:99]
v_cmp_lt_u32 vcc, s55, v102
v_cmp_lt_u32 s[98:99], s55, v103
s_nop 0
v_addc_co_u32 v253, vcc, 0, v253, vcc
v_addc_co_u32 v253, s[98:99], 0, v253, s[98:99]
v_cmp_lt_u32 vcc, s55, v104
v_cmp_lt_u32 s[98:99], s55, v105
s_nop 0
v_addc_co_u32 v253, vcc, 0, v253, vcc
v_addc_co_u32 v253, s[98:99], 0, v253, s[98:99]
v_cmp_lt_u32 vcc, s55, v106
v_cmp_lt_u32 s[98:99], s55, v107
s_nop 0
v_addc_co_u32 v253, vcc, 0, v253, vcc
v_addc_co_u32 v253, s[98:99], 0, v253, s[98:99]
v_cmp_lt_u32 vcc, s55, v108
v_cmp_lt_u32 s[98:99], s55, v109
s_nop 0
v_addc_co_u32 v253, vcc, 0, v253, vcc
v_addc_co_u32 v253, s[98:99], 0, v253, s[98:99]
v_cmp_lt_u32 vcc, s55, v110
v_cmp_lt_u32 s[98:99], s55, v111
s_nop 0
v_addc_co_u32 v253, vcc, 0, v253, vcc
v_addc_co_u32 v253, s[98:99], 0, v253, s[98:99]
v_cmp_lt_u32 vcc, s55, v112
v_cmp_lt_u32 s[98:99], s55, v113
s_nop 0
v_addc_co_u32 v253, vcc, 0, v253, vcc
v_addc_co_u32 v253, s[98:99], 0, v253, s[98:99]
v_cmp_lt_u32 vcc, s55, v114
v_cmp_lt_u32 s[98:99], s55, v115
s_nop 0
v_addc_co_u32 v253, vcc, 0, v253, vcc
v_addc_co_u32 v253, s[98:99], 0, v253, s[98:99]
v_cmp_lt_u32 vcc, s55, v116
v_cmp_lt_u32 s[98:99], s55, v117
s_nop 0
v_addc_co_u32 v253, vcc, 0, v253, vcc
v_addc_co_u32 v253, s[98:99], 0, v253, s[98:99]
v_cmp_lt_u32 vcc, s55, v118
v_cmp_lt_u32 s[98:99], s55, v119
s_nop 0
v_addc_co_u32 v253, vcc, 0, v253, vcc
v_addc_co_u32 v253, s[98:99], 0, v253, s[98:99]
v_cmp_lt_u32 vcc, s55, v120
v_cmp_lt_u32 s[98:99], s55, v121
s_nop 0
v_addc_co_u32 v253, vcc, 0, v253, vcc
v_addc_co_u32 v253, s[98:99], 0, v253, s[98:99]
v_cmp_lt_u32 vcc, s55, v122
v_cmp_lt_u32 s[98:99], s55, v123
s_nop 0
v_addc_co_u32 v253, vcc, 0, v253, vcc
v_addc_co_u32 v253, s[98:99], 0, v253, s[98:99]
v_cmp_lt_u32 vcc, s55, v124
v_cmp_lt_u32 s[98:99], s55, v125
s_nop 0
v_addc_co_u32 v253, vcc, 0, v253, vcc
v_addc_co_u32 v253, s[98:99], 0, v253, s[98:99]
v_cmp_lt_u32 vcc, s55, v126
v_cmp_lt_u32 s[98:99], s55, v127
s_nop 0
v_addc_co_u32 v253, vcc, 0, v253, vcc
v_addc_co_u32 v253, s[98:99], 0, v253, s[98:99]
s_nop 1
v_add_u32_dpp v253, v253, v253 row_ror:8 row_mask:0xf bank_mask:0xf
s_nop 1
v_add_u32_dpp v253, v253, v253 row_ror:4 row_mask:0xf bank_mask:0xf
s_nop 1
v_add_u32_dpp v253, v253, v253 row_ror:2 row_mask:0xf bank_mask:0xf
s_nop 1
v_add_u32_dpp v253, v253, v253 row_ror:1 row_mask:0xf bank_mask:0xf
v_mov_b32 v250, v253
s_nop 1
v_permlane16_swap_b32 v253, v250
v_add_u32 v253, v253, v250
v_mov_b32 v250, v253
s_nop 1
v_permlane32_swap_b32 v253, v250
v_add_u32 v253, v253, v250
v_cmp_gt_u32 vcc, 8, v253
v_mov_b32 v250, s55
v_mov_b32 v251, s69
v_lshlrev_b32 v253, 3, v253
s_mov_b32 s76, 94208
s_cbranch_vccz LRK3_%=
s_mov_b64 exec, 1
buffer_store_dwordx2 v[250:251], v253, s[20:23], s76 offen offset:0 sc1
s_mov_b64 exec, -1
s_nop 1
LRK3_%=:
s_branch LRKb_%=
LRIX_%=:
s_mov_b32 s91, 30000
LRIXl_%=:
s_sleep 2
v_bfe_u32 v251, v248, 4, 3
v_lshlrev_b32 v251, 3, v251
buffer_load_dwordx2 v[252:253], v251, s[20:23], s86 offen offset:0 sc1
s_waitcnt vmcnt(0)
v_cmp_ne_u32 vcc, s69, v253
s_cbranch_vccz LRIXb_%=
s_sub_u32 s91, s91, 1
s_cbranch_scc0 LRIXl_%=
s_branch LRIXb_%=
LRFG_%=:
s_mov_b32 s91, 30000
LRFGl_%=:
s_sleep 2
buffer_load_dword v252, v253, s[20:23], s76 offen offset:0 sc1
s_waitcnt vmcnt(0)
v_cmp_ne_u32 vcc, s69, v252
s_cbranch_vccz LRFGb_%=
s_sub_u32 s91, s91, 1
s_cbranch_scc0 LRFGl_%=
s_branch LRFGb_%=
LRFH_%=:
s_mov_b32 s91, 30000
LRFHl_%=:
s_sleep 2
buffer_load_dword v252, v253, s[20:23], s76 offen offset:0 sc1
s_waitcnt vmcnt(0)
v_cmp_ne_u32 vcc, s69, v252
s_cbranch_vccz LRFHb_%=
s_sub_u32 s91, s91, 1
s_cbranch_scc0 LRFHl_%=
s_branch LRFHb_%=
LRFY_%=:
s_mov_b32 s91, 30000
LRFYl_%=:
s_sleep 2
buffer_load_dword v252, v253, s[20:23], s76 offen offset:0 sc1
s_waitcnt vmcnt(0)
v_cmp_ne_u32 vcc, s69, v252
s_cbranch_vccz LRFYb_%=
s_sub_u32 s91, s91, 1
s_cbranch_scc0 LRFYl_%=
s_branch LRFYb_%=
LRPO0_%=:
s_mov_b32 s97, 30000
buffer_load_dword v242, v243, s[20:23], s44 offen offset:0 sc1
s_sleep 4
LRPO0l_%=:
buffer_load_dword v245, v243, s[20:23], s44 offen offset:0 sc1
s_waitcnt vmcnt(1)
v_cmp_ne_u32 vcc, s69, v242
s_cbranch_vccz LRPO0b_%=
buffer_load_dword v242, v243, s[20:23], s44 offen offset:0 sc1
s_waitcnt vmcnt(1)
v_cmp_ne_u32 vcc, s69, v245
s_cbranch_vccz LRPO0b_%=
s_sub_u32 s97, s97, 1
s_cbranch_scc0 LRPO0l_%=
s_branch LRPO0b_%=
LRPW_%=:
s_mov_b32 s61, 30000
LRPWl_%=:
s_sleep 2
buffer_load_dwordx2 v[96:97], v98, s[20:23], s62 offen offset:0 sc1
s_waitcnt vmcnt(0)
v_xor_b32 v96, s69, v96
v_xor_b32 v97, s69, v97
v_or_b32 v99, v96, v97
v_cmp_ne_u32 vcc, 0, v99
s_cbranch_vccz LRPWb_%=
s_sub_u32 s61, s61, 1
s_cbranch_scc0 LRPWl_%=
s_branch LRPWb_%=
LRPO1_%=:
s_mov_b32 s97, 30000
LRPO1l_%=:
s_sleep 2
buffer_load_dword v242, v243, s[20:23], s96 offen offset:16 sc1
s_waitcnt vmcnt(0)
v_cmp_ne_u32 vcc, s69, v242
s_cbranch_vccz LRPO1b_%=
s_sub_u32 s97, s97, 1
s_cbranch_scc0 LRPO1l_%=
s_branch LRPO1b_%=
LKEND_ka:
LRET_%=:
s_cmp_eq_u32 s73, 0
s_cbranch_scc1 LBACK0_%=
s_cmp_eq_u32 s73, 2
s_cbranch_scc1 LBACK2_%=
s_cmp_eq_u32 s73, 1
s_cbranch_scc1 LBACK1_%=
s_cmp_eq_u32 s73, 3
s_cbranch_scc1 LBACK3_%=
s_cmp_eq_u32 s73, 5
s_cbranch_scc1 LBACK5_%=
s_cmp_eq_u32 s73, 7
s_cbranch_scc1 LBACK7_%=
s_cmp_eq_u32 s73, 9
s_cbranch_scc1 LBACK9_%=
s_cmp_eq_u32 s73, 11
s_cbranch_scc1 LBACK11_%=
s_cmp_eq_u32 s73, 13
s_cbranch_scc1 LBACK13_%=
s_cmp_eq_u32 s73, 15
s_cbranch_scc1 LBACK15_%=
s_cmp_eq_u32 s73, 17
s_cbranch_scc1 LBACK17_%=
s_cmp_eq_u32 s73, 19
s_cbranch_scc1 LBACK19_%=
s_cmp_eq_u32 s73, 21
s_cbranch_scc1 LBACK21_%=
s_cmp_eq_u32 s73, 23
s_cbranch_scc1 LBACK23_%=
s_cmp_eq_u32 s73, 25
s_cbranch_scc1 LBACK25_%=
s_cmp_eq_u32 s73, 27
s_cbranch_scc1 LBACK27_%=
s_cmp_eq_u32 s73, 29
s_cbranch_scc1 LBACK29_%=
s_cmp_eq_u32 s73, 31
s_cbranch_scc1 LBACK31_%=
s_cmp_eq_u32 s73, 33
s_cbranch_scc1 LBACK33_%=
s_cmp_eq_u32 s73, 35
s_cbranch_scc1 LBACK35_%=
s_cmp_eq_u32 s73, 4
s_cbranch_scc1 LBACK4_%=
s_cmp_eq_u32 s73, 6
s_cbranch_scc1 LBACK6_%=
s_cmp_eq_u32 s73, 8
s_cbranch_scc1 LBACK8_%=
s_cmp_eq_u32 s73, 10
s_cbranch_scc1 LBACK10_%=
s_cmp_eq_u32 s73, 12
s_cbranch_scc1 LBACK12_%=
s_cmp_eq_u32 s73, 14
s_cbranch_scc1 LBACK14_%=
s_cmp_eq_u32 s73, 16
s_cbranch_scc1 LBACK16_%=
s_cmp_eq_u32 s73, 18
s_cbranch_scc1 LBACK18_%=
s_cmp_eq_u32 s73, 20
s_cbranch_scc1 LBACK20_%=
s_cmp_eq_u32 s73, 22
s_cbranch_scc1 LBACK22_%=
s_cmp_eq_u32 s73, 24
s_cbranch_scc1 LBACK24_%=
s_cmp_eq_u32 s73, 26
s_cbranch_scc1 LBACK26_%=
s_cmp_eq_u32 s73, 28
s_cbranch_scc1 LBACK28_%=
s_cmp_eq_u32 s73, 30
s_cbranch_scc1 LBACK30_%=
s_cmp_eq_u32 s73, 32
s_cbranch_scc1 LBACK32_%=
s_cmp_eq_u32 s73, 34
s_cbranch_scc1 LBACK34_%=
s_cmp_eq_u32 s73, 36
s_cbranch_scc1 LBACK36_%=
s_cmp_eq_u32 s73, 38
s_cbranch_scc1 LBACK38_%=
s_cmp_eq_u32 s73, 37
s_cbranch_scc1 LBACK37_%=
s_endpgm
LSITE0_%=:
s_mov_b32 s73, 0
s_mov_b32 s74, 0
s_mov_b32 s75, 16384
s_branch LRARE002_%=
LSITE2_%=:
s_mov_b32 s73, 2
s_mov_b32 s74, 32768
s_mov_b32 s75, 49152
s_branch LRARE002_%=
LSITE1_%=:
s_mov_b32 s73, 1
s_mov_b32 s74, 16384
s_mov_b32 s75, 32768
s_branch LRARE102_%=
LSITE3_%=:
s_mov_b32 s73, 3
s_mov_b32 s74, 49152
s_mov_b32 s75, 0
s_branch LRARE104_%=
LSITE5_%=:
s_mov_b32 s73, 5
s_mov_b32 s74, 16384
s_mov_b32 s75, 32768
s_branch LRARE104_%=
LSITE7_%=:
s_mov_b32 s73, 7
s_mov_b32 s74, 49152
s_mov_b32 s75, 0
s_branch LRARE104_%=
LSITE9_%=:
s_mov_b32 s73, 9
s_mov_b32 s74, 16384
s_mov_b32 s75, 32768
s_branch LRARE104_%=
LSITE11_%=:
s_mov_b32 s73, 11
s_mov_b32 s74, 49152
s_mov_b32 s75, 0
s_branch LRARE104_%=
LSITE13_%=:
s_mov_b32 s73, 13
s_mov_b32 s74, 16384
s_mov_b32 s75, 32768
s_branch LRARE104_%=
LSITE15_%=:
s_mov_b32 s73, 15
s_mov_b32 s74, 49152
s_mov_b32 s75, 0
s_branch LRARE104_%=
LSITE17_%=:
s_mov_b32 s73, 17
s_mov_b32 s74, 16384
s_mov_b32 s75, 32768
s_branch LRARE104_%=
LSITE19_%=:
s_mov_b32 s73, 19
s_mov_b32 s74, 49152
s_mov_b32 s75, 0
s_branch LRARE104_%=
LSITE21_%=:
s_mov_b32 s73, 21
s_mov_b32 s74, 16384
s_mov_b32 s75, 32768
s_branch LRARE104_%=
LSITE23_%=:
s_mov_b32 s73, 23
s_mov_b32 s74, 49152
s_mov_b32 s75, 0
s_branch LRARE104_%=
LSITE25_%=:
s_mov_b32 s73, 25
s_mov_b32 s74, 16384
s_mov_b32 s75, 32768
s_branch LRARE104_%=
LSITE27_%=:
s_mov_b32 s73, 27
s_mov_b32 s74, 49152
s_mov_b32 s75, 0
s_branch LRARE104_%=
LSITE29_%=:
s_mov_b32 s73, 29
s_mov_b32 s74, 16384
s_mov_b32 s75, 32768
s_branch LRARE104_%=
LSITE31_%=:
s_mov_b32 s73, 31
s_mov_b32 s74, 49152
s_mov_b32 s75, 0
s_branch LRARE104_%=
LSITE33_%=:
s_mov_b32 s73, 33
s_mov_b32 s74, 16384
s_mov_b32 s75, 32768
s_branch LRARE104_%=
LSITE35_%=:
s_mov_b32 s73, 35
s_mov_b32 s74, 49152
s_mov_b32 s75, 0
s_branch LRARE104_%=
LSITE4_%=:
s_mov_b32 s73, 4
s_mov_b32 s74, 0
s_mov_b32 s75, 16384
s_branch LRARE004_%=
LSITE6_%=:
s_mov_b32 s73, 6
s_mov_b32 s74, 32768
s_mov_b32 s75, 49152
s_branch LRARE004_%=
LSITE8_%=:
s_mov_b32 s73, 8
s_mov_b32 s74, 0
s_mov_b32 s75, 16384
s_branch LRARE004_%=
LSITE10_%=:
s_mov_b32 s73, 10
s_mov_b32 s74, 32768
s_mov_b32 s75, 49152
s_branch LRARE004_%=
LSITE12_%=:
s_mov_b32 s73, 12
s_mov_b32 s74, 0
s_mov_b32 s75, 16384
s_branch LRARE004_%=
LSITE14_%=:
s_mov_b32 s73, 14
s_mov_b32 s74, 32768
s_mov_b32 s75, 49152
s_branch LRARE004_%=
)ASM"
R"ASM(
LSITE16_%=:
s_mov_b32 s73, 16
s_mov_b32 s74, 0
s_mov_b32 s75, 16384
s_branch LRARE004_%=
LSITE18_%=:
s_mov_b32 s73, 18
s_mov_b32 s74, 32768
s_mov_b32 s75, 49152
s_branch LRARE004_%=
LSITE20_%=:
s_mov_b32 s73, 20
s_mov_b32 s74, 0
s_mov_b32 s75, 16384
s_branch LRARE004_%=
LSITE22_%=:
s_mov_b32 s73, 22
s_mov_b32 s74, 32768
s_mov_b32 s75, 49152
s_branch LRARE004_%=
LSITE24_%=:
s_mov_b32 s73, 24
s_mov_b32 s74, 0
s_mov_b32 s75, 16384
s_branch LRARE004_%=
LSITE26_%=:
s_mov_b32 s73, 26
s_mov_b32 s74, 32768
s_mov_b32 s75, 49152
s_branch LRARE004_%=
LSITE28_%=:
s_mov_b32 s73, 28
s_mov_b32 s74, 0
s_mov_b32 s75, 16384
s_branch LRARE004_%=
LSITE30_%=:
s_mov_b32 s73, 30
s_mov_b32 s74, 32768
s_mov_b32 s75, 49152
s_branch LRARE004_%=
LSITE32_%=:
s_mov_b32 s73, 32
s_mov_b32 s74, 0
s_mov_b32 s75, 16384
s_branch LRARE004_%=
LSITE34_%=:
s_mov_b32 s73, 34
s_mov_b32 s74, 32768
s_mov_b32 s75, 49152
s_branch LRARE004_%=
LSITE36_%=:
s_mov_b32 s73, 36
s_mov_b32 s74, 0
s_mov_b32 s75, 16384
s_branch LRARE022_%=
LSITE38_%=:
s_mov_b32 s73, 38
s_mov_b32 s74, 32768
s_mov_b32 s75, 49152
s_branch LRARE022_%=
LSITE37_%=:
s_mov_b32 s73, 37
s_mov_b32 s74, 16384
s_mov_b32 s75, 32768
s_branch LRARE122_%=
LRARE002_%=:
s_nop 15
s_waitcnt vmcnt(0) lgkmcnt(0)
v_add_u32 v249, s74, v224
RD a[192:195],v249 offset:0
RD a[208:211],v249 offset:4096
RD a[224:227],v249 offset:8192
RD a[240:243],v249 offset:12288
v_add_u32 v250, s74, v225
RD a[196:199],v250 offset:0
RD a[212:215],v250 offset:4096
RD a[228:231],v250 offset:8192
RD a[244:247],v250 offset:12288
v_add_u32 v251, s74, v226
RD a[200:203],v251 offset:0
RD a[216:219],v251 offset:4096
RD a[232:235],v251 offset:8192
RD a[248:251],v251 offset:12288
v_add_u32 v254, s74, v227
RD a[204:207],v254 offset:0
RD a[220:223],v254 offset:4096
RD a[236:239],v254 offset:8192
RD a[252:255],v254 offset:12288
s_waitcnt lgkmcnt(0)
MM v[0:3],a[192:195],v[160:163],v[128:131]
MM v[0:3],a[196:199],v[176:179],v[0:3]
MM v[0:3],a[200:203],v[192:195],v[0:3]
MM v[0:3],a[204:207],v[208:211],v[0:3]
MM v[4:7],a[220:223],v[208:211],v[128:131]
MM v[4:7],a[216:219],v[192:195],v[4:7]
MM v[4:7],a[212:215],v[176:179],v[4:7]
MM v[4:7],a[208:211],v[160:163],v[4:7]
MM v[12:15],a[208:211],v[164:167],v[132:135]
MM v[12:15],a[212:215],v[180:183],v[12:15]
MM v[12:15],a[216:219],v[196:199],v[12:15]
MM v[12:15],a[220:223],v[212:215],v[12:15]
MM v[8:11],a[204:207],v[212:215],v[132:135]
MM v[8:11],a[200:203],v[196:199],v[8:11]
MM v[8:11],a[196:199],v[180:183],v[8:11]
MM v[8:11],a[192:195],v[164:167],v[8:11]
MM v[32:35],a[224:227],v[160:163],v[128:131]
MM v[32:35],a[228:231],v[176:179],v[32:35]
MM v[32:35],a[232:235],v[192:195],v[32:35]
MM v[32:35],a[236:239],v[208:211],v[32:35]
MM v[36:39],a[252:255],v[208:211],v[128:131]
MM v[36:39],a[248:251],v[192:195],v[36:39]
MM v[36:39],a[244:247],v[176:179],v[36:39]
MM v[36:39],a[240:243],v[160:163],v[36:39]
MM v[44:47],a[240:243],v[164:167],v[132:135]
MM v[44:47],a[244:247],v[180:183],v[44:47]
MM v[44:47],a[248:251],v[196:199],v[44:47]
MM v[44:47],a[252:255],v[212:215],v[44:47]
MM v[40:43],a[236:239],v[212:215],v[132:135]
MM v[40:43],a[232:235],v[196:199],v[40:43]
MM v[40:43],a[228:231],v[180:183],v[40:43]
MM v[40:43],a[224:227],v[164:167],v[40:43]
s_nop 15
v_add_u32 v249, s75, v224
RD a[192:195],v249 offset:0
RD a[208:211],v249 offset:4096
RD a[224:227],v249 offset:8192
RD a[240:243],v249 offset:12288
v_add_u32 v250, s75, v225
RD a[196:199],v250 offset:0
RD a[212:215],v250 offset:4096
RD a[228:231],v250 offset:8192
RD a[244:247],v250 offset:12288
v_add_u32 v251, s75, v226
RD a[200:203],v251 offset:0
RD a[216:219],v251 offset:4096
RD a[232:235],v251 offset:8192
RD a[248:251],v251 offset:12288
v_add_u32 v254, s75, v227
RD a[204:207],v254 offset:0
RD a[220:223],v254 offset:4096
RD a[236:239],v254 offset:8192
RD a[252:255],v254 offset:12288
s_waitcnt lgkmcnt(0)
v_max3_f32 v249, v0, v1, v2
v_max3_f32 v251, v32, v33, v34
v_max3_f32 v249, v249, v3, v4
v_max3_f32 v251, v251, v35, v36
v_max3_f32 v249, v249, v5, v6
v_max3_f32 v251, v251, v37, v38
v_max3_f32 v249, v249, v7, v39
v_max_f32 v249, v249, v251
v_mov_b32 v251, v249
s_nop 1
v_permlane16_swap_b32 v249, v251
v_max_f32 v249, v249, v251
v_mov_b32 v251, v249
s_nop 1
v_permlane32_swap_b32 v249, v251
v_max_f32 v249, v249, v251
v_log_f32 v250, v244
s_nop 0
v_max3_f32 v249, 0, v249, v250
v_sub_f32 v250, 0, v249
EX v250,v250
v_sub_f32 v128, v128, v249
v_sub_f32 v129, v129, v249
v_sub_f32 v130, v130, v249
v_sub_f32 v131, v131, v249
v_sub_f32 v64, v64, v249
v_sub_f32 v0, v0, v249
v_sub_f32 v65, v65, v249
v_sub_f32 v1, v1, v249
v_sub_f32 v66, v66, v249
v_sub_f32 v2, v2, v249
v_sub_f32 v67, v67, v249
v_sub_f32 v3, v3, v249
v_sub_f32 v68, v68, v249
v_sub_f32 v4, v4, v249
v_sub_f32 v69, v69, v249
v_sub_f32 v5, v5, v249
v_sub_f32 v70, v70, v249
v_sub_f32 v6, v6, v249
v_sub_f32 v71, v71, v249
v_sub_f32 v7, v7, v249
v_sub_f32 v96, v96, v249
v_sub_f32 v32, v32, v249
v_sub_f32 v97, v97, v249
v_sub_f32 v33, v33, v249
v_sub_f32 v98, v98, v249
v_sub_f32 v34, v34, v249
v_sub_f32 v99, v99, v249
v_sub_f32 v35, v35, v249
v_sub_f32 v100, v100, v249
v_sub_f32 v36, v36, v249
v_sub_f32 v101, v101, v249
v_sub_f32 v37, v37, v249
v_sub_f32 v102, v102, v249
v_sub_f32 v38, v38, v249
v_sub_f32 v103, v103, v249
v_sub_f32 v39, v39, v249
v_mul_f32 v144, v244, v250
v_mov_b32 v145, 0
v_bfe_u32 v249, v241, 0, 8
v_lshl_or_b32 v249, v249, 1, 1
v_lshlrev_b32 v249, 22, v249
v_and_b32 v241, 0xffffff00, v241
AR v254,a0
v_sub_f32 v254, v254, v249
v_mul_f32 v254, v254, v250
AW a0,v254
AR v251,a1
v_sub_f32 v251, v251, v249
v_mul_f32 v251, v251, v250
AW a1,v251
AR v254,a2
v_sub_f32 v254, v254, v249
v_mul_f32 v254, v254, v250
AW a2,v254
AR v251,a3
v_sub_f32 v251, v251, v249
v_mul_f32 v251, v251, v250
AW a3,v251
AR v254,a4
v_sub_f32 v254, v254, v249
v_mul_f32 v254, v254, v250
AW a4,v254
AR v251,a5
v_sub_f32 v251, v251, v249
v_mul_f32 v251, v251, v250
AW a5,v251
AR v254,a6
v_sub_f32 v254, v254, v249
v_mul_f32 v254, v254, v250
AW a6,v254
AR v251,a7
v_sub_f32 v251, v251, v249
v_mul_f32 v251, v251, v250
AW a7,v251
AR v254,a32
v_sub_f32 v254, v254, v249
v_mul_f32 v254, v254, v250
AW a32,v254
AR v251,a33
v_sub_f32 v251, v251, v249
v_mul_f32 v251, v251, v250
AW a33,v251
AR v254,a34
v_sub_f32 v254, v254, v249
v_mul_f32 v254, v254, v250
AW a34,v254
AR v251,a35
v_sub_f32 v251, v251, v249
v_mul_f32 v251, v251, v250
AW a35,v251
AR v254,a36
v_sub_f32 v254, v254, v249
v_mul_f32 v254, v254, v250
AW a36,v254
AR v251,a37
v_sub_f32 v251, v251, v249
v_mul_f32 v251, v251, v250
AW a37,v251
AR v254,a38
v_sub_f32 v254, v254, v249
v_mul_f32 v254, v254, v250
AW a38,v254
AR v251,a39
v_sub_f32 v251, v251, v249
v_mul_f32 v251, v251, v250
AW a39,v251
AR v254,a64
v_sub_f32 v254, v254, v249
v_mul_f32 v254, v254, v250
AW a64,v254
AR v251,a65
v_sub_f32 v251, v251, v249
v_mul_f32 v251, v251, v250
AW a65,v251
AR v254,a66
v_sub_f32 v254, v254, v249
v_mul_f32 v254, v254, v250
AW a66,v254
AR v251,a67
v_sub_f32 v251, v251, v249
v_mul_f32 v251, v251, v250
AW a67,v251
AR v254,a68
v_sub_f32 v254, v254, v249
v_mul_f32 v254, v254, v250
AW a68,v254
AR v251,a69
v_sub_f32 v251, v251, v249
v_mul_f32 v251, v251, v250
AW a69,v251
AR v254,a70
v_sub_f32 v254, v254, v249
v_mul_f32 v254, v254, v250
AW a70,v254
AR v251,a71
v_sub_f32 v251, v251, v249
v_mul_f32 v251, v251, v250
AW a71,v251
AR v254,a96
v_sub_f32 v254, v254, v249
v_mul_f32 v254, v254, v250
AW a96,v254
AR v251,a97
v_sub_f32 v251, v251, v249
v_mul_f32 v251, v251, v250
AW a97,v251
AR v254,a98
v_sub_f32 v254, v254, v249
v_mul_f32 v254, v254, v250
AW a98,v254
AR v251,a99
v_sub_f32 v251, v251, v249
v_mul_f32 v251, v251, v250
AW a99,v251
AR v254,a100
v_sub_f32 v254, v254, v249
v_mul_f32 v254, v254, v250
AW a100,v254
AR v251,a101
v_sub_f32 v251, v251, v249
v_mul_f32 v251, v251, v250
AW a101,v251
AR v254,a102
v_sub_f32 v254, v254, v249
v_mul_f32 v254, v254, v250
AW a102,v254
AR v251,a103
v_sub_f32 v251, v251, v249
v_mul_f32 v251, v251, v250
AW a103,v251
v_max3_f32 v249, v8, v9, v10
v_max3_f32 v251, v40, v41, v42
v_max3_f32 v249, v249, v11, v12
v_max3_f32 v251, v251, v43, v44
v_max3_f32 v249, v249, v13, v14
v_max3_f32 v251, v251, v45, v46
v_max3_f32 v249, v249, v15, v47
v_max_f32 v249, v249, v251
v_mov_b32 v251, v249
s_nop 1
v_permlane16_swap_b32 v249, v251
v_max_f32 v249, v249, v251
v_mov_b32 v251, v249
s_nop 1
v_permlane32_swap_b32 v249, v251
v_max_f32 v249, v249, v251
v_log_f32 v250, v245
s_nop 0
v_max3_f32 v249, 0, v249, v250
v_sub_f32 v250, 0, v249
EX v250,v250
v_sub_f32 v132, v132, v249
v_sub_f32 v133, v133, v249
v_sub_f32 v134, v134, v249
v_sub_f32 v135, v135, v249
v_sub_f32 v72, v72, v249
v_sub_f32 v8, v8, v249
v_sub_f32 v73, v73, v249
v_sub_f32 v9, v9, v249
v_sub_f32 v74, v74, v249
v_sub_f32 v10, v10, v249
v_sub_f32 v75, v75, v249
v_sub_f32 v11, v11, v249
v_sub_f32 v76, v76, v249
v_sub_f32 v12, v12, v249
v_sub_f32 v77, v77, v249
v_sub_f32 v13, v13, v249
v_sub_f32 v78, v78, v249
v_sub_f32 v14, v14, v249
v_sub_f32 v79, v79, v249
v_sub_f32 v15, v15, v249
v_sub_f32 v104, v104, v249
v_sub_f32 v40, v40, v249
v_sub_f32 v105, v105, v249
v_sub_f32 v41, v41, v249
v_sub_f32 v106, v106, v249
v_sub_f32 v42, v42, v249
v_sub_f32 v107, v107, v249
v_sub_f32 v43, v43, v249
v_sub_f32 v108, v108, v249
v_sub_f32 v44, v44, v249
v_sub_f32 v109, v109, v249
v_sub_f32 v45, v45, v249
v_sub_f32 v110, v110, v249
v_sub_f32 v46, v46, v249
v_sub_f32 v111, v111, v249
v_sub_f32 v47, v47, v249
v_mul_f32 v148, v245, v250
v_mov_b32 v149, 0
v_bfe_u32 v249, v241, 8, 8
v_lshl_or_b32 v249, v249, 1, 1
v_lshlrev_b32 v249, 22, v249
v_and_b32 v241, 0xffff00ff, v241
AR v254,a8
v_sub_f32 v254, v254, v249
v_mul_f32 v254, v254, v250
AW a8,v254
AR v251,a9
v_sub_f32 v251, v251, v249
v_mul_f32 v251, v251, v250
AW a9,v251
AR v254,a10
v_sub_f32 v254, v254, v249
v_mul_f32 v254, v254, v250
AW a10,v254
AR v251,a11
v_sub_f32 v251, v251, v249
v_mul_f32 v251, v251, v250
AW a11,v251
AR v254,a12
v_sub_f32 v254, v254, v249
v_mul_f32 v254, v254, v250
AW a12,v254
AR v251,a13
v_sub_f32 v251, v251, v249
v_mul_f32 v251, v251, v250
AW a13,v251
AR v254,a14
v_sub_f32 v254, v254, v249
v_mul_f32 v254, v254, v250
AW a14,v254
AR v251,a15
v_sub_f32 v251, v251, v249
v_mul_f32 v251, v251, v250
AW a15,v251
AR v254,a40
v_sub_f32 v254, v254, v249
v_mul_f32 v254, v254, v250
AW a40,v254
AR v251,a41
v_sub_f32 v251, v251, v249
v_mul_f32 v251, v251, v250
AW a41,v251
AR v254,a42
v_sub_f32 v254, v254, v249
v_mul_f32 v254, v254, v250
AW a42,v254
AR v251,a43
v_sub_f32 v251, v251, v249
v_mul_f32 v251, v251, v250
AW a43,v251
AR v254,a44
v_sub_f32 v254, v254, v249
v_mul_f32 v254, v254, v250
AW a44,v254
AR v251,a45
v_sub_f32 v251, v251, v249
v_mul_f32 v251, v251, v250
AW a45,v251
AR v254,a46
v_sub_f32 v254, v254, v249
v_mul_f32 v254, v254, v250
AW a46,v254
AR v251,a47
v_sub_f32 v251, v251, v249
v_mul_f32 v251, v251, v250
AW a47,v251
AR v254,a72
v_sub_f32 v254, v254, v249
v_mul_f32 v254, v254, v250
AW a72,v254
AR v251,a73
v_sub_f32 v251, v251, v249
v_mul_f32 v251, v251, v250
AW a73,v251
AR v254,a74
v_sub_f32 v254, v254, v249
v_mul_f32 v254, v254, v250
AW a74,v254
AR v251,a75
v_sub_f32 v251, v251, v249
v_mul_f32 v251, v251, v250
AW a75,v251
AR v254,a76
v_sub_f32 v254, v254, v249
v_mul_f32 v254, v254, v250
AW a76,v254
AR v251,a77
v_sub_f32 v251, v251, v249
v_mul_f32 v251, v251, v250
AW a77,v251
AR v254,a78
v_sub_f32 v254, v254, v249
v_mul_f32 v254, v254, v250
AW a78,v254
AR v251,a79
v_sub_f32 v251, v251, v249
v_mul_f32 v251, v251, v250
AW a79,v251
AR v254,a104
v_sub_f32 v254, v254, v249
v_mul_f32 v254, v254, v250
AW a104,v254
AR v251,a105
v_sub_f32 v251, v251, v249
v_mul_f32 v251, v251, v250
AW a105,v251
AR v254,a106
v_sub_f32 v254, v254, v249
v_mul_f32 v254, v254, v250
AW a106,v254
AR v251,a107
v_sub_f32 v251, v251, v249
v_mul_f32 v251, v251, v250
AW a107,v251
AR v254,a108
v_sub_f32 v254, v254, v249
v_mul_f32 v254, v254, v250
AW a108,v254
AR v251,a109
v_sub_f32 v251, v251, v249
v_mul_f32 v251, v251, v250
AW a109,v251
AR v254,a110
v_sub_f32 v254, v254, v249
v_mul_f32 v254, v254, v250
AW a110,v254
AR v251,a111
v_sub_f32 v251, v251, v249
v_mul_f32 v251, v251, v250
AW a111,v251
)ASM"
R"ASM(
EX v0,v0
EX v1,v1
EX v2,v2
EX v3,v3
EX v4,v4
EX v5,v5
EX v6,v6
EX v7,v7
EX v8,v8
EX v9,v9
EX v10,v10
EX v11,v11
EX v12,v12
EX v13,v13
EX v14,v14
EX v15,v15
EX v32,v32
EX v33,v33
EX v34,v34
EX v35,v35
EX v36,v36
EX v37,v37
EX v38,v38
EX v39,v39
EX v40,v40
EX v41,v41
EX v42,v42
EX v43,v43
EX v44,v44
EX v45,v45
EX v46,v46
EX v47,v47
CV v0,v0,v1
CV v1,v2,v3
CV v2,v4,v5
CV v3,v6,v7
CV v8,v8,v9
CV v9,v10,v11
CV v10,v12,v13
CV v11,v14,v15
CV v4,v32,v33
CV v5,v34,v35
CV v6,v36,v37
CV v7,v38,v39
CV v12,v40,v41
CV v13,v42,v43
CV v14,v44,v45
CV v15,v46,v47
v_smfmac_f32_16x16x64_bf16 v[144:147], v[236:239], v[0:7], v234
s_nop 0
v_smfmac_f32_16x16x64_bf16 v[148:151], v[236:239], v[8:15], v234
s_nop 9
s_branch LRET_%=
LRARE102_%=:
s_nop 15
s_waitcnt vmcnt(0) lgkmcnt(0)
v_add_u32 v249, s74, v224
RD a[192:195],v249 offset:0
RD a[208:211],v249 offset:4096
RD a[224:227],v249 offset:8192
RD a[240:243],v249 offset:12288
v_add_u32 v250, s74, v225
RD a[196:199],v250 offset:0
RD a[212:215],v250 offset:4096
RD a[228:231],v250 offset:8192
RD a[244:247],v250 offset:12288
v_add_u32 v251, s74, v226
RD a[200:203],v251 offset:0
RD a[216:219],v251 offset:4096
RD a[232:235],v251 offset:8192
RD a[248:251],v251 offset:12288
v_add_u32 v254, s74, v227
RD a[204:207],v254 offset:0
RD a[220:223],v254 offset:4096
RD a[236:239],v254 offset:8192
RD a[252:255],v254 offset:12288
s_waitcnt lgkmcnt(0)
MM v[64:67],a[192:195],v[160:163],v[128:131]
MM v[64:67],a[196:199],v[176:179],v[64:67]
MM v[64:67],a[200:203],v[192:195],v[64:67]
MM v[64:67],a[204:207],v[208:211],v[64:67]
MM v[68:71],a[220:223],v[208:211],v[128:131]
MM v[68:71],a[216:219],v[192:195],v[68:71]
MM v[68:71],a[212:215],v[176:179],v[68:71]
MM v[68:71],a[208:211],v[160:163],v[68:71]
MM v[76:79],a[208:211],v[164:167],v[132:135]
MM v[76:79],a[212:215],v[180:183],v[76:79]
MM v[76:79],a[216:219],v[196:199],v[76:79]
MM v[76:79],a[220:223],v[212:215],v[76:79]
MM v[72:75],a[204:207],v[212:215],v[132:135]
MM v[72:75],a[200:203],v[196:199],v[72:75]
MM v[72:75],a[196:199],v[180:183],v[72:75]
MM v[72:75],a[192:195],v[164:167],v[72:75]
MM v[96:99],a[224:227],v[160:163],v[128:131]
MM v[96:99],a[228:231],v[176:179],v[96:99]
MM v[96:99],a[232:235],v[192:195],v[96:99]
MM v[96:99],a[236:239],v[208:211],v[96:99]
MM v[100:103],a[252:255],v[208:211],v[128:131]
MM v[100:103],a[248:251],v[192:195],v[100:103]
MM v[100:103],a[244:247],v[176:179],v[100:103]
MM v[100:103],a[240:243],v[160:163],v[100:103]
MM v[108:111],a[240:243],v[164:167],v[132:135]
MM v[108:111],a[244:247],v[180:183],v[108:111]
MM v[108:111],a[248:251],v[196:199],v[108:111]
MM v[108:111],a[252:255],v[212:215],v[108:111]
MM v[104:107],a[236:239],v[212:215],v[132:135]
MM v[104:107],a[232:235],v[196:199],v[104:107]
MM v[104:107],a[228:231],v[180:183],v[104:107]
MM v[104:107],a[224:227],v[164:167],v[104:107]
s_nop 15
v_add_u32 v249, s75, v224
RD a[192:195],v249 offset:0
RD a[208:211],v249 offset:4096
RD a[224:227],v249 offset:8192
RD a[240:243],v249 offset:12288
v_add_u32 v250, s75, v225
RD a[196:199],v250 offset:0
RD a[212:215],v250 offset:4096
RD a[228:231],v250 offset:8192
RD a[244:247],v250 offset:12288
v_add_u32 v251, s75, v226
RD a[200:203],v251 offset:0
RD a[216:219],v251 offset:4096
RD a[232:235],v251 offset:8192
RD a[248:251],v251 offset:12288
v_add_u32 v254, s75, v227
RD a[204:207],v254 offset:0
RD a[220:223],v254 offset:4096
RD a[236:239],v254 offset:8192
RD a[252:255],v254 offset:12288
s_waitcnt lgkmcnt(0)
v_max3_f32 v249, v64, v65, v66
v_max3_f32 v251, v96, v97, v98
v_max3_f32 v249, v249, v67, v68
v_max3_f32 v251, v251, v99, v100
v_max3_f32 v249, v249, v69, v70
v_max3_f32 v251, v251, v101, v102
v_max3_f32 v249, v249, v71, v103
v_max_f32 v249, v249, v251
v_mov_b32 v251, v249
s_nop 1
v_permlane16_swap_b32 v249, v251
v_max_f32 v249, v249, v251
v_mov_b32 v251, v249
s_nop 1
v_permlane32_swap_b32 v249, v251
v_max_f32 v249, v249, v251
v_log_f32 v250, v244
s_nop 0
v_max3_f32 v249, 0, v249, v250
v_sub_f32 v250, 0, v249
EX v250,v250
v_sub_f32 v128, v128, v249
v_sub_f32 v129, v129, v249
v_sub_f32 v130, v130, v249
v_sub_f32 v131, v131, v249
v_sub_f32 v0, v0, v249
v_sub_f32 v64, v64, v249
v_sub_f32 v1, v1, v249
v_sub_f32 v65, v65, v249
v_sub_f32 v2, v2, v249
v_sub_f32 v66, v66, v249
v_sub_f32 v3, v3, v249
v_sub_f32 v67, v67, v249
v_sub_f32 v4, v4, v249
v_sub_f32 v68, v68, v249
v_sub_f32 v5, v5, v249
v_sub_f32 v69, v69, v249
v_sub_f32 v6, v6, v249
v_sub_f32 v70, v70, v249
v_sub_f32 v7, v7, v249
v_sub_f32 v71, v71, v249
v_sub_f32 v32, v32, v249
v_sub_f32 v96, v96, v249
v_sub_f32 v33, v33, v249
v_sub_f32 v97, v97, v249
v_sub_f32 v34, v34, v249
v_sub_f32 v98, v98, v249
v_sub_f32 v35, v35, v249
v_sub_f32 v99, v99, v249
v_sub_f32 v36, v36, v249
v_sub_f32 v100, v100, v249
v_sub_f32 v37, v37, v249
v_sub_f32 v101, v101, v249
v_sub_f32 v38, v38, v249
v_sub_f32 v102, v102, v249
v_sub_f32 v39, v39, v249
v_sub_f32 v103, v103, v249
v_mul_f32 v144, v244, v250
v_mov_b32 v145, 0
v_bfe_u32 v249, v241, 0, 8
v_lshl_or_b32 v249, v249, 1, 1
v_lshlrev_b32 v249, 22, v249
v_and_b32 v241, 0xffffff00, v241
AR v254,a0
v_sub_f32 v254, v254, v249
v_mul_f32 v254, v254, v250
AW a0,v254
AR v251,a1
v_sub_f32 v251, v251, v249
v_mul_f32 v251, v251, v250
AW a1,v251
AR v254,a2
v_sub_f32 v254, v254, v249
v_mul_f32 v254, v254, v250
AW a2,v254
AR v251,a3
v_sub_f32 v251, v251, v249
v_mul_f32 v251, v251, v250
AW a3,v251
AR v254,a4
v_sub_f32 v254, v254, v249
v_mul_f32 v254, v254, v250
AW a4,v254
AR v251,a5
v_sub_f32 v251, v251, v249
v_mul_f32 v251, v251, v250
AW a5,v251
AR v254,a6
v_sub_f32 v254, v254, v249
v_mul_f32 v254, v254, v250
AW a6,v254
AR v251,a7
v_sub_f32 v251, v251, v249
v_mul_f32 v251, v251, v250
AW a7,v251
AR v254,a32
v_sub_f32 v254, v254, v249
v_mul_f32 v254, v254, v250
AW a32,v254
AR v251,a33
v_sub_f32 v251, v251, v249
v_mul_f32 v251, v251, v250
AW a33,v251
AR v254,a34
v_sub_f32 v254, v254, v249
v_mul_f32 v254, v254, v250
AW a34,v254
AR v251,a35
v_sub_f32 v251, v251, v249
v_mul_f32 v251, v251, v250
AW a35,v251
AR v254,a36
v_sub_f32 v254, v254, v249
v_mul_f32 v254, v254, v250
AW a36,v254
AR v251,a37
v_sub_f32 v251, v251, v249
v_mul_f32 v251, v251, v250
AW a37,v251
AR v254,a38
v_sub_f32 v254, v254, v249
v_mul_f32 v254, v254, v250
AW a38,v254
AR v251,a39
v_sub_f32 v251, v251, v249
v_mul_f32 v251, v251, v250
AW a39,v251
AR v254,a64
v_sub_f32 v254, v254, v249
v_mul_f32 v254, v254, v250
AW a64,v254
AR v251,a65
v_sub_f32 v251, v251, v249
v_mul_f32 v251, v251, v250
AW a65,v251
AR v254,a66
v_sub_f32 v254, v254, v249
v_mul_f32 v254, v254, v250
AW a66,v254
AR v251,a67
v_sub_f32 v251, v251, v249
v_mul_f32 v251, v251, v250
AW a67,v251
AR v254,a68
v_sub_f32 v254, v254, v249
v_mul_f32 v254, v254, v250
AW a68,v254
AR v251,a69
v_sub_f32 v251, v251, v249
v_mul_f32 v251, v251, v250
AW a69,v251
AR v254,a70
v_sub_f32 v254, v254, v249
v_mul_f32 v254, v254, v250
AW a70,v254
AR v251,a71
v_sub_f32 v251, v251, v249
v_mul_f32 v251, v251, v250
AW a71,v251
AR v254,a96
v_sub_f32 v254, v254, v249
v_mul_f32 v254, v254, v250
AW a96,v254
AR v251,a97
v_sub_f32 v251, v251, v249
v_mul_f32 v251, v251, v250
AW a97,v251
AR v254,a98
v_sub_f32 v254, v254, v249
v_mul_f32 v254, v254, v250
AW a98,v254
AR v251,a99
v_sub_f32 v251, v251, v249
v_mul_f32 v251, v251, v250
AW a99,v251
AR v254,a100
v_sub_f32 v254, v254, v249
v_mul_f32 v254, v254, v250
AW a100,v254
AR v251,a101
v_sub_f32 v251, v251, v249
v_mul_f32 v251, v251, v250
AW a101,v251
AR v254,a102
v_sub_f32 v254, v254, v249
v_mul_f32 v254, v254, v250
AW a102,v254
AR v251,a103
v_sub_f32 v251, v251, v249
v_mul_f32 v251, v251, v250
AW a103,v251
v_max3_f32 v249, v72, v73, v74
v_max3_f32 v251, v104, v105, v106
v_max3_f32 v249, v249, v75, v76
v_max3_f32 v251, v251, v107, v108
v_max3_f32 v249, v249, v77, v78
v_max3_f32 v251, v251, v109, v110
v_max3_f32 v249, v249, v79, v111
v_max_f32 v249, v249, v251
v_mov_b32 v251, v249
s_nop 1
v_permlane16_swap_b32 v249, v251
v_max_f32 v249, v249, v251
v_mov_b32 v251, v249
s_nop 1
v_permlane32_swap_b32 v249, v251
v_max_f32 v249, v249, v251
v_log_f32 v250, v245
s_nop 0
v_max3_f32 v249, 0, v249, v250
v_sub_f32 v250, 0, v249
EX v250,v250
v_sub_f32 v132, v132, v249
v_sub_f32 v133, v133, v249
v_sub_f32 v134, v134, v249
v_sub_f32 v135, v135, v249
v_sub_f32 v8, v8, v249
v_sub_f32 v72, v72, v249
v_sub_f32 v9, v9, v249
v_sub_f32 v73, v73, v249
v_sub_f32 v10, v10, v249
v_sub_f32 v74, v74, v249
v_sub_f32 v11, v11, v249
v_sub_f32 v75, v75, v249
v_sub_f32 v12, v12, v249
v_sub_f32 v76, v76, v249
v_sub_f32 v13, v13, v249
v_sub_f32 v77, v77, v249
v_sub_f32 v14, v14, v249
v_sub_f32 v78, v78, v249
v_sub_f32 v15, v15, v249
v_sub_f32 v79, v79, v249
v_sub_f32 v40, v40, v249
v_sub_f32 v104, v104, v249
v_sub_f32 v41, v41, v249
v_sub_f32 v105, v105, v249
v_sub_f32 v42, v42, v249
v_sub_f32 v106, v106, v249
v_sub_f32 v43, v43, v249
v_sub_f32 v107, v107, v249
v_sub_f32 v44, v44, v249
v_sub_f32 v108, v108, v249
v_sub_f32 v45, v45, v249
v_sub_f32 v109, v109, v249
v_sub_f32 v46, v46, v249
v_sub_f32 v110, v110, v249
v_sub_f32 v47, v47, v249
v_sub_f32 v111, v111, v249
v_mul_f32 v148, v245, v250
v_mov_b32 v149, 0
v_bfe_u32 v249, v241, 8, 8
v_lshl_or_b32 v249, v249, 1, 1
v_lshlrev_b32 v249, 22, v249
v_and_b32 v241, 0xffff00ff, v241
AR v254,a8
v_sub_f32 v254, v254, v249
v_mul_f32 v254, v254, v250
AW a8,v254
AR v251,a9
v_sub_f32 v251, v251, v249
v_mul_f32 v251, v251, v250
AW a9,v251
AR v254,a10
v_sub_f32 v254, v254, v249
v_mul_f32 v254, v254, v250
AW a10,v254
AR v251,a11
v_sub_f32 v251, v251, v249
v_mul_f32 v251, v251, v250
AW a11,v251
AR v254,a12
v_sub_f32 v254, v254, v249
v_mul_f32 v254, v254, v250
AW a12,v254
AR v251,a13
v_sub_f32 v251, v251, v249
v_mul_f32 v251, v251, v250
AW a13,v251
AR v254,a14
v_sub_f32 v254, v254, v249
v_mul_f32 v254, v254, v250
AW a14,v254
AR v251,a15
v_sub_f32 v251, v251, v249
v_mul_f32 v251, v251, v250
AW a15,v251
AR v254,a40
v_sub_f32 v254, v254, v249
v_mul_f32 v254, v254, v250
AW a40,v254
AR v251,a41
v_sub_f32 v251, v251, v249
v_mul_f32 v251, v251, v250
AW a41,v251
AR v254,a42
v_sub_f32 v254, v254, v249
v_mul_f32 v254, v254, v250
AW a42,v254
AR v251,a43
v_sub_f32 v251, v251, v249
v_mul_f32 v251, v251, v250
AW a43,v251
AR v254,a44
v_sub_f32 v254, v254, v249
v_mul_f32 v254, v254, v250
AW a44,v254
AR v251,a45
v_sub_f32 v251, v251, v249
v_mul_f32 v251, v251, v250
AW a45,v251
AR v254,a46
v_sub_f32 v254, v254, v249
v_mul_f32 v254, v254, v250
AW a46,v254
AR v251,a47
v_sub_f32 v251, v251, v249
v_mul_f32 v251, v251, v250
AW a47,v251
AR v254,a72
v_sub_f32 v254, v254, v249
v_mul_f32 v254, v254, v250
AW a72,v254
AR v251,a73
v_sub_f32 v251, v251, v249
v_mul_f32 v251, v251, v250
AW a73,v251
AR v254,a74
v_sub_f32 v254, v254, v249
v_mul_f32 v254, v254, v250
AW a74,v254
AR v251,a75
v_sub_f32 v251, v251, v249
v_mul_f32 v251, v251, v250
AW a75,v251
AR v254,a76
v_sub_f32 v254, v254, v249
v_mul_f32 v254, v254, v250
AW a76,v254
AR v251,a77
v_sub_f32 v251, v251, v249
v_mul_f32 v251, v251, v250
AW a77,v251
AR v254,a78
v_sub_f32 v254, v254, v249
v_mul_f32 v254, v254, v250
AW a78,v254
AR v251,a79
v_sub_f32 v251, v251, v249
v_mul_f32 v251, v251, v250
AW a79,v251
AR v254,a104
v_sub_f32 v254, v254, v249
v_mul_f32 v254, v254, v250
AW a104,v254
AR v251,a105
v_sub_f32 v251, v251, v249
v_mul_f32 v251, v251, v250
AW a105,v251
AR v254,a106
v_sub_f32 v254, v254, v249
v_mul_f32 v254, v254, v250
AW a106,v254
AR v251,a107
v_sub_f32 v251, v251, v249
v_mul_f32 v251, v251, v250
AW a107,v251
AR v254,a108
v_sub_f32 v254, v254, v249
v_mul_f32 v254, v254, v250
AW a108,v254
AR v251,a109
v_sub_f32 v251, v251, v249
v_mul_f32 v251, v251, v250
AW a109,v251
AR v254,a110
v_sub_f32 v254, v254, v249
v_mul_f32 v254, v254, v250
AW a110,v254
AR v251,a111
v_sub_f32 v251, v251, v249
v_mul_f32 v251, v251, v250
AW a111,v251
EX v64,v64
EX v65,v65
EX v66,v66
EX v67,v67
EX v68,v68
EX v69,v69
EX v70,v70
EX v71,v71
EX v72,v72
EX v73,v73
EX v74,v74
EX v75,v75
EX v76,v76
EX v77,v77
EX v78,v78
EX v79,v79
EX v96,v96
EX v97,v97
EX v98,v98
EX v99,v99
EX v100,v100
EX v101,v101
EX v102,v102
EX v103,v103
EX v104,v104
EX v105,v105
EX v106,v106
EX v107,v107
EX v108,v108
EX v109,v109
EX v110,v110
)ASM"
R"ASM(
EX v111,v111
CV v64,v64,v65
CV v65,v66,v67
CV v66,v68,v69
CV v67,v70,v71
CV v72,v72,v73
CV v73,v74,v75
CV v74,v76,v77
CV v75,v78,v79
CV v68,v96,v97
CV v69,v98,v99
CV v70,v100,v101
CV v71,v102,v103
CV v76,v104,v105
CV v77,v106,v107
CV v78,v108,v109
CV v79,v110,v111
v_smfmac_f32_16x16x64_bf16 v[144:147], v[236:239], v[64:71], v234
s_nop 0
v_smfmac_f32_16x16x64_bf16 v[148:151], v[236:239], v[72:79], v234
s_nop 9
s_branch LRET_%=
LRARE104_%=:
s_nop 15
s_waitcnt vmcnt(0) lgkmcnt(0)
v_add_u32 v249, s74, v224
RD a[192:195],v249 offset:0
RD a[208:211],v249 offset:4096
RD a[224:227],v249 offset:8192
RD a[240:243],v249 offset:12288
v_add_u32 v250, s74, v225
RD a[196:199],v250 offset:0
RD a[212:215],v250 offset:4096
RD a[228:231],v250 offset:8192
RD a[244:247],v250 offset:12288
v_add_u32 v251, s74, v226
RD a[200:203],v251 offset:0
RD a[216:219],v251 offset:4096
RD a[232:235],v251 offset:8192
RD a[248:251],v251 offset:12288
v_add_u32 v254, s74, v227
RD a[204:207],v254 offset:0
RD a[220:223],v254 offset:4096
RD a[236:239],v254 offset:8192
RD a[252:255],v254 offset:12288
s_waitcnt lgkmcnt(0)
MM v[64:67],a[192:195],v[160:163],v[128:131]
MM v[64:67],a[196:199],v[176:179],v[64:67]
MM v[64:67],a[200:203],v[192:195],v[64:67]
MM v[64:67],a[204:207],v[208:211],v[64:67]
MM v[68:71],a[220:223],v[208:211],v[128:131]
MM v[68:71],a[216:219],v[192:195],v[68:71]
MM v[68:71],a[212:215],v[176:179],v[68:71]
MM v[68:71],a[208:211],v[160:163],v[68:71]
MM v[76:79],a[208:211],v[164:167],v[132:135]
MM v[76:79],a[212:215],v[180:183],v[76:79]
MM v[76:79],a[216:219],v[196:199],v[76:79]
MM v[76:79],a[220:223],v[212:215],v[76:79]
MM v[72:75],a[204:207],v[212:215],v[132:135]
MM v[72:75],a[200:203],v[196:199],v[72:75]
MM v[72:75],a[196:199],v[180:183],v[72:75]
MM v[72:75],a[192:195],v[164:167],v[72:75]
MM v[80:83],a[192:195],v[168:171],v[136:139]
MM v[80:83],a[196:199],v[184:187],v[80:83]
MM v[80:83],a[200:203],v[200:203],v[80:83]
MM v[80:83],a[204:207],v[216:219],v[80:83]
MM v[84:87],a[220:223],v[216:219],v[136:139]
MM v[84:87],a[216:219],v[200:203],v[84:87]
MM v[84:87],a[212:215],v[184:187],v[84:87]
MM v[84:87],a[208:211],v[168:171],v[84:87]
MM v[92:95],a[208:211],v[172:175],v[140:143]
MM v[92:95],a[212:215],v[188:191],v[92:95]
MM v[92:95],a[216:219],v[204:207],v[92:95]
MM v[92:95],a[220:223],v[220:223],v[92:95]
MM v[88:91],a[204:207],v[220:223],v[140:143]
MM v[88:91],a[200:203],v[204:207],v[88:91]
MM v[88:91],a[196:199],v[188:191],v[88:91]
MM v[88:91],a[192:195],v[172:175],v[88:91]
MM v[96:99],a[224:227],v[160:163],v[128:131]
MM v[96:99],a[228:231],v[176:179],v[96:99]
MM v[96:99],a[232:235],v[192:195],v[96:99]
MM v[96:99],a[236:239],v[208:211],v[96:99]
MM v[100:103],a[252:255],v[208:211],v[128:131]
MM v[100:103],a[248:251],v[192:195],v[100:103]
MM v[100:103],a[244:247],v[176:179],v[100:103]
MM v[100:103],a[240:243],v[160:163],v[100:103]
MM v[108:111],a[240:243],v[164:167],v[132:135]
MM v[108:111],a[244:247],v[180:183],v[108:111]
MM v[108:111],a[248:251],v[196:199],v[108:111]
MM v[108:111],a[252:255],v[212:215],v[108:111]
MM v[104:107],a[236:239],v[212:215],v[132:135]
MM v[104:107],a[232:235],v[196:199],v[104:107]
MM v[104:107],a[228:231],v[180:183],v[104:107]
MM v[104:107],a[224:227],v[164:167],v[104:107]
MM v[112:115],a[224:227],v[168:171],v[136:139]
MM v[112:115],a[228:231],v[184:187],v[112:115]
MM v[112:115],a[232:235],v[200:203],v[112:115]
MM v[112:115],a[236:239],v[216:219],v[112:115]
MM v[116:119],a[252:255],v[216:219],v[136:139]
MM v[116:119],a[248:251],v[200:203],v[116:119]
MM v[116:119],a[244:247],v[184:187],v[116:119]
MM v[116:119],a[240:243],v[168:171],v[116:119]
MM v[124:127],a[240:243],v[172:175],v[140:143]
MM v[124:127],a[244:247],v[188:191],v[124:127]
MM v[124:127],a[248:251],v[204:207],v[124:127]
MM v[124:127],a[252:255],v[220:223],v[124:127]
MM v[120:123],a[236:239],v[220:223],v[140:143]
MM v[120:123],a[232:235],v[204:207],v[120:123]
MM v[120:123],a[228:231],v[188:191],v[120:123]
MM v[120:123],a[224:227],v[172:175],v[120:123]
s_nop 15
v_add_u32 v249, s75, v224
RD a[192:195],v249 offset:0
RD a[208:211],v249 offset:4096
RD a[224:227],v249 offset:8192
RD a[240:243],v249 offset:12288
v_add_u32 v250, s75, v225
RD a[196:199],v250 offset:0
RD a[212:215],v250 offset:4096
RD a[228:231],v250 offset:8192
RD a[244:247],v250 offset:12288
v_add_u32 v251, s75, v226
RD a[200:203],v251 offset:0
RD a[216:219],v251 offset:4096
RD a[232:235],v251 offset:8192
RD a[248:251],v251 offset:12288
v_add_u32 v254, s75, v227
RD a[204:207],v254 offset:0
RD a[220:223],v254 offset:4096
RD a[236:239],v254 offset:8192
RD a[252:255],v254 offset:12288
s_waitcnt lgkmcnt(0)
v_max3_f32 v249, v64, v65, v66
v_max3_f32 v251, v96, v97, v98
v_max3_f32 v249, v249, v67, v68
v_max3_f32 v251, v251, v99, v100
v_max3_f32 v249, v249, v69, v70
v_max3_f32 v251, v251, v101, v102
v_max3_f32 v249, v249, v71, v103
v_max_f32 v249, v249, v251
v_mov_b32 v251, v249
s_nop 1
v_permlane16_swap_b32 v249, v251
v_max_f32 v249, v249, v251
v_mov_b32 v251, v249
s_nop 1
v_permlane32_swap_b32 v249, v251
v_max_f32 v249, v249, v251
v_log_f32 v250, v244
s_nop 0
v_max3_f32 v249, 0, v249, v250
v_sub_f32 v250, 0, v249
EX v250,v250
v_sub_f32 v128, v128, v249
v_sub_f32 v129, v129, v249
v_sub_f32 v130, v130, v249
v_sub_f32 v131, v131, v249
v_sub_f32 v0, v0, v249
v_sub_f32 v64, v64, v249
v_sub_f32 v1, v1, v249
v_sub_f32 v65, v65, v249
v_sub_f32 v2, v2, v249
v_sub_f32 v66, v66, v249
v_sub_f32 v3, v3, v249
v_sub_f32 v67, v67, v249
v_sub_f32 v4, v4, v249
v_sub_f32 v68, v68, v249
v_sub_f32 v5, v5, v249
v_sub_f32 v69, v69, v249
v_sub_f32 v6, v6, v249
v_sub_f32 v70, v70, v249
v_sub_f32 v7, v7, v249
v_sub_f32 v71, v71, v249
v_sub_f32 v32, v32, v249
v_sub_f32 v96, v96, v249
v_sub_f32 v33, v33, v249
v_sub_f32 v97, v97, v249
v_sub_f32 v34, v34, v249
v_sub_f32 v98, v98, v249
v_sub_f32 v35, v35, v249
v_sub_f32 v99, v99, v249
v_sub_f32 v36, v36, v249
v_sub_f32 v100, v100, v249
v_sub_f32 v37, v37, v249
v_sub_f32 v101, v101, v249
v_sub_f32 v38, v38, v249
v_sub_f32 v102, v102, v249
v_sub_f32 v39, v39, v249
v_sub_f32 v103, v103, v249
v_mul_f32 v144, v244, v250
v_mov_b32 v145, 0
v_bfe_u32 v249, v241, 0, 8
v_lshl_or_b32 v249, v249, 1, 1
v_lshlrev_b32 v249, 22, v249
v_and_b32 v241, 0xffffff00, v241
AR v254,a0
v_sub_f32 v254, v254, v249
v_mul_f32 v254, v254, v250
AW a0,v254
AR v251,a1
v_sub_f32 v251, v251, v249
v_mul_f32 v251, v251, v250
AW a1,v251
AR v254,a2
v_sub_f32 v254, v254, v249
v_mul_f32 v254, v254, v250
AW a2,v254
AR v251,a3
v_sub_f32 v251, v251, v249
v_mul_f32 v251, v251, v250
AW a3,v251
AR v254,a4
v_sub_f32 v254, v254, v249
v_mul_f32 v254, v254, v250
AW a4,v254
AR v251,a5
v_sub_f32 v251, v251, v249
v_mul_f32 v251, v251, v250
AW a5,v251
AR v254,a6
v_sub_f32 v254, v254, v249
v_mul_f32 v254, v254, v250
AW a6,v254
AR v251,a7
v_sub_f32 v251, v251, v249
v_mul_f32 v251, v251, v250
AW a7,v251
AR v254,a32
v_sub_f32 v254, v254, v249
v_mul_f32 v254, v254, v250
AW a32,v254
AR v251,a33
v_sub_f32 v251, v251, v249
v_mul_f32 v251, v251, v250
AW a33,v251
AR v254,a34
v_sub_f32 v254, v254, v249
v_mul_f32 v254, v254, v250
AW a34,v254
AR v251,a35
v_sub_f32 v251, v251, v249
v_mul_f32 v251, v251, v250
AW a35,v251
AR v254,a36
v_sub_f32 v254, v254, v249
v_mul_f32 v254, v254, v250
AW a36,v254
AR v251,a37
v_sub_f32 v251, v251, v249
v_mul_f32 v251, v251, v250
AW a37,v251
AR v254,a38
v_sub_f32 v254, v254, v249
v_mul_f32 v254, v254, v250
AW a38,v254
AR v251,a39
v_sub_f32 v251, v251, v249
v_mul_f32 v251, v251, v250
AW a39,v251
AR v254,a64
v_sub_f32 v254, v254, v249
v_mul_f32 v254, v254, v250
AW a64,v254
AR v251,a65
v_sub_f32 v251, v251, v249
v_mul_f32 v251, v251, v250
AW a65,v251
AR v254,a66
v_sub_f32 v254, v254, v249
v_mul_f32 v254, v254, v250
AW a66,v254
AR v251,a67
v_sub_f32 v251, v251, v249
v_mul_f32 v251, v251, v250
AW a67,v251
AR v254,a68
v_sub_f32 v254, v254, v249
v_mul_f32 v254, v254, v250
AW a68,v254
AR v251,a69
v_sub_f32 v251, v251, v249
v_mul_f32 v251, v251, v250
AW a69,v251
AR v254,a70
v_sub_f32 v254, v254, v249
v_mul_f32 v254, v254, v250
AW a70,v254
AR v251,a71
v_sub_f32 v251, v251, v249
v_mul_f32 v251, v251, v250
AW a71,v251
AR v254,a96
v_sub_f32 v254, v254, v249
v_mul_f32 v254, v254, v250
AW a96,v254
AR v251,a97
v_sub_f32 v251, v251, v249
v_mul_f32 v251, v251, v250
AW a97,v251
AR v254,a98
v_sub_f32 v254, v254, v249
v_mul_f32 v254, v254, v250
AW a98,v254
AR v251,a99
v_sub_f32 v251, v251, v249
v_mul_f32 v251, v251, v250
AW a99,v251
AR v254,a100
v_sub_f32 v254, v254, v249
v_mul_f32 v254, v254, v250
AW a100,v254
AR v251,a101
v_sub_f32 v251, v251, v249
v_mul_f32 v251, v251, v250
AW a101,v251
AR v254,a102
v_sub_f32 v254, v254, v249
v_mul_f32 v254, v254, v250
AW a102,v254
AR v251,a103
v_sub_f32 v251, v251, v249
v_mul_f32 v251, v251, v250
AW a103,v251
v_max3_f32 v249, v72, v73, v74
v_max3_f32 v251, v104, v105, v106
v_max3_f32 v249, v249, v75, v76
v_max3_f32 v251, v251, v107, v108
v_max3_f32 v249, v249, v77, v78
v_max3_f32 v251, v251, v109, v110
v_max3_f32 v249, v249, v79, v111
v_max_f32 v249, v249, v251
v_mov_b32 v251, v249
s_nop 1
v_permlane16_swap_b32 v249, v251
v_max_f32 v249, v249, v251
v_mov_b32 v251, v249
s_nop 1
v_permlane32_swap_b32 v249, v251
v_max_f32 v249, v249, v251
v_log_f32 v250, v245
s_nop 0
v_max3_f32 v249, 0, v249, v250
v_sub_f32 v250, 0, v249
EX v250,v250
v_sub_f32 v132, v132, v249
v_sub_f32 v133, v133, v249
v_sub_f32 v134, v134, v249
v_sub_f32 v135, v135, v249
v_sub_f32 v8, v8, v249
v_sub_f32 v72, v72, v249
v_sub_f32 v9, v9, v249
v_sub_f32 v73, v73, v249
v_sub_f32 v10, v10, v249
v_sub_f32 v74, v74, v249
v_sub_f32 v11, v11, v249
v_sub_f32 v75, v75, v249
v_sub_f32 v12, v12, v249
v_sub_f32 v76, v76, v249
v_sub_f32 v13, v13, v249
v_sub_f32 v77, v77, v249
v_sub_f32 v14, v14, v249
v_sub_f32 v78, v78, v249
v_sub_f32 v15, v15, v249
v_sub_f32 v79, v79, v249
v_sub_f32 v40, v40, v249
v_sub_f32 v104, v104, v249
v_sub_f32 v41, v41, v249
v_sub_f32 v105, v105, v249
v_sub_f32 v42, v42, v249
v_sub_f32 v106, v106, v249
v_sub_f32 v43, v43, v249
v_sub_f32 v107, v107, v249
v_sub_f32 v44, v44, v249
v_sub_f32 v108, v108, v249
v_sub_f32 v45, v45, v249
v_sub_f32 v109, v109, v249
v_sub_f32 v46, v46, v249
v_sub_f32 v110, v110, v249
v_sub_f32 v47, v47, v249
v_sub_f32 v111, v111, v249
v_mul_f32 v148, v245, v250
v_mov_b32 v149, 0
v_bfe_u32 v249, v241, 8, 8
v_lshl_or_b32 v249, v249, 1, 1
v_lshlrev_b32 v249, 22, v249
v_and_b32 v241, 0xffff00ff, v241
AR v254,a8
v_sub_f32 v254, v254, v249
v_mul_f32 v254, v254, v250
AW a8,v254
AR v251,a9
v_sub_f32 v251, v251, v249
v_mul_f32 v251, v251, v250
AW a9,v251
AR v254,a10
v_sub_f32 v254, v254, v249
v_mul_f32 v254, v254, v250
AW a10,v254
AR v251,a11
v_sub_f32 v251, v251, v249
v_mul_f32 v251, v251, v250
AW a11,v251
AR v254,a12
v_sub_f32 v254, v254, v249
v_mul_f32 v254, v254, v250
AW a12,v254
AR v251,a13
v_sub_f32 v251, v251, v249
v_mul_f32 v251, v251, v250
AW a13,v251
AR v254,a14
v_sub_f32 v254, v254, v249
v_mul_f32 v254, v254, v250
AW a14,v254
AR v251,a15
v_sub_f32 v251, v251, v249
v_mul_f32 v251, v251, v250
AW a15,v251
AR v254,a40
v_sub_f32 v254, v254, v249
v_mul_f32 v254, v254, v250
AW a40,v254
AR v251,a41
v_sub_f32 v251, v251, v249
v_mul_f32 v251, v251, v250
AW a41,v251
AR v254,a42
v_sub_f32 v254, v254, v249
v_mul_f32 v254, v254, v250
AW a42,v254
AR v251,a43
v_sub_f32 v251, v251, v249
v_mul_f32 v251, v251, v250
AW a43,v251
AR v254,a44
v_sub_f32 v254, v254, v249
v_mul_f32 v254, v254, v250
AW a44,v254
AR v251,a45
v_sub_f32 v251, v251, v249
v_mul_f32 v251, v251, v250
AW a45,v251
AR v254,a46
v_sub_f32 v254, v254, v249
v_mul_f32 v254, v254, v250
AW a46,v254
AR v251,a47
v_sub_f32 v251, v251, v249
v_mul_f32 v251, v251, v250
AW a47,v251
AR v254,a72
v_sub_f32 v254, v254, v249
v_mul_f32 v254, v254, v250
AW a72,v254
AR v251,a73
v_sub_f32 v251, v251, v249
v_mul_f32 v251, v251, v250
AW a73,v251
AR v254,a74
v_sub_f32 v254, v254, v249
v_mul_f32 v254, v254, v250
AW a74,v254
AR v251,a75
v_sub_f32 v251, v251, v249
v_mul_f32 v251, v251, v250
AW a75,v251
AR v254,a76
v_sub_f32 v254, v254, v249
v_mul_f32 v254, v254, v250
AW a76,v254
AR v251,a77
v_sub_f32 v251, v251, v249
v_mul_f32 v251, v251, v250
AW a77,v251
)ASM"
R"ASM(
AR v254,a78
v_sub_f32 v254, v254, v249
v_mul_f32 v254, v254, v250
AW a78,v254
AR v251,a79
v_sub_f32 v251, v251, v249
v_mul_f32 v251, v251, v250
AW a79,v251
AR v254,a104
v_sub_f32 v254, v254, v249
v_mul_f32 v254, v254, v250
AW a104,v254
AR v251,a105
v_sub_f32 v251, v251, v249
v_mul_f32 v251, v251, v250
AW a105,v251
AR v254,a106
v_sub_f32 v254, v254, v249
v_mul_f32 v254, v254, v250
AW a106,v254
AR v251,a107
v_sub_f32 v251, v251, v249
v_mul_f32 v251, v251, v250
AW a107,v251
AR v254,a108
v_sub_f32 v254, v254, v249
v_mul_f32 v254, v254, v250
AW a108,v254
AR v251,a109
v_sub_f32 v251, v251, v249
v_mul_f32 v251, v251, v250
AW a109,v251
AR v254,a110
v_sub_f32 v254, v254, v249
v_mul_f32 v254, v254, v250
AW a110,v254
AR v251,a111
v_sub_f32 v251, v251, v249
v_mul_f32 v251, v251, v250
AW a111,v251
v_max3_f32 v249, v80, v81, v82
v_max3_f32 v251, v112, v113, v114
v_max3_f32 v249, v249, v83, v84
v_max3_f32 v251, v251, v115, v116
v_max3_f32 v249, v249, v85, v86
v_max3_f32 v251, v251, v117, v118
v_max3_f32 v249, v249, v87, v119
v_max_f32 v249, v249, v251
v_mov_b32 v251, v249
s_nop 1
v_permlane16_swap_b32 v249, v251
v_max_f32 v249, v249, v251
v_mov_b32 v251, v249
s_nop 1
v_permlane32_swap_b32 v249, v251
v_max_f32 v249, v249, v251
v_log_f32 v250, v246
s_nop 0
v_max3_f32 v249, 0, v249, v250
v_sub_f32 v250, 0, v249
EX v250,v250
v_sub_f32 v136, v136, v249
v_sub_f32 v137, v137, v249
v_sub_f32 v138, v138, v249
v_sub_f32 v139, v139, v249
v_sub_f32 v16, v16, v249
v_sub_f32 v80, v80, v249
v_sub_f32 v17, v17, v249
v_sub_f32 v81, v81, v249
v_sub_f32 v18, v18, v249
v_sub_f32 v82, v82, v249
v_sub_f32 v19, v19, v249
v_sub_f32 v83, v83, v249
v_sub_f32 v20, v20, v249
v_sub_f32 v84, v84, v249
v_sub_f32 v21, v21, v249
v_sub_f32 v85, v85, v249
v_sub_f32 v22, v22, v249
v_sub_f32 v86, v86, v249
v_sub_f32 v23, v23, v249
v_sub_f32 v87, v87, v249
v_sub_f32 v48, v48, v249
v_sub_f32 v112, v112, v249
v_sub_f32 v49, v49, v249
v_sub_f32 v113, v113, v249
v_sub_f32 v50, v50, v249
v_sub_f32 v114, v114, v249
v_sub_f32 v51, v51, v249
v_sub_f32 v115, v115, v249
v_sub_f32 v52, v52, v249
v_sub_f32 v116, v116, v249
v_sub_f32 v53, v53, v249
v_sub_f32 v117, v117, v249
v_sub_f32 v54, v54, v249
v_sub_f32 v118, v118, v249
v_sub_f32 v55, v55, v249
v_sub_f32 v119, v119, v249
v_mul_f32 v152, v246, v250
v_mov_b32 v153, 0
v_bfe_u32 v249, v241, 16, 8
v_lshl_or_b32 v249, v249, 1, 1
v_lshlrev_b32 v249, 22, v249
v_and_b32 v241, 0xff00ffff, v241
AR v254,a16
v_sub_f32 v254, v254, v249
v_mul_f32 v254, v254, v250
AW a16,v254
AR v251,a17
v_sub_f32 v251, v251, v249
v_mul_f32 v251, v251, v250
AW a17,v251
AR v254,a18
v_sub_f32 v254, v254, v249
v_mul_f32 v254, v254, v250
AW a18,v254
AR v251,a19
v_sub_f32 v251, v251, v249
v_mul_f32 v251, v251, v250
AW a19,v251
AR v254,a20
v_sub_f32 v254, v254, v249
v_mul_f32 v254, v254, v250
AW a20,v254
AR v251,a21
v_sub_f32 v251, v251, v249
v_mul_f32 v251, v251, v250
AW a21,v251
AR v254,a22
v_sub_f32 v254, v254, v249
v_mul_f32 v254, v254, v250
AW a22,v254
AR v251,a23
v_sub_f32 v251, v251, v249
v_mul_f32 v251, v251, v250
AW a23,v251
AR v254,a48
v_sub_f32 v254, v254, v249
v_mul_f32 v254, v254, v250
AW a48,v254
AR v251,a49
v_sub_f32 v251, v251, v249
v_mul_f32 v251, v251, v250
AW a49,v251
AR v254,a50
v_sub_f32 v254, v254, v249
v_mul_f32 v254, v254, v250
AW a50,v254
AR v251,a51
v_sub_f32 v251, v251, v249
v_mul_f32 v251, v251, v250
AW a51,v251
AR v254,a52
v_sub_f32 v254, v254, v249
v_mul_f32 v254, v254, v250
AW a52,v254
AR v251,a53
v_sub_f32 v251, v251, v249
v_mul_f32 v251, v251, v250
AW a53,v251
AR v254,a54
v_sub_f32 v254, v254, v249
v_mul_f32 v254, v254, v250
AW a54,v254
AR v251,a55
v_sub_f32 v251, v251, v249
v_mul_f32 v251, v251, v250
AW a55,v251
AR v254,a80
v_sub_f32 v254, v254, v249
v_mul_f32 v254, v254, v250
AW a80,v254
AR v251,a81
v_sub_f32 v251, v251, v249
v_mul_f32 v251, v251, v250
AW a81,v251
AR v254,a82
v_sub_f32 v254, v254, v249
v_mul_f32 v254, v254, v250
AW a82,v254
AR v251,a83
v_sub_f32 v251, v251, v249
v_mul_f32 v251, v251, v250
AW a83,v251
AR v254,a84
v_sub_f32 v254, v254, v249
v_mul_f32 v254, v254, v250
AW a84,v254
AR v251,a85
v_sub_f32 v251, v251, v249
v_mul_f32 v251, v251, v250
AW a85,v251
AR v254,a86
v_sub_f32 v254, v254, v249
v_mul_f32 v254, v254, v250
AW a86,v254
AR v251,a87
v_sub_f32 v251, v251, v249
v_mul_f32 v251, v251, v250
AW a87,v251
AR v254,a112
v_sub_f32 v254, v254, v249
v_mul_f32 v254, v254, v250
AW a112,v254
AR v251,a113
v_sub_f32 v251, v251, v249
v_mul_f32 v251, v251, v250
AW a113,v251
AR v254,a114
v_sub_f32 v254, v254, v249
v_mul_f32 v254, v254, v250
AW a114,v254
AR v251,a115
v_sub_f32 v251, v251, v249
v_mul_f32 v251, v251, v250
AW a115,v251
AR v254,a116
v_sub_f32 v254, v254, v249
v_mul_f32 v254, v254, v250
AW a116,v254
AR v251,a117
v_sub_f32 v251, v251, v249
v_mul_f32 v251, v251, v250
AW a117,v251
AR v254,a118
v_sub_f32 v254, v254, v249
v_mul_f32 v254, v254, v250
AW a118,v254
AR v251,a119
v_sub_f32 v251, v251, v249
v_mul_f32 v251, v251, v250
AW a119,v251
v_max3_f32 v249, v88, v89, v90
v_max3_f32 v251, v120, v121, v122
v_max3_f32 v249, v249, v91, v92
v_max3_f32 v251, v251, v123, v124
v_max3_f32 v249, v249, v93, v94
v_max3_f32 v251, v251, v125, v126
v_max3_f32 v249, v249, v95, v127
v_max_f32 v249, v249, v251
v_mov_b32 v251, v249
s_nop 1
v_permlane16_swap_b32 v249, v251
v_max_f32 v249, v249, v251
v_mov_b32 v251, v249
s_nop 1
v_permlane32_swap_b32 v249, v251
v_max_f32 v249, v249, v251
v_log_f32 v250, v247
s_nop 0
v_max3_f32 v249, 0, v249, v250
v_sub_f32 v250, 0, v249
EX v250,v250
v_sub_f32 v140, v140, v249
v_sub_f32 v141, v141, v249
v_sub_f32 v142, v142, v249
v_sub_f32 v143, v143, v249
v_sub_f32 v24, v24, v249
v_sub_f32 v88, v88, v249
v_sub_f32 v25, v25, v249
v_sub_f32 v89, v89, v249
v_sub_f32 v26, v26, v249
v_sub_f32 v90, v90, v249
v_sub_f32 v27, v27, v249
v_sub_f32 v91, v91, v249
v_sub_f32 v28, v28, v249
v_sub_f32 v92, v92, v249
v_sub_f32 v29, v29, v249
v_sub_f32 v93, v93, v249
v_sub_f32 v30, v30, v249
v_sub_f32 v94, v94, v249
v_sub_f32 v31, v31, v249
v_sub_f32 v95, v95, v249
v_sub_f32 v56, v56, v249
v_sub_f32 v120, v120, v249
v_sub_f32 v57, v57, v249
v_sub_f32 v121, v121, v249
v_sub_f32 v58, v58, v249
v_sub_f32 v122, v122, v249
v_sub_f32 v59, v59, v249
v_sub_f32 v123, v123, v249
v_sub_f32 v60, v60, v249
v_sub_f32 v124, v124, v249
v_sub_f32 v61, v61, v249
v_sub_f32 v125, v125, v249
v_sub_f32 v62, v62, v249
v_sub_f32 v126, v126, v249
v_sub_f32 v63, v63, v249
v_sub_f32 v127, v127, v249
v_mul_f32 v156, v247, v250
v_mov_b32 v157, 0
v_bfe_u32 v249, v241, 24, 8
v_lshl_or_b32 v249, v249, 1, 1
v_lshlrev_b32 v249, 22, v249
v_and_b32 v241, 0xffffff, v241
AR v254,a24
v_sub_f32 v254, v254, v249
v_mul_f32 v254, v254, v250
AW a24,v254
AR v251,a25
v_sub_f32 v251, v251, v249
v_mul_f32 v251, v251, v250
AW a25,v251
AR v254,a26
v_sub_f32 v254, v254, v249
v_mul_f32 v254, v254, v250
AW a26,v254
AR v251,a27
v_sub_f32 v251, v251, v249
v_mul_f32 v251, v251, v250
AW a27,v251
AR v254,a28
v_sub_f32 v254, v254, v249
v_mul_f32 v254, v254, v250
AW a28,v254
AR v251,a29
v_sub_f32 v251, v251, v249
v_mul_f32 v251, v251, v250
AW a29,v251
AR v254,a30
v_sub_f32 v254, v254, v249
v_mul_f32 v254, v254, v250
AW a30,v254
AR v251,a31
v_sub_f32 v251, v251, v249
v_mul_f32 v251, v251, v250
AW a31,v251
AR v254,a56
v_sub_f32 v254, v254, v249
v_mul_f32 v254, v254, v250
AW a56,v254
AR v251,a57
v_sub_f32 v251, v251, v249
v_mul_f32 v251, v251, v250
AW a57,v251
AR v254,a58
v_sub_f32 v254, v254, v249
v_mul_f32 v254, v254, v250
AW a58,v254
AR v251,a59
v_sub_f32 v251, v251, v249
v_mul_f32 v251, v251, v250
AW a59,v251
AR v254,a60
v_sub_f32 v254, v254, v249
v_mul_f32 v254, v254, v250
AW a60,v254
AR v251,a61
v_sub_f32 v251, v251, v249
v_mul_f32 v251, v251, v250
AW a61,v251
AR v254,a62
v_sub_f32 v254, v254, v249
v_mul_f32 v254, v254, v250
AW a62,v254
AR v251,a63
v_sub_f32 v251, v251, v249
v_mul_f32 v251, v251, v250
AW a63,v251
AR v254,a88
v_sub_f32 v254, v254, v249
v_mul_f32 v254, v254, v250
AW a88,v254
AR v251,a89
v_sub_f32 v251, v251, v249
v_mul_f32 v251, v251, v250
AW a89,v251
AR v254,a90
v_sub_f32 v254, v254, v249
v_mul_f32 v254, v254, v250
AW a90,v254
AR v251,a91
v_sub_f32 v251, v251, v249
v_mul_f32 v251, v251, v250
AW a91,v251
AR v254,a92
v_sub_f32 v254, v254, v249
v_mul_f32 v254, v254, v250
AW a92,v254
AR v251,a93
v_sub_f32 v251, v251, v249
v_mul_f32 v251, v251, v250
AW a93,v251
AR v254,a94
v_sub_f32 v254, v254, v249
v_mul_f32 v254, v254, v250
AW a94,v254
AR v251,a95
v_sub_f32 v251, v251, v249
v_mul_f32 v251, v251, v250
AW a95,v251
AR v254,a120
v_sub_f32 v254, v254, v249
v_mul_f32 v254, v254, v250
AW a120,v254
AR v251,a121
v_sub_f32 v251, v251, v249
v_mul_f32 v251, v251, v250
AW a121,v251
AR v254,a122
v_sub_f32 v254, v254, v249
v_mul_f32 v254, v254, v250
AW a122,v254
AR v251,a123
v_sub_f32 v251, v251, v249
v_mul_f32 v251, v251, v250
AW a123,v251
AR v254,a124
v_sub_f32 v254, v254, v249
v_mul_f32 v254, v254, v250
AW a124,v254
AR v251,a125
v_sub_f32 v251, v251, v249
v_mul_f32 v251, v251, v250
AW a125,v251
AR v254,a126
v_sub_f32 v254, v254, v249
v_mul_f32 v254, v254, v250
AW a126,v254
AR v251,a127
v_sub_f32 v251, v251, v249
v_mul_f32 v251, v251, v250
AW a127,v251
EX v64,v64
EX v65,v65
EX v66,v66
EX v67,v67
EX v68,v68
EX v69,v69
EX v70,v70
EX v71,v71
EX v72,v72
EX v73,v73
EX v74,v74
EX v75,v75
EX v76,v76
EX v77,v77
EX v78,v78
EX v79,v79
EX v80,v80
EX v81,v81
EX v82,v82
EX v83,v83
EX v84,v84
EX v85,v85
EX v86,v86
EX v87,v87
EX v88,v88
EX v89,v89
EX v90,v90
EX v91,v91
EX v92,v92
EX v93,v93
EX v94,v94
EX v95,v95
EX v96,v96
EX v97,v97
EX v98,v98
EX v99,v99
EX v100,v100
EX v101,v101
EX v102,v102
EX v103,v103
EX v104,v104
EX v105,v105
EX v106,v106
EX v107,v107
EX v108,v108
EX v109,v109
EX v110,v110
EX v111,v111
EX v112,v112
EX v113,v113
EX v114,v114
EX v115,v115
EX v116,v116
EX v117,v117
EX v118,v118
EX v119,v119
EX v120,v120
EX v121,v121
EX v122,v122
EX v123,v123
EX v124,v124
EX v125,v125
EX v126,v126
EX v127,v127
CV v64,v64,v65
CV v65,v66,v67
CV v66,v68,v69
CV v67,v70,v71
CV v72,v72,v73
CV v73,v74,v75
CV v74,v76,v77
CV v75,v78,v79
CV v80,v80,v81
CV v81,v82,v83
CV v82,v84,v85
CV v83,v86,v87
CV v88,v88,v89
CV v89,v90,v91
CV v90,v92,v93
CV v91,v94,v95
CV v68,v96,v97
CV v69,v98,v99
CV v70,v100,v101
CV v71,v102,v103
CV v76,v104,v105
CV v77,v106,v107
CV v78,v108,v109
CV v79,v110,v111
CV v84,v112,v113
CV v85,v114,v115
CV v86,v116,v117
CV v87,v118,v119
CV v92,v120,v121
CV v93,v122,v123
CV v94,v124,v125
CV v95,v126,v127
v_smfmac_f32_16x16x64_bf16 v[144:147], v[236:239], v[64:71], v234
v_smfmac_f32_16x16x64_bf16 v[148:151], v[236:239], v[72:79], v234
v_smfmac_f32_16x16x64_bf16 v[152:155], v[236:239], v[80:87], v234
v_smfmac_f32_16x16x64_bf16 v[156:159], v[236:239], v[88:95], v234
s_nop 9
s_branch LRET_%=
LRARE004_%=:
s_nop 15
s_waitcnt vmcnt(0) lgkmcnt(0)
v_add_u32 v249, s74, v224
RD a[192:195],v249 offset:0
RD a[208:211],v249 offset:4096
RD a[224:227],v249 offset:8192
RD a[240:243],v249 offset:12288
v_add_u32 v250, s74, v225
RD a[196:199],v250 offset:0
RD a[212:215],v250 offset:4096
RD a[228:231],v250 offset:8192
RD a[244:247],v250 offset:12288
v_add_u32 v251, s74, v226
RD a[200:203],v251 offset:0
RD a[216:219],v251 offset:4096
RD a[232:235],v251 offset:8192
RD a[248:251],v251 offset:12288
v_add_u32 v254, s74, v227
RD a[204:207],v254 offset:0
RD a[220:223],v254 offset:4096
RD a[236:239],v254 offset:8192
RD a[252:255],v254 offset:12288
s_waitcnt lgkmcnt(0)
MM v[0:3],a[192:195],v[160:163],v[128:131]
MM v[0:3],a[196:199],v[176:179],v[0:3]
MM v[0:3],a[200:203],v[192:195],v[0:3]
MM v[0:3],a[204:207],v[208:211],v[0:3]
MM v[4:7],a[220:223],v[208:211],v[128:131]
MM v[4:7],a[216:219],v[192:195],v[4:7]
MM v[4:7],a[212:215],v[176:179],v[4:7]
MM v[4:7],a[208:211],v[160:163],v[4:7]
MM v[12:15],a[208:211],v[164:167],v[132:135]
MM v[12:15],a[212:215],v[180:183],v[12:15]
MM v[12:15],a[216:219],v[196:199],v[12:15]
MM v[12:15],a[220:223],v[212:215],v[12:15]
MM v[8:11],a[204:207],v[212:215],v[132:135]
MM v[8:11],a[200:203],v[196:199],v[8:11]
MM v[8:11],a[196:199],v[180:183],v[8:11]
MM v[8:11],a[192:195],v[164:167],v[8:11]
MM v[16:19],a[192:195],v[168:171],v[136:139]
MM v[16:19],a[196:199],v[184:187],v[16:19]
)ASM"
R"ASM(
MM v[16:19],a[200:203],v[200:203],v[16:19]
MM v[16:19],a[204:207],v[216:219],v[16:19]
MM v[20:23],a[220:223],v[216:219],v[136:139]
MM v[20:23],a[216:219],v[200:203],v[20:23]
MM v[20:23],a[212:215],v[184:187],v[20:23]
MM v[20:23],a[208:211],v[168:171],v[20:23]
MM v[28:31],a[208:211],v[172:175],v[140:143]
MM v[28:31],a[212:215],v[188:191],v[28:31]
MM v[28:31],a[216:219],v[204:207],v[28:31]
MM v[28:31],a[220:223],v[220:223],v[28:31]
MM v[24:27],a[204:207],v[220:223],v[140:143]
MM v[24:27],a[200:203],v[204:207],v[24:27]
MM v[24:27],a[196:199],v[188:191],v[24:27]
MM v[24:27],a[192:195],v[172:175],v[24:27]
MM v[32:35],a[224:227],v[160:163],v[128:131]
MM v[32:35],a[228:231],v[176:179],v[32:35]
MM v[32:35],a[232:235],v[192:195],v[32:35]
MM v[32:35],a[236:239],v[208:211],v[32:35]
MM v[36:39],a[252:255],v[208:211],v[128:131]
MM v[36:39],a[248:251],v[192:195],v[36:39]
MM v[36:39],a[244:247],v[176:179],v[36:39]
MM v[36:39],a[240:243],v[160:163],v[36:39]
MM v[44:47],a[240:243],v[164:167],v[132:135]
MM v[44:47],a[244:247],v[180:183],v[44:47]
MM v[44:47],a[248:251],v[196:199],v[44:47]
MM v[44:47],a[252:255],v[212:215],v[44:47]
MM v[40:43],a[236:239],v[212:215],v[132:135]
MM v[40:43],a[232:235],v[196:199],v[40:43]
MM v[40:43],a[228:231],v[180:183],v[40:43]
MM v[40:43],a[224:227],v[164:167],v[40:43]
MM v[48:51],a[224:227],v[168:171],v[136:139]
MM v[48:51],a[228:231],v[184:187],v[48:51]
MM v[48:51],a[232:235],v[200:203],v[48:51]
MM v[48:51],a[236:239],v[216:219],v[48:51]
MM v[52:55],a[252:255],v[216:219],v[136:139]
MM v[52:55],a[248:251],v[200:203],v[52:55]
MM v[52:55],a[244:247],v[184:187],v[52:55]
MM v[52:55],a[240:243],v[168:171],v[52:55]
MM v[60:63],a[240:243],v[172:175],v[140:143]
MM v[60:63],a[244:247],v[188:191],v[60:63]
MM v[60:63],a[248:251],v[204:207],v[60:63]
MM v[60:63],a[252:255],v[220:223],v[60:63]
MM v[56:59],a[236:239],v[220:223],v[140:143]
MM v[56:59],a[232:235],v[204:207],v[56:59]
MM v[56:59],a[228:231],v[188:191],v[56:59]
MM v[56:59],a[224:227],v[172:175],v[56:59]
s_nop 15
v_add_u32 v249, s75, v224
RD a[192:195],v249 offset:0
RD a[208:211],v249 offset:4096
RD a[224:227],v249 offset:8192
RD a[240:243],v249 offset:12288
v_add_u32 v250, s75, v225
RD a[196:199],v250 offset:0
RD a[212:215],v250 offset:4096
RD a[228:231],v250 offset:8192
RD a[244:247],v250 offset:12288
v_add_u32 v251, s75, v226
RD a[200:203],v251 offset:0
RD a[216:219],v251 offset:4096
RD a[232:235],v251 offset:8192
RD a[248:251],v251 offset:12288
v_add_u32 v254, s75, v227
RD a[204:207],v254 offset:0
RD a[220:223],v254 offset:4096
RD a[236:239],v254 offset:8192
RD a[252:255],v254 offset:12288
s_waitcnt lgkmcnt(0)
v_max3_f32 v249, v0, v1, v2
v_max3_f32 v251, v32, v33, v34
v_max3_f32 v249, v249, v3, v4
v_max3_f32 v251, v251, v35, v36
v_max3_f32 v249, v249, v5, v6
v_max3_f32 v251, v251, v37, v38
v_max3_f32 v249, v249, v7, v39
v_max_f32 v249, v249, v251
v_mov_b32 v251, v249
s_nop 1
v_permlane16_swap_b32 v249, v251
v_max_f32 v249, v249, v251
v_mov_b32 v251, v249
s_nop 1
v_permlane32_swap_b32 v249, v251
v_max_f32 v249, v249, v251
v_log_f32 v250, v244
s_nop 0
v_max3_f32 v249, 0, v249, v250
v_sub_f32 v250, 0, v249
EX v250,v250
v_sub_f32 v128, v128, v249
v_sub_f32 v129, v129, v249
v_sub_f32 v130, v130, v249
v_sub_f32 v131, v131, v249
v_sub_f32 v64, v64, v249
v_sub_f32 v0, v0, v249
v_sub_f32 v65, v65, v249
v_sub_f32 v1, v1, v249
v_sub_f32 v66, v66, v249
v_sub_f32 v2, v2, v249
v_sub_f32 v67, v67, v249
v_sub_f32 v3, v3, v249
v_sub_f32 v68, v68, v249
v_sub_f32 v4, v4, v249
v_sub_f32 v69, v69, v249
v_sub_f32 v5, v5, v249
v_sub_f32 v70, v70, v249
v_sub_f32 v6, v6, v249
v_sub_f32 v71, v71, v249
v_sub_f32 v7, v7, v249
v_sub_f32 v96, v96, v249
v_sub_f32 v32, v32, v249
v_sub_f32 v97, v97, v249
v_sub_f32 v33, v33, v249
v_sub_f32 v98, v98, v249
v_sub_f32 v34, v34, v249
v_sub_f32 v99, v99, v249
v_sub_f32 v35, v35, v249
v_sub_f32 v100, v100, v249
v_sub_f32 v36, v36, v249
v_sub_f32 v101, v101, v249
v_sub_f32 v37, v37, v249
v_sub_f32 v102, v102, v249
v_sub_f32 v38, v38, v249
v_sub_f32 v103, v103, v249
v_sub_f32 v39, v39, v249
v_mul_f32 v144, v244, v250
v_mov_b32 v145, 0
v_bfe_u32 v249, v241, 0, 8
v_lshl_or_b32 v249, v249, 1, 1
v_lshlrev_b32 v249, 22, v249
v_and_b32 v241, 0xffffff00, v241
AR v254,a0
v_sub_f32 v254, v254, v249
v_mul_f32 v254, v254, v250
AW a0,v254
AR v251,a1
v_sub_f32 v251, v251, v249
v_mul_f32 v251, v251, v250
AW a1,v251
AR v254,a2
v_sub_f32 v254, v254, v249
v_mul_f32 v254, v254, v250
AW a2,v254
AR v251,a3
v_sub_f32 v251, v251, v249
v_mul_f32 v251, v251, v250
AW a3,v251
AR v254,a4
v_sub_f32 v254, v254, v249
v_mul_f32 v254, v254, v250
AW a4,v254
AR v251,a5
v_sub_f32 v251, v251, v249
v_mul_f32 v251, v251, v250
AW a5,v251
AR v254,a6
v_sub_f32 v254, v254, v249
v_mul_f32 v254, v254, v250
AW a6,v254
AR v251,a7
v_sub_f32 v251, v251, v249
v_mul_f32 v251, v251, v250
AW a7,v251
AR v254,a32
v_sub_f32 v254, v254, v249
v_mul_f32 v254, v254, v250
AW a32,v254
AR v251,a33
v_sub_f32 v251, v251, v249
v_mul_f32 v251, v251, v250
AW a33,v251
AR v254,a34
v_sub_f32 v254, v254, v249
v_mul_f32 v254, v254, v250
AW a34,v254
AR v251,a35
v_sub_f32 v251, v251, v249
v_mul_f32 v251, v251, v250
AW a35,v251
AR v254,a36
v_sub_f32 v254, v254, v249
v_mul_f32 v254, v254, v250
AW a36,v254
AR v251,a37
v_sub_f32 v251, v251, v249
v_mul_f32 v251, v251, v250
AW a37,v251
AR v254,a38
v_sub_f32 v254, v254, v249
v_mul_f32 v254, v254, v250
AW a38,v254
AR v251,a39
v_sub_f32 v251, v251, v249
v_mul_f32 v251, v251, v250
AW a39,v251
AR v254,a64
v_sub_f32 v254, v254, v249
v_mul_f32 v254, v254, v250
AW a64,v254
AR v251,a65
v_sub_f32 v251, v251, v249
v_mul_f32 v251, v251, v250
AW a65,v251
AR v254,a66
v_sub_f32 v254, v254, v249
v_mul_f32 v254, v254, v250
AW a66,v254
AR v251,a67
v_sub_f32 v251, v251, v249
v_mul_f32 v251, v251, v250
AW a67,v251
AR v254,a68
v_sub_f32 v254, v254, v249
v_mul_f32 v254, v254, v250
AW a68,v254
AR v251,a69
v_sub_f32 v251, v251, v249
v_mul_f32 v251, v251, v250
AW a69,v251
AR v254,a70
v_sub_f32 v254, v254, v249
v_mul_f32 v254, v254, v250
AW a70,v254
AR v251,a71
v_sub_f32 v251, v251, v249
v_mul_f32 v251, v251, v250
AW a71,v251
AR v254,a96
v_sub_f32 v254, v254, v249
v_mul_f32 v254, v254, v250
AW a96,v254
AR v251,a97
v_sub_f32 v251, v251, v249
v_mul_f32 v251, v251, v250
AW a97,v251
AR v254,a98
v_sub_f32 v254, v254, v249
v_mul_f32 v254, v254, v250
AW a98,v254
AR v251,a99
v_sub_f32 v251, v251, v249
v_mul_f32 v251, v251, v250
AW a99,v251
AR v254,a100
v_sub_f32 v254, v254, v249
v_mul_f32 v254, v254, v250
AW a100,v254
AR v251,a101
v_sub_f32 v251, v251, v249
v_mul_f32 v251, v251, v250
AW a101,v251
AR v254,a102
v_sub_f32 v254, v254, v249
v_mul_f32 v254, v254, v250
AW a102,v254
AR v251,a103
v_sub_f32 v251, v251, v249
v_mul_f32 v251, v251, v250
AW a103,v251
v_max3_f32 v249, v8, v9, v10
v_max3_f32 v251, v40, v41, v42
v_max3_f32 v249, v249, v11, v12
v_max3_f32 v251, v251, v43, v44
v_max3_f32 v249, v249, v13, v14
v_max3_f32 v251, v251, v45, v46
v_max3_f32 v249, v249, v15, v47
v_max_f32 v249, v249, v251
v_mov_b32 v251, v249
s_nop 1
v_permlane16_swap_b32 v249, v251
v_max_f32 v249, v249, v251
v_mov_b32 v251, v249
s_nop 1
v_permlane32_swap_b32 v249, v251
v_max_f32 v249, v249, v251
v_log_f32 v250, v245
s_nop 0
v_max3_f32 v249, 0, v249, v250
v_sub_f32 v250, 0, v249
EX v250,v250
v_sub_f32 v132, v132, v249
v_sub_f32 v133, v133, v249
v_sub_f32 v134, v134, v249
v_sub_f32 v135, v135, v249
v_sub_f32 v72, v72, v249
v_sub_f32 v8, v8, v249
v_sub_f32 v73, v73, v249
v_sub_f32 v9, v9, v249
v_sub_f32 v74, v74, v249
v_sub_f32 v10, v10, v249
v_sub_f32 v75, v75, v249
v_sub_f32 v11, v11, v249
v_sub_f32 v76, v76, v249
v_sub_f32 v12, v12, v249
v_sub_f32 v77, v77, v249
v_sub_f32 v13, v13, v249
v_sub_f32 v78, v78, v249
v_sub_f32 v14, v14, v249
v_sub_f32 v79, v79, v249
v_sub_f32 v15, v15, v249
v_sub_f32 v104, v104, v249
v_sub_f32 v40, v40, v249
v_sub_f32 v105, v105, v249
v_sub_f32 v41, v41, v249
v_sub_f32 v106, v106, v249
v_sub_f32 v42, v42, v249
v_sub_f32 v107, v107, v249
v_sub_f32 v43, v43, v249
v_sub_f32 v108, v108, v249
v_sub_f32 v44, v44, v249
v_sub_f32 v109, v109, v249
v_sub_f32 v45, v45, v249
v_sub_f32 v110, v110, v249
v_sub_f32 v46, v46, v249
v_sub_f32 v111, v111, v249
v_sub_f32 v47, v47, v249
v_mul_f32 v148, v245, v250
v_mov_b32 v149, 0
v_bfe_u32 v249, v241, 8, 8
v_lshl_or_b32 v249, v249, 1, 1
v_lshlrev_b32 v249, 22, v249
v_and_b32 v241, 0xffff00ff, v241
AR v254,a8
v_sub_f32 v254, v254, v249
v_mul_f32 v254, v254, v250
AW a8,v254
AR v251,a9
v_sub_f32 v251, v251, v249
v_mul_f32 v251, v251, v250
AW a9,v251
AR v254,a10
v_sub_f32 v254, v254, v249
v_mul_f32 v254, v254, v250
AW a10,v254
AR v251,a11
v_sub_f32 v251, v251, v249
v_mul_f32 v251, v251, v250
AW a11,v251
AR v254,a12
v_sub_f32 v254, v254, v249
v_mul_f32 v254, v254, v250
AW a12,v254
AR v251,a13
v_sub_f32 v251, v251, v249
v_mul_f32 v251, v251, v250
AW a13,v251
AR v254,a14
v_sub_f32 v254, v254, v249
v_mul_f32 v254, v254, v250
AW a14,v254
AR v251,a15
v_sub_f32 v251, v251, v249
v_mul_f32 v251, v251, v250
AW a15,v251
AR v254,a40
v_sub_f32 v254, v254, v249
v_mul_f32 v254, v254, v250
AW a40,v254
AR v251,a41
v_sub_f32 v251, v251, v249
v_mul_f32 v251, v251, v250
AW a41,v251
AR v254,a42
v_sub_f32 v254, v254, v249
v_mul_f32 v254, v254, v250
AW a42,v254
AR v251,a43
v_sub_f32 v251, v251, v249
v_mul_f32 v251, v251, v250
AW a43,v251
AR v254,a44
v_sub_f32 v254, v254, v249
v_mul_f32 v254, v254, v250
AW a44,v254
AR v251,a45
v_sub_f32 v251, v251, v249
v_mul_f32 v251, v251, v250
AW a45,v251
AR v254,a46
v_sub_f32 v254, v254, v249
v_mul_f32 v254, v254, v250
AW a46,v254
AR v251,a47
v_sub_f32 v251, v251, v249
v_mul_f32 v251, v251, v250
AW a47,v251
AR v254,a72
v_sub_f32 v254, v254, v249
v_mul_f32 v254, v254, v250
AW a72,v254
AR v251,a73
v_sub_f32 v251, v251, v249
v_mul_f32 v251, v251, v250
AW a73,v251
AR v254,a74
v_sub_f32 v254, v254, v249
v_mul_f32 v254, v254, v250
AW a74,v254
AR v251,a75
v_sub_f32 v251, v251, v249
v_mul_f32 v251, v251, v250
AW a75,v251
AR v254,a76
v_sub_f32 v254, v254, v249
v_mul_f32 v254, v254, v250
AW a76,v254
AR v251,a77
v_sub_f32 v251, v251, v249
v_mul_f32 v251, v251, v250
AW a77,v251
AR v254,a78
v_sub_f32 v254, v254, v249
v_mul_f32 v254, v254, v250
AW a78,v254
AR v251,a79
v_sub_f32 v251, v251, v249
v_mul_f32 v251, v251, v250
AW a79,v251
AR v254,a104
v_sub_f32 v254, v254, v249
v_mul_f32 v254, v254, v250
AW a104,v254
AR v251,a105
v_sub_f32 v251, v251, v249
v_mul_f32 v251, v251, v250
AW a105,v251
AR v254,a106
v_sub_f32 v254, v254, v249
v_mul_f32 v254, v254, v250
AW a106,v254
AR v251,a107
v_sub_f32 v251, v251, v249
v_mul_f32 v251, v251, v250
AW a107,v251
AR v254,a108
v_sub_f32 v254, v254, v249
v_mul_f32 v254, v254, v250
AW a108,v254
AR v251,a109
v_sub_f32 v251, v251, v249
v_mul_f32 v251, v251, v250
AW a109,v251
AR v254,a110
v_sub_f32 v254, v254, v249
v_mul_f32 v254, v254, v250
AW a110,v254
AR v251,a111
v_sub_f32 v251, v251, v249
v_mul_f32 v251, v251, v250
AW a111,v251
v_max3_f32 v249, v16, v17, v18
v_max3_f32 v251, v48, v49, v50
v_max3_f32 v249, v249, v19, v20
v_max3_f32 v251, v251, v51, v52
v_max3_f32 v249, v249, v21, v22
v_max3_f32 v251, v251, v53, v54
v_max3_f32 v249, v249, v23, v55
v_max_f32 v249, v249, v251
v_mov_b32 v251, v249
s_nop 1
v_permlane16_swap_b32 v249, v251
v_max_f32 v249, v249, v251
v_mov_b32 v251, v249
s_nop 1
v_permlane32_swap_b32 v249, v251
v_max_f32 v249, v249, v251
v_log_f32 v250, v246
s_nop 0
v_max3_f32 v249, 0, v249, v250
v_sub_f32 v250, 0, v249
EX v250,v250
v_sub_f32 v136, v136, v249
v_sub_f32 v137, v137, v249
v_sub_f32 v138, v138, v249
v_sub_f32 v139, v139, v249
v_sub_f32 v80, v80, v249
v_sub_f32 v16, v16, v249
v_sub_f32 v81, v81, v249
v_sub_f32 v17, v17, v249
v_sub_f32 v82, v82, v249
v_sub_f32 v18, v18, v249
v_sub_f32 v83, v83, v249
v_sub_f32 v19, v19, v249
v_sub_f32 v84, v84, v249
v_sub_f32 v20, v20, v249
v_sub_f32 v85, v85, v249
v_sub_f32 v21, v21, v249
v_sub_f32 v86, v86, v249
v_sub_f32 v22, v22, v249
v_sub_f32 v87, v87, v249
v_sub_f32 v23, v23, v249
v_sub_f32 v112, v112, v249
v_sub_f32 v48, v48, v249
v_sub_f32 v113, v113, v249
v_sub_f32 v49, v49, v249
v_sub_f32 v114, v114, v249
v_sub_f32 v50, v50, v249
v_sub_f32 v115, v115, v249
)ASM"
R"ASM(
v_sub_f32 v51, v51, v249
v_sub_f32 v116, v116, v249
v_sub_f32 v52, v52, v249
v_sub_f32 v117, v117, v249
v_sub_f32 v53, v53, v249
v_sub_f32 v118, v118, v249
v_sub_f32 v54, v54, v249
v_sub_f32 v119, v119, v249
v_sub_f32 v55, v55, v249
v_mul_f32 v152, v246, v250
v_mov_b32 v153, 0
v_bfe_u32 v249, v241, 16, 8
v_lshl_or_b32 v249, v249, 1, 1
v_lshlrev_b32 v249, 22, v249
v_and_b32 v241, 0xff00ffff, v241
AR v254,a16
v_sub_f32 v254, v254, v249
v_mul_f32 v254, v254, v250
AW a16,v254
AR v251,a17
v_sub_f32 v251, v251, v249
v_mul_f32 v251, v251, v250
AW a17,v251
AR v254,a18
v_sub_f32 v254, v254, v249
v_mul_f32 v254, v254, v250
AW a18,v254
AR v251,a19
v_sub_f32 v251, v251, v249
v_mul_f32 v251, v251, v250
AW a19,v251
AR v254,a20
v_sub_f32 v254, v254, v249
v_mul_f32 v254, v254, v250
AW a20,v254
AR v251,a21
v_sub_f32 v251, v251, v249
v_mul_f32 v251, v251, v250
AW a21,v251
AR v254,a22
v_sub_f32 v254, v254, v249
v_mul_f32 v254, v254, v250
AW a22,v254
AR v251,a23
v_sub_f32 v251, v251, v249
v_mul_f32 v251, v251, v250
AW a23,v251
AR v254,a48
v_sub_f32 v254, v254, v249
v_mul_f32 v254, v254, v250
AW a48,v254
AR v251,a49
v_sub_f32 v251, v251, v249
v_mul_f32 v251, v251, v250
AW a49,v251
AR v254,a50
v_sub_f32 v254, v254, v249
v_mul_f32 v254, v254, v250
AW a50,v254
AR v251,a51
v_sub_f32 v251, v251, v249
v_mul_f32 v251, v251, v250
AW a51,v251
AR v254,a52
v_sub_f32 v254, v254, v249
v_mul_f32 v254, v254, v250
AW a52,v254
AR v251,a53
v_sub_f32 v251, v251, v249
v_mul_f32 v251, v251, v250
AW a53,v251
AR v254,a54
v_sub_f32 v254, v254, v249
v_mul_f32 v254, v254, v250
AW a54,v254
AR v251,a55
v_sub_f32 v251, v251, v249
v_mul_f32 v251, v251, v250
AW a55,v251
AR v254,a80
v_sub_f32 v254, v254, v249
v_mul_f32 v254, v254, v250
AW a80,v254
AR v251,a81
v_sub_f32 v251, v251, v249
v_mul_f32 v251, v251, v250
AW a81,v251
AR v254,a82
v_sub_f32 v254, v254, v249
v_mul_f32 v254, v254, v250
AW a82,v254
AR v251,a83
v_sub_f32 v251, v251, v249
v_mul_f32 v251, v251, v250
AW a83,v251
AR v254,a84
v_sub_f32 v254, v254, v249
v_mul_f32 v254, v254, v250
AW a84,v254
AR v251,a85
v_sub_f32 v251, v251, v249
v_mul_f32 v251, v251, v250
AW a85,v251
AR v254,a86
v_sub_f32 v254, v254, v249
v_mul_f32 v254, v254, v250
AW a86,v254
AR v251,a87
v_sub_f32 v251, v251, v249
v_mul_f32 v251, v251, v250
AW a87,v251
AR v254,a112
v_sub_f32 v254, v254, v249
v_mul_f32 v254, v254, v250
AW a112,v254
AR v251,a113
v_sub_f32 v251, v251, v249
v_mul_f32 v251, v251, v250
AW a113,v251
AR v254,a114
v_sub_f32 v254, v254, v249
v_mul_f32 v254, v254, v250
AW a114,v254
AR v251,a115
v_sub_f32 v251, v251, v249
v_mul_f32 v251, v251, v250
AW a115,v251
AR v254,a116
v_sub_f32 v254, v254, v249
v_mul_f32 v254, v254, v250
AW a116,v254
AR v251,a117
v_sub_f32 v251, v251, v249
v_mul_f32 v251, v251, v250
AW a117,v251
AR v254,a118
v_sub_f32 v254, v254, v249
v_mul_f32 v254, v254, v250
AW a118,v254
AR v251,a119
v_sub_f32 v251, v251, v249
v_mul_f32 v251, v251, v250
AW a119,v251
v_max3_f32 v249, v24, v25, v26
v_max3_f32 v251, v56, v57, v58
v_max3_f32 v249, v249, v27, v28
v_max3_f32 v251, v251, v59, v60
v_max3_f32 v249, v249, v29, v30
v_max3_f32 v251, v251, v61, v62
v_max3_f32 v249, v249, v31, v63
v_max_f32 v249, v249, v251
v_mov_b32 v251, v249
s_nop 1
v_permlane16_swap_b32 v249, v251
v_max_f32 v249, v249, v251
v_mov_b32 v251, v249
s_nop 1
v_permlane32_swap_b32 v249, v251
v_max_f32 v249, v249, v251
v_log_f32 v250, v247
s_nop 0
v_max3_f32 v249, 0, v249, v250
v_sub_f32 v250, 0, v249
EX v250,v250
v_sub_f32 v140, v140, v249
v_sub_f32 v141, v141, v249
v_sub_f32 v142, v142, v249
v_sub_f32 v143, v143, v249
v_sub_f32 v88, v88, v249
v_sub_f32 v24, v24, v249
v_sub_f32 v89, v89, v249
v_sub_f32 v25, v25, v249
v_sub_f32 v90, v90, v249
v_sub_f32 v26, v26, v249
v_sub_f32 v91, v91, v249
v_sub_f32 v27, v27, v249
v_sub_f32 v92, v92, v249
v_sub_f32 v28, v28, v249
v_sub_f32 v93, v93, v249
v_sub_f32 v29, v29, v249
v_sub_f32 v94, v94, v249
v_sub_f32 v30, v30, v249
v_sub_f32 v95, v95, v249
v_sub_f32 v31, v31, v249
v_sub_f32 v120, v120, v249
v_sub_f32 v56, v56, v249
v_sub_f32 v121, v121, v249
v_sub_f32 v57, v57, v249
v_sub_f32 v122, v122, v249
v_sub_f32 v58, v58, v249
v_sub_f32 v123, v123, v249
v_sub_f32 v59, v59, v249
v_sub_f32 v124, v124, v249
v_sub_f32 v60, v60, v249
v_sub_f32 v125, v125, v249
v_sub_f32 v61, v61, v249
v_sub_f32 v126, v126, v249
v_sub_f32 v62, v62, v249
v_sub_f32 v127, v127, v249
v_sub_f32 v63, v63, v249
v_mul_f32 v156, v247, v250
v_mov_b32 v157, 0
v_bfe_u32 v249, v241, 24, 8
v_lshl_or_b32 v249, v249, 1, 1
v_lshlrev_b32 v249, 22, v249
v_and_b32 v241, 0xffffff, v241
AR v254,a24
v_sub_f32 v254, v254, v249
v_mul_f32 v254, v254, v250
AW a24,v254
AR v251,a25
v_sub_f32 v251, v251, v249
v_mul_f32 v251, v251, v250
AW a25,v251
AR v254,a26
v_sub_f32 v254, v254, v249
v_mul_f32 v254, v254, v250
AW a26,v254
AR v251,a27
v_sub_f32 v251, v251, v249
v_mul_f32 v251, v251, v250
AW a27,v251
AR v254,a28
v_sub_f32 v254, v254, v249
v_mul_f32 v254, v254, v250
AW a28,v254
AR v251,a29
v_sub_f32 v251, v251, v249
v_mul_f32 v251, v251, v250
AW a29,v251
AR v254,a30
v_sub_f32 v254, v254, v249
v_mul_f32 v254, v254, v250
AW a30,v254
AR v251,a31
v_sub_f32 v251, v251, v249
v_mul_f32 v251, v251, v250
AW a31,v251
AR v254,a56
v_sub_f32 v254, v254, v249
v_mul_f32 v254, v254, v250
AW a56,v254
AR v251,a57
v_sub_f32 v251, v251, v249
v_mul_f32 v251, v251, v250
AW a57,v251
AR v254,a58
v_sub_f32 v254, v254, v249
v_mul_f32 v254, v254, v250
AW a58,v254
AR v251,a59
v_sub_f32 v251, v251, v249
v_mul_f32 v251, v251, v250
AW a59,v251
AR v254,a60
v_sub_f32 v254, v254, v249
v_mul_f32 v254, v254, v250
AW a60,v254
AR v251,a61
v_sub_f32 v251, v251, v249
v_mul_f32 v251, v251, v250
AW a61,v251
AR v254,a62
v_sub_f32 v254, v254, v249
v_mul_f32 v254, v254, v250
AW a62,v254
AR v251,a63
v_sub_f32 v251, v251, v249
v_mul_f32 v251, v251, v250
AW a63,v251
AR v254,a88
v_sub_f32 v254, v254, v249
v_mul_f32 v254, v254, v250
AW a88,v254
AR v251,a89
v_sub_f32 v251, v251, v249
v_mul_f32 v251, v251, v250
AW a89,v251
AR v254,a90
v_sub_f32 v254, v254, v249
v_mul_f32 v254, v254, v250
AW a90,v254
AR v251,a91
v_sub_f32 v251, v251, v249
v_mul_f32 v251, v251, v250
AW a91,v251
AR v254,a92
v_sub_f32 v254, v254, v249
v_mul_f32 v254, v254, v250
AW a92,v254
AR v251,a93
v_sub_f32 v251, v251, v249
v_mul_f32 v251, v251, v250
AW a93,v251
AR v254,a94
v_sub_f32 v254, v254, v249
v_mul_f32 v254, v254, v250
AW a94,v254
AR v251,a95
v_sub_f32 v251, v251, v249
v_mul_f32 v251, v251, v250
AW a95,v251
AR v254,a120
v_sub_f32 v254, v254, v249
v_mul_f32 v254, v254, v250
AW a120,v254
AR v251,a121
v_sub_f32 v251, v251, v249
v_mul_f32 v251, v251, v250
AW a121,v251
AR v254,a122
v_sub_f32 v254, v254, v249
v_mul_f32 v254, v254, v250
AW a122,v254
AR v251,a123
v_sub_f32 v251, v251, v249
v_mul_f32 v251, v251, v250
AW a123,v251
AR v254,a124
v_sub_f32 v254, v254, v249
v_mul_f32 v254, v254, v250
AW a124,v254
AR v251,a125
v_sub_f32 v251, v251, v249
v_mul_f32 v251, v251, v250
AW a125,v251
AR v254,a126
v_sub_f32 v254, v254, v249
v_mul_f32 v254, v254, v250
AW a126,v254
AR v251,a127
v_sub_f32 v251, v251, v249
v_mul_f32 v251, v251, v250
AW a127,v251
EX v0,v0
EX v1,v1
EX v2,v2
EX v3,v3
EX v4,v4
EX v5,v5
EX v6,v6
EX v7,v7
EX v8,v8
EX v9,v9
EX v10,v10
EX v11,v11
EX v12,v12
EX v13,v13
EX v14,v14
EX v15,v15
EX v16,v16
EX v17,v17
EX v18,v18
EX v19,v19
EX v20,v20
EX v21,v21
EX v22,v22
EX v23,v23
EX v24,v24
EX v25,v25
EX v26,v26
EX v27,v27
EX v28,v28
EX v29,v29
EX v30,v30
EX v31,v31
EX v32,v32
EX v33,v33
EX v34,v34
EX v35,v35
EX v36,v36
EX v37,v37
EX v38,v38
EX v39,v39
EX v40,v40
EX v41,v41
EX v42,v42
EX v43,v43
EX v44,v44
EX v45,v45
EX v46,v46
EX v47,v47
EX v48,v48
EX v49,v49
EX v50,v50
EX v51,v51
EX v52,v52
EX v53,v53
EX v54,v54
EX v55,v55
EX v56,v56
EX v57,v57
EX v58,v58
EX v59,v59
EX v60,v60
EX v61,v61
EX v62,v62
EX v63,v63
CV v0,v0,v1
CV v1,v2,v3
CV v2,v4,v5
CV v3,v6,v7
CV v8,v8,v9
CV v9,v10,v11
CV v10,v12,v13
CV v11,v14,v15
CV v16,v16,v17
CV v17,v18,v19
CV v18,v20,v21
CV v19,v22,v23
CV v24,v24,v25
CV v25,v26,v27
CV v26,v28,v29
CV v27,v30,v31
CV v4,v32,v33
CV v5,v34,v35
CV v6,v36,v37
CV v7,v38,v39
CV v12,v40,v41
CV v13,v42,v43
CV v14,v44,v45
CV v15,v46,v47
CV v20,v48,v49
CV v21,v50,v51
CV v22,v52,v53
CV v23,v54,v55
CV v28,v56,v57
CV v29,v58,v59
CV v30,v60,v61
CV v31,v62,v63
v_smfmac_f32_16x16x64_bf16 v[144:147], v[236:239], v[0:7], v234
v_smfmac_f32_16x16x64_bf16 v[148:151], v[236:239], v[8:15], v234
v_smfmac_f32_16x16x64_bf16 v[152:155], v[236:239], v[16:23], v234
v_smfmac_f32_16x16x64_bf16 v[156:159], v[236:239], v[24:31], v234
s_nop 9
s_branch LRET_%=
LRARE022_%=:
s_nop 15
s_waitcnt vmcnt(0) lgkmcnt(0)
v_add_u32 v249, s74, v224
RD a[192:195],v249 offset:0
RD a[208:211],v249 offset:4096
RD a[224:227],v249 offset:8192
RD a[240:243],v249 offset:12288
v_add_u32 v250, s74, v225
RD a[196:199],v250 offset:0
RD a[212:215],v250 offset:4096
RD a[228:231],v250 offset:8192
RD a[244:247],v250 offset:12288
v_add_u32 v251, s74, v226
RD a[200:203],v251 offset:0
RD a[216:219],v251 offset:4096
RD a[232:235],v251 offset:8192
RD a[248:251],v251 offset:12288
v_add_u32 v254, s74, v227
RD a[204:207],v254 offset:0
RD a[220:223],v254 offset:4096
RD a[236:239],v254 offset:8192
RD a[252:255],v254 offset:12288
s_waitcnt lgkmcnt(0)
MM v[16:19],a[192:195],v[168:171],v[136:139]
MM v[16:19],a[196:199],v[184:187],v[16:19]
MM v[16:19],a[200:203],v[200:203],v[16:19]
MM v[16:19],a[204:207],v[216:219],v[16:19]
MM v[20:23],a[220:223],v[216:219],v[136:139]
MM v[20:23],a[216:219],v[200:203],v[20:23]
MM v[20:23],a[212:215],v[184:187],v[20:23]
MM v[20:23],a[208:211],v[168:171],v[20:23]
MM v[28:31],a[208:211],v[172:175],v[140:143]
MM v[28:31],a[212:215],v[188:191],v[28:31]
MM v[28:31],a[216:219],v[204:207],v[28:31]
MM v[28:31],a[220:223],v[220:223],v[28:31]
MM v[24:27],a[204:207],v[220:223],v[140:143]
MM v[24:27],a[200:203],v[204:207],v[24:27]
MM v[24:27],a[196:199],v[188:191],v[24:27]
MM v[24:27],a[192:195],v[172:175],v[24:27]
MM v[48:51],a[224:227],v[168:171],v[136:139]
MM v[48:51],a[228:231],v[184:187],v[48:51]
MM v[48:51],a[232:235],v[200:203],v[48:51]
MM v[48:51],a[236:239],v[216:219],v[48:51]
MM v[52:55],a[252:255],v[216:219],v[136:139]
MM v[52:55],a[248:251],v[200:203],v[52:55]
MM v[52:55],a[244:247],v[184:187],v[52:55]
MM v[52:55],a[240:243],v[168:171],v[52:55]
MM v[60:63],a[240:243],v[172:175],v[140:143]
MM v[60:63],a[244:247],v[188:191],v[60:63]
MM v[60:63],a[248:251],v[204:207],v[60:63]
MM v[60:63],a[252:255],v[220:223],v[60:63]
MM v[56:59],a[236:239],v[220:223],v[140:143]
MM v[56:59],a[232:235],v[204:207],v[56:59]
MM v[56:59],a[228:231],v[188:191],v[56:59]
MM v[56:59],a[224:227],v[172:175],v[56:59]
s_nop 15
v_add_u32 v249, s75, v224
RD a[192:195],v249 offset:0
RD a[208:211],v249 offset:4096
RD a[224:227],v249 offset:8192
RD a[240:243],v249 offset:12288
v_add_u32 v250, s75, v225
RD a[196:199],v250 offset:0
RD a[212:215],v250 offset:4096
RD a[228:231],v250 offset:8192
RD a[244:247],v250 offset:12288
v_add_u32 v251, s75, v226
RD a[200:203],v251 offset:0
RD a[216:219],v251 offset:4096
RD a[232:235],v251 offset:8192
RD a[248:251],v251 offset:12288
v_add_u32 v254, s75, v227
RD a[204:207],v254 offset:0
RD a[220:223],v254 offset:4096
RD a[236:239],v254 offset:8192
RD a[252:255],v254 offset:12288
s_waitcnt lgkmcnt(0)
v_max3_f32 v249, v16, v17, v18
v_max3_f32 v251, v48, v49, v50
v_max3_f32 v249, v249, v19, v20
v_max3_f32 v251, v251, v51, v52
v_max3_f32 v249, v249, v21, v22
v_max3_f32 v251, v251, v53, v54
v_max3_f32 v249, v249, v23, v55
v_max_f32 v249, v249, v251
v_mov_b32 v251, v249
s_nop 1
v_permlane16_swap_b32 v249, v251
v_max_f32 v249, v249, v251
v_mov_b32 v251, v249
s_nop 1
v_permlane32_swap_b32 v249, v251
v_max_f32 v249, v249, v251
v_log_f32 v250, v246
s_nop 0
v_max3_f32 v249, 0, v249, v250
v_sub_f32 v250, 0, v249
EX v250,v250
v_sub_f32 v136, v136, v249
v_sub_f32 v137, v137, v249
v_sub_f32 v138, v138, v249
v_sub_f32 v139, v139, v249
v_sub_f32 v80, v80, v249
v_sub_f32 v16, v16, v249
v_sub_f32 v81, v81, v249
v_sub_f32 v17, v17, v249
v_sub_f32 v82, v82, v249
v_sub_f32 v18, v18, v249
v_sub_f32 v83, v83, v249
v_sub_f32 v19, v19, v249
v_sub_f32 v84, v84, v249
)ASM"
R"ASM(
v_sub_f32 v20, v20, v249
v_sub_f32 v85, v85, v249
v_sub_f32 v21, v21, v249
v_sub_f32 v86, v86, v249
v_sub_f32 v22, v22, v249
v_sub_f32 v87, v87, v249
v_sub_f32 v23, v23, v249
v_sub_f32 v112, v112, v249
v_sub_f32 v48, v48, v249
v_sub_f32 v113, v113, v249
v_sub_f32 v49, v49, v249
v_sub_f32 v114, v114, v249
v_sub_f32 v50, v50, v249
v_sub_f32 v115, v115, v249
v_sub_f32 v51, v51, v249
v_sub_f32 v116, v116, v249
v_sub_f32 v52, v52, v249
v_sub_f32 v117, v117, v249
v_sub_f32 v53, v53, v249
v_sub_f32 v118, v118, v249
v_sub_f32 v54, v54, v249
v_sub_f32 v119, v119, v249
v_sub_f32 v55, v55, v249
v_mul_f32 v152, v246, v250
v_mov_b32 v153, 0
v_bfe_u32 v249, v241, 16, 8
v_lshl_or_b32 v249, v249, 1, 1
v_lshlrev_b32 v249, 22, v249
v_and_b32 v241, 0xff00ffff, v241
AR v254,a16
v_sub_f32 v254, v254, v249
v_mul_f32 v254, v254, v250
AW a16,v254
AR v251,a17
v_sub_f32 v251, v251, v249
v_mul_f32 v251, v251, v250
AW a17,v251
AR v254,a18
v_sub_f32 v254, v254, v249
v_mul_f32 v254, v254, v250
AW a18,v254
AR v251,a19
v_sub_f32 v251, v251, v249
v_mul_f32 v251, v251, v250
AW a19,v251
AR v254,a20
v_sub_f32 v254, v254, v249
v_mul_f32 v254, v254, v250
AW a20,v254
AR v251,a21
v_sub_f32 v251, v251, v249
v_mul_f32 v251, v251, v250
AW a21,v251
AR v254,a22
v_sub_f32 v254, v254, v249
v_mul_f32 v254, v254, v250
AW a22,v254
AR v251,a23
v_sub_f32 v251, v251, v249
v_mul_f32 v251, v251, v250
AW a23,v251
AR v254,a48
v_sub_f32 v254, v254, v249
v_mul_f32 v254, v254, v250
AW a48,v254
AR v251,a49
v_sub_f32 v251, v251, v249
v_mul_f32 v251, v251, v250
AW a49,v251
AR v254,a50
v_sub_f32 v254, v254, v249
v_mul_f32 v254, v254, v250
AW a50,v254
AR v251,a51
v_sub_f32 v251, v251, v249
v_mul_f32 v251, v251, v250
AW a51,v251
AR v254,a52
v_sub_f32 v254, v254, v249
v_mul_f32 v254, v254, v250
AW a52,v254
AR v251,a53
v_sub_f32 v251, v251, v249
v_mul_f32 v251, v251, v250
AW a53,v251
AR v254,a54
v_sub_f32 v254, v254, v249
v_mul_f32 v254, v254, v250
AW a54,v254
AR v251,a55
v_sub_f32 v251, v251, v249
v_mul_f32 v251, v251, v250
AW a55,v251
AR v254,a80
v_sub_f32 v254, v254, v249
v_mul_f32 v254, v254, v250
AW a80,v254
AR v251,a81
v_sub_f32 v251, v251, v249
v_mul_f32 v251, v251, v250
AW a81,v251
AR v254,a82
v_sub_f32 v254, v254, v249
v_mul_f32 v254, v254, v250
AW a82,v254
AR v251,a83
v_sub_f32 v251, v251, v249
v_mul_f32 v251, v251, v250
AW a83,v251
AR v254,a84
v_sub_f32 v254, v254, v249
v_mul_f32 v254, v254, v250
AW a84,v254
AR v251,a85
v_sub_f32 v251, v251, v249
v_mul_f32 v251, v251, v250
AW a85,v251
AR v254,a86
v_sub_f32 v254, v254, v249
v_mul_f32 v254, v254, v250
AW a86,v254
AR v251,a87
v_sub_f32 v251, v251, v249
v_mul_f32 v251, v251, v250
AW a87,v251
AR v254,a112
v_sub_f32 v254, v254, v249
v_mul_f32 v254, v254, v250
AW a112,v254
AR v251,a113
v_sub_f32 v251, v251, v249
v_mul_f32 v251, v251, v250
AW a113,v251
AR v254,a114
v_sub_f32 v254, v254, v249
v_mul_f32 v254, v254, v250
AW a114,v254
AR v251,a115
v_sub_f32 v251, v251, v249
v_mul_f32 v251, v251, v250
AW a115,v251
AR v254,a116
v_sub_f32 v254, v254, v249
v_mul_f32 v254, v254, v250
AW a116,v254
AR v251,a117
v_sub_f32 v251, v251, v249
v_mul_f32 v251, v251, v250
AW a117,v251
AR v254,a118
v_sub_f32 v254, v254, v249
v_mul_f32 v254, v254, v250
AW a118,v254
AR v251,a119
v_sub_f32 v251, v251, v249
v_mul_f32 v251, v251, v250
AW a119,v251
v_max3_f32 v249, v24, v25, v26
v_max3_f32 v251, v56, v57, v58
v_max3_f32 v249, v249, v27, v28
v_max3_f32 v251, v251, v59, v60
v_max3_f32 v249, v249, v29, v30
v_max3_f32 v251, v251, v61, v62
v_max3_f32 v249, v249, v31, v63
v_max_f32 v249, v249, v251
v_mov_b32 v251, v249
s_nop 1
v_permlane16_swap_b32 v249, v251
v_max_f32 v249, v249, v251
v_mov_b32 v251, v249
s_nop 1
v_permlane32_swap_b32 v249, v251
v_max_f32 v249, v249, v251
v_log_f32 v250, v247
s_nop 0
v_max3_f32 v249, 0, v249, v250
v_sub_f32 v250, 0, v249
EX v250,v250
v_sub_f32 v140, v140, v249
v_sub_f32 v141, v141, v249
v_sub_f32 v142, v142, v249
v_sub_f32 v143, v143, v249
v_sub_f32 v88, v88, v249
v_sub_f32 v24, v24, v249
v_sub_f32 v89, v89, v249
v_sub_f32 v25, v25, v249
v_sub_f32 v90, v90, v249
v_sub_f32 v26, v26, v249
v_sub_f32 v91, v91, v249
v_sub_f32 v27, v27, v249
v_sub_f32 v92, v92, v249
v_sub_f32 v28, v28, v249
v_sub_f32 v93, v93, v249
v_sub_f32 v29, v29, v249
v_sub_f32 v94, v94, v249
v_sub_f32 v30, v30, v249
v_sub_f32 v95, v95, v249
v_sub_f32 v31, v31, v249
v_sub_f32 v120, v120, v249
v_sub_f32 v56, v56, v249
v_sub_f32 v121, v121, v249
v_sub_f32 v57, v57, v249
v_sub_f32 v122, v122, v249
v_sub_f32 v58, v58, v249
v_sub_f32 v123, v123, v249
v_sub_f32 v59, v59, v249
v_sub_f32 v124, v124, v249
v_sub_f32 v60, v60, v249
v_sub_f32 v125, v125, v249
v_sub_f32 v61, v61, v249
v_sub_f32 v126, v126, v249
v_sub_f32 v62, v62, v249
v_sub_f32 v127, v127, v249
v_sub_f32 v63, v63, v249
v_mul_f32 v156, v247, v250
v_mov_b32 v157, 0
v_bfe_u32 v249, v241, 24, 8
v_lshl_or_b32 v249, v249, 1, 1
v_lshlrev_b32 v249, 22, v249
v_and_b32 v241, 0xffffff, v241
AR v254,a24
v_sub_f32 v254, v254, v249
v_mul_f32 v254, v254, v250
AW a24,v254
AR v251,a25
v_sub_f32 v251, v251, v249
v_mul_f32 v251, v251, v250
AW a25,v251
AR v254,a26
v_sub_f32 v254, v254, v249
v_mul_f32 v254, v254, v250
AW a26,v254
AR v251,a27
v_sub_f32 v251, v251, v249
v_mul_f32 v251, v251, v250
AW a27,v251
AR v254,a28
v_sub_f32 v254, v254, v249
v_mul_f32 v254, v254, v250
AW a28,v254
AR v251,a29
v_sub_f32 v251, v251, v249
v_mul_f32 v251, v251, v250
AW a29,v251
AR v254,a30
v_sub_f32 v254, v254, v249
v_mul_f32 v254, v254, v250
AW a30,v254
AR v251,a31
v_sub_f32 v251, v251, v249
v_mul_f32 v251, v251, v250
AW a31,v251
AR v254,a56
v_sub_f32 v254, v254, v249
v_mul_f32 v254, v254, v250
AW a56,v254
AR v251,a57
v_sub_f32 v251, v251, v249
v_mul_f32 v251, v251, v250
AW a57,v251
AR v254,a58
v_sub_f32 v254, v254, v249
v_mul_f32 v254, v254, v250
AW a58,v254
AR v251,a59
v_sub_f32 v251, v251, v249
v_mul_f32 v251, v251, v250
AW a59,v251
AR v254,a60
v_sub_f32 v254, v254, v249
v_mul_f32 v254, v254, v250
AW a60,v254
AR v251,a61
v_sub_f32 v251, v251, v249
v_mul_f32 v251, v251, v250
AW a61,v251
AR v254,a62
v_sub_f32 v254, v254, v249
v_mul_f32 v254, v254, v250
AW a62,v254
AR v251,a63
v_sub_f32 v251, v251, v249
v_mul_f32 v251, v251, v250
AW a63,v251
AR v254,a88
v_sub_f32 v254, v254, v249
v_mul_f32 v254, v254, v250
AW a88,v254
AR v251,a89
v_sub_f32 v251, v251, v249
v_mul_f32 v251, v251, v250
AW a89,v251
AR v254,a90
v_sub_f32 v254, v254, v249
v_mul_f32 v254, v254, v250
AW a90,v254
AR v251,a91
v_sub_f32 v251, v251, v249
v_mul_f32 v251, v251, v250
AW a91,v251
AR v254,a92
v_sub_f32 v254, v254, v249
v_mul_f32 v254, v254, v250
AW a92,v254
AR v251,a93
v_sub_f32 v251, v251, v249
v_mul_f32 v251, v251, v250
AW a93,v251
AR v254,a94
v_sub_f32 v254, v254, v249
v_mul_f32 v254, v254, v250
AW a94,v254
AR v251,a95
v_sub_f32 v251, v251, v249
v_mul_f32 v251, v251, v250
AW a95,v251
AR v254,a120
v_sub_f32 v254, v254, v249
v_mul_f32 v254, v254, v250
AW a120,v254
AR v251,a121
v_sub_f32 v251, v251, v249
v_mul_f32 v251, v251, v250
AW a121,v251
AR v254,a122
v_sub_f32 v254, v254, v249
v_mul_f32 v254, v254, v250
AW a122,v254
AR v251,a123
v_sub_f32 v251, v251, v249
v_mul_f32 v251, v251, v250
AW a123,v251
AR v254,a124
v_sub_f32 v254, v254, v249
v_mul_f32 v254, v254, v250
AW a124,v254
AR v251,a125
v_sub_f32 v251, v251, v249
v_mul_f32 v251, v251, v250
AW a125,v251
AR v254,a126
v_sub_f32 v254, v254, v249
v_mul_f32 v254, v254, v250
AW a126,v254
AR v251,a127
v_sub_f32 v251, v251, v249
v_mul_f32 v251, v251, v250
AW a127,v251
EX v16,v16
EX v17,v17
EX v18,v18
EX v19,v19
EX v20,v20
EX v21,v21
EX v22,v22
EX v23,v23
EX v24,v24
EX v25,v25
EX v26,v26
EX v27,v27
EX v28,v28
EX v29,v29
EX v30,v30
EX v31,v31
EX v48,v48
EX v49,v49
EX v50,v50
EX v51,v51
EX v52,v52
EX v53,v53
EX v54,v54
EX v55,v55
EX v56,v56
EX v57,v57
EX v58,v58
EX v59,v59
EX v60,v60
EX v61,v61
EX v62,v62
EX v63,v63
CV v16,v16,v17
CV v17,v18,v19
CV v18,v20,v21
CV v19,v22,v23
CV v24,v24,v25
CV v25,v26,v27
CV v26,v28,v29
CV v27,v30,v31
CV v20,v48,v49
CV v21,v50,v51
CV v22,v52,v53
CV v23,v54,v55
CV v28,v56,v57
CV v29,v58,v59
CV v30,v60,v61
CV v31,v62,v63
v_smfmac_f32_16x16x64_bf16 v[152:155], v[236:239], v[16:23], v234
s_nop 0
v_smfmac_f32_16x16x64_bf16 v[156:159], v[236:239], v[24:31], v234
s_nop 9
s_branch LRET_%=
LRARE122_%=:
s_nop 15
s_waitcnt vmcnt(0) lgkmcnt(0)
v_add_u32 v249, s74, v224
RD a[192:195],v249 offset:0
RD a[208:211],v249 offset:4096
RD a[224:227],v249 offset:8192
RD a[240:243],v249 offset:12288
v_add_u32 v250, s74, v225
RD a[196:199],v250 offset:0
RD a[212:215],v250 offset:4096
RD a[228:231],v250 offset:8192
RD a[244:247],v250 offset:12288
v_add_u32 v251, s74, v226
RD a[200:203],v251 offset:0
RD a[216:219],v251 offset:4096
RD a[232:235],v251 offset:8192
RD a[248:251],v251 offset:12288
v_add_u32 v254, s74, v227
RD a[204:207],v254 offset:0
RD a[220:223],v254 offset:4096
RD a[236:239],v254 offset:8192
RD a[252:255],v254 offset:12288
s_waitcnt lgkmcnt(0)
MM v[80:83],a[192:195],v[168:171],v[136:139]
MM v[80:83],a[196:199],v[184:187],v[80:83]
MM v[80:83],a[200:203],v[200:203],v[80:83]
MM v[80:83],a[204:207],v[216:219],v[80:83]
MM v[84:87],a[220:223],v[216:219],v[136:139]
MM v[84:87],a[216:219],v[200:203],v[84:87]
MM v[84:87],a[212:215],v[184:187],v[84:87]
MM v[84:87],a[208:211],v[168:171],v[84:87]
MM v[92:95],a[208:211],v[172:175],v[140:143]
MM v[92:95],a[212:215],v[188:191],v[92:95]
MM v[92:95],a[216:219],v[204:207],v[92:95]
MM v[92:95],a[220:223],v[220:223],v[92:95]
MM v[88:91],a[204:207],v[220:223],v[140:143]
MM v[88:91],a[200:203],v[204:207],v[88:91]
MM v[88:91],a[196:199],v[188:191],v[88:91]
MM v[88:91],a[192:195],v[172:175],v[88:91]
MM v[112:115],a[224:227],v[168:171],v[136:139]
MM v[112:115],a[228:231],v[184:187],v[112:115]
MM v[112:115],a[232:235],v[200:203],v[112:115]
MM v[112:115],a[236:239],v[216:219],v[112:115]
MM v[116:119],a[252:255],v[216:219],v[136:139]
MM v[116:119],a[248:251],v[200:203],v[116:119]
MM v[116:119],a[244:247],v[184:187],v[116:119]
MM v[116:119],a[240:243],v[168:171],v[116:119]
MM v[124:127],a[240:243],v[172:175],v[140:143]
MM v[124:127],a[244:247],v[188:191],v[124:127]
MM v[124:127],a[248:251],v[204:207],v[124:127]
MM v[124:127],a[252:255],v[220:223],v[124:127]
MM v[120:123],a[236:239],v[220:223],v[140:143]
MM v[120:123],a[232:235],v[204:207],v[120:123]
MM v[120:123],a[228:231],v[188:191],v[120:123]
MM v[120:123],a[224:227],v[172:175],v[120:123]
s_nop 15
v_add_u32 v249, s75, v224
RD a[192:195],v249 offset:0
RD a[208:211],v249 offset:4096
RD a[224:227],v249 offset:8192
RD a[240:243],v249 offset:12288
v_add_u32 v250, s75, v225
RD a[196:199],v250 offset:0
RD a[212:215],v250 offset:4096
RD a[228:231],v250 offset:8192
RD a[244:247],v250 offset:12288
v_add_u32 v251, s75, v226
RD a[200:203],v251 offset:0
RD a[216:219],v251 offset:4096
RD a[232:235],v251 offset:8192
RD a[248:251],v251 offset:12288
v_add_u32 v254, s75, v227
RD a[204:207],v254 offset:0
RD a[220:223],v254 offset:4096
RD a[236:239],v254 offset:8192
RD a[252:255],v254 offset:12288
s_waitcnt lgkmcnt(0)
v_max3_f32 v249, v80, v81, v82
v_max3_f32 v251, v112, v113, v114
v_max3_f32 v249, v249, v83, v84
v_max3_f32 v251, v251, v115, v116
v_max3_f32 v249, v249, v85, v86
v_max3_f32 v251, v251, v117, v118
v_max3_f32 v249, v249, v87, v119
v_max_f32 v249, v249, v251
v_mov_b32 v251, v249
s_nop 1
v_permlane16_swap_b32 v249, v251
v_max_f32 v249, v249, v251
v_mov_b32 v251, v249
s_nop 1
v_permlane32_swap_b32 v249, v251
v_max_f32 v249, v249, v251
v_log_f32 v250, v246
s_nop 0
v_max3_f32 v249, 0, v249, v250
v_sub_f32 v250, 0, v249
EX v250,v250
v_sub_f32 v136, v136, v249
v_sub_f32 v137, v137, v249
v_sub_f32 v138, v138, v249
v_sub_f32 v139, v139, v249
v_sub_f32 v16, v16, v249
v_sub_f32 v80, v80, v249
v_sub_f32 v17, v17, v249
v_sub_f32 v81, v81, v249
v_sub_f32 v18, v18, v249
v_sub_f32 v82, v82, v249
v_sub_f32 v19, v19, v249
v_sub_f32 v83, v83, v249
v_sub_f32 v20, v20, v249
v_sub_f32 v84, v84, v249
v_sub_f32 v21, v21, v249
v_sub_f32 v85, v85, v249
v_sub_f32 v22, v22, v249
v_sub_f32 v86, v86, v249
v_sub_f32 v23, v23, v249
v_sub_f32 v87, v87, v249
v_sub_f32 v48, v48, v249
v_sub_f32 v112, v112, v249
v_sub_f32 v49, v49, v249
)ASM"
R"ASM(
v_sub_f32 v113, v113, v249
v_sub_f32 v50, v50, v249
v_sub_f32 v114, v114, v249
v_sub_f32 v51, v51, v249
v_sub_f32 v115, v115, v249
v_sub_f32 v52, v52, v249
v_sub_f32 v116, v116, v249
v_sub_f32 v53, v53, v249
v_sub_f32 v117, v117, v249
v_sub_f32 v54, v54, v249
v_sub_f32 v118, v118, v249
v_sub_f32 v55, v55, v249
v_sub_f32 v119, v119, v249
v_mul_f32 v152, v246, v250
v_mov_b32 v153, 0
v_bfe_u32 v249, v241, 16, 8
v_lshl_or_b32 v249, v249, 1, 1
v_lshlrev_b32 v249, 22, v249
v_and_b32 v241, 0xff00ffff, v241
AR v254,a16
v_sub_f32 v254, v254, v249
v_mul_f32 v254, v254, v250
AW a16,v254
AR v251,a17
v_sub_f32 v251, v251, v249
v_mul_f32 v251, v251, v250
AW a17,v251
AR v254,a18
v_sub_f32 v254, v254, v249
v_mul_f32 v254, v254, v250
AW a18,v254
AR v251,a19
v_sub_f32 v251, v251, v249
v_mul_f32 v251, v251, v250
AW a19,v251
AR v254,a20
v_sub_f32 v254, v254, v249
v_mul_f32 v254, v254, v250
AW a20,v254
AR v251,a21
v_sub_f32 v251, v251, v249
v_mul_f32 v251, v251, v250
AW a21,v251
AR v254,a22
v_sub_f32 v254, v254, v249
v_mul_f32 v254, v254, v250
AW a22,v254
AR v251,a23
v_sub_f32 v251, v251, v249
v_mul_f32 v251, v251, v250
AW a23,v251
AR v254,a48
v_sub_f32 v254, v254, v249
v_mul_f32 v254, v254, v250
AW a48,v254
AR v251,a49
v_sub_f32 v251, v251, v249
v_mul_f32 v251, v251, v250
AW a49,v251
AR v254,a50
v_sub_f32 v254, v254, v249
v_mul_f32 v254, v254, v250
AW a50,v254
AR v251,a51
v_sub_f32 v251, v251, v249
v_mul_f32 v251, v251, v250
AW a51,v251
AR v254,a52
v_sub_f32 v254, v254, v249
v_mul_f32 v254, v254, v250
AW a52,v254
AR v251,a53
v_sub_f32 v251, v251, v249
v_mul_f32 v251, v251, v250
AW a53,v251
AR v254,a54
v_sub_f32 v254, v254, v249
v_mul_f32 v254, v254, v250
AW a54,v254
AR v251,a55
v_sub_f32 v251, v251, v249
v_mul_f32 v251, v251, v250
AW a55,v251
AR v254,a80
v_sub_f32 v254, v254, v249
v_mul_f32 v254, v254, v250
AW a80,v254
AR v251,a81
v_sub_f32 v251, v251, v249
v_mul_f32 v251, v251, v250
AW a81,v251
AR v254,a82
v_sub_f32 v254, v254, v249
v_mul_f32 v254, v254, v250
AW a82,v254
AR v251,a83
v_sub_f32 v251, v251, v249
v_mul_f32 v251, v251, v250
AW a83,v251
AR v254,a84
v_sub_f32 v254, v254, v249
v_mul_f32 v254, v254, v250
AW a84,v254
AR v251,a85
v_sub_f32 v251, v251, v249
v_mul_f32 v251, v251, v250
AW a85,v251
AR v254,a86
v_sub_f32 v254, v254, v249
v_mul_f32 v254, v254, v250
AW a86,v254
AR v251,a87
v_sub_f32 v251, v251, v249
v_mul_f32 v251, v251, v250
AW a87,v251
AR v254,a112
v_sub_f32 v254, v254, v249
v_mul_f32 v254, v254, v250
AW a112,v254
AR v251,a113
v_sub_f32 v251, v251, v249
v_mul_f32 v251, v251, v250
AW a113,v251
AR v254,a114
v_sub_f32 v254, v254, v249
v_mul_f32 v254, v254, v250
AW a114,v254
AR v251,a115
v_sub_f32 v251, v251, v249
v_mul_f32 v251, v251, v250
AW a115,v251
AR v254,a116
v_sub_f32 v254, v254, v249
v_mul_f32 v254, v254, v250
AW a116,v254
AR v251,a117
v_sub_f32 v251, v251, v249
v_mul_f32 v251, v251, v250
AW a117,v251
AR v254,a118
v_sub_f32 v254, v254, v249
v_mul_f32 v254, v254, v250
AW a118,v254
AR v251,a119
v_sub_f32 v251, v251, v249
v_mul_f32 v251, v251, v250
AW a119,v251
v_max3_f32 v249, v88, v89, v90
v_max3_f32 v251, v120, v121, v122
v_max3_f32 v249, v249, v91, v92
v_max3_f32 v251, v251, v123, v124
v_max3_f32 v249, v249, v93, v94
v_max3_f32 v251, v251, v125, v126
v_max3_f32 v249, v249, v95, v127
v_max_f32 v249, v249, v251
v_mov_b32 v251, v249
s_nop 1
v_permlane16_swap_b32 v249, v251
v_max_f32 v249, v249, v251
v_mov_b32 v251, v249
s_nop 1
v_permlane32_swap_b32 v249, v251
v_max_f32 v249, v249, v251
v_log_f32 v250, v247
s_nop 0
v_max3_f32 v249, 0, v249, v250
v_sub_f32 v250, 0, v249
EX v250,v250
v_sub_f32 v140, v140, v249
v_sub_f32 v141, v141, v249
v_sub_f32 v142, v142, v249
v_sub_f32 v143, v143, v249
v_sub_f32 v24, v24, v249
v_sub_f32 v88, v88, v249
v_sub_f32 v25, v25, v249
v_sub_f32 v89, v89, v249
v_sub_f32 v26, v26, v249
v_sub_f32 v90, v90, v249
v_sub_f32 v27, v27, v249
v_sub_f32 v91, v91, v249
v_sub_f32 v28, v28, v249
v_sub_f32 v92, v92, v249
v_sub_f32 v29, v29, v249
v_sub_f32 v93, v93, v249
v_sub_f32 v30, v30, v249
v_sub_f32 v94, v94, v249
v_sub_f32 v31, v31, v249
v_sub_f32 v95, v95, v249
v_sub_f32 v56, v56, v249
v_sub_f32 v120, v120, v249
v_sub_f32 v57, v57, v249
v_sub_f32 v121, v121, v249
v_sub_f32 v58, v58, v249
v_sub_f32 v122, v122, v249
v_sub_f32 v59, v59, v249
v_sub_f32 v123, v123, v249
v_sub_f32 v60, v60, v249
v_sub_f32 v124, v124, v249
v_sub_f32 v61, v61, v249
v_sub_f32 v125, v125, v249
v_sub_f32 v62, v62, v249
v_sub_f32 v126, v126, v249
v_sub_f32 v63, v63, v249
v_sub_f32 v127, v127, v249
v_mul_f32 v156, v247, v250
v_mov_b32 v157, 0
v_bfe_u32 v249, v241, 24, 8
v_lshl_or_b32 v249, v249, 1, 1
v_lshlrev_b32 v249, 22, v249
v_and_b32 v241, 0xffffff, v241
AR v254,a24
v_sub_f32 v254, v254, v249
v_mul_f32 v254, v254, v250
AW a24,v254
AR v251,a25
v_sub_f32 v251, v251, v249
v_mul_f32 v251, v251, v250
AW a25,v251
AR v254,a26
v_sub_f32 v254, v254, v249
v_mul_f32 v254, v254, v250
AW a26,v254
AR v251,a27
v_sub_f32 v251, v251, v249
v_mul_f32 v251, v251, v250
AW a27,v251
AR v254,a28
v_sub_f32 v254, v254, v249
v_mul_f32 v254, v254, v250
AW a28,v254
AR v251,a29
v_sub_f32 v251, v251, v249
v_mul_f32 v251, v251, v250
AW a29,v251
AR v254,a30
v_sub_f32 v254, v254, v249
v_mul_f32 v254, v254, v250
AW a30,v254
AR v251,a31
v_sub_f32 v251, v251, v249
v_mul_f32 v251, v251, v250
AW a31,v251
AR v254,a56
v_sub_f32 v254, v254, v249
v_mul_f32 v254, v254, v250
AW a56,v254
AR v251,a57
v_sub_f32 v251, v251, v249
v_mul_f32 v251, v251, v250
AW a57,v251
AR v254,a58
v_sub_f32 v254, v254, v249
v_mul_f32 v254, v254, v250
AW a58,v254
AR v251,a59
v_sub_f32 v251, v251, v249
v_mul_f32 v251, v251, v250
AW a59,v251
AR v254,a60
v_sub_f32 v254, v254, v249
v_mul_f32 v254, v254, v250
AW a60,v254
AR v251,a61
v_sub_f32 v251, v251, v249
v_mul_f32 v251, v251, v250
AW a61,v251
AR v254,a62
v_sub_f32 v254, v254, v249
v_mul_f32 v254, v254, v250
AW a62,v254
AR v251,a63
v_sub_f32 v251, v251, v249
v_mul_f32 v251, v251, v250
AW a63,v251
AR v254,a88
v_sub_f32 v254, v254, v249
v_mul_f32 v254, v254, v250
AW a88,v254
AR v251,a89
v_sub_f32 v251, v251, v249
v_mul_f32 v251, v251, v250
AW a89,v251
AR v254,a90
v_sub_f32 v254, v254, v249
v_mul_f32 v254, v254, v250
AW a90,v254
AR v251,a91
v_sub_f32 v251, v251, v249
v_mul_f32 v251, v251, v250
AW a91,v251
AR v254,a92
v_sub_f32 v254, v254, v249
v_mul_f32 v254, v254, v250
AW a92,v254
AR v251,a93
v_sub_f32 v251, v251, v249
v_mul_f32 v251, v251, v250
AW a93,v251
AR v254,a94
v_sub_f32 v254, v254, v249
v_mul_f32 v254, v254, v250
AW a94,v254
AR v251,a95
v_sub_f32 v251, v251, v249
v_mul_f32 v251, v251, v250
AW a95,v251
AR v254,a120
v_sub_f32 v254, v254, v249
v_mul_f32 v254, v254, v250
AW a120,v254
AR v251,a121
v_sub_f32 v251, v251, v249
v_mul_f32 v251, v251, v250
AW a121,v251
AR v254,a122
v_sub_f32 v254, v254, v249
v_mul_f32 v254, v254, v250
AW a122,v254
AR v251,a123
v_sub_f32 v251, v251, v249
v_mul_f32 v251, v251, v250
AW a123,v251
AR v254,a124
v_sub_f32 v254, v254, v249
v_mul_f32 v254, v254, v250
AW a124,v254
AR v251,a125
v_sub_f32 v251, v251, v249
v_mul_f32 v251, v251, v250
AW a125,v251
AR v254,a126
v_sub_f32 v254, v254, v249
v_mul_f32 v254, v254, v250
AW a126,v254
AR v251,a127
v_sub_f32 v251, v251, v249
v_mul_f32 v251, v251, v250
AW a127,v251
EX v80,v80
EX v81,v81
EX v82,v82
EX v83,v83
EX v84,v84
EX v85,v85
EX v86,v86
EX v87,v87
EX v88,v88
EX v89,v89
EX v90,v90
EX v91,v91
EX v92,v92
EX v93,v93
EX v94,v94
EX v95,v95
EX v112,v112
EX v113,v113
EX v114,v114
EX v115,v115
EX v116,v116
EX v117,v117
EX v118,v118
EX v119,v119
EX v120,v120
EX v121,v121
EX v122,v122
EX v123,v123
EX v124,v124
EX v125,v125
EX v126,v126
EX v127,v127
CV v80,v80,v81
CV v81,v82,v83
CV v82,v84,v85
CV v83,v86,v87
CV v88,v88,v89
CV v89,v90,v91
CV v90,v92,v93
CV v91,v94,v95
CV v84,v112,v113
CV v85,v114,v115
CV v86,v116,v117
CV v87,v118,v119
CV v92,v120,v121
CV v93,v122,v123
CV v94,v124,v125
CV v95,v126,v127
v_smfmac_f32_16x16x64_bf16 v[152:155], v[236:239], v[80:87], v234
s_nop 0
v_smfmac_f32_16x16x64_bf16 v[156:159], v[236:239], v[88:95], v234
s_nop 9
s_branch LRET_%=
LEND_%=:
)ASM"

    :: [ka] "s"(__builtin_amdgcn_kernarg_segment_ptr()), [slds] "s"(slds), [swave] "s"(w), [sb] "s"(blockIdx.x)
    : "memory", "vcc", "scc", CLOB_V, CLOB_A, CLOB_S);
}
constexpr int DYN_LDS = 163840;

extern "C" void kernel_launch(void* const* d_in, const int* in_sizes, int n_in, void* d_out, int out_size, void* d_ws, size_t ws_size, hipStream_t stream) {
#if !defined(__HIP_DEVICE_COMPILE__)
  if (DYN_LDS) hipFuncSetAttribute((const void*)ka_fused, hipFuncAttributeMaxDynamicSharedMemorySize, DYN_LDS);
  ka_fused<<<256, 256, DYN_LDS, stream>>>(d_in[3], d_in[0], d_ws, d_in[1], d_in[6], d_in[2], d_in[4], d_in[7], d_in[8], d_in[9], d_out);
#endif
}
```

```cpp
#include <hip/hip_runtime.h>
#include <stdint.h>

using f32x4  = __attribute__((ext_vector_type(4))) float;
using u32x4  = __attribute__((ext_vector_type(4))) unsigned;
using u32x2  = __attribute__((ext_vector_type(2))) unsigned;

constexpr int SEQ = 2048, DM = 640, NH = 32, DH = 128, HD = NH * DH, FF = 1728, TOPC = 8;

constexpr unsigned WS_FO  = 0;
constexpr unsigned WS_EP  = 32768;
constexpr unsigned WS_G1  = 36864;
constexpr unsigned WS_G2  = 53248;
constexpr unsigned WS_FH  = 69632;
constexpr unsigned WS_FY  = 73728;
constexpr unsigned WS_FG  = 77824;
constexpr unsigned WS_FR  = 81920;
constexpr unsigned WS_RK  = 86016;
constexpr unsigned WS_XN  = 131072;
constexpr unsigned WS_OF  = WS_XN + SEQ * DM * 2;
constexpr unsigned WS_H   = WS_OF + (18u << 20);
constexpr unsigned WS_Y   = WS_H + 32768;
constexpr unsigned WS_P   = WS_Y + 1048576;
constexpr unsigned WS_SPAN = 64u << 20;

#define CLOB_V "v0","v1","v2","v3","v4","v5","v6","v7","v8","v9","v10","v11","v12","v13","v14","v15","v16","v17","v18","v19","v20","v21","v22","v23","v24","v25","v26","v27","v28","v29","v30","v31","v32","v33","v34","v35","v36","v37","v38","v39","v40","v41","v42","v43","v44","v45","v46","v47","v48","v49","v50","v51","v52","v53","v54","v55","v56","v57","v58","v59","v60","v61","v62","v63","v64","v65","v66","v67","v68","v69","v70","v71","v72","v73","v74","v75","v76","v77","v78","v79","v80","v81","v82","v83","v84","v85","v86","v87","v88","v89","v90","v91","v92","v93","v94","v95","v96","v97","v98","v99","v100","v101","v102","v103","v104","v105","v106","v107","v108","v109","v110","v111","v112","v113","v114","v115","v116","v117","v118","v119","v120","v121","v122","v123","v124","v125","v126","v127","v128","v129","v130","v131","v132","v133","v134","v135","v136","v137","v138","v139","v140","v141","v142","v143","v144","v145","v146","v147","v148","v149","v150","v151","v152","v153","v154","v155","v156","v157","v158","v159","v160","v161","v162","v163","v164","v165","v166","v167","v168","v169","v170","v171","v172","v173","v174","v175","v176","v177","v178","v179","v180","v181","v182","v183","v184","v185","v186","v187","v188","v189","v190","v191","v192","v193","v194","v195","v196","v197","v198","v199","v200","v201","v202","v203","v204","v205","v206","v207","v208","v209","v210","v211","v212","v213","v214","v215","v216","v217","v218","v219","v220","v221","v222","v223","v224","v225","v226","v227","v228","v229","v230","v231","v232","v233","v234","v235","v236","v237","v238","v239","v240","v241","v242","v243","v244","v245","v246","v247","v248","v249","v250","v251","v252","v253","v254","v255"
#define CLOB_A "a0","a1","a2","a3","a4","a5","a6","a7","a8","a9","a10","a11","a12","a13","a14","a15","a16","a17","a18","a19","a20","a21","a22","a23","a24","a25","a26","a27","a28","a29","a30","a31","a32","a33","a34","a35","a36","a37","a38","a39","a40","a41","a42","a43","a44","a45","a46","a47","a48","a49","a50","a51","a52","a53","a54","a55","a56","a57","a58","a59","a60","a61","a62","a63","a64","a65","a66","a67","a68","a69","a70","a71","a72","a73","a74","a75","a76","a77","a78","a79","a80","a81","a82","a83","a84","a85","a86","a87","a88","a89","a90","a91","a92","a93","a94","a95","a96","a97","a98","a99","a100","a101","a102","a103","a104","a105","a106","a107","a108","a109","a110","a111","a112","a113","a114","a115","a116","a117","a118","a119","a120","a121","a122","a123","a124","a125","a126","a127","a128","a129","a130","a131","a132","a133","a134","a135","a136","a137","a138","a139","a140","a141","a142","a143","a144","a145","a146","a147","a148","a149","a150","a151","a152","a153","a154","a155","a156","a157","a158","a159","a160","a161","a162","a163","a164","a165","a166","a167","a168","a169","a170","a171","a172","a173","a174","a175","a176","a177","a178","a179","a180","a181","a182","a183","a184","a185","a186","a187","a188","a189","a190","a191","a192","a193","a194","a195","a196","a197","a198","a199","a200","a201","a202","a203","a204","a205","a206","a207","a208","a209","a210","a211","a212","a213","a214","a215","a216","a217","a218","a219","a220","a221","a222","a223","a224","a225","a226","a227","a228","a229","a230","a231","a232","a233","a234","a235","a236","a237","a238","a239","a240","a241","a242","a243","a244","a245","a246","a247","a248","a249","a250","a251","a252","a253","a254","a255"
#define CLOB_S "s8","s9","s10","s11","s12","s13","s14","s15","s16","s17","s18","s19","s20","s21","s22","s23","s24","s25","s26","s27","s28","s29","s30","s31","s32","s33","s34","s35","s36","s37","s38","s39","s40","s41","s42","s43","s44","s45","s46","s47","s48","s49","s50","s51","s52","s53","s54","s55","s56","s57","s58","s59","s60","s61","s62","s63","s64","s65","s66","s67","s68","s69","s70","s71","s72","s73","s74","s75","s76","s77","s78","s79","s80","s81","s82","s83","s84","s85","s86","s87","s88","s89","s90","s91","s92","s93","s94","s95","s96","s97","s98","s99"
__global__ __launch_bounds__(256, 1) __attribute__((aligned(4096))) void ka_fused(const void* wq, const void* x, void* ws, const void* Kc, const void* wr, const void* Vc, const void* wo, const void* wg, const void* wu, const void* wd, void* out) {
  extern __shared__ __attribute__((aligned(16))) char lds[];
  const unsigned w = __builtin_amdgcn_readfirstlane(threadIdx.x >> 6);
  const unsigned slds = __builtin_amdgcn_readfirstlane((unsigned)(size_t)lds);
  asm volatile(
R"ASM(
.macro MM a, b, c, d:vararg
v_mfma_f32_16x16x32_bf16 \a, \b, \c, \d
.endm
.macro TR a, b:vararg
ds_read_b64_tr_b16 \a, \b
.endm
.macro CV a, b, c:vararg
v_cvt_pk_bf16_f32 \a, \b, \c
.endm
.macro AR a, b:vararg
v_accvgpr_read_b32 \a, \b
.endm
.macro AW a, b:vararg
v_accvgpr_write_b32 \a, \b
.endm
.macro EX a, b:vararg
v_exp_f32 \a, \b
.endm
.macro RD a, b:vararg
ds_read_b128 \a, \b
.endm
s_load_dwordx2 s[16:17], %[ka], 0
s_load_dwordx2 s[32:33], %[ka], 8
s_lshr_b32 s77, %[sb], 3
v_lshlrev_b32 v118, 4, v0
s_getpc_b64 s[98:99]
s_andn2_b32 s98, s98, 63
v_lshl_add_u32 v118, s77, 12, v118
global_load_dwordx4 a[252:255], v118, s[98:99]
.p2align 6
LPG0_%=:
s_load_dwordx2 s[20:21], %[ka], 16
v_mbcnt_lo_u32_b32 v127, -1, 0
v_mbcnt_hi_u32_b32 v127, -1, v127
v_lshlrev_b32 v248, 4, v127
s_and_b32 s77, %[sb], 7
s_lshl_b32 s77, s77, 2
s_bfe_u32 s84, %[sb], 131075
s_add_u32 s49, s77, s84
s_lshr_b32 s50, %[sb], 5
s_lshl_b32 s76, %[sb], 2
s_add_u32 s76, s76, %[swave]
v_lshrrev_b32 v180, 5, v127
s_mul_i32 s82, s76, 2560
v_and_b32 v182, 496, v248
v_mul_u32_u24 v181, 1280, v180
v_add_u32 v168, v181, v182
v_lshrrev_b32 v210, 1, v182
v_add_u32 v181, v181, v210
s_lshl_b32 s77, %[swave], 12
s_add_u32 s65, s77, %[slds]
s_lshl_b32 s89, %[swave], 17
v_bfe_u32 v210, v127, 2, 3
v_and_b32 v212, 3, v127
v_lshlrev_b32 v213, 4, v212
v_lshl_add_u32 v213, v180, 6, v213
v_lshrrev_b32 v253, 3, v127
v_lshlrev_b32 v253, 13, v253
v_and_b32 v254, 112, v248
v_or_b32 v253, v253, v254
v_and_b32 v254, 16, v127
v_lshlrev_b32 v254, 2, v254
v_xor_b32 v253, v253, v254
v_add_u32 v254, 128, v253
s_mul_i32 s96, %[swave], 4112
s_add_u32 s96, s96, %[slds]
s_lshl_b32 s77, s50, 2
s_add_u32 s77, s77, %[swave]
s_mul_i32 s68, s77, 81920
s_and_b32 s62, %[sb], 1
s_mul_i32 s63, s62, 40960
s_add_u32 s68, s68, s63
s_lshl_b32 s63, s63, 1
s_mul_i32 s83, s77, 560128
s_add_u32 s83, s83, 2752512
s_lshl_b32 s93, s77, 9
s_lshl_b32 s94, %[swave], 13
s_add_u32 s94, s94, %[slds]
v_lshrrev_b32 v211, 3, v127
v_mul_u32_u24 v220, 1280, v211
v_lshrrev_b32 v212, 4, v127
v_and_b32 v211, 7, v127
v_xor_b32 v211, v211, v212
v_lshl_add_u32 v220, v211, 4, v220
v_xor_b32 v221, 64, v220
s_bitcmp1_b32 s84, 0
s_mov_b32 s77, 0x77552200
s_cselect_b32 s77, 0x77552200, s77
s_lshl_b32 s91, s50, 2
s_lshr_b32 s77, s77, s91
s_and_b32 s77, s77, 15
s_lshl_b32 s86, s77, 7
s_lshl_b32 s85, s77, 19
s_lshl_b32 s90, s49, 8
s_sub_u32 s18, 5242880, s90
s_mov_b32 s19, 131072
s_mov_b32 s34, 2621440
s_mov_b32 s35, 131072
s_mov_b32 s22, 67108864
s_mov_b32 s23, 131072
s_add_u32 s87, s89, s85
s_add_u32 s85, s85, 524288
s_cmp_ge_u32 s85, 5242880
s_cselect_b32 s85, 0, s85
s_add_u32 m0, s96, 65536
s_waitcnt lgkmcnt(0)
s_add_u32 s16, s16, s90
s_addc_u32 s17, s17, 0
buffer_load_dwordx4 v253, s[16:19], s87 offen offset:0 lds
s_add_u32 m0, s96, 66560
s_nop 0
buffer_load_dwordx4 v254, s[16:19], s87 offen offset:0 lds
s_add_u32 s87, s87, 65536
s_add_u32 m0, s96, 67592
s_nop 0
buffer_load_dwordx4 v253, s[16:19], s87 offen offset:0 lds
s_add_u32 m0, s96, 68616
s_nop 0
buffer_load_dwordx4 v254, s[16:19], s87 offen offset:0 lds
s_add_u32 s88, s68, s86
s_add_u32 s86, s86, 128
s_cmp_ge_u32 s86, 1280
s_cselect_b32 s86, 0, s86
s_add_u32 m0, s94, 0
s_add_u32 s95, s88, 0
buffer_load_dwordx4 v220, s[32:35], s95 offen offset:0 lds
s_add_u32 s95, s88, 9216
buffer_load_dwordx4 v221, s[32:35], s95 offen offset:1024 lds
s_add_u32 s95, s88, 18432
buffer_load_dwordx4 v220, s[32:35], s95 offen offset:2048 lds
s_add_u32 s95, s88, 27648
buffer_load_dwordx4 v221, s[32:35], s95 offen offset:3072 lds
s_load_dwordx2 s[8:9], %[ka], 24
s_load_dwordx2 s[12:13], %[ka], 40
s_load_dwordx2 s[24:25], %[ka], 48
s_load_dwordx2 s[44:45], %[ka], 32
s_load_dwordx2 s[28:29], %[ka], 72
s_load_dwordx2 s[40:41], %[ka], 80
s_lshl_b32 s77, %[swave], 2
s_and_b32 s77, s77, 8
s_add_u32 s77, s77, 56
s_load_dwordx2 s[36:37], %[ka], s77
s_mov_b32 s66, 32768
buffer_load_dword v169, off, s[20:23], s66 sc1
buffer_load_dwordx4 v[128:131], v168, s[32:35], s82 offen offset:0
buffer_load_dwordx4 v[132:135], v168, s[32:35], s82 offen offset:512
buffer_load_dwordx2 v[136:137], v181, s[32:35], s82 offen offset:1024
s_add_u32 s82, s82, 131072
s_sub_u32 s88, s88, s63
s_add_u32 m0, s94, 4096
s_add_u32 s95, s88, 40960
buffer_load_dwordx4 v220, s[32:35], s95 offen offset:0 lds
s_add_u32 s95, s88, 50176
buffer_load_dwordx4 v221, s[32:35], s95 offen offset:1024 lds
s_add_u32 s95, s88, 59392
buffer_load_dwordx4 v220, s[32:35], s95 offen offset:2048 lds
s_add_u32 s95, s88, 68608
buffer_load_dwordx4 v221, s[32:35], s95 offen offset:3072 lds
v_and_b32 v211, 15, v127
v_bfe_u32 v222, v127, 1, 3
v_xor_b32 v222, v222, v212
v_lshlrev_b32 v222, 4, v222
v_lshl_add_u32 v222, v211, 7, v222
v_xor_b32 v223, 64, v222
v_add_u32 v222, s94, v222
v_add_u32 v223, s94, v223
v_and_b32 v214, 1, v212
v_lshlrev_b32 v215, 5, v214
v_xor_b32 v215, v215, v213
v_lshl_add_u32 v235, v210, 8, v215
v_add_u32 v235, 128, v235
v_and_b32 v234, 1, v127
v_mul_u32_u24 v234, 43690, v234
v_add_u32 v234, 17476, v234
v_bfe_u32 v215, v127, 2, 2
v_and_b32 v216, 3, v127
v_lshlrev_b32 v252, 7, v215
v_lshl_add_u32 v252, v216, 4, v252
v_and_b32 v251, 8, v127
v_lshl_add_u32 v252, v251, 3, v252
v_mul_u32_u24 v251, 2056, v212
s_xor_b32 s77, %[swave], 0
s_and_b32 s91, s77, 1
s_lshl_b32 s91, s91, 6
s_lshr_b32 s77, s77, 1
s_lshl_b32 s77, s77, 10
s_add_u32 s77, s77, %[slds]
s_add_u32 s77, s77, 65536
v_xor_b32 v192, s91, v252
v_add3_u32 v192, s77, v192, v251
v_add_u32 v196, 49344, v192
s_xor_b32 s77, %[swave], 1
s_and_b32 s91, s77, 1
s_lshl_b32 s91, s91, 6
s_lshr_b32 s77, s77, 1
s_lshl_b32 s77, s77, 10
s_add_u32 s77, s77, %[slds]
s_add_u32 s77, s77, 65536
v_xor_b32 v193, s91, v252
v_add3_u32 v193, s77, v193, v251
v_add_u32 v197, 49344, v193
s_xor_b32 s77, %[swave], 2
s_and_b32 s91, s77, 1
s_lshl_b32 s91, s91, 6
s_lshr_b32 s77, s77, 1
s_lshl_b32 s77, s77, 10
s_add_u32 s77, s77, %[slds]
s_add_u32 s77, s77, 65536
v_xor_b32 v194, s91, v252
v_add3_u32 v194, s77, v194, v251
v_add_u32 v198, 49344, v194
s_xor_b32 s77, %[swave], 3
s_and_b32 s91, s77, 1
s_lshl_b32 s91, s91, 6
s_lshr_b32 s77, s77, 1
s_lshl_b32 s77, s77, 10
s_add_u32 s77, s77, %[slds]
s_add_u32 s77, s77, 65536
v_xor_b32 v195, s91, v252
v_add3_u32 v195, s77, v195, v251
v_add_u32 v199, 49344, v195
v_mov_b32 v236, 0x3f803f80
v_mov_b32 v184, 0
v_mov_b32 v237, 0x3f803f80
v_mov_b32 v185, 0
v_mov_b32 v238, 0x3f803f80
v_mov_b32 v188, 0
v_mov_b32 v239, 0x3f803f80
v_mov_b32 v189, 0
s_add_u32 s87, s89, s85
s_add_u32 s85, s85, 524288
s_cmp_ge_u32 s85, 5242880
s_cselect_b32 s85, 0, s85
s_add_u32 m0, s96, 81984
s_nop 0
buffer_load_dwordx4 v253, s[16:19], s87 offen offset:0 lds
s_add_u32 m0, s96, 83008
s_nop 0
buffer_load_dwordx4 v254, s[16:19], s87 offen offset:0 lds
s_add_u32 s87, s87, 65536
s_add_u32 m0, s96, 84040
s_nop 0
buffer_load_dwordx4 v253, s[16:19], s87 offen offset:0 lds
s_add_u32 m0, s96, 85064
s_nop 0
buffer_load_dwordx4 v254, s[16:19], s87 offen offset:0 lds
s_getpc_b64 s[98:99]
LTA0_%=:
s_min_u32 s73, %[swave], 3
s_lshl_b32 s73, s73, 12
s_add_u32 s73, s73, LPG1_%=-LTA0_%=
s_add_u32 s98, s98, s73
s_addc_u32 s99, s99, 0
s_mov_b64 exec, 0
s_setpc_b64 s[98:99]
LTR0_%=:
s_mov_b64 exec, -1
s_waitcnt lgkmcnt(0)
s_add_u32 s88, s68, s86
s_add_u32 s86, s86, 128
s_cmp_ge_u32 s86, 1280
s_cselect_b32 s86, 0, s86
s_add_u32 m0, s94, 32768
s_add_u32 s95, s88, 0
buffer_load_dwordx4 v220, s[32:35], s95 offen offset:0 lds
s_add_u32 s95, s88, 9216
buffer_load_dwordx4 v221, s[32:35], s95 offen offset:1024 lds
s_add_u32 s95, s88, 18432
buffer_load_dwordx4 v220, s[32:35], s95 offen offset:2048 lds
s_add_u32 s95, s88, 27648
buffer_load_dwordx4 v221, s[32:35], s95 offen offset:3072 lds
s_sub_u32 s88, s88, s63
s_add_u32 m0, s94, 36864
s_add_u32 s95, s88, 40960
buffer_load_dwordx4 v220, s[32:35], s95 offen offset:0 lds
s_add_u32 s95, s88, 50176
buffer_load_dwordx4 v221, s[32:35], s95 offen offset:1024 lds
s_add_u32 s95, s88, 59392
buffer_load_dwordx4 v220, s[32:35], s95 offen offset:2048 lds
s_add_u32 s95, s88, 68608
buffer_load_dwordx4 v221, s[32:35], s95 offen offset:3072 lds
s_waitcnt vmcnt(20)
RD a[128:131],v222 offset:0
RD a[132:135],v223 offset:0
RD a[136:139],v222 offset:2048
RD a[140:143],v223 offset:2048
s_barrier
TR a[0:1],v192 offset:0
TR a[2:3],v192 offset:512
TR a[4:5],v192 offset:8224
TR a[6:7],v192 offset:8736
TR a[8:9],v192 offset:8
TR a[10:11],v192 offset:520
TR a[12:13],v192 offset:8232
TR a[14:15],v192 offset:8744
TR a[16:17],v193 offset:0
TR a[18:19],v193 offset:512
TR a[20:21],v193 offset:8224
TR a[22:23],v193 offset:8736
TR a[24:25],v193 offset:8
TR a[26:27],v193 offset:520
TR a[28:29],v193 offset:8232
TR a[30:31],v193 offset:8744
s_waitcnt lgkmcnt(15)
s_waitcnt lgkmcnt(12)
MM v[0:3],a[0:3],a[128:131],0
s_add_u32 s87, s89, s85
TR a[32:33],v194 offset:0
TR a[34:35],v194 offset:512
MM v[0:3],a[4:7],a[132:135],v[0:3]
s_add_u32 s85, s85, 524288
TR a[36:37],v194 offset:8224
TR a[38:39],v194 offset:8736
MM v[8:11],a[4:7],a[140:143],0
s_waitcnt vmcnt(16)
TR a[40:41],v194 offset:8
TR a[42:43],v194 offset:520
MM v[8:11],a[0:3],a[136:139],v[8:11]
s_waitcnt lgkmcnt(14)
TR a[44:45],v194 offset:8232
TR a[46:47],v194 offset:8744
s_cmp_ge_u32 s85, 5242880
s_cselect_b32 s85, 0, s85
MM v[4:7],a[8:11],a[128:131],0
s_add_u32 m0, s96, 98432
v_max3_f32 v200, |v0|, |v1|, |v2|
v_max_f32 v200, v200, |v3|
v_readfirstlane_b32 s69, v169
MM v[4:7],a[12:15],a[132:135],v[4:7]
s_lshl_b32 s90, s76, 1
TR a[48:49],v195 offset:0
TR a[50:51],v195 offset:512
MM v[12:15],a[12:15],a[140:143],0
s_and_b32 s77, s90, 15
buffer_load_dwordx4 v253, s[16:19], s87 offen offset:0 lds
v_max3_f32 v201, |v8|, |v9|, |v10|
v_max_f32 v201, v201, |v11|
MM v[12:15],a[8:11],a[136:139],v[12:15]
s_add_u32 m0, s96, 99456
TR a[52:53],v195 offset:8224
v_min_f32 v200, v200, v201
v_and_b32 v200, 0x7f800000, v200
v_lshlrev_b32 v148, 16, v128
v_smfmac_f32_16x16x64_bf16 v[184:187], v[236:239], a[0:7], v234
s_lshl_b32 s77, s77, 1
v_add_u32 v200, 0x2c00000, v200
v_smfmac_f32_16x16x64_bf16 v[188:191], v[236:239], a[8:15], v234
s_waitcnt lgkmcnt(15)
v_min_u32 v200, 0x78400000, v200
v_mov_b32 v201, v200
v_mov_b32 v202, v200
v_mov_b32 v203, v200
s_nop 1
MM v[32:35],a[16:19],a[128:131],v[200:203]
s_bfe_u32 s91, s90, 65540
TR a[54:55],v195 offset:8736
buffer_load_dwordx4 v254, s[16:19], s87 offen offset:0 lds
v_mov_b32 v250, v200
v_and_b32 v149, 0xffff0000, v128
MM v[32:35],a[20:23],a[132:135],v[32:35]
s_add_u32 s87, s87, 65536
TR a[56:57],v195 offset:8
TR a[58:59],v195 offset:520
MM v[40:43],a[20:23],a[140:143],v[200:203]
s_add_u32 m0, s96, 100488
TR a[60:61],v195 offset:8232
TR a[62:63],v195 offset:8744
MM v[40:43],a[16:19],a[136:139],v[40:43]
s_waitcnt lgkmcnt(15)
buffer_load_dwordx4 v253, s[16:19], s87 offen offset:0 lds
v_lshlrev_b32 v150, 16, v129
MM v[36:39],a[24:27],a[128:131],v[200:203]
s_add_u32 m0, s96, 101512
v_and_b32 v151, 0xffff0000, v129
v_lshlrev_b32 v152, 16, v130
MM v[36:39],a[28:31],a[132:135],v[36:39]
s_add_u32 s77, s77, s91
v_and_b32 v153, 0xffff0000, v130
v_lshlrev_b32 v154, 16, v131
MM v[44:47],a[28:31],a[140:143],v[200:203]
s_and_b32 s91, s90, 0xffffffe0
buffer_load_dwordx4 v254, s[16:19], s87 offen offset:0 lds
MM v[44:47],a[24:27],a[136:139],v[44:47]
s_waitcnt lgkmcnt(12)
s_waitcnt vmcnt(16)
RD a[144:147],v222 offset:4096
v_and_b32 v155, 0xffff0000, v131
MM v[64:67],a[32:35],a[128:131],v[200:203]
s_add_u32 s77, s77, s91
RD a[148:151],v223 offset:4096
RD a[152:155],v222 offset:6144
MM v[64:67],a[36:39],a[132:135],v[64:67]
s_lshl_b32 s77, s77, 3
RD a[156:159],v223 offset:6144
v_lshlrev_b32 v156, 16, v132
MM v[72:75],a[36:39],a[140:143],v[200:203]
s_add_u32 s97, s77, 36864
s_waitcnt vmcnt(12)
s_barrier
v_and_b32 v157, 0xffff0000, v132
MM v[72:75],a[32:35],a[136:139],v[72:75]
s_waitcnt lgkmcnt(12)
global_load_dwordx4 v[138:141], v182, s[44:45] offset:0
MM v[68:71],a[40:43],a[128:131],v[200:203]
s_sub_u32 s79, 65535, s90
v_lshlrev_b32 v158, 16, v133
v_and_b32 v159, 0xffff0000, v133
MM v[68:71],a[44:47],a[132:135],v[68:71]
s_lshl_b32 s78, s90, 2
v_lshlrev_b32 v160, 16, v134
v_and_b32 v161, 0xffff0000, v134
MM v[76:79],a[44:47],a[140:143],v[200:203]
s_add_u32 s78, s78, 53248
global_load_dwordx4 v[142:145], v182, s[44:45] offset:512
MM v[76:79],a[40:43],a[136:139],v[76:79]
s_waitcnt lgkmcnt(8)
v_lshrrev_b32 v182, 1, v182
v_lshlrev_b32 v162, 16, v135
MM v[96:99],a[48:51],a[128:131],v[200:203]
s_lshl_b32 s80, s76, 2
global_load_dwordx2 v[146:147], v182, s[44:45] offset:1024
MM v[96:99],a[52:55],a[132:135],v[96:99]
)ASM"
R"ASM(
s_waitcnt lgkmcnt(0)
s_add_u32 s88, s68, s86
s_add_u32 s86, s86, 128
v_and_b32 v163, 0xffff0000, v135
MM v[104:107],a[52:55],a[140:143],v[200:203]
s_add_u32 s80, s80, 81920
s_cmp_ge_u32 s86, 1280
s_cselect_b32 s86, 0, s86
MM v[104:107],a[48:51],a[136:139],v[104:107]
s_add_u32 m0, s94, 0
s_add_u32 s95, s88, 0
v_lshlrev_b32 v164, 16, v136
MM v[100:103],a[56:59],a[128:131],v[200:203]
s_mov_b32 s42, 2621440
buffer_load_dwordx4 v220, s[32:35], s95 offen offset:0 lds
MM v[100:103],a[60:63],a[132:135],v[100:103]
s_add_u32 s95, s88, 9216
v_and_b32 v165, 0xffff0000, v136
v_lshlrev_b32 v166, 16, v137
MM v[108:111],a[60:63],a[140:143],v[200:203]
s_mov_b32 s43, 131072
buffer_load_dwordx4 v221, s[32:35], s95 offen offset:1024 lds
MM v[108:111],a[56:59],a[136:139],v[108:111]
s_add_u32 s95, s88, 18432
v_and_b32 v167, 0xffff0000, v137
s_add_u32 s69, s69, 1
MM v[16:19],a[0:3],a[144:147],0
v_add_f32 v170, v148, v149
v_add_f32 v171, v150, v151
MM v[16:19],a[4:7],a[148:151],v[16:19]
v_add_f32 v170, v170, v152
v_add_f32 v171, v171, v153
MM v[24:27],a[4:7],a[156:159],0
buffer_load_dwordx4 v220, s[32:35], s95 offen offset:2048 lds
MM v[24:27],a[0:3],a[152:155],v[24:27]
s_add_u32 s95, s88, 27648
v_add_f32 v170, v170, v154
v_add_f32 v171, v171, v155
MM v[20:23],a[8:11],a[144:147],0
v_max3_f32 v204, |v16|, |v17|, |v18|
v_max_f32 v204, v204, |v19|
MM v[20:23],a[12:15],a[148:151],v[20:23]
TR a[0:1],v192 offset:16448
TR a[2:3],v192 offset:16960
v_add_f32 v170, v170, v156
MM v[28:31],a[12:15],a[156:159],0
v_max3_f32 v205, |v24|, |v25|, |v26|
v_max_f32 v205, v205, |v27|
MM v[28:31],a[8:11],a[152:155],v[28:31]
TR a[4:5],v192 offset:24672
v_min_f32 v204, v204, v205
v_and_b32 v204, 0x7f800000, v204
v_add_u32 v204, 0x2c00000, v204
v_min_u32 v204, 0x78400000, v204
v_mov_b32 v205, v204
v_mov_b32 v206, v204
v_mov_b32 v207, v204
s_nop 1
MM v[48:51],a[16:19],a[144:147],v[204:207]
TR a[6:7],v192 offset:25184
buffer_load_dwordx4 v221, s[32:35], s95 offen offset:3072 lds
v_mov_b32 v252, v204
v_add_f32 v171, v171, v157
MM v[48:51],a[20:23],a[148:151],v[48:51]
s_sub_u32 s88, s88, s63
TR a[8:9],v192 offset:16456
TR a[10:11],v192 offset:16968
MM v[56:59],a[20:23],a[156:159],v[204:207]
s_add_u32 m0, s94, 4096
TR a[12:13],v192 offset:24680
TR a[14:15],v192 offset:25192
MM v[56:59],a[16:19],a[152:155],v[56:59]
s_add_u32 s95, s88, 40960
buffer_load_dwordx4 v220, s[32:35], s95 offen offset:0 lds
v_add_f32 v170, v170, v158
MM v[52:55],a[24:27],a[144:147],v[204:207]
s_add_u32 s95, s88, 50176
TR a[16:17],v193 offset:16448
TR a[18:19],v193 offset:16960
MM v[52:55],a[28:31],a[148:151],v[52:55]
TR a[20:21],v193 offset:24672
TR a[22:23],v193 offset:25184
MM v[60:63],a[28:31],a[156:159],v[204:207]
buffer_load_dwordx4 v221, s[32:35], s95 offen offset:1024 lds
v_add_f32 v171, v171, v159
MM v[60:63],a[24:27],a[152:155],v[60:63]
s_add_u32 s95, s88, 59392
v_add_f32 v170, v170, v160
v_add_f32 v171, v171, v161
MM v[80:83],a[32:35],a[144:147],v[204:207]
TR a[24:25],v193 offset:16456
TR a[26:27],v193 offset:16968
MM v[80:83],a[36:39],a[148:151],v[80:83]
TR a[28:29],v193 offset:24680
TR a[30:31],v193 offset:25192
v_add_f32 v170, v170, v162
MM v[88:91],a[36:39],a[156:159],v[204:207]
buffer_load_dwordx4 v220, s[32:35], s95 offen offset:2048 lds
MM v[88:91],a[32:35],a[152:155],v[88:91]
s_add_u32 s95, s88, 68608
v_add_f32 v171, v171, v163
v_add_f32 v170, v170, v164
MM v[84:87],a[40:43],a[144:147],v[204:207]
TR a[32:33],v194 offset:16448
TR a[34:35],v194 offset:16960
MM v[84:87],a[44:47],a[148:151],v[84:87]
TR a[36:37],v194 offset:24672
TR a[38:39],v194 offset:25184
v_add_f32 v171, v171, v165
MM v[92:95],a[44:47],a[156:159],v[204:207]
buffer_load_dwordx4 v221, s[32:35], s95 offen offset:3072 lds
MM v[92:95],a[40:43],a[152:155],v[92:95]
s_add_u32 s87, s89, s85
s_add_u32 s85, s85, 524288
v_add_f32 v170, v170, v166
MM v[112:115],a[48:51],a[144:147],v[204:207]
TR a[40:41],v194 offset:16456
TR a[42:43],v194 offset:16968
MM v[112:115],a[52:55],a[148:151],v[112:115]
TR a[44:45],v194 offset:24680
TR a[46:47],v194 offset:25192
s_cmp_ge_u32 s85, 5242880
s_cselect_b32 s85, 0, s85
v_add_f32 v171, v171, v167
MM v[120:123],a[52:55],a[156:159],v[204:207]
s_add_u32 m0, s96, 114880
s_nop 0
buffer_load_dwordx4 v253, s[16:19], s87 offen offset:0 lds
MM v[120:123],a[48:51],a[152:155],v[120:123]
s_add_u32 m0, s96, 115904
v_add_f32 v172, v170, v171
v_lshlrev_b32 v232, 11, v180
MM v[116:119],a[56:59],a[144:147],v[204:207]
TR a[48:49],v195 offset:16448
TR a[50:51],v195 offset:16960
MM v[116:119],a[60:63],a[148:151],v[116:119]
TR a[52:53],v195 offset:24672
TR a[54:55],v195 offset:25184
MM v[124:127],a[60:63],a[156:159],v[204:207]
buffer_load_dwordx4 v254, s[16:19], s87 offen offset:0 lds
v_add_f32_dpp v172, v172, v172 row_ror:8 row_mask:0xf bank_mask:0xf
MM v[124:127],a[56:59],a[152:155],v[124:127]
s_add_u32 s87, s87, 65536
s_add_u32 m0, s96, 116936
v_add_f32_dpp v172, v172, v172 row_ror:4 row_mask:0xf bank_mask:0xf
buffer_load_dwordx4 v253, s[16:19], s87 offen offset:0 lds
s_add_u32 m0, s96, 117960
s_nop 0
buffer_load_dwordx4 v254, s[16:19], s87 offen offset:0 lds
s_waitcnt vmcnt(19)
RD a[160:163],v222 offset:32768
RD a[164:167],v223 offset:32768
RD a[168:171],v222 offset:34816
RD a[172:175],v223 offset:34816
RD a[176:179],v222 offset:36864
RD a[180:183],v223 offset:36864
RD a[184:187],v222 offset:38912
RD a[188:191],v223 offset:38912
s_waitcnt vmcnt(15) lgkmcnt(0)
s_barrier
MM v[0:3],a[0:3],a[160:163],v[0:3]
TR a[56:57],v195 offset:16456
TR a[58:59],v195 offset:16968
MM v[0:3],a[4:7],a[164:167],v[0:3]
TR a[60:61],v195 offset:24680
TR a[62:63],v195 offset:25192
MM v[8:11],a[4:7],a[172:175],v[8:11]
v_add_f32_dpp v172, v172, v172 row_ror:2 row_mask:0xf bank_mask:0xf
v_lshl_add_u32 v232, v214, 8, v232
MM v[8:11],a[0:3],a[168:171],v[8:11]
s_add_u32 s88, s68, s86
s_add_u32 s86, s86, 128
MM v[16:19],a[0:3],a[176:179],v[16:19]
s_cmp_ge_u32 s86, 1280
s_cselect_b32 s86, 0, s86
s_add_u32 m0, s94, 32768
MM v[16:19],a[4:7],a[180:183],v[16:19]
s_add_u32 s95, s88, 0
buffer_load_dwordx4 v220, s[32:35], s95 offen offset:0 lds
v_add_f32_dpp v172, v172, v172 row_ror:1 row_mask:0xf bank_mask:0xf
MM v[24:27],a[4:7],a[188:191],v[24:27]
s_add_u32 s95, s88, 9216
v_mov_b32 v173, v172
v_lshl_add_u32 v232, v215, 6, v232
MM v[24:27],a[0:3],a[184:187],v[24:27]
buffer_load_dwordx4 v221, s[32:35], s95 offen offset:1024 lds
MM v[4:7],a[8:11],a[160:163],v[4:7]
s_add_u32 s95, s88, 18432
v_permlane16_swap_b32 v172, v173
v_add_f32 v172, v172, v173
MM v[4:7],a[12:15],a[164:167],v[4:7]
v_mul_f32 v172, 0x3acccccd, v172
v_sub_f32 v176, v148, v172
MM v[12:15],a[12:15],a[172:175],v[12:15]
v_sub_f32 v177, v149, v172
v_mul_f32 v170, v176, v176
MM v[12:15],a[8:11],a[168:171],v[12:15]
buffer_load_dwordx4 v220, s[32:35], s95 offen offset:2048 lds
MM v[20:23],a[8:11],a[176:179],v[20:23]
s_add_u32 s95, s88, 27648
v_mul_f32 v171, v177, v177
v_sub_f32 v176, v150, v172
MM v[20:23],a[12:15],a[180:183],v[20:23]
v_sub_f32 v177, v151, v172
v_fma_f32 v170, v176, v176, v170
MM v[28:31],a[12:15],a[188:191],v[28:31]
v_fma_f32 v171, v177, v177, v171
v_sub_f32 v176, v152, v172
MM v[28:31],a[8:11],a[184:187],v[28:31]
buffer_load_dwordx4 v221, s[32:35], s95 offen offset:3072 lds
v_smfmac_f32_16x16x64_bf16 v[184:187], v[236:239], a[0:7], v234
s_sub_u32 s88, s88, s63
s_add_u32 m0, s94, 36864
v_sub_f32 v177, v153, v172
v_smfmac_f32_16x16x64_bf16 v[188:191], v[236:239], a[8:15], v234
s_add_u32 s95, s88, 40960
v_fma_f32 v170, v176, v176, v170
MM v[32:35],a[16:19],a[160:163],v[32:35]
TR a[0:1],v192 offset:32896
TR a[2:3],v192 offset:33408
MM v[32:35],a[20:23],a[164:167],v[32:35]
TR a[4:5],v192 offset:41120
TR a[6:7],v192 offset:41632
v_fma_f32 v171, v177, v177, v171
MM v[40:43],a[20:23],a[172:175],v[40:43]
LPG1_%=:
s_cbranch_execz LPG5_%=
TR a[8:9],v192 offset:32904
TR a[10:11],v192 offset:33416
MM v[40:43],a[16:19],a[168:171],v[40:43]
TR a[12:13],v192 offset:41128
TR a[14:15],v192 offset:41640
buffer_load_dwordx4 v220, s[32:35], s95 offen offset:0 lds
MM v[48:51],a[16:19],a[176:179],v[48:51]
s_add_u32 s95, s88, 50176
v_sub_f32 v176, v154, v172
v_sub_f32 v177, v155, v172
MM v[48:51],a[20:23],a[180:183],v[48:51]
buffer_load_dwordx4 v221, s[32:35], s95 offen offset:1024 lds
MM v[56:59],a[20:23],a[188:191],v[56:59]
s_add_u32 s95, s88, 59392
v_fma_f32 v170, v176, v176, v170
v_fma_f32 v171, v177, v177, v171
MM v[56:59],a[16:19],a[184:187],v[56:59]
v_sub_f32 v176, v156, v172
v_sub_f32 v177, v157, v172
MM v[36:39],a[24:27],a[160:163],v[36:39]
TR a[16:17],v193 offset:32896
TR a[18:19],v193 offset:33408
MM v[36:39],a[28:31],a[164:167],v[36:39]
TR a[20:21],v193 offset:41120
TR a[22:23],v193 offset:41632
v_fma_f32 v170, v176, v176, v170
MM v[44:47],a[28:31],a[172:175],v[44:47]
buffer_load_dwordx4 v220, s[32:35], s95 offen offset:2048 lds
MM v[44:47],a[24:27],a[168:171],v[44:47]
s_add_u32 s95, s88, 68608
v_fma_f32 v171, v177, v177, v171
v_sub_f32 v176, v158, v172
MM v[52:55],a[24:27],a[176:179],v[52:55]
v_sub_f32 v177, v159, v172
v_fma_f32 v170, v176, v176, v170
MM v[52:55],a[28:31],a[180:183],v[52:55]
buffer_load_dwordx4 v221, s[32:35], s95 offen offset:3072 lds
MM v[60:63],a[28:31],a[188:191],v[60:63]
s_add_u32 s87, s89, s85
s_add_u32 s85, s85, 524288
v_fma_f32 v171, v177, v177, v171
MM v[60:63],a[24:27],a[184:187],v[60:63]
s_cmp_ge_u32 s85, 5242880
s_cselect_b32 s85, 0, s85
s_add_u32 m0, s96, 131328
MM v[64:67],a[32:35],a[160:163],v[64:67]
TR a[24:25],v193 offset:32904
TR a[26:27],v193 offset:33416
v_sub_f32 v176, v160, v172
MM v[64:67],a[36:39],a[164:167],v[64:67]
TR a[28:29],v193 offset:41128
TR a[30:31],v193 offset:41640
MM v[72:75],a[36:39],a[172:175],v[72:75]
buffer_load_dwordx4 v253, s[16:19], s87 offen offset:0 lds
MM v[72:75],a[32:35],a[168:171],v[72:75]
s_add_u32 m0, s96, 132352
v_sub_f32 v177, v161, v172
v_fma_f32 v170, v176, v176, v170
MM v[80:83],a[32:35],a[176:179],v[80:83]
v_fma_f32 v171, v177, v177, v171
v_sub_f32 v176, v162, v172
MM v[80:83],a[36:39],a[180:183],v[80:83]
buffer_load_dwordx4 v254, s[16:19], s87 offen offset:0 lds
MM v[88:91],a[36:39],a[188:191],v[88:91]
s_add_u32 s87, s87, 65536
s_add_u32 m0, s96, 133384
v_sub_f32 v177, v163, v172
MM v[88:91],a[32:35],a[184:187],v[88:91]
v_fma_f32 v170, v176, v176, v170
v_fma_f32 v171, v177, v177, v171
MM v[68:71],a[40:43],a[160:163],v[68:71]
TR a[32:33],v194 offset:32896
TR a[34:35],v194 offset:33408
MM v[68:71],a[44:47],a[164:167],v[68:71]
TR a[36:37],v194 offset:41120
TR a[38:39],v194 offset:41632
v_sub_f32 v176, v164, v172
MM v[76:79],a[44:47],a[172:175],v[76:79]
buffer_load_dwordx4 v253, s[16:19], s87 offen offset:0 lds
MM v[76:79],a[40:43],a[168:171],v[76:79]
s_add_u32 m0, s96, 134408
v_sub_f32 v177, v165, v172
v_fma_f32 v170, v176, v176, v170
MM v[84:87],a[40:43],a[176:179],v[84:87]
v_fma_f32 v171, v177, v177, v171
v_sub_f32 v176, v166, v172
MM v[84:87],a[44:47],a[180:183],v[84:87]
buffer_load_dwordx4 v254, s[16:19], s87 offen offset:0 lds
MM v[92:95],a[44:47],a[188:191],v[92:95]
s_waitcnt vmcnt(16)
RD a[192:195],v222 offset:0
v_sub_f32 v177, v167, v172
MM v[92:95],a[40:43],a[184:187],v[92:95]
RD a[196:199],v223 offset:0
v_fma_f32 v170, v176, v176, v170
MM v[96:99],a[48:51],a[160:163],v[96:99]
TR a[40:41],v194 offset:32904
TR a[42:43],v194 offset:33416
MM v[96:99],a[52:55],a[164:167],v[96:99]
TR a[44:45],v194 offset:41128
TR a[46:47],v194 offset:41640
v_fma_f32 v171, v177, v177, v171
MM v[104:107],a[52:55],a[172:175],v[104:107]
RD a[200:203],v222 offset:2048
v_add_f32 v170, v170, v171
MM v[104:107],a[48:51],a[168:171],v[104:107]
RD a[204:207],v223 offset:2048
v_add_f32_dpp v170, v170, v170 row_ror:8 row_mask:0xf bank_mask:0xf
MM v[112:115],a[48:51],a[176:179],v[112:115]
RD a[208:211],v222 offset:4096
v_add_f32_dpp v170, v170, v170 row_ror:4 row_mask:0xf bank_mask:0xf
MM v[112:115],a[52:55],a[180:183],v[112:115]
RD a[212:215],v223 offset:4096
v_add_f32_dpp v170, v170, v170 row_ror:2 row_mask:0xf bank_mask:0xf
MM v[120:123],a[52:55],a[188:191],v[120:123]
RD a[216:219],v222 offset:6144
v_add_f32_dpp v170, v170, v170 row_ror:1 row_mask:0xf bank_mask:0xf
MM v[120:123],a[48:51],a[184:187],v[120:123]
)ASM"
R"ASM(
s_waitcnt lgkmcnt(15)
RD a[220:223],v223 offset:6144
v_mov_b32 v173, v170
MM v[100:103],a[56:59],a[160:163],v[100:103]
TR a[48:49],v195 offset:32896
TR a[50:51],v195 offset:33408
MM v[100:103],a[60:63],a[164:167],v[100:103]
TR a[52:53],v195 offset:41120
TR a[54:55],v195 offset:41632
MM v[108:111],a[60:63],a[172:175],v[108:111]
v_permlane16_swap_b32 v170, v173
v_add_f32 v170, v170, v173
MM v[108:111],a[56:59],a[168:171],v[108:111]
s_waitcnt lgkmcnt(4)
s_add_u32 s88, s68, s86
s_add_u32 s86, s86, 128
MM v[116:119],a[56:59],a[176:179],v[116:119]
s_cmp_ge_u32 s86, 1280
s_cselect_b32 s86, 0, s86
s_add_u32 m0, s94, 0
v_mul_f32 v170, 0x3acccccd, v170
MM v[116:119],a[60:63],a[180:183],v[116:119]
s_add_u32 s95, s88, 0
buffer_load_dwordx4 v220, s[32:35], s95 offen offset:0 lds
MM v[124:127],a[60:63],a[188:191],v[124:127]
s_add_u32 s95, s88, 9216
CV v178,v172,v170
v_lshlrev_b32 v174, 16, v178
MM v[124:127],a[56:59],a[184:187],v[124:127]
buffer_load_dwordx4 v221, s[32:35], s95 offen offset:1024 lds
s_waitcnt vmcnt(14)
s_barrier
MM v[0:3],a[0:3],a[192:195],v[0:3]
s_add_u32 s95, s88, 18432
TR a[56:57],v195 offset:32904
TR a[58:59],v195 offset:33416
v_and_b32 v170, 0xffff0000, v178
MM v[0:3],a[4:7],a[196:199],v[0:3]
TR a[60:61],v195 offset:41128
TR a[62:63],v195 offset:41640
MM v[8:11],a[4:7],a[204:207],v[8:11]
v_add_f32 v170, 0x3727c5ac, v170
CV v170,v170,v170
MM v[8:11],a[0:3],a[200:203],v[8:11]
buffer_load_dwordx4 v220, s[32:35], s95 offen offset:2048 lds
MM v[16:19],a[0:3],a[208:211],v[16:19]
s_add_u32 s95, s88, 27648
v_and_b32 v170, 0xffff0000, v170
v_lshl_add_u32 v232, v216, 3, v232
MM v[16:19],a[4:7],a[212:215],v[16:19]
v_sqrt_f32 v170, v170
MM v[24:27],a[4:7],a[220:223],v[24:27]
v_mov_b32 v179, s69
CV v178,v174,v170
MM v[24:27],a[0:3],a[216:219],v[24:27]
buffer_load_dwordx4 v221, s[32:35], s95 offen offset:3072 lds
MM v[4:7],a[8:11],a[192:195],v[4:7]
s_sub_u32 s88, s88, s63
s_add_u32 m0, s94, 4096
s_add_u32 s95, s88, 40960
v_and_b32 v170, 0xffff0000, v178
MM v[4:7],a[12:15],a[196:199],v[4:7]
v_rcp_f32 v175, v170
MM v[12:15],a[12:15],a[204:207],v[12:15]
v_lshlrev_b32 v173, 4, v180
v_lshl_add_u32 v232, v214, 5, v232
MM v[12:15],a[8:11],a[200:203],v[12:15]
buffer_load_dwordx4 v220, s[32:35], s95 offen offset:0 lds
MM v[20:23],a[8:11],a[208:211],v[20:23]
s_add_u32 s95, s88, 50176
s_mov_b32 exec_lo, 1
s_mov_b32 exec_hi, 1
buffer_store_dwordx2 v[178:179], v173, s[20:23], s97 offen offset:0 sc1
s_mov_b64 exec, -1
MM v[20:23],a[12:15],a[212:215],v[20:23]
v_sub_f32 v148, v148, v174
v_sub_f32 v149, v149, v174
MM v[28:31],a[12:15],a[220:223],v[28:31]
v_sub_f32 v150, v150, v174
v_sub_f32 v151, v151, v174
MM v[28:31],a[8:11],a[216:219],v[28:31]
buffer_load_dwordx4 v221, s[32:35], s95 offen offset:1024 lds
v_smfmac_f32_16x16x64_bf16 v[184:187], v[236:239], a[0:7], v234
s_add_u32 s95, s88, 59392
v_sub_f32 v152, v152, v174
v_smfmac_f32_16x16x64_bf16 v[188:191], v[236:239], a[8:15], v234
v_sub_f32 v153, v153, v174
MM v[32:35],a[16:19],a[192:195],v[32:35]
TR a[0:1],v196 offset:0
TR a[2:3],v196 offset:512
MM v[32:35],a[20:23],a[196:199],v[32:35]
TR a[4:5],v196 offset:8224
TR a[6:7],v196 offset:8736
v_sub_f32 v154, v154, v174
MM v[40:43],a[20:23],a[204:207],v[40:43]
TR a[8:9],v196 offset:8
TR a[10:11],v196 offset:520
MM v[40:43],a[16:19],a[200:203],v[40:43]
TR a[12:13],v196 offset:8232
TR a[14:15],v196 offset:8744
buffer_load_dwordx4 v220, s[32:35], s95 offen offset:2048 lds
MM v[48:51],a[16:19],a[208:211],v[48:51]
s_add_u32 s95, s88, 68608
v_sub_f32 v155, v155, v174
v_sub_f32 v156, v156, v174
MM v[48:51],a[20:23],a[212:215],v[48:51]
buffer_load_dwordx4 v221, s[32:35], s95 offen offset:3072 lds
MM v[56:59],a[20:23],a[220:223],v[56:59]
s_add_u32 s87, s89, s85
s_add_u32 s85, s85, 524288
v_sub_f32 v157, v157, v174
MM v[56:59],a[16:19],a[216:219],v[56:59]
s_cmp_ge_u32 s85, 5242880
s_cselect_b32 s85, 0, s85
s_add_u32 m0, s96, 65536
MM v[36:39],a[24:27],a[192:195],v[36:39]
TR a[16:17],v197 offset:0
TR a[18:19],v197 offset:512
v_sub_f32 v158, v158, v174
MM v[36:39],a[28:31],a[196:199],v[36:39]
TR a[20:21],v197 offset:8224
TR a[22:23],v197 offset:8736
MM v[44:47],a[28:31],a[204:207],v[44:47]
buffer_load_dwordx4 v253, s[16:19], s87 offen offset:0 lds
MM v[44:47],a[24:27],a[200:203],v[44:47]
s_add_u32 m0, s96, 66560
v_sub_f32 v159, v159, v174
v_sub_f32 v160, v160, v174
MM v[52:55],a[24:27],a[208:211],v[52:55]
v_sub_f32 v161, v161, v174
v_sub_f32 v162, v162, v174
MM v[52:55],a[28:31],a[212:215],v[52:55]
buffer_load_dwordx4 v254, s[16:19], s87 offen offset:0 lds
MM v[60:63],a[28:31],a[220:223],v[60:63]
s_add_u32 s87, s87, 65536
s_add_u32 m0, s96, 67592
v_sub_f32 v163, v163, v174
MM v[60:63],a[24:27],a[216:219],v[60:63]
v_sub_f32 v164, v164, v174
v_sub_f32 v165, v165, v174
MM v[64:67],a[32:35],a[192:195],v[64:67]
TR a[24:25],v197 offset:8
TR a[26:27],v197 offset:520
MM v[64:67],a[36:39],a[196:199],v[64:67]
TR a[28:29],v197 offset:8232
TR a[30:31],v197 offset:8744
v_sub_f32 v166, v166, v174
MM v[72:75],a[36:39],a[204:207],v[72:75]
buffer_load_dwordx4 v253, s[16:19], s87 offen offset:0 lds
MM v[72:75],a[32:35],a[200:203],v[72:75]
s_add_u32 m0, s96, 68616
v_sub_f32 v167, v167, v174
CV v128,v148,v149
MM v[80:83],a[32:35],a[208:211],v[80:83]
CV v129,v150,v151
CV v130,v152,v153
MM v[80:83],a[36:39],a[212:215],v[80:83]
buffer_load_dwordx4 v254, s[16:19], s87 offen offset:0 lds
MM v[88:91],a[36:39],a[220:223],v[88:91]
s_waitcnt vmcnt(17)
RD a[224:227],v222 offset:32768
CV v131,v154,v155
MM v[88:91],a[32:35],a[216:219],v[88:91]
RD a[228:231],v223 offset:32768
CV v132,v156,v157
MM v[68:71],a[40:43],a[192:195],v[68:71]
TR a[32:33],v198 offset:0
TR a[34:35],v198 offset:512
MM v[68:71],a[44:47],a[196:199],v[68:71]
TR a[36:37],v198 offset:8224
TR a[38:39],v198 offset:8736
CV v133,v158,v159
MM v[76:79],a[44:47],a[204:207],v[76:79]
RD a[232:235],v222 offset:34816
CV v134,v160,v161
MM v[76:79],a[40:43],a[200:203],v[76:79]
RD a[236:239],v223 offset:34816
CV v135,v162,v163
MM v[84:87],a[40:43],a[208:211],v[84:87]
RD a[240:243],v222 offset:36864
CV v136,v164,v165
MM v[84:87],a[44:47],a[212:215],v[84:87]
RD a[244:247],v223 offset:36864
CV v137,v166,v167
MM v[92:95],a[44:47],a[220:223],v[92:95]
RD a[248:251],v222 offset:38912
v_lshlrev_b32 v148, 16, v128
MM v[92:95],a[40:43],a[216:219],v[92:95]
s_waitcnt lgkmcnt(15)
RD a[252:255],v223 offset:38912
v_and_b32 v149, 0xffff0000, v128
MM v[96:99],a[48:51],a[192:195],v[96:99]
TR a[40:41],v198 offset:8
TR a[42:43],v198 offset:520
MM v[96:99],a[52:55],a[196:199],v[96:99]
TR a[44:45],v198 offset:8232
TR a[46:47],v198 offset:8744
v_lshlrev_b32 v150, 16, v129
MM v[104:107],a[52:55],a[204:207],v[104:107]
v_and_b32 v151, 0xffff0000, v129
v_lshlrev_b32 v152, 16, v130
MM v[104:107],a[48:51],a[200:203],v[104:107]
s_waitcnt lgkmcnt(4)
s_add_u32 s88, s68, s86
s_add_u32 s86, s86, 128
MM v[112:115],a[48:51],a[208:211],v[112:115]
s_cmp_ge_u32 s86, 1280
s_cselect_b32 s86, 0, s86
s_add_u32 m0, s94, 32768
v_and_b32 v153, 0xffff0000, v130
MM v[112:115],a[52:55],a[212:215],v[112:115]
s_add_u32 s95, s88, 0
buffer_load_dwordx4 v220, s[32:35], s95 offen offset:0 lds
MM v[120:123],a[52:55],a[220:223],v[120:123]
s_add_u32 s95, s88, 9216
v_lshlrev_b32 v154, 16, v131
v_and_b32 v155, 0xffff0000, v131
MM v[120:123],a[48:51],a[216:219],v[120:123]
buffer_load_dwordx4 v221, s[32:35], s95 offen offset:1024 lds
MM v[100:103],a[56:59],a[192:195],v[100:103]
s_add_u32 s95, s88, 18432
TR a[48:49],v199 offset:0
TR a[50:51],v199 offset:512
v_lshlrev_b32 v156, 16, v132
MM v[100:103],a[60:63],a[196:199],v[100:103]
TR a[52:53],v199 offset:8224
TR a[54:55],v199 offset:8736
MM v[108:111],a[60:63],a[204:207],v[108:111]
v_and_b32 v157, 0xffff0000, v132
v_lshlrev_b32 v158, 16, v133
MM v[108:111],a[56:59],a[200:203],v[108:111]
buffer_load_dwordx4 v220, s[32:35], s95 offen offset:2048 lds
MM v[116:119],a[56:59],a[208:211],v[116:119]
s_add_u32 s95, s88, 27648
v_and_b32 v159, 0xffff0000, v133
v_lshlrev_b32 v160, 16, v134
MM v[116:119],a[60:63],a[212:215],v[116:119]
v_and_b32 v161, 0xffff0000, v134
v_lshlrev_b32 v162, 16, v135
MM v[124:127],a[60:63],a[220:223],v[124:127]
v_and_b32 v163, 0xffff0000, v135
v_lshlrev_b32 v164, 16, v136
MM v[124:127],a[56:59],a[216:219],v[124:127]
buffer_load_dwordx4 v221, s[32:35], s95 offen offset:3072 lds
s_waitcnt vmcnt(17)
s_barrier
MM v[0:3],a[0:3],a[224:227],v[0:3]
s_sub_u32 s88, s88, s63
TR a[56:57],v199 offset:8
TR a[58:59],v199 offset:520
v_and_b32 v165, 0xffff0000, v136
MM v[0:3],a[4:7],a[228:231],v[0:3]
s_add_u32 m0, s94, 36864
TR a[60:61],v199 offset:8232
TR a[62:63],v199 offset:8744
MM v[8:11],a[4:7],a[236:239],v[8:11]
s_add_u32 s95, s88, 40960
v_lshlrev_b32 v166, 16, v137
v_and_b32 v167, 0xffff0000, v137
MM v[8:11],a[0:3],a[232:235],v[8:11]
buffer_load_dwordx4 v220, s[32:35], s95 offen offset:0 lds
MM v[16:19],a[0:3],a[240:243],v[16:19]
s_add_u32 s95, s88, 50176
v_mul_f32 v148, v148, v175
v_mul_f32 v149, v149, v175
MM v[16:19],a[4:7],a[244:247],v[16:19]
v_mul_f32 v150, v150, v175
v_mul_f32 v151, v151, v175
MM v[24:27],a[4:7],a[252:255],v[24:27]
v_mul_f32 v152, v152, v175
v_mul_f32 v153, v153, v175
MM v[24:27],a[0:3],a[248:251],v[24:27]
buffer_load_dwordx4 v221, s[32:35], s95 offen offset:1024 lds
MM v[4:7],a[8:11],a[224:227],v[4:7]
s_add_u32 s95, s88, 59392
v_mul_f32 v154, v154, v175
v_mul_f32 v155, v155, v175
MM v[4:7],a[12:15],a[228:231],v[4:7]
v_mul_f32 v156, v156, v175
v_mul_f32 v157, v157, v175
MM v[12:15],a[12:15],a[236:239],v[12:15]
v_mul_f32 v158, v158, v175
v_mul_f32 v159, v159, v175
MM v[12:15],a[8:11],a[232:235],v[12:15]
buffer_load_dwordx4 v220, s[32:35], s95 offen offset:2048 lds
MM v[20:23],a[8:11],a[240:243],v[20:23]
s_add_u32 s95, s88, 68608
v_mul_f32 v160, v160, v175
v_mul_f32 v161, v161, v175
MM v[20:23],a[12:15],a[244:247],v[20:23]
v_mul_f32 v162, v162, v175
v_mul_f32 v163, v163, v175
MM v[28:31],a[12:15],a[252:255],v[28:31]
v_mul_f32 v164, v164, v175
v_mul_f32 v165, v165, v175
MM v[28:31],a[8:11],a[248:251],v[28:31]
buffer_load_dwordx4 v221, s[32:35], s95 offen offset:3072 lds
v_smfmac_f32_16x16x64_bf16 v[184:187], v[236:239], a[0:7], v234
s_add_u32 s87, s89, s85
s_add_u32 s85, s85, 524288
v_mul_f32 v166, v166, v175
v_smfmac_f32_16x16x64_bf16 v[188:191], v[236:239], a[8:15], v234
s_cmp_ge_u32 s85, 5242880
s_cselect_b32 s85, 0, s85
MM v[32:35],a[16:19],a[224:227],v[32:35]
s_add_u32 m0, s96, 81984
TR a[0:1],v196 offset:16448
TR a[2:3],v196 offset:16960
MM v[32:35],a[20:23],a[228:231],v[32:35]
TR a[4:5],v196 offset:24672
TR a[6:7],v196 offset:25184
v_mul_f32 v167, v167, v175
MM v[40:43],a[20:23],a[236:239],v[40:43]
TR a[8:9],v196 offset:16456
TR a[10:11],v196 offset:16968
MM v[40:43],a[16:19],a[232:235],v[40:43]
TR a[12:13],v196 offset:24680
TR a[14:15],v196 offset:25192
buffer_load_dwordx4 v253, s[16:19], s87 offen offset:0 lds
MM v[48:51],a[16:19],a[240:243],v[48:51]
s_add_u32 m0, s96, 83008
CV v128,v148,v149
CV v129,v150,v151
MM v[48:51],a[20:23],a[244:247],v[48:51]
buffer_load_dwordx4 v254, s[16:19], s87 offen offset:0 lds
MM v[56:59],a[20:23],a[252:255],v[56:59]
s_add_u32 s87, s87, 65536
s_add_u32 m0, s96, 84040
CV v130,v152,v153
MM v[56:59],a[16:19],a[248:251],v[56:59]
CV v131,v154,v155
CV v132,v156,v157
MM v[36:39],a[24:27],a[224:227],v[36:39]
TR a[16:17],v197 offset:16448
TR a[18:19],v197 offset:16960
MM v[36:39],a[28:31],a[228:231],v[36:39]
TR a[20:21],v197 offset:24672
TR a[22:23],v197 offset:25184
CV v133,v158,v159
MM v[44:47],a[28:31],a[236:239],v[44:47]
buffer_load_dwordx4 v253, s[16:19], s87 offen offset:0 lds
MM v[44:47],a[24:27],a[232:235],v[44:47]
s_add_u32 m0, s96, 85064
CV v134,v160,v161
CV v135,v162,v163
MM v[52:55],a[24:27],a[240:243],v[52:55]
CV v136,v164,v165
CV v137,v166,v167
MM v[52:55],a[28:31],a[244:247],v[52:55]
buffer_load_dwordx4 v254, s[16:19], s87 offen offset:0 lds
MM v[60:63],a[28:31],a[252:255],v[60:63]
s_waitcnt vmcnt(16)
RD a[128:131],v222 offset:0
v_xor_b32 v233, 32, v232
MM v[60:63],a[24:27],a[248:251],v[60:63]
s_add_u32 s77, %[slds], 65536
RD a[132:135],v223 offset:0
v_add_u32 v232, s77, v232
MM v[64:67],a[32:35],a[224:227],v[64:67]
TR a[24:25],v197 offset:16456
)ASM"
R"ASM(
TR a[26:27],v197 offset:16968
MM v[64:67],a[36:39],a[228:231],v[64:67]
TR a[28:29],v197 offset:24680
TR a[30:31],v197 offset:25192
MM v[72:75],a[36:39],a[236:239],v[72:75]
RD a[136:139],v222 offset:2048
v_add_u32 v233, s77, v233
MM v[72:75],a[32:35],a[232:235],v[72:75]
RD a[140:143],v223 offset:2048
buffer_store_dwordx4 v[128:131], v168, s[20:23], s82 offen offset:0 sc1
v_lshlrev_b32 v214, 8, v211
MM v[80:83],a[32:35],a[240:243],v[80:83]
RD a[144:147],v222 offset:4096
v_add_u32 v214, %[slds], v214
MM v[80:83],a[36:39],a[244:247],v[80:83]
RD a[148:151],v223 offset:4096
v_xor_b32 v215, v211, v212
MM v[88:91],a[36:39],a[252:255],v[88:91]
s_lshl_b32 s77, %[swave], 2
RD a[152:155],v222 offset:6144
v_xor_b32 v213, s77, v215
MM v[88:91],a[32:35],a[248:251],v[88:91]
s_waitcnt lgkmcnt(15)
RD a[156:159],v223 offset:6144
buffer_store_dwordx4 v[132:135], v168, s[20:23], s82 offen offset:512 sc1
v_xor_b32 v216, 0, v213
MM v[68:71],a[40:43],a[224:227],v[68:71]
TR a[32:33],v198 offset:16448
TR a[34:35],v198 offset:16960
MM v[68:71],a[44:47],a[228:231],v[68:71]
TR a[36:37],v198 offset:24672
TR a[38:39],v198 offset:25184
MM v[76:79],a[44:47],a[236:239],v[76:79]
buffer_store_dwordx2 v[136:137], v181, s[20:23], s82 offen offset:1024 sc1
MM v[76:79],a[40:43],a[232:235],v[76:79]
s_waitcnt lgkmcnt(4)
s_add_u32 s88, s68, s86
s_add_u32 s86, s86, 128
MM v[84:87],a[40:43],a[240:243],v[84:87]
s_cmp_ge_u32 s86, 1280
s_cselect_b32 s86, 0, s86
MM v[84:87],a[44:47],a[244:247],v[84:87]
s_add_u32 m0, s94, 0
s_add_u32 s95, s88, 0
v_lshlrev_b32 v148, 16, v128
MM v[92:95],a[44:47],a[252:255],v[92:95]
buffer_load_dwordx4 v220, s[32:35], s95 offen offset:0 lds
MM v[92:95],a[40:43],a[248:251],v[92:95]
s_add_u32 s95, s88, 9216
v_and_b32 v149, 0xffff0000, v128
v_lshlrev_b32 v176, 16, v138
MM v[96:99],a[48:51],a[224:227],v[96:99]
TR a[40:41],v198 offset:16456
TR a[42:43],v198 offset:16968
MM v[96:99],a[52:55],a[228:231],v[96:99]
TR a[44:45],v198 offset:24680
TR a[46:47],v198 offset:25192
v_and_b32 v177, 0xffff0000, v138
MM v[104:107],a[52:55],a[236:239],v[104:107]
buffer_load_dwordx4 v221, s[32:35], s95 offen offset:1024 lds
MM v[104:107],a[48:51],a[232:235],v[104:107]
s_add_u32 s95, s88, 18432
v_mul_f32 v170, v148, v176
v_mul_f32 v171, v149, v177
MM v[112:115],a[48:51],a[240:243],v[112:115]
buffer_load_dwordx4 v220, s[32:35], s95 offen offset:2048 lds
MM v[112:115],a[52:55],a[244:247],v[112:115]
s_add_u32 s95, s88, 27648
v_lshlrev_b32 v148, 16, v129
v_and_b32 v149, 0xffff0000, v129
MM v[120:123],a[52:55],a[252:255],v[120:123]
v_lshlrev_b32 v176, 16, v139
v_and_b32 v177, 0xffff0000, v139
MM v[120:123],a[48:51],a[248:251],v[120:123]
v_fma_f32 v170, v148, v176, v170
v_fma_f32 v171, v149, v177, v171
MM v[100:103],a[56:59],a[224:227],v[100:103]
TR a[48:49],v199 offset:16448
TR a[50:51],v199 offset:16960
MM v[100:103],a[60:63],a[228:231],v[100:103]
TR a[52:53],v199 offset:24672
TR a[54:55],v199 offset:25184
v_lshlrev_b32 v148, 16, v130
MM v[108:111],a[60:63],a[236:239],v[108:111]
buffer_load_dwordx4 v221, s[32:35], s95 offen offset:3072 lds
MM v[108:111],a[56:59],a[232:235],v[108:111]
s_sub_u32 s88, s88, s63
s_add_u32 m0, s94, 4096
s_add_u32 s95, s88, 40960
MM v[116:119],a[56:59],a[240:243],v[116:119]
buffer_load_dwordx4 v220, s[32:35], s95 offen offset:0 lds
v_and_b32 v149, 0xffff0000, v130
MM v[116:119],a[60:63],a[244:247],v[116:119]
s_add_u32 s95, s88, 50176
v_lshlrev_b32 v176, 16, v140
v_and_b32 v177, 0xffff0000, v140
s_nop 0
LPG2_%=:
s_cbranch_execz LPG6_%=
MM v[124:127],a[60:63],a[252:255],v[124:127]
v_fma_f32 v170, v148, v176, v170
v_fma_f32 v171, v149, v177, v171
MM v[124:127],a[56:59],a[248:251],v[124:127]
v_lshlrev_b32 v148, 16, v131
v_and_b32 v149, 0xffff0000, v131
s_waitcnt vmcnt(20)
s_barrier
MM v[0:3],a[0:3],a[128:131],v[0:3]
TR a[56:57],v199 offset:16456
TR a[58:59],v199 offset:16968
MM v[0:3],a[4:7],a[132:135],v[0:3]
TR a[60:61],v199 offset:24680
TR a[62:63],v199 offset:25192
v_lshlrev_b32 v176, 16, v141
MM v[8:11],a[4:7],a[140:143],v[8:11]
buffer_load_dwordx4 v221, s[32:35], s95 offen offset:1024 lds
MM v[8:11],a[0:3],a[136:139],v[8:11]
s_add_u32 s95, s88, 59392
v_and_b32 v177, 0xffff0000, v141
v_fma_f32 v170, v148, v176, v170
MM v[16:19],a[0:3],a[144:147],v[16:19]
buffer_load_dwordx4 v220, s[32:35], s95 offen offset:2048 lds
MM v[16:19],a[4:7],a[148:151],v[16:19]
s_add_u32 s95, s88, 68608
v_fma_f32 v171, v149, v177, v171
v_lshlrev_b32 v148, 16, v132
MM v[24:27],a[4:7],a[156:159],v[24:27]
v_and_b32 v149, 0xffff0000, v132
v_lshlrev_b32 v176, 16, v142
MM v[24:27],a[0:3],a[152:155],v[24:27]
v_and_b32 v177, 0xffff0000, v142
v_fma_f32 v170, v148, v176, v170
MM v[4:7],a[8:11],a[128:131],v[4:7]
buffer_load_dwordx4 v221, s[32:35], s95 offen offset:3072 lds
MM v[4:7],a[12:15],a[132:135],v[4:7]
s_add_u32 s87, s89, s85
s_add_u32 s85, s85, 524288
v_fma_f32 v171, v149, v177, v171
MM v[12:15],a[12:15],a[140:143],v[12:15]
s_cmp_ge_u32 s85, 5242880
s_cselect_b32 s85, 0, s85
s_add_u32 m0, s96, 98432
MM v[12:15],a[8:11],a[136:139],v[12:15]
v_lshlrev_b32 v148, 16, v133
v_and_b32 v149, 0xffff0000, v133
MM v[20:23],a[8:11],a[144:147],v[20:23]
buffer_load_dwordx4 v253, s[16:19], s87 offen offset:0 lds
MM v[20:23],a[12:15],a[148:151],v[20:23]
s_add_u32 m0, s96, 99456
v_lshlrev_b32 v176, 16, v143
v_and_b32 v177, 0xffff0000, v143
MM v[28:31],a[12:15],a[156:159],v[28:31]
v_fma_f32 v170, v148, v176, v170
v_fma_f32 v171, v149, v177, v171
MM v[28:31],a[8:11],a[152:155],v[28:31]
v_lshlrev_b32 v148, 16, v134
v_and_b32 v149, 0xffff0000, v134
v_smfmac_f32_16x16x64_bf16 v[184:187], v[236:239], a[0:7], v234
v_lshlrev_b32 v176, 16, v144
v_smfmac_f32_16x16x64_bf16 v[188:191], v[236:239], a[8:15], v234
v_and_b32 v177, 0xffff0000, v144
MM v[32:35],a[16:19],a[128:131],v[32:35]
TR a[0:1],v192 offset:0
TR a[2:3],v192 offset:512
buffer_load_dwordx4 v254, s[16:19], s87 offen offset:0 lds
MM v[32:35],a[20:23],a[132:135],v[32:35]
s_add_u32 s87, s87, 65536
TR a[4:5],v192 offset:8224
TR a[6:7],v192 offset:8736
v_fma_f32 v170, v148, v176, v170
MM v[40:43],a[20:23],a[140:143],v[40:43]
s_add_u32 m0, s96, 100488
TR a[8:9],v192 offset:8
TR a[10:11],v192 offset:520
MM v[40:43],a[16:19],a[136:139],v[40:43]
TR a[12:13],v192 offset:8232
TR a[14:15],v192 offset:8744
MM v[48:51],a[16:19],a[144:147],v[48:51]
buffer_load_dwordx4 v253, s[16:19], s87 offen offset:0 lds
v_fma_f32 v171, v149, v177, v171
MM v[48:51],a[20:23],a[148:151],v[48:51]
s_add_u32 m0, s96, 101512
v_lshlrev_b32 v148, 16, v135
v_and_b32 v149, 0xffff0000, v135
MM v[56:59],a[20:23],a[156:159],v[56:59]
buffer_load_dwordx4 v254, s[16:19], s87 offen offset:0 lds
MM v[56:59],a[16:19],a[152:155],v[56:59]
s_waitcnt vmcnt(19)
RD a[160:163],v222 offset:32768
v_lshlrev_b32 v176, 16, v145
MM v[36:39],a[24:27],a[128:131],v[36:39]
TR a[16:17],v193 offset:0
TR a[18:19],v193 offset:512
MM v[36:39],a[28:31],a[132:135],v[36:39]
TR a[20:21],v193 offset:8224
TR a[22:23],v193 offset:8736
v_and_b32 v177, 0xffff0000, v145
MM v[44:47],a[28:31],a[140:143],v[44:47]
RD a[164:167],v223 offset:32768
v_fma_f32 v170, v148, v176, v170
MM v[44:47],a[24:27],a[136:139],v[44:47]
RD a[168:171],v222 offset:34816
v_fma_f32 v171, v149, v177, v171
MM v[52:55],a[24:27],a[144:147],v[52:55]
RD a[172:175],v223 offset:34816
v_lshlrev_b32 v148, 16, v136
MM v[52:55],a[28:31],a[148:151],v[52:55]
RD a[176:179],v222 offset:36864
v_and_b32 v149, 0xffff0000, v136
MM v[60:63],a[28:31],a[156:159],v[60:63]
RD a[180:183],v223 offset:36864
v_lshlrev_b32 v176, 16, v146
MM v[60:63],a[24:27],a[152:155],v[60:63]
s_waitcnt lgkmcnt(15)
RD a[184:187],v222 offset:38912
v_and_b32 v177, 0xffff0000, v146
MM v[64:67],a[32:35],a[128:131],v[64:67]
TR a[24:25],v193 offset:8
TR a[26:27],v193 offset:520
MM v[64:67],a[36:39],a[132:135],v[64:67]
TR a[28:29],v193 offset:8232
TR a[30:31],v193 offset:8744
v_fma_f32 v170, v148, v176, v170
MM v[72:75],a[36:39],a[140:143],v[72:75]
RD a[188:191],v223 offset:38912
v_fma_f32 v171, v149, v177, v171
MM v[72:75],a[32:35],a[136:139],v[72:75]
v_lshlrev_b32 v148, 16, v137
v_and_b32 v149, 0xffff0000, v137
MM v[80:83],a[32:35],a[144:147],v[80:83]
v_lshlrev_b32 v176, 16, v147
v_and_b32 v177, 0xffff0000, v147
MM v[80:83],a[36:39],a[148:151],v[80:83]
v_fma_f32 v170, v148, v176, v170
v_fma_f32 v171, v149, v177, v171
MM v[88:91],a[36:39],a[156:159],v[88:91]
s_waitcnt lgkmcnt(0)
s_add_u32 s88, s68, s86
s_add_u32 s86, s86, 128
MM v[88:91],a[32:35],a[152:155],v[88:91]
s_cmp_ge_u32 s86, 1280
s_cselect_b32 s86, 0, s86
s_add_u32 m0, s94, 32768
v_add_f32 v170, v170, v171
MM v[68:71],a[40:43],a[128:131],v[68:71]
s_add_u32 s95, s88, 0
TR a[32:33],v194 offset:0
TR a[34:35],v194 offset:512
MM v[68:71],a[44:47],a[132:135],v[68:71]
TR a[36:37],v194 offset:8224
TR a[38:39],v194 offset:8736
MM v[76:79],a[44:47],a[140:143],v[76:79]
buffer_load_dwordx4 v220, s[32:35], s95 offen offset:0 lds
v_add_f32_dpp v170, v170, v170 row_ror:8 row_mask:0xf bank_mask:0xf
MM v[76:79],a[40:43],a[136:139],v[76:79]
s_add_u32 s95, s88, 9216
v_add_f32_dpp v170, v170, v170 row_ror:4 row_mask:0xf bank_mask:0xf
v_lshl_add_u32 v224, v216, 4, v214
MM v[84:87],a[40:43],a[144:147],v[84:87]
buffer_load_dwordx4 v221, s[32:35], s95 offen offset:1024 lds
MM v[84:87],a[44:47],a[148:151],v[84:87]
s_add_u32 s95, s88, 18432
v_add_f32_dpp v170, v170, v170 row_ror:2 row_mask:0xf bank_mask:0xf
v_xor_b32 v216, 4, v213
MM v[92:95],a[44:47],a[156:159],v[92:95]
v_add_f32_dpp v170, v170, v170 row_ror:1 row_mask:0xf bank_mask:0xf
v_mov_b32 v173, v170
MM v[92:95],a[40:43],a[152:155],v[92:95]
s_nop 0
v_permlane16_swap_b32 v170, v173
v_add_f32 v170, v170, v173
MM v[96:99],a[48:51],a[128:131],v[96:99]
TR a[40:41],v194 offset:8
TR a[42:43],v194 offset:520
MM v[96:99],a[52:55],a[132:135],v[96:99]
TR a[44:45],v194 offset:8232
TR a[46:47],v194 offset:8744
CV v170,v170,v170
MM v[104:107],a[52:55],a[140:143],v[104:107]
buffer_load_dwordx4 v220, s[32:35], s95 offen offset:2048 lds
MM v[104:107],a[48:51],a[136:139],v[104:107]
s_add_u32 s95, s88, 27648
v_ashrrev_i32 v171, 31, v170
v_or_b32 v171, 0x80000000, v171
MM v[112:115],a[48:51],a[144:147],v[112:115]
buffer_load_dwordx4 v221, s[32:35], s95 offen offset:3072 lds
MM v[112:115],a[52:55],a[148:151],v[112:115]
s_sub_u32 s88, s88, s63
s_add_u32 m0, s94, 36864
s_add_u32 s95, s88, 40960
v_xor_b32 v170, v170, v171
MM v[120:123],a[52:55],a[156:159],v[120:123]
v_and_b32 v170, 0xffff0000, v170
v_sub_u32 v171, s79, v180
MM v[120:123],a[48:51],a[152:155],v[120:123]
v_or_b32 v170, v170, v171
v_lshlrev_b32 v173, 2, v180
MM v[100:103],a[56:59],a[128:131],v[100:103]
TR a[48:49],v195 offset:0
TR a[50:51],v195 offset:512
MM v[100:103],a[60:63],a[132:135],v[100:103]
TR a[52:53],v195 offset:8224
TR a[54:55],v195 offset:8736
MM v[108:111],a[60:63],a[140:143],v[108:111]
buffer_load_dwordx4 v220, s[32:35], s95 offen offset:0 lds
s_mov_b32 exec_lo, 1
s_mov_b32 exec_hi, 1
buffer_store_dword v170, v173, s[20:23], s78 offen offset:0 sc1
s_mov_b64 exec, -1
MM v[108:111],a[56:59],a[136:139],v[108:111]
s_add_u32 s95, s88, 50176
v_mov_b32 v171, s69
v_lshl_add_u32 v225, v216, 4, v214
MM v[116:119],a[56:59],a[144:147],v[116:119]
buffer_load_dwordx4 v221, s[32:35], s95 offen offset:1024 lds
MM v[116:119],a[60:63],a[148:151],v[116:119]
s_add_u32 s95, s88, 59392
v_xor_b32 v216, 8, v213
v_lshl_add_u32 v226, v216, 4, v214
MM v[124:127],a[60:63],a[156:159],v[124:127]
v_xor_b32 v216, 12, v213
v_lshl_add_u32 v227, v216, 4, v214
MM v[124:127],a[56:59],a[152:155],v[124:127]
v_lshlrev_b32 v214, 8, v212
v_xor_b32 v216, 0, v215
s_waitcnt vmcnt(22)
s_barrier
MM v[0:3],a[0:3],a[160:163],v[0:3]
TR a[56:57],v195 offset:8
TR a[58:59],v195 offset:520
MM v[0:3],a[4:7],a[164:167],v[0:3]
TR a[60:61],v195 offset:8232
TR a[62:63],v195 offset:8744
MM v[8:11],a[4:7],a[172:175],v[8:11]
buffer_load_dwordx4 v220, s[32:35], s95 offen offset:2048 lds
MM v[8:11],a[0:3],a[168:171],v[8:11]
s_add_u32 s95, s88, 68608
v_lshl_add_u32 v228, v216, 4, v214
v_xor_b32 v216, 4, v215
MM v[16:19],a[0:3],a[176:179],v[16:19]
buffer_load_dwordx4 v221, s[32:35], s95 offen offset:3072 lds
MM v[16:19],a[4:7],a[180:183],v[16:19]
s_add_u32 s87, s89, s85
)ASM"
R"ASM(
s_add_u32 s85, s85, 524288
v_lshl_add_u32 v229, v216, 4, v214
MM v[24:27],a[4:7],a[188:191],v[24:27]
s_cmp_ge_u32 s85, 5242880
s_cselect_b32 s85, 0, s85
s_add_u32 m0, s96, 114880
MM v[24:27],a[0:3],a[184:187],v[24:27]
v_xor_b32 v216, 8, v215
v_lshl_add_u32 v230, v216, 4, v214
MM v[4:7],a[8:11],a[160:163],v[4:7]
buffer_load_dwordx4 v253, s[16:19], s87 offen offset:0 lds
MM v[4:7],a[12:15],a[164:167],v[4:7]
s_add_u32 m0, s96, 115904
v_xor_b32 v216, 12, v215
v_lshl_add_u32 v231, v216, 4, v214
MM v[12:15],a[12:15],a[172:175],v[12:15]
s_lshl_b32 s90, s49, 19
s_add_u32 s8, s8, s90
s_addc_u32 s9, s9, 0
MM v[12:15],a[8:11],a[168:171],v[12:15]
s_mov_b32 s10, 524288
s_mov_b32 s11, 131072
MM v[20:23],a[8:11],a[176:179],v[20:23]
buffer_load_dwordx4 v254, s[16:19], s87 offen offset:0 lds
MM v[20:23],a[12:15],a[180:183],v[20:23]
s_add_u32 s87, s87, 65536
s_add_u32 s12, s12, s90
s_addc_u32 s13, s13, 0
MM v[28:31],a[12:15],a[188:191],v[28:31]
s_add_u32 m0, s96, 116936
s_mov_b32 s14, 525312
s_mov_b32 s15, 131072
MM v[28:31],a[8:11],a[184:187],v[28:31]
s_sub_u32 s12, s12, 1024
s_subb_u32 s13, s13, 0
s_lshr_b32 s90, s49, 3
v_smfmac_f32_16x16x64_bf16 v[184:187], v[236:239], a[0:7], v234
s_mul_i32 s90, s90, 1310720
s_mul_i32 s77, s84, 320
v_smfmac_f32_16x16x64_bf16 v[188:191], v[236:239], a[8:15], v234
s_add_u32 s90, s90, s77
MM v[32:35],a[16:19],a[160:163],v[32:35]
TR a[0:1],v192 offset:16448
TR a[2:3],v192 offset:16960
buffer_load_dwordx4 v253, s[16:19], s87 offen offset:0 lds
MM v[32:35],a[20:23],a[164:167],v[32:35]
s_add_u32 m0, s96, 117960
TR a[4:5],v192 offset:24672
TR a[6:7],v192 offset:25184
MM v[40:43],a[20:23],a[172:175],v[40:43]
TR a[8:9],v192 offset:16456
TR a[10:11],v192 offset:16968
MM v[40:43],a[16:19],a[168:171],v[40:43]
TR a[12:13],v192 offset:24680
TR a[14:15],v192 offset:25192
MM v[48:51],a[16:19],a[176:179],v[48:51]
buffer_load_dwordx4 v254, s[16:19], s87 offen offset:0 lds
MM v[48:51],a[20:23],a[180:183],v[48:51]
s_waitcnt vmcnt(17)
s_add_u32 s24, s24, s90
s_addc_u32 s25, s25, 0
MM v[56:59],a[20:23],a[188:191],v[56:59]
s_sub_u32 s26, 5242880, s90
RD a[192:195],v222 offset:0
s_mov_b32 s27, 131072
MM v[56:59],a[16:19],a[184:187],v[56:59]
s_mov_b32 s38, 2211840
RD a[196:199],v223 offset:0
s_mov_b32 s39, 131072
MM v[36:39],a[24:27],a[160:163],v[36:39]
s_mov_b32 s71, 0x4e800000
TR a[16:17],v193 offset:16448
TR a[18:19],v193 offset:16960
MM v[36:39],a[28:31],a[164:167],v[36:39]
s_mov_b32 s72, 0x3e0293ee
TR a[20:21],v193 offset:24672
TR a[22:23],v193 offset:25184
MM v[44:47],a[28:31],a[172:175],v[44:47]
s_lshl_b32 s64, %[swave], 12
RD a[200:203],v222 offset:2048
MM v[44:47],a[24:27],a[168:171],v[44:47]
RD a[204:207],v223 offset:2048
MM v[52:55],a[24:27],a[176:179],v[52:55]
RD a[208:211],v222 offset:4096
MM v[52:55],a[28:31],a[180:183],v[52:55]
RD a[212:215],v223 offset:4096
MM v[60:63],a[28:31],a[188:191],v[60:63]
RD a[216:219],v222 offset:6144
MM v[60:63],a[24:27],a[184:187],v[60:63]
s_waitcnt lgkmcnt(15)
RD a[220:223],v223 offset:6144
MM v[64:67],a[32:35],a[160:163],v[64:67]
TR a[24:25],v193 offset:16456
TR a[26:27],v193 offset:16968
MM v[64:67],a[36:39],a[164:167],v[64:67]
TR a[28:29],v193 offset:24680
TR a[30:31],v193 offset:25192
MM v[72:75],a[36:39],a[172:175],v[72:75]
MM v[72:75],a[32:35],a[168:171],v[72:75]
s_waitcnt lgkmcnt(4)
s_add_u32 s88, s68, s86
s_add_u32 s86, s86, 128
MM v[80:83],a[32:35],a[176:179],v[80:83]
s_cmp_ge_u32 s86, 1280
s_cselect_b32 s86, 0, s86
s_add_u32 m0, s94, 0
MM v[80:83],a[36:39],a[180:183],v[80:83]
s_add_u32 s95, s88, 0
buffer_load_dwordx4 v220, s[32:35], s95 offen offset:0 lds
MM v[88:91],a[36:39],a[188:191],v[88:91]
s_add_u32 s95, s88, 9216
MM v[88:91],a[32:35],a[184:187],v[88:91]
buffer_load_dwordx4 v221, s[32:35], s95 offen offset:1024 lds
MM v[68:71],a[40:43],a[160:163],v[68:71]
s_add_u32 s95, s88, 18432
TR a[32:33],v194 offset:16448
TR a[34:35],v194 offset:16960
MM v[68:71],a[44:47],a[164:167],v[68:71]
TR a[36:37],v194 offset:24672
TR a[38:39],v194 offset:25184
MM v[76:79],a[44:47],a[172:175],v[76:79]
MM v[76:79],a[40:43],a[168:171],v[76:79]
buffer_load_dwordx4 v220, s[32:35], s95 offen offset:2048 lds
MM v[84:87],a[40:43],a[176:179],v[84:87]
s_add_u32 s95, s88, 27648
MM v[84:87],a[44:47],a[180:183],v[84:87]
MM v[92:95],a[44:47],a[188:191],v[92:95]
MM v[92:95],a[40:43],a[184:187],v[92:95]
buffer_load_dwordx4 v221, s[32:35], s95 offen offset:3072 lds
MM v[96:99],a[48:51],a[160:163],v[96:99]
s_sub_u32 s88, s88, s63
TR a[40:41],v194 offset:16456
TR a[42:43],v194 offset:16968
MM v[96:99],a[52:55],a[164:167],v[96:99]
s_add_u32 m0, s94, 4096
TR a[44:45],v194 offset:24680
TR a[46:47],v194 offset:25192
MM v[104:107],a[52:55],a[172:175],v[104:107]
s_add_u32 s95, s88, 40960
MM v[104:107],a[48:51],a[168:171],v[104:107]
buffer_load_dwordx4 v220, s[32:35], s95 offen offset:0 lds
MM v[112:115],a[48:51],a[176:179],v[112:115]
s_add_u32 s95, s88, 50176
MM v[112:115],a[52:55],a[180:183],v[112:115]
MM v[120:123],a[52:55],a[188:191],v[120:123]
MM v[120:123],a[48:51],a[184:187],v[120:123]
buffer_load_dwordx4 v221, s[32:35], s95 offen offset:1024 lds
MM v[100:103],a[56:59],a[160:163],v[100:103]
s_add_u32 s95, s88, 59392
TR a[48:49],v195 offset:16448
TR a[50:51],v195 offset:16960
MM v[100:103],a[60:63],a[164:167],v[100:103]
TR a[52:53],v195 offset:24672
TR a[54:55],v195 offset:25184
MM v[108:111],a[60:63],a[172:175],v[108:111]
MM v[108:111],a[56:59],a[168:171],v[108:111]
buffer_load_dwordx4 v220, s[32:35], s95 offen offset:2048 lds
MM v[116:119],a[56:59],a[176:179],v[116:119]
s_add_u32 s95, s88, 68608
MM v[116:119],a[60:63],a[180:183],v[116:119]
MM v[124:127],a[60:63],a[188:191],v[124:127]
MM v[124:127],a[56:59],a[184:187],v[124:127]
buffer_load_dwordx4 v221, s[32:35], s95 offen offset:3072 lds
s_waitcnt vmcnt(21)
s_barrier
MM v[0:3],a[0:3],a[192:195],v[0:3]
s_add_u32 s87, s89, s85
TR a[56:57],v195 offset:16456
TR a[58:59],v195 offset:16968
MM v[0:3],a[4:7],a[196:199],v[0:3]
s_add_u32 s85, s85, 524288
TR a[60:61],v195 offset:24680
TR a[62:63],v195 offset:25192
MM v[8:11],a[4:7],a[204:207],v[8:11]
s_cmp_ge_u32 s85, 5242880
s_cselect_b32 s85, 0, s85
s_add_u32 m0, s96, 131328
v_and_b32 v208, 240, v248
MM v[8:11],a[0:3],a[200:203],v[8:11]
s_lshl_b32 s62, s62, 8
buffer_load_dwordx4 v253, s[16:19], s87 offen offset:0 lds
MM v[16:19],a[0:3],a[208:211],v[16:19]
s_add_u32 m0, s96, 132352
s_add_u32 s93, s93, 36864
s_add_u32 s62, s62, s93
MM v[16:19],a[4:7],a[212:215],v[16:19]
s_xor_b32 s93, s62, 256
MM v[24:27],a[4:7],a[220:223],v[24:27]
MM v[24:27],a[0:3],a[216:219],v[24:27]
buffer_load_dwordx4 v254, s[16:19], s87 offen offset:0 lds
MM v[4:7],a[8:11],a[192:195],v[4:7]
s_add_u32 s87, s87, 65536
s_add_u32 m0, s96, 133384
MM v[4:7],a[12:15],a[196:199],v[4:7]
MM v[12:15],a[12:15],a[204:207],v[12:15]
MM v[12:15],a[8:11],a[200:203],v[12:15]
buffer_load_dwordx4 v253, s[16:19], s87 offen offset:0 lds
MM v[20:23],a[8:11],a[208:211],v[20:23]
s_add_u32 m0, s96, 134408
MM v[20:23],a[12:15],a[212:215],v[20:23]
MM v[28:31],a[12:15],a[220:223],v[28:31]
MM v[28:31],a[8:11],a[216:219],v[28:31]
buffer_load_dwordx4 v254, s[16:19], s87 offen offset:0 lds
v_smfmac_f32_16x16x64_bf16 v[184:187], v[236:239], a[0:7], v234
s_waitcnt vmcnt(16)
RD a[224:227],v222 offset:32768
v_smfmac_f32_16x16x64_bf16 v[188:191], v[236:239], a[8:15], v234
RD a[228:231],v223 offset:32768
MM v[32:35],a[16:19],a[192:195],v[32:35]
TR a[0:1],v192 offset:32896
TR a[2:3],v192 offset:33408
MM v[32:35],a[20:23],a[196:199],v[32:35]
TR a[4:5],v192 offset:41120
TR a[6:7],v192 offset:41632
MM v[40:43],a[20:23],a[204:207],v[40:43]
TR a[8:9],v192 offset:32904
TR a[10:11],v192 offset:33416
RD a[232:235],v222 offset:34816
MM v[40:43],a[16:19],a[200:203],v[40:43]
TR a[12:13],v192 offset:41128
TR a[14:15],v192 offset:41640
buffer_load_dwordx4 v[200:203], v208, s[20:23], s62 offen offset:0 sc1
MM v[48:51],a[16:19],a[208:211],v[48:51]
RD a[236:239],v223 offset:34816
MM v[48:51],a[20:23],a[212:215],v[48:51]
buffer_load_dwordx4 v[204:207], v208, s[20:23], s93 offen offset:0 sc1
MM v[56:59],a[20:23],a[220:223],v[56:59]
RD a[240:243],v222 offset:36864
MM v[56:59],a[16:19],a[216:219],v[56:59]
s_waitcnt lgkmcnt(15)
RD a[244:247],v223 offset:36864
MM v[36:39],a[24:27],a[192:195],v[36:39]
TR a[16:17],v193 offset:32896
TR a[18:19],v193 offset:33408
MM v[36:39],a[28:31],a[196:199],v[36:39]
TR a[20:21],v193 offset:41120
TR a[22:23],v193 offset:41632
MM v[44:47],a[28:31],a[204:207],v[44:47]
RD a[248:251],v222 offset:38912
MM v[44:47],a[24:27],a[200:203],v[44:47]
RD a[252:255],v223 offset:38912
MM v[52:55],a[24:27],a[208:211],v[52:55]
MM v[52:55],a[28:31],a[212:215],v[52:55]
MM v[60:63],a[28:31],a[220:223],v[60:63]
MM v[60:63],a[24:27],a[216:219],v[60:63]
s_waitcnt lgkmcnt(0)
s_add_u32 s88, s68, s86
s_add_u32 s86, s86, 128
MM v[64:67],a[32:35],a[192:195],v[64:67]
TR a[24:25],v193 offset:32904
TR a[26:27],v193 offset:33416
MM v[64:67],a[36:39],a[196:199],v[64:67]
TR a[28:29],v193 offset:41128
TR a[30:31],v193 offset:41640
s_cmp_ge_u32 s86, 1280
s_cselect_b32 s86, 0, s86
MM v[72:75],a[36:39],a[204:207],v[72:75]
s_add_u32 m0, s94, 32768
s_add_u32 s95, s88, 0
MM v[72:75],a[32:35],a[200:203],v[72:75]
buffer_load_dwordx4 v220, s[32:35], s95 offen offset:0 lds
MM v[80:83],a[32:35],a[208:211],v[80:83]
s_add_u32 s95, s88, 9216
MM v[80:83],a[36:39],a[212:215],v[80:83]
buffer_load_dwordx4 v221, s[32:35], s95 offen offset:1024 lds
MM v[88:91],a[36:39],a[220:223],v[88:91]
s_add_u32 s95, s88, 18432
MM v[88:91],a[32:35],a[216:219],v[88:91]
MM v[68:71],a[40:43],a[192:195],v[68:71]
TR a[32:33],v194 offset:32896
TR a[34:35],v194 offset:33408
MM v[68:71],a[44:47],a[196:199],v[68:71]
TR a[36:37],v194 offset:41120
TR a[38:39],v194 offset:41632
MM v[76:79],a[44:47],a[204:207],v[76:79]
buffer_load_dwordx4 v220, s[32:35], s95 offen offset:2048 lds
MM v[76:79],a[40:43],a[200:203],v[76:79]
s_add_u32 s95, s88, 27648
MM v[84:87],a[40:43],a[208:211],v[84:87]
MM v[84:87],a[44:47],a[212:215],v[84:87]
buffer_load_dwordx4 v221, s[32:35], s95 offen offset:3072 lds
MM v[92:95],a[44:47],a[220:223],v[92:95]
s_waitcnt vmcnt(10)
RD a[128:131],v222 offset:0
MM v[92:95],a[40:43],a[216:219],v[92:95]
RD a[132:135],v223 offset:0
MM v[96:99],a[48:51],a[192:195],v[96:99]
TR a[40:41],v194 offset:32904
TR a[42:43],v194 offset:33416
MM v[96:99],a[52:55],a[196:199],v[96:99]
TR a[44:45],v194 offset:41128
TR a[46:47],v194 offset:41640
MM v[104:107],a[52:55],a[204:207],v[104:107]
RD a[136:139],v222 offset:2048
MM v[104:107],a[48:51],a[200:203],v[104:107]
RD a[140:143],v223 offset:2048
MM v[112:115],a[48:51],a[208:211],v[112:115]
RD a[144:147],v222 offset:4096
MM v[112:115],a[52:55],a[212:215],v[112:115]
RD a[148:151],v223 offset:4096
MM v[120:123],a[52:55],a[220:223],v[120:123]
RD a[152:155],v222 offset:6144
MM v[120:123],a[48:51],a[216:219],v[120:123]
RD a[156:159],v223 offset:6144
MM v[100:103],a[56:59],a[192:195],v[100:103]
s_sub_u32 s88, s88, s63
TR a[48:49],v195 offset:32896
TR a[50:51],v195 offset:33408
MM v[100:103],a[60:63],a[196:199],v[100:103]
s_nop 0
LPG3_%=:
s_cbranch_execz LPG7_%=
s_add_u32 m0, s94, 36864
TR a[52:53],v195 offset:41120
TR a[54:55],v195 offset:41632
MM v[108:111],a[60:63],a[204:207],v[108:111]
s_add_u32 s95, s88, 40960
buffer_load_dwordx4 v220, s[32:35], s95 offen offset:0 lds
MM v[108:111],a[56:59],a[200:203],v[108:111]
s_add_u32 s95, s88, 50176
MM v[116:119],a[56:59],a[208:211],v[116:119]
buffer_load_dwordx4 v221, s[32:35], s95 offen offset:1024 lds
MM v[116:119],a[60:63],a[212:215],v[116:119]
s_add_u32 s95, s88, 59392
MM v[124:127],a[60:63],a[220:223],v[124:127]
MM v[124:127],a[56:59],a[216:219],v[124:127]
buffer_load_dwordx4 v220, s[32:35], s95 offen offset:2048 lds
s_add_u32 s95, s88, 68608
buffer_load_dwordx4 v221, s[32:35], s95 offen offset:3072 lds
s_waitcnt lgkmcnt(4)
s_waitcnt vmcnt(10)
s_barrier
MM v[0:3],a[0:3],a[224:227],v[0:3]
s_add_u32 s77, s64, 0
TR a[56:57],v195 offset:32904
TR a[58:59],v195 offset:33416
MM v[0:3],a[4:7],a[228:231],v[0:3]
s_add_u32 m0, s65, 0
TR a[60:61],v195 offset:41128
TR a[62:63],v195 offset:41640
MM v[8:11],a[4:7],a[236:239],v[8:11]
)ASM"
R"ASM(
s_waitcnt vmcnt(8)
TR a[64:65],v196 offset:0
TR a[66:67],v196 offset:512
MM v[8:11],a[0:3],a[232:235],v[8:11]
s_lshl_b32 s76, %[swave], 7
buffer_load_dwordx4 v228, s[8:11], s77 offen offset:0 lds
v_lshrrev_b32 v253, 8, v248
v_lshlrev_b32 v253, 5, v253
MM v[16:19],a[0:3],a[240:243],v[16:19]
s_add_u32 s76, s76, %[slds]
TR a[68:69],v196 offset:8224
TR a[70:71],v196 offset:8736
v_xor_b32 v201, s69, v201
MM v[16:19],a[4:7],a[244:247],v[16:19]
s_mov_b64 exec, 1
buffer_store_dword v171, off, s[20:23], s80 sc1
s_mov_b64 exec, -1
v_add_u32 v253, s76, v253
v_add_u32 v253, 151552, v253
MM v[24:27],a[4:7],a[252:255],v[24:27]
TR a[72:73],v196 offset:16448
TR a[74:75],v196 offset:16960
MM v[24:27],a[0:3],a[248:251],v[24:27]
TR a[76:77],v196 offset:24672
TR a[78:79],v196 offset:25184
buffer_load_dwordx4 v229, s[8:11], s77 offen offset:1024 lds
v_xor_b32 v203, s69, v203
MM v[4:7],a[8:11],a[224:227],v[4:7]
TR a[80:81],v196 offset:8
TR a[82:83],v196 offset:520
MM v[4:7],a[12:15],a[228:231],v[4:7]
TR a[84:85],v196 offset:8232
TR a[86:87],v196 offset:8744
MM v[12:15],a[12:15],a[236:239],v[12:15]
TR a[88:89],v196 offset:16456
TR a[90:91],v196 offset:16968
v_xor_b32 v205, s69, v205
MM v[12:15],a[8:11],a[232:235],v[12:15]
TR a[92:93],v196 offset:24680
TR a[94:95],v196 offset:25192
buffer_load_dwordx4 v230, s[8:11], s77 offen offset:2048 lds
MM v[20:23],a[8:11],a[240:243],v[20:23]
TR a[96:97],v197 offset:0
TR a[98:99],v197 offset:512
MM v[20:23],a[12:15],a[244:247],v[20:23]
TR a[100:101],v197 offset:8224
TR a[102:103],v197 offset:8736
v_xor_b32 v207, s69, v207
MM v[28:31],a[12:15],a[252:255],v[28:31]
TR a[104:105],v197 offset:16448
TR a[106:107],v197 offset:16960
MM v[28:31],a[8:11],a[248:251],v[28:31]
TR a[108:109],v197 offset:24672
TR a[110:111],v197 offset:25184
buffer_load_dwordx4 v231, s[8:11], s77 offen offset:3072 lds
v_smfmac_f32_16x16x64_bf16 v[184:187], v[236:239], a[0:7], v234
s_add_u32 s77, s64, 16384
TR a[112:113],v197 offset:8
v_or3_b32 v209, v201, v203, v205
v_smfmac_f32_16x16x64_bf16 v[188:191], v[236:239], a[8:15], v234
s_add_u32 m0, s65, 16384
TR a[114:115],v197 offset:520
MM v[32:35],a[16:19],a[224:227],v[32:35]
TR a[116:117],v197 offset:8232
TR a[118:119],v197 offset:8744
MM v[32:35],a[20:23],a[228:231],v[32:35]
TR a[120:121],v197 offset:16456
TR a[122:123],v197 offset:16968
v_or_b32 v209, v209, v207
MM v[40:43],a[20:23],a[236:239],v[40:43]
TR a[124:125],v197 offset:24680
TR a[126:127],v197 offset:25192
MM v[40:43],a[16:19],a[232:235],v[40:43]
TR a[0:1],v198 offset:0
TR a[2:3],v198 offset:512
buffer_load_dwordx4 v228, s[8:11], s77 offen offset:0 lds
MM v[48:51],a[16:19],a[240:243],v[48:51]
TR a[4:5],v198 offset:8224
TR a[6:7],v198 offset:8736
v_cmp_ne_u32 vcc, 0, v209
s_cbranch_vccnz LRG1o_%=
LRG1ob_%=:
MM v[48:51],a[20:23],a[244:247],v[48:51]
TR a[8:9],v198 offset:16448
TR a[10:11],v198 offset:16960
MM v[56:59],a[20:23],a[252:255],v[56:59]
TR a[12:13],v198 offset:24672
TR a[14:15],v198 offset:25184
MM v[56:59],a[16:19],a[248:251],v[56:59]
v_lshlrev_b32 v240, 16, v200
v_and_b32 v200, 0xffff0000, v200
MM v[36:39],a[24:27],a[224:227],v[36:39]
v_rcp_f32 v244, v200
MM v[36:39],a[28:31],a[228:231],v[36:39]
v_xor_b32 v240, 0x80000000, v240
v_mul_f32 v244, s72, v244
MM v[44:47],a[28:31],a[236:239],v[44:47]
v_lshlrev_b32 v241, 16, v202
v_and_b32 v202, 0xffff0000, v202
MM v[44:47],a[24:27],a[232:235],v[44:47]
buffer_load_dwordx4 v229, s[8:11], s77 offen offset:1024 lds
MM v[52:55],a[24:27],a[240:243],v[52:55]
v_rcp_f32 v245, v202
MM v[52:55],a[28:31],a[244:247],v[52:55]
v_xor_b32 v241, 0x80000000, v241
v_mul_f32 v245, s72, v245
MM v[60:63],a[28:31],a[252:255],v[60:63]
v_lshlrev_b32 v242, 16, v204
v_and_b32 v204, 0xffff0000, v204
MM v[60:63],a[24:27],a[248:251],v[60:63]
v_rcp_f32 v246, v204
MM v[64:67],a[32:35],a[224:227],v[64:67]
TR a[16:17],v198 offset:8
TR a[18:19],v198 offset:520
MM v[64:67],a[36:39],a[228:231],v[64:67]
TR a[20:21],v198 offset:8232
TR a[22:23],v198 offset:8744
v_xor_b32 v242, 0x80000000, v242
MM v[72:75],a[36:39],a[236:239],v[72:75]
TR a[24:25],v198 offset:16456
TR a[26:27],v198 offset:16968
MM v[72:75],a[32:35],a[232:235],v[72:75]
TR a[28:29],v198 offset:24680
TR a[30:31],v198 offset:25192
MM v[80:83],a[32:35],a[240:243],v[80:83]
v_mul_f32 v246, s72, v246
v_lshlrev_b32 v243, 16, v206
MM v[80:83],a[36:39],a[244:247],v[80:83]
buffer_load_dwordx4 v230, s[8:11], s77 offen offset:2048 lds
MM v[88:91],a[36:39],a[252:255],v[88:91]
v_and_b32 v206, 0xffff0000, v206
MM v[88:91],a[32:35],a[248:251],v[88:91]
s_waitcnt lgkmcnt(15)
v_rcp_f32 v247, v206
v_smfmac_f32_16x16x64_bf16 v[184:187], v[236:239], a[64:71], v234
v_xor_b32 v243, 0x80000000, v243
v_smfmac_f32_16x16x64_bf16 v[184:187], v[236:239], a[72:79], v234
v_mul_f32 v247, s72, v247
v_smfmac_f32_16x16x64_bf16 v[188:191], v[236:239], a[80:87], v234
v_smfmac_f32_16x16x64_bf16 v[188:191], v[236:239], a[88:95], v234
MM v[68:71],a[40:43],a[224:227],v[68:71]
v_bfe_u32 v183, v248, 6, 2
v_lshlrev_b32 v183, 5, v183
MM v[68:71],a[44:47],a[228:231],v[68:71]
v_bfe_u32 v182, v248, 4, 2
v_lshl_add_u32 v183, v182, 2, v183
MM v[76:79],a[44:47],a[236:239],v[76:79]
v_add_u32 v183, s76, v183
v_add_u32 v183, 151552, v183
MM v[76:79],a[40:43],a[232:235],v[76:79]
buffer_load_dwordx4 v231, s[8:11], s77 offen offset:3072 lds
MM v[84:87],a[40:43],a[240:243],v[84:87]
s_waitcnt vmcnt(9)
RD a[160:163],v222 offset:32768
MM v[84:87],a[44:47],a[244:247],v[84:87]
RD a[164:167],v223 offset:32768
MM v[92:95],a[44:47],a[252:255],v[92:95]
RD a[168:171],v222 offset:34816
MM v[92:95],a[40:43],a[248:251],v[92:95]
v_add_f32 v184, v184, v185
v_add_f32 v188, v188, v189
MM v[96:99],a[48:51],a[224:227],v[96:99]
TR a[32:33],v199 offset:0
TR a[34:35],v199 offset:512
MM v[96:99],a[52:55],a[228:231],v[96:99]
RD a[172:175],v223 offset:34816
ds_write_b32 v183, v184 offset:0
ds_write_b32 v183, v188 offset:16
MM v[104:107],a[52:55],a[236:239],v[104:107]
TR a[36:37],v199 offset:8224
TR a[38:39],v199 offset:8736
MM v[104:107],a[48:51],a[232:235],v[104:107]
TR a[40:41],v199 offset:16448
TR a[42:43],v199 offset:16960
MM v[112:115],a[48:51],a[240:243],v[112:115]
TR a[44:45],v199 offset:24672
TR a[46:47],v199 offset:25184
RD a[176:179],v222 offset:36864
MM v[112:115],a[52:55],a[244:247],v[112:115]
RD a[180:183],v223 offset:36864
MM v[120:123],a[52:55],a[252:255],v[120:123]
RD a[184:187],v222 offset:38912
MM v[120:123],a[48:51],a[248:251],v[120:123]
RD a[188:191],v223 offset:38912
MM v[100:103],a[56:59],a[224:227],v[100:103]
s_add_u32 s77, s64, 0
s_add_u32 s81, s77, 896
s_add_u32 m0, s65, 65536
MM v[100:103],a[60:63],a[228:231],v[100:103]
buffer_load_dwordx4 v235, s[12:15], s81 offen offset:0 lds
MM v[108:111],a[60:63],a[236:239],v[108:111]
MM v[108:111],a[56:59],a[232:235],v[108:111]
MM v[116:119],a[56:59],a[240:243],v[116:119]
MM v[116:119],a[60:63],a[244:247],v[116:119]
MM v[124:127],a[60:63],a[252:255],v[124:127]
MM v[124:127],a[56:59],a[248:251],v[124:127]
buffer_load_dwordx4 v235, s[12:15], s77 offen offset:1024 lds
s_waitcnt lgkmcnt(0)
s_barrier
MM v[0:3],a[64:67],a[128:131],v[0:3]
s_memrealtime s[52:53]
TR a[48:49],v199 offset:8
TR a[50:51],v199 offset:520
MM v[0:3],a[68:71],a[132:135],v[0:3]
s_memtime s[54:55]
TR a[52:53],v199 offset:8232
TR a[54:55],v199 offset:8744
MM v[0:3],a[72:75],a[160:163],v[0:3]
TR a[56:57],v199 offset:16456
TR a[58:59],v199 offset:16968
MM v[0:3],a[76:79],a[164:167],v[0:3]
TR a[60:61],v199 offset:24680
TR a[62:63],v199 offset:25192
MM v[8:11],a[76:79],a[172:175],v[8:11]
v_xor_b32 v151, 0, v253
v_xor_b32 v143, 0, v253
MM v[8:11],a[72:75],a[168:171],v[8:11]
RD v[144:147],v151 offset:0
RD v[148:151],v151 offset:16
MM v[8:11],a[68:71],a[140:143],v[8:11]
RD v[136:139],v143 offset:0
RD v[140:143],v143 offset:16
MM v[8:11],a[64:67],a[136:139],v[8:11]
MM v[4:7],a[80:83],a[128:131],v[4:7]
MM v[4:7],a[84:87],a[132:135],v[4:7]
s_waitcnt lgkmcnt(2)
buffer_load_dwordx4 v235, s[12:15], s81 offen offset:2048 lds
MM v[4:7],a[88:91],a[160:163],v[4:7]
s_waitcnt lgkmcnt(0)
MM v[4:7],a[92:95],a[164:167],v[4:7]
MM v[12:15],a[92:95],a[172:175],v[12:15]
MM v[12:15],a[88:91],a[168:171],v[12:15]
MM v[12:15],a[84:87],a[140:143],v[12:15]
MM v[12:15],a[80:83],a[136:139],v[12:15]
MM v[32:35],a[96:99],a[128:131],v[32:35]
MM v[32:35],a[100:103],a[132:135],v[32:35]
MM v[32:35],a[104:107],a[160:163],v[32:35]
MM v[32:35],a[108:111],a[164:167],v[32:35]
buffer_load_dwordx4 v235, s[12:15], s77 offen offset:3072 lds
v_fmac_f32 v0, v240, v144
v_fmac_f32 v1, v240, v145
v_fmac_f32 v2, v240, v146
MM v[40:43],a[108:111],a[172:175],v[40:43]
s_add_u32 s77, s64, 32768
s_add_u32 m0, s65, 32768
v_fmac_f32 v3, v240, v147
v_fmac_f32 v4, v240, v148
MM v[40:43],a[104:107],a[168:171],v[40:43]
v_fmac_f32 v5, v240, v149
v_fmac_f32 v6, v240, v150
MM v[40:43],a[100:103],a[140:143],v[40:43]
v_fmac_f32 v7, v240, v151
v_mul_f32 v0, v244, v0
v_mul_f32 v1, v244, v1
MM v[40:43],a[96:99],a[136:139],v[40:43]
v_mul_f32 v2, v244, v2
v_mul_f32 v3, v244, v3
MM v[36:39],a[112:115],a[128:131],v[36:39]
v_mul_f32 v4, v244, v4
v_mul_f32 v5, v244, v5
MM v[36:39],a[116:119],a[132:135],v[36:39]
v_mul_f32 v6, v244, v6
v_mul_f32 v7, v244, v7
CV v160,v0,v1
MM v[36:39],a[120:123],a[160:163],v[36:39]
CV v161,v2,v3
CV v162,v4,v5
MM v[36:39],a[124:127],a[164:167],v[36:39]
CV v163,v6,v7
v_fmac_f32 v8, v241, v144
MM v[44:47],a[124:127],a[172:175],v[44:47]
v_fmac_f32 v9, v241, v145
v_fmac_f32 v10, v241, v146
v_fmac_f32 v11, v241, v147
MM v[44:47],a[120:123],a[168:171],v[44:47]
buffer_load_dwordx4 v228, s[8:11], s77 offen offset:0 lds
v_fmac_f32 v12, v241, v148
v_fmac_f32 v13, v241, v149
MM v[44:47],a[116:119],a[140:143],v[44:47]
v_fmac_f32 v14, v241, v150
v_fmac_f32 v15, v241, v151
v_mul_f32 v8, v245, v8
MM v[44:47],a[112:115],a[136:139],v[44:47]
v_mul_f32 v9, v245, v9
v_mul_f32 v10, v245, v10
MM v[64:67],a[0:3],a[128:131],v[64:67]
v_mul_f32 v11, v245, v11
v_mul_f32 v12, v245, v12
MM v[64:67],a[4:7],a[132:135],v[64:67]
v_mul_f32 v13, v245, v13
v_mul_f32 v14, v245, v14
v_mul_f32 v15, v245, v15
MM v[64:67],a[8:11],a[160:163],v[64:67]
CV v164,v8,v9
CV v165,v10,v11
MM v[64:67],a[12:15],a[164:167],v[64:67]
CV v166,v12,v13
CV v167,v14,v15
MM v[72:75],a[12:15],a[172:175],v[72:75]
v_xor_b32 v159, 128, v253
MM v[72:75],a[8:11],a[168:171],v[72:75]
RD v[152:155],v159 offset:0
RD v[156:159],v159 offset:16
MM v[72:75],a[4:7],a[140:143],v[72:75]
MM v[72:75],a[0:3],a[136:139],v[72:75]
buffer_load_dwordx4 v229, s[8:11], s77 offen offset:1024 lds
MM v[68:71],a[16:19],a[128:131],v[68:71]
MM v[68:71],a[20:23],a[132:135],v[68:71]
s_waitcnt lgkmcnt(0)
v_fmac_f32 v32, v240, v152
v_fmac_f32 v33, v240, v153
v_fmac_f32 v34, v240, v154
v_fmac_f32 v35, v240, v155
v_fmac_f32 v36, v240, v156
v_fmac_f32 v37, v240, v157
v_fmac_f32 v38, v240, v158
v_fmac_f32 v39, v240, v159
v_mul_f32 v179, -v250, v244
v_fma_f32 v32, v244, v32, v179
v_fma_f32 v33, v244, v33, v179
v_fma_f32 v34, v244, v34, v179
MM v[68:71],a[24:27],a[160:163],v[68:71]
v_fma_f32 v35, v244, v35, v179
v_fma_f32 v36, v244, v36, v179
MM v[68:71],a[28:31],a[164:167],v[68:71]
v_fma_f32 v37, v244, v37, v179
v_fma_f32 v38, v244, v38, v179
v_fma_f32 v39, v244, v39, v179
MM v[76:79],a[28:31],a[172:175],v[76:79]
CV v176,v32,v33
CV v177,v34,v35
MM v[76:79],a[24:27],a[168:171],v[76:79]
CV v178,v36,v37
CV v179,v38,v39
v_fmac_f32 v40, v241, v152
MM v[76:79],a[20:23],a[140:143],v[76:79]
v_fmac_f32 v41, v241, v153
v_fmac_f32 v42, v241, v154
v_fmac_f32 v43, v241, v155
MM v[76:79],a[16:19],a[136:139],v[76:79]
v_fmac_f32 v44, v241, v156
v_fmac_f32 v45, v241, v157
MM v[96:99],a[32:35],a[128:131],v[96:99]
v_fmac_f32 v46, v241, v158
v_fmac_f32 v47, v241, v159
MM v[96:99],a[36:39],a[132:135],v[96:99]
buffer_load_dwordx4 v230, s[8:11], s77 offen offset:2048 lds
v_mul_f32 v183, -v250, v245
v_fma_f32 v40, v245, v40, v183
v_fma_f32 v41, v245, v41, v183
MM v[96:99],a[40:43],a[160:163],v[96:99]
v_fma_f32 v42, v245, v42, v183
v_fma_f32 v43, v245, v43, v183
MM v[96:99],a[44:47],a[164:167],v[96:99]
v_fma_f32 v44, v245, v44, v183
v_fma_f32 v45, v245, v45, v183
MM v[104:107],a[44:47],a[172:175],v[104:107]
v_fma_f32 v46, v245, v46, v183
)ASM"
R"ASM(
v_fma_f32 v47, v245, v47, v183
CV v180,v40,v41
MM v[104:107],a[40:43],a[168:171],v[104:107]
CV v181,v42,v43
CV v182,v44,v45
MM v[104:107],a[36:39],a[140:143],v[104:107]
CV v183,v46,v47
v_xor_b32 v151, 256, v253
MM v[104:107],a[32:35],a[136:139],v[104:107]
RD v[144:147],v151 offset:0
RD v[148:151],v151 offset:16
MM v[100:103],a[48:51],a[128:131],v[100:103]
MM v[100:103],a[52:55],a[132:135],v[100:103]
MM v[100:103],a[56:59],a[160:163],v[100:103]
MM v[100:103],a[60:63],a[164:167],v[100:103]
s_waitcnt lgkmcnt(0)
buffer_load_dwordx4 v231, s[8:11], s77 offen offset:3072 lds
v_fmac_f32 v64, v240, v144
v_fmac_f32 v65, v240, v145
v_fmac_f32 v66, v240, v146
v_fmac_f32 v67, v240, v147
v_fmac_f32 v68, v240, v148
v_fmac_f32 v69, v240, v149
v_fmac_f32 v70, v240, v150
v_fmac_f32 v71, v240, v151
v_mul_f32 v195, -v250, v244
v_fma_f32 v64, v244, v64, v195
v_fma_f32 v65, v244, v65, v195
MM v[108:111],a[60:63],a[172:175],v[108:111]
s_add_u32 s77, s64, 16384
s_add_u32 s81, s77, 896
s_add_u32 m0, s65, 81920
v_fma_f32 v66, v244, v66, v195
v_fma_f32 v67, v244, v67, v195
MM v[108:111],a[56:59],a[168:171],v[108:111]
v_fma_f32 v68, v244, v68, v195
v_fma_f32 v69, v244, v69, v195
MM v[108:111],a[52:55],a[140:143],v[108:111]
v_fma_f32 v70, v244, v70, v195
v_fma_f32 v71, v244, v71, v195
CV v192,v64,v65
MM v[108:111],a[48:51],a[136:139],v[108:111]
CV v193,v66,v67
CV v194,v68,v69
MM v[16:19],a[64:67],a[144:147],v[16:19]
CV v195,v70,v71
v_fmac_f32 v72, v241, v144
v_fmac_f32 v73, v241, v145
MM v[16:19],a[68:71],a[148:151],v[16:19]
v_fmac_f32 v74, v241, v146
v_fmac_f32 v75, v241, v147
v_fmac_f32 v76, v241, v148
MM v[16:19],a[72:75],a[176:179],v[16:19]
v_fmac_f32 v77, v241, v149
v_fmac_f32 v78, v241, v150
MM v[16:19],a[76:79],a[180:183],v[16:19]
v_fmac_f32 v79, v241, v151
v_mul_f32 v199, -v250, v245
v_fma_f32 v72, v245, v72, v199
MM v[24:27],a[76:79],a[188:191],v[24:27]
v_fma_f32 v73, v245, v73, v199
v_fma_f32 v74, v245, v74, v199
MM v[24:27],a[72:75],a[184:187],v[24:27]
buffer_load_dwordx4 v235, s[12:15], s81 offen offset:0 lds
v_fma_f32 v75, v245, v75, v199
v_fma_f32 v76, v245, v76, v199
MM v[24:27],a[68:71],a[156:159],v[24:27]
s_waitcnt vmcnt(13)
s_barrier
RD a[192:195],v224 offset:0
v_fma_f32 v77, v245, v77, v199
v_fma_f32 v78, v245, v78, v199
v_fma_f32 v79, v245, v79, v199
MM v[24:27],a[64:67],a[152:155],v[24:27]
RD a[196:199],v225 offset:0
RD a[200:203],v226 offset:0
CV v196,v72,v73
CV v197,v74,v75
MM v[20:23],a[80:83],a[144:147],v[20:23]
RD a[204:207],v227 offset:0
RD a[208:211],v224 offset:4096
CV v198,v76,v77
CV v199,v78,v79
MM v[20:23],a[84:87],a[148:151],v[20:23]
RD a[212:215],v225 offset:4096
RD a[216:219],v226 offset:4096
v_xor_b32 v159, 384, v253
MM v[20:23],a[88:91],a[176:179],v[20:23]
RD a[220:223],v227 offset:4096
RD a[224:227],v224 offset:8192
RD v[152:155],v159 offset:0
RD v[156:159],v159 offset:16
MM v[20:23],a[92:95],a[180:183],v[20:23]
RD a[228:231],v225 offset:8192
RD a[232:235],v226 offset:8192
MM v[28:31],a[92:95],a[188:191],v[28:31]
RD a[236:239],v227 offset:8192
RD a[240:243],v224 offset:12288
MM v[28:31],a[88:91],a[184:187],v[28:31]
RD a[244:247],v225 offset:12288
RD a[248:251],v226 offset:12288
MM v[28:31],a[84:87],a[156:159],v[28:31]
s_waitcnt lgkmcnt(6)
RD a[252:255],v227 offset:12288
v_fmac_f32 v96, v240, v152
v_fmac_f32 v97, v240, v153
v_fmac_f32 v98, v240, v154
v_fmac_f32 v99, v240, v155
v_fmac_f32 v100, v240, v156
v_fmac_f32 v101, v240, v157
v_fmac_f32 v102, v240, v158
v_fmac_f32 v103, v240, v159
v_mul_f32 v211, -v250, v244
v_fma_f32 v96, v244, v96, v211
v_fma_f32 v97, v244, v97, v211
v_fma_f32 v98, v244, v98, v211
MM v[28:31],a[80:83],a[152:155],v[28:31]
buffer_load_dwordx4 v235, s[12:15], s77 offen offset:1024 lds
v_fma_f32 v99, v244, v99, v211
v_fma_f32 v100, v244, v100, v211
MM v[48:51],a[96:99],a[144:147],v[48:51]
v_fma_f32 v101, v244, v101, v211
v_fma_f32 v102, v244, v102, v211
v_fma_f32 v103, v244, v103, v211
MM v[48:51],a[100:103],a[148:151],v[48:51]
CV v208,v96,v97
CV v209,v98,v99
MM v[48:51],a[104:107],a[176:179],v[48:51]
CV v210,v100,v101
CV v211,v102,v103
v_fmac_f32 v104, v241, v152
MM v[48:51],a[108:111],a[180:183],v[48:51]
v_fmac_f32 v105, v241, v153
v_fmac_f32 v106, v241, v154
v_fmac_f32 v107, v241, v155
MM v[56:59],a[108:111],a[188:191],v[56:59]
v_fmac_f32 v108, v241, v156
v_fmac_f32 v109, v241, v157
MM v[56:59],a[104:107],a[184:187],v[56:59]
v_fmac_f32 v110, v241, v158
v_fmac_f32 v111, v241, v159
MM v[56:59],a[100:103],a[156:159],v[56:59]
v_mul_f32 v215, -v250, v245
v_fma_f32 v104, v245, v104, v215
v_fma_f32 v105, v245, v105, v215
MM v[56:59],a[96:99],a[152:155],v[56:59]
v_fma_f32 v106, v245, v106, v215
v_fma_f32 v107, v245, v107, v215
MM v[52:55],a[112:115],a[144:147],v[52:55]
v_fma_f32 v108, v245, v108, v215
v_fma_f32 v109, v245, v109, v215
MM v[52:55],a[116:119],a[148:151],v[52:55]
buffer_load_dwordx4 v235, s[12:15], s81 offen offset:2048 lds
v_fma_f32 v110, v245, v110, v215
v_fma_f32 v111, v245, v111, v215
CV v212,v104,v105
MM v[52:55],a[120:123],a[176:179],v[52:55]
CV v213,v106,v107
CV v214,v108,v109
CV v215,v110,v111
MM v[52:55],a[124:127],a[180:183],v[52:55]
v_mov_b32 v241, 0
v_mov_b32 v244, 0
s_waitcnt lgkmcnt(0)
MM v[0:3],a[192:195],v[160:163],0
v_mov_b32 v144, 0
v_mov_b32 v145, 0
MM v[0:3],a[196:199],v[176:179],v[0:3]
v_mov_b32 v245, 0
v_mov_b32 v148, 0
MM v[0:3],a[200:203],v[192:195],v[0:3]
v_mov_b32 v149, 0
v_fmac_f32 v16, v242, v136
MM v[0:3],a[204:207],v[208:211],v[0:3]
v_fmac_f32 v17, v242, v137
v_fmac_f32 v18, v242, v138
MM v[8:11],a[192:195],v[164:167],0
v_fmac_f32 v19, v242, v139
v_fmac_f32 v20, v242, v140
MM v[8:11],a[196:199],v[180:183],v[8:11]
v_fmac_f32 v21, v242, v141
v_fmac_f32 v22, v242, v142
MM v[8:11],a[200:203],v[196:199],v[8:11]
v_fmac_f32 v23, v242, v143
v_mul_f32 v16, v246, v16
MM v[8:11],a[204:207],v[212:215],v[8:11]
buffer_load_dwordx4 v235, s[12:15], s77 offen offset:3072 lds
MM v[4:7],a[208:211],v[160:163],0
s_add_u32 s77, s64, 32768
s_add_u32 s81, s77, 896
s_add_u32 m0, s65, 98304
MM v[4:7],a[212:215],v[176:179],v[4:7]
v_max3_f32 v146, v0, v1, v2
v_max_f32 v146, v146, v3
MM v[4:7],a[216:219],v[192:195],v[4:7]
v_mov_b32 v147, v146
v_mul_f32 v17, v246, v17
MM v[4:7],a[220:223],v[208:211],v[4:7]
v_permlane16_swap_b32 v146, v147
v_max_f32 v146, v146, v147
MM v[12:15],a[208:211],v[164:167],0
s_waitcnt vmcnt(8)
LPG4_%=:
s_cbranch_execz LPG8_%=
s_barrier
RD a[192:195],v224 offset:16384
MM v[12:15],a[212:215],v[180:183],v[12:15]
RD a[196:199],v225 offset:16384
RD a[200:203],v226 offset:16384
MM v[12:15],a[216:219],v[196:199],v[12:15]
RD a[204:207],v227 offset:16384
v_mov_b32 v147, v146
MM v[12:15],a[220:223],v[212:215],v[12:15]
s_nop 0
v_permlane32_swap_b32 v146, v147
v_max_f32 v146, v146, v147
v_sub_f32 v128, 0xc1600000, v146
v_sub_f32 v129, 0xc1600000, v146
MM v[32:35],a[224:227],v[160:163],0
RD a[208:211],v224 offset:20480
RD a[212:215],v225 offset:20480
v_sub_f32 v130, 0xc1600000, v146
v_sub_f32 v131, 0xc1600000, v146
v_max3_f32 v150, v8, v9, v10
MM v[32:35],a[228:231],v[176:179],v[32:35]
buffer_load_dwordx4 v235, s[12:15], s81 offen offset:0 lds
v_max_f32 v150, v150, v11
v_mov_b32 v151, v150
MM v[32:35],a[232:235],v[192:195],v[32:35]
RD a[216:219],v226 offset:20480
RD a[220:223],v227 offset:20480
v_permlane16_swap_b32 v150, v151
v_max_f32 v150, v150, v151
v_mov_b32 v151, v150
MM v[32:35],a[236:239],v[208:211],v[32:35]
s_nop 0
v_permlane32_swap_b32 v150, v151
v_max_f32 v150, v150, v151
v_add_f32 v0, v0, v128
v_sub_f32 v132, 0xc1600000, v150
MM v[40:43],a[224:227],v[164:167],0
v_sub_f32 v133, 0xc1600000, v150
v_sub_f32 v134, 0xc1600000, v150
v_sub_f32 v135, 0xc1600000, v150
v_add_f32 v1, v1, v128
EX v0,v0
MM v[40:43],a[228:231],v[180:183],v[40:43]
EX v1,v1
MM v[40:43],a[232:235],v[196:199],v[40:43]
v_add_f32 v2, v2, v128
v_add_f32 v3, v3, v128
MM v[40:43],a[236:239],v[212:215],v[40:43]
EX v2,v2
MM v[36:39],a[240:243],v[160:163],v[128:131]
RD a[224:227],v224 offset:24576
RD a[228:231],v225 offset:24576
EX v3,v3
MM v[36:39],a[244:247],v[176:179],v[36:39]
RD a[232:235],v226 offset:24576
RD a[236:239],v227 offset:24576
CV v0,v0,v1
v_add_f32 v4, v4, v128
MM v[36:39],a[248:251],v[192:195],v[36:39]
v_add_f32 v5, v5, v128
v_mul_f32 v18, v246, v18
EX v4,v4
MM v[36:39],a[252:255],v[208:211],v[36:39]
buffer_load_dwordx4 v235, s[12:15], s77 offen offset:1024 lds
EX v5,v5
MM v[44:47],a[240:243],v[164:167],v[132:135]
CV v1,v2,v3
v_add_f32 v6, v6, v128
MM v[44:47],a[244:247],v[180:183],v[44:47]
v_add_f32 v7, v7, v128
v_mul_f32 v19, v246, v19
MM v[44:47],a[248:251],v[196:199],v[44:47]
EX v6,v6
EX v7,v7
MM v[44:47],a[252:255],v[212:215],v[44:47]
CV v2,v4,v5
v_add_f32 v8, v8, v132
MM v[60:63],a[124:127],a[188:191],v[60:63]
RD a[240:243],v224 offset:28672
RD a[244:247],v225 offset:28672
v_add_f32 v9, v9, v132
MM v[60:63],a[120:123],a[184:187],v[60:63]
RD a[248:251],v226 offset:28672
RD a[252:255],v227 offset:28672
EX v8,v8
MM v[60:63],a[116:119],a[156:159],v[60:63]
EX v9,v9
MM v[60:63],a[112:115],a[152:155],v[60:63]
CV v3,v6,v7
v_add_f32 v10, v10, v132
MM v[80:83],a[0:3],a[144:147],v[80:83]
v_add_f32 v11, v11, v132
v_mul_f32 v20, v246, v20
EX v10,v10
MM v[80:83],a[4:7],a[148:151],v[80:83]
buffer_load_dwordx4 v235, s[12:15], s81 offen offset:2048 lds
EX v11,v11
MM v[80:83],a[8:11],a[176:179],v[80:83]
CV v8,v8,v9
v_add_f32 v12, v12, v132
MM v[80:83],a[12:15],a[180:183],v[80:83]
v_add_f32 v13, v13, v132
v_mul_f32 v21, v246, v21
EX v12,v12
MM v[88:91],a[12:15],a[188:191],v[88:91]
EX v13,v13
MM v[88:91],a[8:11],a[184:187],v[88:91]
CV v9,v10,v11
v_add_f32 v14, v14, v132
MM v[88:91],a[4:7],a[156:159],v[88:91]
v_add_f32 v15, v15, v132
v_mul_f32 v22, v246, v22
EX v14,v14
MM v[88:91],a[0:3],a[152:155],v[88:91]
EX v15,v15
MM v[84:87],a[16:19],a[144:147],v[84:87]
CV v10,v12,v13
v_add_f32 v32, v32, v128
MM v[84:87],a[20:23],a[148:151],v[84:87]
v_add_f32 v33, v33, v128
v_mul_f32 v23, v246, v23
MM v[84:87],a[24:27],a[176:179],v[84:87]
EX v32,v32
MM v[84:87],a[28:31],a[180:183],v[84:87]
buffer_load_dwordx4 v235, s[12:15], s77 offen offset:3072 lds
EX v33,v33
MM v[92:95],a[28:31],a[188:191],v[92:95]
s_add_u32 s77, s64, 49152
s_add_u32 m0, s65, 49152
CV v11,v14,v15
v_add_f32 v34, v34, v128
v_add_f32 v35, v35, v128
MM v[92:95],a[24:27],a[184:187],v[92:95]
buffer_load_dwordx4 v228, s[8:11], s77 offen offset:0 lds
EX v34,v34
MM v[92:95],a[20:23],a[156:159],v[92:95]
EX v35,v35
MM v[92:95],a[16:19],a[152:155],v[92:95]
CV v4,v32,v33
CV v168,v16,v17
EX v36,v36
MM v[112:115],a[32:35],a[144:147],v[112:115]
EX v37,v37
MM v[112:115],a[36:39],a[148:151],v[112:115]
buffer_load_dwordx4 v229, s[8:11], s77 offen offset:1024 lds
CV v5,v34,v35
MM v[112:115],a[40:43],a[176:179],v[112:115]
EX v38,v38
EX v39,v39
MM v[112:115],a[44:47],a[180:183],v[112:115]
CV v6,v36,v37
v_add_f32 v40, v40, v132
MM v[120:123],a[44:47],a[188:191],v[120:123]
v_add_f32 v41, v41, v132
CV v169,v18,v19
MM v[120:123],a[40:43],a[184:187],v[120:123]
buffer_load_dwordx4 v230, s[8:11], s77 offen offset:2048 lds
EX v40,v40
MM v[120:123],a[36:39],a[156:159],v[120:123]
EX v41,v41
MM v[120:123],a[32:35],a[152:155],v[120:123]
CV v7,v38,v39
v_add_f32 v42, v42, v132
MM v[116:119],a[48:51],a[144:147],v[116:119]
v_add_f32 v43, v43, v132
CV v170,v20,v21
EX v42,v42
MM v[116:119],a[52:55],a[148:151],v[116:119]
buffer_load_dwordx4 v231, s[8:11], s77 offen offset:3072 lds
EX v43,v43
MM v[116:119],a[56:59],a[176:179],v[116:119]
s_add_u32 s77, s64, 49152
s_add_u32 s81, s77, 896
s_add_u32 m0, s65, 114688
CV v12,v40,v41
EX v44,v44
MM v[116:119],a[60:63],a[180:183],v[116:119]
EX v45,v45
MM v[124:127],a[60:63],a[188:191],v[124:127]
CV v13,v42,v43
CV v171,v22,v23
MM v[124:127],a[56:59],a[184:187],v[124:127]
buffer_load_dwordx4 v235, s[12:15], s81 offen offset:0 lds
EX v46,v46
MM v[124:127],a[52:55],a[156:159],v[124:127]
EX v47,v47
CV v14,v44,v45
CV v15,v46,v47
MM v[124:127],a[48:51],a[152:155],v[124:127]
v_fmac_f32 v24, v243, v136
v_fmac_f32 v25, v243, v137
v_smfmac_f32_16x16x64_bf16 v[144:147], v[236:239], v[0:7], v234
v_fmac_f32 v26, v243, v138
v_smfmac_f32_16x16x64_bf16 v[148:151], v[236:239], v[8:15], v234
s_waitcnt lgkmcnt(0)
)ASM"
R"ASM(
v_fmac_f32 v27, v243, v139
buffer_load_dwordx4 v235, s[12:15], s77 offen offset:1024 lds
buffer_load_dwordx4 v235, s[12:15], s81 offen offset:2048 lds
buffer_load_dwordx4 v235, s[12:15], s77 offen offset:3072 lds
s_add_u32 s64, s64, 49152
MM v[64:67],a[192:195],v[160:163],v[128:131]
s_add_u32 s64, s64, 16384
v_fmac_f32 v28, v243, v140
v_fmac_f32 v29, v243, v141
MM v[64:67],a[196:199],v[176:179],v[64:67]
s_add_u32 s81, s64, 896
TR a[128:129],v232 offset:0
v_fmac_f32 v30, v243, v142
MM v[64:67],a[200:203],v[192:195],v[64:67]
v_fmac_f32 v31, v243, v143
v_mul_f32 v24, v247, v24
MM v[64:67],a[204:207],v[208:211],v[64:67]
v_max3_f32 v249, v144, v145, v148
v_max_f32 v249, v249, v149
v_mul_f32 v25, v247, v25
MM v[68:71],a[220:223],v[208:211],v[128:131]
TR a[130:131],v232 offset:4096
TR a[132:133],v232 offset:8192
v_cmp_lt_f32 vcc, s71, v249
v_mul_f32 v26, v247, v26
v_mul_f32 v27, v247, v27
MM v[68:71],a[216:219],v[192:195],v[68:71]
s_cbranch_vccnz LSITE0_%=
LBACK0_%=:
TR a[134:135],v232 offset:12288
v_add_f32 v244, v144, v145
v_mul_f32 v28, v247, v28
v_mul_f32 v29, v247, v29
v_mul_f32 v30, v247, v30
MM v[68:71],a[212:215],v[176:179],v[68:71]
TR a[136:137],v233 offset:0
v_add_f32 v245, v148, v149
v_mul_f32 v31, v247, v31
CV v172,v24,v25
MM v[68:71],a[208:211],v[160:163],v[68:71]
TR a[138:139],v233 offset:4096
CV v173,v26,v27
CV v174,v28,v29
MM v[76:79],a[208:211],v[164:167],v[132:135]
TR a[140:141],v233 offset:8192
TR a[142:143],v233 offset:12288
CV v175,v30,v31
v_xor_b32 v143, 128, v253
MM v[76:79],a[212:215],v[180:183],v[76:79]
RD v[136:139],v143 offset:0
RD v[140:143],v143 offset:16
TR a[144:145],v232 offset:512
MM v[76:79],a[216:219],v[196:199],v[76:79]
s_waitcnt lgkmcnt(9)
TR a[146:147],v232 offset:4608
TR a[148:149],v232 offset:8704
MM v[76:79],a[220:223],v[212:215],v[76:79]
EX v64,v64
MM v[32:35],a[128:131],v[0:3],0
MM v[36:39],a[128:131],v[8:11],0
MM v[72:75],a[204:207],v[212:215],v[132:135]
TR a[150:151],v232 offset:12800
TR a[152:153],v233 offset:512
MM v[72:75],a[200:203],v[196:199],v[72:75]
s_waitcnt lgkmcnt(5)
TR a[154:155],v233 offset:4608
v_fmac_f32 v48, v242, v136
v_fmac_f32 v49, v242, v137
v_fmac_f32 v50, v242, v138
v_fmac_f32 v51, v242, v139
v_fmac_f32 v52, v242, v140
v_fmac_f32 v53, v242, v141
v_fmac_f32 v54, v242, v142
v_fmac_f32 v55, v242, v143
v_mul_f32 v187, -v252, v246
v_fma_f32 v48, v246, v48, v187
v_fma_f32 v49, v246, v49, v187
EX v65,v65
MM v[72:75],a[196:199],v[180:183],v[72:75]
TR a[156:157],v233 offset:8704
v_fma_f32 v50, v246, v50, v187
v_fma_f32 v51, v246, v51, v187
v_fma_f32 v52, v246, v52, v187
MM v[72:75],a[192:195],v[164:167],v[72:75]
TR a[158:159],v233 offset:12800
v_fma_f32 v53, v246, v53, v187
EX v66,v66
v_fma_f32 v54, v246, v54, v187
MM v[96:99],a[224:227],v[160:163],v[128:131]
TR a[160:161],v232 offset:1024
v_max3_f32 v32, |v32|, |v33|, |v34|
v_max_f32 v32, v32, |v35|
v_fma_f32 v55, v246, v55, v187
CV v184,v48,v49
EX v67,v67
MM v[96:99],a[228:231],v[176:179],v[96:99]
TR a[162:163],v232 offset:5120
TR a[164:165],v232 offset:9216
v_bfe_u32 v32, v32, 23, 8
v_add_u32 v32, 7, v32
CV v185,v50,v51
CV v186,v52,v53
CV v187,v54,v55
v_fmac_f32 v56, v243, v136
MM v[96:99],a[232:235],v[192:195],v[96:99]
TR a[166:167],v232 offset:13312
v_min_u32 v32, 240, v32
v_lshl_or_b32 v241, v32, 0, v241
v_fmac_f32 v57, v243, v137
v_fmac_f32 v58, v243, v138
EX v68,v68
MM v[96:99],a[236:239],v[208:211],v[96:99]
TR a[168:169],v233 offset:1024
v_lshl_or_b32 v32, v32, 1, 1
v_lshlrev_b32 v32, 22, v32
v_fmac_f32 v59, v243, v139
v_fmac_f32 v60, v243, v140
MM v[100:103],a[252:255],v[208:211],v[128:131]
TR a[170:171],v233 offset:5120
AW a100,v32
AW a101,v32
v_fmac_f32 v61, v243, v141
v_fmac_f32 v62, v243, v142
EX v69,v69
v_fmac_f32 v63, v243, v143
MM v[100:103],a[248:251],v[192:195],v[100:103]
TR a[172:173],v233 offset:9216
TR a[174:175],v233 offset:13312
AW a102,v32
AW a103,v32
v_mul_f32 v191, -v252, v247
v_fma_f32 v56, v247, v56, v191
MM v[100:103],a[244:247],v[176:179],v[100:103]
TR a[176:177],v232 offset:1536
v_max3_f32 v36, |v36|, |v37|, |v38|
v_max_f32 v36, v36, |v39|
v_fma_f32 v57, v247, v57, v191
EX v70,v70
v_fma_f32 v58, v247, v58, v191
MM v[100:103],a[240:243],v[160:163],v[100:103]
TR a[178:179],v232 offset:5632
v_bfe_u32 v36, v36, 23, 8
v_add_u32 v36, 7, v36
v_fma_f32 v59, v247, v59, v191
v_fma_f32 v60, v247, v60, v191
EX v71,v71
v_fma_f32 v61, v247, v61, v191
MM v[108:111],a[240:243],v[164:167],v[132:135]
TR a[180:181],v232 offset:9728
v_min_u32 v36, 240, v36
v_lshl_or_b32 v241, v36, 8, v241
v_fma_f32 v62, v247, v62, v191
v_fma_f32 v63, v247, v63, v191
CV v64,v64,v65
MM v[108:111],a[244:247],v[180:183],v[108:111]
TR a[182:183],v232 offset:13824
v_lshl_or_b32 v36, v36, 1, 1
v_lshlrev_b32 v36, 22, v36
CV v188,v56,v57
CV v189,v58,v59
EX v72,v72
CV v190,v60,v61
MM v[108:111],a[248:251],v[196:199],v[108:111]
TR a[184:185],v233 offset:1536
TR a[186:187],v233 offset:5632
TR a[188:189],v233 offset:9728
TR a[190:191],v233 offset:13824
AW a108,v36
AW a109,v36
CV v191,v62,v63
v_xor_b32 v143, 256, v253
EX v73,v73
MM v[108:111],a[252:255],v[212:215],v[108:111]
RD v[136:139],v143 offset:0
RD v[140:143],v143 offset:16
AW a110,v36
AW a111,v36
CV v65,v66,v67
MM v[104:107],a[236:239],v[212:215],v[132:135]
EX v74,v74
MM v[104:107],a[232:235],v[196:199],v[104:107]
EX v75,v75
MM v[104:107],a[228:231],v[180:183],v[104:107]
EX v76,v76
CV v66,v68,v69
MM v[104:107],a[224:227],v[164:167],v[104:107]
s_waitcnt vmcnt(12) lgkmcnt(0)
s_barrier
v_fmac_f32 v80, v242, v136
v_fmac_f32 v81, v242, v137
v_fmac_f32 v82, v242, v138
v_fmac_f32 v83, v242, v139
v_fmac_f32 v84, v242, v140
v_fmac_f32 v85, v242, v141
v_fmac_f32 v86, v242, v142
v_fmac_f32 v87, v242, v143
v_mul_f32 v203, -v252, v246
v_fma_f32 v80, v246, v80, v203
MM a[0:3],a[128:131],v[0:3],a[100:103]
s_add_u32 m0, s65, 0
v_fma_f32 v81, v246, v81, v203
v_fma_f32 v82, v246, v82, v203
v_fma_f32 v83, v246, v83, v203
MM a[0:3],a[132:135],v[4:7],a[0:3]
v_fma_f32 v84, v246, v84, v203
v_fma_f32 v85, v246, v85, v203
EX v77,v77
CV v67,v70,v71
MM a[8:11],a[132:135],v[12:15],a[108:111]
v_fma_f32 v86, v246, v86, v203
v_fma_f32 v87, v246, v87, v203
MM a[8:11],a[128:131],v[8:11],a[8:11]
CV v200,v80,v81
CV v201,v82,v83
EX v78,v78
CV v202,v84,v85
MM a[12:15],a[136:139],v[8:11],a[108:111]
CV v203,v86,v87
v_fmac_f32 v88, v243, v136
v_fmac_f32 v89, v243, v137
EX v79,v79
MM a[12:15],a[140:143],v[12:15],a[12:15]
v_fmac_f32 v90, v243, v138
v_fmac_f32 v91, v243, v139
CV v72,v72,v73
MM a[4:7],a[140:143],v[4:7],a[100:103]
v_fmac_f32 v92, v243, v140
v_fmac_f32 v93, v243, v141
EX v96,v96
v_fmac_f32 v94, v243, v142
MM a[4:7],a[136:139],v[0:3],a[4:7]
v_fmac_f32 v95, v243, v143
v_mul_f32 v207, -v252, v247
MM a[32:35],a[144:147],v[0:3],a[100:103]
RD a[192:195],v224 offset:32768
v_fma_f32 v88, v247, v88, v207
v_fma_f32 v89, v247, v89, v207
EX v97,v97
CV v73,v74,v75
v_fma_f32 v90, v247, v90, v207
MM a[32:35],a[148:151],v[4:7],a[32:35]
buffer_load_dwordx4 v228, s[8:11], s64 offen offset:0 lds
v_fma_f32 v91, v247, v91, v207
v_fma_f32 v92, v247, v92, v207
EX v98,v98
MM a[40:43],a[148:151],v[12:15],a[108:111]
RD a[196:199],v225 offset:32768
v_fma_f32 v93, v247, v93, v207
v_fma_f32 v94, v247, v94, v207
MM a[40:43],a[144:147],v[8:11],a[40:43]
RD a[200:203],v226 offset:32768
v_fma_f32 v95, v247, v95, v207
CV v204,v88,v89
EX v99,v99
CV v74,v76,v77
CV v205,v90,v91
MM a[44:47],a[152:155],v[8:11],a[108:111]
CV v206,v92,v93
CV v207,v94,v95
MM a[44:47],a[156:159],v[12:15],a[44:47]
RD a[204:207],v227 offset:32768
buffer_load_dwordx4 v229, s[8:11], s64 offen offset:1024 lds
v_xor_b32 v143, 384, v253
EX v100,v100
MM a[36:39],a[156:159],v[4:7],a[100:103]
RD v[136:139],v143 offset:0
RD v[140:143],v143 offset:16
EX v101,v101
CV v75,v78,v79
MM a[36:39],a[152:155],v[0:3],a[36:39]
RD a[208:211],v224 offset:36864
RD a[212:215],v225 offset:36864
MM a[64:67],a[160:163],v[0:3],a[100:103]
EX v102,v102
MM a[64:67],a[164:167],v[4:7],a[64:67]
buffer_load_dwordx4 v230, s[8:11], s64 offen offset:2048 lds
MM a[72:75],a[164:167],v[12:15],a[108:111]
s_waitcnt lgkmcnt(2)
v_fmac_f32 v112, v242, v136
v_fmac_f32 v113, v242, v137
v_fmac_f32 v114, v242, v138
v_fmac_f32 v115, v242, v139
v_fmac_f32 v116, v242, v140
v_fmac_f32 v117, v242, v141
v_fmac_f32 v118, v242, v142
v_fmac_f32 v119, v242, v143
v_mul_f32 v219, -v252, v246
v_fma_f32 v112, v246, v112, v219
v_fma_f32 v113, v246, v113, v219
EX v103,v103
CV v68,v96,v97
v_fma_f32 v114, v246, v114, v219
MM a[72:75],a[160:163],v[8:11],a[72:75]
RD a[216:219],v226 offset:36864
v_fma_f32 v115, v246, v115, v219
v_fma_f32 v116, v246, v116, v219
EX v104,v104
MM a[76:79],a[168:171],v[8:11],a[108:111]
buffer_load_dwordx4 v231, s[8:11], s64 offen offset:3072 lds
RD a[220:223],v227 offset:36864
v_fma_f32 v117, v246, v117, v219
v_fma_f32 v118, v246, v118, v219
MM a[76:79],a[172:175],v[12:15],a[76:79]
s_add_u32 m0, s65, 65536
v_fma_f32 v119, v246, v119, v219
CV v216,v112,v113
EX v105,v105
CV v217,v114,v115
CV v69,v98,v99
MM a[68:71],a[172:175],v[4:7],a[100:103]
RD a[224:227],v224 offset:40960
CV v218,v116,v117
CV v219,v118,v119
v_fmac_f32 v120, v243, v136
MM a[68:71],a[168:171],v[0:3],a[68:71]
v_fmac_f32 v121, v243, v137
v_fmac_f32 v122, v243, v138
EX v106,v106
v_fmac_f32 v123, v243, v139
MM a[96:99],a[176:179],v[0:3],a[100:103]
RD a[228:231],v225 offset:40960
buffer_load_dwordx4 v235, s[12:15], s81 offen offset:0 lds
v_fmac_f32 v124, v243, v140
v_fmac_f32 v125, v243, v141
EX v107,v107
CV v70,v100,v101
MM a[96:99],a[180:183],v[4:7],a[96:99]
v_fmac_f32 v126, v243, v142
v_fmac_f32 v127, v243, v143
MM a[104:107],a[180:183],v[12:15],a[108:111]
RD a[232:235],v226 offset:40960
v_mul_f32 v223, -v252, v247
v_fma_f32 v120, v247, v120, v223
EX v108,v108
v_fma_f32 v121, v247, v121, v223
MM a[104:107],a[176:179],v[8:11],a[104:107]
v_fma_f32 v122, v247, v122, v223
v_fma_f32 v123, v247, v123, v223
CV v71,v102,v103
MM a[108:111],a[184:187],v[8:11],a[108:111]
RD a[236:239],v227 offset:40960
buffer_load_dwordx4 v235, s[12:15], s64 offen offset:1024 lds
v_fma_f32 v124, v247, v124, v223
v_fma_f32 v125, v247, v125, v223
EX v109,v109
MM a[108:111],a[188:191],v[12:15],a[108:111]
v_fma_f32 v126, v247, v126, v223
v_fma_f32 v127, v247, v127, v223
EX v110,v110
CV v220,v120,v121
MM a[100:103],a[188:191],v[4:7],a[100:103]
RD a[240:243],v224 offset:45056
RD a[244:247],v225 offset:45056
RD a[248:251],v226 offset:45056
RD a[252:255],v227 offset:45056
CV v221,v122,v123
EX v111,v111
CV v222,v124,v125
CV v223,v126,v127
MM a[100:103],a[184:187],v[0:3],a[100:103]
buffer_load_dwordx4 v235, s[12:15], s81 offen offset:2048 lds
CV v76,v104,v105
v_smfmac_f32_16x16x64_bf16 v[144:147], v[236:239], v[64:71], v234
CV v77,v106,v107
CV v78,v108,v109
CV v79,v110,v111
s_nop 1
v_smfmac_f32_16x16x64_bf16 v[148:151], v[236:239], v[72:79], v234
s_waitcnt lgkmcnt(0)
buffer_load_dwordx4 v235, s[12:15], s64 offen offset:3072 lds
MM v[0:3],a[192:195],v[160:163],v[128:131]
s_add_u32 s64, s64, 16384
TR a[128:129],v232 offset:16384
TR a[130:131],v232 offset:20480
MM v[0:3],a[196:199],v[176:179],v[0:3]
s_add_u32 s81, s64, 896
TR a[132:133],v232 offset:24576
TR a[134:135],v232 offset:28672
MM v[0:3],a[200:203],v[192:195],v[0:3]
TR a[136:137],v233 offset:16384
TR a[138:139],v233 offset:20480
MM v[0:3],a[204:207],v[208:211],v[0:3]
v_max3_f32 v249, v144, v145, v148
v_max_f32 v249, v249, v149
MM v[4:7],a[220:223],v[208:211],v[128:131]
TR a[140:141],v233 offset:24576
v_cmp_lt_f32 vcc, s71, v249
MM v[4:7],a[216:219],v[192:195],v[4:7]
s_cbranch_vccnz LSITE1_%=
LBACK1_%=:
TR a[142:143],v233 offset:28672
v_add_f32 v244, v144, v145
MM v[4:7],a[212:215],v[176:179],v[4:7]
TR a[144:145],v232 offset:16896
v_add_f32 v245, v148, v149
MM v[4:7],a[208:211],v[160:163],v[4:7]
TR a[146:147],v232 offset:20992
TR a[148:149],v232 offset:25088
MM v[12:15],a[208:211],v[164:167],v[132:135]
EX v0,v0
MM v[12:15],a[212:215],v[180:183],v[12:15]
EX v1,v1
MM v[12:15],a[216:219],v[196:199],v[12:15]
TR a[150:151],v232 offset:29184
CV v0,v0,v1
MM v[12:15],a[220:223],v[212:215],v[12:15]
TR a[152:153],v233 offset:16896
TR a[154:155],v233 offset:20992
MM v[8:11],a[204:207],v[212:215],v[132:135]
)ASM"
R"ASM(
EX v2,v2
MM v[8:11],a[200:203],v[196:199],v[8:11]
EX v3,v3
MM v[8:11],a[196:199],v[180:183],v[8:11]
TR a[156:157],v233 offset:25088
CV v1,v2,v3
MM v[8:11],a[192:195],v[164:167],v[8:11]
TR a[158:159],v233 offset:29184
s_nop 0
LPG5_%=:
s_cbranch_execz LPG9_%=
TR a[160:161],v232 offset:17408
MM v[32:35],a[224:227],v[160:163],v[128:131]
EX v4,v4
MM v[32:35],a[228:231],v[176:179],v[32:35]
TR a[162:163],v232 offset:21504
TR a[164:165],v232 offset:25600
MM v[32:35],a[232:235],v[192:195],v[32:35]
EX v5,v5
MM v[32:35],a[236:239],v[208:211],v[32:35]
TR a[166:167],v232 offset:29696
CV v2,v4,v5
MM v[36:39],a[252:255],v[208:211],v[128:131]
EX v6,v6
MM v[36:39],a[248:251],v[192:195],v[36:39]
TR a[168:169],v233 offset:17408
TR a[170:171],v233 offset:21504
MM v[36:39],a[244:247],v[176:179],v[36:39]
EX v7,v7
MM v[36:39],a[240:243],v[160:163],v[36:39]
TR a[172:173],v233 offset:25600
CV v3,v6,v7
MM v[44:47],a[240:243],v[164:167],v[132:135]
EX v8,v8
MM v[44:47],a[244:247],v[180:183],v[44:47]
TR a[174:175],v233 offset:29696
TR a[176:177],v232 offset:17920
MM v[44:47],a[248:251],v[196:199],v[44:47]
EX v9,v9
MM v[44:47],a[252:255],v[212:215],v[44:47]
TR a[178:179],v232 offset:22016
TR a[180:181],v232 offset:26112
MM v[40:43],a[236:239],v[212:215],v[132:135]
EX v10,v10
MM v[40:43],a[232:235],v[196:199],v[40:43]
TR a[182:183],v232 offset:30208
CV v8,v8,v9
MM v[40:43],a[228:231],v[180:183],v[40:43]
EX v11,v11
MM v[40:43],a[224:227],v[164:167],v[40:43]
TR a[184:185],v233 offset:17920
TR a[186:187],v233 offset:22016
MM v[16:19],a[192:195],v[168:171],0
EX v12,v12
MM v[16:19],a[196:199],v[184:187],v[16:19]
TR a[188:189],v233 offset:26112
TR a[190:191],v233 offset:30208
MM v[16:19],a[200:203],v[200:203],v[16:19]
EX v13,v13
MM v[16:19],a[204:207],v[216:219],v[16:19]
EX v14,v14
MM v[24:27],a[192:195],v[172:175],0
EX v15,v15
MM v[24:27],a[196:199],v[188:191],v[24:27]
EX v32,v32
MM v[24:27],a[200:203],v[204:207],v[24:27]
EX v33,v33
MM v[24:27],a[204:207],v[220:223],v[24:27]
s_waitcnt vmcnt(12) lgkmcnt(0)
s_barrier
CV v9,v10,v11
MM a[0:3],a[128:131],v[64:67],a[0:3]
s_add_u32 m0, s65, 16384
CV v10,v12,v13
CV v11,v14,v15
MM a[0:3],a[132:135],v[68:71],a[0:3]
RD a[192:195],v224 offset:49152
CV v4,v32,v33
MM a[8:11],a[132:135],v[76:79],a[8:11]
v_max3_f32 v154, v16, v17, v18
v_max_f32 v154, v154, v19
MM a[8:11],a[128:131],v[72:75],a[8:11]
buffer_load_dwordx4 v228, s[8:11], s64 offen offset:0 lds
MM a[12:15],a[136:139],v[72:75],a[12:15]
EX v34,v34
MM a[12:15],a[140:143],v[76:79],a[12:15]
RD a[196:199],v225 offset:49152
v_mov_b32 v155, v154
MM a[4:7],a[140:143],v[68:71],a[4:7]
s_nop 0
v_permlane16_swap_b32 v154, v155
v_max_f32 v154, v154, v155
MM a[4:7],a[136:139],v[64:67],a[4:7]
EX v35,v35
MM a[32:35],a[144:147],v[64:67],a[32:35]
RD a[200:203],v226 offset:49152
v_mov_b32 v155, v154
MM a[32:35],a[148:151],v[68:71],a[32:35]
s_nop 0
v_permlane32_swap_b32 v154, v155
v_max_f32 v154, v154, v155
MM a[40:43],a[148:151],v[76:79],a[40:43]
EX v36,v36
MM a[40:43],a[144:147],v[72:75],a[40:43]
RD a[204:207],v227 offset:49152
v_sub_f32 v136, 0xc1600000, v154
MM a[44:47],a[152:155],v[72:75],a[44:47]
EX v37,v37
MM a[44:47],a[156:159],v[76:79],a[44:47]
buffer_load_dwordx4 v229, s[8:11], s64 offen offset:1024 lds
v_sub_f32 v137, 0xc1600000, v154
v_sub_f32 v138, 0xc1600000, v154
MM a[36:39],a[156:159],v[68:71],a[36:39]
EX v38,v38
MM a[36:39],a[152:155],v[64:67],a[36:39]
RD a[208:211],v224 offset:53248
v_sub_f32 v139, 0xc1600000, v154
v_mov_b32 v246, 0
MM a[64:67],a[160:163],v[64:67],a[64:67]
EX v39,v39
v_mov_b32 v152, 0
MM a[64:67],a[164:167],v[68:71],a[64:67]
buffer_load_dwordx4 v230, s[8:11], s64 offen offset:2048 lds
RD a[212:215],v225 offset:53248
v_mov_b32 v153, 0
MM a[72:75],a[164:167],v[76:79],a[72:75]
EX v40,v40
v_max3_f32 v158, v24, v25, v26
MM a[72:75],a[160:163],v[72:75],a[72:75]
RD a[216:219],v226 offset:53248
v_max_f32 v158, v158, v27
CV v5,v34,v35
MM a[76:79],a[168:171],v[72:75],a[76:79]
buffer_load_dwordx4 v231, s[8:11], s64 offen offset:3072 lds
RD a[220:223],v227 offset:53248
EX v41,v41
v_mov_b32 v159, v158
MM a[76:79],a[172:175],v[76:79],a[76:79]
s_add_u32 m0, s65, 81920
v_permlane16_swap_b32 v158, v159
v_max_f32 v158, v158, v159
MM a[68:71],a[172:175],v[68:71],a[68:71]
RD a[224:227],v224 offset:57344
EX v42,v42
v_mov_b32 v159, v158
MM a[68:71],a[168:171],v[64:67],a[68:71]
s_nop 0
v_permlane32_swap_b32 v158, v159
CV v6,v36,v37
EX v43,v43
MM a[96:99],a[176:179],v[64:67],a[96:99]
RD a[228:231],v225 offset:57344
buffer_load_dwordx4 v235, s[12:15], s81 offen offset:0 lds
v_max_f32 v158, v158, v159
MM a[96:99],a[180:183],v[68:71],a[96:99]
v_sub_f32 v140, 0xc1600000, v158
CV v7,v38,v39
EX v44,v44
MM a[104:107],a[180:183],v[76:79],a[104:107]
RD a[232:235],v226 offset:57344
v_sub_f32 v141, 0xc1600000, v158
MM a[104:107],a[176:179],v[72:75],a[104:107]
v_sub_f32 v142, 0xc1600000, v158
v_sub_f32 v143, 0xc1600000, v158
EX v45,v45
MM a[108:111],a[184:187],v[72:75],a[108:111]
RD a[236:239],v227 offset:57344
buffer_load_dwordx4 v235, s[12:15], s64 offen offset:1024 lds
v_mov_b32 v247, 0
v_mov_b32 v156, 0
v_mov_b32 v157, 0
MM a[108:111],a[188:191],v[76:79],a[108:111]
EX v46,v46
MM a[100:103],a[188:191],v[68:71],a[100:103]
RD a[240:243],v224 offset:61440
RD a[244:247],v225 offset:61440
RD a[248:251],v226 offset:61440
RD a[252:255],v227 offset:61440
EX v47,v47
MM a[100:103],a[184:187],v[64:67],a[100:103]
buffer_load_dwordx4 v235, s[12:15], s81 offen offset:2048 lds
CV v12,v40,v41
v_smfmac_f32_16x16x64_bf16 v[144:147], v[236:239], v[0:7], v234
CV v13,v42,v43
CV v14,v44,v45
CV v15,v46,v47
s_nop 1
v_smfmac_f32_16x16x64_bf16 v[148:151], v[236:239], v[8:15], v234
s_waitcnt lgkmcnt(0)
buffer_load_dwordx4 v235, s[12:15], s64 offen offset:3072 lds
MM v[64:67],a[192:195],v[160:163],v[128:131]
s_add_u32 s64, s64, 16384
TR a[128:129],v232 offset:32768
s_add_u32 s81, s64, 896
MM v[64:67],a[196:199],v[176:179],v[64:67]
TR a[130:131],v232 offset:36864
TR a[132:133],v232 offset:40960
MM v[64:67],a[200:203],v[192:195],v[64:67]
TR a[134:135],v232 offset:45056
TR a[136:137],v233 offset:32768
MM v[64:67],a[204:207],v[208:211],v[64:67]
v_max3_f32 v249, v144, v145, v148
v_max_f32 v249, v249, v149
MM v[68:71],a[220:223],v[208:211],v[128:131]
TR a[138:139],v233 offset:36864
v_cmp_lt_f32 vcc, s71, v249
MM v[68:71],a[216:219],v[192:195],v[68:71]
s_cbranch_vccnz LSITE2_%=
LBACK2_%=:
TR a[140:141],v233 offset:40960
v_add_f32 v244, v144, v145
MM v[68:71],a[212:215],v[176:179],v[68:71]
TR a[142:143],v233 offset:45056
v_add_f32 v245, v148, v149
MM v[68:71],a[208:211],v[160:163],v[68:71]
TR a[144:145],v232 offset:33280
TR a[146:147],v232 offset:37376
MM v[76:79],a[208:211],v[164:167],v[132:135]
s_lshl_b32 s90, %[swave], 10
s_add_u32 s90, s90, 81920
TR a[148:149],v232 offset:41472
MM v[76:79],a[212:215],v[180:183],v[76:79]
buffer_load_dwordx4 v[56:59], v248, s[20:23], s90 offen offset:0 sc1
MM v[76:79],a[216:219],v[196:199],v[76:79]
EX v64,v64
MM v[76:79],a[220:223],v[212:215],v[76:79]
EX v65,v65
MM v[72:75],a[204:207],v[212:215],v[132:135]
EX v66,v66
MM v[72:75],a[200:203],v[196:199],v[72:75]
EX v67,v67
MM v[72:75],a[196:199],v[180:183],v[72:75]
CV v64,v64,v65
CV v65,v66,v67
MM v[72:75],a[192:195],v[164:167],v[72:75]
EX v68,v68
MM v[80:83],a[192:195],v[168:171],v[136:139]
EX v69,v69
MM v[80:83],a[196:199],v[184:187],v[80:83]
EX v70,v70
MM v[80:83],a[200:203],v[200:203],v[80:83]
EX v71,v71
MM v[80:83],a[204:207],v[216:219],v[80:83]
CV v66,v68,v69
CV v67,v70,v71
MM v[84:87],a[220:223],v[216:219],v[136:139]
EX v72,v72
MM v[84:87],a[216:219],v[200:203],v[84:87]
EX v73,v73
MM v[84:87],a[212:215],v[184:187],v[84:87]
TR a[150:151],v232 offset:45568
TR a[152:153],v233 offset:33280
MM v[84:87],a[208:211],v[168:171],v[84:87]
EX v74,v74
MM v[92:95],a[208:211],v[172:175],v[140:143]
EX v75,v75
MM v[92:95],a[212:215],v[188:191],v[92:95]
TR a[154:155],v233 offset:37376
CV v72,v72,v73
MM v[92:95],a[216:219],v[204:207],v[92:95]
EX v76,v76
MM v[92:95],a[220:223],v[220:223],v[92:95]
EX v77,v77
MM v[88:91],a[204:207],v[220:223],v[140:143]
TR a[156:157],v233 offset:41472
CV v73,v74,v75
MM v[88:91],a[200:203],v[204:207],v[88:91]
EX v78,v78
MM v[88:91],a[196:199],v[188:191],v[88:91]
EX v79,v79
MM v[88:91],a[192:195],v[172:175],v[88:91]
TR a[158:159],v233 offset:45568
CV v74,v76,v77
MM v[96:99],a[224:227],v[160:163],v[128:131]
EX v80,v80
MM v[96:99],a[228:231],v[176:179],v[96:99]
EX v81,v81
MM v[96:99],a[232:235],v[192:195],v[96:99]
TR a[160:161],v232 offset:33792
TR a[162:163],v232 offset:37888
MM v[96:99],a[236:239],v[208:211],v[96:99]
EX v82,v82
MM v[100:103],a[252:255],v[208:211],v[128:131]
EX v83,v83
MM v[100:103],a[248:251],v[192:195],v[100:103]
TR a[164:165],v232 offset:41984
CV v75,v78,v79
MM v[100:103],a[244:247],v[176:179],v[100:103]
EX v84,v84
MM v[100:103],a[240:243],v[160:163],v[100:103]
EX v85,v85
MM v[108:111],a[240:243],v[164:167],v[132:135]
TR a[166:167],v232 offset:46080
CV v80,v80,v81
MM v[108:111],a[244:247],v[180:183],v[108:111]
EX v86,v86
MM v[108:111],a[248:251],v[196:199],v[108:111]
EX v87,v87
MM v[108:111],a[252:255],v[212:215],v[108:111]
TR a[168:169],v233 offset:33792
TR a[170:171],v233 offset:37888
MM v[104:107],a[236:239],v[212:215],v[132:135]
EX v88,v88
MM v[104:107],a[232:235],v[196:199],v[104:107]
EX v89,v89
MM v[104:107],a[228:231],v[180:183],v[104:107]
TR a[172:173],v233 offset:41984
CV v81,v82,v83
EX v90,v90
MM v[104:107],a[224:227],v[164:167],v[104:107]
TR a[174:175],v233 offset:46080
CV v82,v84,v85
MM v[112:115],a[224:227],v[168:171],v[136:139]
EX v91,v91
MM v[112:115],a[228:231],v[184:187],v[112:115]
EX v92,v92
MM v[112:115],a[232:235],v[200:203],v[112:115]
TR a[176:177],v232 offset:34304
EX v93,v93
MM v[112:115],a[236:239],v[216:219],v[112:115]
TR a[178:179],v232 offset:38400
CV v83,v86,v87
MM v[116:119],a[252:255],v[216:219],v[136:139]
EX v94,v94
MM v[116:119],a[248:251],v[200:203],v[116:119]
EX v95,v95
MM v[116:119],a[244:247],v[184:187],v[116:119]
TR a[180:181],v232 offset:42496
CV v88,v88,v89
MM v[116:119],a[240:243],v[168:171],v[116:119]
s_memrealtime s[56:57]
EX v96,v96
MM v[124:127],a[240:243],v[172:175],v[140:143]
s_memtime s[58:59]
TR a[182:183],v232 offset:46592
EX v97,v97
CV v89,v90,v91
MM v[124:127],a[244:247],v[188:191],v[124:127]
EX v98,v98
MM v[124:127],a[248:251],v[204:207],v[124:127]
TR a[184:185],v233 offset:34304
TR a[186:187],v233 offset:38400
TR a[188:189],v233 offset:42496
TR a[190:191],v233 offset:46592
MM v[124:127],a[252:255],v[220:223],v[124:127]
EX v99,v99
CV v90,v92,v93
MM v[120:123],a[236:239],v[220:223],v[140:143]
EX v100,v100
MM v[120:123],a[232:235],v[204:207],v[120:123]
EX v101,v101
CV v91,v94,v95
MM v[120:123],a[228:231],v[188:191],v[120:123]
EX v102,v102
MM v[120:123],a[224:227],v[172:175],v[120:123]
s_waitcnt vmcnt(13) lgkmcnt(0)
s_barrier
CV v68,v96,v97
MM a[0:3],a[128:131],v[0:3],a[0:3]
s_sub_u32 s52, s56, s52
s_sub_u32 s54, s58, s54
v_cvt_f32_u32 v250, s52
EX v103,v103
MM a[0:3],a[132:135],v[4:7],a[0:3]
s_add_u32 m0, s65, 32768
v_cvt_f32_u32 v251, s54
CV v69,v98,v99
EX v104,v104
MM a[8:11],a[132:135],v[12:15],a[8:11]
v_rcp_f32 v251, v251
MM a[8:11],a[128:131],v[8:11],a[8:11]
v_mul_f32 v250, 0x47abe000, v250
s_lshl_b32 s52, %[swave], 11
EX v105,v105
MM a[12:15],a[136:139],v[8:11],a[12:15]
v_mul_f32 v250, v250, v251
s_lshl_b32 s54, %[sb], 3
EX v106,v106
MM a[12:15],a[140:143],v[12:15],a[12:15]
v_cvt_u32_f32 v250, v250
s_add_u32 s52, s52, s54
EX v107,v107
MM a[4:7],a[140:143],v[4:7],a[4:7]
s_waitcnt vmcnt(0)
v_add_u32 v250, s56, v250
v_mov_b32 v251, s69
CV v70,v100,v101
MM a[4:7],a[136:139],v[0:3],a[4:7]
s_add_u32 s52, s52, 86016
s_mov_b64 exec, 1
buffer_store_dwordx2 v[250:251], off, s[20:23], s52 sc1
s_mov_b64 exec, -1
s_nop 0
EX v108,v108
MM a[32:35],a[144:147],v[0:3],a[32:35]
RD a[192:195],v224 offset:0
v_xor_b32 v56, s69, v56
v_xor_b32 v57, s69, v57
EX v109,v109
CV v71,v102,v103
MM a[32:35],a[148:151],v[4:7],a[32:35]
buffer_load_dwordx4 v228, s[8:11], s64 offen offset:0 lds
v_xor_b32 v58, s69, v58
v_xor_b32 v59, s69, v59
MM a[40:43],a[148:151],v[12:15],a[40:43]
RD a[196:199],v225 offset:0
v_or3_b32 v252, v56, v57, v58
)ASM"
R"ASM(
v_or_b32 v252, v252, v59
EX v110,v110
MM a[40:43],a[144:147],v[8:11],a[40:43]
v_cmp_ne_u32 vcc, 0, v252
s_cbranch_vccnz LRFR_%=
LRFRb_%=:
EX v111,v111
CV v76,v104,v105
MM a[44:47],a[152:155],v[8:11],a[44:47]
s_lshl_b32 s90, %[swave], 11
s_add_u32 s78, s90, %[slds]
s_add_u32 s78, s78, 155648
RD a[200:203],v226 offset:0
EX v112,v112
MM a[44:47],a[156:159],v[12:15],a[44:47]
s_add_u32 s90, s90, 53248
buffer_load_dwordx4 v229, s[8:11], s64 offen offset:1024 lds
MM a[36:39],a[156:159],v[4:7],a[36:39]
RD a[204:207],v227 offset:0
EX v113,v113
CV v77,v106,v107
MM a[36:39],a[152:155],v[0:3],a[36:39]
EX v114,v114
MM a[64:67],a[160:163],v[0:3],a[64:67]
RD a[208:211],v224 offset:4096
EX v115,v115
CV v78,v108,v109
MM a[64:67],a[164:167],v[4:7],a[64:67]
buffer_load_dwordx4 v230, s[8:11], s64 offen offset:2048 lds
MM a[72:75],a[164:167],v[12:15],a[72:75]
RD a[212:215],v225 offset:4096
EX v116,v116
MM a[72:75],a[160:163],v[8:11],a[72:75]
EX v117,v117
CV v79,v110,v111
MM a[76:79],a[168:171],v[8:11],a[76:79]
RD a[216:219],v226 offset:4096
CV v84,v112,v113
EX v118,v118
MM a[76:79],a[172:175],v[12:15],a[76:79]
buffer_load_dwordx4 v231, s[8:11], s64 offen offset:3072 lds
MM a[68:71],a[172:175],v[4:7],a[68:71]
s_add_u32 m0, s65, 98304
RD a[220:223],v227 offset:4096
EX v119,v119
MM a[68:71],a[168:171],v[0:3],a[68:71]
EX v120,v120
MM a[96:99],a[176:179],v[0:3],a[96:99]
RD a[224:227],v224 offset:8192
CV v85,v114,v115
EX v121,v121
MM a[96:99],a[180:183],v[4:7],a[96:99]
RD a[228:231],v225 offset:8192
CV v86,v116,v117
MM a[104:107],a[180:183],v[12:15],a[104:107]
buffer_load_dwordx4 v235, s[12:15], s81 offen offset:0 lds
EX v122,v122
MM a[104:107],a[176:179],v[8:11],a[104:107]
EX v123,v123
MM a[108:111],a[184:187],v[8:11],a[108:111]
RD a[232:235],v226 offset:8192
CV v87,v118,v119
MM a[108:111],a[188:191],v[12:15],a[108:111]
EX v124,v124
MM a[100:103],a[188:191],v[4:7],a[100:103]
RD a[236:239],v227 offset:8192
buffer_load_dwordx4 v235, s[12:15], s64 offen offset:1024 lds
EX v125,v125
MM a[100:103],a[184:187],v[0:3],a[100:103]
EX v126,v126
v_smfmac_f32_16x16x64_bf16 v[144:147], v[236:239], v[64:71], v234
RD a[240:243],v224 offset:12288
RD a[244:247],v225 offset:12288
RD a[248:251],v226 offset:12288
RD a[252:255],v227 offset:12288
EX v127,v127
v_smfmac_f32_16x16x64_bf16 v[148:151], v[236:239], v[72:79], v234
CV v92,v120,v121
v_smfmac_f32_16x16x64_bf16 v[152:155], v[236:239], v[80:87], v234
buffer_load_dwordx4 v235, s[12:15], s81 offen offset:2048 lds
CV v93,v122,v123
CV v94,v124,v125
CV v95,v126,v127
s_nop 1
v_smfmac_f32_16x16x64_bf16 v[156:159], v[236:239], v[88:95], v234
s_waitcnt lgkmcnt(0)
buffer_load_dwordx4 v235, s[12:15], s64 offen offset:3072 lds
MM v[0:3],a[192:195],v[160:163],v[128:131]
s_add_u32 s64, s64, 16384
TR a[128:129],v232 offset:49152
s_add_u32 s81, s64, 896
MM v[0:3],a[196:199],v[176:179],v[0:3]
TR a[130:131],v232 offset:53248
v_max3_f32 v249, v144, v145, v148
MM v[0:3],a[200:203],v[192:195],v[0:3]
TR a[132:133],v232 offset:57344
v_max3_f32 v249, v249, v149, v152
MM v[0:3],a[204:207],v[208:211],v[0:3]
TR a[134:135],v232 offset:61440
v_max3_f32 v249, v249, v153, v156
MM v[4:7],a[220:223],v[208:211],v[128:131]
TR a[136:137],v233 offset:49152
v_max_f32 v249, v249, v157
MM v[4:7],a[216:219],v[192:195],v[4:7]
TR a[138:139],v233 offset:53248
v_cmp_lt_f32 vcc, s71, v249
MM v[4:7],a[212:215],v[176:179],v[4:7]
TR a[140:141],v233 offset:57344
TR a[142:143],v233 offset:61440
MM v[4:7],a[208:211],v[160:163],v[4:7]
s_cbranch_vccnz LSITE3_%=
LBACK3_%=:
MM v[12:15],a[208:211],v[164:167],v[132:135]
s_mov_b32 m0, s78
EX v0,v0
MM v[12:15],a[212:215],v[180:183],v[12:15]
buffer_load_dwordx4 v248, s[20:23], s90 offen offset:0 sc1 lds
MM v[12:15],a[216:219],v[196:199],v[12:15]
s_waitcnt lgkmcnt(6)
buffer_load_dwordx4 v248, s[20:23], s90 offen offset:1024 sc1 lds
MM v[12:15],a[220:223],v[212:215],v[12:15]
EX v1,v1
MM v[112:115],a[128:131],v[80:83],0
MM v[116:119],a[128:131],v[88:91],0
MM v[8:11],a[204:207],v[212:215],v[132:135]
EX v2,v2
MM v[8:11],a[200:203],v[196:199],v[8:11]
TR a[144:145],v232 offset:49664
CV v0,v0,v1
MM v[8:11],a[196:199],v[180:183],v[8:11]
EX v3,v3
MM v[8:11],a[192:195],v[164:167],v[8:11]
TR a[146:147],v232 offset:53760
CV v1,v2,v3
MM v[16:19],a[192:195],v[168:171],v[136:139]
v_max3_f32 v112, |v112|, |v113|, |v114|
v_max_f32 v112, v112, |v115|
MM v[16:19],a[196:199],v[184:187],v[16:19]
v_bfe_u32 v112, v112, 23, 8
v_add_u32 v112, 7, v112
MM v[16:19],a[200:203],v[200:203],v[16:19]
v_min_u32 v112, 240, v112
v_lshl_or_b32 v241, v112, 16, v241
MM v[16:19],a[204:207],v[216:219],v[16:19]
v_lshl_or_b32 v112, v112, 1, 1
v_lshlrev_b32 v112, 22, v112
EX v4,v4
MM v[20:23],a[220:223],v[216:219],v[136:139]
AW a116,v112
AW a117,v112
MM v[20:23],a[216:219],v[200:203],v[20:23]
AW a118,v112
AW a119,v112
EX v5,v5
MM v[20:23],a[212:215],v[184:187],v[20:23]
v_max3_f32 v116, |v116|, |v117|, |v118|
v_max_f32 v116, v116, |v119|
MM v[20:23],a[208:211],v[168:171],v[20:23]
v_bfe_u32 v116, v116, 23, 8
v_add_u32 v116, 7, v116
EX v6,v6
MM v[28:31],a[208:211],v[172:175],v[140:143]
v_min_u32 v116, 240, v116
v_lshl_or_b32 v241, v116, 24, v241
MM v[28:31],a[212:215],v[188:191],v[28:31]
TR a[148:149],v232 offset:57856
v_lshl_or_b32 v116, v116, 1, 1
v_lshlrev_b32 v116, 22, v116
EX v7,v7
MM v[28:31],a[216:219],v[204:207],v[28:31]
AW a124,v116
AW a125,v116
MM v[28:31],a[220:223],v[220:223],v[28:31]
TR a[150:151],v232 offset:61952
AW a126,v116
AW a127,v116
EX v8,v8
MM v[24:27],a[204:207],v[220:223],v[140:143]
TR a[152:153],v233 offset:49664
TR a[154:155],v233 offset:53760
MM v[24:27],a[200:203],v[204:207],v[24:27]
EX v9,v9
MM v[24:27],a[196:199],v[188:191],v[24:27]
EX v10,v10
MM v[24:27],a[192:195],v[172:175],v[24:27]
TR a[156:157],v233 offset:57856
CV v2,v4,v5
MM v[32:35],a[224:227],v[160:163],v[128:131]
EX v11,v11
MM v[32:35],a[228:231],v[176:179],v[32:35]
TR a[158:159],v233 offset:61952
CV v3,v6,v7
MM v[32:35],a[232:235],v[192:195],v[32:35]
EX v12,v12
MM v[32:35],a[236:239],v[208:211],v[32:35]
TR a[160:161],v232 offset:50176
TR a[162:163],v232 offset:54272
MM v[36:39],a[252:255],v[208:211],v[128:131]
EX v13,v13
MM v[36:39],a[248:251],v[192:195],v[36:39]
EX v14,v14
MM v[36:39],a[244:247],v[176:179],v[36:39]
TR a[164:165],v232 offset:58368
CV v8,v8,v9
MM v[36:39],a[240:243],v[160:163],v[36:39]
EX v15,v15
MM v[44:47],a[240:243],v[164:167],v[132:135]
s_lshl_b32 s76, s50, 1
s_nop 0
LPG6_%=:
s_cbranch_execz LPG10_%=
TR a[166:167],v232 offset:62464
CV v9,v10,v11
MM v[44:47],a[244:247],v[180:183],v[44:47]
s_bfe_u32 s77, s49, 65538
EX v16,v16
MM v[44:47],a[248:251],v[196:199],v[44:47]
s_add_u32 s76, s76, s77
TR a[168:169],v233 offset:50176
TR a[170:171],v233 offset:54272
MM v[44:47],a[252:255],v[212:215],v[44:47]
s_lshl_b32 s76, s76, 2
EX v17,v17
MM v[40:43],a[236:239],v[212:215],v[132:135]
s_add_u32 s76, s76, s84
TR a[172:173],v233 offset:58368
CV v10,v12,v13
MM v[40:43],a[232:235],v[196:199],v[40:43]
s_lshl_b32 s76, s76, 2
EX v18,v18
MM v[40:43],a[228:231],v[180:183],v[40:43]
s_lshr_b32 s77, s49, 3
EX v19,v19
MM v[40:43],a[224:227],v[164:167],v[40:43]
s_add_u32 s76, s76, s77
TR a[174:175],v233 offset:62464
CV v11,v14,v15
MM v[48:51],a[224:227],v[168:171],v[136:139]
s_mul_i32 s95, s76, 33792
EX v20,v20
MM v[48:51],a[228:231],v[184:187],v[48:51]
s_lshl_b32 s76, %[swave], 13
TR a[176:177],v232 offset:50688
TR a[178:179],v232 offset:54784
MM v[48:51],a[232:235],v[200:203],v[48:51]
s_add_u32 s95, s95, s76
EX v21,v21
MM v[48:51],a[236:239],v[216:219],v[48:51]
s_add_u32 s95, s95, 22708224
v_mov_b32 v243, -1
CV v16,v16,v17
MM v[52:55],a[252:255],v[216:219],v[136:139]
s_mov_b32 s67, s95
EX v22,v22
MM v[52:55],a[248:251],v[200:203],v[52:55]
s_add_u32 s87, s95, 6144
buffer_store_dword v243, v248, s[20:23], s87 offen offset:12 sc1
MM v[52:55],a[244:247],v[184:187],v[52:55]
TR a[180:181],v232 offset:58880
TR a[182:183],v232 offset:62976
MM v[52:55],a[240:243],v[168:171],v[52:55]
EX v23,v23
MM v[60:63],a[240:243],v[172:175],v[140:143]
EX v24,v24
MM v[60:63],a[244:247],v[188:191],v[60:63]
TR a[184:185],v233 offset:50688
CV v17,v18,v19
MM v[60:63],a[248:251],v[204:207],v[60:63]
TR a[186:187],v233 offset:54784
TR a[188:189],v233 offset:58880
TR a[190:191],v233 offset:62976
EX v25,v25
MM v[60:63],a[252:255],v[220:223],v[60:63]
s_add_u32 s87, s95, 7168
buffer_store_dword v243, v248, s[20:23], s87 offen offset:12 sc1
MM v[56:59],a[236:239],v[220:223],v[140:143]
EX v26,v26
MM v[56:59],a[232:235],v[204:207],v[56:59]
EX v27,v27
MM v[56:59],a[228:231],v[188:191],v[56:59]
EX v28,v28
CV v18,v20,v21
MM v[56:59],a[224:227],v[172:175],v[56:59]
s_waitcnt lgkmcnt(0)
s_barrier
MM a[0:3],a[128:131],v[64:67],a[0:3]
s_add_u32 m0, s65, 49152
CV v19,v22,v23
CV v24,v24,v25
MM a[0:3],a[132:135],v[68:71],a[0:3]
EX v29,v29
MM a[8:11],a[132:135],v[76:79],a[8:11]
EX v30,v30
MM a[8:11],a[128:131],v[72:75],a[8:11]
CV v25,v26,v27
CV v26,v28,v29
MM a[16:19],a[128:131],v[80:83],a[116:119]
EX v31,v31
MM a[16:19],a[132:135],v[84:87],a[16:19]
EX v32,v32
MM a[24:27],a[132:135],v[92:95],a[124:127]
RD a[192:195],v224 offset:16384
CV v27,v30,v31
MM a[24:27],a[128:131],v[88:91],a[24:27]
EX v33,v33
MM a[28:31],a[136:139],v[88:91],a[124:127]
buffer_load_dwordx4 v228, s[8:11], s64 offen offset:0 lds
MM a[28:31],a[140:143],v[92:95],a[28:31]
EX v34,v34
MM a[20:23],a[140:143],v[84:87],a[116:119]
RD a[196:199],v225 offset:16384
CV v4,v32,v33
MM a[20:23],a[136:139],v[80:83],a[20:23]
EX v35,v35
MM a[12:15],a[136:139],v[72:75],a[12:15]
EX v36,v36
MM a[12:15],a[140:143],v[76:79],a[12:15]
RD a[200:203],v226 offset:16384
CV v5,v34,v35
MM a[4:7],a[140:143],v[68:71],a[4:7]
EX v37,v37
MM a[4:7],a[136:139],v[64:67],a[4:7]
buffer_load_dwordx4 v229, s[8:11], s64 offen offset:1024 lds
MM a[32:35],a[144:147],v[64:67],a[32:35]
EX v38,v38
MM a[32:35],a[148:151],v[68:71],a[32:35]
RD a[204:207],v227 offset:16384
CV v6,v36,v37
MM a[40:43],a[148:151],v[76:79],a[40:43]
EX v39,v39
MM a[40:43],a[144:147],v[72:75],a[40:43]
RD a[208:211],v224 offset:20480
CV v7,v38,v39
MM a[48:51],a[144:147],v[80:83],a[116:119]
EX v40,v40
MM a[48:51],a[148:151],v[84:87],a[48:51]
EX v41,v41
MM a[56:59],a[148:151],v[92:95],a[124:127]
buffer_load_dwordx4 v230, s[8:11], s64 offen offset:2048 lds
MM a[56:59],a[144:147],v[88:91],a[56:59]
EX v42,v42
MM a[60:63],a[152:155],v[88:91],a[124:127]
RD a[212:215],v225 offset:20480
CV v12,v40,v41
MM a[60:63],a[156:159],v[92:95],a[60:63]
EX v43,v43
MM a[52:55],a[156:159],v[84:87],a[116:119]
RD a[216:219],v226 offset:20480
CV v13,v42,v43
MM a[52:55],a[152:155],v[80:83],a[52:55]
EX v44,v44
MM a[44:47],a[152:155],v[72:75],a[44:47]
EX v45,v45
MM a[44:47],a[156:159],v[76:79],a[44:47]
buffer_load_dwordx4 v231, s[8:11], s64 offen offset:3072 lds
MM a[36:39],a[156:159],v[68:71],a[36:39]
s_add_u32 m0, s65, 114688
RD a[220:223],v227 offset:20480
CV v14,v44,v45
MM a[36:39],a[152:155],v[64:67],a[36:39]
EX v46,v46
MM a[64:67],a[160:163],v[64:67],a[64:67]
EX v47,v47
MM a[64:67],a[164:167],v[68:71],a[64:67]
RD a[224:227],v224 offset:24576
CV v15,v46,v47
MM a[72:75],a[164:167],v[76:79],a[72:75]
EX v48,v48
MM a[72:75],a[160:163],v[72:75],a[72:75]
buffer_load_dwordx4 v235, s[12:15], s81 offen offset:0 lds
MM a[80:83],a[160:163],v[80:83],a[116:119]
EX v49,v49
MM a[80:83],a[164:167],v[84:87],a[80:83]
RD a[228:231],v225 offset:24576
CV v20,v48,v49
MM a[88:91],a[164:167],v[92:95],a[124:127]
EX v50,v50
MM a[88:91],a[160:163],v[88:91],a[88:91]
EX v51,v51
MM a[92:95],a[168:171],v[88:91],a[124:127]
RD a[232:235],v226 offset:24576
CV v21,v50,v51
MM a[92:95],a[172:175],v[92:95],a[92:95]
EX v52,v52
MM a[84:87],a[172:175],v[84:87],a[116:119]
buffer_load_dwordx4 v235, s[12:15], s64 offen offset:1024 lds
MM a[84:87],a[168:171],v[80:83],a[84:87]
EX v53,v53
MM a[76:79],a[168:171],v[72:75],a[76:79]
RD a[236:239],v227 offset:24576
CV v22,v52,v53
MM a[76:79],a[172:175],v[76:79],a[76:79]
EX v54,v54
MM a[68:71],a[172:175],v[68:71],a[68:71]
EX v55,v55
MM a[68:71],a[168:171],v[64:67],a[68:71]
RD a[240:243],v224 offset:28672
CV v23,v54,v55
MM a[96:99],a[176:179],v[64:67],a[96:99]
EX v56,v56
MM a[96:99],a[180:183],v[68:71],a[96:99]
buffer_load_dwordx4 v235, s[12:15], s81 offen offset:2048 lds
)ASM"
R"ASM(
MM a[104:107],a[180:183],v[76:79],a[104:107]
EX v57,v57
MM a[104:107],a[176:179],v[72:75],a[104:107]
RD a[244:247],v225 offset:28672
CV v28,v56,v57
MM a[112:115],a[176:179],v[80:83],a[116:119]
EX v58,v58
MM a[112:115],a[180:183],v[84:87],a[112:115]
RD a[248:251],v226 offset:28672
RD a[252:255],v227 offset:28672
MM a[120:123],a[180:183],v[92:95],a[124:127]
EX v59,v59
MM a[120:123],a[176:179],v[88:91],a[120:123]
EX v60,v60
MM a[124:127],a[184:187],v[88:91],a[124:127]
v_add_f32 v244, v144, v145
v_add_f32 v245, v148, v149
MM a[124:127],a[188:191],v[92:95],a[124:127]
v_add_f32 v246, v152, v153
v_add_f32 v247, v156, v157
MM a[116:119],a[188:191],v[84:87],a[116:119]
EX v61,v61
MM a[116:119],a[184:187],v[80:83],a[116:119]
buffer_load_dwordx4 v235, s[12:15], s64 offen offset:3072 lds
MM a[108:111],a[184:187],v[72:75],a[108:111]
EX v62,v62
MM a[108:111],a[188:191],v[76:79],a[108:111]
EX v63,v63
MM a[100:103],a[188:191],v[68:71],a[100:103]
CV v29,v58,v59
CV v30,v60,v61
MM a[100:103],a[184:187],v[64:67],a[100:103]
CV v31,v62,v63
v_smfmac_f32_16x16x64_bf16 v[144:147], v[236:239], v[0:7], v234
v_smfmac_f32_16x16x64_bf16 v[148:151], v[236:239], v[8:15], v234
v_smfmac_f32_16x16x64_bf16 v[152:155], v[236:239], v[16:23], v234
v_smfmac_f32_16x16x64_bf16 v[156:159], v[236:239], v[24:31], v234
s_waitcnt lgkmcnt(0)
MM v[64:67],a[192:195],v[160:163],v[128:131]
s_add_u32 s64, s64, 16384
TR a[128:129],v232 offset:0
s_add_u32 s81, s64, 896
MM v[64:67],a[196:199],v[176:179],v[64:67]
TR a[130:131],v232 offset:4096
v_max3_f32 v249, v144, v145, v148
MM v[64:67],a[200:203],v[192:195],v[64:67]
TR a[132:133],v232 offset:8192
v_max3_f32 v249, v249, v149, v152
MM v[64:67],a[204:207],v[208:211],v[64:67]
TR a[134:135],v232 offset:12288
v_max3_f32 v249, v249, v153, v156
MM v[68:71],a[220:223],v[208:211],v[128:131]
TR a[136:137],v233 offset:0
v_max_f32 v249, v249, v157
MM v[68:71],a[216:219],v[192:195],v[68:71]
TR a[138:139],v233 offset:4096
v_cmp_lt_f32 vcc, s71, v249
MM v[68:71],a[212:215],v[176:179],v[68:71]
TR a[140:141],v233 offset:8192
TR a[142:143],v233 offset:12288
MM v[68:71],a[208:211],v[160:163],v[68:71]
s_cbranch_vccnz LSITE4_%=
LBACK4_%=:
MM v[76:79],a[208:211],v[164:167],v[132:135]
s_cmp_lg_u32 %[sb], 0
s_cbranch_scc1 LEP_%=
v_mov_b32 v250, s69
s_mov_b32 s76, 32768
s_mov_b64 exec, 1
buffer_store_dword v250, off, s[20:23], s76 sc1
s_mov_b64 exec, -1
s_nop 1
LEP_%=:
MM v[76:79],a[212:215],v[180:183],v[76:79]
EX v64,v64
MM v[76:79],a[216:219],v[196:199],v[76:79]
EX v65,v65
MM v[76:79],a[220:223],v[212:215],v[76:79]
TR a[144:145],v232 offset:512
CV v64,v64,v65
MM v[72:75],a[204:207],v[212:215],v[132:135]
TR a[146:147],v232 offset:4608
v_lshrrev_b32 v34, 1, v248
MM v[72:75],a[200:203],v[196:199],v[72:75]
s_mov_b32 s90, 86016
buffer_load_dwordx2 v[32:33], v34, s[20:23], s90 offen offset:0 sc1
MM v[72:75],a[196:199],v[180:183],v[72:75]
EX v66,v66
MM v[72:75],a[192:195],v[164:167],v[72:75]
EX v67,v67
MM v[80:83],a[192:195],v[168:171],v[136:139]
EX v68,v68
MM v[80:83],a[196:199],v[184:187],v[80:83]
TR a[148:149],v232 offset:8704
CV v65,v66,v67
MM v[80:83],a[200:203],v[200:203],v[80:83]
EX v69,v69
MM v[80:83],a[204:207],v[216:219],v[80:83]
TR a[150:151],v232 offset:12800
CV v66,v68,v69
MM v[84:87],a[220:223],v[216:219],v[136:139]
EX v70,v70
MM v[84:87],a[216:219],v[200:203],v[84:87]
TR a[152:153],v233 offset:512
TR a[154:155],v233 offset:4608
MM v[84:87],a[212:215],v[184:187],v[84:87]
EX v71,v71
MM v[84:87],a[208:211],v[168:171],v[84:87]
TR a[156:157],v233 offset:8704
CV v67,v70,v71
MM v[92:95],a[208:211],v[172:175],v[140:143]
EX v72,v72
MM v[92:95],a[212:215],v[188:191],v[92:95]
EX v73,v73
MM v[92:95],a[216:219],v[204:207],v[92:95]
TR a[158:159],v233 offset:12800
CV v72,v72,v73
MM v[92:95],a[220:223],v[220:223],v[92:95]
EX v74,v74
MM v[88:91],a[204:207],v[220:223],v[140:143]
TR a[160:161],v232 offset:1024
TR a[162:163],v232 offset:5120
MM v[88:91],a[200:203],v[204:207],v[88:91]
EX v75,v75
MM v[88:91],a[196:199],v[188:191],v[88:91]
TR a[164:165],v232 offset:9216
CV v73,v74,v75
MM v[88:91],a[192:195],v[172:175],v[88:91]
EX v76,v76
MM v[96:99],a[224:227],v[160:163],v[128:131]
EX v77,v77
MM v[96:99],a[228:231],v[176:179],v[96:99]
TR a[166:167],v232 offset:13312
CV v74,v76,v77
MM v[96:99],a[232:235],v[192:195],v[96:99]
EX v78,v78
MM v[96:99],a[236:239],v[208:211],v[96:99]
TR a[168:169],v233 offset:1024
TR a[170:171],v233 offset:5120
MM v[100:103],a[252:255],v[208:211],v[128:131]
EX v79,v79
MM v[100:103],a[248:251],v[192:195],v[100:103]
TR a[172:173],v233 offset:9216
CV v75,v78,v79
MM v[100:103],a[244:247],v[176:179],v[100:103]
EX v80,v80
MM v[100:103],a[240:243],v[160:163],v[100:103]
EX v81,v81
MM v[108:111],a[240:243],v[164:167],v[132:135]
TR a[174:175],v233 offset:13312
CV v80,v80,v81
MM v[108:111],a[244:247],v[180:183],v[108:111]
EX v82,v82
MM v[108:111],a[248:251],v[196:199],v[108:111]
TR a[176:177],v232 offset:1536
TR a[178:179],v232 offset:5632
MM v[108:111],a[252:255],v[212:215],v[108:111]
EX v83,v83
MM v[104:107],a[236:239],v[212:215],v[132:135]
TR a[180:181],v232 offset:9728
CV v81,v82,v83
MM v[104:107],a[232:235],v[196:199],v[104:107]
EX v84,v84
MM v[104:107],a[228:231],v[180:183],v[104:107]
TR a[182:183],v232 offset:13824
TR a[184:185],v233 offset:1536
MM v[104:107],a[224:227],v[164:167],v[104:107]
EX v85,v85
MM v[112:115],a[224:227],v[168:171],v[136:139]
TR a[186:187],v233 offset:5632
CV v82,v84,v85
MM v[112:115],a[228:231],v[184:187],v[112:115]
EX v86,v86
MM v[112:115],a[232:235],v[200:203],v[112:115]
EX v87,v87
MM v[112:115],a[236:239],v[216:219],v[112:115]
TR a[188:189],v233 offset:9728
CV v83,v86,v87
MM v[116:119],a[252:255],v[216:219],v[136:139]
EX v88,v88
MM v[116:119],a[248:251],v[200:203],v[116:119]
EX v89,v89
MM v[116:119],a[244:247],v[184:187],v[116:119]
TR a[190:191],v233 offset:13824
CV v88,v88,v89
MM v[116:119],a[240:243],v[168:171],v[116:119]
EX v90,v90
MM v[124:127],a[240:243],v[172:175],v[140:143]
EX v91,v91
MM v[124:127],a[244:247],v[188:191],v[124:127]
EX v92,v92
MM v[124:127],a[248:251],v[204:207],v[124:127]
EX v93,v93
MM v[124:127],a[252:255],v[220:223],v[124:127]
EX v94,v94
MM v[120:123],a[236:239],v[220:223],v[140:143]
EX v95,v95
MM v[120:123],a[232:235],v[204:207],v[120:123]
EX v96,v96
MM v[120:123],a[228:231],v[188:191],v[120:123]
EX v97,v97
MM v[120:123],a[224:227],v[172:175],v[120:123]
s_waitcnt vmcnt(11) lgkmcnt(0)
s_barrier
MM a[0:3],a[128:131],v[0:3],a[0:3]
s_add_u32 m0, s65, 0
CV v89,v90,v91
CV v90,v92,v93
MM a[0:3],a[132:135],v[4:7],a[0:3]
RD a[192:195],v224 offset:32768
CV v91,v94,v95
MM a[8:11],a[132:135],v[12:15],a[8:11]
buffer_load_dwordx4 v228, s[8:11], s64 offen offset:0 lds
MM a[8:11],a[128:131],v[8:11],a[8:11]
RD a[196:199],v225 offset:32768
CV v68,v96,v97
MM a[16:19],a[128:131],v[16:19],a[16:19]
RD a[200:203],v226 offset:32768
RD a[204:207],v227 offset:32768
MM a[16:19],a[132:135],v[20:23],a[16:19]
buffer_load_dwordx4 v229, s[8:11], s64 offen offset:1024 lds
MM a[24:27],a[132:135],v[28:31],a[24:27]
EX v98,v98
MM a[24:27],a[128:131],v[24:27],a[24:27]
RD a[208:211],v224 offset:36864
RD a[212:215],v225 offset:36864
MM a[28:31],a[136:139],v[24:27],a[28:31]
EX v99,v99
MM a[28:31],a[140:143],v[28:31],a[28:31]
RD a[216:219],v226 offset:36864
CV v69,v98,v99
MM a[20:23],a[140:143],v[20:23],a[20:23]
EX v100,v100
MM a[20:23],a[136:139],v[16:19],a[20:23]
EX v101,v101
MM a[12:15],a[136:139],v[8:11],a[12:15]
s_waitcnt vmcnt(2)
v_cmp_ne_u32 vcc, s69, v33
s_cbranch_vccnz LRRK_%=
LRRKb_%=:
MM a[12:15],a[140:143],v[12:15],a[12:15]
v_add_u32_dpp v32, v32, v32 row_ror:8 row_mask:0xf bank_mask:0xf
CV v70,v100,v101
MM a[4:7],a[140:143],v[4:7],a[4:7]
s_and_b32 s76, %[sb], 7
RD a[220:223],v227 offset:36864
v_mov_b32 v35, v32
MM a[4:7],a[136:139],v[0:3],a[4:7]
s_nop 0
v_permlane16_swap_b32 v32, v35
v_add_u32 v32, v32, v35
MM a[32:35],a[144:147],v[0:3],a[32:35]
RD a[224:227],v224 offset:40960
v_mov_b32 v35, v32
MM a[32:35],a[148:151],v[4:7],a[32:35]
s_nop 0
v_permlane32_swap_b32 v32, v35
v_add_u32 v32, v32, v35
MM a[40:43],a[148:151],v[12:15],a[40:43]
RD a[228:231],v225 offset:40960
v_readlane_b32 s77, v32, s76
MM a[40:43],a[144:147],v[8:11],a[40:43]
s_bfm_b32 s76, s76, 0
EX v102,v102
MM a[48:51],a[144:147],v[16:19],a[48:51]
v_cmp_gt_u32 s[60:61], s77, v32
v_cmp_eq_u32 s[62:63], s77, v32
MM a[48:51],a[148:151],v[20:23],a[48:51]
s_and_b32 s62, s62, s76
EX v103,v103
MM a[56:59],a[148:151],v[28:31],a[56:59]
s_or_b32 s60, s60, s62
EX v104,v104
MM a[56:59],a[144:147],v[24:27],a[56:59]
s_and_b32 s60, s60, 255
EX v105,v105
MM a[60:63],a[152:155],v[24:27],a[60:63]
s_bcnt1_i32_b32 s51, s60
s_cmp_lt_u32 s51, 4
s_cselect_b32 s82, 3, 99
MM a[60:63],a[156:159],v[28:31],a[60:63]
s_lshr_b32 s16, %[sb], 3
EX v106,v106
MM a[52:55],a[156:159],v[20:23],a[52:55]
s_lshl_b32 s76, s51, 5
CV v71,v102,v103
CV v76,v104,v105
MM a[52:55],a[152:155],v[16:19],a[52:55]
s_add_u32 s16, s16, s76
EX v107,v107
MM a[44:47],a[152:155],v[8:11],a[44:47]
buffer_load_dwordx4 v230, s[8:11], s64 offen offset:2048 lds
MM a[44:47],a[156:159],v[12:15],a[44:47]
EX v108,v108
MM a[36:39],a[156:159],v[4:7],a[36:39]
EX v109,v109
MM a[36:39],a[152:155],v[0:3],a[36:39]
CV v77,v106,v107
CV v78,v108,v109
MM a[64:67],a[160:163],v[0:3],a[64:67]
EX v110,v110
MM a[64:67],a[164:167],v[4:7],a[64:67]
EX v111,v111
MM a[72:75],a[164:167],v[12:15],a[72:75]
buffer_load_dwordx4 v231, s[8:11], s64 offen offset:3072 lds
MM a[72:75],a[160:163],v[8:11],a[72:75]
s_add_u32 m0, s65, 65536
EX v112,v112
MM a[80:83],a[160:163],v[16:19],a[80:83]
RD a[232:235],v226 offset:40960
CV v79,v110,v111
MM a[80:83],a[164:167],v[20:23],a[80:83]
EX v113,v113
MM a[88:91],a[164:167],v[28:31],a[88:91]
EX v114,v114
MM a[88:91],a[160:163],v[24:27],a[88:91]
buffer_load_dwordx4 v235, s[12:15], s81 offen offset:0 lds
MM a[92:95],a[168:171],v[24:27],a[92:95]
EX v115,v115
MM a[92:95],a[172:175],v[28:31],a[92:95]
CV v84,v112,v113
CV v85,v114,v115
MM a[84:87],a[172:175],v[20:23],a[84:87]
EX v116,v116
MM a[84:87],a[168:171],v[16:19],a[84:87]
EX v117,v117
MM a[76:79],a[168:171],v[8:11],a[76:79]
RD a[236:239],v227 offset:40960
CV v86,v116,v117
MM a[76:79],a[172:175],v[12:15],a[76:79]
EX v118,v118
MM a[68:71],a[172:175],v[4:7],a[68:71]
buffer_load_dwordx4 v235, s[12:15], s64 offen offset:1024 lds
MM a[68:71],a[168:171],v[0:3],a[68:71]
EX v119,v119
MM a[96:99],a[176:179],v[0:3],a[96:99]
RD a[240:243],v224 offset:45056
CV v87,v118,v119
MM a[96:99],a[180:183],v[4:7],a[96:99]
EX v120,v120
MM a[104:107],a[180:183],v[12:15],a[104:107]
EX v121,v121
MM a[104:107],a[176:179],v[8:11],a[104:107]
RD a[244:247],v225 offset:45056
CV v92,v120,v121
MM a[112:115],a[176:179],v[16:19],a[112:115]
buffer_load_dwordx4 v235, s[12:15], s81 offen offset:2048 lds
MM a[112:115],a[180:183],v[20:23],a[112:115]
EX v122,v122
MM a[120:123],a[180:183],v[28:31],a[120:123]
EX v123,v123
MM a[120:123],a[176:179],v[24:27],a[120:123]
RD a[248:251],v226 offset:45056
CV v93,v122,v123
MM a[124:127],a[184:187],v[24:27],a[124:127]
v_add_f32 v244, v144, v145
v_add_f32 v245, v148, v149
MM a[124:127],a[188:191],v[28:31],a[124:127]
v_add_f32 v246, v152, v153
v_add_f32 v247, v156, v157
MM a[116:119],a[188:191],v[20:23],a[116:119]
EX v124,v124
MM a[116:119],a[184:187],v[16:19],a[116:119]
EX v125,v125
MM a[108:111],a[184:187],v[8:11],a[108:111]
EX v126,v126
MM a[108:111],a[188:191],v[12:15],a[108:111]
RD a[252:255],v227 offset:45056
CV v94,v124,v125
MM a[100:103],a[188:191],v[4:7],a[100:103]
EX v127,v127
MM a[100:103],a[184:187],v[0:3],a[100:103]
buffer_load_dwordx4 v235, s[12:15], s64 offen offset:3072 lds
v_smfmac_f32_16x16x64_bf16 v[144:147], v[236:239], v[64:71], v234
CV v95,v126,v127
v_smfmac_f32_16x16x64_bf16 v[148:151], v[236:239], v[72:79], v234
v_smfmac_f32_16x16x64_bf16 v[152:155], v[236:239], v[80:87], v234
v_smfmac_f32_16x16x64_bf16 v[156:159], v[236:239], v[88:95], v234
s_waitcnt lgkmcnt(0)
s_cmp_lt_u32 s51, 4
s_cbranch_scc1 LLAR_%=
s_mov_b32 s70, 6
s_branch LLAP_%=
s_nop 0
s_nop 0
s_nop 0
s_nop 0
s_nop 0
s_nop 0
s_nop 0
s_nop 0
s_nop 0
s_nop 0
s_nop 0
s_nop 0
s_nop 0
s_nop 0
s_nop 0
s_nop 0
s_nop 0
s_nop 0
s_nop 0
s_nop 0
s_nop 0
)ASM"
R"ASM(
s_nop 0
s_nop 0
s_nop 0
s_nop 0
s_nop 0
s_nop 0
s_nop 0
s_nop 0
s_nop 0
s_nop 0
s_nop 0
s_nop 0
s_nop 0
s_nop 0
s_nop 0
s_nop 0
s_nop 0
s_nop 0
s_nop 0
s_nop 0
s_nop 0
s_nop 0
s_nop 0
s_nop 0
s_nop 0
s_nop 0
s_nop 0
s_nop 0
s_nop 0
s_nop 0
s_nop 0
LPG7_%=:
s_cbranch_execz LPG11_%=
s_nop 0
s_nop 0
s_nop 0
s_nop 0
s_nop 0
s_nop 0
s_nop 0
s_nop 0
s_nop 0
s_nop 0
s_nop 0
s_nop 0
s_nop 0
s_nop 0
s_nop 0
s_nop 0
s_nop 0
s_nop 0
s_nop 0
s_nop 0
s_nop 0
s_nop 0
s_nop 0
s_nop 0
s_nop 0
s_nop 0
s_nop 0
s_nop 0
s_nop 0
s_nop 0
s_nop 0
LLAP_%=:
MM v[0:3],a[192:195],v[160:163],v[128:131]
s_add_u32 s64, s64, 16384
TR a[128:129],v232 offset:16384
s_add_u32 s81, s64, 896
MM v[0:3],a[196:199],v[176:179],v[0:3]
TR a[130:131],v232 offset:20480
v_max3_f32 v249, v144, v145, v148
MM v[0:3],a[200:203],v[192:195],v[0:3]
TR a[132:133],v232 offset:24576
v_max3_f32 v249, v249, v149, v152
MM v[0:3],a[204:207],v[208:211],v[0:3]
TR a[134:135],v232 offset:28672
v_max3_f32 v249, v249, v153, v156
MM v[4:7],a[220:223],v[208:211],v[128:131]
TR a[136:137],v233 offset:16384
v_max_f32 v249, v249, v157
MM v[4:7],a[216:219],v[192:195],v[4:7]
TR a[138:139],v233 offset:20480
v_cmp_lt_f32 vcc, s71, v249
MM v[4:7],a[212:215],v[176:179],v[4:7]
TR a[140:141],v233 offset:24576
TR a[142:143],v233 offset:28672
MM v[4:7],a[208:211],v[160:163],v[4:7]
s_cbranch_vccnz LSITE5_%=
LBACK5_%=:
MM v[12:15],a[208:211],v[164:167],v[132:135]
EX v0,v0
MM v[12:15],a[212:215],v[180:183],v[12:15]
EX v1,v1
MM v[12:15],a[216:219],v[196:199],v[12:15]
TR a[144:145],v232 offset:16896
CV v0,v0,v1
MM v[12:15],a[220:223],v[212:215],v[12:15]
EX v2,v2
MM v[8:11],a[204:207],v[212:215],v[132:135]
EX v3,v3
MM v[8:11],a[200:203],v[196:199],v[8:11]
TR a[146:147],v232 offset:20992
CV v1,v2,v3
MM v[8:11],a[196:199],v[180:183],v[8:11]
EX v4,v4
MM v[8:11],a[192:195],v[164:167],v[8:11]
TR a[148:149],v232 offset:25088
TR a[150:151],v232 offset:29184
MM v[16:19],a[192:195],v[168:171],v[136:139]
EX v5,v5
MM v[16:19],a[196:199],v[184:187],v[16:19]
TR a[152:153],v233 offset:16896
CV v2,v4,v5
MM v[16:19],a[200:203],v[200:203],v[16:19]
EX v6,v6
MM v[16:19],a[204:207],v[216:219],v[16:19]
TR a[154:155],v233 offset:20992
TR a[156:157],v233 offset:25088
MM v[20:23],a[220:223],v[216:219],v[136:139]
EX v7,v7
MM v[20:23],a[216:219],v[200:203],v[20:23]
TR a[158:159],v233 offset:29184
CV v3,v6,v7
MM v[20:23],a[212:215],v[184:187],v[20:23]
EX v8,v8
MM v[20:23],a[208:211],v[168:171],v[20:23]
EX v9,v9
MM v[28:31],a[208:211],v[172:175],v[140:143]
TR a[160:161],v232 offset:17408
CV v8,v8,v9
MM v[28:31],a[212:215],v[188:191],v[28:31]
EX v10,v10
MM v[28:31],a[216:219],v[204:207],v[28:31]
TR a[162:163],v232 offset:21504
TR a[164:165],v232 offset:25600
MM v[28:31],a[220:223],v[220:223],v[28:31]
EX v11,v11
MM v[24:27],a[204:207],v[220:223],v[140:143]
TR a[166:167],v232 offset:29696
CV v9,v10,v11
MM v[24:27],a[200:203],v[204:207],v[24:27]
EX v12,v12
MM v[24:27],a[196:199],v[188:191],v[24:27]
EX v13,v13
MM v[24:27],a[192:195],v[172:175],v[24:27]
TR a[168:169],v233 offset:17408
CV v10,v12,v13
MM v[32:35],a[224:227],v[160:163],v[128:131]
EX v14,v14
MM v[32:35],a[228:231],v[176:179],v[32:35]
TR a[170:171],v233 offset:21504
TR a[172:173],v233 offset:25600
MM v[32:35],a[232:235],v[192:195],v[32:35]
EX v15,v15
MM v[32:35],a[236:239],v[208:211],v[32:35]
TR a[174:175],v233 offset:29696
CV v11,v14,v15
MM v[36:39],a[252:255],v[208:211],v[128:131]
EX v16,v16
MM v[36:39],a[248:251],v[192:195],v[36:39]
EX v17,v17
MM v[36:39],a[244:247],v[176:179],v[36:39]
TR a[176:177],v232 offset:17920
CV v16,v16,v17
MM v[36:39],a[240:243],v[160:163],v[36:39]
EX v18,v18
MM v[44:47],a[240:243],v[164:167],v[132:135]
TR a[178:179],v232 offset:22016
TR a[180:181],v232 offset:26112
MM v[44:47],a[244:247],v[180:183],v[44:47]
EX v19,v19
MM v[44:47],a[248:251],v[196:199],v[44:47]
TR a[182:183],v232 offset:30208
CV v17,v18,v19
MM v[44:47],a[252:255],v[212:215],v[44:47]
EX v20,v20
MM v[40:43],a[236:239],v[212:215],v[132:135]
TR a[184:185],v233 offset:17920
TR a[186:187],v233 offset:22016
MM v[40:43],a[232:235],v[196:199],v[40:43]
EX v21,v21
MM v[40:43],a[228:231],v[180:183],v[40:43]
TR a[188:189],v233 offset:26112
CV v18,v20,v21
MM v[40:43],a[224:227],v[164:167],v[40:43]
EX v22,v22
MM v[48:51],a[224:227],v[168:171],v[136:139]
EX v23,v23
MM v[48:51],a[228:231],v[184:187],v[48:51]
TR a[190:191],v233 offset:30208
CV v19,v22,v23
MM v[48:51],a[232:235],v[200:203],v[48:51]
EX v24,v24
MM v[48:51],a[236:239],v[216:219],v[48:51]
EX v25,v25
MM v[52:55],a[252:255],v[216:219],v[136:139]
EX v26,v26
MM v[52:55],a[248:251],v[200:203],v[52:55]
EX v27,v27
MM v[52:55],a[244:247],v[184:187],v[52:55]
EX v28,v28
MM v[52:55],a[240:243],v[168:171],v[52:55]
EX v29,v29
MM v[60:63],a[240:243],v[172:175],v[140:143]
EX v30,v30
MM v[60:63],a[244:247],v[188:191],v[60:63]
EX v31,v31
MM v[60:63],a[248:251],v[204:207],v[60:63]
EX v32,v32
MM v[60:63],a[252:255],v[220:223],v[60:63]
EX v33,v33
MM v[56:59],a[236:239],v[220:223],v[140:143]
EX v34,v34
MM v[56:59],a[232:235],v[204:207],v[56:59]
EX v35,v35
MM v[56:59],a[228:231],v[188:191],v[56:59]
EX v36,v36
MM v[56:59],a[224:227],v[172:175],v[56:59]
s_waitcnt vmcnt(12) lgkmcnt(0)
s_barrier
MM a[0:3],a[128:131],v[64:67],a[0:3]
s_add_u32 m0, s65, 16384
CV v24,v24,v25
CV v25,v26,v27
MM a[0:3],a[132:135],v[68:71],a[0:3]
RD a[192:195],v224 offset:49152
CV v26,v28,v29
MM a[8:11],a[132:135],v[76:79],a[8:11]
RD a[196:199],v225 offset:49152
CV v27,v30,v31
MM a[8:11],a[128:131],v[72:75],a[8:11]
buffer_load_dwordx4 v228, s[8:11], s64 offen offset:0 lds
MM a[16:19],a[128:131],v[80:83],a[16:19]
RD a[200:203],v226 offset:49152
CV v4,v32,v33
MM a[16:19],a[132:135],v[84:87],a[16:19]
RD a[204:207],v227 offset:49152
CV v5,v34,v35
MM a[24:27],a[132:135],v[92:95],a[24:27]
buffer_load_dwordx4 v229, s[8:11], s64 offen offset:1024 lds
MM a[24:27],a[128:131],v[88:91],a[24:27]
RD a[208:211],v224 offset:53248
RD a[212:215],v225 offset:53248
MM a[28:31],a[136:139],v[88:91],a[28:31]
buffer_load_dwordx4 v230, s[8:11], s64 offen offset:2048 lds
MM a[28:31],a[140:143],v[92:95],a[28:31]
RD a[216:219],v226 offset:53248
RD a[220:223],v227 offset:53248
MM a[20:23],a[140:143],v[84:87],a[20:23]
EX v37,v37
MM a[20:23],a[136:139],v[80:83],a[20:23]
RD a[224:227],v224 offset:57344
CV v6,v36,v37
MM a[12:15],a[136:139],v[72:75],a[12:15]
EX v38,v38
MM a[12:15],a[140:143],v[76:79],a[12:15]
buffer_load_dwordx4 v231, s[8:11], s64 offen offset:3072 lds
MM a[4:7],a[140:143],v[68:71],a[4:7]
s_add_u32 m0, s65, 81920
EX v39,v39
MM a[4:7],a[136:139],v[64:67],a[4:7]
RD a[228:231],v225 offset:57344
CV v7,v38,v39
MM a[32:35],a[144:147],v[64:67],a[32:35]
EX v40,v40
MM a[32:35],a[148:151],v[68:71],a[32:35]
EX v41,v41
MM a[40:43],a[148:151],v[76:79],a[40:43]
RD a[232:235],v226 offset:57344
CV v12,v40,v41
MM a[40:43],a[144:147],v[72:75],a[40:43]
EX v42,v42
MM a[48:51],a[144:147],v[80:83],a[48:51]
buffer_load_dwordx4 v235, s[12:15], s81 offen offset:0 lds
MM a[48:51],a[148:151],v[84:87],a[48:51]
EX v43,v43
MM a[56:59],a[148:151],v[92:95],a[56:59]
RD a[236:239],v227 offset:57344
CV v13,v42,v43
MM a[56:59],a[144:147],v[88:91],a[56:59]
EX v44,v44
MM a[60:63],a[152:155],v[88:91],a[60:63]
EX v45,v45
MM a[60:63],a[156:159],v[92:95],a[60:63]
RD a[240:243],v224 offset:61440
CV v14,v44,v45
MM a[52:55],a[156:159],v[84:87],a[52:55]
EX v46,v46
MM a[52:55],a[152:155],v[80:83],a[52:55]
buffer_load_dwordx4 v235, s[12:15], s64 offen offset:1024 lds
MM a[44:47],a[152:155],v[72:75],a[44:47]
EX v47,v47
MM a[44:47],a[156:159],v[76:79],a[44:47]
RD a[244:247],v225 offset:61440
CV v15,v46,v47
MM a[36:39],a[156:159],v[68:71],a[36:39]
EX v48,v48
MM a[36:39],a[152:155],v[64:67],a[36:39]
EX v49,v49
MM a[64:67],a[160:163],v[64:67],a[64:67]
RD a[248:251],v226 offset:61440
CV v20,v48,v49
MM a[64:67],a[164:167],v[68:71],a[64:67]
EX v50,v50
MM a[72:75],a[164:167],v[76:79],a[72:75]
buffer_load_dwordx4 v235, s[12:15], s81 offen offset:2048 lds
MM a[72:75],a[160:163],v[72:75],a[72:75]
EX v51,v51
MM a[80:83],a[160:163],v[80:83],a[80:83]
RD a[252:255],v227 offset:61440
CV v21,v50,v51
MM a[80:83],a[164:167],v[84:87],a[80:83]
EX v52,v52
MM a[88:91],a[164:167],v[92:95],a[88:91]
EX v53,v53
MM a[88:91],a[160:163],v[88:91],a[88:91]
buffer_load_dwordx4 v235, s[12:15], s64 offen offset:3072 lds
MM a[92:95],a[168:171],v[88:91],a[92:95]
EX v54,v54
MM a[92:95],a[172:175],v[92:95],a[92:95]
EX v55,v55
MM a[84:87],a[172:175],v[84:87],a[84:87]
EX v56,v56
MM a[84:87],a[168:171],v[80:83],a[84:87]
EX v57,v57
MM a[76:79],a[168:171],v[72:75],a[76:79]
EX v58,v58
MM a[76:79],a[172:175],v[76:79],a[76:79]
EX v59,v59
MM a[68:71],a[172:175],v[68:71],a[68:71]
EX v60,v60
MM a[68:71],a[168:171],v[64:67],a[68:71]
EX v61,v61
MM a[96:99],a[176:179],v[64:67],a[96:99]
EX v62,v62
MM a[96:99],a[180:183],v[68:71],a[96:99]
EX v63,v63
MM a[104:107],a[180:183],v[76:79],a[104:107]
CV v22,v52,v53
CV v23,v54,v55
MM a[104:107],a[176:179],v[72:75],a[104:107]
CV v28,v56,v57
CV v29,v58,v59
MM a[112:115],a[176:179],v[80:83],a[112:115]
CV v30,v60,v61
CV v31,v62,v63
MM a[112:115],a[180:183],v[84:87],a[112:115]
MM a[120:123],a[180:183],v[92:95],a[120:123]
MM a[120:123],a[176:179],v[88:91],a[120:123]
MM a[124:127],a[184:187],v[88:91],a[124:127]
v_add_f32 v244, v144, v145
v_add_f32 v245, v148, v149
MM a[124:127],a[188:191],v[92:95],a[124:127]
v_add_f32 v246, v152, v153
v_add_f32 v247, v156, v157
MM a[116:119],a[188:191],v[84:87],a[116:119]
s_cmp_eq_u32 s70, s82
s_cbranch_scc1 LWW_%=
LLALWW_%=:
MM a[116:119],a[184:187],v[80:83],a[116:119]
MM a[108:111],a[184:187],v[72:75],a[108:111]
MM a[108:111],a[188:191],v[76:79],a[108:111]
MM a[100:103],a[188:191],v[68:71],a[100:103]
MM a[100:103],a[184:187],v[64:67],a[100:103]
v_smfmac_f32_16x16x64_bf16 v[144:147], v[236:239], v[0:7], v234
v_smfmac_f32_16x16x64_bf16 v[148:151], v[236:239], v[8:15], v234
v_smfmac_f32_16x16x64_bf16 v[152:155], v[236:239], v[16:23], v234
v_smfmac_f32_16x16x64_bf16 v[156:159], v[236:239], v[24:31], v234
s_waitcnt lgkmcnt(0)
MM v[64:67],a[192:195],v[160:163],v[128:131]
s_add_u32 s64, s64, 16384
TR a[128:129],v232 offset:32768
s_add_u32 s81, s64, 896
MM v[64:67],a[196:199],v[176:179],v[64:67]
TR a[130:131],v232 offset:36864
v_max3_f32 v249, v144, v145, v148
MM v[64:67],a[200:203],v[192:195],v[64:67]
TR a[132:133],v232 offset:40960
v_max3_f32 v249, v249, v149, v152
MM v[64:67],a[204:207],v[208:211],v[64:67]
TR a[134:135],v232 offset:45056
v_max3_f32 v249, v249, v153, v156
MM v[68:71],a[220:223],v[208:211],v[128:131]
TR a[136:137],v233 offset:32768
v_max_f32 v249, v249, v157
MM v[68:71],a[216:219],v[192:195],v[68:71]
TR a[138:139],v233 offset:36864
v_cmp_lt_f32 vcc, s71, v249
MM v[68:71],a[212:215],v[176:179],v[68:71]
TR a[140:141],v233 offset:40960
TR a[142:143],v233 offset:45056
MM v[68:71],a[208:211],v[160:163],v[68:71]
s_cbranch_vccnz LSITE6_%=
LBACK6_%=:
MM v[76:79],a[208:211],v[164:167],v[132:135]
EX v64,v64
MM v[76:79],a[212:215],v[180:183],v[76:79]
EX v65,v65
MM v[76:79],a[216:219],v[196:199],v[76:79]
TR a[144:145],v232 offset:33280
CV v64,v64,v65
MM v[76:79],a[220:223],v[212:215],v[76:79]
EX v66,v66
MM v[72:75],a[204:207],v[212:215],v[132:135]
EX v67,v67
MM v[72:75],a[200:203],v[196:199],v[72:75]
TR a[146:147],v232 offset:37376
CV v65,v66,v67
MM v[72:75],a[196:199],v[180:183],v[72:75]
EX v68,v68
MM v[72:75],a[192:195],v[164:167],v[72:75]
TR a[148:149],v232 offset:41472
TR a[150:151],v232 offset:45568
MM v[80:83],a[192:195],v[168:171],v[136:139]
EX v69,v69
MM v[80:83],a[196:199],v[184:187],v[80:83]
TR a[152:153],v233 offset:33280
CV v66,v68,v69
MM v[80:83],a[200:203],v[200:203],v[80:83]
EX v70,v70
MM v[80:83],a[204:207],v[216:219],v[80:83]
TR a[154:155],v233 offset:37376
TR a[156:157],v233 offset:41472
MM v[84:87],a[220:223],v[216:219],v[136:139]
EX v71,v71
MM v[84:87],a[216:219],v[200:203],v[84:87]
TR a[158:159],v233 offset:45568
CV v67,v70,v71
MM v[84:87],a[212:215],v[184:187],v[84:87]
EX v72,v72
MM v[84:87],a[208:211],v[168:171],v[84:87]
EX v73,v73
MM v[92:95],a[208:211],v[172:175],v[140:143]
TR a[160:161],v232 offset:33792
CV v72,v72,v73
MM v[92:95],a[212:215],v[188:191],v[92:95]
EX v74,v74
)ASM"
R"ASM(
MM v[92:95],a[216:219],v[204:207],v[92:95]
TR a[162:163],v232 offset:37888
TR a[164:165],v232 offset:41984
MM v[92:95],a[220:223],v[220:223],v[92:95]
EX v75,v75
MM v[88:91],a[204:207],v[220:223],v[140:143]
TR a[166:167],v232 offset:46080
CV v73,v74,v75
MM v[88:91],a[200:203],v[204:207],v[88:91]
EX v76,v76
MM v[88:91],a[196:199],v[188:191],v[88:91]
EX v77,v77
MM v[88:91],a[192:195],v[172:175],v[88:91]
TR a[168:169],v233 offset:33792
CV v74,v76,v77
MM v[96:99],a[224:227],v[160:163],v[128:131]
EX v78,v78
MM v[96:99],a[228:231],v[176:179],v[96:99]
TR a[170:171],v233 offset:37888
TR a[172:173],v233 offset:41984
MM v[96:99],a[232:235],v[192:195],v[96:99]
EX v79,v79
MM v[96:99],a[236:239],v[208:211],v[96:99]
TR a[174:175],v233 offset:46080
CV v75,v78,v79
MM v[100:103],a[252:255],v[208:211],v[128:131]
EX v80,v80
MM v[100:103],a[248:251],v[192:195],v[100:103]
EX v81,v81
MM v[100:103],a[244:247],v[176:179],v[100:103]
TR a[176:177],v232 offset:34304
CV v80,v80,v81
MM v[100:103],a[240:243],v[160:163],v[100:103]
EX v82,v82
MM v[108:111],a[240:243],v[164:167],v[132:135]
buffer_store_dword v243, v248, s[20:23], s67 offen offset:12 sc1
MM v[108:111],a[244:247],v[180:183],v[108:111]
s_add_u32 s67, s67, 1024
TR a[178:179],v232 offset:38400
TR a[180:181],v232 offset:42496
MM v[108:111],a[248:251],v[196:199],v[108:111]
EX v83,v83
MM v[108:111],a[252:255],v[212:215],v[108:111]
TR a[182:183],v232 offset:46592
CV v81,v82,v83
MM v[104:107],a[236:239],v[212:215],v[132:135]
EX v84,v84
MM v[104:107],a[232:235],v[196:199],v[104:107]
EX v85,v85
MM v[104:107],a[228:231],v[180:183],v[104:107]
TR a[184:185],v233 offset:34304
CV v82,v84,v85
MM v[104:107],a[224:227],v[164:167],v[104:107]
EX v86,v86
MM v[112:115],a[224:227],v[168:171],v[136:139]
TR a[186:187],v233 offset:38400
TR a[188:189],v233 offset:42496
MM v[112:115],a[228:231],v[184:187],v[112:115]
EX v87,v87
MM v[112:115],a[232:235],v[200:203],v[112:115]
TR a[190:191],v233 offset:46592
CV v83,v86,v87
MM v[112:115],a[236:239],v[216:219],v[112:115]
EX v88,v88
MM v[116:119],a[252:255],v[216:219],v[136:139]
EX v89,v89
MM v[116:119],a[248:251],v[200:203],v[116:119]
EX v90,v90
MM v[116:119],a[244:247],v[184:187],v[116:119]
EX v91,v91
MM v[116:119],a[240:243],v[168:171],v[116:119]
EX v92,v92
MM v[124:127],a[240:243],v[172:175],v[140:143]
EX v93,v93
MM v[124:127],a[244:247],v[188:191],v[124:127]
EX v94,v94
MM v[124:127],a[248:251],v[204:207],v[124:127]
EX v95,v95
MM v[124:127],a[252:255],v[220:223],v[124:127]
EX v96,v96
MM v[120:123],a[236:239],v[220:223],v[140:143]
EX v97,v97
MM v[120:123],a[232:235],v[204:207],v[120:123]
EX v98,v98
MM v[120:123],a[228:231],v[188:191],v[120:123]
EX v99,v99
MM v[120:123],a[224:227],v[172:175],v[120:123]
s_waitcnt vmcnt(12) lgkmcnt(0)
s_barrier
MM a[0:3],a[128:131],v[0:3],a[0:3]
s_add_u32 m0, s65, 32768
CV v88,v88,v89
CV v89,v90,v91
MM a[0:3],a[132:135],v[4:7],a[0:3]
RD a[192:195],v224 offset:0
CV v90,v92,v93
MM a[8:11],a[132:135],v[12:15],a[8:11]
RD a[196:199],v225 offset:0
CV v91,v94,v95
MM a[8:11],a[128:131],v[8:11],a[8:11]
buffer_load_dwordx4 v228, s[8:11], s64 offen offset:0 lds
MM a[16:19],a[128:131],v[16:19],a[16:19]
RD a[200:203],v226 offset:0
CV v68,v96,v97
MM a[16:19],a[132:135],v[20:23],a[16:19]
RD a[204:207],v227 offset:0
CV v69,v98,v99
MM a[24:27],a[132:135],v[28:31],a[24:27]
buffer_load_dwordx4 v229, s[8:11], s64 offen offset:1024 lds
MM a[24:27],a[128:131],v[24:27],a[24:27]
RD a[208:211],v224 offset:4096
RD a[212:215],v225 offset:4096
MM a[28:31],a[136:139],v[24:27],a[28:31]
buffer_load_dwordx4 v230, s[8:11], s64 offen offset:2048 lds
MM a[28:31],a[140:143],v[28:31],a[28:31]
EX v100,v100
MM a[20:23],a[140:143],v[20:23],a[20:23]
EX v101,v101
MM a[20:23],a[136:139],v[16:19],a[20:23]
RD a[216:219],v226 offset:4096
CV v70,v100,v101
MM a[12:15],a[136:139],v[8:11],a[12:15]
EX v102,v102
MM a[12:15],a[140:143],v[12:15],a[12:15]
RD a[220:223],v227 offset:4096
RD a[224:227],v224 offset:8192
MM a[4:7],a[140:143],v[4:7],a[4:7]
EX v103,v103
MM a[4:7],a[136:139],v[0:3],a[4:7]
RD a[228:231],v225 offset:8192
CV v71,v102,v103
MM a[32:35],a[144:147],v[0:3],a[32:35]
EX v104,v104
MM a[32:35],a[148:151],v[4:7],a[32:35]
buffer_load_dwordx4 v231, s[8:11], s64 offen offset:3072 lds
MM a[40:43],a[148:151],v[12:15],a[40:43]
s_add_u32 m0, s65, 98304
EX v105,v105
MM a[40:43],a[144:147],v[8:11],a[40:43]
RD a[232:235],v226 offset:8192
CV v76,v104,v105
MM a[48:51],a[144:147],v[16:19],a[48:51]
EX v106,v106
MM a[48:51],a[148:151],v[20:23],a[48:51]
EX v107,v107
MM a[56:59],a[148:151],v[28:31],a[56:59]
RD a[236:239],v227 offset:8192
CV v77,v106,v107
MM a[56:59],a[144:147],v[24:27],a[56:59]
EX v108,v108
MM a[60:63],a[152:155],v[24:27],a[60:63]
buffer_load_dwordx4 v235, s[12:15], s81 offen offset:0 lds
MM a[60:63],a[156:159],v[28:31],a[60:63]
EX v109,v109
MM a[52:55],a[156:159],v[20:23],a[52:55]
RD a[240:243],v224 offset:12288
CV v78,v108,v109
MM a[52:55],a[152:155],v[16:19],a[52:55]
EX v110,v110
MM a[44:47],a[152:155],v[8:11],a[44:47]
EX v111,v111
MM a[44:47],a[156:159],v[12:15],a[44:47]
RD a[244:247],v225 offset:12288
CV v79,v110,v111
MM a[36:39],a[156:159],v[4:7],a[36:39]
buffer_load_dwordx4 v235, s[12:15], s64 offen offset:1024 lds
MM a[36:39],a[152:155],v[0:3],a[36:39]
EX v112,v112
MM a[64:67],a[160:163],v[0:3],a[64:67]
EX v113,v113
MM a[64:67],a[164:167],v[4:7],a[64:67]
RD a[248:251],v226 offset:12288
CV v84,v112,v113
MM a[72:75],a[164:167],v[12:15],a[72:75]
EX v114,v114
MM a[72:75],a[160:163],v[8:11],a[72:75]
EX v115,v115
MM a[80:83],a[160:163],v[16:19],a[80:83]
RD a[252:255],v227 offset:12288
CV v85,v114,v115
MM a[80:83],a[164:167],v[20:23],a[80:83]
buffer_load_dwordx4 v235, s[12:15], s81 offen offset:2048 lds
MM a[88:91],a[164:167],v[28:31],a[88:91]
EX v116,v116
MM a[88:91],a[160:163],v[24:27],a[88:91]
EX v117,v117
MM a[92:95],a[168:171],v[24:27],a[92:95]
EX v118,v118
MM a[92:95],a[172:175],v[28:31],a[92:95]
buffer_load_dwordx4 v235, s[12:15], s64 offen offset:3072 lds
MM a[84:87],a[172:175],v[20:23],a[84:87]
EX v119,v119
MM a[84:87],a[168:171],v[16:19],a[84:87]
EX v120,v120
MM a[76:79],a[168:171],v[8:11],a[76:79]
LPG8_%=:
s_cbranch_execz LPG12_%=
EX v121,v121
MM a[76:79],a[172:175],v[12:15],a[76:79]
EX v122,v122
MM a[68:71],a[172:175],v[4:7],a[68:71]
EX v123,v123
MM a[68:71],a[168:171],v[0:3],a[68:71]
EX v124,v124
MM a[96:99],a[176:179],v[0:3],a[96:99]
EX v125,v125
MM a[96:99],a[180:183],v[4:7],a[96:99]
EX v126,v126
MM a[104:107],a[180:183],v[12:15],a[104:107]
EX v127,v127
MM a[104:107],a[176:179],v[8:11],a[104:107]
CV v86,v116,v117
CV v87,v118,v119
MM a[112:115],a[176:179],v[16:19],a[112:115]
CV v92,v120,v121
CV v93,v122,v123
MM a[112:115],a[180:183],v[20:23],a[112:115]
CV v94,v124,v125
CV v95,v126,v127
MM a[120:123],a[180:183],v[28:31],a[120:123]
MM a[120:123],a[176:179],v[24:27],a[120:123]
MM a[124:127],a[184:187],v[24:27],a[124:127]
v_add_f32 v244, v144, v145
v_add_f32 v245, v148, v149
MM a[124:127],a[188:191],v[28:31],a[124:127]
v_add_f32 v246, v152, v153
v_add_f32 v247, v156, v157
MM a[116:119],a[188:191],v[20:23],a[116:119]
MM a[116:119],a[184:187],v[16:19],a[116:119]
MM a[108:111],a[184:187],v[8:11],a[108:111]
MM a[108:111],a[188:191],v[12:15],a[108:111]
MM a[100:103],a[188:191],v[4:7],a[100:103]
MM a[100:103],a[184:187],v[0:3],a[100:103]
v_smfmac_f32_16x16x64_bf16 v[144:147], v[236:239], v[64:71], v234
v_smfmac_f32_16x16x64_bf16 v[148:151], v[236:239], v[72:79], v234
v_smfmac_f32_16x16x64_bf16 v[152:155], v[236:239], v[80:87], v234
v_smfmac_f32_16x16x64_bf16 v[156:159], v[236:239], v[88:95], v234
s_waitcnt lgkmcnt(0)
MM v[0:3],a[192:195],v[160:163],v[128:131]
s_add_u32 s64, s64, 16384
TR a[128:129],v232 offset:49152
s_add_u32 s81, s64, 896
MM v[0:3],a[196:199],v[176:179],v[0:3]
TR a[130:131],v232 offset:53248
v_max3_f32 v249, v144, v145, v148
MM v[0:3],a[200:203],v[192:195],v[0:3]
TR a[132:133],v232 offset:57344
v_max3_f32 v249, v249, v149, v152
MM v[0:3],a[204:207],v[208:211],v[0:3]
TR a[134:135],v232 offset:61440
v_max3_f32 v249, v249, v153, v156
MM v[4:7],a[220:223],v[208:211],v[128:131]
TR a[136:137],v233 offset:49152
v_max_f32 v249, v249, v157
MM v[4:7],a[216:219],v[192:195],v[4:7]
TR a[138:139],v233 offset:53248
v_cmp_lt_f32 vcc, s71, v249
MM v[4:7],a[212:215],v[176:179],v[4:7]
TR a[140:141],v233 offset:57344
TR a[142:143],v233 offset:61440
MM v[4:7],a[208:211],v[160:163],v[4:7]
s_cbranch_vccnz LSITE7_%=
LBACK7_%=:
MM v[12:15],a[208:211],v[164:167],v[132:135]
EX v0,v0
MM v[12:15],a[212:215],v[180:183],v[12:15]
EX v1,v1
MM v[12:15],a[216:219],v[196:199],v[12:15]
TR a[144:145],v232 offset:49664
CV v0,v0,v1
MM v[12:15],a[220:223],v[212:215],v[12:15]
EX v2,v2
MM v[8:11],a[204:207],v[212:215],v[132:135]
EX v3,v3
MM v[8:11],a[200:203],v[196:199],v[8:11]
TR a[146:147],v232 offset:53760
CV v1,v2,v3
MM v[8:11],a[196:199],v[180:183],v[8:11]
EX v4,v4
MM v[8:11],a[192:195],v[164:167],v[8:11]
TR a[148:149],v232 offset:57856
TR a[150:151],v232 offset:61952
MM v[16:19],a[192:195],v[168:171],v[136:139]
EX v5,v5
MM v[16:19],a[196:199],v[184:187],v[16:19]
TR a[152:153],v233 offset:49664
CV v2,v4,v5
MM v[16:19],a[200:203],v[200:203],v[16:19]
EX v6,v6
MM v[16:19],a[204:207],v[216:219],v[16:19]
TR a[154:155],v233 offset:53760
TR a[156:157],v233 offset:57856
MM v[20:23],a[220:223],v[216:219],v[136:139]
EX v7,v7
MM v[20:23],a[216:219],v[200:203],v[20:23]
TR a[158:159],v233 offset:61952
CV v3,v6,v7
MM v[20:23],a[212:215],v[184:187],v[20:23]
EX v8,v8
MM v[20:23],a[208:211],v[168:171],v[20:23]
EX v9,v9
MM v[28:31],a[208:211],v[172:175],v[140:143]
TR a[160:161],v232 offset:50176
CV v8,v8,v9
MM v[28:31],a[212:215],v[188:191],v[28:31]
EX v10,v10
MM v[28:31],a[216:219],v[204:207],v[28:31]
TR a[162:163],v232 offset:54272
TR a[164:165],v232 offset:58368
MM v[28:31],a[220:223],v[220:223],v[28:31]
EX v11,v11
MM v[24:27],a[204:207],v[220:223],v[140:143]
TR a[166:167],v232 offset:62464
CV v9,v10,v11
MM v[24:27],a[200:203],v[204:207],v[24:27]
EX v12,v12
MM v[24:27],a[196:199],v[188:191],v[24:27]
EX v13,v13
MM v[24:27],a[192:195],v[172:175],v[24:27]
TR a[168:169],v233 offset:50176
CV v10,v12,v13
MM v[32:35],a[224:227],v[160:163],v[128:131]
EX v14,v14
MM v[32:35],a[228:231],v[176:179],v[32:35]
TR a[170:171],v233 offset:54272
TR a[172:173],v233 offset:58368
MM v[32:35],a[232:235],v[192:195],v[32:35]
EX v15,v15
MM v[32:35],a[236:239],v[208:211],v[32:35]
TR a[174:175],v233 offset:62464
CV v11,v14,v15
MM v[36:39],a[252:255],v[208:211],v[128:131]
EX v16,v16
MM v[36:39],a[248:251],v[192:195],v[36:39]
EX v17,v17
MM v[36:39],a[244:247],v[176:179],v[36:39]
TR a[176:177],v232 offset:50688
CV v16,v16,v17
MM v[36:39],a[240:243],v[160:163],v[36:39]
EX v18,v18
MM v[44:47],a[240:243],v[164:167],v[132:135]
TR a[178:179],v232 offset:54784
TR a[180:181],v232 offset:58880
MM v[44:47],a[244:247],v[180:183],v[44:47]
EX v19,v19
MM v[44:47],a[248:251],v[196:199],v[44:47]
TR a[182:183],v232 offset:62976
CV v17,v18,v19
MM v[44:47],a[252:255],v[212:215],v[44:47]
EX v20,v20
MM v[40:43],a[236:239],v[212:215],v[132:135]
TR a[184:185],v233 offset:50688
TR a[186:187],v233 offset:54784
MM v[40:43],a[232:235],v[196:199],v[40:43]
EX v21,v21
MM v[40:43],a[228:231],v[180:183],v[40:43]
TR a[188:189],v233 offset:58880
CV v18,v20,v21
MM v[40:43],a[224:227],v[164:167],v[40:43]
EX v22,v22
MM v[48:51],a[224:227],v[168:171],v[136:139]
EX v23,v23
MM v[48:51],a[228:231],v[184:187],v[48:51]
TR a[190:191],v233 offset:62976
CV v19,v22,v23
MM v[48:51],a[232:235],v[200:203],v[48:51]
EX v24,v24
MM v[48:51],a[236:239],v[216:219],v[48:51]
EX v25,v25
MM v[52:55],a[252:255],v[216:219],v[136:139]
EX v26,v26
MM v[52:55],a[248:251],v[200:203],v[52:55]
EX v27,v27
MM v[52:55],a[244:247],v[184:187],v[52:55]
EX v28,v28
MM v[52:55],a[240:243],v[168:171],v[52:55]
EX v29,v29
MM v[60:63],a[240:243],v[172:175],v[140:143]
EX v30,v30
MM v[60:63],a[244:247],v[188:191],v[60:63]
EX v31,v31
MM v[60:63],a[248:251],v[204:207],v[60:63]
EX v32,v32
MM v[60:63],a[252:255],v[220:223],v[60:63]
EX v33,v33
MM v[56:59],a[236:239],v[220:223],v[140:143]
EX v34,v34
MM v[56:59],a[232:235],v[204:207],v[56:59]
EX v35,v35
MM v[56:59],a[228:231],v[188:191],v[56:59]
)ASM"
R"ASM(
EX v36,v36
MM v[56:59],a[224:227],v[172:175],v[56:59]
s_waitcnt vmcnt(12) lgkmcnt(0)
s_barrier
MM a[0:3],a[128:131],v[64:67],a[0:3]
s_add_u32 m0, s65, 49152
CV v24,v24,v25
CV v25,v26,v27
MM a[0:3],a[132:135],v[68:71],a[0:3]
RD a[192:195],v224 offset:16384
CV v26,v28,v29
MM a[8:11],a[132:135],v[76:79],a[8:11]
RD a[196:199],v225 offset:16384
CV v27,v30,v31
MM a[8:11],a[128:131],v[72:75],a[8:11]
buffer_load_dwordx4 v228, s[8:11], s64 offen offset:0 lds
MM a[16:19],a[128:131],v[80:83],a[16:19]
RD a[200:203],v226 offset:16384
CV v4,v32,v33
MM a[16:19],a[132:135],v[84:87],a[16:19]
RD a[204:207],v227 offset:16384
CV v5,v34,v35
MM a[24:27],a[132:135],v[92:95],a[24:27]
buffer_load_dwordx4 v229, s[8:11], s64 offen offset:1024 lds
MM a[24:27],a[128:131],v[88:91],a[24:27]
RD a[208:211],v224 offset:20480
RD a[212:215],v225 offset:20480
MM a[28:31],a[136:139],v[88:91],a[28:31]
buffer_load_dwordx4 v230, s[8:11], s64 offen offset:2048 lds
MM a[28:31],a[140:143],v[92:95],a[28:31]
RD a[216:219],v226 offset:20480
RD a[220:223],v227 offset:20480
MM a[20:23],a[140:143],v[84:87],a[20:23]
EX v37,v37
MM a[20:23],a[136:139],v[80:83],a[20:23]
RD a[224:227],v224 offset:24576
CV v6,v36,v37
MM a[12:15],a[136:139],v[72:75],a[12:15]
EX v38,v38
MM a[12:15],a[140:143],v[76:79],a[12:15]
buffer_load_dwordx4 v231, s[8:11], s64 offen offset:3072 lds
MM a[4:7],a[140:143],v[68:71],a[4:7]
s_add_u32 m0, s65, 114688
EX v39,v39
MM a[4:7],a[136:139],v[64:67],a[4:7]
RD a[228:231],v225 offset:24576
CV v7,v38,v39
MM a[32:35],a[144:147],v[64:67],a[32:35]
EX v40,v40
MM a[32:35],a[148:151],v[68:71],a[32:35]
EX v41,v41
MM a[40:43],a[148:151],v[76:79],a[40:43]
RD a[232:235],v226 offset:24576
CV v12,v40,v41
MM a[40:43],a[144:147],v[72:75],a[40:43]
EX v42,v42
MM a[48:51],a[144:147],v[80:83],a[48:51]
buffer_load_dwordx4 v235, s[12:15], s81 offen offset:0 lds
MM a[48:51],a[148:151],v[84:87],a[48:51]
EX v43,v43
MM a[56:59],a[148:151],v[92:95],a[56:59]
RD a[236:239],v227 offset:24576
CV v13,v42,v43
MM a[56:59],a[144:147],v[88:91],a[56:59]
EX v44,v44
MM a[60:63],a[152:155],v[88:91],a[60:63]
EX v45,v45
MM a[60:63],a[156:159],v[92:95],a[60:63]
RD a[240:243],v224 offset:28672
CV v14,v44,v45
MM a[52:55],a[156:159],v[84:87],a[52:55]
EX v46,v46
MM a[52:55],a[152:155],v[80:83],a[52:55]
buffer_load_dwordx4 v235, s[12:15], s64 offen offset:1024 lds
MM a[44:47],a[152:155],v[72:75],a[44:47]
EX v47,v47
MM a[44:47],a[156:159],v[76:79],a[44:47]
RD a[244:247],v225 offset:28672
CV v15,v46,v47
MM a[36:39],a[156:159],v[68:71],a[36:39]
EX v48,v48
MM a[36:39],a[152:155],v[64:67],a[36:39]
EX v49,v49
MM a[64:67],a[160:163],v[64:67],a[64:67]
RD a[248:251],v226 offset:28672
CV v20,v48,v49
MM a[64:67],a[164:167],v[68:71],a[64:67]
EX v50,v50
MM a[72:75],a[164:167],v[76:79],a[72:75]
buffer_load_dwordx4 v235, s[12:15], s81 offen offset:2048 lds
MM a[72:75],a[160:163],v[72:75],a[72:75]
EX v51,v51
MM a[80:83],a[160:163],v[80:83],a[80:83]
RD a[252:255],v227 offset:28672
CV v21,v50,v51
MM a[80:83],a[164:167],v[84:87],a[80:83]
EX v52,v52
MM a[88:91],a[164:167],v[92:95],a[88:91]
EX v53,v53
MM a[88:91],a[160:163],v[88:91],a[88:91]
buffer_load_dwordx4 v235, s[12:15], s64 offen offset:3072 lds
MM a[92:95],a[168:171],v[88:91],a[92:95]
EX v54,v54
MM a[92:95],a[172:175],v[92:95],a[92:95]
EX v55,v55
MM a[84:87],a[172:175],v[84:87],a[84:87]
EX v56,v56
MM a[84:87],a[168:171],v[80:83],a[84:87]
EX v57,v57
MM a[76:79],a[168:171],v[72:75],a[76:79]
EX v58,v58
MM a[76:79],a[172:175],v[76:79],a[76:79]
EX v59,v59
MM a[68:71],a[172:175],v[68:71],a[68:71]
EX v60,v60
MM a[68:71],a[168:171],v[64:67],a[68:71]
EX v61,v61
MM a[96:99],a[176:179],v[64:67],a[96:99]
EX v62,v62
MM a[96:99],a[180:183],v[68:71],a[96:99]
EX v63,v63
MM a[104:107],a[180:183],v[76:79],a[104:107]
CV v22,v52,v53
CV v23,v54,v55
MM a[104:107],a[176:179],v[72:75],a[104:107]
CV v28,v56,v57
CV v29,v58,v59
MM a[112:115],a[176:179],v[80:83],a[112:115]
CV v30,v60,v61
CV v31,v62,v63
MM a[112:115],a[180:183],v[84:87],a[112:115]
MM a[120:123],a[180:183],v[92:95],a[120:123]
MM a[120:123],a[176:179],v[88:91],a[120:123]
MM a[124:127],a[184:187],v[88:91],a[124:127]
v_add_f32 v244, v144, v145
v_add_f32 v245, v148, v149
MM a[124:127],a[188:191],v[92:95],a[124:127]
v_add_f32 v246, v152, v153
v_add_f32 v247, v156, v157
MM a[116:119],a[188:191],v[84:87],a[116:119]
s_cmp_eq_u32 s70, 2
s_cbranch_scc1 LIP_%=
LLALIP_%=:
MM a[116:119],a[184:187],v[80:83],a[116:119]
MM a[108:111],a[184:187],v[72:75],a[108:111]
MM a[108:111],a[188:191],v[76:79],a[108:111]
MM a[100:103],a[188:191],v[68:71],a[100:103]
MM a[100:103],a[184:187],v[64:67],a[100:103]
v_smfmac_f32_16x16x64_bf16 v[144:147], v[236:239], v[0:7], v234
v_smfmac_f32_16x16x64_bf16 v[148:151], v[236:239], v[8:15], v234
v_smfmac_f32_16x16x64_bf16 v[152:155], v[236:239], v[16:23], v234
v_smfmac_f32_16x16x64_bf16 v[156:159], v[236:239], v[24:31], v234
s_waitcnt lgkmcnt(0)
MM v[64:67],a[192:195],v[160:163],v[128:131]
s_add_u32 s64, s64, 16384
TR a[128:129],v232 offset:0
s_and_b32 s64, s64, 524287
MM v[64:67],a[196:199],v[176:179],v[64:67]
s_add_u32 s81, s64, 896
TR a[130:131],v232 offset:4096
v_max3_f32 v249, v144, v145, v148
MM v[64:67],a[200:203],v[192:195],v[64:67]
TR a[132:133],v232 offset:8192
v_max3_f32 v249, v249, v149, v152
MM v[64:67],a[204:207],v[208:211],v[64:67]
TR a[134:135],v232 offset:12288
v_max3_f32 v249, v249, v153, v156
MM v[68:71],a[220:223],v[208:211],v[128:131]
TR a[136:137],v233 offset:0
v_max_f32 v249, v249, v157
MM v[68:71],a[216:219],v[192:195],v[68:71]
TR a[138:139],v233 offset:4096
v_cmp_lt_f32 vcc, s71, v249
MM v[68:71],a[212:215],v[176:179],v[68:71]
TR a[140:141],v233 offset:8192
TR a[142:143],v233 offset:12288
MM v[68:71],a[208:211],v[160:163],v[68:71]
s_cbranch_vccnz LSITE8_%=
LBACK8_%=:
MM v[76:79],a[208:211],v[164:167],v[132:135]
EX v64,v64
MM v[76:79],a[212:215],v[180:183],v[76:79]
EX v65,v65
MM v[76:79],a[216:219],v[196:199],v[76:79]
TR a[144:145],v232 offset:512
CV v64,v64,v65
MM v[76:79],a[220:223],v[212:215],v[76:79]
EX v66,v66
MM v[72:75],a[204:207],v[212:215],v[132:135]
EX v67,v67
MM v[72:75],a[200:203],v[196:199],v[72:75]
TR a[146:147],v232 offset:4608
CV v65,v66,v67
MM v[72:75],a[196:199],v[180:183],v[72:75]
EX v68,v68
MM v[72:75],a[192:195],v[164:167],v[72:75]
TR a[148:149],v232 offset:8704
TR a[150:151],v232 offset:12800
MM v[80:83],a[192:195],v[168:171],v[136:139]
EX v69,v69
MM v[80:83],a[196:199],v[184:187],v[80:83]
TR a[152:153],v233 offset:512
CV v66,v68,v69
MM v[80:83],a[200:203],v[200:203],v[80:83]
EX v70,v70
MM v[80:83],a[204:207],v[216:219],v[80:83]
TR a[154:155],v233 offset:4608
TR a[156:157],v233 offset:8704
MM v[84:87],a[220:223],v[216:219],v[136:139]
EX v71,v71
MM v[84:87],a[216:219],v[200:203],v[84:87]
TR a[158:159],v233 offset:12800
CV v67,v70,v71
MM v[84:87],a[212:215],v[184:187],v[84:87]
EX v72,v72
MM v[84:87],a[208:211],v[168:171],v[84:87]
EX v73,v73
MM v[92:95],a[208:211],v[172:175],v[140:143]
TR a[160:161],v232 offset:1024
CV v72,v72,v73
MM v[92:95],a[212:215],v[188:191],v[92:95]
EX v74,v74
MM v[92:95],a[216:219],v[204:207],v[92:95]
TR a[162:163],v232 offset:5120
TR a[164:165],v232 offset:9216
MM v[92:95],a[220:223],v[220:223],v[92:95]
EX v75,v75
MM v[88:91],a[204:207],v[220:223],v[140:143]
TR a[166:167],v232 offset:13312
CV v73,v74,v75
MM v[88:91],a[200:203],v[204:207],v[88:91]
EX v76,v76
MM v[88:91],a[196:199],v[188:191],v[88:91]
EX v77,v77
MM v[88:91],a[192:195],v[172:175],v[88:91]
TR a[168:169],v233 offset:1024
CV v74,v76,v77
MM v[96:99],a[224:227],v[160:163],v[128:131]
EX v78,v78
MM v[96:99],a[228:231],v[176:179],v[96:99]
TR a[170:171],v233 offset:5120
TR a[172:173],v233 offset:9216
MM v[96:99],a[232:235],v[192:195],v[96:99]
EX v79,v79
MM v[96:99],a[236:239],v[208:211],v[96:99]
TR a[174:175],v233 offset:13312
CV v75,v78,v79
MM v[100:103],a[252:255],v[208:211],v[128:131]
EX v80,v80
MM v[100:103],a[248:251],v[192:195],v[100:103]
EX v81,v81
MM v[100:103],a[244:247],v[176:179],v[100:103]
TR a[176:177],v232 offset:1536
CV v80,v80,v81
MM v[100:103],a[240:243],v[160:163],v[100:103]
EX v82,v82
MM v[108:111],a[240:243],v[164:167],v[132:135]
TR a[178:179],v232 offset:5632
TR a[180:181],v232 offset:9728
MM v[108:111],a[244:247],v[180:183],v[108:111]
EX v83,v83
MM v[108:111],a[248:251],v[196:199],v[108:111]
TR a[182:183],v232 offset:13824
CV v81,v82,v83
MM v[108:111],a[252:255],v[212:215],v[108:111]
EX v84,v84
MM v[104:107],a[236:239],v[212:215],v[132:135]
TR a[184:185],v233 offset:1536
TR a[186:187],v233 offset:5632
MM v[104:107],a[232:235],v[196:199],v[104:107]
EX v85,v85
MM v[104:107],a[228:231],v[180:183],v[104:107]
TR a[188:189],v233 offset:9728
CV v82,v84,v85
MM v[104:107],a[224:227],v[164:167],v[104:107]
EX v86,v86
MM v[112:115],a[224:227],v[168:171],v[136:139]
EX v87,v87
MM v[112:115],a[228:231],v[184:187],v[112:115]
TR a[190:191],v233 offset:13824
CV v83,v86,v87
MM v[112:115],a[232:235],v[200:203],v[112:115]
EX v88,v88
MM v[112:115],a[236:239],v[216:219],v[112:115]
EX v89,v89
MM v[116:119],a[252:255],v[216:219],v[136:139]
EX v90,v90
MM v[116:119],a[248:251],v[200:203],v[116:119]
EX v91,v91
MM v[116:119],a[244:247],v[184:187],v[116:119]
EX v92,v92
MM v[116:119],a[240:243],v[168:171],v[116:119]
EX v93,v93
MM v[124:127],a[240:243],v[172:175],v[140:143]
EX v94,v94
MM v[124:127],a[244:247],v[188:191],v[124:127]
EX v95,v95
MM v[124:127],a[248:251],v[204:207],v[124:127]
EX v96,v96
MM v[124:127],a[252:255],v[220:223],v[124:127]
EX v97,v97
MM v[120:123],a[236:239],v[220:223],v[140:143]
EX v98,v98
MM v[120:123],a[232:235],v[204:207],v[120:123]
EX v99,v99
MM v[120:123],a[228:231],v[188:191],v[120:123]
s_cmp_eq_u32 s70, 1
s_cbranch_scc1 LTP_%=
LLALTP_%=:
MM v[120:123],a[224:227],v[172:175],v[120:123]
s_waitcnt vmcnt(12) lgkmcnt(0)
s_barrier
MM a[0:3],a[128:131],v[0:3],a[0:3]
s_add_u32 m0, s65, 0
CV v88,v88,v89
CV v89,v90,v91
MM a[0:3],a[132:135],v[4:7],a[0:3]
RD a[192:195],v224 offset:32768
CV v90,v92,v93
MM a[8:11],a[132:135],v[12:15],a[8:11]
RD a[196:199],v225 offset:32768
CV v91,v94,v95
MM a[8:11],a[128:131],v[8:11],a[8:11]
buffer_load_dwordx4 v228, s[8:11], s64 offen offset:0 lds
MM a[16:19],a[128:131],v[16:19],a[16:19]
RD a[200:203],v226 offset:32768
CV v68,v96,v97
MM a[16:19],a[132:135],v[20:23],a[16:19]
RD a[204:207],v227 offset:32768
CV v69,v98,v99
MM a[24:27],a[132:135],v[28:31],a[24:27]
buffer_load_dwordx4 v229, s[8:11], s64 offen offset:1024 lds
MM a[24:27],a[128:131],v[24:27],a[24:27]
RD a[208:211],v224 offset:36864
RD a[212:215],v225 offset:36864
MM a[28:31],a[136:139],v[24:27],a[28:31]
buffer_load_dwordx4 v230, s[8:11], s64 offen offset:2048 lds
MM a[28:31],a[140:143],v[28:31],a[28:31]
EX v100,v100
MM a[20:23],a[140:143],v[20:23],a[20:23]
EX v101,v101
MM a[20:23],a[136:139],v[16:19],a[20:23]
RD a[216:219],v226 offset:36864
CV v70,v100,v101
MM a[12:15],a[136:139],v[8:11],a[12:15]
EX v102,v102
MM a[12:15],a[140:143],v[12:15],a[12:15]
RD a[220:223],v227 offset:36864
RD a[224:227],v224 offset:40960
MM a[4:7],a[140:143],v[4:7],a[4:7]
EX v103,v103
MM a[4:7],a[136:139],v[0:3],a[4:7]
RD a[228:231],v225 offset:40960
CV v71,v102,v103
MM a[32:35],a[144:147],v[0:3],a[32:35]
EX v104,v104
MM a[32:35],a[148:151],v[4:7],a[32:35]
buffer_load_dwordx4 v231, s[8:11], s64 offen offset:3072 lds
MM a[40:43],a[148:151],v[12:15],a[40:43]
s_add_u32 m0, s65, 65536
EX v105,v105
MM a[40:43],a[144:147],v[8:11],a[40:43]
RD a[232:235],v226 offset:40960
CV v76,v104,v105
MM a[48:51],a[144:147],v[16:19],a[48:51]
EX v106,v106
MM a[48:51],a[148:151],v[20:23],a[48:51]
EX v107,v107
MM a[56:59],a[148:151],v[28:31],a[56:59]
RD a[236:239],v227 offset:40960
CV v77,v106,v107
MM a[56:59],a[144:147],v[24:27],a[56:59]
EX v108,v108
MM a[60:63],a[152:155],v[24:27],a[60:63]
buffer_load_dwordx4 v235, s[12:15], s81 offen offset:0 lds
MM a[60:63],a[156:159],v[28:31],a[60:63]
EX v109,v109
MM a[52:55],a[156:159],v[20:23],a[52:55]
RD a[240:243],v224 offset:45056
CV v78,v108,v109
MM a[52:55],a[152:155],v[16:19],a[52:55]
EX v110,v110
MM a[44:47],a[152:155],v[8:11],a[44:47]
EX v111,v111
MM a[44:47],a[156:159],v[12:15],a[44:47]
RD a[244:247],v225 offset:45056
CV v79,v110,v111
MM a[36:39],a[156:159],v[4:7],a[36:39]
)ASM"
R"ASM(
EX v112,v112
MM a[36:39],a[152:155],v[0:3],a[36:39]
buffer_load_dwordx4 v235, s[12:15], s64 offen offset:1024 lds
MM a[64:67],a[160:163],v[0:3],a[64:67]
EX v113,v113
MM a[64:67],a[164:167],v[4:7],a[64:67]
RD a[248:251],v226 offset:45056
s_nop 0
LPG9_%=:
s_cbranch_execz LTR0_%=
CV v84,v112,v113
MM a[72:75],a[164:167],v[12:15],a[72:75]
EX v114,v114
MM a[72:75],a[160:163],v[8:11],a[72:75]
EX v115,v115
MM a[80:83],a[160:163],v[16:19],a[80:83]
RD a[252:255],v227 offset:45056
CV v85,v114,v115
MM a[80:83],a[164:167],v[20:23],a[80:83]
buffer_load_dwordx4 v235, s[12:15], s81 offen offset:2048 lds
MM a[88:91],a[164:167],v[28:31],a[88:91]
EX v116,v116
MM a[88:91],a[160:163],v[24:27],a[88:91]
EX v117,v117
MM a[92:95],a[168:171],v[24:27],a[92:95]
EX v118,v118
MM a[92:95],a[172:175],v[28:31],a[92:95]
buffer_load_dwordx4 v235, s[12:15], s64 offen offset:3072 lds
MM a[84:87],a[172:175],v[20:23],a[84:87]
EX v119,v119
MM a[84:87],a[168:171],v[16:19],a[84:87]
EX v120,v120
MM a[76:79],a[168:171],v[8:11],a[76:79]
EX v121,v121
MM a[76:79],a[172:175],v[12:15],a[76:79]
EX v122,v122
MM a[68:71],a[172:175],v[4:7],a[68:71]
EX v123,v123
MM a[68:71],a[168:171],v[0:3],a[68:71]
EX v124,v124
MM a[96:99],a[176:179],v[0:3],a[96:99]
EX v125,v125
MM a[96:99],a[180:183],v[4:7],a[96:99]
EX v126,v126
MM a[104:107],a[180:183],v[12:15],a[104:107]
EX v127,v127
MM a[104:107],a[176:179],v[8:11],a[104:107]
CV v86,v116,v117
CV v87,v118,v119
MM a[112:115],a[176:179],v[16:19],a[112:115]
CV v92,v120,v121
CV v93,v122,v123
MM a[112:115],a[180:183],v[20:23],a[112:115]
CV v94,v124,v125
CV v95,v126,v127
MM a[120:123],a[180:183],v[28:31],a[120:123]
MM a[120:123],a[176:179],v[24:27],a[120:123]
MM a[124:127],a[184:187],v[24:27],a[124:127]
v_add_f32 v244, v144, v145
v_add_f32 v245, v148, v149
MM a[124:127],a[188:191],v[28:31],a[124:127]
v_add_f32 v246, v152, v153
v_add_f32 v247, v156, v157
MM a[116:119],a[188:191],v[20:23],a[116:119]
s_cmp_eq_u32 s70, s82
s_cbranch_scc1 LWV_%=
LLALWV_%=:
MM a[116:119],a[184:187],v[16:19],a[116:119]
MM a[108:111],a[184:187],v[8:11],a[108:111]
MM a[108:111],a[188:191],v[12:15],a[108:111]
MM a[100:103],a[188:191],v[4:7],a[100:103]
MM a[100:103],a[184:187],v[0:3],a[100:103]
v_smfmac_f32_16x16x64_bf16 v[144:147], v[236:239], v[64:71], v234
v_smfmac_f32_16x16x64_bf16 v[148:151], v[236:239], v[72:79], v234
v_smfmac_f32_16x16x64_bf16 v[152:155], v[236:239], v[80:87], v234
v_smfmac_f32_16x16x64_bf16 v[156:159], v[236:239], v[88:95], v234
s_waitcnt lgkmcnt(0)
s_sub_u32 s70, s70, 1
s_cmp_lg_u32 s70, 0
s_cbranch_scc1 LLAP_%=
s_branch LLAJ_%=
LWW_%=:
v_lshlrev_b32 v250, 3, v248
v_min_u32 v250, 4992, v250
s_sub_u32 s91, 5242880, s26
s_sub_u32 s60, s24, s91
s_subb_u32 s61, s25, 0
s_mov_b32 s62, 5242880
s_mov_b32 s63, 131072
s_lshl_b32 s90, s16, 2
s_add_u32 s90, s90, %[swave]
s_mul_i32 s90, s90, 10240
s_add_u32 s90, s90, 0
s_add_u32 s91, %[slds], 135168
s_mov_b32 m0, s91
s_nop 0
buffer_load_dword v250, s[60:63], s90 offen offset:0 lds
s_branch LLALWW_%=
LIP_%=:
s_lshr_b32 s77, %[sb], 3
s_lshl_b32 s77, s77, 2
s_add_u32 s77, s77, %[swave]
s_lshl_b32 s77, s77, 9
s_add_u32 s77, s77, LLAJ_%=-LPDP_%=
v_lshrrev_b32 v253, 1, v248
v_add_u32 v253, s77, v253
s_mov_b32 s77, LKEND_ka-LPDP_%=-16
v_min_u32 v253, s77, v253
s_getpc_b64 s[98:99]
LPDP_%=:
global_load_dword v253, v253, s[98:99]
s_branch LLALIP_%=
LTP_%=:
s_getpc_b64 s[98:99]
LTA1_%=:
s_min_u32 s73, %[swave], 3
s_lshl_b32 s73, s73, 12
s_add_u32 s73, s73, LPG23_%=-LTA1_%=
s_add_u32 s98, s98, s73
s_addc_u32 s99, s99, 0
s_mov_b64 exec, 0
s_setpc_b64 s[98:99]
LTR1_%=:
s_mov_b64 exec, -1
s_branch LLALTP_%=
LWV_%=:
v_lshlrev_b32 v250, 3, v248
v_min_u32 v250, 4992, v250
s_sub_u32 s91, 5242880, s26
s_sub_u32 s60, s24, s91
s_subb_u32 s61, s25, 0
s_mov_b32 s62, 5242880
s_mov_b32 s63, 131072
s_lshl_b32 s90, s16, 2
s_add_u32 s90, s90, %[swave]
s_mul_i32 s90, s90, 10240
s_add_u32 s90, s90, 5120
s_add_u32 s91, %[slds], 135168
s_mov_b32 m0, s91
s_nop 0
buffer_load_dword v250, s[60:63], s90 offen offset:0 lds
s_branch LLALWV_%=
s_nop 0
s_nop 0
s_nop 0
s_nop 0
s_nop 0
s_nop 0
s_nop 0
s_nop 0
s_nop 0
s_nop 0
s_nop 0
s_nop 0
s_nop 0
s_nop 0
s_nop 0
s_nop 0
s_nop 0
s_nop 0
s_nop 0
s_nop 0
s_nop 0
s_nop 0
s_nop 0
s_nop 0
s_nop 0
s_nop 0
s_nop 0
s_nop 0
s_nop 0
s_nop 0
s_nop 0
s_nop 0
s_nop 0
s_nop 0
s_nop 0
s_nop 0
s_nop 0
s_nop 0
s_nop 0
s_nop 0
s_nop 0
s_nop 0
s_nop 0
s_nop 0
s_nop 0
s_nop 0
s_nop 0
s_nop 0
s_nop 0
s_nop 0
s_nop 0
s_nop 0
s_nop 0
s_nop 0
s_nop 0
s_nop 0
s_nop 0
s_nop 0
s_nop 0
s_nop 0
s_nop 0
s_nop 0
s_nop 0
s_nop 0
s_nop 0
s_nop 0
s_nop 0
s_nop 0
s_nop 0
s_nop 0
s_nop 0
s_nop 0
s_nop 0
s_nop 0
s_nop 0
s_nop 0
s_nop 0
s_nop 0
s_nop 0
s_nop 0
s_nop 0
s_nop 0
s_nop 0
s_nop 0
s_nop 0
s_nop 0
s_nop 0
s_nop 0
s_nop 0
s_nop 0
s_nop 0
s_nop 0
s_nop 0
s_nop 0
s_nop 0
s_nop 0
s_nop 0
s_nop 0
s_nop 0
s_nop 0
s_nop 0
s_nop 0
s_nop 0
s_nop 0
s_nop 0
s_nop 0
s_nop 0
s_nop 0
s_nop 0
s_nop 0
s_nop 0
s_nop 0
s_nop 0
s_nop 0
s_nop 0
s_nop 0
s_nop 0
s_nop 0
s_nop 0
s_nop 0
s_nop 0
s_nop 0
s_nop 0
s_nop 0
s_nop 0
s_nop 0
s_nop 0
s_nop 0
s_nop 0
s_nop 0
s_nop 0
s_nop 0
s_nop 0
s_nop 0
s_nop 0
s_nop 0
s_nop 0
s_nop 0
s_nop 0
s_nop 0
s_nop 0
s_nop 0
s_nop 0
s_nop 0
s_nop 0
s_nop 0
s_nop 0
s_nop 0
s_nop 0
s_nop 0
s_nop 0
s_nop 0
s_nop 0
s_nop 0
s_nop 0
s_nop 0
s_nop 0
s_nop 0
s_nop 0
s_nop 0
s_nop 0
s_nop 0
s_nop 0
s_nop 0
s_nop 0
s_nop 0
s_nop 0
s_nop 0
s_nop 0
s_nop 0
s_nop 0
s_nop 0
s_nop 0
s_nop 0
s_nop 0
s_nop 0
s_nop 0
s_nop 0
s_nop 0
s_nop 0
s_nop 0
s_nop 0
s_nop 0
s_nop 0
s_nop 0
s_nop 0
s_nop 0
s_nop 0
s_nop 0
s_nop 0
s_nop 0
s_nop 0
s_nop 0
s_nop 0
s_nop 0
s_nop 0
s_nop 0
s_nop 0
s_nop 0
s_nop 0
s_nop 0
s_nop 0
s_nop 0
s_nop 0
s_nop 0
s_nop 0
s_nop 0
s_nop 0
s_nop 0
s_nop 0
s_nop 0
s_nop 0
s_nop 0
s_nop 0
s_nop 0
s_nop 0
s_nop 0
s_nop 0
s_nop 0
s_nop 0
s_nop 0
s_nop 0
s_nop 0
s_nop 0
s_nop 0
s_nop 0
s_nop 0
s_nop 0
s_nop 0
s_nop 0
s_nop 0
s_nop 0
s_nop 0
s_nop 0
s_nop 0
s_nop 0
s_nop 0
s_nop 0
s_nop 0
s_nop 0
s_nop 0
s_nop 0
s_nop 0
s_nop 0
s_nop 0
s_nop 0
s_nop 0
s_nop 0
s_nop 0
s_nop 0
s_nop 0
s_nop 0
s_nop 0
s_nop 0
s_nop 0
s_nop 0
s_nop 0
s_nop 0
s_nop 0
s_nop 0
s_nop 0
s_nop 0
s_nop 0
s_nop 0
s_nop 0
s_nop 0
s_nop 0
s_nop 0
s_nop 0
s_nop 0
s_nop 0
s_nop 0
s_nop 0
s_nop 0
s_nop 0
s_nop 0
s_nop 0
s_nop 0
s_nop 0
s_nop 0
s_nop 0
s_nop 0
s_nop 0
s_nop 0
s_nop 0
s_nop 0
s_nop 0
s_nop 0
s_nop 0
s_nop 0
s_nop 0
s_nop 0
s_nop 0
s_nop 0
s_nop 0
s_nop 0
s_nop 0
s_nop 0
s_nop 0
s_nop 0
s_nop 0
s_nop 0
s_nop 0
s_nop 0
s_nop 0
s_nop 0
s_nop 0
s_nop 0
s_nop 0
s_nop 0
s_nop 0
s_nop 0
s_nop 0
s_nop 0
s_nop 0
s_nop 0
s_nop 0
s_nop 0
s_nop 0
s_nop 0
s_nop 0
s_nop 0
s_nop 0
s_nop 0
s_nop 0
s_nop 0
s_nop 0
s_nop 0
s_nop 0
s_nop 0
s_nop 0
s_nop 0
s_nop 0
s_nop 0
s_nop 0
s_nop 0
s_nop 0
s_nop 0
s_nop 0
s_nop 0
s_nop 0
s_nop 0
s_nop 0
s_nop 0
s_nop 0
s_nop 0
s_nop 0
s_nop 0
s_nop 0
s_nop 0
s_nop 0
s_nop 0
s_nop 0
s_nop 0
s_nop 0
s_nop 0
s_nop 0
s_nop 0
s_nop 0
s_nop 0
s_nop 0
s_nop 0
s_nop 0
s_nop 0
s_nop 0
s_nop 0
s_nop 0
s_nop 0
s_nop 0
s_nop 0
s_nop 0
s_nop 0
s_nop 0
s_nop 0
s_nop 0
s_nop 0
s_nop 0
s_nop 0
s_nop 0
s_nop 0
s_nop 0
s_nop 0
s_nop 0
s_nop 0
s_nop 0
s_nop 0
s_nop 0
s_nop 0
s_nop 0
s_nop 0
s_nop 0
s_nop 0
s_nop 0
s_nop 0
s_nop 0
s_nop 0
s_nop 0
s_nop 0
s_nop 0
s_nop 0
s_nop 0
s_nop 0
s_nop 0
s_nop 0
s_nop 0
s_nop 0
s_nop 0
LLAR_%=:
MM v[0:3],a[192:195],v[160:163],v[128:131]
s_add_u32 s64, s64, 16384
TR a[128:129],v232 offset:16384
s_add_u32 s81, s64, 896
MM v[0:3],a[196:199],v[176:179],v[0:3]
TR a[130:131],v232 offset:20480
v_max3_f32 v249, v144, v145, v148
MM v[0:3],a[200:203],v[192:195],v[0:3]
TR a[132:133],v232 offset:24576
v_max3_f32 v249, v249, v149, v152
MM v[0:3],a[204:207],v[208:211],v[0:3]
TR a[134:135],v232 offset:28672
v_max3_f32 v249, v249, v153, v156
MM v[4:7],a[220:223],v[208:211],v[128:131]
TR a[136:137],v233 offset:16384
v_max_f32 v249, v249, v157
MM v[4:7],a[216:219],v[192:195],v[4:7]
TR a[138:139],v233 offset:20480
v_cmp_lt_f32 vcc, s71, v249
MM v[4:7],a[212:215],v[176:179],v[4:7]
TR a[140:141],v233 offset:24576
TR a[142:143],v233 offset:28672
MM v[4:7],a[208:211],v[160:163],v[4:7]
s_cbranch_vccnz LSITE9_%=
LBACK9_%=:
MM v[12:15],a[208:211],v[164:167],v[132:135]
s_lshl_b32 s17, s16, 2
EX v0,v0
MM v[12:15],a[212:215],v[180:183],v[12:15]
s_add_u32 s17, s17, %[swave]
EX v1,v1
MM v[12:15],a[216:219],v[196:199],v[12:15]
s_sub_u32 s90, s16, 108
TR a[144:145],v232 offset:16896
CV v0,v0,v1
MM v[12:15],a[220:223],v[212:215],v[12:15]
s_min_u32 s90, s90, s16
EX v2,v2
MM v[8:11],a[204:207],v[212:215],v[132:135]
s_lshl_b32 s48, s90, 5
EX v3,v3
MM v[8:11],a[200:203],v[196:199],v[8:11]
s_add_u32 s76, %[slds], 155648
v_add_u32 v250, s76, v248
CV v1,v2,v3
MM v[8:11],a[196:199],v[180:183],v[8:11]
s_lshl_b32 s77, s17, 4
TR a[146:147],v232 offset:20992
TR a[148:149],v232 offset:25088
MM v[8:11],a[192:195],v[164:167],v[8:11]
s_add_u32 s76, s76, s77
TR a[150:151],v232 offset:29184
v_mov_b32 v251, s76
MM v[16:19],a[192:195],v[168:171],v[136:139]
RD v[124:127],v251 offset:0
RD v[96:99],v250 offset:0
MM v[16:19],a[196:199],v[184:187],v[16:19]
RD v[100:103],v250 offset:1024
RD v[104:107],v250 offset:2048
MM v[16:19],a[200:203],v[200:203],v[16:19]
RD v[108:111],v250 offset:3072
RD v[112:115],v250 offset:4096
MM v[16:19],a[204:207],v[216:219],v[16:19]
RD v[116:119],v250 offset:5120
RD v[120:123],v250 offset:6144
MM v[20:23],a[220:223],v[216:219],v[136:139]
EX v4,v4
MM v[20:23],a[216:219],v[200:203],v[20:23]
EX v5,v5
MM v[20:23],a[212:215],v[184:187],v[20:23]
s_waitcnt lgkmcnt(7)
v_readfirstlane_b32 s52, v124
v_readfirstlane_b32 s53, v125
MM v[20:23],a[208:211],v[168:171],v[20:23]
v_readfirstlane_b32 s54, v126
v_readfirstlane_b32 s55, v127
EX v6,v6
MM v[28:31],a[208:211],v[172:175],v[140:143]
RD v[124:127],v250 offset:7168
TR a[152:153],v233 offset:16896
MM v[28:31],a[212:215],v[188:191],v[28:31]
s_waitcnt lgkmcnt(2)
v_max3_u32 v253, v96, v97, v98
v_max3_u32 v251, v99, v100, v101
EX v7,v7
MM v[28:31],a[216:219],v[204:207],v[28:31]
v_max3_u32 v253, v253, v102, v103
v_max3_u32 v251, v251, v104, v105
MM v[28:31],a[220:223],v[220:223],v[28:31]
v_max3_u32 v253, v253, v106, v107
v_max3_u32 v251, v251, v108, v109
EX v8,v8
MM v[24:27],a[204:207],v[220:223],v[140:143]
v_max3_u32 v253, v253, v110, v111
v_max3_u32 v251, v251, v112, v113
MM v[24:27],a[200:203],v[204:207],v[24:27]
v_max3_u32 v253, v253, v114, v115
v_max3_u32 v251, v251, v116, v117
EX v9,v9
MM v[24:27],a[196:199],v[188:191],v[24:27]
TR a[154:155],v233 offset:20992
v_max3_u32 v253, v253, v118, v119
v_max3_u32 v251, v251, v120, v121
MM v[24:27],a[192:195],v[172:175],v[24:27]
v_max3_u32 v253, v253, v122, v123
CV v2,v4,v5
EX v10,v10
MM v[32:35],a[224:227],v[160:163],v[128:131]
s_waitcnt lgkmcnt(2)
TR a[156:157],v233 offset:25088
v_max3_u32 v251, v251, v124, v125
v_max3_u32 v253, v253, v126, v127
EX v11,v11
MM v[32:35],a[228:231],v[176:179],v[32:35]
v_max_u32 v253, v253, v251
v_cmp_lt_u32 s[60:61], s52, v253
MM v[32:35],a[232:235],v[192:195],v[32:35]
TR a[158:159],v233 offset:29184
v_cmp_lt_u32 s[62:63], s53, v253
v_cmp_lt_u32 s[56:57], s54, v253
EX v12,v12
MM v[32:35],a[236:239],v[208:211],v[32:35]
v_cmp_lt_u32 s[58:59], s55, v253
CV v3,v6,v7
MM v[36:39],a[252:255],v[208:211],v[128:131]
TR a[160:161],v232 offset:17408
EX v13,v13
MM v[36:39],a[248:251],v[192:195],v[36:39]
s_bcnt1_i32_b64 s60, s[60:61]
TR a[162:163],v232 offset:21504
CV v8,v8,v9
MM v[36:39],a[244:247],v[176:179],v[36:39]
s_bcnt1_i32_b64 s62, s[62:63]
EX v14,v14
MM v[36:39],a[240:243],v[160:163],v[36:39]
s_bcnt1_i32_b64 s56, s[56:57]
TR a[164:165],v232 offset:25600
TR a[166:167],v232 offset:29696
MM v[44:47],a[240:243],v[164:167],v[132:135]
s_bcnt1_i32_b64 s58, s[58:59]
EX v15,v15
MM v[44:47],a[244:247],v[180:183],v[44:47]
s_min_u32 s76, s60, s62
EX v16,v16
MM v[44:47],a[248:251],v[196:199],v[44:47]
s_min_u32 s77, s56, s58
TR a[168:169],v233 offset:17408
CV v9,v10,v11
MM v[44:47],a[252:255],v[212:215],v[44:47]
s_min_u32 s76, s76, s77
s_cmp_lt_u32 s76, 8
s_cbranch_scc1 LRK_%=
LRKb_%=:
EX v17,v17
MM v[40:43],a[236:239],v[212:215],v[132:135]
v_lshrrev_b32 v252, 6, v248
v_mul_u32_u24 v252, 3456, v252
)ASM"
R"ASM(
MM v[40:43],a[232:235],v[196:199],v[40:43]
TR a[170:171],v233 offset:21504
v_and_b32 v251, 48, v248
v_add_u32 v252, v252, v251
EX v18,v18
MM v[40:43],a[228:231],v[180:183],v[40:43]
s_and_b32 s90, %[swave], 1
TR a[172:173],v233 offset:25600
CV v10,v12,v13
MM v[40:43],a[224:227],v[164:167],v[40:43]
s_mul_i32 s90, s90, 276480
EX v19,v19
MM v[48:51],a[224:227],v[168:171],v[136:139]
s_bfe_u32 s91, s48, 131077
TR a[174:175],v233 offset:29696
CV v11,v14,v15
MM v[48:51],a[228:231],v[184:187],v[48:51]
s_mul_i32 s91, s91, 552960
EX v20,v20
MM v[48:51],a[232:235],v[200:203],v[48:51]
s_add_u32 s90, s90, s91
TR a[176:177],v232 offset:17920
TR a[178:179],v232 offset:22016
MM v[48:51],a[236:239],v[216:219],v[48:51]
s_and_b32 s91, s48, 0xffffff80
EX v21,v21
MM v[52:55],a[252:255],v[216:219],v[136:139]
s_add_u32 s90, s90, s91
EX v22,v22
MM v[52:55],a[248:251],v[200:203],v[52:55]
s_and_b32 s91, s48, 16
TR a[180:181],v232 offset:26112
CV v16,v16,v17
MM v[52:55],a[244:247],v[184:187],v[52:55]
s_lshl_b32 s91, s91, 2
EX v23,v23
MM v[52:55],a[240:243],v[168:171],v[52:55]
s_add_u32 s66, s90, s91
TR a[182:183],v232 offset:30208
CV v17,v18,v19
MM v[60:63],a[240:243],v[172:175],v[140:143]
s_mul_i32 s91, %[swave], 5120
EX v24,v24
MM v[60:63],a[244:247],v[188:191],v[60:63]
s_add_u32 s91, s91, %[slds]
TR a[184:185],v233 offset:17920
TR a[186:187],v233 offset:22016
MM v[60:63],a[248:251],v[204:207],v[60:63]
s_add_u32 s67, s91, 131072
TR a[188:189],v233 offset:26112
TR a[190:191],v233 offset:30208
EX v25,v25
MM v[60:63],a[252:255],v[220:223],v[60:63]
EX v26,v26
MM v[56:59],a[236:239],v[220:223],v[140:143]
EX v27,v27
MM v[56:59],a[232:235],v[204:207],v[56:59]
EX v28,v28
MM v[56:59],a[228:231],v[188:191],v[56:59]
EX v29,v29
CV v18,v20,v21
MM v[56:59],a[224:227],v[172:175],v[56:59]
s_waitcnt lgkmcnt(0)
s_barrier
MM a[0:3],a[128:131],v[64:67],a[0:3]
s_add_u32 m0, s65, 16384
CV v19,v22,v23
CV v24,v24,v25
MM a[0:3],a[132:135],v[68:71],a[0:3]
CV v25,v26,v27
CV v26,v28,v29
MM a[8:11],a[132:135],v[76:79],a[8:11]
EX v30,v30
MM a[8:11],a[128:131],v[72:75],a[8:11]
EX v31,v31
MM a[16:19],a[128:131],v[80:83],a[16:19]
RD a[192:195],v224 offset:49152
CV v27,v30,v31
MM a[16:19],a[132:135],v[84:87],a[16:19]
EX v32,v32
MM a[24:27],a[132:135],v[92:95],a[24:27]
buffer_load_dwordx4 v228, s[8:11], s64 offen offset:0 lds
MM a[24:27],a[128:131],v[88:91],a[24:27]
EX v33,v33
MM a[28:31],a[136:139],v[88:91],a[28:31]
RD a[196:199],v225 offset:49152
CV v4,v32,v33
MM a[28:31],a[140:143],v[92:95],a[28:31]
EX v34,v34
MM a[20:23],a[140:143],v[84:87],a[20:23]
EX v35,v35
MM a[20:23],a[136:139],v[80:83],a[20:23]
RD a[200:203],v226 offset:49152
CV v5,v34,v35
MM a[12:15],a[136:139],v[72:75],a[12:15]
EX v36,v36
MM a[12:15],a[140:143],v[76:79],a[12:15]
s_nop 0
LPG10_%=:
s_cbranch_execz LTR0_%=
buffer_load_dwordx4 v229, s[8:11], s64 offen offset:1024 lds
MM a[4:7],a[140:143],v[68:71],a[4:7]
EX v37,v37
MM a[4:7],a[136:139],v[64:67],a[4:7]
RD a[204:207],v227 offset:49152
CV v6,v36,v37
MM a[32:35],a[144:147],v[64:67],a[32:35]
EX v38,v38
MM a[32:35],a[148:151],v[68:71],a[32:35]
EX v39,v39
MM a[40:43],a[148:151],v[76:79],a[40:43]
RD a[208:211],v224 offset:53248
CV v7,v38,v39
MM a[40:43],a[144:147],v[72:75],a[40:43]
EX v40,v40
MM a[48:51],a[144:147],v[80:83],a[48:51]
buffer_load_dwordx4 v230, s[8:11], s64 offen offset:2048 lds
MM a[48:51],a[148:151],v[84:87],a[48:51]
EX v41,v41
MM a[56:59],a[148:151],v[92:95],a[56:59]
RD a[212:215],v225 offset:53248
CV v12,v40,v41
MM a[56:59],a[144:147],v[88:91],a[56:59]
EX v42,v42
MM a[60:63],a[152:155],v[88:91],a[60:63]
EX v43,v43
MM a[60:63],a[156:159],v[92:95],a[60:63]
RD a[216:219],v226 offset:53248
CV v13,v42,v43
MM a[52:55],a[156:159],v[84:87],a[52:55]
EX v44,v44
MM a[52:55],a[152:155],v[80:83],a[52:55]
buffer_load_dwordx4 v231, s[8:11], s64 offen offset:3072 lds
MM a[44:47],a[152:155],v[72:75],a[44:47]
s_add_u32 m0, s65, 81920
EX v45,v45
MM a[44:47],a[156:159],v[76:79],a[44:47]
RD a[220:223],v227 offset:53248
CV v14,v44,v45
MM a[36:39],a[156:159],v[68:71],a[36:39]
EX v46,v46
MM a[36:39],a[152:155],v[64:67],a[36:39]
RD a[224:227],v224 offset:57344
RD a[228:231],v225 offset:57344
MM a[64:67],a[160:163],v[64:67],a[64:67]
EX v47,v47
MM a[64:67],a[164:167],v[68:71],a[64:67]
RD a[232:235],v226 offset:57344
CV v15,v46,v47
MM a[72:75],a[164:167],v[76:79],a[72:75]
EX v48,v48
MM a[72:75],a[160:163],v[72:75],a[72:75]
buffer_load_dwordx4 v235, s[12:15], s81 offen offset:0 lds
MM a[80:83],a[160:163],v[80:83],a[80:83]
s_add_u32 m0, s67, 0
s_add_u32 s85, s66, 0
buffer_load_dwordx4 v252, s[36:39], s85 offen offset:0 lds
MM a[80:83],a[164:167],v[84:87],a[80:83]
EX v49,v49
MM a[88:91],a[164:167],v[92:95],a[88:91]
EX v50,v50
MM a[88:91],a[160:163],v[88:91],a[88:91]
RD a[236:239],v227 offset:57344
CV v20,v48,v49
MM a[92:95],a[168:171],v[88:91],a[92:95]
EX v51,v51
MM a[92:95],a[172:175],v[92:95],a[92:95]
RD a[240:243],v224 offset:61440
CV v21,v50,v51
MM a[84:87],a[172:175],v[84:87],a[84:87]
EX v52,v52
MM a[84:87],a[168:171],v[80:83],a[84:87]
EX v53,v53
MM a[76:79],a[168:171],v[72:75],a[76:79]
RD a[244:247],v225 offset:61440
CV v22,v52,v53
MM a[76:79],a[172:175],v[76:79],a[76:79]
EX v54,v54
MM a[68:71],a[172:175],v[68:71],a[68:71]
s_add_u32 m0, s65, 81920
s_nop 0
buffer_load_dwordx4 v235, s[12:15], s64 offen offset:1024 lds
MM a[68:71],a[168:171],v[64:67],a[68:71]
EX v55,v55
MM a[96:99],a[176:179],v[64:67],a[96:99]
RD a[248:251],v226 offset:61440
CV v23,v54,v55
MM a[96:99],a[180:183],v[68:71],a[96:99]
EX v56,v56
MM a[104:107],a[180:183],v[76:79],a[104:107]
EX v57,v57
MM a[104:107],a[176:179],v[72:75],a[104:107]
RD a[252:255],v227 offset:61440
CV v28,v56,v57
MM a[112:115],a[176:179],v[80:83],a[112:115]
buffer_load_dwordx4 v235, s[12:15], s81 offen offset:2048 lds
MM a[112:115],a[180:183],v[84:87],a[112:115]
EX v58,v58
MM a[120:123],a[180:183],v[92:95],a[120:123]
EX v59,v59
MM a[120:123],a[176:179],v[88:91],a[120:123]
EX v60,v60
MM a[124:127],a[184:187],v[88:91],a[124:127]
v_add_f32 v244, v144, v145
v_add_f32 v245, v148, v149
MM a[124:127],a[188:191],v[92:95],a[124:127]
v_add_f32 v246, v152, v153
v_add_f32 v247, v156, v157
MM a[116:119],a[188:191],v[84:87],a[116:119]
EX v61,v61
MM a[116:119],a[184:187],v[80:83],a[116:119]
buffer_load_dwordx4 v235, s[12:15], s64 offen offset:3072 lds
MM a[108:111],a[184:187],v[72:75],a[108:111]
EX v62,v62
MM a[108:111],a[188:191],v[76:79],a[108:111]
EX v63,v63
MM a[100:103],a[188:191],v[68:71],a[100:103]
CV v29,v58,v59
CV v30,v60,v61
MM a[100:103],a[184:187],v[64:67],a[100:103]
CV v31,v62,v63
v_smfmac_f32_16x16x64_bf16 v[144:147], v[236:239], v[0:7], v234
v_smfmac_f32_16x16x64_bf16 v[148:151], v[236:239], v[8:15], v234
v_smfmac_f32_16x16x64_bf16 v[152:155], v[236:239], v[16:23], v234
v_smfmac_f32_16x16x64_bf16 v[156:159], v[236:239], v[24:31], v234
s_waitcnt lgkmcnt(0)
MM v[64:67],a[192:195],v[160:163],v[128:131]
s_add_u32 s64, s64, 16384
TR a[128:129],v232 offset:32768
s_add_u32 s81, s64, 896
MM v[64:67],a[196:199],v[176:179],v[64:67]
TR a[130:131],v232 offset:36864
v_max3_f32 v249, v144, v145, v148
MM v[64:67],a[200:203],v[192:195],v[64:67]
TR a[132:133],v232 offset:40960
v_max3_f32 v249, v249, v149, v152
MM v[64:67],a[204:207],v[208:211],v[64:67]
TR a[134:135],v232 offset:45056
v_max3_f32 v249, v249, v153, v156
MM v[68:71],a[220:223],v[208:211],v[128:131]
TR a[136:137],v233 offset:32768
v_max_f32 v249, v249, v157
MM v[68:71],a[216:219],v[192:195],v[68:71]
TR a[138:139],v233 offset:36864
v_cmp_lt_f32 vcc, s71, v249
MM v[68:71],a[212:215],v[176:179],v[68:71]
TR a[140:141],v233 offset:40960
TR a[142:143],v233 offset:45056
MM v[68:71],a[208:211],v[160:163],v[68:71]
s_cbranch_vccnz LSITE10_%=
LBACK10_%=:
MM v[76:79],a[208:211],v[164:167],v[132:135]
s_add_u32 m0, s67, 1024
s_add_u32 s85, s66, 55296
buffer_load_dwordx4 v252, s[36:39], s85 offen offset:0 lds
MM v[76:79],a[212:215],v[180:183],v[76:79]
EX v64,v64
MM v[76:79],a[216:219],v[196:199],v[76:79]
EX v65,v65
MM v[76:79],a[220:223],v[212:215],v[76:79]
TR a[144:145],v232 offset:33280
CV v64,v64,v65
MM v[72:75],a[204:207],v[212:215],v[132:135]
EX v66,v66
MM v[72:75],a[200:203],v[196:199],v[72:75]
EX v67,v67
MM v[72:75],a[196:199],v[180:183],v[72:75]
TR a[146:147],v232 offset:37376
CV v65,v66,v67
MM v[72:75],a[192:195],v[164:167],v[72:75]
EX v68,v68
MM v[80:83],a[192:195],v[168:171],v[136:139]
TR a[148:149],v232 offset:41472
TR a[150:151],v232 offset:45568
MM v[80:83],a[196:199],v[184:187],v[80:83]
EX v69,v69
MM v[80:83],a[200:203],v[200:203],v[80:83]
TR a[152:153],v233 offset:33280
CV v66,v68,v69
MM v[80:83],a[204:207],v[216:219],v[80:83]
EX v70,v70
MM v[84:87],a[220:223],v[216:219],v[136:139]
EX v71,v71
MM v[84:87],a[216:219],v[200:203],v[84:87]
TR a[154:155],v233 offset:37376
CV v67,v70,v71
MM v[84:87],a[212:215],v[184:187],v[84:87]
EX v72,v72
MM v[84:87],a[208:211],v[168:171],v[84:87]
TR a[156:157],v233 offset:41472
TR a[158:159],v233 offset:45568
MM v[92:95],a[208:211],v[172:175],v[140:143]
EX v73,v73
MM v[92:95],a[212:215],v[188:191],v[92:95]
TR a[160:161],v232 offset:33792
CV v72,v72,v73
MM v[92:95],a[216:219],v[204:207],v[92:95]
EX v74,v74
MM v[92:95],a[220:223],v[220:223],v[92:95]
EX v75,v75
MM v[88:91],a[204:207],v[220:223],v[140:143]
TR a[162:163],v232 offset:37888
CV v73,v74,v75
MM v[88:91],a[200:203],v[204:207],v[88:91]
TR a[164:165],v232 offset:41984
TR a[166:167],v232 offset:46080
MM v[88:91],a[196:199],v[188:191],v[88:91]
EX v76,v76
MM v[88:91],a[192:195],v[172:175],v[88:91]
EX v77,v77
MM v[96:99],a[224:227],v[160:163],v[128:131]
TR a[168:169],v233 offset:33792
CV v74,v76,v77
MM v[96:99],a[228:231],v[176:179],v[96:99]
EX v78,v78
MM v[96:99],a[232:235],v[192:195],v[96:99]
TR a[170:171],v233 offset:37888
TR a[172:173],v233 offset:41984
MM v[96:99],a[236:239],v[208:211],v[96:99]
EX v79,v79
MM v[100:103],a[252:255],v[208:211],v[128:131]
TR a[174:175],v233 offset:46080
CV v75,v78,v79
MM v[100:103],a[248:251],v[192:195],v[100:103]
EX v80,v80
MM v[100:103],a[244:247],v[176:179],v[100:103]
EX v81,v81
MM v[100:103],a[240:243],v[160:163],v[100:103]
TR a[176:177],v232 offset:34304
CV v80,v80,v81
MM v[108:111],a[240:243],v[164:167],v[132:135]
s_add_u32 m0, s67, 2048
s_add_u32 s85, s66, 110592
buffer_load_dwordx4 v252, s[36:39], s85 offen offset:0 lds
MM v[108:111],a[244:247],v[180:183],v[108:111]
EX v82,v82
MM v[108:111],a[248:251],v[196:199],v[108:111]
TR a[178:179],v232 offset:38400
TR a[180:181],v232 offset:42496
MM v[108:111],a[252:255],v[212:215],v[108:111]
EX v83,v83
MM v[104:107],a[236:239],v[212:215],v[132:135]
TR a[182:183],v232 offset:46592
CV v81,v82,v83
MM v[104:107],a[232:235],v[196:199],v[104:107]
EX v84,v84
MM v[104:107],a[228:231],v[180:183],v[104:107]
EX v85,v85
MM v[104:107],a[224:227],v[164:167],v[104:107]
TR a[184:185],v233 offset:34304
CV v82,v84,v85
MM v[112:115],a[224:227],v[168:171],v[136:139]
EX v86,v86
MM v[112:115],a[228:231],v[184:187],v[112:115]
TR a[186:187],v233 offset:38400
TR a[188:189],v233 offset:42496
MM v[112:115],a[232:235],v[200:203],v[112:115]
EX v87,v87
MM v[112:115],a[236:239],v[216:219],v[112:115]
TR a[190:191],v233 offset:46592
CV v83,v86,v87
MM v[116:119],a[252:255],v[216:219],v[136:139]
EX v88,v88
MM v[116:119],a[248:251],v[200:203],v[116:119]
EX v89,v89
MM v[116:119],a[244:247],v[184:187],v[116:119]
EX v90,v90
MM v[116:119],a[240:243],v[168:171],v[116:119]
EX v91,v91
MM v[124:127],a[240:243],v[172:175],v[140:143]
EX v92,v92
MM v[124:127],a[244:247],v[188:191],v[124:127]
EX v93,v93
MM v[124:127],a[248:251],v[204:207],v[124:127]
EX v94,v94
MM v[124:127],a[252:255],v[220:223],v[124:127]
EX v95,v95
MM v[120:123],a[236:239],v[220:223],v[140:143]
EX v96,v96
MM v[120:123],a[232:235],v[204:207],v[120:123]
EX v97,v97
MM v[120:123],a[228:231],v[188:191],v[120:123]
EX v98,v98
MM v[120:123],a[224:227],v[172:175],v[120:123]
s_waitcnt vmcnt(15) lgkmcnt(0)
s_barrier
MM a[0:3],a[128:131],v[0:3],a[0:3]
s_add_u32 m0, s65, 32768
CV v88,v88,v89
CV v89,v90,v91
MM a[0:3],a[132:135],v[4:7],a[0:3]
RD a[192:195],v224 offset:0
CV v90,v92,v93
MM a[8:11],a[132:135],v[12:15],a[8:11]
RD a[196:199],v225 offset:0
CV v91,v94,v95
MM a[8:11],a[128:131],v[8:11],a[8:11]
)ASM"
R"ASM(
buffer_load_dwordx4 v228, s[8:11], s64 offen offset:0 lds
MM a[16:19],a[128:131],v[16:19],a[16:19]
RD a[200:203],v226 offset:0
CV v68,v96,v97
MM a[16:19],a[132:135],v[20:23],a[16:19]
buffer_load_dwordx4 v229, s[8:11], s64 offen offset:1024 lds
MM a[24:27],a[132:135],v[28:31],a[24:27]
RD a[204:207],v227 offset:0
RD a[208:211],v224 offset:4096
MM a[24:27],a[128:131],v[24:27],a[24:27]
EX v99,v99
MM a[28:31],a[136:139],v[24:27],a[28:31]
RD a[212:215],v225 offset:4096
CV v69,v98,v99
MM a[28:31],a[140:143],v[28:31],a[28:31]
EX v100,v100
MM a[20:23],a[140:143],v[20:23],a[20:23]
buffer_load_dwordx4 v230, s[8:11], s64 offen offset:2048 lds
MM a[20:23],a[136:139],v[16:19],a[20:23]
EX v101,v101
MM a[12:15],a[136:139],v[8:11],a[12:15]
RD a[216:219],v226 offset:4096
CV v70,v100,v101
MM a[12:15],a[140:143],v[12:15],a[12:15]
EX v102,v102
MM a[4:7],a[140:143],v[4:7],a[4:7]
EX v103,v103
MM a[4:7],a[136:139],v[0:3],a[4:7]
RD a[220:223],v227 offset:4096
CV v71,v102,v103
MM a[32:35],a[144:147],v[0:3],a[32:35]
EX v104,v104
MM a[32:35],a[148:151],v[4:7],a[32:35]
buffer_load_dwordx4 v231, s[8:11], s64 offen offset:3072 lds
MM a[40:43],a[148:151],v[12:15],a[40:43]
s_add_u32 m0, s65, 98304
EX v105,v105
MM a[40:43],a[144:147],v[8:11],a[40:43]
RD a[224:227],v224 offset:8192
CV v76,v104,v105
MM a[48:51],a[144:147],v[16:19],a[48:51]
EX v106,v106
MM a[48:51],a[148:151],v[20:23],a[48:51]
EX v107,v107
MM a[56:59],a[148:151],v[28:31],a[56:59]
RD a[228:231],v225 offset:8192
CV v77,v106,v107
MM a[56:59],a[144:147],v[24:27],a[56:59]
EX v108,v108
MM a[60:63],a[152:155],v[24:27],a[60:63]
buffer_load_dwordx4 v235, s[12:15], s81 offen offset:0 lds
MM a[60:63],a[156:159],v[28:31],a[60:63]
EX v109,v109
MM a[52:55],a[156:159],v[20:23],a[52:55]
RD a[232:235],v226 offset:8192
CV v78,v108,v109
MM a[52:55],a[152:155],v[16:19],a[52:55]
EX v110,v110
MM a[44:47],a[152:155],v[8:11],a[44:47]
EX v111,v111
MM a[44:47],a[156:159],v[12:15],a[44:47]
RD a[236:239],v227 offset:8192
CV v79,v110,v111
MM a[36:39],a[156:159],v[4:7],a[36:39]
buffer_load_dwordx4 v235, s[12:15], s64 offen offset:1024 lds
MM a[36:39],a[152:155],v[0:3],a[36:39]
EX v112,v112
MM a[64:67],a[160:163],v[0:3],a[64:67]
EX v113,v113
MM a[64:67],a[164:167],v[4:7],a[64:67]
RD a[240:243],v224 offset:12288
CV v84,v112,v113
MM a[72:75],a[164:167],v[12:15],a[72:75]
EX v114,v114
MM a[72:75],a[160:163],v[8:11],a[72:75]
RD a[244:247],v225 offset:12288
RD a[248:251],v226 offset:12288
MM a[80:83],a[160:163],v[16:19],a[80:83]
s_add_u32 m0, s67, 3072
s_add_u32 s85, s66, 165888
buffer_load_dwordx4 v252, s[36:39], s85 offen offset:0 lds
MM a[80:83],a[164:167],v[20:23],a[80:83]
EX v115,v115
MM a[88:91],a[164:167],v[28:31],a[88:91]
RD a[252:255],v227 offset:12288
CV v85,v114,v115
MM a[88:91],a[160:163],v[24:27],a[88:91]
EX v116,v116
MM a[92:95],a[168:171],v[24:27],a[92:95]
EX v117,v117
MM a[92:95],a[172:175],v[28:31],a[92:95]
s_add_u32 m0, s65, 98304
s_nop 0
buffer_load_dwordx4 v235, s[12:15], s81 offen offset:2048 lds
MM a[84:87],a[172:175],v[20:23],a[84:87]
EX v118,v118
MM a[84:87],a[168:171],v[16:19],a[84:87]
EX v119,v119
MM a[76:79],a[168:171],v[8:11],a[76:79]
buffer_load_dwordx4 v235, s[12:15], s64 offen offset:3072 lds
MM a[76:79],a[172:175],v[12:15],a[76:79]
EX v120,v120
MM a[68:71],a[172:175],v[4:7],a[68:71]
EX v121,v121
MM a[68:71],a[168:171],v[0:3],a[68:71]
EX v122,v122
MM a[96:99],a[176:179],v[0:3],a[96:99]
EX v123,v123
MM a[96:99],a[180:183],v[4:7],a[96:99]
EX v124,v124
MM a[104:107],a[180:183],v[12:15],a[104:107]
EX v125,v125
MM a[104:107],a[176:179],v[8:11],a[104:107]
EX v126,v126
MM a[112:115],a[176:179],v[16:19],a[112:115]
EX v127,v127
MM a[112:115],a[180:183],v[20:23],a[112:115]
CV v86,v116,v117
CV v87,v118,v119
MM a[120:123],a[180:183],v[28:31],a[120:123]
CV v92,v120,v121
CV v93,v122,v123
MM a[120:123],a[176:179],v[24:27],a[120:123]
CV v94,v124,v125
CV v95,v126,v127
MM a[124:127],a[184:187],v[24:27],a[124:127]
v_add_f32 v244, v144, v145
v_add_f32 v245, v148, v149
MM a[124:127],a[188:191],v[28:31],a[124:127]
v_add_f32 v246, v152, v153
v_add_f32 v247, v156, v157
MM a[116:119],a[188:191],v[20:23],a[116:119]
MM a[116:119],a[184:187],v[16:19],a[116:119]
MM a[108:111],a[184:187],v[8:11],a[108:111]
MM a[108:111],a[188:191],v[12:15],a[108:111]
MM a[100:103],a[188:191],v[4:7],a[100:103]
MM a[100:103],a[184:187],v[0:3],a[100:103]
v_smfmac_f32_16x16x64_bf16 v[144:147], v[236:239], v[64:71], v234
v_smfmac_f32_16x16x64_bf16 v[148:151], v[236:239], v[72:79], v234
v_smfmac_f32_16x16x64_bf16 v[152:155], v[236:239], v[80:87], v234
v_smfmac_f32_16x16x64_bf16 v[156:159], v[236:239], v[88:95], v234
s_waitcnt lgkmcnt(0)
MM v[0:3],a[192:195],v[160:163],v[128:131]
s_add_u32 s64, s64, 16384
TR a[128:129],v232 offset:49152
s_add_u32 s81, s64, 896
MM v[0:3],a[196:199],v[176:179],v[0:3]
TR a[130:131],v232 offset:53248
v_max3_f32 v249, v144, v145, v148
MM v[0:3],a[200:203],v[192:195],v[0:3]
TR a[132:133],v232 offset:57344
v_max3_f32 v249, v249, v149, v152
MM v[0:3],a[204:207],v[208:211],v[0:3]
TR a[134:135],v232 offset:61440
v_max3_f32 v249, v249, v153, v156
MM v[4:7],a[220:223],v[208:211],v[128:131]
TR a[136:137],v233 offset:49152
v_max_f32 v249, v249, v157
MM v[4:7],a[216:219],v[192:195],v[4:7]
TR a[138:139],v233 offset:53248
v_cmp_lt_f32 vcc, s71, v249
MM v[4:7],a[212:215],v[176:179],v[4:7]
TR a[140:141],v233 offset:57344
TR a[142:143],v233 offset:61440
MM v[4:7],a[208:211],v[160:163],v[4:7]
s_cbranch_vccnz LSITE11_%=
LBACK11_%=:
MM v[12:15],a[208:211],v[164:167],v[132:135]
s_add_u32 m0, s67, 4096
s_add_u32 s85, s66, 221184
buffer_load_dwordx4 v252, s[36:39], s85 offen offset:0 lds
MM v[12:15],a[212:215],v[180:183],v[12:15]
EX v0,v0
MM v[12:15],a[216:219],v[196:199],v[12:15]
EX v1,v1
MM v[12:15],a[220:223],v[212:215],v[12:15]
TR a[144:145],v232 offset:49664
CV v0,v0,v1
MM v[8:11],a[204:207],v[212:215],v[132:135]
EX v2,v2
MM v[8:11],a[200:203],v[196:199],v[8:11]
EX v3,v3
MM v[8:11],a[196:199],v[180:183],v[8:11]
TR a[146:147],v232 offset:53760
CV v1,v2,v3
MM v[8:11],a[192:195],v[164:167],v[8:11]
EX v4,v4
MM v[16:19],a[192:195],v[168:171],v[136:139]
TR a[148:149],v232 offset:57856
TR a[150:151],v232 offset:61952
MM v[16:19],a[196:199],v[184:187],v[16:19]
EX v5,v5
MM v[16:19],a[200:203],v[200:203],v[16:19]
TR a[152:153],v233 offset:49664
CV v2,v4,v5
MM v[16:19],a[204:207],v[216:219],v[16:19]
EX v6,v6
MM v[20:23],a[220:223],v[216:219],v[136:139]
EX v7,v7
MM v[20:23],a[216:219],v[200:203],v[20:23]
TR a[154:155],v233 offset:53760
CV v3,v6,v7
MM v[20:23],a[212:215],v[184:187],v[20:23]
EX v8,v8
MM v[20:23],a[208:211],v[168:171],v[20:23]
TR a[156:157],v233 offset:57856
TR a[158:159],v233 offset:61952
MM v[28:31],a[208:211],v[172:175],v[140:143]
EX v9,v9
MM v[28:31],a[212:215],v[188:191],v[28:31]
TR a[160:161],v232 offset:50176
CV v8,v8,v9
MM v[28:31],a[216:219],v[204:207],v[28:31]
EX v10,v10
MM v[28:31],a[220:223],v[220:223],v[28:31]
EX v11,v11
MM v[24:27],a[204:207],v[220:223],v[140:143]
TR a[162:163],v232 offset:54272
CV v9,v10,v11
MM v[24:27],a[200:203],v[204:207],v[24:27]
TR a[164:165],v232 offset:58368
TR a[166:167],v232 offset:62464
MM v[24:27],a[196:199],v[188:191],v[24:27]
EX v12,v12
MM v[24:27],a[192:195],v[172:175],v[24:27]
EX v13,v13
MM v[32:35],a[224:227],v[160:163],v[128:131]
TR a[168:169],v233 offset:50176
CV v10,v12,v13
MM v[32:35],a[228:231],v[176:179],v[32:35]
EX v14,v14
MM v[32:35],a[232:235],v[192:195],v[32:35]
TR a[170:171],v233 offset:54272
TR a[172:173],v233 offset:58368
MM v[32:35],a[236:239],v[208:211],v[32:35]
EX v15,v15
MM v[36:39],a[252:255],v[208:211],v[128:131]
TR a[174:175],v233 offset:62464
CV v11,v14,v15
MM v[36:39],a[248:251],v[192:195],v[36:39]
EX v16,v16
MM v[36:39],a[244:247],v[176:179],v[36:39]
EX v17,v17
MM v[36:39],a[240:243],v[160:163],v[36:39]
TR a[176:177],v232 offset:50688
CV v16,v16,v17
MM v[44:47],a[240:243],v[164:167],v[132:135]
EX v18,v18
MM v[44:47],a[244:247],v[180:183],v[44:47]
TR a[178:179],v232 offset:54784
TR a[180:181],v232 offset:58880
MM v[44:47],a[248:251],v[196:199],v[44:47]
EX v19,v19
MM v[44:47],a[252:255],v[212:215],v[44:47]
TR a[182:183],v232 offset:62976
CV v17,v18,v19
MM v[40:43],a[236:239],v[212:215],v[132:135]
EX v20,v20
MM v[40:43],a[232:235],v[196:199],v[40:43]
EX v21,v21
MM v[40:43],a[228:231],v[180:183],v[40:43]
TR a[184:185],v233 offset:50688
CV v18,v20,v21
MM v[40:43],a[224:227],v[164:167],v[40:43]
EX v22,v22
MM v[48:51],a[224:227],v[168:171],v[136:139]
TR a[186:187],v233 offset:54784
TR a[188:189],v233 offset:58880
MM v[48:51],a[228:231],v[184:187],v[48:51]
EX v23,v23
MM v[48:51],a[232:235],v[200:203],v[48:51]
TR a[190:191],v233 offset:62976
CV v19,v22,v23
MM v[48:51],a[236:239],v[216:219],v[48:51]
EX v24,v24
MM v[52:55],a[252:255],v[216:219],v[136:139]
EX v25,v25
MM v[52:55],a[248:251],v[200:203],v[52:55]
EX v26,v26
MM v[52:55],a[244:247],v[184:187],v[52:55]
EX v27,v27
MM v[52:55],a[240:243],v[168:171],v[52:55]
EX v28,v28
MM v[60:63],a[240:243],v[172:175],v[140:143]
EX v29,v29
MM v[60:63],a[244:247],v[188:191],v[60:63]
EX v30,v30
MM v[60:63],a[248:251],v[204:207],v[60:63]
EX v31,v31
LPG11_%=:
s_cbranch_execz LTR0_%=
MM v[60:63],a[252:255],v[220:223],v[60:63]
EX v32,v32
MM v[56:59],a[236:239],v[220:223],v[140:143]
EX v33,v33
MM v[56:59],a[232:235],v[204:207],v[56:59]
EX v34,v34
MM v[56:59],a[228:231],v[188:191],v[56:59]
EX v35,v35
MM v[56:59],a[224:227],v[172:175],v[56:59]
s_waitcnt vmcnt(17) lgkmcnt(0)
s_barrier
MM a[0:3],a[128:131],v[64:67],a[0:3]
s_mov_b32 s86, 94208
v_bfe_u32 v251, v248, 4, 3
v_lshlrev_b32 v251, 3, v251
buffer_load_dwordx2 v[252:253], v251, s[20:23], s86 offen offset:0 sc1
MM a[0:3],a[132:135],v[68:71],a[0:3]
s_add_u32 m0, s65, 49152
CV v24,v24,v25
CV v25,v26,v27
MM a[8:11],a[132:135],v[76:79],a[8:11]
RD a[192:195],v224 offset:16384
CV v26,v28,v29
MM a[8:11],a[128:131],v[72:75],a[8:11]
RD a[196:199],v225 offset:16384
CV v27,v30,v31
MM a[16:19],a[128:131],v[80:83],a[16:19]
buffer_load_dwordx4 v228, s[8:11], s64 offen offset:0 lds
MM a[16:19],a[132:135],v[84:87],a[16:19]
RD a[200:203],v226 offset:16384
CV v4,v32,v33
MM a[24:27],a[132:135],v[92:95],a[24:27]
RD a[204:207],v227 offset:16384
CV v5,v34,v35
MM a[24:27],a[128:131],v[88:91],a[24:27]
buffer_load_dwordx4 v229, s[8:11], s64 offen offset:1024 lds
MM a[28:31],a[136:139],v[88:91],a[28:31]
RD a[208:211],v224 offset:20480
RD a[212:215],v225 offset:20480
MM a[28:31],a[140:143],v[92:95],a[28:31]
EX v36,v36
MM a[20:23],a[140:143],v[84:87],a[20:23]
buffer_load_dwordx4 v230, s[8:11], s64 offen offset:2048 lds
MM a[20:23],a[136:139],v[80:83],a[20:23]
EX v37,v37
MM a[12:15],a[136:139],v[72:75],a[12:15]
RD a[216:219],v226 offset:20480
CV v6,v36,v37
MM a[12:15],a[140:143],v[76:79],a[12:15]
EX v38,v38
MM a[4:7],a[140:143],v[68:71],a[4:7]
EX v39,v39
MM a[4:7],a[136:139],v[64:67],a[4:7]
RD a[220:223],v227 offset:20480
CV v7,v38,v39
MM a[32:35],a[144:147],v[64:67],a[32:35]
EX v40,v40
MM a[32:35],a[148:151],v[68:71],a[32:35]
buffer_load_dwordx4 v231, s[8:11], s64 offen offset:3072 lds
MM a[40:43],a[148:151],v[76:79],a[40:43]
s_add_u32 m0, s65, 114688
EX v41,v41
MM a[40:43],a[144:147],v[72:75],a[40:43]
RD a[224:227],v224 offset:24576
CV v12,v40,v41
MM a[48:51],a[144:147],v[80:83],a[48:51]
EX v42,v42
MM a[48:51],a[148:151],v[84:87],a[48:51]
EX v43,v43
MM a[56:59],a[148:151],v[92:95],a[56:59]
RD a[228:231],v225 offset:24576
CV v13,v42,v43
MM a[56:59],a[144:147],v[88:91],a[56:59]
EX v44,v44
MM a[60:63],a[152:155],v[88:91],a[60:63]
buffer_load_dwordx4 v235, s[12:15], s81 offen offset:0 lds
MM a[60:63],a[156:159],v[92:95],a[60:63]
EX v45,v45
MM a[52:55],a[156:159],v[84:87],a[52:55]
RD a[232:235],v226 offset:24576
CV v14,v44,v45
MM a[52:55],a[152:155],v[80:83],a[52:55]
EX v46,v46
MM a[44:47],a[152:155],v[72:75],a[44:47]
EX v47,v47
MM a[44:47],a[156:159],v[76:79],a[44:47]
RD a[236:239],v227 offset:24576
CV v15,v46,v47
MM a[36:39],a[156:159],v[68:71],a[36:39]
buffer_load_dwordx4 v235, s[12:15], s64 offen offset:1024 lds
MM a[36:39],a[152:155],v[64:67],a[36:39]
EX v48,v48
MM a[64:67],a[160:163],v[64:67],a[64:67]
EX v49,v49
MM a[64:67],a[164:167],v[68:71],a[64:67]
RD a[240:243],v224 offset:28672
)ASM"
R"ASM(
CV v20,v48,v49
MM a[72:75],a[164:167],v[76:79],a[72:75]
EX v50,v50
MM a[72:75],a[160:163],v[72:75],a[72:75]
RD a[244:247],v225 offset:28672
RD a[248:251],v226 offset:28672
MM a[80:83],a[160:163],v[80:83],a[80:83]
EX v51,v51
MM a[80:83],a[164:167],v[84:87],a[80:83]
RD a[252:255],v227 offset:28672
CV v21,v50,v51
MM a[88:91],a[164:167],v[92:95],a[88:91]
EX v52,v52
MM a[88:91],a[160:163],v[88:91],a[88:91]
buffer_load_dwordx4 v235, s[12:15], s81 offen offset:2048 lds
MM a[92:95],a[168:171],v[88:91],a[92:95]
EX v53,v53
MM a[92:95],a[172:175],v[92:95],a[92:95]
EX v54,v54
MM a[84:87],a[172:175],v[84:87],a[84:87]
buffer_load_dwordx4 v235, s[12:15], s64 offen offset:3072 lds
MM a[84:87],a[168:171],v[80:83],a[84:87]
EX v55,v55
MM a[76:79],a[168:171],v[72:75],a[76:79]
EX v56,v56
MM a[76:79],a[172:175],v[76:79],a[76:79]
EX v57,v57
MM a[68:71],a[172:175],v[68:71],a[68:71]
EX v58,v58
MM a[68:71],a[168:171],v[64:67],a[68:71]
EX v59,v59
MM a[96:99],a[176:179],v[64:67],a[96:99]
EX v60,v60
MM a[96:99],a[180:183],v[68:71],a[96:99]
EX v61,v61
MM a[104:107],a[180:183],v[76:79],a[104:107]
EX v62,v62
MM a[104:107],a[176:179],v[72:75],a[104:107]
EX v63,v63
MM a[112:115],a[176:179],v[80:83],a[112:115]
CV v22,v52,v53
CV v23,v54,v55
MM a[112:115],a[180:183],v[84:87],a[112:115]
CV v28,v56,v57
CV v29,v58,v59
MM a[120:123],a[180:183],v[92:95],a[120:123]
CV v30,v60,v61
CV v31,v62,v63
MM a[120:123],a[176:179],v[88:91],a[120:123]
MM a[124:127],a[184:187],v[88:91],a[124:127]
v_add_f32 v244, v144, v145
v_add_f32 v245, v148, v149
MM a[124:127],a[188:191],v[92:95],a[124:127]
v_add_f32 v246, v152, v153
v_add_f32 v247, v156, v157
MM a[116:119],a[188:191],v[84:87],a[116:119]
MM a[116:119],a[184:187],v[80:83],a[116:119]
MM a[108:111],a[184:187],v[72:75],a[108:111]
MM a[108:111],a[188:191],v[76:79],a[108:111]
MM a[100:103],a[188:191],v[68:71],a[100:103]
MM a[100:103],a[184:187],v[64:67],a[100:103]
v_smfmac_f32_16x16x64_bf16 v[144:147], v[236:239], v[0:7], v234
v_smfmac_f32_16x16x64_bf16 v[148:151], v[236:239], v[8:15], v234
v_smfmac_f32_16x16x64_bf16 v[152:155], v[236:239], v[16:23], v234
v_smfmac_f32_16x16x64_bf16 v[156:159], v[236:239], v[24:31], v234
s_waitcnt lgkmcnt(0)
MM v[64:67],a[192:195],v[160:163],v[128:131]
s_add_u32 s64, s64, 16384
TR a[128:129],v232 offset:0
s_add_u32 s81, s64, 896
MM v[64:67],a[196:199],v[176:179],v[64:67]
TR a[130:131],v232 offset:4096
v_max3_f32 v249, v144, v145, v148
MM v[64:67],a[200:203],v[192:195],v[64:67]
TR a[132:133],v232 offset:8192
v_max3_f32 v249, v249, v149, v152
MM v[64:67],a[204:207],v[208:211],v[64:67]
TR a[134:135],v232 offset:12288
v_max3_f32 v249, v249, v153, v156
MM v[68:71],a[220:223],v[208:211],v[128:131]
TR a[136:137],v233 offset:0
v_max_f32 v249, v249, v157
MM v[68:71],a[216:219],v[192:195],v[68:71]
TR a[138:139],v233 offset:4096
v_cmp_lt_f32 vcc, s71, v249
MM v[68:71],a[212:215],v[176:179],v[68:71]
TR a[140:141],v233 offset:8192
TR a[142:143],v233 offset:12288
MM v[68:71],a[208:211],v[160:163],v[68:71]
s_cbranch_vccnz LSITE12_%=
LBACK12_%=:
MM v[76:79],a[208:211],v[164:167],v[132:135]
s_waitcnt vmcnt(8)
v_cmp_ne_u32 vcc, s69, v253
s_cbranch_vccnz LRIX_%=
LRIXb_%=:
MM v[76:79],a[212:215],v[180:183],v[76:79]
s_and_b32 s90, s17, 7
v_and_b32 v253, 65535, v252
v_readlane_b32 s19, v252, s90
MM v[76:79],a[216:219],v[196:199],v[76:79]
v_sub_u32 v253, 65535, v253
v_mul_u32_u24 v253, 1280, v253
MM v[76:79],a[220:223],v[212:215],v[76:79]
v_lshrrev_b32 v252, 7, v248
v_lshl_add_u32 v253, v252, 4, v253
MM v[72:75],a[204:207],v[212:215],v[132:135]
s_mul_i32 s91, %[swave], 3072
EX v64,v64
MM v[72:75],a[200:203],v[196:199],v[72:75]
s_add_u32 s91, s91, %[slds]
EX v65,v65
MM v[72:75],a[196:199],v[180:183],v[72:75]
s_bfe_u32 s90, s48, 131077
EX v66,v66
MM v[72:75],a[192:195],v[164:167],v[72:75]
s_mul_i32 s90, s90, 320
EX v67,v67
MM v[80:83],a[192:195],v[168:171],v[136:139]
s_add_u32 s90, s90, 131072
s_add_u32 m0, s91, 151552
s_nop 0
buffer_load_dwordx4 v253, s[20:23], s90 offen offset:0 sc1 lds
MM v[80:83],a[196:199],v[184:187],v[80:83]
TR a[144:145],v232 offset:512
CV v64,v64,v65
MM v[80:83],a[200:203],v[200:203],v[80:83]
s_add_u32 m0, s91, 152448
s_nop 0
buffer_load_dwordx4 v253, s[20:23], s90 offen offset:128 sc1 lds
MM v[80:83],a[204:207],v[216:219],v[80:83]
EX v68,v68
MM v[84:87],a[220:223],v[216:219],v[136:139]
s_add_u32 m0, s91, 153344
s_nop 0
buffer_load_dwordx4 v253, s[20:23], s90 offen offset:256 sc1 lds
MM v[84:87],a[216:219],v[200:203],v[84:87]
EX v69,v69
MM v[84:87],a[212:215],v[184:187],v[84:87]
s_lshr_b32 s77, %[sb], 3
TR a[146:147],v232 offset:4608
TR a[148:149],v232 offset:8704
MM v[84:87],a[208:211],v[168:171],v[84:87]
s_lshl_b32 s77, s77, 2
EX v70,v70
MM v[92:95],a[208:211],v[172:175],v[140:143]
s_add_u32 s77, s77, %[swave]
EX v71,v71
MM v[92:95],a[212:215],v[188:191],v[92:95]
s_lshl_b32 s77, s77, 9
TR a[150:151],v232 offset:12800
CV v65,v66,v67
MM v[92:95],a[216:219],v[204:207],v[92:95]
s_add_u32 s77, s77, 0
v_lshrrev_b32 v253, 1, v248
v_add_u32 v253, s77, v253
MM v[92:95],a[220:223],v[220:223],v[92:95]
s_mov_b32 s77, LKEND_ka-LPD0_%=-16
TR a[152:153],v233 offset:512
v_min_u32 v253, s77, v253
EX v72,v72
MM v[88:91],a[204:207],v[220:223],v[140:143]
s_getpc_b64 s[98:99]
LPD0_%=:
global_load_dword v253, v253, s[98:99]
MM v[88:91],a[200:203],v[204:207],v[88:91]
s_add_u32 s67, s95, 1024
EX v73,v73
MM v[88:91],a[196:199],v[188:191],v[88:91]
s_mov_b32 s70, 5
TR a[154:155],v233 offset:4608
CV v66,v68,v69
MM v[88:91],a[192:195],v[172:175],v[88:91]
EX v74,v74
MM v[96:99],a[224:227],v[160:163],v[128:131]
TR a[156:157],v233 offset:8704
TR a[158:159],v233 offset:12800
EX v75,v75
MM v[96:99],a[228:231],v[176:179],v[96:99]
TR a[160:161],v232 offset:1024
CV v67,v70,v71
MM v[96:99],a[232:235],v[192:195],v[96:99]
EX v76,v76
MM v[96:99],a[236:239],v[208:211],v[96:99]
EX v77,v77
MM v[100:103],a[252:255],v[208:211],v[128:131]
TR a[162:163],v232 offset:5120
CV v72,v72,v73
MM v[100:103],a[248:251],v[192:195],v[100:103]
EX v78,v78
MM v[100:103],a[244:247],v[176:179],v[100:103]
TR a[164:165],v232 offset:9216
CV v73,v74,v75
MM v[100:103],a[240:243],v[160:163],v[100:103]
EX v79,v79
MM v[108:111],a[240:243],v[164:167],v[132:135]
TR a[166:167],v232 offset:13312
TR a[168:169],v233 offset:1024
MM v[108:111],a[244:247],v[180:183],v[108:111]
EX v80,v80
MM v[108:111],a[248:251],v[196:199],v[108:111]
EX v81,v81
MM v[108:111],a[252:255],v[212:215],v[108:111]
TR a[170:171],v233 offset:5120
CV v74,v76,v77
MM v[104:107],a[236:239],v[212:215],v[132:135]
EX v82,v82
MM v[104:107],a[232:235],v[196:199],v[104:107]
TR a[172:173],v233 offset:9216
CV v75,v78,v79
MM v[104:107],a[228:231],v[180:183],v[104:107]
EX v83,v83
MM v[104:107],a[224:227],v[164:167],v[104:107]
TR a[174:175],v233 offset:13312
TR a[176:177],v232 offset:1536
MM v[112:115],a[224:227],v[168:171],v[136:139]
EX v84,v84
MM v[112:115],a[228:231],v[184:187],v[112:115]
TR a[178:179],v232 offset:5632
CV v80,v80,v81
MM v[112:115],a[232:235],v[200:203],v[112:115]
EX v85,v85
MM v[112:115],a[236:239],v[216:219],v[112:115]
TR a[180:181],v232 offset:9728
CV v81,v82,v83
MM v[116:119],a[252:255],v[216:219],v[136:139]
EX v86,v86
MM v[116:119],a[248:251],v[200:203],v[116:119]
TR a[182:183],v232 offset:13824
TR a[184:185],v233 offset:1536
MM v[116:119],a[244:247],v[184:187],v[116:119]
EX v87,v87
MM v[116:119],a[240:243],v[168:171],v[116:119]
EX v88,v88
MM v[124:127],a[240:243],v[172:175],v[140:143]
TR a[186:187],v233 offset:5632
CV v82,v84,v85
MM v[124:127],a[244:247],v[188:191],v[124:127]
EX v89,v89
MM v[124:127],a[248:251],v[204:207],v[124:127]
TR a[188:189],v233 offset:9728
TR a[190:191],v233 offset:13824
MM v[124:127],a[252:255],v[220:223],v[124:127]
EX v90,v90
MM v[120:123],a[236:239],v[220:223],v[140:143]
CV v83,v86,v87
CV v88,v88,v89
MM v[120:123],a[232:235],v[204:207],v[120:123]
EX v91,v91
MM v[120:123],a[228:231],v[188:191],v[120:123]
EX v92,v92
MM v[120:123],a[224:227],v[172:175],v[120:123]
s_waitcnt lgkmcnt(0)
s_barrier
MM a[0:3],a[128:131],v[0:3],a[0:3]
s_add_u32 m0, s65, 0
EX v93,v93
MM a[0:3],a[132:135],v[4:7],a[0:3]
CV v89,v90,v91
CV v90,v92,v93
MM a[8:11],a[132:135],v[12:15],a[8:11]
EX v94,v94
MM a[8:11],a[128:131],v[8:11],a[8:11]
EX v95,v95
MM a[16:19],a[128:131],v[16:19],a[16:19]
RD a[192:195],v224 offset:32768
CV v91,v94,v95
MM a[16:19],a[132:135],v[20:23],a[16:19]
EX v96,v96
MM a[24:27],a[132:135],v[28:31],a[24:27]
buffer_load_dwordx4 v228, s[8:11], s64 offen offset:0 lds
MM a[24:27],a[128:131],v[24:27],a[24:27]
EX v97,v97
MM a[28:31],a[136:139],v[24:27],a[28:31]
RD a[196:199],v225 offset:32768
CV v68,v96,v97
MM a[28:31],a[140:143],v[28:31],a[28:31]
EX v98,v98
MM a[20:23],a[140:143],v[20:23],a[20:23]
EX v99,v99
MM a[20:23],a[136:139],v[16:19],a[20:23]
RD a[200:203],v226 offset:32768
CV v69,v98,v99
MM a[12:15],a[136:139],v[8:11],a[12:15]
EX v100,v100
MM a[12:15],a[140:143],v[12:15],a[12:15]
buffer_load_dwordx4 v229, s[8:11], s64 offen offset:1024 lds
MM a[4:7],a[140:143],v[4:7],a[4:7]
EX v101,v101
MM a[4:7],a[136:139],v[0:3],a[4:7]
RD a[204:207],v227 offset:32768
CV v70,v100,v101
MM a[32:35],a[144:147],v[0:3],a[32:35]
EX v102,v102
MM a[32:35],a[148:151],v[4:7],a[32:35]
EX v103,v103
MM a[40:43],a[148:151],v[12:15],a[40:43]
RD a[208:211],v224 offset:36864
CV v71,v102,v103
MM a[40:43],a[144:147],v[8:11],a[40:43]
EX v104,v104
MM a[48:51],a[144:147],v[16:19],a[48:51]
buffer_load_dwordx4 v230, s[8:11], s64 offen offset:2048 lds
MM a[48:51],a[148:151],v[20:23],a[48:51]
EX v105,v105
MM a[56:59],a[148:151],v[28:31],a[56:59]
RD a[212:215],v225 offset:36864
CV v76,v104,v105
MM a[56:59],a[144:147],v[24:27],a[56:59]
EX v106,v106
MM a[60:63],a[152:155],v[24:27],a[60:63]
EX v107,v107
MM a[60:63],a[156:159],v[28:31],a[60:63]
RD a[216:219],v226 offset:36864
CV v77,v106,v107
MM a[52:55],a[156:159],v[20:23],a[52:55]
EX v108,v108
MM a[52:55],a[152:155],v[16:19],a[52:55]
buffer_load_dwordx4 v231, s[8:11], s64 offen offset:3072 lds
MM a[44:47],a[152:155],v[8:11],a[44:47]
s_add_u32 m0, s65, 65536
EX v109,v109
MM a[44:47],a[156:159],v[12:15],a[44:47]
RD a[220:223],v227 offset:36864
CV v78,v108,v109
MM a[36:39],a[156:159],v[4:7],a[36:39]
EX v110,v110
MM a[36:39],a[152:155],v[0:3],a[36:39]
RD a[224:227],v224 offset:40960
RD a[228:231],v225 offset:40960
MM a[64:67],a[160:163],v[0:3],a[64:67]
EX v111,v111
MM a[64:67],a[164:167],v[4:7],a[64:67]
RD a[232:235],v226 offset:40960
CV v79,v110,v111
MM a[72:75],a[164:167],v[12:15],a[72:75]
EX v112,v112
MM a[72:75],a[160:163],v[8:11],a[72:75]
buffer_load_dwordx4 v235, s[12:15], s81 offen offset:0 lds
MM a[80:83],a[160:163],v[16:19],a[80:83]
EX v113,v113
MM a[80:83],a[164:167],v[20:23],a[80:83]
RD a[236:239],v227 offset:40960
CV v84,v112,v113
MM a[88:91],a[164:167],v[28:31],a[88:91]
EX v114,v114
MM a[88:91],a[160:163],v[24:27],a[88:91]
EX v115,v115
MM a[92:95],a[168:171],v[24:27],a[92:95]
RD a[240:243],v224 offset:45056
CV v85,v114,v115
MM a[92:95],a[172:175],v[28:31],a[92:95]
EX v116,v116
MM a[84:87],a[172:175],v[20:23],a[84:87]
buffer_load_dwordx4 v235, s[12:15], s64 offen offset:1024 lds
MM a[84:87],a[168:171],v[16:19],a[84:87]
EX v117,v117
MM a[76:79],a[168:171],v[8:11],a[76:79]
RD a[244:247],v225 offset:45056
CV v86,v116,v117
MM a[76:79],a[172:175],v[12:15],a[76:79]
EX v118,v118
MM a[68:71],a[172:175],v[4:7],a[68:71]
EX v119,v119
MM a[68:71],a[168:171],v[0:3],a[68:71]
RD a[248:251],v226 offset:45056
CV v87,v118,v119
MM a[96:99],a[176:179],v[0:3],a[96:99]
EX v120,v120
MM a[96:99],a[180:183],v[4:7],a[96:99]
buffer_load_dwordx4 v235, s[12:15], s81 offen offset:2048 lds
MM a[104:107],a[180:183],v[12:15],a[104:107]
EX v121,v121
MM a[104:107],a[176:179],v[8:11],a[104:107]
RD a[252:255],v227 offset:45056
CV v92,v120,v121
MM a[112:115],a[176:179],v[16:19],a[112:115]
EX v122,v122
MM a[112:115],a[180:183],v[20:23],a[112:115]
EX v123,v123
MM a[120:123],a[180:183],v[28:31],a[120:123]
s_add_u32 s87, s95, 0
buffer_store_dword v243, v248, s[20:23], s87 offen offset:12 sc1
MM a[120:123],a[176:179],v[24:27],a[120:123]
EX v124,v124
MM a[124:127],a[184:187],v[24:27],a[124:127]
v_add_f32 v244, v144, v145
v_add_f32 v245, v148, v149
MM a[124:127],a[188:191],v[28:31],a[124:127]
)ASM"
R"ASM(
v_add_f32 v246, v152, v153
v_add_f32 v247, v156, v157
MM a[116:119],a[188:191],v[20:23],a[116:119]
EX v125,v125
MM a[116:119],a[184:187],v[16:19],a[116:119]
buffer_load_dwordx4 v235, s[12:15], s64 offen offset:3072 lds
MM a[108:111],a[184:187],v[8:11],a[108:111]
EX v126,v126
MM a[108:111],a[188:191],v[12:15],a[108:111]
EX v127,v127
MM a[100:103],a[188:191],v[4:7],a[100:103]
CV v93,v122,v123
CV v94,v124,v125
MM a[100:103],a[184:187],v[0:3],a[100:103]
CV v95,v126,v127
v_smfmac_f32_16x16x64_bf16 v[144:147], v[236:239], v[64:71], v234
v_smfmac_f32_16x16x64_bf16 v[148:151], v[236:239], v[72:79], v234
v_smfmac_f32_16x16x64_bf16 v[152:155], v[236:239], v[80:87], v234
v_smfmac_f32_16x16x64_bf16 v[156:159], v[236:239], v[88:95], v234
s_waitcnt lgkmcnt(0)
s_cmp_ge_u32 s16, 108
s_cbranch_scc1 LLAP_%=
MM v[0:3],a[192:195],v[160:163],v[128:131]
s_add_u32 s64, s64, 16384
TR a[128:129],v232 offset:16384
s_add_u32 s81, s64, 896
MM v[0:3],a[196:199],v[176:179],v[0:3]
TR a[130:131],v232 offset:20480
v_max3_f32 v249, v144, v145, v148
MM v[0:3],a[200:203],v[192:195],v[0:3]
TR a[132:133],v232 offset:24576
v_max3_f32 v249, v249, v149, v152
MM v[0:3],a[204:207],v[208:211],v[0:3]
TR a[134:135],v232 offset:28672
v_max3_f32 v249, v249, v153, v156
MM v[4:7],a[220:223],v[208:211],v[128:131]
TR a[136:137],v233 offset:16384
v_max_f32 v249, v249, v157
MM v[4:7],a[216:219],v[192:195],v[4:7]
TR a[138:139],v233 offset:20480
v_cmp_lt_f32 vcc, s71, v249
MM v[4:7],a[212:215],v[176:179],v[4:7]
TR a[140:141],v233 offset:24576
TR a[142:143],v233 offset:28672
MM v[4:7],a[208:211],v[160:163],v[4:7]
s_cbranch_vccnz LSITE13_%=
LBACK13_%=:
MM v[12:15],a[208:211],v[164:167],v[132:135]
v_lshrrev_b32 v127, 8, v248
v_lshlrev_b32 v124, 9, v127
MM v[12:15],a[212:215],v[180:183],v[12:15]
v_and_b32 v126, 192, v248
v_add_u32 v124, v124, v126
MM v[12:15],a[216:219],v[196:199],v[12:15]
v_bfe_u32 v126, v248, 4, 2
v_lshl_add_u32 v124, v126, 3, v124
MM v[12:15],a[220:223],v[212:215],v[12:15]
s_lshr_b32 s76, %[swave], 1
EX v0,v0
MM v[8:11],a[204:207],v[212:215],v[132:135]
s_mul_i32 s76, s76, 10240
EX v1,v1
MM v[8:11],a[200:203],v[196:199],v[8:11]
s_and_b32 s77, %[swave], 1
EX v2,v2
MM v[8:11],a[196:199],v[180:183],v[8:11]
s_lshl_b32 s77, s77, 5
TR a[144:145],v232 offset:16896
CV v0,v0,v1
MM v[8:11],a[192:195],v[164:167],v[8:11]
s_add_u32 s76, s76, s77
EX v3,v3
MM v[16:19],a[192:195],v[168:171],v[136:139]
s_add_u32 s76, s76, %[slds]
EX v4,v4
MM v[16:19],a[196:199],v[184:187],v[16:19]
s_add_u32 s76, s76, 131072
v_add_u32 v124, s76, v124
v_bfe_u32 v126, v248, 4, 3
MM v[16:19],a[200:203],v[200:203],v[16:19]
v_lshl_add_u32 v126, v127, 3, v126
v_lshlrev_b32 v126, 4, v126
MM v[16:19],a[204:207],v[216:219],v[16:19]
s_mul_i32 s76, %[swave], 3072
EX v5,v5
MM v[20:23],a[220:223],v[216:219],v[136:139]
s_add_u32 s76, s76, %[slds]
TR a[146:147],v232 offset:20992
CV v1,v2,v3
MM v[20:23],a[216:219],v[200:203],v[20:23]
s_add_u32 s76, s76, 151552
TR a[148:149],v232 offset:25088
v_add_u32 v125, s76, v126
MM v[20:23],a[212:215],v[184:187],v[20:23]
s_lshr_b32 s76, s48, 7
EX v6,v6
MM v[20:23],a[208:211],v[168:171],v[20:23]
s_lshl_b32 s76, s76, 5
EX v7,v7
MM v[28:31],a[208:211],v[172:175],v[140:143]
s_and_b32 s77, s48, 16
TR a[150:151],v232 offset:29184
TR a[152:153],v233 offset:16896
MM v[28:31],a[212:215],v[188:191],v[28:31]
s_add_u32 s76, s76, s77
EX v8,v8
MM v[28:31],a[216:219],v[204:207],v[28:31]
s_bfe_u32 s77, s48, 131077
TR a[154:155],v233 offset:20992
CV v2,v4,v5
MM v[28:31],a[220:223],v[220:223],v[28:31]
s_lshl_b32 s77, s77, 2
EX v9,v9
s_nop 0
LPG12_%=:
s_cbranch_execz LTR0_%=
MM v[24:27],a[204:207],v[220:223],v[140:143]
s_add_u32 s76, s76, s77
TR a[156:157],v233 offset:25088
CV v3,v6,v7
MM v[24:27],a[200:203],v[204:207],v[24:27]
s_add_u32 s76, s76, %[swave]
EX v10,v10
MM v[24:27],a[196:199],v[188:191],v[24:27]
s_lshl_b32 s97, s76, 2
TR a[158:159],v233 offset:29184
CV v8,v8,v9
MM v[24:27],a[192:195],v[172:175],v[24:27]
s_add_u32 s97, s97, 77824
EX v11,v11
MM v[32:35],a[224:227],v[160:163],v[128:131]
s_lshl_b32 s76, s76, 9
TR a[160:161],v232 offset:17408
TR a[162:163],v232 offset:21504
MM v[32:35],a[228:231],v[176:179],v[32:35]
s_add_u32 s76, s76, 22183936
EX v12,v12
MM v[32:35],a[232:235],v[192:195],v[32:35]
EX v13,v13
MM v[32:35],a[236:239],v[208:211],v[32:35]
TR a[164:165],v232 offset:25600
CV v9,v10,v11
MM v[36:39],a[252:255],v[208:211],v[128:131]
EX v14,v14
MM v[36:39],a[248:251],v[192:195],v[36:39]
TR a[166:167],v232 offset:29696
CV v10,v12,v13
MM v[36:39],a[244:247],v[176:179],v[36:39]
EX v15,v15
MM v[36:39],a[240:243],v[160:163],v[36:39]
TR a[168:169],v233 offset:17408
TR a[170:171],v233 offset:21504
MM v[44:47],a[240:243],v[164:167],v[132:135]
EX v16,v16
MM v[44:47],a[244:247],v[180:183],v[44:47]
EX v17,v17
MM v[44:47],a[248:251],v[196:199],v[44:47]
TR a[172:173],v233 offset:25600
CV v11,v14,v15
MM v[44:47],a[252:255],v[212:215],v[44:47]
EX v18,v18
MM v[40:43],a[236:239],v[212:215],v[132:135]
TR a[174:175],v233 offset:29696
CV v16,v16,v17
MM v[40:43],a[232:235],v[196:199],v[40:43]
EX v19,v19
MM v[40:43],a[228:231],v[180:183],v[40:43]
TR a[176:177],v232 offset:17920
TR a[178:179],v232 offset:22016
MM v[40:43],a[224:227],v[164:167],v[40:43]
EX v20,v20
MM v[48:51],a[224:227],v[168:171],v[136:139]
EX v21,v21
MM v[48:51],a[228:231],v[184:187],v[48:51]
TR a[180:181],v232 offset:26112
CV v17,v18,v19
MM v[48:51],a[232:235],v[200:203],v[48:51]
EX v22,v22
MM v[48:51],a[236:239],v[216:219],v[48:51]
TR a[182:183],v232 offset:30208
CV v18,v20,v21
MM v[52:55],a[252:255],v[216:219],v[136:139]
EX v23,v23
MM v[52:55],a[248:251],v[200:203],v[52:55]
TR a[184:185],v233 offset:17920
TR a[186:187],v233 offset:22016
MM v[52:55],a[244:247],v[184:187],v[52:55]
EX v24,v24
MM v[52:55],a[240:243],v[168:171],v[52:55]
TR a[188:189],v233 offset:26112
CV v19,v22,v23
MM v[60:63],a[240:243],v[172:175],v[140:143]
EX v25,v25
MM v[60:63],a[244:247],v[188:191],v[60:63]
TR a[190:191],v233 offset:30208
CV v24,v24,v25
MM v[60:63],a[248:251],v[204:207],v[60:63]
EX v26,v26
MM v[60:63],a[252:255],v[220:223],v[60:63]
EX v27,v27
MM v[56:59],a[236:239],v[220:223],v[140:143]
EX v28,v28
MM v[56:59],a[232:235],v[204:207],v[56:59]
EX v29,v29
MM v[56:59],a[228:231],v[188:191],v[56:59]
s_getpc_b64 s[98:99]
LTA2_%=:
s_min_u32 s73, %[swave], 3
s_lshl_b32 s73, s73, 12
s_add_u32 s73, s73, LPG13_%=-LTA2_%=
s_add_u32 s98, s98, s73
s_addc_u32 s99, s99, 0
s_mov_b64 exec, 0
s_setpc_b64 s[98:99]
LTR2_%=:
s_mov_b64 exec, -1
CV v25,v26,v27
MM v[56:59],a[224:227],v[172:175],v[56:59]
s_waitcnt vmcnt(17) lgkmcnt(0)
s_barrier
MM a[0:3],a[128:131],v[64:67],a[0:3]
s_add_u32 m0, s65, 16384
RD a[192:195],v224 offset:49152
CV v26,v28,v29
MM a[0:3],a[132:135],v[68:71],a[0:3]
EX v30,v30
MM a[8:11],a[132:135],v[76:79],a[8:11]
EX v31,v31
MM a[8:11],a[128:131],v[72:75],a[8:11]
RD a[196:199],v225 offset:49152
CV v27,v30,v31
MM a[16:19],a[128:131],v[80:83],a[16:19]
s_waitcnt vmcnt(10)
TR v[96:97],v124 offset:0
TR v[98:99],v124 offset:256
MM a[16:19],a[132:135],v[84:87],a[16:19]
RD v[108:111],v125 offset:0
TR v[100:101],v124 offset:2048
MM a[24:27],a[132:135],v[92:95],a[24:27]
TR v[102:103],v124 offset:2304
RD v[112:115],v125 offset:512
MM a[24:27],a[128:131],v[88:91],a[24:27]
TR v[104:105],v124 offset:4096
TR v[106:107],v124 offset:4352
MM a[28:31],a[136:139],v[88:91],a[28:31]
RD v[116:119],v125 offset:1024
RD a[200:203],v226 offset:49152
EX v32,v32
MM a[28:31],a[140:143],v[92:95],a[28:31]
EX v33,v33
MM a[20:23],a[140:143],v[84:87],a[20:23]
EX v34,v34
MM a[20:23],a[136:139],v[80:83],a[20:23]
buffer_load_dwordx4 v228, s[8:11], s64 offen offset:0 lds
MM a[12:15],a[136:139],v[72:75],a[12:15]
EX v35,v35
MM a[12:15],a[140:143],v[76:79],a[12:15]
EX v36,v36
MM a[4:7],a[140:143],v[68:71],a[4:7]
CV v4,v32,v33
CV v5,v34,v35
MM a[4:7],a[136:139],v[64:67],a[4:7]
EX v37,v37
MM a[32:35],a[144:147],v[64:67],a[32:35]
EX v38,v38
MM a[32:35],a[148:151],v[68:71],a[32:35]
s_waitcnt lgkmcnt(1)
buffer_load_dwordx4 v229, s[8:11], s64 offen offset:1024 lds
MM a[40:43],a[148:151],v[76:79],a[40:43]
EX v39,v39
MM v[120:123],v[96:99],v[108:111],0
MM v[120:123],v[100:103],v[112:115],v[120:123]
MM v[120:123],v[104:107],v[116:119],v[120:123]
MM a[40:43],a[144:147],v[72:75],a[40:43]
TR v[96:97],v124 offset:6144
TR v[98:99],v124 offset:6400
MM a[48:51],a[144:147],v[80:83],a[48:51]
RD v[108:111],v125 offset:1536
TR v[100:101],v124 offset:8192
MM a[48:51],a[148:151],v[84:87],a[48:51]
TR v[102:103],v124 offset:8448
RD v[112:115],v125 offset:2048
MM a[56:59],a[148:151],v[92:95],a[56:59]
EX v40,v40
MM a[56:59],a[144:147],v[88:91],a[56:59]
EX v41,v41
MM a[60:63],a[152:155],v[88:91],a[60:63]
RD a[204:207],v227 offset:49152
CV v6,v36,v37
MM a[60:63],a[156:159],v[92:95],a[60:63]
EX v42,v42
MM a[52:55],a[156:159],v[84:87],a[52:55]
RD a[208:211],v224 offset:53248
CV v7,v38,v39
MM a[52:55],a[152:155],v[80:83],a[52:55]
EX v43,v43
MM a[44:47],a[152:155],v[72:75],a[44:47]
buffer_load_dwordx4 v230, s[8:11], s64 offen offset:2048 lds
MM a[44:47],a[156:159],v[76:79],a[44:47]
EX v44,v44
MM a[36:39],a[156:159],v[68:71],a[36:39]
s_waitcnt lgkmcnt(2)
EX v45,v45
MM a[36:39],a[152:155],v[64:67],a[36:39]
RD a[212:215],v225 offset:53248
CV v12,v40,v41
MM v[120:123],v[96:99],v[108:111],v[120:123]
MM v[120:123],v[100:103],v[112:115],v[120:123]
MM a[64:67],a[160:163],v[64:67],a[64:67]
EX v46,v46
MM a[64:67],a[164:167],v[68:71],a[64:67]
RD a[216:219],v226 offset:53248
CV v13,v42,v43
MM a[72:75],a[164:167],v[76:79],a[72:75]
EX v47,v47
MM a[72:75],a[160:163],v[72:75],a[72:75]
buffer_load_dwordx4 v231, s[8:11], s64 offen offset:3072 lds
MM a[80:83],a[160:163],v[80:83],a[80:83]
s_add_u32 m0, s65, 81920
EX v48,v48
MM a[80:83],a[164:167],v[84:87],a[80:83]
RD a[220:223],v227 offset:53248
CV v14,v44,v45
MM a[88:91],a[164:167],v[92:95],a[88:91]
s_mov_b32 exec_lo, 0x00ff00ff
s_mov_b32 exec_hi, 0x00ff00ff
buffer_store_dwordx4 v[120:123], v126, s[20:23], s76 offen offset:0 sc1
s_mov_b64 exec, -1
s_nop 0
MM a[88:91],a[160:163],v[88:91],a[88:91]
EX v49,v49
MM a[92:95],a[168:171],v[88:91],a[92:95]
EX v50,v50
MM a[92:95],a[172:175],v[92:95],a[92:95]
RD a[224:227],v224 offset:57344
CV v15,v46,v47
MM a[84:87],a[172:175],v[84:87],a[84:87]
EX v51,v51
MM a[84:87],a[168:171],v[80:83],a[84:87]
EX v52,v52
MM a[76:79],a[168:171],v[72:75],a[76:79]
buffer_load_dwordx4 v235, s[12:15], s81 offen offset:0 lds
MM a[76:79],a[172:175],v[76:79],a[76:79]
EX v53,v53
MM a[68:71],a[172:175],v[68:71],a[68:71]
RD a[228:231],v225 offset:57344
CV v20,v48,v49
MM a[68:71],a[168:171],v[64:67],a[68:71]
EX v54,v54
MM a[96:99],a[176:179],v[64:67],a[96:99]
RD a[232:235],v226 offset:57344
CV v21,v50,v51
MM a[96:99],a[180:183],v[68:71],a[96:99]
EX v55,v55
MM a[104:107],a[180:183],v[76:79],a[104:107]
RD a[236:239],v227 offset:57344
CV v22,v52,v53
MM a[104:107],a[176:179],v[72:75],a[104:107]
EX v56,v56
MM a[112:115],a[176:179],v[80:83],a[112:115]
buffer_load_dwordx4 v235, s[12:15], s64 offen offset:1024 lds
MM a[112:115],a[180:183],v[84:87],a[112:115]
EX v57,v57
MM a[120:123],a[180:183],v[92:95],a[120:123]
EX v58,v58
MM a[120:123],a[176:179],v[88:91],a[120:123]
RD a[240:243],v224 offset:61440
CV v23,v54,v55
MM a[124:127],a[184:187],v[88:91],a[124:127]
v_add_f32 v244, v144, v145
v_add_f32 v245, v148, v149
MM a[124:127],a[188:191],v[92:95],a[124:127]
v_add_f32 v246, v152, v153
v_add_f32 v247, v156, v157
MM a[116:119],a[188:191],v[84:87],a[116:119]
EX v59,v59
MM a[116:119],a[184:187],v[80:83],a[116:119]
EX v60,v60
MM a[108:111],a[184:187],v[72:75],a[108:111]
EX v61,v61
MM a[108:111],a[188:191],v[76:79],a[108:111]
RD a[244:247],v225 offset:61440
CV v28,v56,v57
MM a[100:103],a[188:191],v[68:71],a[100:103]
buffer_load_dwordx4 v235, s[12:15], s81 offen offset:2048 lds
MM a[100:103],a[184:187],v[64:67],a[100:103]
EX v62,v62
v_smfmac_f32_16x16x64_bf16 v[144:147], v[236:239], v[0:7], v234
RD a[248:251],v226 offset:61440
RD a[252:255],v227 offset:61440
EX v63,v63
v_smfmac_f32_16x16x64_bf16 v[148:151], v[236:239], v[8:15], v234
CV v29,v58,v59
v_smfmac_f32_16x16x64_bf16 v[152:155], v[236:239], v[16:23], v234
CV v30,v60,v61
CV v31,v62,v63
s_nop 1
)ASM"
R"ASM(
v_smfmac_f32_16x16x64_bf16 v[156:159], v[236:239], v[24:31], v234
s_waitcnt lgkmcnt(0)
buffer_load_dwordx4 v235, s[12:15], s64 offen offset:3072 lds
MM v[64:67],a[192:195],v[160:163],v[128:131]
s_add_u32 s64, s64, 16384
TR a[128:129],v232 offset:32768
s_add_u32 s81, s64, 896
MM v[64:67],a[196:199],v[176:179],v[64:67]
TR a[130:131],v232 offset:36864
v_max3_f32 v249, v144, v145, v148
MM v[64:67],a[200:203],v[192:195],v[64:67]
TR a[132:133],v232 offset:40960
v_max3_f32 v249, v249, v149, v152
MM v[64:67],a[204:207],v[208:211],v[64:67]
TR a[134:135],v232 offset:45056
v_max3_f32 v249, v249, v153, v156
MM v[68:71],a[220:223],v[208:211],v[128:131]
TR a[136:137],v233 offset:32768
v_max_f32 v249, v249, v157
MM v[68:71],a[216:219],v[192:195],v[68:71]
TR a[138:139],v233 offset:36864
v_cmp_lt_f32 vcc, s71, v249
MM v[68:71],a[212:215],v[176:179],v[68:71]
TR a[140:141],v233 offset:40960
TR a[142:143],v233 offset:45056
MM v[68:71],a[208:211],v[160:163],v[68:71]
s_cbranch_vccnz LSITE14_%=
LBACK14_%=:
MM v[76:79],a[208:211],v[164:167],v[132:135]
s_add_u32 s48, s48, 16
v_lshrrev_b32 v252, 6, v248
v_mul_u32_u24 v252, 3456, v252
MM v[76:79],a[212:215],v[180:183],v[76:79]
v_and_b32 v251, 48, v248
v_add_u32 v252, v252, v251
MM v[76:79],a[216:219],v[196:199],v[76:79]
s_and_b32 s90, %[swave], 1
EX v64,v64
MM v[76:79],a[220:223],v[212:215],v[76:79]
s_mul_i32 s90, s90, 276480
EX v65,v65
MM v[72:75],a[204:207],v[212:215],v[132:135]
s_bfe_u32 s91, s48, 131077
EX v66,v66
MM v[72:75],a[200:203],v[196:199],v[72:75]
s_mul_i32 s91, s91, 552960
TR a[144:145],v232 offset:33280
CV v64,v64,v65
MM v[72:75],a[196:199],v[180:183],v[72:75]
s_add_u32 s90, s90, s91
EX v67,v67
MM v[72:75],a[192:195],v[164:167],v[72:75]
s_and_b32 s91, s48, 0xffffff80
TR a[146:147],v232 offset:37376
CV v65,v66,v67
MM v[80:83],a[192:195],v[168:171],v[136:139]
s_add_u32 s90, s90, s91
EX v68,v68
MM v[80:83],a[196:199],v[184:187],v[80:83]
s_and_b32 s91, s48, 16
TR a[148:149],v232 offset:41472
TR a[150:151],v232 offset:45568
MM v[80:83],a[200:203],v[200:203],v[80:83]
s_lshl_b32 s91, s91, 2
EX v69,v69
MM v[80:83],a[204:207],v[216:219],v[80:83]
s_add_u32 s66, s90, s91
TR a[152:153],v233 offset:33280
CV v66,v68,v69
MM v[84:87],a[220:223],v[216:219],v[136:139]
s_mul_i32 s91, %[swave], 5120
EX v70,v70
MM v[84:87],a[216:219],v[200:203],v[84:87]
s_add_u32 s91, s91, %[slds]
EX v71,v71
MM v[84:87],a[212:215],v[184:187],v[84:87]
s_add_u32 s67, s91, 131072
TR a[154:155],v233 offset:37376
CV v67,v70,v71
MM v[84:87],a[208:211],v[168:171],v[84:87]
s_and_b32 s18, s17, 255
EX v72,v72
MM v[92:95],a[208:211],v[172:175],v[140:143]
s_sub_u32 s90, s18, 40
s_cmp_ge_u32 s18, 216
s_cselect_b32 s18, s90, s18
MM v[92:95],a[212:215],v[188:191],v[92:95]
s_lshl_b32 s18, s18, 4
EX v73,v73
MM v[92:95],a[216:219],v[204:207],v[92:95]
s_sub_u32 s90, s16, 90
TR a[156:157],v233 offset:41472
CV v72,v72,v73
MM v[92:95],a[220:223],v[220:223],v[92:95]
s_min_u32 s47, s90, s16
EX v74,v74
MM v[88:91],a[204:207],v[220:223],v[140:143]
s_mul_i32 s90, s47, 205
TR a[158:159],v233 offset:45568
TR a[160:161],v232 offset:33792
MM v[88:91],a[200:203],v[204:207],v[88:91]
s_lshr_b32 s46, s90, 11
EX v75,v75
MM v[88:91],a[196:199],v[188:191],v[88:91]
s_mul_i32 s90, s46, 244480
TR a[162:163],v232 offset:37888
CV v73,v74,v75
MM v[88:91],a[192:195],v[172:175],v[88:91]
s_lshl_b32 s91, s47, 7
EX v76,v76
MM v[96:99],a[224:227],v[160:163],v[128:131]
s_add_u32 s90, s90, s91
s_add_u32 s28, s28, s90
s_addc_u32 s29, s29, 0
MM v[96:99],a[228:231],v[176:179],v[96:99]
s_sub_u32 s30, 2211840, s90
EX v77,v77
MM v[96:99],a[232:235],v[192:195],v[96:99]
s_mov_b32 s31, 131072
TR a[164:165],v232 offset:41984
CV v74,v76,v77
MM v[96:99],a[236:239],v[208:211],v[96:99]
EX v78,v78
MM v[100:103],a[252:255],v[208:211],v[128:131]
TR a[166:167],v232 offset:46080
TR a[168:169],v233 offset:33792
MM v[100:103],a[248:251],v[192:195],v[100:103]
EX v79,v79
MM v[100:103],a[244:247],v[176:179],v[100:103]
TR a[170:171],v233 offset:37888
CV v75,v78,v79
MM v[100:103],a[240:243],v[160:163],v[100:103]
EX v80,v80
MM v[108:111],a[240:243],v[164:167],v[132:135]
TR a[172:173],v233 offset:41984
TR a[174:175],v233 offset:46080
MM v[108:111],a[244:247],v[180:183],v[108:111]
EX v81,v81
MM v[108:111],a[248:251],v[196:199],v[108:111]
EX v82,v82
MM v[108:111],a[252:255],v[212:215],v[108:111]
TR a[176:177],v232 offset:34304
CV v80,v80,v81
MM v[104:107],a[236:239],v[212:215],v[132:135]
EX v83,v83
MM v[104:107],a[232:235],v[196:199],v[104:107]
TR a[178:179],v232 offset:38400
CV v81,v82,v83
MM v[104:107],a[228:231],v[180:183],v[104:107]
EX v84,v84
MM v[104:107],a[224:227],v[164:167],v[104:107]
TR a[180:181],v232 offset:42496
TR a[182:183],v232 offset:46592
MM v[112:115],a[224:227],v[168:171],v[136:139]
EX v85,v85
MM v[112:115],a[228:231],v[184:187],v[112:115]
TR a[184:185],v233 offset:34304
CV v82,v84,v85
MM v[112:115],a[232:235],v[200:203],v[112:115]
EX v86,v86
MM v[112:115],a[236:239],v[216:219],v[112:115]
EX v87,v87
MM v[116:119],a[252:255],v[216:219],v[136:139]
TR a[186:187],v233 offset:38400
CV v83,v86,v87
MM v[116:119],a[248:251],v[200:203],v[116:119]
EX v88,v88
MM v[116:119],a[244:247],v[184:187],v[116:119]
TR a[188:189],v233 offset:42496
TR a[190:191],v233 offset:46592
MM v[116:119],a[240:243],v[168:171],v[116:119]
EX v89,v89
MM v[124:127],a[240:243],v[172:175],v[140:143]
EX v90,v90
MM v[124:127],a[244:247],v[188:191],v[124:127]
EX v91,v91
MM v[124:127],a[248:251],v[204:207],v[124:127]
EX v92,v92
MM v[124:127],a[252:255],v[220:223],v[124:127]
EX v93,v93
MM v[120:123],a[236:239],v[220:223],v[140:143]
EX v94,v94
MM v[120:123],a[232:235],v[204:207],v[120:123]
EX v95,v95
MM v[120:123],a[228:231],v[188:191],v[120:123]
EX v96,v96
MM v[120:123],a[224:227],v[172:175],v[120:123]
s_waitcnt vmcnt(14) lgkmcnt(0)
s_barrier
MM a[0:3],a[128:131],v[0:3],a[0:3]
s_add_u32 m0, s65, 32768
CV v88,v88,v89
CV v89,v90,v91
MM a[0:3],a[132:135],v[4:7],a[0:3]
RD a[192:195],v224 offset:0
CV v90,v92,v93
MM a[8:11],a[132:135],v[12:15],a[8:11]
RD a[196:199],v225 offset:0
CV v91,v94,v95
MM a[8:11],a[128:131],v[8:11],a[8:11]
buffer_load_dwordx4 v228, s[8:11], s64 offen offset:0 lds
MM a[16:19],a[128:131],v[16:19],a[16:19]
RD a[200:203],v226 offset:0
RD a[204:207],v227 offset:0
MM a[16:19],a[132:135],v[20:23],a[16:19]
EX v97,v97
MM a[24:27],a[132:135],v[28:31],a[24:27]
RD a[208:211],v224 offset:4096
CV v68,v96,v97
MM a[24:27],a[128:131],v[24:27],a[24:27]
EX v98,v98
MM a[28:31],a[136:139],v[24:27],a[28:31]
buffer_load_dwordx4 v229, s[8:11], s64 offen offset:1024 lds
MM a[28:31],a[140:143],v[28:31],a[28:31]
EX v99,v99
MM a[20:23],a[140:143],v[20:23],a[20:23]
RD a[212:215],v225 offset:4096
CV v69,v98,v99
MM a[20:23],a[136:139],v[16:19],a[20:23]
EX v100,v100
MM a[12:15],a[136:139],v[8:11],a[12:15]
EX v101,v101
MM a[12:15],a[140:143],v[12:15],a[12:15]
RD a[216:219],v226 offset:4096
CV v70,v100,v101
MM a[4:7],a[140:143],v[4:7],a[4:7]
EX v102,v102
MM a[4:7],a[136:139],v[0:3],a[4:7]
buffer_load_dwordx4 v230, s[8:11], s64 offen offset:2048 lds
MM a[32:35],a[144:147],v[0:3],a[32:35]
EX v103,v103
MM a[32:35],a[148:151],v[4:7],a[32:35]
RD a[220:223],v227 offset:4096
CV v71,v102,v103
MM a[40:43],a[148:151],v[12:15],a[40:43]
EX v104,v104
MM a[40:43],a[144:147],v[8:11],a[40:43]
EX v105,v105
MM a[48:51],a[144:147],v[16:19],a[48:51]
RD a[224:227],v224 offset:8192
CV v76,v104,v105
MM a[48:51],a[148:151],v[20:23],a[48:51]
EX v106,v106
MM a[56:59],a[148:151],v[28:31],a[56:59]
buffer_load_dwordx4 v231, s[8:11], s64 offen offset:3072 lds
MM a[56:59],a[144:147],v[24:27],a[56:59]
s_add_u32 m0, s65, 98304
EX v107,v107
MM a[60:63],a[152:155],v[24:27],a[60:63]
RD a[228:231],v225 offset:8192
CV v77,v106,v107
MM a[60:63],a[156:159],v[28:31],a[60:63]
EX v108,v108
MM a[52:55],a[156:159],v[20:23],a[52:55]
EX v109,v109
MM a[52:55],a[152:155],v[16:19],a[52:55]
RD a[232:235],v226 offset:8192
CV v78,v108,v109
MM a[44:47],a[152:155],v[8:11],a[44:47]
EX v110,v110
MM a[44:47],a[156:159],v[12:15],a[44:47]
buffer_load_dwordx4 v235, s[12:15], s81 offen offset:0 lds
MM a[36:39],a[156:159],v[4:7],a[36:39]
EX v111,v111
MM a[36:39],a[152:155],v[0:3],a[36:39]
RD a[236:239],v227 offset:8192
CV v79,v110,v111
MM a[64:67],a[160:163],v[0:3],a[64:67]
EX v112,v112
MM a[64:67],a[164:167],v[4:7],a[64:67]
EX v113,v113
MM a[72:75],a[164:167],v[12:15],a[72:75]
RD a[240:243],v224 offset:12288
CV v84,v112,v113
MM a[72:75],a[160:163],v[8:11],a[72:75]
buffer_load_dwordx4 v235, s[12:15], s64 offen offset:1024 lds
MM a[80:83],a[160:163],v[16:19],a[80:83]
s_waitcnt vmcnt(10)
v_mov_b32 v250, s69
s_mov_b64 exec, 1
buffer_store_dword v250, off, s[20:23], s97 offset:0 sc1
s_mov_b64 exec, -1
MM a[80:83],a[164:167],v[20:23],a[80:83]
s_add_u32 m0, s67, 0
s_add_u32 s85, s66, 0
buffer_load_dwordx4 v252, s[36:39], s85 offen offset:0 lds
MM a[88:91],a[164:167],v[28:31],a[88:91]
EX v114,v114
MM a[88:91],a[160:163],v[24:27],a[88:91]
EX v115,v115
MM a[92:95],a[168:171],v[24:27],a[92:95]
RD a[244:247],v225 offset:12288
CV v85,v114,v115
MM a[92:95],a[172:175],v[28:31],a[92:95]
EX v116,v116
MM a[84:87],a[172:175],v[20:23],a[84:87]
EX v117,v117
MM a[84:87],a[168:171],v[16:19],a[84:87]
RD a[248:251],v226 offset:12288
CV v86,v116,v117
MM a[76:79],a[168:171],v[8:11],a[76:79]
EX v118,v118
MM a[76:79],a[172:175],v[12:15],a[76:79]
EX v119,v119
MM a[68:71],a[172:175],v[4:7],a[68:71]
RD a[252:255],v227 offset:12288
CV v87,v118,v119
MM a[68:71],a[168:171],v[0:3],a[68:71]
s_add_u32 m0, s65, 98304
s_nop 0
buffer_load_dwordx4 v235, s[12:15], s81 offen offset:2048 lds
MM a[96:99],a[176:179],v[0:3],a[96:99]
EX v120,v120
MM a[96:99],a[180:183],v[4:7],a[96:99]
EX v121,v121
MM a[104:107],a[180:183],v[12:15],a[104:107]
EX v122,v122
MM a[104:107],a[176:179],v[8:11],a[104:107]
s_nop 0
LPG13_%=:
s_cbranch_execz LTR2_%=
buffer_load_dwordx4 v235, s[12:15], s64 offen offset:3072 lds
MM a[112:115],a[176:179],v[16:19],a[112:115]
EX v123,v123
MM a[112:115],a[180:183],v[20:23],a[112:115]
EX v124,v124
MM a[120:123],a[180:183],v[28:31],a[120:123]
EX v125,v125
MM a[120:123],a[176:179],v[24:27],a[120:123]
EX v126,v126
MM a[124:127],a[184:187],v[24:27],a[124:127]
v_add_f32 v244, v144, v145
v_add_f32 v245, v148, v149
MM a[124:127],a[188:191],v[28:31],a[124:127]
v_add_f32 v246, v152, v153
v_add_f32 v247, v156, v157
MM a[116:119],a[188:191],v[20:23],a[116:119]
EX v127,v127
MM a[116:119],a[184:187],v[16:19],a[116:119]
CV v92,v120,v121
CV v93,v122,v123
MM a[108:111],a[184:187],v[8:11],a[108:111]
CV v94,v124,v125
CV v95,v126,v127
MM a[108:111],a[188:191],v[12:15],a[108:111]
MM a[100:103],a[188:191],v[4:7],a[100:103]
MM a[100:103],a[184:187],v[0:3],a[100:103]
v_smfmac_f32_16x16x64_bf16 v[144:147], v[236:239], v[64:71], v234
v_smfmac_f32_16x16x64_bf16 v[148:151], v[236:239], v[72:79], v234
v_smfmac_f32_16x16x64_bf16 v[152:155], v[236:239], v[80:87], v234
v_smfmac_f32_16x16x64_bf16 v[156:159], v[236:239], v[88:95], v234
s_waitcnt lgkmcnt(0)
MM v[0:3],a[192:195],v[160:163],v[128:131]
s_add_u32 s64, s64, 16384
TR a[128:129],v232 offset:49152
s_add_u32 s81, s64, 896
MM v[0:3],a[196:199],v[176:179],v[0:3]
TR a[130:131],v232 offset:53248
v_max3_f32 v249, v144, v145, v148
MM v[0:3],a[200:203],v[192:195],v[0:3]
TR a[132:133],v232 offset:57344
v_max3_f32 v249, v249, v149, v152
MM v[0:3],a[204:207],v[208:211],v[0:3]
TR a[134:135],v232 offset:61440
v_max3_f32 v249, v249, v153, v156
MM v[4:7],a[220:223],v[208:211],v[128:131]
TR a[136:137],v233 offset:49152
v_max_f32 v249, v249, v157
MM v[4:7],a[216:219],v[192:195],v[4:7]
TR a[138:139],v233 offset:53248
v_cmp_lt_f32 vcc, s71, v249
MM v[4:7],a[212:215],v[176:179],v[4:7]
TR a[140:141],v233 offset:57344
TR a[142:143],v233 offset:61440
MM v[4:7],a[208:211],v[160:163],v[4:7]
s_cbranch_vccnz LSITE15_%=
LBACK15_%=:
MM v[12:15],a[208:211],v[164:167],v[132:135]
s_add_u32 m0, s67, 1024
s_add_u32 s85, s66, 55296
buffer_load_dwordx4 v252, s[36:39], s85 offen offset:0 lds
MM v[12:15],a[212:215],v[180:183],v[12:15]
EX v0,v0
MM v[12:15],a[216:219],v[196:199],v[12:15]
EX v1,v1
MM v[12:15],a[220:223],v[212:215],v[12:15]
TR a[144:145],v232 offset:49664
CV v0,v0,v1
MM v[8:11],a[204:207],v[212:215],v[132:135]
)ASM"
R"ASM(
EX v2,v2
MM v[8:11],a[200:203],v[196:199],v[8:11]
EX v3,v3
MM v[8:11],a[196:199],v[180:183],v[8:11]
TR a[146:147],v232 offset:53760
CV v1,v2,v3
MM v[8:11],a[192:195],v[164:167],v[8:11]
EX v4,v4
MM v[16:19],a[192:195],v[168:171],v[136:139]
TR a[148:149],v232 offset:57856
TR a[150:151],v232 offset:61952
MM v[16:19],a[196:199],v[184:187],v[16:19]
EX v5,v5
MM v[16:19],a[200:203],v[200:203],v[16:19]
TR a[152:153],v233 offset:49664
CV v2,v4,v5
MM v[16:19],a[204:207],v[216:219],v[16:19]
EX v6,v6
MM v[20:23],a[220:223],v[216:219],v[136:139]
EX v7,v7
MM v[20:23],a[216:219],v[200:203],v[20:23]
TR a[154:155],v233 offset:53760
CV v3,v6,v7
MM v[20:23],a[212:215],v[184:187],v[20:23]
EX v8,v8
MM v[20:23],a[208:211],v[168:171],v[20:23]
TR a[156:157],v233 offset:57856
TR a[158:159],v233 offset:61952
MM v[28:31],a[208:211],v[172:175],v[140:143]
EX v9,v9
MM v[28:31],a[212:215],v[188:191],v[28:31]
TR a[160:161],v232 offset:50176
CV v8,v8,v9
MM v[28:31],a[216:219],v[204:207],v[28:31]
EX v10,v10
MM v[28:31],a[220:223],v[220:223],v[28:31]
EX v11,v11
MM v[24:27],a[204:207],v[220:223],v[140:143]
TR a[162:163],v232 offset:54272
CV v9,v10,v11
MM v[24:27],a[200:203],v[204:207],v[24:27]
TR a[164:165],v232 offset:58368
TR a[166:167],v232 offset:62464
MM v[24:27],a[196:199],v[188:191],v[24:27]
EX v12,v12
MM v[24:27],a[192:195],v[172:175],v[24:27]
EX v13,v13
MM v[32:35],a[224:227],v[160:163],v[128:131]
TR a[168:169],v233 offset:50176
CV v10,v12,v13
MM v[32:35],a[228:231],v[176:179],v[32:35]
EX v14,v14
MM v[32:35],a[232:235],v[192:195],v[32:35]
TR a[170:171],v233 offset:54272
TR a[172:173],v233 offset:58368
MM v[32:35],a[236:239],v[208:211],v[32:35]
EX v15,v15
MM v[36:39],a[252:255],v[208:211],v[128:131]
TR a[174:175],v233 offset:62464
CV v11,v14,v15
MM v[36:39],a[248:251],v[192:195],v[36:39]
EX v16,v16
MM v[36:39],a[244:247],v[176:179],v[36:39]
EX v17,v17
MM v[36:39],a[240:243],v[160:163],v[36:39]
TR a[176:177],v232 offset:50688
CV v16,v16,v17
MM v[44:47],a[240:243],v[164:167],v[132:135]
s_add_u32 m0, s67, 2048
s_add_u32 s85, s66, 110592
buffer_load_dwordx4 v252, s[36:39], s85 offen offset:0 lds
MM v[44:47],a[244:247],v[180:183],v[44:47]
EX v18,v18
MM v[44:47],a[248:251],v[196:199],v[44:47]
TR a[178:179],v232 offset:54784
TR a[180:181],v232 offset:58880
MM v[44:47],a[252:255],v[212:215],v[44:47]
EX v19,v19
MM v[40:43],a[236:239],v[212:215],v[132:135]
TR a[182:183],v232 offset:62976
CV v17,v18,v19
MM v[40:43],a[232:235],v[196:199],v[40:43]
EX v20,v20
MM v[40:43],a[228:231],v[180:183],v[40:43]
EX v21,v21
MM v[40:43],a[224:227],v[164:167],v[40:43]
TR a[184:185],v233 offset:50688
CV v18,v20,v21
MM v[48:51],a[224:227],v[168:171],v[136:139]
EX v22,v22
MM v[48:51],a[228:231],v[184:187],v[48:51]
TR a[186:187],v233 offset:54784
TR a[188:189],v233 offset:58880
MM v[48:51],a[232:235],v[200:203],v[48:51]
EX v23,v23
MM v[48:51],a[236:239],v[216:219],v[48:51]
TR a[190:191],v233 offset:62976
CV v19,v22,v23
MM v[52:55],a[252:255],v[216:219],v[136:139]
EX v24,v24
MM v[52:55],a[248:251],v[200:203],v[52:55]
EX v25,v25
MM v[52:55],a[244:247],v[184:187],v[52:55]
EX v26,v26
MM v[52:55],a[240:243],v[168:171],v[52:55]
EX v27,v27
MM v[60:63],a[240:243],v[172:175],v[140:143]
EX v28,v28
MM v[60:63],a[244:247],v[188:191],v[60:63]
EX v29,v29
MM v[60:63],a[248:251],v[204:207],v[60:63]
EX v30,v30
MM v[60:63],a[252:255],v[220:223],v[60:63]
EX v31,v31
MM v[56:59],a[236:239],v[220:223],v[140:143]
EX v32,v32
MM v[56:59],a[232:235],v[204:207],v[56:59]
EX v33,v33
MM v[56:59],a[228:231],v[188:191],v[56:59]
EX v34,v34
MM v[56:59],a[224:227],v[172:175],v[56:59]
s_waitcnt lgkmcnt(0)
s_barrier
MM a[0:3],a[128:131],v[64:67],a[0:3]
s_add_u32 m0, s65, 49152
CV v24,v24,v25
CV v25,v26,v27
MM a[0:3],a[132:135],v[68:71],a[0:3]
RD a[192:195],v224 offset:16384
CV v26,v28,v29
MM a[8:11],a[132:135],v[76:79],a[8:11]
RD a[196:199],v225 offset:16384
CV v27,v30,v31
MM a[8:11],a[128:131],v[72:75],a[8:11]
buffer_load_dwordx4 v228, s[8:11], s64 offen offset:0 lds
MM a[16:19],a[128:131],v[80:83],a[16:19]
s_add_u32 m0, s67, 3072
s_add_u32 s85, s66, 165888
buffer_load_dwordx4 v252, s[36:39], s85 offen offset:0 lds
MM a[16:19],a[132:135],v[84:87],a[16:19]
RD a[200:203],v226 offset:16384
CV v4,v32,v33
MM a[24:27],a[132:135],v[92:95],a[24:27]
RD a[204:207],v227 offset:16384
RD a[208:211],v224 offset:20480
MM a[24:27],a[128:131],v[88:91],a[24:27]
s_add_u32 m0, s65, 49152
s_nop 0
buffer_load_dwordx4 v229, s[8:11], s64 offen offset:1024 lds
MM a[28:31],a[136:139],v[88:91],a[28:31]
EX v35,v35
MM a[28:31],a[140:143],v[92:95],a[28:31]
RD a[212:215],v225 offset:20480
CV v5,v34,v35
MM a[20:23],a[140:143],v[84:87],a[20:23]
EX v36,v36
MM a[20:23],a[136:139],v[80:83],a[20:23]
EX v37,v37
MM a[12:15],a[136:139],v[72:75],a[12:15]
RD a[216:219],v226 offset:20480
CV v6,v36,v37
MM a[12:15],a[140:143],v[76:79],a[12:15]
buffer_load_dwordx4 v230, s[8:11], s64 offen offset:2048 lds
MM a[4:7],a[140:143],v[68:71],a[4:7]
EX v38,v38
MM a[4:7],a[136:139],v[64:67],a[4:7]
EX v39,v39
MM a[32:35],a[144:147],v[64:67],a[32:35]
RD a[220:223],v227 offset:20480
CV v7,v38,v39
MM a[32:35],a[148:151],v[68:71],a[32:35]
EX v40,v40
MM a[40:43],a[148:151],v[76:79],a[40:43]
EX v41,v41
MM a[40:43],a[144:147],v[72:75],a[40:43]
RD a[224:227],v224 offset:24576
CV v12,v40,v41
MM a[48:51],a[144:147],v[80:83],a[48:51]
buffer_load_dwordx4 v231, s[8:11], s64 offen offset:3072 lds
MM a[48:51],a[148:151],v[84:87],a[48:51]
s_add_u32 m0, s65, 114688
EX v42,v42
MM a[56:59],a[148:151],v[92:95],a[56:59]
EX v43,v43
MM a[56:59],a[144:147],v[88:91],a[56:59]
RD a[228:231],v225 offset:24576
CV v13,v42,v43
MM a[60:63],a[152:155],v[88:91],a[60:63]
EX v44,v44
MM a[60:63],a[156:159],v[92:95],a[60:63]
buffer_load_dwordx4 v235, s[12:15], s81 offen offset:0 lds
MM a[52:55],a[156:159],v[84:87],a[52:55]
EX v45,v45
MM a[52:55],a[152:155],v[80:83],a[52:55]
RD a[232:235],v226 offset:24576
CV v14,v44,v45
MM a[44:47],a[152:155],v[72:75],a[44:47]
EX v46,v46
MM a[44:47],a[156:159],v[76:79],a[44:47]
EX v47,v47
MM a[36:39],a[156:159],v[68:71],a[36:39]
RD a[236:239],v227 offset:24576
CV v15,v46,v47
MM a[36:39],a[152:155],v[64:67],a[36:39]
EX v48,v48
MM a[64:67],a[160:163],v[64:67],a[64:67]
buffer_load_dwordx4 v235, s[12:15], s64 offen offset:1024 lds
MM a[64:67],a[164:167],v[68:71],a[64:67]
EX v49,v49
MM a[72:75],a[164:167],v[76:79],a[72:75]
RD a[240:243],v224 offset:28672
CV v20,v48,v49
MM a[72:75],a[160:163],v[72:75],a[72:75]
EX v50,v50
MM a[80:83],a[160:163],v[80:83],a[80:83]
EX v51,v51
MM a[80:83],a[164:167],v[84:87],a[80:83]
RD a[244:247],v225 offset:28672
CV v21,v50,v51
MM a[88:91],a[164:167],v[92:95],a[88:91]
EX v52,v52
MM a[88:91],a[160:163],v[88:91],a[88:91]
buffer_load_dwordx4 v235, s[12:15], s81 offen offset:2048 lds
MM a[92:95],a[168:171],v[88:91],a[92:95]
EX v53,v53
MM a[92:95],a[172:175],v[92:95],a[92:95]
RD a[248:251],v226 offset:28672
CV v22,v52,v53
MM a[84:87],a[172:175],v[84:87],a[84:87]
EX v54,v54
MM a[84:87],a[168:171],v[80:83],a[84:87]
EX v55,v55
MM a[76:79],a[168:171],v[72:75],a[76:79]
RD a[252:255],v227 offset:28672
CV v23,v54,v55
MM a[76:79],a[172:175],v[76:79],a[76:79]
EX v56,v56
MM a[68:71],a[172:175],v[68:71],a[68:71]
buffer_load_dwordx4 v235, s[12:15], s64 offen offset:3072 lds
MM a[68:71],a[168:171],v[64:67],a[68:71]
EX v57,v57
MM a[96:99],a[176:179],v[64:67],a[96:99]
EX v58,v58
MM a[96:99],a[180:183],v[68:71],a[96:99]
EX v59,v59
MM a[104:107],a[180:183],v[76:79],a[104:107]
EX v60,v60
MM a[104:107],a[176:179],v[72:75],a[104:107]
EX v61,v61
MM a[112:115],a[176:179],v[80:83],a[112:115]
EX v62,v62
MM a[112:115],a[180:183],v[84:87],a[112:115]
EX v63,v63
MM a[120:123],a[180:183],v[92:95],a[120:123]
s_add_u32 m0, s67, 4096
s_add_u32 s85, s66, 221184
buffer_load_dwordx4 v252, s[36:39], s85 offen offset:0 lds
MM a[120:123],a[176:179],v[88:91],a[120:123]
CV v28,v56,v57
CV v29,v58,v59
MM a[124:127],a[184:187],v[88:91],a[124:127]
v_add_f32 v244, v144, v145
v_add_f32 v245, v148, v149
MM a[124:127],a[188:191],v[92:95],a[124:127]
v_add_f32 v246, v152, v153
v_add_f32 v247, v156, v157
MM a[116:119],a[188:191],v[84:87],a[116:119]
CV v30,v60,v61
CV v31,v62,v63
MM a[116:119],a[184:187],v[80:83],a[116:119]
MM a[108:111],a[184:187],v[72:75],a[108:111]
MM a[108:111],a[188:191],v[76:79],a[108:111]
MM a[100:103],a[188:191],v[68:71],a[100:103]
MM a[100:103],a[184:187],v[64:67],a[100:103]
v_smfmac_f32_16x16x64_bf16 v[144:147], v[236:239], v[0:7], v234
v_smfmac_f32_16x16x64_bf16 v[148:151], v[236:239], v[8:15], v234
v_smfmac_f32_16x16x64_bf16 v[152:155], v[236:239], v[16:23], v234
v_smfmac_f32_16x16x64_bf16 v[156:159], v[236:239], v[24:31], v234
s_waitcnt lgkmcnt(0)
MM v[64:67],a[192:195],v[160:163],v[128:131]
s_add_u32 s64, s64, 16384
TR a[128:129],v232 offset:0
s_add_u32 s81, s64, 896
MM v[64:67],a[196:199],v[176:179],v[64:67]
TR a[130:131],v232 offset:4096
v_max3_f32 v249, v144, v145, v148
MM v[64:67],a[200:203],v[192:195],v[64:67]
TR a[132:133],v232 offset:8192
v_max3_f32 v249, v249, v149, v152
MM v[64:67],a[204:207],v[208:211],v[64:67]
TR a[134:135],v232 offset:12288
v_max3_f32 v249, v249, v153, v156
MM v[68:71],a[220:223],v[208:211],v[128:131]
TR a[136:137],v233 offset:0
v_max_f32 v249, v249, v157
MM v[68:71],a[216:219],v[192:195],v[68:71]
TR a[138:139],v233 offset:4096
v_cmp_lt_f32 vcc, s71, v249
MM v[68:71],a[212:215],v[176:179],v[68:71]
TR a[140:141],v233 offset:8192
TR a[142:143],v233 offset:12288
MM v[68:71],a[208:211],v[160:163],v[68:71]
s_cbranch_vccnz LSITE16_%=
LBACK16_%=:
MM v[76:79],a[208:211],v[164:167],v[132:135]
EX v64,v64
MM v[76:79],a[212:215],v[180:183],v[76:79]
EX v65,v65
MM v[76:79],a[216:219],v[196:199],v[76:79]
TR a[144:145],v232 offset:512
CV v64,v64,v65
MM v[76:79],a[220:223],v[212:215],v[76:79]
EX v66,v66
MM v[72:75],a[204:207],v[212:215],v[132:135]
EX v67,v67
MM v[72:75],a[200:203],v[196:199],v[72:75]
TR a[146:147],v232 offset:4608
CV v65,v66,v67
MM v[72:75],a[196:199],v[180:183],v[72:75]
EX v68,v68
MM v[72:75],a[192:195],v[164:167],v[72:75]
TR a[148:149],v232 offset:8704
TR a[150:151],v232 offset:12800
MM v[80:83],a[192:195],v[168:171],v[136:139]
EX v69,v69
MM v[80:83],a[196:199],v[184:187],v[80:83]
TR a[152:153],v233 offset:512
CV v66,v68,v69
MM v[80:83],a[200:203],v[200:203],v[80:83]
EX v70,v70
MM v[80:83],a[204:207],v[216:219],v[80:83]
TR a[154:155],v233 offset:4608
TR a[156:157],v233 offset:8704
MM v[84:87],a[220:223],v[216:219],v[136:139]
s_add_u32 s87, s95, 1024
buffer_store_dword v243, v248, s[20:23], s87 offen offset:12 sc1
MM v[84:87],a[216:219],v[200:203],v[84:87]
EX v71,v71
MM v[84:87],a[212:215],v[184:187],v[84:87]
TR a[158:159],v233 offset:12800
CV v67,v70,v71
MM v[84:87],a[208:211],v[168:171],v[84:87]
EX v72,v72
MM v[92:95],a[208:211],v[172:175],v[140:143]
EX v73,v73
MM v[92:95],a[212:215],v[188:191],v[92:95]
TR a[160:161],v232 offset:1024
CV v72,v72,v73
MM v[92:95],a[216:219],v[204:207],v[92:95]
EX v74,v74
MM v[92:95],a[220:223],v[220:223],v[92:95]
EX v75,v75
MM v[88:91],a[204:207],v[220:223],v[140:143]
TR a[162:163],v232 offset:5120
CV v73,v74,v75
MM v[88:91],a[200:203],v[204:207],v[88:91]
TR a[164:165],v232 offset:9216
TR a[166:167],v232 offset:13312
MM v[88:91],a[196:199],v[188:191],v[88:91]
EX v76,v76
MM v[88:91],a[192:195],v[172:175],v[88:91]
EX v77,v77
MM v[96:99],a[224:227],v[160:163],v[128:131]
TR a[168:169],v233 offset:1024
CV v74,v76,v77
MM v[96:99],a[228:231],v[176:179],v[96:99]
EX v78,v78
MM v[96:99],a[232:235],v[192:195],v[96:99]
TR a[170:171],v233 offset:5120
TR a[172:173],v233 offset:9216
MM v[96:99],a[236:239],v[208:211],v[96:99]
EX v79,v79
MM v[100:103],a[252:255],v[208:211],v[128:131]
TR a[174:175],v233 offset:13312
CV v75,v78,v79
MM v[100:103],a[248:251],v[192:195],v[100:103]
EX v80,v80
MM v[100:103],a[244:247],v[176:179],v[100:103]
EX v81,v81
MM v[100:103],a[240:243],v[160:163],v[100:103]
TR a[176:177],v232 offset:1536
CV v80,v80,v81
MM v[108:111],a[240:243],v[164:167],v[132:135]
EX v82,v82
MM v[108:111],a[244:247],v[180:183],v[108:111]
TR a[178:179],v232 offset:5632
TR a[180:181],v232 offset:9728
MM v[108:111],a[248:251],v[196:199],v[108:111]
EX v83,v83
MM v[108:111],a[252:255],v[212:215],v[108:111]
)ASM"
R"ASM(
TR a[182:183],v232 offset:13824
CV v81,v82,v83
MM v[104:107],a[236:239],v[212:215],v[132:135]
EX v84,v84
MM v[104:107],a[232:235],v[196:199],v[104:107]
EX v85,v85
MM v[104:107],a[228:231],v[180:183],v[104:107]
TR a[184:185],v233 offset:1536
CV v82,v84,v85
MM v[104:107],a[224:227],v[164:167],v[104:107]
EX v86,v86
MM v[112:115],a[224:227],v[168:171],v[136:139]
TR a[186:187],v233 offset:5632
TR a[188:189],v233 offset:9728
MM v[112:115],a[228:231],v[184:187],v[112:115]
EX v87,v87
MM v[112:115],a[232:235],v[200:203],v[112:115]
TR a[190:191],v233 offset:13824
CV v83,v86,v87
MM v[112:115],a[236:239],v[216:219],v[112:115]
EX v88,v88
MM v[116:119],a[252:255],v[216:219],v[136:139]
EX v89,v89
MM v[116:119],a[248:251],v[200:203],v[116:119]
EX v90,v90
MM v[116:119],a[244:247],v[184:187],v[116:119]
EX v91,v91
MM v[116:119],a[240:243],v[168:171],v[116:119]
EX v92,v92
MM v[124:127],a[240:243],v[172:175],v[140:143]
EX v93,v93
MM v[124:127],a[244:247],v[188:191],v[124:127]
EX v94,v94
MM v[124:127],a[248:251],v[204:207],v[124:127]
EX v95,v95
MM v[124:127],a[252:255],v[220:223],v[124:127]
EX v96,v96
MM v[120:123],a[236:239],v[220:223],v[140:143]
EX v97,v97
MM v[120:123],a[232:235],v[204:207],v[120:123]
EX v98,v98
MM v[120:123],a[228:231],v[188:191],v[120:123]
EX v99,v99
MM v[120:123],a[224:227],v[172:175],v[120:123]
s_waitcnt vmcnt(19) lgkmcnt(0)
s_barrier
MM a[0:3],a[128:131],v[0:3],a[0:3]
s_add_u32 m0, s65, 0
CV v88,v88,v89
CV v89,v90,v91
MM a[0:3],a[132:135],v[4:7],a[0:3]
RD a[192:195],v224 offset:32768
CV v90,v92,v93
MM a[8:11],a[132:135],v[12:15],a[8:11]
RD a[196:199],v225 offset:32768
CV v91,v94,v95
MM a[8:11],a[128:131],v[8:11],a[8:11]
buffer_load_dwordx4 v228, s[8:11], s64 offen offset:0 lds
MM a[16:19],a[128:131],v[16:19],a[16:19]
RD a[200:203],v226 offset:32768
CV v68,v96,v97
MM a[16:19],a[132:135],v[20:23],a[16:19]
RD a[204:207],v227 offset:32768
CV v69,v98,v99
MM a[24:27],a[132:135],v[28:31],a[24:27]
buffer_load_dwordx4 v229, s[8:11], s64 offen offset:1024 lds
MM a[24:27],a[128:131],v[24:27],a[24:27]
RD a[208:211],v224 offset:36864
RD a[212:215],v225 offset:36864
MM a[28:31],a[136:139],v[24:27],a[28:31]
buffer_load_dwordx4 v230, s[8:11], s64 offen offset:2048 lds
MM a[28:31],a[140:143],v[28:31],a[28:31]
EX v100,v100
MM a[20:23],a[140:143],v[20:23],a[20:23]
EX v101,v101
MM a[20:23],a[136:139],v[16:19],a[20:23]
RD a[216:219],v226 offset:36864
CV v70,v100,v101
MM a[12:15],a[136:139],v[8:11],a[12:15]
EX v102,v102
MM a[12:15],a[140:143],v[12:15],a[12:15]
RD a[220:223],v227 offset:36864
RD a[224:227],v224 offset:40960
MM a[4:7],a[140:143],v[4:7],a[4:7]
EX v103,v103
MM a[4:7],a[136:139],v[0:3],a[4:7]
RD a[228:231],v225 offset:40960
CV v71,v102,v103
MM a[32:35],a[144:147],v[0:3],a[32:35]
EX v104,v104
MM a[32:35],a[148:151],v[4:7],a[32:35]
buffer_load_dwordx4 v231, s[8:11], s64 offen offset:3072 lds
MM a[40:43],a[148:151],v[12:15],a[40:43]
s_add_u32 m0, s65, 65536
EX v105,v105
MM a[40:43],a[144:147],v[8:11],a[40:43]
RD a[232:235],v226 offset:40960
CV v76,v104,v105
MM a[48:51],a[144:147],v[16:19],a[48:51]
EX v106,v106
MM a[48:51],a[148:151],v[20:23],a[48:51]
EX v107,v107
MM a[56:59],a[148:151],v[28:31],a[56:59]
RD a[236:239],v227 offset:40960
CV v77,v106,v107
MM a[56:59],a[144:147],v[24:27],a[56:59]
EX v108,v108
MM a[60:63],a[152:155],v[24:27],a[60:63]
buffer_load_dwordx4 v235, s[12:15], s81 offen offset:0 lds
MM a[60:63],a[156:159],v[28:31],a[60:63]
EX v109,v109
MM a[52:55],a[156:159],v[20:23],a[52:55]
s_add_u32 s87, s95, 2048
buffer_store_dword v243, v248, s[20:23], s87 offen offset:12 sc1
MM a[52:55],a[152:155],v[16:19],a[52:55]
RD a[240:243],v224 offset:45056
CV v78,v108,v109
MM a[44:47],a[152:155],v[8:11],a[44:47]
EX v110,v110
MM a[44:47],a[156:159],v[12:15],a[44:47]
EX v111,v111
MM a[36:39],a[156:159],v[4:7],a[36:39]
RD a[244:247],v225 offset:45056
CV v79,v110,v111
MM a[36:39],a[152:155],v[0:3],a[36:39]
LPG14_%=:
s_cbranch_execz LTR2_%=
EX v112,v112
MM a[64:67],a[160:163],v[0:3],a[64:67]
buffer_load_dwordx4 v235, s[12:15], s64 offen offset:1024 lds
MM a[64:67],a[164:167],v[4:7],a[64:67]
EX v113,v113
MM a[72:75],a[164:167],v[12:15],a[72:75]
RD a[248:251],v226 offset:45056
CV v84,v112,v113
MM a[72:75],a[160:163],v[8:11],a[72:75]
EX v114,v114
MM a[80:83],a[160:163],v[16:19],a[80:83]
EX v115,v115
MM a[80:83],a[164:167],v[20:23],a[80:83]
RD a[252:255],v227 offset:45056
CV v85,v114,v115
MM a[88:91],a[164:167],v[28:31],a[88:91]
EX v116,v116
MM a[88:91],a[160:163],v[24:27],a[88:91]
buffer_load_dwordx4 v235, s[12:15], s81 offen offset:2048 lds
MM a[92:95],a[168:171],v[24:27],a[92:95]
EX v117,v117
MM a[92:95],a[172:175],v[28:31],a[92:95]
EX v118,v118
MM a[84:87],a[172:175],v[20:23],a[84:87]
buffer_load_dwordx4 v235, s[12:15], s64 offen offset:3072 lds
MM a[84:87],a[168:171],v[16:19],a[84:87]
EX v119,v119
MM a[76:79],a[168:171],v[8:11],a[76:79]
EX v120,v120
MM a[76:79],a[172:175],v[12:15],a[76:79]
EX v121,v121
MM a[68:71],a[172:175],v[4:7],a[68:71]
EX v122,v122
MM a[68:71],a[168:171],v[0:3],a[68:71]
EX v123,v123
MM a[96:99],a[176:179],v[0:3],a[96:99]
EX v124,v124
MM a[96:99],a[180:183],v[4:7],a[96:99]
EX v125,v125
MM a[104:107],a[180:183],v[12:15],a[104:107]
EX v126,v126
MM a[104:107],a[176:179],v[8:11],a[104:107]
EX v127,v127
MM a[112:115],a[176:179],v[16:19],a[112:115]
CV v86,v116,v117
CV v87,v118,v119
MM a[112:115],a[180:183],v[20:23],a[112:115]
CV v92,v120,v121
CV v93,v122,v123
MM a[120:123],a[180:183],v[28:31],a[120:123]
CV v94,v124,v125
CV v95,v126,v127
MM a[120:123],a[176:179],v[24:27],a[120:123]
MM a[124:127],a[184:187],v[24:27],a[124:127]
v_add_f32 v244, v144, v145
v_add_f32 v245, v148, v149
MM a[124:127],a[188:191],v[28:31],a[124:127]
v_add_f32 v246, v152, v153
v_add_f32 v247, v156, v157
MM a[116:119],a[188:191],v[20:23],a[116:119]
MM a[116:119],a[184:187],v[16:19],a[116:119]
MM a[108:111],a[184:187],v[8:11],a[108:111]
MM a[108:111],a[188:191],v[12:15],a[108:111]
MM a[100:103],a[188:191],v[4:7],a[100:103]
MM a[100:103],a[184:187],v[0:3],a[100:103]
v_smfmac_f32_16x16x64_bf16 v[144:147], v[236:239], v[64:71], v234
v_smfmac_f32_16x16x64_bf16 v[148:151], v[236:239], v[72:79], v234
v_smfmac_f32_16x16x64_bf16 v[152:155], v[236:239], v[80:87], v234
v_smfmac_f32_16x16x64_bf16 v[156:159], v[236:239], v[88:95], v234
s_waitcnt lgkmcnt(0)
MM v[0:3],a[192:195],v[160:163],v[128:131]
s_add_u32 s64, s64, 16384
TR a[128:129],v232 offset:16384
s_add_u32 s81, s64, 896
MM v[0:3],a[196:199],v[176:179],v[0:3]
TR a[130:131],v232 offset:20480
v_max3_f32 v249, v144, v145, v148
MM v[0:3],a[200:203],v[192:195],v[0:3]
TR a[132:133],v232 offset:24576
v_max3_f32 v249, v249, v149, v152
MM v[0:3],a[204:207],v[208:211],v[0:3]
TR a[134:135],v232 offset:28672
v_max3_f32 v249, v249, v153, v156
MM v[4:7],a[220:223],v[208:211],v[128:131]
TR a[136:137],v233 offset:16384
v_max_f32 v249, v249, v157
MM v[4:7],a[216:219],v[192:195],v[4:7]
TR a[138:139],v233 offset:20480
v_cmp_lt_f32 vcc, s71, v249
MM v[4:7],a[212:215],v[176:179],v[4:7]
TR a[140:141],v233 offset:24576
TR a[142:143],v233 offset:28672
MM v[4:7],a[208:211],v[160:163],v[4:7]
s_cbranch_vccnz LSITE17_%=
LBACK17_%=:
MM v[12:15],a[208:211],v[164:167],v[132:135]
v_lshrrev_b32 v127, 8, v248
v_lshlrev_b32 v124, 9, v127
MM v[12:15],a[212:215],v[180:183],v[12:15]
v_and_b32 v126, 192, v248
v_add_u32 v124, v124, v126
MM v[12:15],a[216:219],v[196:199],v[12:15]
v_bfe_u32 v126, v248, 4, 2
v_lshl_add_u32 v124, v126, 3, v124
MM v[12:15],a[220:223],v[212:215],v[12:15]
s_lshr_b32 s76, %[swave], 1
EX v0,v0
MM v[8:11],a[204:207],v[212:215],v[132:135]
s_mul_i32 s76, s76, 10240
EX v1,v1
MM v[8:11],a[200:203],v[196:199],v[8:11]
s_and_b32 s77, %[swave], 1
EX v2,v2
MM v[8:11],a[196:199],v[180:183],v[8:11]
s_lshl_b32 s77, s77, 5
TR a[144:145],v232 offset:16896
CV v0,v0,v1
MM v[8:11],a[192:195],v[164:167],v[8:11]
s_add_u32 s76, s76, s77
EX v3,v3
MM v[16:19],a[192:195],v[168:171],v[136:139]
s_add_u32 s76, s76, %[slds]
EX v4,v4
MM v[16:19],a[196:199],v[184:187],v[16:19]
s_add_u32 s76, s76, 131072
v_add_u32 v124, s76, v124
v_bfe_u32 v126, v248, 4, 3
MM v[16:19],a[200:203],v[200:203],v[16:19]
v_lshl_add_u32 v126, v127, 3, v126
v_lshlrev_b32 v126, 4, v126
MM v[16:19],a[204:207],v[216:219],v[16:19]
s_mul_i32 s76, %[swave], 3072
EX v5,v5
MM v[20:23],a[220:223],v[216:219],v[136:139]
s_add_u32 s76, s76, %[slds]
TR a[146:147],v232 offset:20992
CV v1,v2,v3
MM v[20:23],a[216:219],v[200:203],v[20:23]
s_add_u32 s76, s76, 151552
TR a[148:149],v232 offset:25088
v_add_u32 v125, s76, v126
MM v[20:23],a[212:215],v[184:187],v[20:23]
s_lshr_b32 s76, s48, 7
EX v6,v6
MM v[20:23],a[208:211],v[168:171],v[20:23]
s_lshl_b32 s76, s76, 5
EX v7,v7
MM v[28:31],a[208:211],v[172:175],v[140:143]
s_and_b32 s77, s48, 16
TR a[150:151],v232 offset:29184
TR a[152:153],v233 offset:16896
MM v[28:31],a[212:215],v[188:191],v[28:31]
s_add_u32 s76, s76, s77
EX v8,v8
MM v[28:31],a[216:219],v[204:207],v[28:31]
s_bfe_u32 s77, s48, 131077
TR a[154:155],v233 offset:20992
CV v2,v4,v5
MM v[28:31],a[220:223],v[220:223],v[28:31]
s_lshl_b32 s77, s77, 2
EX v9,v9
MM v[24:27],a[204:207],v[220:223],v[140:143]
s_add_u32 s76, s76, s77
TR a[156:157],v233 offset:25088
CV v3,v6,v7
MM v[24:27],a[200:203],v[204:207],v[24:27]
s_add_u32 s76, s76, %[swave]
EX v10,v10
MM v[24:27],a[196:199],v[188:191],v[24:27]
s_lshl_b32 s97, s76, 2
TR a[158:159],v233 offset:29184
CV v8,v8,v9
MM v[24:27],a[192:195],v[172:175],v[24:27]
s_add_u32 s97, s97, 77824
EX v11,v11
MM v[32:35],a[224:227],v[160:163],v[128:131]
s_lshl_b32 s76, s76, 9
TR a[160:161],v232 offset:17408
TR a[162:163],v232 offset:21504
MM v[32:35],a[228:231],v[176:179],v[32:35]
s_add_u32 s76, s76, 22183936
EX v12,v12
MM v[32:35],a[232:235],v[192:195],v[32:35]
EX v13,v13
MM v[32:35],a[236:239],v[208:211],v[32:35]
TR a[164:165],v232 offset:25600
CV v9,v10,v11
MM v[36:39],a[252:255],v[208:211],v[128:131]
EX v14,v14
MM v[36:39],a[248:251],v[192:195],v[36:39]
TR a[166:167],v232 offset:29696
CV v10,v12,v13
MM v[36:39],a[244:247],v[176:179],v[36:39]
EX v15,v15
MM v[36:39],a[240:243],v[160:163],v[36:39]
TR a[168:169],v233 offset:17408
TR a[170:171],v233 offset:21504
MM v[44:47],a[240:243],v[164:167],v[132:135]
EX v16,v16
MM v[44:47],a[244:247],v[180:183],v[44:47]
EX v17,v17
MM v[44:47],a[248:251],v[196:199],v[44:47]
TR a[172:173],v233 offset:25600
CV v11,v14,v15
MM v[44:47],a[252:255],v[212:215],v[44:47]
EX v18,v18
MM v[40:43],a[236:239],v[212:215],v[132:135]
TR a[174:175],v233 offset:29696
CV v16,v16,v17
MM v[40:43],a[232:235],v[196:199],v[40:43]
EX v19,v19
MM v[40:43],a[228:231],v[180:183],v[40:43]
TR a[176:177],v232 offset:17920
TR a[178:179],v232 offset:22016
MM v[40:43],a[224:227],v[164:167],v[40:43]
EX v20,v20
MM v[48:51],a[224:227],v[168:171],v[136:139]
EX v21,v21
MM v[48:51],a[228:231],v[184:187],v[48:51]
TR a[180:181],v232 offset:26112
CV v17,v18,v19
MM v[48:51],a[232:235],v[200:203],v[48:51]
EX v22,v22
MM v[48:51],a[236:239],v[216:219],v[48:51]
TR a[182:183],v232 offset:30208
CV v18,v20,v21
MM v[52:55],a[252:255],v[216:219],v[136:139]
EX v23,v23
MM v[52:55],a[248:251],v[200:203],v[52:55]
TR a[184:185],v233 offset:17920
TR a[186:187],v233 offset:22016
MM v[52:55],a[244:247],v[184:187],v[52:55]
EX v24,v24
MM v[52:55],a[240:243],v[168:171],v[52:55]
TR a[188:189],v233 offset:26112
CV v19,v22,v23
MM v[60:63],a[240:243],v[172:175],v[140:143]
EX v25,v25
MM v[60:63],a[244:247],v[188:191],v[60:63]
TR a[190:191],v233 offset:30208
CV v24,v24,v25
MM v[60:63],a[248:251],v[204:207],v[60:63]
EX v26,v26
MM v[60:63],a[252:255],v[220:223],v[60:63]
EX v27,v27
MM v[56:59],a[236:239],v[220:223],v[140:143]
EX v28,v28
MM v[56:59],a[232:235],v[204:207],v[56:59]
EX v29,v29
MM v[56:59],a[228:231],v[188:191],v[56:59]
EX v30,v30
MM v[56:59],a[224:227],v[172:175],v[56:59]
s_waitcnt vmcnt(10) lgkmcnt(0)
s_barrier
MM a[0:3],a[128:131],v[64:67],a[0:3]
s_add_u32 m0, s65, 16384
CV v25,v26,v27
CV v26,v28,v29
MM a[0:3],a[132:135],v[68:71],a[0:3]
RD a[192:195],v224 offset:49152
RD a[196:199],v225 offset:49152
MM a[8:11],a[132:135],v[76:79],a[8:11]
EX v31,v31
MM a[8:11],a[128:131],v[72:75],a[8:11]
RD a[200:203],v226 offset:49152
CV v27,v30,v31
MM a[16:19],a[128:131],v[80:83],a[16:19]
)ASM"
R"ASM(
TR v[96:97],v124 offset:0
TR v[98:99],v124 offset:256
MM a[16:19],a[132:135],v[84:87],a[16:19]
RD v[108:111],v125 offset:0
TR v[100:101],v124 offset:2048
MM a[24:27],a[132:135],v[92:95],a[24:27]
TR v[102:103],v124 offset:2304
RD v[112:115],v125 offset:512
MM a[24:27],a[128:131],v[88:91],a[24:27]
TR v[104:105],v124 offset:4096
TR v[106:107],v124 offset:4352
MM a[28:31],a[136:139],v[88:91],a[28:31]
RD v[116:119],v125 offset:1024
RD a[204:207],v227 offset:49152
EX v32,v32
MM a[28:31],a[140:143],v[92:95],a[28:31]
EX v33,v33
MM a[20:23],a[140:143],v[84:87],a[20:23]
EX v34,v34
MM a[20:23],a[136:139],v[80:83],a[20:23]
buffer_load_dwordx4 v228, s[8:11], s64 offen offset:0 lds
MM a[12:15],a[136:139],v[72:75],a[12:15]
EX v35,v35
MM a[12:15],a[140:143],v[76:79],a[12:15]
EX v36,v36
MM a[4:7],a[140:143],v[68:71],a[4:7]
CV v4,v32,v33
CV v5,v34,v35
MM a[4:7],a[136:139],v[64:67],a[4:7]
EX v37,v37
MM a[32:35],a[144:147],v[64:67],a[32:35]
EX v38,v38
MM a[32:35],a[148:151],v[68:71],a[32:35]
s_waitcnt lgkmcnt(1)
buffer_load_dwordx4 v229, s[8:11], s64 offen offset:1024 lds
MM a[40:43],a[148:151],v[76:79],a[40:43]
EX v39,v39
MM v[120:123],v[96:99],v[108:111],0
MM v[120:123],v[100:103],v[112:115],v[120:123]
MM v[120:123],v[104:107],v[116:119],v[120:123]
MM a[40:43],a[144:147],v[72:75],a[40:43]
TR v[96:97],v124 offset:6144
TR v[98:99],v124 offset:6400
MM a[48:51],a[144:147],v[80:83],a[48:51]
RD v[108:111],v125 offset:1536
TR v[100:101],v124 offset:8192
MM a[48:51],a[148:151],v[84:87],a[48:51]
TR v[102:103],v124 offset:8448
RD v[112:115],v125 offset:2048
MM a[56:59],a[148:151],v[92:95],a[56:59]
EX v40,v40
MM a[56:59],a[144:147],v[88:91],a[56:59]
EX v41,v41
MM a[60:63],a[152:155],v[88:91],a[60:63]
CV v6,v36,v37
CV v7,v38,v39
MM a[60:63],a[156:159],v[92:95],a[60:63]
EX v42,v42
MM a[52:55],a[156:159],v[84:87],a[52:55]
RD a[208:211],v224 offset:53248
RD a[212:215],v225 offset:53248
MM a[52:55],a[152:155],v[80:83],a[52:55]
EX v43,v43
MM a[44:47],a[152:155],v[72:75],a[44:47]
buffer_load_dwordx4 v230, s[8:11], s64 offen offset:2048 lds
MM a[44:47],a[156:159],v[76:79],a[44:47]
EX v44,v44
MM a[36:39],a[156:159],v[68:71],a[36:39]
s_waitcnt lgkmcnt(2)
EX v45,v45
MM a[36:39],a[152:155],v[64:67],a[36:39]
CV v12,v40,v41
CV v13,v42,v43
MM v[120:123],v[96:99],v[108:111],v[120:123]
MM v[120:123],v[100:103],v[112:115],v[120:123]
MM a[64:67],a[160:163],v[64:67],a[64:67]
EX v46,v46
MM a[64:67],a[164:167],v[68:71],a[64:67]
RD a[216:219],v226 offset:53248
CV v14,v44,v45
MM a[72:75],a[164:167],v[76:79],a[72:75]
EX v47,v47
MM a[72:75],a[160:163],v[72:75],a[72:75]
buffer_load_dwordx4 v231, s[8:11], s64 offen offset:3072 lds
MM a[80:83],a[160:163],v[80:83],a[80:83]
s_add_u32 m0, s65, 81920
EX v48,v48
MM a[80:83],a[164:167],v[84:87],a[80:83]
RD a[220:223],v227 offset:53248
CV v15,v46,v47
MM a[88:91],a[164:167],v[92:95],a[88:91]
s_mov_b32 exec_lo, 0x00ff00ff
s_mov_b32 exec_hi, 0x00ff00ff
buffer_store_dwordx4 v[120:123], v126, s[20:23], s76 offen offset:0 sc1
s_mov_b64 exec, -1
s_nop 0
MM a[88:91],a[160:163],v[88:91],a[88:91]
EX v49,v49
MM a[92:95],a[168:171],v[88:91],a[92:95]
EX v50,v50
MM a[92:95],a[172:175],v[92:95],a[92:95]
EX v51,v51
MM a[84:87],a[172:175],v[84:87],a[84:87]
RD a[224:227],v224 offset:57344
CV v20,v48,v49
MM a[84:87],a[168:171],v[80:83],a[84:87]
EX v52,v52
MM a[76:79],a[168:171],v[72:75],a[76:79]
buffer_load_dwordx4 v235, s[12:15], s81 offen offset:0 lds
MM a[76:79],a[172:175],v[76:79],a[76:79]
EX v53,v53
MM a[68:71],a[172:175],v[68:71],a[68:71]
RD a[228:231],v225 offset:57344
CV v21,v50,v51
MM a[68:71],a[168:171],v[64:67],a[68:71]
EX v54,v54
MM a[96:99],a[176:179],v[64:67],a[96:99]
RD a[232:235],v226 offset:57344
CV v22,v52,v53
MM a[96:99],a[180:183],v[68:71],a[96:99]
EX v55,v55
MM a[104:107],a[180:183],v[76:79],a[104:107]
EX v56,v56
MM a[104:107],a[176:179],v[72:75],a[104:107]
buffer_load_dwordx4 v235, s[12:15], s64 offen offset:1024 lds
MM a[112:115],a[176:179],v[80:83],a[112:115]
EX v57,v57
MM a[112:115],a[180:183],v[84:87],a[112:115]
RD a[236:239],v227 offset:57344
CV v23,v54,v55
MM a[120:123],a[180:183],v[92:95],a[120:123]
EX v58,v58
MM a[120:123],a[176:179],v[88:91],a[120:123]
RD a[240:243],v224 offset:61440
CV v28,v56,v57
MM a[124:127],a[184:187],v[88:91],a[124:127]
v_add_f32 v244, v144, v145
v_add_f32 v245, v148, v149
MM a[124:127],a[188:191],v[92:95],a[124:127]
v_add_f32 v246, v152, v153
v_add_f32 v247, v156, v157
MM a[116:119],a[188:191],v[84:87],a[116:119]
EX v59,v59
MM a[116:119],a[184:187],v[80:83],a[116:119]
EX v60,v60
MM a[108:111],a[184:187],v[72:75],a[108:111]
EX v61,v61
MM a[108:111],a[188:191],v[76:79],a[108:111]
buffer_load_dwordx4 v235, s[12:15], s81 offen offset:2048 lds
MM a[100:103],a[188:191],v[68:71],a[100:103]
EX v62,v62
MM a[100:103],a[184:187],v[64:67],a[100:103]
RD a[244:247],v225 offset:61440
CV v29,v58,v59
v_smfmac_f32_16x16x64_bf16 v[144:147], v[236:239], v[0:7], v234
RD a[248:251],v226 offset:61440
RD a[252:255],v227 offset:61440
EX v63,v63
v_smfmac_f32_16x16x64_bf16 v[148:151], v[236:239], v[8:15], v234
CV v30,v60,v61
v_smfmac_f32_16x16x64_bf16 v[152:155], v[236:239], v[16:23], v234
CV v31,v62,v63
s_nop 1
v_smfmac_f32_16x16x64_bf16 v[156:159], v[236:239], v[24:31], v234
s_waitcnt lgkmcnt(0)
buffer_load_dwordx4 v235, s[12:15], s64 offen offset:3072 lds
MM v[64:67],a[192:195],v[160:163],v[128:131]
s_add_u32 s64, s64, 16384
TR a[128:129],v232 offset:32768
s_add_u32 s81, s64, 896
MM v[64:67],a[196:199],v[176:179],v[64:67]
TR a[130:131],v232 offset:36864
v_max3_f32 v249, v144, v145, v148
MM v[64:67],a[200:203],v[192:195],v[64:67]
TR a[132:133],v232 offset:40960
v_max3_f32 v249, v249, v149, v152
MM v[64:67],a[204:207],v[208:211],v[64:67]
TR a[134:135],v232 offset:45056
v_max3_f32 v249, v249, v153, v156
MM v[68:71],a[220:223],v[208:211],v[128:131]
TR a[136:137],v233 offset:32768
v_max_f32 v249, v249, v157
MM v[68:71],a[216:219],v[192:195],v[68:71]
TR a[138:139],v233 offset:36864
v_cmp_lt_f32 vcc, s71, v249
MM v[68:71],a[212:215],v[176:179],v[68:71]
TR a[140:141],v233 offset:40960
TR a[142:143],v233 offset:45056
MM v[68:71],a[208:211],v[160:163],v[68:71]
s_cbranch_vccnz LSITE18_%=
LBACK18_%=:
MM v[76:79],a[208:211],v[164:167],v[132:135]
EX v64,v64
MM v[76:79],a[212:215],v[180:183],v[76:79]
EX v65,v65
MM v[76:79],a[216:219],v[196:199],v[76:79]
TR a[144:145],v232 offset:33280
CV v64,v64,v65
MM v[76:79],a[220:223],v[212:215],v[76:79]
EX v66,v66
MM v[72:75],a[204:207],v[212:215],v[132:135]
EX v67,v67
MM v[72:75],a[200:203],v[196:199],v[72:75]
TR a[146:147],v232 offset:37376
CV v65,v66,v67
MM v[72:75],a[196:199],v[180:183],v[72:75]
EX v68,v68
MM v[72:75],a[192:195],v[164:167],v[72:75]
TR a[148:149],v232 offset:41472
TR a[150:151],v232 offset:45568
MM v[80:83],a[192:195],v[168:171],v[136:139]
EX v69,v69
MM v[80:83],a[196:199],v[184:187],v[80:83]
TR a[152:153],v233 offset:33280
CV v66,v68,v69
MM v[80:83],a[200:203],v[200:203],v[80:83]
EX v70,v70
MM v[80:83],a[204:207],v[216:219],v[80:83]
TR a[154:155],v233 offset:37376
TR a[156:157],v233 offset:41472
MM v[84:87],a[220:223],v[216:219],v[136:139]
EX v71,v71
MM v[84:87],a[216:219],v[200:203],v[84:87]
TR a[158:159],v233 offset:45568
CV v67,v70,v71
MM v[84:87],a[212:215],v[184:187],v[84:87]
EX v72,v72
MM v[84:87],a[208:211],v[168:171],v[84:87]
EX v73,v73
MM v[92:95],a[208:211],v[172:175],v[140:143]
TR a[160:161],v232 offset:33792
CV v72,v72,v73
MM v[92:95],a[212:215],v[188:191],v[92:95]
EX v74,v74
MM v[92:95],a[216:219],v[204:207],v[92:95]
TR a[162:163],v232 offset:37888
TR a[164:165],v232 offset:41984
MM v[92:95],a[220:223],v[220:223],v[92:95]
EX v75,v75
MM v[88:91],a[204:207],v[220:223],v[140:143]
TR a[166:167],v232 offset:46080
CV v73,v74,v75
MM v[88:91],a[200:203],v[204:207],v[88:91]
EX v76,v76
MM v[88:91],a[196:199],v[188:191],v[88:91]
EX v77,v77
MM v[88:91],a[192:195],v[172:175],v[88:91]
TR a[168:169],v233 offset:33792
CV v74,v76,v77
MM v[96:99],a[224:227],v[160:163],v[128:131]
EX v78,v78
MM v[96:99],a[228:231],v[176:179],v[96:99]
TR a[170:171],v233 offset:37888
TR a[172:173],v233 offset:41984
MM v[96:99],a[232:235],v[192:195],v[96:99]
EX v79,v79
MM v[96:99],a[236:239],v[208:211],v[96:99]
TR a[174:175],v233 offset:46080
CV v75,v78,v79
MM v[100:103],a[252:255],v[208:211],v[128:131]
EX v80,v80
MM v[100:103],a[248:251],v[192:195],v[100:103]
EX v81,v81
MM v[100:103],a[244:247],v[176:179],v[100:103]
TR a[176:177],v232 offset:34304
CV v80,v80,v81
MM v[100:103],a[240:243],v[160:163],v[100:103]
EX v82,v82
MM v[108:111],a[240:243],v[164:167],v[132:135]
v_lshrrev_b32 v253, 7, v248
v_mul_u32_u24 v253, 1280, v253
MM v[108:111],a[244:247],v[180:183],v[108:111]
v_and_b32 v252, 112, v248
v_add_u32 v253, v253, v252
MM v[108:111],a[248:251],v[196:199],v[108:111]
s_mul_i32 s90, %[swave], 61440
TR a[178:179],v232 offset:38400
TR a[180:181],v232 offset:42496
MM v[108:111],a[252:255],v[212:215],v[108:111]
s_mul_i32 s91, %[swave], 6144
EX v83,v83
MM v[104:107],a[236:239],v[212:215],v[132:135]
s_add_u32 s91, s91, %[slds]
TR a[182:183],v232 offset:46592
CV v81,v82,v83
MM v[104:107],a[232:235],v[196:199],v[104:107]
EX v84,v84
MM v[104:107],a[228:231],v[180:183],v[104:107]
EX v85,v85
MM v[104:107],a[224:227],v[164:167],v[104:107]
TR a[184:185],v233 offset:34304
CV v82,v84,v85
MM v[112:115],a[224:227],v[168:171],v[136:139]
EX v86,v86
MM v[112:115],a[228:231],v[184:187],v[112:115]
TR a[186:187],v233 offset:38400
TR a[188:189],v233 offset:42496
MM v[112:115],a[232:235],v[200:203],v[112:115]
EX v87,v87
MM v[112:115],a[236:239],v[216:219],v[112:115]
TR a[190:191],v233 offset:46592
CV v83,v86,v87
MM v[116:119],a[252:255],v[216:219],v[136:139]
EX v88,v88
MM v[116:119],a[248:251],v[200:203],v[116:119]
EX v89,v89
MM v[116:119],a[244:247],v[184:187],v[116:119]
EX v90,v90
MM v[116:119],a[240:243],v[168:171],v[116:119]
EX v91,v91
MM v[124:127],a[240:243],v[172:175],v[140:143]
EX v92,v92
MM v[124:127],a[244:247],v[188:191],v[124:127]
EX v93,v93
MM v[124:127],a[248:251],v[204:207],v[124:127]
EX v94,v94
MM v[124:127],a[252:255],v[220:223],v[124:127]
EX v95,v95
MM v[120:123],a[236:239],v[220:223],v[140:143]
EX v96,v96
MM v[120:123],a[232:235],v[204:207],v[120:123]
EX v97,v97
MM v[120:123],a[228:231],v[188:191],v[120:123]
EX v98,v98
MM v[120:123],a[224:227],v[172:175],v[120:123]
s_waitcnt vmcnt(14) lgkmcnt(0)
s_barrier
MM a[0:3],a[128:131],v[0:3],a[0:3]
s_nop 0
LPG15_%=:
s_cbranch_execz LTR2_%=
s_add_u32 m0, s65, 32768
CV v88,v88,v89
CV v89,v90,v91
MM a[0:3],a[132:135],v[4:7],a[0:3]
RD a[192:195],v224 offset:0
CV v90,v92,v93
MM a[8:11],a[132:135],v[12:15],a[8:11]
RD a[196:199],v225 offset:0
CV v91,v94,v95
MM a[8:11],a[128:131],v[8:11],a[8:11]
buffer_load_dwordx4 v228, s[8:11], s64 offen offset:0 lds
MM a[16:19],a[128:131],v[16:19],a[16:19]
RD a[200:203],v226 offset:0
CV v68,v96,v97
MM a[16:19],a[132:135],v[20:23],a[16:19]
buffer_load_dwordx4 v229, s[8:11], s64 offen offset:1024 lds
MM a[24:27],a[132:135],v[28:31],a[24:27]
RD a[204:207],v227 offset:0
RD a[208:211],v224 offset:4096
MM a[24:27],a[128:131],v[24:27],a[24:27]
EX v99,v99
MM a[28:31],a[136:139],v[24:27],a[28:31]
RD a[212:215],v225 offset:4096
CV v69,v98,v99
MM a[28:31],a[140:143],v[28:31],a[28:31]
EX v100,v100
MM a[20:23],a[140:143],v[20:23],a[20:23]
buffer_load_dwordx4 v230, s[8:11], s64 offen offset:2048 lds
MM a[20:23],a[136:139],v[16:19],a[20:23]
EX v101,v101
MM a[12:15],a[136:139],v[8:11],a[12:15]
RD a[216:219],v226 offset:4096
CV v70,v100,v101
MM a[12:15],a[140:143],v[12:15],a[12:15]
EX v102,v102
MM a[4:7],a[140:143],v[4:7],a[4:7]
EX v103,v103
MM a[4:7],a[136:139],v[0:3],a[4:7]
RD a[220:223],v227 offset:4096
CV v71,v102,v103
MM a[32:35],a[144:147],v[0:3],a[32:35]
EX v104,v104
MM a[32:35],a[148:151],v[4:7],a[32:35]
buffer_load_dwordx4 v231, s[8:11], s64 offen offset:3072 lds
MM a[40:43],a[148:151],v[12:15],a[40:43]
s_add_u32 m0, s65, 98304
EX v105,v105
MM a[40:43],a[144:147],v[8:11],a[40:43]
RD a[224:227],v224 offset:8192
CV v76,v104,v105
MM a[48:51],a[144:147],v[16:19],a[48:51]
EX v106,v106
MM a[48:51],a[148:151],v[20:23],a[48:51]
EX v107,v107
MM a[56:59],a[148:151],v[28:31],a[56:59]
RD a[228:231],v225 offset:8192
)ASM"
R"ASM(
CV v77,v106,v107
MM a[56:59],a[144:147],v[24:27],a[56:59]
EX v108,v108
MM a[60:63],a[152:155],v[24:27],a[60:63]
buffer_load_dwordx4 v235, s[12:15], s81 offen offset:0 lds
MM a[60:63],a[156:159],v[28:31],a[60:63]
EX v109,v109
MM a[52:55],a[156:159],v[20:23],a[52:55]
RD a[232:235],v226 offset:8192
CV v78,v108,v109
MM a[52:55],a[152:155],v[16:19],a[52:55]
EX v110,v110
MM a[44:47],a[152:155],v[8:11],a[44:47]
EX v111,v111
MM a[44:47],a[156:159],v[12:15],a[44:47]
RD a[236:239],v227 offset:8192
CV v79,v110,v111
MM a[36:39],a[156:159],v[4:7],a[36:39]
buffer_load_dwordx4 v235, s[12:15], s64 offen offset:1024 lds
MM a[36:39],a[152:155],v[0:3],a[36:39]
EX v112,v112
MM a[64:67],a[160:163],v[0:3],a[64:67]
EX v113,v113
MM a[64:67],a[164:167],v[4:7],a[64:67]
RD a[240:243],v224 offset:12288
CV v84,v112,v113
MM a[72:75],a[164:167],v[12:15],a[72:75]
EX v114,v114
MM a[72:75],a[160:163],v[8:11],a[72:75]
RD a[244:247],v225 offset:12288
RD a[248:251],v226 offset:12288
MM a[80:83],a[160:163],v[16:19],a[80:83]
s_waitcnt vmcnt(10)
v_mov_b32 v250, s69
s_mov_b64 exec, 1
buffer_store_dword v250, off, s[20:23], s97 offset:0 sc1
s_mov_b64 exec, -1
MM a[80:83],a[164:167],v[20:23],a[80:83]
s_add_u32 m0, s91, 139264
s_add_u32 s77, s90, 0
buffer_load_dwordx4 v253, s[28:31], s77 offen offset:0 lds
MM a[88:91],a[164:167],v[28:31],a[88:91]
EX v115,v115
MM a[88:91],a[160:163],v[24:27],a[88:91]
RD a[252:255],v227 offset:12288
CV v85,v114,v115
MM a[92:95],a[168:171],v[24:27],a[92:95]
EX v116,v116
MM a[92:95],a[172:175],v[28:31],a[92:95]
EX v117,v117
MM a[84:87],a[172:175],v[20:23],a[84:87]
s_add_u32 m0, s65, 98304
s_nop 0
buffer_load_dwordx4 v235, s[12:15], s81 offen offset:2048 lds
MM a[84:87],a[168:171],v[16:19],a[84:87]
EX v118,v118
MM a[76:79],a[168:171],v[8:11],a[76:79]
EX v119,v119
MM a[76:79],a[172:175],v[12:15],a[76:79]
EX v120,v120
MM a[68:71],a[172:175],v[4:7],a[68:71]
buffer_load_dwordx4 v235, s[12:15], s64 offen offset:3072 lds
MM a[68:71],a[168:171],v[0:3],a[68:71]
EX v121,v121
MM a[96:99],a[176:179],v[0:3],a[96:99]
EX v122,v122
MM a[96:99],a[180:183],v[4:7],a[96:99]
EX v123,v123
MM a[104:107],a[180:183],v[12:15],a[104:107]
EX v124,v124
MM a[104:107],a[176:179],v[8:11],a[104:107]
EX v125,v125
MM a[112:115],a[176:179],v[16:19],a[112:115]
EX v126,v126
MM a[112:115],a[180:183],v[20:23],a[112:115]
EX v127,v127
MM a[120:123],a[180:183],v[28:31],a[120:123]
CV v86,v116,v117
CV v87,v118,v119
MM a[120:123],a[176:179],v[24:27],a[120:123]
CV v92,v120,v121
CV v93,v122,v123
MM a[124:127],a[184:187],v[24:27],a[124:127]
v_add_f32 v244, v144, v145
v_add_f32 v245, v148, v149
MM a[124:127],a[188:191],v[28:31],a[124:127]
v_add_f32 v246, v152, v153
v_add_f32 v247, v156, v157
MM a[116:119],a[188:191],v[20:23],a[116:119]
CV v94,v124,v125
CV v95,v126,v127
MM a[116:119],a[184:187],v[16:19],a[116:119]
MM a[108:111],a[184:187],v[8:11],a[108:111]
MM a[108:111],a[188:191],v[12:15],a[108:111]
MM a[100:103],a[188:191],v[4:7],a[100:103]
MM a[100:103],a[184:187],v[0:3],a[100:103]
v_smfmac_f32_16x16x64_bf16 v[144:147], v[236:239], v[64:71], v234
v_smfmac_f32_16x16x64_bf16 v[148:151], v[236:239], v[72:79], v234
v_smfmac_f32_16x16x64_bf16 v[152:155], v[236:239], v[80:87], v234
v_smfmac_f32_16x16x64_bf16 v[156:159], v[236:239], v[88:95], v234
s_waitcnt lgkmcnt(0)
MM v[0:3],a[192:195],v[160:163],v[128:131]
s_add_u32 s64, s64, 16384
TR a[128:129],v232 offset:49152
s_add_u32 s81, s64, 896
MM v[0:3],a[196:199],v[176:179],v[0:3]
TR a[130:131],v232 offset:53248
v_max3_f32 v249, v144, v145, v148
MM v[0:3],a[200:203],v[192:195],v[0:3]
TR a[132:133],v232 offset:57344
v_max3_f32 v249, v249, v149, v152
MM v[0:3],a[204:207],v[208:211],v[0:3]
TR a[134:135],v232 offset:61440
v_max3_f32 v249, v249, v153, v156
MM v[4:7],a[220:223],v[208:211],v[128:131]
TR a[136:137],v233 offset:49152
v_max_f32 v249, v249, v157
MM v[4:7],a[216:219],v[192:195],v[4:7]
TR a[138:139],v233 offset:53248
v_cmp_lt_f32 vcc, s71, v249
MM v[4:7],a[212:215],v[176:179],v[4:7]
TR a[140:141],v233 offset:57344
TR a[142:143],v233 offset:61440
MM v[4:7],a[208:211],v[160:163],v[4:7]
s_cbranch_vccnz LSITE19_%=
LBACK19_%=:
MM v[12:15],a[208:211],v[164:167],v[132:135]
s_add_u32 m0, s91, 140288
s_add_u32 s77, s90, 10240
buffer_load_dwordx4 v253, s[28:31], s77 offen offset:0 lds
MM v[12:15],a[212:215],v[180:183],v[12:15]
EX v0,v0
MM v[12:15],a[216:219],v[196:199],v[12:15]
EX v1,v1
MM v[12:15],a[220:223],v[212:215],v[12:15]
TR a[144:145],v232 offset:49664
CV v0,v0,v1
MM v[8:11],a[204:207],v[212:215],v[132:135]
EX v2,v2
MM v[8:11],a[200:203],v[196:199],v[8:11]
EX v3,v3
MM v[8:11],a[196:199],v[180:183],v[8:11]
TR a[146:147],v232 offset:53760
CV v1,v2,v3
MM v[8:11],a[192:195],v[164:167],v[8:11]
EX v4,v4
MM v[16:19],a[192:195],v[168:171],v[136:139]
TR a[148:149],v232 offset:57856
TR a[150:151],v232 offset:61952
MM v[16:19],a[196:199],v[184:187],v[16:19]
EX v5,v5
MM v[16:19],a[200:203],v[200:203],v[16:19]
TR a[152:153],v233 offset:49664
CV v2,v4,v5
MM v[16:19],a[204:207],v[216:219],v[16:19]
EX v6,v6
MM v[20:23],a[220:223],v[216:219],v[136:139]
EX v7,v7
MM v[20:23],a[216:219],v[200:203],v[20:23]
TR a[154:155],v233 offset:53760
CV v3,v6,v7
MM v[20:23],a[212:215],v[184:187],v[20:23]
EX v8,v8
MM v[20:23],a[208:211],v[168:171],v[20:23]
TR a[156:157],v233 offset:57856
TR a[158:159],v233 offset:61952
MM v[28:31],a[208:211],v[172:175],v[140:143]
EX v9,v9
MM v[28:31],a[212:215],v[188:191],v[28:31]
TR a[160:161],v232 offset:50176
CV v8,v8,v9
MM v[28:31],a[216:219],v[204:207],v[28:31]
EX v10,v10
MM v[28:31],a[220:223],v[220:223],v[28:31]
EX v11,v11
MM v[24:27],a[204:207],v[220:223],v[140:143]
TR a[162:163],v232 offset:54272
CV v9,v10,v11
MM v[24:27],a[200:203],v[204:207],v[24:27]
TR a[164:165],v232 offset:58368
TR a[166:167],v232 offset:62464
MM v[24:27],a[196:199],v[188:191],v[24:27]
EX v12,v12
MM v[24:27],a[192:195],v[172:175],v[24:27]
EX v13,v13
MM v[32:35],a[224:227],v[160:163],v[128:131]
TR a[168:169],v233 offset:50176
CV v10,v12,v13
MM v[32:35],a[228:231],v[176:179],v[32:35]
EX v14,v14
MM v[32:35],a[232:235],v[192:195],v[32:35]
TR a[170:171],v233 offset:54272
TR a[172:173],v233 offset:58368
MM v[32:35],a[236:239],v[208:211],v[32:35]
EX v15,v15
MM v[36:39],a[252:255],v[208:211],v[128:131]
TR a[174:175],v233 offset:62464
CV v11,v14,v15
MM v[36:39],a[248:251],v[192:195],v[36:39]
EX v16,v16
MM v[36:39],a[244:247],v[176:179],v[36:39]
EX v17,v17
MM v[36:39],a[240:243],v[160:163],v[36:39]
TR a[176:177],v232 offset:50688
CV v16,v16,v17
MM v[44:47],a[240:243],v[164:167],v[132:135]
s_add_u32 m0, s91, 141312
s_add_u32 s77, s90, 20480
buffer_load_dwordx4 v253, s[28:31], s77 offen offset:0 lds
MM v[44:47],a[244:247],v[180:183],v[44:47]
EX v18,v18
MM v[44:47],a[248:251],v[196:199],v[44:47]
TR a[178:179],v232 offset:54784
TR a[180:181],v232 offset:58880
MM v[44:47],a[252:255],v[212:215],v[44:47]
EX v19,v19
MM v[40:43],a[236:239],v[212:215],v[132:135]
TR a[182:183],v232 offset:62976
CV v17,v18,v19
MM v[40:43],a[232:235],v[196:199],v[40:43]
EX v20,v20
MM v[40:43],a[228:231],v[180:183],v[40:43]
EX v21,v21
MM v[40:43],a[224:227],v[164:167],v[40:43]
TR a[184:185],v233 offset:50688
CV v18,v20,v21
MM v[48:51],a[224:227],v[168:171],v[136:139]
EX v22,v22
MM v[48:51],a[228:231],v[184:187],v[48:51]
TR a[186:187],v233 offset:54784
TR a[188:189],v233 offset:58880
MM v[48:51],a[232:235],v[200:203],v[48:51]
EX v23,v23
MM v[48:51],a[236:239],v[216:219],v[48:51]
TR a[190:191],v233 offset:62976
CV v19,v22,v23
MM v[52:55],a[252:255],v[216:219],v[136:139]
EX v24,v24
MM v[52:55],a[248:251],v[200:203],v[52:55]
EX v25,v25
MM v[52:55],a[244:247],v[184:187],v[52:55]
EX v26,v26
MM v[52:55],a[240:243],v[168:171],v[52:55]
EX v27,v27
MM v[60:63],a[240:243],v[172:175],v[140:143]
EX v28,v28
MM v[60:63],a[244:247],v[188:191],v[60:63]
EX v29,v29
MM v[60:63],a[248:251],v[204:207],v[60:63]
EX v30,v30
MM v[60:63],a[252:255],v[220:223],v[60:63]
EX v31,v31
MM v[56:59],a[236:239],v[220:223],v[140:143]
EX v32,v32
MM v[56:59],a[232:235],v[204:207],v[56:59]
EX v33,v33
MM v[56:59],a[228:231],v[188:191],v[56:59]
EX v34,v34
MM v[56:59],a[224:227],v[172:175],v[56:59]
s_waitcnt lgkmcnt(0)
s_barrier
MM a[0:3],a[128:131],v[64:67],a[0:3]
s_add_u32 m0, s65, 49152
CV v24,v24,v25
CV v25,v26,v27
MM a[0:3],a[132:135],v[68:71],a[0:3]
RD a[192:195],v224 offset:16384
CV v26,v28,v29
MM a[8:11],a[132:135],v[76:79],a[8:11]
RD a[196:199],v225 offset:16384
CV v27,v30,v31
MM a[8:11],a[128:131],v[72:75],a[8:11]
buffer_load_dwordx4 v228, s[8:11], s64 offen offset:0 lds
MM a[16:19],a[128:131],v[80:83],a[16:19]
s_add_u32 m0, s91, 142336
s_add_u32 s77, s90, 30720
buffer_load_dwordx4 v253, s[28:31], s77 offen offset:0 lds
MM a[16:19],a[132:135],v[84:87],a[16:19]
RD a[200:203],v226 offset:16384
CV v4,v32,v33
MM a[24:27],a[132:135],v[92:95],a[24:27]
RD a[204:207],v227 offset:16384
RD a[208:211],v224 offset:20480
MM a[24:27],a[128:131],v[88:91],a[24:27]
s_add_u32 m0, s65, 49152
s_nop 0
buffer_load_dwordx4 v229, s[8:11], s64 offen offset:1024 lds
MM a[28:31],a[136:139],v[88:91],a[28:31]
EX v35,v35
MM a[28:31],a[140:143],v[92:95],a[28:31]
RD a[212:215],v225 offset:20480
CV v5,v34,v35
MM a[20:23],a[140:143],v[84:87],a[20:23]
EX v36,v36
MM a[20:23],a[136:139],v[80:83],a[20:23]
EX v37,v37
MM a[12:15],a[136:139],v[72:75],a[12:15]
RD a[216:219],v226 offset:20480
CV v6,v36,v37
MM a[12:15],a[140:143],v[76:79],a[12:15]
buffer_load_dwordx4 v230, s[8:11], s64 offen offset:2048 lds
MM a[4:7],a[140:143],v[68:71],a[4:7]
EX v38,v38
MM a[4:7],a[136:139],v[64:67],a[4:7]
EX v39,v39
MM a[32:35],a[144:147],v[64:67],a[32:35]
RD a[220:223],v227 offset:20480
CV v7,v38,v39
MM a[32:35],a[148:151],v[68:71],a[32:35]
EX v40,v40
MM a[40:43],a[148:151],v[76:79],a[40:43]
EX v41,v41
MM a[40:43],a[144:147],v[72:75],a[40:43]
RD a[224:227],v224 offset:24576
CV v12,v40,v41
MM a[48:51],a[144:147],v[80:83],a[48:51]
buffer_load_dwordx4 v231, s[8:11], s64 offen offset:3072 lds
MM a[48:51],a[148:151],v[84:87],a[48:51]
s_add_u32 m0, s65, 114688
EX v42,v42
MM a[56:59],a[148:151],v[92:95],a[56:59]
EX v43,v43
MM a[56:59],a[144:147],v[88:91],a[56:59]
RD a[228:231],v225 offset:24576
CV v13,v42,v43
MM a[60:63],a[152:155],v[88:91],a[60:63]
EX v44,v44
MM a[60:63],a[156:159],v[92:95],a[60:63]
buffer_load_dwordx4 v235, s[12:15], s81 offen offset:0 lds
MM a[52:55],a[156:159],v[84:87],a[52:55]
EX v45,v45
MM a[52:55],a[152:155],v[80:83],a[52:55]
RD a[232:235],v226 offset:24576
CV v14,v44,v45
MM a[44:47],a[152:155],v[72:75],a[44:47]
EX v46,v46
MM a[44:47],a[156:159],v[76:79],a[44:47]
EX v47,v47
MM a[36:39],a[156:159],v[68:71],a[36:39]
RD a[236:239],v227 offset:24576
CV v15,v46,v47
MM a[36:39],a[152:155],v[64:67],a[36:39]
EX v48,v48
MM a[64:67],a[160:163],v[64:67],a[64:67]
buffer_load_dwordx4 v235, s[12:15], s64 offen offset:1024 lds
MM a[64:67],a[164:167],v[68:71],a[64:67]
EX v49,v49
MM a[72:75],a[164:167],v[76:79],a[72:75]
RD a[240:243],v224 offset:28672
CV v20,v48,v49
MM a[72:75],a[160:163],v[72:75],a[72:75]
EX v50,v50
MM a[80:83],a[160:163],v[80:83],a[80:83]
s_add_u32 m0, s91, 143360
s_add_u32 s77, s90, 40960
buffer_load_dwordx4 v253, s[28:31], s77 offen offset:0 lds
MM a[80:83],a[164:167],v[84:87],a[80:83]
EX v51,v51
MM a[88:91],a[164:167],v[92:95],a[88:91]
RD a[244:247],v225 offset:28672
CV v21,v50,v51
MM a[88:91],a[160:163],v[88:91],a[88:91]
EX v52,v52
MM a[92:95],a[168:171],v[88:91],a[92:95]
EX v53,v53
MM a[92:95],a[172:175],v[92:95],a[92:95]
RD a[248:251],v226 offset:28672
CV v22,v52,v53
MM a[84:87],a[172:175],v[84:87],a[84:87]
s_add_u32 m0, s65, 114688
s_nop 0
buffer_load_dwordx4 v235, s[12:15], s81 offen offset:2048 lds
MM a[84:87],a[168:171],v[80:83],a[84:87]
EX v54,v54
MM a[76:79],a[168:171],v[72:75],a[76:79]
EX v55,v55
MM a[76:79],a[172:175],v[76:79],a[76:79]
RD a[252:255],v227 offset:28672
CV v23,v54,v55
MM a[68:71],a[172:175],v[68:71],a[68:71]
EX v56,v56
MM a[68:71],a[168:171],v[64:67],a[68:71]
EX v57,v57
MM a[96:99],a[176:179],v[64:67],a[96:99]
buffer_load_dwordx4 v235, s[12:15], s64 offen offset:3072 lds
MM a[96:99],a[180:183],v[68:71],a[96:99]
EX v58,v58
MM a[104:107],a[180:183],v[76:79],a[104:107]
EX v59,v59
MM a[104:107],a[176:179],v[72:75],a[104:107]
)ASM"
R"ASM(
EX v60,v60
MM a[112:115],a[176:179],v[80:83],a[112:115]
EX v61,v61
MM a[112:115],a[180:183],v[84:87],a[112:115]
EX v62,v62
MM a[120:123],a[180:183],v[92:95],a[120:123]
s_add_u32 m0, s91, 144384
s_add_u32 s77, s90, 51200
buffer_load_dwordx4 v253, s[28:31], s77 offen offset:0 lds
MM a[120:123],a[176:179],v[88:91],a[120:123]
EX v63,v63
MM a[124:127],a[184:187],v[88:91],a[124:127]
v_add_f32 v244, v144, v145
v_add_f32 v245, v148, v149
MM a[124:127],a[188:191],v[92:95],a[124:127]
v_add_f32 v246, v152, v153
v_add_f32 v247, v156, v157
MM a[116:119],a[188:191],v[84:87],a[116:119]
CV v28,v56,v57
CV v29,v58,v59
MM a[116:119],a[184:187],v[80:83],a[116:119]
CV v30,v60,v61
CV v31,v62,v63
MM a[108:111],a[184:187],v[72:75],a[108:111]
MM a[108:111],a[188:191],v[76:79],a[108:111]
MM a[100:103],a[188:191],v[68:71],a[100:103]
MM a[100:103],a[184:187],v[64:67],a[100:103]
v_smfmac_f32_16x16x64_bf16 v[144:147], v[236:239], v[0:7], v234
v_smfmac_f32_16x16x64_bf16 v[148:151], v[236:239], v[8:15], v234
v_smfmac_f32_16x16x64_bf16 v[152:155], v[236:239], v[16:23], v234
v_smfmac_f32_16x16x64_bf16 v[156:159], v[236:239], v[24:31], v234
s_waitcnt lgkmcnt(0)
MM v[64:67],a[192:195],v[160:163],v[128:131]
s_add_u32 s64, s64, 16384
TR a[128:129],v232 offset:0
s_add_u32 s81, s64, 896
MM v[64:67],a[196:199],v[176:179],v[64:67]
TR a[130:131],v232 offset:4096
v_max3_f32 v249, v144, v145, v148
MM v[64:67],a[200:203],v[192:195],v[64:67]
TR a[132:133],v232 offset:8192
v_max3_f32 v249, v249, v149, v152
MM v[64:67],a[204:207],v[208:211],v[64:67]
TR a[134:135],v232 offset:12288
v_max3_f32 v249, v249, v153, v156
MM v[68:71],a[220:223],v[208:211],v[128:131]
TR a[136:137],v233 offset:0
v_max_f32 v249, v249, v157
MM v[68:71],a[216:219],v[192:195],v[68:71]
TR a[138:139],v233 offset:4096
v_cmp_lt_f32 vcc, s71, v249
MM v[68:71],a[212:215],v[176:179],v[68:71]
TR a[140:141],v233 offset:8192
TR a[142:143],v233 offset:12288
MM v[68:71],a[208:211],v[160:163],v[68:71]
s_cbranch_vccnz LSITE20_%=
LBACK20_%=:
MM v[76:79],a[208:211],v[164:167],v[132:135]
v_bfe_u32 v253, v248, 4, 4
v_lshlrev_b32 v253, 2, v253
MM v[76:79],a[212:215],v[180:183],v[76:79]
s_and_b32 s76, s18, 0xffffffc0
EX v64,v64
MM v[76:79],a[216:219],v[196:199],v[76:79]
s_add_u32 s76, s76, 77824
buffer_load_dword v252, v253, s[20:23], s76 offen offset:0 sc1
MM v[76:79],a[220:223],v[212:215],v[76:79]
EX v65,v65
MM v[72:75],a[204:207],v[212:215],v[132:135]
EX v66,v66
MM v[72:75],a[200:203],v[196:199],v[72:75]
TR a[144:145],v232 offset:512
CV v64,v64,v65
MM v[72:75],a[196:199],v[180:183],v[72:75]
EX v67,v67
MM v[72:75],a[192:195],v[164:167],v[72:75]
TR a[146:147],v232 offset:4608
CV v65,v66,v67
MM v[80:83],a[192:195],v[168:171],v[136:139]
EX v68,v68
MM v[80:83],a[196:199],v[184:187],v[80:83]
TR a[148:149],v232 offset:8704
TR a[150:151],v232 offset:12800
MM v[80:83],a[200:203],v[200:203],v[80:83]
EX v69,v69
MM v[80:83],a[204:207],v[216:219],v[80:83]
TR a[152:153],v233 offset:512
CV v66,v68,v69
MM v[84:87],a[220:223],v[216:219],v[136:139]
EX v70,v70
MM v[84:87],a[216:219],v[200:203],v[84:87]
EX v71,v71
MM v[84:87],a[212:215],v[184:187],v[84:87]
TR a[154:155],v233 offset:4608
CV v67,v70,v71
MM v[84:87],a[208:211],v[168:171],v[84:87]
EX v72,v72
MM v[92:95],a[208:211],v[172:175],v[140:143]
TR a[156:157],v233 offset:8704
TR a[158:159],v233 offset:12800
MM v[92:95],a[212:215],v[188:191],v[92:95]
EX v73,v73
MM v[92:95],a[216:219],v[204:207],v[92:95]
TR a[160:161],v232 offset:1024
CV v72,v72,v73
MM v[92:95],a[220:223],v[220:223],v[92:95]
EX v74,v74
MM v[88:91],a[204:207],v[220:223],v[140:143]
EX v75,v75
MM v[88:91],a[200:203],v[204:207],v[88:91]
TR a[162:163],v232 offset:5120
CV v73,v74,v75
MM v[88:91],a[196:199],v[188:191],v[88:91]
EX v76,v76
MM v[88:91],a[192:195],v[172:175],v[88:91]
TR a[164:165],v232 offset:9216
TR a[166:167],v232 offset:13312
MM v[96:99],a[224:227],v[160:163],v[128:131]
EX v77,v77
MM v[96:99],a[228:231],v[176:179],v[96:99]
TR a[168:169],v233 offset:1024
CV v74,v76,v77
MM v[96:99],a[232:235],v[192:195],v[96:99]
EX v78,v78
MM v[96:99],a[236:239],v[208:211],v[96:99]
EX v79,v79
MM v[100:103],a[252:255],v[208:211],v[128:131]
TR a[170:171],v233 offset:5120
CV v75,v78,v79
MM v[100:103],a[248:251],v[192:195],v[100:103]
EX v80,v80
MM v[100:103],a[244:247],v[176:179],v[100:103]
TR a[172:173],v233 offset:9216
TR a[174:175],v233 offset:13312
MM v[100:103],a[240:243],v[160:163],v[100:103]
EX v81,v81
s_nop 0
LPG16_%=:
s_cbranch_execz LTR2_%=
MM v[108:111],a[240:243],v[164:167],v[132:135]
TR a[176:177],v232 offset:1536
CV v80,v80,v81
MM v[108:111],a[244:247],v[180:183],v[108:111]
EX v82,v82
MM v[108:111],a[248:251],v[196:199],v[108:111]
TR a[178:179],v232 offset:5632
TR a[180:181],v232 offset:9728
MM v[108:111],a[252:255],v[212:215],v[108:111]
EX v83,v83
MM v[104:107],a[236:239],v[212:215],v[132:135]
TR a[182:183],v232 offset:13824
CV v81,v82,v83
MM v[104:107],a[232:235],v[196:199],v[104:107]
EX v84,v84
MM v[104:107],a[228:231],v[180:183],v[104:107]
EX v85,v85
MM v[104:107],a[224:227],v[164:167],v[104:107]
TR a[184:185],v233 offset:1536
CV v82,v84,v85
MM v[112:115],a[224:227],v[168:171],v[136:139]
EX v86,v86
MM v[112:115],a[228:231],v[184:187],v[112:115]
TR a[186:187],v233 offset:5632
TR a[188:189],v233 offset:9728
MM v[112:115],a[232:235],v[200:203],v[112:115]
EX v87,v87
MM v[112:115],a[236:239],v[216:219],v[112:115]
TR a[190:191],v233 offset:13824
CV v83,v86,v87
MM v[116:119],a[252:255],v[216:219],v[136:139]
EX v88,v88
MM v[116:119],a[248:251],v[200:203],v[116:119]
EX v89,v89
MM v[116:119],a[244:247],v[184:187],v[116:119]
EX v90,v90
MM v[116:119],a[240:243],v[168:171],v[116:119]
EX v91,v91
MM v[124:127],a[240:243],v[172:175],v[140:143]
EX v92,v92
MM v[124:127],a[244:247],v[188:191],v[124:127]
EX v93,v93
MM v[124:127],a[248:251],v[204:207],v[124:127]
EX v94,v94
MM v[124:127],a[252:255],v[220:223],v[124:127]
EX v95,v95
MM v[120:123],a[236:239],v[220:223],v[140:143]
EX v96,v96
MM v[120:123],a[232:235],v[204:207],v[120:123]
EX v97,v97
MM v[120:123],a[228:231],v[188:191],v[120:123]
s_getpc_b64 s[98:99]
LTA3_%=:
s_min_u32 s73, %[swave], 3
s_lshl_b32 s73, s73, 12
s_add_u32 s73, s73, LPG17_%=-LTA3_%=
s_add_u32 s98, s98, s73
s_addc_u32 s99, s99, 0
s_mov_b64 exec, 0
s_setpc_b64 s[98:99]
LTR3_%=:
s_mov_b64 exec, -1
CV v88,v88,v89
MM v[120:123],a[224:227],v[172:175],v[120:123]
s_waitcnt vmcnt(20) lgkmcnt(0)
s_barrier
MM a[0:3],a[128:131],v[0:3],a[0:3]
s_add_u32 m0, s65, 0
CV v89,v90,v91
CV v90,v92,v93
MM a[0:3],a[132:135],v[4:7],a[0:3]
RD a[192:195],v224 offset:32768
CV v91,v94,v95
MM a[8:11],a[132:135],v[12:15],a[8:11]
buffer_load_dwordx4 v228, s[8:11], s64 offen offset:0 lds
MM a[8:11],a[128:131],v[8:11],a[8:11]
RD a[196:199],v225 offset:32768
CV v68,v96,v97
MM a[16:19],a[128:131],v[16:19],a[16:19]
RD a[200:203],v226 offset:32768
RD a[204:207],v227 offset:32768
MM a[16:19],a[132:135],v[20:23],a[16:19]
buffer_load_dwordx4 v229, s[8:11], s64 offen offset:1024 lds
MM a[24:27],a[132:135],v[28:31],a[24:27]
EX v98,v98
MM a[24:27],a[128:131],v[24:27],a[24:27]
RD a[208:211],v224 offset:36864
RD a[212:215],v225 offset:36864
MM a[28:31],a[136:139],v[24:27],a[28:31]
EX v99,v99
MM a[28:31],a[140:143],v[28:31],a[28:31]
RD a[216:219],v226 offset:36864
CV v69,v98,v99
MM a[20:23],a[140:143],v[20:23],a[20:23]
EX v100,v100
MM a[20:23],a[136:139],v[16:19],a[20:23]
EX v101,v101
MM a[12:15],a[136:139],v[8:11],a[12:15]
RD a[220:223],v227 offset:36864
CV v70,v100,v101
MM a[12:15],a[140:143],v[12:15],a[12:15]
buffer_load_dwordx4 v230, s[8:11], s64 offen offset:2048 lds
MM a[4:7],a[140:143],v[4:7],a[4:7]
EX v102,v102
MM a[4:7],a[136:139],v[0:3],a[4:7]
EX v103,v103
MM a[32:35],a[144:147],v[0:3],a[32:35]
RD a[224:227],v224 offset:40960
CV v71,v102,v103
MM a[32:35],a[148:151],v[4:7],a[32:35]
EX v104,v104
MM a[40:43],a[148:151],v[12:15],a[40:43]
EX v105,v105
MM a[40:43],a[144:147],v[8:11],a[40:43]
RD a[228:231],v225 offset:40960
CV v76,v104,v105
MM a[48:51],a[144:147],v[16:19],a[48:51]
buffer_load_dwordx4 v231, s[8:11], s64 offen offset:3072 lds
MM a[48:51],a[148:151],v[20:23],a[48:51]
s_add_u32 m0, s65, 65536
EX v106,v106
MM a[56:59],a[148:151],v[28:31],a[56:59]
EX v107,v107
MM a[56:59],a[144:147],v[24:27],a[56:59]
RD a[232:235],v226 offset:40960
CV v77,v106,v107
MM a[60:63],a[152:155],v[24:27],a[60:63]
EX v108,v108
MM a[60:63],a[156:159],v[28:31],a[60:63]
buffer_load_dwordx4 v235, s[12:15], s81 offen offset:0 lds
MM a[52:55],a[156:159],v[20:23],a[52:55]
EX v109,v109
MM a[52:55],a[152:155],v[16:19],a[52:55]
RD a[236:239],v227 offset:40960
CV v78,v108,v109
MM a[44:47],a[152:155],v[8:11],a[44:47]
EX v110,v110
MM a[44:47],a[156:159],v[12:15],a[44:47]
EX v111,v111
MM a[36:39],a[156:159],v[4:7],a[36:39]
RD a[240:243],v224 offset:45056
CV v79,v110,v111
MM a[36:39],a[152:155],v[0:3],a[36:39]
EX v112,v112
MM a[64:67],a[160:163],v[0:3],a[64:67]
buffer_load_dwordx4 v235, s[12:15], s64 offen offset:1024 lds
MM a[64:67],a[164:167],v[4:7],a[64:67]
EX v113,v113
MM a[72:75],a[164:167],v[12:15],a[72:75]
RD a[244:247],v225 offset:45056
CV v84,v112,v113
MM a[72:75],a[160:163],v[8:11],a[72:75]
EX v114,v114
MM a[80:83],a[160:163],v[16:19],a[80:83]
EX v115,v115
MM a[80:83],a[164:167],v[20:23],a[80:83]
RD a[248:251],v226 offset:45056
CV v85,v114,v115
MM a[88:91],a[164:167],v[28:31],a[88:91]
EX v116,v116
MM a[88:91],a[160:163],v[24:27],a[88:91]
buffer_load_dwordx4 v235, s[12:15], s81 offen offset:2048 lds
MM a[92:95],a[168:171],v[24:27],a[92:95]
EX v117,v117
MM a[92:95],a[172:175],v[28:31],a[92:95]
RD a[252:255],v227 offset:45056
CV v86,v116,v117
MM a[84:87],a[172:175],v[20:23],a[84:87]
EX v118,v118
MM a[84:87],a[168:171],v[16:19],a[84:87]
EX v119,v119
MM a[76:79],a[168:171],v[8:11],a[76:79]
CV v87,v118,v119
MM a[76:79],a[172:175],v[12:15],a[76:79]
EX v120,v120
MM a[68:71],a[172:175],v[4:7],a[68:71]
buffer_load_dwordx4 v235, s[12:15], s64 offen offset:3072 lds
MM a[68:71],a[168:171],v[0:3],a[68:71]
EX v121,v121
MM a[96:99],a[176:179],v[0:3],a[96:99]
EX v122,v122
MM a[96:99],a[180:183],v[4:7],a[96:99]
EX v123,v123
MM a[104:107],a[180:183],v[12:15],a[104:107]
EX v124,v124
MM a[104:107],a[176:179],v[8:11],a[104:107]
EX v125,v125
MM a[112:115],a[176:179],v[16:19],a[112:115]
EX v126,v126
MM a[112:115],a[180:183],v[20:23],a[112:115]
EX v127,v127
MM a[120:123],a[180:183],v[28:31],a[120:123]
s_waitcnt vmcnt(8)
v_cmp_ne_u32 vcc, s69, v252
s_cbranch_vccnz LRFG_%=
LRFGb_%=:
MM a[120:123],a[176:179],v[24:27],a[120:123]
v_lshrrev_b32 v253, 8, v248
v_lshlrev_b32 v253, 10, v253
MM a[124:127],a[184:187],v[24:27],a[124:127]
v_add_f32 v244, v144, v145
v_add_f32 v245, v148, v149
MM a[124:127],a[188:191],v[28:31],a[124:127]
v_add_f32 v246, v152, v153
v_add_f32 v247, v156, v157
v_and_b32 v252, 240, v248
MM a[116:119],a[188:191],v[20:23],a[116:119]
v_add_u32 v253, v253, v252
CV v92,v120,v121
MM a[116:119],a[184:187],v[16:19],a[116:119]
s_lshr_b32 s76, s18, 6
CV v93,v122,v123
CV v94,v124,v125
MM a[108:111],a[184:187],v[8:11],a[108:111]
s_lshl_b32 s76, s76, 13
s_bfe_u32 s90, s18, 131076
CV v95,v126,v127
MM a[108:111],a[188:191],v[12:15],a[108:111]
s_lshl_b32 s90, s90, 8
s_add_u32 s76, s76, s90
s_add_u32 s76, s76, 22183936
MM a[100:103],a[188:191],v[4:7],a[100:103]
s_lshl_b32 s90, %[swave], 11
s_add_u32 s90, s90, %[slds]
MM a[100:103],a[184:187],v[0:3],a[100:103]
s_add_u32 m0, s90, 131072
s_nop 0
buffer_load_dwordx4 v253, s[20:23], s76 offen offset:0 sc1 lds
v_smfmac_f32_16x16x64_bf16 v[144:147], v[236:239], v[64:71], v234
v_smfmac_f32_16x16x64_bf16 v[148:151], v[236:239], v[72:79], v234
v_smfmac_f32_16x16x64_bf16 v[152:155], v[236:239], v[80:87], v234
s_add_u32 m0, s90, 132096
s_add_u32 s76, s76, 4096
buffer_load_dwordx4 v253, s[20:23], s76 offen offset:0 sc1 lds
v_smfmac_f32_16x16x64_bf16 v[156:159], v[236:239], v[88:95], v234
s_waitcnt lgkmcnt(0)
s_add_u32 s67, s95, 3072
s_mov_b32 s70, 3
s_cmp_ge_u32 s16, 90
s_cbranch_scc1 LLAP_%=
MM v[0:3],a[192:195],v[160:163],v[128:131]
s_add_u32 s64, s64, 16384
TR a[128:129],v232 offset:16384
s_add_u32 s81, s64, 896
MM v[0:3],a[196:199],v[176:179],v[0:3]
TR a[130:131],v232 offset:20480
v_max3_f32 v249, v144, v145, v148
)ASM"
R"ASM(
MM v[0:3],a[200:203],v[192:195],v[0:3]
TR a[132:133],v232 offset:24576
v_max3_f32 v249, v249, v149, v152
MM v[0:3],a[204:207],v[208:211],v[0:3]
TR a[134:135],v232 offset:28672
v_max3_f32 v249, v249, v153, v156
MM v[4:7],a[220:223],v[208:211],v[128:131]
TR a[136:137],v233 offset:16384
v_max_f32 v249, v249, v157
MM v[4:7],a[216:219],v[192:195],v[4:7]
TR a[138:139],v233 offset:20480
v_cmp_lt_f32 vcc, s71, v249
MM v[4:7],a[212:215],v[176:179],v[4:7]
TR a[140:141],v233 offset:24576
TR a[142:143],v233 offset:28672
MM v[4:7],a[208:211],v[160:163],v[4:7]
s_cbranch_vccnz LSITE21_%=
LBACK21_%=:
MM v[12:15],a[208:211],v[164:167],v[132:135]
EX v0,v0
MM v[12:15],a[212:215],v[180:183],v[12:15]
EX v1,v1
MM v[12:15],a[216:219],v[196:199],v[12:15]
TR a[144:145],v232 offset:16896
CV v0,v0,v1
MM v[12:15],a[220:223],v[212:215],v[12:15]
EX v2,v2
MM v[8:11],a[204:207],v[212:215],v[132:135]
EX v3,v3
MM v[8:11],a[200:203],v[196:199],v[8:11]
TR a[146:147],v232 offset:20992
CV v1,v2,v3
MM v[8:11],a[196:199],v[180:183],v[8:11]
EX v4,v4
MM v[8:11],a[192:195],v[164:167],v[8:11]
TR a[148:149],v232 offset:25088
TR a[150:151],v232 offset:29184
MM v[16:19],a[192:195],v[168:171],v[136:139]
EX v5,v5
MM v[16:19],a[196:199],v[184:187],v[16:19]
TR a[152:153],v233 offset:16896
CV v2,v4,v5
MM v[16:19],a[200:203],v[200:203],v[16:19]
EX v6,v6
MM v[16:19],a[204:207],v[216:219],v[16:19]
TR a[154:155],v233 offset:20992
TR a[156:157],v233 offset:25088
MM v[20:23],a[220:223],v[216:219],v[136:139]
s_add_u32 s87, s95, 3072
buffer_store_dword v243, v248, s[20:23], s87 offen offset:12 sc1
MM v[20:23],a[216:219],v[200:203],v[20:23]
EX v7,v7
MM v[20:23],a[212:215],v[184:187],v[20:23]
TR a[158:159],v233 offset:29184
CV v3,v6,v7
MM v[20:23],a[208:211],v[168:171],v[20:23]
EX v8,v8
MM v[28:31],a[208:211],v[172:175],v[140:143]
EX v9,v9
MM v[28:31],a[212:215],v[188:191],v[28:31]
TR a[160:161],v232 offset:17408
CV v8,v8,v9
MM v[28:31],a[216:219],v[204:207],v[28:31]
EX v10,v10
MM v[28:31],a[220:223],v[220:223],v[28:31]
EX v11,v11
MM v[24:27],a[204:207],v[220:223],v[140:143]
TR a[162:163],v232 offset:21504
CV v9,v10,v11
MM v[24:27],a[200:203],v[204:207],v[24:27]
TR a[164:165],v232 offset:25600
TR a[166:167],v232 offset:29696
MM v[24:27],a[196:199],v[188:191],v[24:27]
EX v12,v12
MM v[24:27],a[192:195],v[172:175],v[24:27]
EX v13,v13
MM v[32:35],a[224:227],v[160:163],v[128:131]
TR a[168:169],v233 offset:17408
CV v10,v12,v13
MM v[32:35],a[228:231],v[176:179],v[32:35]
EX v14,v14
MM v[32:35],a[232:235],v[192:195],v[32:35]
TR a[170:171],v233 offset:21504
TR a[172:173],v233 offset:25600
MM v[32:35],a[236:239],v[208:211],v[32:35]
EX v15,v15
MM v[36:39],a[252:255],v[208:211],v[128:131]
TR a[174:175],v233 offset:29696
CV v11,v14,v15
MM v[36:39],a[248:251],v[192:195],v[36:39]
EX v16,v16
MM v[36:39],a[244:247],v[176:179],v[36:39]
EX v17,v17
MM v[36:39],a[240:243],v[160:163],v[36:39]
TR a[176:177],v232 offset:17920
CV v16,v16,v17
MM v[44:47],a[240:243],v[164:167],v[132:135]
EX v18,v18
MM v[44:47],a[244:247],v[180:183],v[44:47]
TR a[178:179],v232 offset:22016
TR a[180:181],v232 offset:26112
MM v[44:47],a[248:251],v[196:199],v[44:47]
EX v19,v19
MM v[44:47],a[252:255],v[212:215],v[44:47]
TR a[182:183],v232 offset:30208
CV v17,v18,v19
MM v[40:43],a[236:239],v[212:215],v[132:135]
EX v20,v20
MM v[40:43],a[232:235],v[196:199],v[40:43]
EX v21,v21
MM v[40:43],a[228:231],v[180:183],v[40:43]
TR a[184:185],v233 offset:17920
CV v18,v20,v21
MM v[40:43],a[224:227],v[164:167],v[40:43]
EX v22,v22
MM v[48:51],a[224:227],v[168:171],v[136:139]
TR a[186:187],v233 offset:22016
TR a[188:189],v233 offset:26112
MM v[48:51],a[228:231],v[184:187],v[48:51]
EX v23,v23
MM v[48:51],a[232:235],v[200:203],v[48:51]
TR a[190:191],v233 offset:30208
CV v19,v22,v23
MM v[48:51],a[236:239],v[216:219],v[48:51]
EX v24,v24
MM v[52:55],a[252:255],v[216:219],v[136:139]
EX v25,v25
MM v[52:55],a[248:251],v[200:203],v[52:55]
EX v26,v26
MM v[52:55],a[244:247],v[184:187],v[52:55]
EX v27,v27
MM v[52:55],a[240:243],v[168:171],v[52:55]
EX v28,v28
MM v[60:63],a[240:243],v[172:175],v[140:143]
EX v29,v29
MM v[60:63],a[244:247],v[188:191],v[60:63]
EX v30,v30
MM v[60:63],a[248:251],v[204:207],v[60:63]
EX v31,v31
MM v[60:63],a[252:255],v[220:223],v[60:63]
EX v32,v32
MM v[56:59],a[236:239],v[220:223],v[140:143]
EX v33,v33
MM v[56:59],a[232:235],v[204:207],v[56:59]
EX v34,v34
MM v[56:59],a[228:231],v[188:191],v[56:59]
EX v35,v35
MM v[56:59],a[224:227],v[172:175],v[56:59]
s_waitcnt lgkmcnt(0)
s_barrier
MM a[0:3],a[128:131],v[64:67],a[0:3]
s_add_u32 m0, s65, 16384
CV v24,v24,v25
CV v25,v26,v27
MM a[0:3],a[132:135],v[68:71],a[0:3]
RD a[192:195],v224 offset:49152
CV v26,v28,v29
MM a[8:11],a[132:135],v[76:79],a[8:11]
RD a[196:199],v225 offset:49152
CV v27,v30,v31
MM a[8:11],a[128:131],v[72:75],a[8:11]
buffer_load_dwordx4 v228, s[8:11], s64 offen offset:0 lds
MM a[16:19],a[128:131],v[80:83],a[16:19]
RD a[200:203],v226 offset:49152
CV v4,v32,v33
MM a[16:19],a[132:135],v[84:87],a[16:19]
RD a[204:207],v227 offset:49152
CV v5,v34,v35
MM a[24:27],a[132:135],v[92:95],a[24:27]
buffer_load_dwordx4 v229, s[8:11], s64 offen offset:1024 lds
MM a[24:27],a[128:131],v[88:91],a[24:27]
RD a[208:211],v224 offset:53248
RD a[212:215],v225 offset:53248
MM a[28:31],a[136:139],v[88:91],a[28:31]
buffer_load_dwordx4 v230, s[8:11], s64 offen offset:2048 lds
MM a[28:31],a[140:143],v[92:95],a[28:31]
EX v36,v36
MM a[20:23],a[140:143],v[84:87],a[20:23]
EX v37,v37
MM a[20:23],a[136:139],v[80:83],a[20:23]
RD a[216:219],v226 offset:53248
CV v6,v36,v37
MM a[12:15],a[136:139],v[72:75],a[12:15]
EX v38,v38
MM a[12:15],a[140:143],v[76:79],a[12:15]
RD a[220:223],v227 offset:53248
RD a[224:227],v224 offset:57344
MM a[4:7],a[140:143],v[68:71],a[4:7]
EX v39,v39
MM a[4:7],a[136:139],v[64:67],a[4:7]
RD a[228:231],v225 offset:57344
CV v7,v38,v39
MM a[32:35],a[144:147],v[64:67],a[32:35]
EX v40,v40
MM a[32:35],a[148:151],v[68:71],a[32:35]
buffer_load_dwordx4 v231, s[8:11], s64 offen offset:3072 lds
MM a[40:43],a[148:151],v[76:79],a[40:43]
s_add_u32 m0, s65, 81920
EX v41,v41
MM a[40:43],a[144:147],v[72:75],a[40:43]
RD a[232:235],v226 offset:57344
CV v12,v40,v41
MM a[48:51],a[144:147],v[80:83],a[48:51]
EX v42,v42
MM a[48:51],a[148:151],v[84:87],a[48:51]
EX v43,v43
MM a[56:59],a[148:151],v[92:95],a[56:59]
RD a[236:239],v227 offset:57344
CV v13,v42,v43
MM a[56:59],a[144:147],v[88:91],a[56:59]
EX v44,v44
MM a[60:63],a[152:155],v[88:91],a[60:63]
buffer_load_dwordx4 v235, s[12:15], s81 offen offset:0 lds
MM a[60:63],a[156:159],v[92:95],a[60:63]
EX v45,v45
MM a[52:55],a[156:159],v[84:87],a[52:55]
RD a[240:243],v224 offset:61440
CV v14,v44,v45
MM a[52:55],a[152:155],v[80:83],a[52:55]
EX v46,v46
MM a[44:47],a[152:155],v[72:75],a[44:47]
EX v47,v47
MM a[44:47],a[156:159],v[76:79],a[44:47]
RD a[244:247],v225 offset:61440
CV v15,v46,v47
MM a[36:39],a[156:159],v[68:71],a[36:39]
buffer_load_dwordx4 v235, s[12:15], s64 offen offset:1024 lds
MM a[36:39],a[152:155],v[64:67],a[36:39]
EX v48,v48
MM a[64:67],a[160:163],v[64:67],a[64:67]
EX v49,v49
MM a[64:67],a[164:167],v[68:71],a[64:67]
RD a[248:251],v226 offset:61440
CV v20,v48,v49
MM a[72:75],a[164:167],v[76:79],a[72:75]
EX v50,v50
MM a[72:75],a[160:163],v[72:75],a[72:75]
EX v51,v51
MM a[80:83],a[160:163],v[80:83],a[80:83]
RD a[252:255],v227 offset:61440
CV v21,v50,v51
MM a[80:83],a[164:167],v[84:87],a[80:83]
buffer_load_dwordx4 v235, s[12:15], s81 offen offset:2048 lds
MM a[88:91],a[164:167],v[92:95],a[88:91]
EX v52,v52
MM a[88:91],a[160:163],v[88:91],a[88:91]
EX v53,v53
MM a[92:95],a[168:171],v[88:91],a[92:95]
EX v54,v54
MM a[92:95],a[172:175],v[92:95],a[92:95]
buffer_load_dwordx4 v235, s[12:15], s64 offen offset:3072 lds
MM a[84:87],a[172:175],v[84:87],a[84:87]
EX v55,v55
MM a[84:87],a[168:171],v[80:83],a[84:87]
EX v56,v56
MM a[76:79],a[168:171],v[72:75],a[76:79]
EX v57,v57
MM a[76:79],a[172:175],v[76:79],a[76:79]
EX v58,v58
MM a[68:71],a[172:175],v[68:71],a[68:71]
EX v59,v59
MM a[68:71],a[168:171],v[64:67],a[68:71]
EX v60,v60
MM a[96:99],a[176:179],v[64:67],a[96:99]
EX v61,v61
MM a[96:99],a[180:183],v[68:71],a[96:99]
EX v62,v62
MM a[104:107],a[180:183],v[76:79],a[104:107]
EX v63,v63
MM a[104:107],a[176:179],v[72:75],a[104:107]
CV v22,v52,v53
CV v23,v54,v55
MM a[112:115],a[176:179],v[80:83],a[112:115]
CV v28,v56,v57
CV v29,v58,v59
MM a[112:115],a[180:183],v[84:87],a[112:115]
CV v30,v60,v61
CV v31,v62,v63
MM a[120:123],a[180:183],v[92:95],a[120:123]
MM a[120:123],a[176:179],v[88:91],a[120:123]
MM a[124:127],a[184:187],v[88:91],a[124:127]
v_add_f32 v244, v144, v145
v_add_f32 v245, v148, v149
MM a[124:127],a[188:191],v[92:95],a[124:127]
v_add_f32 v246, v152, v153
v_add_f32 v247, v156, v157
MM a[116:119],a[188:191],v[84:87],a[116:119]
MM a[116:119],a[184:187],v[80:83],a[116:119]
MM a[108:111],a[184:187],v[72:75],a[108:111]
MM a[108:111],a[188:191],v[76:79],a[108:111]
MM a[100:103],a[188:191],v[68:71],a[100:103]
MM a[100:103],a[184:187],v[64:67],a[100:103]
v_smfmac_f32_16x16x64_bf16 v[144:147], v[236:239], v[0:7], v234
v_smfmac_f32_16x16x64_bf16 v[148:151], v[236:239], v[8:15], v234
v_smfmac_f32_16x16x64_bf16 v[152:155], v[236:239], v[16:23], v234
v_smfmac_f32_16x16x64_bf16 v[156:159], v[236:239], v[24:31], v234
s_waitcnt lgkmcnt(0)
MM v[64:67],a[192:195],v[160:163],v[128:131]
s_add_u32 s64, s64, 16384
TR a[128:129],v232 offset:32768
s_add_u32 s81, s64, 896
MM v[64:67],a[196:199],v[176:179],v[64:67]
TR a[130:131],v232 offset:36864
v_max3_f32 v249, v144, v145, v148
MM v[64:67],a[200:203],v[192:195],v[64:67]
TR a[132:133],v232 offset:40960
v_max3_f32 v249, v249, v149, v152
MM v[64:67],a[204:207],v[208:211],v[64:67]
TR a[134:135],v232 offset:45056
v_max3_f32 v249, v249, v153, v156
MM v[68:71],a[220:223],v[208:211],v[128:131]
TR a[136:137],v233 offset:32768
v_max_f32 v249, v249, v157
MM v[68:71],a[216:219],v[192:195],v[68:71]
TR a[138:139],v233 offset:36864
v_cmp_lt_f32 vcc, s71, v249
MM v[68:71],a[212:215],v[176:179],v[68:71]
TR a[140:141],v233 offset:40960
TR a[142:143],v233 offset:45056
MM v[68:71],a[208:211],v[160:163],v[68:71]
s_cbranch_vccnz LSITE22_%=
LBACK22_%=:
MM v[76:79],a[208:211],v[164:167],v[132:135]
v_lshrrev_b32 v43, 7, v248
v_bfe_u32 v42, v248, 6, 1
MM v[76:79],a[212:215],v[180:183],v[76:79]
v_lshl_add_u32 v40, v42, 3, v43
v_lshlrev_b32 v40, 4, v40
MM v[76:79],a[216:219],v[196:199],v[76:79]
v_bfe_u32 v42, v248, 4, 2
v_lshl_add_u32 v40, v42, 2, v40
MM v[76:79],a[220:223],v[212:215],v[76:79]
s_lshl_b32 s76, %[swave], 11
EX v64,v64
MM v[72:75],a[204:207],v[212:215],v[132:135]
s_add_u32 s76, s76, %[slds]
EX v65,v65
MM v[72:75],a[200:203],v[196:199],v[72:75]
s_add_u32 s76, s76, 131072
v_add_u32 v40, s76, v40
CV v64,v64,v65
MM v[72:75],a[196:199],v[180:183],v[72:75]
s_waitcnt vmcnt(9)
ds_read_b32 v32, v40 offset:0
ds_read_b32 v36, v40 offset:256
MM v[72:75],a[192:195],v[164:167],v[72:75]
ds_read_b32 v33, v40 offset:512
ds_read_b32 v37, v40 offset:768
MM v[80:83],a[192:195],v[168:171],v[136:139]
ds_read_b32 v34, v40 offset:1024
s_nop 0
LPG17_%=:
s_cbranch_execz LTR3_%=
ds_read_b32 v38, v40 offset:1280
EX v66,v66
MM v[80:83],a[196:199],v[184:187],v[80:83]
ds_read_b32 v35, v40 offset:1536
ds_read_b32 v39, v40 offset:1792
EX v67,v67
MM v[80:83],a[200:203],v[200:203],v[80:83]
v_mul_u32_u24 v41, 3456, v43
v_bfe_u32 v42, v248, 4, 3
MM v[80:83],a[204:207],v[216:219],v[80:83]
TR a[144:145],v232 offset:33280
v_lshl_add_u32 v41, v42, 1, v41
EX v68,v68
MM v[84:87],a[220:223],v[216:219],v[136:139]
s_add_u32 s76, s18, 21626880
EX v69,v69
MM v[84:87],a[216:219],v[200:203],v[84:87]
TR a[146:147],v232 offset:37376
TR a[148:149],v232 offset:41472
MM v[84:87],a[212:215],v[184:187],v[84:87]
EX v70,v70
MM v[84:87],a[208:211],v[168:171],v[84:87]
TR a[150:151],v232 offset:45568
CV v65,v66,v67
MM v[92:95],a[208:211],v[172:175],v[140:143]
EX v71,v71
MM v[92:95],a[212:215],v[188:191],v[92:95]
EX v72,v72
MM v[92:95],a[216:219],v[204:207],v[92:95]
TR a[152:153],v233 offset:33280
CV v66,v68,v69
MM v[92:95],a[220:223],v[220:223],v[92:95]
EX v73,v73
MM v[88:91],a[204:207],v[220:223],v[140:143]
s_waitcnt lgkmcnt(5)
v_add_f32 v32, v32, v33
v_add_f32 v36, v36, v37
)ASM"
R"ASM(
MM v[88:91],a[200:203],v[204:207],v[88:91]
v_add_f32 v34, v34, v35
v_add_f32 v38, v38, v39
MM v[88:91],a[196:199],v[188:191],v[88:91]
TR a[154:155],v233 offset:37376
v_add_f32 v32, v32, v34
v_add_f32 v36, v36, v38
MM v[88:91],a[192:195],v[172:175],v[88:91]
CV v42,v32,v36
v_lshlrev_b32 v32, 16, v42
EX v74,v74
MM v[96:99],a[224:227],v[160:163],v[128:131]
TR a[156:157],v233 offset:41472
v_and_b32 v36, 0xffff0000, v42
v_mul_f32 v42, 0xbfb8aa3b, v32
EX v75,v75
MM v[96:99],a[228:231],v[176:179],v[96:99]
EX v42,v42
MM v[96:99],a[232:235],v[192:195],v[96:99]
TR a[158:159],v233 offset:45568
v_add_f32 v42, 1.0, v42
EX v76,v76
MM v[96:99],a[236:239],v[208:211],v[96:99]
v_rcp_f32 v42, v42
MM v[100:103],a[252:255],v[208:211],v[128:131]
TR a[160:161],v232 offset:33792
v_mul_f32 v42, v32, v42
CV v42,v42,v42
EX v77,v77
CV v67,v70,v71
MM v[100:103],a[248:251],v[192:195],v[100:103]
v_lshlrev_b32 v42, 16, v42
v_mul_f32 v32, v42, v36
MM v[100:103],a[244:247],v[176:179],v[100:103]
TR a[162:163],v232 offset:37888
CV v43,v32,v32
EX v78,v78
MM v[100:103],a[240:243],v[160:163],v[100:103]
buffer_store_short v43, v41, s[20:23], s76 offen offset:0 sc1
s_nop 1
TR a[164:165],v232 offset:41984
MM v[108:111],a[240:243],v[164:167],v[132:135]
EX v79,v79
CV v72,v72,v73
MM v[108:111],a[244:247],v[180:183],v[108:111]
TR a[166:167],v232 offset:46080
CV v73,v74,v75
MM v[108:111],a[248:251],v[196:199],v[108:111]
EX v80,v80
MM v[108:111],a[252:255],v[212:215],v[108:111]
TR a[168:169],v233 offset:33792
TR a[170:171],v233 offset:37888
EX v81,v81
MM v[104:107],a[236:239],v[212:215],v[132:135]
TR a[172:173],v233 offset:41984
CV v74,v76,v77
MM v[104:107],a[232:235],v[196:199],v[104:107]
EX v82,v82
MM v[104:107],a[228:231],v[180:183],v[104:107]
EX v83,v83
MM v[104:107],a[224:227],v[164:167],v[104:107]
TR a[174:175],v233 offset:46080
CV v75,v78,v79
MM v[112:115],a[224:227],v[168:171],v[136:139]
EX v84,v84
MM v[112:115],a[228:231],v[184:187],v[112:115]
TR a[176:177],v232 offset:34304
TR a[178:179],v232 offset:38400
MM v[112:115],a[232:235],v[200:203],v[112:115]
EX v85,v85
MM v[112:115],a[236:239],v[216:219],v[112:115]
TR a[180:181],v232 offset:42496
CV v80,v80,v81
MM v[116:119],a[252:255],v[216:219],v[136:139]
EX v86,v86
MM v[116:119],a[248:251],v[200:203],v[116:119]
TR a[182:183],v232 offset:46592
CV v81,v82,v83
MM v[116:119],a[244:247],v[184:187],v[116:119]
EX v87,v87
MM v[116:119],a[240:243],v[168:171],v[116:119]
TR a[184:185],v233 offset:34304
CV v82,v84,v85
MM v[124:127],a[240:243],v[172:175],v[140:143]
EX v88,v88
MM v[124:127],a[244:247],v[188:191],v[124:127]
TR a[186:187],v233 offset:38400
TR a[188:189],v233 offset:42496
MM v[124:127],a[248:251],v[204:207],v[124:127]
TR a[190:191],v233 offset:46592
CV v83,v86,v87
EX v89,v89
MM v[124:127],a[252:255],v[220:223],v[124:127]
EX v90,v90
MM v[120:123],a[236:239],v[220:223],v[140:143]
EX v91,v91
MM v[120:123],a[232:235],v[204:207],v[120:123]
EX v92,v92
MM v[120:123],a[228:231],v[188:191],v[120:123]
EX v93,v93
MM v[120:123],a[224:227],v[172:175],v[120:123]
s_waitcnt lgkmcnt(0)
s_barrier
MM a[0:3],a[128:131],v[0:3],a[0:3]
s_add_u32 m0, s65, 32768
CV v88,v88,v89
CV v89,v90,v91
MM a[0:3],a[132:135],v[4:7],a[0:3]
EX v94,v94
MM a[8:11],a[132:135],v[12:15],a[8:11]
RD a[192:195],v224 offset:0
CV v90,v92,v93
MM a[8:11],a[128:131],v[8:11],a[8:11]
EX v95,v95
MM a[16:19],a[128:131],v[16:19],a[16:19]
RD a[196:199],v225 offset:0
CV v91,v94,v95
MM a[16:19],a[132:135],v[20:23],a[16:19]
EX v96,v96
MM a[24:27],a[132:135],v[28:31],a[24:27]
buffer_load_dwordx4 v228, s[8:11], s64 offen offset:0 lds
MM a[24:27],a[128:131],v[24:27],a[24:27]
EX v97,v97
MM a[28:31],a[136:139],v[24:27],a[28:31]
EX v98,v98
MM a[28:31],a[140:143],v[28:31],a[28:31]
RD a[200:203],v226 offset:0
CV v68,v96,v97
MM a[20:23],a[140:143],v[20:23],a[20:23]
EX v99,v99
MM a[20:23],a[136:139],v[16:19],a[20:23]
RD a[204:207],v227 offset:0
CV v69,v98,v99
MM a[12:15],a[136:139],v[8:11],a[12:15]
EX v100,v100
MM a[12:15],a[140:143],v[12:15],a[12:15]
buffer_load_dwordx4 v229, s[8:11], s64 offen offset:1024 lds
MM a[4:7],a[140:143],v[4:7],a[4:7]
EX v101,v101
MM a[4:7],a[136:139],v[0:3],a[4:7]
RD a[208:211],v224 offset:4096
CV v70,v100,v101
MM a[32:35],a[144:147],v[0:3],a[32:35]
EX v102,v102
MM a[32:35],a[148:151],v[4:7],a[32:35]
EX v103,v103
MM a[40:43],a[148:151],v[12:15],a[40:43]
RD a[212:215],v225 offset:4096
CV v71,v102,v103
MM a[40:43],a[144:147],v[8:11],a[40:43]
EX v104,v104
MM a[48:51],a[144:147],v[16:19],a[48:51]
buffer_load_dwordx4 v230, s[8:11], s64 offen offset:2048 lds
MM a[48:51],a[148:151],v[20:23],a[48:51]
EX v105,v105
MM a[56:59],a[148:151],v[28:31],a[56:59]
RD a[216:219],v226 offset:4096
CV v76,v104,v105
MM a[56:59],a[144:147],v[24:27],a[56:59]
EX v106,v106
MM a[60:63],a[152:155],v[24:27],a[60:63]
EX v107,v107
MM a[60:63],a[156:159],v[28:31],a[60:63]
RD a[220:223],v227 offset:4096
CV v77,v106,v107
MM a[52:55],a[156:159],v[20:23],a[52:55]
EX v108,v108
MM a[52:55],a[152:155],v[16:19],a[52:55]
buffer_load_dwordx4 v231, s[8:11], s64 offen offset:3072 lds
MM a[44:47],a[152:155],v[8:11],a[44:47]
s_add_u32 m0, s65, 98304
EX v109,v109
MM a[44:47],a[156:159],v[12:15],a[44:47]
RD a[224:227],v224 offset:8192
CV v78,v108,v109
MM a[36:39],a[156:159],v[4:7],a[36:39]
EX v110,v110
MM a[36:39],a[152:155],v[0:3],a[36:39]
EX v111,v111
MM a[64:67],a[160:163],v[0:3],a[64:67]
RD a[228:231],v225 offset:8192
CV v79,v110,v111
MM a[64:67],a[164:167],v[4:7],a[64:67]
EX v112,v112
MM a[72:75],a[164:167],v[12:15],a[72:75]
buffer_load_dwordx4 v235, s[12:15], s81 offen offset:0 lds
MM a[72:75],a[160:163],v[8:11],a[72:75]
EX v113,v113
MM a[80:83],a[160:163],v[16:19],a[80:83]
RD a[232:235],v226 offset:8192
CV v84,v112,v113
MM a[80:83],a[164:167],v[20:23],a[80:83]
EX v114,v114
MM a[88:91],a[164:167],v[28:31],a[88:91]
EX v115,v115
MM a[88:91],a[160:163],v[24:27],a[88:91]
RD a[236:239],v227 offset:8192
CV v85,v114,v115
MM a[92:95],a[168:171],v[24:27],a[92:95]
EX v116,v116
MM a[92:95],a[172:175],v[28:31],a[92:95]
buffer_load_dwordx4 v235, s[12:15], s64 offen offset:1024 lds
MM a[84:87],a[172:175],v[20:23],a[84:87]
EX v117,v117
MM a[84:87],a[168:171],v[16:19],a[84:87]
RD a[240:243],v224 offset:12288
CV v86,v116,v117
MM a[76:79],a[168:171],v[8:11],a[76:79]
EX v118,v118
MM a[76:79],a[172:175],v[12:15],a[76:79]
EX v119,v119
MM a[68:71],a[172:175],v[4:7],a[68:71]
RD a[244:247],v225 offset:12288
CV v87,v118,v119
MM a[68:71],a[168:171],v[0:3],a[68:71]
buffer_load_dwordx4 v235, s[12:15], s81 offen offset:2048 lds
MM a[96:99],a[176:179],v[0:3],a[96:99]
EX v120,v120
MM a[96:99],a[180:183],v[4:7],a[96:99]
EX v121,v121
MM a[104:107],a[180:183],v[12:15],a[104:107]
RD a[248:251],v226 offset:12288
CV v92,v120,v121
MM a[104:107],a[176:179],v[8:11],a[104:107]
EX v122,v122
MM a[112:115],a[176:179],v[16:19],a[112:115]
EX v123,v123
MM a[112:115],a[180:183],v[20:23],a[112:115]
RD a[252:255],v227 offset:12288
CV v93,v122,v123
MM a[120:123],a[180:183],v[28:31],a[120:123]
buffer_load_dwordx4 v235, s[12:15], s64 offen offset:3072 lds
MM a[120:123],a[176:179],v[24:27],a[120:123]
EX v124,v124
MM a[124:127],a[184:187],v[24:27],a[124:127]
v_add_f32 v244, v144, v145
v_add_f32 v245, v148, v149
MM a[124:127],a[188:191],v[28:31],a[124:127]
v_add_f32 v246, v152, v153
v_add_f32 v247, v156, v157
MM a[116:119],a[188:191],v[20:23],a[116:119]
EX v125,v125
MM a[116:119],a[184:187],v[16:19],a[116:119]
EX v126,v126
MM a[108:111],a[184:187],v[8:11],a[108:111]
EX v127,v127
MM a[108:111],a[188:191],v[12:15],a[108:111]
CV v94,v124,v125
CV v95,v126,v127
MM a[100:103],a[188:191],v[4:7],a[100:103]
MM a[100:103],a[184:187],v[0:3],a[100:103]
v_smfmac_f32_16x16x64_bf16 v[144:147], v[236:239], v[64:71], v234
v_smfmac_f32_16x16x64_bf16 v[148:151], v[236:239], v[72:79], v234
v_smfmac_f32_16x16x64_bf16 v[152:155], v[236:239], v[80:87], v234
v_smfmac_f32_16x16x64_bf16 v[156:159], v[236:239], v[88:95], v234
s_waitcnt lgkmcnt(0)
MM v[0:3],a[192:195],v[160:163],v[128:131]
s_add_u32 s64, s64, 16384
TR a[128:129],v232 offset:49152
s_add_u32 s81, s64, 896
MM v[0:3],a[196:199],v[176:179],v[0:3]
TR a[130:131],v232 offset:53248
v_max3_f32 v249, v144, v145, v148
MM v[0:3],a[200:203],v[192:195],v[0:3]
TR a[132:133],v232 offset:57344
v_max3_f32 v249, v249, v149, v152
MM v[0:3],a[204:207],v[208:211],v[0:3]
TR a[134:135],v232 offset:61440
v_max3_f32 v249, v249, v153, v156
MM v[4:7],a[220:223],v[208:211],v[128:131]
TR a[136:137],v233 offset:49152
v_max_f32 v249, v249, v157
MM v[4:7],a[216:219],v[192:195],v[4:7]
TR a[138:139],v233 offset:53248
v_cmp_lt_f32 vcc, s71, v249
MM v[4:7],a[212:215],v[176:179],v[4:7]
TR a[140:141],v233 offset:57344
TR a[142:143],v233 offset:61440
MM v[4:7],a[208:211],v[160:163],v[4:7]
s_cbranch_vccnz LSITE23_%=
LBACK23_%=:
MM v[12:15],a[208:211],v[164:167],v[132:135]
EX v0,v0
MM v[12:15],a[212:215],v[180:183],v[12:15]
EX v1,v1
MM v[12:15],a[216:219],v[196:199],v[12:15]
TR a[144:145],v232 offset:49664
CV v0,v0,v1
MM v[12:15],a[220:223],v[212:215],v[12:15]
EX v2,v2
MM v[8:11],a[204:207],v[212:215],v[132:135]
EX v3,v3
MM v[8:11],a[200:203],v[196:199],v[8:11]
TR a[146:147],v232 offset:53760
CV v1,v2,v3
MM v[8:11],a[196:199],v[180:183],v[8:11]
EX v4,v4
MM v[8:11],a[192:195],v[164:167],v[8:11]
TR a[148:149],v232 offset:57856
TR a[150:151],v232 offset:61952
MM v[16:19],a[192:195],v[168:171],v[136:139]
EX v5,v5
MM v[16:19],a[196:199],v[184:187],v[16:19]
TR a[152:153],v233 offset:49664
CV v2,v4,v5
MM v[16:19],a[200:203],v[200:203],v[16:19]
EX v6,v6
MM v[16:19],a[204:207],v[216:219],v[16:19]
TR a[154:155],v233 offset:53760
TR a[156:157],v233 offset:57856
MM v[20:23],a[220:223],v[216:219],v[136:139]
EX v7,v7
MM v[20:23],a[216:219],v[200:203],v[20:23]
TR a[158:159],v233 offset:61952
CV v3,v6,v7
MM v[20:23],a[212:215],v[184:187],v[20:23]
EX v8,v8
MM v[20:23],a[208:211],v[168:171],v[20:23]
EX v9,v9
MM v[28:31],a[208:211],v[172:175],v[140:143]
TR a[160:161],v232 offset:50176
CV v8,v8,v9
MM v[28:31],a[212:215],v[188:191],v[28:31]
EX v10,v10
MM v[28:31],a[216:219],v[204:207],v[28:31]
TR a[162:163],v232 offset:54272
TR a[164:165],v232 offset:58368
MM v[28:31],a[220:223],v[220:223],v[28:31]
EX v11,v11
MM v[24:27],a[204:207],v[220:223],v[140:143]
TR a[166:167],v232 offset:62464
CV v9,v10,v11
MM v[24:27],a[200:203],v[204:207],v[24:27]
EX v12,v12
MM v[24:27],a[196:199],v[188:191],v[24:27]
EX v13,v13
MM v[24:27],a[192:195],v[172:175],v[24:27]
TR a[168:169],v233 offset:50176
CV v10,v12,v13
MM v[32:35],a[224:227],v[160:163],v[128:131]
EX v14,v14
MM v[32:35],a[228:231],v[176:179],v[32:35]
TR a[170:171],v233 offset:54272
TR a[172:173],v233 offset:58368
MM v[32:35],a[232:235],v[192:195],v[32:35]
EX v15,v15
MM v[32:35],a[236:239],v[208:211],v[32:35]
TR a[174:175],v233 offset:62464
CV v11,v14,v15
MM v[36:39],a[252:255],v[208:211],v[128:131]
EX v16,v16
MM v[36:39],a[248:251],v[192:195],v[36:39]
EX v17,v17
MM v[36:39],a[244:247],v[176:179],v[36:39]
TR a[176:177],v232 offset:50688
CV v16,v16,v17
MM v[36:39],a[240:243],v[160:163],v[36:39]
EX v18,v18
MM v[44:47],a[240:243],v[164:167],v[132:135]
TR a[178:179],v232 offset:54784
TR a[180:181],v232 offset:58880
MM v[44:47],a[244:247],v[180:183],v[44:47]
EX v19,v19
MM v[44:47],a[248:251],v[196:199],v[44:47]
TR a[182:183],v232 offset:62976
CV v17,v18,v19
MM v[44:47],a[252:255],v[212:215],v[44:47]
EX v20,v20
MM v[40:43],a[236:239],v[212:215],v[132:135]
TR a[184:185],v233 offset:50688
TR a[186:187],v233 offset:54784
MM v[40:43],a[232:235],v[196:199],v[40:43]
EX v21,v21
MM v[40:43],a[228:231],v[180:183],v[40:43]
TR a[188:189],v233 offset:58880
CV v18,v20,v21
MM v[40:43],a[224:227],v[164:167],v[40:43]
EX v22,v22
MM v[48:51],a[224:227],v[168:171],v[136:139]
EX v23,v23
MM v[48:51],a[228:231],v[184:187],v[48:51]
TR a[190:191],v233 offset:62976
CV v19,v22,v23
MM v[48:51],a[232:235],v[200:203],v[48:51]
EX v24,v24
MM v[48:51],a[236:239],v[216:219],v[48:51]
EX v25,v25
MM v[52:55],a[252:255],v[216:219],v[136:139]
EX v26,v26
MM v[52:55],a[248:251],v[200:203],v[52:55]
EX v27,v27
MM v[52:55],a[244:247],v[184:187],v[52:55]
EX v28,v28
MM v[52:55],a[240:243],v[168:171],v[52:55]
EX v29,v29
MM v[60:63],a[240:243],v[172:175],v[140:143]
)ASM"
R"ASM(
EX v30,v30
MM v[60:63],a[244:247],v[188:191],v[60:63]
EX v31,v31
MM v[60:63],a[248:251],v[204:207],v[60:63]
EX v32,v32
MM v[60:63],a[252:255],v[220:223],v[60:63]
EX v33,v33
MM v[56:59],a[236:239],v[220:223],v[140:143]
EX v34,v34
MM v[56:59],a[232:235],v[204:207],v[56:59]
EX v35,v35
MM v[56:59],a[228:231],v[188:191],v[56:59]
EX v36,v36
MM v[56:59],a[224:227],v[172:175],v[56:59]
s_waitcnt vmcnt(13) lgkmcnt(0)
s_barrier
MM a[0:3],a[128:131],v[64:67],a[0:3]
s_add_u32 m0, s65, 49152
CV v24,v24,v25
CV v25,v26,v27
MM a[0:3],a[132:135],v[68:71],a[0:3]
RD a[192:195],v224 offset:16384
CV v26,v28,v29
MM a[8:11],a[132:135],v[76:79],a[8:11]
RD a[196:199],v225 offset:16384
CV v27,v30,v31
MM a[8:11],a[128:131],v[72:75],a[8:11]
buffer_load_dwordx4 v228, s[8:11], s64 offen offset:0 lds
MM a[16:19],a[128:131],v[80:83],a[16:19]
RD a[200:203],v226 offset:16384
CV v4,v32,v33
MM a[16:19],a[132:135],v[84:87],a[16:19]
RD a[204:207],v227 offset:16384
CV v5,v34,v35
MM a[24:27],a[132:135],v[92:95],a[24:27]
buffer_load_dwordx4 v229, s[8:11], s64 offen offset:1024 lds
MM a[24:27],a[128:131],v[88:91],a[24:27]
RD a[208:211],v224 offset:20480
RD a[212:215],v225 offset:20480
MM a[28:31],a[136:139],v[88:91],a[28:31]
buffer_load_dwordx4 v230, s[8:11], s64 offen offset:2048 lds
MM a[28:31],a[140:143],v[92:95],a[28:31]
RD a[216:219],v226 offset:20480
RD a[220:223],v227 offset:20480
MM a[20:23],a[140:143],v[84:87],a[20:23]
EX v37,v37
MM a[20:23],a[136:139],v[80:83],a[20:23]
RD a[224:227],v224 offset:24576
CV v6,v36,v37
MM a[12:15],a[136:139],v[72:75],a[12:15]
EX v38,v38
MM a[12:15],a[140:143],v[76:79],a[12:15]
buffer_load_dwordx4 v231, s[8:11], s64 offen offset:3072 lds
MM a[4:7],a[140:143],v[68:71],a[4:7]
s_add_u32 m0, s65, 114688
EX v39,v39
MM a[4:7],a[136:139],v[64:67],a[4:7]
RD a[228:231],v225 offset:24576
CV v7,v38,v39
MM a[32:35],a[144:147],v[64:67],a[32:35]
EX v40,v40
MM a[32:35],a[148:151],v[68:71],a[32:35]
EX v41,v41
MM a[40:43],a[148:151],v[76:79],a[40:43]
RD a[232:235],v226 offset:24576
CV v12,v40,v41
MM a[40:43],a[144:147],v[72:75],a[40:43]
EX v42,v42
MM a[48:51],a[144:147],v[80:83],a[48:51]
buffer_load_dwordx4 v235, s[12:15], s81 offen offset:0 lds
MM a[48:51],a[148:151],v[84:87],a[48:51]
EX v43,v43
MM a[56:59],a[148:151],v[92:95],a[56:59]
RD a[236:239],v227 offset:24576
CV v13,v42,v43
MM a[56:59],a[144:147],v[88:91],a[56:59]
EX v44,v44
MM a[60:63],a[152:155],v[88:91],a[60:63]
EX v45,v45
MM a[60:63],a[156:159],v[92:95],a[60:63]
RD a[240:243],v224 offset:28672
CV v14,v44,v45
MM a[52:55],a[156:159],v[84:87],a[52:55]
EX v46,v46
MM a[52:55],a[152:155],v[80:83],a[52:55]
buffer_load_dwordx4 v235, s[12:15], s64 offen offset:1024 lds
MM a[44:47],a[152:155],v[72:75],a[44:47]
EX v47,v47
MM a[44:47],a[156:159],v[76:79],a[44:47]
RD a[244:247],v225 offset:28672
CV v15,v46,v47
MM a[36:39],a[156:159],v[68:71],a[36:39]
EX v48,v48
MM a[36:39],a[152:155],v[64:67],a[36:39]
EX v49,v49
MM a[64:67],a[160:163],v[64:67],a[64:67]
RD a[248:251],v226 offset:28672
CV v20,v48,v49
MM a[64:67],a[164:167],v[68:71],a[64:67]
EX v50,v50
MM a[72:75],a[164:167],v[76:79],a[72:75]
buffer_load_dwordx4 v235, s[12:15], s81 offen offset:2048 lds
MM a[72:75],a[160:163],v[72:75],a[72:75]
EX v51,v51
MM a[80:83],a[160:163],v[80:83],a[80:83]
RD a[252:255],v227 offset:28672
CV v21,v50,v51
MM a[80:83],a[164:167],v[84:87],a[80:83]
EX v52,v52
MM a[88:91],a[164:167],v[92:95],a[88:91]
EX v53,v53
MM a[88:91],a[160:163],v[88:91],a[88:91]
buffer_load_dwordx4 v235, s[12:15], s64 offen offset:3072 lds
MM a[92:95],a[168:171],v[88:91],a[92:95]
EX v54,v54
MM a[92:95],a[172:175],v[92:95],a[92:95]
EX v55,v55
MM a[84:87],a[172:175],v[84:87],a[84:87]
EX v56,v56
MM a[84:87],a[168:171],v[80:83],a[84:87]
EX v57,v57
MM a[76:79],a[168:171],v[72:75],a[76:79]
EX v58,v58
MM a[76:79],a[172:175],v[76:79],a[76:79]
EX v59,v59
MM a[68:71],a[172:175],v[68:71],a[68:71]
EX v60,v60
MM a[68:71],a[168:171],v[64:67],a[68:71]
EX v61,v61
MM a[96:99],a[176:179],v[64:67],a[96:99]
EX v62,v62
MM a[96:99],a[180:183],v[68:71],a[96:99]
EX v63,v63
MM a[104:107],a[180:183],v[76:79],a[104:107]
CV v22,v52,v53
CV v23,v54,v55
MM a[104:107],a[176:179],v[72:75],a[104:107]
CV v28,v56,v57
CV v29,v58,v59
MM a[112:115],a[176:179],v[80:83],a[112:115]
CV v30,v60,v61
CV v31,v62,v63
MM a[112:115],a[180:183],v[84:87],a[112:115]
MM a[120:123],a[180:183],v[92:95],a[120:123]
s_lshr_b32 s91, s18, 2
s_add_u32 s91, s91, 69632
s_waitcnt vmcnt(16)
MM a[120:123],a[176:179],v[88:91],a[120:123]
v_mov_b32 v250, s69
s_mov_b64 exec, 1
buffer_store_dword v250, off, s[20:23], s91 offset:0 sc1
s_mov_b64 exec, -1
MM a[124:127],a[184:187],v[88:91],a[124:127]
v_add_f32 v244, v144, v145
v_add_f32 v245, v148, v149
MM a[124:127],a[188:191],v[92:95],a[124:127]
v_add_f32 v246, v152, v153
v_add_f32 v247, v156, v157
v_lshlrev_b32 v96, 3, v248
MM a[116:119],a[188:191],v[84:87],a[116:119]
v_min_u32 v96, 4992, v96
LPG18_%=:
s_cbranch_execz LTR3_%=
s_sub_u32 s91, 5242880, s26
MM a[116:119],a[184:187],v[80:83],a[116:119]
s_sub_u32 s60, s24, s91
s_subb_u32 s61, s25, 0
s_mov_b32 s62, 5242880
MM a[108:111],a[184:187],v[72:75],a[108:111]
s_mov_b32 s63, 131072
s_lshl_b32 s90, s16, 2
s_add_u32 s90, s90, %[swave]
MM a[108:111],a[188:191],v[76:79],a[108:111]
s_mul_i32 s90, s90, 10240
s_add_u32 s90, s90, 0
s_add_u32 s91, %[slds], 135168
MM a[100:103],a[188:191],v[68:71],a[100:103]
s_mov_b32 m0, s91
s_nop 0
buffer_load_dword v96, s[60:63], s90 offen offset:0 lds
MM a[100:103],a[184:187],v[64:67],a[100:103]
s_lshr_b32 s77, %[sb], 3
s_lshl_b32 s77, s77, 2
s_add_u32 s77, s77, %[swave]
v_smfmac_f32_16x16x64_bf16 v[144:147], v[236:239], v[0:7], v234
s_lshl_b32 s77, s77, 9
s_add_u32 s77, s77, 0
v_smfmac_f32_16x16x64_bf16 v[148:151], v[236:239], v[8:15], v234
v_lshrrev_b32 v253, 1, v248
v_smfmac_f32_16x16x64_bf16 v[152:155], v[236:239], v[16:23], v234
v_add_u32 v253, s77, v253
v_smfmac_f32_16x16x64_bf16 v[156:159], v[236:239], v[24:31], v234
s_waitcnt lgkmcnt(0)
s_mov_b32 s77, LKEND_ka-LPD1_%=-16
v_min_u32 v253, s77, v253
s_getpc_b64 s[98:99]
LPD1_%=:
global_load_dword v253, v253, s[98:99]
MM v[64:67],a[192:195],v[160:163],v[128:131]
s_add_u32 s64, s64, 16384
TR a[128:129],v232 offset:0
s_add_u32 s81, s64, 896
MM v[64:67],a[196:199],v[176:179],v[64:67]
TR a[130:131],v232 offset:4096
v_max3_f32 v249, v144, v145, v148
MM v[64:67],a[200:203],v[192:195],v[64:67]
TR a[132:133],v232 offset:8192
v_max3_f32 v249, v249, v149, v152
MM v[64:67],a[204:207],v[208:211],v[64:67]
TR a[134:135],v232 offset:12288
v_max3_f32 v249, v249, v153, v156
MM v[68:71],a[220:223],v[208:211],v[128:131]
TR a[136:137],v233 offset:0
v_max_f32 v249, v249, v157
MM v[68:71],a[216:219],v[192:195],v[68:71]
TR a[138:139],v233 offset:4096
v_cmp_lt_f32 vcc, s71, v249
MM v[68:71],a[212:215],v[176:179],v[68:71]
TR a[140:141],v233 offset:8192
TR a[142:143],v233 offset:12288
MM v[68:71],a[208:211],v[160:163],v[68:71]
s_cbranch_vccnz LSITE24_%=
LBACK24_%=:
MM v[76:79],a[208:211],v[164:167],v[132:135]
EX v64,v64
MM v[76:79],a[212:215],v[180:183],v[76:79]
EX v65,v65
MM v[76:79],a[216:219],v[196:199],v[76:79]
TR a[144:145],v232 offset:512
CV v64,v64,v65
MM v[76:79],a[220:223],v[212:215],v[76:79]
EX v66,v66
MM v[72:75],a[204:207],v[212:215],v[132:135]
EX v67,v67
MM v[72:75],a[200:203],v[196:199],v[72:75]
TR a[146:147],v232 offset:4608
CV v65,v66,v67
MM v[72:75],a[196:199],v[180:183],v[72:75]
EX v68,v68
MM v[72:75],a[192:195],v[164:167],v[72:75]
TR a[148:149],v232 offset:8704
TR a[150:151],v232 offset:12800
MM v[80:83],a[192:195],v[168:171],v[136:139]
EX v69,v69
MM v[80:83],a[196:199],v[184:187],v[80:83]
TR a[152:153],v233 offset:512
CV v66,v68,v69
MM v[80:83],a[200:203],v[200:203],v[80:83]
EX v70,v70
MM v[80:83],a[204:207],v[216:219],v[80:83]
TR a[154:155],v233 offset:4608
TR a[156:157],v233 offset:8704
MM v[84:87],a[220:223],v[216:219],v[136:139]
s_add_u32 s87, s95, 4096
buffer_store_dword v243, v248, s[20:23], s87 offen offset:12 sc1
MM v[84:87],a[216:219],v[200:203],v[84:87]
EX v71,v71
MM v[84:87],a[212:215],v[184:187],v[84:87]
TR a[158:159],v233 offset:12800
CV v67,v70,v71
MM v[84:87],a[208:211],v[168:171],v[84:87]
EX v72,v72
MM v[92:95],a[208:211],v[172:175],v[140:143]
EX v73,v73
MM v[92:95],a[212:215],v[188:191],v[92:95]
TR a[160:161],v232 offset:1024
CV v72,v72,v73
MM v[92:95],a[216:219],v[204:207],v[92:95]
EX v74,v74
MM v[92:95],a[220:223],v[220:223],v[92:95]
EX v75,v75
MM v[88:91],a[204:207],v[220:223],v[140:143]
TR a[162:163],v232 offset:5120
CV v73,v74,v75
MM v[88:91],a[200:203],v[204:207],v[88:91]
TR a[164:165],v232 offset:9216
TR a[166:167],v232 offset:13312
MM v[88:91],a[196:199],v[188:191],v[88:91]
EX v76,v76
MM v[88:91],a[192:195],v[172:175],v[88:91]
EX v77,v77
MM v[96:99],a[224:227],v[160:163],v[128:131]
TR a[168:169],v233 offset:1024
CV v74,v76,v77
MM v[96:99],a[228:231],v[176:179],v[96:99]
EX v78,v78
MM v[96:99],a[232:235],v[192:195],v[96:99]
TR a[170:171],v233 offset:5120
TR a[172:173],v233 offset:9216
MM v[96:99],a[236:239],v[208:211],v[96:99]
EX v79,v79
MM v[100:103],a[252:255],v[208:211],v[128:131]
TR a[174:175],v233 offset:13312
CV v75,v78,v79
MM v[100:103],a[248:251],v[192:195],v[100:103]
EX v80,v80
MM v[100:103],a[244:247],v[176:179],v[100:103]
EX v81,v81
MM v[100:103],a[240:243],v[160:163],v[100:103]
TR a[176:177],v232 offset:1536
CV v80,v80,v81
MM v[108:111],a[240:243],v[164:167],v[132:135]
EX v82,v82
MM v[108:111],a[244:247],v[180:183],v[108:111]
TR a[178:179],v232 offset:5632
TR a[180:181],v232 offset:9728
MM v[108:111],a[248:251],v[196:199],v[108:111]
EX v83,v83
MM v[108:111],a[252:255],v[212:215],v[108:111]
TR a[182:183],v232 offset:13824
CV v81,v82,v83
MM v[104:107],a[236:239],v[212:215],v[132:135]
EX v84,v84
MM v[104:107],a[232:235],v[196:199],v[104:107]
EX v85,v85
MM v[104:107],a[228:231],v[180:183],v[104:107]
TR a[184:185],v233 offset:1536
CV v82,v84,v85
MM v[104:107],a[224:227],v[164:167],v[104:107]
EX v86,v86
MM v[112:115],a[224:227],v[168:171],v[136:139]
TR a[186:187],v233 offset:5632
TR a[188:189],v233 offset:9728
MM v[112:115],a[228:231],v[184:187],v[112:115]
EX v87,v87
MM v[112:115],a[232:235],v[200:203],v[112:115]
TR a[190:191],v233 offset:13824
CV v83,v86,v87
MM v[112:115],a[236:239],v[216:219],v[112:115]
EX v88,v88
MM v[116:119],a[252:255],v[216:219],v[136:139]
EX v89,v89
MM v[116:119],a[248:251],v[200:203],v[116:119]
EX v90,v90
MM v[116:119],a[244:247],v[184:187],v[116:119]
EX v91,v91
MM v[116:119],a[240:243],v[168:171],v[116:119]
EX v92,v92
MM v[124:127],a[240:243],v[172:175],v[140:143]
EX v93,v93
MM v[124:127],a[244:247],v[188:191],v[124:127]
EX v94,v94
MM v[124:127],a[248:251],v[204:207],v[124:127]
EX v95,v95
MM v[124:127],a[252:255],v[220:223],v[124:127]
EX v96,v96
MM v[120:123],a[236:239],v[220:223],v[140:143]
EX v97,v97
MM v[120:123],a[232:235],v[204:207],v[120:123]
EX v98,v98
MM v[120:123],a[228:231],v[188:191],v[120:123]
EX v99,v99
MM v[120:123],a[224:227],v[172:175],v[120:123]
s_waitcnt vmcnt(16) lgkmcnt(0)
s_barrier
MM a[0:3],a[128:131],v[0:3],a[0:3]
s_add_u32 m0, s65, 0
CV v88,v88,v89
CV v89,v90,v91
MM a[0:3],a[132:135],v[4:7],a[0:3]
RD a[192:195],v224 offset:32768
CV v90,v92,v93
MM a[8:11],a[132:135],v[12:15],a[8:11]
RD a[196:199],v225 offset:32768
CV v91,v94,v95
MM a[8:11],a[128:131],v[8:11],a[8:11]
buffer_load_dwordx4 v228, s[8:11], s64 offen offset:0 lds
MM a[16:19],a[128:131],v[16:19],a[16:19]
RD a[200:203],v226 offset:32768
CV v68,v96,v97
MM a[16:19],a[132:135],v[20:23],a[16:19]
RD a[204:207],v227 offset:32768
CV v69,v98,v99
MM a[24:27],a[132:135],v[28:31],a[24:27]
buffer_load_dwordx4 v229, s[8:11], s64 offen offset:1024 lds
MM a[24:27],a[128:131],v[24:27],a[24:27]
RD a[208:211],v224 offset:36864
RD a[212:215],v225 offset:36864
MM a[28:31],a[136:139],v[24:27],a[28:31]
buffer_load_dwordx4 v230, s[8:11], s64 offen offset:2048 lds
MM a[28:31],a[140:143],v[28:31],a[28:31]
EX v100,v100
MM a[20:23],a[140:143],v[20:23],a[20:23]
EX v101,v101
MM a[20:23],a[136:139],v[16:19],a[20:23]
RD a[216:219],v226 offset:36864
CV v70,v100,v101
MM a[12:15],a[136:139],v[8:11],a[12:15]
EX v102,v102
MM a[12:15],a[140:143],v[12:15],a[12:15]
RD a[220:223],v227 offset:36864
)ASM"
R"ASM(
RD a[224:227],v224 offset:40960
MM a[4:7],a[140:143],v[4:7],a[4:7]
EX v103,v103
MM a[4:7],a[136:139],v[0:3],a[4:7]
RD a[228:231],v225 offset:40960
CV v71,v102,v103
MM a[32:35],a[144:147],v[0:3],a[32:35]
EX v104,v104
MM a[32:35],a[148:151],v[4:7],a[32:35]
buffer_load_dwordx4 v231, s[8:11], s64 offen offset:3072 lds
MM a[40:43],a[148:151],v[12:15],a[40:43]
s_add_u32 m0, s65, 65536
EX v105,v105
MM a[40:43],a[144:147],v[8:11],a[40:43]
RD a[232:235],v226 offset:40960
CV v76,v104,v105
MM a[48:51],a[144:147],v[16:19],a[48:51]
EX v106,v106
MM a[48:51],a[148:151],v[20:23],a[48:51]
EX v107,v107
MM a[56:59],a[148:151],v[28:31],a[56:59]
RD a[236:239],v227 offset:40960
CV v77,v106,v107
MM a[56:59],a[144:147],v[24:27],a[56:59]
EX v108,v108
MM a[60:63],a[152:155],v[24:27],a[60:63]
buffer_load_dwordx4 v235, s[12:15], s81 offen offset:0 lds
MM a[60:63],a[156:159],v[28:31],a[60:63]
EX v109,v109
MM a[52:55],a[156:159],v[20:23],a[52:55]
RD a[240:243],v224 offset:45056
CV v78,v108,v109
MM a[52:55],a[152:155],v[16:19],a[52:55]
EX v110,v110
MM a[44:47],a[152:155],v[8:11],a[44:47]
EX v111,v111
MM a[44:47],a[156:159],v[12:15],a[44:47]
RD a[244:247],v225 offset:45056
CV v79,v110,v111
MM a[36:39],a[156:159],v[4:7],a[36:39]
buffer_load_dwordx4 v235, s[12:15], s64 offen offset:1024 lds
MM a[36:39],a[152:155],v[0:3],a[36:39]
EX v112,v112
MM a[64:67],a[160:163],v[0:3],a[64:67]
EX v113,v113
MM a[64:67],a[164:167],v[4:7],a[64:67]
RD a[248:251],v226 offset:45056
CV v84,v112,v113
MM a[72:75],a[164:167],v[12:15],a[72:75]
EX v114,v114
MM a[72:75],a[160:163],v[8:11],a[72:75]
EX v115,v115
MM a[80:83],a[160:163],v[16:19],a[80:83]
RD a[252:255],v227 offset:45056
CV v85,v114,v115
MM a[80:83],a[164:167],v[20:23],a[80:83]
buffer_load_dwordx4 v235, s[12:15], s81 offen offset:2048 lds
MM a[88:91],a[164:167],v[28:31],a[88:91]
EX v116,v116
MM a[88:91],a[160:163],v[24:27],a[88:91]
EX v117,v117
MM a[92:95],a[168:171],v[24:27],a[92:95]
EX v118,v118
MM a[92:95],a[172:175],v[28:31],a[92:95]
buffer_load_dwordx4 v235, s[12:15], s64 offen offset:3072 lds
MM a[84:87],a[172:175],v[20:23],a[84:87]
EX v119,v119
MM a[84:87],a[168:171],v[16:19],a[84:87]
EX v120,v120
MM a[76:79],a[168:171],v[8:11],a[76:79]
EX v121,v121
MM a[76:79],a[172:175],v[12:15],a[76:79]
EX v122,v122
MM a[68:71],a[172:175],v[4:7],a[68:71]
EX v123,v123
MM a[68:71],a[168:171],v[0:3],a[68:71]
EX v124,v124
MM a[96:99],a[176:179],v[0:3],a[96:99]
EX v125,v125
MM a[96:99],a[180:183],v[4:7],a[96:99]
EX v126,v126
MM a[104:107],a[180:183],v[12:15],a[104:107]
EX v127,v127
MM a[104:107],a[176:179],v[8:11],a[104:107]
CV v86,v116,v117
CV v87,v118,v119
MM a[112:115],a[176:179],v[16:19],a[112:115]
CV v92,v120,v121
CV v93,v122,v123
MM a[112:115],a[180:183],v[20:23],a[112:115]
CV v94,v124,v125
CV v95,v126,v127
MM a[120:123],a[180:183],v[28:31],a[120:123]
MM a[120:123],a[176:179],v[24:27],a[120:123]
MM a[124:127],a[184:187],v[24:27],a[124:127]
v_add_f32 v244, v144, v145
v_add_f32 v245, v148, v149
MM a[124:127],a[188:191],v[28:31],a[124:127]
v_add_f32 v246, v152, v153
v_add_f32 v247, v156, v157
MM a[116:119],a[188:191],v[20:23],a[116:119]
MM a[116:119],a[184:187],v[16:19],a[116:119]
MM a[108:111],a[184:187],v[8:11],a[108:111]
MM a[108:111],a[188:191],v[12:15],a[108:111]
MM a[100:103],a[188:191],v[4:7],a[100:103]
MM a[100:103],a[184:187],v[0:3],a[100:103]
v_smfmac_f32_16x16x64_bf16 v[144:147], v[236:239], v[64:71], v234
v_smfmac_f32_16x16x64_bf16 v[148:151], v[236:239], v[72:79], v234
v_smfmac_f32_16x16x64_bf16 v[152:155], v[236:239], v[80:87], v234
v_smfmac_f32_16x16x64_bf16 v[156:159], v[236:239], v[88:95], v234
s_waitcnt lgkmcnt(0)
MM v[0:3],a[192:195],v[160:163],v[128:131]
s_add_u32 s64, s64, 16384
TR a[128:129],v232 offset:16384
s_add_u32 s81, s64, 896
MM v[0:3],a[196:199],v[176:179],v[0:3]
TR a[130:131],v232 offset:20480
v_max3_f32 v249, v144, v145, v148
MM v[0:3],a[200:203],v[192:195],v[0:3]
TR a[132:133],v232 offset:24576
v_max3_f32 v249, v249, v149, v152
MM v[0:3],a[204:207],v[208:211],v[0:3]
TR a[134:135],v232 offset:28672
v_max3_f32 v249, v249, v153, v156
MM v[4:7],a[220:223],v[208:211],v[128:131]
TR a[136:137],v233 offset:16384
v_max_f32 v249, v249, v157
MM v[4:7],a[216:219],v[192:195],v[4:7]
TR a[138:139],v233 offset:20480
v_cmp_lt_f32 vcc, s71, v249
MM v[4:7],a[212:215],v[176:179],v[4:7]
TR a[140:141],v233 offset:24576
TR a[142:143],v233 offset:28672
MM v[4:7],a[208:211],v[160:163],v[4:7]
s_cbranch_vccnz LSITE25_%=
LBACK25_%=:
MM v[12:15],a[208:211],v[164:167],v[132:135]
s_waitcnt vmcnt(9)
EX v0,v0
MM v[12:15],a[212:215],v[180:183],v[12:15]
s_mul_i32 s76, s46, 96
EX v1,v1
MM v[12:15],a[216:219],v[196:199],v[12:15]
s_add_u32 s76, s76, 69632
v_lshrrev_b32 v253, 2, v248
v_min_u32 v253, 92, v253
MM v[12:15],a[220:223],v[212:215],v[12:15]
buffer_load_dword v252, v253, s[20:23], s76 offen offset:0 sc1
MM v[8:11],a[204:207],v[212:215],v[132:135]
EX v2,v2
MM v[8:11],a[200:203],v[196:199],v[8:11]
TR a[144:145],v232 offset:16896
CV v0,v0,v1
MM v[8:11],a[196:199],v[180:183],v[8:11]
EX v3,v3
MM v[8:11],a[192:195],v[164:167],v[8:11]
TR a[146:147],v232 offset:20992
CV v1,v2,v3
MM v[16:19],a[192:195],v[168:171],v[136:139]
EX v4,v4
MM v[16:19],a[196:199],v[184:187],v[16:19]
TR a[148:149],v232 offset:25088
TR a[150:151],v232 offset:29184
MM v[16:19],a[200:203],v[200:203],v[16:19]
EX v5,v5
MM v[16:19],a[204:207],v[216:219],v[16:19]
TR a[152:153],v233 offset:16896
CV v2,v4,v5
MM v[20:23],a[220:223],v[216:219],v[136:139]
EX v6,v6
MM v[20:23],a[216:219],v[200:203],v[20:23]
EX v7,v7
MM v[20:23],a[212:215],v[184:187],v[20:23]
TR a[154:155],v233 offset:20992
CV v3,v6,v7
MM v[20:23],a[208:211],v[168:171],v[20:23]
EX v8,v8
MM v[28:31],a[208:211],v[172:175],v[140:143]
TR a[156:157],v233 offset:25088
TR a[158:159],v233 offset:29184
MM v[28:31],a[212:215],v[188:191],v[28:31]
EX v9,v9
MM v[28:31],a[216:219],v[204:207],v[28:31]
TR a[160:161],v232 offset:17408
CV v8,v8,v9
MM v[28:31],a[220:223],v[220:223],v[28:31]
EX v10,v10
MM v[24:27],a[204:207],v[220:223],v[140:143]
EX v11,v11
MM v[24:27],a[200:203],v[204:207],v[24:27]
TR a[162:163],v232 offset:21504
CV v9,v10,v11
MM v[24:27],a[196:199],v[188:191],v[24:27]
EX v12,v12
MM v[24:27],a[192:195],v[172:175],v[24:27]
TR a[164:165],v232 offset:25600
TR a[166:167],v232 offset:29696
MM v[32:35],a[224:227],v[160:163],v[128:131]
EX v13,v13
MM v[32:35],a[228:231],v[176:179],v[32:35]
TR a[168:169],v233 offset:17408
CV v10,v12,v13
MM v[32:35],a[232:235],v[192:195],v[32:35]
EX v14,v14
MM v[32:35],a[236:239],v[208:211],v[32:35]
EX v15,v15
MM v[36:39],a[252:255],v[208:211],v[128:131]
TR a[170:171],v233 offset:21504
CV v11,v14,v15
MM v[36:39],a[248:251],v[192:195],v[36:39]
EX v16,v16
MM v[36:39],a[244:247],v[176:179],v[36:39]
TR a[172:173],v233 offset:25600
TR a[174:175],v233 offset:29696
MM v[36:39],a[240:243],v[160:163],v[36:39]
EX v17,v17
MM v[44:47],a[240:243],v[164:167],v[132:135]
TR a[176:177],v232 offset:17920
CV v16,v16,v17
MM v[44:47],a[244:247],v[180:183],v[44:47]
EX v18,v18
MM v[44:47],a[248:251],v[196:199],v[44:47]
TR a[178:179],v232 offset:22016
TR a[180:181],v232 offset:26112
MM v[44:47],a[252:255],v[212:215],v[44:47]
EX v19,v19
MM v[40:43],a[236:239],v[212:215],v[132:135]
TR a[182:183],v232 offset:30208
CV v17,v18,v19
MM v[40:43],a[232:235],v[196:199],v[40:43]
EX v20,v20
MM v[40:43],a[228:231],v[180:183],v[40:43]
EX v21,v21
MM v[40:43],a[224:227],v[164:167],v[40:43]
TR a[184:185],v233 offset:17920
CV v18,v20,v21
MM v[48:51],a[224:227],v[168:171],v[136:139]
EX v22,v22
MM v[48:51],a[228:231],v[184:187],v[48:51]
TR a[186:187],v233 offset:22016
TR a[188:189],v233 offset:26112
MM v[48:51],a[232:235],v[200:203],v[48:51]
EX v23,v23
MM v[48:51],a[236:239],v[216:219],v[48:51]
TR a[190:191],v233 offset:30208
CV v19,v22,v23
MM v[52:55],a[252:255],v[216:219],v[136:139]
EX v24,v24
MM v[52:55],a[248:251],v[200:203],v[52:55]
EX v25,v25
MM v[52:55],a[244:247],v[184:187],v[52:55]
EX v26,v26
MM v[52:55],a[240:243],v[168:171],v[52:55]
EX v27,v27
MM v[60:63],a[240:243],v[172:175],v[140:143]
EX v28,v28
MM v[60:63],a[244:247],v[188:191],v[60:63]
EX v29,v29
MM v[60:63],a[248:251],v[204:207],v[60:63]
EX v30,v30
MM v[60:63],a[252:255],v[220:223],v[60:63]
EX v31,v31
MM v[56:59],a[236:239],v[220:223],v[140:143]
EX v32,v32
MM v[56:59],a[232:235],v[204:207],v[56:59]
EX v33,v33
MM v[56:59],a[228:231],v[188:191],v[56:59]
EX v34,v34
MM v[56:59],a[224:227],v[172:175],v[56:59]
s_waitcnt lgkmcnt(0)
s_barrier
MM a[0:3],a[128:131],v[64:67],a[0:3]
s_add_u32 m0, s65, 16384
CV v24,v24,v25
CV v25,v26,v27
MM a[0:3],a[132:135],v[68:71],a[0:3]
RD a[192:195],v224 offset:49152
CV v26,v28,v29
MM a[8:11],a[132:135],v[76:79],a[8:11]
RD a[196:199],v225 offset:49152
CV v27,v30,v31
MM a[8:11],a[128:131],v[72:75],a[8:11]
buffer_load_dwordx4 v228, s[8:11], s64 offen offset:0 lds
MM a[16:19],a[128:131],v[80:83],a[16:19]
RD a[200:203],v226 offset:49152
CV v4,v32,v33
MM a[16:19],a[132:135],v[84:87],a[16:19]
buffer_load_dwordx4 v229, s[8:11], s64 offen offset:1024 lds
MM a[24:27],a[132:135],v[92:95],a[24:27]
RD a[204:207],v227 offset:49152
RD a[208:211],v224 offset:53248
MM a[24:27],a[128:131],v[88:91],a[24:27]
EX v35,v35
MM a[28:31],a[136:139],v[88:91],a[28:31]
RD a[212:215],v225 offset:53248
CV v5,v34,v35
MM a[28:31],a[140:143],v[92:95],a[28:31]
EX v36,v36
MM a[20:23],a[140:143],v[84:87],a[20:23]
buffer_load_dwordx4 v230, s[8:11], s64 offen offset:2048 lds
MM a[20:23],a[136:139],v[80:83],a[20:23]
EX v37,v37
MM a[12:15],a[136:139],v[72:75],a[12:15]
RD a[216:219],v226 offset:53248
CV v6,v36,v37
MM a[12:15],a[140:143],v[76:79],a[12:15]
EX v38,v38
MM a[4:7],a[140:143],v[68:71],a[4:7]
EX v39,v39
MM a[4:7],a[136:139],v[64:67],a[4:7]
RD a[220:223],v227 offset:53248
CV v7,v38,v39
MM a[32:35],a[144:147],v[64:67],a[32:35]
EX v40,v40
MM a[32:35],a[148:151],v[68:71],a[32:35]
buffer_load_dwordx4 v231, s[8:11], s64 offen offset:3072 lds
MM a[40:43],a[148:151],v[76:79],a[40:43]
s_add_u32 m0, s65, 81920
EX v41,v41
MM a[40:43],a[144:147],v[72:75],a[40:43]
RD a[224:227],v224 offset:57344
CV v12,v40,v41
MM a[48:51],a[144:147],v[80:83],a[48:51]
EX v42,v42
MM a[48:51],a[148:151],v[84:87],a[48:51]
EX v43,v43
MM a[56:59],a[148:151],v[92:95],a[56:59]
RD a[228:231],v225 offset:57344
CV v13,v42,v43
MM a[56:59],a[144:147],v[88:91],a[56:59]
EX v44,v44
MM a[60:63],a[152:155],v[88:91],a[60:63]
buffer_load_dwordx4 v235, s[12:15], s81 offen offset:0 lds
MM a[60:63],a[156:159],v[92:95],a[60:63]
EX v45,v45
MM a[52:55],a[156:159],v[84:87],a[52:55]
RD a[232:235],v226 offset:57344
CV v14,v44,v45
MM a[52:55],a[152:155],v[80:83],a[52:55]
EX v46,v46
MM a[44:47],a[152:155],v[72:75],a[44:47]
EX v47,v47
MM a[44:47],a[156:159],v[76:79],a[44:47]
RD a[236:239],v227 offset:57344
CV v15,v46,v47
MM a[36:39],a[156:159],v[68:71],a[36:39]
buffer_load_dwordx4 v235, s[12:15], s64 offen offset:1024 lds
MM a[36:39],a[152:155],v[64:67],a[36:39]
EX v48,v48
MM a[64:67],a[160:163],v[64:67],a[64:67]
EX v49,v49
MM a[64:67],a[164:167],v[68:71],a[64:67]
RD a[240:243],v224 offset:61440
CV v20,v48,v49
MM a[72:75],a[164:167],v[76:79],a[72:75]
EX v50,v50
MM a[72:75],a[160:163],v[72:75],a[72:75]
RD a[244:247],v225 offset:61440
RD a[248:251],v226 offset:61440
MM a[80:83],a[160:163],v[80:83],a[80:83]
EX v51,v51
MM a[80:83],a[164:167],v[84:87],a[80:83]
LPG19_%=:
s_cbranch_execz LTR3_%=
RD a[252:255],v227 offset:61440
CV v21,v50,v51
MM a[88:91],a[164:167],v[92:95],a[88:91]
EX v52,v52
MM a[88:91],a[160:163],v[88:91],a[88:91]
buffer_load_dwordx4 v235, s[12:15], s81 offen offset:2048 lds
MM a[92:95],a[168:171],v[88:91],a[92:95]
EX v53,v53
MM a[92:95],a[172:175],v[92:95],a[92:95]
EX v54,v54
MM a[84:87],a[172:175],v[84:87],a[84:87]
buffer_load_dwordx4 v235, s[12:15], s64 offen offset:3072 lds
MM a[84:87],a[168:171],v[80:83],a[84:87]
EX v55,v55
MM a[76:79],a[168:171],v[72:75],a[76:79]
EX v56,v56
MM a[76:79],a[172:175],v[76:79],a[76:79]
EX v57,v57
MM a[68:71],a[172:175],v[68:71],a[68:71]
EX v58,v58
MM a[68:71],a[168:171],v[64:67],a[68:71]
EX v59,v59
MM a[96:99],a[176:179],v[64:67],a[96:99]
EX v60,v60
MM a[96:99],a[180:183],v[68:71],a[96:99]
EX v61,v61
MM a[104:107],a[180:183],v[76:79],a[104:107]
)ASM"
R"ASM(
EX v62,v62
MM a[104:107],a[176:179],v[72:75],a[104:107]
EX v63,v63
MM a[112:115],a[176:179],v[80:83],a[112:115]
CV v22,v52,v53
CV v23,v54,v55
MM a[112:115],a[180:183],v[84:87],a[112:115]
CV v28,v56,v57
CV v29,v58,v59
MM a[120:123],a[180:183],v[92:95],a[120:123]
s_waitcnt vmcnt(8)
v_cmp_ne_u32 vcc, s69, v252
s_cbranch_vccnz LRFH_%=
LRFHb_%=:
MM a[120:123],a[176:179],v[88:91],a[120:123]
v_bfe_u32 v253, v248, 4, 3
v_mul_u32_u24 v253, 3456, v253
MM a[124:127],a[184:187],v[88:91],a[124:127]
v_add_f32 v244, v144, v145
v_add_f32 v245, v148, v149
MM a[124:127],a[188:191],v[92:95],a[124:127]
v_add_f32 v246, v152, v153
v_add_f32 v247, v156, v157
v_lshrrev_b32 v252, 7, v248
MM a[116:119],a[188:191],v[84:87],a[116:119]
v_lshl_add_u32 v253, v252, 4, v253
CV v30,v60,v61
MM a[116:119],a[184:187],v[80:83],a[116:119]
s_min_u32 s90, %[swave], 2
s_mul_i32 s76, s46, 384
CV v31,v62,v63
MM a[108:111],a[184:187],v[72:75],a[108:111]
s_lshl_b32 s91, s90, 7
s_add_u32 s76, s76, s91
s_add_u32 s76, s76, 21626880
MM a[108:111],a[188:191],v[76:79],a[108:111]
s_lshl_b32 s90, s90, 10
s_add_u32 s90, s90, %[slds]
MM a[100:103],a[188:191],v[68:71],a[100:103]
s_add_u32 m0, s90, 131072
s_nop 0
buffer_load_dwordx4 v253, s[20:23], s76 offen offset:0 sc1 lds
MM a[100:103],a[184:187],v[64:67],a[100:103]
v_smfmac_f32_16x16x64_bf16 v[144:147], v[236:239], v[0:7], v234
v_smfmac_f32_16x16x64_bf16 v[148:151], v[236:239], v[8:15], v234
v_smfmac_f32_16x16x64_bf16 v[152:155], v[236:239], v[16:23], v234
v_smfmac_f32_16x16x64_bf16 v[156:159], v[236:239], v[24:31], v234
s_waitcnt lgkmcnt(0)
MM v[64:67],a[192:195],v[160:163],v[128:131]
s_add_u32 s64, s64, 16384
TR a[128:129],v232 offset:32768
s_add_u32 s81, s64, 896
MM v[64:67],a[196:199],v[176:179],v[64:67]
TR a[130:131],v232 offset:36864
v_max3_f32 v249, v144, v145, v148
MM v[64:67],a[200:203],v[192:195],v[64:67]
TR a[132:133],v232 offset:40960
v_max3_f32 v249, v249, v149, v152
MM v[64:67],a[204:207],v[208:211],v[64:67]
TR a[134:135],v232 offset:45056
v_max3_f32 v249, v249, v153, v156
MM v[68:71],a[220:223],v[208:211],v[128:131]
TR a[136:137],v233 offset:32768
v_max_f32 v249, v249, v157
MM v[68:71],a[216:219],v[192:195],v[68:71]
TR a[138:139],v233 offset:36864
v_cmp_lt_f32 vcc, s71, v249
MM v[68:71],a[212:215],v[176:179],v[68:71]
TR a[140:141],v233 offset:40960
TR a[142:143],v233 offset:45056
MM v[68:71],a[208:211],v[160:163],v[68:71]
s_cbranch_vccnz LSITE26_%=
LBACK26_%=:
MM v[76:79],a[208:211],v[164:167],v[132:135]
EX v64,v64
MM v[76:79],a[212:215],v[180:183],v[76:79]
EX v65,v65
MM v[76:79],a[216:219],v[196:199],v[76:79]
TR a[144:145],v232 offset:33280
CV v64,v64,v65
MM v[76:79],a[220:223],v[212:215],v[76:79]
EX v66,v66
MM v[72:75],a[204:207],v[212:215],v[132:135]
EX v67,v67
MM v[72:75],a[200:203],v[196:199],v[72:75]
TR a[146:147],v232 offset:37376
CV v65,v66,v67
MM v[72:75],a[196:199],v[180:183],v[72:75]
EX v68,v68
MM v[72:75],a[192:195],v[164:167],v[72:75]
TR a[148:149],v232 offset:41472
TR a[150:151],v232 offset:45568
MM v[80:83],a[192:195],v[168:171],v[136:139]
EX v69,v69
MM v[80:83],a[196:199],v[184:187],v[80:83]
TR a[152:153],v233 offset:33280
CV v66,v68,v69
MM v[80:83],a[200:203],v[200:203],v[80:83]
EX v70,v70
MM v[80:83],a[204:207],v[216:219],v[80:83]
TR a[154:155],v233 offset:37376
TR a[156:157],v233 offset:41472
MM v[84:87],a[220:223],v[216:219],v[136:139]
EX v71,v71
MM v[84:87],a[216:219],v[200:203],v[84:87]
TR a[158:159],v233 offset:45568
CV v67,v70,v71
MM v[84:87],a[212:215],v[184:187],v[84:87]
EX v72,v72
MM v[84:87],a[208:211],v[168:171],v[84:87]
EX v73,v73
MM v[92:95],a[208:211],v[172:175],v[140:143]
TR a[160:161],v232 offset:33792
CV v72,v72,v73
MM v[92:95],a[212:215],v[188:191],v[92:95]
EX v74,v74
MM v[92:95],a[216:219],v[204:207],v[92:95]
TR a[162:163],v232 offset:37888
TR a[164:165],v232 offset:41984
MM v[92:95],a[220:223],v[220:223],v[92:95]
EX v75,v75
MM v[88:91],a[204:207],v[220:223],v[140:143]
TR a[166:167],v232 offset:46080
CV v73,v74,v75
MM v[88:91],a[200:203],v[204:207],v[88:91]
EX v76,v76
MM v[88:91],a[196:199],v[188:191],v[88:91]
EX v77,v77
MM v[88:91],a[192:195],v[172:175],v[88:91]
TR a[168:169],v233 offset:33792
CV v74,v76,v77
MM v[96:99],a[224:227],v[160:163],v[128:131]
EX v78,v78
MM v[96:99],a[228:231],v[176:179],v[96:99]
TR a[170:171],v233 offset:37888
TR a[172:173],v233 offset:41984
MM v[96:99],a[232:235],v[192:195],v[96:99]
EX v79,v79
MM v[96:99],a[236:239],v[208:211],v[96:99]
TR a[174:175],v233 offset:46080
CV v75,v78,v79
MM v[100:103],a[252:255],v[208:211],v[128:131]
EX v80,v80
MM v[100:103],a[248:251],v[192:195],v[100:103]
EX v81,v81
MM v[100:103],a[244:247],v[176:179],v[100:103]
TR a[176:177],v232 offset:34304
CV v80,v80,v81
MM v[100:103],a[240:243],v[160:163],v[100:103]
EX v82,v82
MM v[108:111],a[240:243],v[164:167],v[132:135]
TR a[178:179],v232 offset:38400
TR a[180:181],v232 offset:42496
MM v[108:111],a[244:247],v[180:183],v[108:111]
EX v83,v83
MM v[108:111],a[248:251],v[196:199],v[108:111]
TR a[182:183],v232 offset:46592
CV v81,v82,v83
MM v[108:111],a[252:255],v[212:215],v[108:111]
EX v84,v84
MM v[104:107],a[236:239],v[212:215],v[132:135]
TR a[184:185],v233 offset:34304
TR a[186:187],v233 offset:38400
MM v[104:107],a[232:235],v[196:199],v[104:107]
EX v85,v85
MM v[104:107],a[228:231],v[180:183],v[104:107]
TR a[188:189],v233 offset:42496
CV v82,v84,v85
MM v[104:107],a[224:227],v[164:167],v[104:107]
EX v86,v86
MM v[112:115],a[224:227],v[168:171],v[136:139]
EX v87,v87
MM v[112:115],a[228:231],v[184:187],v[112:115]
TR a[190:191],v233 offset:46592
CV v83,v86,v87
MM v[112:115],a[232:235],v[200:203],v[112:115]
EX v88,v88
MM v[112:115],a[236:239],v[216:219],v[112:115]
EX v89,v89
MM v[116:119],a[252:255],v[216:219],v[136:139]
EX v90,v90
MM v[116:119],a[248:251],v[200:203],v[116:119]
EX v91,v91
MM v[116:119],a[244:247],v[184:187],v[116:119]
EX v92,v92
MM v[116:119],a[240:243],v[168:171],v[116:119]
EX v93,v93
MM v[124:127],a[240:243],v[172:175],v[140:143]
EX v94,v94
MM v[124:127],a[244:247],v[188:191],v[124:127]
EX v95,v95
MM v[124:127],a[248:251],v[204:207],v[124:127]
EX v96,v96
MM v[124:127],a[252:255],v[220:223],v[124:127]
EX v97,v97
MM v[120:123],a[236:239],v[220:223],v[140:143]
EX v98,v98
MM v[120:123],a[232:235],v[204:207],v[120:123]
EX v99,v99
MM v[120:123],a[228:231],v[188:191],v[120:123]
s_getpc_b64 s[98:99]
LTA4_%=:
s_min_u32 s73, %[swave], 1
s_lshl_b32 s73, s73, 12
s_add_u32 s73, s73, LPG21_%=-LTA4_%=
s_add_u32 s98, s98, s73
s_addc_u32 s99, s99, 0
s_mov_b64 exec, 0
s_setpc_b64 s[98:99]
LTR4_%=:
s_mov_b64 exec, -1
CV v88,v88,v89
MM v[120:123],a[224:227],v[172:175],v[120:123]
s_waitcnt lgkmcnt(0)
s_barrier
MM a[0:3],a[128:131],v[0:3],a[0:3]
s_add_u32 m0, s65, 32768
CV v89,v90,v91
CV v90,v92,v93
MM a[0:3],a[132:135],v[4:7],a[0:3]
RD a[192:195],v224 offset:0
CV v91,v94,v95
MM a[8:11],a[132:135],v[12:15],a[8:11]
buffer_load_dwordx4 v228, s[8:11], s64 offen offset:0 lds
MM a[8:11],a[128:131],v[8:11],a[8:11]
RD a[196:199],v225 offset:0
CV v68,v96,v97
MM a[16:19],a[128:131],v[16:19],a[16:19]
RD a[200:203],v226 offset:0
CV v69,v98,v99
MM a[16:19],a[132:135],v[20:23],a[16:19]
buffer_load_dwordx4 v229, s[8:11], s64 offen offset:1024 lds
MM a[24:27],a[132:135],v[28:31],a[24:27]
RD a[204:207],v227 offset:0
RD a[208:211],v224 offset:4096
MM a[24:27],a[128:131],v[24:27],a[24:27]
RD a[212:215],v225 offset:4096
RD a[216:219],v226 offset:4096
MM a[28:31],a[136:139],v[24:27],a[28:31]
buffer_load_dwordx4 v230, s[8:11], s64 offen offset:2048 lds
MM a[28:31],a[140:143],v[28:31],a[28:31]
EX v100,v100
MM a[20:23],a[140:143],v[20:23],a[20:23]
EX v101,v101
MM a[20:23],a[136:139],v[16:19],a[20:23]
RD a[220:223],v227 offset:4096
CV v70,v100,v101
MM a[12:15],a[136:139],v[8:11],a[12:15]
EX v102,v102
MM a[12:15],a[140:143],v[12:15],a[12:15]
buffer_load_dwordx4 v231, s[8:11], s64 offen offset:3072 lds
MM a[4:7],a[140:143],v[4:7],a[4:7]
s_add_u32 m0, s65, 98304
EX v103,v103
MM a[4:7],a[136:139],v[0:3],a[4:7]
RD a[224:227],v224 offset:8192
CV v71,v102,v103
MM a[32:35],a[144:147],v[0:3],a[32:35]
EX v104,v104
MM a[32:35],a[148:151],v[4:7],a[32:35]
EX v105,v105
MM a[40:43],a[148:151],v[12:15],a[40:43]
RD a[228:231],v225 offset:8192
CV v76,v104,v105
MM a[40:43],a[144:147],v[8:11],a[40:43]
EX v106,v106
MM a[48:51],a[144:147],v[16:19],a[48:51]
buffer_load_dwordx4 v235, s[12:15], s81 offen offset:0 lds
MM a[48:51],a[148:151],v[20:23],a[48:51]
EX v107,v107
MM a[56:59],a[148:151],v[28:31],a[56:59]
RD a[232:235],v226 offset:8192
CV v77,v106,v107
MM a[56:59],a[144:147],v[24:27],a[56:59]
EX v108,v108
MM a[60:63],a[152:155],v[24:27],a[60:63]
EX v109,v109
MM a[60:63],a[156:159],v[28:31],a[60:63]
RD a[236:239],v227 offset:8192
CV v78,v108,v109
MM a[52:55],a[156:159],v[20:23],a[52:55]
EX v110,v110
MM a[52:55],a[152:155],v[16:19],a[52:55]
buffer_load_dwordx4 v235, s[12:15], s64 offen offset:1024 lds
MM a[44:47],a[152:155],v[8:11],a[44:47]
EX v111,v111
MM a[44:47],a[156:159],v[12:15],a[44:47]
RD a[240:243],v224 offset:12288
CV v79,v110,v111
MM a[36:39],a[156:159],v[4:7],a[36:39]
EX v112,v112
MM a[36:39],a[152:155],v[0:3],a[36:39]
EX v113,v113
MM a[64:67],a[160:163],v[0:3],a[64:67]
RD a[244:247],v225 offset:12288
CV v84,v112,v113
MM a[64:67],a[164:167],v[4:7],a[64:67]
EX v114,v114
MM a[72:75],a[164:167],v[12:15],a[72:75]
buffer_load_dwordx4 v235, s[12:15], s81 offen offset:2048 lds
MM a[72:75],a[160:163],v[8:11],a[72:75]
EX v115,v115
MM a[80:83],a[160:163],v[16:19],a[80:83]
RD a[248:251],v226 offset:12288
CV v85,v114,v115
MM a[80:83],a[164:167],v[20:23],a[80:83]
EX v116,v116
MM a[88:91],a[164:167],v[28:31],a[88:91]
EX v117,v117
MM a[88:91],a[160:163],v[24:27],a[88:91]
RD a[252:255],v227 offset:12288
CV v86,v116,v117
MM a[92:95],a[168:171],v[24:27],a[92:95]
EX v118,v118
MM a[92:95],a[172:175],v[28:31],a[92:95]
buffer_load_dwordx4 v235, s[12:15], s64 offen offset:3072 lds
MM a[84:87],a[172:175],v[20:23],a[84:87]
EX v119,v119
MM a[84:87],a[168:171],v[16:19],a[84:87]
CV v87,v118,v119
MM a[76:79],a[168:171],v[8:11],a[76:79]
EX v120,v120
MM a[76:79],a[172:175],v[12:15],a[76:79]
EX v121,v121
MM a[68:71],a[172:175],v[4:7],a[68:71]
EX v122,v122
MM a[68:71],a[168:171],v[0:3],a[68:71]
EX v123,v123
MM a[96:99],a[176:179],v[0:3],a[96:99]
EX v124,v124
MM a[96:99],a[180:183],v[4:7],a[96:99]
EX v125,v125
MM a[104:107],a[180:183],v[12:15],a[104:107]
EX v126,v126
MM a[104:107],a[176:179],v[8:11],a[104:107]
EX v127,v127
MM a[112:115],a[176:179],v[16:19],a[112:115]
CV v92,v120,v121
CV v93,v122,v123
MM a[112:115],a[180:183],v[20:23],a[112:115]
CV v94,v124,v125
CV v95,v126,v127
MM a[120:123],a[180:183],v[28:31],a[120:123]
MM a[120:123],a[176:179],v[24:27],a[120:123]
MM a[124:127],a[184:187],v[24:27],a[124:127]
v_add_f32 v244, v144, v145
v_add_f32 v245, v148, v149
MM a[124:127],a[188:191],v[28:31],a[124:127]
v_add_f32 v246, v152, v153
v_add_f32 v247, v156, v157
MM a[116:119],a[188:191],v[20:23],a[116:119]
MM a[116:119],a[184:187],v[16:19],a[116:119]
MM a[108:111],a[184:187],v[8:11],a[108:111]
MM a[108:111],a[188:191],v[12:15],a[108:111]
MM a[100:103],a[188:191],v[4:7],a[100:103]
MM a[100:103],a[184:187],v[0:3],a[100:103]
v_smfmac_f32_16x16x64_bf16 v[144:147], v[236:239], v[64:71], v234
v_smfmac_f32_16x16x64_bf16 v[148:151], v[236:239], v[72:79], v234
v_smfmac_f32_16x16x64_bf16 v[152:155], v[236:239], v[80:87], v234
v_smfmac_f32_16x16x64_bf16 v[156:159], v[236:239], v[88:95], v234
s_waitcnt lgkmcnt(0)
MM v[0:3],a[192:195],v[160:163],v[128:131]
s_add_u32 s64, s64, 16384
TR a[128:129],v232 offset:49152
s_add_u32 s81, s64, 896
MM v[0:3],a[196:199],v[176:179],v[0:3]
TR a[130:131],v232 offset:53248
v_max3_f32 v249, v144, v145, v148
MM v[0:3],a[200:203],v[192:195],v[0:3]
TR a[132:133],v232 offset:57344
v_max3_f32 v249, v249, v149, v152
MM v[0:3],a[204:207],v[208:211],v[0:3]
TR a[134:135],v232 offset:61440
v_max3_f32 v249, v249, v153, v156
MM v[4:7],a[220:223],v[208:211],v[128:131]
TR a[136:137],v233 offset:49152
v_max_f32 v249, v249, v157
MM v[4:7],a[216:219],v[192:195],v[4:7]
TR a[138:139],v233 offset:53248
v_cmp_lt_f32 vcc, s71, v249
MM v[4:7],a[212:215],v[176:179],v[4:7]
TR a[140:141],v233 offset:57344
TR a[142:143],v233 offset:61440
)ASM"
R"ASM(
MM v[4:7],a[208:211],v[160:163],v[4:7]
s_cbranch_vccnz LSITE27_%=
LBACK27_%=:
MM v[12:15],a[208:211],v[164:167],v[132:135]
v_lshrrev_b32 v125, 8, v248
v_lshlrev_b32 v124, 10, v125
MM v[12:15],a[212:215],v[180:183],v[12:15]
v_bfe_u32 v126, v248, 6, 2
v_lshl_add_u32 v124, v126, 7, v124
MM v[12:15],a[216:219],v[196:199],v[12:15]
v_bfe_u32 v126, v248, 4, 2
v_lshl_add_u32 v124, v126, 3, v124
MM v[12:15],a[220:223],v[212:215],v[12:15]
s_lshl_b32 s91, %[swave], 5
EX v0,v0
MM v[8:11],a[204:207],v[212:215],v[132:135]
s_add_u32 s91, s91, %[slds]
EX v1,v1
MM v[8:11],a[200:203],v[196:199],v[8:11]
s_add_u32 s91, s91, 139264
v_add_u32 v124, s91, v124
v_and_b32 v126, 112, v248
MM v[8:11],a[196:199],v[180:183],v[8:11]
s_add_u32 s91, %[slds], 131072
v_lshl_add_u32 v126, v125, 7, v126
v_add_u32 v126, s91, v126
MM v[8:11],a[192:195],v[164:167],v[8:11]
v_bfe_u32 v127, v248, 4, 3
v_mul_u32_u24 v127, 2560, v127
MM v[16:19],a[192:195],v[168:171],v[136:139]
v_lshl_add_u32 v127, v125, 4, v127
CV v0,v0,v1
EX v2,v2
MM v[16:19],a[196:199],v[184:187],v[16:19]
s_lshl_b32 s90, %[swave], 6
EX v3,v3
MM v[16:19],a[200:203],v[200:203],v[16:19]
s_lshl_b32 s91, s47, 8
EX v4,v4
MM v[16:19],a[204:207],v[216:219],v[16:19]
s_add_u32 s90, s90, s91
TR a[144:145],v232 offset:49664
TR a[146:147],v232 offset:53760
MM v[20:23],a[220:223],v[216:219],v[136:139]
s_mul_i32 s91, s46, 17920
EX v5,v5
MM v[20:23],a[216:219],v[200:203],v[20:23]
s_add_u32 s90, s90, s91
TR a[148:149],v232 offset:57856
CV v1,v2,v3
MM v[20:23],a[212:215],v[184:187],v[20:23]
s_add_u32 s90, s90, 21659648
EX v6,v6
MM v[20:23],a[208:211],v[168:171],v[20:23]
EX v7,v7
MM v[28:31],a[208:211],v[172:175],v[140:143]
TR a[150:151],v232 offset:61952
TR a[152:153],v233 offset:49664
MM v[28:31],a[212:215],v[188:191],v[28:31]
EX v8,v8
MM v[28:31],a[216:219],v[204:207],v[28:31]
TR a[154:155],v233 offset:53760
CV v2,v4,v5
MM v[28:31],a[220:223],v[220:223],v[28:31]
EX v9,v9
MM v[24:27],a[204:207],v[220:223],v[140:143]
TR a[156:157],v233 offset:57856
CV v3,v6,v7
MM v[24:27],a[200:203],v[204:207],v[24:27]
EX v10,v10
MM v[24:27],a[196:199],v[188:191],v[24:27]
TR a[158:159],v233 offset:61952
CV v8,v8,v9
MM v[24:27],a[192:195],v[172:175],v[24:27]
EX v11,v11
MM v[32:35],a[224:227],v[160:163],v[128:131]
TR a[160:161],v232 offset:50176
TR a[162:163],v232 offset:54272
MM v[32:35],a[228:231],v[176:179],v[32:35]
EX v12,v12
MM v[32:35],a[232:235],v[192:195],v[32:35]
EX v13,v13
MM v[32:35],a[236:239],v[208:211],v[32:35]
TR a[164:165],v232 offset:58368
CV v9,v10,v11
MM v[36:39],a[252:255],v[208:211],v[128:131]
EX v14,v14
MM v[36:39],a[248:251],v[192:195],v[36:39]
TR a[166:167],v232 offset:62464
CV v10,v12,v13
MM v[36:39],a[244:247],v[176:179],v[36:39]
EX v15,v15
MM v[36:39],a[240:243],v[160:163],v[36:39]
TR a[168:169],v233 offset:50176
TR a[170:171],v233 offset:54272
MM v[44:47],a[240:243],v[164:167],v[132:135]
EX v16,v16
MM v[44:47],a[244:247],v[180:183],v[44:47]
EX v17,v17
MM v[44:47],a[248:251],v[196:199],v[44:47]
TR a[172:173],v233 offset:58368
CV v11,v14,v15
MM v[44:47],a[252:255],v[212:215],v[44:47]
EX v18,v18
MM v[40:43],a[236:239],v[212:215],v[132:135]
TR a[174:175],v233 offset:62464
CV v16,v16,v17
MM v[40:43],a[232:235],v[196:199],v[40:43]
EX v19,v19
MM v[40:43],a[228:231],v[180:183],v[40:43]
TR a[176:177],v232 offset:50688
TR a[178:179],v232 offset:54784
MM v[40:43],a[224:227],v[164:167],v[40:43]
EX v20,v20
MM v[48:51],a[224:227],v[168:171],v[136:139]
EX v21,v21
MM v[48:51],a[228:231],v[184:187],v[48:51]
TR a[180:181],v232 offset:58880
CV v17,v18,v19
MM v[48:51],a[232:235],v[200:203],v[48:51]
EX v22,v22
MM v[48:51],a[236:239],v[216:219],v[48:51]
TR a[182:183],v232 offset:62976
CV v18,v20,v21
MM v[52:55],a[252:255],v[216:219],v[136:139]
EX v23,v23
MM v[52:55],a[248:251],v[200:203],v[52:55]
TR a[184:185],v233 offset:50688
TR a[186:187],v233 offset:54784
MM v[52:55],a[244:247],v[184:187],v[52:55]
EX v24,v24
MM v[52:55],a[240:243],v[168:171],v[52:55]
TR a[188:189],v233 offset:58880
CV v19,v22,v23
MM v[60:63],a[240:243],v[172:175],v[140:143]
EX v25,v25
MM v[60:63],a[244:247],v[188:191],v[60:63]
TR a[190:191],v233 offset:62976
CV v24,v24,v25
MM v[60:63],a[248:251],v[204:207],v[60:63]
EX v26,v26
MM v[60:63],a[252:255],v[220:223],v[60:63]
EX v27,v27
MM v[56:59],a[236:239],v[220:223],v[140:143]
EX v28,v28
MM v[56:59],a[232:235],v[204:207],v[56:59]
EX v29,v29
MM v[56:59],a[228:231],v[188:191],v[56:59]
EX v30,v30
MM v[56:59],a[224:227],v[172:175],v[56:59]
s_waitcnt vmcnt(8) lgkmcnt(0)
s_barrier
MM a[0:3],a[128:131],v[64:67],a[0:3]
s_add_u32 m0, s65, 49152
CV v25,v26,v27
CV v26,v28,v29
MM a[0:3],a[132:135],v[68:71],a[0:3]
RD a[192:195],v224 offset:16384
RD a[196:199],v225 offset:16384
MM a[8:11],a[132:135],v[76:79],a[8:11]
EX v31,v31
MM a[8:11],a[128:131],v[72:75],a[8:11]
RD a[200:203],v226 offset:16384
CV v27,v30,v31
MM a[16:19],a[128:131],v[80:83],a[16:19]
TR v[96:97],v124 offset:0
TR v[98:99],v124 offset:512
MM a[16:19],a[132:135],v[84:87],a[16:19]
RD v[108:111],v126 offset:0
TR v[100:101],v124 offset:4096
MM a[24:27],a[132:135],v[92:95],a[24:27]
TR v[102:103],v124 offset:4608
RD v[112:115],v126 offset:512
MM a[24:27],a[128:131],v[88:91],a[24:27]
TR v[104:105],v124 offset:8192
TR v[106:107],v124 offset:8704
MM a[28:31],a[136:139],v[88:91],a[28:31]
RD v[116:119],v126 offset:1024
RD a[204:207],v227 offset:16384
EX v32,v32
MM a[28:31],a[140:143],v[92:95],a[28:31]
EX v33,v33
MM a[20:23],a[140:143],v[84:87],a[20:23]
EX v34,v34
MM a[20:23],a[136:139],v[80:83],a[20:23]
buffer_load_dwordx4 v228, s[8:11], s64 offen offset:0 lds
LPG20_%=:
s_cbranch_execz LTR3_%=
MM a[12:15],a[136:139],v[72:75],a[12:15]
EX v35,v35
MM a[12:15],a[140:143],v[76:79],a[12:15]
EX v36,v36
MM a[4:7],a[140:143],v[68:71],a[4:7]
CV v4,v32,v33
CV v5,v34,v35
MM a[4:7],a[136:139],v[64:67],a[4:7]
EX v37,v37
MM a[32:35],a[144:147],v[64:67],a[32:35]
EX v38,v38
MM a[32:35],a[148:151],v[68:71],a[32:35]
buffer_load_dwordx4 v229, s[8:11], s64 offen offset:1024 lds
MM a[40:43],a[148:151],v[76:79],a[40:43]
EX v39,v39
MM a[40:43],a[144:147],v[72:75],a[40:43]
RD a[208:211],v224 offset:20480
CV v6,v36,v37
MM a[48:51],a[144:147],v[80:83],a[48:51]
s_waitcnt lgkmcnt(2)
EX v40,v40
MM a[48:51],a[148:151],v[84:87],a[48:51]
RD a[212:215],v225 offset:20480
CV v7,v38,v39
MM v[120:123],v[96:99],v[108:111],0
MM v[120:123],v[100:103],v[112:115],v[120:123]
MM v[120:123],v[104:107],v[116:119],v[120:123]
MM a[56:59],a[148:151],v[92:95],a[56:59]
TR v[96:97],v124 offset:12288
TR v[98:99],v124 offset:12800
MM a[56:59],a[144:147],v[88:91],a[56:59]
RD v[108:111],v126 offset:1536
TR v[100:101],v124 offset:16384
MM a[60:63],a[152:155],v[88:91],a[60:63]
TR v[102:103],v124 offset:16896
RD v[112:115],v126 offset:2048
EX v41,v41
MM a[60:63],a[156:159],v[92:95],a[60:63]
TR v[104:105],v124 offset:20480
TR v[106:107],v124 offset:20992
MM a[52:55],a[156:159],v[84:87],a[52:55]
RD v[116:119],v126 offset:2560
CV v12,v40,v41
EX v42,v42
MM a[52:55],a[152:155],v[80:83],a[52:55]
EX v43,v43
MM a[44:47],a[152:155],v[72:75],a[44:47]
buffer_load_dwordx4 v230, s[8:11], s64 offen offset:2048 lds
MM a[44:47],a[156:159],v[76:79],a[44:47]
EX v44,v44
MM a[36:39],a[156:159],v[68:71],a[36:39]
EX v45,v45
MM a[36:39],a[152:155],v[64:67],a[36:39]
RD a[216:219],v226 offset:20480
CV v13,v42,v43
MM a[64:67],a[160:163],v[64:67],a[64:67]
EX v46,v46
MM a[64:67],a[164:167],v[68:71],a[64:67]
EX v47,v47
MM a[72:75],a[164:167],v[76:79],a[72:75]
buffer_load_dwordx4 v231, s[8:11], s64 offen offset:3072 lds
MM a[72:75],a[160:163],v[72:75],a[72:75]
s_add_u32 m0, s65, 114688
RD a[220:223],v227 offset:20480
CV v14,v44,v45
MM a[80:83],a[160:163],v[80:83],a[80:83]
EX v48,v48
MM a[80:83],a[164:167],v[84:87],a[80:83]
EX v49,v49
MM a[88:91],a[164:167],v[92:95],a[88:91]
s_waitcnt lgkmcnt(2)
RD a[224:227],v224 offset:24576
CV v15,v46,v47
MM a[88:91],a[160:163],v[88:91],a[88:91]
EX v50,v50
MM v[120:123],v[96:99],v[108:111],v[120:123]
MM v[120:123],v[100:103],v[112:115],v[120:123]
MM v[120:123],v[104:107],v[116:119],v[120:123]
MM a[92:95],a[168:171],v[88:91],a[92:95]
EX v51,v51
MM a[92:95],a[172:175],v[92:95],a[92:95]
buffer_load_dwordx4 v235, s[12:15], s81 offen offset:0 lds
MM a[84:87],a[172:175],v[84:87],a[84:87]
RD a[228:231],v225 offset:24576
CV v20,v48,v49
MM a[84:87],a[168:171],v[80:83],a[84:87]
EX v52,v52
MM a[76:79],a[168:171],v[72:75],a[76:79]
EX v53,v53
MM a[76:79],a[172:175],v[76:79],a[76:79]
RD a[232:235],v226 offset:24576
CV v21,v50,v51
MM a[68:71],a[172:175],v[68:71],a[68:71]
s_mov_b32 exec_lo, 0x00ff00ff
s_mov_b32 exec_hi, 0x00ff00ff
buffer_store_dwordx4 v[120:123], v127, s[20:23], s90 offen offset:0 sc1
s_mov_b64 exec, -1
s_nop 0
MM a[68:71],a[168:171],v[64:67],a[68:71]
EX v54,v54
MM a[96:99],a[176:179],v[64:67],a[96:99]
EX v55,v55
MM a[96:99],a[180:183],v[68:71],a[96:99]
RD a[236:239],v227 offset:24576
CV v22,v52,v53
MM a[104:107],a[180:183],v[76:79],a[104:107]
EX v56,v56
MM a[104:107],a[176:179],v[72:75],a[104:107]
buffer_load_dwordx4 v235, s[12:15], s64 offen offset:1024 lds
MM a[112:115],a[176:179],v[80:83],a[112:115]
EX v57,v57
MM a[112:115],a[180:183],v[84:87],a[112:115]
RD a[240:243],v224 offset:28672
CV v23,v54,v55
MM a[120:123],a[180:183],v[92:95],a[120:123]
EX v58,v58
MM a[120:123],a[176:179],v[88:91],a[120:123]
EX v59,v59
MM a[124:127],a[184:187],v[88:91],a[124:127]
v_add_f32 v244, v144, v145
v_add_f32 v245, v148, v149
MM a[124:127],a[188:191],v[92:95],a[124:127]
v_add_f32 v246, v152, v153
v_add_f32 v247, v156, v157
MM a[116:119],a[188:191],v[84:87],a[116:119]
EX v60,v60
MM a[116:119],a[184:187],v[80:83],a[116:119]
RD a[244:247],v225 offset:28672
CV v28,v56,v57
MM a[108:111],a[184:187],v[72:75],a[108:111]
EX v61,v61
MM a[108:111],a[188:191],v[76:79],a[108:111]
buffer_load_dwordx4 v235, s[12:15], s81 offen offset:2048 lds
MM a[100:103],a[188:191],v[68:71],a[100:103]
EX v62,v62
MM a[100:103],a[184:187],v[64:67],a[100:103]
RD a[248:251],v226 offset:28672
CV v29,v58,v59
v_smfmac_f32_16x16x64_bf16 v[144:147], v[236:239], v[0:7], v234
RD a[252:255],v227 offset:28672
EX v63,v63
v_smfmac_f32_16x16x64_bf16 v[148:151], v[236:239], v[8:15], v234
CV v30,v60,v61
v_smfmac_f32_16x16x64_bf16 v[152:155], v[236:239], v[16:23], v234
CV v31,v62,v63
s_nop 1
v_smfmac_f32_16x16x64_bf16 v[156:159], v[236:239], v[24:31], v234
s_waitcnt lgkmcnt(0)
buffer_load_dwordx4 v235, s[12:15], s64 offen offset:3072 lds
MM v[64:67],a[192:195],v[160:163],v[128:131]
s_add_u32 s64, s64, 16384
TR a[128:129],v232 offset:0
s_add_u32 s81, s64, 896
MM v[64:67],a[196:199],v[176:179],v[64:67]
TR a[130:131],v232 offset:4096
v_max3_f32 v249, v144, v145, v148
MM v[64:67],a[200:203],v[192:195],v[64:67]
TR a[132:133],v232 offset:8192
v_max3_f32 v249, v249, v149, v152
MM v[64:67],a[204:207],v[208:211],v[64:67]
TR a[134:135],v232 offset:12288
v_max3_f32 v249, v249, v153, v156
MM v[68:71],a[220:223],v[208:211],v[128:131]
TR a[136:137],v233 offset:0
v_max_f32 v249, v249, v157
MM v[68:71],a[216:219],v[192:195],v[68:71]
TR a[138:139],v233 offset:4096
v_cmp_lt_f32 vcc, s71, v249
MM v[68:71],a[212:215],v[176:179],v[68:71]
TR a[140:141],v233 offset:8192
TR a[142:143],v233 offset:12288
MM v[68:71],a[208:211],v[160:163],v[68:71]
s_cbranch_vccnz LSITE28_%=
LBACK28_%=:
MM v[76:79],a[208:211],v[164:167],v[132:135]
EX v64,v64
MM v[76:79],a[212:215],v[180:183],v[76:79]
EX v65,v65
MM v[76:79],a[216:219],v[196:199],v[76:79]
TR a[144:145],v232 offset:512
CV v64,v64,v65
MM v[76:79],a[220:223],v[212:215],v[76:79]
EX v66,v66
MM v[72:75],a[204:207],v[212:215],v[132:135]
EX v67,v67
MM v[72:75],a[200:203],v[196:199],v[72:75]
TR a[146:147],v232 offset:4608
CV v65,v66,v67
MM v[72:75],a[196:199],v[180:183],v[72:75]
EX v68,v68
MM v[72:75],a[192:195],v[164:167],v[72:75]
TR a[148:149],v232 offset:8704
TR a[150:151],v232 offset:12800
MM v[80:83],a[192:195],v[168:171],v[136:139]
EX v69,v69
MM v[80:83],a[196:199],v[184:187],v[80:83]
TR a[152:153],v233 offset:512
CV v66,v68,v69
MM v[80:83],a[200:203],v[200:203],v[80:83]
EX v70,v70
MM v[80:83],a[204:207],v[216:219],v[80:83]
TR a[154:155],v233 offset:4608
TR a[156:157],v233 offset:8704
MM v[84:87],a[220:223],v[216:219],v[136:139]
)ASM"
R"ASM(
v_lshlrev_b32 v32, 3, v248
v_min_u32 v32, 4992, v32
MM v[84:87],a[216:219],v[200:203],v[84:87]
s_sub_u32 s91, 5242880, s26
s_sub_u32 s60, s24, s91
s_subb_u32 s61, s25, 0
MM v[84:87],a[212:215],v[184:187],v[84:87]
s_mov_b32 s62, 5242880
EX v71,v71
MM v[84:87],a[208:211],v[168:171],v[84:87]
s_mov_b32 s63, 131072
TR a[158:159],v233 offset:12800
CV v67,v70,v71
MM v[92:95],a[208:211],v[172:175],v[140:143]
s_lshl_b32 s90, s16, 2
EX v72,v72
MM v[92:95],a[212:215],v[188:191],v[92:95]
s_add_u32 s90, s90, %[swave]
EX v73,v73
MM v[92:95],a[216:219],v[204:207],v[92:95]
s_mul_i32 s90, s90, 10240
TR a[160:161],v232 offset:1024
CV v72,v72,v73
MM v[92:95],a[220:223],v[220:223],v[92:95]
s_add_u32 s90, s90, 5120
EX v74,v74
MM v[88:91],a[204:207],v[220:223],v[140:143]
s_add_u32 s91, %[slds], 135168
s_mov_b32 m0, s91
s_nop 0
buffer_load_dword v32, s[60:63], s90 offen offset:0 lds
MM v[88:91],a[200:203],v[204:207],v[88:91]
EX v75,v75
MM v[88:91],a[196:199],v[188:191],v[88:91]
TR a[162:163],v232 offset:5120
CV v73,v74,v75
MM v[88:91],a[192:195],v[172:175],v[88:91]
EX v76,v76
MM v[96:99],a[224:227],v[160:163],v[128:131]
TR a[164:165],v232 offset:9216
TR a[166:167],v232 offset:13312
MM v[96:99],a[228:231],v[176:179],v[96:99]
EX v77,v77
MM v[96:99],a[232:235],v[192:195],v[96:99]
TR a[168:169],v233 offset:1024
CV v74,v76,v77
MM v[96:99],a[236:239],v[208:211],v[96:99]
EX v78,v78
MM v[100:103],a[252:255],v[208:211],v[128:131]
EX v79,v79
MM v[100:103],a[248:251],v[192:195],v[100:103]
TR a[170:171],v233 offset:5120
CV v75,v78,v79
MM v[100:103],a[244:247],v[176:179],v[100:103]
EX v80,v80
MM v[100:103],a[240:243],v[160:163],v[100:103]
TR a[172:173],v233 offset:9216
TR a[174:175],v233 offset:13312
MM v[108:111],a[240:243],v[164:167],v[132:135]
s_add_u32 s67, s95, 5120
EX v81,v81
MM v[108:111],a[244:247],v[180:183],v[108:111]
s_mov_b32 s70, 1
TR a[176:177],v232 offset:1536
CV v80,v80,v81
MM v[108:111],a[248:251],v[196:199],v[108:111]
EX v82,v82
MM v[108:111],a[252:255],v[212:215],v[108:111]
EX v83,v83
MM v[104:107],a[236:239],v[212:215],v[132:135]
TR a[178:179],v232 offset:5632
CV v81,v82,v83
MM v[104:107],a[232:235],v[196:199],v[104:107]
EX v84,v84
MM v[104:107],a[228:231],v[180:183],v[104:107]
TR a[180:181],v232 offset:9728
TR a[182:183],v232 offset:13824
MM v[104:107],a[224:227],v[164:167],v[104:107]
EX v85,v85
MM v[112:115],a[224:227],v[168:171],v[136:139]
TR a[184:185],v233 offset:1536
CV v82,v84,v85
MM v[112:115],a[228:231],v[184:187],v[112:115]
EX v86,v86
MM v[112:115],a[232:235],v[200:203],v[112:115]
EX v87,v87
MM v[112:115],a[236:239],v[216:219],v[112:115]
TR a[186:187],v233 offset:5632
CV v83,v86,v87
MM v[116:119],a[252:255],v[216:219],v[136:139]
EX v88,v88
MM v[116:119],a[248:251],v[200:203],v[116:119]
TR a[188:189],v233 offset:9728
TR a[190:191],v233 offset:13824
MM v[116:119],a[244:247],v[184:187],v[116:119]
EX v89,v89
MM v[116:119],a[240:243],v[168:171],v[116:119]
EX v90,v90
MM v[124:127],a[240:243],v[172:175],v[140:143]
EX v91,v91
MM v[124:127],a[244:247],v[188:191],v[124:127]
EX v92,v92
MM v[124:127],a[248:251],v[204:207],v[124:127]
EX v93,v93
MM v[124:127],a[252:255],v[220:223],v[124:127]
EX v94,v94
MM v[120:123],a[236:239],v[220:223],v[140:143]
EX v95,v95
MM v[120:123],a[232:235],v[204:207],v[120:123]
EX v96,v96
MM v[120:123],a[228:231],v[188:191],v[120:123]
EX v97,v97
MM v[120:123],a[224:227],v[172:175],v[120:123]
s_waitcnt vmcnt(14) lgkmcnt(0)
s_barrier
MM a[0:3],a[128:131],v[0:3],a[0:3]
s_add_u32 m0, s65, 0
CV v88,v88,v89
CV v89,v90,v91
MM a[0:3],a[132:135],v[4:7],a[0:3]
RD a[192:195],v224 offset:32768
CV v90,v92,v93
MM a[8:11],a[132:135],v[12:15],a[8:11]
RD a[196:199],v225 offset:32768
CV v91,v94,v95
MM a[8:11],a[128:131],v[8:11],a[8:11]
buffer_load_dwordx4 v228, s[8:11], s64 offen offset:0 lds
MM a[16:19],a[128:131],v[16:19],a[16:19]
RD a[200:203],v226 offset:32768
CV v68,v96,v97
MM a[16:19],a[132:135],v[20:23],a[16:19]
buffer_load_dwordx4 v229, s[8:11], s64 offen offset:1024 lds
MM a[24:27],a[132:135],v[28:31],a[24:27]
EX v98,v98
MM a[24:27],a[128:131],v[24:27],a[24:27]
RD a[204:207],v227 offset:32768
RD a[208:211],v224 offset:36864
MM a[28:31],a[136:139],v[24:27],a[28:31]
EX v99,v99
MM a[28:31],a[140:143],v[28:31],a[28:31]
RD a[212:215],v225 offset:36864
CV v69,v98,v99
MM a[20:23],a[140:143],v[20:23],a[20:23]
EX v100,v100
MM a[20:23],a[136:139],v[16:19],a[20:23]
EX v101,v101
MM a[12:15],a[136:139],v[8:11],a[12:15]
RD a[216:219],v226 offset:36864
CV v70,v100,v101
MM a[12:15],a[140:143],v[12:15],a[12:15]
buffer_load_dwordx4 v230, s[8:11], s64 offen offset:2048 lds
MM a[4:7],a[140:143],v[4:7],a[4:7]
EX v102,v102
MM a[4:7],a[136:139],v[0:3],a[4:7]
EX v103,v103
MM a[32:35],a[144:147],v[0:3],a[32:35]
RD a[220:223],v227 offset:36864
CV v71,v102,v103
MM a[32:35],a[148:151],v[4:7],a[32:35]
EX v104,v104
MM a[40:43],a[148:151],v[12:15],a[40:43]
EX v105,v105
MM a[40:43],a[144:147],v[8:11],a[40:43]
RD a[224:227],v224 offset:40960
CV v76,v104,v105
MM a[48:51],a[144:147],v[16:19],a[48:51]
buffer_load_dwordx4 v231, s[8:11], s64 offen offset:3072 lds
MM a[48:51],a[148:151],v[20:23],a[48:51]
s_add_u32 m0, s65, 65536
EX v106,v106
MM a[56:59],a[148:151],v[28:31],a[56:59]
EX v107,v107
MM a[56:59],a[144:147],v[24:27],a[56:59]
RD a[228:231],v225 offset:40960
CV v77,v106,v107
MM a[60:63],a[152:155],v[24:27],a[60:63]
EX v108,v108
MM a[60:63],a[156:159],v[28:31],a[60:63]
buffer_load_dwordx4 v235, s[12:15], s81 offen offset:0 lds
MM a[52:55],a[156:159],v[20:23],a[52:55]
EX v109,v109
MM a[52:55],a[152:155],v[16:19],a[52:55]
RD a[232:235],v226 offset:40960
CV v78,v108,v109
MM a[44:47],a[152:155],v[8:11],a[44:47]
EX v110,v110
MM a[44:47],a[156:159],v[12:15],a[44:47]
EX v111,v111
MM a[36:39],a[156:159],v[4:7],a[36:39]
RD a[236:239],v227 offset:40960
CV v79,v110,v111
MM a[36:39],a[152:155],v[0:3],a[36:39]
EX v112,v112
MM a[64:67],a[160:163],v[0:3],a[64:67]
buffer_load_dwordx4 v235, s[12:15], s64 offen offset:1024 lds
MM a[64:67],a[164:167],v[4:7],a[64:67]
EX v113,v113
MM a[72:75],a[164:167],v[12:15],a[72:75]
RD a[240:243],v224 offset:45056
CV v84,v112,v113
MM a[72:75],a[160:163],v[8:11],a[72:75]
EX v114,v114
MM a[80:83],a[160:163],v[16:19],a[80:83]
EX v115,v115
MM a[80:83],a[164:167],v[20:23],a[80:83]
RD a[244:247],v225 offset:45056
CV v85,v114,v115
MM a[88:91],a[164:167],v[28:31],a[88:91]
EX v116,v116
MM a[88:91],a[160:163],v[24:27],a[88:91]
buffer_load_dwordx4 v235, s[12:15], s81 offen offset:2048 lds
MM a[92:95],a[168:171],v[24:27],a[92:95]
EX v117,v117
MM a[92:95],a[172:175],v[28:31],a[92:95]
RD a[248:251],v226 offset:45056
CV v86,v116,v117
MM a[84:87],a[172:175],v[20:23],a[84:87]
EX v118,v118
MM a[84:87],a[168:171],v[16:19],a[84:87]
EX v119,v119
MM a[76:79],a[168:171],v[8:11],a[76:79]
RD a[252:255],v227 offset:45056
CV v87,v118,v119
MM a[76:79],a[172:175],v[12:15],a[76:79]
EX v120,v120
MM a[68:71],a[172:175],v[4:7],a[68:71]
buffer_load_dwordx4 v235, s[12:15], s64 offen offset:3072 lds
MM a[68:71],a[168:171],v[0:3],a[68:71]
EX v121,v121
MM a[96:99],a[176:179],v[0:3],a[96:99]
EX v122,v122
MM a[96:99],a[180:183],v[4:7],a[96:99]
EX v123,v123
MM a[104:107],a[180:183],v[12:15],a[104:107]
EX v124,v124
MM a[104:107],a[176:179],v[8:11],a[104:107]
EX v125,v125
MM a[112:115],a[176:179],v[16:19],a[112:115]
EX v126,v126
MM a[112:115],a[180:183],v[20:23],a[112:115]
EX v127,v127
MM a[120:123],a[180:183],v[28:31],a[120:123]
s_lshl_b32 s91, s47, 2
CV v92,v120,v121
CV v93,v122,v123
MM a[120:123],a[176:179],v[24:27],a[120:123]
s_add_u32 s91, s91, %[swave]
CV v94,v124,v125
CV v95,v126,v127
MM a[124:127],a[184:187],v[24:27],a[124:127]
s_lshl_b32 s91, s91, 2
v_add_f32 v244, v144, v145
v_add_f32 v245, v148, v149
MM a[124:127],a[188:191],v[28:31],a[124:127]
s_add_u32 s91, s91, 73728
v_add_f32 v246, v152, v153
v_add_f32 v247, v156, v157
MM a[116:119],a[188:191],v[20:23],a[116:119]
s_waitcnt vmcnt(12)
v_mov_b32 v250, s69
s_mov_b64 exec, 1
buffer_store_dword v250, off, s[20:23], s91 offset:0 sc1
s_mov_b64 exec, -1
MM a[116:119],a[184:187],v[16:19],a[116:119]
MM a[108:111],a[184:187],v[8:11],a[108:111]
MM a[108:111],a[188:191],v[12:15],a[108:111]
MM a[100:103],a[188:191],v[4:7],a[100:103]
MM a[100:103],a[184:187],v[0:3],a[100:103]
v_smfmac_f32_16x16x64_bf16 v[144:147], v[236:239], v[64:71], v234
v_smfmac_f32_16x16x64_bf16 v[148:151], v[236:239], v[72:79], v234
v_smfmac_f32_16x16x64_bf16 v[152:155], v[236:239], v[80:87], v234
v_smfmac_f32_16x16x64_bf16 v[156:159], v[236:239], v[88:95], v234
s_waitcnt lgkmcnt(0)
s_cmp_ge_u32 s16, 20
s_cbranch_scc1 LLAP_%=
MM v[0:3],a[192:195],v[160:163],v[128:131]
s_add_u32 s64, s64, 16384
TR a[128:129],v232 offset:16384
s_add_u32 s81, s64, 896
MM v[0:3],a[196:199],v[176:179],v[0:3]
TR a[130:131],v232 offset:20480
v_max3_f32 v249, v144, v145, v148
MM v[0:3],a[200:203],v[192:195],v[0:3]
TR a[132:133],v232 offset:24576
v_max3_f32 v249, v249, v149, v152
MM v[0:3],a[204:207],v[208:211],v[0:3]
TR a[134:135],v232 offset:28672
v_max3_f32 v249, v249, v153, v156
MM v[4:7],a[220:223],v[208:211],v[128:131]
TR a[136:137],v233 offset:16384
v_max_f32 v249, v249, v157
MM v[4:7],a[216:219],v[192:195],v[4:7]
TR a[138:139],v233 offset:20480
v_cmp_lt_f32 vcc, s71, v249
MM v[4:7],a[212:215],v[176:179],v[4:7]
TR a[140:141],v233 offset:24576
TR a[142:143],v233 offset:28672
MM v[4:7],a[208:211],v[160:163],v[4:7]
s_cbranch_vccnz LSITE29_%=
LBACK29_%=:
MM v[12:15],a[208:211],v[164:167],v[132:135]
EX v0,v0
MM v[12:15],a[212:215],v[180:183],v[12:15]
EX v1,v1
MM v[12:15],a[216:219],v[196:199],v[12:15]
TR a[144:145],v232 offset:16896
CV v0,v0,v1
MM v[12:15],a[220:223],v[212:215],v[12:15]
EX v2,v2
MM v[8:11],a[204:207],v[212:215],v[132:135]
EX v3,v3
MM v[8:11],a[200:203],v[196:199],v[8:11]
TR a[146:147],v232 offset:20992
CV v1,v2,v3
MM v[8:11],a[196:199],v[180:183],v[8:11]
EX v4,v4
MM v[8:11],a[192:195],v[164:167],v[8:11]
TR a[148:149],v232 offset:25088
TR a[150:151],v232 offset:29184
MM v[16:19],a[192:195],v[168:171],v[136:139]
EX v5,v5
MM v[16:19],a[196:199],v[184:187],v[16:19]
TR a[152:153],v233 offset:16896
CV v2,v4,v5
MM v[16:19],a[200:203],v[200:203],v[16:19]
EX v6,v6
MM v[16:19],a[204:207],v[216:219],v[16:19]
TR a[154:155],v233 offset:20992
TR a[156:157],v233 offset:25088
MM v[20:23],a[220:223],v[216:219],v[136:139]
s_add_u32 s87, s95, 5120
buffer_store_dword v243, v248, s[20:23], s87 offen offset:12 sc1
MM v[20:23],a[216:219],v[200:203],v[20:23]
EX v7,v7
MM v[20:23],a[212:215],v[184:187],v[20:23]
TR a[158:159],v233 offset:29184
CV v3,v6,v7
MM v[20:23],a[208:211],v[168:171],v[20:23]
EX v8,v8
MM v[28:31],a[208:211],v[172:175],v[140:143]
EX v9,v9
MM v[28:31],a[212:215],v[188:191],v[28:31]
TR a[160:161],v232 offset:17408
CV v8,v8,v9
MM v[28:31],a[216:219],v[204:207],v[28:31]
EX v10,v10
MM v[28:31],a[220:223],v[220:223],v[28:31]
EX v11,v11
MM v[24:27],a[204:207],v[220:223],v[140:143]
TR a[162:163],v232 offset:21504
CV v9,v10,v11
MM v[24:27],a[200:203],v[204:207],v[24:27]
TR a[164:165],v232 offset:25600
TR a[166:167],v232 offset:29696
MM v[24:27],a[196:199],v[188:191],v[24:27]
EX v12,v12
MM v[24:27],a[192:195],v[172:175],v[24:27]
EX v13,v13
MM v[32:35],a[224:227],v[160:163],v[128:131]
TR a[168:169],v233 offset:17408
CV v10,v12,v13
MM v[32:35],a[228:231],v[176:179],v[32:35]
EX v14,v14
MM v[32:35],a[232:235],v[192:195],v[32:35]
TR a[170:171],v233 offset:21504
TR a[172:173],v233 offset:25600
MM v[32:35],a[236:239],v[208:211],v[32:35]
EX v15,v15
MM v[36:39],a[252:255],v[208:211],v[128:131]
TR a[174:175],v233 offset:29696
CV v11,v14,v15
MM v[36:39],a[248:251],v[192:195],v[36:39]
EX v16,v16
MM v[36:39],a[244:247],v[176:179],v[36:39]
EX v17,v17
MM v[36:39],a[240:243],v[160:163],v[36:39]
TR a[176:177],v232 offset:17920
CV v16,v16,v17
MM v[44:47],a[240:243],v[164:167],v[132:135]
EX v18,v18
MM v[44:47],a[244:247],v[180:183],v[44:47]
TR a[178:179],v232 offset:22016
TR a[180:181],v232 offset:26112
MM v[44:47],a[248:251],v[196:199],v[44:47]
EX v19,v19
MM v[44:47],a[252:255],v[212:215],v[44:47]
s_nop 0
LPG21_%=:
s_cbranch_execz LTR4_%=
TR a[182:183],v232 offset:30208
CV v17,v18,v19
MM v[40:43],a[236:239],v[212:215],v[132:135]
EX v20,v20
MM v[40:43],a[232:235],v[196:199],v[40:43]
)ASM"
R"ASM(
EX v21,v21
MM v[40:43],a[228:231],v[180:183],v[40:43]
TR a[184:185],v233 offset:17920
CV v18,v20,v21
MM v[40:43],a[224:227],v[164:167],v[40:43]
EX v22,v22
MM v[48:51],a[224:227],v[168:171],v[136:139]
TR a[186:187],v233 offset:22016
TR a[188:189],v233 offset:26112
MM v[48:51],a[228:231],v[184:187],v[48:51]
EX v23,v23
MM v[48:51],a[232:235],v[200:203],v[48:51]
TR a[190:191],v233 offset:30208
CV v19,v22,v23
MM v[48:51],a[236:239],v[216:219],v[48:51]
EX v24,v24
MM v[52:55],a[252:255],v[216:219],v[136:139]
EX v25,v25
MM v[52:55],a[248:251],v[200:203],v[52:55]
EX v26,v26
MM v[52:55],a[244:247],v[184:187],v[52:55]
EX v27,v27
MM v[52:55],a[240:243],v[168:171],v[52:55]
EX v28,v28
MM v[60:63],a[240:243],v[172:175],v[140:143]
EX v29,v29
MM v[60:63],a[244:247],v[188:191],v[60:63]
EX v30,v30
MM v[60:63],a[248:251],v[204:207],v[60:63]
EX v31,v31
MM v[60:63],a[252:255],v[220:223],v[60:63]
EX v32,v32
MM v[56:59],a[236:239],v[220:223],v[140:143]
EX v33,v33
MM v[56:59],a[232:235],v[204:207],v[56:59]
EX v34,v34
MM v[56:59],a[228:231],v[188:191],v[56:59]
EX v35,v35
MM v[56:59],a[224:227],v[172:175],v[56:59]
s_waitcnt lgkmcnt(0)
s_barrier
MM a[0:3],a[128:131],v[64:67],a[0:3]
s_add_u32 m0, s65, 16384
CV v24,v24,v25
CV v25,v26,v27
MM a[0:3],a[132:135],v[68:71],a[0:3]
RD a[192:195],v224 offset:49152
CV v26,v28,v29
MM a[8:11],a[132:135],v[76:79],a[8:11]
RD a[196:199],v225 offset:49152
CV v27,v30,v31
MM a[8:11],a[128:131],v[72:75],a[8:11]
buffer_load_dwordx4 v228, s[8:11], s64 offen offset:0 lds
MM a[16:19],a[128:131],v[80:83],a[16:19]
RD a[200:203],v226 offset:49152
CV v4,v32,v33
MM a[16:19],a[132:135],v[84:87],a[16:19]
RD a[204:207],v227 offset:49152
CV v5,v34,v35
MM a[24:27],a[132:135],v[92:95],a[24:27]
buffer_load_dwordx4 v229, s[8:11], s64 offen offset:1024 lds
MM a[24:27],a[128:131],v[88:91],a[24:27]
RD a[208:211],v224 offset:53248
RD a[212:215],v225 offset:53248
MM a[28:31],a[136:139],v[88:91],a[28:31]
buffer_load_dwordx4 v230, s[8:11], s64 offen offset:2048 lds
MM a[28:31],a[140:143],v[92:95],a[28:31]
EX v36,v36
MM a[20:23],a[140:143],v[84:87],a[20:23]
EX v37,v37
MM a[20:23],a[136:139],v[80:83],a[20:23]
RD a[216:219],v226 offset:53248
CV v6,v36,v37
MM a[12:15],a[136:139],v[72:75],a[12:15]
EX v38,v38
MM a[12:15],a[140:143],v[76:79],a[12:15]
RD a[220:223],v227 offset:53248
RD a[224:227],v224 offset:57344
MM a[4:7],a[140:143],v[68:71],a[4:7]
EX v39,v39
MM a[4:7],a[136:139],v[64:67],a[4:7]
RD a[228:231],v225 offset:57344
CV v7,v38,v39
MM a[32:35],a[144:147],v[64:67],a[32:35]
EX v40,v40
MM a[32:35],a[148:151],v[68:71],a[32:35]
buffer_load_dwordx4 v231, s[8:11], s64 offen offset:3072 lds
MM a[40:43],a[148:151],v[76:79],a[40:43]
s_add_u32 m0, s65, 81920
EX v41,v41
MM a[40:43],a[144:147],v[72:75],a[40:43]
RD a[232:235],v226 offset:57344
CV v12,v40,v41
MM a[48:51],a[144:147],v[80:83],a[48:51]
EX v42,v42
MM a[48:51],a[148:151],v[84:87],a[48:51]
EX v43,v43
MM a[56:59],a[148:151],v[92:95],a[56:59]
RD a[236:239],v227 offset:57344
CV v13,v42,v43
MM a[56:59],a[144:147],v[88:91],a[56:59]
EX v44,v44
MM a[60:63],a[152:155],v[88:91],a[60:63]
buffer_load_dwordx4 v235, s[12:15], s81 offen offset:0 lds
MM a[60:63],a[156:159],v[92:95],a[60:63]
EX v45,v45
MM a[52:55],a[156:159],v[84:87],a[52:55]
RD a[240:243],v224 offset:61440
CV v14,v44,v45
MM a[52:55],a[152:155],v[80:83],a[52:55]
EX v46,v46
MM a[44:47],a[152:155],v[72:75],a[44:47]
EX v47,v47
MM a[44:47],a[156:159],v[76:79],a[44:47]
RD a[244:247],v225 offset:61440
CV v15,v46,v47
MM a[36:39],a[156:159],v[68:71],a[36:39]
buffer_load_dwordx4 v235, s[12:15], s64 offen offset:1024 lds
MM a[36:39],a[152:155],v[64:67],a[36:39]
EX v48,v48
MM a[64:67],a[160:163],v[64:67],a[64:67]
EX v49,v49
MM a[64:67],a[164:167],v[68:71],a[64:67]
RD a[248:251],v226 offset:61440
CV v20,v48,v49
MM a[72:75],a[164:167],v[76:79],a[72:75]
EX v50,v50
MM a[72:75],a[160:163],v[72:75],a[72:75]
EX v51,v51
MM a[80:83],a[160:163],v[80:83],a[80:83]
RD a[252:255],v227 offset:61440
CV v21,v50,v51
MM a[80:83],a[164:167],v[84:87],a[80:83]
buffer_load_dwordx4 v235, s[12:15], s81 offen offset:2048 lds
MM a[88:91],a[164:167],v[92:95],a[88:91]
EX v52,v52
MM a[88:91],a[160:163],v[88:91],a[88:91]
EX v53,v53
MM a[92:95],a[168:171],v[88:91],a[92:95]
EX v54,v54
MM a[92:95],a[172:175],v[92:95],a[92:95]
buffer_load_dwordx4 v235, s[12:15], s64 offen offset:3072 lds
MM a[84:87],a[172:175],v[84:87],a[84:87]
EX v55,v55
MM a[84:87],a[168:171],v[80:83],a[84:87]
EX v56,v56
MM a[76:79],a[168:171],v[72:75],a[76:79]
EX v57,v57
MM a[76:79],a[172:175],v[76:79],a[76:79]
EX v58,v58
MM a[68:71],a[172:175],v[68:71],a[68:71]
EX v59,v59
MM a[68:71],a[168:171],v[64:67],a[68:71]
EX v60,v60
MM a[96:99],a[176:179],v[64:67],a[96:99]
EX v61,v61
MM a[96:99],a[180:183],v[68:71],a[96:99]
EX v62,v62
MM a[104:107],a[180:183],v[76:79],a[104:107]
EX v63,v63
MM a[104:107],a[176:179],v[72:75],a[104:107]
CV v22,v52,v53
CV v23,v54,v55
MM a[112:115],a[176:179],v[80:83],a[112:115]
CV v28,v56,v57
CV v29,v58,v59
MM a[112:115],a[180:183],v[84:87],a[112:115]
CV v30,v60,v61
CV v31,v62,v63
MM a[120:123],a[180:183],v[92:95],a[120:123]
MM a[120:123],a[176:179],v[88:91],a[120:123]
MM a[124:127],a[184:187],v[88:91],a[124:127]
v_add_f32 v244, v144, v145
v_add_f32 v245, v148, v149
MM a[124:127],a[188:191],v[92:95],a[124:127]
v_add_f32 v246, v152, v153
v_add_f32 v247, v156, v157
MM a[116:119],a[188:191],v[84:87],a[116:119]
MM a[116:119],a[184:187],v[80:83],a[116:119]
MM a[108:111],a[184:187],v[72:75],a[108:111]
MM a[108:111],a[188:191],v[76:79],a[108:111]
MM a[100:103],a[188:191],v[68:71],a[100:103]
MM a[100:103],a[184:187],v[64:67],a[100:103]
v_smfmac_f32_16x16x64_bf16 v[144:147], v[236:239], v[0:7], v234
v_smfmac_f32_16x16x64_bf16 v[148:151], v[236:239], v[8:15], v234
v_smfmac_f32_16x16x64_bf16 v[152:155], v[236:239], v[16:23], v234
v_smfmac_f32_16x16x64_bf16 v[156:159], v[236:239], v[24:31], v234
s_waitcnt lgkmcnt(0)
MM v[64:67],a[192:195],v[160:163],v[128:131]
s_add_u32 s64, s64, 16384
TR a[128:129],v232 offset:32768
s_add_u32 s81, s64, 896
MM v[64:67],a[196:199],v[176:179],v[64:67]
TR a[130:131],v232 offset:36864
v_max3_f32 v249, v144, v145, v148
MM v[64:67],a[200:203],v[192:195],v[64:67]
TR a[132:133],v232 offset:40960
v_max3_f32 v249, v249, v149, v152
MM v[64:67],a[204:207],v[208:211],v[64:67]
TR a[134:135],v232 offset:45056
v_max3_f32 v249, v249, v153, v156
MM v[68:71],a[220:223],v[208:211],v[128:131]
TR a[136:137],v233 offset:32768
v_max_f32 v249, v249, v157
MM v[68:71],a[216:219],v[192:195],v[68:71]
TR a[138:139],v233 offset:36864
v_cmp_lt_f32 vcc, s71, v249
MM v[68:71],a[212:215],v[176:179],v[68:71]
TR a[140:141],v233 offset:40960
TR a[142:143],v233 offset:45056
MM v[68:71],a[208:211],v[160:163],v[68:71]
s_cbranch_vccnz LSITE30_%=
LBACK30_%=:
MM v[76:79],a[208:211],v[164:167],v[132:135]
s_and_b32 s90, s17, 63
s_cmp_lt_u32 s17, 80
s_cselect_b32 s17, s17, s90
s_cselect_b64 s[44:45], -1, 0
MM v[76:79],a[212:215],v[180:183],v[76:79]
v_lshrrev_b32 v253, 4, v248
v_min_u32 v253, 35, v253
MM v[76:79],a[216:219],v[196:199],v[76:79]
v_lshrrev_b32 v252, 2, v253
v_mul_u32_u24 v252, 144, v252
MM v[76:79],a[220:223],v[212:215],v[76:79]
TR a[144:145],v232 offset:33280
v_lshl_add_u32 v253, v253, 2, v252
MM v[72:75],a[204:207],v[212:215],v[132:135]
s_lshr_b32 s90, s17, 3
EX v64,v64
MM v[72:75],a[200:203],v[196:199],v[72:75]
s_lshl_b32 s76, s90, 4
EX v65,v65
MM v[72:75],a[196:199],v[180:183],v[72:75]
s_add_u32 s76, s76, 73728
buffer_load_dword v252, v253, s[20:23], s76 offen offset:0 sc1
MM v[72:75],a[192:195],v[164:167],v[72:75]
EX v66,v66
MM v[80:83],a[192:195],v[168:171],v[136:139]
EX v67,v67
MM v[80:83],a[196:199],v[184:187],v[80:83]
EX v68,v68
MM v[80:83],a[200:203],v[200:203],v[80:83]
TR a[146:147],v232 offset:37376
CV v64,v64,v65
MM v[80:83],a[204:207],v[216:219],v[80:83]
EX v69,v69
MM v[84:87],a[220:223],v[216:219],v[136:139]
TR a[148:149],v232 offset:41472
CV v65,v66,v67
MM v[84:87],a[216:219],v[200:203],v[84:87]
EX v70,v70
MM v[84:87],a[212:215],v[184:187],v[84:87]
TR a[150:151],v232 offset:45568
CV v66,v68,v69
MM v[84:87],a[208:211],v[168:171],v[84:87]
EX v71,v71
MM v[92:95],a[208:211],v[172:175],v[140:143]
TR a[152:153],v233 offset:33280
TR a[154:155],v233 offset:37376
MM v[92:95],a[212:215],v[188:191],v[92:95]
EX v72,v72
MM v[92:95],a[216:219],v[204:207],v[92:95]
EX v73,v73
MM v[92:95],a[220:223],v[220:223],v[92:95]
TR a[156:157],v233 offset:41472
CV v67,v70,v71
MM v[88:91],a[204:207],v[220:223],v[140:143]
EX v74,v74
MM v[88:91],a[200:203],v[204:207],v[88:91]
TR a[158:159],v233 offset:45568
CV v72,v72,v73
MM v[88:91],a[196:199],v[188:191],v[88:91]
EX v75,v75
MM v[88:91],a[192:195],v[172:175],v[88:91]
TR a[160:161],v232 offset:33792
TR a[162:163],v232 offset:37888
MM v[96:99],a[224:227],v[160:163],v[128:131]
EX v76,v76
MM v[96:99],a[228:231],v[176:179],v[96:99]
TR a[164:165],v232 offset:41984
CV v73,v74,v75
MM v[96:99],a[232:235],v[192:195],v[96:99]
EX v77,v77
MM v[96:99],a[236:239],v[208:211],v[96:99]
EX v78,v78
MM v[100:103],a[252:255],v[208:211],v[128:131]
TR a[166:167],v232 offset:46080
CV v74,v76,v77
MM v[100:103],a[248:251],v[192:195],v[100:103]
EX v79,v79
MM v[100:103],a[244:247],v[176:179],v[100:103]
TR a[168:169],v233 offset:33792
CV v75,v78,v79
MM v[100:103],a[240:243],v[160:163],v[100:103]
EX v80,v80
MM v[108:111],a[240:243],v[164:167],v[132:135]
TR a[170:171],v233 offset:37888
TR a[172:173],v233 offset:41984
MM v[108:111],a[244:247],v[180:183],v[108:111]
EX v81,v81
MM v[108:111],a[248:251],v[196:199],v[108:111]
TR a[174:175],v233 offset:46080
CV v80,v80,v81
MM v[108:111],a[252:255],v[212:215],v[108:111]
EX v82,v82
MM v[104:107],a[236:239],v[212:215],v[132:135]
EX v83,v83
MM v[104:107],a[232:235],v[196:199],v[104:107]
TR a[176:177],v232 offset:34304
TR a[178:179],v232 offset:38400
MM v[104:107],a[228:231],v[180:183],v[104:107]
EX v84,v84
MM v[104:107],a[224:227],v[164:167],v[104:107]
TR a[180:181],v232 offset:42496
CV v81,v82,v83
MM v[112:115],a[224:227],v[168:171],v[136:139]
EX v85,v85
MM v[112:115],a[228:231],v[184:187],v[112:115]
TR a[182:183],v232 offset:46592
CV v82,v84,v85
MM v[112:115],a[232:235],v[200:203],v[112:115]
EX v86,v86
MM v[112:115],a[236:239],v[216:219],v[112:115]
TR a[184:185],v233 offset:34304
TR a[186:187],v233 offset:38400
MM v[116:119],a[252:255],v[216:219],v[136:139]
EX v87,v87
MM v[116:119],a[248:251],v[200:203],v[116:119]
TR a[188:189],v233 offset:42496
CV v83,v86,v87
MM v[116:119],a[244:247],v[184:187],v[116:119]
EX v88,v88
MM v[116:119],a[240:243],v[168:171],v[116:119]
EX v89,v89
MM v[124:127],a[240:243],v[172:175],v[140:143]
TR a[190:191],v233 offset:46592
CV v88,v88,v89
MM v[124:127],a[244:247],v[188:191],v[124:127]
EX v90,v90
MM v[124:127],a[248:251],v[204:207],v[124:127]
EX v91,v91
MM v[124:127],a[252:255],v[220:223],v[124:127]
EX v92,v92
MM v[120:123],a[236:239],v[220:223],v[140:143]
EX v93,v93
MM v[120:123],a[232:235],v[204:207],v[120:123]
EX v94,v94
MM v[120:123],a[228:231],v[188:191],v[120:123]
EX v95,v95
MM v[120:123],a[224:227],v[172:175],v[120:123]
s_waitcnt vmcnt(15) lgkmcnt(0)
s_barrier
MM a[0:3],a[128:131],v[0:3],a[0:3]
s_add_u32 m0, s65, 32768
CV v89,v90,v91
CV v90,v92,v93
MM a[0:3],a[132:135],v[4:7],a[0:3]
RD a[192:195],v224 offset:0
CV v91,v94,v95
MM a[8:11],a[132:135],v[12:15],a[8:11]
buffer_load_dwordx4 v228, s[8:11], s64 offen offset:0 lds
MM a[8:11],a[128:131],v[8:11],a[8:11]
RD a[196:199],v225 offset:0
RD a[200:203],v226 offset:0
MM a[16:19],a[128:131],v[16:19],a[16:19]
EX v96,v96
MM a[16:19],a[132:135],v[20:23],a[16:19]
EX v97,v97
MM a[24:27],a[132:135],v[28:31],a[24:27]
RD a[204:207],v227 offset:0
CV v68,v96,v97
MM a[24:27],a[128:131],v[24:27],a[24:27]
EX v98,v98
MM a[28:31],a[136:139],v[24:27],a[28:31]
buffer_load_dwordx4 v229, s[8:11], s64 offen offset:1024 lds
MM a[28:31],a[140:143],v[28:31],a[28:31]
EX v99,v99
MM a[20:23],a[140:143],v[20:23],a[20:23]
RD a[208:211],v224 offset:4096
CV v69,v98,v99
MM a[20:23],a[136:139],v[16:19],a[20:23]
EX v100,v100
MM a[12:15],a[136:139],v[8:11],a[12:15]
EX v101,v101
MM a[12:15],a[140:143],v[12:15],a[12:15]
RD a[212:215],v225 offset:4096
CV v70,v100,v101
MM a[4:7],a[140:143],v[4:7],a[4:7]
)ASM"
R"ASM(
EX v102,v102
MM a[4:7],a[136:139],v[0:3],a[4:7]
buffer_load_dwordx4 v230, s[8:11], s64 offen offset:2048 lds
MM a[32:35],a[144:147],v[0:3],a[32:35]
EX v103,v103
MM a[32:35],a[148:151],v[4:7],a[32:35]
RD a[216:219],v226 offset:4096
CV v71,v102,v103
MM a[40:43],a[148:151],v[12:15],a[40:43]
EX v104,v104
MM a[40:43],a[144:147],v[8:11],a[40:43]
EX v105,v105
MM a[48:51],a[144:147],v[16:19],a[48:51]
RD a[220:223],v227 offset:4096
CV v76,v104,v105
MM a[48:51],a[148:151],v[20:23],a[48:51]
EX v106,v106
MM a[56:59],a[148:151],v[28:31],a[56:59]
buffer_load_dwordx4 v231, s[8:11], s64 offen offset:3072 lds
MM a[56:59],a[144:147],v[24:27],a[56:59]
s_add_u32 m0, s65, 98304
EX v107,v107
MM a[60:63],a[152:155],v[24:27],a[60:63]
RD a[224:227],v224 offset:8192
CV v77,v106,v107
MM a[60:63],a[156:159],v[28:31],a[60:63]
EX v108,v108
MM a[52:55],a[156:159],v[20:23],a[52:55]
EX v109,v109
MM a[52:55],a[152:155],v[16:19],a[52:55]
RD a[228:231],v225 offset:8192
CV v78,v108,v109
MM a[44:47],a[152:155],v[8:11],a[44:47]
EX v110,v110
MM a[44:47],a[156:159],v[12:15],a[44:47]
buffer_load_dwordx4 v235, s[12:15], s81 offen offset:0 lds
MM a[36:39],a[156:159],v[4:7],a[36:39]
EX v111,v111
MM a[36:39],a[152:155],v[0:3],a[36:39]
RD a[232:235],v226 offset:8192
CV v79,v110,v111
MM a[64:67],a[160:163],v[0:3],a[64:67]
EX v112,v112
MM a[64:67],a[164:167],v[4:7],a[64:67]
EX v113,v113
MM a[72:75],a[164:167],v[12:15],a[72:75]
RD a[236:239],v227 offset:8192
CV v84,v112,v113
MM a[72:75],a[160:163],v[8:11],a[72:75]
buffer_load_dwordx4 v235, s[12:15], s64 offen offset:1024 lds
MM a[80:83],a[160:163],v[16:19],a[80:83]
EX v114,v114
MM a[80:83],a[164:167],v[20:23],a[80:83]
EX v115,v115
MM a[88:91],a[164:167],v[28:31],a[88:91]
RD a[240:243],v224 offset:12288
CV v85,v114,v115
MM a[88:91],a[160:163],v[24:27],a[88:91]
EX v116,v116
MM a[92:95],a[168:171],v[24:27],a[92:95]
EX v117,v117
MM a[92:95],a[172:175],v[28:31],a[92:95]
RD a[244:247],v225 offset:12288
CV v86,v116,v117
MM a[84:87],a[172:175],v[20:23],a[84:87]
buffer_load_dwordx4 v235, s[12:15], s81 offen offset:2048 lds
MM a[84:87],a[168:171],v[16:19],a[84:87]
EX v118,v118
MM a[76:79],a[168:171],v[8:11],a[76:79]
EX v119,v119
MM a[76:79],a[172:175],v[12:15],a[76:79]
RD a[248:251],v226 offset:12288
CV v87,v118,v119
MM a[68:71],a[172:175],v[4:7],a[68:71]
EX v120,v120
MM a[68:71],a[168:171],v[0:3],a[68:71]
EX v121,v121
MM a[96:99],a[176:179],v[0:3],a[96:99]
RD a[252:255],v227 offset:12288
CV v92,v120,v121
MM a[96:99],a[180:183],v[4:7],a[96:99]
buffer_load_dwordx4 v235, s[12:15], s64 offen offset:3072 lds
MM a[104:107],a[180:183],v[12:15],a[104:107]
EX v122,v122
MM a[104:107],a[176:179],v[8:11],a[104:107]
EX v123,v123
MM a[112:115],a[176:179],v[16:19],a[112:115]
EX v124,v124
MM a[112:115],a[180:183],v[20:23],a[112:115]
EX v125,v125
MM a[120:123],a[180:183],v[28:31],a[120:123]
EX v126,v126
MM a[120:123],a[176:179],v[24:27],a[120:123]
EX v127,v127
MM a[124:127],a[184:187],v[24:27],a[124:127]
v_add_f32 v244, v144, v145
v_add_f32 v245, v148, v149
MM a[124:127],a[188:191],v[28:31],a[124:127]
v_add_f32 v246, v152, v153
v_add_f32 v247, v156, v157
MM a[116:119],a[188:191],v[20:23],a[116:119]
CV v93,v122,v123
CV v94,v124,v125
MM a[116:119],a[184:187],v[16:19],a[116:119]
CV v95,v126,v127
MM a[108:111],a[184:187],v[8:11],a[108:111]
MM a[108:111],a[188:191],v[12:15],a[108:111]
MM a[100:103],a[188:191],v[4:7],a[100:103]
MM a[100:103],a[184:187],v[0:3],a[100:103]
v_smfmac_f32_16x16x64_bf16 v[144:147], v[236:239], v[64:71], v234
v_smfmac_f32_16x16x64_bf16 v[148:151], v[236:239], v[72:79], v234
v_smfmac_f32_16x16x64_bf16 v[152:155], v[236:239], v[80:87], v234
v_smfmac_f32_16x16x64_bf16 v[156:159], v[236:239], v[88:95], v234
s_waitcnt lgkmcnt(0)
MM v[0:3],a[192:195],v[160:163],v[128:131]
s_add_u32 s64, s64, 16384
TR a[128:129],v232 offset:49152
s_add_u32 s81, s64, 896
MM v[0:3],a[196:199],v[176:179],v[0:3]
TR a[130:131],v232 offset:53248
v_max3_f32 v249, v144, v145, v148
MM v[0:3],a[200:203],v[192:195],v[0:3]
TR a[132:133],v232 offset:57344
v_max3_f32 v249, v249, v149, v152
MM v[0:3],a[204:207],v[208:211],v[0:3]
TR a[134:135],v232 offset:61440
v_max3_f32 v249, v249, v153, v156
MM v[4:7],a[220:223],v[208:211],v[128:131]
TR a[136:137],v233 offset:49152
v_max_f32 v249, v249, v157
MM v[4:7],a[216:219],v[192:195],v[4:7]
TR a[138:139],v233 offset:53248
v_cmp_lt_f32 vcc, s71, v249
MM v[4:7],a[212:215],v[176:179],v[4:7]
TR a[140:141],v233 offset:57344
TR a[142:143],v233 offset:61440
MM v[4:7],a[208:211],v[160:163],v[4:7]
s_cbranch_vccnz LSITE31_%=
LBACK31_%=:
MM v[12:15],a[208:211],v[164:167],v[132:135]
s_waitcnt vmcnt(8)
v_cmp_ne_u32 vcc, s69, v252
s_cbranch_vccnz LRFY_%=
LRFYb_%=:
MM v[12:15],a[212:215],v[180:183],v[12:15]
v_lshrrev_b32 v109, 2, v248
v_lshrrev_b32 v110, 3, v248
MM v[12:15],a[216:219],v[196:199],v[12:15]
s_and_b32 s76, s17, 7
EX v0,v0
MM v[12:15],a[220:223],v[212:215],v[12:15]
s_mul_i32 s76, s76, 2560
EX v1,v1
MM v[8:11],a[204:207],v[212:215],v[132:135]
s_lshl_b32 s91, s90, 8
EX v2,v2
MM v[8:11],a[200:203],v[196:199],v[8:11]
s_add_u32 s76, s76, s91
TR a[144:145],v232 offset:49664
CV v0,v0,v1
MM v[8:11],a[196:199],v[180:183],v[8:11]
s_add_u32 s76, s76, 21659648
buffer_load_dword v96, v109, s[20:23], s76 offen offset:0 sc1
MM v[8:11],a[192:195],v[164:167],v[8:11]
s_add_u32 s76, s76, 20480
EX v3,v3
MM v[16:19],a[192:195],v[168:171],v[136:139]
buffer_load_dword v97, v109, s[20:23], s76 offen offset:0 sc1
MM v[16:19],a[196:199],v[184:187],v[16:19]
s_add_u32 s76, s76, 20480
EX v4,v4
MM v[16:19],a[200:203],v[200:203],v[16:19]
buffer_load_dword v98, v109, s[20:23], s76 offen offset:0 sc1
MM v[16:19],a[204:207],v[216:219],v[16:19]
s_add_u32 s76, s76, 20480
EX v5,v5
MM v[20:23],a[220:223],v[216:219],v[136:139]
buffer_load_dword v99, v109, s[20:23], s76 offen offset:0 sc1
MM v[20:23],a[216:219],v[200:203],v[20:23]
s_add_u32 s76, s76, 20480
TR a[146:147],v232 offset:53760
TR a[148:149],v232 offset:57856
MM v[20:23],a[212:215],v[184:187],v[20:23]
buffer_load_dword v100, v109, s[20:23], s76 offen offset:0 sc1
MM v[20:23],a[208:211],v[168:171],v[20:23]
s_add_u32 s76, s76, 20480
EX v6,v6
MM v[28:31],a[208:211],v[172:175],v[140:143]
buffer_load_dword v101, v109, s[20:23], s76 offen offset:0 sc1
MM v[28:31],a[212:215],v[188:191],v[28:31]
s_add_u32 s76, s76, 20480
EX v7,v7
MM v[28:31],a[216:219],v[204:207],v[28:31]
buffer_load_dword v102, v109, s[20:23], s76 offen offset:0 sc1
MM v[28:31],a[220:223],v[220:223],v[28:31]
s_add_u32 s76, s76, 20480
s_nop 0
LPG22_%=:
s_cbranch_execz LTR4_%=
TR a[150:151],v232 offset:61952
EX v8,v8
MM v[24:27],a[204:207],v[220:223],v[140:143]
buffer_load_dword v103, v109, s[20:23], s76 offen offset:0 sc1
MM v[24:27],a[200:203],v[204:207],v[24:27]
s_add_u32 s76, s76, 20480
TR a[152:153],v233 offset:49664
EX v9,v9
CV v1,v2,v3
MM v[24:27],a[196:199],v[188:191],v[24:27]
buffer_load_dword v104, v109, s[20:23], s76 offen offset:0 sc1
TR a[154:155],v233 offset:53760
MM v[24:27],a[192:195],v[172:175],v[24:27]
s_and_b32 s76, s19, 65535
EX v10,v10
MM v[32:35],a[224:227],v[160:163],v[128:131]
s_sub_u32 s76, 65535, s76
TR a[156:157],v233 offset:57856
CV v2,v4,v5
EX v11,v11
MM v[32:35],a[228:231],v[176:179],v[32:35]
s_mul_i32 s76, s76, 1280
TR a[158:159],v233 offset:61952
CV v3,v6,v7
MM v[32:35],a[232:235],v[192:195],v[32:35]
s_lshl_b32 s91, s90, 7
EX v12,v12
MM v[32:35],a[236:239],v[208:211],v[32:35]
s_add_u32 s76, s76, s91
TR a[160:161],v232 offset:50176
TR a[162:163],v232 offset:54272
MM v[36:39],a[252:255],v[208:211],v[128:131]
s_add_u32 s76, s76, 131072
buffer_load_ushort v108, v110, s[20:23], s76 offen sc1
EX v13,v13
MM v[36:39],a[248:251],v[192:195],v[36:39]
s_ashr_i32 s91, s19, 31
EX v14,v14
MM v[36:39],a[244:247],v[176:179],v[36:39]
s_not_b32 s91, s91
TR a[164:165],v232 offset:58368
CV v8,v8,v9
MM v[36:39],a[240:243],v[160:163],v[36:39]
s_or_b32 s91, s91, 0x80000000
EX v15,v15
MM v[44:47],a[240:243],v[164:167],v[132:135]
s_xor_b32 s19, s19, s91
TR a[166:167],v232 offset:62464
CV v9,v10,v11
MM v[44:47],a[244:247],v[180:183],v[44:47]
s_and_b32 s19, s19, 0xffff0000
v_mov_b32 v111, 0xbfb8aa3b
v_mul_f32 v111, s19, v111
MM v[44:47],a[248:251],v[196:199],v[44:47]
EX v111,v111
EX v16,v16
MM v[44:47],a[252:255],v[212:215],v[44:47]
TR a[168:169],v233 offset:50176
v_add_f32 v111, 1.0, v111
EX v17,v17
MM v[40:43],a[236:239],v[212:215],v[132:135]
v_rcp_f32 v111, v111
MM v[40:43],a[232:235],v[196:199],v[40:43]
TR a[170:171],v233 offset:54272
CV v111,v111,v111
v_and_b32 v111, 0xffff0000, v111
EX v18,v18
MM v[40:43],a[228:231],v[180:183],v[40:43]
TR a[172:173],v233 offset:58368
CV v10,v12,v13
MM v[40:43],a[224:227],v[164:167],v[40:43]
EX v19,v19
MM v[48:51],a[224:227],v[168:171],v[136:139]
TR a[174:175],v233 offset:62464
CV v11,v14,v15
MM v[48:51],a[228:231],v[184:187],v[48:51]
EX v20,v20
MM v[48:51],a[232:235],v[200:203],v[48:51]
TR a[176:177],v232 offset:50688
TR a[178:179],v232 offset:54784
MM v[48:51],a[236:239],v[216:219],v[48:51]
EX v21,v21
MM v[52:55],a[252:255],v[216:219],v[136:139]
EX v22,v22
MM v[52:55],a[248:251],v[200:203],v[52:55]
TR a[180:181],v232 offset:58880
CV v16,v16,v17
MM v[52:55],a[244:247],v[184:187],v[52:55]
EX v23,v23
MM v[52:55],a[240:243],v[168:171],v[52:55]
TR a[182:183],v232 offset:62976
CV v17,v18,v19
MM v[60:63],a[240:243],v[172:175],v[140:143]
EX v24,v24
MM v[60:63],a[244:247],v[188:191],v[60:63]
TR a[184:185],v233 offset:50688
TR a[186:187],v233 offset:54784
MM v[60:63],a[248:251],v[204:207],v[60:63]
TR a[188:189],v233 offset:58880
TR a[190:191],v233 offset:62976
EX v25,v25
MM v[60:63],a[252:255],v[220:223],v[60:63]
EX v26,v26
MM v[56:59],a[236:239],v[220:223],v[140:143]
EX v27,v27
MM v[56:59],a[232:235],v[204:207],v[56:59]
EX v28,v28
MM v[56:59],a[228:231],v[188:191],v[56:59]
EX v29,v29
CV v18,v20,v21
MM v[56:59],a[224:227],v[172:175],v[56:59]
s_waitcnt lgkmcnt(0)
s_barrier
MM a[0:3],a[128:131],v[64:67],a[0:3]
s_add_u32 m0, s65, 49152
CV v19,v22,v23
CV v24,v24,v25
MM a[0:3],a[132:135],v[68:71],a[0:3]
CV v25,v26,v27
CV v26,v28,v29
MM a[8:11],a[132:135],v[76:79],a[8:11]
EX v30,v30
MM a[8:11],a[128:131],v[72:75],a[8:11]
EX v31,v31
MM a[16:19],a[128:131],v[80:83],a[16:19]
RD a[192:195],v224 offset:16384
CV v27,v30,v31
MM a[16:19],a[132:135],v[84:87],a[16:19]
EX v32,v32
MM a[24:27],a[132:135],v[92:95],a[24:27]
buffer_load_dwordx4 v228, s[8:11], s64 offen offset:0 lds
MM a[24:27],a[128:131],v[88:91],a[24:27]
EX v33,v33
MM a[28:31],a[136:139],v[88:91],a[28:31]
RD a[196:199],v225 offset:16384
CV v4,v32,v33
MM a[28:31],a[140:143],v[92:95],a[28:31]
EX v34,v34
MM a[20:23],a[140:143],v[84:87],a[20:23]
EX v35,v35
MM a[20:23],a[136:139],v[80:83],a[20:23]
RD a[200:203],v226 offset:16384
CV v5,v34,v35
MM a[12:15],a[136:139],v[72:75],a[12:15]
EX v36,v36
MM a[12:15],a[140:143],v[76:79],a[12:15]
buffer_load_dwordx4 v229, s[8:11], s64 offen offset:1024 lds
MM a[4:7],a[140:143],v[68:71],a[4:7]
EX v37,v37
MM a[4:7],a[136:139],v[64:67],a[4:7]
RD a[204:207],v227 offset:16384
CV v6,v36,v37
MM a[32:35],a[144:147],v[64:67],a[32:35]
EX v38,v38
MM a[32:35],a[148:151],v[68:71],a[32:35]
EX v39,v39
MM a[40:43],a[148:151],v[76:79],a[40:43]
RD a[208:211],v224 offset:20480
CV v7,v38,v39
MM a[40:43],a[144:147],v[72:75],a[40:43]
EX v40,v40
MM a[48:51],a[144:147],v[80:83],a[48:51]
buffer_load_dwordx4 v230, s[8:11], s64 offen offset:2048 lds
MM a[48:51],a[148:151],v[84:87],a[48:51]
EX v41,v41
MM a[56:59],a[148:151],v[92:95],a[56:59]
RD a[212:215],v225 offset:20480
CV v12,v40,v41
MM a[56:59],a[144:147],v[88:91],a[56:59]
EX v42,v42
MM a[60:63],a[152:155],v[88:91],a[60:63]
EX v43,v43
MM a[60:63],a[156:159],v[92:95],a[60:63]
RD a[216:219],v226 offset:20480
CV v13,v42,v43
MM a[52:55],a[156:159],v[84:87],a[52:55]
EX v44,v44
MM a[52:55],a[152:155],v[80:83],a[52:55]
buffer_load_dwordx4 v231, s[8:11], s64 offen offset:3072 lds
MM a[44:47],a[152:155],v[72:75],a[44:47]
s_add_u32 m0, s65, 114688
EX v45,v45
MM a[44:47],a[156:159],v[76:79],a[44:47]
RD a[220:223],v227 offset:20480
CV v14,v44,v45
MM a[36:39],a[156:159],v[68:71],a[36:39]
EX v46,v46
MM a[36:39],a[152:155],v[64:67],a[36:39]
RD a[224:227],v224 offset:24576
)ASM"
R"ASM(
RD a[228:231],v225 offset:24576
MM a[64:67],a[160:163],v[64:67],a[64:67]
EX v47,v47
MM a[64:67],a[164:167],v[68:71],a[64:67]
RD a[232:235],v226 offset:24576
CV v15,v46,v47
MM a[72:75],a[164:167],v[76:79],a[72:75]
EX v48,v48
MM a[72:75],a[160:163],v[72:75],a[72:75]
buffer_load_dwordx4 v235, s[12:15], s81 offen offset:0 lds
MM a[80:83],a[160:163],v[80:83],a[80:83]
EX v49,v49
MM a[80:83],a[164:167],v[84:87],a[80:83]
RD a[236:239],v227 offset:24576
CV v20,v48,v49
MM a[88:91],a[164:167],v[92:95],a[88:91]
EX v50,v50
MM a[88:91],a[160:163],v[88:91],a[88:91]
EX v51,v51
MM a[92:95],a[168:171],v[88:91],a[92:95]
RD a[240:243],v224 offset:28672
CV v21,v50,v51
MM a[92:95],a[172:175],v[92:95],a[92:95]
EX v52,v52
MM a[84:87],a[172:175],v[84:87],a[84:87]
buffer_load_dwordx4 v235, s[12:15], s64 offen offset:1024 lds
MM a[84:87],a[168:171],v[80:83],a[84:87]
EX v53,v53
MM a[76:79],a[168:171],v[72:75],a[76:79]
RD a[244:247],v225 offset:28672
CV v22,v52,v53
MM a[76:79],a[172:175],v[76:79],a[76:79]
EX v54,v54
MM a[68:71],a[172:175],v[68:71],a[68:71]
s_waitcnt vmcnt(6)
v_add_f32 v96, v96, v100
v_add_f32 v97, v97, v101
MM a[68:71],a[168:171],v[64:67],a[68:71]
v_add_f32 v98, v98, v102
v_add_f32 v99, v99, v103
MM a[96:99],a[176:179],v[64:67],a[96:99]
v_add_f32 v96, v96, v98
v_add_f32 v97, v97, v99
MM a[96:99],a[180:183],v[68:71],a[96:99]
v_add_f32 v96, v96, v104
v_lshlrev_b32 v108, 16, v108
MM a[104:107],a[180:183],v[76:79],a[104:107]
v_add_f32 v96, v96, v97
CV v96,v96,v96
EX v55,v55
MM a[104:107],a[176:179],v[72:75],a[104:107]
v_and_b32 v96, 0xffff0000, v96
v_mul_f32 v96, v96, v111
MM a[112:115],a[176:179],v[80:83],a[112:115]
CV v96,v96,v96
v_and_b32 v96, 0xffff0000, v96
EX v56,v56
MM a[112:115],a[180:183],v[84:87],a[112:115]
v_add_f32 v96, v96, v108
CV v96,v96,v96
MM a[120:123],a[180:183],v[92:95],a[120:123]
s_mov_b64 exec, s[44:45]
buffer_store_short v96, v110, s[20:23], s76 offen offset:0 sc1
s_mov_b64 exec, -1
s_nop 0
EX v57,v57
MM a[120:123],a[176:179],v[88:91],a[120:123]
EX v58,v58
MM a[124:127],a[184:187],v[88:91],a[124:127]
v_add_f32 v244, v144, v145
v_add_f32 v245, v148, v149
MM a[124:127],a[188:191],v[92:95],a[124:127]
v_add_f32 v246, v152, v153
v_add_f32 v247, v156, v157
MM a[116:119],a[188:191],v[84:87],a[116:119]
EX v59,v59
MM a[116:119],a[184:187],v[80:83],a[116:119]
EX v60,v60
MM a[108:111],a[184:187],v[72:75],a[108:111]
CV v23,v54,v55
CV v28,v56,v57
MM a[108:111],a[188:191],v[76:79],a[108:111]
EX v61,v61
MM a[100:103],a[188:191],v[68:71],a[100:103]
buffer_load_dwordx4 v235, s[12:15], s81 offen offset:2048 lds
MM a[100:103],a[184:187],v[64:67],a[100:103]
EX v62,v62
v_smfmac_f32_16x16x64_bf16 v[144:147], v[236:239], v[0:7], v234
RD a[248:251],v226 offset:28672
RD a[252:255],v227 offset:28672
EX v63,v63
v_smfmac_f32_16x16x64_bf16 v[148:151], v[236:239], v[8:15], v234
CV v29,v58,v59
v_smfmac_f32_16x16x64_bf16 v[152:155], v[236:239], v[16:23], v234
CV v30,v60,v61
CV v31,v62,v63
s_nop 1
v_smfmac_f32_16x16x64_bf16 v[156:159], v[236:239], v[24:31], v234
s_waitcnt lgkmcnt(0)
buffer_load_dwordx4 v235, s[12:15], s64 offen offset:3072 lds
MM v[64:67],a[192:195],v[160:163],v[128:131]
s_sub_u32 s64, s64, 507904
TR a[128:129],v232 offset:0
s_add_u32 s81, s64, 896
MM v[64:67],a[196:199],v[176:179],v[64:67]
TR a[130:131],v232 offset:4096
v_max3_f32 v249, v144, v145, v148
MM v[64:67],a[200:203],v[192:195],v[64:67]
TR a[132:133],v232 offset:8192
v_max3_f32 v249, v249, v149, v152
MM v[64:67],a[204:207],v[208:211],v[64:67]
TR a[134:135],v232 offset:12288
v_max3_f32 v249, v249, v153, v156
MM v[68:71],a[220:223],v[208:211],v[128:131]
TR a[136:137],v233 offset:0
v_max_f32 v249, v249, v157
MM v[68:71],a[216:219],v[192:195],v[68:71]
TR a[138:139],v233 offset:4096
v_cmp_lt_f32 vcc, s71, v249
MM v[68:71],a[212:215],v[176:179],v[68:71]
TR a[140:141],v233 offset:8192
TR a[142:143],v233 offset:12288
MM v[68:71],a[208:211],v[160:163],v[68:71]
s_cbranch_vccnz LSITE32_%=
LBACK32_%=:
MM v[76:79],a[208:211],v[164:167],v[132:135]
EX v64,v64
MM v[76:79],a[212:215],v[180:183],v[76:79]
EX v65,v65
MM v[76:79],a[216:219],v[196:199],v[76:79]
TR a[144:145],v232 offset:512
CV v64,v64,v65
MM v[76:79],a[220:223],v[212:215],v[76:79]
EX v66,v66
MM v[72:75],a[204:207],v[212:215],v[132:135]
EX v67,v67
MM v[72:75],a[200:203],v[196:199],v[72:75]
TR a[146:147],v232 offset:4608
CV v65,v66,v67
MM v[72:75],a[196:199],v[180:183],v[72:75]
EX v68,v68
MM v[72:75],a[192:195],v[164:167],v[72:75]
TR a[148:149],v232 offset:8704
TR a[150:151],v232 offset:12800
MM v[80:83],a[192:195],v[168:171],v[136:139]
EX v69,v69
MM v[80:83],a[196:199],v[184:187],v[80:83]
TR a[152:153],v233 offset:512
CV v66,v68,v69
MM v[80:83],a[200:203],v[200:203],v[80:83]
EX v70,v70
MM v[80:83],a[204:207],v[216:219],v[80:83]
TR a[154:155],v233 offset:4608
TR a[156:157],v233 offset:8704
MM v[84:87],a[220:223],v[216:219],v[136:139]
EX v71,v71
MM v[84:87],a[216:219],v[200:203],v[84:87]
TR a[158:159],v233 offset:12800
CV v67,v70,v71
MM v[84:87],a[212:215],v[184:187],v[84:87]
EX v72,v72
MM v[84:87],a[208:211],v[168:171],v[84:87]
EX v73,v73
MM v[92:95],a[208:211],v[172:175],v[140:143]
TR a[160:161],v232 offset:1024
CV v72,v72,v73
MM v[92:95],a[212:215],v[188:191],v[92:95]
EX v74,v74
MM v[92:95],a[216:219],v[204:207],v[92:95]
TR a[162:163],v232 offset:5120
TR a[164:165],v232 offset:9216
MM v[92:95],a[220:223],v[220:223],v[92:95]
EX v75,v75
MM v[88:91],a[204:207],v[220:223],v[140:143]
TR a[166:167],v232 offset:13312
CV v73,v74,v75
MM v[88:91],a[200:203],v[204:207],v[88:91]
EX v76,v76
MM v[88:91],a[196:199],v[188:191],v[88:91]
EX v77,v77
MM v[88:91],a[192:195],v[172:175],v[88:91]
TR a[168:169],v233 offset:1024
CV v74,v76,v77
MM v[96:99],a[224:227],v[160:163],v[128:131]
EX v78,v78
MM v[96:99],a[228:231],v[176:179],v[96:99]
TR a[170:171],v233 offset:5120
TR a[172:173],v233 offset:9216
MM v[96:99],a[232:235],v[192:195],v[96:99]
EX v79,v79
MM v[96:99],a[236:239],v[208:211],v[96:99]
TR a[174:175],v233 offset:13312
CV v75,v78,v79
MM v[100:103],a[252:255],v[208:211],v[128:131]
EX v80,v80
MM v[100:103],a[248:251],v[192:195],v[100:103]
EX v81,v81
MM v[100:103],a[244:247],v[176:179],v[100:103]
TR a[176:177],v232 offset:1536
CV v80,v80,v81
MM v[100:103],a[240:243],v[160:163],v[100:103]
EX v82,v82
MM v[108:111],a[240:243],v[164:167],v[132:135]
TR a[178:179],v232 offset:5632
TR a[180:181],v232 offset:9728
MM v[108:111],a[244:247],v[180:183],v[108:111]
EX v83,v83
MM v[108:111],a[248:251],v[196:199],v[108:111]
TR a[182:183],v232 offset:13824
CV v81,v82,v83
MM v[108:111],a[252:255],v[212:215],v[108:111]
EX v84,v84
MM v[104:107],a[236:239],v[212:215],v[132:135]
TR a[184:185],v233 offset:1536
TR a[186:187],v233 offset:5632
MM v[104:107],a[232:235],v[196:199],v[104:107]
EX v85,v85
MM v[104:107],a[228:231],v[180:183],v[104:107]
TR a[188:189],v233 offset:9728
CV v82,v84,v85
MM v[104:107],a[224:227],v[164:167],v[104:107]
EX v86,v86
MM v[112:115],a[224:227],v[168:171],v[136:139]
EX v87,v87
MM v[112:115],a[228:231],v[184:187],v[112:115]
TR a[190:191],v233 offset:13824
CV v83,v86,v87
MM v[112:115],a[232:235],v[200:203],v[112:115]
EX v88,v88
MM v[112:115],a[236:239],v[216:219],v[112:115]
EX v89,v89
MM v[116:119],a[252:255],v[216:219],v[136:139]
EX v90,v90
MM v[116:119],a[248:251],v[200:203],v[116:119]
EX v91,v91
MM v[116:119],a[244:247],v[184:187],v[116:119]
EX v92,v92
MM v[116:119],a[240:243],v[168:171],v[116:119]
EX v93,v93
MM v[124:127],a[240:243],v[172:175],v[140:143]
EX v94,v94
MM v[124:127],a[244:247],v[188:191],v[124:127]
EX v95,v95
MM v[124:127],a[248:251],v[204:207],v[124:127]
EX v96,v96
MM v[124:127],a[252:255],v[220:223],v[124:127]
EX v97,v97
MM v[120:123],a[236:239],v[220:223],v[140:143]
EX v98,v98
MM v[120:123],a[232:235],v[204:207],v[120:123]
EX v99,v99
MM v[120:123],a[228:231],v[188:191],v[120:123]
EX v100,v100
MM v[120:123],a[224:227],v[172:175],v[120:123]
s_waitcnt lgkmcnt(0)
s_barrier
MM a[0:3],a[128:131],v[0:3],a[0:3]
s_add_u32 m0, s65, 0
CV v88,v88,v89
CV v89,v90,v91
MM a[0:3],a[132:135],v[4:7],a[0:3]
RD a[192:195],v224 offset:32768
CV v90,v92,v93
MM a[8:11],a[132:135],v[12:15],a[8:11]
RD a[196:199],v225 offset:32768
CV v91,v94,v95
MM a[8:11],a[128:131],v[8:11],a[8:11]
buffer_load_dwordx4 v228, s[8:11], s64 offen offset:0 lds
MM a[16:19],a[128:131],v[16:19],a[16:19]
RD a[200:203],v226 offset:32768
CV v68,v96,v97
MM a[16:19],a[132:135],v[20:23],a[16:19]
RD a[204:207],v227 offset:32768
CV v69,v98,v99
MM a[24:27],a[132:135],v[28:31],a[24:27]
buffer_load_dwordx4 v229, s[8:11], s64 offen offset:1024 lds
MM a[24:27],a[128:131],v[24:27],a[24:27]
RD a[208:211],v224 offset:36864
RD a[212:215],v225 offset:36864
MM a[28:31],a[136:139],v[24:27],a[28:31]
buffer_load_dwordx4 v230, s[8:11], s64 offen offset:2048 lds
MM a[28:31],a[140:143],v[28:31],a[28:31]
RD a[216:219],v226 offset:36864
RD a[220:223],v227 offset:36864
MM a[20:23],a[140:143],v[20:23],a[20:23]
EX v101,v101
MM a[20:23],a[136:139],v[16:19],a[20:23]
RD a[224:227],v224 offset:40960
CV v70,v100,v101
MM a[12:15],a[136:139],v[8:11],a[12:15]
EX v102,v102
MM a[12:15],a[140:143],v[12:15],a[12:15]
buffer_load_dwordx4 v231, s[8:11], s64 offen offset:3072 lds
MM a[4:7],a[140:143],v[4:7],a[4:7]
s_add_u32 m0, s65, 65536
EX v103,v103
MM a[4:7],a[136:139],v[0:3],a[4:7]
RD a[228:231],v225 offset:40960
CV v71,v102,v103
MM a[32:35],a[144:147],v[0:3],a[32:35]
EX v104,v104
MM a[32:35],a[148:151],v[4:7],a[32:35]
EX v105,v105
MM a[40:43],a[148:151],v[12:15],a[40:43]
RD a[232:235],v226 offset:40960
CV v76,v104,v105
MM a[40:43],a[144:147],v[8:11],a[40:43]
EX v106,v106
MM a[48:51],a[144:147],v[16:19],a[48:51]
buffer_load_dwordx4 v235, s[12:15], s81 offen offset:0 lds
MM a[48:51],a[148:151],v[20:23],a[48:51]
EX v107,v107
MM a[56:59],a[148:151],v[28:31],a[56:59]
RD a[236:239],v227 offset:40960
CV v77,v106,v107
MM a[56:59],a[144:147],v[24:27],a[56:59]
EX v108,v108
MM a[60:63],a[152:155],v[24:27],a[60:63]
EX v109,v109
MM a[60:63],a[156:159],v[28:31],a[60:63]
RD a[240:243],v224 offset:45056
CV v78,v108,v109
MM a[52:55],a[156:159],v[20:23],a[52:55]
EX v110,v110
MM a[52:55],a[152:155],v[16:19],a[52:55]
buffer_load_dwordx4 v235, s[12:15], s64 offen offset:1024 lds
MM a[44:47],a[152:155],v[8:11],a[44:47]
EX v111,v111
MM a[44:47],a[156:159],v[12:15],a[44:47]
RD a[244:247],v225 offset:45056
CV v79,v110,v111
MM a[36:39],a[156:159],v[4:7],a[36:39]
EX v112,v112
MM a[36:39],a[152:155],v[0:3],a[36:39]
EX v113,v113
MM a[64:67],a[160:163],v[0:3],a[64:67]
RD a[248:251],v226 offset:45056
CV v84,v112,v113
MM a[64:67],a[164:167],v[4:7],a[64:67]
EX v114,v114
MM a[72:75],a[164:167],v[12:15],a[72:75]
buffer_load_dwordx4 v235, s[12:15], s81 offen offset:2048 lds
MM a[72:75],a[160:163],v[8:11],a[72:75]
EX v115,v115
MM a[80:83],a[160:163],v[16:19],a[80:83]
RD a[252:255],v227 offset:45056
CV v85,v114,v115
MM a[80:83],a[164:167],v[20:23],a[80:83]
EX v116,v116
MM a[88:91],a[164:167],v[28:31],a[88:91]
EX v117,v117
MM a[88:91],a[160:163],v[24:27],a[88:91]
buffer_load_dwordx4 v235, s[12:15], s64 offen offset:3072 lds
MM a[92:95],a[168:171],v[24:27],a[92:95]
EX v118,v118
MM a[92:95],a[172:175],v[28:31],a[92:95]
EX v119,v119
MM a[84:87],a[172:175],v[20:23],a[84:87]
EX v120,v120
MM a[84:87],a[168:171],v[16:19],a[84:87]
EX v121,v121
MM a[76:79],a[168:171],v[8:11],a[76:79]
EX v122,v122
MM a[76:79],a[172:175],v[12:15],a[76:79]
EX v123,v123
MM a[68:71],a[172:175],v[4:7],a[68:71]
EX v124,v124
MM a[68:71],a[168:171],v[0:3],a[68:71]
EX v125,v125
MM a[96:99],a[176:179],v[0:3],a[96:99]
EX v126,v126
MM a[96:99],a[180:183],v[4:7],a[96:99]
EX v127,v127
MM a[104:107],a[180:183],v[12:15],a[104:107]
CV v86,v116,v117
CV v87,v118,v119
MM a[104:107],a[176:179],v[8:11],a[104:107]
CV v92,v120,v121
CV v93,v122,v123
MM a[112:115],a[176:179],v[16:19],a[112:115]
CV v94,v124,v125
CV v95,v126,v127
MM a[112:115],a[180:183],v[20:23],a[112:115]
MM a[120:123],a[180:183],v[28:31],a[120:123]
s_lshl_b32 s91, s17, 2
s_add_u32 s91, s91, 94464
s_and_b64 s[44:45], s[44:45], 1
MM a[120:123],a[176:179],v[24:27],a[120:123]
s_waitcnt vmcnt(10)
v_mov_b32 v250, s69
s_mov_b64 exec, s[44:45]
buffer_store_dword v250, off, s[20:23], s91 sc1
s_mov_b64 exec, -1
)ASM"
R"ASM(
MM a[124:127],a[184:187],v[24:27],a[124:127]
v_add_f32 v244, v144, v145
v_add_f32 v245, v148, v149
MM a[124:127],a[188:191],v[28:31],a[124:127]
v_add_f32 v246, v152, v153
v_add_f32 v247, v156, v157
MM a[116:119],a[188:191],v[20:23],a[116:119]
MM a[116:119],a[184:187],v[16:19],a[116:119]
MM a[108:111],a[184:187],v[8:11],a[108:111]
MM a[108:111],a[188:191],v[12:15],a[108:111]
MM a[100:103],a[188:191],v[4:7],a[100:103]
MM a[100:103],a[184:187],v[0:3],a[100:103]
v_smfmac_f32_16x16x64_bf16 v[144:147], v[236:239], v[64:71], v234
v_smfmac_f32_16x16x64_bf16 v[148:151], v[236:239], v[72:79], v234
s_nop 0
LPG23_%=:
s_cbranch_execz LPG27_%=
v_smfmac_f32_16x16x64_bf16 v[152:155], v[236:239], v[80:87], v234
v_smfmac_f32_16x16x64_bf16 v[156:159], v[236:239], v[88:95], v234
s_waitcnt lgkmcnt(0)
LLAJ_%=:
MM v[0:3],a[192:195],v[160:163],v[128:131]
s_add_u32 s64, s64, 16384
TR a[128:129],v232 offset:16384
s_add_u32 s81, s64, 896
MM v[0:3],a[196:199],v[176:179],v[0:3]
TR a[130:131],v232 offset:20480
v_max3_f32 v249, v144, v145, v148
MM v[0:3],a[200:203],v[192:195],v[0:3]
TR a[132:133],v232 offset:24576
v_max3_f32 v249, v249, v149, v152
MM v[0:3],a[204:207],v[208:211],v[0:3]
TR a[134:135],v232 offset:28672
v_max3_f32 v249, v249, v153, v156
MM v[4:7],a[220:223],v[208:211],v[128:131]
TR a[136:137],v233 offset:16384
v_max_f32 v249, v249, v157
MM v[4:7],a[216:219],v[192:195],v[4:7]
TR a[138:139],v233 offset:20480
v_cmp_lt_f32 vcc, s71, v249
MM v[4:7],a[212:215],v[176:179],v[4:7]
TR a[140:141],v233 offset:24576
TR a[142:143],v233 offset:28672
MM v[4:7],a[208:211],v[160:163],v[4:7]
s_cbranch_vccnz LSITE33_%=
LBACK33_%=:
MM v[12:15],a[208:211],v[164:167],v[132:135]
v_bfe_u32 v242, v248, 6, 4
v_mul_u32_u24 v242, 1280, v242
MM v[12:15],a[212:215],v[180:183],v[12:15]
v_and_b32 v252, 48, v248
v_bfe_u32 v243, v248, 8, 1
MM v[12:15],a[216:219],v[196:199],v[12:15]
v_lshlrev_b32 v243, 5, v243
v_xor_b32 v252, v252, v243
MM v[12:15],a[220:223],v[212:215],v[12:15]
v_add_u32 v252, v252, v242
v_lshrrev_b32 v242, 3, v248
MM v[8:11],a[204:207],v[212:215],v[132:135]
v_xor_b32 v243, v242, v248
v_and_b32 v243, 96, v243
MM v[8:11],a[200:203],v[196:199],v[8:11]
TR a[144:145],v232 offset:16896
v_and_or_b32 v243, v248, 16, v243
MM v[8:11],a[196:199],v[180:183],v[8:11]
s_and_b32 s66, s49, 8
EX v0,v0
MM v[8:11],a[192:195],v[164:167],v[8:11]
s_lshl_b32 s66, s66, 3
v_lshrrev_b32 v242, 7, v248
v_xor_b32 v243, s66, v243
EX v1,v1
MM v[16:19],a[192:195],v[168:171],v[136:139]
v_mul_u32_u24 v242, 1280, v242
v_add_u32 v240, v242, v243
EX v2,v2
MM v[16:19],a[196:199],v[184:187],v[16:19]
s_mul_i32 s88, %[swave], 40960
EX v3,v3
MM v[16:19],a[200:203],v[200:203],v[16:19]
s_and_b32 s66, s84, 1
EX v4,v4
MM v[16:19],a[204:207],v[216:219],v[16:19]
s_lshl_b32 s86, s66, 6
TR a[146:147],v232 offset:20992
CV v0,v0,v1
MM v[20:23],a[220:223],v[216:219],v[136:139]
s_add_u32 s86, s86, s88
EX v5,v5
MM v[20:23],a[216:219],v[200:203],v[20:23]
s_xor_b32 s66, s66, 1
TR a[148:149],v232 offset:25088
CV v1,v2,v3
MM v[20:23],a[212:215],v[184:187],v[20:23]
s_lshl_b32 s66, s66, 8
EX v6,v6
MM v[20:23],a[208:211],v[168:171],v[20:23]
s_add_u32 s88, s88, s66
EX v7,v7
MM v[28:31],a[208:211],v[172:175],v[140:143]
s_and_b32 s66, s49, 16
TR a[150:151],v232 offset:29184
TR a[152:153],v233 offset:16896
MM v[28:31],a[212:215],v[188:191],v[28:31]
s_lshl_b32 s66, s66, 3
EX v8,v8
MM v[28:31],a[216:219],v[204:207],v[28:31]
s_sub_u32 s87, s86, s66
TR a[154:155],v233 offset:20992
CV v2,v4,v5
MM v[28:31],a[220:223],v[220:223],v[28:31]
s_add_u32 s87, s87, 128
EX v9,v9
MM v[24:27],a[204:207],v[220:223],v[140:143]
s_add_u32 s86, s86, s66
TR a[156:157],v233 offset:25088
CV v3,v6,v7
MM v[24:27],a[200:203],v[204:207],v[24:27]
s_and_b32 s94, s49, 7
EX v10,v10
MM v[24:27],a[196:199],v[188:191],v[24:27]
s_lshl_b32 s89, %[swave], 11
TR a[158:159],v233 offset:29184
CV v8,v8,v9
MM v[24:27],a[192:195],v[172:175],v[24:27]
s_add_u32 s89, s89, %[slds]
EX v11,v11
MM v[32:35],a[224:227],v[160:163],v[128:131]
s_xor_b32 s66, s94, 4
TR a[160:161],v232 offset:17408
TR a[162:163],v232 offset:21504
MM v[32:35],a[228:231],v[176:179],v[32:35]
s_mul_i32 s66, s66, 163840
EX v12,v12
MM v[32:35],a[232:235],v[192:195],v[32:35]
s_add_u32 s66, s66, s88
s_add_u32 m0, s89, 131072
s_nop 0
buffer_load_dwordx4 v252, s[24:27], s66 offen offset:0 lds
MM v[32:35],a[236:239],v[208:211],v[32:35]
s_add_u32 s66, s66, 19456
EX v13,v13
MM v[36:39],a[252:255],v[208:211],v[128:131]
TR a[164:165],v232 offset:25600
CV v9,v10,v11
MM v[36:39],a[248:251],v[192:195],v[36:39]
EX v14,v14
MM v[36:39],a[244:247],v[176:179],v[36:39]
EX v15,v15
MM v[36:39],a[240:243],v[160:163],v[36:39]
TR a[166:167],v232 offset:29696
TR a[168:169],v233 offset:17408
MM v[44:47],a[240:243],v[164:167],v[132:135]
EX v16,v16
MM v[44:47],a[244:247],v[180:183],v[44:47]
TR a[170:171],v233 offset:21504
CV v10,v12,v13
MM v[44:47],a[248:251],v[196:199],v[44:47]
EX v17,v17
MM v[44:47],a[252:255],v[212:215],v[44:47]
s_add_u32 m0, s89, 131072
s_nop 0
buffer_load_dwordx4 v252, s[24:27], s66 offen offset:1024 lds
MM v[40:43],a[236:239],v[212:215],v[132:135]
EX v18,v18
MM v[40:43],a[232:235],v[196:199],v[40:43]
TR a[172:173],v233 offset:25600
CV v11,v14,v15
MM v[40:43],a[228:231],v[180:183],v[40:43]
EX v19,v19
MM v[40:43],a[224:227],v[164:167],v[40:43]
TR a[174:175],v233 offset:29696
TR a[176:177],v232 offset:17920
MM v[48:51],a[224:227],v[168:171],v[136:139]
EX v20,v20
MM v[48:51],a[228:231],v[184:187],v[48:51]
TR a[178:179],v232 offset:22016
CV v16,v16,v17
MM v[48:51],a[232:235],v[200:203],v[48:51]
EX v21,v21
MM v[48:51],a[236:239],v[216:219],v[48:51]
s_lshl_b32 s60, s50, 2
TR a[180:181],v232 offset:26112
CV v17,v18,v19
MM v[52:55],a[252:255],v[216:219],v[136:139]
s_add_u32 s60, s60, %[swave]
EX v22,v22
MM v[52:55],a[248:251],v[200:203],v[52:55]
s_lshl_b32 s60, s60, 1
TR a[182:183],v232 offset:30208
TR a[184:185],v233 offset:17920
MM v[52:55],a[244:247],v[184:187],v[52:55]
s_bfe_u32 s61, s49, 65538
EX v23,v23
MM v[52:55],a[240:243],v[168:171],v[52:55]
s_xor_b32 s61, s61, 1
EX v24,v24
MM v[60:63],a[240:243],v[172:175],v[140:143]
s_add_u32 s60, s60, s61
TR a[186:187],v233 offset:22016
CV v18,v20,v21
MM v[60:63],a[244:247],v[188:191],v[60:63]
s_mul_i32 s18, s60, 40960
EX v25,v25
MM v[60:63],a[248:251],v[204:207],v[60:63]
s_mul_i32 s60, s84, 320
TR a[188:189],v233 offset:26112
TR a[190:191],v233 offset:30208
MM v[60:63],a[252:255],v[220:223],v[60:63]
s_add_u32 s18, s18, s60
EX v26,v26
MM v[56:59],a[236:239],v[220:223],v[140:143]
s_lshr_b32 s62, s49, 3
CV v19,v22,v23
CV v24,v24,v25
MM v[56:59],a[232:235],v[204:207],v[56:59]
s_mul_i32 s63, s62, 10240
EX v27,v27
MM v[56:59],a[228:231],v[188:191],v[56:59]
s_add_u32 s63, s63, s18
EX v28,v28
MM v[56:59],a[224:227],v[172:175],v[56:59]
s_waitcnt vmcnt(14) lgkmcnt(0)
s_barrier
MM a[0:3],a[128:131],v[64:67],a[0:3]
s_and_b32 s60, s84, 1
EX v29,v29
MM a[0:3],a[132:135],v[68:71],a[0:3]
s_add_u32 s61, s60, s62
CV v25,v26,v27
CV v26,v28,v29
MM a[8:11],a[132:135],v[76:79],a[8:11]
s_lshl_b32 s61, s61, 6
EX v30,v30
MM a[8:11],a[128:131],v[72:75],a[8:11]
s_add_u32 s18, s18, s61
EX v31,v31
MM a[16:19],a[128:131],v[80:83],a[16:19]
s_xor_b32 s60, s60, 1
RD a[192:195],v224 offset:49152
CV v27,v30,v31
MM a[16:19],a[132:135],v[84:87],a[16:19]
s_lshl_b32 s60, s60, 8
EX v32,v32
MM a[24:27],a[132:135],v[92:95],a[24:27]
s_add_u32 s63, s63, s60
s_add_u32 m0, s65, 16384
RD a[196:199],v225 offset:49152
MM a[24:27],a[128:131],v[88:91],a[24:27]
s_mul_i32 s60, s62, 33792
EX v33,v33
MM a[28:31],a[136:139],v[88:91],a[28:31]
s_sub_u32 s60, s95, s60
buffer_load_dwordx4 v228, s[8:11], s64 offen offset:0 lds
MM a[28:31],a[140:143],v[92:95],a[28:31]
s_xor_b32 s61, s62, 1
EX v34,v34
MM a[20:23],a[140:143],v[84:87],a[20:23]
s_mul_i32 s61, s61, 33792
EX v35,v35
MM a[20:23],a[136:139],v[80:83],a[20:23]
s_add_u32 s61, s61, s60
RD a[200:203],v226 offset:49152
CV v4,v32,v33
MM a[12:15],a[136:139],v[72:75],a[12:15]
s_add_u32 s36, s61, 2048
EX v36,v36
MM a[12:15],a[140:143],v[76:79],a[12:15]
s_xor_b32 s61, s62, 2
CV v5,v34,v35
MM a[4:7],a[140:143],v[68:71],a[4:7]
s_mul_i32 s61, s61, 33792
EX v37,v37
MM a[4:7],a[136:139],v[64:67],a[4:7]
s_add_u32 s61, s61, s60
buffer_load_dwordx4 v229, s[8:11], s64 offen offset:1024 lds
MM a[32:35],a[144:147],v[64:67],a[32:35]
s_add_u32 s37, s61, 4096
EX v38,v38
MM a[32:35],a[148:151],v[68:71],a[32:35]
s_xor_b32 s61, s62, 3
RD a[204:207],v227 offset:49152
CV v6,v36,v37
MM a[40:43],a[148:151],v[76:79],a[40:43]
s_mul_i32 s61, s61, 33792
EX v39,v39
MM a[40:43],a[144:147],v[72:75],a[40:43]
s_add_u32 s61, s61, s60
RD a[208:211],v224 offset:53248
CV v7,v38,v39
MM a[48:51],a[144:147],v[80:83],a[48:51]
s_add_u32 s38, s61, 6144
EX v40,v40
MM a[48:51],a[148:151],v[84:87],a[48:51]
s_lshl_b32 s61, s62, 9
EX v41,v41
MM a[56:59],a[148:151],v[92:95],a[56:59]
s_add_u32 s39, s60, s61
RD a[212:215],v225 offset:53248
MM a[56:59],a[144:147],v[88:91],a[56:59]
s_mov_b32 s17, 15
buffer_load_dwordx4 v230, s[8:11], s64 offen offset:2048 lds
MM a[60:63],a[152:155],v[88:91],a[60:63]
s_mov_b32 s58, 0
EX v42,v42
MM a[60:63],a[156:159],v[92:95],a[60:63]
s_lshl_b32 s60, s50, 1
EX v43,v43
MM a[52:55],a[156:159],v[84:87],a[52:55]
s_bfe_u32 s61, s49, 65538
RD a[216:219],v226 offset:53248
CV v12,v40,v41
MM a[52:55],a[152:155],v[80:83],a[52:55]
s_add_u32 s60, s60, s61
EX v44,v44
MM a[44:47],a[152:155],v[72:75],a[44:47]
s_lshl_b32 s60, s60, 2
CV v13,v42,v43
MM a[44:47],a[156:159],v[76:79],a[44:47]
s_add_u32 s60, s60, s84
EX v45,v45
MM a[36:39],a[156:159],v[68:71],a[36:39]
s_lshl_b32 s60, s60, 2
buffer_load_dwordx4 v231, s[8:11], s64 offen offset:3072 lds
MM a[36:39],a[152:155],v[64:67],a[36:39]
s_add_u32 s68, s60, %[swave]
EX v46,v46
MM a[64:67],a[160:163],v[64:67],a[64:67]
s_lshl_b32 s68, s68, 6
RD a[220:223],v227 offset:53248
CV v14,v44,v45
MM a[64:67],a[164:167],v[68:71],a[64:67]
s_add_u32 s68, s68, 16384
EX v47,v47
MM a[72:75],a[164:167],v[76:79],a[72:75]
s_lshr_b32 s61, s49, 3
EX v48,v48
MM a[72:75],a[160:163],v[72:75],a[72:75]
s_lshl_b32 s19, s61, 4
RD a[224:227],v224 offset:57344
CV v15,v46,v47
MM a[80:83],a[160:163],v[80:83],a[80:83]
s_add_u32 s19, s19, s68
EX v49,v49
MM a[80:83],a[164:167],v[84:87],a[80:83]
s_lshr_b32 s96, s49, 2
s_add_u32 m0, s65, 81920
CV v20,v48,v49
MM a[88:91],a[164:167],v[92:95],a[88:91]
s_lshl_b32 s97, s50, 3
EX v50,v50
MM a[88:91],a[160:163],v[88:91],a[88:91]
s_add_u32 s96, s96, s97
buffer_load_dwordx4 v235, s[12:15], s81 offen offset:0 lds
MM a[92:95],a[168:171],v[88:91],a[92:95]
s_lshl_b32 s96, s96, 2
EX v51,v51
MM a[92:95],a[172:175],v[92:95],a[92:95]
s_add_u32 s96, s96, %[swave]
RD a[228:231],v225 offset:57344
CV v21,v50,v51
MM a[84:87],a[172:175],v[84:87],a[84:87]
s_lshl_b32 s96, s96, 6
EX v52,v52
MM a[84:87],a[168:171],v[80:83],a[84:87]
s_add_u32 s96, s96, 0
RD a[232:235],v226 offset:57344
MM a[76:79],a[168:171],v[72:75],a[76:79]
s_xor_b32 s44, s96, 256
EX v53,v53
MM a[76:79],a[172:175],v[76:79],a[76:79]
s_xor_b32 s66, s94, 5
RD a[236:239],v227 offset:57344
CV v22,v52,v53
MM a[68:71],a[172:175],v[68:71],a[68:71]
s_mul_i32 s66, s66, 163840
EX v54,v54
MM a[68:71],a[168:171],v[64:67],a[68:71]
s_add_u32 s66, s66, s88
s_add_u32 m0, s89, 139264
s_nop 0
buffer_load_dwordx4 v252, s[24:27], s66 offen offset:0 lds
MM a[96:99],a[176:179],v[64:67],a[96:99]
s_add_u32 s66, s66, 19456
EX v55,v55
MM a[96:99],a[180:183],v[68:71],a[96:99]
s_add_u32 m0, s65, 81920
s_nop 0
buffer_load_dwordx4 v235, s[12:15], s64 offen offset:1024 lds
MM a[104:107],a[180:183],v[76:79],a[104:107]
EX v56,v56
MM a[104:107],a[176:179],v[72:75],a[104:107]
RD a[240:243],v224 offset:61440
CV v23,v54,v55
MM a[112:115],a[176:179],v[80:83],a[112:115]
EX v57,v57
MM a[112:115],a[180:183],v[84:87],a[112:115]
EX v58,v58
MM a[120:123],a[180:183],v[92:95],a[120:123]
RD a[244:247],v225 offset:61440
CV v28,v56,v57
MM a[120:123],a[176:179],v[88:91],a[120:123]
EX v59,v59
MM a[124:127],a[184:187],v[88:91],a[124:127]
v_add_f32 v244, v144, v145
v_add_f32 v245, v148, v149
MM a[124:127],a[188:191],v[92:95],a[124:127]
v_add_f32 v246, v152, v153
v_add_f32 v247, v156, v157
MM a[116:119],a[188:191],v[84:87],a[116:119]
)ASM"
R"ASM(
s_add_u32 m0, s89, 139264
s_nop 0
buffer_load_dwordx4 v252, s[24:27], s66 offen offset:1024 lds
MM a[116:119],a[184:187],v[80:83],a[116:119]
EX v60,v60
MM a[108:111],a[184:187],v[72:75],a[108:111]
EX v61,v61
MM a[108:111],a[188:191],v[76:79],a[108:111]
s_add_u32 m0, s65, 81920
s_nop 0
buffer_load_dwordx4 v235, s[12:15], s81 offen offset:2048 lds
MM a[100:103],a[188:191],v[68:71],a[100:103]
EX v62,v62
MM a[100:103],a[184:187],v[64:67],a[100:103]
RD a[248:251],v226 offset:61440
CV v29,v58,v59
v_smfmac_f32_16x16x64_bf16 v[144:147], v[236:239], v[0:7], v234
RD a[252:255],v227 offset:61440
EX v63,v63
v_smfmac_f32_16x16x64_bf16 v[148:151], v[236:239], v[8:15], v234
CV v30,v60,v61
v_smfmac_f32_16x16x64_bf16 v[152:155], v[236:239], v[16:23], v234
CV v31,v62,v63
s_nop 1
v_smfmac_f32_16x16x64_bf16 v[156:159], v[236:239], v[24:31], v234
s_waitcnt lgkmcnt(0)
buffer_load_dwordx4 v235, s[12:15], s64 offen offset:3072 lds
MM v[64:67],a[192:195],v[160:163],v[128:131]
s_add_u32 s64, s64, 16384
TR a[128:129],v232 offset:32768
s_add_u32 s81, s64, 896
MM v[64:67],a[196:199],v[176:179],v[64:67]
TR a[130:131],v232 offset:36864
v_max3_f32 v249, v144, v145, v148
MM v[64:67],a[200:203],v[192:195],v[64:67]
TR a[132:133],v232 offset:40960
v_max3_f32 v249, v249, v149, v152
MM v[64:67],a[204:207],v[208:211],v[64:67]
TR a[134:135],v232 offset:45056
v_max3_f32 v249, v249, v153, v156
MM v[68:71],a[220:223],v[208:211],v[128:131]
TR a[136:137],v233 offset:32768
v_max_f32 v249, v249, v157
MM v[68:71],a[216:219],v[192:195],v[68:71]
TR a[138:139],v233 offset:36864
v_cmp_lt_f32 vcc, s71, v249
MM v[68:71],a[212:215],v[176:179],v[68:71]
TR a[140:141],v233 offset:40960
TR a[142:143],v233 offset:45056
MM v[68:71],a[208:211],v[160:163],v[68:71]
s_cbranch_vccnz LSITE34_%=
LBACK34_%=:
MM v[76:79],a[208:211],v[164:167],v[132:135]
buffer_load_dword v242, off, s[20:23], s96 sc1
MM v[76:79],a[212:215],v[180:183],v[76:79]
buffer_load_dword v243, off, s[20:23], s19 sc1
MM v[76:79],a[216:219],v[196:199],v[76:79]
s_xor_b32 s66, s94, 6
v_and_b32 v32, 496, v248
v_mul_u32_u24 v32, 80, v32
MM v[76:79],a[220:223],v[212:215],v[76:79]
s_mul_i32 s66, s66, 163840
EX v64,v64
MM v[72:75],a[204:207],v[212:215],v[132:135]
s_add_u32 s66, s66, s88
s_add_u32 m0, s89, 147456
s_nop 0
buffer_load_dwordx4 v252, s[24:27], s66 offen offset:0 lds
MM v[72:75],a[200:203],v[196:199],v[72:75]
s_add_u32 s66, s66, 19456
EX v65,v65
MM v[72:75],a[196:199],v[180:183],v[72:75]
s_sub_u32 s60, s63, s18
v_add_u32 v32, s18, v32
CV v64,v64,v65
MM v[72:75],a[192:195],v[164:167],v[72:75]
s_mov_b32 s61, 131072
s_mov_b32 exec_lo, 0
v_add_u32 v32, s60, v32
s_mov_b64 exec, -1
MM v[80:83],a[192:195],v[168:171],v[136:139]
s_mov_b32 exec_hi, 0xff
buffer_load_dword v36, v32, s[32:35], 0 offen offset:0
s_mov_b64 exec, -1
EX v66,v66
MM v[80:83],a[196:199],v[184:187],v[80:83]
EX v67,v67
MM v[80:83],a[200:203],v[200:203],v[80:83]
EX v68,v68
MM v[80:83],a[204:207],v[216:219],v[80:83]
s_mov_b32 exec_hi, 0xff
buffer_load_dword v37, v32, s[20:23], s61 offen offset:0 sc1
s_mov_b64 exec, -1
MM v[84:87],a[220:223],v[216:219],v[136:139]
TR a[144:145],v232 offset:33280
TR a[146:147],v232 offset:37376
MM v[84:87],a[216:219],v[200:203],v[84:87]
s_add_u32 m0, s89, 147456
s_nop 0
buffer_load_dwordx4 v252, s[24:27], s66 offen offset:1024 lds
EX v69,v69
MM v[84:87],a[212:215],v[184:187],v[84:87]
EX v70,v70
MM v[84:87],a[208:211],v[168:171],v[84:87]
TR a[148:149],v232 offset:41472
CV v65,v66,v67
MM v[92:95],a[208:211],v[172:175],v[140:143]
EX v71,v71
MM v[92:95],a[212:215],v[188:191],v[92:95]
TR a[150:151],v232 offset:45568
TR a[152:153],v233 offset:33280
MM v[92:95],a[216:219],v[204:207],v[92:95]
EX v72,v72
MM v[92:95],a[220:223],v[220:223],v[92:95]
EX v73,v73
MM v[88:91],a[204:207],v[220:223],v[140:143]
TR a[154:155],v233 offset:37376
CV v66,v68,v69
MM v[88:91],a[200:203],v[204:207],v[88:91]
s_xor_b32 s66, s94, 7
EX v74,v74
MM v[88:91],a[196:199],v[188:191],v[88:91]
s_mul_i32 s66, s66, 163840
TR a[156:157],v233 offset:41472
CV v67,v70,v71
MM v[88:91],a[192:195],v[172:175],v[88:91]
s_add_u32 s66, s66, s88
s_add_u32 m0, s89, 155648
s_nop 0
buffer_load_dwordx4 v252, s[24:27], s66 offen offset:0 lds
MM v[96:99],a[224:227],v[160:163],v[128:131]
s_add_u32 s66, s66, 19456
EX v75,v75
MM v[96:99],a[228:231],v[176:179],v[96:99]
TR a[158:159],v233 offset:45568
TR a[160:161],v232 offset:33792
MM v[96:99],a[232:235],v[192:195],v[96:99]
EX v76,v76
MM v[96:99],a[236:239],v[208:211],v[96:99]
EX v77,v77
MM v[100:103],a[252:255],v[208:211],v[128:131]
TR a[162:163],v232 offset:37888
CV v72,v72,v73
MM v[100:103],a[248:251],v[192:195],v[100:103]
EX v78,v78
MM v[100:103],a[244:247],v[176:179],v[100:103]
TR a[164:165],v232 offset:41984
CV v73,v74,v75
MM v[100:103],a[240:243],v[160:163],v[100:103]
EX v79,v79
MM v[108:111],a[240:243],v[164:167],v[132:135]
s_add_u32 m0, s89, 155648
s_nop 0
buffer_load_dwordx4 v252, s[24:27], s66 offen offset:1024 lds
MM v[108:111],a[244:247],v[180:183],v[108:111]
TR a[166:167],v232 offset:46080
TR a[168:169],v233 offset:33792
MM v[108:111],a[248:251],v[196:199],v[108:111]
EX v80,v80
MM v[108:111],a[252:255],v[212:215],v[108:111]
EX v81,v81
MM v[104:107],a[236:239],v[212:215],v[132:135]
TR a[170:171],v233 offset:37888
CV v74,v76,v77
MM v[104:107],a[232:235],v[196:199],v[104:107]
EX v82,v82
MM v[104:107],a[228:231],v[180:183],v[104:107]
TR a[172:173],v233 offset:41984
TR a[174:175],v233 offset:46080
MM v[104:107],a[224:227],v[164:167],v[104:107]
EX v83,v83
MM v[112:115],a[224:227],v[168:171],v[136:139]
TR a[176:177],v232 offset:34304
CV v75,v78,v79
MM v[112:115],a[228:231],v[184:187],v[112:115]
EX v84,v84
MM v[112:115],a[232:235],v[200:203],v[112:115]
EX v85,v85
MM v[112:115],a[236:239],v[216:219],v[112:115]
TR a[178:179],v232 offset:38400
CV v80,v80,v81
MM v[116:119],a[252:255],v[216:219],v[136:139]
EX v86,v86
MM v[116:119],a[248:251],v[200:203],v[116:119]
TR a[180:181],v232 offset:42496
TR a[182:183],v232 offset:46592
MM v[116:119],a[244:247],v[184:187],v[116:119]
EX v87,v87
MM v[116:119],a[240:243],v[168:171],v[116:119]
TR a[184:185],v233 offset:34304
CV v81,v82,v83
MM v[124:127],a[240:243],v[172:175],v[140:143]
EX v88,v88
MM v[124:127],a[244:247],v[188:191],v[124:127]
TR a[186:187],v233 offset:38400
CV v82,v84,v85
MM v[124:127],a[248:251],v[204:207],v[124:127]
TR a[188:189],v233 offset:42496
TR a[190:191],v233 offset:46592
EX v89,v89
MM v[124:127],a[252:255],v[220:223],v[124:127]
EX v90,v90
MM v[120:123],a[236:239],v[220:223],v[140:143]
CV v83,v86,v87
CV v88,v88,v89
MM v[120:123],a[232:235],v[204:207],v[120:123]
EX v91,v91
MM v[120:123],a[228:231],v[188:191],v[120:123]
EX v92,v92
MM v[120:123],a[224:227],v[172:175],v[120:123]
s_waitcnt vmcnt(24) lgkmcnt(0)
s_barrier
MM a[0:3],a[128:131],v[0:3],a[0:3]
s_add_u32 m0, s65, 32768
EX v93,v93
MM a[0:3],a[132:135],v[4:7],a[0:3]
CV v89,v90,v91
CV v90,v92,v93
MM a[8:11],a[132:135],v[12:15],a[8:11]
EX v94,v94
MM a[8:11],a[128:131],v[8:11],a[8:11]
EX v95,v95
MM a[16:19],a[128:131],v[16:19],a[16:19]
RD a[192:195],v224 offset:0
LPG24_%=:
s_cbranch_execz LPG28_%=
CV v91,v94,v95
MM a[16:19],a[132:135],v[20:23],a[16:19]
EX v96,v96
MM a[24:27],a[132:135],v[28:31],a[24:27]
buffer_load_dwordx4 v228, s[8:11], s64 offen offset:0 lds
MM a[24:27],a[128:131],v[24:27],a[24:27]
EX v97,v97
MM a[28:31],a[136:139],v[24:27],a[28:31]
RD a[196:199],v225 offset:0
CV v68,v96,v97
MM a[28:31],a[140:143],v[28:31],a[28:31]
EX v98,v98
MM a[20:23],a[140:143],v[20:23],a[20:23]
EX v99,v99
MM a[20:23],a[136:139],v[16:19],a[20:23]
TR a[128:129],v232 offset:49152
RD a[200:203],v226 offset:0
MM a[12:15],a[136:139],v[8:11],a[12:15]
EX v100,v100
MM a[12:15],a[140:143],v[12:15],a[12:15]
TR a[130:131],v232 offset:53248
CV v69,v98,v99
MM a[4:7],a[140:143],v[4:7],a[4:7]
EX v101,v101
MM a[4:7],a[136:139],v[0:3],a[4:7]
TR a[132:133],v232 offset:57344
RD a[204:207],v227 offset:0
MM a[32:35],a[144:147],v[0:3],a[32:35]
EX v102,v102
MM a[32:35],a[148:151],v[4:7],a[32:35]
buffer_load_dwordx4 v229, s[8:11], s64 offen offset:1024 lds
MM a[40:43],a[148:151],v[12:15],a[40:43]
TR a[134:135],v232 offset:61440
CV v70,v100,v101
MM a[40:43],a[144:147],v[8:11],a[40:43]
EX v103,v103
MM a[48:51],a[144:147],v[16:19],a[48:51]
TR a[136:137],v233 offset:49152
TR a[138:139],v233 offset:53248
MM a[48:51],a[148:151],v[20:23],a[48:51]
EX v104,v104
MM a[56:59],a[148:151],v[28:31],a[56:59]
EX v105,v105
MM a[56:59],a[144:147],v[24:27],a[56:59]
TR a[140:141],v233 offset:57344
CV v71,v102,v103
MM a[60:63],a[152:155],v[24:27],a[60:63]
EX v106,v106
MM a[60:63],a[156:159],v[28:31],a[60:63]
RD a[208:211],v224 offset:4096
TR a[142:143],v233 offset:61440
MM a[52:55],a[156:159],v[20:23],a[52:55]
EX v107,v107
MM a[52:55],a[152:155],v[16:19],a[52:55]
buffer_load_dwordx4 v230, s[8:11], s64 offen offset:2048 lds
MM a[44:47],a[152:155],v[8:11],a[44:47]
TR a[144:145],v232 offset:49664
RD a[212:215],v225 offset:4096
MM a[44:47],a[156:159],v[12:15],a[44:47]
EX v108,v108
MM a[36:39],a[156:159],v[4:7],a[36:39]
TR a[146:147],v232 offset:53760
CV v76,v104,v105
MM a[36:39],a[152:155],v[0:3],a[36:39]
EX v109,v109
MM a[64:67],a[160:163],v[0:3],a[64:67]
TR a[148:149],v232 offset:57856
CV v77,v106,v107
MM a[64:67],a[164:167],v[4:7],a[64:67]
EX v110,v110
MM a[72:75],a[164:167],v[12:15],a[72:75]
EX v111,v111
MM a[72:75],a[160:163],v[8:11],a[72:75]
TR a[150:151],v232 offset:61952
RD a[216:219],v226 offset:4096
MM a[80:83],a[160:163],v[16:19],a[80:83]
TR a[152:153],v233 offset:49664
CV v78,v108,v109
MM a[80:83],a[164:167],v[20:23],a[80:83]
EX v112,v112
MM a[88:91],a[164:167],v[28:31],a[88:91]
EX v113,v113
MM a[88:91],a[160:163],v[24:27],a[88:91]
TR a[154:155],v233 offset:53760
RD a[220:223],v227 offset:4096
MM a[92:95],a[168:171],v[24:27],a[92:95]
buffer_load_dwordx4 v231, s[8:11], s64 offen offset:3072 lds
MM a[92:95],a[172:175],v[28:31],a[92:95]
s_add_u32 m0, s65, 98304
EX v114,v114
MM a[84:87],a[172:175],v[20:23],a[84:87]
TR a[156:157],v233 offset:57856
TR a[158:159],v233 offset:61952
MM a[84:87],a[168:171],v[16:19],a[84:87]
EX v115,v115
MM a[76:79],a[168:171],v[8:11],a[76:79]
EX v116,v116
MM a[76:79],a[172:175],v[12:15],a[76:79]
TR a[160:161],v232 offset:50176
CV v79,v110,v111
MM a[68:71],a[172:175],v[4:7],a[68:71]
EX v117,v117
MM a[68:71],a[168:171],v[0:3],a[68:71]
TR a[162:163],v232 offset:54272
RD a[224:227],v224 offset:8192
MM a[96:99],a[176:179],v[0:3],a[96:99]
EX v118,v118
MM a[96:99],a[180:183],v[4:7],a[96:99]
TR a[164:165],v232 offset:58368
CV v84,v112,v113
MM a[104:107],a[180:183],v[12:15],a[104:107]
buffer_load_dwordx4 v235, s[12:15], s81 offen offset:0 lds
TR a[166:167],v232 offset:62464
EX v119,v119
MM a[104:107],a[176:179],v[8:11],a[104:107]
RD a[228:231],v225 offset:8192
TR a[168:169],v233 offset:50176
MM a[112:115],a[176:179],v[16:19],a[112:115]
EX v120,v120
MM a[112:115],a[180:183],v[20:23],a[112:115]
TR a[170:171],v233 offset:54272
CV v85,v114,v115
MM a[120:123],a[180:183],v[28:31],a[120:123]
s_waitcnt vmcnt(8)
EX v121,v121
MM a[120:123],a[176:179],v[24:27],a[120:123]
s_lshr_b32 s77, %[sb], 3
RD a[232:235],v226 offset:8192
TR a[172:173],v233 offset:58368
MM a[124:127],a[184:187],v[24:27],a[124:127]
s_lshl_b32 s77, s77, 2
v_add_f32 v244, v144, v145
v_add_f32 v245, v148, v149
EX v122,v122
MM a[124:127],a[188:191],v[28:31],a[124:127]
s_add_u32 s77, s77, %[swave]
v_add_f32 v246, v152, v153
v_add_f32 v247, v156, v157
CV v86,v116,v117
MM a[116:119],a[188:191],v[20:23],a[116:119]
s_lshl_b32 s77, s77, 9
TR a[174:175],v233 offset:62464
buffer_load_dwordx4 v235, s[12:15], s64 offen offset:1024 lds
EX v123,v123
MM a[116:119],a[184:187],v[16:19],a[116:119]
s_add_u32 s77, s77, 0
TR a[176:177],v232 offset:50688
v_lshrrev_b32 v253, 1, v248
v_add_u32 v253, s77, v253
MM a[108:111],a[184:187],v[8:11],a[108:111]
s_mov_b32 s77, LKEND_ka-LPD2_%=-16
RD a[236:239],v227 offset:8192
v_min_u32 v253, s77, v253
EX v124,v124
CV v87,v118,v119
MM a[108:111],a[188:191],v[12:15],a[108:111]
s_getpc_b64 s[98:99]
LPD2_%=:
global_load_dword v253, v253, s[98:99]
TR a[178:179],v232 offset:54784
EX v125,v125
MM a[100:103],a[188:191],v[4:7],a[100:103]
TR a[180:181],v232 offset:58880
)ASM"
R"ASM(
RD a[240:243],v224 offset:12288
MM a[100:103],a[184:187],v[0:3],a[100:103]
EX v126,v126
v_smfmac_f32_16x16x64_bf16 v[144:147], v[236:239], v[64:71], v234
RD a[244:247],v225 offset:12288
RD a[248:251],v226 offset:12288
RD a[252:255],v227 offset:12288
EX v127,v127
v_smfmac_f32_16x16x64_bf16 v[148:151], v[236:239], v[72:79], v234
TR a[182:183],v232 offset:62976
CV v92,v120,v121
v_smfmac_f32_16x16x64_bf16 v[152:155], v[236:239], v[80:87], v234
buffer_load_dwordx4 v235, s[12:15], s81 offen offset:2048 lds
CV v93,v122,v123
CV v94,v124,v125
CV v95,v126,v127
s_nop 1
v_smfmac_f32_16x16x64_bf16 v[156:159], v[236:239], v[88:95], v234
TR a[184:185],v233 offset:50688
TR a[186:187],v233 offset:54784
TR a[188:189],v233 offset:58880
TR a[190:191],v233 offset:62976
s_waitcnt lgkmcnt(5)
buffer_load_dwordx4 v235, s[12:15], s64 offen offset:3072 lds
MM v[16:19],a[192:195],v[168:171],v[136:139]
MM v[16:19],a[196:199],v[184:187],v[16:19]
MM v[16:19],a[200:203],v[200:203],v[16:19]
v_max3_f32 v249, v144, v145, v148
v_max3_f32 v249, v249, v149, v152
MM v[16:19],a[204:207],v[216:219],v[16:19]
v_max3_f32 v249, v249, v153, v156
v_max_f32 v249, v249, v157
MM v[20:23],a[220:223],v[216:219],v[136:139]
v_cmp_lt_f32 vcc, s71, v249
MM v[20:23],a[216:219],v[200:203],v[20:23]
s_cbranch_vccnz LSITE35_%=
LBACK35_%=:
v_add_f32 v244, v144, v145
MM v[20:23],a[212:215],v[184:187],v[20:23]
v_add_f32 v245, v148, v149
v_add_f32 v246, v152, v153
MM v[20:23],a[208:211],v[168:171],v[20:23]
s_waitcnt lgkmcnt(0)
v_add_f32 v247, v156, v157
MM a[0:3],a[128:131],v[64:67],a[0:3]
EX v16,v16
MM a[0:3],a[132:135],v[68:71],a[0:3]
v_rcp_f32 v145, v244
MM a[8:11],a[132:135],v[76:79],a[8:11]
EX v17,v17
MM a[8:11],a[128:131],v[72:75],a[8:11]
EX v18,v18
MM a[12:15],a[136:139],v[72:75],a[12:15]
v_rcp_f32 v149, v245
MM a[12:15],a[140:143],v[76:79],a[12:15]
CV v16,v16,v17
v_bfe_u32 v144, v241, 0, 8
MM a[4:7],a[140:143],v[68:71],a[4:7]
EX v19,v19
MM a[4:7],a[136:139],v[64:67],a[4:7]
CV v17,v18,v19
v_lshl_or_b32 v144, v144, 1, 1
MM a[32:35],a[144:147],v[64:67],a[32:35]
EX v20,v20
MM a[32:35],a[148:151],v[68:71],a[32:35]
v_lshlrev_b32 v144, 22, v144
v_mul_f32 v144, -v144, v145
MM a[40:43],a[148:151],v[76:79],a[40:43]
v_bfe_u32 v148, v241, 8, 8
v_lshl_or_b32 v148, v148, 1, 1
MM a[40:43],a[144:147],v[72:75],a[40:43]
v_lshlrev_b32 v148, 22, v148
v_mul_f32 v148, -v148, v149
MM a[44:47],a[152:155],v[72:75],a[44:47]
s_mul_i32 s78, s49, 17408
s_add_u32 s78, s83, s78
s_add_u32 s78, s78, 0
s_add_u32 s79, s78, 4096
AR v0,a0
AR v1,a1
AR v2,a2
MM a[44:47],a[156:159],v[76:79],a[44:47]
AR v3,a3
AR v4,a4
AR v5,a5
MM a[36:39],a[156:159],v[68:71],a[36:39]
AR v6,a6
AR v7,a7
v_fma_f32 v0, v0, v145, v144
v_fma_f32 v1, v1, v145, v144
MM a[36:39],a[152:155],v[64:67],a[36:39]
v_fma_f32 v2, v2, v145, v144
v_fma_f32 v3, v3, v145, v144
v_fma_f32 v4, v4, v145, v144
v_fma_f32 v5, v5, v145, v144
MM a[64:67],a[160:163],v[64:67],a[64:67]
v_fma_f32 v6, v6, v145, v144
v_fma_f32 v7, v7, v145, v144
CV v160,v0,v1
CV v161,v2,v3
MM a[64:67],a[164:167],v[68:71],a[64:67]
CV v162,v4,v5
CV v163,v6,v7
MM a[72:75],a[164:167],v[76:79],a[72:75]
buffer_store_dwordx4 v[160:163], v248, s[20:23], s78 offen offset:0 sc1
AR v8,a8
AR v9,a9
AR v10,a10
MM a[72:75],a[160:163],v[72:75],a[72:75]
AR v11,a11
AR v12,a12
AR v13,a13
EX v21,v21
AR v14,a14
MM a[76:79],a[168:171],v[72:75],a[76:79]
AR v15,a15
v_fma_f32 v8, v8, v149, v148
v_fma_f32 v9, v9, v149, v148
v_fma_f32 v10, v10, v149, v148
MM a[76:79],a[172:175],v[76:79],a[76:79]
v_fma_f32 v11, v11, v149, v148
v_fma_f32 v12, v12, v149, v148
v_fma_f32 v13, v13, v149, v148
v_fma_f32 v14, v14, v149, v148
EX v22,v22
MM a[68:71],a[172:175],v[68:71],a[68:71]
v_fma_f32 v15, v15, v149, v148
CV v164,v8,v9
CV v165,v10,v11
CV v166,v12,v13
MM a[68:71],a[168:171],v[64:67],a[68:71]
CV v167,v14,v15
CV v18,v20,v21
MM a[96:99],a[176:179],v[64:67],a[96:99]
buffer_store_dwordx4 v[164:167], v248, s[20:23], s79 offen offset:0 sc1
AR v32,a32
AR v33,a33
AR v34,a34
EX v23,v23
AR v35,a35
MM a[96:99],a[180:183],v[68:71],a[96:99]
AR v36,a36
AR v37,a37
AR v38,a38
AR v39,a39
MM a[104:107],a[180:183],v[76:79],a[104:107]
v_fma_f32 v32, v32, v145, v144
v_fma_f32 v33, v33, v145, v144
v_fma_f32 v34, v34, v145, v144
v_fma_f32 v35, v35, v145, v144
MM a[104:107],a[176:179],v[72:75],a[104:107]
v_fma_f32 v36, v36, v145, v144
v_fma_f32 v37, v37, v145, v144
v_fma_f32 v38, v38, v145, v144
MM a[108:111],a[184:187],v[72:75],a[108:111]
v_fma_f32 v39, v39, v145, v144
CV v176,v32,v33
CV v177,v34,v35
CV v178,v36,v37
MM a[108:111],a[188:191],v[76:79],a[108:111]
CV v179,v38,v39
CV v19,v22,v23
MM a[100:103],a[188:191],v[68:71],a[100:103]
buffer_store_dwordx4 v[176:179], v248, s[20:23], s78 offen offset:1024 sc1
AR v40,a40
AR v41,a41
AR v42,a42
AR v43,a43
AR v44,a44
MM a[100:103],a[184:187],v[64:67],a[100:103]
AR v45,a45
AR v46,a46
AR v47,a47
v_fma_f32 v40, v40, v149, v148
MM v[28:31],a[208:211],v[172:175],v[140:143]
v_fma_f32 v41, v41, v149, v148
v_fma_f32 v42, v42, v149, v148
v_fma_f32 v43, v43, v149, v148
MM v[28:31],a[212:215],v[188:191],v[28:31]
v_fma_f32 v44, v44, v149, v148
v_fma_f32 v45, v45, v149, v148
v_fma_f32 v46, v46, v149, v148
v_fma_f32 v47, v47, v149, v148
MM v[28:31],a[216:219],v[204:207],v[28:31]
CV v180,v40,v41
CV v181,v42,v43
CV v182,v44,v45
CV v183,v46,v47
MM v[28:31],a[220:223],v[220:223],v[28:31]
buffer_store_dwordx4 v[180:183], v248, s[20:23], s79 offen offset:1024 sc1
AR v0,a64
AR v1,a65
MM v[24:27],a[204:207],v[220:223],v[140:143]
AR v2,a66
AR v3,a67
AR v4,a68
MM v[24:27],a[200:203],v[204:207],v[24:27]
AR v5,a69
AR v6,a70
AR v7,a71
v_fma_f32 v0, v0, v145, v144
MM v[24:27],a[196:199],v[188:191],v[24:27]
v_fma_f32 v1, v1, v145, v144
v_fma_f32 v2, v2, v145, v144
v_fma_f32 v3, v3, v145, v144
v_fma_f32 v4, v4, v145, v144
MM v[24:27],a[192:195],v[172:175],v[24:27]
v_fma_f32 v5, v5, v145, v144
v_fma_f32 v6, v6, v145, v144
v_fma_f32 v7, v7, v145, v144
CV v192,v0,v1
MM v[48:51],a[224:227],v[168:171],v[136:139]
CV v193,v2,v3
CV v194,v4,v5
CV v195,v6,v7
MM v[48:51],a[228:231],v[184:187],v[48:51]
buffer_store_dwordx4 v[192:195], v248, s[20:23], s78 offen offset:2048 sc1
AR v8,a72
AR v9,a73
MM v[48:51],a[232:235],v[200:203],v[48:51]
AR v10,a74
AR v11,a75
AR v12,a76
AR v13,a77
MM v[48:51],a[236:239],v[216:219],v[48:51]
AR v14,a78
AR v15,a79
v_fma_f32 v8, v8, v149, v148
v_fma_f32 v9, v9, v149, v148
MM v[52:55],a[252:255],v[216:219],v[136:139]
EX v24,v24
EX v25,v25
EX v26,v26
EX v27,v27
EX v28,v28
EX v29,v29
v_fma_f32 v10, v10, v149, v148
v_fma_f32 v11, v11, v149, v148
EX v30,v30
v_fma_f32 v12, v12, v149, v148
v_fma_f32 v13, v13, v149, v148
MM v[52:55],a[248:251],v[200:203],v[52:55]
v_fma_f32 v14, v14, v149, v148
v_fma_f32 v15, v15, v149, v148
CV v196,v8,v9
EX v31,v31
CV v24,v24,v25
CV v197,v10,v11
MM v[52:55],a[244:247],v[184:187],v[52:55]
CV v198,v12,v13
CV v199,v14,v15
MM v[52:55],a[240:243],v[168:171],v[52:55]
buffer_store_dwordx4 v[196:199], v248, s[20:23], s79 offen offset:2048 sc1
AR v32,a96
AR v33,a97
AR v34,a98
MM v[60:63],a[240:243],v[172:175],v[140:143]
AR v35,a99
AR v36,a100
AR v37,a101
EX v48,v48
AR v38,a102
MM v[60:63],a[244:247],v[188:191],v[60:63]
AR v39,a103
v_fma_f32 v32, v32, v145, v144
v_fma_f32 v33, v33, v145, v144
v_fma_f32 v34, v34, v145, v144
CV v25,v26,v27
MM v[60:63],a[248:251],v[204:207],v[60:63]
v_fma_f32 v35, v35, v145, v144
v_fma_f32 v36, v36, v145, v144
v_fma_f32 v37, v37, v145, v144
MM v[60:63],a[252:255],v[220:223],v[60:63]
v_fma_f32 v38, v38, v145, v144
v_fma_f32 v39, v39, v145, v144
CV v208,v32,v33
EX v49,v49
CV v209,v34,v35
MM v[56:59],a[236:239],v[220:223],v[140:143]
CV v210,v36,v37
CV v211,v38,v39
MM v[56:59],a[232:235],v[204:207],v[56:59]
buffer_store_dwordx4 v[208:211], v248, s[20:23], s78 offen offset:3072 sc1
AR v40,a104
AR v41,a105
AR v42,a106
AR v43,a107
EX v50,v50
MM v[56:59],a[228:231],v[188:191],v[56:59]
AR v44,a108
AR v45,a109
AR v46,a110
AR v47,a111
CV v26,v28,v29
MM v[56:59],a[224:227],v[172:175],v[56:59]
s_waitcnt lgkmcnt(0)
s_barrier
v_fma_f32 v40, v40, v149, v148
v_fma_f32 v41, v41, v149, v148
v_fma_f32 v42, v42, v149, v148
MM a[16:19],a[128:131],v[80:83],a[16:19]
s_xor_b32 s66, s94, 4
v_fma_f32 v43, v43, v149, v148
v_fma_f32 v44, v44, v149, v148
v_fma_f32 v45, v45, v149, v148
v_fma_f32 v46, v46, v149, v148
EX v51,v51
MM a[16:19],a[132:135],v[84:87],a[16:19]
s_mul_i32 s66, s66, 163840
v_fma_f32 v47, v47, v149, v148
CV v212,v40,v41
CV v213,v42,v43
CV v214,v44,v45
MM a[24:27],a[132:135],v[92:95],a[24:27]
s_add_u32 s66, s66, s87
CV v215,v46,v47
CV v27,v30,v31
MM a[24:27],a[128:131],v[88:91],a[24:27]
s_add_u32 m0, s65, 114688
buffer_store_dwordx4 v[212:215], v248, s[20:23], s79 offen offset:3072 sc1
EX v52,v52
MM a[28:31],a[136:139],v[88:91],a[28:31]
EX v53,v53
MM a[28:31],a[140:143],v[92:95],a[28:31]
RD a[192:195],v224 offset:16384
CV v20,v48,v49
MM a[20:23],a[140:143],v[84:87],a[20:23]
buffer_load_dwordx4 v240, s[24:27], s66 offen offset:0 lds
MM a[20:23],a[136:139],v[80:83],a[20:23]
s_add_u32 s66, s66, 9216
EX v54,v54
MM a[48:51],a[144:147],v[80:83],a[48:51]
RD a[196:199],v225 offset:16384
CV v21,v50,v51
MM a[48:51],a[148:151],v[84:87],a[48:51]
EX v55,v55
MM a[56:59],a[148:151],v[92:95],a[56:59]
RD a[200:203],v226 offset:16384
RD a[204:207],v227 offset:16384
MM a[56:59],a[144:147],v[88:91],a[56:59]
buffer_load_dwordx4 v240, s[24:27], s66 offen offset:1024 lds
MM a[60:63],a[152:155],v[88:91],a[60:63]
s_add_u32 s66, s66, 9216
EX v56,v56
MM a[60:63],a[156:159],v[92:95],a[60:63]
RD a[208:211],v224 offset:20480
CV v22,v52,v53
MM a[52:55],a[156:159],v[84:87],a[52:55]
buffer_load_dwordx4 v240, s[24:27], s66 offen offset:2048 lds
MM a[52:55],a[152:155],v[80:83],a[52:55]
s_add_u32 s66, s66, 9216
EX v57,v57
MM a[80:83],a[160:163],v[80:83],a[80:83]
RD a[212:215],v225 offset:20480
RD a[216:219],v226 offset:20480
MM a[80:83],a[164:167],v[84:87],a[80:83]
buffer_load_dwordx4 v240, s[24:27], s66 offen offset:3072 lds
MM a[88:91],a[164:167],v[92:95],a[88:91]
s_xor_b32 s66, s94, 4
EX v58,v58
MM a[88:91],a[160:163],v[88:91],a[88:91]
s_mul_i32 s66, s66, 163840
RD a[220:223],v227 offset:20480
CV v23,v54,v55
MM a[92:95],a[168:171],v[88:91],a[92:95]
s_add_u32 s66, s66, s86
EX v59,v59
MM a[92:95],a[172:175],v[92:95],a[92:95]
s_add_u32 m0, s65, 49152
RD a[224:227],v224 offset:24576
RD a[228:231],v225 offset:24576
MM a[84:87],a[172:175],v[84:87],a[84:87]
buffer_load_dwordx4 v240, s[24:27], s66 offen offset:0 lds
MM a[84:87],a[168:171],v[80:83],a[84:87]
s_add_u32 s66, s66, 9216
EX v60,v60
MM a[112:115],a[176:179],v[80:83],a[112:115]
RD a[232:235],v226 offset:24576
CV v28,v56,v57
MM a[112:115],a[180:183],v[84:87],a[112:115]
EX v61,v61
MM a[120:123],a[180:183],v[92:95],a[120:123]
buffer_load_dwordx4 v240, s[24:27], s66 offen offset:1024 lds
MM a[120:123],a[176:179],v[88:91],a[120:123]
s_add_u32 s66, s66, 9216
RD a[236:239],v227 offset:24576
RD a[240:243],v224 offset:28672
MM a[124:127],a[184:187],v[88:91],a[124:127]
EX v62,v62
MM a[124:127],a[188:191],v[92:95],a[124:127]
buffer_load_dwordx4 v240, s[24:27], s66 offen offset:2048 lds
MM a[116:119],a[188:191],v[84:87],a[116:119]
s_add_u32 s66, s66, 9216
RD a[244:247],v225 offset:28672
RD a[248:251],v226 offset:28672
RD a[252:255],v227 offset:28672
EX v63,v63
MM a[116:119],a[184:187],v[80:83],a[116:119]
buffer_load_dwordx4 v240, s[24:27], s66 offen offset:3072 lds
v_smfmac_f32_16x16x64_bf16 v[152:155], v[236:239], v[16:23], v234
s_nop 0
LPG25_%=:
s_cbranch_execz LPG29_%=
CV v29,v58,v59
CV v30,v60,v61
CV v31,v62,v63
s_nop 1
v_smfmac_f32_16x16x64_bf16 v[156:159], v[236:239], v[24:31], v234
s_waitcnt lgkmcnt(0)
MM v[80:83],a[192:195],v[168:171],v[136:139]
TR a[128:129],v232 offset:0
TR a[130:131],v232 offset:4096
MM v[80:83],a[196:199],v[184:187],v[80:83]
TR a[132:133],v232 offset:8192
TR a[134:135],v232 offset:12288
MM v[80:83],a[200:203],v[200:203],v[80:83]
TR a[136:137],v233 offset:0
TR a[138:139],v233 offset:4096
MM v[80:83],a[204:207],v[216:219],v[80:83]
v_max3_f32 v249, v152, v153, v156
v_max_f32 v249, v249, v157
MM v[84:87],a[220:223],v[216:219],v[136:139]
TR a[140:141],v233 offset:8192
v_cmp_lt_f32 vcc, s71, v249
MM v[84:87],a[216:219],v[200:203],v[84:87]
)ASM"
R"ASM(
s_cbranch_vccnz LSITE36_%=
LBACK36_%=:
TR a[142:143],v233 offset:12288
v_add_f32 v246, v152, v153
MM v[84:87],a[212:215],v[184:187],v[84:87]
TR a[144:145],v232 offset:512
v_add_f32 v247, v156, v157
MM v[84:87],a[208:211],v[168:171],v[84:87]
TR a[146:147],v232 offset:4608
TR a[148:149],v232 offset:8704
MM v[92:95],a[208:211],v[172:175],v[140:143]
s_lshl_b32 s97, s84, 2
s_add_u32 s97, s97, s96
s_add_u32 s97, s97, 0
MM v[92:95],a[212:215],v[188:191],v[92:95]
s_waitcnt vmcnt(8)
v_mov_b32 v242, s69
s_mov_b64 exec, 1
buffer_store_dword v242, off, s[20:23], s97 offset:0 sc1
s_mov_b64 exec, -1
MM v[92:95],a[216:219],v[204:207],v[92:95]
v_lshrrev_b32 v98, 1, v248
v_min_u32 v98, 312, v98
MM v[92:95],a[220:223],v[220:223],v[92:95]
s_mov_b32 s62, 94464
buffer_load_dwordx2 v[96:97], v98, s[20:23], s62 offen offset:0 sc1
MM v[88:91],a[204:207],v[220:223],v[140:143]
TR a[150:151],v232 offset:12800
EX v80,v80
MM v[88:91],a[200:203],v[204:207],v[88:91]
TR a[152:153],v233 offset:512
TR a[154:155],v233 offset:4608
EX v81,v81
MM v[88:91],a[196:199],v[188:191],v[88:91]
TR a[156:157],v233 offset:8704
TR a[158:159],v233 offset:12800
MM v[88:91],a[192:195],v[172:175],v[88:91]
EX v82,v82
MM v[112:115],a[224:227],v[168:171],v[136:139]
TR a[160:161],v232 offset:1024
TR a[162:163],v232 offset:5120
EX v83,v83
MM v[112:115],a[228:231],v[184:187],v[112:115]
TR a[164:165],v232 offset:9216
TR a[166:167],v232 offset:13312
MM v[112:115],a[232:235],v[200:203],v[112:115]
EX v84,v84
MM v[112:115],a[236:239],v[216:219],v[112:115]
TR a[168:169],v233 offset:1024
TR a[170:171],v233 offset:5120
MM v[116:119],a[252:255],v[216:219],v[136:139]
EX v85,v85
MM v[116:119],a[248:251],v[200:203],v[116:119]
TR a[172:173],v233 offset:9216
TR a[174:175],v233 offset:13312
MM v[116:119],a[244:247],v[184:187],v[116:119]
TR a[176:177],v232 offset:1536
EX v86,v86
MM v[116:119],a[240:243],v[168:171],v[116:119]
TR a[178:179],v232 offset:5632
CV v80,v80,v81
EX v87,v87
MM v[124:127],a[240:243],v[172:175],v[140:143]
TR a[180:181],v232 offset:9728
TR a[182:183],v232 offset:13824
MM v[124:127],a[244:247],v[188:191],v[124:127]
EX v88,v88
MM v[124:127],a[248:251],v[204:207],v[124:127]
TR a[184:185],v233 offset:1536
TR a[186:187],v233 offset:5632
TR a[188:189],v233 offset:9728
TR a[190:191],v233 offset:13824
EX v89,v89
MM v[124:127],a[252:255],v[220:223],v[124:127]
CV v81,v82,v83
CV v82,v84,v85
MM v[120:123],a[236:239],v[220:223],v[140:143]
EX v90,v90
MM v[120:123],a[232:235],v[204:207],v[120:123]
EX v91,v91
MM v[120:123],a[228:231],v[188:191],v[120:123]
CV v83,v86,v87
CV v88,v88,v89
MM v[120:123],a[224:227],v[172:175],v[120:123]
s_waitcnt lgkmcnt(0)
s_barrier
CV v89,v90,v91
EX v92,v92
MM a[16:19],a[128:131],v[16:19],a[16:19]
s_xor_b32 s66, s94, 5
EX v93,v93
MM a[16:19],a[132:135],v[20:23],a[16:19]
s_mul_i32 s66, s66, 163840
EX v94,v94
MM a[24:27],a[132:135],v[28:31],a[24:27]
s_add_u32 s66, s66, s87
EX v95,v95
MM a[24:27],a[128:131],v[24:27],a[24:27]
s_add_u32 m0, s65, 65536
RD a[192:195],v224 offset:32768
CV v90,v92,v93
MM a[28:31],a[136:139],v[24:27],a[28:31]
EX v112,v112
MM a[28:31],a[140:143],v[28:31],a[28:31]
buffer_load_dwordx4 v240, s[24:27], s66 offen offset:0 lds
MM a[20:23],a[140:143],v[20:23],a[20:23]
s_add_u32 s66, s66, 9216
EX v113,v113
MM a[20:23],a[136:139],v[16:19],a[20:23]
RD a[196:199],v225 offset:32768
CV v91,v94,v95
MM a[48:51],a[144:147],v[16:19],a[48:51]
v_lshrrev_b32 v243, 2, v248
v_min_u32 v243, 12, v243
MM a[48:51],a[148:151],v[20:23],a[48:51]
buffer_load_dword v242, v243, s[20:23], s44 offen offset:0 sc1
EX v114,v114
MM a[56:59],a[148:151],v[28:31],a[56:59]
RD a[200:203],v226 offset:32768
RD a[204:207],v227 offset:32768
MM a[56:59],a[144:147],v[24:27],a[56:59]
EX v115,v115
MM a[60:63],a[152:155],v[24:27],a[60:63]
buffer_load_dwordx4 v240, s[24:27], s66 offen offset:1024 lds
MM a[60:63],a[156:159],v[28:31],a[60:63]
s_add_u32 s66, s66, 9216
EX v116,v116
MM a[52:55],a[156:159],v[20:23],a[52:55]
EX v117,v117
MM a[52:55],a[152:155],v[16:19],a[52:55]
RD a[208:211],v224 offset:36864
RD a[212:215],v225 offset:36864
buffer_load_dwordx4 v240, s[24:27], s66 offen offset:2048 lds
MM a[80:83],a[160:163],v[16:19],a[80:83]
s_add_u32 s66, s66, 9216
EX v118,v118
MM a[80:83],a[164:167],v[20:23],a[80:83]
RD a[216:219],v226 offset:36864
CV v84,v112,v113
MM a[88:91],a[164:167],v[28:31],a[88:91]
EX v119,v119
MM a[88:91],a[160:163],v[24:27],a[88:91]
buffer_load_dwordx4 v240, s[24:27], s66 offen offset:3072 lds
EX v120,v120
MM a[92:95],a[168:171],v[24:27],a[92:95]
s_xor_b32 s66, s94, 5
RD a[220:223],v227 offset:36864
CV v85,v114,v115
MM a[92:95],a[172:175],v[28:31],a[92:95]
s_mul_i32 s66, s66, 163840
EX v121,v121
MM a[84:87],a[172:175],v[20:23],a[84:87]
s_add_u32 s66, s66, s86
RD a[224:227],v224 offset:40960
CV v86,v116,v117
MM a[84:87],a[168:171],v[16:19],a[84:87]
s_add_u32 m0, s65, 0
EX v122,v122
MM a[112:115],a[176:179],v[16:19],a[112:115]
buffer_load_dwordx4 v240, s[24:27], s66 offen offset:0 lds
RD a[228:231],v225 offset:40960
EX v123,v123
MM a[112:115],a[180:183],v[20:23],a[112:115]
s_add_u32 s66, s66, 9216
RD a[232:235],v226 offset:40960
CV v87,v118,v119
MM a[120:123],a[180:183],v[28:31],a[120:123]
EX v124,v124
MM a[120:123],a[176:179],v[24:27],a[120:123]
buffer_load_dwordx4 v240, s[24:27], s66 offen offset:1024 lds
MM a[124:127],a[184:187],v[24:27],a[124:127]
s_add_u32 s66, s66, 9216
RD a[236:239],v227 offset:40960
EX v125,v125
MM a[124:127],a[188:191],v[28:31],a[124:127]
EX v126,v126
MM a[116:119],a[188:191],v[20:23],a[116:119]
RD a[240:243],v224 offset:45056
RD a[244:247],v225 offset:45056
RD a[248:251],v226 offset:45056
RD a[252:255],v227 offset:45056
EX v127,v127
MM a[116:119],a[184:187],v[16:19],a[116:119]
buffer_load_dwordx4 v240, s[24:27], s66 offen offset:2048 lds
CV v92,v120,v121
v_smfmac_f32_16x16x64_bf16 v[152:155], v[236:239], v[80:87], v234
s_add_u32 s66, s66, 9216
CV v93,v122,v123
CV v94,v124,v125
CV v95,v126,v127
s_nop 1
v_smfmac_f32_16x16x64_bf16 v[156:159], v[236:239], v[88:95], v234
s_waitcnt lgkmcnt(0)
buffer_load_dwordx4 v240, s[24:27], s66 offen offset:3072 lds
v_lshrrev_b32 v145, 9, v248
v_mul_u32_u24 v145, 1536, v145
v_sub_u32 v244, v232, v145
v_sub_u32 v235, v233, v145
v_add_u32 v244, 65536, v244
v_add_u32 v235, 65536, v235
v_and_b32 v145, 960, v248
v_lshlrev_b32 v145, 1, v145
v_bfe_u32 v146, v248, 7, 2
v_lshl_add_u32 v145, v146, 5, v145
v_bfe_u32 v146, v248, 4, 2
v_lshl_add_u32 v145, v146, 3, v145
v_add_u32 v228, %[slds], v145
v_xor_b32 v229, 32, v228
v_xor_b32 v230, 64, v228
v_xor_b32 v231, 96, v228
v_add_u32 v144, 65536, v228
v_add_u32 v145, 65536, v229
v_add_u32 v146, 65536, v230
v_add_u32 v147, 65536, v231
MM v[16:19],a[192:195],v[168:171],v[136:139]
TR a[128:129],v232 offset:16384
TR a[130:131],v232 offset:20480
MM v[16:19],a[196:199],v[184:187],v[16:19]
TR a[132:133],v232 offset:24576
TR a[134:135],v232 offset:28672
MM v[16:19],a[200:203],v[200:203],v[16:19]
TR a[136:137],v233 offset:16384
TR a[138:139],v233 offset:20480
MM v[16:19],a[204:207],v[216:219],v[16:19]
v_max3_f32 v249, v152, v153, v156
v_max_f32 v249, v249, v157
MM v[20:23],a[220:223],v[216:219],v[136:139]
TR a[140:141],v233 offset:24576
v_cmp_lt_f32 vcc, s71, v249
MM v[20:23],a[216:219],v[200:203],v[20:23]
s_cbranch_vccnz LSITE37_%=
LBACK37_%=:
TR a[142:143],v233 offset:28672
v_add_f32 v246, v152, v153
MM v[20:23],a[212:215],v[184:187],v[20:23]
TR a[144:145],v232 offset:16896
v_add_f32 v247, v156, v157
MM v[20:23],a[208:211],v[168:171],v[20:23]
TR a[146:147],v232 offset:20992
TR a[148:149],v232 offset:25088
MM v[28:31],a[208:211],v[172:175],v[140:143]
s_waitcnt vmcnt(7)
v_cmp_ne_u32 vcc, s69, v242
s_cbranch_vccnz LRPO0_%=
LRPO0b_%=:
MM v[28:31],a[212:215],v[188:191],v[28:31]
s_xor_b32 s92, s49, 4
s_mul_i32 s92, s92, 17408
s_add_u32 s92, s83, s92
MM v[28:31],a[216:219],v[204:207],v[28:31]
buffer_load_dwordx4 a[0:3], v248, s[20:23], s92 offen offset:0 sc1
MM v[28:31],a[220:223],v[220:223],v[28:31]
buffer_load_dwordx4 a[32:35], v248, s[20:23], s92 offen offset:1024 sc1
MM v[24:27],a[204:207],v[220:223],v[140:143]
TR a[150:151],v232 offset:29184
v_xor_b32 v96, s69, v96
v_xor_b32 v97, s69, v97
EX v16,v16
MM v[24:27],a[200:203],v[204:207],v[24:27]
TR a[152:153],v233 offset:16896
TR a[154:155],v233 offset:20992
v_or_b32 v99, v96, v97
EX v17,v17
MM v[24:27],a[196:199],v[188:191],v[24:27]
buffer_load_dwordx4 a[64:67], v248, s[20:23], s92 offen offset:2048 sc1
TR a[156:157],v233 offset:25088
MM v[24:27],a[192:195],v[172:175],v[24:27]
TR a[158:159],v233 offset:29184
v_cmp_ne_u32 vcc, 0, v99
s_cbranch_vccnz LRPW_%=
LRPWb_%=:
EX v18,v18
MM v[48:51],a[224:227],v[168:171],v[136:139]
TR a[160:161],v232 offset:17408
TR a[162:163],v232 offset:21504
EX v19,v19
MM v[48:51],a[228:231],v[184:187],v[48:51]
buffer_load_dwordx4 a[96:99], v248, s[20:23], s92 offen offset:3072 sc1
TR a[164:165],v232 offset:25600
MM v[48:51],a[232:235],v[200:203],v[48:51]
s_add_u32 s92, s92, 4096
TR a[166:167],v232 offset:29696
TR a[168:169],v233 offset:17408
EX v20,v20
MM v[48:51],a[236:239],v[216:219],v[48:51]
TR a[170:171],v233 offset:21504
TR a[172:173],v233 offset:25600
MM v[52:55],a[252:255],v[216:219],v[136:139]
buffer_load_dwordx4 a[4:7], v248, s[20:23], s92 offen offset:0 sc1
EX v21,v21
MM v[52:55],a[248:251],v[200:203],v[52:55]
TR a[174:175],v233 offset:29696
CV v16,v16,v17
MM v[52:55],a[244:247],v[184:187],v[52:55]
TR a[176:177],v232 offset:17920
EX v22,v22
MM v[52:55],a[240:243],v[168:171],v[52:55]
buffer_load_dwordx4 a[36:39], v248, s[20:23], s92 offen offset:1024 sc1
TR a[178:179],v232 offset:22016
EX v23,v23
MM v[60:63],a[240:243],v[172:175],v[140:143]
TR a[180:181],v232 offset:26112
TR a[182:183],v232 offset:30208
MM v[60:63],a[244:247],v[188:191],v[60:63]
EX v24,v24
MM v[60:63],a[248:251],v[204:207],v[60:63]
buffer_load_dwordx4 a[68:71], v248, s[20:23], s92 offen offset:2048 sc1
TR a[184:185],v233 offset:17920
TR a[186:187],v233 offset:22016
TR a[188:189],v233 offset:26112
TR a[190:191],v233 offset:30208
EX v25,v25
MM v[60:63],a[252:255],v[220:223],v[60:63]
EX v26,v26
CV v17,v18,v19
MM v[56:59],a[236:239],v[220:223],v[140:143]
EX v27,v27
MM v[56:59],a[232:235],v[204:207],v[56:59]
buffer_load_dwordx4 a[100:103], v248, s[20:23], s92 offen offset:3072 sc1
MM v[56:59],a[228:231],v[188:191],v[56:59]
EX v28,v28
CV v18,v20,v21
MM v[56:59],a[224:227],v[172:175],v[56:59]
s_waitcnt lgkmcnt(0)
s_barrier
CV v19,v22,v23
MM a[16:19],a[128:131],v[80:83],a[16:19]
s_xor_b32 s66, s94, 6
EX v29,v29
MM a[16:19],a[132:135],v[84:87],a[16:19]
s_mul_i32 s66, s66, 163840
CV v24,v24,v25
CV v25,v26,v27
MM a[24:27],a[132:135],v[92:95],a[24:27]
s_add_u32 s66, s66, s87
EX v30,v30
MM a[24:27],a[128:131],v[88:91],a[24:27]
s_add_u32 m0, s65, 81920
EX v31,v31
MM a[28:31],a[136:139],v[88:91],a[28:31]
EX v48,v48
MM a[28:31],a[140:143],v[92:95],a[28:31]
CV v26,v28,v29
CV v27,v30,v31
MM a[20:23],a[140:143],v[84:87],a[20:23]
buffer_load_dwordx4 v240, s[24:27], s66 offen offset:0 lds
MM a[20:23],a[136:139],v[80:83],a[20:23]
s_add_u32 s66, s66, 9216
EX v49,v49
MM a[48:51],a[144:147],v[80:83],a[48:51]
EX v50,v50
MM a[48:51],a[148:151],v[84:87],a[48:51]
buffer_load_dwordx4 v240, s[24:27], s66 offen offset:1024 lds
MM a[56:59],a[148:151],v[92:95],a[56:59]
s_add_u32 s66, s66, 9216
EX v51,v51
MM a[56:59],a[144:147],v[88:91],a[56:59]
TR a[128:129],v232 offset:32768
CV v20,v48,v49
MM a[60:63],a[152:155],v[88:91],a[60:63]
EX v52,v52
MM a[60:63],a[156:159],v[92:95],a[60:63]
TR a[130:131],v232 offset:36864
TR a[132:133],v232 offset:40960
MM a[52:55],a[156:159],v[84:87],a[52:55]
EX v53,v53
MM a[52:55],a[152:155],v[80:83],a[52:55]
TR a[134:135],v232 offset:45056
TR a[136:137],v233 offset:32768
buffer_load_dwordx4 v240, s[24:27], s66 offen offset:2048 lds
MM a[80:83],a[160:163],v[80:83],a[80:83]
s_add_u32 s66, s66, 9216
TR a[138:139],v233 offset:36864
TR a[140:141],v233 offset:40960
EX v54,v54
MM a[80:83],a[164:167],v[84:87],a[80:83]
TR a[142:143],v233 offset:45056
TR a[144:145],v232 offset:33280
MM a[88:91],a[164:167],v[92:95],a[88:91]
s_xor_b32 s92, s49, 5
s_mul_i32 s92, s92, 17408
s_add_u32 s92, s83, s92
EX v55,v55
)ASM"
R"ASM(
MM a[88:91],a[160:163],v[88:91],a[88:91]
buffer_load_dwordx4 a[8:11], v248, s[20:23], s92 offen offset:0 sc1
TR a[146:147],v232 offset:37376
EX v56,v56
MM a[92:95],a[168:171],v[88:91],a[92:95]
TR a[148:149],v232 offset:41472
TR a[150:151],v232 offset:45568
buffer_load_dwordx4 v240, s[24:27], s66 offen offset:3072 lds
MM a[92:95],a[172:175],v[92:95],a[92:95]
s_xor_b32 s66, s94, 6
TR a[152:153],v233 offset:33280
EX v57,v57
CV v21,v50,v51
MM a[84:87],a[172:175],v[84:87],a[84:87]
s_mul_i32 s66, s66, 163840
TR a[154:155],v233 offset:37376
TR a[156:157],v233 offset:41472
MM a[84:87],a[168:171],v[80:83],a[84:87]
s_add_u32 s66, s66, s86
buffer_load_dwordx4 a[40:43], v248, s[20:23], s92 offen offset:1024 sc1
TR a[158:159],v233 offset:45568
s_add_u32 m0, s65, 16384
EX v58,v58
MM a[112:115],a[176:179],v[80:83],a[112:115]
TR a[160:161],v232 offset:33792
CV v22,v52,v53
EX v59,v59
MM a[112:115],a[180:183],v[84:87],a[112:115]
TR a[162:163],v232 offset:37888
TR a[164:165],v232 offset:41984
MM a[120:123],a[180:183],v[92:95],a[120:123]
buffer_load_dwordx4 a[72:75], v248, s[20:23], s92 offen offset:2048 sc1
TR a[166:167],v232 offset:46080
EX v60,v60
MM a[120:123],a[176:179],v[88:91],a[120:123]
TR a[168:169],v233 offset:33792
TR a[170:171],v233 offset:37888
CV v23,v54,v55
MM a[124:127],a[184:187],v[88:91],a[124:127]
TR a[172:173],v233 offset:41984
EX v61,v61
MM a[124:127],a[188:191],v[92:95],a[124:127]
buffer_load_dwordx4 a[104:107], v248, s[20:23], s92 offen offset:3072 sc1
TR a[174:175],v233 offset:46080
EX v62,v62
MM a[116:119],a[188:191],v[84:87],a[116:119]
TR a[176:177],v232 offset:34304
TR a[178:179],v232 offset:38400
EX v63,v63
MM a[116:119],a[184:187],v[80:83],a[116:119]
s_add_u32 s92, s92, 4096
TR a[180:181],v232 offset:42496
CV v28,v56,v57
v_smfmac_f32_16x16x64_bf16 v[152:155], v[236:239], v[16:23], v234
buffer_load_dwordx4 v240, s[24:27], s66 offen offset:0 lds
s_add_u32 s66, s66, 9216
TR a[182:183],v232 offset:46592
CV v29,v58,v59
CV v30,v60,v61
CV v31,v62,v63
s_nop 1
v_smfmac_f32_16x16x64_bf16 v[156:159], v[236:239], v[24:31], v234
TR a[184:185],v233 offset:34304
TR a[186:187],v233 offset:38400
TR a[188:189],v233 offset:42496
TR a[190:191],v233 offset:46592
s_waitcnt lgkmcnt(0)
buffer_load_dwordx4 v240, s[24:27], s66 offen offset:1024 lds
s_add_u32 s66, s66, 9216
buffer_load_dwordx4 v240, s[24:27], s66 offen offset:2048 lds
s_add_u32 s66, s66, 9216
buffer_load_dwordx4 v240, s[24:27], s66 offen offset:3072 lds
s_nop 6
v_max3_f32 v249, v152, v153, v156
v_max_f32 v249, v249, v157
v_cmp_lt_f32 vcc, s71, v249
s_cbranch_vccnz LSITE38_%=
LBACK38_%=:
v_add_f32 v246, v152, v153
v_add_f32 v247, v156, v157
s_waitcnt lgkmcnt(0)
MM a[16:19],a[128:131],v[16:19],a[16:19]
s_barrier
MM a[16:19],a[132:135],v[20:23],a[16:19]
buffer_load_dwordx4 a[12:15], v248, s[20:23], s92 offen offset:0 sc1
MM a[24:27],a[132:135],v[28:31],a[24:27]
v_rcp_f32 v153, v246
MM a[24:27],a[128:131],v[24:27],a[24:27]
v_rcp_f32 v157, v247
MM a[28:31],a[136:139],v[24:27],a[28:31]
v_bfe_u32 v152, v241, 16, 8
v_lshl_or_b32 v152, v152, 1, 1
MM a[28:31],a[140:143],v[28:31],a[28:31]
buffer_load_dwordx4 a[44:47], v248, s[20:23], s92 offen offset:1024 sc1
MM a[20:23],a[140:143],v[20:23],a[20:23]
v_lshlrev_b32 v152, 22, v152
v_mul_f32 v152, -v152, v153
MM a[20:23],a[136:139],v[16:19],a[20:23]
v_bfe_u32 v156, v241, 24, 8
v_lshl_or_b32 v156, v156, 1, 1
MM a[48:51],a[144:147],v[16:19],a[48:51]
TR a[128:129],v244 offset:0
TR a[130:131],v244 offset:1024
MM a[48:51],a[148:151],v[20:23],a[48:51]
buffer_load_dwordx4 a[76:79], v248, s[20:23], s92 offen offset:2048 sc1
MM a[56:59],a[148:151],v[28:31],a[56:59]
TR a[132:133],v244 offset:2048
TR a[134:135],v244 offset:3072
MM a[56:59],a[144:147],v[24:27],a[56:59]
v_lshlrev_b32 v156, 22, v156
v_mul_f32 v156, -v156, v157
MM a[60:63],a[152:155],v[24:27],a[60:63]
s_mul_i32 s78, s49, 17408
TR a[136:137],v244 offset:4096
TR a[138:139],v244 offset:5120
MM a[60:63],a[156:159],v[28:31],a[60:63]
s_add_u32 s78, s83, s78
buffer_load_dwordx4 a[108:111], v248, s[20:23], s92 offen offset:3072 sc1
MM a[52:55],a[156:159],v[20:23],a[52:55]
s_add_u32 s78, s78, 8192
TR a[140:141],v244 offset:6144
TR a[142:143],v244 offset:7168
MM a[52:55],a[152:155],v[16:19],a[52:55]
s_add_u32 s79, s78, 4096
AR v80,a16
AR v81,a17
MM a[80:83],a[160:163],v[16:19],a[80:83]
TR a[144:145],v235 offset:0
TR a[146:147],v235 offset:1024
MM a[80:83],a[164:167],v[20:23],a[80:83]
AR v82,a18
AR v83,a19
MM a[88:91],a[164:167],v[28:31],a[88:91]
TR a[148:149],v235 offset:2048
TR a[150:151],v235 offset:3072
MM a[88:91],a[160:163],v[24:27],a[88:91]
AR v84,a20
AR v85,a21
MM a[92:95],a[168:171],v[24:27],a[92:95]
TR a[152:153],v235 offset:4096
TR a[154:155],v235 offset:5120
MM a[92:95],a[172:175],v[28:31],a[92:95]
s_xor_b32 s66, s94, 7
AR v86,a22
AR v87,a23
MM a[84:87],a[172:175],v[20:23],a[84:87]
s_mul_i32 s66, s66, 163840
TR a[156:157],v235 offset:6144
TR a[158:159],v235 offset:7168
v_fma_f32 v80, v80, v153, v152
MM a[84:87],a[168:171],v[16:19],a[84:87]
s_add_u32 s66, s66, s87
v_fma_f32 v81, v81, v153, v152
v_fma_f32 v82, v82, v153, v152
MM a[112:115],a[176:179],v[16:19],a[112:115]
s_add_u32 m0, s65, 98304
TR a[160:161],v144 offset:49152
TR a[162:163],v144 offset:51200
v_fma_f32 v83, v83, v153, v152
MM a[112:115],a[180:183],v[20:23],a[112:115]
buffer_load_dwordx4 v240, s[24:27], s66 offen offset:0 lds
v_fma_f32 v84, v84, v153, v152
v_fma_f32 v85, v85, v153, v152
MM a[120:123],a[180:183],v[28:31],a[120:123]
s_add_u32 s66, s66, 9216
TR a[164:165],v144 offset:53248
TR a[166:167],v144 offset:55296
v_fma_f32 v86, v86, v153, v152
v_fma_f32 v87, v87, v153, v152
MM a[120:123],a[176:179],v[24:27],a[120:123]
CV v168,v80,v81
CV v169,v82,v83
MM a[124:127],a[184:187],v[24:27],a[124:127]
TR a[168:169],v144 offset:57344
TR a[170:171],v144 offset:59392
CV v170,v84,v85
MM a[124:127],a[188:191],v[28:31],a[124:127]
buffer_load_dwordx4 v240, s[24:27], s66 offen offset:1024 lds
CV v171,v86,v87
MM a[116:119],a[188:191],v[20:23],a[116:119]
LPG26_%=:
s_cbranch_execz LPG30_%=
s_add_u32 s66, s66, 9216
TR a[172:173],v144 offset:61440
TR a[174:175],v144 offset:63488
MM a[116:119],a[184:187],v[16:19],a[116:119]
buffer_store_dwordx4 v[168:171], v248, s[20:23], s78 offen offset:0 sc1
AR v88,a24
AR v89,a25
s_waitcnt vmcnt(23)
s_waitcnt lgkmcnt(15)
MM v[128:131],a[128:131],a[0:3],0
TR a[176:177],v145 offset:49152
TR a[178:179],v145 offset:51200
AR v90,a26
AR v91,a27
MM v[128:131],a[132:135],a[32:35],v[128:131]
v_bfe_u32 v249, v248, 6, 2
v_lshlrev_b32 v249, 8, v249
AR v92,a28
AR v93,a29
MM v[128:131],a[136:139],a[64:67],v[128:131]
TR a[180:181],v145 offset:53248
TR a[182:183],v145 offset:55296
AR v94,a30
MM v[128:131],a[140:143],a[96:99],v[128:131]
s_waitcnt vmcnt(19)
v_lshrrev_b32 v250, 8, v248
v_lshl_add_u32 v249, v250, 6, v249
AR v95,a31
v_fma_f32 v88, v88, v157, v156
MM v[136:139],a[140:143],a[100:103],0
TR a[184:185],v145 offset:57344
TR a[186:187],v145 offset:59392
v_fma_f32 v89, v89, v157, v156
v_fma_f32 v90, v90, v157, v156
MM v[136:139],a[136:139],a[68:71],v[136:139]
buffer_load_dwordx4 v240, s[24:27], s66 offen offset:2048 lds
v_and_b32 v251, 48, v248
v_add_u32 v249, v249, v251
v_fma_f32 v91, v91, v157, v156
MM v[136:139],a[132:135],a[36:39],v[136:139]
s_add_u32 s66, s66, 9216
TR a[188:189],v145 offset:61440
TR a[190:191],v145 offset:63488
v_fma_f32 v92, v92, v157, v156
v_fma_f32 v93, v93, v157, v156
MM v[136:139],a[128:131],a[4:7],v[136:139]
s_waitcnt lgkmcnt(15)
v_fma_f32 v94, v94, v157, v156
v_fma_f32 v95, v95, v157, v156
MM v[140:143],a[144:147],a[4:7],0
s_lshr_b32 s60, s49, 3
TR a[128:129],v146 offset:49152
TR a[130:131],v146 offset:51200
CV v172,v88,v89
CV v173,v90,v91
MM v[140:143],a[148:151],a[36:39],v[140:143]
buffer_load_dwordx4 v240, s[24:27], s66 offen offset:3072 lds
CV v174,v92,v93
MM v[140:143],a[152:155],a[68:71],v[140:143]
TR a[132:133],v146 offset:53248
TR a[134:135],v146 offset:55296
CV v175,v94,v95
MM v[140:143],a[156:159],a[100:103],v[140:143]
s_xor_b32 s66, s94, 7
buffer_store_dwordx4 v[172:175], v248, s[20:23], s79 offen offset:0 sc1
MM v[132:135],a[156:159],a[96:99],0
s_mul_i32 s66, s66, 163840
TR a[136:137],v146 offset:57344
TR a[138:139],v146 offset:59392
AR v112,a48
AR v113,a49
MM v[132:135],a[152:155],a[64:67],v[132:135]
s_add_u32 s66, s66, s86
AR v114,a50
AR v115,a51
MM v[132:135],a[148:151],a[32:35],v[132:135]
s_add_u32 m0, s65, 32768
TR a[140:141],v146 offset:61440
TR a[142:143],v146 offset:63488
AR v116,a52
MM v[132:135],a[144:147],a[0:3],v[132:135]
s_waitcnt lgkmcnt(15)
buffer_load_dwordx4 v240, s[24:27], s66 offen offset:0 lds
AR v117,a53
AR v118,a54
MM v[64:67],a[160:163],a[0:3],0
s_xor_b32 s92, s49, 6
TR a[144:145],v147 offset:49152
TR a[146:147],v147 offset:51200
AR v119,a55
v_fma_f32 v112, v112, v153, v152
MM v[64:67],a[164:167],a[32:35],v[64:67]
s_mul_i32 s92, s92, 17408
s_add_u32 s92, s83, s92
v_fma_f32 v113, v113, v153, v152
v_fma_f32 v114, v114, v153, v152
MM v[64:67],a[168:171],a[64:67],v[64:67]
s_add_u32 s66, s66, 9216
TR a[148:149],v147 offset:53248
TR a[150:151],v147 offset:55296
v_fma_f32 v115, v115, v153, v152
MM v[64:67],a[172:175],a[96:99],v[64:67]
buffer_load_dwordx4 v[160:163], v248, s[20:23], s92 offen offset:0 sc1
v_fma_f32 v116, v116, v153, v152
v_fma_f32 v117, v117, v153, v152
MM v[72:75],a[172:175],a[100:103],0
TR a[152:153],v147 offset:57344
TR a[154:155],v147 offset:59392
v_fma_f32 v118, v118, v153, v152
v_fma_f32 v119, v119, v153, v152
MM v[72:75],a[168:171],a[68:71],v[72:75]
CV v184,v112,v113
CV v185,v114,v115
MM v[72:75],a[164:167],a[36:39],v[72:75]
TR a[156:157],v147 offset:61440
TR a[158:159],v147 offset:63488
CV v186,v116,v117
MM v[72:75],a[160:163],a[4:7],v[72:75]
s_waitcnt lgkmcnt(15)
buffer_load_dwordx4 v[176:179], v248, s[20:23], s92 offen offset:1024 sc1
CV v187,v118,v119
MM v[76:79],a[176:179],a[4:7],0
TR a[160:161],v228 offset:49152
TR a[162:163],v228 offset:51200
MM v[76:79],a[180:183],a[36:39],v[76:79]
v_xor_b32 v253, s60, v250
v_and_b32 v238, 240, v248
MM v[76:79],a[184:187],a[68:71],v[76:79]
TR a[164:165],v228 offset:53248
TR a[166:167],v228 offset:55296
MM v[76:79],a[188:191],a[100:103],v[76:79]
buffer_store_dwordx4 v[184:187], v248, s[20:23], s78 offen offset:1024 sc1
AR v120,a56
AR v121,a57
AR v122,a58
AR v123,a59
AR v124,a60
AR v125,a61
MM v[68:71],a[188:191],a[96:99],0
TR a[168:169],v228 offset:57344
TR a[170:171],v228 offset:59392
AR v126,a62
MM v[68:71],a[184:187],a[64:67],v[68:71]
AR v127,a63
v_fma_f32 v120, v120, v157, v156
MM v[68:71],a[180:183],a[32:35],v[68:71]
TR a[172:173],v228 offset:61440
TR a[174:175],v228 offset:63488
v_fma_f32 v121, v121, v157, v156
v_fma_f32 v122, v122, v157, v156
MM v[68:71],a[176:179],a[0:3],v[68:71]
s_waitcnt lgkmcnt(15)
buffer_load_dwordx4 v[192:195], v248, s[20:23], s92 offen offset:2048 sc1
v_fma_f32 v123, v123, v157, v156
MM v[96:99],a[128:131],a[0:3],0
TR a[176:177],v229 offset:49152
TR a[178:179],v229 offset:51200
v_fma_f32 v124, v124, v157, v156
v_fma_f32 v125, v125, v157, v156
MM v[96:99],a[132:135],a[32:35],v[96:99]
v_fma_f32 v126, v126, v157, v156
v_fma_f32 v127, v127, v157, v156
MM v[96:99],a[136:139],a[64:67],v[96:99]
TR a[180:181],v229 offset:53248
TR a[182:183],v229 offset:55296
CV v188,v120,v121
MM v[96:99],a[140:143],a[96:99],v[96:99]
buffer_load_dwordx4 v[208:211], v248, s[20:23], s92 offen offset:3072 sc1
CV v189,v122,v123
CV v190,v124,v125
MM v[104:107],a[140:143],a[100:103],0
TR a[184:185],v229 offset:57344
TR a[186:187],v229 offset:59392
CV v191,v126,v127
MM v[104:107],a[136:139],a[68:71],v[104:107]
s_add_u32 s92, s92, 4096
v_lshl_add_u32 v232, v250, 2, v238
s_add_u32 s61, s65, 131072
MM v[104:107],a[132:135],a[36:39],v[104:107]
TR a[188:189],v229 offset:61440
TR a[190:191],v229 offset:63488
MM v[104:107],a[128:131],a[4:7],v[104:107]
s_waitcnt lgkmcnt(15)
buffer_store_dwordx4 v[188:191], v248, s[20:23], s79 offen offset:1024 sc1
AR v80,a80
AR v81,a81
AR v82,a82
AR v83,a83
MM v[108:111],a[144:147],a[4:7],0
TR a[128:129],v230 offset:49152
TR a[130:131],v230 offset:51200
AR v84,a84
MM v[108:111],a[148:151],a[36:39],v[108:111]
AR v85,a85
AR v86,a86
)ASM"
R"ASM(
MM v[108:111],a[152:155],a[68:71],v[108:111]
TR a[132:133],v230 offset:53248
TR a[134:135],v230 offset:55296
AR v87,a87
v_fma_f32 v80, v80, v153, v152
MM v[108:111],a[156:159],a[100:103],v[108:111]
buffer_load_dwordx4 v[164:167], v248, s[20:23], s92 offen offset:0 sc1
v_fma_f32 v81, v81, v153, v152
v_fma_f32 v82, v82, v153, v152
MM v[100:103],a[156:159],a[96:99],0
TR a[136:137],v230 offset:57344
TR a[138:139],v230 offset:59392
v_fma_f32 v83, v83, v153, v152
MM v[100:103],a[152:155],a[64:67],v[100:103]
v_fma_f32 v84, v84, v153, v152
v_fma_f32 v85, v85, v153, v152
MM v[100:103],a[148:151],a[32:35],v[100:103]
TR a[140:141],v230 offset:61440
TR a[142:143],v230 offset:63488
v_fma_f32 v86, v86, v153, v152
v_fma_f32 v87, v87, v153, v152
MM v[100:103],a[144:147],a[0:3],v[100:103]
s_waitcnt lgkmcnt(15)
buffer_load_dwordx4 v[180:183], v248, s[20:23], s92 offen offset:1024 sc1
CV v200,v80,v81
MM v[0:3],a[160:163],a[0:3],0
TR a[144:145],v231 offset:49152
TR a[146:147],v231 offset:51200
CV v201,v82,v83
CV v202,v84,v85
MM v[0:3],a[164:167],a[32:35],v[0:3]
CV v203,v86,v87
v_mul_u32_u24 v224, 5, v238
MM v[0:3],a[168:171],a[64:67],v[0:3]
TR a[148:149],v231 offset:53248
TR a[150:151],v231 offset:55296
MM v[0:3],a[172:175],a[96:99],v[0:3]
buffer_store_dwordx4 v[200:203], v248, s[20:23], s78 offen offset:2048 sc1
AR v88,a88
AR v89,a89
MM v[8:11],a[172:175],a[100:103],0
TR a[152:153],v231 offset:57344
TR a[154:155],v231 offset:59392
AR v90,a90
AR v91,a91
MM v[8:11],a[168:171],a[68:71],v[8:11]
AR v92,a92
AR v93,a93
MM v[8:11],a[164:167],a[36:39],v[8:11]
TR a[156:157],v231 offset:61440
TR a[158:159],v231 offset:63488
AR v94,a94
MM v[8:11],a[160:163],a[4:7],v[8:11]
s_waitcnt lgkmcnt(15)
buffer_load_dwordx4 v[196:199], v248, s[20:23], s92 offen offset:2048 sc1
AR v95,a95
v_fma_f32 v88, v88, v157, v156
s_barrier
MM v[12:15],a[176:179],a[4:7],0
TR a[160:161],v244 offset:8192
TR a[162:163],v244 offset:9216
v_fma_f32 v89, v89, v157, v156
v_fma_f32 v90, v90, v157, v156
MM v[12:15],a[180:183],a[36:39],v[12:15]
v_fma_f32 v91, v91, v157, v156
v_fma_f32 v92, v92, v157, v156
MM v[12:15],a[184:187],a[68:71],v[12:15]
TR a[164:165],v244 offset:10240
TR a[166:167],v244 offset:11264
v_fma_f32 v93, v93, v157, v156
MM v[12:15],a[188:191],a[100:103],v[12:15]
buffer_load_dwordx4 v[212:215], v248, s[20:23], s92 offen offset:3072 sc1
v_fma_f32 v94, v94, v157, v156
v_fma_f32 v95, v95, v157, v156
MM v[4:7],a[188:191],a[96:99],0
TR a[168:169],v244 offset:12288
TR a[170:171],v244 offset:13312
CV v204,v88,v89
MM v[4:7],a[184:187],a[64:67],v[4:7]
CV v205,v90,v91
CV v206,v92,v93
MM v[4:7],a[180:183],a[32:35],v[4:7]
TR a[172:173],v244 offset:14336
TR a[174:175],v244 offset:15360
CV v207,v94,v95
MM v[4:7],a[176:179],a[0:3],v[4:7]
s_waitcnt lgkmcnt(15)
buffer_store_dwordx4 v[204:207], v248, s[20:23], s79 offen offset:2048 sc1
MM v[32:35],a[128:131],a[0:3],0
TR a[176:177],v235 offset:8192
TR a[178:179],v235 offset:9216
AR v112,a112
AR v113,a113
MM v[32:35],a[132:135],a[32:35],v[32:35]
AR v114,a114
AR v115,a115
MM v[32:35],a[136:139],a[64:67],v[32:35]
TR a[180:181],v235 offset:10240
TR a[182:183],v235 offset:11264
AR v116,a116
MM v[32:35],a[140:143],a[96:99],v[32:35]
buffer_load_dwordx4 v240, s[24:27], s66 offen offset:1024 lds
AR v117,a117
AR v118,a118
MM v[40:43],a[140:143],a[100:103],0
s_add_u32 s66, s66, 9216
TR a[184:185],v235 offset:12288
TR a[186:187],v235 offset:13312
AR v119,a119
v_fma_f32 v112, v112, v153, v152
MM v[40:43],a[136:139],a[68:71],v[40:43]
v_fma_f32 v113, v113, v153, v152
v_fma_f32 v114, v114, v153, v152
MM v[40:43],a[132:135],a[36:39],v[40:43]
TR a[188:189],v235 offset:14336
TR a[190:191],v235 offset:15360
v_fma_f32 v115, v115, v153, v152
MM v[40:43],a[128:131],a[4:7],v[40:43]
s_waitcnt lgkmcnt(15)
buffer_load_dwordx4 v240, s[24:27], s66 offen offset:2048 lds
v_fma_f32 v116, v116, v153, v152
v_fma_f32 v117, v117, v153, v152
MM v[44:47],a[144:147],a[4:7],0
s_add_u32 s66, s66, 9216
TR a[128:129],v144 offset:0
TR a[130:131],v144 offset:2048
v_fma_f32 v118, v118, v153, v152
v_fma_f32 v119, v119, v153, v152
MM v[44:47],a[148:151],a[36:39],v[44:47]
CV v216,v112,v113
CV v217,v114,v115
MM v[44:47],a[152:155],a[68:71],v[44:47]
TR a[132:133],v144 offset:4096
TR a[134:135],v144 offset:6144
CV v218,v116,v117
MM v[44:47],a[156:159],a[100:103],v[44:47]
buffer_load_dwordx4 v240, s[24:27], s66 offen offset:3072 lds
CV v219,v118,v119
MM v[36:39],a[156:159],a[96:99],0
TR a[136:137],v144 offset:8192
TR a[138:139],v144 offset:10240
MM v[36:39],a[152:155],a[64:67],v[36:39]
s_xor_b32 s66, s94, 0
v_lshl_add_u32 v224, v250, 3, v224
v_add_u32 v224, s61, v224
MM v[36:39],a[148:151],a[32:35],v[36:39]
s_mul_i32 s66, s66, 163840
TR a[140:141],v144 offset:12288
TR a[142:143],v144 offset:14336
MM v[36:39],a[144:147],a[0:3],v[36:39]
s_waitcnt lgkmcnt(15)
buffer_store_dwordx4 v[216:219], v248, s[20:23], s78 offen offset:3072 sc1
AR v120,a120
AR v121,a121
AR v122,a122
AR v123,a123
AR v124,a124
s_waitcnt vmcnt(31)
MM v[128:131],a[160:163],a[8:11],v[128:131]
s_add_u32 s66, s66, s88
TR a[144:145],v145 offset:0
TR a[146:147],v145 offset:2048
AR v125,a125
AR v126,a126
MM v[128:131],a[164:167],a[40:43],v[128:131]
s_add_u32 m0, s89, 131072
AR v127,a127
v_fma_f32 v120, v120, v157, v156
MM v[128:131],a[168:171],a[72:75],v[128:131]
TR a[148:149],v145 offset:4096
TR a[150:151],v145 offset:6144
v_fma_f32 v121, v121, v157, v156
v_fma_f32 v122, v122, v157, v156
MM v[128:131],a[172:175],a[104:107],v[128:131]
s_waitcnt vmcnt(23)
buffer_load_dwordx4 v252, s[24:27], s66 offen offset:0 lds
v_fma_f32 v123, v123, v157, v156
MM v[136:139],a[172:175],a[108:111],v[136:139]
s_add_u32 s66, s66, 19456
TR a[152:153],v145 offset:8192
TR a[154:155],v145 offset:10240
v_fma_f32 v124, v124, v157, v156
v_fma_f32 v125, v125, v157, v156
MM v[136:139],a[168:171],a[76:79],v[136:139]
v_fma_f32 v126, v126, v157, v156
v_fma_f32 v127, v127, v157, v156
MM v[136:139],a[164:167],a[44:47],v[136:139]
TR a[156:157],v145 offset:12288
TR a[158:159],v145 offset:14336
CV v220,v120,v121
MM v[136:139],a[160:163],a[12:15],v[136:139]
s_waitcnt lgkmcnt(15)
buffer_load_dwordx4 v252, s[24:27], s66 offen offset:1024 lds
CV v221,v122,v123
CV v222,v124,v125
MM v[140:143],a[176:179],a[12:15],v[140:143]
TR a[160:161],v146 offset:0
TR a[162:163],v146 offset:2048
CV v223,v126,v127
MM v[140:143],a[180:183],a[44:47],v[140:143]
s_xor_b32 s66, s94, 0
v_mul_u32_u24 v254, 1280, v250
v_mul_u32_u24 v250, 33792, v253
MM v[140:143],a[184:187],a[76:79],v[140:143]
s_mul_i32 s66, s66, 163840
TR a[164:165],v146 offset:4096
TR a[166:167],v146 offset:6144
MM v[140:143],a[188:191],a[108:111],v[140:143]
s_add_u32 s66, s66, s87
buffer_store_dwordx4 v[220:223], v248, s[20:23], s79 offen offset:3072 sc1
MM v[132:135],a[188:191],a[104:107],v[132:135]
s_add_u32 m0, s65, 114688
TR a[168:169],v146 offset:8192
TR a[170:171],v146 offset:10240
MM v[132:135],a[184:187],a[72:75],v[132:135]
v_add_u32 v250, v250, v238
v_lshlrev_b32 v253, 4, v253
MM v[132:135],a[180:183],a[40:43],v[132:135]
TR a[172:173],v146 offset:12288
TR a[174:175],v146 offset:14336
MM v[132:135],a[176:179],a[8:11],v[132:135]
s_waitcnt lgkmcnt(15)
buffer_load_dwordx4 v240, s[24:27], s66 offen offset:0 lds
MM v[64:67],a[128:131],a[8:11],v[64:67]
s_xor_b32 s92, s49, 7
TR a[176:177],v147 offset:0
TR a[178:179],v147 offset:2048
MM v[64:67],a[132:135],a[40:43],v[64:67]
s_mul_i32 s92, s92, 17408
s_add_u32 s92, s83, s92
v_bfe_u32 v238, v248, 4, 3
MM v[64:67],a[136:139],a[72:75],v[64:67]
s_add_u32 s66, s66, 9216
TR a[180:181],v147 offset:4096
TR a[182:183],v147 offset:6144
MM v[64:67],a[140:143],a[104:107],v[64:67]
buffer_load_dwordx4 a[192:195], v248, s[20:23], s92 offen offset:0 sc1
MM v[72:75],a[140:143],a[108:111],v[72:75]
TR a[184:185],v147 offset:8192
TR a[186:187],v147 offset:10240
MM v[72:75],a[136:139],a[76:79],v[72:75]
v_lshl_add_u32 v254, v238, 3, v254
v_bfe_u32 v238, v248, 7, 1
MM v[72:75],a[132:135],a[44:47],v[72:75]
TR a[188:189],v147 offset:12288
TR a[190:191],v147 offset:14336
MM v[72:75],a[128:131],a[12:15],v[72:75]
s_waitcnt lgkmcnt(15)
buffer_load_dwordx4 a[208:211], v248, s[20:23], s92 offen offset:1024 sc1
MM v[76:79],a[144:147],a[12:15],v[76:79]
TR a[128:129],v228 offset:0
TR a[130:131],v228 offset:2048
MM v[76:79],a[148:151],a[44:47],v[76:79]
v_mul_u32_u24 v238, 5120, v238
v_add_u32 v254, v254, v238
MM v[76:79],a[152:155],a[76:79],v[76:79]
TR a[132:133],v228 offset:4096
TR a[134:135],v228 offset:6144
MM v[76:79],a[156:159],a[108:111],v[76:79]
buffer_load_dwordx4 a[224:227], v248, s[20:23], s92 offen offset:2048 sc1
MM v[68:71],a[156:159],a[104:107],v[68:71]
TR a[136:137],v228 offset:8192
TR a[138:139],v228 offset:10240
MM v[68:71],a[152:155],a[72:75],v[68:71]
v_lshrrev_b32 v238, 6, v248
v_mul_u32_u24 v225, 80, v238
MM v[68:71],a[148:151],a[40:43],v[68:71]
TR a[140:141],v228 offset:12288
TR a[142:143],v228 offset:14336
MM v[68:71],a[144:147],a[8:11],v[68:71]
s_waitcnt lgkmcnt(15)
buffer_load_dwordx4 a[240:243], v248, s[20:23], s92 offen offset:3072 sc1
MM v[96:99],a[160:163],a[8:11],v[96:99]
TR a[144:145],v229 offset:0
TR a[146:147],v229 offset:2048
MM v[96:99],a[164:167],a[40:43],v[96:99]
s_add_u32 s92, s92, 4096
v_add3_u32 v225, v225, v251, s61
v_mul_u32_u24 v238, 1280, v238
MM v[96:99],a[168:171],a[72:75],v[96:99]
TR a[148:149],v229 offset:4096
TR a[150:151],v229 offset:6144
MM v[96:99],a[172:175],a[104:107],v[96:99]
buffer_load_dwordx4 a[196:199], v248, s[20:23], s92 offen offset:0 sc1
MM v[104:107],a[172:175],a[108:111],v[104:107]
TR a[152:153],v229 offset:8192
TR a[154:155],v229 offset:10240
MM v[104:107],a[168:171],a[76:79],v[104:107]
v_add_u32 v251, v251, v238
MM v[104:107],a[164:167],a[44:47],v[104:107]
TR a[156:157],v229 offset:12288
TR a[158:159],v229 offset:14336
MM v[104:107],a[160:163],a[12:15],v[104:107]
s_waitcnt lgkmcnt(15)
buffer_load_dwordx4 a[212:215], v248, s[20:23], s92 offen offset:1024 sc1
MM v[108:111],a[176:179],a[12:15],v[108:111]
TR a[160:161],v230 offset:0
TR a[162:163],v230 offset:2048
MM v[108:111],a[180:183],a[44:47],v[108:111]
MM v[108:111],a[184:187],a[76:79],v[108:111]
TR a[164:165],v230 offset:4096
TR a[166:167],v230 offset:6144
MM v[108:111],a[188:191],a[108:111],v[108:111]
buffer_load_dwordx4 a[228:231], v248, s[20:23], s92 offen offset:2048 sc1
MM v[100:103],a[188:191],a[104:107],v[100:103]
TR a[168:169],v230 offset:8192
TR a[170:171],v230 offset:10240
MM v[100:103],a[184:187],a[72:75],v[100:103]
MM v[100:103],a[180:183],a[40:43],v[100:103]
TR a[172:173],v230 offset:12288
TR a[174:175],v230 offset:14336
MM v[100:103],a[176:179],a[8:11],v[100:103]
s_waitcnt lgkmcnt(15)
buffer_load_dwordx4 a[244:247], v248, s[20:23], s92 offen offset:3072 sc1
MM v[0:3],a[128:131],a[8:11],v[0:3]
TR a[176:177],v231 offset:0
TR a[178:179],v231 offset:2048
MM v[0:3],a[132:135],a[40:43],v[0:3]
MM v[0:3],a[136:139],a[72:75],v[0:3]
TR a[180:181],v231 offset:4096
TR a[182:183],v231 offset:6144
MM v[0:3],a[140:143],a[104:107],v[0:3]
buffer_load_dwordx4 v240, s[24:27], s66 offen offset:1024 lds
MM v[8:11],a[140:143],a[108:111],v[8:11]
s_add_u32 s66, s66, 9216
TR a[184:185],v231 offset:8192
TR a[186:187],v231 offset:10240
MM v[8:11],a[136:139],a[76:79],v[8:11]
MM v[8:11],a[132:135],a[44:47],v[8:11]
TR a[188:189],v231 offset:12288
TR a[190:191],v231 offset:14336
MM v[8:11],a[128:131],a[12:15],v[8:11]
s_waitcnt lgkmcnt(15)
buffer_load_dwordx4 v240, s[24:27], s66 offen offset:2048 lds
s_barrier
MM v[12:15],a[144:147],a[12:15],v[12:15]
s_add_u32 s66, s66, 9216
TR a[128:129],v244 offset:16384
TR a[130:131],v244 offset:17408
MM v[12:15],a[148:151],a[44:47],v[12:15]
LPG27_%=:
s_cbranch_execz LTR1_%=
MM v[12:15],a[152:155],a[76:79],v[12:15]
TR a[132:133],v244 offset:18432
TR a[134:135],v244 offset:19456
MM v[12:15],a[156:159],a[108:111],v[12:15]
buffer_load_dwordx4 v240, s[24:27], s66 offen offset:3072 lds
MM v[4:7],a[156:159],a[104:107],v[4:7]
TR a[136:137],v244 offset:20480
TR a[138:139],v244 offset:21504
MM v[4:7],a[152:155],a[72:75],v[4:7]
s_xor_b32 s66, s94, 0
s_mul_i32 s66, s66, 163840
s_add_u32 s66, s66, s86
MM v[4:7],a[148:151],a[40:43],v[4:7]
)ASM"
R"ASM(
s_add_u32 m0, s65, 49152
TR a[140:141],v244 offset:22528
TR a[142:143],v244 offset:23552
MM v[4:7],a[144:147],a[8:11],v[4:7]
s_waitcnt lgkmcnt(15)
buffer_load_dwordx4 v240, s[24:27], s66 offen offset:0 lds
MM v[32:35],a[160:163],a[8:11],v[32:35]
s_add_u32 s66, s66, 9216
TR a[144:145],v235 offset:16384
TR a[146:147],v235 offset:17408
MM v[32:35],a[164:167],a[40:43],v[32:35]
MM v[32:35],a[168:171],a[72:75],v[32:35]
TR a[148:149],v235 offset:18432
TR a[150:151],v235 offset:19456
MM v[32:35],a[172:175],a[104:107],v[32:35]
buffer_load_dwordx4 v240, s[24:27], s66 offen offset:1024 lds
MM v[40:43],a[172:175],a[108:111],v[40:43]
s_add_u32 s66, s66, 9216
TR a[152:153],v235 offset:20480
TR a[154:155],v235 offset:21504
MM v[40:43],a[168:171],a[76:79],v[40:43]
MM v[40:43],a[164:167],a[44:47],v[40:43]
TR a[156:157],v235 offset:22528
TR a[158:159],v235 offset:23552
MM v[40:43],a[160:163],a[12:15],v[40:43]
s_waitcnt lgkmcnt(15)
buffer_load_dwordx4 v240, s[24:27], s66 offen offset:2048 lds
MM v[44:47],a[176:179],a[12:15],v[44:47]
s_lshl_b32 s97, s84, 2
TR a[160:161],v144 offset:16384
TR a[162:163],v144 offset:18432
MM v[44:47],a[180:183],a[44:47],v[44:47]
s_add_u32 s97, s97, s96
s_add_u32 s97, s97, 16
s_waitcnt vmcnt(15)
MM v[44:47],a[184:187],a[76:79],v[44:47]
s_add_u32 s66, s66, 9216
TR a[164:165],v144 offset:20480
TR a[166:167],v144 offset:22528
MM v[44:47],a[188:191],a[108:111],v[44:47]
v_mov_b32 v242, s69
s_mov_b64 exec, 1
buffer_store_dword v242, off, s[20:23], s97 offset:0 sc1
s_mov_b64 exec, -1
MM v[36:39],a[188:191],a[104:107],v[36:39]
TR a[168:169],v144 offset:24576
TR a[170:171],v144 offset:26624
MM v[36:39],a[184:187],a[72:75],v[36:39]
buffer_load_dwordx4 v240, s[24:27], s66 offen offset:3072 lds
MM v[36:39],a[180:183],a[40:43],v[36:39]
TR a[172:173],v144 offset:28672
TR a[174:175],v144 offset:30720
MM v[36:39],a[176:179],a[8:11],v[36:39]
s_waitcnt lgkmcnt(15)
s_xor_b32 s66, s94, 1
s_mul_i32 s66, s66, 163840
MM v[128:131],a[128:131],v[160:163],v[128:131]
s_add_u32 s66, s66, s88
TR a[176:177],v145 offset:16384
TR a[178:179],v145 offset:18432
MM v[128:131],a[132:135],v[176:179],v[128:131]
s_add_u32 m0, s89, 139264
MM v[128:131],a[136:139],v[192:195],v[128:131]
TR a[180:181],v145 offset:20480
TR a[182:183],v145 offset:22528
MM v[128:131],a[140:143],v[208:211],v[128:131]
buffer_load_dwordx4 v252, s[24:27], s66 offen offset:0 lds
MM v[136:139],a[140:143],v[212:215],v[136:139]
s_add_u32 s66, s66, 19456
TR a[184:185],v145 offset:24576
TR a[186:187],v145 offset:26624
MM v[136:139],a[136:139],v[196:199],v[136:139]
MM v[136:139],a[132:135],v[180:183],v[136:139]
TR a[188:189],v145 offset:28672
TR a[190:191],v145 offset:30720
MM v[136:139],a[128:131],v[164:167],v[136:139]
s_waitcnt lgkmcnt(15)
buffer_load_dwordx4 v252, s[24:27], s66 offen offset:1024 lds
MM v[140:143],a[144:147],v[164:167],v[140:143]
TR a[128:129],v146 offset:16384
TR a[130:131],v146 offset:18432
MM v[140:143],a[148:151],v[180:183],v[140:143]
s_xor_b32 s66, s94, 1
s_mul_i32 s66, s66, 163840
s_add_u32 s66, s66, s87
MM v[140:143],a[152:155],v[196:199],v[140:143]
s_add_u32 m0, s65, 65536
TR a[132:133],v146 offset:20480
TR a[134:135],v146 offset:22528
MM v[140:143],a[156:159],v[212:215],v[140:143]
buffer_load_dwordx4 v240, s[24:27], s66 offen offset:0 lds
MM v[132:135],a[156:159],v[208:211],v[132:135]
s_add_u32 s66, s66, 9216
TR a[136:137],v146 offset:24576
TR a[138:139],v146 offset:26624
MM v[132:135],a[152:155],v[192:195],v[132:135]
MM v[132:135],a[148:151],v[176:179],v[132:135]
TR a[140:141],v146 offset:28672
TR a[142:143],v146 offset:30720
MM v[132:135],a[144:147],v[160:163],v[132:135]
s_waitcnt lgkmcnt(15)
buffer_load_dwordx4 v240, s[24:27], s66 offen offset:1024 lds
MM v[64:67],a[160:163],v[160:163],v[64:67]
s_add_u32 s66, s66, 9216
TR a[144:145],v147 offset:16384
TR a[146:147],v147 offset:18432
MM v[64:67],a[164:167],v[176:179],v[64:67]
MM v[64:67],a[168:171],v[192:195],v[64:67]
TR a[148:149],v147 offset:20480
TR a[150:151],v147 offset:22528
MM v[64:67],a[172:175],v[208:211],v[64:67]
buffer_load_dwordx4 v240, s[24:27], s66 offen offset:2048 lds
MM v[72:75],a[172:175],v[212:215],v[72:75]
s_add_u32 s66, s66, 9216
TR a[152:153],v147 offset:24576
TR a[154:155],v147 offset:26624
MM v[72:75],a[168:171],v[196:199],v[72:75]
MM v[72:75],a[164:167],v[180:183],v[72:75]
TR a[156:157],v147 offset:28672
TR a[158:159],v147 offset:30720
MM v[72:75],a[160:163],v[164:167],v[72:75]
s_waitcnt lgkmcnt(15)
buffer_load_dwordx4 v240, s[24:27], s66 offen offset:3072 lds
MM v[76:79],a[176:179],v[164:167],v[76:79]
TR a[160:161],v228 offset:16384
TR a[162:163],v228 offset:18432
MM v[76:79],a[180:183],v[180:183],v[76:79]
MM v[76:79],a[184:187],v[196:199],v[76:79]
TR a[164:165],v228 offset:20480
TR a[166:167],v228 offset:22528
MM v[76:79],a[188:191],v[212:215],v[76:79]
MM v[68:71],a[188:191],v[208:211],v[68:71]
TR a[168:169],v228 offset:24576
TR a[170:171],v228 offset:26624
MM v[68:71],a[184:187],v[192:195],v[68:71]
MM v[68:71],a[180:183],v[176:179],v[68:71]
TR a[172:173],v228 offset:28672
TR a[174:175],v228 offset:30720
MM v[68:71],a[176:179],v[160:163],v[68:71]
s_waitcnt lgkmcnt(15)
MM v[96:99],a[128:131],v[160:163],v[96:99]
s_add_u32 s80, s18, 131072
TR a[176:177],v229 offset:16384
TR a[178:179],v229 offset:18432
MM v[96:99],a[132:135],v[176:179],v[96:99]
buffer_load_dwordx4 v[16:19], v251, s[20:23], s80 offen offset:0 sc1
MM v[96:99],a[136:139],v[192:195],v[96:99]
TR a[180:181],v229 offset:20480
TR a[182:183],v229 offset:22528
MM v[96:99],a[140:143],v[208:211],v[96:99]
v_mov_b32 v237, 0x3f80
v_and_b32 v236, 16, v248
MM v[104:107],a[140:143],v[212:215],v[104:107]
TR a[184:185],v229 offset:24576
TR a[186:187],v229 offset:26624
MM v[104:107],a[136:139],v[196:199],v[104:107]
s_add_u32 s80, s18, 151552
buffer_load_dwordx4 v[20:23], v251, s[20:23], s80 offen offset:0 sc1
MM v[104:107],a[132:135],v[180:183],v[104:107]
TR a[188:189],v229 offset:28672
TR a[190:191],v229 offset:30720
MM v[104:107],a[128:131],v[164:167],v[104:107]
s_waitcnt lgkmcnt(15)
v_lshlrev_b32 v236, v236, v237
s_mov_b32 s28, 0x30003
MM v[108:111],a[144:147],v[164:167],v[108:111]
s_mov_b32 s29, s28
TR a[128:129],v230 offset:16384
TR a[130:131],v230 offset:18432
MM v[108:111],a[148:151],v[180:183],v[108:111]
s_add_u32 s80, s63, 131072
v_lshrrev_b32 v243, 2, v248
v_min_u32 v243, 12, v243
MM v[108:111],a[152:155],v[196:199],v[108:111]
TR a[132:133],v230 offset:20480
TR a[134:135],v230 offset:22528
MM v[108:111],a[156:159],v[212:215],v[108:111]
buffer_load_dword v242, v243, s[20:23], s96 offen offset:16 sc1
buffer_load_dwordx2 v[150:151], v254, s[20:23], s80 offen offset:0 sc1
MM v[100:103],a[156:159],v[208:211],v[100:103]
TR a[136:137],v230 offset:24576
TR a[138:139],v230 offset:26624
MM v[100:103],a[152:155],v[192:195],v[100:103]
v_cndmask_b32 v48, 0, v236, s[28:29]
s_lshl_b64 s[28:29], s[28:29], 2
MM v[100:103],a[148:151],v[176:179],v[100:103]
TR a[140:141],v230 offset:28672
TR a[142:143],v230 offset:30720
MM v[100:103],a[144:147],v[160:163],v[100:103]
s_waitcnt lgkmcnt(15)
v_cndmask_b32 v49, 0, v236, s[28:29]
s_lshl_b64 s[28:29], s[28:29], 2
MM v[0:3],a[160:163],v[160:163],v[0:3]
TR a[144:145],v231 offset:16384
TR a[146:147],v231 offset:18432
MM v[0:3],a[164:167],v[176:179],v[0:3]
v_cndmask_b32 v50, 0, v236, s[28:29]
s_lshl_b64 s[28:29], s[28:29], 2
MM v[0:3],a[168:171],v[192:195],v[0:3]
TR a[148:149],v231 offset:20480
TR a[150:151],v231 offset:22528
MM v[0:3],a[172:175],v[208:211],v[0:3]
v_cndmask_b32 v51, 0, v236, s[28:29]
s_lshl_b64 s[28:29], s[28:29], 2
MM v[8:11],a[172:175],v[212:215],v[8:11]
TR a[152:153],v231 offset:24576
TR a[154:155],v231 offset:26624
MM v[8:11],a[168:171],v[196:199],v[8:11]
v_cndmask_b32 v52, 0, v236, s[28:29]
s_lshl_b64 s[28:29], s[28:29], 2
MM v[8:11],a[164:167],v[180:183],v[8:11]
TR a[156:157],v231 offset:28672
TR a[158:159],v231 offset:30720
MM v[8:11],a[160:163],v[164:167],v[8:11]
s_waitcnt lgkmcnt(15)
v_cndmask_b32 v53, 0, v236, s[28:29]
s_lshl_b64 s[28:29], s[28:29], 2
s_barrier
MM v[12:15],a[176:179],v[164:167],v[12:15]
s_xor_b32 s66, s94, 2
TR a[160:161],v244 offset:24576
TR a[162:163],v244 offset:25600
MM v[12:15],a[180:183],v[180:183],v[12:15]
s_mul_i32 s66, s66, 163840
v_cndmask_b32 v54, 0, v236, s[28:29]
s_lshl_b64 s[28:29], s[28:29], 2
MM v[12:15],a[184:187],v[196:199],v[12:15]
s_add_u32 s66, s66, s88
TR a[164:165],v244 offset:26624
TR a[166:167],v244 offset:27648
MM v[12:15],a[188:191],v[212:215],v[12:15]
s_add_u32 m0, s89, 147456
v_cndmask_b32 v55, 0, v236, s[28:29]
s_mov_b32 s28, 0x300003
MM v[4:7],a[188:191],v[208:211],v[4:7]
s_mov_b32 s29, 0x30000300
TR a[168:169],v244 offset:28672
TR a[170:171],v244 offset:29696
MM v[4:7],a[184:187],v[192:195],v[4:7]
buffer_load_dwordx4 v252, s[24:27], s66 offen offset:0 lds
MM v[4:7],a[180:183],v[176:179],v[4:7]
s_add_u32 s66, s66, 19456
TR a[172:173],v244 offset:30720
TR a[174:175],v244 offset:31744
MM v[4:7],a[176:179],v[160:163],v[4:7]
s_waitcnt lgkmcnt(15)
v_mov_b32 v56, 0
v_mov_b32 v57, 0
MM v[32:35],a[128:131],v[160:163],v[32:35]
TR a[176:177],v235 offset:24576
TR a[178:179],v235 offset:25600
MM v[32:35],a[132:135],v[176:179],v[32:35]
buffer_load_dwordx4 v252, s[24:27], s66 offen offset:1024 lds
MM v[32:35],a[136:139],v[192:195],v[32:35]
TR a[180:181],v235 offset:26624
TR a[182:183],v235 offset:27648
MM v[32:35],a[140:143],v[208:211],v[32:35]
s_xor_b32 s66, s94, 2
v_mov_b32 v60, 0
v_mov_b32 v61, 0
MM v[40:43],a[140:143],v[212:215],v[40:43]
s_mul_i32 s66, s66, 163840
TR a[184:185],v235 offset:28672
TR a[186:187],v235 offset:29696
MM v[40:43],a[136:139],v[196:199],v[40:43]
s_add_u32 s66, s66, s87
v_cndmask_b32 v58, 0, v236, s[28:29]
s_lshl_b64 s[28:29], s[28:29], 2
MM v[40:43],a[132:135],v[180:183],v[40:43]
s_add_u32 m0, s65, 81920
TR a[188:189],v235 offset:30720
TR a[190:191],v235 offset:31744
MM v[40:43],a[128:131],v[164:167],v[40:43]
s_waitcnt lgkmcnt(15)
buffer_load_dwordx4 v240, s[24:27], s66 offen offset:0 lds
MM v[44:47],a[144:147],v[164:167],v[44:47]
s_add_u32 s66, s66, 9216
TR a[128:129],v144 offset:32768
TR a[130:131],v144 offset:34816
MM v[44:47],a[148:151],v[180:183],v[44:47]
v_cndmask_b32 v59, 0, v236, s[28:29]
MM v[44:47],a[152:155],v[196:199],v[44:47]
TR a[132:133],v144 offset:36864
TR a[134:135],v144 offset:38912
MM v[44:47],a[156:159],v[212:215],v[44:47]
buffer_load_dwordx4 v240, s[24:27], s66 offen offset:1024 lds
MM v[36:39],a[156:159],v[208:211],v[36:39]
s_add_u32 s66, s66, 9216
TR a[136:137],v144 offset:40960
TR a[138:139],v144 offset:43008
MM v[36:39],a[152:155],v[192:195],v[36:39]
MM v[36:39],a[148:151],v[176:179],v[36:39]
TR a[140:141],v144 offset:45056
TR a[142:143],v144 offset:47104
MM v[36:39],a[144:147],v[160:163],v[36:39]
s_waitcnt lgkmcnt(15)
buffer_load_dwordx4 v240, s[24:27], s66 offen offset:2048 lds
s_waitcnt vmcnt(27)
MM v[128:131],a[160:163],a[192:195],v[128:131]
s_add_u32 s80, s18, 0
TR a[144:145],v145 offset:32768
TR a[146:147],v145 offset:34816
MM v[128:131],a[164:167],a[208:211],v[128:131]
s_add_u32 s66, s66, 9216
buffer_load_dwordx4 v[24:27], v251, s[32:35], s80 offen offset:0
MM v[128:131],a[168:171],a[224:227],v[128:131]
TR a[148:149],v145 offset:36864
TR a[150:151],v145 offset:38912
MM v[128:131],a[172:175],a[240:243],v[128:131]
s_waitcnt vmcnt(24)
MM v[136:139],a[172:175],a[244:247],v[136:139]
TR a[152:153],v145 offset:40960
TR a[154:155],v145 offset:43008
MM v[136:139],a[168:171],a[228:231],v[136:139]
s_add_u32 s80, s18, 20480
buffer_load_dwordx4 v[28:31], v251, s[32:35], s80 offen offset:0
MM v[136:139],a[164:167],a[212:215],v[136:139]
TR a[156:157],v145 offset:45056
TR a[158:159],v145 offset:47104
MM v[136:139],a[160:163],a[196:199],v[136:139]
s_waitcnt lgkmcnt(15)
MM v[140:143],a[176:179],a[196:199],v[140:143]
TR a[160:161],v146 offset:32768
TR a[162:163],v146 offset:34816
MM v[140:143],a[180:183],a[212:215],v[140:143]
s_add_u32 s80, s63, 0
buffer_load_dwordx2 v[62:63], v254, s[32:35], s80 offen offset:0
MM v[140:143],a[184:187],a[228:231],v[140:143]
TR a[164:165],v146 offset:36864
TR a[166:167],v146 offset:38912
MM v[140:143],a[188:191],a[244:247],v[140:143]
)ASM"
R"ASM(
buffer_load_dwordx4 v240, s[24:27], s66 offen offset:3072 lds
MM v[132:135],a[188:191],a[240:243],v[132:135]
TR a[168:169],v146 offset:40960
TR a[170:171],v146 offset:43008
MM v[132:135],a[184:187],a[224:227],v[132:135]
MM v[132:135],a[180:183],a[208:211],v[132:135]
TR a[172:173],v146 offset:45056
TR a[174:175],v146 offset:47104
MM v[132:135],a[176:179],a[192:195],v[132:135]
s_waitcnt lgkmcnt(15)
MM v[64:67],a[128:131],a[192:195],v[64:67]
s_waitcnt vmcnt(10)
TR a[176:177],v147 offset:32768
TR a[178:179],v147 offset:34816
MM v[64:67],a[132:135],a[208:211],v[64:67]
v_cmp_ne_u32 vcc, s69, v242
s_cbranch_vccnz LRPO1_%=
LRPO1b_%=:
MM v[64:67],a[136:139],a[224:227],v[64:67]
s_xor_b32 s92, s49, 1
TR a[180:181],v147 offset:36864
TR a[182:183],v147 offset:38912
MM v[64:67],a[140:143],a[240:243],v[64:67]
s_mul_i32 s92, s92, 17408
s_add_u32 s92, s83, s92
s_add_u32 s92, s92, 8192
MM v[72:75],a[140:143],a[244:247],v[72:75]
TR a[184:185],v147 offset:40960
TR a[186:187],v147 offset:43008
MM v[72:75],a[136:139],a[228:231],v[72:75]
buffer_load_dwordx4 a[16:19], v248, s[20:23], s92 offen offset:0 sc1
MM v[72:75],a[132:135],a[212:215],v[72:75]
TR a[188:189],v147 offset:45056
TR a[190:191],v147 offset:47104
MM v[72:75],a[128:131],a[196:199],v[72:75]
s_waitcnt lgkmcnt(15)
buffer_load_dwordx4 a[48:51], v248, s[20:23], s92 offen offset:1024 sc1
MM v[76:79],a[144:147],a[196:199],v[76:79]
TR a[128:129],v228 offset:32768
TR a[130:131],v228 offset:34816
MM v[76:79],a[148:151],a[212:215],v[76:79]
MM v[76:79],a[152:155],a[228:231],v[76:79]
TR a[132:133],v228 offset:36864
TR a[134:135],v228 offset:38912
MM v[76:79],a[156:159],a[244:247],v[76:79]
buffer_load_dwordx4 a[80:83], v248, s[20:23], s92 offen offset:2048 sc1
MM v[68:71],a[156:159],a[240:243],v[68:71]
TR a[136:137],v228 offset:40960
TR a[138:139],v228 offset:43008
MM v[68:71],a[152:155],a[224:227],v[68:71]
MM v[68:71],a[148:151],a[208:211],v[68:71]
TR a[140:141],v228 offset:45056
TR a[142:143],v228 offset:47104
MM v[68:71],a[144:147],a[192:195],v[68:71]
s_waitcnt lgkmcnt(15)
buffer_load_dwordx4 a[112:115], v248, s[20:23], s92 offen offset:3072 sc1
MM v[96:99],a[160:163],a[192:195],v[96:99]
s_add_u32 s92, s92, 4096
TR a[144:145],v229 offset:32768
TR a[146:147],v229 offset:34816
MM v[96:99],a[164:167],a[208:211],v[96:99]
MM v[96:99],a[168:171],a[224:227],v[96:99]
TR a[148:149],v229 offset:36864
TR a[150:151],v229 offset:38912
MM v[96:99],a[172:175],a[240:243],v[96:99]
buffer_load_dwordx4 a[20:23], v248, s[20:23], s92 offen offset:0 sc1
MM v[104:107],a[172:175],a[244:247],v[104:107]
TR a[152:153],v229 offset:40960
TR a[154:155],v229 offset:43008
MM v[104:107],a[168:171],a[228:231],v[104:107]
MM v[104:107],a[164:167],a[212:215],v[104:107]
TR a[156:157],v229 offset:45056
TR a[158:159],v229 offset:47104
MM v[104:107],a[160:163],a[196:199],v[104:107]
s_waitcnt lgkmcnt(15)
buffer_load_dwordx4 a[52:55], v248, s[20:23], s92 offen offset:1024 sc1
MM v[108:111],a[176:179],a[196:199],v[108:111]
TR a[160:161],v230 offset:32768
TR a[162:163],v230 offset:34816
MM v[108:111],a[180:183],a[212:215],v[108:111]
MM v[108:111],a[184:187],a[228:231],v[108:111]
TR a[164:165],v230 offset:36864
TR a[166:167],v230 offset:38912
MM v[108:111],a[188:191],a[244:247],v[108:111]
buffer_load_dwordx4 a[84:87], v248, s[20:23], s92 offen offset:2048 sc1
MM v[100:103],a[188:191],a[240:243],v[100:103]
TR a[168:169],v230 offset:40960
TR a[170:171],v230 offset:43008
MM v[100:103],a[184:187],a[224:227],v[100:103]
MM v[100:103],a[180:183],a[208:211],v[100:103]
TR a[172:173],v230 offset:45056
TR a[174:175],v230 offset:47104
MM v[100:103],a[176:179],a[192:195],v[100:103]
s_waitcnt lgkmcnt(15)
buffer_load_dwordx4 a[116:119], v248, s[20:23], s92 offen offset:3072 sc1
MM v[0:3],a[128:131],a[192:195],v[0:3]
TR a[176:177],v231 offset:32768
TR a[178:179],v231 offset:34816
MM v[0:3],a[132:135],a[208:211],v[0:3]
MM v[0:3],a[136:139],a[224:227],v[0:3]
TR a[180:181],v231 offset:36864
TR a[182:183],v231 offset:38912
MM v[0:3],a[140:143],a[240:243],v[0:3]
MM v[8:11],a[140:143],a[244:247],v[8:11]
TR a[184:185],v231 offset:40960
TR a[186:187],v231 offset:43008
MM v[8:11],a[136:139],a[228:231],v[8:11]
MM v[8:11],a[132:135],a[212:215],v[8:11]
TR a[188:189],v231 offset:45056
TR a[190:191],v231 offset:47104
MM v[8:11],a[128:131],a[196:199],v[8:11]
s_waitcnt lgkmcnt(15)
s_barrier
MM v[12:15],a[144:147],a[196:199],v[12:15]
s_xor_b32 s66, s94, 3
TR a[128:129],v244 offset:0
TR a[130:131],v244 offset:1024
MM v[12:15],a[148:151],a[212:215],v[12:15]
s_mul_i32 s66, s66, 163840
s_add_u32 s66, s66, s88
s_add_u32 m0, s89, 155648
MM v[12:15],a[152:155],a[228:231],v[12:15]
TR a[132:133],v244 offset:2048
TR a[134:135],v244 offset:3072
MM v[12:15],a[156:159],a[244:247],v[12:15]
buffer_load_dwordx4 v252, s[24:27], s66 offen offset:0 lds
MM v[4:7],a[156:159],a[240:243],v[4:7]
s_add_u32 s66, s66, 19456
TR a[136:137],v244 offset:4096
TR a[138:139],v244 offset:5120
MM v[4:7],a[152:155],a[224:227],v[4:7]
MM v[4:7],a[148:151],a[208:211],v[4:7]
TR a[140:141],v244 offset:6144
TR a[142:143],v244 offset:7168
MM v[4:7],a[144:147],a[192:195],v[4:7]
s_waitcnt lgkmcnt(15)
buffer_load_dwordx4 v252, s[24:27], s66 offen offset:1024 lds
MM v[32:35],a[160:163],a[192:195],v[32:35]
TR a[144:145],v235 offset:0
TR a[146:147],v235 offset:1024
MM v[32:35],a[164:167],a[208:211],v[32:35]
s_xor_b32 s66, s94, 3
s_mul_i32 s66, s66, 163840
s_add_u32 s66, s66, s87
MM v[32:35],a[168:171],a[224:227],v[32:35]
s_add_u32 m0, s65, 98304
TR a[148:149],v235 offset:2048
TR a[150:151],v235 offset:3072
MM v[32:35],a[172:175],a[240:243],v[32:35]
buffer_load_dwordx4 v240, s[24:27], s66 offen offset:0 lds
MM v[40:43],a[172:175],a[244:247],v[40:43]
s_add_u32 s66, s66, 9216
TR a[152:153],v235 offset:4096
TR a[154:155],v235 offset:5120
MM v[40:43],a[168:171],a[228:231],v[40:43]
MM v[40:43],a[164:167],a[212:215],v[40:43]
TR a[156:157],v235 offset:6144
TR a[158:159],v235 offset:7168
MM v[40:43],a[160:163],a[196:199],v[40:43]
s_waitcnt lgkmcnt(15)
buffer_load_dwordx4 v240, s[24:27], s66 offen offset:1024 lds
MM v[44:47],a[176:179],a[196:199],v[44:47]
s_add_u32 s66, s66, 9216
TR a[160:161],v144 offset:49152
TR a[162:163],v144 offset:51200
MM v[44:47],a[180:183],a[212:215],v[44:47]
MM v[44:47],a[184:187],a[228:231],v[44:47]
TR a[164:165],v144 offset:53248
TR a[166:167],v144 offset:55296
MM v[44:47],a[188:191],a[244:247],v[44:47]
buffer_load_dwordx4 v240, s[24:27], s66 offen offset:2048 lds
MM v[36:39],a[188:191],a[240:243],v[36:39]
s_add_u32 s66, s66, 9216
TR a[168:169],v144 offset:57344
TR a[170:171],v144 offset:59392
MM v[36:39],a[184:187],a[224:227],v[36:39]
MM v[36:39],a[180:183],a[208:211],v[36:39]
TR a[172:173],v144 offset:61440
TR a[174:175],v144 offset:63488
MM v[36:39],a[176:179],a[192:195],v[36:39]
s_waitcnt lgkmcnt(15)
buffer_load_dwordx4 v240, s[24:27], s66 offen offset:3072 lds
MM v[128:131],a[128:131],v[168:171],v[128:131]
TR a[176:177],v145 offset:49152
TR a[178:179],v145 offset:51200
MM v[128:131],a[132:135],v[184:187],v[128:131]
MM v[128:131],a[136:139],v[200:203],v[128:131]
TR a[180:181],v145 offset:53248
TR a[182:183],v145 offset:55296
MM v[128:131],a[140:143],v[216:219],v[128:131]
MM v[136:139],a[140:143],v[220:223],v[136:139]
TR a[184:185],v145 offset:57344
TR a[186:187],v145 offset:59392
MM v[136:139],a[136:139],v[204:207],v[136:139]
MM v[136:139],a[132:135],v[188:191],v[136:139]
TR a[188:189],v145 offset:61440
TR a[190:191],v145 offset:63488
MM v[136:139],a[128:131],v[172:175],v[136:139]
s_waitcnt lgkmcnt(15)
MM v[140:143],a[144:147],v[172:175],v[140:143]
s_xor_b32 s92, s49, 2
TR a[128:129],v146 offset:49152
TR a[130:131],v146 offset:51200
MM v[140:143],a[148:151],v[188:191],v[140:143]
s_mul_i32 s92, s92, 17408
s_add_u32 s92, s83, s92
LPG28_%=:
s_cbranch_execz LTR1_%=
s_add_u32 s92, s92, 8192
MM v[140:143],a[152:155],v[204:207],v[140:143]
TR a[132:133],v146 offset:53248
TR a[134:135],v146 offset:55296
MM v[140:143],a[156:159],v[220:223],v[140:143]
buffer_load_dwordx4 a[24:27], v248, s[20:23], s92 offen offset:0 sc1
MM v[132:135],a[156:159],v[216:219],v[132:135]
TR a[136:137],v146 offset:57344
TR a[138:139],v146 offset:59392
MM v[132:135],a[152:155],v[200:203],v[132:135]
buffer_load_dwordx4 a[56:59], v248, s[20:23], s92 offen offset:1024 sc1
MM v[132:135],a[148:151],v[184:187],v[132:135]
TR a[140:141],v146 offset:61440
TR a[142:143],v146 offset:63488
MM v[132:135],a[144:147],v[168:171],v[132:135]
s_waitcnt lgkmcnt(15)
MM v[64:67],a[160:163],v[168:171],v[64:67]
TR a[144:145],v147 offset:49152
TR a[146:147],v147 offset:51200
MM v[64:67],a[164:167],v[184:187],v[64:67]
buffer_load_dwordx4 a[88:91], v248, s[20:23], s92 offen offset:2048 sc1
MM v[64:67],a[168:171],v[200:203],v[64:67]
TR a[148:149],v147 offset:53248
TR a[150:151],v147 offset:55296
MM v[64:67],a[172:175],v[216:219],v[64:67]
MM v[72:75],a[172:175],v[220:223],v[72:75]
TR a[152:153],v147 offset:57344
TR a[154:155],v147 offset:59392
MM v[72:75],a[168:171],v[204:207],v[72:75]
buffer_load_dwordx4 a[120:123], v248, s[20:23], s92 offen offset:3072 sc1
MM v[72:75],a[164:167],v[188:191],v[72:75]
s_add_u32 s92, s92, 4096
TR a[156:157],v147 offset:61440
TR a[158:159],v147 offset:63488
MM v[72:75],a[160:163],v[172:175],v[72:75]
s_waitcnt lgkmcnt(15)
MM v[76:79],a[176:179],v[172:175],v[76:79]
TR a[160:161],v230 offset:49152
TR a[162:163],v230 offset:51200
MM v[76:79],a[180:183],v[188:191],v[76:79]
buffer_load_dwordx4 a[28:31], v248, s[20:23], s92 offen offset:0 sc1
MM v[76:79],a[184:187],v[204:207],v[76:79]
TR a[164:165],v230 offset:53248
TR a[166:167],v230 offset:55296
MM v[76:79],a[188:191],v[220:223],v[76:79]
MM v[68:71],a[188:191],v[216:219],v[68:71]
TR a[168:169],v230 offset:57344
TR a[170:171],v230 offset:59392
MM v[68:71],a[184:187],v[200:203],v[68:71]
buffer_load_dwordx4 a[60:63], v248, s[20:23], s92 offen offset:1024 sc1
MM v[68:71],a[180:183],v[184:187],v[68:71]
TR a[172:173],v230 offset:61440
TR a[174:175],v230 offset:63488
MM v[68:71],a[176:179],v[168:171],v[68:71]
s_waitcnt lgkmcnt(15)
MM v[96:99],a[128:131],v[168:171],v[96:99]
TR a[176:177],v231 offset:49152
TR a[178:179],v231 offset:51200
MM v[96:99],a[132:135],v[184:187],v[96:99]
buffer_load_dwordx4 a[92:95], v248, s[20:23], s92 offen offset:2048 sc1
MM v[96:99],a[136:139],v[200:203],v[96:99]
TR a[180:181],v231 offset:53248
TR a[182:183],v231 offset:55296
MM v[96:99],a[140:143],v[216:219],v[96:99]
MM v[104:107],a[140:143],v[220:223],v[104:107]
TR a[184:185],v231 offset:57344
TR a[186:187],v231 offset:59392
MM v[104:107],a[136:139],v[204:207],v[104:107]
buffer_load_dwordx4 a[124:127], v248, s[20:23], s92 offen offset:3072 sc1
MM v[104:107],a[132:135],v[188:191],v[104:107]
s_xor_b32 s92, s49, 3
TR a[188:189],v231 offset:61440
TR a[190:191],v231 offset:63488
MM v[104:107],a[128:131],v[172:175],v[104:107]
s_waitcnt lgkmcnt(15)
s_mul_i32 s92, s92, 17408
s_add_u32 s92, s83, s92
s_barrier
MM v[108:111],a[144:147],v[172:175],v[108:111]
s_add_u32 s92, s92, 8192
TR a[128:129],v244 offset:8192
TR a[130:131],v244 offset:9216
MM v[108:111],a[148:151],v[188:191],v[108:111]
buffer_load_dwordx4 a[200:203], v248, s[20:23], s92 offen offset:0 sc1
MM v[108:111],a[152:155],v[204:207],v[108:111]
TR a[132:133],v244 offset:10240
TR a[134:135],v244 offset:11264
MM v[108:111],a[156:159],v[220:223],v[108:111]
MM v[100:103],a[156:159],v[216:219],v[100:103]
TR a[136:137],v244 offset:12288
TR a[138:139],v244 offset:13312
MM v[100:103],a[152:155],v[200:203],v[100:103]
buffer_load_dwordx4 a[216:219], v248, s[20:23], s92 offen offset:1024 sc1
MM v[100:103],a[148:151],v[184:187],v[100:103]
TR a[140:141],v244 offset:14336
TR a[142:143],v244 offset:15360
MM v[100:103],a[144:147],v[168:171],v[100:103]
s_waitcnt lgkmcnt(15)
MM v[32:35],a[160:163],v[168:171],v[32:35]
TR a[144:145],v235 offset:8192
TR a[146:147],v235 offset:9216
MM v[32:35],a[164:167],v[184:187],v[32:35]
buffer_load_dwordx4 a[232:235], v248, s[20:23], s92 offen offset:2048 sc1
MM v[32:35],a[168:171],v[200:203],v[32:35]
TR a[148:149],v235 offset:10240
TR a[150:151],v235 offset:11264
MM v[32:35],a[172:175],v[216:219],v[32:35]
)ASM"
R"ASM(
MM v[40:43],a[172:175],v[220:223],v[40:43]
TR a[152:153],v235 offset:12288
TR a[154:155],v235 offset:13312
MM v[40:43],a[168:171],v[204:207],v[40:43]
buffer_load_dwordx4 a[248:251], v248, s[20:23], s92 offen offset:3072 sc1
MM v[40:43],a[164:167],v[188:191],v[40:43]
s_add_u32 s92, s92, 4096
TR a[156:157],v235 offset:14336
TR a[158:159],v235 offset:15360
MM v[40:43],a[160:163],v[172:175],v[40:43]
s_waitcnt lgkmcnt(15)
MM v[44:47],a[176:179],v[172:175],v[44:47]
TR a[160:161],v144 offset:0
TR a[162:163],v144 offset:2048
MM v[44:47],a[180:183],v[188:191],v[44:47]
buffer_load_dwordx4 a[204:207], v248, s[20:23], s92 offen offset:0 sc1
MM v[44:47],a[184:187],v[204:207],v[44:47]
TR a[164:165],v144 offset:4096
TR a[166:167],v144 offset:6144
MM v[44:47],a[188:191],v[220:223],v[44:47]
MM v[36:39],a[188:191],v[216:219],v[36:39]
TR a[168:169],v144 offset:8192
TR a[170:171],v144 offset:10240
MM v[36:39],a[184:187],v[200:203],v[36:39]
buffer_load_dwordx4 a[220:223], v248, s[20:23], s92 offen offset:1024 sc1
MM v[36:39],a[180:183],v[184:187],v[36:39]
TR a[172:173],v144 offset:12288
TR a[174:175],v144 offset:14336
MM v[36:39],a[176:179],v[168:171],v[36:39]
s_waitcnt lgkmcnt(15)
s_waitcnt vmcnt(24)
MM v[128:131],a[128:131],a[16:19],v[128:131]
TR a[176:177],v145 offset:0
TR a[178:179],v145 offset:2048
MM v[128:131],a[132:135],a[48:51],v[128:131]
buffer_load_dwordx4 a[236:239], v248, s[20:23], s92 offen offset:2048 sc1
MM v[128:131],a[136:139],a[80:83],v[128:131]
TR a[180:181],v145 offset:4096
TR a[182:183],v145 offset:6144
MM v[128:131],a[140:143],a[112:115],v[128:131]
s_waitcnt vmcnt(21)
MM v[136:139],a[140:143],a[116:119],v[136:139]
TR a[184:185],v145 offset:8192
TR a[186:187],v145 offset:10240
MM v[136:139],a[136:139],a[84:87],v[136:139]
buffer_load_dwordx4 a[252:255], v248, s[20:23], s92 offen offset:3072 sc1
MM v[136:139],a[132:135],a[52:55],v[136:139]
TR a[188:189],v145 offset:12288
TR a[190:191],v145 offset:14336
MM v[136:139],a[128:131],a[20:23],v[136:139]
s_waitcnt lgkmcnt(15)
MM v[140:143],a[144:147],a[20:23],v[140:143]
TR a[128:129],v146 offset:0
TR a[130:131],v146 offset:2048
MM v[140:143],a[148:151],a[52:55],v[140:143]
MM v[140:143],a[152:155],a[84:87],v[140:143]
TR a[132:133],v146 offset:4096
TR a[134:135],v146 offset:6144
MM v[140:143],a[156:159],a[116:119],v[140:143]
MM v[132:135],a[156:159],a[112:115],v[132:135]
TR a[136:137],v146 offset:8192
TR a[138:139],v146 offset:10240
MM v[132:135],a[152:155],a[80:83],v[132:135]
MM v[132:135],a[148:151],a[48:51],v[132:135]
TR a[140:141],v146 offset:12288
TR a[142:143],v146 offset:14336
MM v[132:135],a[144:147],a[16:19],v[132:135]
s_waitcnt lgkmcnt(15)
MM v[64:67],a[160:163],a[16:19],v[64:67]
TR a[144:145],v147 offset:0
TR a[146:147],v147 offset:2048
MM v[64:67],a[164:167],a[48:51],v[64:67]
MM v[64:67],a[168:171],a[80:83],v[64:67]
TR a[148:149],v147 offset:4096
TR a[150:151],v147 offset:6144
MM v[64:67],a[172:175],a[112:115],v[64:67]
MM v[72:75],a[172:175],a[116:119],v[72:75]
TR a[152:153],v147 offset:8192
TR a[154:155],v147 offset:10240
MM v[72:75],a[168:171],a[84:87],v[72:75]
MM v[72:75],a[164:167],a[52:55],v[72:75]
TR a[156:157],v147 offset:12288
TR a[158:159],v147 offset:14336
MM v[72:75],a[160:163],a[20:23],v[72:75]
s_waitcnt lgkmcnt(15)
s_waitcnt vmcnt(16)
s_barrier
MM v[76:79],a[176:179],a[20:23],v[76:79]
s_xor_b32 s66, s94, 1
TR a[160:161],v244 offset:16384
TR a[162:163],v244 offset:17408
MM v[76:79],a[180:183],a[52:55],v[76:79]
s_mul_i32 s66, s66, 163840
s_add_u32 s66, s66, s86
s_add_u32 m0, s65, 0
MM v[76:79],a[184:187],a[84:87],v[76:79]
TR a[164:165],v244 offset:18432
TR a[166:167],v244 offset:19456
MM v[76:79],a[188:191],a[116:119],v[76:79]
buffer_load_dwordx4 v240, s[24:27], s66 offen offset:0 lds
MM v[68:71],a[188:191],a[112:115],v[68:71]
s_add_u32 s66, s66, 9216
TR a[168:169],v244 offset:20480
TR a[170:171],v244 offset:21504
MM v[68:71],a[184:187],a[80:83],v[68:71]
MM v[68:71],a[180:183],a[48:51],v[68:71]
TR a[172:173],v244 offset:22528
TR a[174:175],v244 offset:23552
MM v[68:71],a[176:179],a[16:19],v[68:71]
s_waitcnt lgkmcnt(15)
buffer_load_dwordx4 v240, s[24:27], s66 offen offset:1024 lds
MM v[96:99],a[128:131],a[16:19],v[96:99]
s_add_u32 s66, s66, 9216
TR a[176:177],v244 offset:24576
TR a[178:179],v244 offset:25600
MM v[96:99],a[132:135],a[48:51],v[96:99]
MM v[96:99],a[136:139],a[80:83],v[96:99]
TR a[180:181],v244 offset:26624
TR a[182:183],v244 offset:27648
MM v[96:99],a[140:143],a[112:115],v[96:99]
buffer_load_dwordx4 v240, s[24:27], s66 offen offset:2048 lds
MM v[104:107],a[140:143],a[116:119],v[104:107]
s_add_u32 s66, s66, 9216
TR a[184:185],v244 offset:28672
TR a[186:187],v244 offset:29696
MM v[104:107],a[136:139],a[84:87],v[104:107]
MM v[104:107],a[132:135],a[52:55],v[104:107]
TR a[188:189],v244 offset:30720
TR a[190:191],v244 offset:31744
MM v[104:107],a[128:131],a[20:23],v[104:107]
s_waitcnt lgkmcnt(15)
buffer_load_dwordx4 v240, s[24:27], s66 offen offset:3072 lds
MM v[108:111],a[144:147],a[20:23],v[108:111]
TR a[128:129],v235 offset:16384
TR a[130:131],v235 offset:17408
MM v[108:111],a[148:151],a[52:55],v[108:111]
s_xor_b32 s66, s94, 2
s_mul_i32 s66, s66, 163840
s_add_u32 s66, s66, s86
MM v[108:111],a[152:155],a[84:87],v[108:111]
s_add_u32 m0, s65, 16384
TR a[132:133],v235 offset:18432
TR a[134:135],v235 offset:19456
MM v[108:111],a[156:159],a[116:119],v[108:111]
buffer_load_dwordx4 v240, s[24:27], s66 offen offset:0 lds
MM v[100:103],a[156:159],a[112:115],v[100:103]
s_add_u32 s66, s66, 9216
TR a[136:137],v235 offset:20480
TR a[138:139],v235 offset:21504
MM v[100:103],a[152:155],a[80:83],v[100:103]
MM v[100:103],a[148:151],a[48:51],v[100:103]
TR a[140:141],v235 offset:22528
TR a[142:143],v235 offset:23552
MM v[100:103],a[144:147],a[16:19],v[100:103]
s_waitcnt lgkmcnt(15)
buffer_load_dwordx4 v240, s[24:27], s66 offen offset:1024 lds
s_waitcnt vmcnt(18)
MM v[128:131],a[160:163],a[24:27],v[128:131]
s_add_u32 s66, s66, 9216
TR a[144:145],v235 offset:24576
TR a[146:147],v235 offset:25600
MM v[128:131],a[164:167],a[56:59],v[128:131]
MM v[128:131],a[168:171],a[88:91],v[128:131]
TR a[148:149],v235 offset:26624
TR a[150:151],v235 offset:27648
MM v[128:131],a[172:175],a[120:123],v[128:131]
s_waitcnt vmcnt(14)
buffer_load_dwordx4 v240, s[24:27], s66 offen offset:2048 lds
MM v[136:139],a[172:175],a[124:127],v[136:139]
s_add_u32 s66, s66, 9216
TR a[152:153],v235 offset:28672
TR a[154:155],v235 offset:29696
MM v[136:139],a[168:171],a[92:95],v[136:139]
MM v[136:139],a[164:167],a[60:63],v[136:139]
TR a[156:157],v235 offset:30720
TR a[158:159],v235 offset:31744
MM v[136:139],a[160:163],a[28:31],v[136:139]
s_waitcnt lgkmcnt(15)
buffer_load_dwordx4 v240, s[24:27], s66 offen offset:3072 lds
s_waitcnt vmcnt(12)
MM v[128:131],a[176:179],a[200:203],v[128:131]
s_memrealtime s[56:57]
TR a[160:161],v144 offset:16384
TR a[162:163],v144 offset:18432
MM v[128:131],a[180:183],a[216:219],v[128:131]
s_xor_b32 s66, s94, 3
s_mul_i32 s66, s66, 163840
s_add_u32 s66, s66, s86
MM v[128:131],a[184:187],a[232:235],v[128:131]
s_add_u32 m0, s65, 32768
TR a[164:165],v144 offset:20480
TR a[166:167],v144 offset:22528
MM v[128:131],a[188:191],a[248:251],v[128:131]
s_waitcnt vmcnt(8)
buffer_load_dwordx4 v240, s[24:27], s66 offen offset:0 lds
MM v[136:139],a[188:191],a[252:255],v[136:139]
s_add_u32 s66, s66, 9216
TR a[168:169],v144 offset:24576
TR a[170:171],v144 offset:26624
MM v[136:139],a[184:187],a[236:239],v[136:139]
MM v[136:139],a[180:183],a[220:223],v[136:139]
TR a[172:173],v144 offset:28672
TR a[174:175],v144 offset:30720
MM v[136:139],a[176:179],a[204:207],v[136:139]
s_waitcnt lgkmcnt(0)
buffer_load_dwordx4 v240, s[24:27], s66 offen offset:1024 lds
MM v[140:143],a[128:131],a[28:31],v[140:143]
s_add_u32 s66, s66, 9216
TR a[176:177],v144 offset:32768
TR a[178:179],v144 offset:34816
MM v[140:143],a[132:135],a[60:63],v[140:143]
v_mov_b32 v158, s56
v_mov_b32 v159, s69
MM v[140:143],a[136:139],a[92:95],v[140:143]
TR a[180:181],v144 offset:36864
TR a[182:183],v144 offset:38912
MM v[140:143],a[140:143],a[124:127],v[140:143]
s_add_u32 s67, s95, 0
CV v128,v128,v129
CV v129,v130,v131
MM v[132:135],a[140:143],a[120:123],v[132:135]
buffer_load_dwordx4 v240, s[24:27], s66 offen offset:2048 lds
s_mov_b64 exec, 1
buffer_store_dwordx2 v[158:159], off, s[20:23], s19 offset:8 sc1
s_mov_b64 exec, -1
s_nop 0
TR a[184:185],v144 offset:40960
TR a[186:187],v144 offset:43008
MM v[132:135],a[136:139],a[88:91],v[132:135]
s_add_u32 s66, s66, 9216
CV v136,v136,v137
CV v137,v138,v139
MM v[132:135],a[132:135],a[56:59],v[132:135]
TR a[188:189],v144 offset:45056
TR a[190:191],v144 offset:47104
MM v[132:135],a[128:131],a[24:27],v[132:135]
buffer_load_dwordx4 v240, s[24:27], s66 offen offset:3072 lds
MM v[132:135],a[144:147],a[200:203],v[132:135]
TR a[128:129],v145 offset:16384
TR a[130:131],v145 offset:18432
MM v[132:135],a[148:151],a[216:219],v[132:135]
MM v[132:135],a[152:155],a[232:235],v[132:135]
TR a[132:133],v145 offset:20480
TR a[134:135],v145 offset:22528
MM v[132:135],a[156:159],a[248:251],v[132:135]
MM v[140:143],a[156:159],a[252:255],v[140:143]
TR a[136:137],v145 offset:24576
TR a[138:139],v145 offset:26624
MM v[140:143],a[152:155],a[236:239],v[140:143]
MM v[140:143],a[148:151],a[220:223],v[140:143]
TR a[140:141],v145 offset:28672
TR a[142:143],v145 offset:30720
MM v[140:143],a[144:147],a[204:207],v[140:143]
CV v130,v132,v133
CV v131,v134,v135
MM v[72:75],a[160:163],a[28:31],v[72:75]
TR a[144:145],v145 offset:32768
TR a[146:147],v145 offset:34816
MM v[72:75],a[164:167],a[60:63],v[72:75]
v_min_u32 v131, -2, v131
MM v[72:75],a[168:171],a[92:95],v[72:75]
TR a[148:149],v145 offset:36864
TR a[150:151],v145 offset:38912
MM v[72:75],a[172:175],a[124:127],v[72:75]
buffer_store_dwordx4 v[128:131], v249, s[20:23], s67 offen offset:0 sc1
MM v[64:67],a[172:175],a[120:123],v[64:67]
TR a[152:153],v145 offset:40960
TR a[154:155],v145 offset:43008
MM v[64:67],a[168:171],a[88:91],v[64:67]
CV v138,v140,v141
CV v139,v142,v143
MM v[64:67],a[164:167],a[56:59],v[64:67]
TR a[156:157],v145 offset:45056
TR a[158:159],v145 offset:47104
MM v[64:67],a[160:163],a[24:27],v[64:67]
s_waitcnt lgkmcnt(15)
v_min_u32 v139, -2, v139
MM v[64:67],a[176:179],a[200:203],v[64:67]
TR a[160:161],v146 offset:16384
TR a[162:163],v146 offset:18432
MM v[64:67],a[180:183],a[216:219],v[64:67]
buffer_store_dwordx4 v[136:139], v249, s[20:23], s67 offen offset:1024 sc1
MM v[64:67],a[184:187],a[232:235],v[64:67]
TR a[164:165],v146 offset:20480
TR a[166:167],v146 offset:22528
MM v[64:67],a[188:191],a[248:251],v[64:67]
MM v[72:75],a[188:191],a[252:255],v[72:75]
TR a[168:169],v146 offset:24576
TR a[170:171],v146 offset:26624
MM v[72:75],a[184:187],a[236:239],v[72:75]
MM v[72:75],a[180:183],a[220:223],v[72:75]
TR a[172:173],v146 offset:28672
TR a[174:175],v146 offset:30720
MM v[72:75],a[176:179],a[204:207],v[72:75]
s_waitcnt lgkmcnt(15)
MM v[76:79],a[128:131],a[28:31],v[76:79]
TR a[176:177],v146 offset:32768
TR a[178:179],v146 offset:34816
MM v[76:79],a[132:135],a[60:63],v[76:79]
v_lshlrev_b32 v136, 16, v150
v_and_b32 v137, 0xffff0000, v150
MM v[76:79],a[136:139],a[92:95],v[76:79]
TR a[180:181],v146 offset:36864
TR a[182:183],v146 offset:38912
MM v[76:79],a[140:143],a[124:127],v[76:79]
s_add_u32 s67, s95, 4096
CV v64,v64,v65
CV v65,v66,v67
MM v[68:71],a[140:143],a[120:123],v[68:71]
TR a[184:185],v146 offset:40960
TR a[186:187],v146 offset:43008
MM v[68:71],a[136:139],a[88:91],v[68:71]
CV v72,v72,v73
CV v73,v74,v75
MM v[68:71],a[132:135],a[56:59],v[68:71]
TR a[188:189],v146 offset:45056
TR a[190:191],v146 offset:47104
MM v[68:71],a[128:131],a[24:27],v[68:71]
s_waitcnt lgkmcnt(15)
v_lshlrev_b32 v138, 16, v151
v_and_b32 v139, 0xffff0000, v151
MM v[68:71],a[144:147],a[200:203],v[68:71]
TR a[128:129],v147 offset:16384
TR a[130:131],v147 offset:18432
MM v[68:71],a[148:151],a[216:219],v[68:71]
buffer_load_dwordx2 v[148:149], v253, s[20:23], s68 offen offset:8 sc1
MM v[68:71],a[152:155],a[232:235],v[68:71]
TR a[132:133],v147 offset:20480
TR a[134:135],v147 offset:22528
MM v[68:71],a[156:159],a[248:251],v[68:71]
v_mul_f32 v140, 0xbfb8aa3b, v136
)ASM"
R"ASM(
v_mul_f32 v141, 0xbfb8aa3b, v137
MM v[76:79],a[156:159],a[252:255],v[76:79]
TR a[136:137],v147 offset:24576
TR a[138:139],v147 offset:26624
MM v[76:79],a[152:155],a[236:239],v[76:79]
v_mul_f32 v142, 0xbfb8aa3b, v138
v_mul_f32 v143, 0xbfb8aa3b, v139
MM v[76:79],a[148:151],a[220:223],v[76:79]
TR a[140:141],v147 offset:28672
TR a[142:143],v147 offset:30720
MM v[76:79],a[144:147],a[204:207],v[76:79]
s_waitcnt lgkmcnt(15)
CV v66,v68,v69
CV v67,v70,v71
s_waitcnt vmcnt(3)
s_barrier
MM v[104:107],a[160:163],a[28:31],v[104:107]
TR a[144:145],v147 offset:32768
TR a[146:147],v147 offset:34816
MM v[104:107],a[164:167],a[60:63],v[104:107]
EX v140,v140
v_min_u32 v67, -2, v67
MM v[104:107],a[168:171],a[92:95],v[104:107]
TR a[148:149],v147 offset:36864
TR a[150:151],v147 offset:38912
MM v[104:107],a[172:175],a[124:127],v[104:107]
buffer_store_dwordx4 v[64:67], v248, s[20:23], s67 offen offset:0 sc1
MM v[96:99],a[172:175],a[120:123],v[96:99]
TR a[152:153],v147 offset:40960
TR a[154:155],v147 offset:43008
MM v[96:99],a[168:171],a[88:91],v[96:99]
CV v74,v76,v77
CV v75,v78,v79
MM v[96:99],a[164:167],a[56:59],v[96:99]
TR a[156:157],v147 offset:45056
TR a[158:159],v147 offset:47104
MM v[96:99],a[160:163],a[24:27],v[96:99]
s_waitcnt lgkmcnt(15)
EX v141,v141
v_min_u32 v75, -2, v75
MM v[96:99],a[176:179],a[200:203],v[96:99]
TR a[160:161],v230 offset:0
TR a[162:163],v230 offset:2048
MM v[96:99],a[180:183],a[216:219],v[96:99]
buffer_store_dwordx4 v[72:75], v248, s[20:23], s67 offen offset:1024 sc1
MM v[96:99],a[184:187],a[232:235],v[96:99]
TR a[164:165],v230 offset:4096
TR a[166:167],v230 offset:6144
MM v[96:99],a[188:191],a[248:251],v[96:99]
EX v142,v142
MM v[104:107],a[188:191],a[252:255],v[104:107]
TR a[168:169],v230 offset:8192
TR a[170:171],v230 offset:10240
MM v[104:107],a[184:187],a[236:239],v[104:107]
EX v143,v143
MM v[104:107],a[180:183],a[220:223],v[104:107]
TR a[172:173],v230 offset:12288
TR a[174:175],v230 offset:14336
MM v[104:107],a[176:179],a[204:207],v[104:107]
s_waitcnt lgkmcnt(15)
v_add_f32 v140, 1.0, v140
v_add_f32 v141, 1.0, v141
MM v[108:111],a[128:131],a[28:31],v[108:111]
TR a[176:177],v230 offset:16384
TR a[178:179],v230 offset:18432
MM v[108:111],a[132:135],a[60:63],v[108:111]
v_add_f32 v142, 1.0, v142
v_add_f32 v143, 1.0, v143
MM v[108:111],a[136:139],a[92:95],v[108:111]
TR a[180:181],v230 offset:20480
TR a[182:183],v230 offset:22528
MM v[108:111],a[140:143],a[124:127],v[108:111]
s_add_u32 s67, s95, 4096
CV v96,v96,v97
CV v97,v98,v99
MM v[100:103],a[140:143],a[120:123],v[100:103]
TR a[184:185],v230 offset:24576
TR a[186:187],v230 offset:26624
MM v[100:103],a[136:139],a[88:91],v[100:103]
CV v104,v104,v105
CV v105,v106,v107
MM v[100:103],a[132:135],a[56:59],v[100:103]
TR a[188:189],v230 offset:28672
TR a[190:191],v230 offset:30720
MM v[100:103],a[128:131],a[24:27],v[100:103]
s_waitcnt lgkmcnt(15)
v_rcp_f32 v140, v140
MM v[100:103],a[144:147],a[200:203],v[100:103]
TR a[128:129],v230 offset:32768
TR a[130:131],v230 offset:34816
MM v[100:103],a[148:151],a[216:219],v[100:103]
v_rcp_f32 v141, v141
MM v[100:103],a[152:155],a[232:235],v[100:103]
TR a[132:133],v230 offset:36864
TR a[134:135],v230 offset:38912
MM v[100:103],a[156:159],a[248:251],v[100:103]
v_rcp_f32 v142, v142
MM v[108:111],a[156:159],a[252:255],v[108:111]
TR a[136:137],v230 offset:40960
TR a[138:139],v230 offset:43008
MM v[108:111],a[152:155],a[236:239],v[108:111]
v_rcp_f32 v143, v143
MM v[108:111],a[148:151],a[220:223],v[108:111]
TR a[140:141],v230 offset:45056
TR a[142:143],v230 offset:47104
MM v[108:111],a[144:147],a[204:207],v[108:111]
s_waitcnt lgkmcnt(15)
LPG29_%=:
s_cbranch_execz LTR1_%=
CV v98,v100,v101
CV v99,v102,v103
MM v[40:43],a[160:163],a[20:23],v[40:43]
TR a[144:145],v231 offset:0
TR a[146:147],v231 offset:2048
MM v[40:43],a[164:167],a[52:55],v[40:43]
v_min_u32 v99, -2, v99
v_mul_f32 v140, v136, v140
MM v[40:43],a[168:171],a[84:87],v[40:43]
TR a[148:149],v231 offset:4096
TR a[150:151],v231 offset:6144
MM v[40:43],a[172:175],a[116:119],v[40:43]
buffer_store_dwordx4 v[96:99], v248, s[20:23], s67 offen offset:2048 sc1
MM v[32:35],a[172:175],a[112:115],v[32:35]
TR a[152:153],v231 offset:8192
TR a[154:155],v231 offset:10240
MM v[32:35],a[168:171],a[80:83],v[32:35]
CV v106,v108,v109
CV v107,v110,v111
MM v[32:35],a[164:167],a[48:51],v[32:35]
TR a[156:157],v231 offset:12288
TR a[158:159],v231 offset:14336
MM v[32:35],a[160:163],a[16:19],v[32:35]
s_waitcnt lgkmcnt(15)
v_min_u32 v107, -2, v107
v_mul_f32 v141, v137, v141
MM v[32:35],a[176:179],a[24:27],v[32:35]
TR a[160:161],v231 offset:16384
TR a[162:163],v231 offset:18432
MM v[32:35],a[180:183],a[56:59],v[32:35]
buffer_store_dwordx4 v[104:107], v248, s[20:23], s67 offen offset:3072 sc1
MM v[32:35],a[184:187],a[88:91],v[32:35]
TR a[164:165],v231 offset:20480
TR a[166:167],v231 offset:22528
MM v[32:35],a[188:191],a[120:123],v[32:35]
v_mul_f32 v142, v138, v142
v_mul_f32 v143, v139, v143
MM v[40:43],a[188:191],a[124:127],v[40:43]
TR a[168:169],v231 offset:24576
TR a[170:171],v231 offset:26624
MM v[40:43],a[184:187],a[92:95],v[40:43]
CV v150,v140,v141
CV v151,v142,v143
MM v[40:43],a[180:183],a[60:63],v[40:43]
TR a[172:173],v231 offset:28672
TR a[174:175],v231 offset:30720
MM v[40:43],a[176:179],a[28:31],v[40:43]
s_waitcnt lgkmcnt(15)
v_lshlrev_b32 v160, 16, v16
v_and_b32 v161, 0xffff0000, v16
MM v[40:43],a[128:131],a[204:207],v[40:43]
TR a[176:177],v231 offset:32768
TR a[178:179],v231 offset:34816
MM v[40:43],a[132:135],a[220:223],v[40:43]
v_lshlrev_b32 v162, 16, v17
v_and_b32 v163, 0xffff0000, v17
MM v[40:43],a[136:139],a[236:239],v[40:43]
TR a[180:181],v231 offset:36864
TR a[182:183],v231 offset:38912
MM v[40:43],a[140:143],a[252:255],v[40:43]
v_lshlrev_b32 v164, 16, v18
v_and_b32 v165, 0xffff0000, v18
MM v[32:35],a[140:143],a[248:251],v[32:35]
TR a[184:185],v231 offset:40960
TR a[186:187],v231 offset:43008
MM v[32:35],a[136:139],a[232:235],v[32:35]
v_lshlrev_b32 v166, 16, v19
v_and_b32 v167, 0xffff0000, v19
MM v[32:35],a[132:135],a[216:219],v[32:35]
TR a[188:189],v231 offset:45056
TR a[190:191],v231 offset:47104
MM v[32:35],a[128:131],a[200:203],v[32:35]
s_waitcnt lgkmcnt(15)
v_mul_f32 v136, 0xbfb8aa3b, v160
v_mul_f32 v137, 0xbfb8aa3b, v161
MM v[36:39],a[144:147],a[16:19],v[36:39]
TR a[128:129],v228 offset:49152
TR a[130:131],v228 offset:51200
MM v[36:39],a[148:151],a[48:51],v[36:39]
v_mul_f32 v138, 0xbfb8aa3b, v162
v_mul_f32 v139, 0xbfb8aa3b, v163
MM v[36:39],a[152:155],a[80:83],v[36:39]
TR a[132:133],v228 offset:53248
TR a[134:135],v228 offset:55296
MM v[36:39],a[156:159],a[112:115],v[36:39]
s_add_u32 s67, s95, 0
CV v32,v32,v33
CV v33,v34,v35
MM v[44:47],a[156:159],a[116:119],v[44:47]
TR a[136:137],v228 offset:57344
TR a[138:139],v228 offset:59392
MM v[44:47],a[152:155],a[84:87],v[44:47]
CV v40,v40,v41
CV v41,v42,v43
MM v[44:47],a[148:151],a[52:55],v[44:47]
TR a[140:141],v228 offset:61440
TR a[142:143],v228 offset:63488
MM v[44:47],a[144:147],a[20:23],v[44:47]
s_waitcnt lgkmcnt(15)
v_mul_f32 v140, 0xbfb8aa3b, v164
v_mul_f32 v141, 0xbfb8aa3b, v165
MM v[44:47],a[160:163],a[28:31],v[44:47]
s_waitcnt vmcnt(4)
TR a[144:145],v228 offset:0
TR a[146:147],v228 offset:2048
MM v[44:47],a[164:167],a[60:63],v[44:47]
v_cmp_eq_u32 s[8:9], s69, v149
v_subrev_u32 v226, s56, v148
MM v[44:47],a[168:171],a[92:95],v[44:47]
TR a[148:149],v228 offset:4096
TR a[150:151],v228 offset:6144
MM v[44:47],a[172:175],a[124:127],v[44:47]
v_cndmask_b32 v226, 4.0, v226, s[8:9]
v_mul_f32 v142, 0xbfb8aa3b, v166
MM v[36:39],a[172:175],a[120:123],v[36:39]
TR a[152:153],v228 offset:8192
TR a[154:155],v228 offset:10240
MM v[36:39],a[168:171],a[88:91],v[36:39]
v_readlane_b32 s13, v226, 16
v_readlane_b32 s14, v226, 32
MM v[36:39],a[164:167],a[56:59],v[36:39]
TR a[156:157],v228 offset:12288
TR a[158:159],v228 offset:14336
MM v[36:39],a[160:163],a[24:27],v[36:39]
s_waitcnt lgkmcnt(15)
v_readlane_b32 s15, v226, 48
v_mul_f32 v143, 0xbfb8aa3b, v167
MM v[36:39],a[176:179],a[200:203],v[36:39]
s_max_i32 s12, s13, s14
TR a[160:161],v228 offset:16384
TR a[162:163],v228 offset:18432
MM v[36:39],a[180:183],a[216:219],v[36:39]
s_max_i32 s12, s12, s15
EX v136,v136
MM v[36:39],a[184:187],a[232:235],v[36:39]
s_max_i32 s12, s12, 0
TR a[164:165],v228 offset:20480
TR a[166:167],v228 offset:22528
MM v[36:39],a[188:191],a[248:251],v[36:39]
buffer_load_dwordx2 v[148:149], v253, s[20:23], s68 offen offset:8 sc1
MM v[44:47],a[188:191],a[252:255],v[44:47]
TR a[168:169],v228 offset:24576
TR a[170:171],v228 offset:26624
MM v[44:47],a[184:187],a[236:239],v[44:47]
EX v137,v137
MM v[44:47],a[180:183],a[220:223],v[44:47]
TR a[172:173],v228 offset:28672
TR a[174:175],v228 offset:30720
MM v[44:47],a[176:179],a[204:207],v[44:47]
s_waitcnt lgkmcnt(15)
CV v34,v36,v37
CV v35,v38,v39
v_min_u32 v35, -2, v35
buffer_store_dwordx4 v[32:35], v248, s[20:23], s67 offen offset:2048 sc1
MM v[8:11],a[128:131],v[172:175],v[8:11]
TR a[176:177],v228 offset:32768
TR a[178:179],v228 offset:34816
MM v[8:11],a[132:135],v[188:191],v[8:11]
EX v138,v138
MM v[8:11],a[136:139],v[204:207],v[8:11]
TR a[180:181],v228 offset:36864
TR a[182:183],v228 offset:38912
MM v[8:11],a[140:143],v[220:223],v[8:11]
CV v42,v44,v45
CV v43,v46,v47
v_min_u32 v43, -2, v43
buffer_store_dwordx4 v[40:43], v248, s[20:23], s67 offen offset:3072 sc1
MM v[0:3],a[140:143],v[216:219],v[0:3]
TR a[184:185],v228 offset:40960
TR a[186:187],v228 offset:43008
MM v[0:3],a[136:139],v[200:203],v[0:3]
EX v139,v139
MM v[0:3],a[132:135],v[184:187],v[0:3]
TR a[188:189],v228 offset:45056
TR a[190:191],v228 offset:47104
MM v[0:3],a[128:131],v[168:171],v[0:3]
s_waitcnt lgkmcnt(15)
EX v140,v140
MM v[0:3],a[144:147],a[16:19],v[0:3]
TR a[128:129],v229 offset:49152
TR a[130:131],v229 offset:51200
MM v[0:3],a[148:151],a[48:51],v[0:3]
s_cmp_le_i32 s13, -42
s_cselect_b32 s60, 2, 0
s_cselect_b64 s[8:9], -1, 0
MM v[0:3],a[152:155],a[80:83],v[0:3]
s_or_b32 s58, s58, s60
TR a[132:133],v229 offset:53248
TR a[134:135],v229 offset:55296
MM v[0:3],a[156:159],a[112:115],v[0:3]
s_mov_b64 exec, s[8:9]
buffer_load_dwordx4 v[80:83], v248, s[20:23], s36 offen offset:0 sc1
s_mov_b64 exec, -1
MM v[8:11],a[156:159],a[116:119],v[8:11]
TR a[136:137],v229 offset:57344
TR a[138:139],v229 offset:59392
MM v[8:11],a[152:155],a[84:87],v[8:11]
s_mov_b64 exec, s[8:9]
buffer_load_dwordx4 v[84:87], v248, s[20:23], s36 offen offset:1024 sc1
s_mov_b64 exec, -1
MM v[8:11],a[148:151],a[52:55],v[8:11]
TR a[140:141],v229 offset:61440
TR a[142:143],v229 offset:63488
MM v[8:11],a[144:147],a[20:23],v[8:11]
s_waitcnt lgkmcnt(15)
EX v141,v141
MM v[8:11],a[160:163],a[28:31],v[8:11]
TR a[144:145],v229 offset:0
TR a[146:147],v229 offset:2048
MM v[8:11],a[164:167],a[60:63],v[8:11]
s_cmp_le_i32 s15, 17
s_cselect_b32 s60, 8, 0
s_cselect_b64 s[8:9], -1, 0
MM v[8:11],a[168:171],a[92:95],v[8:11]
s_or_b32 s58, s58, s60
TR a[148:149],v229 offset:4096
TR a[150:151],v229 offset:6144
MM v[8:11],a[172:175],a[124:127],v[8:11]
s_mov_b64 exec, s[8:9]
buffer_load_dwordx4 v[112:115], v248, s[20:23], s38 offen offset:0 sc1
s_mov_b64 exec, -1
MM v[0:3],a[172:175],a[120:123],v[0:3]
TR a[152:153],v229 offset:8192
TR a[154:155],v229 offset:10240
MM v[0:3],a[168:171],a[88:91],v[0:3]
s_mov_b64 exec, s[8:9]
buffer_load_dwordx4 v[116:119], v248, s[20:23], s38 offen offset:1024 sc1
s_mov_b64 exec, -1
MM v[0:3],a[164:167],a[56:59],v[0:3]
TR a[156:157],v229 offset:12288
TR a[158:159],v229 offset:14336
MM v[0:3],a[160:163],a[24:27],v[0:3]
s_waitcnt lgkmcnt(15)
EX v142,v142
MM v[0:3],a[176:179],a[200:203],v[0:3]
TR a[160:161],v229 offset:16384
TR a[162:163],v229 offset:18432
MM v[0:3],a[180:183],a[216:219],v[0:3]
s_cmp_le_i32 s14, 59
s_cselect_b32 s60, 4, 0
s_cselect_b64 s[8:9], -1, 0
MM v[0:3],a[184:187],a[232:235],v[0:3]
s_or_b32 s58, s58, s60
TR a[164:165],v229 offset:20480
TR a[166:167],v229 offset:22528
MM v[0:3],a[188:191],a[248:251],v[0:3]
s_mov_b64 exec, s[8:9]
buffer_load_dwordx4 v[88:91], v248, s[20:23], s37 offen offset:0 sc1
s_mov_b64 exec, -1
MM v[8:11],a[188:191],a[252:255],v[8:11]
TR a[168:169],v229 offset:24576
TR a[170:171],v229 offset:26624
MM v[8:11],a[184:187],a[236:239],v[8:11]
)ASM"
R"ASM(
s_mov_b64 exec, s[8:9]
buffer_load_dwordx4 v[92:95], v248, s[20:23], s37 offen offset:1024 sc1
s_mov_b64 exec, -1
MM v[8:11],a[180:183],a[220:223],v[8:11]
TR a[172:173],v229 offset:28672
TR a[174:175],v229 offset:30720
MM v[8:11],a[176:179],a[204:207],v[8:11]
s_waitcnt lgkmcnt(15)
EX v143,v143
MM v[12:15],a[128:131],v[172:175],v[12:15]
TR a[176:177],v229 offset:32768
TR a[178:179],v229 offset:34816
MM v[12:15],a[132:135],v[188:191],v[12:15]
s_cmp_le_i32 s12, 102
s_cselect_b32 s60, 1, 0
s_cselect_b64 s[8:9], -1, 0
MM v[12:15],a[136:139],v[204:207],v[12:15]
s_or_b32 s58, s58, s60
TR a[180:181],v229 offset:36864
TR a[182:183],v229 offset:38912
MM v[12:15],a[140:143],v[220:223],v[12:15]
s_mov_b64 exec, s[8:9]
buffer_load_dwordx4 v[120:123], v250, s[20:23], s39 offen offset:0 sc1
s_mov_b64 exec, -1
MM v[4:7],a[140:143],v[216:219],v[4:7]
TR a[184:185],v229 offset:40960
TR a[186:187],v229 offset:43008
MM v[4:7],a[136:139],v[200:203],v[4:7]
s_mov_b64 exec, s[8:9]
buffer_load_dwordx4 v[124:127], v250, s[20:23], s39 offen offset:256 sc1
s_mov_b64 exec, -1
MM v[4:7],a[132:135],v[184:187],v[4:7]
TR a[188:189],v229 offset:45056
TR a[190:191],v229 offset:47104
MM v[4:7],a[128:131],v[168:171],v[4:7]
s_waitcnt lgkmcnt(15)
v_add_f32 v136, 1.0, v136
v_add_f32 v137, 1.0, v137
MM v[4:7],a[144:147],a[16:19],v[4:7]
v_add_f32 v138, 1.0, v138
v_add_f32 v139, 1.0, v139
MM v[4:7],a[148:151],a[48:51],v[4:7]
v_add_f32 v140, 1.0, v140
v_add_f32 v141, 1.0, v141
MM v[4:7],a[152:155],a[80:83],v[4:7]
v_add_f32 v142, 1.0, v142
v_add_f32 v143, 1.0, v143
MM v[4:7],a[156:159],a[112:115],v[4:7]
v_rcp_f32 v136, v136
MM v[12:15],a[156:159],a[116:119],v[12:15]
v_rcp_f32 v137, v137
MM v[12:15],a[152:155],a[84:87],v[12:15]
v_rcp_f32 v138, v138
MM v[12:15],a[148:151],a[52:55],v[12:15]
v_rcp_f32 v139, v139
MM v[12:15],a[144:147],a[20:23],v[12:15]
s_waitcnt lgkmcnt(8)
v_rcp_f32 v140, v140
MM v[12:15],a[160:163],a[28:31],v[12:15]
s_cmp_le_i32 s13, 0
s_cselect_b32 s60, 2, 0
s_andn2_b32 s60, s60, s58
s_cselect_b64 s[8:9], -1, 0
MM v[12:15],a[164:167],a[60:63],v[12:15]
s_or_b32 s58, s58, s60
s_mov_b64 exec, s[8:9]
buffer_load_dwordx4 v[80:83], v248, s[20:23], s36 offen offset:0 sc1
s_mov_b64 exec, -1
MM v[12:15],a[168:171],a[92:95],v[12:15]
v_rcp_f32 v141, v141
MM v[12:15],a[172:175],a[124:127],v[12:15]
s_mov_b64 exec, s[8:9]
buffer_load_dwordx4 v[84:87], v248, s[20:23], s36 offen offset:1024 sc1
s_mov_b64 exec, -1
MM v[4:7],a[172:175],a[120:123],v[4:7]
v_rcp_f32 v142, v142
MM v[4:7],a[168:171],a[88:91],v[4:7]
v_rcp_f32 v143, v143
MM v[4:7],a[164:167],a[56:59],v[4:7]
v_mul_f32 v136, v160, v136
v_mul_f32 v137, v161, v137
MM v[4:7],a[160:163],a[24:27],v[4:7]
s_waitcnt lgkmcnt(0)
v_mul_f32 v138, v162, v138
v_mul_f32 v139, v163, v139
MM v[4:7],a[176:179],a[200:203],v[4:7]
v_mul_f32 v140, v164, v140
v_mul_f32 v141, v165, v141
MM v[4:7],a[180:183],a[216:219],v[4:7]
v_mul_f32 v142, v166, v142
v_mul_f32 v143, v167, v143
MM v[4:7],a[184:187],a[232:235],v[4:7]
CV v16,v136,v137
CV v17,v138,v139
MM v[4:7],a[188:191],a[248:251],v[4:7]
CV v18,v140,v141
CV v19,v142,v143
MM v[12:15],a[188:191],a[252:255],v[12:15]
v_lshlrev_b32 v160, 16, v16
v_and_b32 v161, 0xffff0000, v16
MM v[12:15],a[184:187],a[236:239],v[12:15]
v_lshlrev_b32 v162, 16, v17
v_and_b32 v163, 0xffff0000, v17
MM v[12:15],a[180:183],a[220:223],v[12:15]
v_lshlrev_b32 v164, 16, v18
v_and_b32 v165, 0xffff0000, v18
MM v[12:15],a[176:179],a[204:207],v[12:15]
v_lshlrev_b32 v166, 16, v19
v_and_b32 v167, 0xffff0000, v19
s_memrealtime s[54:55]
s_cmp_eq_u32 s58, 15
s_cbranch_scc1 LSK1_%=
s_memrealtime s[98:99]
s_andn2_b32 s91, s17, s58
s_mov_b32 s77, 0x7fffffff
s_waitcnt lgkmcnt(0)
s_sub_u32 s76, s98, s54
s_bitcmp0_b32 s91, 1
s_cbranch_scc1 LSM11_%=
s_sub_u32 s90, s13, s76
s_sub_u32 s90, s90, 12
s_cmp_gt_i32 s90, 0
s_cbranch_scc1 LSM11_%=
buffer_load_dwordx4 v[80:83], v248, s[20:23], s36 offen offset:0 sc1
buffer_load_dwordx4 v[84:87], v248, s[20:23], s36 offen offset:1024 sc1
s_bitset1_b32 s58, 1
LSM11_%=:
s_bitcmp0_b32 s91, 3
s_cbranch_scc1 LSM13_%=
s_sub_u32 s90, s15, s76
s_sub_u32 s90, s90, 63
s_cmp_gt_i32 s90, 0
s_cbranch_scc1 LSM13_%=
buffer_load_dwordx4 v[112:115], v248, s[20:23], s38 offen offset:0 sc1
buffer_load_dwordx4 v[116:119], v248, s[20:23], s38 offen offset:1024 sc1
s_bitset1_b32 s58, 3
LSM13_%=:
s_bitcmp0_b32 s91, 2
s_cbranch_scc1 LSM12_%=
s_sub_u32 s90, s14, s76
s_sub_u32 s90, s90, 97
s_cmp_gt_i32 s90, 0
s_cbranch_scc1 LSM12_%=
buffer_load_dwordx4 v[88:91], v248, s[20:23], s37 offen offset:0 sc1
buffer_load_dwordx4 v[92:95], v248, s[20:23], s37 offen offset:1024 sc1
s_bitset1_b32 s58, 2
LSM12_%=:
s_bitcmp0_b32 s91, 0
s_cbranch_scc1 LSM10_%=
s_sub_u32 s90, s12, s76
s_sub_u32 s90, s90, 131
s_cmp_gt_i32 s90, 0
s_cbranch_scc1 LSM10_%=
buffer_load_dwordx4 v[120:123], v250, s[20:23], s39 offen offset:0 sc1
buffer_load_dwordx4 v[124:127], v250, s[20:23], s39 offen offset:256 sc1
s_bitset1_b32 s58, 0
LSM10_%=:
LSK1_%=:
v_lshlrev_b32 v176, 16, v20
v_and_b32 v177, 0xffff0000, v20
v_lshlrev_b32 v178, 16, v21
v_and_b32 v179, 0xffff0000, v21
v_lshlrev_b32 v180, 16, v22
v_and_b32 v181, 0xffff0000, v22
v_lshlrev_b32 v182, 16, v23
v_and_b32 v183, 0xffff0000, v23
v_mul_f32 v136, 0xbfb8aa3b, v176
v_mul_f32 v137, 0xbfb8aa3b, v177
v_mul_f32 v138, 0xbfb8aa3b, v178
v_mul_f32 v139, 0xbfb8aa3b, v179
v_mul_f32 v140, 0xbfb8aa3b, v180
v_mul_f32 v141, 0xbfb8aa3b, v181
v_mul_f32 v142, 0xbfb8aa3b, v182
v_mul_f32 v143, 0xbfb8aa3b, v183
EX v136,v136
EX v137,v137
EX v138,v138
EX v139,v139
EX v140,v140
EX v141,v141
EX v142,v142
EX v143,v143
v_add_f32 v136, 1.0, v136
v_add_f32 v137, 1.0, v137
v_add_f32 v138, 1.0, v138
v_add_f32 v139, 1.0, v139
v_add_f32 v140, 1.0, v140
v_add_f32 v141, 1.0, v141
v_add_f32 v142, 1.0, v142
v_add_f32 v143, 1.0, v143
v_rcp_f32 v136, v136
v_rcp_f32 v137, v137
v_rcp_f32 v138, v138
v_rcp_f32 v139, v139
s_waitcnt vmcnt(12)
v_cmp_eq_u32 s[8:9], s69, v149
v_subrev_u32 v226, s56, v148
s_nop 0
v_cndmask_b32 v226, 4.0, v226, s[8:9]
s_nop 0
v_readlane_b32 s13, v226, 16
v_readlane_b32 s14, v226, 32
v_readlane_b32 s15, v226, 48
s_max_i32 s12, s13, s14
s_max_i32 s12, s12, s15
s_max_i32 s12, s12, 0
s_cmp_eq_u32 s58, 15
s_cbranch_scc1 LSK2_%=
s_memrealtime s[98:99]
s_andn2_b32 s91, s17, s58
s_mov_b32 s77, 0x7fffffff
s_waitcnt lgkmcnt(0)
s_sub_u32 s76, s98, s54
s_bitcmp0_b32 s91, 1
s_cbranch_scc1 LSM21_%=
s_sub_u32 s90, s13, s76
s_sub_u32 s90, s90, 12
s_cmp_gt_i32 s90, 0
s_cbranch_scc1 LSM21_%=
buffer_load_dwordx4 v[80:83], v248, s[20:23], s36 offen offset:0 sc1
buffer_load_dwordx4 v[84:87], v248, s[20:23], s36 offen offset:1024 sc1
s_bitset1_b32 s58, 1
LSM21_%=:
s_bitcmp0_b32 s91, 3
s_cbranch_scc1 LSM23_%=
s_sub_u32 s90, s15, s76
s_sub_u32 s90, s90, 63
s_cmp_gt_i32 s90, 0
s_cbranch_scc1 LSM23_%=
buffer_load_dwordx4 v[112:115], v248, s[20:23], s38 offen offset:0 sc1
buffer_load_dwordx4 v[116:119], v248, s[20:23], s38 offen offset:1024 sc1
s_bitset1_b32 s58, 3
LSM23_%=:
s_bitcmp0_b32 s91, 2
s_cbranch_scc1 LSM22_%=
s_sub_u32 s90, s14, s76
s_sub_u32 s90, s90, 97
s_cmp_gt_i32 s90, 0
s_cbranch_scc1 LSM22_%=
buffer_load_dwordx4 v[88:91], v248, s[20:23], s37 offen offset:0 sc1
buffer_load_dwordx4 v[92:95], v248, s[20:23], s37 offen offset:1024 sc1
s_bitset1_b32 s58, 2
LSM22_%=:
s_bitcmp0_b32 s91, 0
s_cbranch_scc1 LSM20_%=
s_sub_u32 s90, s12, s76
s_sub_u32 s90, s90, 131
s_cmp_gt_i32 s90, 0
s_cbranch_scc1 LSM20_%=
buffer_load_dwordx4 v[120:123], v250, s[20:23], s39 offen offset:0 sc1
buffer_load_dwordx4 v[124:127], v250, s[20:23], s39 offen offset:256 sc1
s_bitset1_b32 s58, 0
LSM20_%=:
LSK2_%=:
v_rcp_f32 v140, v140
v_rcp_f32 v141, v141
v_rcp_f32 v142, v142
v_rcp_f32 v143, v143
v_mul_f32 v136, v176, v136
v_mul_f32 v137, v177, v137
v_mul_f32 v138, v178, v138
v_mul_f32 v139, v179, v139
v_mul_f32 v140, v180, v140
v_mul_f32 v141, v181, v141
v_mul_f32 v142, v182, v142
v_mul_f32 v143, v183, v143
CV v20,v136,v137
CV v21,v138,v139
CV v22,v140,v141
CV v23,v142,v143
v_lshlrev_b32 v176, 16, v20
v_and_b32 v177, 0xffff0000, v20
v_lshlrev_b32 v178, 16, v21
v_and_b32 v179, 0xffff0000, v21
v_lshlrev_b32 v180, 16, v22
v_and_b32 v181, 0xffff0000, v22
v_lshlrev_b32 v182, 16, v23
v_and_b32 v183, 0xffff0000, v23
v_lshlrev_b32 v192, 16, v24
v_and_b32 v193, 0xffff0000, v24
v_lshlrev_b32 v194, 16, v25
v_and_b32 v195, 0xffff0000, v25
v_lshlrev_b32 v196, 16, v26
v_and_b32 v197, 0xffff0000, v26
v_lshlrev_b32 v198, 16, v27
v_and_b32 v199, 0xffff0000, v27
v_lshlrev_b32 v208, 16, v28
v_and_b32 v209, 0xffff0000, v28
v_lshlrev_b32 v210, 16, v29
v_and_b32 v211, 0xffff0000, v29
v_lshlrev_b32 v212, 16, v30
v_and_b32 v213, 0xffff0000, v30
v_lshlrev_b32 v214, 16, v31
v_and_b32 v215, 0xffff0000, v31
s_mov_b32 s73, 30000
s_cmp_lg_u32 s58, 15
s_cbranch_scc1 LSI_%=
s_waitcnt vmcnt(0)
v_max3_u32 v236, v123, v127, v83
v_max3_u32 v237, v87, v91, v95
v_max3_u32 v236, v236, v115, v119
v_max_u32 v236, v236, v237
v_cmp_eq_u32 vcc, -1, v236
s_cbranch_vccnz LSI_%=
LSF_%=:
s_waitcnt lgkmcnt(0)
MM v[152:155],v[48:51],v[120:123],0
MM v[152:155],v[52:55],v[124:127],v[152:155]
MM v[0:3],v[58:61],v[88:91],v[0:3]
v_lshlrev_b32 v128, 16, v150
v_and_b32 v129, 0xffff0000, v150
MM v[0:3],v[58:61],v[112:115],v[0:3]
v_lshlrev_b32 v130, 16, v151
v_and_b32 v131, 0xffff0000, v151
MM v[0:3],v[58:61],v[80:83],v[0:3]
v_lshlrev_b32 v132, 16, v62
v_and_b32 v133, 0xffff0000, v62
MM v[4:7],v[56:59],v[88:91],v[4:7]
v_lshlrev_b32 v134, 16, v63
v_and_b32 v135, 0xffff0000, v63
MM v[4:7],v[56:59],v[112:115],v[4:7]
CV v236,v152,v153
CV v237,v154,v155
MM v[4:7],v[56:59],v[80:83],v[4:7]
ds_bpermute_b32 v236, v232, v236
ds_bpermute_b32 v237, v232, v237
MM v[8:11],v[58:61],v[92:95],v[8:11]
MM v[8:11],v[58:61],v[116:119],v[8:11]
MM v[8:11],v[58:61],v[84:87],v[8:11]
s_add_u32 s80, s18, 20480
MM v[12:15],v[56:59],v[92:95],v[12:15]
MM v[12:15],v[56:59],v[116:119],v[12:15]
CV v32,v0,v1
CV v33,v2,v3
MM v[12:15],v[56:59],v[84:87],v[12:15]
CV v34,v4,v5
CV v35,v6,v7
ds_write2_b64 v224, v[32:33], v[34:35] offset0:0 offset1:4
RD v[40:43],v225 offset:0
CV v36,v8,v9
CV v37,v10,v11
s_nop 1
CV v38,v12,v13
CV v39,v14,v15
ds_write2_b64 v224, v[36:37], v[38:39] offset0:160 offset1:164
RD v[44:47],v225 offset:1280
s_waitcnt lgkmcnt(4)
v_lshlrev_b32 v152, 16, v236
v_and_b32 v153, 0xffff0000, v236
v_lshlrev_b32 v154, 16, v237
v_and_b32 v155, 0xffff0000, v237
v_pk_mul_f32 v[152:153], v[152:153], v[128:129]
v_pk_mul_f32 v[154:155], v[154:155], v[130:131]
CV v236,v152,v153
CV v237,v154,v155
v_lshlrev_b32 v152, 16, v236
v_and_b32 v153, 0xffff0000, v236
v_lshlrev_b32 v154, 16, v237
v_and_b32 v155, 0xffff0000, v237
v_pk_add_f32 v[152:153], v[152:153], v[132:133]
v_pk_add_f32 v[154:155], v[154:155], v[134:135]
CV v238,v152,v153
CV v239,v154,v155
buffer_store_dwordx2 v[238:239], v254, s[40:43], s63 offen offset:0 nt
s_nop 0
s_waitcnt lgkmcnt(2)
v_lshlrev_b32 v0, 16, v40
v_and_b32 v1, 0xffff0000, v40
v_lshlrev_b32 v2, 16, v41
v_and_b32 v3, 0xffff0000, v41
v_lshlrev_b32 v4, 16, v42
v_and_b32 v5, 0xffff0000, v42
v_lshlrev_b32 v6, 16, v43
v_and_b32 v7, 0xffff0000, v43
v_pk_mul_f32 v[0:1], v[0:1], v[160:161]
v_pk_mul_f32 v[2:3], v[2:3], v[162:163]
v_pk_mul_f32 v[4:5], v[4:5], v[164:165]
v_pk_mul_f32 v[6:7], v[6:7], v[166:167]
CV v40,v0,v1
CV v41,v2,v3
CV v42,v4,v5
CV v43,v6,v7
v_lshlrev_b32 v0, 16, v40
v_and_b32 v1, 0xffff0000, v40
v_lshlrev_b32 v2, 16, v41
v_and_b32 v3, 0xffff0000, v41
v_lshlrev_b32 v4, 16, v42
v_and_b32 v5, 0xffff0000, v42
v_lshlrev_b32 v6, 16, v43
v_and_b32 v7, 0xffff0000, v43
v_pk_add_f32 v[0:1], v[0:1], v[192:193]
v_pk_add_f32 v[2:3], v[2:3], v[194:195]
v_pk_add_f32 v[4:5], v[4:5], v[196:197]
v_pk_add_f32 v[6:7], v[6:7], v[198:199]
s_nop 0
LPG30_%=:
s_cbranch_execz LTR1_%=
CV v40,v0,v1
CV v41,v2,v3
CV v42,v4,v5
CV v43,v6,v7
buffer_store_dwordx4 v[40:43], v251, s[40:43], s18 offen offset:0 nt
s_waitcnt lgkmcnt(0)
v_lshlrev_b32 v8, 16, v44
v_and_b32 v9, 0xffff0000, v44
v_lshlrev_b32 v10, 16, v45
v_and_b32 v11, 0xffff0000, v45
v_lshlrev_b32 v12, 16, v46
v_and_b32 v13, 0xffff0000, v46
v_lshlrev_b32 v14, 16, v47
v_and_b32 v15, 0xffff0000, v47
v_pk_mul_f32 v[8:9], v[8:9], v[176:177]
v_pk_mul_f32 v[10:11], v[10:11], v[178:179]
)ASM"
R"ASM(
v_pk_mul_f32 v[12:13], v[12:13], v[180:181]
v_pk_mul_f32 v[14:15], v[14:15], v[182:183]
CV v44,v8,v9
CV v45,v10,v11
CV v46,v12,v13
CV v47,v14,v15
v_lshlrev_b32 v8, 16, v44
v_and_b32 v9, 0xffff0000, v44
v_lshlrev_b32 v10, 16, v45
v_and_b32 v11, 0xffff0000, v45
v_lshlrev_b32 v12, 16, v46
v_and_b32 v13, 0xffff0000, v46
v_lshlrev_b32 v14, 16, v47
v_and_b32 v15, 0xffff0000, v47
v_pk_add_f32 v[8:9], v[8:9], v[208:209]
v_pk_add_f32 v[10:11], v[10:11], v[210:211]
v_pk_add_f32 v[12:13], v[12:13], v[212:213]
v_pk_add_f32 v[14:15], v[14:15], v[214:215]
CV v44,v8,v9
CV v45,v10,v11
CV v46,v12,v13
CV v47,v14,v15
buffer_store_dwordx4 v[44:47], v251, s[40:43], s80 offen offset:0 nt
s_waitcnt vmcnt(0)
s_endpgm
LSI_%=:
s_memrealtime s[98:99]
s_andn2_b32 s91, s17, s58
s_mov_b32 s77, 0x7fffffff
s_waitcnt lgkmcnt(0)
s_sub_u32 s76, s98, s54
s_bitcmp0_b32 s91, 1
s_cbranch_scc1 LSM1_%=
s_sub_u32 s90, s13, s76
s_sub_u32 s90, s90, 12
s_cmp_gt_i32 s90, 0
s_cbranch_scc1 LSN1_%=
buffer_load_dwordx4 v[80:83], v248, s[20:23], s36 offen offset:0 sc1
buffer_load_dwordx4 v[84:87], v248, s[20:23], s36 offen offset:1024 sc1
s_bitset1_b32 s58, 1
s_branch LSM1_%=
LSN1_%=:
s_min_i32 s77, s77, s90
LSM1_%=:
s_bitcmp0_b32 s91, 3
s_cbranch_scc1 LSM3_%=
s_sub_u32 s90, s15, s76
s_sub_u32 s90, s90, 63
s_cmp_gt_i32 s90, 0
s_cbranch_scc1 LSN3_%=
buffer_load_dwordx4 v[112:115], v248, s[20:23], s38 offen offset:0 sc1
buffer_load_dwordx4 v[116:119], v248, s[20:23], s38 offen offset:1024 sc1
s_bitset1_b32 s58, 3
s_branch LSM3_%=
LSN3_%=:
s_min_i32 s77, s77, s90
LSM3_%=:
s_bitcmp0_b32 s91, 2
s_cbranch_scc1 LSM2_%=
s_sub_u32 s90, s14, s76
s_sub_u32 s90, s90, 97
s_cmp_gt_i32 s90, 0
s_cbranch_scc1 LSN2_%=
buffer_load_dwordx4 v[88:91], v248, s[20:23], s37 offen offset:0 sc1
buffer_load_dwordx4 v[92:95], v248, s[20:23], s37 offen offset:1024 sc1
s_bitset1_b32 s58, 2
s_branch LSM2_%=
LSN2_%=:
s_min_i32 s77, s77, s90
LSM2_%=:
s_bitcmp0_b32 s91, 0
s_cbranch_scc1 LSM0_%=
s_sub_u32 s90, s12, s76
s_sub_u32 s90, s90, 131
s_cmp_gt_i32 s90, 0
s_cbranch_scc1 LSN0_%=
buffer_load_dwordx4 v[120:123], v250, s[20:23], s39 offen offset:0 sc1
buffer_load_dwordx4 v[124:127], v250, s[20:23], s39 offen offset:256 sc1
s_bitset1_b32 s58, 0
s_branch LSM0_%=
LSN0_%=:
s_min_i32 s77, s77, s90
LSM0_%=:
s_cmp_eq_u32 s12, 4.0
s_cselect_b32 s90, 30, 45
s_cmp_lt_i32 s77, s90
s_cbranch_scc1 LSI_%=
buffer_load_dwordx2 v[148:149], v253, s[20:23], s68 offen offset:8 sc1
s_waitcnt vmcnt(1)
s_bitcmp0_b32 s58, 1
s_cbranch_scc1 LSB1_%=
v_max_u32 v237, v83, v87
v_cmp_eq_u32 vcc, -1, v237
s_cbranch_vccnz LSB1_%=
s_bitset0_b32 s17, 1
LSB1_%=:
s_bitcmp0_b32 s58, 3
s_cbranch_scc1 LSB3_%=
v_max_u32 v239, v115, v119
v_cmp_eq_u32 vcc, -1, v239
s_cbranch_vccnz LSB3_%=
s_bitset0_b32 s17, 3
LSB3_%=:
s_bitcmp0_b32 s58, 2
s_cbranch_scc1 LSB2_%=
v_max_u32 v238, v91, v95
v_cmp_eq_u32 vcc, -1, v238
s_cbranch_vccnz LSB2_%=
s_bitset0_b32 s17, 2
LSB2_%=:
s_bitcmp0_b32 s58, 0
s_cbranch_scc1 LSB0_%=
v_max_u32 v236, v123, v127
v_cmp_eq_u32 vcc, -1, v236
s_cbranch_vccnz LSB0_%=
s_bitset0_b32 s17, 0
LSB0_%=:
s_cmp_eq_u32 s17, 0
s_cbranch_scc1 LSF_%=
s_mov_b32 s58, 0
s_waitcnt vmcnt(0)
v_cmp_eq_u32 s[8:9], s69, v149
v_subrev_u32 v226, s56, v148
s_nop 0
v_cndmask_b32 v226, 4.0, v226, s[8:9]
s_nop 0
v_readlane_b32 s13, v226, 16
v_readlane_b32 s14, v226, 32
v_readlane_b32 s15, v226, 48
s_max_i32 s12, s13, s14
s_max_i32 s12, s12, s15
s_max_i32 s12, s12, 0
s_sub_u32 s73, s73, 1
s_cbranch_scc0 LSI_%=
LSE_%=:
s_waitcnt vmcnt(0)
s_endpgm
LRG1o_%=:
s_mov_b32 s70, 30000
LRG1ol_%=:
s_sleep 2
buffer_load_dwordx4 v[200:203], v208, s[20:23], s62 offen offset:0 sc1
buffer_load_dwordx4 v[204:207], v208, s[20:23], s93 offen offset:0 sc1
s_waitcnt vmcnt(0)
v_xor_b32 v201, s69, v201
v_xor_b32 v203, s69, v203
v_xor_b32 v205, s69, v205
v_xor_b32 v207, s69, v207
v_or3_b32 v209, v201, v203, v205
v_or_b32 v209, v209, v207
v_cmp_ne_u32 vcc, 0, v209
s_cbranch_vccz LRG1ob_%=
s_sub_u32 s70, s70, 1
s_cbranch_scc0 LRG1ol_%=
s_branch LRG1ob_%=
LRFR_%=:
s_mov_b32 s91, 30000
LRFRl_%=:
s_sleep 2
buffer_load_dwordx4 v[56:59], v248, s[20:23], s90 offen offset:0 sc1
s_waitcnt vmcnt(0)
v_xor_b32 v56, s69, v56
v_xor_b32 v57, s69, v57
v_xor_b32 v58, s69, v58
v_xor_b32 v59, s69, v59
v_or3_b32 v252, v56, v57, v58
v_or_b32 v252, v252, v59
v_cmp_ne_u32 vcc, 0, v252
s_cbranch_vccz LRFRb_%=
s_sub_u32 s91, s91, 1
s_cbranch_scc0 LRFRl_%=
s_branch LRFRb_%=
LRRK_%=:
s_mov_b32 s91, 30000
LRRKl_%=:
s_sleep 2
buffer_load_dwordx2 v[32:33], v34, s[20:23], s90 offen offset:0 sc1
s_waitcnt vmcnt(0)
v_cmp_ne_u32 vcc, s69, v33
s_cbranch_vccz LRRKb_%=
s_sub_u32 s91, s91, 1
s_cbranch_scc0 LRRKl_%=
s_branch LRRKb_%=
LRK_%=:
s_cmp_ge_u32 s60, 8
s_cbranch_scc1 LRK0_%=
v_mov_b32 v253, 0
v_cmp_lt_u32 vcc, s52, v96
v_cmp_lt_u32 s[98:99], s52, v97
s_nop 0
v_addc_co_u32 v253, vcc, 0, v253, vcc
v_addc_co_u32 v253, s[98:99], 0, v253, s[98:99]
v_cmp_lt_u32 vcc, s52, v98
v_cmp_lt_u32 s[98:99], s52, v99
s_nop 0
v_addc_co_u32 v253, vcc, 0, v253, vcc
v_addc_co_u32 v253, s[98:99], 0, v253, s[98:99]
v_cmp_lt_u32 vcc, s52, v100
v_cmp_lt_u32 s[98:99], s52, v101
s_nop 0
v_addc_co_u32 v253, vcc, 0, v253, vcc
v_addc_co_u32 v253, s[98:99], 0, v253, s[98:99]
v_cmp_lt_u32 vcc, s52, v102
v_cmp_lt_u32 s[98:99], s52, v103
s_nop 0
v_addc_co_u32 v253, vcc, 0, v253, vcc
v_addc_co_u32 v253, s[98:99], 0, v253, s[98:99]
v_cmp_lt_u32 vcc, s52, v104
v_cmp_lt_u32 s[98:99], s52, v105
s_nop 0
v_addc_co_u32 v253, vcc, 0, v253, vcc
v_addc_co_u32 v253, s[98:99], 0, v253, s[98:99]
v_cmp_lt_u32 vcc, s52, v106
v_cmp_lt_u32 s[98:99], s52, v107
s_nop 0
v_addc_co_u32 v253, vcc, 0, v253, vcc
v_addc_co_u32 v253, s[98:99], 0, v253, s[98:99]
v_cmp_lt_u32 vcc, s52, v108
v_cmp_lt_u32 s[98:99], s52, v109
s_nop 0
v_addc_co_u32 v253, vcc, 0, v253, vcc
v_addc_co_u32 v253, s[98:99], 0, v253, s[98:99]
v_cmp_lt_u32 vcc, s52, v110
v_cmp_lt_u32 s[98:99], s52, v111
s_nop 0
v_addc_co_u32 v253, vcc, 0, v253, vcc
v_addc_co_u32 v253, s[98:99], 0, v253, s[98:99]
v_cmp_lt_u32 vcc, s52, v112
v_cmp_lt_u32 s[98:99], s52, v113
s_nop 0
v_addc_co_u32 v253, vcc, 0, v253, vcc
v_addc_co_u32 v253, s[98:99], 0, v253, s[98:99]
v_cmp_lt_u32 vcc, s52, v114
v_cmp_lt_u32 s[98:99], s52, v115
s_nop 0
v_addc_co_u32 v253, vcc, 0, v253, vcc
v_addc_co_u32 v253, s[98:99], 0, v253, s[98:99]
v_cmp_lt_u32 vcc, s52, v116
v_cmp_lt_u32 s[98:99], s52, v117
s_nop 0
v_addc_co_u32 v253, vcc, 0, v253, vcc
v_addc_co_u32 v253, s[98:99], 0, v253, s[98:99]
v_cmp_lt_u32 vcc, s52, v118
v_cmp_lt_u32 s[98:99], s52, v119
s_nop 0
v_addc_co_u32 v253, vcc, 0, v253, vcc
v_addc_co_u32 v253, s[98:99], 0, v253, s[98:99]
v_cmp_lt_u32 vcc, s52, v120
v_cmp_lt_u32 s[98:99], s52, v121
s_nop 0
v_addc_co_u32 v253, vcc, 0, v253, vcc
v_addc_co_u32 v253, s[98:99], 0, v253, s[98:99]
v_cmp_lt_u32 vcc, s52, v122
v_cmp_lt_u32 s[98:99], s52, v123
s_nop 0
v_addc_co_u32 v253, vcc, 0, v253, vcc
v_addc_co_u32 v253, s[98:99], 0, v253, s[98:99]
v_cmp_lt_u32 vcc, s52, v124
v_cmp_lt_u32 s[98:99], s52, v125
s_nop 0
v_addc_co_u32 v253, vcc, 0, v253, vcc
v_addc_co_u32 v253, s[98:99], 0, v253, s[98:99]
v_cmp_lt_u32 vcc, s52, v126
v_cmp_lt_u32 s[98:99], s52, v127
s_nop 0
v_addc_co_u32 v253, vcc, 0, v253, vcc
v_addc_co_u32 v253, s[98:99], 0, v253, s[98:99]
s_nop 1
v_add_u32_dpp v253, v253, v253 row_ror:8 row_mask:0xf bank_mask:0xf
s_nop 1
v_add_u32_dpp v253, v253, v253 row_ror:4 row_mask:0xf bank_mask:0xf
s_nop 1
v_add_u32_dpp v253, v253, v253 row_ror:2 row_mask:0xf bank_mask:0xf
s_nop 1
v_add_u32_dpp v253, v253, v253 row_ror:1 row_mask:0xf bank_mask:0xf
v_mov_b32 v250, v253
s_nop 1
v_permlane16_swap_b32 v253, v250
v_add_u32 v253, v253, v250
v_mov_b32 v250, v253
s_nop 1
v_permlane32_swap_b32 v253, v250
v_add_u32 v253, v253, v250
v_cmp_gt_u32 vcc, 8, v253
v_mov_b32 v250, s52
v_mov_b32 v251, s69
v_lshlrev_b32 v253, 3, v253
s_mov_b32 s76, 94208
s_cbranch_vccz LRK0_%=
s_mov_b64 exec, 1
buffer_store_dwordx2 v[250:251], v253, s[20:23], s76 offen offset:0 sc1
s_mov_b64 exec, -1
s_nop 1
LRK0_%=:
s_cmp_ge_u32 s62, 8
s_cbranch_scc1 LRK1_%=
v_mov_b32 v253, 0
v_cmp_lt_u32 vcc, s53, v96
v_cmp_lt_u32 s[98:99], s53, v97
s_nop 0
v_addc_co_u32 v253, vcc, 0, v253, vcc
v_addc_co_u32 v253, s[98:99], 0, v253, s[98:99]
v_cmp_lt_u32 vcc, s53, v98
v_cmp_lt_u32 s[98:99], s53, v99
s_nop 0
v_addc_co_u32 v253, vcc, 0, v253, vcc
v_addc_co_u32 v253, s[98:99], 0, v253, s[98:99]
v_cmp_lt_u32 vcc, s53, v100
v_cmp_lt_u32 s[98:99], s53, v101
s_nop 0
v_addc_co_u32 v253, vcc, 0, v253, vcc
v_addc_co_u32 v253, s[98:99], 0, v253, s[98:99]
v_cmp_lt_u32 vcc, s53, v102
v_cmp_lt_u32 s[98:99], s53, v103
s_nop 0
v_addc_co_u32 v253, vcc, 0, v253, vcc
v_addc_co_u32 v253, s[98:99], 0, v253, s[98:99]
v_cmp_lt_u32 vcc, s53, v104
v_cmp_lt_u32 s[98:99], s53, v105
s_nop 0
v_addc_co_u32 v253, vcc, 0, v253, vcc
v_addc_co_u32 v253, s[98:99], 0, v253, s[98:99]
v_cmp_lt_u32 vcc, s53, v106
v_cmp_lt_u32 s[98:99], s53, v107
s_nop 0
v_addc_co_u32 v253, vcc, 0, v253, vcc
v_addc_co_u32 v253, s[98:99], 0, v253, s[98:99]
v_cmp_lt_u32 vcc, s53, v108
v_cmp_lt_u32 s[98:99], s53, v109
s_nop 0
v_addc_co_u32 v253, vcc, 0, v253, vcc
v_addc_co_u32 v253, s[98:99], 0, v253, s[98:99]
v_cmp_lt_u32 vcc, s53, v110
v_cmp_lt_u32 s[98:99], s53, v111
s_nop 0
v_addc_co_u32 v253, vcc, 0, v253, vcc
v_addc_co_u32 v253, s[98:99], 0, v253, s[98:99]
v_cmp_lt_u32 vcc, s53, v112
v_cmp_lt_u32 s[98:99], s53, v113
s_nop 0
v_addc_co_u32 v253, vcc, 0, v253, vcc
v_addc_co_u32 v253, s[98:99], 0, v253, s[98:99]
v_cmp_lt_u32 vcc, s53, v114
v_cmp_lt_u32 s[98:99], s53, v115
s_nop 0
v_addc_co_u32 v253, vcc, 0, v253, vcc
v_addc_co_u32 v253, s[98:99], 0, v253, s[98:99]
v_cmp_lt_u32 vcc, s53, v116
v_cmp_lt_u32 s[98:99], s53, v117
s_nop 0
v_addc_co_u32 v253, vcc, 0, v253, vcc
v_addc_co_u32 v253, s[98:99], 0, v253, s[98:99]
v_cmp_lt_u32 vcc, s53, v118
v_cmp_lt_u32 s[98:99], s53, v119
s_nop 0
v_addc_co_u32 v253, vcc, 0, v253, vcc
v_addc_co_u32 v253, s[98:99], 0, v253, s[98:99]
v_cmp_lt_u32 vcc, s53, v120
v_cmp_lt_u32 s[98:99], s53, v121
s_nop 0
v_addc_co_u32 v253, vcc, 0, v253, vcc
v_addc_co_u32 v253, s[98:99], 0, v253, s[98:99]
v_cmp_lt_u32 vcc, s53, v122
v_cmp_lt_u32 s[98:99], s53, v123
s_nop 0
v_addc_co_u32 v253, vcc, 0, v253, vcc
v_addc_co_u32 v253, s[98:99], 0, v253, s[98:99]
v_cmp_lt_u32 vcc, s53, v124
v_cmp_lt_u32 s[98:99], s53, v125
s_nop 0
v_addc_co_u32 v253, vcc, 0, v253, vcc
v_addc_co_u32 v253, s[98:99], 0, v253, s[98:99]
v_cmp_lt_u32 vcc, s53, v126
v_cmp_lt_u32 s[98:99], s53, v127
s_nop 0
v_addc_co_u32 v253, vcc, 0, v253, vcc
v_addc_co_u32 v253, s[98:99], 0, v253, s[98:99]
s_nop 1
v_add_u32_dpp v253, v253, v253 row_ror:8 row_mask:0xf bank_mask:0xf
s_nop 1
v_add_u32_dpp v253, v253, v253 row_ror:4 row_mask:0xf bank_mask:0xf
s_nop 1
v_add_u32_dpp v253, v253, v253 row_ror:2 row_mask:0xf bank_mask:0xf
s_nop 1
v_add_u32_dpp v253, v253, v253 row_ror:1 row_mask:0xf bank_mask:0xf
v_mov_b32 v250, v253
s_nop 1
v_permlane16_swap_b32 v253, v250
v_add_u32 v253, v253, v250
v_mov_b32 v250, v253
s_nop 1
v_permlane32_swap_b32 v253, v250
v_add_u32 v253, v253, v250
v_cmp_gt_u32 vcc, 8, v253
v_mov_b32 v250, s53
v_mov_b32 v251, s69
v_lshlrev_b32 v253, 3, v253
s_mov_b32 s76, 94208
s_cbranch_vccz LRK1_%=
s_mov_b64 exec, 1
buffer_store_dwordx2 v[250:251], v253, s[20:23], s76 offen offset:0 sc1
s_mov_b64 exec, -1
s_nop 1
LRK1_%=:
s_cmp_ge_u32 s56, 8
s_cbranch_scc1 LRK2_%=
v_mov_b32 v253, 0
v_cmp_lt_u32 vcc, s54, v96
v_cmp_lt_u32 s[98:99], s54, v97
s_nop 0
v_addc_co_u32 v253, vcc, 0, v253, vcc
v_addc_co_u32 v253, s[98:99], 0, v253, s[98:99]
v_cmp_lt_u32 vcc, s54, v98
v_cmp_lt_u32 s[98:99], s54, v99
s_nop 0
v_addc_co_u32 v253, vcc, 0, v253, vcc
v_addc_co_u32 v253, s[98:99], 0, v253, s[98:99]
v_cmp_lt_u32 vcc, s54, v100
v_cmp_lt_u32 s[98:99], s54, v101
s_nop 0
v_addc_co_u32 v253, vcc, 0, v253, vcc
v_addc_co_u32 v253, s[98:99], 0, v253, s[98:99]
v_cmp_lt_u32 vcc, s54, v102
v_cmp_lt_u32 s[98:99], s54, v103
s_nop 0
v_addc_co_u32 v253, vcc, 0, v253, vcc
v_addc_co_u32 v253, s[98:99], 0, v253, s[98:99]
v_cmp_lt_u32 vcc, s54, v104
v_cmp_lt_u32 s[98:99], s54, v105
s_nop 0
v_addc_co_u32 v253, vcc, 0, v253, vcc
v_addc_co_u32 v253, s[98:99], 0, v253, s[98:99]
)ASM"
R"ASM(
v_cmp_lt_u32 vcc, s54, v106
v_cmp_lt_u32 s[98:99], s54, v107
s_nop 0
v_addc_co_u32 v253, vcc, 0, v253, vcc
v_addc_co_u32 v253, s[98:99], 0, v253, s[98:99]
v_cmp_lt_u32 vcc, s54, v108
v_cmp_lt_u32 s[98:99], s54, v109
s_nop 0
v_addc_co_u32 v253, vcc, 0, v253, vcc
v_addc_co_u32 v253, s[98:99], 0, v253, s[98:99]
v_cmp_lt_u32 vcc, s54, v110
v_cmp_lt_u32 s[98:99], s54, v111
s_nop 0
v_addc_co_u32 v253, vcc, 0, v253, vcc
v_addc_co_u32 v253, s[98:99], 0, v253, s[98:99]
v_cmp_lt_u32 vcc, s54, v112
v_cmp_lt_u32 s[98:99], s54, v113
s_nop 0
v_addc_co_u32 v253, vcc, 0, v253, vcc
v_addc_co_u32 v253, s[98:99], 0, v253, s[98:99]
v_cmp_lt_u32 vcc, s54, v114
v_cmp_lt_u32 s[98:99], s54, v115
s_nop 0
v_addc_co_u32 v253, vcc, 0, v253, vcc
v_addc_co_u32 v253, s[98:99], 0, v253, s[98:99]
v_cmp_lt_u32 vcc, s54, v116
v_cmp_lt_u32 s[98:99], s54, v117
s_nop 0
v_addc_co_u32 v253, vcc, 0, v253, vcc
v_addc_co_u32 v253, s[98:99], 0, v253, s[98:99]
v_cmp_lt_u32 vcc, s54, v118
v_cmp_lt_u32 s[98:99], s54, v119
s_nop 0
v_addc_co_u32 v253, vcc, 0, v253, vcc
v_addc_co_u32 v253, s[98:99], 0, v253, s[98:99]
v_cmp_lt_u32 vcc, s54, v120
v_cmp_lt_u32 s[98:99], s54, v121
s_nop 0
v_addc_co_u32 v253, vcc, 0, v253, vcc
v_addc_co_u32 v253, s[98:99], 0, v253, s[98:99]
v_cmp_lt_u32 vcc, s54, v122
v_cmp_lt_u32 s[98:99], s54, v123
s_nop 0
v_addc_co_u32 v253, vcc, 0, v253, vcc
v_addc_co_u32 v253, s[98:99], 0, v253, s[98:99]
v_cmp_lt_u32 vcc, s54, v124
v_cmp_lt_u32 s[98:99], s54, v125
s_nop 0
v_addc_co_u32 v253, vcc, 0, v253, vcc
v_addc_co_u32 v253, s[98:99], 0, v253, s[98:99]
v_cmp_lt_u32 vcc, s54, v126
v_cmp_lt_u32 s[98:99], s54, v127
s_nop 0
v_addc_co_u32 v253, vcc, 0, v253, vcc
v_addc_co_u32 v253, s[98:99], 0, v253, s[98:99]
s_nop 1
v_add_u32_dpp v253, v253, v253 row_ror:8 row_mask:0xf bank_mask:0xf
s_nop 1
v_add_u32_dpp v253, v253, v253 row_ror:4 row_mask:0xf bank_mask:0xf
s_nop 1
v_add_u32_dpp v253, v253, v253 row_ror:2 row_mask:0xf bank_mask:0xf
s_nop 1
v_add_u32_dpp v253, v253, v253 row_ror:1 row_mask:0xf bank_mask:0xf
v_mov_b32 v250, v253
s_nop 1
v_permlane16_swap_b32 v253, v250
v_add_u32 v253, v253, v250
v_mov_b32 v250, v253
s_nop 1
v_permlane32_swap_b32 v253, v250
v_add_u32 v253, v253, v250
v_cmp_gt_u32 vcc, 8, v253
v_mov_b32 v250, s54
v_mov_b32 v251, s69
v_lshlrev_b32 v253, 3, v253
s_mov_b32 s76, 94208
s_cbranch_vccz LRK2_%=
s_mov_b64 exec, 1
buffer_store_dwordx2 v[250:251], v253, s[20:23], s76 offen offset:0 sc1
s_mov_b64 exec, -1
s_nop 1
LRK2_%=:
s_cmp_ge_u32 s58, 8
s_cbranch_scc1 LRK3_%=
v_mov_b32 v253, 0
v_cmp_lt_u32 vcc, s55, v96
v_cmp_lt_u32 s[98:99], s55, v97
s_nop 0
v_addc_co_u32 v253, vcc, 0, v253, vcc
v_addc_co_u32 v253, s[98:99], 0, v253, s[98:99]
v_cmp_lt_u32 vcc, s55, v98
v_cmp_lt_u32 s[98:99], s55, v99
s_nop 0
v_addc_co_u32 v253, vcc, 0, v253, vcc
v_addc_co_u32 v253, s[98:99], 0, v253, s[98:99]
v_cmp_lt_u32 vcc, s55, v100
v_cmp_lt_u32 s[98:99], s55, v101
s_nop 0
v_addc_co_u32 v253, vcc, 0, v253, vcc
v_addc_co_u32 v253, s[98:99], 0, v253, s[98:99]
v_cmp_lt_u32 vcc, s55, v102
v_cmp_lt_u32 s[98:99], s55, v103
s_nop 0
v_addc_co_u32 v253, vcc, 0, v253, vcc
v_addc_co_u32 v253, s[98:99], 0, v253, s[98:99]
v_cmp_lt_u32 vcc, s55, v104
v_cmp_lt_u32 s[98:99], s55, v105
s_nop 0
v_addc_co_u32 v253, vcc, 0, v253, vcc
v_addc_co_u32 v253, s[98:99], 0, v253, s[98:99]
v_cmp_lt_u32 vcc, s55, v106
v_cmp_lt_u32 s[98:99], s55, v107
s_nop 0
v_addc_co_u32 v253, vcc, 0, v253, vcc
v_addc_co_u32 v253, s[98:99], 0, v253, s[98:99]
v_cmp_lt_u32 vcc, s55, v108
v_cmp_lt_u32 s[98:99], s55, v109
s_nop 0
v_addc_co_u32 v253, vcc, 0, v253, vcc
v_addc_co_u32 v253, s[98:99], 0, v253, s[98:99]
v_cmp_lt_u32 vcc, s55, v110
v_cmp_lt_u32 s[98:99], s55, v111
s_nop 0
v_addc_co_u32 v253, vcc, 0, v253, vcc
v_addc_co_u32 v253, s[98:99], 0, v253, s[98:99]
v_cmp_lt_u32 vcc, s55, v112
v_cmp_lt_u32 s[98:99], s55, v113
s_nop 0
v_addc_co_u32 v253, vcc, 0, v253, vcc
v_addc_co_u32 v253, s[98:99], 0, v253, s[98:99]
v_cmp_lt_u32 vcc, s55, v114
v_cmp_lt_u32 s[98:99], s55, v115
s_nop 0
v_addc_co_u32 v253, vcc, 0, v253, vcc
v_addc_co_u32 v253, s[98:99], 0, v253, s[98:99]
v_cmp_lt_u32 vcc, s55, v116
v_cmp_lt_u32 s[98:99], s55, v117
s_nop 0
v_addc_co_u32 v253, vcc, 0, v253, vcc
v_addc_co_u32 v253, s[98:99], 0, v253, s[98:99]
v_cmp_lt_u32 vcc, s55, v118
v_cmp_lt_u32 s[98:99], s55, v119
s_nop 0
v_addc_co_u32 v253, vcc, 0, v253, vcc
v_addc_co_u32 v253, s[98:99], 0, v253, s[98:99]
v_cmp_lt_u32 vcc, s55, v120
v_cmp_lt_u32 s[98:99], s55, v121
s_nop 0
v_addc_co_u32 v253, vcc, 0, v253, vcc
v_addc_co_u32 v253, s[98:99], 0, v253, s[98:99]
v_cmp_lt_u32 vcc, s55, v122
v_cmp_lt_u32 s[98:99], s55, v123
s_nop 0
v_addc_co_u32 v253, vcc, 0, v253, vcc
v_addc_co_u32 v253, s[98:99], 0, v253, s[98:99]
v_cmp_lt_u32 vcc, s55, v124
v_cmp_lt_u32 s[98:99], s55, v125
s_nop 0
v_addc_co_u32 v253, vcc, 0, v253, vcc
v_addc_co_u32 v253, s[98:99], 0, v253, s[98:99]
v_cmp_lt_u32 vcc, s55, v126
v_cmp_lt_u32 s[98:99], s55, v127
s_nop 0
v_addc_co_u32 v253, vcc, 0, v253, vcc
v_addc_co_u32 v253, s[98:99], 0, v253, s[98:99]
s_nop 1
v_add_u32_dpp v253, v253, v253 row_ror:8 row_mask:0xf bank_mask:0xf
s_nop 1
v_add_u32_dpp v253, v253, v253 row_ror:4 row_mask:0xf bank_mask:0xf
s_nop 1
v_add_u32_dpp v253, v253, v253 row_ror:2 row_mask:0xf bank_mask:0xf
s_nop 1
v_add_u32_dpp v253, v253, v253 row_ror:1 row_mask:0xf bank_mask:0xf
v_mov_b32 v250, v253
s_nop 1
v_permlane16_swap_b32 v253, v250
v_add_u32 v253, v253, v250
v_mov_b32 v250, v253
s_nop 1
v_permlane32_swap_b32 v253, v250
v_add_u32 v253, v253, v250
v_cmp_gt_u32 vcc, 8, v253
v_mov_b32 v250, s55
v_mov_b32 v251, s69
v_lshlrev_b32 v253, 3, v253
s_mov_b32 s76, 94208
s_cbranch_vccz LRK3_%=
s_mov_b64 exec, 1
buffer_store_dwordx2 v[250:251], v253, s[20:23], s76 offen offset:0 sc1
s_mov_b64 exec, -1
s_nop 1
LRK3_%=:
s_branch LRKb_%=
LRIX_%=:
s_mov_b32 s91, 30000
LRIXl_%=:
s_sleep 2
v_bfe_u32 v251, v248, 4, 3
v_lshlrev_b32 v251, 3, v251
buffer_load_dwordx2 v[252:253], v251, s[20:23], s86 offen offset:0 sc1
s_waitcnt vmcnt(0)
v_cmp_ne_u32 vcc, s69, v253
s_cbranch_vccz LRIXb_%=
s_sub_u32 s91, s91, 1
s_cbranch_scc0 LRIXl_%=
s_branch LRIXb_%=
LRFG_%=:
s_mov_b32 s91, 30000
LRFGl_%=:
s_sleep 2
buffer_load_dword v252, v253, s[20:23], s76 offen offset:0 sc1
s_waitcnt vmcnt(0)
v_cmp_ne_u32 vcc, s69, v252
s_cbranch_vccz LRFGb_%=
s_sub_u32 s91, s91, 1
s_cbranch_scc0 LRFGl_%=
s_branch LRFGb_%=
LRFH_%=:
s_mov_b32 s91, 30000
LRFHl_%=:
s_sleep 2
buffer_load_dword v252, v253, s[20:23], s76 offen offset:0 sc1
s_waitcnt vmcnt(0)
v_cmp_ne_u32 vcc, s69, v252
s_cbranch_vccz LRFHb_%=
s_sub_u32 s91, s91, 1
s_cbranch_scc0 LRFHl_%=
s_branch LRFHb_%=
LRFY_%=:
s_mov_b32 s91, 30000
LRFYl_%=:
s_sleep 2
buffer_load_dword v252, v253, s[20:23], s76 offen offset:0 sc1
s_waitcnt vmcnt(0)
v_cmp_ne_u32 vcc, s69, v252
s_cbranch_vccz LRFYb_%=
s_sub_u32 s91, s91, 1
s_cbranch_scc0 LRFYl_%=
s_branch LRFYb_%=
LRPO0_%=:
s_mov_b32 s97, 30000
buffer_load_dword v242, v243, s[20:23], s44 offen offset:0 sc1
s_sleep 4
LRPO0l_%=:
buffer_load_dword v245, v243, s[20:23], s44 offen offset:0 sc1
s_waitcnt vmcnt(1)
v_cmp_ne_u32 vcc, s69, v242
s_cbranch_vccz LRPO0b_%=
buffer_load_dword v242, v243, s[20:23], s44 offen offset:0 sc1
s_waitcnt vmcnt(1)
v_cmp_ne_u32 vcc, s69, v245
s_cbranch_vccz LRPO0b_%=
s_sub_u32 s97, s97, 1
s_cbranch_scc0 LRPO0l_%=
s_branch LRPO0b_%=
LRPW_%=:
s_mov_b32 s61, 30000
LRPWl_%=:
s_sleep 2
buffer_load_dwordx2 v[96:97], v98, s[20:23], s62 offen offset:0 sc1
s_waitcnt vmcnt(0)
v_xor_b32 v96, s69, v96
v_xor_b32 v97, s69, v97
v_or_b32 v99, v96, v97
v_cmp_ne_u32 vcc, 0, v99
s_cbranch_vccz LRPWb_%=
s_sub_u32 s61, s61, 1
s_cbranch_scc0 LRPWl_%=
s_branch LRPWb_%=
LRPO1_%=:
s_mov_b32 s97, 30000
LRPO1l_%=:
s_sleep 2
buffer_load_dword v242, v243, s[20:23], s96 offen offset:16 sc1
s_waitcnt vmcnt(0)
v_cmp_ne_u32 vcc, s69, v242
s_cbranch_vccz LRPO1b_%=
s_sub_u32 s97, s97, 1
s_cbranch_scc0 LRPO1l_%=
s_branch LRPO1b_%=
LKEND_ka:
LRET_%=:
s_cmp_eq_u32 s73, 0
s_cbranch_scc1 LBACK0_%=
s_cmp_eq_u32 s73, 2
s_cbranch_scc1 LBACK2_%=
s_cmp_eq_u32 s73, 1
s_cbranch_scc1 LBACK1_%=
s_cmp_eq_u32 s73, 3
s_cbranch_scc1 LBACK3_%=
s_cmp_eq_u32 s73, 5
s_cbranch_scc1 LBACK5_%=
s_cmp_eq_u32 s73, 7
s_cbranch_scc1 LBACK7_%=
s_cmp_eq_u32 s73, 9
s_cbranch_scc1 LBACK9_%=
s_cmp_eq_u32 s73, 11
s_cbranch_scc1 LBACK11_%=
s_cmp_eq_u32 s73, 13
s_cbranch_scc1 LBACK13_%=
s_cmp_eq_u32 s73, 15
s_cbranch_scc1 LBACK15_%=
s_cmp_eq_u32 s73, 17
s_cbranch_scc1 LBACK17_%=
s_cmp_eq_u32 s73, 19
s_cbranch_scc1 LBACK19_%=
s_cmp_eq_u32 s73, 21
s_cbranch_scc1 LBACK21_%=
s_cmp_eq_u32 s73, 23
s_cbranch_scc1 LBACK23_%=
s_cmp_eq_u32 s73, 25
s_cbranch_scc1 LBACK25_%=
s_cmp_eq_u32 s73, 27
s_cbranch_scc1 LBACK27_%=
s_cmp_eq_u32 s73, 29
s_cbranch_scc1 LBACK29_%=
s_cmp_eq_u32 s73, 31
s_cbranch_scc1 LBACK31_%=
s_cmp_eq_u32 s73, 33
s_cbranch_scc1 LBACK33_%=
s_cmp_eq_u32 s73, 35
s_cbranch_scc1 LBACK35_%=
s_cmp_eq_u32 s73, 4
s_cbranch_scc1 LBACK4_%=
s_cmp_eq_u32 s73, 6
s_cbranch_scc1 LBACK6_%=
s_cmp_eq_u32 s73, 8
s_cbranch_scc1 LBACK8_%=
s_cmp_eq_u32 s73, 10
s_cbranch_scc1 LBACK10_%=
s_cmp_eq_u32 s73, 12
s_cbranch_scc1 LBACK12_%=
s_cmp_eq_u32 s73, 14
s_cbranch_scc1 LBACK14_%=
s_cmp_eq_u32 s73, 16
s_cbranch_scc1 LBACK16_%=
s_cmp_eq_u32 s73, 18
s_cbranch_scc1 LBACK18_%=
s_cmp_eq_u32 s73, 20
s_cbranch_scc1 LBACK20_%=
s_cmp_eq_u32 s73, 22
s_cbranch_scc1 LBACK22_%=
s_cmp_eq_u32 s73, 24
s_cbranch_scc1 LBACK24_%=
s_cmp_eq_u32 s73, 26
s_cbranch_scc1 LBACK26_%=
s_cmp_eq_u32 s73, 28
s_cbranch_scc1 LBACK28_%=
s_cmp_eq_u32 s73, 30
s_cbranch_scc1 LBACK30_%=
s_cmp_eq_u32 s73, 32
s_cbranch_scc1 LBACK32_%=
s_cmp_eq_u32 s73, 34
s_cbranch_scc1 LBACK34_%=
s_cmp_eq_u32 s73, 36
s_cbranch_scc1 LBACK36_%=
s_cmp_eq_u32 s73, 38
s_cbranch_scc1 LBACK38_%=
s_cmp_eq_u32 s73, 37
s_cbranch_scc1 LBACK37_%=
s_endpgm
LSITE0_%=:
s_mov_b32 s73, 0
s_mov_b32 s74, 0
s_mov_b32 s75, 16384
s_branch LRARE002_%=
LSITE2_%=:
s_mov_b32 s73, 2
s_mov_b32 s74, 32768
s_mov_b32 s75, 49152
s_branch LRARE002_%=
LSITE1_%=:
s_mov_b32 s73, 1
s_mov_b32 s74, 16384
s_mov_b32 s75, 32768
s_branch LRARE102_%=
LSITE3_%=:
s_mov_b32 s73, 3
s_mov_b32 s74, 49152
s_mov_b32 s75, 0
s_branch LRARE104_%=
LSITE5_%=:
s_mov_b32 s73, 5
s_mov_b32 s74, 16384
s_mov_b32 s75, 32768
s_branch LRARE104_%=
LSITE7_%=:
s_mov_b32 s73, 7
s_mov_b32 s74, 49152
s_mov_b32 s75, 0
s_branch LRARE104_%=
LSITE9_%=:
s_mov_b32 s73, 9
s_mov_b32 s74, 16384
s_mov_b32 s75, 32768
s_branch LRARE104_%=
LSITE11_%=:
s_mov_b32 s73, 11
s_mov_b32 s74, 49152
s_mov_b32 s75, 0
s_branch LRARE104_%=
LSITE13_%=:
s_mov_b32 s73, 13
s_mov_b32 s74, 16384
s_mov_b32 s75, 32768
s_branch LRARE104_%=
LSITE15_%=:
s_mov_b32 s73, 15
s_mov_b32 s74, 49152
s_mov_b32 s75, 0
s_branch LRARE104_%=
LSITE17_%=:
s_mov_b32 s73, 17
s_mov_b32 s74, 16384
s_mov_b32 s75, 32768
s_branch LRARE104_%=
LSITE19_%=:
s_mov_b32 s73, 19
s_mov_b32 s74, 49152
s_mov_b32 s75, 0
s_branch LRARE104_%=
LSITE21_%=:
s_mov_b32 s73, 21
s_mov_b32 s74, 16384
s_mov_b32 s75, 32768
s_branch LRARE104_%=
LSITE23_%=:
s_mov_b32 s73, 23
s_mov_b32 s74, 49152
s_mov_b32 s75, 0
s_branch LRARE104_%=
LSITE25_%=:
s_mov_b32 s73, 25
s_mov_b32 s74, 16384
s_mov_b32 s75, 32768
s_branch LRARE104_%=
LSITE27_%=:
s_mov_b32 s73, 27
s_mov_b32 s74, 49152
s_mov_b32 s75, 0
s_branch LRARE104_%=
LSITE29_%=:
s_mov_b32 s73, 29
s_mov_b32 s74, 16384
s_mov_b32 s75, 32768
s_branch LRARE104_%=
LSITE31_%=:
s_mov_b32 s73, 31
s_mov_b32 s74, 49152
s_mov_b32 s75, 0
s_branch LRARE104_%=
LSITE33_%=:
s_mov_b32 s73, 33
s_mov_b32 s74, 16384
s_mov_b32 s75, 32768
s_branch LRARE104_%=
LSITE35_%=:
s_mov_b32 s73, 35
s_mov_b32 s74, 49152
s_mov_b32 s75, 0
s_branch LRARE104_%=
LSITE4_%=:
s_mov_b32 s73, 4
s_mov_b32 s74, 0
s_mov_b32 s75, 16384
s_branch LRARE004_%=
LSITE6_%=:
s_mov_b32 s73, 6
s_mov_b32 s74, 32768
s_mov_b32 s75, 49152
s_branch LRARE004_%=
LSITE8_%=:
s_mov_b32 s73, 8
s_mov_b32 s74, 0
s_mov_b32 s75, 16384
s_branch LRARE004_%=
LSITE10_%=:
s_mov_b32 s73, 10
s_mov_b32 s74, 32768
s_mov_b32 s75, 49152
s_branch LRARE004_%=
LSITE12_%=:
s_mov_b32 s73, 12
s_mov_b32 s74, 0
s_mov_b32 s75, 16384
s_branch LRARE004_%=
LSITE14_%=:
s_mov_b32 s73, 14
s_mov_b32 s74, 32768
s_mov_b32 s75, 49152
s_branch LRARE004_%=
)ASM"
R"ASM(
LSITE16_%=:
s_mov_b32 s73, 16
s_mov_b32 s74, 0
s_mov_b32 s75, 16384
s_branch LRARE004_%=
LSITE18_%=:
s_mov_b32 s73, 18
s_mov_b32 s74, 32768
s_mov_b32 s75, 49152
s_branch LRARE004_%=
LSITE20_%=:
s_mov_b32 s73, 20
s_mov_b32 s74, 0
s_mov_b32 s75, 16384
s_branch LRARE004_%=
LSITE22_%=:
s_mov_b32 s73, 22
s_mov_b32 s74, 32768
s_mov_b32 s75, 49152
s_branch LRARE004_%=
LSITE24_%=:
s_mov_b32 s73, 24
s_mov_b32 s74, 0
s_mov_b32 s75, 16384
s_branch LRARE004_%=
LSITE26_%=:
s_mov_b32 s73, 26
s_mov_b32 s74, 32768
s_mov_b32 s75, 49152
s_branch LRARE004_%=
LSITE28_%=:
s_mov_b32 s73, 28
s_mov_b32 s74, 0
s_mov_b32 s75, 16384
s_branch LRARE004_%=
LSITE30_%=:
s_mov_b32 s73, 30
s_mov_b32 s74, 32768
s_mov_b32 s75, 49152
s_branch LRARE004_%=
LSITE32_%=:
s_mov_b32 s73, 32
s_mov_b32 s74, 0
s_mov_b32 s75, 16384
s_branch LRARE004_%=
LSITE34_%=:
s_mov_b32 s73, 34
s_mov_b32 s74, 32768
s_mov_b32 s75, 49152
s_branch LRARE004_%=
LSITE36_%=:
s_mov_b32 s73, 36
s_mov_b32 s74, 0
s_mov_b32 s75, 16384
s_branch LRARE022_%=
LSITE38_%=:
s_mov_b32 s73, 38
s_mov_b32 s74, 32768
s_mov_b32 s75, 49152
s_branch LRARE022_%=
LSITE37_%=:
s_mov_b32 s73, 37
s_mov_b32 s74, 16384
s_mov_b32 s75, 32768
s_branch LRARE122_%=
LRARE002_%=:
s_nop 15
s_waitcnt vmcnt(0) lgkmcnt(0)
v_add_u32 v249, s74, v224
RD a[192:195],v249 offset:0
RD a[208:211],v249 offset:4096
RD a[224:227],v249 offset:8192
RD a[240:243],v249 offset:12288
v_add_u32 v250, s74, v225
RD a[196:199],v250 offset:0
RD a[212:215],v250 offset:4096
RD a[228:231],v250 offset:8192
RD a[244:247],v250 offset:12288
v_add_u32 v251, s74, v226
RD a[200:203],v251 offset:0
RD a[216:219],v251 offset:4096
RD a[232:235],v251 offset:8192
RD a[248:251],v251 offset:12288
v_add_u32 v254, s74, v227
RD a[204:207],v254 offset:0
RD a[220:223],v254 offset:4096
RD a[236:239],v254 offset:8192
RD a[252:255],v254 offset:12288
s_waitcnt lgkmcnt(0)
MM v[0:3],a[192:195],v[160:163],v[128:131]
MM v[0:3],a[196:199],v[176:179],v[0:3]
MM v[0:3],a[200:203],v[192:195],v[0:3]
MM v[0:3],a[204:207],v[208:211],v[0:3]
MM v[4:7],a[220:223],v[208:211],v[128:131]
MM v[4:7],a[216:219],v[192:195],v[4:7]
MM v[4:7],a[212:215],v[176:179],v[4:7]
MM v[4:7],a[208:211],v[160:163],v[4:7]
MM v[12:15],a[208:211],v[164:167],v[132:135]
MM v[12:15],a[212:215],v[180:183],v[12:15]
MM v[12:15],a[216:219],v[196:199],v[12:15]
MM v[12:15],a[220:223],v[212:215],v[12:15]
MM v[8:11],a[204:207],v[212:215],v[132:135]
MM v[8:11],a[200:203],v[196:199],v[8:11]
MM v[8:11],a[196:199],v[180:183],v[8:11]
MM v[8:11],a[192:195],v[164:167],v[8:11]
MM v[32:35],a[224:227],v[160:163],v[128:131]
MM v[32:35],a[228:231],v[176:179],v[32:35]
MM v[32:35],a[232:235],v[192:195],v[32:35]
MM v[32:35],a[236:239],v[208:211],v[32:35]
MM v[36:39],a[252:255],v[208:211],v[128:131]
MM v[36:39],a[248:251],v[192:195],v[36:39]
MM v[36:39],a[244:247],v[176:179],v[36:39]
MM v[36:39],a[240:243],v[160:163],v[36:39]
MM v[44:47],a[240:243],v[164:167],v[132:135]
MM v[44:47],a[244:247],v[180:183],v[44:47]
MM v[44:47],a[248:251],v[196:199],v[44:47]
MM v[44:47],a[252:255],v[212:215],v[44:47]
MM v[40:43],a[236:239],v[212:215],v[132:135]
MM v[40:43],a[232:235],v[196:199],v[40:43]
MM v[40:43],a[228:231],v[180:183],v[40:43]
MM v[40:43],a[224:227],v[164:167],v[40:43]
s_nop 15
v_add_u32 v249, s75, v224
RD a[192:195],v249 offset:0
RD a[208:211],v249 offset:4096
RD a[224:227],v249 offset:8192
RD a[240:243],v249 offset:12288
v_add_u32 v250, s75, v225
RD a[196:199],v250 offset:0
RD a[212:215],v250 offset:4096
RD a[228:231],v250 offset:8192
RD a[244:247],v250 offset:12288
v_add_u32 v251, s75, v226
RD a[200:203],v251 offset:0
RD a[216:219],v251 offset:4096
RD a[232:235],v251 offset:8192
RD a[248:251],v251 offset:12288
v_add_u32 v254, s75, v227
RD a[204:207],v254 offset:0
RD a[220:223],v254 offset:4096
RD a[236:239],v254 offset:8192
RD a[252:255],v254 offset:12288
s_waitcnt lgkmcnt(0)
v_max3_f32 v249, v0, v1, v2
v_max3_f32 v251, v32, v33, v34
v_max3_f32 v249, v249, v3, v4
v_max3_f32 v251, v251, v35, v36
v_max3_f32 v249, v249, v5, v6
v_max3_f32 v251, v251, v37, v38
v_max3_f32 v249, v249, v7, v39
v_max_f32 v249, v249, v251
v_mov_b32 v251, v249
s_nop 1
v_permlane16_swap_b32 v249, v251
v_max_f32 v249, v249, v251
v_mov_b32 v251, v249
s_nop 1
v_permlane32_swap_b32 v249, v251
v_max_f32 v249, v249, v251
v_log_f32 v250, v244
s_nop 0
v_max3_f32 v249, 0, v249, v250
v_sub_f32 v250, 0, v249
EX v250,v250
v_sub_f32 v128, v128, v249
v_sub_f32 v129, v129, v249
v_sub_f32 v130, v130, v249
v_sub_f32 v131, v131, v249
v_sub_f32 v64, v64, v249
v_sub_f32 v0, v0, v249
v_sub_f32 v65, v65, v249
v_sub_f32 v1, v1, v249
v_sub_f32 v66, v66, v249
v_sub_f32 v2, v2, v249
v_sub_f32 v67, v67, v249
v_sub_f32 v3, v3, v249
v_sub_f32 v68, v68, v249
v_sub_f32 v4, v4, v249
v_sub_f32 v69, v69, v249
v_sub_f32 v5, v5, v249
v_sub_f32 v70, v70, v249
v_sub_f32 v6, v6, v249
v_sub_f32 v71, v71, v249
v_sub_f32 v7, v7, v249
v_sub_f32 v96, v96, v249
v_sub_f32 v32, v32, v249
v_sub_f32 v97, v97, v249
v_sub_f32 v33, v33, v249
v_sub_f32 v98, v98, v249
v_sub_f32 v34, v34, v249
v_sub_f32 v99, v99, v249
v_sub_f32 v35, v35, v249
v_sub_f32 v100, v100, v249
v_sub_f32 v36, v36, v249
v_sub_f32 v101, v101, v249
v_sub_f32 v37, v37, v249
v_sub_f32 v102, v102, v249
v_sub_f32 v38, v38, v249
v_sub_f32 v103, v103, v249
v_sub_f32 v39, v39, v249
v_mul_f32 v144, v244, v250
v_mov_b32 v145, 0
v_bfe_u32 v249, v241, 0, 8
v_lshl_or_b32 v249, v249, 1, 1
v_lshlrev_b32 v249, 22, v249
v_and_b32 v241, 0xffffff00, v241
AR v254,a0
v_sub_f32 v254, v254, v249
v_mul_f32 v254, v254, v250
AW a0,v254
AR v251,a1
v_sub_f32 v251, v251, v249
v_mul_f32 v251, v251, v250
AW a1,v251
AR v254,a2
v_sub_f32 v254, v254, v249
v_mul_f32 v254, v254, v250
AW a2,v254
AR v251,a3
v_sub_f32 v251, v251, v249
v_mul_f32 v251, v251, v250
AW a3,v251
AR v254,a4
v_sub_f32 v254, v254, v249
v_mul_f32 v254, v254, v250
AW a4,v254
AR v251,a5
v_sub_f32 v251, v251, v249
v_mul_f32 v251, v251, v250
AW a5,v251
AR v254,a6
v_sub_f32 v254, v254, v249
v_mul_f32 v254, v254, v250
AW a6,v254
AR v251,a7
v_sub_f32 v251, v251, v249
v_mul_f32 v251, v251, v250
AW a7,v251
AR v254,a32
v_sub_f32 v254, v254, v249
v_mul_f32 v254, v254, v250
AW a32,v254
AR v251,a33
v_sub_f32 v251, v251, v249
v_mul_f32 v251, v251, v250
AW a33,v251
AR v254,a34
v_sub_f32 v254, v254, v249
v_mul_f32 v254, v254, v250
AW a34,v254
AR v251,a35
v_sub_f32 v251, v251, v249
v_mul_f32 v251, v251, v250
AW a35,v251
AR v254,a36
v_sub_f32 v254, v254, v249
v_mul_f32 v254, v254, v250
AW a36,v254
AR v251,a37
v_sub_f32 v251, v251, v249
v_mul_f32 v251, v251, v250
AW a37,v251
AR v254,a38
v_sub_f32 v254, v254, v249
v_mul_f32 v254, v254, v250
AW a38,v254
AR v251,a39
v_sub_f32 v251, v251, v249
v_mul_f32 v251, v251, v250
AW a39,v251
AR v254,a64
v_sub_f32 v254, v254, v249
v_mul_f32 v254, v254, v250
AW a64,v254
AR v251,a65
v_sub_f32 v251, v251, v249
v_mul_f32 v251, v251, v250
AW a65,v251
AR v254,a66
v_sub_f32 v254, v254, v249
v_mul_f32 v254, v254, v250
AW a66,v254
AR v251,a67
v_sub_f32 v251, v251, v249
v_mul_f32 v251, v251, v250
AW a67,v251
AR v254,a68
v_sub_f32 v254, v254, v249
v_mul_f32 v254, v254, v250
AW a68,v254
AR v251,a69
v_sub_f32 v251, v251, v249
v_mul_f32 v251, v251, v250
AW a69,v251
AR v254,a70
v_sub_f32 v254, v254, v249
v_mul_f32 v254, v254, v250
AW a70,v254
AR v251,a71
v_sub_f32 v251, v251, v249
v_mul_f32 v251, v251, v250
AW a71,v251
AR v254,a96
v_sub_f32 v254, v254, v249
v_mul_f32 v254, v254, v250
AW a96,v254
AR v251,a97
v_sub_f32 v251, v251, v249
v_mul_f32 v251, v251, v250
AW a97,v251
AR v254,a98
v_sub_f32 v254, v254, v249
v_mul_f32 v254, v254, v250
AW a98,v254
AR v251,a99
v_sub_f32 v251, v251, v249
v_mul_f32 v251, v251, v250
AW a99,v251
AR v254,a100
v_sub_f32 v254, v254, v249
v_mul_f32 v254, v254, v250
AW a100,v254
AR v251,a101
v_sub_f32 v251, v251, v249
v_mul_f32 v251, v251, v250
AW a101,v251
AR v254,a102
v_sub_f32 v254, v254, v249
v_mul_f32 v254, v254, v250
AW a102,v254
AR v251,a103
v_sub_f32 v251, v251, v249
v_mul_f32 v251, v251, v250
AW a103,v251
v_max3_f32 v249, v8, v9, v10
v_max3_f32 v251, v40, v41, v42
v_max3_f32 v249, v249, v11, v12
v_max3_f32 v251, v251, v43, v44
v_max3_f32 v249, v249, v13, v14
v_max3_f32 v251, v251, v45, v46
v_max3_f32 v249, v249, v15, v47
v_max_f32 v249, v249, v251
v_mov_b32 v251, v249
s_nop 1
v_permlane16_swap_b32 v249, v251
v_max_f32 v249, v249, v251
v_mov_b32 v251, v249
s_nop 1
v_permlane32_swap_b32 v249, v251
v_max_f32 v249, v249, v251
v_log_f32 v250, v245
s_nop 0
v_max3_f32 v249, 0, v249, v250
v_sub_f32 v250, 0, v249
EX v250,v250
v_sub_f32 v132, v132, v249
v_sub_f32 v133, v133, v249
v_sub_f32 v134, v134, v249
v_sub_f32 v135, v135, v249
v_sub_f32 v72, v72, v249
v_sub_f32 v8, v8, v249
v_sub_f32 v73, v73, v249
v_sub_f32 v9, v9, v249
v_sub_f32 v74, v74, v249
v_sub_f32 v10, v10, v249
v_sub_f32 v75, v75, v249
v_sub_f32 v11, v11, v249
v_sub_f32 v76, v76, v249
v_sub_f32 v12, v12, v249
v_sub_f32 v77, v77, v249
v_sub_f32 v13, v13, v249
v_sub_f32 v78, v78, v249
v_sub_f32 v14, v14, v249
v_sub_f32 v79, v79, v249
v_sub_f32 v15, v15, v249
v_sub_f32 v104, v104, v249
v_sub_f32 v40, v40, v249
v_sub_f32 v105, v105, v249
v_sub_f32 v41, v41, v249
v_sub_f32 v106, v106, v249
v_sub_f32 v42, v42, v249
v_sub_f32 v107, v107, v249
v_sub_f32 v43, v43, v249
v_sub_f32 v108, v108, v249
v_sub_f32 v44, v44, v249
v_sub_f32 v109, v109, v249
v_sub_f32 v45, v45, v249
v_sub_f32 v110, v110, v249
v_sub_f32 v46, v46, v249
v_sub_f32 v111, v111, v249
v_sub_f32 v47, v47, v249
v_mul_f32 v148, v245, v250
v_mov_b32 v149, 0
v_bfe_u32 v249, v241, 8, 8
v_lshl_or_b32 v249, v249, 1, 1
v_lshlrev_b32 v249, 22, v249
v_and_b32 v241, 0xffff00ff, v241
AR v254,a8
v_sub_f32 v254, v254, v249
v_mul_f32 v254, v254, v250
AW a8,v254
AR v251,a9
v_sub_f32 v251, v251, v249
v_mul_f32 v251, v251, v250
AW a9,v251
AR v254,a10
v_sub_f32 v254, v254, v249
v_mul_f32 v254, v254, v250
AW a10,v254
AR v251,a11
v_sub_f32 v251, v251, v249
v_mul_f32 v251, v251, v250
AW a11,v251
AR v254,a12
v_sub_f32 v254, v254, v249
v_mul_f32 v254, v254, v250
AW a12,v254
AR v251,a13
v_sub_f32 v251, v251, v249
v_mul_f32 v251, v251, v250
AW a13,v251
AR v254,a14
v_sub_f32 v254, v254, v249
v_mul_f32 v254, v254, v250
AW a14,v254
AR v251,a15
v_sub_f32 v251, v251, v249
v_mul_f32 v251, v251, v250
AW a15,v251
AR v254,a40
v_sub_f32 v254, v254, v249
v_mul_f32 v254, v254, v250
AW a40,v254
AR v251,a41
v_sub_f32 v251, v251, v249
v_mul_f32 v251, v251, v250
AW a41,v251
AR v254,a42
v_sub_f32 v254, v254, v249
v_mul_f32 v254, v254, v250
AW a42,v254
AR v251,a43
v_sub_f32 v251, v251, v249
v_mul_f32 v251, v251, v250
AW a43,v251
AR v254,a44
v_sub_f32 v254, v254, v249
v_mul_f32 v254, v254, v250
AW a44,v254
AR v251,a45
v_sub_f32 v251, v251, v249
v_mul_f32 v251, v251, v250
AW a45,v251
AR v254,a46
v_sub_f32 v254, v254, v249
v_mul_f32 v254, v254, v250
AW a46,v254
AR v251,a47
v_sub_f32 v251, v251, v249
v_mul_f32 v251, v251, v250
AW a47,v251
AR v254,a72
v_sub_f32 v254, v254, v249
v_mul_f32 v254, v254, v250
AW a72,v254
AR v251,a73
v_sub_f32 v251, v251, v249
v_mul_f32 v251, v251, v250
AW a73,v251
AR v254,a74
v_sub_f32 v254, v254, v249
v_mul_f32 v254, v254, v250
AW a74,v254
AR v251,a75
v_sub_f32 v251, v251, v249
v_mul_f32 v251, v251, v250
AW a75,v251
AR v254,a76
v_sub_f32 v254, v254, v249
v_mul_f32 v254, v254, v250
AW a76,v254
AR v251,a77
v_sub_f32 v251, v251, v249
v_mul_f32 v251, v251, v250
AW a77,v251
AR v254,a78
v_sub_f32 v254, v254, v249
v_mul_f32 v254, v254, v250
AW a78,v254
AR v251,a79
v_sub_f32 v251, v251, v249
v_mul_f32 v251, v251, v250
AW a79,v251
AR v254,a104
v_sub_f32 v254, v254, v249
v_mul_f32 v254, v254, v250
AW a104,v254
AR v251,a105
v_sub_f32 v251, v251, v249
v_mul_f32 v251, v251, v250
AW a105,v251
AR v254,a106
v_sub_f32 v254, v254, v249
v_mul_f32 v254, v254, v250
AW a106,v254
AR v251,a107
v_sub_f32 v251, v251, v249
v_mul_f32 v251, v251, v250
AW a107,v251
AR v254,a108
v_sub_f32 v254, v254, v249
v_mul_f32 v254, v254, v250
AW a108,v254
AR v251,a109
v_sub_f32 v251, v251, v249
v_mul_f32 v251, v251, v250
AW a109,v251
AR v254,a110
v_sub_f32 v254, v254, v249
v_mul_f32 v254, v254, v250
AW a110,v254
AR v251,a111
v_sub_f32 v251, v251, v249
v_mul_f32 v251, v251, v250
AW a111,v251
)ASM"
R"ASM(
EX v0,v0
EX v1,v1
EX v2,v2
EX v3,v3
EX v4,v4
EX v5,v5
EX v6,v6
EX v7,v7
EX v8,v8
EX v9,v9
EX v10,v10
EX v11,v11
EX v12,v12
EX v13,v13
EX v14,v14
EX v15,v15
EX v32,v32
EX v33,v33
EX v34,v34
EX v35,v35
EX v36,v36
EX v37,v37
EX v38,v38
EX v39,v39
EX v40,v40
EX v41,v41
EX v42,v42
EX v43,v43
EX v44,v44
EX v45,v45
EX v46,v46
EX v47,v47
CV v0,v0,v1
CV v1,v2,v3
CV v2,v4,v5
CV v3,v6,v7
CV v8,v8,v9
CV v9,v10,v11
CV v10,v12,v13
CV v11,v14,v15
CV v4,v32,v33
CV v5,v34,v35
CV v6,v36,v37
CV v7,v38,v39
CV v12,v40,v41
CV v13,v42,v43
CV v14,v44,v45
CV v15,v46,v47
v_smfmac_f32_16x16x64_bf16 v[144:147], v[236:239], v[0:7], v234
s_nop 0
v_smfmac_f32_16x16x64_bf16 v[148:151], v[236:239], v[8:15], v234
s_nop 9
s_branch LRET_%=
LRARE102_%=:
s_nop 15
s_waitcnt vmcnt(0) lgkmcnt(0)
v_add_u32 v249, s74, v224
RD a[192:195],v249 offset:0
RD a[208:211],v249 offset:4096
RD a[224:227],v249 offset:8192
RD a[240:243],v249 offset:12288
v_add_u32 v250, s74, v225
RD a[196:199],v250 offset:0
RD a[212:215],v250 offset:4096
RD a[228:231],v250 offset:8192
RD a[244:247],v250 offset:12288
v_add_u32 v251, s74, v226
RD a[200:203],v251 offset:0
RD a[216:219],v251 offset:4096
RD a[232:235],v251 offset:8192
RD a[248:251],v251 offset:12288
v_add_u32 v254, s74, v227
RD a[204:207],v254 offset:0
RD a[220:223],v254 offset:4096
RD a[236:239],v254 offset:8192
RD a[252:255],v254 offset:12288
s_waitcnt lgkmcnt(0)
MM v[64:67],a[192:195],v[160:163],v[128:131]
MM v[64:67],a[196:199],v[176:179],v[64:67]
MM v[64:67],a[200:203],v[192:195],v[64:67]
MM v[64:67],a[204:207],v[208:211],v[64:67]
MM v[68:71],a[220:223],v[208:211],v[128:131]
MM v[68:71],a[216:219],v[192:195],v[68:71]
MM v[68:71],a[212:215],v[176:179],v[68:71]
MM v[68:71],a[208:211],v[160:163],v[68:71]
MM v[76:79],a[208:211],v[164:167],v[132:135]
MM v[76:79],a[212:215],v[180:183],v[76:79]
MM v[76:79],a[216:219],v[196:199],v[76:79]
MM v[76:79],a[220:223],v[212:215],v[76:79]
MM v[72:75],a[204:207],v[212:215],v[132:135]
MM v[72:75],a[200:203],v[196:199],v[72:75]
MM v[72:75],a[196:199],v[180:183],v[72:75]
MM v[72:75],a[192:195],v[164:167],v[72:75]
MM v[96:99],a[224:227],v[160:163],v[128:131]
MM v[96:99],a[228:231],v[176:179],v[96:99]
MM v[96:99],a[232:235],v[192:195],v[96:99]
MM v[96:99],a[236:239],v[208:211],v[96:99]
MM v[100:103],a[252:255],v[208:211],v[128:131]
MM v[100:103],a[248:251],v[192:195],v[100:103]
MM v[100:103],a[244:247],v[176:179],v[100:103]
MM v[100:103],a[240:243],v[160:163],v[100:103]
MM v[108:111],a[240:243],v[164:167],v[132:135]
MM v[108:111],a[244:247],v[180:183],v[108:111]
MM v[108:111],a[248:251],v[196:199],v[108:111]
MM v[108:111],a[252:255],v[212:215],v[108:111]
MM v[104:107],a[236:239],v[212:215],v[132:135]
MM v[104:107],a[232:235],v[196:199],v[104:107]
MM v[104:107],a[228:231],v[180:183],v[104:107]
MM v[104:107],a[224:227],v[164:167],v[104:107]
s_nop 15
v_add_u32 v249, s75, v224
RD a[192:195],v249 offset:0
RD a[208:211],v249 offset:4096
RD a[224:227],v249 offset:8192
RD a[240:243],v249 offset:12288
v_add_u32 v250, s75, v225
RD a[196:199],v250 offset:0
RD a[212:215],v250 offset:4096
RD a[228:231],v250 offset:8192
RD a[244:247],v250 offset:12288
v_add_u32 v251, s75, v226
RD a[200:203],v251 offset:0
RD a[216:219],v251 offset:4096
RD a[232:235],v251 offset:8192
RD a[248:251],v251 offset:12288
v_add_u32 v254, s75, v227
RD a[204:207],v254 offset:0
RD a[220:223],v254 offset:4096
RD a[236:239],v254 offset:8192
RD a[252:255],v254 offset:12288
s_waitcnt lgkmcnt(0)
v_max3_f32 v249, v64, v65, v66
v_max3_f32 v251, v96, v97, v98
v_max3_f32 v249, v249, v67, v68
v_max3_f32 v251, v251, v99, v100
v_max3_f32 v249, v249, v69, v70
v_max3_f32 v251, v251, v101, v102
v_max3_f32 v249, v249, v71, v103
v_max_f32 v249, v249, v251
v_mov_b32 v251, v249
s_nop 1
v_permlane16_swap_b32 v249, v251
v_max_f32 v249, v249, v251
v_mov_b32 v251, v249
s_nop 1
v_permlane32_swap_b32 v249, v251
v_max_f32 v249, v249, v251
v_log_f32 v250, v244
s_nop 0
v_max3_f32 v249, 0, v249, v250
v_sub_f32 v250, 0, v249
EX v250,v250
v_sub_f32 v128, v128, v249
v_sub_f32 v129, v129, v249
v_sub_f32 v130, v130, v249
v_sub_f32 v131, v131, v249
v_sub_f32 v0, v0, v249
v_sub_f32 v64, v64, v249
v_sub_f32 v1, v1, v249
v_sub_f32 v65, v65, v249
v_sub_f32 v2, v2, v249
v_sub_f32 v66, v66, v249
v_sub_f32 v3, v3, v249
v_sub_f32 v67, v67, v249
v_sub_f32 v4, v4, v249
v_sub_f32 v68, v68, v249
v_sub_f32 v5, v5, v249
v_sub_f32 v69, v69, v249
v_sub_f32 v6, v6, v249
v_sub_f32 v70, v70, v249
v_sub_f32 v7, v7, v249
v_sub_f32 v71, v71, v249
v_sub_f32 v32, v32, v249
v_sub_f32 v96, v96, v249
v_sub_f32 v33, v33, v249
v_sub_f32 v97, v97, v249
v_sub_f32 v34, v34, v249
v_sub_f32 v98, v98, v249
v_sub_f32 v35, v35, v249
v_sub_f32 v99, v99, v249
v_sub_f32 v36, v36, v249
v_sub_f32 v100, v100, v249
v_sub_f32 v37, v37, v249
v_sub_f32 v101, v101, v249
v_sub_f32 v38, v38, v249
v_sub_f32 v102, v102, v249
v_sub_f32 v39, v39, v249
v_sub_f32 v103, v103, v249
v_mul_f32 v144, v244, v250
v_mov_b32 v145, 0
v_bfe_u32 v249, v241, 0, 8
v_lshl_or_b32 v249, v249, 1, 1
v_lshlrev_b32 v249, 22, v249
v_and_b32 v241, 0xffffff00, v241
AR v254,a0
v_sub_f32 v254, v254, v249
v_mul_f32 v254, v254, v250
AW a0,v254
AR v251,a1
v_sub_f32 v251, v251, v249
v_mul_f32 v251, v251, v250
AW a1,v251
AR v254,a2
v_sub_f32 v254, v254, v249
v_mul_f32 v254, v254, v250
AW a2,v254
AR v251,a3
v_sub_f32 v251, v251, v249
v_mul_f32 v251, v251, v250
AW a3,v251
AR v254,a4
v_sub_f32 v254, v254, v249
v_mul_f32 v254, v254, v250
AW a4,v254
AR v251,a5
v_sub_f32 v251, v251, v249
v_mul_f32 v251, v251, v250
AW a5,v251
AR v254,a6
v_sub_f32 v254, v254, v249
v_mul_f32 v254, v254, v250
AW a6,v254
AR v251,a7
v_sub_f32 v251, v251, v249
v_mul_f32 v251, v251, v250
AW a7,v251
AR v254,a32
v_sub_f32 v254, v254, v249
v_mul_f32 v254, v254, v250
AW a32,v254
AR v251,a33
v_sub_f32 v251, v251, v249
v_mul_f32 v251, v251, v250
AW a33,v251
AR v254,a34
v_sub_f32 v254, v254, v249
v_mul_f32 v254, v254, v250
AW a34,v254
AR v251,a35
v_sub_f32 v251, v251, v249
v_mul_f32 v251, v251, v250
AW a35,v251
AR v254,a36
v_sub_f32 v254, v254, v249
v_mul_f32 v254, v254, v250
AW a36,v254
AR v251,a37
v_sub_f32 v251, v251, v249
v_mul_f32 v251, v251, v250
AW a37,v251
AR v254,a38
v_sub_f32 v254, v254, v249
v_mul_f32 v254, v254, v250
AW a38,v254
AR v251,a39
v_sub_f32 v251, v251, v249
v_mul_f32 v251, v251, v250
AW a39,v251
AR v254,a64
v_sub_f32 v254, v254, v249
v_mul_f32 v254, v254, v250
AW a64,v254
AR v251,a65
v_sub_f32 v251, v251, v249
v_mul_f32 v251, v251, v250
AW a65,v251
AR v254,a66
v_sub_f32 v254, v254, v249
v_mul_f32 v254, v254, v250
AW a66,v254
AR v251,a67
v_sub_f32 v251, v251, v249
v_mul_f32 v251, v251, v250
AW a67,v251
AR v254,a68
v_sub_f32 v254, v254, v249
v_mul_f32 v254, v254, v250
AW a68,v254
AR v251,a69
v_sub_f32 v251, v251, v249
v_mul_f32 v251, v251, v250
AW a69,v251
AR v254,a70
v_sub_f32 v254, v254, v249
v_mul_f32 v254, v254, v250
AW a70,v254
AR v251,a71
v_sub_f32 v251, v251, v249
v_mul_f32 v251, v251, v250
AW a71,v251
AR v254,a96
v_sub_f32 v254, v254, v249
v_mul_f32 v254, v254, v250
AW a96,v254
AR v251,a97
v_sub_f32 v251, v251, v249
v_mul_f32 v251, v251, v250
AW a97,v251
AR v254,a98
v_sub_f32 v254, v254, v249
v_mul_f32 v254, v254, v250
AW a98,v254
AR v251,a99
v_sub_f32 v251, v251, v249
v_mul_f32 v251, v251, v250
AW a99,v251
AR v254,a100
v_sub_f32 v254, v254, v249
v_mul_f32 v254, v254, v250
AW a100,v254
AR v251,a101
v_sub_f32 v251, v251, v249
v_mul_f32 v251, v251, v250
AW a101,v251
AR v254,a102
v_sub_f32 v254, v254, v249
v_mul_f32 v254, v254, v250
AW a102,v254
AR v251,a103
v_sub_f32 v251, v251, v249
v_mul_f32 v251, v251, v250
AW a103,v251
v_max3_f32 v249, v72, v73, v74
v_max3_f32 v251, v104, v105, v106
v_max3_f32 v249, v249, v75, v76
v_max3_f32 v251, v251, v107, v108
v_max3_f32 v249, v249, v77, v78
v_max3_f32 v251, v251, v109, v110
v_max3_f32 v249, v249, v79, v111
v_max_f32 v249, v249, v251
v_mov_b32 v251, v249
s_nop 1
v_permlane16_swap_b32 v249, v251
v_max_f32 v249, v249, v251
v_mov_b32 v251, v249
s_nop 1
v_permlane32_swap_b32 v249, v251
v_max_f32 v249, v249, v251
v_log_f32 v250, v245
s_nop 0
v_max3_f32 v249, 0, v249, v250
v_sub_f32 v250, 0, v249
EX v250,v250
v_sub_f32 v132, v132, v249
v_sub_f32 v133, v133, v249
v_sub_f32 v134, v134, v249
v_sub_f32 v135, v135, v249
v_sub_f32 v8, v8, v249
v_sub_f32 v72, v72, v249
v_sub_f32 v9, v9, v249
v_sub_f32 v73, v73, v249
v_sub_f32 v10, v10, v249
v_sub_f32 v74, v74, v249
v_sub_f32 v11, v11, v249
v_sub_f32 v75, v75, v249
v_sub_f32 v12, v12, v249
v_sub_f32 v76, v76, v249
v_sub_f32 v13, v13, v249
v_sub_f32 v77, v77, v249
v_sub_f32 v14, v14, v249
v_sub_f32 v78, v78, v249
v_sub_f32 v15, v15, v249
v_sub_f32 v79, v79, v249
v_sub_f32 v40, v40, v249
v_sub_f32 v104, v104, v249
v_sub_f32 v41, v41, v249
v_sub_f32 v105, v105, v249
v_sub_f32 v42, v42, v249
v_sub_f32 v106, v106, v249
v_sub_f32 v43, v43, v249
v_sub_f32 v107, v107, v249
v_sub_f32 v44, v44, v249
v_sub_f32 v108, v108, v249
v_sub_f32 v45, v45, v249
v_sub_f32 v109, v109, v249
v_sub_f32 v46, v46, v249
v_sub_f32 v110, v110, v249
v_sub_f32 v47, v47, v249
v_sub_f32 v111, v111, v249
v_mul_f32 v148, v245, v250
v_mov_b32 v149, 0
v_bfe_u32 v249, v241, 8, 8
v_lshl_or_b32 v249, v249, 1, 1
v_lshlrev_b32 v249, 22, v249
v_and_b32 v241, 0xffff00ff, v241
AR v254,a8
v_sub_f32 v254, v254, v249
v_mul_f32 v254, v254, v250
AW a8,v254
AR v251,a9
v_sub_f32 v251, v251, v249
v_mul_f32 v251, v251, v250
AW a9,v251
AR v254,a10
v_sub_f32 v254, v254, v249
v_mul_f32 v254, v254, v250
AW a10,v254
AR v251,a11
v_sub_f32 v251, v251, v249
v_mul_f32 v251, v251, v250
AW a11,v251
AR v254,a12
v_sub_f32 v254, v254, v249
v_mul_f32 v254, v254, v250
AW a12,v254
AR v251,a13
v_sub_f32 v251, v251, v249
v_mul_f32 v251, v251, v250
AW a13,v251
AR v254,a14
v_sub_f32 v254, v254, v249
v_mul_f32 v254, v254, v250
AW a14,v254
AR v251,a15
v_sub_f32 v251, v251, v249
v_mul_f32 v251, v251, v250
AW a15,v251
AR v254,a40
v_sub_f32 v254, v254, v249
v_mul_f32 v254, v254, v250
AW a40,v254
AR v251,a41
v_sub_f32 v251, v251, v249
v_mul_f32 v251, v251, v250
AW a41,v251
AR v254,a42
v_sub_f32 v254, v254, v249
v_mul_f32 v254, v254, v250
AW a42,v254
AR v251,a43
v_sub_f32 v251, v251, v249
v_mul_f32 v251, v251, v250
AW a43,v251
AR v254,a44
v_sub_f32 v254, v254, v249
v_mul_f32 v254, v254, v250
AW a44,v254
AR v251,a45
v_sub_f32 v251, v251, v249
v_mul_f32 v251, v251, v250
AW a45,v251
AR v254,a46
v_sub_f32 v254, v254, v249
v_mul_f32 v254, v254, v250
AW a46,v254
AR v251,a47
v_sub_f32 v251, v251, v249
v_mul_f32 v251, v251, v250
AW a47,v251
AR v254,a72
v_sub_f32 v254, v254, v249
v_mul_f32 v254, v254, v250
AW a72,v254
AR v251,a73
v_sub_f32 v251, v251, v249
v_mul_f32 v251, v251, v250
AW a73,v251
AR v254,a74
v_sub_f32 v254, v254, v249
v_mul_f32 v254, v254, v250
AW a74,v254
AR v251,a75
v_sub_f32 v251, v251, v249
v_mul_f32 v251, v251, v250
AW a75,v251
AR v254,a76
v_sub_f32 v254, v254, v249
v_mul_f32 v254, v254, v250
AW a76,v254
AR v251,a77
v_sub_f32 v251, v251, v249
v_mul_f32 v251, v251, v250
AW a77,v251
AR v254,a78
v_sub_f32 v254, v254, v249
v_mul_f32 v254, v254, v250
AW a78,v254
AR v251,a79
v_sub_f32 v251, v251, v249
v_mul_f32 v251, v251, v250
AW a79,v251
AR v254,a104
v_sub_f32 v254, v254, v249
v_mul_f32 v254, v254, v250
AW a104,v254
AR v251,a105
v_sub_f32 v251, v251, v249
v_mul_f32 v251, v251, v250
AW a105,v251
AR v254,a106
v_sub_f32 v254, v254, v249
v_mul_f32 v254, v254, v250
AW a106,v254
AR v251,a107
v_sub_f32 v251, v251, v249
v_mul_f32 v251, v251, v250
AW a107,v251
AR v254,a108
v_sub_f32 v254, v254, v249
v_mul_f32 v254, v254, v250
AW a108,v254
AR v251,a109
v_sub_f32 v251, v251, v249
v_mul_f32 v251, v251, v250
AW a109,v251
AR v254,a110
v_sub_f32 v254, v254, v249
v_mul_f32 v254, v254, v250
AW a110,v254
AR v251,a111
v_sub_f32 v251, v251, v249
v_mul_f32 v251, v251, v250
AW a111,v251
EX v64,v64
EX v65,v65
EX v66,v66
EX v67,v67
EX v68,v68
EX v69,v69
EX v70,v70
EX v71,v71
EX v72,v72
EX v73,v73
EX v74,v74
EX v75,v75
EX v76,v76
EX v77,v77
EX v78,v78
EX v79,v79
EX v96,v96
EX v97,v97
EX v98,v98
EX v99,v99
EX v100,v100
EX v101,v101
EX v102,v102
EX v103,v103
EX v104,v104
EX v105,v105
EX v106,v106
EX v107,v107
EX v108,v108
EX v109,v109
EX v110,v110
)ASM"
R"ASM(
EX v111,v111
CV v64,v64,v65
CV v65,v66,v67
CV v66,v68,v69
CV v67,v70,v71
CV v72,v72,v73
CV v73,v74,v75
CV v74,v76,v77
CV v75,v78,v79
CV v68,v96,v97
CV v69,v98,v99
CV v70,v100,v101
CV v71,v102,v103
CV v76,v104,v105
CV v77,v106,v107
CV v78,v108,v109
CV v79,v110,v111
v_smfmac_f32_16x16x64_bf16 v[144:147], v[236:239], v[64:71], v234
s_nop 0
v_smfmac_f32_16x16x64_bf16 v[148:151], v[236:239], v[72:79], v234
s_nop 9
s_branch LRET_%=
LRARE104_%=:
s_nop 15
s_waitcnt vmcnt(0) lgkmcnt(0)
v_add_u32 v249, s74, v224
RD a[192:195],v249 offset:0
RD a[208:211],v249 offset:4096
RD a[224:227],v249 offset:8192
RD a[240:243],v249 offset:12288
v_add_u32 v250, s74, v225
RD a[196:199],v250 offset:0
RD a[212:215],v250 offset:4096
RD a[228:231],v250 offset:8192
RD a[244:247],v250 offset:12288
v_add_u32 v251, s74, v226
RD a[200:203],v251 offset:0
RD a[216:219],v251 offset:4096
RD a[232:235],v251 offset:8192
RD a[248:251],v251 offset:12288
v_add_u32 v254, s74, v227
RD a[204:207],v254 offset:0
RD a[220:223],v254 offset:4096
RD a[236:239],v254 offset:8192
RD a[252:255],v254 offset:12288
s_waitcnt lgkmcnt(0)
MM v[64:67],a[192:195],v[160:163],v[128:131]
MM v[64:67],a[196:199],v[176:179],v[64:67]
MM v[64:67],a[200:203],v[192:195],v[64:67]
MM v[64:67],a[204:207],v[208:211],v[64:67]
MM v[68:71],a[220:223],v[208:211],v[128:131]
MM v[68:71],a[216:219],v[192:195],v[68:71]
MM v[68:71],a[212:215],v[176:179],v[68:71]
MM v[68:71],a[208:211],v[160:163],v[68:71]
MM v[76:79],a[208:211],v[164:167],v[132:135]
MM v[76:79],a[212:215],v[180:183],v[76:79]
MM v[76:79],a[216:219],v[196:199],v[76:79]
MM v[76:79],a[220:223],v[212:215],v[76:79]
MM v[72:75],a[204:207],v[212:215],v[132:135]
MM v[72:75],a[200:203],v[196:199],v[72:75]
MM v[72:75],a[196:199],v[180:183],v[72:75]
MM v[72:75],a[192:195],v[164:167],v[72:75]
MM v[80:83],a[192:195],v[168:171],v[136:139]
MM v[80:83],a[196:199],v[184:187],v[80:83]
MM v[80:83],a[200:203],v[200:203],v[80:83]
MM v[80:83],a[204:207],v[216:219],v[80:83]
MM v[84:87],a[220:223],v[216:219],v[136:139]
MM v[84:87],a[216:219],v[200:203],v[84:87]
MM v[84:87],a[212:215],v[184:187],v[84:87]
MM v[84:87],a[208:211],v[168:171],v[84:87]
MM v[92:95],a[208:211],v[172:175],v[140:143]
MM v[92:95],a[212:215],v[188:191],v[92:95]
MM v[92:95],a[216:219],v[204:207],v[92:95]
MM v[92:95],a[220:223],v[220:223],v[92:95]
MM v[88:91],a[204:207],v[220:223],v[140:143]
MM v[88:91],a[200:203],v[204:207],v[88:91]
MM v[88:91],a[196:199],v[188:191],v[88:91]
MM v[88:91],a[192:195],v[172:175],v[88:91]
MM v[96:99],a[224:227],v[160:163],v[128:131]
MM v[96:99],a[228:231],v[176:179],v[96:99]
MM v[96:99],a[232:235],v[192:195],v[96:99]
MM v[96:99],a[236:239],v[208:211],v[96:99]
MM v[100:103],a[252:255],v[208:211],v[128:131]
MM v[100:103],a[248:251],v[192:195],v[100:103]
MM v[100:103],a[244:247],v[176:179],v[100:103]
MM v[100:103],a[240:243],v[160:163],v[100:103]
MM v[108:111],a[240:243],v[164:167],v[132:135]
MM v[108:111],a[244:247],v[180:183],v[108:111]
MM v[108:111],a[248:251],v[196:199],v[108:111]
MM v[108:111],a[252:255],v[212:215],v[108:111]
MM v[104:107],a[236:239],v[212:215],v[132:135]
MM v[104:107],a[232:235],v[196:199],v[104:107]
MM v[104:107],a[228:231],v[180:183],v[104:107]
MM v[104:107],a[224:227],v[164:167],v[104:107]
MM v[112:115],a[224:227],v[168:171],v[136:139]
MM v[112:115],a[228:231],v[184:187],v[112:115]
MM v[112:115],a[232:235],v[200:203],v[112:115]
MM v[112:115],a[236:239],v[216:219],v[112:115]
MM v[116:119],a[252:255],v[216:219],v[136:139]
MM v[116:119],a[248:251],v[200:203],v[116:119]
MM v[116:119],a[244:247],v[184:187],v[116:119]
MM v[116:119],a[240:243],v[168:171],v[116:119]
MM v[124:127],a[240:243],v[172:175],v[140:143]
MM v[124:127],a[244:247],v[188:191],v[124:127]
MM v[124:127],a[248:251],v[204:207],v[124:127]
MM v[124:127],a[252:255],v[220:223],v[124:127]
MM v[120:123],a[236:239],v[220:223],v[140:143]
MM v[120:123],a[232:235],v[204:207],v[120:123]
MM v[120:123],a[228:231],v[188:191],v[120:123]
MM v[120:123],a[224:227],v[172:175],v[120:123]
s_nop 15
v_add_u32 v249, s75, v224
RD a[192:195],v249 offset:0
RD a[208:211],v249 offset:4096
RD a[224:227],v249 offset:8192
RD a[240:243],v249 offset:12288
v_add_u32 v250, s75, v225
RD a[196:199],v250 offset:0
RD a[212:215],v250 offset:4096
RD a[228:231],v250 offset:8192
RD a[244:247],v250 offset:12288
v_add_u32 v251, s75, v226
RD a[200:203],v251 offset:0
RD a[216:219],v251 offset:4096
RD a[232:235],v251 offset:8192
RD a[248:251],v251 offset:12288
v_add_u32 v254, s75, v227
RD a[204:207],v254 offset:0
RD a[220:223],v254 offset:4096
RD a[236:239],v254 offset:8192
RD a[252:255],v254 offset:12288
s_waitcnt lgkmcnt(0)
v_max3_f32 v249, v64, v65, v66
v_max3_f32 v251, v96, v97, v98
v_max3_f32 v249, v249, v67, v68
v_max3_f32 v251, v251, v99, v100
v_max3_f32 v249, v249, v69, v70
v_max3_f32 v251, v251, v101, v102
v_max3_f32 v249, v249, v71, v103
v_max_f32 v249, v249, v251
v_mov_b32 v251, v249
s_nop 1
v_permlane16_swap_b32 v249, v251
v_max_f32 v249, v249, v251
v_mov_b32 v251, v249
s_nop 1
v_permlane32_swap_b32 v249, v251
v_max_f32 v249, v249, v251
v_log_f32 v250, v244
s_nop 0
v_max3_f32 v249, 0, v249, v250
v_sub_f32 v250, 0, v249
EX v250,v250
v_sub_f32 v128, v128, v249
v_sub_f32 v129, v129, v249
v_sub_f32 v130, v130, v249
v_sub_f32 v131, v131, v249
v_sub_f32 v0, v0, v249
v_sub_f32 v64, v64, v249
v_sub_f32 v1, v1, v249
v_sub_f32 v65, v65, v249
v_sub_f32 v2, v2, v249
v_sub_f32 v66, v66, v249
v_sub_f32 v3, v3, v249
v_sub_f32 v67, v67, v249
v_sub_f32 v4, v4, v249
v_sub_f32 v68, v68, v249
v_sub_f32 v5, v5, v249
v_sub_f32 v69, v69, v249
v_sub_f32 v6, v6, v249
v_sub_f32 v70, v70, v249
v_sub_f32 v7, v7, v249
v_sub_f32 v71, v71, v249
v_sub_f32 v32, v32, v249
v_sub_f32 v96, v96, v249
v_sub_f32 v33, v33, v249
v_sub_f32 v97, v97, v249
v_sub_f32 v34, v34, v249
v_sub_f32 v98, v98, v249
v_sub_f32 v35, v35, v249
v_sub_f32 v99, v99, v249
v_sub_f32 v36, v36, v249
v_sub_f32 v100, v100, v249
v_sub_f32 v37, v37, v249
v_sub_f32 v101, v101, v249
v_sub_f32 v38, v38, v249
v_sub_f32 v102, v102, v249
v_sub_f32 v39, v39, v249
v_sub_f32 v103, v103, v249
v_mul_f32 v144, v244, v250
v_mov_b32 v145, 0
v_bfe_u32 v249, v241, 0, 8
v_lshl_or_b32 v249, v249, 1, 1
v_lshlrev_b32 v249, 22, v249
v_and_b32 v241, 0xffffff00, v241
AR v254,a0
v_sub_f32 v254, v254, v249
v_mul_f32 v254, v254, v250
AW a0,v254
AR v251,a1
v_sub_f32 v251, v251, v249
v_mul_f32 v251, v251, v250
AW a1,v251
AR v254,a2
v_sub_f32 v254, v254, v249
v_mul_f32 v254, v254, v250
AW a2,v254
AR v251,a3
v_sub_f32 v251, v251, v249
v_mul_f32 v251, v251, v250
AW a3,v251
AR v254,a4
v_sub_f32 v254, v254, v249
v_mul_f32 v254, v254, v250
AW a4,v254
AR v251,a5
v_sub_f32 v251, v251, v249
v_mul_f32 v251, v251, v250
AW a5,v251
AR v254,a6
v_sub_f32 v254, v254, v249
v_mul_f32 v254, v254, v250
AW a6,v254
AR v251,a7
v_sub_f32 v251, v251, v249
v_mul_f32 v251, v251, v250
AW a7,v251
AR v254,a32
v_sub_f32 v254, v254, v249
v_mul_f32 v254, v254, v250
AW a32,v254
AR v251,a33
v_sub_f32 v251, v251, v249
v_mul_f32 v251, v251, v250
AW a33,v251
AR v254,a34
v_sub_f32 v254, v254, v249
v_mul_f32 v254, v254, v250
AW a34,v254
AR v251,a35
v_sub_f32 v251, v251, v249
v_mul_f32 v251, v251, v250
AW a35,v251
AR v254,a36
v_sub_f32 v254, v254, v249
v_mul_f32 v254, v254, v250
AW a36,v254
AR v251,a37
v_sub_f32 v251, v251, v249
v_mul_f32 v251, v251, v250
AW a37,v251
AR v254,a38
v_sub_f32 v254, v254, v249
v_mul_f32 v254, v254, v250
AW a38,v254
AR v251,a39
v_sub_f32 v251, v251, v249
v_mul_f32 v251, v251, v250
AW a39,v251
AR v254,a64
v_sub_f32 v254, v254, v249
v_mul_f32 v254, v254, v250
AW a64,v254
AR v251,a65
v_sub_f32 v251, v251, v249
v_mul_f32 v251, v251, v250
AW a65,v251
AR v254,a66
v_sub_f32 v254, v254, v249
v_mul_f32 v254, v254, v250
AW a66,v254
AR v251,a67
v_sub_f32 v251, v251, v249
v_mul_f32 v251, v251, v250
AW a67,v251
AR v254,a68
v_sub_f32 v254, v254, v249
v_mul_f32 v254, v254, v250
AW a68,v254
AR v251,a69
v_sub_f32 v251, v251, v249
v_mul_f32 v251, v251, v250
AW a69,v251
AR v254,a70
v_sub_f32 v254, v254, v249
v_mul_f32 v254, v254, v250
AW a70,v254
AR v251,a71
v_sub_f32 v251, v251, v249
v_mul_f32 v251, v251, v250
AW a71,v251
AR v254,a96
v_sub_f32 v254, v254, v249
v_mul_f32 v254, v254, v250
AW a96,v254
AR v251,a97
v_sub_f32 v251, v251, v249
v_mul_f32 v251, v251, v250
AW a97,v251
AR v254,a98
v_sub_f32 v254, v254, v249
v_mul_f32 v254, v254, v250
AW a98,v254
AR v251,a99
v_sub_f32 v251, v251, v249
v_mul_f32 v251, v251, v250
AW a99,v251
AR v254,a100
v_sub_f32 v254, v254, v249
v_mul_f32 v254, v254, v250
AW a100,v254
AR v251,a101
v_sub_f32 v251, v251, v249
v_mul_f32 v251, v251, v250
AW a101,v251
AR v254,a102
v_sub_f32 v254, v254, v249
v_mul_f32 v254, v254, v250
AW a102,v254
AR v251,a103
v_sub_f32 v251, v251, v249
v_mul_f32 v251, v251, v250
AW a103,v251
v_max3_f32 v249, v72, v73, v74
v_max3_f32 v251, v104, v105, v106
v_max3_f32 v249, v249, v75, v76
v_max3_f32 v251, v251, v107, v108
v_max3_f32 v249, v249, v77, v78
v_max3_f32 v251, v251, v109, v110
v_max3_f32 v249, v249, v79, v111
v_max_f32 v249, v249, v251
v_mov_b32 v251, v249
s_nop 1
v_permlane16_swap_b32 v249, v251
v_max_f32 v249, v249, v251
v_mov_b32 v251, v249
s_nop 1
v_permlane32_swap_b32 v249, v251
v_max_f32 v249, v249, v251
v_log_f32 v250, v245
s_nop 0
v_max3_f32 v249, 0, v249, v250
v_sub_f32 v250, 0, v249
EX v250,v250
v_sub_f32 v132, v132, v249
v_sub_f32 v133, v133, v249
v_sub_f32 v134, v134, v249
v_sub_f32 v135, v135, v249
v_sub_f32 v8, v8, v249
v_sub_f32 v72, v72, v249
v_sub_f32 v9, v9, v249
v_sub_f32 v73, v73, v249
v_sub_f32 v10, v10, v249
v_sub_f32 v74, v74, v249
v_sub_f32 v11, v11, v249
v_sub_f32 v75, v75, v249
v_sub_f32 v12, v12, v249
v_sub_f32 v76, v76, v249
v_sub_f32 v13, v13, v249
v_sub_f32 v77, v77, v249
v_sub_f32 v14, v14, v249
v_sub_f32 v78, v78, v249
v_sub_f32 v15, v15, v249
v_sub_f32 v79, v79, v249
v_sub_f32 v40, v40, v249
v_sub_f32 v104, v104, v249
v_sub_f32 v41, v41, v249
v_sub_f32 v105, v105, v249
v_sub_f32 v42, v42, v249
v_sub_f32 v106, v106, v249
v_sub_f32 v43, v43, v249
v_sub_f32 v107, v107, v249
v_sub_f32 v44, v44, v249
v_sub_f32 v108, v108, v249
v_sub_f32 v45, v45, v249
v_sub_f32 v109, v109, v249
v_sub_f32 v46, v46, v249
v_sub_f32 v110, v110, v249
v_sub_f32 v47, v47, v249
v_sub_f32 v111, v111, v249
v_mul_f32 v148, v245, v250
v_mov_b32 v149, 0
v_bfe_u32 v249, v241, 8, 8
v_lshl_or_b32 v249, v249, 1, 1
v_lshlrev_b32 v249, 22, v249
v_and_b32 v241, 0xffff00ff, v241
AR v254,a8
v_sub_f32 v254, v254, v249
v_mul_f32 v254, v254, v250
AW a8,v254
AR v251,a9
v_sub_f32 v251, v251, v249
v_mul_f32 v251, v251, v250
AW a9,v251
AR v254,a10
v_sub_f32 v254, v254, v249
v_mul_f32 v254, v254, v250
AW a10,v254
AR v251,a11
v_sub_f32 v251, v251, v249
v_mul_f32 v251, v251, v250
AW a11,v251
AR v254,a12
v_sub_f32 v254, v254, v249
v_mul_f32 v254, v254, v250
AW a12,v254
AR v251,a13
v_sub_f32 v251, v251, v249
v_mul_f32 v251, v251, v250
AW a13,v251
AR v254,a14
v_sub_f32 v254, v254, v249
v_mul_f32 v254, v254, v250
AW a14,v254
AR v251,a15
v_sub_f32 v251, v251, v249
v_mul_f32 v251, v251, v250
AW a15,v251
AR v254,a40
v_sub_f32 v254, v254, v249
v_mul_f32 v254, v254, v250
AW a40,v254
AR v251,a41
v_sub_f32 v251, v251, v249
v_mul_f32 v251, v251, v250
AW a41,v251
AR v254,a42
v_sub_f32 v254, v254, v249
v_mul_f32 v254, v254, v250
AW a42,v254
AR v251,a43
v_sub_f32 v251, v251, v249
v_mul_f32 v251, v251, v250
AW a43,v251
AR v254,a44
v_sub_f32 v254, v254, v249
v_mul_f32 v254, v254, v250
AW a44,v254
AR v251,a45
v_sub_f32 v251, v251, v249
v_mul_f32 v251, v251, v250
AW a45,v251
AR v254,a46
v_sub_f32 v254, v254, v249
v_mul_f32 v254, v254, v250
AW a46,v254
AR v251,a47
v_sub_f32 v251, v251, v249
v_mul_f32 v251, v251, v250
AW a47,v251
AR v254,a72
v_sub_f32 v254, v254, v249
v_mul_f32 v254, v254, v250
AW a72,v254
AR v251,a73
v_sub_f32 v251, v251, v249
v_mul_f32 v251, v251, v250
AW a73,v251
AR v254,a74
v_sub_f32 v254, v254, v249
v_mul_f32 v254, v254, v250
AW a74,v254
AR v251,a75
v_sub_f32 v251, v251, v249
v_mul_f32 v251, v251, v250
AW a75,v251
AR v254,a76
v_sub_f32 v254, v254, v249
v_mul_f32 v254, v254, v250
AW a76,v254
AR v251,a77
v_sub_f32 v251, v251, v249
v_mul_f32 v251, v251, v250
AW a77,v251
)ASM"
R"ASM(
AR v254,a78
v_sub_f32 v254, v254, v249
v_mul_f32 v254, v254, v250
AW a78,v254
AR v251,a79
v_sub_f32 v251, v251, v249
v_mul_f32 v251, v251, v250
AW a79,v251
AR v254,a104
v_sub_f32 v254, v254, v249
v_mul_f32 v254, v254, v250
AW a104,v254
AR v251,a105
v_sub_f32 v251, v251, v249
v_mul_f32 v251, v251, v250
AW a105,v251
AR v254,a106
v_sub_f32 v254, v254, v249
v_mul_f32 v254, v254, v250
AW a106,v254
AR v251,a107
v_sub_f32 v251, v251, v249
v_mul_f32 v251, v251, v250
AW a107,v251
AR v254,a108
v_sub_f32 v254, v254, v249
v_mul_f32 v254, v254, v250
AW a108,v254
AR v251,a109
v_sub_f32 v251, v251, v249
v_mul_f32 v251, v251, v250
AW a109,v251
AR v254,a110
v_sub_f32 v254, v254, v249
v_mul_f32 v254, v254, v250
AW a110,v254
AR v251,a111
v_sub_f32 v251, v251, v249
v_mul_f32 v251, v251, v250
AW a111,v251
v_max3_f32 v249, v80, v81, v82
v_max3_f32 v251, v112, v113, v114
v_max3_f32 v249, v249, v83, v84
v_max3_f32 v251, v251, v115, v116
v_max3_f32 v249, v249, v85, v86
v_max3_f32 v251, v251, v117, v118
v_max3_f32 v249, v249, v87, v119
v_max_f32 v249, v249, v251
v_mov_b32 v251, v249
s_nop 1
v_permlane16_swap_b32 v249, v251
v_max_f32 v249, v249, v251
v_mov_b32 v251, v249
s_nop 1
v_permlane32_swap_b32 v249, v251
v_max_f32 v249, v249, v251
v_log_f32 v250, v246
s_nop 0
v_max3_f32 v249, 0, v249, v250
v_sub_f32 v250, 0, v249
EX v250,v250
v_sub_f32 v136, v136, v249
v_sub_f32 v137, v137, v249
v_sub_f32 v138, v138, v249
v_sub_f32 v139, v139, v249
v_sub_f32 v16, v16, v249
v_sub_f32 v80, v80, v249
v_sub_f32 v17, v17, v249
v_sub_f32 v81, v81, v249
v_sub_f32 v18, v18, v249
v_sub_f32 v82, v82, v249
v_sub_f32 v19, v19, v249
v_sub_f32 v83, v83, v249
v_sub_f32 v20, v20, v249
v_sub_f32 v84, v84, v249
v_sub_f32 v21, v21, v249
v_sub_f32 v85, v85, v249
v_sub_f32 v22, v22, v249
v_sub_f32 v86, v86, v249
v_sub_f32 v23, v23, v249
v_sub_f32 v87, v87, v249
v_sub_f32 v48, v48, v249
v_sub_f32 v112, v112, v249
v_sub_f32 v49, v49, v249
v_sub_f32 v113, v113, v249
v_sub_f32 v50, v50, v249
v_sub_f32 v114, v114, v249
v_sub_f32 v51, v51, v249
v_sub_f32 v115, v115, v249
v_sub_f32 v52, v52, v249
v_sub_f32 v116, v116, v249
v_sub_f32 v53, v53, v249
v_sub_f32 v117, v117, v249
v_sub_f32 v54, v54, v249
v_sub_f32 v118, v118, v249
v_sub_f32 v55, v55, v249
v_sub_f32 v119, v119, v249
v_mul_f32 v152, v246, v250
v_mov_b32 v153, 0
v_bfe_u32 v249, v241, 16, 8
v_lshl_or_b32 v249, v249, 1, 1
v_lshlrev_b32 v249, 22, v249
v_and_b32 v241, 0xff00ffff, v241
AR v254,a16
v_sub_f32 v254, v254, v249
v_mul_f32 v254, v254, v250
AW a16,v254
AR v251,a17
v_sub_f32 v251, v251, v249
v_mul_f32 v251, v251, v250
AW a17,v251
AR v254,a18
v_sub_f32 v254, v254, v249
v_mul_f32 v254, v254, v250
AW a18,v254
AR v251,a19
v_sub_f32 v251, v251, v249
v_mul_f32 v251, v251, v250
AW a19,v251
AR v254,a20
v_sub_f32 v254, v254, v249
v_mul_f32 v254, v254, v250
AW a20,v254
AR v251,a21
v_sub_f32 v251, v251, v249
v_mul_f32 v251, v251, v250
AW a21,v251
AR v254,a22
v_sub_f32 v254, v254, v249
v_mul_f32 v254, v254, v250
AW a22,v254
AR v251,a23
v_sub_f32 v251, v251, v249
v_mul_f32 v251, v251, v250
AW a23,v251
AR v254,a48
v_sub_f32 v254, v254, v249
v_mul_f32 v254, v254, v250
AW a48,v254
AR v251,a49
v_sub_f32 v251, v251, v249
v_mul_f32 v251, v251, v250
AW a49,v251
AR v254,a50
v_sub_f32 v254, v254, v249
v_mul_f32 v254, v254, v250
AW a50,v254
AR v251,a51
v_sub_f32 v251, v251, v249
v_mul_f32 v251, v251, v250
AW a51,v251
AR v254,a52
v_sub_f32 v254, v254, v249
v_mul_f32 v254, v254, v250
AW a52,v254
AR v251,a53
v_sub_f32 v251, v251, v249
v_mul_f32 v251, v251, v250
AW a53,v251
AR v254,a54
v_sub_f32 v254, v254, v249
v_mul_f32 v254, v254, v250
AW a54,v254
AR v251,a55
v_sub_f32 v251, v251, v249
v_mul_f32 v251, v251, v250
AW a55,v251
AR v254,a80
v_sub_f32 v254, v254, v249
v_mul_f32 v254, v254, v250
AW a80,v254
AR v251,a81
v_sub_f32 v251, v251, v249
v_mul_f32 v251, v251, v250
AW a81,v251
AR v254,a82
v_sub_f32 v254, v254, v249
v_mul_f32 v254, v254, v250
AW a82,v254
AR v251,a83
v_sub_f32 v251, v251, v249
v_mul_f32 v251, v251, v250
AW a83,v251
AR v254,a84
v_sub_f32 v254, v254, v249
v_mul_f32 v254, v254, v250
AW a84,v254
AR v251,a85
v_sub_f32 v251, v251, v249
v_mul_f32 v251, v251, v250
AW a85,v251
AR v254,a86
v_sub_f32 v254, v254, v249
v_mul_f32 v254, v254, v250
AW a86,v254
AR v251,a87
v_sub_f32 v251, v251, v249
v_mul_f32 v251, v251, v250
AW a87,v251
AR v254,a112
v_sub_f32 v254, v254, v249
v_mul_f32 v254, v254, v250
AW a112,v254
AR v251,a113
v_sub_f32 v251, v251, v249
v_mul_f32 v251, v251, v250
AW a113,v251
AR v254,a114
v_sub_f32 v254, v254, v249
v_mul_f32 v254, v254, v250
AW a114,v254
AR v251,a115
v_sub_f32 v251, v251, v249
v_mul_f32 v251, v251, v250
AW a115,v251
AR v254,a116
v_sub_f32 v254, v254, v249
v_mul_f32 v254, v254, v250
AW a116,v254
AR v251,a117
v_sub_f32 v251, v251, v249
v_mul_f32 v251, v251, v250
AW a117,v251
AR v254,a118
v_sub_f32 v254, v254, v249
v_mul_f32 v254, v254, v250
AW a118,v254
AR v251,a119
v_sub_f32 v251, v251, v249
v_mul_f32 v251, v251, v250
AW a119,v251
v_max3_f32 v249, v88, v89, v90
v_max3_f32 v251, v120, v121, v122
v_max3_f32 v249, v249, v91, v92
v_max3_f32 v251, v251, v123, v124
v_max3_f32 v249, v249, v93, v94
v_max3_f32 v251, v251, v125, v126
v_max3_f32 v249, v249, v95, v127
v_max_f32 v249, v249, v251
v_mov_b32 v251, v249
s_nop 1
v_permlane16_swap_b32 v249, v251
v_max_f32 v249, v249, v251
v_mov_b32 v251, v249
s_nop 1
v_permlane32_swap_b32 v249, v251
v_max_f32 v249, v249, v251
v_log_f32 v250, v247
s_nop 0
v_max3_f32 v249, 0, v249, v250
v_sub_f32 v250, 0, v249
EX v250,v250
v_sub_f32 v140, v140, v249
v_sub_f32 v141, v141, v249
v_sub_f32 v142, v142, v249
v_sub_f32 v143, v143, v249
v_sub_f32 v24, v24, v249
v_sub_f32 v88, v88, v249
v_sub_f32 v25, v25, v249
v_sub_f32 v89, v89, v249
v_sub_f32 v26, v26, v249
v_sub_f32 v90, v90, v249
v_sub_f32 v27, v27, v249
v_sub_f32 v91, v91, v249
v_sub_f32 v28, v28, v249
v_sub_f32 v92, v92, v249
v_sub_f32 v29, v29, v249
v_sub_f32 v93, v93, v249
v_sub_f32 v30, v30, v249
v_sub_f32 v94, v94, v249
v_sub_f32 v31, v31, v249
v_sub_f32 v95, v95, v249
v_sub_f32 v56, v56, v249
v_sub_f32 v120, v120, v249
v_sub_f32 v57, v57, v249
v_sub_f32 v121, v121, v249
v_sub_f32 v58, v58, v249
v_sub_f32 v122, v122, v249
v_sub_f32 v59, v59, v249
v_sub_f32 v123, v123, v249
v_sub_f32 v60, v60, v249
v_sub_f32 v124, v124, v249
v_sub_f32 v61, v61, v249
v_sub_f32 v125, v125, v249
v_sub_f32 v62, v62, v249
v_sub_f32 v126, v126, v249
v_sub_f32 v63, v63, v249
v_sub_f32 v127, v127, v249
v_mul_f32 v156, v247, v250
v_mov_b32 v157, 0
v_bfe_u32 v249, v241, 24, 8
v_lshl_or_b32 v249, v249, 1, 1
v_lshlrev_b32 v249, 22, v249
v_and_b32 v241, 0xffffff, v241
AR v254,a24
v_sub_f32 v254, v254, v249
v_mul_f32 v254, v254, v250
AW a24,v254
AR v251,a25
v_sub_f32 v251, v251, v249
v_mul_f32 v251, v251, v250
AW a25,v251
AR v254,a26
v_sub_f32 v254, v254, v249
v_mul_f32 v254, v254, v250
AW a26,v254
AR v251,a27
v_sub_f32 v251, v251, v249
v_mul_f32 v251, v251, v250
AW a27,v251
AR v254,a28
v_sub_f32 v254, v254, v249
v_mul_f32 v254, v254, v250
AW a28,v254
AR v251,a29
v_sub_f32 v251, v251, v249
v_mul_f32 v251, v251, v250
AW a29,v251
AR v254,a30
v_sub_f32 v254, v254, v249
v_mul_f32 v254, v254, v250
AW a30,v254
AR v251,a31
v_sub_f32 v251, v251, v249
v_mul_f32 v251, v251, v250
AW a31,v251
AR v254,a56
v_sub_f32 v254, v254, v249
v_mul_f32 v254, v254, v250
AW a56,v254
AR v251,a57
v_sub_f32 v251, v251, v249
v_mul_f32 v251, v251, v250
AW a57,v251
AR v254,a58
v_sub_f32 v254, v254, v249
v_mul_f32 v254, v254, v250
AW a58,v254
AR v251,a59
v_sub_f32 v251, v251, v249
v_mul_f32 v251, v251, v250
AW a59,v251
AR v254,a60
v_sub_f32 v254, v254, v249
v_mul_f32 v254, v254, v250
AW a60,v254
AR v251,a61
v_sub_f32 v251, v251, v249
v_mul_f32 v251, v251, v250
AW a61,v251
AR v254,a62
v_sub_f32 v254, v254, v249
v_mul_f32 v254, v254, v250
AW a62,v254
AR v251,a63
v_sub_f32 v251, v251, v249
v_mul_f32 v251, v251, v250
AW a63,v251
AR v254,a88
v_sub_f32 v254, v254, v249
v_mul_f32 v254, v254, v250
AW a88,v254
AR v251,a89
v_sub_f32 v251, v251, v249
v_mul_f32 v251, v251, v250
AW a89,v251
AR v254,a90
v_sub_f32 v254, v254, v249
v_mul_f32 v254, v254, v250
AW a90,v254
AR v251,a91
v_sub_f32 v251, v251, v249
v_mul_f32 v251, v251, v250
AW a91,v251
AR v254,a92
v_sub_f32 v254, v254, v249
v_mul_f32 v254, v254, v250
AW a92,v254
AR v251,a93
v_sub_f32 v251, v251, v249
v_mul_f32 v251, v251, v250
AW a93,v251
AR v254,a94
v_sub_f32 v254, v254, v249
v_mul_f32 v254, v254, v250
AW a94,v254
AR v251,a95
v_sub_f32 v251, v251, v249
v_mul_f32 v251, v251, v250
AW a95,v251
AR v254,a120
v_sub_f32 v254, v254, v249
v_mul_f32 v254, v254, v250
AW a120,v254
AR v251,a121
v_sub_f32 v251, v251, v249
v_mul_f32 v251, v251, v250
AW a121,v251
AR v254,a122
v_sub_f32 v254, v254, v249
v_mul_f32 v254, v254, v250
AW a122,v254
AR v251,a123
v_sub_f32 v251, v251, v249
v_mul_f32 v251, v251, v250
AW a123,v251
AR v254,a124
v_sub_f32 v254, v254, v249
v_mul_f32 v254, v254, v250
AW a124,v254
AR v251,a125
v_sub_f32 v251, v251, v249
v_mul_f32 v251, v251, v250
AW a125,v251
AR v254,a126
v_sub_f32 v254, v254, v249
v_mul_f32 v254, v254, v250
AW a126,v254
AR v251,a127
v_sub_f32 v251, v251, v249
v_mul_f32 v251, v251, v250
AW a127,v251
EX v64,v64
EX v65,v65
EX v66,v66
EX v67,v67
EX v68,v68
EX v69,v69
EX v70,v70
EX v71,v71
EX v72,v72
EX v73,v73
EX v74,v74
EX v75,v75
EX v76,v76
EX v77,v77
EX v78,v78
EX v79,v79
EX v80,v80
EX v81,v81
EX v82,v82
EX v83,v83
EX v84,v84
EX v85,v85
EX v86,v86
EX v87,v87
EX v88,v88
EX v89,v89
EX v90,v90
EX v91,v91
EX v92,v92
EX v93,v93
EX v94,v94
EX v95,v95
EX v96,v96
EX v97,v97
EX v98,v98
EX v99,v99
EX v100,v100
EX v101,v101
EX v102,v102
EX v103,v103
EX v104,v104
EX v105,v105
EX v106,v106
EX v107,v107
EX v108,v108
EX v109,v109
EX v110,v110
EX v111,v111
EX v112,v112
EX v113,v113
EX v114,v114
EX v115,v115
EX v116,v116
EX v117,v117
EX v118,v118
EX v119,v119
EX v120,v120
EX v121,v121
EX v122,v122
EX v123,v123
EX v124,v124
EX v125,v125
EX v126,v126
EX v127,v127
CV v64,v64,v65
CV v65,v66,v67
CV v66,v68,v69
CV v67,v70,v71
CV v72,v72,v73
CV v73,v74,v75
CV v74,v76,v77
CV v75,v78,v79
CV v80,v80,v81
CV v81,v82,v83
CV v82,v84,v85
CV v83,v86,v87
CV v88,v88,v89
CV v89,v90,v91
CV v90,v92,v93
CV v91,v94,v95
CV v68,v96,v97
CV v69,v98,v99
CV v70,v100,v101
CV v71,v102,v103
CV v76,v104,v105
CV v77,v106,v107
CV v78,v108,v109
CV v79,v110,v111
CV v84,v112,v113
CV v85,v114,v115
CV v86,v116,v117
CV v87,v118,v119
CV v92,v120,v121
CV v93,v122,v123
CV v94,v124,v125
CV v95,v126,v127
v_smfmac_f32_16x16x64_bf16 v[144:147], v[236:239], v[64:71], v234
v_smfmac_f32_16x16x64_bf16 v[148:151], v[236:239], v[72:79], v234
v_smfmac_f32_16x16x64_bf16 v[152:155], v[236:239], v[80:87], v234
v_smfmac_f32_16x16x64_bf16 v[156:159], v[236:239], v[88:95], v234
s_nop 9
s_branch LRET_%=
LRARE004_%=:
s_nop 15
s_waitcnt vmcnt(0) lgkmcnt(0)
v_add_u32 v249, s74, v224
RD a[192:195],v249 offset:0
RD a[208:211],v249 offset:4096
RD a[224:227],v249 offset:8192
RD a[240:243],v249 offset:12288
v_add_u32 v250, s74, v225
RD a[196:199],v250 offset:0
RD a[212:215],v250 offset:4096
RD a[228:231],v250 offset:8192
RD a[244:247],v250 offset:12288
v_add_u32 v251, s74, v226
RD a[200:203],v251 offset:0
RD a[216:219],v251 offset:4096
RD a[232:235],v251 offset:8192
RD a[248:251],v251 offset:12288
v_add_u32 v254, s74, v227
RD a[204:207],v254 offset:0
RD a[220:223],v254 offset:4096
RD a[236:239],v254 offset:8192
RD a[252:255],v254 offset:12288
s_waitcnt lgkmcnt(0)
MM v[0:3],a[192:195],v[160:163],v[128:131]
MM v[0:3],a[196:199],v[176:179],v[0:3]
MM v[0:3],a[200:203],v[192:195],v[0:3]
MM v[0:3],a[204:207],v[208:211],v[0:3]
MM v[4:7],a[220:223],v[208:211],v[128:131]
MM v[4:7],a[216:219],v[192:195],v[4:7]
MM v[4:7],a[212:215],v[176:179],v[4:7]
MM v[4:7],a[208:211],v[160:163],v[4:7]
MM v[12:15],a[208:211],v[164:167],v[132:135]
MM v[12:15],a[212:215],v[180:183],v[12:15]
MM v[12:15],a[216:219],v[196:199],v[12:15]
MM v[12:15],a[220:223],v[212:215],v[12:15]
MM v[8:11],a[204:207],v[212:215],v[132:135]
MM v[8:11],a[200:203],v[196:199],v[8:11]
MM v[8:11],a[196:199],v[180:183],v[8:11]
MM v[8:11],a[192:195],v[164:167],v[8:11]
MM v[16:19],a[192:195],v[168:171],v[136:139]
MM v[16:19],a[196:199],v[184:187],v[16:19]
)ASM"
R"ASM(
MM v[16:19],a[200:203],v[200:203],v[16:19]
MM v[16:19],a[204:207],v[216:219],v[16:19]
MM v[20:23],a[220:223],v[216:219],v[136:139]
MM v[20:23],a[216:219],v[200:203],v[20:23]
MM v[20:23],a[212:215],v[184:187],v[20:23]
MM v[20:23],a[208:211],v[168:171],v[20:23]
MM v[28:31],a[208:211],v[172:175],v[140:143]
MM v[28:31],a[212:215],v[188:191],v[28:31]
MM v[28:31],a[216:219],v[204:207],v[28:31]
MM v[28:31],a[220:223],v[220:223],v[28:31]
MM v[24:27],a[204:207],v[220:223],v[140:143]
MM v[24:27],a[200:203],v[204:207],v[24:27]
MM v[24:27],a[196:199],v[188:191],v[24:27]
MM v[24:27],a[192:195],v[172:175],v[24:27]
MM v[32:35],a[224:227],v[160:163],v[128:131]
MM v[32:35],a[228:231],v[176:179],v[32:35]
MM v[32:35],a[232:235],v[192:195],v[32:35]
MM v[32:35],a[236:239],v[208:211],v[32:35]
MM v[36:39],a[252:255],v[208:211],v[128:131]
MM v[36:39],a[248:251],v[192:195],v[36:39]
MM v[36:39],a[244:247],v[176:179],v[36:39]
MM v[36:39],a[240:243],v[160:163],v[36:39]
MM v[44:47],a[240:243],v[164:167],v[132:135]
MM v[44:47],a[244:247],v[180:183],v[44:47]
MM v[44:47],a[248:251],v[196:199],v[44:47]
MM v[44:47],a[252:255],v[212:215],v[44:47]
MM v[40:43],a[236:239],v[212:215],v[132:135]
MM v[40:43],a[232:235],v[196:199],v[40:43]
MM v[40:43],a[228:231],v[180:183],v[40:43]
MM v[40:43],a[224:227],v[164:167],v[40:43]
MM v[48:51],a[224:227],v[168:171],v[136:139]
MM v[48:51],a[228:231],v[184:187],v[48:51]
MM v[48:51],a[232:235],v[200:203],v[48:51]
MM v[48:51],a[236:239],v[216:219],v[48:51]
MM v[52:55],a[252:255],v[216:219],v[136:139]
MM v[52:55],a[248:251],v[200:203],v[52:55]
MM v[52:55],a[244:247],v[184:187],v[52:55]
MM v[52:55],a[240:243],v[168:171],v[52:55]
MM v[60:63],a[240:243],v[172:175],v[140:143]
MM v[60:63],a[244:247],v[188:191],v[60:63]
MM v[60:63],a[248:251],v[204:207],v[60:63]
MM v[60:63],a[252:255],v[220:223],v[60:63]
MM v[56:59],a[236:239],v[220:223],v[140:143]
MM v[56:59],a[232:235],v[204:207],v[56:59]
MM v[56:59],a[228:231],v[188:191],v[56:59]
MM v[56:59],a[224:227],v[172:175],v[56:59]
s_nop 15
v_add_u32 v249, s75, v224
RD a[192:195],v249 offset:0
RD a[208:211],v249 offset:4096
RD a[224:227],v249 offset:8192
RD a[240:243],v249 offset:12288
v_add_u32 v250, s75, v225
RD a[196:199],v250 offset:0
RD a[212:215],v250 offset:4096
RD a[228:231],v250 offset:8192
RD a[244:247],v250 offset:12288
v_add_u32 v251, s75, v226
RD a[200:203],v251 offset:0
RD a[216:219],v251 offset:4096
RD a[232:235],v251 offset:8192
RD a[248:251],v251 offset:12288
v_add_u32 v254, s75, v227
RD a[204:207],v254 offset:0
RD a[220:223],v254 offset:4096
RD a[236:239],v254 offset:8192
RD a[252:255],v254 offset:12288
s_waitcnt lgkmcnt(0)
v_max3_f32 v249, v0, v1, v2
v_max3_f32 v251, v32, v33, v34
v_max3_f32 v249, v249, v3, v4
v_max3_f32 v251, v251, v35, v36
v_max3_f32 v249, v249, v5, v6
v_max3_f32 v251, v251, v37, v38
v_max3_f32 v249, v249, v7, v39
v_max_f32 v249, v249, v251
v_mov_b32 v251, v249
s_nop 1
v_permlane16_swap_b32 v249, v251
v_max_f32 v249, v249, v251
v_mov_b32 v251, v249
s_nop 1
v_permlane32_swap_b32 v249, v251
v_max_f32 v249, v249, v251
v_log_f32 v250, v244
s_nop 0
v_max3_f32 v249, 0, v249, v250
v_sub_f32 v250, 0, v249
EX v250,v250
v_sub_f32 v128, v128, v249
v_sub_f32 v129, v129, v249
v_sub_f32 v130, v130, v249
v_sub_f32 v131, v131, v249
v_sub_f32 v64, v64, v249
v_sub_f32 v0, v0, v249
v_sub_f32 v65, v65, v249
v_sub_f32 v1, v1, v249
v_sub_f32 v66, v66, v249
v_sub_f32 v2, v2, v249
v_sub_f32 v67, v67, v249
v_sub_f32 v3, v3, v249
v_sub_f32 v68, v68, v249
v_sub_f32 v4, v4, v249
v_sub_f32 v69, v69, v249
v_sub_f32 v5, v5, v249
v_sub_f32 v70, v70, v249
v_sub_f32 v6, v6, v249
v_sub_f32 v71, v71, v249
v_sub_f32 v7, v7, v249
v_sub_f32 v96, v96, v249
v_sub_f32 v32, v32, v249
v_sub_f32 v97, v97, v249
v_sub_f32 v33, v33, v249
v_sub_f32 v98, v98, v249
v_sub_f32 v34, v34, v249
v_sub_f32 v99, v99, v249
v_sub_f32 v35, v35, v249
v_sub_f32 v100, v100, v249
v_sub_f32 v36, v36, v249
v_sub_f32 v101, v101, v249
v_sub_f32 v37, v37, v249
v_sub_f32 v102, v102, v249
v_sub_f32 v38, v38, v249
v_sub_f32 v103, v103, v249
v_sub_f32 v39, v39, v249
v_mul_f32 v144, v244, v250
v_mov_b32 v145, 0
v_bfe_u32 v249, v241, 0, 8
v_lshl_or_b32 v249, v249, 1, 1
v_lshlrev_b32 v249, 22, v249
v_and_b32 v241, 0xffffff00, v241
AR v254,a0
v_sub_f32 v254, v254, v249
v_mul_f32 v254, v254, v250
AW a0,v254
AR v251,a1
v_sub_f32 v251, v251, v249
v_mul_f32 v251, v251, v250
AW a1,v251
AR v254,a2
v_sub_f32 v254, v254, v249
v_mul_f32 v254, v254, v250
AW a2,v254
AR v251,a3
v_sub_f32 v251, v251, v249
v_mul_f32 v251, v251, v250
AW a3,v251
AR v254,a4
v_sub_f32 v254, v254, v249
v_mul_f32 v254, v254, v250
AW a4,v254
AR v251,a5
v_sub_f32 v251, v251, v249
v_mul_f32 v251, v251, v250
AW a5,v251
AR v254,a6
v_sub_f32 v254, v254, v249
v_mul_f32 v254, v254, v250
AW a6,v254
AR v251,a7
v_sub_f32 v251, v251, v249
v_mul_f32 v251, v251, v250
AW a7,v251
AR v254,a32
v_sub_f32 v254, v254, v249
v_mul_f32 v254, v254, v250
AW a32,v254
AR v251,a33
v_sub_f32 v251, v251, v249
v_mul_f32 v251, v251, v250
AW a33,v251
AR v254,a34
v_sub_f32 v254, v254, v249
v_mul_f32 v254, v254, v250
AW a34,v254
AR v251,a35
v_sub_f32 v251, v251, v249
v_mul_f32 v251, v251, v250
AW a35,v251
AR v254,a36
v_sub_f32 v254, v254, v249
v_mul_f32 v254, v254, v250
AW a36,v254
AR v251,a37
v_sub_f32 v251, v251, v249
v_mul_f32 v251, v251, v250
AW a37,v251
AR v254,a38
v_sub_f32 v254, v254, v249
v_mul_f32 v254, v254, v250
AW a38,v254
AR v251,a39
v_sub_f32 v251, v251, v249
v_mul_f32 v251, v251, v250
AW a39,v251
AR v254,a64
v_sub_f32 v254, v254, v249
v_mul_f32 v254, v254, v250
AW a64,v254
AR v251,a65
v_sub_f32 v251, v251, v249
v_mul_f32 v251, v251, v250
AW a65,v251
AR v254,a66
v_sub_f32 v254, v254, v249
v_mul_f32 v254, v254, v250
AW a66,v254
AR v251,a67
v_sub_f32 v251, v251, v249
v_mul_f32 v251, v251, v250
AW a67,v251
AR v254,a68
v_sub_f32 v254, v254, v249
v_mul_f32 v254, v254, v250
AW a68,v254
AR v251,a69
v_sub_f32 v251, v251, v249
v_mul_f32 v251, v251, v250
AW a69,v251
AR v254,a70
v_sub_f32 v254, v254, v249
v_mul_f32 v254, v254, v250
AW a70,v254
AR v251,a71
v_sub_f32 v251, v251, v249
v_mul_f32 v251, v251, v250
AW a71,v251
AR v254,a96
v_sub_f32 v254, v254, v249
v_mul_f32 v254, v254, v250
AW a96,v254
AR v251,a97
v_sub_f32 v251, v251, v249
v_mul_f32 v251, v251, v250
AW a97,v251
AR v254,a98
v_sub_f32 v254, v254, v249
v_mul_f32 v254, v254, v250
AW a98,v254
AR v251,a99
v_sub_f32 v251, v251, v249
v_mul_f32 v251, v251, v250
AW a99,v251
AR v254,a100
v_sub_f32 v254, v254, v249
v_mul_f32 v254, v254, v250
AW a100,v254
AR v251,a101
v_sub_f32 v251, v251, v249
v_mul_f32 v251, v251, v250
AW a101,v251
AR v254,a102
v_sub_f32 v254, v254, v249
v_mul_f32 v254, v254, v250
AW a102,v254
AR v251,a103
v_sub_f32 v251, v251, v249
v_mul_f32 v251, v251, v250
AW a103,v251
v_max3_f32 v249, v8, v9, v10
v_max3_f32 v251, v40, v41, v42
v_max3_f32 v249, v249, v11, v12
v_max3_f32 v251, v251, v43, v44
v_max3_f32 v249, v249, v13, v14
v_max3_f32 v251, v251, v45, v46
v_max3_f32 v249, v249, v15, v47
v_max_f32 v249, v249, v251
v_mov_b32 v251, v249
s_nop 1
v_permlane16_swap_b32 v249, v251
v_max_f32 v249, v249, v251
v_mov_b32 v251, v249
s_nop 1
v_permlane32_swap_b32 v249, v251
v_max_f32 v249, v249, v251
v_log_f32 v250, v245
s_nop 0
v_max3_f32 v249, 0, v249, v250
v_sub_f32 v250, 0, v249
EX v250,v250
v_sub_f32 v132, v132, v249
v_sub_f32 v133, v133, v249
v_sub_f32 v134, v134, v249
v_sub_f32 v135, v135, v249
v_sub_f32 v72, v72, v249
v_sub_f32 v8, v8, v249
v_sub_f32 v73, v73, v249
v_sub_f32 v9, v9, v249
v_sub_f32 v74, v74, v249
v_sub_f32 v10, v10, v249
v_sub_f32 v75, v75, v249
v_sub_f32 v11, v11, v249
v_sub_f32 v76, v76, v249
v_sub_f32 v12, v12, v249
v_sub_f32 v77, v77, v249
v_sub_f32 v13, v13, v249
v_sub_f32 v78, v78, v249
v_sub_f32 v14, v14, v249
v_sub_f32 v79, v79, v249
v_sub_f32 v15, v15, v249
v_sub_f32 v104, v104, v249
v_sub_f32 v40, v40, v249
v_sub_f32 v105, v105, v249
v_sub_f32 v41, v41, v249
v_sub_f32 v106, v106, v249
v_sub_f32 v42, v42, v249
v_sub_f32 v107, v107, v249
v_sub_f32 v43, v43, v249
v_sub_f32 v108, v108, v249
v_sub_f32 v44, v44, v249
v_sub_f32 v109, v109, v249
v_sub_f32 v45, v45, v249
v_sub_f32 v110, v110, v249
v_sub_f32 v46, v46, v249
v_sub_f32 v111, v111, v249
v_sub_f32 v47, v47, v249
v_mul_f32 v148, v245, v250
v_mov_b32 v149, 0
v_bfe_u32 v249, v241, 8, 8
v_lshl_or_b32 v249, v249, 1, 1
v_lshlrev_b32 v249, 22, v249
v_and_b32 v241, 0xffff00ff, v241
AR v254,a8
v_sub_f32 v254, v254, v249
v_mul_f32 v254, v254, v250
AW a8,v254
AR v251,a9
v_sub_f32 v251, v251, v249
v_mul_f32 v251, v251, v250
AW a9,v251
AR v254,a10
v_sub_f32 v254, v254, v249
v_mul_f32 v254, v254, v250
AW a10,v254
AR v251,a11
v_sub_f32 v251, v251, v249
v_mul_f32 v251, v251, v250
AW a11,v251
AR v254,a12
v_sub_f32 v254, v254, v249
v_mul_f32 v254, v254, v250
AW a12,v254
AR v251,a13
v_sub_f32 v251, v251, v249
v_mul_f32 v251, v251, v250
AW a13,v251
AR v254,a14
v_sub_f32 v254, v254, v249
v_mul_f32 v254, v254, v250
AW a14,v254
AR v251,a15
v_sub_f32 v251, v251, v249
v_mul_f32 v251, v251, v250
AW a15,v251
AR v254,a40
v_sub_f32 v254, v254, v249
v_mul_f32 v254, v254, v250
AW a40,v254
AR v251,a41
v_sub_f32 v251, v251, v249
v_mul_f32 v251, v251, v250
AW a41,v251
AR v254,a42
v_sub_f32 v254, v254, v249
v_mul_f32 v254, v254, v250
AW a42,v254
AR v251,a43
v_sub_f32 v251, v251, v249
v_mul_f32 v251, v251, v250
AW a43,v251
AR v254,a44
v_sub_f32 v254, v254, v249
v_mul_f32 v254, v254, v250
AW a44,v254
AR v251,a45
v_sub_f32 v251, v251, v249
v_mul_f32 v251, v251, v250
AW a45,v251
AR v254,a46
v_sub_f32 v254, v254, v249
v_mul_f32 v254, v254, v250
AW a46,v254
AR v251,a47
v_sub_f32 v251, v251, v249
v_mul_f32 v251, v251, v250
AW a47,v251
AR v254,a72
v_sub_f32 v254, v254, v249
v_mul_f32 v254, v254, v250
AW a72,v254
AR v251,a73
v_sub_f32 v251, v251, v249
v_mul_f32 v251, v251, v250
AW a73,v251
AR v254,a74
v_sub_f32 v254, v254, v249
v_mul_f32 v254, v254, v250
AW a74,v254
AR v251,a75
v_sub_f32 v251, v251, v249
v_mul_f32 v251, v251, v250
AW a75,v251
AR v254,a76
v_sub_f32 v254, v254, v249
v_mul_f32 v254, v254, v250
AW a76,v254
AR v251,a77
v_sub_f32 v251, v251, v249
v_mul_f32 v251, v251, v250
AW a77,v251
AR v254,a78
v_sub_f32 v254, v254, v249
v_mul_f32 v254, v254, v250
AW a78,v254
AR v251,a79
v_sub_f32 v251, v251, v249
v_mul_f32 v251, v251, v250
AW a79,v251
AR v254,a104
v_sub_f32 v254, v254, v249
v_mul_f32 v254, v254, v250
AW a104,v254
AR v251,a105
v_sub_f32 v251, v251, v249
v_mul_f32 v251, v251, v250
AW a105,v251
AR v254,a106
v_sub_f32 v254, v254, v249
v_mul_f32 v254, v254, v250
AW a106,v254
AR v251,a107
v_sub_f32 v251, v251, v249
v_mul_f32 v251, v251, v250
AW a107,v251
AR v254,a108
v_sub_f32 v254, v254, v249
v_mul_f32 v254, v254, v250
AW a108,v254
AR v251,a109
v_sub_f32 v251, v251, v249
v_mul_f32 v251, v251, v250
AW a109,v251
AR v254,a110
v_sub_f32 v254, v254, v249
v_mul_f32 v254, v254, v250
AW a110,v254
AR v251,a111
v_sub_f32 v251, v251, v249
v_mul_f32 v251, v251, v250
AW a111,v251
v_max3_f32 v249, v16, v17, v18
v_max3_f32 v251, v48, v49, v50
v_max3_f32 v249, v249, v19, v20
v_max3_f32 v251, v251, v51, v52
v_max3_f32 v249, v249, v21, v22
v_max3_f32 v251, v251, v53, v54
v_max3_f32 v249, v249, v23, v55
v_max_f32 v249, v249, v251
v_mov_b32 v251, v249
s_nop 1
v_permlane16_swap_b32 v249, v251
v_max_f32 v249, v249, v251
v_mov_b32 v251, v249
s_nop 1
v_permlane32_swap_b32 v249, v251
v_max_f32 v249, v249, v251
v_log_f32 v250, v246
s_nop 0
v_max3_f32 v249, 0, v249, v250
v_sub_f32 v250, 0, v249
EX v250,v250
v_sub_f32 v136, v136, v249
v_sub_f32 v137, v137, v249
v_sub_f32 v138, v138, v249
v_sub_f32 v139, v139, v249
v_sub_f32 v80, v80, v249
v_sub_f32 v16, v16, v249
v_sub_f32 v81, v81, v249
v_sub_f32 v17, v17, v249
v_sub_f32 v82, v82, v249
v_sub_f32 v18, v18, v249
v_sub_f32 v83, v83, v249
v_sub_f32 v19, v19, v249
v_sub_f32 v84, v84, v249
v_sub_f32 v20, v20, v249
v_sub_f32 v85, v85, v249
v_sub_f32 v21, v21, v249
v_sub_f32 v86, v86, v249
v_sub_f32 v22, v22, v249
v_sub_f32 v87, v87, v249
v_sub_f32 v23, v23, v249
v_sub_f32 v112, v112, v249
v_sub_f32 v48, v48, v249
v_sub_f32 v113, v113, v249
v_sub_f32 v49, v49, v249
v_sub_f32 v114, v114, v249
v_sub_f32 v50, v50, v249
v_sub_f32 v115, v115, v249
)ASM"
R"ASM(
v_sub_f32 v51, v51, v249
v_sub_f32 v116, v116, v249
v_sub_f32 v52, v52, v249
v_sub_f32 v117, v117, v249
v_sub_f32 v53, v53, v249
v_sub_f32 v118, v118, v249
v_sub_f32 v54, v54, v249
v_sub_f32 v119, v119, v249
v_sub_f32 v55, v55, v249
v_mul_f32 v152, v246, v250
v_mov_b32 v153, 0
v_bfe_u32 v249, v241, 16, 8
v_lshl_or_b32 v249, v249, 1, 1
v_lshlrev_b32 v249, 22, v249
v_and_b32 v241, 0xff00ffff, v241
AR v254,a16
v_sub_f32 v254, v254, v249
v_mul_f32 v254, v254, v250
AW a16,v254
AR v251,a17
v_sub_f32 v251, v251, v249
v_mul_f32 v251, v251, v250
AW a17,v251
AR v254,a18
v_sub_f32 v254, v254, v249
v_mul_f32 v254, v254, v250
AW a18,v254
AR v251,a19
v_sub_f32 v251, v251, v249
v_mul_f32 v251, v251, v250
AW a19,v251
AR v254,a20
v_sub_f32 v254, v254, v249
v_mul_f32 v254, v254, v250
AW a20,v254
AR v251,a21
v_sub_f32 v251, v251, v249
v_mul_f32 v251, v251, v250
AW a21,v251
AR v254,a22
v_sub_f32 v254, v254, v249
v_mul_f32 v254, v254, v250
AW a22,v254
AR v251,a23
v_sub_f32 v251, v251, v249
v_mul_f32 v251, v251, v250
AW a23,v251
AR v254,a48
v_sub_f32 v254, v254, v249
v_mul_f32 v254, v254, v250
AW a48,v254
AR v251,a49
v_sub_f32 v251, v251, v249
v_mul_f32 v251, v251, v250
AW a49,v251
AR v254,a50
v_sub_f32 v254, v254, v249
v_mul_f32 v254, v254, v250
AW a50,v254
AR v251,a51
v_sub_f32 v251, v251, v249
v_mul_f32 v251, v251, v250
AW a51,v251
AR v254,a52
v_sub_f32 v254, v254, v249
v_mul_f32 v254, v254, v250
AW a52,v254
AR v251,a53
v_sub_f32 v251, v251, v249
v_mul_f32 v251, v251, v250
AW a53,v251
AR v254,a54
v_sub_f32 v254, v254, v249
v_mul_f32 v254, v254, v250
AW a54,v254
AR v251,a55
v_sub_f32 v251, v251, v249
v_mul_f32 v251, v251, v250
AW a55,v251
AR v254,a80
v_sub_f32 v254, v254, v249
v_mul_f32 v254, v254, v250
AW a80,v254
AR v251,a81
v_sub_f32 v251, v251, v249
v_mul_f32 v251, v251, v250
AW a81,v251
AR v254,a82
v_sub_f32 v254, v254, v249
v_mul_f32 v254, v254, v250
AW a82,v254
AR v251,a83
v_sub_f32 v251, v251, v249
v_mul_f32 v251, v251, v250
AW a83,v251
AR v254,a84
v_sub_f32 v254, v254, v249
v_mul_f32 v254, v254, v250
AW a84,v254
AR v251,a85
v_sub_f32 v251, v251, v249
v_mul_f32 v251, v251, v250
AW a85,v251
AR v254,a86
v_sub_f32 v254, v254, v249
v_mul_f32 v254, v254, v250
AW a86,v254
AR v251,a87
v_sub_f32 v251, v251, v249
v_mul_f32 v251, v251, v250
AW a87,v251
AR v254,a112
v_sub_f32 v254, v254, v249
v_mul_f32 v254, v254, v250
AW a112,v254
AR v251,a113
v_sub_f32 v251, v251, v249
v_mul_f32 v251, v251, v250
AW a113,v251
AR v254,a114
v_sub_f32 v254, v254, v249
v_mul_f32 v254, v254, v250
AW a114,v254
AR v251,a115
v_sub_f32 v251, v251, v249
v_mul_f32 v251, v251, v250
AW a115,v251
AR v254,a116
v_sub_f32 v254, v254, v249
v_mul_f32 v254, v254, v250
AW a116,v254
AR v251,a117
v_sub_f32 v251, v251, v249
v_mul_f32 v251, v251, v250
AW a117,v251
AR v254,a118
v_sub_f32 v254, v254, v249
v_mul_f32 v254, v254, v250
AW a118,v254
AR v251,a119
v_sub_f32 v251, v251, v249
v_mul_f32 v251, v251, v250
AW a119,v251
v_max3_f32 v249, v24, v25, v26
v_max3_f32 v251, v56, v57, v58
v_max3_f32 v249, v249, v27, v28
v_max3_f32 v251, v251, v59, v60
v_max3_f32 v249, v249, v29, v30
v_max3_f32 v251, v251, v61, v62
v_max3_f32 v249, v249, v31, v63
v_max_f32 v249, v249, v251
v_mov_b32 v251, v249
s_nop 1
v_permlane16_swap_b32 v249, v251
v_max_f32 v249, v249, v251
v_mov_b32 v251, v249
s_nop 1
v_permlane32_swap_b32 v249, v251
v_max_f32 v249, v249, v251
v_log_f32 v250, v247
s_nop 0
v_max3_f32 v249, 0, v249, v250
v_sub_f32 v250, 0, v249
EX v250,v250
v_sub_f32 v140, v140, v249
v_sub_f32 v141, v141, v249
v_sub_f32 v142, v142, v249
v_sub_f32 v143, v143, v249
v_sub_f32 v88, v88, v249
v_sub_f32 v24, v24, v249
v_sub_f32 v89, v89, v249
v_sub_f32 v25, v25, v249
v_sub_f32 v90, v90, v249
v_sub_f32 v26, v26, v249
v_sub_f32 v91, v91, v249
v_sub_f32 v27, v27, v249
v_sub_f32 v92, v92, v249
v_sub_f32 v28, v28, v249
v_sub_f32 v93, v93, v249
v_sub_f32 v29, v29, v249
v_sub_f32 v94, v94, v249
v_sub_f32 v30, v30, v249
v_sub_f32 v95, v95, v249
v_sub_f32 v31, v31, v249
v_sub_f32 v120, v120, v249
v_sub_f32 v56, v56, v249
v_sub_f32 v121, v121, v249
v_sub_f32 v57, v57, v249
v_sub_f32 v122, v122, v249
v_sub_f32 v58, v58, v249
v_sub_f32 v123, v123, v249
v_sub_f32 v59, v59, v249
v_sub_f32 v124, v124, v249
v_sub_f32 v60, v60, v249
v_sub_f32 v125, v125, v249
v_sub_f32 v61, v61, v249
v_sub_f32 v126, v126, v249
v_sub_f32 v62, v62, v249
v_sub_f32 v127, v127, v249
v_sub_f32 v63, v63, v249
v_mul_f32 v156, v247, v250
v_mov_b32 v157, 0
v_bfe_u32 v249, v241, 24, 8
v_lshl_or_b32 v249, v249, 1, 1
v_lshlrev_b32 v249, 22, v249
v_and_b32 v241, 0xffffff, v241
AR v254,a24
v_sub_f32 v254, v254, v249
v_mul_f32 v254, v254, v250
AW a24,v254
AR v251,a25
v_sub_f32 v251, v251, v249
v_mul_f32 v251, v251, v250
AW a25,v251
AR v254,a26
v_sub_f32 v254, v254, v249
v_mul_f32 v254, v254, v250
AW a26,v254
AR v251,a27
v_sub_f32 v251, v251, v249
v_mul_f32 v251, v251, v250
AW a27,v251
AR v254,a28
v_sub_f32 v254, v254, v249
v_mul_f32 v254, v254, v250
AW a28,v254
AR v251,a29
v_sub_f32 v251, v251, v249
v_mul_f32 v251, v251, v250
AW a29,v251
AR v254,a30
v_sub_f32 v254, v254, v249
v_mul_f32 v254, v254, v250
AW a30,v254
AR v251,a31
v_sub_f32 v251, v251, v249
v_mul_f32 v251, v251, v250
AW a31,v251
AR v254,a56
v_sub_f32 v254, v254, v249
v_mul_f32 v254, v254, v250
AW a56,v254
AR v251,a57
v_sub_f32 v251, v251, v249
v_mul_f32 v251, v251, v250
AW a57,v251
AR v254,a58
v_sub_f32 v254, v254, v249
v_mul_f32 v254, v254, v250
AW a58,v254
AR v251,a59
v_sub_f32 v251, v251, v249
v_mul_f32 v251, v251, v250
AW a59,v251
AR v254,a60
v_sub_f32 v254, v254, v249
v_mul_f32 v254, v254, v250
AW a60,v254
AR v251,a61
v_sub_f32 v251, v251, v249
v_mul_f32 v251, v251, v250
AW a61,v251
AR v254,a62
v_sub_f32 v254, v254, v249
v_mul_f32 v254, v254, v250
AW a62,v254
AR v251,a63
v_sub_f32 v251, v251, v249
v_mul_f32 v251, v251, v250
AW a63,v251
AR v254,a88
v_sub_f32 v254, v254, v249
v_mul_f32 v254, v254, v250
AW a88,v254
AR v251,a89
v_sub_f32 v251, v251, v249
v_mul_f32 v251, v251, v250
AW a89,v251
AR v254,a90
v_sub_f32 v254, v254, v249
v_mul_f32 v254, v254, v250
AW a90,v254
AR v251,a91
v_sub_f32 v251, v251, v249
v_mul_f32 v251, v251, v250
AW a91,v251
AR v254,a92
v_sub_f32 v254, v254, v249
v_mul_f32 v254, v254, v250
AW a92,v254
AR v251,a93
v_sub_f32 v251, v251, v249
v_mul_f32 v251, v251, v250
AW a93,v251
AR v254,a94
v_sub_f32 v254, v254, v249
v_mul_f32 v254, v254, v250
AW a94,v254
AR v251,a95
v_sub_f32 v251, v251, v249
v_mul_f32 v251, v251, v250
AW a95,v251
AR v254,a120
v_sub_f32 v254, v254, v249
v_mul_f32 v254, v254, v250
AW a120,v254
AR v251,a121
v_sub_f32 v251, v251, v249
v_mul_f32 v251, v251, v250
AW a121,v251
AR v254,a122
v_sub_f32 v254, v254, v249
v_mul_f32 v254, v254, v250
AW a122,v254
AR v251,a123
v_sub_f32 v251, v251, v249
v_mul_f32 v251, v251, v250
AW a123,v251
AR v254,a124
v_sub_f32 v254, v254, v249
v_mul_f32 v254, v254, v250
AW a124,v254
AR v251,a125
v_sub_f32 v251, v251, v249
v_mul_f32 v251, v251, v250
AW a125,v251
AR v254,a126
v_sub_f32 v254, v254, v249
v_mul_f32 v254, v254, v250
AW a126,v254
AR v251,a127
v_sub_f32 v251, v251, v249
v_mul_f32 v251, v251, v250
AW a127,v251
EX v0,v0
EX v1,v1
EX v2,v2
EX v3,v3
EX v4,v4
EX v5,v5
EX v6,v6
EX v7,v7
EX v8,v8
EX v9,v9
EX v10,v10
EX v11,v11
EX v12,v12
EX v13,v13
EX v14,v14
EX v15,v15
EX v16,v16
EX v17,v17
EX v18,v18
EX v19,v19
EX v20,v20
EX v21,v21
EX v22,v22
EX v23,v23
EX v24,v24
EX v25,v25
EX v26,v26
EX v27,v27
EX v28,v28
EX v29,v29
EX v30,v30
EX v31,v31
EX v32,v32
EX v33,v33
EX v34,v34
EX v35,v35
EX v36,v36
EX v37,v37
EX v38,v38
EX v39,v39
EX v40,v40
EX v41,v41
EX v42,v42
EX v43,v43
EX v44,v44
EX v45,v45
EX v46,v46
EX v47,v47
EX v48,v48
EX v49,v49
EX v50,v50
EX v51,v51
EX v52,v52
EX v53,v53
EX v54,v54
EX v55,v55
EX v56,v56
EX v57,v57
EX v58,v58
EX v59,v59
EX v60,v60
EX v61,v61
EX v62,v62
EX v63,v63
CV v0,v0,v1
CV v1,v2,v3
CV v2,v4,v5
CV v3,v6,v7
CV v8,v8,v9
CV v9,v10,v11
CV v10,v12,v13
CV v11,v14,v15
CV v16,v16,v17
CV v17,v18,v19
CV v18,v20,v21
CV v19,v22,v23
CV v24,v24,v25
CV v25,v26,v27
CV v26,v28,v29
CV v27,v30,v31
CV v4,v32,v33
CV v5,v34,v35
CV v6,v36,v37
CV v7,v38,v39
CV v12,v40,v41
CV v13,v42,v43
CV v14,v44,v45
CV v15,v46,v47
CV v20,v48,v49
CV v21,v50,v51
CV v22,v52,v53
CV v23,v54,v55
CV v28,v56,v57
CV v29,v58,v59
CV v30,v60,v61
CV v31,v62,v63
v_smfmac_f32_16x16x64_bf16 v[144:147], v[236:239], v[0:7], v234
v_smfmac_f32_16x16x64_bf16 v[148:151], v[236:239], v[8:15], v234
v_smfmac_f32_16x16x64_bf16 v[152:155], v[236:239], v[16:23], v234
v_smfmac_f32_16x16x64_bf16 v[156:159], v[236:239], v[24:31], v234
s_nop 9
s_branch LRET_%=
LRARE022_%=:
s_nop 15
s_waitcnt vmcnt(0) lgkmcnt(0)
v_add_u32 v249, s74, v224
RD a[192:195],v249 offset:0
RD a[208:211],v249 offset:4096
RD a[224:227],v249 offset:8192
RD a[240:243],v249 offset:12288
v_add_u32 v250, s74, v225
RD a[196:199],v250 offset:0
RD a[212:215],v250 offset:4096
RD a[228:231],v250 offset:8192
RD a[244:247],v250 offset:12288
v_add_u32 v251, s74, v226
RD a[200:203],v251 offset:0
RD a[216:219],v251 offset:4096
RD a[232:235],v251 offset:8192
RD a[248:251],v251 offset:12288
v_add_u32 v254, s74, v227
RD a[204:207],v254 offset:0
RD a[220:223],v254 offset:4096
RD a[236:239],v254 offset:8192
RD a[252:255],v254 offset:12288
s_waitcnt lgkmcnt(0)
MM v[16:19],a[192:195],v[168:171],v[136:139]
MM v[16:19],a[196:199],v[184:187],v[16:19]
MM v[16:19],a[200:203],v[200:203],v[16:19]
MM v[16:19],a[204:207],v[216:219],v[16:19]
MM v[20:23],a[220:223],v[216:219],v[136:139]
MM v[20:23],a[216:219],v[200:203],v[20:23]
MM v[20:23],a[212:215],v[184:187],v[20:23]
MM v[20:23],a[208:211],v[168:171],v[20:23]
MM v[28:31],a[208:211],v[172:175],v[140:143]
MM v[28:31],a[212:215],v[188:191],v[28:31]
MM v[28:31],a[216:219],v[204:207],v[28:31]
MM v[28:31],a[220:223],v[220:223],v[28:31]
MM v[24:27],a[204:207],v[220:223],v[140:143]
MM v[24:27],a[200:203],v[204:207],v[24:27]
MM v[24:27],a[196:199],v[188:191],v[24:27]
MM v[24:27],a[192:195],v[172:175],v[24:27]
MM v[48:51],a[224:227],v[168:171],v[136:139]
MM v[48:51],a[228:231],v[184:187],v[48:51]
MM v[48:51],a[232:235],v[200:203],v[48:51]
MM v[48:51],a[236:239],v[216:219],v[48:51]
MM v[52:55],a[252:255],v[216:219],v[136:139]
MM v[52:55],a[248:251],v[200:203],v[52:55]
MM v[52:55],a[244:247],v[184:187],v[52:55]
MM v[52:55],a[240:243],v[168:171],v[52:55]
MM v[60:63],a[240:243],v[172:175],v[140:143]
MM v[60:63],a[244:247],v[188:191],v[60:63]
MM v[60:63],a[248:251],v[204:207],v[60:63]
MM v[60:63],a[252:255],v[220:223],v[60:63]
MM v[56:59],a[236:239],v[220:223],v[140:143]
MM v[56:59],a[232:235],v[204:207],v[56:59]
MM v[56:59],a[228:231],v[188:191],v[56:59]
MM v[56:59],a[224:227],v[172:175],v[56:59]
s_nop 15
v_add_u32 v249, s75, v224
RD a[192:195],v249 offset:0
RD a[208:211],v249 offset:4096
RD a[224:227],v249 offset:8192
RD a[240:243],v249 offset:12288
v_add_u32 v250, s75, v225
RD a[196:199],v250 offset:0
RD a[212:215],v250 offset:4096
RD a[228:231],v250 offset:8192
RD a[244:247],v250 offset:12288
v_add_u32 v251, s75, v226
RD a[200:203],v251 offset:0
RD a[216:219],v251 offset:4096
RD a[232:235],v251 offset:8192
RD a[248:251],v251 offset:12288
v_add_u32 v254, s75, v227
RD a[204:207],v254 offset:0
RD a[220:223],v254 offset:4096
RD a[236:239],v254 offset:8192
RD a[252:255],v254 offset:12288
s_waitcnt lgkmcnt(0)
v_max3_f32 v249, v16, v17, v18
v_max3_f32 v251, v48, v49, v50
v_max3_f32 v249, v249, v19, v20
v_max3_f32 v251, v251, v51, v52
v_max3_f32 v249, v249, v21, v22
v_max3_f32 v251, v251, v53, v54
v_max3_f32 v249, v249, v23, v55
v_max_f32 v249, v249, v251
v_mov_b32 v251, v249
s_nop 1
v_permlane16_swap_b32 v249, v251
v_max_f32 v249, v249, v251
v_mov_b32 v251, v249
s_nop 1
v_permlane32_swap_b32 v249, v251
v_max_f32 v249, v249, v251
v_log_f32 v250, v246
s_nop 0
v_max3_f32 v249, 0, v249, v250
v_sub_f32 v250, 0, v249
EX v250,v250
v_sub_f32 v136, v136, v249
v_sub_f32 v137, v137, v249
v_sub_f32 v138, v138, v249
v_sub_f32 v139, v139, v249
v_sub_f32 v80, v80, v249
v_sub_f32 v16, v16, v249
v_sub_f32 v81, v81, v249
v_sub_f32 v17, v17, v249
v_sub_f32 v82, v82, v249
v_sub_f32 v18, v18, v249
v_sub_f32 v83, v83, v249
v_sub_f32 v19, v19, v249
v_sub_f32 v84, v84, v249
)ASM"
R"ASM(
v_sub_f32 v20, v20, v249
v_sub_f32 v85, v85, v249
v_sub_f32 v21, v21, v249
v_sub_f32 v86, v86, v249
v_sub_f32 v22, v22, v249
v_sub_f32 v87, v87, v249
v_sub_f32 v23, v23, v249
v_sub_f32 v112, v112, v249
v_sub_f32 v48, v48, v249
v_sub_f32 v113, v113, v249
v_sub_f32 v49, v49, v249
v_sub_f32 v114, v114, v249
v_sub_f32 v50, v50, v249
v_sub_f32 v115, v115, v249
v_sub_f32 v51, v51, v249
v_sub_f32 v116, v116, v249
v_sub_f32 v52, v52, v249
v_sub_f32 v117, v117, v249
v_sub_f32 v53, v53, v249
v_sub_f32 v118, v118, v249
v_sub_f32 v54, v54, v249
v_sub_f32 v119, v119, v249
v_sub_f32 v55, v55, v249
v_mul_f32 v152, v246, v250
v_mov_b32 v153, 0
v_bfe_u32 v249, v241, 16, 8
v_lshl_or_b32 v249, v249, 1, 1
v_lshlrev_b32 v249, 22, v249
v_and_b32 v241, 0xff00ffff, v241
AR v254,a16
v_sub_f32 v254, v254, v249
v_mul_f32 v254, v254, v250
AW a16,v254
AR v251,a17
v_sub_f32 v251, v251, v249
v_mul_f32 v251, v251, v250
AW a17,v251
AR v254,a18
v_sub_f32 v254, v254, v249
v_mul_f32 v254, v254, v250
AW a18,v254
AR v251,a19
v_sub_f32 v251, v251, v249
v_mul_f32 v251, v251, v250
AW a19,v251
AR v254,a20
v_sub_f32 v254, v254, v249
v_mul_f32 v254, v254, v250
AW a20,v254
AR v251,a21
v_sub_f32 v251, v251, v249
v_mul_f32 v251, v251, v250
AW a21,v251
AR v254,a22
v_sub_f32 v254, v254, v249
v_mul_f32 v254, v254, v250
AW a22,v254
AR v251,a23
v_sub_f32 v251, v251, v249
v_mul_f32 v251, v251, v250
AW a23,v251
AR v254,a48
v_sub_f32 v254, v254, v249
v_mul_f32 v254, v254, v250
AW a48,v254
AR v251,a49
v_sub_f32 v251, v251, v249
v_mul_f32 v251, v251, v250
AW a49,v251
AR v254,a50
v_sub_f32 v254, v254, v249
v_mul_f32 v254, v254, v250
AW a50,v254
AR v251,a51
v_sub_f32 v251, v251, v249
v_mul_f32 v251, v251, v250
AW a51,v251
AR v254,a52
v_sub_f32 v254, v254, v249
v_mul_f32 v254, v254, v250
AW a52,v254
AR v251,a53
v_sub_f32 v251, v251, v249
v_mul_f32 v251, v251, v250
AW a53,v251
AR v254,a54
v_sub_f32 v254, v254, v249
v_mul_f32 v254, v254, v250
AW a54,v254
AR v251,a55
v_sub_f32 v251, v251, v249
v_mul_f32 v251, v251, v250
AW a55,v251
AR v254,a80
v_sub_f32 v254, v254, v249
v_mul_f32 v254, v254, v250
AW a80,v254
AR v251,a81
v_sub_f32 v251, v251, v249
v_mul_f32 v251, v251, v250
AW a81,v251
AR v254,a82
v_sub_f32 v254, v254, v249
v_mul_f32 v254, v254, v250
AW a82,v254
AR v251,a83
v_sub_f32 v251, v251, v249
v_mul_f32 v251, v251, v250
AW a83,v251
AR v254,a84
v_sub_f32 v254, v254, v249
v_mul_f32 v254, v254, v250
AW a84,v254
AR v251,a85
v_sub_f32 v251, v251, v249
v_mul_f32 v251, v251, v250
AW a85,v251
AR v254,a86
v_sub_f32 v254, v254, v249
v_mul_f32 v254, v254, v250
AW a86,v254
AR v251,a87
v_sub_f32 v251, v251, v249
v_mul_f32 v251, v251, v250
AW a87,v251
AR v254,a112
v_sub_f32 v254, v254, v249
v_mul_f32 v254, v254, v250
AW a112,v254
AR v251,a113
v_sub_f32 v251, v251, v249
v_mul_f32 v251, v251, v250
AW a113,v251
AR v254,a114
v_sub_f32 v254, v254, v249
v_mul_f32 v254, v254, v250
AW a114,v254
AR v251,a115
v_sub_f32 v251, v251, v249
v_mul_f32 v251, v251, v250
AW a115,v251
AR v254,a116
v_sub_f32 v254, v254, v249
v_mul_f32 v254, v254, v250
AW a116,v254
AR v251,a117
v_sub_f32 v251, v251, v249
v_mul_f32 v251, v251, v250
AW a117,v251
AR v254,a118
v_sub_f32 v254, v254, v249
v_mul_f32 v254, v254, v250
AW a118,v254
AR v251,a119
v_sub_f32 v251, v251, v249
v_mul_f32 v251, v251, v250
AW a119,v251
v_max3_f32 v249, v24, v25, v26
v_max3_f32 v251, v56, v57, v58
v_max3_f32 v249, v249, v27, v28
v_max3_f32 v251, v251, v59, v60
v_max3_f32 v249, v249, v29, v30
v_max3_f32 v251, v251, v61, v62
v_max3_f32 v249, v249, v31, v63
v_max_f32 v249, v249, v251
v_mov_b32 v251, v249
s_nop 1
v_permlane16_swap_b32 v249, v251
v_max_f32 v249, v249, v251
v_mov_b32 v251, v249
s_nop 1
v_permlane32_swap_b32 v249, v251
v_max_f32 v249, v249, v251
v_log_f32 v250, v247
s_nop 0
v_max3_f32 v249, 0, v249, v250
v_sub_f32 v250, 0, v249
EX v250,v250
v_sub_f32 v140, v140, v249
v_sub_f32 v141, v141, v249
v_sub_f32 v142, v142, v249
v_sub_f32 v143, v143, v249
v_sub_f32 v88, v88, v249
v_sub_f32 v24, v24, v249
v_sub_f32 v89, v89, v249
v_sub_f32 v25, v25, v249
v_sub_f32 v90, v90, v249
v_sub_f32 v26, v26, v249
v_sub_f32 v91, v91, v249
v_sub_f32 v27, v27, v249
v_sub_f32 v92, v92, v249
v_sub_f32 v28, v28, v249
v_sub_f32 v93, v93, v249
v_sub_f32 v29, v29, v249
v_sub_f32 v94, v94, v249
v_sub_f32 v30, v30, v249
v_sub_f32 v95, v95, v249
v_sub_f32 v31, v31, v249
v_sub_f32 v120, v120, v249
v_sub_f32 v56, v56, v249
v_sub_f32 v121, v121, v249
v_sub_f32 v57, v57, v249
v_sub_f32 v122, v122, v249
v_sub_f32 v58, v58, v249
v_sub_f32 v123, v123, v249
v_sub_f32 v59, v59, v249
v_sub_f32 v124, v124, v249
v_sub_f32 v60, v60, v249
v_sub_f32 v125, v125, v249
v_sub_f32 v61, v61, v249
v_sub_f32 v126, v126, v249
v_sub_f32 v62, v62, v249
v_sub_f32 v127, v127, v249
v_sub_f32 v63, v63, v249
v_mul_f32 v156, v247, v250
v_mov_b32 v157, 0
v_bfe_u32 v249, v241, 24, 8
v_lshl_or_b32 v249, v249, 1, 1
v_lshlrev_b32 v249, 22, v249
v_and_b32 v241, 0xffffff, v241
AR v254,a24
v_sub_f32 v254, v254, v249
v_mul_f32 v254, v254, v250
AW a24,v254
AR v251,a25
v_sub_f32 v251, v251, v249
v_mul_f32 v251, v251, v250
AW a25,v251
AR v254,a26
v_sub_f32 v254, v254, v249
v_mul_f32 v254, v254, v250
AW a26,v254
AR v251,a27
v_sub_f32 v251, v251, v249
v_mul_f32 v251, v251, v250
AW a27,v251
AR v254,a28
v_sub_f32 v254, v254, v249
v_mul_f32 v254, v254, v250
AW a28,v254
AR v251,a29
v_sub_f32 v251, v251, v249
v_mul_f32 v251, v251, v250
AW a29,v251
AR v254,a30
v_sub_f32 v254, v254, v249
v_mul_f32 v254, v254, v250
AW a30,v254
AR v251,a31
v_sub_f32 v251, v251, v249
v_mul_f32 v251, v251, v250
AW a31,v251
AR v254,a56
v_sub_f32 v254, v254, v249
v_mul_f32 v254, v254, v250
AW a56,v254
AR v251,a57
v_sub_f32 v251, v251, v249
v_mul_f32 v251, v251, v250
AW a57,v251
AR v254,a58
v_sub_f32 v254, v254, v249
v_mul_f32 v254, v254, v250
AW a58,v254
AR v251,a59
v_sub_f32 v251, v251, v249
v_mul_f32 v251, v251, v250
AW a59,v251
AR v254,a60
v_sub_f32 v254, v254, v249
v_mul_f32 v254, v254, v250
AW a60,v254
AR v251,a61
v_sub_f32 v251, v251, v249
v_mul_f32 v251, v251, v250
AW a61,v251
AR v254,a62
v_sub_f32 v254, v254, v249
v_mul_f32 v254, v254, v250
AW a62,v254
AR v251,a63
v_sub_f32 v251, v251, v249
v_mul_f32 v251, v251, v250
AW a63,v251
AR v254,a88
v_sub_f32 v254, v254, v249
v_mul_f32 v254, v254, v250
AW a88,v254
AR v251,a89
v_sub_f32 v251, v251, v249
v_mul_f32 v251, v251, v250
AW a89,v251
AR v254,a90
v_sub_f32 v254, v254, v249
v_mul_f32 v254, v254, v250
AW a90,v254
AR v251,a91
v_sub_f32 v251, v251, v249
v_mul_f32 v251, v251, v250
AW a91,v251
AR v254,a92
v_sub_f32 v254, v254, v249
v_mul_f32 v254, v254, v250
AW a92,v254
AR v251,a93
v_sub_f32 v251, v251, v249
v_mul_f32 v251, v251, v250
AW a93,v251
AR v254,a94
v_sub_f32 v254, v254, v249
v_mul_f32 v254, v254, v250
AW a94,v254
AR v251,a95
v_sub_f32 v251, v251, v249
v_mul_f32 v251, v251, v250
AW a95,v251
AR v254,a120
v_sub_f32 v254, v254, v249
v_mul_f32 v254, v254, v250
AW a120,v254
AR v251,a121
v_sub_f32 v251, v251, v249
v_mul_f32 v251, v251, v250
AW a121,v251
AR v254,a122
v_sub_f32 v254, v254, v249
v_mul_f32 v254, v254, v250
AW a122,v254
AR v251,a123
v_sub_f32 v251, v251, v249
v_mul_f32 v251, v251, v250
AW a123,v251
AR v254,a124
v_sub_f32 v254, v254, v249
v_mul_f32 v254, v254, v250
AW a124,v254
AR v251,a125
v_sub_f32 v251, v251, v249
v_mul_f32 v251, v251, v250
AW a125,v251
AR v254,a126
v_sub_f32 v254, v254, v249
v_mul_f32 v254, v254, v250
AW a126,v254
AR v251,a127
v_sub_f32 v251, v251, v249
v_mul_f32 v251, v251, v250
AW a127,v251
EX v16,v16
EX v17,v17
EX v18,v18
EX v19,v19
EX v20,v20
EX v21,v21
EX v22,v22
EX v23,v23
EX v24,v24
EX v25,v25
EX v26,v26
EX v27,v27
EX v28,v28
EX v29,v29
EX v30,v30
EX v31,v31
EX v48,v48
EX v49,v49
EX v50,v50
EX v51,v51
EX v52,v52
EX v53,v53
EX v54,v54
EX v55,v55
EX v56,v56
EX v57,v57
EX v58,v58
EX v59,v59
EX v60,v60
EX v61,v61
EX v62,v62
EX v63,v63
CV v16,v16,v17
CV v17,v18,v19
CV v18,v20,v21
CV v19,v22,v23
CV v24,v24,v25
CV v25,v26,v27
CV v26,v28,v29
CV v27,v30,v31
CV v20,v48,v49
CV v21,v50,v51
CV v22,v52,v53
CV v23,v54,v55
CV v28,v56,v57
CV v29,v58,v59
CV v30,v60,v61
CV v31,v62,v63
v_smfmac_f32_16x16x64_bf16 v[152:155], v[236:239], v[16:23], v234
s_nop 0
v_smfmac_f32_16x16x64_bf16 v[156:159], v[236:239], v[24:31], v234
s_nop 9
s_branch LRET_%=
LRARE122_%=:
s_nop 15
s_waitcnt vmcnt(0) lgkmcnt(0)
v_add_u32 v249, s74, v224
RD a[192:195],v249 offset:0
RD a[208:211],v249 offset:4096
RD a[224:227],v249 offset:8192
RD a[240:243],v249 offset:12288
v_add_u32 v250, s74, v225
RD a[196:199],v250 offset:0
RD a[212:215],v250 offset:4096
RD a[228:231],v250 offset:8192
RD a[244:247],v250 offset:12288
v_add_u32 v251, s74, v226
RD a[200:203],v251 offset:0
RD a[216:219],v251 offset:4096
RD a[232:235],v251 offset:8192
RD a[248:251],v251 offset:12288
v_add_u32 v254, s74, v227
RD a[204:207],v254 offset:0
RD a[220:223],v254 offset:4096
RD a[236:239],v254 offset:8192
RD a[252:255],v254 offset:12288
s_waitcnt lgkmcnt(0)
MM v[80:83],a[192:195],v[168:171],v[136:139]
MM v[80:83],a[196:199],v[184:187],v[80:83]
MM v[80:83],a[200:203],v[200:203],v[80:83]
MM v[80:83],a[204:207],v[216:219],v[80:83]
MM v[84:87],a[220:223],v[216:219],v[136:139]
MM v[84:87],a[216:219],v[200:203],v[84:87]
MM v[84:87],a[212:215],v[184:187],v[84:87]
MM v[84:87],a[208:211],v[168:171],v[84:87]
MM v[92:95],a[208:211],v[172:175],v[140:143]
MM v[92:95],a[212:215],v[188:191],v[92:95]
MM v[92:95],a[216:219],v[204:207],v[92:95]
MM v[92:95],a[220:223],v[220:223],v[92:95]
MM v[88:91],a[204:207],v[220:223],v[140:143]
MM v[88:91],a[200:203],v[204:207],v[88:91]
MM v[88:91],a[196:199],v[188:191],v[88:91]
MM v[88:91],a[192:195],v[172:175],v[88:91]
MM v[112:115],a[224:227],v[168:171],v[136:139]
MM v[112:115],a[228:231],v[184:187],v[112:115]
MM v[112:115],a[232:235],v[200:203],v[112:115]
MM v[112:115],a[236:239],v[216:219],v[112:115]
MM v[116:119],a[252:255],v[216:219],v[136:139]
MM v[116:119],a[248:251],v[200:203],v[116:119]
MM v[116:119],a[244:247],v[184:187],v[116:119]
MM v[116:119],a[240:243],v[168:171],v[116:119]
MM v[124:127],a[240:243],v[172:175],v[140:143]
MM v[124:127],a[244:247],v[188:191],v[124:127]
MM v[124:127],a[248:251],v[204:207],v[124:127]
MM v[124:127],a[252:255],v[220:223],v[124:127]
MM v[120:123],a[236:239],v[220:223],v[140:143]
MM v[120:123],a[232:235],v[204:207],v[120:123]
MM v[120:123],a[228:231],v[188:191],v[120:123]
MM v[120:123],a[224:227],v[172:175],v[120:123]
s_nop 15
v_add_u32 v249, s75, v224
RD a[192:195],v249 offset:0
RD a[208:211],v249 offset:4096
RD a[224:227],v249 offset:8192
RD a[240:243],v249 offset:12288
v_add_u32 v250, s75, v225
RD a[196:199],v250 offset:0
RD a[212:215],v250 offset:4096
RD a[228:231],v250 offset:8192
RD a[244:247],v250 offset:12288
v_add_u32 v251, s75, v226
RD a[200:203],v251 offset:0
RD a[216:219],v251 offset:4096
RD a[232:235],v251 offset:8192
RD a[248:251],v251 offset:12288
v_add_u32 v254, s75, v227
RD a[204:207],v254 offset:0
RD a[220:223],v254 offset:4096
RD a[236:239],v254 offset:8192
RD a[252:255],v254 offset:12288
s_waitcnt lgkmcnt(0)
v_max3_f32 v249, v80, v81, v82
v_max3_f32 v251, v112, v113, v114
v_max3_f32 v249, v249, v83, v84
v_max3_f32 v251, v251, v115, v116
v_max3_f32 v249, v249, v85, v86
v_max3_f32 v251, v251, v117, v118
v_max3_f32 v249, v249, v87, v119
v_max_f32 v249, v249, v251
v_mov_b32 v251, v249
s_nop 1
v_permlane16_swap_b32 v249, v251
v_max_f32 v249, v249, v251
v_mov_b32 v251, v249
s_nop 1
v_permlane32_swap_b32 v249, v251
v_max_f32 v249, v249, v251
v_log_f32 v250, v246
s_nop 0
v_max3_f32 v249, 0, v249, v250
v_sub_f32 v250, 0, v249
EX v250,v250
v_sub_f32 v136, v136, v249
v_sub_f32 v137, v137, v249
v_sub_f32 v138, v138, v249
v_sub_f32 v139, v139, v249
v_sub_f32 v16, v16, v249
v_sub_f32 v80, v80, v249
v_sub_f32 v17, v17, v249
v_sub_f32 v81, v81, v249
v_sub_f32 v18, v18, v249
v_sub_f32 v82, v82, v249
v_sub_f32 v19, v19, v249
v_sub_f32 v83, v83, v249
v_sub_f32 v20, v20, v249
v_sub_f32 v84, v84, v249
v_sub_f32 v21, v21, v249
v_sub_f32 v85, v85, v249
v_sub_f32 v22, v22, v249
v_sub_f32 v86, v86, v249
v_sub_f32 v23, v23, v249
v_sub_f32 v87, v87, v249
v_sub_f32 v48, v48, v249
v_sub_f32 v112, v112, v249
v_sub_f32 v49, v49, v249
)ASM"
R"ASM(
v_sub_f32 v113, v113, v249
v_sub_f32 v50, v50, v249
v_sub_f32 v114, v114, v249
v_sub_f32 v51, v51, v249
v_sub_f32 v115, v115, v249
v_sub_f32 v52, v52, v249
v_sub_f32 v116, v116, v249
v_sub_f32 v53, v53, v249
v_sub_f32 v117, v117, v249
v_sub_f32 v54, v54, v249
v_sub_f32 v118, v118, v249
v_sub_f32 v55, v55, v249
v_sub_f32 v119, v119, v249
v_mul_f32 v152, v246, v250
v_mov_b32 v153, 0
v_bfe_u32 v249, v241, 16, 8
v_lshl_or_b32 v249, v249, 1, 1
v_lshlrev_b32 v249, 22, v249
v_and_b32 v241, 0xff00ffff, v241
AR v254,a16
v_sub_f32 v254, v254, v249
v_mul_f32 v254, v254, v250
AW a16,v254
AR v251,a17
v_sub_f32 v251, v251, v249
v_mul_f32 v251, v251, v250
AW a17,v251
AR v254,a18
v_sub_f32 v254, v254, v249
v_mul_f32 v254, v254, v250
AW a18,v254
AR v251,a19
v_sub_f32 v251, v251, v249
v_mul_f32 v251, v251, v250
AW a19,v251
AR v254,a20
v_sub_f32 v254, v254, v249
v_mul_f32 v254, v254, v250
AW a20,v254
AR v251,a21
v_sub_f32 v251, v251, v249
v_mul_f32 v251, v251, v250
AW a21,v251
AR v254,a22
v_sub_f32 v254, v254, v249
v_mul_f32 v254, v254, v250
AW a22,v254
AR v251,a23
v_sub_f32 v251, v251, v249
v_mul_f32 v251, v251, v250
AW a23,v251
AR v254,a48
v_sub_f32 v254, v254, v249
v_mul_f32 v254, v254, v250
AW a48,v254
AR v251,a49
v_sub_f32 v251, v251, v249
v_mul_f32 v251, v251, v250
AW a49,v251
AR v254,a50
v_sub_f32 v254, v254, v249
v_mul_f32 v254, v254, v250
AW a50,v254
AR v251,a51
v_sub_f32 v251, v251, v249
v_mul_f32 v251, v251, v250
AW a51,v251
AR v254,a52
v_sub_f32 v254, v254, v249
v_mul_f32 v254, v254, v250
AW a52,v254
AR v251,a53
v_sub_f32 v251, v251, v249
v_mul_f32 v251, v251, v250
AW a53,v251
AR v254,a54
v_sub_f32 v254, v254, v249
v_mul_f32 v254, v254, v250
AW a54,v254
AR v251,a55
v_sub_f32 v251, v251, v249
v_mul_f32 v251, v251, v250
AW a55,v251
AR v254,a80
v_sub_f32 v254, v254, v249
v_mul_f32 v254, v254, v250
AW a80,v254
AR v251,a81
v_sub_f32 v251, v251, v249
v_mul_f32 v251, v251, v250
AW a81,v251
AR v254,a82
v_sub_f32 v254, v254, v249
v_mul_f32 v254, v254, v250
AW a82,v254
AR v251,a83
v_sub_f32 v251, v251, v249
v_mul_f32 v251, v251, v250
AW a83,v251
AR v254,a84
v_sub_f32 v254, v254, v249
v_mul_f32 v254, v254, v250
AW a84,v254
AR v251,a85
v_sub_f32 v251, v251, v249
v_mul_f32 v251, v251, v250
AW a85,v251
AR v254,a86
v_sub_f32 v254, v254, v249
v_mul_f32 v254, v254, v250
AW a86,v254
AR v251,a87
v_sub_f32 v251, v251, v249
v_mul_f32 v251, v251, v250
AW a87,v251
AR v254,a112
v_sub_f32 v254, v254, v249
v_mul_f32 v254, v254, v250
AW a112,v254
AR v251,a113
v_sub_f32 v251, v251, v249
v_mul_f32 v251, v251, v250
AW a113,v251
AR v254,a114
v_sub_f32 v254, v254, v249
v_mul_f32 v254, v254, v250
AW a114,v254
AR v251,a115
v_sub_f32 v251, v251, v249
v_mul_f32 v251, v251, v250
AW a115,v251
AR v254,a116
v_sub_f32 v254, v254, v249
v_mul_f32 v254, v254, v250
AW a116,v254
AR v251,a117
v_sub_f32 v251, v251, v249
v_mul_f32 v251, v251, v250
AW a117,v251
AR v254,a118
v_sub_f32 v254, v254, v249
v_mul_f32 v254, v254, v250
AW a118,v254
AR v251,a119
v_sub_f32 v251, v251, v249
v_mul_f32 v251, v251, v250
AW a119,v251
v_max3_f32 v249, v88, v89, v90
v_max3_f32 v251, v120, v121, v122
v_max3_f32 v249, v249, v91, v92
v_max3_f32 v251, v251, v123, v124
v_max3_f32 v249, v249, v93, v94
v_max3_f32 v251, v251, v125, v126
v_max3_f32 v249, v249, v95, v127
v_max_f32 v249, v249, v251
v_mov_b32 v251, v249
s_nop 1
v_permlane16_swap_b32 v249, v251
v_max_f32 v249, v249, v251
v_mov_b32 v251, v249
s_nop 1
v_permlane32_swap_b32 v249, v251
v_max_f32 v249, v249, v251
v_log_f32 v250, v247
s_nop 0
v_max3_f32 v249, 0, v249, v250
v_sub_f32 v250, 0, v249
EX v250,v250
v_sub_f32 v140, v140, v249
v_sub_f32 v141, v141, v249
v_sub_f32 v142, v142, v249
v_sub_f32 v143, v143, v249
v_sub_f32 v24, v24, v249
v_sub_f32 v88, v88, v249
v_sub_f32 v25, v25, v249
v_sub_f32 v89, v89, v249
v_sub_f32 v26, v26, v249
v_sub_f32 v90, v90, v249
v_sub_f32 v27, v27, v249
v_sub_f32 v91, v91, v249
v_sub_f32 v28, v28, v249
v_sub_f32 v92, v92, v249
v_sub_f32 v29, v29, v249
v_sub_f32 v93, v93, v249
v_sub_f32 v30, v30, v249
v_sub_f32 v94, v94, v249
v_sub_f32 v31, v31, v249
v_sub_f32 v95, v95, v249
v_sub_f32 v56, v56, v249
v_sub_f32 v120, v120, v249
v_sub_f32 v57, v57, v249
v_sub_f32 v121, v121, v249
v_sub_f32 v58, v58, v249
v_sub_f32 v122, v122, v249
v_sub_f32 v59, v59, v249
v_sub_f32 v123, v123, v249
v_sub_f32 v60, v60, v249
v_sub_f32 v124, v124, v249
v_sub_f32 v61, v61, v249
v_sub_f32 v125, v125, v249
v_sub_f32 v62, v62, v249
v_sub_f32 v126, v126, v249
v_sub_f32 v63, v63, v249
v_sub_f32 v127, v127, v249
v_mul_f32 v156, v247, v250
v_mov_b32 v157, 0
v_bfe_u32 v249, v241, 24, 8
v_lshl_or_b32 v249, v249, 1, 1
v_lshlrev_b32 v249, 22, v249
v_and_b32 v241, 0xffffff, v241
AR v254,a24
v_sub_f32 v254, v254, v249
v_mul_f32 v254, v254, v250
AW a24,v254
AR v251,a25
v_sub_f32 v251, v251, v249
v_mul_f32 v251, v251, v250
AW a25,v251
AR v254,a26
v_sub_f32 v254, v254, v249
v_mul_f32 v254, v254, v250
AW a26,v254
AR v251,a27
v_sub_f32 v251, v251, v249
v_mul_f32 v251, v251, v250
AW a27,v251
AR v254,a28
v_sub_f32 v254, v254, v249
v_mul_f32 v254, v254, v250
AW a28,v254
AR v251,a29
v_sub_f32 v251, v251, v249
v_mul_f32 v251, v251, v250
AW a29,v251
AR v254,a30
v_sub_f32 v254, v254, v249
v_mul_f32 v254, v254, v250
AW a30,v254
AR v251,a31
v_sub_f32 v251, v251, v249
v_mul_f32 v251, v251, v250
AW a31,v251
AR v254,a56
v_sub_f32 v254, v254, v249
v_mul_f32 v254, v254, v250
AW a56,v254
AR v251,a57
v_sub_f32 v251, v251, v249
v_mul_f32 v251, v251, v250
AW a57,v251
AR v254,a58
v_sub_f32 v254, v254, v249
v_mul_f32 v254, v254, v250
AW a58,v254
AR v251,a59
v_sub_f32 v251, v251, v249
v_mul_f32 v251, v251, v250
AW a59,v251
AR v254,a60
v_sub_f32 v254, v254, v249
v_mul_f32 v254, v254, v250
AW a60,v254
AR v251,a61
v_sub_f32 v251, v251, v249
v_mul_f32 v251, v251, v250
AW a61,v251
AR v254,a62
v_sub_f32 v254, v254, v249
v_mul_f32 v254, v254, v250
AW a62,v254
AR v251,a63
v_sub_f32 v251, v251, v249
v_mul_f32 v251, v251, v250
AW a63,v251
AR v254,a88
v_sub_f32 v254, v254, v249
v_mul_f32 v254, v254, v250
AW a88,v254
AR v251,a89
v_sub_f32 v251, v251, v249
v_mul_f32 v251, v251, v250
AW a89,v251
AR v254,a90
v_sub_f32 v254, v254, v249
v_mul_f32 v254, v254, v250
AW a90,v254
AR v251,a91
v_sub_f32 v251, v251, v249
v_mul_f32 v251, v251, v250
AW a91,v251
AR v254,a92
v_sub_f32 v254, v254, v249
v_mul_f32 v254, v254, v250
AW a92,v254
AR v251,a93
v_sub_f32 v251, v251, v249
v_mul_f32 v251, v251, v250
AW a93,v251
AR v254,a94
v_sub_f32 v254, v254, v249
v_mul_f32 v254, v254, v250
AW a94,v254
AR v251,a95
v_sub_f32 v251, v251, v249
v_mul_f32 v251, v251, v250
AW a95,v251
AR v254,a120
v_sub_f32 v254, v254, v249
v_mul_f32 v254, v254, v250
AW a120,v254
AR v251,a121
v_sub_f32 v251, v251, v249
v_mul_f32 v251, v251, v250
AW a121,v251
AR v254,a122
v_sub_f32 v254, v254, v249
v_mul_f32 v254, v254, v250
AW a122,v254
AR v251,a123
v_sub_f32 v251, v251, v249
v_mul_f32 v251, v251, v250
AW a123,v251
AR v254,a124
v_sub_f32 v254, v254, v249
v_mul_f32 v254, v254, v250
AW a124,v254
AR v251,a125
v_sub_f32 v251, v251, v249
v_mul_f32 v251, v251, v250
AW a125,v251
AR v254,a126
v_sub_f32 v254, v254, v249
v_mul_f32 v254, v254, v250
AW a126,v254
AR v251,a127
v_sub_f32 v251, v251, v249
v_mul_f32 v251, v251, v250
AW a127,v251
EX v80,v80
EX v81,v81
EX v82,v82
EX v83,v83
EX v84,v84
EX v85,v85
EX v86,v86
EX v87,v87
EX v88,v88
EX v89,v89
EX v90,v90
EX v91,v91
EX v92,v92
EX v93,v93
EX v94,v94
EX v95,v95
EX v112,v112
EX v113,v113
EX v114,v114
EX v115,v115
EX v116,v116
EX v117,v117
EX v118,v118
EX v119,v119
EX v120,v120
EX v121,v121
EX v122,v122
EX v123,v123
EX v124,v124
EX v125,v125
EX v126,v126
EX v127,v127
CV v80,v80,v81
CV v81,v82,v83
CV v82,v84,v85
CV v83,v86,v87
CV v88,v88,v89
CV v89,v90,v91
CV v90,v92,v93
CV v91,v94,v95
CV v84,v112,v113
CV v85,v114,v115
CV v86,v116,v117
CV v87,v118,v119
CV v92,v120,v121
CV v93,v122,v123
CV v94,v124,v125
CV v95,v126,v127
v_smfmac_f32_16x16x64_bf16 v[152:155], v[236:239], v[80:87], v234
s_nop 0
v_smfmac_f32_16x16x64_bf16 v[156:159], v[236:239], v[88:95], v234
s_nop 9
s_branch LRET_%=
LEND_%=:
)ASM"

    :: [ka] "s"(__builtin_amdgcn_kernarg_segment_ptr()), [slds] "s"(slds), [swave] "s"(w), [sb] "s"(blockIdx.x)
    : "memory", "vcc", "scc", CLOB_V, CLOB_A, CLOB_S);
}
constexpr int DYN_LDS = 163840;

extern "C" void kernel_launch(void* const* d_in, const int* in_sizes, int n_in, void* d_out, int out_size, void* d_ws, size_t ws_size, hipStream_t stream) {
#if !defined(__HIP_DEVICE_COMPILE__)
  if (DYN_LDS) hipFuncSetAttribute((const void*)ka_fused, hipFuncAttributeMaxDynamicSharedMemorySize, DYN_LDS);
  ka_fused<<<256, 256, DYN_LDS, stream>>>(d_in[3], d_in[0], d_ws, d_in[1], d_in[6], d_in[2], d_in[4], d_in[7], d_in[8], d_in[9], d_out);
#endif
}
```

```cpp
#include <hip/hip_runtime.h>
#include <stdint.h>

using f32x4  = __attribute__((ext_vector_type(4))) float;
using u32x4  = __attribute__((ext_vector_type(4))) unsigned;
using u32x2  = __attribute__((ext_vector_type(2))) unsigned;

constexpr int SEQ = 2048, DM = 640, NH = 32, DH = 128, HD = NH * DH, FF = 1728, TOPC = 8;

constexpr unsigned WS_FO  = 0;
constexpr unsigned WS_EP  = 32768;
constexpr unsigned WS_G1  = 36864;
constexpr unsigned WS_G2  = 53248;
constexpr unsigned WS_FH  = 69632;
constexpr unsigned WS_FY  = 73728;
constexpr unsigned WS_FG  = 77824;
constexpr unsigned WS_FR  = 81920;
constexpr unsigned WS_RK  = 86016;
constexpr unsigned WS_XN  = 131072;
constexpr unsigned WS_OF  = WS_XN + SEQ * DM * 2;
constexpr unsigned WS_H   = WS_OF + (18u << 20);
constexpr unsigned WS_Y   = WS_H + 32768;
constexpr unsigned WS_P   = WS_Y + 1048576;
constexpr unsigned WS_SPAN = 64u << 20;

#define CLOB_V "v0","v1","v2","v3","v4","v5","v6","v7","v8","v9","v10","v11","v12","v13","v14","v15","v16","v17","v18","v19","v20","v21","v22","v23","v24","v25","v26","v27","v28","v29","v30","v31","v32","v33","v34","v35","v36","v37","v38","v39","v40","v41","v42","v43","v44","v45","v46","v47","v48","v49","v50","v51","v52","v53","v54","v55","v56","v57","v58","v59","v60","v61","v62","v63","v64","v65","v66","v67","v68","v69","v70","v71","v72","v73","v74","v75","v76","v77","v78","v79","v80","v81","v82","v83","v84","v85","v86","v87","v88","v89","v90","v91","v92","v93","v94","v95","v96","v97","v98","v99","v100","v101","v102","v103","v104","v105","v106","v107","v108","v109","v110","v111","v112","v113","v114","v115","v116","v117","v118","v119","v120","v121","v122","v123","v124","v125","v126","v127","v128","v129","v130","v131","v132","v133","v134","v135","v136","v137","v138","v139","v140","v141","v142","v143","v144","v145","v146","v147","v148","v149","v150","v151","v152","v153","v154","v155","v156","v157","v158","v159","v160","v161","v162","v163","v164","v165","v166","v167","v168","v169","v170","v171","v172","v173","v174","v175","v176","v177","v178","v179","v180","v181","v182","v183","v184","v185","v186","v187","v188","v189","v190","v191","v192","v193","v194","v195","v196","v197","v198","v199","v200","v201","v202","v203","v204","v205","v206","v207","v208","v209","v210","v211","v212","v213","v214","v215","v216","v217","v218","v219","v220","v221","v222","v223","v224","v225","v226","v227","v228","v229","v230","v231","v232","v233","v234","v235","v236","v237","v238","v239","v240","v241","v242","v243","v244","v245","v246","v247","v248","v249","v250","v251","v252","v253","v254","v255"
#define CLOB_A "a0","a1","a2","a3","a4","a5","a6","a7","a8","a9","a10","a11","a12","a13","a14","a15","a16","a17","a18","a19","a20","a21","a22","a23","a24","a25","a26","a27","a28","a29","a30","a31","a32","a33","a34","a35","a36","a37","a38","a39","a40","a41","a42","a43","a44","a45","a46","a47","a48","a49","a50","a51","a52","a53","a54","a55","a56","a57","a58","a59","a60","a61","a62","a63","a64","a65","a66","a67","a68","a69","a70","a71","a72","a73","a74","a75","a76","a77","a78","a79","a80","a81","a82","a83","a84","a85","a86","a87","a88","a89","a90","a91","a92","a93","a94","a95","a96","a97","a98","a99","a100","a101","a102","a103","a104","a105","a106","a107","a108","a109","a110","a111","a112","a113","a114","a115","a116","a117","a118","a119","a120","a121","a122","a123","a124","a125","a126","a127","a128","a129","a130","a131","a132","a133","a134","a135","a136","a137","a138","a139","a140","a141","a142","a143","a144","a145","a146","a147","a148","a149","a150","a151","a152","a153","a154","a155","a156","a157","a158","a159","a160","a161","a162","a163","a164","a165","a166","a167","a168","a169","a170","a171","a172","a173","a174","a175","a176","a177","a178","a179","a180","a181","a182","a183","a184","a185","a186","a187","a188","a189","a190","a191","a192","a193","a194","a195","a196","a197","a198","a199","a200","a201","a202","a203","a204","a205","a206","a207","a208","a209","a210","a211","a212","a213","a214","a215","a216","a217","a218","a219","a220","a221","a222","a223","a224","a225","a226","a227","a228","a229","a230","a231","a232","a233","a234","a235","a236","a237","a238","a239","a240","a241","a242","a243","a244","a245","a246","a247","a248","a249","a250","a251","a252","a253","a254","a255"
#define CLOB_S "s8","s9","s10","s11","s12","s13","s14","s15","s16","s17","s18","s19","s20","s21","s22","s23","s24","s25","s26","s27","s28","s29","s30","s31","s32","s33","s34","s35","s36","s37","s38","s39","s40","s41","s42","s43","s44","s45","s46","s47","s48","s49","s50","s51","s52","s53","s54","s55","s56","s57","s58","s59","s60","s61","s62","s63","s64","s65","s66","s67","s68","s69","s70","s71","s72","s73","s74","s75","s76","s77","s78","s79","s80","s81","s82","s83","s84","s85","s86","s87","s88","s89","s90","s91","s92","s93","s94","s95","s96","s97","s98","s99"
__global__ __launch_bounds__(256, 1) __attribute__((aligned(4096))) void ka_fused(const void* wq, const void* x, void* ws, const void* Kc, const void* wr, const void* Vc, const void* wo, const void* wg, const void* wu, const void* wd, void* out) {
  extern __shared__ __attribute__((aligned(16))) char lds[];
  const unsigned w = __builtin_amdgcn_readfirstlane(threadIdx.x >> 6);
  const unsigned slds = __builtin_amdgcn_readfirstlane((unsigned)(size_t)lds);
  asm volatile(
R"ASM(
.macro MM a, b, c, d:vararg
v_mfma_f32_16x16x32_bf16 \a, \b, \c, \d
.endm
.macro TR a, b:vararg
ds_read_b64_tr_b16 \a, \b
.endm
.macro CV a, b, c:vararg
v_cvt_pk_bf16_f32 \a, \b, \c
.endm
.macro AR a, b:vararg
v_accvgpr_read_b32 \a, \b
.endm
.macro AW a, b:vararg
v_accvgpr_write_b32 \a, \b
.endm
.macro EX a, b:vararg
v_exp_f32 \a, \b
.endm
.macro RD a, b:vararg
ds_read_b128 \a, \b
.endm
s_load_dwordx2 s[16:17], %[ka], 0
s_load_dwordx2 s[32:33], %[ka], 8
s_lshr_b32 s77, %[sb], 3
v_lshlrev_b32 v118, 4, v0
s_getpc_b64 s[98:99]
s_andn2_b32 s98, s98, 63
v_lshl_add_u32 v118, s77, 12, v118
global_load_dwordx4 a[252:255], v118, s[98:99]
.p2align 6
LPG0_%=:
s_load_dwordx2 s[20:21], %[ka], 16
v_mbcnt_lo_u32_b32 v127, -1, 0
v_mbcnt_hi_u32_b32 v127, -1, v127
v_lshlrev_b32 v248, 4, v127
s_and_b32 s77, %[sb], 7
s_lshl_b32 s77, s77, 2
s_bfe_u32 s84, %[sb], 131075
s_add_u32 s49, s77, s84
s_lshr_b32 s50, %[sb], 5
s_lshl_b32 s76, %[sb], 2
s_add_u32 s76, s76, %[swave]
v_lshrrev_b32 v180, 5, v127
s_mul_i32 s82, s76, 2560
v_and_b32 v182, 496, v248
v_mul_u32_u24 v181, 1280, v180
v_add_u32 v168, v181, v182
v_lshrrev_b32 v210, 1, v182
v_add_u32 v181, v181, v210
s_lshl_b32 s77, %[swave], 12
s_add_u32 s65, s77, %[slds]
s_lshl_b32 s89, %[swave], 17
v_bfe_u32 v210, v127, 2, 3
v_and_b32 v212, 3, v127
v_lshlrev_b32 v213, 4, v212
v_lshl_add_u32 v213, v180, 6, v213
v_lshrrev_b32 v253, 3, v127
v_lshlrev_b32 v253, 13, v253
v_and_b32 v254, 112, v248
v_or_b32 v253, v253, v254
v_and_b32 v254, 16, v127
v_lshlrev_b32 v254, 2, v254
v_xor_b32 v253, v253, v254
v_add_u32 v254, 128, v253
s_mul_i32 s96, %[swave], 4112
s_add_u32 s96, s96, %[slds]
s_lshl_b32 s77, s50, 2
s_add_u32 s77, s77, %[swave]
s_mul_i32 s68, s77, 81920
s_and_b32 s62, %[sb], 1
s_mul_i32 s63, s62, 40960
s_add_u32 s68, s68, s63
s_lshl_b32 s63, s63, 1
s_mul_i32 s83, s77, 560128
s_add_u32 s83, s83, 2752512
s_lshl_b32 s93, s77, 9
s_lshl_b32 s94, %[swave], 13
s_add_u32 s94, s94, %[slds]
v_lshrrev_b32 v211, 3, v127
v_mul_u32_u24 v220, 1280, v211
v_lshrrev_b32 v212, 4, v127
v_and_b32 v211, 7, v127
v_xor_b32 v211, v211, v212
v_lshl_add_u32 v220, v211, 4, v220
v_xor_b32 v221, 64, v220
s_bitcmp1_b32 s84, 0
s_mov_b32 s77, 0x77552200
s_cselect_b32 s77, 0x77552200, s77
s_lshl_b32 s91, s50, 2
s_lshr_b32 s77, s77, s91
s_and_b32 s77, s77, 15
s_lshl_b32 s86, s77, 7
s_lshl_b32 s85, s77, 19
s_lshl_b32 s90, s49, 8
s_sub_u32 s18, 5242880, s90
s_mov_b32 s19, 131072
s_mov_b32 s34, 2621440
s_mov_b32 s35, 131072
s_mov_b32 s22, 67108864
s_mov_b32 s23, 131072
s_add_u32 s87, s89, s85
s_add_u32 s85, s85, 524288
s_cmp_ge_u32 s85, 5242880
s_cselect_b32 s85, 0, s85
s_add_u32 m0, s96, 65536
s_waitcnt lgkmcnt(0)
s_add_u32 s16, s16, s90
s_addc_u32 s17, s17, 0
buffer_load_dwordx4 v253, s[16:19], s87 offen offset:0 lds
s_add_u32 m0, s96, 66560
s_nop 0
buffer_load_dwordx4 v254, s[16:19], s87 offen offset:0 lds
s_add_u32 s87, s87, 65536
s_add_u32 m0, s96, 67592
s_nop 0
buffer_load_dwordx4 v253, s[16:19], s87 offen offset:0 lds
s_add_u32 m0, s96, 68616
s_nop 0
buffer_load_dwordx4 v254, s[16:19], s87 offen offset:0 lds
s_add_u32 s88, s68, s86
s_add_u32 s86, s86, 128
s_cmp_ge_u32 s86, 1280
s_cselect_b32 s86, 0, s86
s_add_u32 m0, s94, 0
s_add_u32 s95, s88, 0
buffer_load_dwordx4 v220, s[32:35], s95 offen offset:0 lds
s_add_u32 s95, s88, 9216
buffer_load_dwordx4 v221, s[32:35], s95 offen offset:1024 lds
s_add_u32 s95, s88, 18432
buffer_load_dwordx4 v220, s[32:35], s95 offen offset:2048 lds
s_add_u32 s95, s88, 27648
buffer_load_dwordx4 v221, s[32:35], s95 offen offset:3072 lds
s_load_dwordx2 s[8:9], %[ka], 24
s_load_dwordx2 s[12:13], %[ka], 40
s_load_dwordx2 s[24:25], %[ka], 48
s_load_dwordx2 s[44:45], %[ka], 32
s_load_dwordx2 s[28:29], %[ka], 72
s_load_dwordx2 s[40:41], %[ka], 80
s_lshl_b32 s77, %[swave], 2
s_and_b32 s77, s77, 8
s_add_u32 s77, s77, 56
s_load_dwordx2 s[36:37], %[ka], s77
s_mov_b32 s66, 32768
buffer_load_dword v169, off, s[20:23], s66 sc1
buffer_load_dwordx4 v[128:131], v168, s[32:35], s82 offen offset:0
buffer_load_dwordx4 v[132:135], v168, s[32:35], s82 offen offset:512
buffer_load_dwordx2 v[136:137], v181, s[32:35], s82 offen offset:1024
s_add_u32 s82, s82, 131072
s_sub_u32 s88, s88, s63
s_add_u32 m0, s94, 4096
s_add_u32 s95, s88, 40960
buffer_load_dwordx4 v220, s[32:35], s95 offen offset:0 lds
s_add_u32 s95, s88, 50176
buffer_load_dwordx4 v221, s[32:35], s95 offen offset:1024 lds
s_add_u32 s95, s88, 59392
buffer_load_dwordx4 v220, s[32:35], s95 offen offset:2048 lds
s_add_u32 s95, s88, 68608
buffer_load_dwordx4 v221, s[32:35], s95 offen offset:3072 lds
v_and_b32 v211, 15, v127
v_bfe_u32 v222, v127, 1, 3
v_xor_b32 v222, v222, v212
v_lshlrev_b32 v222, 4, v222
v_lshl_add_u32 v222, v211, 7, v222
v_xor_b32 v223, 64, v222
v_add_u32 v222, s94, v222
v_add_u32 v223, s94, v223
v_and_b32 v214, 1, v212
v_lshlrev_b32 v215, 5, v214
v_xor_b32 v215, v215, v213
v_lshl_add_u32 v235, v210, 8, v215
v_add_u32 v235, 128, v235
v_and_b32 v234, 1, v127
v_mul_u32_u24 v234, 43690, v234
v_add_u32 v234, 17476, v234
v_bfe_u32 v215, v127, 2, 2
v_and_b32 v216, 3, v127
v_lshlrev_b32 v252, 7, v215
v_lshl_add_u32 v252, v216, 4, v252
v_and_b32 v251, 8, v127
v_lshl_add_u32 v252, v251, 3, v252
v_mul_u32_u24 v251, 2056, v212
s_xor_b32 s77, %[swave], 0
s_and_b32 s91, s77, 1
s_lshl_b32 s91, s91, 6
s_lshr_b32 s77, s77, 1
s_lshl_b32 s77, s77, 10
s_add_u32 s77, s77, %[slds]
s_add_u32 s77, s77, 65536
v_xor_b32 v192, s91, v252
v_add3_u32 v192, s77, v192, v251
v_add_u32 v196, 49344, v192
s_xor_b32 s77, %[swave], 1
s_and_b32 s91, s77, 1
s_lshl_b32 s91, s91, 6
s_lshr_b32 s77, s77, 1
s_lshl_b32 s77, s77, 10
s_add_u32 s77, s77, %[slds]
s_add_u32 s77, s77, 65536
v_xor_b32 v193, s91, v252
v_add3_u32 v193, s77, v193, v251
v_add_u32 v197, 49344, v193
s_xor_b32 s77, %[swave], 2
s_and_b32 s91, s77, 1
s_lshl_b32 s91, s91, 6
s_lshr_b32 s77, s77, 1
s_lshl_b32 s77, s77, 10
s_add_u32 s77, s77, %[slds]
s_add_u32 s77, s77, 65536
v_xor_b32 v194, s91, v252
v_add3_u32 v194, s77, v194, v251
v_add_u32 v198, 49344, v194
s_xor_b32 s77, %[swave], 3
s_and_b32 s91, s77, 1
s_lshl_b32 s91, s91, 6
s_lshr_b32 s77, s77, 1
s_lshl_b32 s77, s77, 10
s_add_u32 s77, s77, %[slds]
s_add_u32 s77, s77, 65536
v_xor_b32 v195, s91, v252
v_add3_u32 v195, s77, v195, v251
v_add_u32 v199, 49344, v195
v_mov_b32 v236, 0x3f803f80
v_mov_b32 v184, 0
v_mov_b32 v237, 0x3f803f80
v_mov_b32 v185, 0
v_mov_b32 v238, 0x3f803f80
v_mov_b32 v188, 0
v_mov_b32 v239, 0x3f803f80
v_mov_b32 v189, 0
s_add_u32 s87, s89, s85
s_add_u32 s85, s85, 524288
s_cmp_ge_u32 s85, 5242880
s_cselect_b32 s85, 0, s85
s_add_u32 m0, s96, 81984
s_nop 0
buffer_load_dwordx4 v253, s[16:19], s87 offen offset:0 lds
s_add_u32 m0, s96, 83008
s_nop 0
buffer_load_dwordx4 v254, s[16:19], s87 offen offset:0 lds
s_add_u32 s87, s87, 65536
s_add_u32 m0, s96, 84040
s_nop 0
buffer_load_dwordx4 v253, s[16:19], s87 offen offset:0 lds
s_add_u32 m0, s96, 85064
s_nop 0
buffer_load_dwordx4 v254, s[16:19], s87 offen offset:0 lds
s_getpc_b64 s[98:99]
LTA0_%=:
s_min_u32 s73, %[swave], 3
s_lshl_b32 s73, s73, 12
s_add_u32 s73, s73, LPG1_%=-LTA0_%=
s_add_u32 s98, s98, s73
s_addc_u32 s99, s99, 0
s_mov_b64 exec, 0
s_setpc_b64 s[98:99]
LTR0_%=:
s_mov_b64 exec, -1
s_waitcnt lgkmcnt(0)
s_add_u32 s88, s68, s86
s_add_u32 s86, s86, 128
s_cmp_ge_u32 s86, 1280
s_cselect_b32 s86, 0, s86
s_add_u32 m0, s94, 32768
s_add_u32 s95, s88, 0
buffer_load_dwordx4 v220, s[32:35], s95 offen offset:0 lds
s_add_u32 s95, s88, 9216
buffer_load_dwordx4 v221, s[32:35], s95 offen offset:1024 lds
s_add_u32 s95, s88, 18432
buffer_load_dwordx4 v220, s[32:35], s95 offen offset:2048 lds
s_add_u32 s95, s88, 27648
buffer_load_dwordx4 v221, s[32:35], s95 offen offset:3072 lds
s_sub_u32 s88, s88, s63
s_add_u32 m0, s94, 36864
s_add_u32 s95, s88, 40960
buffer_load_dwordx4 v220, s[32:35], s95 offen offset:0 lds
s_add_u32 s95, s88, 50176
buffer_load_dwordx4 v221, s[32:35], s95 offen offset:1024 lds
s_add_u32 s95, s88, 59392
buffer_load_dwordx4 v220, s[32:35], s95 offen offset:2048 lds
s_add_u32 s95, s88, 68608
buffer_load_dwordx4 v221, s[32:35], s95 offen offset:3072 lds
s_waitcnt vmcnt(20)
RD a[128:131],v222 offset:0
RD a[132:135],v223 offset:0
RD a[136:139],v222 offset:2048
RD a[140:143],v223 offset:2048
s_barrier
TR a[0:1],v192 offset:0
TR a[2:3],v192 offset:512
TR a[4:5],v192 offset:8224
TR a[6:7],v192 offset:8736
TR a[8:9],v192 offset:8
TR a[10:11],v192 offset:520
TR a[12:13],v192 offset:8232
TR a[14:15],v192 offset:8744
TR a[16:17],v193 offset:0
TR a[18:19],v193 offset:512
TR a[20:21],v193 offset:8224
TR a[22:23],v193 offset:8736
TR a[24:25],v193 offset:8
TR a[26:27],v193 offset:520
TR a[28:29],v193 offset:8232
TR a[30:31],v193 offset:8744
s_waitcnt lgkmcnt(15)
s_waitcnt lgkmcnt(12)
MM v[0:3],a[0:3],a[128:131],0
s_add_u32 s87, s89, s85
TR a[32:33],v194 offset:0
TR a[34:35],v194 offset:512
MM v[0:3],a[4:7],a[132:135],v[0:3]
s_add_u32 s85, s85, 524288
TR a[36:37],v194 offset:8224
TR a[38:39],v194 offset:8736
MM v[8:11],a[4:7],a[140:143],0
s_waitcnt vmcnt(16)
TR a[40:41],v194 offset:8
TR a[42:43],v194 offset:520
MM v[8:11],a[0:3],a[136:139],v[8:11]
s_waitcnt lgkmcnt(14)
TR a[44:45],v194 offset:8232
TR a[46:47],v194 offset:8744
s_cmp_ge_u32 s85, 5242880
s_cselect_b32 s85, 0, s85
MM v[4:7],a[8:11],a[128:131],0
s_add_u32 m0, s96, 98432
v_max3_f32 v200, |v0|, |v1|, |v2|
v_max_f32 v200, v200, |v3|
v_readfirstlane_b32 s69, v169
MM v[4:7],a[12:15],a[132:135],v[4:7]
s_lshl_b32 s90, s76, 1
TR a[48:49],v195 offset:0
TR a[50:51],v195 offset:512
MM v[12:15],a[12:15],a[140:143],0
s_and_b32 s77, s90, 15
buffer_load_dwordx4 v253, s[16:19], s87 offen offset:0 lds
v_max3_f32 v201, |v8|, |v9|, |v10|
v_max_f32 v201, v201, |v11|
MM v[12:15],a[8:11],a[136:139],v[12:15]
s_add_u32 m0, s96, 99456
TR a[52:53],v195 offset:8224
v_min_f32 v200, v200, v201
v_and_b32 v200, 0x7f800000, v200
v_lshlrev_b32 v148, 16, v128
v_smfmac_f32_16x16x64_bf16 v[184:187], v[236:239], a[0:7], v234
s_lshl_b32 s77, s77, 1
v_add_u32 v200, 0x2c00000, v200
v_smfmac_f32_16x16x64_bf16 v[188:191], v[236:239], a[8:15], v234
s_waitcnt lgkmcnt(15)
v_min_u32 v200, 0x78400000, v200
v_mov_b32 v201, v200
v_mov_b32 v202, v200
v_mov_b32 v203, v200
s_nop 1
MM v[32:35],a[16:19],a[128:131],v[200:203]
s_bfe_u32 s91, s90, 65540
TR a[54:55],v195 offset:8736
buffer_load_dwordx4 v254, s[16:19], s87 offen offset:0 lds
v_mov_b32 v250, v200
v_and_b32 v149, 0xffff0000, v128
MM v[32:35],a[20:23],a[132:135],v[32:35]
s_add_u32 s87, s87, 65536
TR a[56:57],v195 offset:8
TR a[58:59],v195 offset:520
MM v[40:43],a[20:23],a[140:143],v[200:203]
s_add_u32 m0, s96, 100488
TR a[60:61],v195 offset:8232
TR a[62:63],v195 offset:8744
MM v[40:43],a[16:19],a[136:139],v[40:43]
s_waitcnt lgkmcnt(15)
buffer_load_dwordx4 v253, s[16:19], s87 offen offset:0 lds
v_lshlrev_b32 v150, 16, v129
MM v[36:39],a[24:27],a[128:131],v[200:203]
s_add_u32 m0, s96, 101512
v_and_b32 v151, 0xffff0000, v129
v_lshlrev_b32 v152, 16, v130
MM v[36:39],a[28:31],a[132:135],v[36:39]
s_add_u32 s77, s77, s91
v_and_b32 v153, 0xffff0000, v130
v_lshlrev_b32 v154, 16, v131
MM v[44:47],a[28:31],a[140:143],v[200:203]
s_and_b32 s91, s90, 0xffffffe0
buffer_load_dwordx4 v254, s[16:19], s87 offen offset:0 lds
MM v[44:47],a[24:27],a[136:139],v[44:47]
s_waitcnt lgkmcnt(12)
s_waitcnt vmcnt(16)
RD a[144:147],v222 offset:4096
v_and_b32 v155, 0xffff0000, v131
MM v[64:67],a[32:35],a[128:131],v[200:203]
s_add_u32 s77, s77, s91
RD a[148:151],v223 offset:4096
RD a[152:155],v222 offset:6144
MM v[64:67],a[36:39],a[132:135],v[64:67]
s_lshl_b32 s77, s77, 3
RD a[156:159],v223 offset:6144
v_lshlrev_b32 v156, 16, v132
MM v[72:75],a[36:39],a[140:143],v[200:203]
s_add_u32 s97, s77, 36864
s_waitcnt vmcnt(12)
s_barrier
v_and_b32 v157, 0xffff0000, v132
MM v[72:75],a[32:35],a[136:139],v[72:75]
s_waitcnt lgkmcnt(12)
global_load_dwordx4 v[138:141], v182, s[44:45] offset:0
MM v[68:71],a[40:43],a[128:131],v[200:203]
s_sub_u32 s79, 65535, s90
v_lshlrev_b32 v158, 16, v133
v_and_b32 v159, 0xffff0000, v133
MM v[68:71],a[44:47],a[132:135],v[68:71]
s_lshl_b32 s78, s90, 2
v_lshlrev_b32 v160, 16, v134
v_and_b32 v161, 0xffff0000, v134
MM v[76:79],a[44:47],a[140:143],v[200:203]
s_add_u32 s78, s78, 53248
global_load_dwordx4 v[142:145], v182, s[44:45] offset:512
MM v[76:79],a[40:43],a[136:139],v[76:79]
s_waitcnt lgkmcnt(8)
v_lshrrev_b32 v182, 1, v182
v_lshlrev_b32 v162, 16, v135
MM v[96:99],a[48:51],a[128:131],v[200:203]
s_lshl_b32 s80, s76, 2
global_load_dwordx2 v[146:147], v182, s[44:45] offset:1024
MM v[96:99],a[52:55],a[132:135],v[96:99]
)ASM"
R"ASM(
s_waitcnt lgkmcnt(0)
s_add_u32 s88, s68, s86
s_add_u32 s86, s86, 128
v_and_b32 v163, 0xffff0000, v135
MM v[104:107],a[52:55],a[140:143],v[200:203]
s_add_u32 s80, s80, 81920
s_cmp_ge_u32 s86, 1280
s_cselect_b32 s86, 0, s86
MM v[104:107],a[48:51],a[136:139],v[104:107]
s_add_u32 m0, s94, 0
s_add_u32 s95, s88, 0
v_lshlrev_b32 v164, 16, v136
MM v[100:103],a[56:59],a[128:131],v[200:203]
s_mov_b32 s42, 2621440
buffer_load_dwordx4 v220, s[32:35], s95 offen offset:0 lds
MM v[100:103],a[60:63],a[132:135],v[100:103]
s_add_u32 s95, s88, 9216
v_and_b32 v165, 0xffff0000, v136
v_lshlrev_b32 v166, 16, v137
MM v[108:111],a[60:63],a[140:143],v[200:203]
s_mov_b32 s43, 131072
buffer_load_dwordx4 v221, s[32:35], s95 offen offset:1024 lds
MM v[108:111],a[56:59],a[136:139],v[108:111]
s_add_u32 s95, s88, 18432
v_and_b32 v167, 0xffff0000, v137
s_add_u32 s69, s69, 1
MM v[16:19],a[0:3],a[144:147],0
v_add_f32 v170, v148, v149
v_add_f32 v171, v150, v151
MM v[16:19],a[4:7],a[148:151],v[16:19]
v_add_f32 v170, v170, v152
v_add_f32 v171, v171, v153
MM v[24:27],a[4:7],a[156:159],0
buffer_load_dwordx4 v220, s[32:35], s95 offen offset:2048 lds
MM v[24:27],a[0:3],a[152:155],v[24:27]
s_add_u32 s95, s88, 27648
v_add_f32 v170, v170, v154
v_add_f32 v171, v171, v155
MM v[20:23],a[8:11],a[144:147],0
v_max3_f32 v204, |v16|, |v17|, |v18|
v_max_f32 v204, v204, |v19|
MM v[20:23],a[12:15],a[148:151],v[20:23]
TR a[0:1],v192 offset:16448
TR a[2:3],v192 offset:16960
v_add_f32 v170, v170, v156
MM v[28:31],a[12:15],a[156:159],0
v_max3_f32 v205, |v24|, |v25|, |v26|
v_max_f32 v205, v205, |v27|
MM v[28:31],a[8:11],a[152:155],v[28:31]
TR a[4:5],v192 offset:24672
v_min_f32 v204, v204, v205
v_and_b32 v204, 0x7f800000, v204
v_add_u32 v204, 0x2c00000, v204
v_min_u32 v204, 0x78400000, v204
v_mov_b32 v205, v204
v_mov_b32 v206, v204
v_mov_b32 v207, v204
s_nop 1
MM v[48:51],a[16:19],a[144:147],v[204:207]
TR a[6:7],v192 offset:25184
buffer_load_dwordx4 v221, s[32:35], s95 offen offset:3072 lds
v_mov_b32 v252, v204
v_add_f32 v171, v171, v157
MM v[48:51],a[20:23],a[148:151],v[48:51]
s_sub_u32 s88, s88, s63
TR a[8:9],v192 offset:16456
TR a[10:11],v192 offset:16968
MM v[56:59],a[20:23],a[156:159],v[204:207]
s_add_u32 m0, s94, 4096
TR a[12:13],v192 offset:24680
TR a[14:15],v192 offset:25192
MM v[56:59],a[16:19],a[152:155],v[56:59]
s_add_u32 s95, s88, 40960
buffer_load_dwordx4 v220, s[32:35], s95 offen offset:0 lds
v_add_f32 v170, v170, v158
MM v[52:55],a[24:27],a[144:147],v[204:207]
s_add_u32 s95, s88, 50176
TR a[16:17],v193 offset:16448
TR a[18:19],v193 offset:16960
MM v[52:55],a[28:31],a[148:151],v[52:55]
TR a[20:21],v193 offset:24672
TR a[22:23],v193 offset:25184
MM v[60:63],a[28:31],a[156:159],v[204:207]
buffer_load_dwordx4 v221, s[32:35], s95 offen offset:1024 lds
v_add_f32 v171, v171, v159
MM v[60:63],a[24:27],a[152:155],v[60:63]
s_add_u32 s95, s88, 59392
v_add_f32 v170, v170, v160
v_add_f32 v171, v171, v161
MM v[80:83],a[32:35],a[144:147],v[204:207]
TR a[24:25],v193 offset:16456
TR a[26:27],v193 offset:16968
MM v[80:83],a[36:39],a[148:151],v[80:83]
TR a[28:29],v193 offset:24680
TR a[30:31],v193 offset:25192
v_add_f32 v170, v170, v162
MM v[88:91],a[36:39],a[156:159],v[204:207]
buffer_load_dwordx4 v220, s[32:35], s95 offen offset:2048 lds
MM v[88:91],a[32:35],a[152:155],v[88:91]
s_add_u32 s95, s88, 68608
v_add_f32 v171, v171, v163
v_add_f32 v170, v170, v164
MM v[84:87],a[40:43],a[144:147],v[204:207]
TR a[32:33],v194 offset:16448
TR a[34:35],v194 offset:16960
MM v[84:87],a[44:47],a[148:151],v[84:87]
TR a[36:37],v194 offset:24672
TR a[38:39],v194 offset:25184
v_add_f32 v171, v171, v165
MM v[92:95],a[44:47],a[156:159],v[204:207]
buffer_load_dwordx4 v221, s[32:35], s95 offen offset:3072 lds
MM v[92:95],a[40:43],a[152:155],v[92:95]
s_add_u32 s87, s89, s85
s_add_u32 s85, s85, 524288
v_add_f32 v170, v170, v166
MM v[112:115],a[48:51],a[144:147],v[204:207]
TR a[40:41],v194 offset:16456
TR a[42:43],v194 offset:16968
MM v[112:115],a[52:55],a[148:151],v[112:115]
TR a[44:45],v194 offset:24680
TR a[46:47],v194 offset:25192
s_cmp_ge_u32 s85, 5242880
s_cselect_b32 s85, 0, s85
v_add_f32 v171, v171, v167
MM v[120:123],a[52:55],a[156:159],v[204:207]
s_add_u32 m0, s96, 114880
s_nop 0
buffer_load_dwordx4 v253, s[16:19], s87 offen offset:0 lds
MM v[120:123],a[48:51],a[152:155],v[120:123]
s_add_u32 m0, s96, 115904
v_add_f32 v172, v170, v171
v_lshlrev_b32 v232, 11, v180
MM v[116:119],a[56:59],a[144:147],v[204:207]
TR a[48:49],v195 offset:16448
TR a[50:51],v195 offset:16960
MM v[116:119],a[60:63],a[148:151],v[116:119]
TR a[52:53],v195 offset:24672
TR a[54:55],v195 offset:25184
MM v[124:127],a[60:63],a[156:159],v[204:207]
buffer_load_dwordx4 v254, s[16:19], s87 offen offset:0 lds
v_add_f32_dpp v172, v172, v172 row_ror:8 row_mask:0xf bank_mask:0xf
MM v[124:127],a[56:59],a[152:155],v[124:127]
s_add_u32 s87, s87, 65536
s_add_u32 m0, s96, 116936
v_add_f32_dpp v172, v172, v172 row_ror:4 row_mask:0xf bank_mask:0xf
buffer_load_dwordx4 v253, s[16:19], s87 offen offset:0 lds
s_add_u32 m0, s96, 117960
s_nop 0
buffer_load_dwordx4 v254, s[16:19], s87 offen offset:0 lds
s_waitcnt vmcnt(19)
RD a[160:163],v222 offset:32768
RD a[164:167],v223 offset:32768
RD a[168:171],v222 offset:34816
RD a[172:175],v223 offset:34816
RD a[176:179],v222 offset:36864
RD a[180:183],v223 offset:36864
RD a[184:187],v222 offset:38912
RD a[188:191],v223 offset:38912
s_waitcnt vmcnt(15) lgkmcnt(0)
s_barrier
MM v[0:3],a[0:3],a[160:163],v[0:3]
TR a[56:57],v195 offset:16456
TR a[58:59],v195 offset:16968
MM v[0:3],a[4:7],a[164:167],v[0:3]
TR a[60:61],v195 offset:24680
TR a[62:63],v195 offset:25192
MM v[8:11],a[4:7],a[172:175],v[8:11]
v_add_f32_dpp v172, v172, v172 row_ror:2 row_mask:0xf bank_mask:0xf
v_lshl_add_u32 v232, v214, 8, v232
MM v[8:11],a[0:3],a[168:171],v[8:11]
s_add_u32 s88, s68, s86
s_add_u32 s86, s86, 128
MM v[16:19],a[0:3],a[176:179],v[16:19]
s_cmp_ge_u32 s86, 1280
s_cselect_b32 s86, 0, s86
s_add_u32 m0, s94, 32768
MM v[16:19],a[4:7],a[180:183],v[16:19]
s_add_u32 s95, s88, 0
buffer_load_dwordx4 v220, s[32:35], s95 offen offset:0 lds
v_add_f32_dpp v172, v172, v172 row_ror:1 row_mask:0xf bank_mask:0xf
MM v[24:27],a[4:7],a[188:191],v[24:27]
s_add_u32 s95, s88, 9216
v_mov_b32 v173, v172
v_lshl_add_u32 v232, v215, 6, v232
MM v[24:27],a[0:3],a[184:187],v[24:27]
buffer_load_dwordx4 v221, s[32:35], s95 offen offset:1024 lds
MM v[4:7],a[8:11],a[160:163],v[4:7]
s_add_u32 s95, s88, 18432
v_permlane16_swap_b32 v172, v173
v_add_f32 v172, v172, v173
MM v[4:7],a[12:15],a[164:167],v[4:7]
v_mul_f32 v172, 0x3acccccd, v172
v_sub_f32 v176, v148, v172
MM v[12:15],a[12:15],a[172:175],v[12:15]
v_sub_f32 v177, v149, v172
v_mul_f32 v170, v176, v176
MM v[12:15],a[8:11],a[168:171],v[12:15]
buffer_load_dwordx4 v220, s[32:35], s95 offen offset:2048 lds
MM v[20:23],a[8:11],a[176:179],v[20:23]
s_add_u32 s95, s88, 27648
v_mul_f32 v171, v177, v177
v_sub_f32 v176, v150, v172
MM v[20:23],a[12:15],a[180:183],v[20:23]
v_sub_f32 v177, v151, v172
v_fma_f32 v170, v176, v176, v170
MM v[28:31],a[12:15],a[188:191],v[28:31]
v_fma_f32 v171, v177, v177, v171
v_sub_f32 v176, v152, v172
MM v[28:31],a[8:11],a[184:187],v[28:31]
buffer_load_dwordx4 v221, s[32:35], s95 offen offset:3072 lds
v_smfmac_f32_16x16x64_bf16 v[184:187], v[236:239], a[0:7], v234
s_sub_u32 s88, s88, s63
s_add_u32 m0, s94, 36864
v_sub_f32 v177, v153, v172
v_smfmac_f32_16x16x64_bf16 v[188:191], v[236:239], a[8:15], v234
s_add_u32 s95, s88, 40960
v_fma_f32 v170, v176, v176, v170
MM v[32:35],a[16:19],a[160:163],v[32:35]
TR a[0:1],v192 offset:32896
TR a[2:3],v192 offset:33408
MM v[32:35],a[20:23],a[164:167],v[32:35]
TR a[4:5],v192 offset:41120
TR a[6:7],v192 offset:41632
v_fma_f32 v171, v177, v177, v171
MM v[40:43],a[20:23],a[172:175],v[40:43]
LPG1_%=:
s_cbranch_execz LPG5_%=
TR a[8:9],v192 offset:32904
TR a[10:11],v192 offset:33416
MM v[40:43],a[16:19],a[168:171],v[40:43]
TR a[12:13],v192 offset:41128
TR a[14:15],v192 offset:41640
buffer_load_dwordx4 v220, s[32:35], s95 offen offset:0 lds
MM v[48:51],a[16:19],a[176:179],v[48:51]
s_add_u32 s95, s88, 50176
v_sub_f32 v176, v154, v172
v_sub_f32 v177, v155, v172
MM v[48:51],a[20:23],a[180:183],v[48:51]
buffer_load_dwordx4 v221, s[32:35], s95 offen offset:1024 lds
MM v[56:59],a[20:23],a[188:191],v[56:59]
s_add_u32 s95, s88, 59392
v_fma_f32 v170, v176, v176, v170
v_fma_f32 v171, v177, v177, v171
MM v[56:59],a[16:19],a[184:187],v[56:59]
v_sub_f32 v176, v156, v172
v_sub_f32 v177, v157, v172
MM v[36:39],a[24:27],a[160:163],v[36:39]
TR a[16:17],v193 offset:32896
TR a[18:19],v193 offset:33408
MM v[36:39],a[28:31],a[164:167],v[36:39]
TR a[20:21],v193 offset:41120
TR a[22:23],v193 offset:41632
v_fma_f32 v170, v176, v176, v170
MM v[44:47],a[28:31],a[172:175],v[44:47]
buffer_load_dwordx4 v220, s[32:35], s95 offen offset:2048 lds
MM v[44:47],a[24:27],a[168:171],v[44:47]
s_add_u32 s95, s88, 68608
v_fma_f32 v171, v177, v177, v171
v_sub_f32 v176, v158, v172
MM v[52:55],a[24:27],a[176:179],v[52:55]
v_sub_f32 v177, v159, v172
v_fma_f32 v170, v176, v176, v170
MM v[52:55],a[28:31],a[180:183],v[52:55]
buffer_load_dwordx4 v221, s[32:35], s95 offen offset:3072 lds
MM v[60:63],a[28:31],a[188:191],v[60:63]
s_add_u32 s87, s89, s85
s_add_u32 s85, s85, 524288
v_fma_f32 v171, v177, v177, v171
MM v[60:63],a[24:27],a[184:187],v[60:63]
s_cmp_ge_u32 s85, 5242880
s_cselect_b32 s85, 0, s85
s_add_u32 m0, s96, 131328
MM v[64:67],a[32:35],a[160:163],v[64:67]
TR a[24:25],v193 offset:32904
TR a[26:27],v193 offset:33416
v_sub_f32 v176, v160, v172
MM v[64:67],a[36:39],a[164:167],v[64:67]
TR a[28:29],v193 offset:41128
TR a[30:31],v193 offset:41640
MM v[72:75],a[36:39],a[172:175],v[72:75]
buffer_load_dwordx4 v253, s[16:19], s87 offen offset:0 lds
MM v[72:75],a[32:35],a[168:171],v[72:75]
s_add_u32 m0, s96, 132352
v_sub_f32 v177, v161, v172
v_fma_f32 v170, v176, v176, v170
MM v[80:83],a[32:35],a[176:179],v[80:83]
v_fma_f32 v171, v177, v177, v171
v_sub_f32 v176, v162, v172
MM v[80:83],a[36:39],a[180:183],v[80:83]
buffer_load_dwordx4 v254, s[16:19], s87 offen offset:0 lds
MM v[88:91],a[36:39],a[188:191],v[88:91]
s_add_u32 s87, s87, 65536
s_add_u32 m0, s96, 133384
v_sub_f32 v177, v163, v172
MM v[88:91],a[32:35],a[184:187],v[88:91]
v_fma_f32 v170, v176, v176, v170
v_fma_f32 v171, v177, v177, v171
MM v[68:71],a[40:43],a[160:163],v[68:71]
TR a[32:33],v194 offset:32896
TR a[34:35],v194 offset:33408
MM v[68:71],a[44:47],a[164:167],v[68:71]
TR a[36:37],v194 offset:41120
TR a[38:39],v194 offset:41632
v_sub_f32 v176, v164, v172
MM v[76:79],a[44:47],a[172:175],v[76:79]
buffer_load_dwordx4 v253, s[16:19], s87 offen offset:0 lds
MM v[76:79],a[40:43],a[168:171],v[76:79]
s_add_u32 m0, s96, 134408
v_sub_f32 v177, v165, v172
v_fma_f32 v170, v176, v176, v170
MM v[84:87],a[40:43],a[176:179],v[84:87]
v_fma_f32 v171, v177, v177, v171
v_sub_f32 v176, v166, v172
MM v[84:87],a[44:47],a[180:183],v[84:87]
buffer_load_dwordx4 v254, s[16:19], s87 offen offset:0 lds
MM v[92:95],a[44:47],a[188:191],v[92:95]
s_waitcnt vmcnt(16)
RD a[192:195],v222 offset:0
v_sub_f32 v177, v167, v172
MM v[92:95],a[40:43],a[184:187],v[92:95]
RD a[196:199],v223 offset:0
v_fma_f32 v170, v176, v176, v170
MM v[96:99],a[48:51],a[160:163],v[96:99]
TR a[40:41],v194 offset:32904
TR a[42:43],v194 offset:33416
MM v[96:99],a[52:55],a[164:167],v[96:99]
TR a[44:45],v194 offset:41128
TR a[46:47],v194 offset:41640
v_fma_f32 v171, v177, v177, v171
MM v[104:107],a[52:55],a[172:175],v[104:107]
RD a[200:203],v222 offset:2048
v_add_f32 v170, v170, v171
MM v[104:107],a[48:51],a[168:171],v[104:107]
RD a[204:207],v223 offset:2048
v_add_f32_dpp v170, v170, v170 row_ror:8 row_mask:0xf bank_mask:0xf
MM v[112:115],a[48:51],a[176:179],v[112:115]
RD a[208:211],v222 offset:4096
v_add_f32_dpp v170, v170, v170 row_ror:4 row_mask:0xf bank_mask:0xf
MM v[112:115],a[52:55],a[180:183],v[112:115]
RD a[212:215],v223 offset:4096
v_add_f32_dpp v170, v170, v170 row_ror:2 row_mask:0xf bank_mask:0xf
MM v[120:123],a[52:55],a[188:191],v[120:123]
RD a[216:219],v222 offset:6144
v_add_f32_dpp v170, v170, v170 row_ror:1 row_mask:0xf bank_mask:0xf
MM v[120:123],a[48:51],a[184:187],v[120:123]
)ASM"
R"ASM(
s_waitcnt lgkmcnt(15)
RD a[220:223],v223 offset:6144
v_mov_b32 v173, v170
MM v[100:103],a[56:59],a[160:163],v[100:103]
TR a[48:49],v195 offset:32896
TR a[50:51],v195 offset:33408
MM v[100:103],a[60:63],a[164:167],v[100:103]
TR a[52:53],v195 offset:41120
TR a[54:55],v195 offset:41632
MM v[108:111],a[60:63],a[172:175],v[108:111]
v_permlane16_swap_b32 v170, v173
v_add_f32 v170, v170, v173
MM v[108:111],a[56:59],a[168:171],v[108:111]
s_waitcnt lgkmcnt(4)
s_add_u32 s88, s68, s86
s_add_u32 s86, s86, 128
MM v[116:119],a[56:59],a[176:179],v[116:119]
s_cmp_ge_u32 s86, 1280
s_cselect_b32 s86, 0, s86
s_add_u32 m0, s94, 0
v_mul_f32 v170, 0x3acccccd, v170
MM v[116:119],a[60:63],a[180:183],v[116:119]
s_add_u32 s95, s88, 0
buffer_load_dwordx4 v220, s[32:35], s95 offen offset:0 lds
MM v[124:127],a[60:63],a[188:191],v[124:127]
s_add_u32 s95, s88, 9216
CV v178,v172,v170
v_lshlrev_b32 v174, 16, v178
MM v[124:127],a[56:59],a[184:187],v[124:127]
buffer_load_dwordx4 v221, s[32:35], s95 offen offset:1024 lds
s_waitcnt vmcnt(14)
s_barrier
MM v[0:3],a[0:3],a[192:195],v[0:3]
s_add_u32 s95, s88, 18432
TR a[56:57],v195 offset:32904
TR a[58:59],v195 offset:33416
v_and_b32 v170, 0xffff0000, v178
MM v[0:3],a[4:7],a[196:199],v[0:3]
TR a[60:61],v195 offset:41128
TR a[62:63],v195 offset:41640
MM v[8:11],a[4:7],a[204:207],v[8:11]
v_add_f32 v170, 0x3727c5ac, v170
CV v170,v170,v170
MM v[8:11],a[0:3],a[200:203],v[8:11]
buffer_load_dwordx4 v220, s[32:35], s95 offen offset:2048 lds
MM v[16:19],a[0:3],a[208:211],v[16:19]
s_add_u32 s95, s88, 27648
v_and_b32 v170, 0xffff0000, v170
v_lshl_add_u32 v232, v216, 3, v232
MM v[16:19],a[4:7],a[212:215],v[16:19]
v_sqrt_f32 v170, v170
MM v[24:27],a[4:7],a[220:223],v[24:27]
v_mov_b32 v179, s69
CV v178,v174,v170
MM v[24:27],a[0:3],a[216:219],v[24:27]
buffer_load_dwordx4 v221, s[32:35], s95 offen offset:3072 lds
MM v[4:7],a[8:11],a[192:195],v[4:7]
s_sub_u32 s88, s88, s63
s_add_u32 m0, s94, 4096
s_add_u32 s95, s88, 40960
v_and_b32 v170, 0xffff0000, v178
MM v[4:7],a[12:15],a[196:199],v[4:7]
v_rcp_f32 v175, v170
MM v[12:15],a[12:15],a[204:207],v[12:15]
v_lshlrev_b32 v173, 4, v180
v_lshl_add_u32 v232, v214, 5, v232
MM v[12:15],a[8:11],a[200:203],v[12:15]
buffer_load_dwordx4 v220, s[32:35], s95 offen offset:0 lds
MM v[20:23],a[8:11],a[208:211],v[20:23]
s_add_u32 s95, s88, 50176
s_mov_b32 exec_lo, 1
s_mov_b32 exec_hi, 1
buffer_store_dwordx2 v[178:179], v173, s[20:23], s97 offen offset:0 sc1
s_mov_b64 exec, -1
MM v[20:23],a[12:15],a[212:215],v[20:23]
v_sub_f32 v148, v148, v174
v_sub_f32 v149, v149, v174
MM v[28:31],a[12:15],a[220:223],v[28:31]
v_sub_f32 v150, v150, v174
v_sub_f32 v151, v151, v174
MM v[28:31],a[8:11],a[216:219],v[28:31]
buffer_load_dwordx4 v221, s[32:35], s95 offen offset:1024 lds
v_smfmac_f32_16x16x64_bf16 v[184:187], v[236:239], a[0:7], v234
s_add_u32 s95, s88, 59392
v_sub_f32 v152, v152, v174
v_smfmac_f32_16x16x64_bf16 v[188:191], v[236:239], a[8:15], v234
v_sub_f32 v153, v153, v174
MM v[32:35],a[16:19],a[192:195],v[32:35]
TR a[0:1],v196 offset:0
TR a[2:3],v196 offset:512
MM v[32:35],a[20:23],a[196:199],v[32:35]
TR a[4:5],v196 offset:8224
TR a[6:7],v196 offset:8736
v_sub_f32 v154, v154, v174
MM v[40:43],a[20:23],a[204:207],v[40:43]
TR a[8:9],v196 offset:8
TR a[10:11],v196 offset:520
MM v[40:43],a[16:19],a[200:203],v[40:43]
TR a[12:13],v196 offset:8232
TR a[14:15],v196 offset:8744
buffer_load_dwordx4 v220, s[32:35], s95 offen offset:2048 lds
MM v[48:51],a[16:19],a[208:211],v[48:51]
s_add_u32 s95, s88, 68608
v_sub_f32 v155, v155, v174
v_sub_f32 v156, v156, v174
MM v[48:51],a[20:23],a[212:215],v[48:51]
buffer_load_dwordx4 v221, s[32:35], s95 offen offset:3072 lds
MM v[56:59],a[20:23],a[220:223],v[56:59]
s_add_u32 s87, s89, s85
s_add_u32 s85, s85, 524288
v_sub_f32 v157, v157, v174
MM v[56:59],a[16:19],a[216:219],v[56:59]
s_cmp_ge_u32 s85, 5242880
s_cselect_b32 s85, 0, s85
s_add_u32 m0, s96, 65536
MM v[36:39],a[24:27],a[192:195],v[36:39]
TR a[16:17],v197 offset:0
TR a[18:19],v197 offset:512
v_sub_f32 v158, v158, v174
MM v[36:39],a[28:31],a[196:199],v[36:39]
TR a[20:21],v197 offset:8224
TR a[22:23],v197 offset:8736
MM v[44:47],a[28:31],a[204:207],v[44:47]
buffer_load_dwordx4 v253, s[16:19], s87 offen offset:0 lds
MM v[44:47],a[24:27],a[200:203],v[44:47]
s_add_u32 m0, s96, 66560
v_sub_f32 v159, v159, v174
v_sub_f32 v160, v160, v174
MM v[52:55],a[24:27],a[208:211],v[52:55]
v_sub_f32 v161, v161, v174
v_sub_f32 v162, v162, v174
MM v[52:55],a[28:31],a[212:215],v[52:55]
buffer_load_dwordx4 v254, s[16:19], s87 offen offset:0 lds
MM v[60:63],a[28:31],a[220:223],v[60:63]
s_add_u32 s87, s87, 65536
s_add_u32 m0, s96, 67592
v_sub_f32 v163, v163, v174
MM v[60:63],a[24:27],a[216:219],v[60:63]
v_sub_f32 v164, v164, v174
v_sub_f32 v165, v165, v174
MM v[64:67],a[32:35],a[192:195],v[64:67]
TR a[24:25],v197 offset:8
TR a[26:27],v197 offset:520
MM v[64:67],a[36:39],a[196:199],v[64:67]
TR a[28:29],v197 offset:8232
TR a[30:31],v197 offset:8744
v_sub_f32 v166, v166, v174
MM v[72:75],a[36:39],a[204:207],v[72:75]
buffer_load_dwordx4 v253, s[16:19], s87 offen offset:0 lds
MM v[72:75],a[32:35],a[200:203],v[72:75]
s_add_u32 m0, s96, 68616
v_sub_f32 v167, v167, v174
CV v128,v148,v149
MM v[80:83],a[32:35],a[208:211],v[80:83]
CV v129,v150,v151
CV v130,v152,v153
MM v[80:83],a[36:39],a[212:215],v[80:83]
buffer_load_dwordx4 v254, s[16:19], s87 offen offset:0 lds
MM v[88:91],a[36:39],a[220:223],v[88:91]
s_waitcnt vmcnt(17)
RD a[224:227],v222 offset:32768
CV v131,v154,v155
MM v[88:91],a[32:35],a[216:219],v[88:91]
RD a[228:231],v223 offset:32768
CV v132,v156,v157
MM v[68:71],a[40:43],a[192:195],v[68:71]
TR a[32:33],v198 offset:0
TR a[34:35],v198 offset:512
MM v[68:71],a[44:47],a[196:199],v[68:71]
TR a[36:37],v198 offset:8224
TR a[38:39],v198 offset:8736
CV v133,v158,v159
MM v[76:79],a[44:47],a[204:207],v[76:79]
RD a[232:235],v222 offset:34816
CV v134,v160,v161
MM v[76:79],a[40:43],a[200:203],v[76:79]
RD a[236:239],v223 offset:34816
CV v135,v162,v163
MM v[84:87],a[40:43],a[208:211],v[84:87]
RD a[240:243],v222 offset:36864
CV v136,v164,v165
MM v[84:87],a[44:47],a[212:215],v[84:87]
RD a[244:247],v223 offset:36864
CV v137,v166,v167
MM v[92:95],a[44:47],a[220:223],v[92:95]
RD a[248:251],v222 offset:38912
v_lshlrev_b32 v148, 16, v128
MM v[92:95],a[40:43],a[216:219],v[92:95]
s_waitcnt lgkmcnt(15)
RD a[252:255],v223 offset:38912
v_and_b32 v149, 0xffff0000, v128
MM v[96:99],a[48:51],a[192:195],v[96:99]
TR a[40:41],v198 offset:8
TR a[42:43],v198 offset:520
MM v[96:99],a[52:55],a[196:199],v[96:99]
TR a[44:45],v198 offset:8232
TR a[46:47],v198 offset:8744
v_lshlrev_b32 v150, 16, v129
MM v[104:107],a[52:55],a[204:207],v[104:107]
v_and_b32 v151, 0xffff0000, v129
v_lshlrev_b32 v152, 16, v130
MM v[104:107],a[48:51],a[200:203],v[104:107]
s_waitcnt lgkmcnt(4)
s_add_u32 s88, s68, s86
s_add_u32 s86, s86, 128
MM v[112:115],a[48:51],a[208:211],v[112:115]
s_cmp_ge_u32 s86, 1280
s_cselect_b32 s86, 0, s86
s_add_u32 m0, s94, 32768
v_and_b32 v153, 0xffff0000, v130
MM v[112:115],a[52:55],a[212:215],v[112:115]
s_add_u32 s95, s88, 0
buffer_load_dwordx4 v220, s[32:35], s95 offen offset:0 lds
MM v[120:123],a[52:55],a[220:223],v[120:123]
s_add_u32 s95, s88, 9216
v_lshlrev_b32 v154, 16, v131
v_and_b32 v155, 0xffff0000, v131
MM v[120:123],a[48:51],a[216:219],v[120:123]
buffer_load_dwordx4 v221, s[32:35], s95 offen offset:1024 lds
MM v[100:103],a[56:59],a[192:195],v[100:103]
s_add_u32 s95, s88, 18432
TR a[48:49],v199 offset:0
TR a[50:51],v199 offset:512
v_lshlrev_b32 v156, 16, v132
MM v[100:103],a[60:63],a[196:199],v[100:103]
TR a[52:53],v199 offset:8224
TR a[54:55],v199 offset:8736
MM v[108:111],a[60:63],a[204:207],v[108:111]
v_and_b32 v157, 0xffff0000, v132
v_lshlrev_b32 v158, 16, v133
MM v[108:111],a[56:59],a[200:203],v[108:111]
buffer_load_dwordx4 v220, s[32:35], s95 offen offset:2048 lds
MM v[116:119],a[56:59],a[208:211],v[116:119]
s_add_u32 s95, s88, 27648
v_and_b32 v159, 0xffff0000, v133
v_lshlrev_b32 v160, 16, v134
MM v[116:119],a[60:63],a[212:215],v[116:119]
v_and_b32 v161, 0xffff0000, v134
v_lshlrev_b32 v162, 16, v135
MM v[124:127],a[60:63],a[220:223],v[124:127]
v_and_b32 v163, 0xffff0000, v135
v_lshlrev_b32 v164, 16, v136
MM v[124:127],a[56:59],a[216:219],v[124:127]
buffer_load_dwordx4 v221, s[32:35], s95 offen offset:3072 lds
s_waitcnt vmcnt(17)
s_barrier
MM v[0:3],a[0:3],a[224:227],v[0:3]
s_sub_u32 s88, s88, s63
TR a[56:57],v199 offset:8
TR a[58:59],v199 offset:520
v_and_b32 v165, 0xffff0000, v136
MM v[0:3],a[4:7],a[228:231],v[0:3]
s_add_u32 m0, s94, 36864
TR a[60:61],v199 offset:8232
TR a[62:63],v199 offset:8744
MM v[8:11],a[4:7],a[236:239],v[8:11]
s_add_u32 s95, s88, 40960
v_lshlrev_b32 v166, 16, v137
v_and_b32 v167, 0xffff0000, v137
MM v[8:11],a[0:3],a[232:235],v[8:11]
buffer_load_dwordx4 v220, s[32:35], s95 offen offset:0 lds
MM v[16:19],a[0:3],a[240:243],v[16:19]
s_add_u32 s95, s88, 50176
v_mul_f32 v148, v148, v175
v_mul_f32 v149, v149, v175
MM v[16:19],a[4:7],a[244:247],v[16:19]
v_mul_f32 v150, v150, v175
v_mul_f32 v151, v151, v175
MM v[24:27],a[4:7],a[252:255],v[24:27]
v_mul_f32 v152, v152, v175
v_mul_f32 v153, v153, v175
MM v[24:27],a[0:3],a[248:251],v[24:27]
buffer_load_dwordx4 v221, s[32:35], s95 offen offset:1024 lds
MM v[4:7],a[8:11],a[224:227],v[4:7]
s_add_u32 s95, s88, 59392
v_mul_f32 v154, v154, v175
v_mul_f32 v155, v155, v175
MM v[4:7],a[12:15],a[228:231],v[4:7]
v_mul_f32 v156, v156, v175
v_mul_f32 v157, v157, v175
MM v[12:15],a[12:15],a[236:239],v[12:15]
v_mul_f32 v158, v158, v175
v_mul_f32 v159, v159, v175
MM v[12:15],a[8:11],a[232:235],v[12:15]
buffer_load_dwordx4 v220, s[32:35], s95 offen offset:2048 lds
MM v[20:23],a[8:11],a[240:243],v[20:23]
s_add_u32 s95, s88, 68608
v_mul_f32 v160, v160, v175
v_mul_f32 v161, v161, v175
MM v[20:23],a[12:15],a[244:247],v[20:23]
v_mul_f32 v162, v162, v175
v_mul_f32 v163, v163, v175
MM v[28:31],a[12:15],a[252:255],v[28:31]
v_mul_f32 v164, v164, v175
v_mul_f32 v165, v165, v175
MM v[28:31],a[8:11],a[248:251],v[28:31]
buffer_load_dwordx4 v221, s[32:35], s95 offen offset:3072 lds
v_smfmac_f32_16x16x64_bf16 v[184:187], v[236:239], a[0:7], v234
s_add_u32 s87, s89, s85
s_add_u32 s85, s85, 524288
v_mul_f32 v166, v166, v175
v_smfmac_f32_16x16x64_bf16 v[188:191], v[236:239], a[8:15], v234
s_cmp_ge_u32 s85, 5242880
s_cselect_b32 s85, 0, s85
MM v[32:35],a[16:19],a[224:227],v[32:35]
s_add_u32 m0, s96, 81984
TR a[0:1],v196 offset:16448
TR a[2:3],v196 offset:16960
MM v[32:35],a[20:23],a[228:231],v[32:35]
TR a[4:5],v196 offset:24672
TR a[6:7],v196 offset:25184
v_mul_f32 v167, v167, v175
MM v[40:43],a[20:23],a[236:239],v[40:43]
TR a[8:9],v196 offset:16456
TR a[10:11],v196 offset:16968
MM v[40:43],a[16:19],a[232:235],v[40:43]
TR a[12:13],v196 offset:24680
TR a[14:15],v196 offset:25192
buffer_load_dwordx4 v253, s[16:19], s87 offen offset:0 lds
MM v[48:51],a[16:19],a[240:243],v[48:51]
s_add_u32 m0, s96, 83008
CV v128,v148,v149
CV v129,v150,v151
MM v[48:51],a[20:23],a[244:247],v[48:51]
buffer_load_dwordx4 v254, s[16:19], s87 offen offset:0 lds
MM v[56:59],a[20:23],a[252:255],v[56:59]
s_add_u32 s87, s87, 65536
s_add_u32 m0, s96, 84040
CV v130,v152,v153
MM v[56:59],a[16:19],a[248:251],v[56:59]
CV v131,v154,v155
CV v132,v156,v157
MM v[36:39],a[24:27],a[224:227],v[36:39]
TR a[16:17],v197 offset:16448
TR a[18:19],v197 offset:16960
MM v[36:39],a[28:31],a[228:231],v[36:39]
TR a[20:21],v197 offset:24672
TR a[22:23],v197 offset:25184
CV v133,v158,v159
MM v[44:47],a[28:31],a[236:239],v[44:47]
buffer_load_dwordx4 v253, s[16:19], s87 offen offset:0 lds
MM v[44:47],a[24:27],a[232:235],v[44:47]
s_add_u32 m0, s96, 85064
CV v134,v160,v161
CV v135,v162,v163
MM v[52:55],a[24:27],a[240:243],v[52:55]
CV v136,v164,v165
CV v137,v166,v167
MM v[52:55],a[28:31],a[244:247],v[52:55]
buffer_load_dwordx4 v254, s[16:19], s87 offen offset:0 lds
MM v[60:63],a[28:31],a[252:255],v[60:63]
s_waitcnt vmcnt(16)
RD a[128:131],v222 offset:0
v_xor_b32 v233, 32, v232
MM v[60:63],a[24:27],a[248:251],v[60:63]
s_add_u32 s77, %[slds], 65536
RD a[132:135],v223 offset:0
v_add_u32 v232, s77, v232
MM v[64:67],a[32:35],a[224:227],v[64:67]
TR a[24:25],v197 offset:16456
)ASM"
R"ASM(
TR a[26:27],v197 offset:16968
MM v[64:67],a[36:39],a[228:231],v[64:67]
TR a[28:29],v197 offset:24680
TR a[30:31],v197 offset:25192
MM v[72:75],a[36:39],a[236:239],v[72:75]
RD a[136:139],v222 offset:2048
v_add_u32 v233, s77, v233
MM v[72:75],a[32:35],a[232:235],v[72:75]
RD a[140:143],v223 offset:2048
buffer_store_dwordx4 v[128:131], v168, s[20:23], s82 offen offset:0 sc1
v_lshlrev_b32 v214, 8, v211
MM v[80:83],a[32:35],a[240:243],v[80:83]
RD a[144:147],v222 offset:4096
v_add_u32 v214, %[slds], v214
MM v[80:83],a[36:39],a[244:247],v[80:83]
RD a[148:151],v223 offset:4096
v_xor_b32 v215, v211, v212
MM v[88:91],a[36:39],a[252:255],v[88:91]
s_lshl_b32 s77, %[swave], 2
RD a[152:155],v222 offset:6144
v_xor_b32 v213, s77, v215
MM v[88:91],a[32:35],a[248:251],v[88:91]
s_waitcnt lgkmcnt(15)
RD a[156:159],v223 offset:6144
buffer_store_dwordx4 v[132:135], v168, s[20:23], s82 offen offset:512 sc1
v_xor_b32 v216, 0, v213
MM v[68:71],a[40:43],a[224:227],v[68:71]
TR a[32:33],v198 offset:16448
TR a[34:35],v198 offset:16960
MM v[68:71],a[44:47],a[228:231],v[68:71]
TR a[36:37],v198 offset:24672
TR a[38:39],v198 offset:25184
MM v[76:79],a[44:47],a[236:239],v[76:79]
buffer_store_dwordx2 v[136:137], v181, s[20:23], s82 offen offset:1024 sc1
MM v[76:79],a[40:43],a[232:235],v[76:79]
s_waitcnt lgkmcnt(4)
s_add_u32 s88, s68, s86
s_add_u32 s86, s86, 128
MM v[84:87],a[40:43],a[240:243],v[84:87]
s_cmp_ge_u32 s86, 1280
s_cselect_b32 s86, 0, s86
MM v[84:87],a[44:47],a[244:247],v[84:87]
s_add_u32 m0, s94, 0
s_add_u32 s95, s88, 0
v_lshlrev_b32 v148, 16, v128
MM v[92:95],a[44:47],a[252:255],v[92:95]
buffer_load_dwordx4 v220, s[32:35], s95 offen offset:0 lds
MM v[92:95],a[40:43],a[248:251],v[92:95]
s_add_u32 s95, s88, 9216
v_and_b32 v149, 0xffff0000, v128
v_lshlrev_b32 v176, 16, v138
MM v[96:99],a[48:51],a[224:227],v[96:99]
TR a[40:41],v198 offset:16456
TR a[42:43],v198 offset:16968
MM v[96:99],a[52:55],a[228:231],v[96:99]
TR a[44:45],v198 offset:24680
TR a[46:47],v198 offset:25192
v_and_b32 v177, 0xffff0000, v138
MM v[104:107],a[52:55],a[236:239],v[104:107]
buffer_load_dwordx4 v221, s[32:35], s95 offen offset:1024 lds
MM v[104:107],a[48:51],a[232:235],v[104:107]
s_add_u32 s95, s88, 18432
v_mul_f32 v170, v148, v176
v_mul_f32 v171, v149, v177
MM v[112:115],a[48:51],a[240:243],v[112:115]
buffer_load_dwordx4 v220, s[32:35], s95 offen offset:2048 lds
MM v[112:115],a[52:55],a[244:247],v[112:115]
s_add_u32 s95, s88, 27648
v_lshlrev_b32 v148, 16, v129
v_and_b32 v149, 0xffff0000, v129
MM v[120:123],a[52:55],a[252:255],v[120:123]
v_lshlrev_b32 v176, 16, v139
v_and_b32 v177, 0xffff0000, v139
MM v[120:123],a[48:51],a[248:251],v[120:123]
v_fma_f32 v170, v148, v176, v170
v_fma_f32 v171, v149, v177, v171
MM v[100:103],a[56:59],a[224:227],v[100:103]
TR a[48:49],v199 offset:16448
TR a[50:51],v199 offset:16960
MM v[100:103],a[60:63],a[228:231],v[100:103]
TR a[52:53],v199 offset:24672
TR a[54:55],v199 offset:25184
v_lshlrev_b32 v148, 16, v130
MM v[108:111],a[60:63],a[236:239],v[108:111]
buffer_load_dwordx4 v221, s[32:35], s95 offen offset:3072 lds
MM v[108:111],a[56:59],a[232:235],v[108:111]
s_sub_u32 s88, s88, s63
s_add_u32 m0, s94, 4096
s_add_u32 s95, s88, 40960
MM v[116:119],a[56:59],a[240:243],v[116:119]
buffer_load_dwordx4 v220, s[32:35], s95 offen offset:0 lds
v_and_b32 v149, 0xffff0000, v130
MM v[116:119],a[60:63],a[244:247],v[116:119]
s_add_u32 s95, s88, 50176
v_lshlrev_b32 v176, 16, v140
v_and_b32 v177, 0xffff0000, v140
s_nop 0
LPG2_%=:
s_cbranch_execz LPG6_%=
MM v[124:127],a[60:63],a[252:255],v[124:127]
v_fma_f32 v170, v148, v176, v170
v_fma_f32 v171, v149, v177, v171
MM v[124:127],a[56:59],a[248:251],v[124:127]
v_lshlrev_b32 v148, 16, v131
v_and_b32 v149, 0xffff0000, v131
s_waitcnt vmcnt(20)
s_barrier
MM v[0:3],a[0:3],a[128:131],v[0:3]
TR a[56:57],v199 offset:16456
TR a[58:59],v199 offset:16968
MM v[0:3],a[4:7],a[132:135],v[0:3]
TR a[60:61],v199 offset:24680
TR a[62:63],v199 offset:25192
v_lshlrev_b32 v176, 16, v141
MM v[8:11],a[4:7],a[140:143],v[8:11]
buffer_load_dwordx4 v221, s[32:35], s95 offen offset:1024 lds
MM v[8:11],a[0:3],a[136:139],v[8:11]
s_add_u32 s95, s88, 59392
v_and_b32 v177, 0xffff0000, v141
v_fma_f32 v170, v148, v176, v170
MM v[16:19],a[0:3],a[144:147],v[16:19]
buffer_load_dwordx4 v220, s[32:35], s95 offen offset:2048 lds
MM v[16:19],a[4:7],a[148:151],v[16:19]
s_add_u32 s95, s88, 68608
v_fma_f32 v171, v149, v177, v171
v_lshlrev_b32 v148, 16, v132
MM v[24:27],a[4:7],a[156:159],v[24:27]
v_and_b32 v149, 0xffff0000, v132
v_lshlrev_b32 v176, 16, v142
MM v[24:27],a[0:3],a[152:155],v[24:27]
v_and_b32 v177, 0xffff0000, v142
v_fma_f32 v170, v148, v176, v170
MM v[4:7],a[8:11],a[128:131],v[4:7]
buffer_load_dwordx4 v221, s[32:35], s95 offen offset:3072 lds
MM v[4:7],a[12:15],a[132:135],v[4:7]
s_add_u32 s87, s89, s85
s_add_u32 s85, s85, 524288
v_fma_f32 v171, v149, v177, v171
MM v[12:15],a[12:15],a[140:143],v[12:15]
s_cmp_ge_u32 s85, 5242880
s_cselect_b32 s85, 0, s85
s_add_u32 m0, s96, 98432
MM v[12:15],a[8:11],a[136:139],v[12:15]
v_lshlrev_b32 v148, 16, v133
v_and_b32 v149, 0xffff0000, v133
MM v[20:23],a[8:11],a[144:147],v[20:23]
buffer_load_dwordx4 v253, s[16:19], s87 offen offset:0 lds
MM v[20:23],a[12:15],a[148:151],v[20:23]
s_add_u32 m0, s96, 99456
v_lshlrev_b32 v176, 16, v143
v_and_b32 v177, 0xffff0000, v143
MM v[28:31],a[12:15],a[156:159],v[28:31]
v_fma_f32 v170, v148, v176, v170
v_fma_f32 v171, v149, v177, v171
MM v[28:31],a[8:11],a[152:155],v[28:31]
v_lshlrev_b32 v148, 16, v134
v_and_b32 v149, 0xffff0000, v134
v_smfmac_f32_16x16x64_bf16 v[184:187], v[236:239], a[0:7], v234
v_lshlrev_b32 v176, 16, v144
v_smfmac_f32_16x16x64_bf16 v[188:191], v[236:239], a[8:15], v234
v_and_b32 v177, 0xffff0000, v144
MM v[32:35],a[16:19],a[128:131],v[32:35]
TR a[0:1],v192 offset:0
TR a[2:3],v192 offset:512
buffer_load_dwordx4 v254, s[16:19], s87 offen offset:0 lds
MM v[32:35],a[20:23],a[132:135],v[32:35]
s_add_u32 s87, s87, 65536
TR a[4:5],v192 offset:8224
TR a[6:7],v192 offset:8736
v_fma_f32 v170, v148, v176, v170
MM v[40:43],a[20:23],a[140:143],v[40:43]
s_add_u32 m0, s96, 100488
TR a[8:9],v192 offset:8
TR a[10:11],v192 offset:520
MM v[40:43],a[16:19],a[136:139],v[40:43]
TR a[12:13],v192 offset:8232
TR a[14:15],v192 offset:8744
MM v[48:51],a[16:19],a[144:147],v[48:51]
buffer_load_dwordx4 v253, s[16:19], s87 offen offset:0 lds
v_fma_f32 v171, v149, v177, v171
MM v[48:51],a[20:23],a[148:151],v[48:51]
s_add_u32 m0, s96, 101512
v_lshlrev_b32 v148, 16, v135
v_and_b32 v149, 0xffff0000, v135
MM v[56:59],a[20:23],a[156:159],v[56:59]
buffer_load_dwordx4 v254, s[16:19], s87 offen offset:0 lds
MM v[56:59],a[16:19],a[152:155],v[56:59]
s_waitcnt vmcnt(19)
RD a[160:163],v222 offset:32768
v_lshlrev_b32 v176, 16, v145
MM v[36:39],a[24:27],a[128:131],v[36:39]
TR a[16:17],v193 offset:0
TR a[18:19],v193 offset:512
MM v[36:39],a[28:31],a[132:135],v[36:39]
TR a[20:21],v193 offset:8224
TR a[22:23],v193 offset:8736
v_and_b32 v177, 0xffff0000, v145
MM v[44:47],a[28:31],a[140:143],v[44:47]
RD a[164:167],v223 offset:32768
v_fma_f32 v170, v148, v176, v170
MM v[44:47],a[24:27],a[136:139],v[44:47]
RD a[168:171],v222 offset:34816
v_fma_f32 v171, v149, v177, v171
MM v[52:55],a[24:27],a[144:147],v[52:55]
RD a[172:175],v223 offset:34816
v_lshlrev_b32 v148, 16, v136
MM v[52:55],a[28:31],a[148:151],v[52:55]
RD a[176:179],v222 offset:36864
v_and_b32 v149, 0xffff0000, v136
MM v[60:63],a[28:31],a[156:159],v[60:63]
RD a[180:183],v223 offset:36864
v_lshlrev_b32 v176, 16, v146
MM v[60:63],a[24:27],a[152:155],v[60:63]
s_waitcnt lgkmcnt(15)
RD a[184:187],v222 offset:38912
v_and_b32 v177, 0xffff0000, v146
MM v[64:67],a[32:35],a[128:131],v[64:67]
TR a[24:25],v193 offset:8
TR a[26:27],v193 offset:520
MM v[64:67],a[36:39],a[132:135],v[64:67]
TR a[28:29],v193 offset:8232
TR a[30:31],v193 offset:8744
v_fma_f32 v170, v148, v176, v170
MM v[72:75],a[36:39],a[140:143],v[72:75]
RD a[188:191],v223 offset:38912
v_fma_f32 v171, v149, v177, v171
MM v[72:75],a[32:35],a[136:139],v[72:75]
v_lshlrev_b32 v148, 16, v137
v_and_b32 v149, 0xffff0000, v137
MM v[80:83],a[32:35],a[144:147],v[80:83]
v_lshlrev_b32 v176, 16, v147
v_and_b32 v177, 0xffff0000, v147
MM v[80:83],a[36:39],a[148:151],v[80:83]
v_fma_f32 v170, v148, v176, v170
v_fma_f32 v171, v149, v177, v171
MM v[88:91],a[36:39],a[156:159],v[88:91]
s_waitcnt lgkmcnt(0)
s_add_u32 s88, s68, s86
s_add_u32 s86, s86, 128
MM v[88:91],a[32:35],a[152:155],v[88:91]
s_cmp_ge_u32 s86, 1280
s_cselect_b32 s86, 0, s86
s_add_u32 m0, s94, 32768
v_add_f32 v170, v170, v171
MM v[68:71],a[40:43],a[128:131],v[68:71]
s_add_u32 s95, s88, 0
TR a[32:33],v194 offset:0
TR a[34:35],v194 offset:512
MM v[68:71],a[44:47],a[132:135],v[68:71]
TR a[36:37],v194 offset:8224
TR a[38:39],v194 offset:8736
MM v[76:79],a[44:47],a[140:143],v[76:79]
buffer_load_dwordx4 v220, s[32:35], s95 offen offset:0 lds
v_add_f32_dpp v170, v170, v170 row_ror:8 row_mask:0xf bank_mask:0xf
MM v[76:79],a[40:43],a[136:139],v[76:79]
s_add_u32 s95, s88, 9216
v_add_f32_dpp v170, v170, v170 row_ror:4 row_mask:0xf bank_mask:0xf
v_lshl_add_u32 v224, v216, 4, v214
MM v[84:87],a[40:43],a[144:147],v[84:87]
buffer_load_dwordx4 v221, s[32:35], s95 offen offset:1024 lds
MM v[84:87],a[44:47],a[148:151],v[84:87]
s_add_u32 s95, s88, 18432
v_add_f32_dpp v170, v170, v170 row_ror:2 row_mask:0xf bank_mask:0xf
v_xor_b32 v216, 4, v213
MM v[92:95],a[44:47],a[156:159],v[92:95]
v_add_f32_dpp v170, v170, v170 row_ror:1 row_mask:0xf bank_mask:0xf
v_mov_b32 v173, v170
MM v[92:95],a[40:43],a[152:155],v[92:95]
s_nop 0
v_permlane16_swap_b32 v170, v173
v_add_f32 v170, v170, v173
MM v[96:99],a[48:51],a[128:131],v[96:99]
TR a[40:41],v194 offset:8
TR a[42:43],v194 offset:520
MM v[96:99],a[52:55],a[132:135],v[96:99]
TR a[44:45],v194 offset:8232
TR a[46:47],v194 offset:8744
CV v170,v170,v170
MM v[104:107],a[52:55],a[140:143],v[104:107]
buffer_load_dwordx4 v220, s[32:35], s95 offen offset:2048 lds
MM v[104:107],a[48:51],a[136:139],v[104:107]
s_add_u32 s95, s88, 27648
v_ashrrev_i32 v171, 31, v170
v_or_b32 v171, 0x80000000, v171
MM v[112:115],a[48:51],a[144:147],v[112:115]
buffer_load_dwordx4 v221, s[32:35], s95 offen offset:3072 lds
MM v[112:115],a[52:55],a[148:151],v[112:115]
s_sub_u32 s88, s88, s63
s_add_u32 m0, s94, 36864
s_add_u32 s95, s88, 40960
v_xor_b32 v170, v170, v171
MM v[120:123],a[52:55],a[156:159],v[120:123]
v_and_b32 v170, 0xffff0000, v170
v_sub_u32 v171, s79, v180
MM v[120:123],a[48:51],a[152:155],v[120:123]
v_or_b32 v170, v170, v171
v_lshlrev_b32 v173, 2, v180
MM v[100:103],a[56:59],a[128:131],v[100:103]
TR a[48:49],v195 offset:0
TR a[50:51],v195 offset:512
MM v[100:103],a[60:63],a[132:135],v[100:103]
TR a[52:53],v195 offset:8224
TR a[54:55],v195 offset:8736
MM v[108:111],a[60:63],a[140:143],v[108:111]
buffer_load_dwordx4 v220, s[32:35], s95 offen offset:0 lds
s_mov_b32 exec_lo, 1
s_mov_b32 exec_hi, 1
buffer_store_dword v170, v173, s[20:23], s78 offen offset:0 sc1
s_mov_b64 exec, -1
MM v[108:111],a[56:59],a[136:139],v[108:111]
s_add_u32 s95, s88, 50176
v_mov_b32 v171, s69
v_lshl_add_u32 v225, v216, 4, v214
MM v[116:119],a[56:59],a[144:147],v[116:119]
buffer_load_dwordx4 v221, s[32:35], s95 offen offset:1024 lds
MM v[116:119],a[60:63],a[148:151],v[116:119]
s_add_u32 s95, s88, 59392
v_xor_b32 v216, 8, v213
v_lshl_add_u32 v226, v216, 4, v214
MM v[124:127],a[60:63],a[156:159],v[124:127]
v_xor_b32 v216, 12, v213
v_lshl_add_u32 v227, v216, 4, v214
MM v[124:127],a[56:59],a[152:155],v[124:127]
v_lshlrev_b32 v214, 8, v212
v_xor_b32 v216, 0, v215
s_waitcnt vmcnt(22)
s_barrier
MM v[0:3],a[0:3],a[160:163],v[0:3]
TR a[56:57],v195 offset:8
TR a[58:59],v195 offset:520
MM v[0:3],a[4:7],a[164:167],v[0:3]
TR a[60:61],v195 offset:8232
TR a[62:63],v195 offset:8744
MM v[8:11],a[4:7],a[172:175],v[8:11]
buffer_load_dwordx4 v220, s[32:35], s95 offen offset:2048 lds
MM v[8:11],a[0:3],a[168:171],v[8:11]
s_add_u32 s95, s88, 68608
v_lshl_add_u32 v228, v216, 4, v214
v_xor_b32 v216, 4, v215
MM v[16:19],a[0:3],a[176:179],v[16:19]
buffer_load_dwordx4 v221, s[32:35], s95 offen offset:3072 lds
MM v[16:19],a[4:7],a[180:183],v[16:19]
s_add_u32 s87, s89, s85
)ASM"
R"ASM(
s_add_u32 s85, s85, 524288
v_lshl_add_u32 v229, v216, 4, v214
MM v[24:27],a[4:7],a[188:191],v[24:27]
s_cmp_ge_u32 s85, 5242880
s_cselect_b32 s85, 0, s85
s_add_u32 m0, s96, 114880
MM v[24:27],a[0:3],a[184:187],v[24:27]
v_xor_b32 v216, 8, v215
v_lshl_add_u32 v230, v216, 4, v214
MM v[4:7],a[8:11],a[160:163],v[4:7]
buffer_load_dwordx4 v253, s[16:19], s87 offen offset:0 lds
MM v[4:7],a[12:15],a[164:167],v[4:7]
s_add_u32 m0, s96, 115904
v_xor_b32 v216, 12, v215
v_lshl_add_u32 v231, v216, 4, v214
MM v[12:15],a[12:15],a[172:175],v[12:15]
s_lshl_b32 s90, s49, 19
s_add_u32 s8, s8, s90
s_addc_u32 s9, s9, 0
MM v[12:15],a[8:11],a[168:171],v[12:15]
s_mov_b32 s10, 524288
s_mov_b32 s11, 131072
MM v[20:23],a[8:11],a[176:179],v[20:23]
buffer_load_dwordx4 v254, s[16:19], s87 offen offset:0 lds
MM v[20:23],a[12:15],a[180:183],v[20:23]
s_add_u32 s87, s87, 65536
s_add_u32 s12, s12, s90
s_addc_u32 s13, s13, 0
MM v[28:31],a[12:15],a[188:191],v[28:31]
s_add_u32 m0, s96, 116936
s_mov_b32 s14, 525312
s_mov_b32 s15, 131072
MM v[28:31],a[8:11],a[184:187],v[28:31]
s_sub_u32 s12, s12, 1024
s_subb_u32 s13, s13, 0
s_lshr_b32 s90, s49, 3
v_smfmac_f32_16x16x64_bf16 v[184:187], v[236:239], a[0:7], v234
s_mul_i32 s90, s90, 1310720
s_mul_i32 s77, s84, 320
v_smfmac_f32_16x16x64_bf16 v[188:191], v[236:239], a[8:15], v234
s_add_u32 s90, s90, s77
MM v[32:35],a[16:19],a[160:163],v[32:35]
TR a[0:1],v192 offset:16448
TR a[2:3],v192 offset:16960
buffer_load_dwordx4 v253, s[16:19], s87 offen offset:0 lds
MM v[32:35],a[20:23],a[164:167],v[32:35]
s_add_u32 m0, s96, 117960
TR a[4:5],v192 offset:24672
TR a[6:7],v192 offset:25184
MM v[40:43],a[20:23],a[172:175],v[40:43]
TR a[8:9],v192 offset:16456
TR a[10:11],v192 offset:16968
MM v[40:43],a[16:19],a[168:171],v[40:43]
TR a[12:13],v192 offset:24680
TR a[14:15],v192 offset:25192
MM v[48:51],a[16:19],a[176:179],v[48:51]
buffer_load_dwordx4 v254, s[16:19], s87 offen offset:0 lds
MM v[48:51],a[20:23],a[180:183],v[48:51]
s_waitcnt vmcnt(17)
s_add_u32 s24, s24, s90
s_addc_u32 s25, s25, 0
MM v[56:59],a[20:23],a[188:191],v[56:59]
s_sub_u32 s26, 5242880, s90
RD a[192:195],v222 offset:0
s_mov_b32 s27, 131072
MM v[56:59],a[16:19],a[184:187],v[56:59]
s_mov_b32 s38, 2211840
RD a[196:199],v223 offset:0
s_mov_b32 s39, 131072
MM v[36:39],a[24:27],a[160:163],v[36:39]
s_mov_b32 s71, 0x4e800000
TR a[16:17],v193 offset:16448
TR a[18:19],v193 offset:16960
MM v[36:39],a[28:31],a[164:167],v[36:39]
s_mov_b32 s72, 0x3e0293ee
TR a[20:21],v193 offset:24672
TR a[22:23],v193 offset:25184
MM v[44:47],a[28:31],a[172:175],v[44:47]
s_lshl_b32 s64, %[swave], 12
RD a[200:203],v222 offset:2048
MM v[44:47],a[24:27],a[168:171],v[44:47]
RD a[204:207],v223 offset:2048
MM v[52:55],a[24:27],a[176:179],v[52:55]
RD a[208:211],v222 offset:4096
MM v[52:55],a[28:31],a[180:183],v[52:55]
RD a[212:215],v223 offset:4096
MM v[60:63],a[28:31],a[188:191],v[60:63]
RD a[216:219],v222 offset:6144
MM v[60:63],a[24:27],a[184:187],v[60:63]
s_waitcnt lgkmcnt(15)
RD a[220:223],v223 offset:6144
MM v[64:67],a[32:35],a[160:163],v[64:67]
TR a[24:25],v193 offset:16456
TR a[26:27],v193 offset:16968
MM v[64:67],a[36:39],a[164:167],v[64:67]
TR a[28:29],v193 offset:24680
TR a[30:31],v193 offset:25192
MM v[72:75],a[36:39],a[172:175],v[72:75]
MM v[72:75],a[32:35],a[168:171],v[72:75]
s_waitcnt lgkmcnt(4)
s_add_u32 s88, s68, s86
s_add_u32 s86, s86, 128
MM v[80:83],a[32:35],a[176:179],v[80:83]
s_cmp_ge_u32 s86, 1280
s_cselect_b32 s86, 0, s86
s_add_u32 m0, s94, 0
MM v[80:83],a[36:39],a[180:183],v[80:83]
s_add_u32 s95, s88, 0
buffer_load_dwordx4 v220, s[32:35], s95 offen offset:0 lds
MM v[88:91],a[36:39],a[188:191],v[88:91]
s_add_u32 s95, s88, 9216
MM v[88:91],a[32:35],a[184:187],v[88:91]
buffer_load_dwordx4 v221, s[32:35], s95 offen offset:1024 lds
MM v[68:71],a[40:43],a[160:163],v[68:71]
s_add_u32 s95, s88, 18432
TR a[32:33],v194 offset:16448
TR a[34:35],v194 offset:16960
MM v[68:71],a[44:47],a[164:167],v[68:71]
TR a[36:37],v194 offset:24672
TR a[38:39],v194 offset:25184
MM v[76:79],a[44:47],a[172:175],v[76:79]
MM v[76:79],a[40:43],a[168:171],v[76:79]
buffer_load_dwordx4 v220, s[32:35], s95 offen offset:2048 lds
MM v[84:87],a[40:43],a[176:179],v[84:87]
s_add_u32 s95, s88, 27648
MM v[84:87],a[44:47],a[180:183],v[84:87]
MM v[92:95],a[44:47],a[188:191],v[92:95]
MM v[92:95],a[40:43],a[184:187],v[92:95]
buffer_load_dwordx4 v221, s[32:35], s95 offen offset:3072 lds
MM v[96:99],a[48:51],a[160:163],v[96:99]
s_sub_u32 s88, s88, s63
TR a[40:41],v194 offset:16456
TR a[42:43],v194 offset:16968
MM v[96:99],a[52:55],a[164:167],v[96:99]
s_add_u32 m0, s94, 4096
TR a[44:45],v194 offset:24680
TR a[46:47],v194 offset:25192
MM v[104:107],a[52:55],a[172:175],v[104:107]
s_add_u32 s95, s88, 40960
MM v[104:107],a[48:51],a[168:171],v[104:107]
buffer_load_dwordx4 v220, s[32:35], s95 offen offset:0 lds
MM v[112:115],a[48:51],a[176:179],v[112:115]
s_add_u32 s95, s88, 50176
MM v[112:115],a[52:55],a[180:183],v[112:115]
MM v[120:123],a[52:55],a[188:191],v[120:123]
MM v[120:123],a[48:51],a[184:187],v[120:123]
buffer_load_dwordx4 v221, s[32:35], s95 offen offset:1024 lds
MM v[100:103],a[56:59],a[160:163],v[100:103]
s_add_u32 s95, s88, 59392
TR a[48:49],v195 offset:16448
TR a[50:51],v195 offset:16960
MM v[100:103],a[60:63],a[164:167],v[100:103]
TR a[52:53],v195 offset:24672
TR a[54:55],v195 offset:25184
MM v[108:111],a[60:63],a[172:175],v[108:111]
MM v[108:111],a[56:59],a[168:171],v[108:111]
buffer_load_dwordx4 v220, s[32:35], s95 offen offset:2048 lds
MM v[116:119],a[56:59],a[176:179],v[116:119]
s_add_u32 s95, s88, 68608
MM v[116:119],a[60:63],a[180:183],v[116:119]
MM v[124:127],a[60:63],a[188:191],v[124:127]
MM v[124:127],a[56:59],a[184:187],v[124:127]
buffer_load_dwordx4 v221, s[32:35], s95 offen offset:3072 lds
s_waitcnt vmcnt(21)
s_barrier
MM v[0:3],a[0:3],a[192:195],v[0:3]
s_add_u32 s87, s89, s85
TR a[56:57],v195 offset:16456
TR a[58:59],v195 offset:16968
MM v[0:3],a[4:7],a[196:199],v[0:3]
s_add_u32 s85, s85, 524288
TR a[60:61],v195 offset:24680
TR a[62:63],v195 offset:25192
MM v[8:11],a[4:7],a[204:207],v[8:11]
s_cmp_ge_u32 s85, 5242880
s_cselect_b32 s85, 0, s85
s_add_u32 m0, s96, 131328
v_and_b32 v208, 240, v248
MM v[8:11],a[0:3],a[200:203],v[8:11]
s_lshl_b32 s62, s62, 8
buffer_load_dwordx4 v253, s[16:19], s87 offen offset:0 lds
MM v[16:19],a[0:3],a[208:211],v[16:19]
s_add_u32 m0, s96, 132352
s_add_u32 s93, s93, 36864
s_add_u32 s62, s62, s93
MM v[16:19],a[4:7],a[212:215],v[16:19]
s_xor_b32 s93, s62, 256
MM v[24:27],a[4:7],a[220:223],v[24:27]
MM v[24:27],a[0:3],a[216:219],v[24:27]
buffer_load_dwordx4 v254, s[16:19], s87 offen offset:0 lds
MM v[4:7],a[8:11],a[192:195],v[4:7]
s_add_u32 s87, s87, 65536
s_add_u32 m0, s96, 133384
MM v[4:7],a[12:15],a[196:199],v[4:7]
MM v[12:15],a[12:15],a[204:207],v[12:15]
MM v[12:15],a[8:11],a[200:203],v[12:15]
buffer_load_dwordx4 v253, s[16:19], s87 offen offset:0 lds
MM v[20:23],a[8:11],a[208:211],v[20:23]
s_add_u32 m0, s96, 134408
MM v[20:23],a[12:15],a[212:215],v[20:23]
MM v[28:31],a[12:15],a[220:223],v[28:31]
MM v[28:31],a[8:11],a[216:219],v[28:31]
buffer_load_dwordx4 v254, s[16:19], s87 offen offset:0 lds
v_smfmac_f32_16x16x64_bf16 v[184:187], v[236:239], a[0:7], v234
s_waitcnt vmcnt(16)
RD a[224:227],v222 offset:32768
v_smfmac_f32_16x16x64_bf16 v[188:191], v[236:239], a[8:15], v234
RD a[228:231],v223 offset:32768
MM v[32:35],a[16:19],a[192:195],v[32:35]
TR a[0:1],v192 offset:32896
TR a[2:3],v192 offset:33408
MM v[32:35],a[20:23],a[196:199],v[32:35]
TR a[4:5],v192 offset:41120
TR a[6:7],v192 offset:41632
MM v[40:43],a[20:23],a[204:207],v[40:43]
TR a[8:9],v192 offset:32904
TR a[10:11],v192 offset:33416
RD a[232:235],v222 offset:34816
MM v[40:43],a[16:19],a[200:203],v[40:43]
TR a[12:13],v192 offset:41128
TR a[14:15],v192 offset:41640
buffer_load_dwordx4 v[200:203], v208, s[20:23], s62 offen offset:0 sc1
MM v[48:51],a[16:19],a[208:211],v[48:51]
RD a[236:239],v223 offset:34816
MM v[48:51],a[20:23],a[212:215],v[48:51]
buffer_load_dwordx4 v[204:207], v208, s[20:23], s93 offen offset:0 sc1
MM v[56:59],a[20:23],a[220:223],v[56:59]
RD a[240:243],v222 offset:36864
MM v[56:59],a[16:19],a[216:219],v[56:59]
s_waitcnt lgkmcnt(15)
RD a[244:247],v223 offset:36864
MM v[36:39],a[24:27],a[192:195],v[36:39]
TR a[16:17],v193 offset:32896
TR a[18:19],v193 offset:33408
MM v[36:39],a[28:31],a[196:199],v[36:39]
TR a[20:21],v193 offset:41120
TR a[22:23],v193 offset:41632
MM v[44:47],a[28:31],a[204:207],v[44:47]
RD a[248:251],v222 offset:38912
MM v[44:47],a[24:27],a[200:203],v[44:47]
RD a[252:255],v223 offset:38912
MM v[52:55],a[24:27],a[208:211],v[52:55]
MM v[52:55],a[28:31],a[212:215],v[52:55]
MM v[60:63],a[28:31],a[220:223],v[60:63]
MM v[60:63],a[24:27],a[216:219],v[60:63]
s_waitcnt lgkmcnt(0)
s_add_u32 s88, s68, s86
s_add_u32 s86, s86, 128
MM v[64:67],a[32:35],a[192:195],v[64:67]
TR a[24:25],v193 offset:32904
TR a[26:27],v193 offset:33416
MM v[64:67],a[36:39],a[196:199],v[64:67]
TR a[28:29],v193 offset:41128
TR a[30:31],v193 offset:41640
s_cmp_ge_u32 s86, 1280
s_cselect_b32 s86, 0, s86
MM v[72:75],a[36:39],a[204:207],v[72:75]
s_add_u32 m0, s94, 32768
s_add_u32 s95, s88, 0
MM v[72:75],a[32:35],a[200:203],v[72:75]
buffer_load_dwordx4 v220, s[32:35], s95 offen offset:0 lds
MM v[80:83],a[32:35],a[208:211],v[80:83]
s_add_u32 s95, s88, 9216
MM v[80:83],a[36:39],a[212:215],v[80:83]
buffer_load_dwordx4 v221, s[32:35], s95 offen offset:1024 lds
MM v[88:91],a[36:39],a[220:223],v[88:91]
s_add_u32 s95, s88, 18432
MM v[88:91],a[32:35],a[216:219],v[88:91]
MM v[68:71],a[40:43],a[192:195],v[68:71]
TR a[32:33],v194 offset:32896
TR a[34:35],v194 offset:33408
MM v[68:71],a[44:47],a[196:199],v[68:71]
TR a[36:37],v194 offset:41120
TR a[38:39],v194 offset:41632
MM v[76:79],a[44:47],a[204:207],v[76:79]
buffer_load_dwordx4 v220, s[32:35], s95 offen offset:2048 lds
MM v[76:79],a[40:43],a[200:203],v[76:79]
s_add_u32 s95, s88, 27648
MM v[84:87],a[40:43],a[208:211],v[84:87]
MM v[84:87],a[44:47],a[212:215],v[84:87]
buffer_load_dwordx4 v221, s[32:35], s95 offen offset:3072 lds
MM v[92:95],a[44:47],a[220:223],v[92:95]
s_waitcnt vmcnt(10)
RD a[128:131],v222 offset:0
MM v[92:95],a[40:43],a[216:219],v[92:95]
RD a[132:135],v223 offset:0
MM v[96:99],a[48:51],a[192:195],v[96:99]
TR a[40:41],v194 offset:32904
TR a[42:43],v194 offset:33416
MM v[96:99],a[52:55],a[196:199],v[96:99]
TR a[44:45],v194 offset:41128
TR a[46:47],v194 offset:41640
MM v[104:107],a[52:55],a[204:207],v[104:107]
RD a[136:139],v222 offset:2048
MM v[104:107],a[48:51],a[200:203],v[104:107]
RD a[140:143],v223 offset:2048
MM v[112:115],a[48:51],a[208:211],v[112:115]
RD a[144:147],v222 offset:4096
MM v[112:115],a[52:55],a[212:215],v[112:115]
RD a[148:151],v223 offset:4096
MM v[120:123],a[52:55],a[220:223],v[120:123]
RD a[152:155],v222 offset:6144
MM v[120:123],a[48:51],a[216:219],v[120:123]
RD a[156:159],v223 offset:6144
MM v[100:103],a[56:59],a[192:195],v[100:103]
s_sub_u32 s88, s88, s63
TR a[48:49],v195 offset:32896
TR a[50:51],v195 offset:33408
MM v[100:103],a[60:63],a[196:199],v[100:103]
s_nop 0
LPG3_%=:
s_cbranch_execz LPG7_%=
s_add_u32 m0, s94, 36864
TR a[52:53],v195 offset:41120
TR a[54:55],v195 offset:41632
MM v[108:111],a[60:63],a[204:207],v[108:111]
s_add_u32 s95, s88, 40960
buffer_load_dwordx4 v220, s[32:35], s95 offen offset:0 lds
MM v[108:111],a[56:59],a[200:203],v[108:111]
s_add_u32 s95, s88, 50176
MM v[116:119],a[56:59],a[208:211],v[116:119]
buffer_load_dwordx4 v221, s[32:35], s95 offen offset:1024 lds
MM v[116:119],a[60:63],a[212:215],v[116:119]
s_add_u32 s95, s88, 59392
MM v[124:127],a[60:63],a[220:223],v[124:127]
MM v[124:127],a[56:59],a[216:219],v[124:127]
buffer_load_dwordx4 v220, s[32:35], s95 offen offset:2048 lds
s_add_u32 s95, s88, 68608
buffer_load_dwordx4 v221, s[32:35], s95 offen offset:3072 lds
s_waitcnt lgkmcnt(4)
s_waitcnt vmcnt(10)
s_barrier
MM v[0:3],a[0:3],a[224:227],v[0:3]
s_add_u32 s77, s64, 0
TR a[56:57],v195 offset:32904
TR a[58:59],v195 offset:33416
MM v[0:3],a[4:7],a[228:231],v[0:3]
s_add_u32 m0, s65, 0
TR a[60:61],v195 offset:41128
TR a[62:63],v195 offset:41640
MM v[8:11],a[4:7],a[236:239],v[8:11]
)ASM"
R"ASM(
s_waitcnt vmcnt(8)
TR a[64:65],v196 offset:0
TR a[66:67],v196 offset:512
MM v[8:11],a[0:3],a[232:235],v[8:11]
s_lshl_b32 s76, %[swave], 7
buffer_load_dwordx4 v228, s[8:11], s77 offen offset:0 lds
v_lshrrev_b32 v253, 8, v248
v_lshlrev_b32 v253, 5, v253
MM v[16:19],a[0:3],a[240:243],v[16:19]
s_add_u32 s76, s76, %[slds]
TR a[68:69],v196 offset:8224
TR a[70:71],v196 offset:8736
v_xor_b32 v201, s69, v201
MM v[16:19],a[4:7],a[244:247],v[16:19]
s_mov_b64 exec, 1
buffer_store_dword v171, off, s[20:23], s80 sc1
s_mov_b64 exec, -1
v_add_u32 v253, s76, v253
v_add_u32 v253, 151552, v253
MM v[24:27],a[4:7],a[252:255],v[24:27]
TR a[72:73],v196 offset:16448
TR a[74:75],v196 offset:16960
MM v[24:27],a[0:3],a[248:251],v[24:27]
TR a[76:77],v196 offset:24672
TR a[78:79],v196 offset:25184
buffer_load_dwordx4 v229, s[8:11], s77 offen offset:1024 lds
v_xor_b32 v203, s69, v203
MM v[4:7],a[8:11],a[224:227],v[4:7]
TR a[80:81],v196 offset:8
TR a[82:83],v196 offset:520
MM v[4:7],a[12:15],a[228:231],v[4:7]
TR a[84:85],v196 offset:8232
TR a[86:87],v196 offset:8744
MM v[12:15],a[12:15],a[236:239],v[12:15]
TR a[88:89],v196 offset:16456
TR a[90:91],v196 offset:16968
v_xor_b32 v205, s69, v205
MM v[12:15],a[8:11],a[232:235],v[12:15]
TR a[92:93],v196 offset:24680
TR a[94:95],v196 offset:25192
buffer_load_dwordx4 v230, s[8:11], s77 offen offset:2048 lds
MM v[20:23],a[8:11],a[240:243],v[20:23]
TR a[96:97],v197 offset:0
TR a[98:99],v197 offset:512
MM v[20:23],a[12:15],a[244:247],v[20:23]
TR a[100:101],v197 offset:8224
TR a[102:103],v197 offset:8736
v_xor_b32 v207, s69, v207
MM v[28:31],a[12:15],a[252:255],v[28:31]
TR a[104:105],v197 offset:16448
TR a[106:107],v197 offset:16960
MM v[28:31],a[8:11],a[248:251],v[28:31]
TR a[108:109],v197 offset:24672
TR a[110:111],v197 offset:25184
buffer_load_dwordx4 v231, s[8:11], s77 offen offset:3072 lds
v_smfmac_f32_16x16x64_bf16 v[184:187], v[236:239], a[0:7], v234
s_add_u32 s77, s64, 16384
TR a[112:113],v197 offset:8
v_or3_b32 v209, v201, v203, v205
v_smfmac_f32_16x16x64_bf16 v[188:191], v[236:239], a[8:15], v234
s_add_u32 m0, s65, 16384
TR a[114:115],v197 offset:520
MM v[32:35],a[16:19],a[224:227],v[32:35]
TR a[116:117],v197 offset:8232
TR a[118:119],v197 offset:8744
MM v[32:35],a[20:23],a[228:231],v[32:35]
TR a[120:121],v197 offset:16456
TR a[122:123],v197 offset:16968
v_or_b32 v209, v209, v207
MM v[40:43],a[20:23],a[236:239],v[40:43]
TR a[124:125],v197 offset:24680
TR a[126:127],v197 offset:25192
MM v[40:43],a[16:19],a[232:235],v[40:43]
TR a[0:1],v198 offset:0
TR a[2:3],v198 offset:512
buffer_load_dwordx4 v228, s[8:11], s77 offen offset:0 lds
MM v[48:51],a[16:19],a[240:243],v[48:51]
TR a[4:5],v198 offset:8224
TR a[6:7],v198 offset:8736
v_cmp_ne_u32 vcc, 0, v209
s_cbranch_vccnz LRG1o_%=
LRG1ob_%=:
MM v[48:51],a[20:23],a[244:247],v[48:51]
TR a[8:9],v198 offset:16448
TR a[10:11],v198 offset:16960
MM v[56:59],a[20:23],a[252:255],v[56:59]
TR a[12:13],v198 offset:24672
TR a[14:15],v198 offset:25184
MM v[56:59],a[16:19],a[248:251],v[56:59]
v_lshlrev_b32 v240, 16, v200
v_and_b32 v200, 0xffff0000, v200
MM v[36:39],a[24:27],a[224:227],v[36:39]
v_rcp_f32 v244, v200
MM v[36:39],a[28:31],a[228:231],v[36:39]
v_xor_b32 v240, 0x80000000, v240
v_mul_f32 v244, s72, v244
MM v[44:47],a[28:31],a[236:239],v[44:47]
v_lshlrev_b32 v241, 16, v202
v_and_b32 v202, 0xffff0000, v202
MM v[44:47],a[24:27],a[232:235],v[44:47]
buffer_load_dwordx4 v229, s[8:11], s77 offen offset:1024 lds
MM v[52:55],a[24:27],a[240:243],v[52:55]
v_rcp_f32 v245, v202
MM v[52:55],a[28:31],a[244:247],v[52:55]
v_xor_b32 v241, 0x80000000, v241
v_mul_f32 v245, s72, v245
MM v[60:63],a[28:31],a[252:255],v[60:63]
v_lshlrev_b32 v242, 16, v204
v_and_b32 v204, 0xffff0000, v204
MM v[60:63],a[24:27],a[248:251],v[60:63]
v_rcp_f32 v246, v204
MM v[64:67],a[32:35],a[224:227],v[64:67]
TR a[16:17],v198 offset:8
TR a[18:19],v198 offset:520
MM v[64:67],a[36:39],a[228:231],v[64:67]
TR a[20:21],v198 offset:8232
TR a[22:23],v198 offset:8744
v_xor_b32 v242, 0x80000000, v242
MM v[72:75],a[36:39],a[236:239],v[72:75]
TR a[24:25],v198 offset:16456
TR a[26:27],v198 offset:16968
MM v[72:75],a[32:35],a[232:235],v[72:75]
TR a[28:29],v198 offset:24680
TR a[30:31],v198 offset:25192
MM v[80:83],a[32:35],a[240:243],v[80:83]
v_mul_f32 v246, s72, v246
v_lshlrev_b32 v243, 16, v206
MM v[80:83],a[36:39],a[244:247],v[80:83]
buffer_load_dwordx4 v230, s[8:11], s77 offen offset:2048 lds
MM v[88:91],a[36:39],a[252:255],v[88:91]
v_and_b32 v206, 0xffff0000, v206
MM v[88:91],a[32:35],a[248:251],v[88:91]
s_waitcnt lgkmcnt(15)
v_rcp_f32 v247, v206
v_smfmac_f32_16x16x64_bf16 v[184:187], v[236:239], a[64:71], v234
v_xor_b32 v243, 0x80000000, v243
v_smfmac_f32_16x16x64_bf16 v[184:187], v[236:239], a[72:79], v234
v_mul_f32 v247, s72, v247
v_smfmac_f32_16x16x64_bf16 v[188:191], v[236:239], a[80:87], v234
v_smfmac_f32_16x16x64_bf16 v[188:191], v[236:239], a[88:95], v234
MM v[68:71],a[40:43],a[224:227],v[68:71]
v_bfe_u32 v183, v248, 6, 2
v_lshlrev_b32 v183, 5, v183
MM v[68:71],a[44:47],a[228:231],v[68:71]
v_bfe_u32 v182, v248, 4, 2
v_lshl_add_u32 v183, v182, 2, v183
MM v[76:79],a[44:47],a[236:239],v[76:79]
v_add_u32 v183, s76, v183
v_add_u32 v183, 151552, v183
MM v[76:79],a[40:43],a[232:235],v[76:79]
buffer_load_dwordx4 v231, s[8:11], s77 offen offset:3072 lds
MM v[84:87],a[40:43],a[240:243],v[84:87]
s_waitcnt vmcnt(9)
RD a[160:163],v222 offset:32768
MM v[84:87],a[44:47],a[244:247],v[84:87]
RD a[164:167],v223 offset:32768
MM v[92:95],a[44:47],a[252:255],v[92:95]
RD a[168:171],v222 offset:34816
MM v[92:95],a[40:43],a[248:251],v[92:95]
v_add_f32 v184, v184, v185
v_add_f32 v188, v188, v189
MM v[96:99],a[48:51],a[224:227],v[96:99]
TR a[32:33],v199 offset:0
TR a[34:35],v199 offset:512
MM v[96:99],a[52:55],a[228:231],v[96:99]
RD a[172:175],v223 offset:34816
ds_write_b32 v183, v184 offset:0
ds_write_b32 v183, v188 offset:16
MM v[104:107],a[52:55],a[236:239],v[104:107]
TR a[36:37],v199 offset:8224
TR a[38:39],v199 offset:8736
MM v[104:107],a[48:51],a[232:235],v[104:107]
TR a[40:41],v199 offset:16448
TR a[42:43],v199 offset:16960
MM v[112:115],a[48:51],a[240:243],v[112:115]
TR a[44:45],v199 offset:24672
TR a[46:47],v199 offset:25184
RD a[176:179],v222 offset:36864
MM v[112:115],a[52:55],a[244:247],v[112:115]
RD a[180:183],v223 offset:36864
MM v[120:123],a[52:55],a[252:255],v[120:123]
RD a[184:187],v222 offset:38912
MM v[120:123],a[48:51],a[248:251],v[120:123]
RD a[188:191],v223 offset:38912
MM v[100:103],a[56:59],a[224:227],v[100:103]
s_add_u32 s77, s64, 0
s_add_u32 s81, s77, 896
s_add_u32 m0, s65, 65536
MM v[100:103],a[60:63],a[228:231],v[100:103]
buffer_load_dwordx4 v235, s[12:15], s81 offen offset:0 lds
MM v[108:111],a[60:63],a[236:239],v[108:111]
MM v[108:111],a[56:59],a[232:235],v[108:111]
MM v[116:119],a[56:59],a[240:243],v[116:119]
MM v[116:119],a[60:63],a[244:247],v[116:119]
MM v[124:127],a[60:63],a[252:255],v[124:127]
MM v[124:127],a[56:59],a[248:251],v[124:127]
buffer_load_dwordx4 v235, s[12:15], s77 offen offset:1024 lds
s_waitcnt lgkmcnt(0)
s_barrier
MM v[0:3],a[64:67],a[128:131],v[0:3]
s_memrealtime s[52:53]
TR a[48:49],v199 offset:8
TR a[50:51],v199 offset:520
MM v[0:3],a[68:71],a[132:135],v[0:3]
s_memtime s[54:55]
TR a[52:53],v199 offset:8232
TR a[54:55],v199 offset:8744
MM v[0:3],a[72:75],a[160:163],v[0:3]
TR a[56:57],v199 offset:16456
TR a[58:59],v199 offset:16968
MM v[0:3],a[76:79],a[164:167],v[0:3]
TR a[60:61],v199 offset:24680
TR a[62:63],v199 offset:25192
MM v[8:11],a[76:79],a[172:175],v[8:11]
v_xor_b32 v151, 0, v253
v_xor_b32 v143, 0, v253
MM v[8:11],a[72:75],a[168:171],v[8:11]
RD v[144:147],v151 offset:0
RD v[148:151],v151 offset:16
MM v[8:11],a[68:71],a[140:143],v[8:11]
RD v[136:139],v143 offset:0
RD v[140:143],v143 offset:16
MM v[8:11],a[64:67],a[136:139],v[8:11]
MM v[4:7],a[80:83],a[128:131],v[4:7]
MM v[4:7],a[84:87],a[132:135],v[4:7]
s_waitcnt lgkmcnt(2)
buffer_load_dwordx4 v235, s[12:15], s81 offen offset:2048 lds
MM v[4:7],a[88:91],a[160:163],v[4:7]
s_waitcnt lgkmcnt(0)
MM v[4:7],a[92:95],a[164:167],v[4:7]
MM v[12:15],a[92:95],a[172:175],v[12:15]
MM v[12:15],a[88:91],a[168:171],v[12:15]
MM v[12:15],a[84:87],a[140:143],v[12:15]
MM v[12:15],a[80:83],a[136:139],v[12:15]
MM v[32:35],a[96:99],a[128:131],v[32:35]
MM v[32:35],a[100:103],a[132:135],v[32:35]
MM v[32:35],a[104:107],a[160:163],v[32:35]
MM v[32:35],a[108:111],a[164:167],v[32:35]
buffer_load_dwordx4 v235, s[12:15], s77 offen offset:3072 lds
v_fmac_f32 v0, v240, v144
v_fmac_f32 v1, v240, v145
v_fmac_f32 v2, v240, v146
MM v[40:43],a[108:111],a[172:175],v[40:43]
s_add_u32 s77, s64, 32768
s_add_u32 m0, s65, 32768
v_fmac_f32 v3, v240, v147
v_fmac_f32 v4, v240, v148
MM v[40:43],a[104:107],a[168:171],v[40:43]
v_fmac_f32 v5, v240, v149
v_fmac_f32 v6, v240, v150
MM v[40:43],a[100:103],a[140:143],v[40:43]
v_fmac_f32 v7, v240, v151
v_mul_f32 v0, v244, v0
v_mul_f32 v1, v244, v1
MM v[40:43],a[96:99],a[136:139],v[40:43]
v_mul_f32 v2, v244, v2
v_mul_f32 v3, v244, v3
MM v[36:39],a[112:115],a[128:131],v[36:39]
v_mul_f32 v4, v244, v4
v_mul_f32 v5, v244, v5
MM v[36:39],a[116:119],a[132:135],v[36:39]
v_mul_f32 v6, v244, v6
v_mul_f32 v7, v244, v7
CV v160,v0,v1
MM v[36:39],a[120:123],a[160:163],v[36:39]
CV v161,v2,v3
CV v162,v4,v5
MM v[36:39],a[124:127],a[164:167],v[36:39]
CV v163,v6,v7
v_fmac_f32 v8, v241, v144
MM v[44:47],a[124:127],a[172:175],v[44:47]
v_fmac_f32 v9, v241, v145
v_fmac_f32 v10, v241, v146
v_fmac_f32 v11, v241, v147
MM v[44:47],a[120:123],a[168:171],v[44:47]
buffer_load_dwordx4 v228, s[8:11], s77 offen offset:0 lds
v_fmac_f32 v12, v241, v148
v_fmac_f32 v13, v241, v149
MM v[44:47],a[116:119],a[140:143],v[44:47]
v_fmac_f32 v14, v241, v150
v_fmac_f32 v15, v241, v151
v_mul_f32 v8, v245, v8
MM v[44:47],a[112:115],a[136:139],v[44:47]
v_mul_f32 v9, v245, v9
v_mul_f32 v10, v245, v10
MM v[64:67],a[0:3],a[128:131],v[64:67]
v_mul_f32 v11, v245, v11
v_mul_f32 v12, v245, v12
MM v[64:67],a[4:7],a[132:135],v[64:67]
v_mul_f32 v13, v245, v13
v_mul_f32 v14, v245, v14
v_mul_f32 v15, v245, v15
MM v[64:67],a[8:11],a[160:163],v[64:67]
CV v164,v8,v9
CV v165,v10,v11
MM v[64:67],a[12:15],a[164:167],v[64:67]
CV v166,v12,v13
CV v167,v14,v15
MM v[72:75],a[12:15],a[172:175],v[72:75]
v_xor_b32 v159, 128, v253
MM v[72:75],a[8:11],a[168:171],v[72:75]
RD v[152:155],v159 offset:0
RD v[156:159],v159 offset:16
MM v[72:75],a[4:7],a[140:143],v[72:75]
MM v[72:75],a[0:3],a[136:139],v[72:75]
buffer_load_dwordx4 v229, s[8:11], s77 offen offset:1024 lds
MM v[68:71],a[16:19],a[128:131],v[68:71]
MM v[68:71],a[20:23],a[132:135],v[68:71]
s_waitcnt lgkmcnt(0)
v_fmac_f32 v32, v240, v152
v_fmac_f32 v33, v240, v153
v_fmac_f32 v34, v240, v154
v_fmac_f32 v35, v240, v155
v_fmac_f32 v36, v240, v156
v_fmac_f32 v37, v240, v157
v_fmac_f32 v38, v240, v158
v_fmac_f32 v39, v240, v159
v_mul_f32 v179, -v250, v244
v_fma_f32 v32, v244, v32, v179
v_fma_f32 v33, v244, v33, v179
v_fma_f32 v34, v244, v34, v179
MM v[68:71],a[24:27],a[160:163],v[68:71]
v_fma_f32 v35, v244, v35, v179
v_fma_f32 v36, v244, v36, v179
MM v[68:71],a[28:31],a[164:167],v[68:71]
v_fma_f32 v37, v244, v37, v179
v_fma_f32 v38, v244, v38, v179
v_fma_f32 v39, v244, v39, v179
MM v[76:79],a[28:31],a[172:175],v[76:79]
CV v176,v32,v33
CV v177,v34,v35
MM v[76:79],a[24:27],a[168:171],v[76:79]
CV v178,v36,v37
CV v179,v38,v39
v_fmac_f32 v40, v241, v152
MM v[76:79],a[20:23],a[140:143],v[76:79]
v_fmac_f32 v41, v241, v153
v_fmac_f32 v42, v241, v154
v_fmac_f32 v43, v241, v155
MM v[76:79],a[16:19],a[136:139],v[76:79]
v_fmac_f32 v44, v241, v156
v_fmac_f32 v45, v241, v157
MM v[96:99],a[32:35],a[128:131],v[96:99]
v_fmac_f32 v46, v241, v158
v_fmac_f32 v47, v241, v159
MM v[96:99],a[36:39],a[132:135],v[96:99]
buffer_load_dwordx4 v230, s[8:11], s77 offen offset:2048 lds
v_mul_f32 v183, -v250, v245
v_fma_f32 v40, v245, v40, v183
v_fma_f32 v41, v245, v41, v183
MM v[96:99],a[40:43],a[160:163],v[96:99]
v_fma_f32 v42, v245, v42, v183
v_fma_f32 v43, v245, v43, v183
MM v[96:99],a[44:47],a[164:167],v[96:99]
v_fma_f32 v44, v245, v44, v183
v_fma_f32 v45, v245, v45, v183
MM v[104:107],a[44:47],a[172:175],v[104:107]
v_fma_f32 v46, v245, v46, v183
)ASM"
R"ASM(
v_fma_f32 v47, v245, v47, v183
CV v180,v40,v41
MM v[104:107],a[40:43],a[168:171],v[104:107]
CV v181,v42,v43
CV v182,v44,v45
MM v[104:107],a[36:39],a[140:143],v[104:107]
CV v183,v46,v47
v_xor_b32 v151, 256, v253
MM v[104:107],a[32:35],a[136:139],v[104:107]
RD v[144:147],v151 offset:0
RD v[148:151],v151 offset:16
MM v[100:103],a[48:51],a[128:131],v[100:103]
MM v[100:103],a[52:55],a[132:135],v[100:103]
MM v[100:103],a[56:59],a[160:163],v[100:103]
MM v[100:103],a[60:63],a[164:167],v[100:103]
s_waitcnt lgkmcnt(0)
buffer_load_dwordx4 v231, s[8:11], s77 offen offset:3072 lds
v_fmac_f32 v64, v240, v144
v_fmac_f32 v65, v240, v145
v_fmac_f32 v66, v240, v146
v_fmac_f32 v67, v240, v147
v_fmac_f32 v68, v240, v148
v_fmac_f32 v69, v240, v149
v_fmac_f32 v70, v240, v150
v_fmac_f32 v71, v240, v151
v_mul_f32 v195, -v250, v244
v_fma_f32 v64, v244, v64, v195
v_fma_f32 v65, v244, v65, v195
MM v[108:111],a[60:63],a[172:175],v[108:111]
s_add_u32 s77, s64, 16384
s_add_u32 s81, s77, 896
s_add_u32 m0, s65, 81920
v_fma_f32 v66, v244, v66, v195
v_fma_f32 v67, v244, v67, v195
MM v[108:111],a[56:59],a[168:171],v[108:111]
v_fma_f32 v68, v244, v68, v195
v_fma_f32 v69, v244, v69, v195
MM v[108:111],a[52:55],a[140:143],v[108:111]
v_fma_f32 v70, v244, v70, v195
v_fma_f32 v71, v244, v71, v195
CV v192,v64,v65
MM v[108:111],a[48:51],a[136:139],v[108:111]
CV v193,v66,v67
CV v194,v68,v69
MM v[16:19],a[64:67],a[144:147],v[16:19]
CV v195,v70,v71
v_fmac_f32 v72, v241, v144
v_fmac_f32 v73, v241, v145
MM v[16:19],a[68:71],a[148:151],v[16:19]
v_fmac_f32 v74, v241, v146
v_fmac_f32 v75, v241, v147
v_fmac_f32 v76, v241, v148
MM v[16:19],a[72:75],a[176:179],v[16:19]
v_fmac_f32 v77, v241, v149
v_fmac_f32 v78, v241, v150
MM v[16:19],a[76:79],a[180:183],v[16:19]
v_fmac_f32 v79, v241, v151
v_mul_f32 v199, -v250, v245
v_fma_f32 v72, v245, v72, v199
MM v[24:27],a[76:79],a[188:191],v[24:27]
v_fma_f32 v73, v245, v73, v199
v_fma_f32 v74, v245, v74, v199
MM v[24:27],a[72:75],a[184:187],v[24:27]
buffer_load_dwordx4 v235, s[12:15], s81 offen offset:0 lds
v_fma_f32 v75, v245, v75, v199
v_fma_f32 v76, v245, v76, v199
MM v[24:27],a[68:71],a[156:159],v[24:27]
s_waitcnt vmcnt(13)
s_barrier
RD a[192:195],v224 offset:0
v_fma_f32 v77, v245, v77, v199
v_fma_f32 v78, v245, v78, v199
v_fma_f32 v79, v245, v79, v199
MM v[24:27],a[64:67],a[152:155],v[24:27]
RD a[196:199],v225 offset:0
RD a[200:203],v226 offset:0
CV v196,v72,v73
CV v197,v74,v75
MM v[20:23],a[80:83],a[144:147],v[20:23]
RD a[204:207],v227 offset:0
RD a[208:211],v224 offset:4096
CV v198,v76,v77
CV v199,v78,v79
MM v[20:23],a[84:87],a[148:151],v[20:23]
RD a[212:215],v225 offset:4096
RD a[216:219],v226 offset:4096
v_xor_b32 v159, 384, v253
MM v[20:23],a[88:91],a[176:179],v[20:23]
RD a[220:223],v227 offset:4096
RD a[224:227],v224 offset:8192
RD v[152:155],v159 offset:0
RD v[156:159],v159 offset:16
MM v[20:23],a[92:95],a[180:183],v[20:23]
RD a[228:231],v225 offset:8192
RD a[232:235],v226 offset:8192
MM v[28:31],a[92:95],a[188:191],v[28:31]
RD a[236:239],v227 offset:8192
RD a[240:243],v224 offset:12288
MM v[28:31],a[88:91],a[184:187],v[28:31]
RD a[244:247],v225 offset:12288
RD a[248:251],v226 offset:12288
MM v[28:31],a[84:87],a[156:159],v[28:31]
s_waitcnt lgkmcnt(6)
RD a[252:255],v227 offset:12288
v_fmac_f32 v96, v240, v152
v_fmac_f32 v97, v240, v153
v_fmac_f32 v98, v240, v154
v_fmac_f32 v99, v240, v155
v_fmac_f32 v100, v240, v156
v_fmac_f32 v101, v240, v157
v_fmac_f32 v102, v240, v158
v_fmac_f32 v103, v240, v159
v_mul_f32 v211, -v250, v244
v_fma_f32 v96, v244, v96, v211
v_fma_f32 v97, v244, v97, v211
v_fma_f32 v98, v244, v98, v211
MM v[28:31],a[80:83],a[152:155],v[28:31]
buffer_load_dwordx4 v235, s[12:15], s77 offen offset:1024 lds
v_fma_f32 v99, v244, v99, v211
v_fma_f32 v100, v244, v100, v211
MM v[48:51],a[96:99],a[144:147],v[48:51]
v_fma_f32 v101, v244, v101, v211
v_fma_f32 v102, v244, v102, v211
v_fma_f32 v103, v244, v103, v211
MM v[48:51],a[100:103],a[148:151],v[48:51]
CV v208,v96,v97
CV v209,v98,v99
MM v[48:51],a[104:107],a[176:179],v[48:51]
CV v210,v100,v101
CV v211,v102,v103
v_fmac_f32 v104, v241, v152
MM v[48:51],a[108:111],a[180:183],v[48:51]
v_fmac_f32 v105, v241, v153
v_fmac_f32 v106, v241, v154
v_fmac_f32 v107, v241, v155
MM v[56:59],a[108:111],a[188:191],v[56:59]
v_fmac_f32 v108, v241, v156
v_fmac_f32 v109, v241, v157
MM v[56:59],a[104:107],a[184:187],v[56:59]
v_fmac_f32 v110, v241, v158
v_fmac_f32 v111, v241, v159
MM v[56:59],a[100:103],a[156:159],v[56:59]
v_mul_f32 v215, -v250, v245
v_fma_f32 v104, v245, v104, v215
v_fma_f32 v105, v245, v105, v215
MM v[56:59],a[96:99],a[152:155],v[56:59]
v_fma_f32 v106, v245, v106, v215
v_fma_f32 v107, v245, v107, v215
MM v[52:55],a[112:115],a[144:147],v[52:55]
v_fma_f32 v108, v245, v108, v215
v_fma_f32 v109, v245, v109, v215
MM v[52:55],a[116:119],a[148:151],v[52:55]
buffer_load_dwordx4 v235, s[12:15], s81 offen offset:2048 lds
v_fma_f32 v110, v245, v110, v215
v_fma_f32 v111, v245, v111, v215
CV v212,v104,v105
MM v[52:55],a[120:123],a[176:179],v[52:55]
CV v213,v106,v107
CV v214,v108,v109
CV v215,v110,v111
MM v[52:55],a[124:127],a[180:183],v[52:55]
v_mov_b32 v241, 0
v_mov_b32 v244, 0
s_waitcnt lgkmcnt(0)
MM v[0:3],a[192:195],v[160:163],0
v_mov_b32 v144, 0
v_mov_b32 v145, 0
MM v[0:3],a[196:199],v[176:179],v[0:3]
v_mov_b32 v245, 0
v_mov_b32 v148, 0
MM v[0:3],a[200:203],v[192:195],v[0:3]
v_mov_b32 v149, 0
v_fmac_f32 v16, v242, v136
MM v[0:3],a[204:207],v[208:211],v[0:3]
v_fmac_f32 v17, v242, v137
v_fmac_f32 v18, v242, v138
MM v[8:11],a[192:195],v[164:167],0
v_fmac_f32 v19, v242, v139
v_fmac_f32 v20, v242, v140
MM v[8:11],a[196:199],v[180:183],v[8:11]
v_fmac_f32 v21, v242, v141
v_fmac_f32 v22, v242, v142
MM v[8:11],a[200:203],v[196:199],v[8:11]
v_fmac_f32 v23, v242, v143
v_mul_f32 v16, v246, v16
MM v[8:11],a[204:207],v[212:215],v[8:11]
buffer_load_dwordx4 v235, s[12:15], s77 offen offset:3072 lds
MM v[4:7],a[208:211],v[160:163],0
s_add_u32 s77, s64, 32768
s_add_u32 s81, s77, 896
s_add_u32 m0, s65, 98304
MM v[4:7],a[212:215],v[176:179],v[4:7]
v_max3_f32 v146, v0, v1, v2
v_max_f32 v146, v146, v3
MM v[4:7],a[216:219],v[192:195],v[4:7]
v_mov_b32 v147, v146
v_mul_f32 v17, v246, v17
MM v[4:7],a[220:223],v[208:211],v[4:7]
v_permlane16_swap_b32 v146, v147
v_max_f32 v146, v146, v147
MM v[12:15],a[208:211],v[164:167],0
s_waitcnt vmcnt(8)
LPG4_%=:
s_cbranch_execz LPG8_%=
s_barrier
RD a[192:195],v224 offset:16384
MM v[12:15],a[212:215],v[180:183],v[12:15]
RD a[196:199],v225 offset:16384
RD a[200:203],v226 offset:16384
MM v[12:15],a[216:219],v[196:199],v[12:15]
RD a[204:207],v227 offset:16384
v_mov_b32 v147, v146
MM v[12:15],a[220:223],v[212:215],v[12:15]
s_nop 0
v_permlane32_swap_b32 v146, v147
v_max_f32 v146, v146, v147
v_sub_f32 v128, 0xc1600000, v146
v_sub_f32 v129, 0xc1600000, v146
MM v[32:35],a[224:227],v[160:163],0
RD a[208:211],v224 offset:20480
RD a[212:215],v225 offset:20480
v_sub_f32 v130, 0xc1600000, v146
v_sub_f32 v131, 0xc1600000, v146
v_max3_f32 v150, v8, v9, v10
MM v[32:35],a[228:231],v[176:179],v[32:35]
buffer_load_dwordx4 v235, s[12:15], s81 offen offset:0 lds
v_max_f32 v150, v150, v11
v_mov_b32 v151, v150
MM v[32:35],a[232:235],v[192:195],v[32:35]
RD a[216:219],v226 offset:20480
RD a[220:223],v227 offset:20480
v_permlane16_swap_b32 v150, v151
v_max_f32 v150, v150, v151
v_mov_b32 v151, v150
MM v[32:35],a[236:239],v[208:211],v[32:35]
s_nop 0
v_permlane32_swap_b32 v150, v151
v_max_f32 v150, v150, v151
v_add_f32 v0, v0, v128
v_sub_f32 v132, 0xc1600000, v150
MM v[40:43],a[224:227],v[164:167],0
v_sub_f32 v133, 0xc1600000, v150
v_sub_f32 v134, 0xc1600000, v150
v_sub_f32 v135, 0xc1600000, v150
v_add_f32 v1, v1, v128
EX v0,v0
MM v[40:43],a[228:231],v[180:183],v[40:43]
EX v1,v1
MM v[40:43],a[232:235],v[196:199],v[40:43]
v_add_f32 v2, v2, v128
v_add_f32 v3, v3, v128
MM v[40:43],a[236:239],v[212:215],v[40:43]
EX v2,v2
MM v[36:39],a[240:243],v[160:163],v[128:131]
RD a[224:227],v224 offset:24576
RD a[228:231],v225 offset:24576
EX v3,v3
MM v[36:39],a[244:247],v[176:179],v[36:39]
RD a[232:235],v226 offset:24576
RD a[236:239],v227 offset:24576
CV v0,v0,v1
v_add_f32 v4, v4, v128
MM v[36:39],a[248:251],v[192:195],v[36:39]
v_add_f32 v5, v5, v128
v_mul_f32 v18, v246, v18
EX v4,v4
MM v[36:39],a[252:255],v[208:211],v[36:39]
buffer_load_dwordx4 v235, s[12:15], s77 offen offset:1024 lds
EX v5,v5
MM v[44:47],a[240:243],v[164:167],v[132:135]
CV v1,v2,v3
v_add_f32 v6, v6, v128
MM v[44:47],a[244:247],v[180:183],v[44:47]
v_add_f32 v7, v7, v128
v_mul_f32 v19, v246, v19
MM v[44:47],a[248:251],v[196:199],v[44:47]
EX v6,v6
EX v7,v7
MM v[44:47],a[252:255],v[212:215],v[44:47]
CV v2,v4,v5
v_add_f32 v8, v8, v132
MM v[60:63],a[124:127],a[188:191],v[60:63]
RD a[240:243],v224 offset:28672
RD a[244:247],v225 offset:28672
v_add_f32 v9, v9, v132
MM v[60:63],a[120:123],a[184:187],v[60:63]
RD a[248:251],v226 offset:28672
RD a[252:255],v227 offset:28672
EX v8,v8
MM v[60:63],a[116:119],a[156:159],v[60:63]
EX v9,v9
MM v[60:63],a[112:115],a[152:155],v[60:63]
CV v3,v6,v7
v_add_f32 v10, v10, v132
MM v[80:83],a[0:3],a[144:147],v[80:83]
v_add_f32 v11, v11, v132
v_mul_f32 v20, v246, v20
EX v10,v10
MM v[80:83],a[4:7],a[148:151],v[80:83]
buffer_load_dwordx4 v235, s[12:15], s81 offen offset:2048 lds
EX v11,v11
MM v[80:83],a[8:11],a[176:179],v[80:83]
CV v8,v8,v9
v_add_f32 v12, v12, v132
MM v[80:83],a[12:15],a[180:183],v[80:83]
v_add_f32 v13, v13, v132
v_mul_f32 v21, v246, v21
EX v12,v12
MM v[88:91],a[12:15],a[188:191],v[88:91]
EX v13,v13
MM v[88:91],a[8:11],a[184:187],v[88:91]
CV v9,v10,v11
v_add_f32 v14, v14, v132
MM v[88:91],a[4:7],a[156:159],v[88:91]
v_add_f32 v15, v15, v132
v_mul_f32 v22, v246, v22
EX v14,v14
MM v[88:91],a[0:3],a[152:155],v[88:91]
EX v15,v15
MM v[84:87],a[16:19],a[144:147],v[84:87]
CV v10,v12,v13
v_add_f32 v32, v32, v128
MM v[84:87],a[20:23],a[148:151],v[84:87]
v_add_f32 v33, v33, v128
v_mul_f32 v23, v246, v23
MM v[84:87],a[24:27],a[176:179],v[84:87]
EX v32,v32
MM v[84:87],a[28:31],a[180:183],v[84:87]
buffer_load_dwordx4 v235, s[12:15], s77 offen offset:3072 lds
EX v33,v33
MM v[92:95],a[28:31],a[188:191],v[92:95]
s_add_u32 s77, s64, 49152
s_add_u32 m0, s65, 49152
CV v11,v14,v15
v_add_f32 v34, v34, v128
v_add_f32 v35, v35, v128
MM v[92:95],a[24:27],a[184:187],v[92:95]
buffer_load_dwordx4 v228, s[8:11], s77 offen offset:0 lds
EX v34,v34
MM v[92:95],a[20:23],a[156:159],v[92:95]
EX v35,v35
MM v[92:95],a[16:19],a[152:155],v[92:95]
CV v4,v32,v33
CV v168,v16,v17
EX v36,v36
MM v[112:115],a[32:35],a[144:147],v[112:115]
EX v37,v37
MM v[112:115],a[36:39],a[148:151],v[112:115]
buffer_load_dwordx4 v229, s[8:11], s77 offen offset:1024 lds
CV v5,v34,v35
MM v[112:115],a[40:43],a[176:179],v[112:115]
EX v38,v38
EX v39,v39
MM v[112:115],a[44:47],a[180:183],v[112:115]
CV v6,v36,v37
v_add_f32 v40, v40, v132
MM v[120:123],a[44:47],a[188:191],v[120:123]
v_add_f32 v41, v41, v132
CV v169,v18,v19
MM v[120:123],a[40:43],a[184:187],v[120:123]
buffer_load_dwordx4 v230, s[8:11], s77 offen offset:2048 lds
EX v40,v40
MM v[120:123],a[36:39],a[156:159],v[120:123]
EX v41,v41
MM v[120:123],a[32:35],a[152:155],v[120:123]
CV v7,v38,v39
v_add_f32 v42, v42, v132
MM v[116:119],a[48:51],a[144:147],v[116:119]
v_add_f32 v43, v43, v132
CV v170,v20,v21
EX v42,v42
MM v[116:119],a[52:55],a[148:151],v[116:119]
buffer_load_dwordx4 v231, s[8:11], s77 offen offset:3072 lds
EX v43,v43
MM v[116:119],a[56:59],a[176:179],v[116:119]
s_add_u32 s77, s64, 49152
s_add_u32 s81, s77, 896
s_add_u32 m0, s65, 114688
CV v12,v40,v41
EX v44,v44
MM v[116:119],a[60:63],a[180:183],v[116:119]
EX v45,v45
MM v[124:127],a[60:63],a[188:191],v[124:127]
CV v13,v42,v43
CV v171,v22,v23
MM v[124:127],a[56:59],a[184:187],v[124:127]
buffer_load_dwordx4 v235, s[12:15], s81 offen offset:0 lds
EX v46,v46
MM v[124:127],a[52:55],a[156:159],v[124:127]
EX v47,v47
CV v14,v44,v45
CV v15,v46,v47
MM v[124:127],a[48:51],a[152:155],v[124:127]
v_fmac_f32 v24, v243, v136
v_fmac_f32 v25, v243, v137
v_smfmac_f32_16x16x64_bf16 v[144:147], v[236:239], v[0:7], v234
v_fmac_f32 v26, v243, v138
v_smfmac_f32_16x16x64_bf16 v[148:151], v[236:239], v[8:15], v234
s_waitcnt lgkmcnt(0)
)ASM"
R"ASM(
v_fmac_f32 v27, v243, v139
buffer_load_dwordx4 v235, s[12:15], s77 offen offset:1024 lds
buffer_load_dwordx4 v235, s[12:15], s81 offen offset:2048 lds
buffer_load_dwordx4 v235, s[12:15], s77 offen offset:3072 lds
s_add_u32 s64, s64, 49152
MM v[64:67],a[192:195],v[160:163],v[128:131]
s_add_u32 s64, s64, 16384
v_fmac_f32 v28, v243, v140
v_fmac_f32 v29, v243, v141
MM v[64:67],a[196:199],v[176:179],v[64:67]
s_add_u32 s81, s64, 896
TR a[128:129],v232 offset:0
v_fmac_f32 v30, v243, v142
MM v[64:67],a[200:203],v[192:195],v[64:67]
v_fmac_f32 v31, v243, v143
v_mul_f32 v24, v247, v24
MM v[64:67],a[204:207],v[208:211],v[64:67]
v_max3_f32 v249, v144, v145, v148
v_max_f32 v249, v249, v149
v_mul_f32 v25, v247, v25
MM v[68:71],a[220:223],v[208:211],v[128:131]
TR a[130:131],v232 offset:4096
TR a[132:133],v232 offset:8192
v_cmp_lt_f32 vcc, s71, v249
v_mul_f32 v26, v247, v26
v_mul_f32 v27, v247, v27
MM v[68:71],a[216:219],v[192:195],v[68:71]
s_cbranch_vccnz LSITE0_%=
LBACK0_%=:
TR a[134:135],v232 offset:12288
v_add_f32 v244, v144, v145
v_mul_f32 v28, v247, v28
v_mul_f32 v29, v247, v29
v_mul_f32 v30, v247, v30
MM v[68:71],a[212:215],v[176:179],v[68:71]
TR a[136:137],v233 offset:0
v_add_f32 v245, v148, v149
v_mul_f32 v31, v247, v31
CV v172,v24,v25
MM v[68:71],a[208:211],v[160:163],v[68:71]
TR a[138:139],v233 offset:4096
CV v173,v26,v27
CV v174,v28,v29
MM v[76:79],a[208:211],v[164:167],v[132:135]
TR a[140:141],v233 offset:8192
TR a[142:143],v233 offset:12288
CV v175,v30,v31
v_xor_b32 v143, 128, v253
MM v[76:79],a[212:215],v[180:183],v[76:79]
RD v[136:139],v143 offset:0
RD v[140:143],v143 offset:16
TR a[144:145],v232 offset:512
MM v[76:79],a[216:219],v[196:199],v[76:79]
s_waitcnt lgkmcnt(9)
TR a[146:147],v232 offset:4608
TR a[148:149],v232 offset:8704
MM v[76:79],a[220:223],v[212:215],v[76:79]
EX v64,v64
MM v[32:35],a[128:131],v[0:3],0
MM v[36:39],a[128:131],v[8:11],0
MM v[72:75],a[204:207],v[212:215],v[132:135]
TR a[150:151],v232 offset:12800
TR a[152:153],v233 offset:512
MM v[72:75],a[200:203],v[196:199],v[72:75]
s_waitcnt lgkmcnt(5)
TR a[154:155],v233 offset:4608
v_fmac_f32 v48, v242, v136
v_fmac_f32 v49, v242, v137
v_fmac_f32 v50, v242, v138
v_fmac_f32 v51, v242, v139
v_fmac_f32 v52, v242, v140
v_fmac_f32 v53, v242, v141
v_fmac_f32 v54, v242, v142
v_fmac_f32 v55, v242, v143
v_mul_f32 v187, -v252, v246
v_fma_f32 v48, v246, v48, v187
v_fma_f32 v49, v246, v49, v187
EX v65,v65
MM v[72:75],a[196:199],v[180:183],v[72:75]
TR a[156:157],v233 offset:8704
v_fma_f32 v50, v246, v50, v187
v_fma_f32 v51, v246, v51, v187
v_fma_f32 v52, v246, v52, v187
MM v[72:75],a[192:195],v[164:167],v[72:75]
TR a[158:159],v233 offset:12800
v_fma_f32 v53, v246, v53, v187
EX v66,v66
v_fma_f32 v54, v246, v54, v187
MM v[96:99],a[224:227],v[160:163],v[128:131]
TR a[160:161],v232 offset:1024
v_max3_f32 v32, |v32|, |v33|, |v34|
v_max_f32 v32, v32, |v35|
v_fma_f32 v55, v246, v55, v187
CV v184,v48,v49
EX v67,v67
MM v[96:99],a[228:231],v[176:179],v[96:99]
TR a[162:163],v232 offset:5120
TR a[164:165],v232 offset:9216
v_bfe_u32 v32, v32, 23, 8
v_add_u32 v32, 7, v32
CV v185,v50,v51
CV v186,v52,v53
CV v187,v54,v55
v_fmac_f32 v56, v243, v136
MM v[96:99],a[232:235],v[192:195],v[96:99]
TR a[166:167],v232 offset:13312
v_min_u32 v32, 240, v32
v_lshl_or_b32 v241, v32, 0, v241
v_fmac_f32 v57, v243, v137
v_fmac_f32 v58, v243, v138
EX v68,v68
MM v[96:99],a[236:239],v[208:211],v[96:99]
TR a[168:169],v233 offset:1024
v_lshl_or_b32 v32, v32, 1, 1
v_lshlrev_b32 v32, 22, v32
v_fmac_f32 v59, v243, v139
v_fmac_f32 v60, v243, v140
MM v[100:103],a[252:255],v[208:211],v[128:131]
TR a[170:171],v233 offset:5120
AW a100,v32
AW a101,v32
v_fmac_f32 v61, v243, v141
v_fmac_f32 v62, v243, v142
EX v69,v69
v_fmac_f32 v63, v243, v143
MM v[100:103],a[248:251],v[192:195],v[100:103]
TR a[172:173],v233 offset:9216
TR a[174:175],v233 offset:13312
AW a102,v32
AW a103,v32
v_mul_f32 v191, -v252, v247
v_fma_f32 v56, v247, v56, v191
MM v[100:103],a[244:247],v[176:179],v[100:103]
TR a[176:177],v232 offset:1536
v_max3_f32 v36, |v36|, |v37|, |v38|
v_max_f32 v36, v36, |v39|
v_fma_f32 v57, v247, v57, v191
EX v70,v70
v_fma_f32 v58, v247, v58, v191
MM v[100:103],a[240:243],v[160:163],v[100:103]
TR a[178:179],v232 offset:5632
v_bfe_u32 v36, v36, 23, 8
v_add_u32 v36, 7, v36
v_fma_f32 v59, v247, v59, v191
v_fma_f32 v60, v247, v60, v191
EX v71,v71
v_fma_f32 v61, v247, v61, v191
MM v[108:111],a[240:243],v[164:167],v[132:135]
TR a[180:181],v232 offset:9728
v_min_u32 v36, 240, v36
v_lshl_or_b32 v241, v36, 8, v241
v_fma_f32 v62, v247, v62, v191
v_fma_f32 v63, v247, v63, v191
CV v64,v64,v65
MM v[108:111],a[244:247],v[180:183],v[108:111]
TR a[182:183],v232 offset:13824
v_lshl_or_b32 v36, v36, 1, 1
v_lshlrev_b32 v36, 22, v36
CV v188,v56,v57
CV v189,v58,v59
EX v72,v72
CV v190,v60,v61
MM v[108:111],a[248:251],v[196:199],v[108:111]
TR a[184:185],v233 offset:1536
TR a[186:187],v233 offset:5632
TR a[188:189],v233 offset:9728
TR a[190:191],v233 offset:13824
AW a108,v36
AW a109,v36
CV v191,v62,v63
v_xor_b32 v143, 256, v253
EX v73,v73
MM v[108:111],a[252:255],v[212:215],v[108:111]
RD v[136:139],v143 offset:0
RD v[140:143],v143 offset:16
AW a110,v36
AW a111,v36
CV v65,v66,v67
MM v[104:107],a[236:239],v[212:215],v[132:135]
EX v74,v74
MM v[104:107],a[232:235],v[196:199],v[104:107]
EX v75,v75
MM v[104:107],a[228:231],v[180:183],v[104:107]
EX v76,v76
CV v66,v68,v69
MM v[104:107],a[224:227],v[164:167],v[104:107]
s_waitcnt vmcnt(12) lgkmcnt(0)
s_barrier
v_fmac_f32 v80, v242, v136
v_fmac_f32 v81, v242, v137
v_fmac_f32 v82, v242, v138
v_fmac_f32 v83, v242, v139
v_fmac_f32 v84, v242, v140
v_fmac_f32 v85, v242, v141
v_fmac_f32 v86, v242, v142
v_fmac_f32 v87, v242, v143
v_mul_f32 v203, -v252, v246
v_fma_f32 v80, v246, v80, v203
MM a[0:3],a[128:131],v[0:3],a[100:103]
s_add_u32 m0, s65, 0
v_fma_f32 v81, v246, v81, v203
v_fma_f32 v82, v246, v82, v203
v_fma_f32 v83, v246, v83, v203
MM a[0:3],a[132:135],v[4:7],a[0:3]
v_fma_f32 v84, v246, v84, v203
v_fma_f32 v85, v246, v85, v203
EX v77,v77
CV v67,v70,v71
MM a[8:11],a[132:135],v[12:15],a[108:111]
v_fma_f32 v86, v246, v86, v203
v_fma_f32 v87, v246, v87, v203
MM a[8:11],a[128:131],v[8:11],a[8:11]
CV v200,v80,v81
CV v201,v82,v83
EX v78,v78
CV v202,v84,v85
MM a[12:15],a[136:139],v[8:11],a[108:111]
CV v203,v86,v87
v_fmac_f32 v88, v243, v136
v_fmac_f32 v89, v243, v137
EX v79,v79
MM a[12:15],a[140:143],v[12:15],a[12:15]
v_fmac_f32 v90, v243, v138
v_fmac_f32 v91, v243, v139
CV v72,v72,v73
MM a[4:7],a[140:143],v[4:7],a[100:103]
v_fmac_f32 v92, v243, v140
v_fmac_f32 v93, v243, v141
EX v96,v96
v_fmac_f32 v94, v243, v142
MM a[4:7],a[136:139],v[0:3],a[4:7]
v_fmac_f32 v95, v243, v143
v_mul_f32 v207, -v252, v247
MM a[32:35],a[144:147],v[0:3],a[100:103]
RD a[192:195],v224 offset:32768
v_fma_f32 v88, v247, v88, v207
v_fma_f32 v89, v247, v89, v207
EX v97,v97
CV v73,v74,v75
v_fma_f32 v90, v247, v90, v207
MM a[32:35],a[148:151],v[4:7],a[32:35]
buffer_load_dwordx4 v228, s[8:11], s64 offen offset:0 lds
v_fma_f32 v91, v247, v91, v207
v_fma_f32 v92, v247, v92, v207
EX v98,v98
MM a[40:43],a[148:151],v[12:15],a[108:111]
RD a[196:199],v225 offset:32768
v_fma_f32 v93, v247, v93, v207
v_fma_f32 v94, v247, v94, v207
MM a[40:43],a[144:147],v[8:11],a[40:43]
RD a[200:203],v226 offset:32768
v_fma_f32 v95, v247, v95, v207
CV v204,v88,v89
EX v99,v99
CV v74,v76,v77
CV v205,v90,v91
MM a[44:47],a[152:155],v[8:11],a[108:111]
CV v206,v92,v93
CV v207,v94,v95
MM a[44:47],a[156:159],v[12:15],a[44:47]
RD a[204:207],v227 offset:32768
buffer_load_dwordx4 v229, s[8:11], s64 offen offset:1024 lds
v_xor_b32 v143, 384, v253
EX v100,v100
MM a[36:39],a[156:159],v[4:7],a[100:103]
RD v[136:139],v143 offset:0
RD v[140:143],v143 offset:16
EX v101,v101
CV v75,v78,v79
MM a[36:39],a[152:155],v[0:3],a[36:39]
RD a[208:211],v224 offset:36864
RD a[212:215],v225 offset:36864
MM a[64:67],a[160:163],v[0:3],a[100:103]
EX v102,v102
MM a[64:67],a[164:167],v[4:7],a[64:67]
buffer_load_dwordx4 v230, s[8:11], s64 offen offset:2048 lds
MM a[72:75],a[164:167],v[12:15],a[108:111]
s_waitcnt lgkmcnt(2)
v_fmac_f32 v112, v242, v136
v_fmac_f32 v113, v242, v137
v_fmac_f32 v114, v242, v138
v_fmac_f32 v115, v242, v139
v_fmac_f32 v116, v242, v140
v_fmac_f32 v117, v242, v141
v_fmac_f32 v118, v242, v142
v_fmac_f32 v119, v242, v143
v_mul_f32 v219, -v252, v246
v_fma_f32 v112, v246, v112, v219
v_fma_f32 v113, v246, v113, v219
EX v103,v103
CV v68,v96,v97
v_fma_f32 v114, v246, v114, v219
MM a[72:75],a[160:163],v[8:11],a[72:75]
RD a[216:219],v226 offset:36864
v_fma_f32 v115, v246, v115, v219
v_fma_f32 v116, v246, v116, v219
EX v104,v104
MM a[76:79],a[168:171],v[8:11],a[108:111]
buffer_load_dwordx4 v231, s[8:11], s64 offen offset:3072 lds
RD a[220:223],v227 offset:36864
v_fma_f32 v117, v246, v117, v219
v_fma_f32 v118, v246, v118, v219
MM a[76:79],a[172:175],v[12:15],a[76:79]
s_add_u32 m0, s65, 65536
v_fma_f32 v119, v246, v119, v219
CV v216,v112,v113
EX v105,v105
CV v217,v114,v115
CV v69,v98,v99
MM a[68:71],a[172:175],v[4:7],a[100:103]
RD a[224:227],v224 offset:40960
CV v218,v116,v117
CV v219,v118,v119
v_fmac_f32 v120, v243, v136
MM a[68:71],a[168:171],v[0:3],a[68:71]
v_fmac_f32 v121, v243, v137
v_fmac_f32 v122, v243, v138
EX v106,v106
v_fmac_f32 v123, v243, v139
MM a[96:99],a[176:179],v[0:3],a[100:103]
RD a[228:231],v225 offset:40960
buffer_load_dwordx4 v235, s[12:15], s81 offen offset:0 lds
v_fmac_f32 v124, v243, v140
v_fmac_f32 v125, v243, v141
EX v107,v107
CV v70,v100,v101
MM a[96:99],a[180:183],v[4:7],a[96:99]
v_fmac_f32 v126, v243, v142
v_fmac_f32 v127, v243, v143
MM a[104:107],a[180:183],v[12:15],a[108:111]
RD a[232:235],v226 offset:40960
v_mul_f32 v223, -v252, v247
v_fma_f32 v120, v247, v120, v223
EX v108,v108
v_fma_f32 v121, v247, v121, v223
MM a[104:107],a[176:179],v[8:11],a[104:107]
v_fma_f32 v122, v247, v122, v223
v_fma_f32 v123, v247, v123, v223
CV v71,v102,v103
MM a[108:111],a[184:187],v[8:11],a[108:111]
RD a[236:239],v227 offset:40960
buffer_load_dwordx4 v235, s[12:15], s64 offen offset:1024 lds
v_fma_f32 v124, v247, v124, v223
v_fma_f32 v125, v247, v125, v223
EX v109,v109
MM a[108:111],a[188:191],v[12:15],a[108:111]
v_fma_f32 v126, v247, v126, v223
v_fma_f32 v127, v247, v127, v223
EX v110,v110
CV v220,v120,v121
MM a[100:103],a[188:191],v[4:7],a[100:103]
RD a[240:243],v224 offset:45056
RD a[244:247],v225 offset:45056
RD a[248:251],v226 offset:45056
RD a[252:255],v227 offset:45056
CV v221,v122,v123
EX v111,v111
CV v222,v124,v125
CV v223,v126,v127
MM a[100:103],a[184:187],v[0:3],a[100:103]
buffer_load_dwordx4 v235, s[12:15], s81 offen offset:2048 lds
CV v76,v104,v105
v_smfmac_f32_16x16x64_bf16 v[144:147], v[236:239], v[64:71], v234
CV v77,v106,v107
CV v78,v108,v109
CV v79,v110,v111
s_nop 1
v_smfmac_f32_16x16x64_bf16 v[148:151], v[236:239], v[72:79], v234
s_waitcnt lgkmcnt(0)
buffer_load_dwordx4 v235, s[12:15], s64 offen offset:3072 lds
MM v[0:3],a[192:195],v[160:163],v[128:131]
s_add_u32 s64, s64, 16384
TR a[128:129],v232 offset:16384
TR a[130:131],v232 offset:20480
MM v[0:3],a[196:199],v[176:179],v[0:3]
s_add_u32 s81, s64, 896
TR a[132:133],v232 offset:24576
TR a[134:135],v232 offset:28672
MM v[0:3],a[200:203],v[192:195],v[0:3]
TR a[136:137],v233 offset:16384
TR a[138:139],v233 offset:20480
MM v[0:3],a[204:207],v[208:211],v[0:3]
v_max3_f32 v249, v144, v145, v148
v_max_f32 v249, v249, v149
MM v[4:7],a[220:223],v[208:211],v[128:131]
TR a[140:141],v233 offset:24576
v_cmp_lt_f32 vcc, s71, v249
MM v[4:7],a[216:219],v[192:195],v[4:7]
s_cbranch_vccnz LSITE1_%=
LBACK1_%=:
TR a[142:143],v233 offset:28672
v_add_f32 v244, v144, v145
MM v[4:7],a[212:215],v[176:179],v[4:7]
TR a[144:145],v232 offset:16896
v_add_f32 v245, v148, v149
MM v[4:7],a[208:211],v[160:163],v[4:7]
TR a[146:147],v232 offset:20992
TR a[148:149],v232 offset:25088
MM v[12:15],a[208:211],v[164:167],v[132:135]
EX v0,v0
MM v[12:15],a[212:215],v[180:183],v[12:15]
EX v1,v1
MM v[12:15],a[216:219],v[196:199],v[12:15]
TR a[150:151],v232 offset:29184
CV v0,v0,v1
MM v[12:15],a[220:223],v[212:215],v[12:15]
TR a[152:153],v233 offset:16896
TR a[154:155],v233 offset:20992
MM v[8:11],a[204:207],v[212:215],v[132:135]
)ASM"
R"ASM(
EX v2,v2
MM v[8:11],a[200:203],v[196:199],v[8:11]
EX v3,v3
MM v[8:11],a[196:199],v[180:183],v[8:11]
TR a[156:157],v233 offset:25088
CV v1,v2,v3
MM v[8:11],a[192:195],v[164:167],v[8:11]
TR a[158:159],v233 offset:29184
s_nop 0
LPG5_%=:
s_cbranch_execz LPG9_%=
TR a[160:161],v232 offset:17408
MM v[32:35],a[224:227],v[160:163],v[128:131]
EX v4,v4
MM v[32:35],a[228:231],v[176:179],v[32:35]
TR a[162:163],v232 offset:21504
TR a[164:165],v232 offset:25600
MM v[32:35],a[232:235],v[192:195],v[32:35]
EX v5,v5
MM v[32:35],a[236:239],v[208:211],v[32:35]
TR a[166:167],v232 offset:29696
CV v2,v4,v5
MM v[36:39],a[252:255],v[208:211],v[128:131]
EX v6,v6
MM v[36:39],a[248:251],v[192:195],v[36:39]
TR a[168:169],v233 offset:17408
TR a[170:171],v233 offset:21504
MM v[36:39],a[244:247],v[176:179],v[36:39]
EX v7,v7
MM v[36:39],a[240:243],v[160:163],v[36:39]
TR a[172:173],v233 offset:25600
CV v3,v6,v7
MM v[44:47],a[240:243],v[164:167],v[132:135]
EX v8,v8
MM v[44:47],a[244:247],v[180:183],v[44:47]
TR a[174:175],v233 offset:29696
TR a[176:177],v232 offset:17920
MM v[44:47],a[248:251],v[196:199],v[44:47]
EX v9,v9
MM v[44:47],a[252:255],v[212:215],v[44:47]
TR a[178:179],v232 offset:22016
TR a[180:181],v232 offset:26112
MM v[40:43],a[236:239],v[212:215],v[132:135]
EX v10,v10
MM v[40:43],a[232:235],v[196:199],v[40:43]
TR a[182:183],v232 offset:30208
CV v8,v8,v9
MM v[40:43],a[228:231],v[180:183],v[40:43]
EX v11,v11
MM v[40:43],a[224:227],v[164:167],v[40:43]
TR a[184:185],v233 offset:17920
TR a[186:187],v233 offset:22016
MM v[16:19],a[192:195],v[168:171],0
EX v12,v12
MM v[16:19],a[196:199],v[184:187],v[16:19]
TR a[188:189],v233 offset:26112
TR a[190:191],v233 offset:30208
MM v[16:19],a[200:203],v[200:203],v[16:19]
EX v13,v13
MM v[16:19],a[204:207],v[216:219],v[16:19]
EX v14,v14
MM v[24:27],a[192:195],v[172:175],0
EX v15,v15
MM v[24:27],a[196:199],v[188:191],v[24:27]
EX v32,v32
MM v[24:27],a[200:203],v[204:207],v[24:27]
EX v33,v33
MM v[24:27],a[204:207],v[220:223],v[24:27]
s_waitcnt vmcnt(12) lgkmcnt(0)
s_barrier
CV v9,v10,v11
MM a[0:3],a[128:131],v[64:67],a[0:3]
s_add_u32 m0, s65, 16384
CV v10,v12,v13
CV v11,v14,v15
MM a[0:3],a[132:135],v[68:71],a[0:3]
RD a[192:195],v224 offset:49152
CV v4,v32,v33
MM a[8:11],a[132:135],v[76:79],a[8:11]
v_max3_f32 v154, v16, v17, v18
v_max_f32 v154, v154, v19
MM a[8:11],a[128:131],v[72:75],a[8:11]
buffer_load_dwordx4 v228, s[8:11], s64 offen offset:0 lds
MM a[12:15],a[136:139],v[72:75],a[12:15]
EX v34,v34
MM a[12:15],a[140:143],v[76:79],a[12:15]
RD a[196:199],v225 offset:49152
v_mov_b32 v155, v154
MM a[4:7],a[140:143],v[68:71],a[4:7]
s_nop 0
v_permlane16_swap_b32 v154, v155
v_max_f32 v154, v154, v155
MM a[4:7],a[136:139],v[64:67],a[4:7]
EX v35,v35
MM a[32:35],a[144:147],v[64:67],a[32:35]
RD a[200:203],v226 offset:49152
v_mov_b32 v155, v154
MM a[32:35],a[148:151],v[68:71],a[32:35]
s_nop 0
v_permlane32_swap_b32 v154, v155
v_max_f32 v154, v154, v155
MM a[40:43],a[148:151],v[76:79],a[40:43]
EX v36,v36
MM a[40:43],a[144:147],v[72:75],a[40:43]
RD a[204:207],v227 offset:49152
v_sub_f32 v136, 0xc1600000, v154
MM a[44:47],a[152:155],v[72:75],a[44:47]
EX v37,v37
MM a[44:47],a[156:159],v[76:79],a[44:47]
buffer_load_dwordx4 v229, s[8:11], s64 offen offset:1024 lds
v_sub_f32 v137, 0xc1600000, v154
v_sub_f32 v138, 0xc1600000, v154
MM a[36:39],a[156:159],v[68:71],a[36:39]
EX v38,v38
MM a[36:39],a[152:155],v[64:67],a[36:39]
RD a[208:211],v224 offset:53248
v_sub_f32 v139, 0xc1600000, v154
v_mov_b32 v246, 0
MM a[64:67],a[160:163],v[64:67],a[64:67]
EX v39,v39
v_mov_b32 v152, 0
MM a[64:67],a[164:167],v[68:71],a[64:67]
buffer_load_dwordx4 v230, s[8:11], s64 offen offset:2048 lds
RD a[212:215],v225 offset:53248
v_mov_b32 v153, 0
MM a[72:75],a[164:167],v[76:79],a[72:75]
EX v40,v40
v_max3_f32 v158, v24, v25, v26
MM a[72:75],a[160:163],v[72:75],a[72:75]
RD a[216:219],v226 offset:53248
v_max_f32 v158, v158, v27
CV v5,v34,v35
MM a[76:79],a[168:171],v[72:75],a[76:79]
buffer_load_dwordx4 v231, s[8:11], s64 offen offset:3072 lds
RD a[220:223],v227 offset:53248
EX v41,v41
v_mov_b32 v159, v158
MM a[76:79],a[172:175],v[76:79],a[76:79]
s_add_u32 m0, s65, 81920
v_permlane16_swap_b32 v158, v159
v_max_f32 v158, v158, v159
MM a[68:71],a[172:175],v[68:71],a[68:71]
RD a[224:227],v224 offset:57344
EX v42,v42
v_mov_b32 v159, v158
MM a[68:71],a[168:171],v[64:67],a[68:71]
s_nop 0
v_permlane32_swap_b32 v158, v159
CV v6,v36,v37
EX v43,v43
MM a[96:99],a[176:179],v[64:67],a[96:99]
RD a[228:231],v225 offset:57344
buffer_load_dwordx4 v235, s[12:15], s81 offen offset:0 lds
v_max_f32 v158, v158, v159
MM a[96:99],a[180:183],v[68:71],a[96:99]
v_sub_f32 v140, 0xc1600000, v158
CV v7,v38,v39
EX v44,v44
MM a[104:107],a[180:183],v[76:79],a[104:107]
RD a[232:235],v226 offset:57344
v_sub_f32 v141, 0xc1600000, v158
MM a[104:107],a[176:179],v[72:75],a[104:107]
v_sub_f32 v142, 0xc1600000, v158
v_sub_f32 v143, 0xc1600000, v158
EX v45,v45
MM a[108:111],a[184:187],v[72:75],a[108:111]
RD a[236:239],v227 offset:57344
buffer_load_dwordx4 v235, s[12:15], s64 offen offset:1024 lds
v_mov_b32 v247, 0
v_mov_b32 v156, 0
v_mov_b32 v157, 0
MM a[108:111],a[188:191],v[76:79],a[108:111]
EX v46,v46
MM a[100:103],a[188:191],v[68:71],a[100:103]
RD a[240:243],v224 offset:61440
RD a[244:247],v225 offset:61440
RD a[248:251],v226 offset:61440
RD a[252:255],v227 offset:61440
EX v47,v47
MM a[100:103],a[184:187],v[64:67],a[100:103]
buffer_load_dwordx4 v235, s[12:15], s81 offen offset:2048 lds
CV v12,v40,v41
v_smfmac_f32_16x16x64_bf16 v[144:147], v[236:239], v[0:7], v234
CV v13,v42,v43
CV v14,v44,v45
CV v15,v46,v47
s_nop 1
v_smfmac_f32_16x16x64_bf16 v[148:151], v[236:239], v[8:15], v234
s_waitcnt lgkmcnt(0)
buffer_load_dwordx4 v235, s[12:15], s64 offen offset:3072 lds
MM v[64:67],a[192:195],v[160:163],v[128:131]
s_add_u32 s64, s64, 16384
TR a[128:129],v232 offset:32768
s_add_u32 s81, s64, 896
MM v[64:67],a[196:199],v[176:179],v[64:67]
TR a[130:131],v232 offset:36864
TR a[132:133],v232 offset:40960
MM v[64:67],a[200:203],v[192:195],v[64:67]
TR a[134:135],v232 offset:45056
TR a[136:137],v233 offset:32768
MM v[64:67],a[204:207],v[208:211],v[64:67]
v_max3_f32 v249, v144, v145, v148
v_max_f32 v249, v249, v149
MM v[68:71],a[220:223],v[208:211],v[128:131]
TR a[138:139],v233 offset:36864
v_cmp_lt_f32 vcc, s71, v249
MM v[68:71],a[216:219],v[192:195],v[68:71]
s_cbranch_vccnz LSITE2_%=
LBACK2_%=:
TR a[140:141],v233 offset:40960
v_add_f32 v244, v144, v145
MM v[68:71],a[212:215],v[176:179],v[68:71]
TR a[142:143],v233 offset:45056
v_add_f32 v245, v148, v149
MM v[68:71],a[208:211],v[160:163],v[68:71]
TR a[144:145],v232 offset:33280
TR a[146:147],v232 offset:37376
MM v[76:79],a[208:211],v[164:167],v[132:135]
s_lshl_b32 s90, %[swave], 10
s_add_u32 s90, s90, 81920
TR a[148:149],v232 offset:41472
MM v[76:79],a[212:215],v[180:183],v[76:79]
buffer_load_dwordx4 v[56:59], v248, s[20:23], s90 offen offset:0 sc1
MM v[76:79],a[216:219],v[196:199],v[76:79]
EX v64,v64
MM v[76:79],a[220:223],v[212:215],v[76:79]
EX v65,v65
MM v[72:75],a[204:207],v[212:215],v[132:135]
EX v66,v66
MM v[72:75],a[200:203],v[196:199],v[72:75]
EX v67,v67
MM v[72:75],a[196:199],v[180:183],v[72:75]
CV v64,v64,v65
CV v65,v66,v67
MM v[72:75],a[192:195],v[164:167],v[72:75]
EX v68,v68
MM v[80:83],a[192:195],v[168:171],v[136:139]
EX v69,v69
MM v[80:83],a[196:199],v[184:187],v[80:83]
EX v70,v70
MM v[80:83],a[200:203],v[200:203],v[80:83]
EX v71,v71
MM v[80:83],a[204:207],v[216:219],v[80:83]
CV v66,v68,v69
CV v67,v70,v71
MM v[84:87],a[220:223],v[216:219],v[136:139]
EX v72,v72
MM v[84:87],a[216:219],v[200:203],v[84:87]
EX v73,v73
MM v[84:87],a[212:215],v[184:187],v[84:87]
TR a[150:151],v232 offset:45568
TR a[152:153],v233 offset:33280
MM v[84:87],a[208:211],v[168:171],v[84:87]
EX v74,v74
MM v[92:95],a[208:211],v[172:175],v[140:143]
EX v75,v75
MM v[92:95],a[212:215],v[188:191],v[92:95]
TR a[154:155],v233 offset:37376
CV v72,v72,v73
MM v[92:95],a[216:219],v[204:207],v[92:95]
EX v76,v76
MM v[92:95],a[220:223],v[220:223],v[92:95]
EX v77,v77
MM v[88:91],a[204:207],v[220:223],v[140:143]
TR a[156:157],v233 offset:41472
CV v73,v74,v75
MM v[88:91],a[200:203],v[204:207],v[88:91]
EX v78,v78
MM v[88:91],a[196:199],v[188:191],v[88:91]
EX v79,v79
MM v[88:91],a[192:195],v[172:175],v[88:91]
TR a[158:159],v233 offset:45568
CV v74,v76,v77
MM v[96:99],a[224:227],v[160:163],v[128:131]
EX v80,v80
MM v[96:99],a[228:231],v[176:179],v[96:99]
EX v81,v81
MM v[96:99],a[232:235],v[192:195],v[96:99]
TR a[160:161],v232 offset:33792
TR a[162:163],v232 offset:37888
MM v[96:99],a[236:239],v[208:211],v[96:99]
EX v82,v82
MM v[100:103],a[252:255],v[208:211],v[128:131]
EX v83,v83
MM v[100:103],a[248:251],v[192:195],v[100:103]
TR a[164:165],v232 offset:41984
CV v75,v78,v79
MM v[100:103],a[244:247],v[176:179],v[100:103]
EX v84,v84
MM v[100:103],a[240:243],v[160:163],v[100:103]
EX v85,v85
MM v[108:111],a[240:243],v[164:167],v[132:135]
TR a[166:167],v232 offset:46080
CV v80,v80,v81
MM v[108:111],a[244:247],v[180:183],v[108:111]
EX v86,v86
MM v[108:111],a[248:251],v[196:199],v[108:111]
EX v87,v87
MM v[108:111],a[252:255],v[212:215],v[108:111]
TR a[168:169],v233 offset:33792
TR a[170:171],v233 offset:37888
MM v[104:107],a[236:239],v[212:215],v[132:135]
EX v88,v88
MM v[104:107],a[232:235],v[196:199],v[104:107]
EX v89,v89
MM v[104:107],a[228:231],v[180:183],v[104:107]
TR a[172:173],v233 offset:41984
CV v81,v82,v83
EX v90,v90
MM v[104:107],a[224:227],v[164:167],v[104:107]
TR a[174:175],v233 offset:46080
CV v82,v84,v85
MM v[112:115],a[224:227],v[168:171],v[136:139]
EX v91,v91
MM v[112:115],a[228:231],v[184:187],v[112:115]
EX v92,v92
MM v[112:115],a[232:235],v[200:203],v[112:115]
TR a[176:177],v232 offset:34304
EX v93,v93
MM v[112:115],a[236:239],v[216:219],v[112:115]
TR a[178:179],v232 offset:38400
CV v83,v86,v87
MM v[116:119],a[252:255],v[216:219],v[136:139]
EX v94,v94
MM v[116:119],a[248:251],v[200:203],v[116:119]
EX v95,v95
MM v[116:119],a[244:247],v[184:187],v[116:119]
TR a[180:181],v232 offset:42496
CV v88,v88,v89
MM v[116:119],a[240:243],v[168:171],v[116:119]
s_memrealtime s[56:57]
EX v96,v96
MM v[124:127],a[240:243],v[172:175],v[140:143]
s_memtime s[58:59]
TR a[182:183],v232 offset:46592
EX v97,v97
CV v89,v90,v91
MM v[124:127],a[244:247],v[188:191],v[124:127]
EX v98,v98
MM v[124:127],a[248:251],v[204:207],v[124:127]
TR a[184:185],v233 offset:34304
TR a[186:187],v233 offset:38400
TR a[188:189],v233 offset:42496
TR a[190:191],v233 offset:46592
MM v[124:127],a[252:255],v[220:223],v[124:127]
EX v99,v99
CV v90,v92,v93
MM v[120:123],a[236:239],v[220:223],v[140:143]
EX v100,v100
MM v[120:123],a[232:235],v[204:207],v[120:123]
EX v101,v101
CV v91,v94,v95
MM v[120:123],a[228:231],v[188:191],v[120:123]
EX v102,v102
MM v[120:123],a[224:227],v[172:175],v[120:123]
s_waitcnt vmcnt(13) lgkmcnt(0)
s_barrier
CV v68,v96,v97
MM a[0:3],a[128:131],v[0:3],a[0:3]
s_sub_u32 s52, s56, s52
s_sub_u32 s54, s58, s54
v_cvt_f32_u32 v250, s52
EX v103,v103
MM a[0:3],a[132:135],v[4:7],a[0:3]
s_add_u32 m0, s65, 32768
v_cvt_f32_u32 v251, s54
CV v69,v98,v99
EX v104,v104
MM a[8:11],a[132:135],v[12:15],a[8:11]
v_rcp_f32 v251, v251
MM a[8:11],a[128:131],v[8:11],a[8:11]
v_mul_f32 v250, 0x47abe000, v250
s_lshl_b32 s52, %[swave], 11
EX v105,v105
MM a[12:15],a[136:139],v[8:11],a[12:15]
v_mul_f32 v250, v250, v251
s_lshl_b32 s54, %[sb], 3
EX v106,v106
MM a[12:15],a[140:143],v[12:15],a[12:15]
v_cvt_u32_f32 v250, v250
s_add_u32 s52, s52, s54
EX v107,v107
MM a[4:7],a[140:143],v[4:7],a[4:7]
s_waitcnt vmcnt(0)
v_add_u32 v250, s56, v250
v_mov_b32 v251, s69
CV v70,v100,v101
MM a[4:7],a[136:139],v[0:3],a[4:7]
s_add_u32 s52, s52, 86016
s_mov_b64 exec, 1
buffer_store_dwordx2 v[250:251], off, s[20:23], s52 sc1
s_mov_b64 exec, -1
s_nop 0
EX v108,v108
MM a[32:35],a[144:147],v[0:3],a[32:35]
RD a[192:195],v224 offset:0
v_xor_b32 v56, s69, v56
v_xor_b32 v57, s69, v57
EX v109,v109
CV v71,v102,v103
MM a[32:35],a[148:151],v[4:7],a[32:35]
buffer_load_dwordx4 v228, s[8:11], s64 offen offset:0 lds
v_xor_b32 v58, s69, v58
v_xor_b32 v59, s69, v59
MM a[40:43],a[148:151],v[12:15],a[40:43]
RD a[196:199],v225 offset:0
v_or3_b32 v252, v56, v57, v58
)ASM"
R"ASM(
v_or_b32 v252, v252, v59
EX v110,v110
MM a[40:43],a[144:147],v[8:11],a[40:43]
v_cmp_ne_u32 vcc, 0, v252
s_cbranch_vccnz LRFR_%=
LRFRb_%=:
EX v111,v111
CV v76,v104,v105
MM a[44:47],a[152:155],v[8:11],a[44:47]
s_lshl_b32 s90, %[swave], 11
s_add_u32 s78, s90, %[slds]
s_add_u32 s78, s78, 155648
RD a[200:203],v226 offset:0
EX v112,v112
MM a[44:47],a[156:159],v[12:15],a[44:47]
s_add_u32 s90, s90, 53248
buffer_load_dwordx4 v229, s[8:11], s64 offen offset:1024 lds
MM a[36:39],a[156:159],v[4:7],a[36:39]
RD a[204:207],v227 offset:0
EX v113,v113
CV v77,v106,v107
MM a[36:39],a[152:155],v[0:3],a[36:39]
EX v114,v114
MM a[64:67],a[160:163],v[0:3],a[64:67]
RD a[208:211],v224 offset:4096
EX v115,v115
CV v78,v108,v109
MM a[64:67],a[164:167],v[4:7],a[64:67]
buffer_load_dwordx4 v230, s[8:11], s64 offen offset:2048 lds
MM a[72:75],a[164:167],v[12:15],a[72:75]
RD a[212:215],v225 offset:4096
EX v116,v116
MM a[72:75],a[160:163],v[8:11],a[72:75]
EX v117,v117
CV v79,v110,v111
MM a[76:79],a[168:171],v[8:11],a[76:79]
RD a[216:219],v226 offset:4096
CV v84,v112,v113
EX v118,v118
MM a[76:79],a[172:175],v[12:15],a[76:79]
buffer_load_dwordx4 v231, s[8:11], s64 offen offset:3072 lds
MM a[68:71],a[172:175],v[4:7],a[68:71]
s_add_u32 m0, s65, 98304
RD a[220:223],v227 offset:4096
EX v119,v119
MM a[68:71],a[168:171],v[0:3],a[68:71]
EX v120,v120
MM a[96:99],a[176:179],v[0:3],a[96:99]
RD a[224:227],v224 offset:8192
CV v85,v114,v115
EX v121,v121
MM a[96:99],a[180:183],v[4:7],a[96:99]
RD a[228:231],v225 offset:8192
CV v86,v116,v117
MM a[104:107],a[180:183],v[12:15],a[104:107]
buffer_load_dwordx4 v235, s[12:15], s81 offen offset:0 lds
EX v122,v122
MM a[104:107],a[176:179],v[8:11],a[104:107]
EX v123,v123
MM a[108:111],a[184:187],v[8:11],a[108:111]
RD a[232:235],v226 offset:8192
CV v87,v118,v119
MM a[108:111],a[188:191],v[12:15],a[108:111]
EX v124,v124
MM a[100:103],a[188:191],v[4:7],a[100:103]
RD a[236:239],v227 offset:8192
buffer_load_dwordx4 v235, s[12:15], s64 offen offset:1024 lds
EX v125,v125
MM a[100:103],a[184:187],v[0:3],a[100:103]
EX v126,v126
v_smfmac_f32_16x16x64_bf16 v[144:147], v[236:239], v[64:71], v234
RD a[240:243],v224 offset:12288
RD a[244:247],v225 offset:12288
RD a[248:251],v226 offset:12288
RD a[252:255],v227 offset:12288
EX v127,v127
v_smfmac_f32_16x16x64_bf16 v[148:151], v[236:239], v[72:79], v234
CV v92,v120,v121
v_smfmac_f32_16x16x64_bf16 v[152:155], v[236:239], v[80:87], v234
buffer_load_dwordx4 v235, s[12:15], s81 offen offset:2048 lds
CV v93,v122,v123
CV v94,v124,v125
CV v95,v126,v127
s_nop 1
v_smfmac_f32_16x16x64_bf16 v[156:159], v[236:239], v[88:95], v234
s_waitcnt lgkmcnt(0)
buffer_load_dwordx4 v235, s[12:15], s64 offen offset:3072 lds
MM v[0:3],a[192:195],v[160:163],v[128:131]
s_add_u32 s64, s64, 16384
TR a[128:129],v232 offset:49152
s_add_u32 s81, s64, 896
MM v[0:3],a[196:199],v[176:179],v[0:3]
TR a[130:131],v232 offset:53248
v_max3_f32 v249, v144, v145, v148
MM v[0:3],a[200:203],v[192:195],v[0:3]
TR a[132:133],v232 offset:57344
v_max3_f32 v249, v249, v149, v152
MM v[0:3],a[204:207],v[208:211],v[0:3]
TR a[134:135],v232 offset:61440
v_max3_f32 v249, v249, v153, v156
MM v[4:7],a[220:223],v[208:211],v[128:131]
TR a[136:137],v233 offset:49152
v_max_f32 v249, v249, v157
MM v[4:7],a[216:219],v[192:195],v[4:7]
TR a[138:139],v233 offset:53248
v_cmp_lt_f32 vcc, s71, v249
MM v[4:7],a[212:215],v[176:179],v[4:7]
TR a[140:141],v233 offset:57344
TR a[142:143],v233 offset:61440
MM v[4:7],a[208:211],v[160:163],v[4:7]
s_cbranch_vccnz LSITE3_%=
LBACK3_%=:
MM v[12:15],a[208:211],v[164:167],v[132:135]
s_mov_b32 m0, s78
EX v0,v0
MM v[12:15],a[212:215],v[180:183],v[12:15]
s_nop 0
buffer_load_dwordx4 v248, s[20:23], s90 offen offset:0 sc1 lds
MM v[12:15],a[216:219],v[196:199],v[12:15]
s_waitcnt lgkmcnt(6)
buffer_load_dwordx4 v248, s[20:23], s90 offen offset:1024 sc1 lds
MM v[12:15],a[220:223],v[212:215],v[12:15]
EX v1,v1
MM v[112:115],a[128:131],v[80:83],0
MM v[116:119],a[128:131],v[88:91],0
MM v[8:11],a[204:207],v[212:215],v[132:135]
EX v2,v2
MM v[8:11],a[200:203],v[196:199],v[8:11]
TR a[144:145],v232 offset:49664
CV v0,v0,v1
MM v[8:11],a[196:199],v[180:183],v[8:11]
EX v3,v3
MM v[8:11],a[192:195],v[164:167],v[8:11]
TR a[146:147],v232 offset:53760
CV v1,v2,v3
MM v[16:19],a[192:195],v[168:171],v[136:139]
v_max3_f32 v112, |v112|, |v113|, |v114|
v_max_f32 v112, v112, |v115|
MM v[16:19],a[196:199],v[184:187],v[16:19]
v_bfe_u32 v112, v112, 23, 8
v_add_u32 v112, 7, v112
MM v[16:19],a[200:203],v[200:203],v[16:19]
v_min_u32 v112, 240, v112
v_lshl_or_b32 v241, v112, 16, v241
MM v[16:19],a[204:207],v[216:219],v[16:19]
v_lshl_or_b32 v112, v112, 1, 1
v_lshlrev_b32 v112, 22, v112
EX v4,v4
MM v[20:23],a[220:223],v[216:219],v[136:139]
AW a116,v112
AW a117,v112
MM v[20:23],a[216:219],v[200:203],v[20:23]
AW a118,v112
AW a119,v112
EX v5,v5
MM v[20:23],a[212:215],v[184:187],v[20:23]
v_max3_f32 v116, |v116|, |v117|, |v118|
v_max_f32 v116, v116, |v119|
MM v[20:23],a[208:211],v[168:171],v[20:23]
v_bfe_u32 v116, v116, 23, 8
v_add_u32 v116, 7, v116
EX v6,v6
MM v[28:31],a[208:211],v[172:175],v[140:143]
v_min_u32 v116, 240, v116
v_lshl_or_b32 v241, v116, 24, v241
MM v[28:31],a[212:215],v[188:191],v[28:31]
TR a[148:149],v232 offset:57856
v_lshl_or_b32 v116, v116, 1, 1
v_lshlrev_b32 v116, 22, v116
EX v7,v7
MM v[28:31],a[216:219],v[204:207],v[28:31]
AW a124,v116
AW a125,v116
MM v[28:31],a[220:223],v[220:223],v[28:31]
TR a[150:151],v232 offset:61952
AW a126,v116
AW a127,v116
EX v8,v8
MM v[24:27],a[204:207],v[220:223],v[140:143]
TR a[152:153],v233 offset:49664
TR a[154:155],v233 offset:53760
MM v[24:27],a[200:203],v[204:207],v[24:27]
EX v9,v9
MM v[24:27],a[196:199],v[188:191],v[24:27]
EX v10,v10
MM v[24:27],a[192:195],v[172:175],v[24:27]
TR a[156:157],v233 offset:57856
CV v2,v4,v5
MM v[32:35],a[224:227],v[160:163],v[128:131]
EX v11,v11
MM v[32:35],a[228:231],v[176:179],v[32:35]
TR a[158:159],v233 offset:61952
CV v3,v6,v7
MM v[32:35],a[232:235],v[192:195],v[32:35]
EX v12,v12
MM v[32:35],a[236:239],v[208:211],v[32:35]
TR a[160:161],v232 offset:50176
TR a[162:163],v232 offset:54272
MM v[36:39],a[252:255],v[208:211],v[128:131]
EX v13,v13
MM v[36:39],a[248:251],v[192:195],v[36:39]
EX v14,v14
MM v[36:39],a[244:247],v[176:179],v[36:39]
TR a[164:165],v232 offset:58368
CV v8,v8,v9
MM v[36:39],a[240:243],v[160:163],v[36:39]
EX v15,v15
MM v[44:47],a[240:243],v[164:167],v[132:135]
s_lshl_b32 s76, s50, 1
LPG6_%=:
s_cbranch_execz LPG10_%=
TR a[166:167],v232 offset:62464
CV v9,v10,v11
MM v[44:47],a[244:247],v[180:183],v[44:47]
s_bfe_u32 s77, s49, 65538
EX v16,v16
MM v[44:47],a[248:251],v[196:199],v[44:47]
s_add_u32 s76, s76, s77
TR a[168:169],v233 offset:50176
TR a[170:171],v233 offset:54272
MM v[44:47],a[252:255],v[212:215],v[44:47]
s_lshl_b32 s76, s76, 2
EX v17,v17
MM v[40:43],a[236:239],v[212:215],v[132:135]
s_add_u32 s76, s76, s84
TR a[172:173],v233 offset:58368
CV v10,v12,v13
MM v[40:43],a[232:235],v[196:199],v[40:43]
s_lshl_b32 s76, s76, 2
EX v18,v18
MM v[40:43],a[228:231],v[180:183],v[40:43]
s_lshr_b32 s77, s49, 3
EX v19,v19
MM v[40:43],a[224:227],v[164:167],v[40:43]
s_add_u32 s76, s76, s77
TR a[174:175],v233 offset:62464
CV v11,v14,v15
MM v[48:51],a[224:227],v[168:171],v[136:139]
s_mul_i32 s95, s76, 33792
EX v20,v20
MM v[48:51],a[228:231],v[184:187],v[48:51]
s_lshl_b32 s76, %[swave], 13
TR a[176:177],v232 offset:50688
TR a[178:179],v232 offset:54784
MM v[48:51],a[232:235],v[200:203],v[48:51]
s_add_u32 s95, s95, s76
EX v21,v21
MM v[48:51],a[236:239],v[216:219],v[48:51]
s_add_u32 s95, s95, 22708224
v_mov_b32 v243, -1
CV v16,v16,v17
MM v[52:55],a[252:255],v[216:219],v[136:139]
s_mov_b32 s67, s95
EX v22,v22
MM v[52:55],a[248:251],v[200:203],v[52:55]
s_add_u32 s87, s95, 6144
buffer_store_dword v243, v248, s[20:23], s87 offen offset:12 sc1
MM v[52:55],a[244:247],v[184:187],v[52:55]
TR a[180:181],v232 offset:58880
TR a[182:183],v232 offset:62976
MM v[52:55],a[240:243],v[168:171],v[52:55]
EX v23,v23
MM v[60:63],a[240:243],v[172:175],v[140:143]
EX v24,v24
MM v[60:63],a[244:247],v[188:191],v[60:63]
TR a[184:185],v233 offset:50688
CV v17,v18,v19
MM v[60:63],a[248:251],v[204:207],v[60:63]
TR a[186:187],v233 offset:54784
TR a[188:189],v233 offset:58880
TR a[190:191],v233 offset:62976
EX v25,v25
MM v[60:63],a[252:255],v[220:223],v[60:63]
s_add_u32 s87, s95, 7168
buffer_store_dword v243, v248, s[20:23], s87 offen offset:12 sc1
MM v[56:59],a[236:239],v[220:223],v[140:143]
EX v26,v26
MM v[56:59],a[232:235],v[204:207],v[56:59]
EX v27,v27
MM v[56:59],a[228:231],v[188:191],v[56:59]
EX v28,v28
CV v18,v20,v21
MM v[56:59],a[224:227],v[172:175],v[56:59]
s_waitcnt lgkmcnt(0)
s_barrier
MM a[0:3],a[128:131],v[64:67],a[0:3]
s_add_u32 m0, s65, 49152
CV v19,v22,v23
CV v24,v24,v25
MM a[0:3],a[132:135],v[68:71],a[0:3]
EX v29,v29
MM a[8:11],a[132:135],v[76:79],a[8:11]
EX v30,v30
MM a[8:11],a[128:131],v[72:75],a[8:11]
CV v25,v26,v27
CV v26,v28,v29
MM a[16:19],a[128:131],v[80:83],a[116:119]
EX v31,v31
MM a[16:19],a[132:135],v[84:87],a[16:19]
EX v32,v32
MM a[24:27],a[132:135],v[92:95],a[124:127]
RD a[192:195],v224 offset:16384
CV v27,v30,v31
MM a[24:27],a[128:131],v[88:91],a[24:27]
EX v33,v33
MM a[28:31],a[136:139],v[88:91],a[124:127]
buffer_load_dwordx4 v228, s[8:11], s64 offen offset:0 lds
MM a[28:31],a[140:143],v[92:95],a[28:31]
EX v34,v34
MM a[20:23],a[140:143],v[84:87],a[116:119]
RD a[196:199],v225 offset:16384
CV v4,v32,v33
MM a[20:23],a[136:139],v[80:83],a[20:23]
EX v35,v35
MM a[12:15],a[136:139],v[72:75],a[12:15]
EX v36,v36
MM a[12:15],a[140:143],v[76:79],a[12:15]
RD a[200:203],v226 offset:16384
CV v5,v34,v35
MM a[4:7],a[140:143],v[68:71],a[4:7]
EX v37,v37
MM a[4:7],a[136:139],v[64:67],a[4:7]
buffer_load_dwordx4 v229, s[8:11], s64 offen offset:1024 lds
MM a[32:35],a[144:147],v[64:67],a[32:35]
EX v38,v38
MM a[32:35],a[148:151],v[68:71],a[32:35]
RD a[204:207],v227 offset:16384
CV v6,v36,v37
MM a[40:43],a[148:151],v[76:79],a[40:43]
EX v39,v39
MM a[40:43],a[144:147],v[72:75],a[40:43]
RD a[208:211],v224 offset:20480
CV v7,v38,v39
MM a[48:51],a[144:147],v[80:83],a[116:119]
EX v40,v40
MM a[48:51],a[148:151],v[84:87],a[48:51]
EX v41,v41
MM a[56:59],a[148:151],v[92:95],a[124:127]
buffer_load_dwordx4 v230, s[8:11], s64 offen offset:2048 lds
MM a[56:59],a[144:147],v[88:91],a[56:59]
EX v42,v42
MM a[60:63],a[152:155],v[88:91],a[124:127]
RD a[212:215],v225 offset:20480
CV v12,v40,v41
MM a[60:63],a[156:159],v[92:95],a[60:63]
EX v43,v43
MM a[52:55],a[156:159],v[84:87],a[116:119]
RD a[216:219],v226 offset:20480
CV v13,v42,v43
MM a[52:55],a[152:155],v[80:83],a[52:55]
EX v44,v44
MM a[44:47],a[152:155],v[72:75],a[44:47]
EX v45,v45
MM a[44:47],a[156:159],v[76:79],a[44:47]
buffer_load_dwordx4 v231, s[8:11], s64 offen offset:3072 lds
MM a[36:39],a[156:159],v[68:71],a[36:39]
s_add_u32 m0, s65, 114688
RD a[220:223],v227 offset:20480
CV v14,v44,v45
MM a[36:39],a[152:155],v[64:67],a[36:39]
EX v46,v46
MM a[64:67],a[160:163],v[64:67],a[64:67]
EX v47,v47
MM a[64:67],a[164:167],v[68:71],a[64:67]
RD a[224:227],v224 offset:24576
CV v15,v46,v47
MM a[72:75],a[164:167],v[76:79],a[72:75]
EX v48,v48
MM a[72:75],a[160:163],v[72:75],a[72:75]
buffer_load_dwordx4 v235, s[12:15], s81 offen offset:0 lds
MM a[80:83],a[160:163],v[80:83],a[116:119]
EX v49,v49
MM a[80:83],a[164:167],v[84:87],a[80:83]
RD a[228:231],v225 offset:24576
CV v20,v48,v49
MM a[88:91],a[164:167],v[92:95],a[124:127]
EX v50,v50
MM a[88:91],a[160:163],v[88:91],a[88:91]
EX v51,v51
MM a[92:95],a[168:171],v[88:91],a[124:127]
RD a[232:235],v226 offset:24576
CV v21,v50,v51
MM a[92:95],a[172:175],v[92:95],a[92:95]
EX v52,v52
MM a[84:87],a[172:175],v[84:87],a[116:119]
buffer_load_dwordx4 v235, s[12:15], s64 offen offset:1024 lds
MM a[84:87],a[168:171],v[80:83],a[84:87]
EX v53,v53
MM a[76:79],a[168:171],v[72:75],a[76:79]
RD a[236:239],v227 offset:24576
CV v22,v52,v53
MM a[76:79],a[172:175],v[76:79],a[76:79]
EX v54,v54
MM a[68:71],a[172:175],v[68:71],a[68:71]
EX v55,v55
MM a[68:71],a[168:171],v[64:67],a[68:71]
RD a[240:243],v224 offset:28672
CV v23,v54,v55
MM a[96:99],a[176:179],v[64:67],a[96:99]
EX v56,v56
MM a[96:99],a[180:183],v[68:71],a[96:99]
buffer_load_dwordx4 v235, s[12:15], s81 offen offset:2048 lds
)ASM"
R"ASM(
MM a[104:107],a[180:183],v[76:79],a[104:107]
EX v57,v57
MM a[104:107],a[176:179],v[72:75],a[104:107]
RD a[244:247],v225 offset:28672
CV v28,v56,v57
MM a[112:115],a[176:179],v[80:83],a[116:119]
EX v58,v58
MM a[112:115],a[180:183],v[84:87],a[112:115]
RD a[248:251],v226 offset:28672
RD a[252:255],v227 offset:28672
MM a[120:123],a[180:183],v[92:95],a[124:127]
EX v59,v59
MM a[120:123],a[176:179],v[88:91],a[120:123]
EX v60,v60
MM a[124:127],a[184:187],v[88:91],a[124:127]
v_add_f32 v244, v144, v145
v_add_f32 v245, v148, v149
MM a[124:127],a[188:191],v[92:95],a[124:127]
v_add_f32 v246, v152, v153
v_add_f32 v247, v156, v157
MM a[116:119],a[188:191],v[84:87],a[116:119]
EX v61,v61
MM a[116:119],a[184:187],v[80:83],a[116:119]
buffer_load_dwordx4 v235, s[12:15], s64 offen offset:3072 lds
MM a[108:111],a[184:187],v[72:75],a[108:111]
EX v62,v62
MM a[108:111],a[188:191],v[76:79],a[108:111]
EX v63,v63
MM a[100:103],a[188:191],v[68:71],a[100:103]
CV v29,v58,v59
CV v30,v60,v61
MM a[100:103],a[184:187],v[64:67],a[100:103]
CV v31,v62,v63
v_smfmac_f32_16x16x64_bf16 v[144:147], v[236:239], v[0:7], v234
v_smfmac_f32_16x16x64_bf16 v[148:151], v[236:239], v[8:15], v234
v_smfmac_f32_16x16x64_bf16 v[152:155], v[236:239], v[16:23], v234
v_smfmac_f32_16x16x64_bf16 v[156:159], v[236:239], v[24:31], v234
s_waitcnt lgkmcnt(0)
MM v[64:67],a[192:195],v[160:163],v[128:131]
s_add_u32 s64, s64, 16384
TR a[128:129],v232 offset:0
s_add_u32 s81, s64, 896
MM v[64:67],a[196:199],v[176:179],v[64:67]
TR a[130:131],v232 offset:4096
v_max3_f32 v249, v144, v145, v148
MM v[64:67],a[200:203],v[192:195],v[64:67]
TR a[132:133],v232 offset:8192
v_max3_f32 v249, v249, v149, v152
MM v[64:67],a[204:207],v[208:211],v[64:67]
TR a[134:135],v232 offset:12288
v_max3_f32 v249, v249, v153, v156
MM v[68:71],a[220:223],v[208:211],v[128:131]
TR a[136:137],v233 offset:0
v_max_f32 v249, v249, v157
MM v[68:71],a[216:219],v[192:195],v[68:71]
TR a[138:139],v233 offset:4096
v_cmp_lt_f32 vcc, s71, v249
MM v[68:71],a[212:215],v[176:179],v[68:71]
TR a[140:141],v233 offset:8192
TR a[142:143],v233 offset:12288
MM v[68:71],a[208:211],v[160:163],v[68:71]
s_cbranch_vccnz LSITE4_%=
LBACK4_%=:
MM v[76:79],a[208:211],v[164:167],v[132:135]
s_cmp_lg_u32 %[sb], 0
s_cbranch_scc1 LEP_%=
v_mov_b32 v250, s69
s_mov_b32 s76, 32768
s_mov_b64 exec, 1
buffer_store_dword v250, off, s[20:23], s76 sc1
s_mov_b64 exec, -1
s_nop 1
LEP_%=:
MM v[76:79],a[212:215],v[180:183],v[76:79]
EX v64,v64
MM v[76:79],a[216:219],v[196:199],v[76:79]
EX v65,v65
MM v[76:79],a[220:223],v[212:215],v[76:79]
TR a[144:145],v232 offset:512
CV v64,v64,v65
MM v[72:75],a[204:207],v[212:215],v[132:135]
TR a[146:147],v232 offset:4608
v_lshrrev_b32 v34, 1, v248
MM v[72:75],a[200:203],v[196:199],v[72:75]
s_mov_b32 s90, 86016
buffer_load_dwordx2 v[32:33], v34, s[20:23], s90 offen offset:0 sc1
MM v[72:75],a[196:199],v[180:183],v[72:75]
EX v66,v66
MM v[72:75],a[192:195],v[164:167],v[72:75]
EX v67,v67
MM v[80:83],a[192:195],v[168:171],v[136:139]
EX v68,v68
MM v[80:83],a[196:199],v[184:187],v[80:83]
TR a[148:149],v232 offset:8704
CV v65,v66,v67
MM v[80:83],a[200:203],v[200:203],v[80:83]
EX v69,v69
MM v[80:83],a[204:207],v[216:219],v[80:83]
TR a[150:151],v232 offset:12800
CV v66,v68,v69
MM v[84:87],a[220:223],v[216:219],v[136:139]
EX v70,v70
MM v[84:87],a[216:219],v[200:203],v[84:87]
TR a[152:153],v233 offset:512
TR a[154:155],v233 offset:4608
MM v[84:87],a[212:215],v[184:187],v[84:87]
EX v71,v71
MM v[84:87],a[208:211],v[168:171],v[84:87]
TR a[156:157],v233 offset:8704
CV v67,v70,v71
MM v[92:95],a[208:211],v[172:175],v[140:143]
EX v72,v72
MM v[92:95],a[212:215],v[188:191],v[92:95]
EX v73,v73
MM v[92:95],a[216:219],v[204:207],v[92:95]
TR a[158:159],v233 offset:12800
CV v72,v72,v73
MM v[92:95],a[220:223],v[220:223],v[92:95]
EX v74,v74
MM v[88:91],a[204:207],v[220:223],v[140:143]
TR a[160:161],v232 offset:1024
TR a[162:163],v232 offset:5120
MM v[88:91],a[200:203],v[204:207],v[88:91]
EX v75,v75
MM v[88:91],a[196:199],v[188:191],v[88:91]
TR a[164:165],v232 offset:9216
CV v73,v74,v75
MM v[88:91],a[192:195],v[172:175],v[88:91]
EX v76,v76
MM v[96:99],a[224:227],v[160:163],v[128:131]
EX v77,v77
MM v[96:99],a[228:231],v[176:179],v[96:99]
TR a[166:167],v232 offset:13312
CV v74,v76,v77
MM v[96:99],a[232:235],v[192:195],v[96:99]
EX v78,v78
MM v[96:99],a[236:239],v[208:211],v[96:99]
TR a[168:169],v233 offset:1024
TR a[170:171],v233 offset:5120
MM v[100:103],a[252:255],v[208:211],v[128:131]
EX v79,v79
MM v[100:103],a[248:251],v[192:195],v[100:103]
TR a[172:173],v233 offset:9216
CV v75,v78,v79
MM v[100:103],a[244:247],v[176:179],v[100:103]
EX v80,v80
MM v[100:103],a[240:243],v[160:163],v[100:103]
EX v81,v81
MM v[108:111],a[240:243],v[164:167],v[132:135]
TR a[174:175],v233 offset:13312
CV v80,v80,v81
MM v[108:111],a[244:247],v[180:183],v[108:111]
EX v82,v82
MM v[108:111],a[248:251],v[196:199],v[108:111]
TR a[176:177],v232 offset:1536
TR a[178:179],v232 offset:5632
MM v[108:111],a[252:255],v[212:215],v[108:111]
EX v83,v83
MM v[104:107],a[236:239],v[212:215],v[132:135]
TR a[180:181],v232 offset:9728
CV v81,v82,v83
MM v[104:107],a[232:235],v[196:199],v[104:107]
EX v84,v84
MM v[104:107],a[228:231],v[180:183],v[104:107]
TR a[182:183],v232 offset:13824
TR a[184:185],v233 offset:1536
MM v[104:107],a[224:227],v[164:167],v[104:107]
EX v85,v85
MM v[112:115],a[224:227],v[168:171],v[136:139]
TR a[186:187],v233 offset:5632
CV v82,v84,v85
MM v[112:115],a[228:231],v[184:187],v[112:115]
EX v86,v86
MM v[112:115],a[232:235],v[200:203],v[112:115]
EX v87,v87
MM v[112:115],a[236:239],v[216:219],v[112:115]
TR a[188:189],v233 offset:9728
CV v83,v86,v87
MM v[116:119],a[252:255],v[216:219],v[136:139]
EX v88,v88
MM v[116:119],a[248:251],v[200:203],v[116:119]
EX v89,v89
MM v[116:119],a[244:247],v[184:187],v[116:119]
TR a[190:191],v233 offset:13824
CV v88,v88,v89
MM v[116:119],a[240:243],v[168:171],v[116:119]
EX v90,v90
MM v[124:127],a[240:243],v[172:175],v[140:143]
EX v91,v91
MM v[124:127],a[244:247],v[188:191],v[124:127]
EX v92,v92
MM v[124:127],a[248:251],v[204:207],v[124:127]
EX v93,v93
MM v[124:127],a[252:255],v[220:223],v[124:127]
EX v94,v94
MM v[120:123],a[236:239],v[220:223],v[140:143]
EX v95,v95
MM v[120:123],a[232:235],v[204:207],v[120:123]
EX v96,v96
MM v[120:123],a[228:231],v[188:191],v[120:123]
EX v97,v97
MM v[120:123],a[224:227],v[172:175],v[120:123]
s_waitcnt vmcnt(11) lgkmcnt(0)
s_barrier
MM a[0:3],a[128:131],v[0:3],a[0:3]
s_add_u32 m0, s65, 0
CV v89,v90,v91
CV v90,v92,v93
MM a[0:3],a[132:135],v[4:7],a[0:3]
RD a[192:195],v224 offset:32768
CV v91,v94,v95
MM a[8:11],a[132:135],v[12:15],a[8:11]
buffer_load_dwordx4 v228, s[8:11], s64 offen offset:0 lds
MM a[8:11],a[128:131],v[8:11],a[8:11]
RD a[196:199],v225 offset:32768
CV v68,v96,v97
MM a[16:19],a[128:131],v[16:19],a[16:19]
RD a[200:203],v226 offset:32768
RD a[204:207],v227 offset:32768
MM a[16:19],a[132:135],v[20:23],a[16:19]
buffer_load_dwordx4 v229, s[8:11], s64 offen offset:1024 lds
MM a[24:27],a[132:135],v[28:31],a[24:27]
EX v98,v98
MM a[24:27],a[128:131],v[24:27],a[24:27]
RD a[208:211],v224 offset:36864
RD a[212:215],v225 offset:36864
MM a[28:31],a[136:139],v[24:27],a[28:31]
EX v99,v99
MM a[28:31],a[140:143],v[28:31],a[28:31]
RD a[216:219],v226 offset:36864
CV v69,v98,v99
MM a[20:23],a[140:143],v[20:23],a[20:23]
EX v100,v100
MM a[20:23],a[136:139],v[16:19],a[20:23]
EX v101,v101
MM a[12:15],a[136:139],v[8:11],a[12:15]
s_waitcnt vmcnt(2)
v_cmp_ne_u32 vcc, s69, v33
s_cbranch_vccnz LRRK_%=
LRRKb_%=:
MM a[12:15],a[140:143],v[12:15],a[12:15]
v_add_u32_dpp v32, v32, v32 row_ror:8 row_mask:0xf bank_mask:0xf
CV v70,v100,v101
MM a[4:7],a[140:143],v[4:7],a[4:7]
s_and_b32 s76, %[sb], 7
RD a[220:223],v227 offset:36864
v_mov_b32 v35, v32
MM a[4:7],a[136:139],v[0:3],a[4:7]
s_nop 0
v_permlane16_swap_b32 v32, v35
v_add_u32 v32, v32, v35
MM a[32:35],a[144:147],v[0:3],a[32:35]
RD a[224:227],v224 offset:40960
v_mov_b32 v35, v32
MM a[32:35],a[148:151],v[4:7],a[32:35]
s_nop 0
v_permlane32_swap_b32 v32, v35
v_add_u32 v32, v32, v35
MM a[40:43],a[148:151],v[12:15],a[40:43]
RD a[228:231],v225 offset:40960
v_readlane_b32 s77, v32, s76
MM a[40:43],a[144:147],v[8:11],a[40:43]
s_bfm_b32 s76, s76, 0
EX v102,v102
MM a[48:51],a[144:147],v[16:19],a[48:51]
v_cmp_gt_u32 s[60:61], s77, v32
v_cmp_eq_u32 s[62:63], s77, v32
MM a[48:51],a[148:151],v[20:23],a[48:51]
s_and_b32 s62, s62, s76
EX v103,v103
MM a[56:59],a[148:151],v[28:31],a[56:59]
s_or_b32 s60, s60, s62
EX v104,v104
MM a[56:59],a[144:147],v[24:27],a[56:59]
s_and_b32 s60, s60, 255
EX v105,v105
MM a[60:63],a[152:155],v[24:27],a[60:63]
s_bcnt1_i32_b32 s51, s60
s_cmp_lt_u32 s51, 4
s_cselect_b32 s82, 3, 99
MM a[60:63],a[156:159],v[28:31],a[60:63]
s_lshr_b32 s16, %[sb], 3
EX v106,v106
MM a[52:55],a[156:159],v[20:23],a[52:55]
s_lshl_b32 s76, s51, 5
CV v71,v102,v103
CV v76,v104,v105
MM a[52:55],a[152:155],v[16:19],a[52:55]
s_add_u32 s16, s16, s76
EX v107,v107
MM a[44:47],a[152:155],v[8:11],a[44:47]
s_lshl_b32 s54, s50, 2
buffer_load_dwordx4 v230, s[8:11], s64 offen offset:2048 lds
MM a[44:47],a[156:159],v[12:15],a[44:47]
s_add_u32 s54, s54, %[swave]
EX v108,v108
MM a[36:39],a[156:159],v[4:7],a[36:39]
s_lshl_b32 s54, s54, 1
EX v109,v109
MM a[36:39],a[152:155],v[0:3],a[36:39]
s_bfe_u32 s55, s49, 65538
CV v77,v106,v107
MM a[64:67],a[160:163],v[0:3],a[64:67]
s_xor_b32 s55, s55, 1
EX v110,v110
MM a[64:67],a[164:167],v[4:7],a[64:67]
s_add_u32 s54, s54, s55
EX v111,v111
MM a[72:75],a[164:167],v[12:15],a[72:75]
s_mul_i32 s52, s54, 40960
buffer_load_dwordx4 v231, s[8:11], s64 offen offset:3072 lds
MM a[72:75],a[160:163],v[8:11],a[72:75]
s_lshr_b32 s54, s84, 1
CV v78,v108,v109
CV v79,v110,v111
MM a[80:83],a[160:163],v[16:19],a[80:83]
s_mul_i32 s54, s54, 640
EX v112,v112
MM a[80:83],a[164:167],v[20:23],a[80:83]
s_add_u32 s52, s52, s54
EX v113,v113
MM a[88:91],a[164:167],v[28:31],a[88:91]
s_lshr_b32 s56, s49, 3
EX v114,v114
MM a[88:91],a[160:163],v[24:27],a[88:91]
s_and_b32 s54, s84, 1
s_add_u32 m0, s65, 65536
CV v84,v112,v113
MM a[92:95],a[168:171],v[24:27],a[92:95]
s_lshl_b32 s55, s56, 1
EX v115,v115
MM a[92:95],a[172:175],v[28:31],a[92:95]
s_add_u32 s55, s55, s54
buffer_load_dwordx4 v235, s[12:15], s81 offen offset:0 lds
MM a[84:87],a[172:175],v[20:23],a[84:87]
s_mul_i32 s53, s55, 5120
EX v116,v116
MM a[84:87],a[168:171],v[16:19],a[84:87]
s_add_u32 s53, s53, s52
RD a[232:235],v226 offset:40960
CV v85,v114,v115
MM a[76:79],a[168:171],v[8:11],a[76:79]
s_mul_i32 s54, s54, 384
EX v117,v117
MM a[76:79],a[172:175],v[12:15],a[76:79]
s_add_u32 s52, s52, s54
CV v86,v116,v117
MM a[68:71],a[172:175],v[4:7],a[68:71]
s_and_b32 s55, s56, 2
EX v118,v118
MM a[68:71],a[168:171],v[0:3],a[68:71]
s_lshl_b32 s55, s55, 6
buffer_load_dwordx4 v235, s[12:15], s64 offen offset:1024 lds
MM a[96:99],a[176:179],v[0:3],a[96:99]
s_add_u32 s52, s52, s55
EX v119,v119
MM a[96:99],a[180:183],v[4:7],a[96:99]
s_and_b32 s55, s56, 1
RD a[236:239],v227 offset:40960
CV v87,v118,v119
MM a[104:107],a[180:183],v[12:15],a[104:107]
s_mul_i32 s55, s55, 20480
EX v120,v120
MM a[104:107],a[176:179],v[8:11],a[104:107]
s_add_u32 s52, s52, s55
v_lshrrev_b32 v32, 7, v248
v_mul_u32_u24 v32, 1280, v32
MM a[112:115],a[176:179],v[16:19],a[112:115]
v_and_b32 v33, 112, v248
v_add_u32 v32, v32, v33
MM a[112:115],a[180:183],v[20:23],a[112:115]
buffer_store_dwordx4 v[236:239], v32, s[40:43], s52 offen offset:0 sc1
MM a[120:123],a[180:183],v[28:31],a[120:123]
s_add_u32 s52, s52, 10240
EX v121,v121
MM a[120:123],a[176:179],v[24:27],a[120:123]
EX v122,v122
MM a[124:127],a[184:187],v[24:27],a[124:127]
v_add_f32 v244, v144, v145
v_add_f32 v245, v148, v149
MM a[124:127],a[188:191],v[28:31],a[124:127]
v_add_f32 v246, v152, v153
v_add_f32 v247, v156, v157
MM a[116:119],a[188:191],v[20:23],a[116:119]
EX v123,v123
MM a[116:119],a[184:187],v[16:19],a[116:119]
EX v124,v124
MM a[108:111],a[184:187],v[8:11],a[108:111]
buffer_store_dwordx4 v[236:239], v32, s[40:43], s52 offen offset:0 sc1
MM a[108:111],a[188:191],v[12:15],a[108:111]
EX v125,v125
MM a[100:103],a[188:191],v[4:7],a[100:103]
RD a[240:243],v224 offset:45056
CV v92,v120,v121
MM a[100:103],a[184:187],v[0:3],a[100:103]
)ASM"
R"ASM(
EX v126,v126
v_smfmac_f32_16x16x64_bf16 v[144:147], v[236:239], v[64:71], v234
RD a[244:247],v225 offset:45056
RD a[248:251],v226 offset:45056
RD a[252:255],v227 offset:45056
EX v127,v127
v_smfmac_f32_16x16x64_bf16 v[148:151], v[236:239], v[72:79], v234
CV v93,v122,v123
v_smfmac_f32_16x16x64_bf16 v[152:155], v[236:239], v[80:87], v234
buffer_load_dwordx4 v235, s[12:15], s81 offen offset:2048 lds
CV v94,v124,v125
CV v95,v126,v127
s_nop 1
v_smfmac_f32_16x16x64_bf16 v[156:159], v[236:239], v[88:95], v234
s_waitcnt lgkmcnt(0)
buffer_load_dwordx4 v235, s[12:15], s64 offen offset:3072 lds
s_mov_b32 exec_hi, 0
buffer_store_dwordx4 v[236:239], v32, s[40:43], s53 offen offset:256 sc1
s_mov_b64 exec, -1
s_nop 0
s_cmp_lt_u32 s51, 4
s_cbranch_scc1 LLAR_%=
s_mov_b32 s70, 6
s_branch LLAP_%=
s_nop 0
s_nop 0
s_nop 0
s_nop 0
LPG7_%=:
s_cbranch_execz LPG11_%=
s_nop 0
s_nop 0
s_nop 0
s_nop 0
s_nop 0
s_nop 0
s_nop 0
s_nop 0
s_nop 0
s_nop 0
s_nop 0
s_nop 0
s_nop 0
s_nop 0
s_nop 0
s_nop 0
s_nop 0
s_nop 0
s_nop 0
s_nop 0
s_nop 0
s_nop 0
s_nop 0
s_nop 0
s_nop 0
s_nop 0
s_nop 0
s_nop 0
s_nop 0
s_nop 0
s_nop 0
s_nop 0
s_nop 0
s_nop 0
s_nop 0
s_nop 0
s_nop 0
s_nop 0
s_nop 0
s_nop 0
s_nop 0
s_nop 0
s_nop 0
s_nop 0
s_nop 0
s_nop 0
s_nop 0
s_nop 0
s_nop 0
s_nop 0
s_nop 0
s_nop 0
s_nop 0
s_nop 0
s_nop 0
s_nop 0
s_nop 0
s_nop 0
s_nop 0
s_nop 0
s_nop 0
s_nop 0
s_nop 0
LLAP_%=:
MM v[0:3],a[192:195],v[160:163],v[128:131]
s_add_u32 s64, s64, 16384
TR a[128:129],v232 offset:16384
s_add_u32 s81, s64, 896
MM v[0:3],a[196:199],v[176:179],v[0:3]
TR a[130:131],v232 offset:20480
v_max3_f32 v249, v144, v145, v148
MM v[0:3],a[200:203],v[192:195],v[0:3]
TR a[132:133],v232 offset:24576
v_max3_f32 v249, v249, v149, v152
MM v[0:3],a[204:207],v[208:211],v[0:3]
TR a[134:135],v232 offset:28672
v_max3_f32 v249, v249, v153, v156
MM v[4:7],a[220:223],v[208:211],v[128:131]
TR a[136:137],v233 offset:16384
v_max_f32 v249, v249, v157
MM v[4:7],a[216:219],v[192:195],v[4:7]
TR a[138:139],v233 offset:20480
v_cmp_lt_f32 vcc, s71, v249
MM v[4:7],a[212:215],v[176:179],v[4:7]
TR a[140:141],v233 offset:24576
TR a[142:143],v233 offset:28672
MM v[4:7],a[208:211],v[160:163],v[4:7]
s_cbranch_vccnz LSITE5_%=
LBACK5_%=:
MM v[12:15],a[208:211],v[164:167],v[132:135]
EX v0,v0
MM v[12:15],a[212:215],v[180:183],v[12:15]
EX v1,v1
MM v[12:15],a[216:219],v[196:199],v[12:15]
TR a[144:145],v232 offset:16896
CV v0,v0,v1
MM v[12:15],a[220:223],v[212:215],v[12:15]
EX v2,v2
MM v[8:11],a[204:207],v[212:215],v[132:135]
EX v3,v3
MM v[8:11],a[200:203],v[196:199],v[8:11]
TR a[146:147],v232 offset:20992
CV v1,v2,v3
MM v[8:11],a[196:199],v[180:183],v[8:11]
EX v4,v4
MM v[8:11],a[192:195],v[164:167],v[8:11]
TR a[148:149],v232 offset:25088
TR a[150:151],v232 offset:29184
MM v[16:19],a[192:195],v[168:171],v[136:139]
EX v5,v5
MM v[16:19],a[196:199],v[184:187],v[16:19]
TR a[152:153],v233 offset:16896
CV v2,v4,v5
MM v[16:19],a[200:203],v[200:203],v[16:19]
EX v6,v6
MM v[16:19],a[204:207],v[216:219],v[16:19]
TR a[154:155],v233 offset:20992
TR a[156:157],v233 offset:25088
MM v[20:23],a[220:223],v[216:219],v[136:139]
EX v7,v7
MM v[20:23],a[216:219],v[200:203],v[20:23]
TR a[158:159],v233 offset:29184
CV v3,v6,v7
MM v[20:23],a[212:215],v[184:187],v[20:23]
EX v8,v8
MM v[20:23],a[208:211],v[168:171],v[20:23]
EX v9,v9
MM v[28:31],a[208:211],v[172:175],v[140:143]
TR a[160:161],v232 offset:17408
CV v8,v8,v9
MM v[28:31],a[212:215],v[188:191],v[28:31]
EX v10,v10
MM v[28:31],a[216:219],v[204:207],v[28:31]
TR a[162:163],v232 offset:21504
TR a[164:165],v232 offset:25600
MM v[28:31],a[220:223],v[220:223],v[28:31]
EX v11,v11
MM v[24:27],a[204:207],v[220:223],v[140:143]
TR a[166:167],v232 offset:29696
CV v9,v10,v11
MM v[24:27],a[200:203],v[204:207],v[24:27]
EX v12,v12
MM v[24:27],a[196:199],v[188:191],v[24:27]
EX v13,v13
MM v[24:27],a[192:195],v[172:175],v[24:27]
TR a[168:169],v233 offset:17408
CV v10,v12,v13
MM v[32:35],a[224:227],v[160:163],v[128:131]
EX v14,v14
MM v[32:35],a[228:231],v[176:179],v[32:35]
TR a[170:171],v233 offset:21504
TR a[172:173],v233 offset:25600
MM v[32:35],a[232:235],v[192:195],v[32:35]
EX v15,v15
MM v[32:35],a[236:239],v[208:211],v[32:35]
TR a[174:175],v233 offset:29696
CV v11,v14,v15
MM v[36:39],a[252:255],v[208:211],v[128:131]
EX v16,v16
MM v[36:39],a[248:251],v[192:195],v[36:39]
EX v17,v17
MM v[36:39],a[244:247],v[176:179],v[36:39]
TR a[176:177],v232 offset:17920
CV v16,v16,v17
MM v[36:39],a[240:243],v[160:163],v[36:39]
EX v18,v18
MM v[44:47],a[240:243],v[164:167],v[132:135]
TR a[178:179],v232 offset:22016
TR a[180:181],v232 offset:26112
MM v[44:47],a[244:247],v[180:183],v[44:47]
EX v19,v19
MM v[44:47],a[248:251],v[196:199],v[44:47]
TR a[182:183],v232 offset:30208
CV v17,v18,v19
MM v[44:47],a[252:255],v[212:215],v[44:47]
EX v20,v20
MM v[40:43],a[236:239],v[212:215],v[132:135]
TR a[184:185],v233 offset:17920
TR a[186:187],v233 offset:22016
MM v[40:43],a[232:235],v[196:199],v[40:43]
EX v21,v21
MM v[40:43],a[228:231],v[180:183],v[40:43]
TR a[188:189],v233 offset:26112
CV v18,v20,v21
MM v[40:43],a[224:227],v[164:167],v[40:43]
EX v22,v22
MM v[48:51],a[224:227],v[168:171],v[136:139]
EX v23,v23
MM v[48:51],a[228:231],v[184:187],v[48:51]
TR a[190:191],v233 offset:30208
CV v19,v22,v23
MM v[48:51],a[232:235],v[200:203],v[48:51]
EX v24,v24
MM v[48:51],a[236:239],v[216:219],v[48:51]
EX v25,v25
MM v[52:55],a[252:255],v[216:219],v[136:139]
EX v26,v26
MM v[52:55],a[248:251],v[200:203],v[52:55]
EX v27,v27
MM v[52:55],a[244:247],v[184:187],v[52:55]
EX v28,v28
MM v[52:55],a[240:243],v[168:171],v[52:55]
EX v29,v29
MM v[60:63],a[240:243],v[172:175],v[140:143]
EX v30,v30
MM v[60:63],a[244:247],v[188:191],v[60:63]
EX v31,v31
MM v[60:63],a[248:251],v[204:207],v[60:63]
EX v32,v32
MM v[60:63],a[252:255],v[220:223],v[60:63]
EX v33,v33
MM v[56:59],a[236:239],v[220:223],v[140:143]
EX v34,v34
MM v[56:59],a[232:235],v[204:207],v[56:59]
EX v35,v35
MM v[56:59],a[228:231],v[188:191],v[56:59]
EX v36,v36
MM v[56:59],a[224:227],v[172:175],v[56:59]
s_waitcnt vmcnt(12) lgkmcnt(0)
s_barrier
MM a[0:3],a[128:131],v[64:67],a[0:3]
s_add_u32 m0, s65, 16384
CV v24,v24,v25
CV v25,v26,v27
MM a[0:3],a[132:135],v[68:71],a[0:3]
RD a[192:195],v224 offset:49152
CV v26,v28,v29
MM a[8:11],a[132:135],v[76:79],a[8:11]
RD a[196:199],v225 offset:49152
CV v27,v30,v31
MM a[8:11],a[128:131],v[72:75],a[8:11]
buffer_load_dwordx4 v228, s[8:11], s64 offen offset:0 lds
MM a[16:19],a[128:131],v[80:83],a[16:19]
RD a[200:203],v226 offset:49152
CV v4,v32,v33
MM a[16:19],a[132:135],v[84:87],a[16:19]
RD a[204:207],v227 offset:49152
CV v5,v34,v35
MM a[24:27],a[132:135],v[92:95],a[24:27]
buffer_load_dwordx4 v229, s[8:11], s64 offen offset:1024 lds
MM a[24:27],a[128:131],v[88:91],a[24:27]
RD a[208:211],v224 offset:53248
RD a[212:215],v225 offset:53248
MM a[28:31],a[136:139],v[88:91],a[28:31]
buffer_load_dwordx4 v230, s[8:11], s64 offen offset:2048 lds
MM a[28:31],a[140:143],v[92:95],a[28:31]
RD a[216:219],v226 offset:53248
RD a[220:223],v227 offset:53248
MM a[20:23],a[140:143],v[84:87],a[20:23]
EX v37,v37
MM a[20:23],a[136:139],v[80:83],a[20:23]
RD a[224:227],v224 offset:57344
CV v6,v36,v37
MM a[12:15],a[136:139],v[72:75],a[12:15]
EX v38,v38
MM a[12:15],a[140:143],v[76:79],a[12:15]
buffer_load_dwordx4 v231, s[8:11], s64 offen offset:3072 lds
MM a[4:7],a[140:143],v[68:71],a[4:7]
s_add_u32 m0, s65, 81920
EX v39,v39
MM a[4:7],a[136:139],v[64:67],a[4:7]
RD a[228:231],v225 offset:57344
CV v7,v38,v39
MM a[32:35],a[144:147],v[64:67],a[32:35]
EX v40,v40
MM a[32:35],a[148:151],v[68:71],a[32:35]
EX v41,v41
MM a[40:43],a[148:151],v[76:79],a[40:43]
RD a[232:235],v226 offset:57344
CV v12,v40,v41
MM a[40:43],a[144:147],v[72:75],a[40:43]
EX v42,v42
MM a[48:51],a[144:147],v[80:83],a[48:51]
buffer_load_dwordx4 v235, s[12:15], s81 offen offset:0 lds
MM a[48:51],a[148:151],v[84:87],a[48:51]
EX v43,v43
MM a[56:59],a[148:151],v[92:95],a[56:59]
RD a[236:239],v227 offset:57344
CV v13,v42,v43
MM a[56:59],a[144:147],v[88:91],a[56:59]
EX v44,v44
MM a[60:63],a[152:155],v[88:91],a[60:63]
EX v45,v45
MM a[60:63],a[156:159],v[92:95],a[60:63]
RD a[240:243],v224 offset:61440
CV v14,v44,v45
MM a[52:55],a[156:159],v[84:87],a[52:55]
EX v46,v46
MM a[52:55],a[152:155],v[80:83],a[52:55]
buffer_load_dwordx4 v235, s[12:15], s64 offen offset:1024 lds
MM a[44:47],a[152:155],v[72:75],a[44:47]
EX v47,v47
MM a[44:47],a[156:159],v[76:79],a[44:47]
RD a[244:247],v225 offset:61440
CV v15,v46,v47
MM a[36:39],a[156:159],v[68:71],a[36:39]
EX v48,v48
MM a[36:39],a[152:155],v[64:67],a[36:39]
EX v49,v49
MM a[64:67],a[160:163],v[64:67],a[64:67]
RD a[248:251],v226 offset:61440
CV v20,v48,v49
MM a[64:67],a[164:167],v[68:71],a[64:67]
EX v50,v50
MM a[72:75],a[164:167],v[76:79],a[72:75]
buffer_load_dwordx4 v235, s[12:15], s81 offen offset:2048 lds
MM a[72:75],a[160:163],v[72:75],a[72:75]
EX v51,v51
MM a[80:83],a[160:163],v[80:83],a[80:83]
RD a[252:255],v227 offset:61440
CV v21,v50,v51
MM a[80:83],a[164:167],v[84:87],a[80:83]
EX v52,v52
MM a[88:91],a[164:167],v[92:95],a[88:91]
EX v53,v53
MM a[88:91],a[160:163],v[88:91],a[88:91]
buffer_load_dwordx4 v235, s[12:15], s64 offen offset:3072 lds
MM a[92:95],a[168:171],v[88:91],a[92:95]
EX v54,v54
MM a[92:95],a[172:175],v[92:95],a[92:95]
EX v55,v55
MM a[84:87],a[172:175],v[84:87],a[84:87]
EX v56,v56
MM a[84:87],a[168:171],v[80:83],a[84:87]
EX v57,v57
MM a[76:79],a[168:171],v[72:75],a[76:79]
EX v58,v58
MM a[76:79],a[172:175],v[76:79],a[76:79]
EX v59,v59
MM a[68:71],a[172:175],v[68:71],a[68:71]
EX v60,v60
MM a[68:71],a[168:171],v[64:67],a[68:71]
EX v61,v61
MM a[96:99],a[176:179],v[64:67],a[96:99]
EX v62,v62
MM a[96:99],a[180:183],v[68:71],a[96:99]
EX v63,v63
MM a[104:107],a[180:183],v[76:79],a[104:107]
CV v22,v52,v53
CV v23,v54,v55
MM a[104:107],a[176:179],v[72:75],a[104:107]
CV v28,v56,v57
CV v29,v58,v59
MM a[112:115],a[176:179],v[80:83],a[112:115]
CV v30,v60,v61
CV v31,v62,v63
MM a[112:115],a[180:183],v[84:87],a[112:115]
MM a[120:123],a[180:183],v[92:95],a[120:123]
MM a[120:123],a[176:179],v[88:91],a[120:123]
MM a[124:127],a[184:187],v[88:91],a[124:127]
v_add_f32 v244, v144, v145
v_add_f32 v245, v148, v149
MM a[124:127],a[188:191],v[92:95],a[124:127]
v_add_f32 v246, v152, v153
v_add_f32 v247, v156, v157
MM a[116:119],a[188:191],v[84:87],a[116:119]
s_cmp_eq_u32 s70, s82
s_cbranch_scc1 LWW_%=
LLALWW_%=:
MM a[116:119],a[184:187],v[80:83],a[116:119]
MM a[108:111],a[184:187],v[72:75],a[108:111]
MM a[108:111],a[188:191],v[76:79],a[108:111]
MM a[100:103],a[188:191],v[68:71],a[100:103]
MM a[100:103],a[184:187],v[64:67],a[100:103]
v_smfmac_f32_16x16x64_bf16 v[144:147], v[236:239], v[0:7], v234
v_smfmac_f32_16x16x64_bf16 v[148:151], v[236:239], v[8:15], v234
v_smfmac_f32_16x16x64_bf16 v[152:155], v[236:239], v[16:23], v234
v_smfmac_f32_16x16x64_bf16 v[156:159], v[236:239], v[24:31], v234
s_waitcnt lgkmcnt(0)
MM v[64:67],a[192:195],v[160:163],v[128:131]
s_add_u32 s64, s64, 16384
TR a[128:129],v232 offset:32768
s_add_u32 s81, s64, 896
MM v[64:67],a[196:199],v[176:179],v[64:67]
TR a[130:131],v232 offset:36864
v_max3_f32 v249, v144, v145, v148
MM v[64:67],a[200:203],v[192:195],v[64:67]
TR a[132:133],v232 offset:40960
v_max3_f32 v249, v249, v149, v152
MM v[64:67],a[204:207],v[208:211],v[64:67]
TR a[134:135],v232 offset:45056
v_max3_f32 v249, v249, v153, v156
MM v[68:71],a[220:223],v[208:211],v[128:131]
TR a[136:137],v233 offset:32768
v_max_f32 v249, v249, v157
MM v[68:71],a[216:219],v[192:195],v[68:71]
TR a[138:139],v233 offset:36864
v_cmp_lt_f32 vcc, s71, v249
MM v[68:71],a[212:215],v[176:179],v[68:71]
TR a[140:141],v233 offset:40960
TR a[142:143],v233 offset:45056
MM v[68:71],a[208:211],v[160:163],v[68:71]
s_cbranch_vccnz LSITE6_%=
LBACK6_%=:
MM v[76:79],a[208:211],v[164:167],v[132:135]
EX v64,v64
MM v[76:79],a[212:215],v[180:183],v[76:79]
EX v65,v65
MM v[76:79],a[216:219],v[196:199],v[76:79]
TR a[144:145],v232 offset:33280
CV v64,v64,v65
MM v[76:79],a[220:223],v[212:215],v[76:79]
EX v66,v66
MM v[72:75],a[204:207],v[212:215],v[132:135]
EX v67,v67
MM v[72:75],a[200:203],v[196:199],v[72:75]
TR a[146:147],v232 offset:37376
CV v65,v66,v67
MM v[72:75],a[196:199],v[180:183],v[72:75]
)ASM"
R"ASM(
EX v68,v68
MM v[72:75],a[192:195],v[164:167],v[72:75]
TR a[148:149],v232 offset:41472
TR a[150:151],v232 offset:45568
MM v[80:83],a[192:195],v[168:171],v[136:139]
EX v69,v69
MM v[80:83],a[196:199],v[184:187],v[80:83]
TR a[152:153],v233 offset:33280
CV v66,v68,v69
MM v[80:83],a[200:203],v[200:203],v[80:83]
EX v70,v70
MM v[80:83],a[204:207],v[216:219],v[80:83]
TR a[154:155],v233 offset:37376
TR a[156:157],v233 offset:41472
MM v[84:87],a[220:223],v[216:219],v[136:139]
EX v71,v71
MM v[84:87],a[216:219],v[200:203],v[84:87]
TR a[158:159],v233 offset:45568
CV v67,v70,v71
MM v[84:87],a[212:215],v[184:187],v[84:87]
EX v72,v72
MM v[84:87],a[208:211],v[168:171],v[84:87]
EX v73,v73
MM v[92:95],a[208:211],v[172:175],v[140:143]
TR a[160:161],v232 offset:33792
CV v72,v72,v73
MM v[92:95],a[212:215],v[188:191],v[92:95]
EX v74,v74
MM v[92:95],a[216:219],v[204:207],v[92:95]
TR a[162:163],v232 offset:37888
TR a[164:165],v232 offset:41984
MM v[92:95],a[220:223],v[220:223],v[92:95]
EX v75,v75
MM v[88:91],a[204:207],v[220:223],v[140:143]
TR a[166:167],v232 offset:46080
CV v73,v74,v75
MM v[88:91],a[200:203],v[204:207],v[88:91]
EX v76,v76
MM v[88:91],a[196:199],v[188:191],v[88:91]
EX v77,v77
MM v[88:91],a[192:195],v[172:175],v[88:91]
TR a[168:169],v233 offset:33792
CV v74,v76,v77
MM v[96:99],a[224:227],v[160:163],v[128:131]
EX v78,v78
MM v[96:99],a[228:231],v[176:179],v[96:99]
TR a[170:171],v233 offset:37888
TR a[172:173],v233 offset:41984
MM v[96:99],a[232:235],v[192:195],v[96:99]
EX v79,v79
MM v[96:99],a[236:239],v[208:211],v[96:99]
TR a[174:175],v233 offset:46080
CV v75,v78,v79
MM v[100:103],a[252:255],v[208:211],v[128:131]
EX v80,v80
MM v[100:103],a[248:251],v[192:195],v[100:103]
EX v81,v81
MM v[100:103],a[244:247],v[176:179],v[100:103]
TR a[176:177],v232 offset:34304
CV v80,v80,v81
MM v[100:103],a[240:243],v[160:163],v[100:103]
EX v82,v82
MM v[108:111],a[240:243],v[164:167],v[132:135]
buffer_store_dword v243, v248, s[20:23], s67 offen offset:12 sc1
MM v[108:111],a[244:247],v[180:183],v[108:111]
s_add_u32 s67, s67, 1024
TR a[178:179],v232 offset:38400
TR a[180:181],v232 offset:42496
MM v[108:111],a[248:251],v[196:199],v[108:111]
EX v83,v83
MM v[108:111],a[252:255],v[212:215],v[108:111]
TR a[182:183],v232 offset:46592
CV v81,v82,v83
MM v[104:107],a[236:239],v[212:215],v[132:135]
EX v84,v84
MM v[104:107],a[232:235],v[196:199],v[104:107]
EX v85,v85
MM v[104:107],a[228:231],v[180:183],v[104:107]
TR a[184:185],v233 offset:34304
CV v82,v84,v85
MM v[104:107],a[224:227],v[164:167],v[104:107]
EX v86,v86
MM v[112:115],a[224:227],v[168:171],v[136:139]
TR a[186:187],v233 offset:38400
TR a[188:189],v233 offset:42496
MM v[112:115],a[228:231],v[184:187],v[112:115]
EX v87,v87
MM v[112:115],a[232:235],v[200:203],v[112:115]
TR a[190:191],v233 offset:46592
CV v83,v86,v87
MM v[112:115],a[236:239],v[216:219],v[112:115]
EX v88,v88
MM v[116:119],a[252:255],v[216:219],v[136:139]
EX v89,v89
MM v[116:119],a[248:251],v[200:203],v[116:119]
EX v90,v90
MM v[116:119],a[244:247],v[184:187],v[116:119]
EX v91,v91
MM v[116:119],a[240:243],v[168:171],v[116:119]
EX v92,v92
MM v[124:127],a[240:243],v[172:175],v[140:143]
EX v93,v93
MM v[124:127],a[244:247],v[188:191],v[124:127]
EX v94,v94
MM v[124:127],a[248:251],v[204:207],v[124:127]
EX v95,v95
MM v[124:127],a[252:255],v[220:223],v[124:127]
EX v96,v96
MM v[120:123],a[236:239],v[220:223],v[140:143]
EX v97,v97
MM v[120:123],a[232:235],v[204:207],v[120:123]
EX v98,v98
MM v[120:123],a[228:231],v[188:191],v[120:123]
EX v99,v99
MM v[120:123],a[224:227],v[172:175],v[120:123]
s_waitcnt vmcnt(12) lgkmcnt(0)
s_barrier
MM a[0:3],a[128:131],v[0:3],a[0:3]
s_add_u32 m0, s65, 32768
CV v88,v88,v89
CV v89,v90,v91
MM a[0:3],a[132:135],v[4:7],a[0:3]
RD a[192:195],v224 offset:0
CV v90,v92,v93
MM a[8:11],a[132:135],v[12:15],a[8:11]
RD a[196:199],v225 offset:0
CV v91,v94,v95
MM a[8:11],a[128:131],v[8:11],a[8:11]
buffer_load_dwordx4 v228, s[8:11], s64 offen offset:0 lds
MM a[16:19],a[128:131],v[16:19],a[16:19]
RD a[200:203],v226 offset:0
CV v68,v96,v97
MM a[16:19],a[132:135],v[20:23],a[16:19]
RD a[204:207],v227 offset:0
CV v69,v98,v99
MM a[24:27],a[132:135],v[28:31],a[24:27]
buffer_load_dwordx4 v229, s[8:11], s64 offen offset:1024 lds
MM a[24:27],a[128:131],v[24:27],a[24:27]
RD a[208:211],v224 offset:4096
RD a[212:215],v225 offset:4096
MM a[28:31],a[136:139],v[24:27],a[28:31]
buffer_load_dwordx4 v230, s[8:11], s64 offen offset:2048 lds
MM a[28:31],a[140:143],v[28:31],a[28:31]
EX v100,v100
MM a[20:23],a[140:143],v[20:23],a[20:23]
EX v101,v101
MM a[20:23],a[136:139],v[16:19],a[20:23]
RD a[216:219],v226 offset:4096
CV v70,v100,v101
MM a[12:15],a[136:139],v[8:11],a[12:15]
EX v102,v102
MM a[12:15],a[140:143],v[12:15],a[12:15]
RD a[220:223],v227 offset:4096
RD a[224:227],v224 offset:8192
MM a[4:7],a[140:143],v[4:7],a[4:7]
EX v103,v103
MM a[4:7],a[136:139],v[0:3],a[4:7]
RD a[228:231],v225 offset:8192
CV v71,v102,v103
MM a[32:35],a[144:147],v[0:3],a[32:35]
EX v104,v104
MM a[32:35],a[148:151],v[4:7],a[32:35]
buffer_load_dwordx4 v231, s[8:11], s64 offen offset:3072 lds
MM a[40:43],a[148:151],v[12:15],a[40:43]
s_add_u32 m0, s65, 98304
EX v105,v105
MM a[40:43],a[144:147],v[8:11],a[40:43]
RD a[232:235],v226 offset:8192
CV v76,v104,v105
MM a[48:51],a[144:147],v[16:19],a[48:51]
EX v106,v106
MM a[48:51],a[148:151],v[20:23],a[48:51]
EX v107,v107
MM a[56:59],a[148:151],v[28:31],a[56:59]
RD a[236:239],v227 offset:8192
CV v77,v106,v107
MM a[56:59],a[144:147],v[24:27],a[56:59]
EX v108,v108
MM a[60:63],a[152:155],v[24:27],a[60:63]
buffer_load_dwordx4 v235, s[12:15], s81 offen offset:0 lds
MM a[60:63],a[156:159],v[28:31],a[60:63]
EX v109,v109
MM a[52:55],a[156:159],v[20:23],a[52:55]
RD a[240:243],v224 offset:12288
CV v78,v108,v109
MM a[52:55],a[152:155],v[16:19],a[52:55]
EX v110,v110
MM a[44:47],a[152:155],v[8:11],a[44:47]
EX v111,v111
MM a[44:47],a[156:159],v[12:15],a[44:47]
RD a[244:247],v225 offset:12288
CV v79,v110,v111
MM a[36:39],a[156:159],v[4:7],a[36:39]
buffer_load_dwordx4 v235, s[12:15], s64 offen offset:1024 lds
MM a[36:39],a[152:155],v[0:3],a[36:39]
EX v112,v112
MM a[64:67],a[160:163],v[0:3],a[64:67]
EX v113,v113
MM a[64:67],a[164:167],v[4:7],a[64:67]
RD a[248:251],v226 offset:12288
CV v84,v112,v113
MM a[72:75],a[164:167],v[12:15],a[72:75]
EX v114,v114
MM a[72:75],a[160:163],v[8:11],a[72:75]
LPG8_%=:
s_cbranch_execz LPG12_%=
EX v115,v115
MM a[80:83],a[160:163],v[16:19],a[80:83]
RD a[252:255],v227 offset:12288
CV v85,v114,v115
MM a[80:83],a[164:167],v[20:23],a[80:83]
buffer_load_dwordx4 v235, s[12:15], s81 offen offset:2048 lds
MM a[88:91],a[164:167],v[28:31],a[88:91]
EX v116,v116
MM a[88:91],a[160:163],v[24:27],a[88:91]
EX v117,v117
MM a[92:95],a[168:171],v[24:27],a[92:95]
EX v118,v118
MM a[92:95],a[172:175],v[28:31],a[92:95]
buffer_load_dwordx4 v235, s[12:15], s64 offen offset:3072 lds
MM a[84:87],a[172:175],v[20:23],a[84:87]
EX v119,v119
MM a[84:87],a[168:171],v[16:19],a[84:87]
EX v120,v120
MM a[76:79],a[168:171],v[8:11],a[76:79]
EX v121,v121
MM a[76:79],a[172:175],v[12:15],a[76:79]
EX v122,v122
MM a[68:71],a[172:175],v[4:7],a[68:71]
EX v123,v123
MM a[68:71],a[168:171],v[0:3],a[68:71]
EX v124,v124
MM a[96:99],a[176:179],v[0:3],a[96:99]
EX v125,v125
MM a[96:99],a[180:183],v[4:7],a[96:99]
EX v126,v126
MM a[104:107],a[180:183],v[12:15],a[104:107]
EX v127,v127
MM a[104:107],a[176:179],v[8:11],a[104:107]
CV v86,v116,v117
CV v87,v118,v119
MM a[112:115],a[176:179],v[16:19],a[112:115]
CV v92,v120,v121
CV v93,v122,v123
MM a[112:115],a[180:183],v[20:23],a[112:115]
CV v94,v124,v125
CV v95,v126,v127
MM a[120:123],a[180:183],v[28:31],a[120:123]
MM a[120:123],a[176:179],v[24:27],a[120:123]
MM a[124:127],a[184:187],v[24:27],a[124:127]
v_add_f32 v244, v144, v145
v_add_f32 v245, v148, v149
MM a[124:127],a[188:191],v[28:31],a[124:127]
v_add_f32 v246, v152, v153
v_add_f32 v247, v156, v157
MM a[116:119],a[188:191],v[20:23],a[116:119]
MM a[116:119],a[184:187],v[16:19],a[116:119]
MM a[108:111],a[184:187],v[8:11],a[108:111]
MM a[108:111],a[188:191],v[12:15],a[108:111]
MM a[100:103],a[188:191],v[4:7],a[100:103]
MM a[100:103],a[184:187],v[0:3],a[100:103]
v_smfmac_f32_16x16x64_bf16 v[144:147], v[236:239], v[64:71], v234
v_smfmac_f32_16x16x64_bf16 v[148:151], v[236:239], v[72:79], v234
v_smfmac_f32_16x16x64_bf16 v[152:155], v[236:239], v[80:87], v234
v_smfmac_f32_16x16x64_bf16 v[156:159], v[236:239], v[88:95], v234
s_waitcnt lgkmcnt(0)
MM v[0:3],a[192:195],v[160:163],v[128:131]
s_add_u32 s64, s64, 16384
TR a[128:129],v232 offset:49152
s_add_u32 s81, s64, 896
MM v[0:3],a[196:199],v[176:179],v[0:3]
TR a[130:131],v232 offset:53248
v_max3_f32 v249, v144, v145, v148
MM v[0:3],a[200:203],v[192:195],v[0:3]
TR a[132:133],v232 offset:57344
v_max3_f32 v249, v249, v149, v152
MM v[0:3],a[204:207],v[208:211],v[0:3]
TR a[134:135],v232 offset:61440
v_max3_f32 v249, v249, v153, v156
MM v[4:7],a[220:223],v[208:211],v[128:131]
TR a[136:137],v233 offset:49152
v_max_f32 v249, v249, v157
MM v[4:7],a[216:219],v[192:195],v[4:7]
TR a[138:139],v233 offset:53248
v_cmp_lt_f32 vcc, s71, v249
MM v[4:7],a[212:215],v[176:179],v[4:7]
TR a[140:141],v233 offset:57344
TR a[142:143],v233 offset:61440
MM v[4:7],a[208:211],v[160:163],v[4:7]
s_cbranch_vccnz LSITE7_%=
LBACK7_%=:
MM v[12:15],a[208:211],v[164:167],v[132:135]
EX v0,v0
MM v[12:15],a[212:215],v[180:183],v[12:15]
EX v1,v1
MM v[12:15],a[216:219],v[196:199],v[12:15]
TR a[144:145],v232 offset:49664
CV v0,v0,v1
MM v[12:15],a[220:223],v[212:215],v[12:15]
EX v2,v2
MM v[8:11],a[204:207],v[212:215],v[132:135]
EX v3,v3
MM v[8:11],a[200:203],v[196:199],v[8:11]
TR a[146:147],v232 offset:53760
CV v1,v2,v3
MM v[8:11],a[196:199],v[180:183],v[8:11]
EX v4,v4
MM v[8:11],a[192:195],v[164:167],v[8:11]
TR a[148:149],v232 offset:57856
TR a[150:151],v232 offset:61952
MM v[16:19],a[192:195],v[168:171],v[136:139]
EX v5,v5
MM v[16:19],a[196:199],v[184:187],v[16:19]
TR a[152:153],v233 offset:49664
CV v2,v4,v5
MM v[16:19],a[200:203],v[200:203],v[16:19]
EX v6,v6
MM v[16:19],a[204:207],v[216:219],v[16:19]
TR a[154:155],v233 offset:53760
TR a[156:157],v233 offset:57856
MM v[20:23],a[220:223],v[216:219],v[136:139]
EX v7,v7
MM v[20:23],a[216:219],v[200:203],v[20:23]
TR a[158:159],v233 offset:61952
CV v3,v6,v7
MM v[20:23],a[212:215],v[184:187],v[20:23]
EX v8,v8
MM v[20:23],a[208:211],v[168:171],v[20:23]
EX v9,v9
MM v[28:31],a[208:211],v[172:175],v[140:143]
TR a[160:161],v232 offset:50176
CV v8,v8,v9
MM v[28:31],a[212:215],v[188:191],v[28:31]
EX v10,v10
MM v[28:31],a[216:219],v[204:207],v[28:31]
TR a[162:163],v232 offset:54272
TR a[164:165],v232 offset:58368
MM v[28:31],a[220:223],v[220:223],v[28:31]
EX v11,v11
MM v[24:27],a[204:207],v[220:223],v[140:143]
TR a[166:167],v232 offset:62464
CV v9,v10,v11
MM v[24:27],a[200:203],v[204:207],v[24:27]
EX v12,v12
MM v[24:27],a[196:199],v[188:191],v[24:27]
EX v13,v13
MM v[24:27],a[192:195],v[172:175],v[24:27]
TR a[168:169],v233 offset:50176
CV v10,v12,v13
MM v[32:35],a[224:227],v[160:163],v[128:131]
EX v14,v14
MM v[32:35],a[228:231],v[176:179],v[32:35]
TR a[170:171],v233 offset:54272
TR a[172:173],v233 offset:58368
MM v[32:35],a[232:235],v[192:195],v[32:35]
EX v15,v15
MM v[32:35],a[236:239],v[208:211],v[32:35]
TR a[174:175],v233 offset:62464
CV v11,v14,v15
MM v[36:39],a[252:255],v[208:211],v[128:131]
EX v16,v16
MM v[36:39],a[248:251],v[192:195],v[36:39]
EX v17,v17
MM v[36:39],a[244:247],v[176:179],v[36:39]
TR a[176:177],v232 offset:50688
CV v16,v16,v17
MM v[36:39],a[240:243],v[160:163],v[36:39]
EX v18,v18
MM v[44:47],a[240:243],v[164:167],v[132:135]
TR a[178:179],v232 offset:54784
TR a[180:181],v232 offset:58880
MM v[44:47],a[244:247],v[180:183],v[44:47]
EX v19,v19
MM v[44:47],a[248:251],v[196:199],v[44:47]
TR a[182:183],v232 offset:62976
CV v17,v18,v19
MM v[44:47],a[252:255],v[212:215],v[44:47]
EX v20,v20
MM v[40:43],a[236:239],v[212:215],v[132:135]
TR a[184:185],v233 offset:50688
TR a[186:187],v233 offset:54784
MM v[40:43],a[232:235],v[196:199],v[40:43]
EX v21,v21
MM v[40:43],a[228:231],v[180:183],v[40:43]
TR a[188:189],v233 offset:58880
CV v18,v20,v21
MM v[40:43],a[224:227],v[164:167],v[40:43]
EX v22,v22
MM v[48:51],a[224:227],v[168:171],v[136:139]
)ASM"
R"ASM(
EX v23,v23
MM v[48:51],a[228:231],v[184:187],v[48:51]
TR a[190:191],v233 offset:62976
CV v19,v22,v23
MM v[48:51],a[232:235],v[200:203],v[48:51]
EX v24,v24
MM v[48:51],a[236:239],v[216:219],v[48:51]
EX v25,v25
MM v[52:55],a[252:255],v[216:219],v[136:139]
EX v26,v26
MM v[52:55],a[248:251],v[200:203],v[52:55]
EX v27,v27
MM v[52:55],a[244:247],v[184:187],v[52:55]
EX v28,v28
MM v[52:55],a[240:243],v[168:171],v[52:55]
EX v29,v29
MM v[60:63],a[240:243],v[172:175],v[140:143]
EX v30,v30
MM v[60:63],a[244:247],v[188:191],v[60:63]
EX v31,v31
MM v[60:63],a[248:251],v[204:207],v[60:63]
EX v32,v32
MM v[60:63],a[252:255],v[220:223],v[60:63]
EX v33,v33
MM v[56:59],a[236:239],v[220:223],v[140:143]
EX v34,v34
MM v[56:59],a[232:235],v[204:207],v[56:59]
EX v35,v35
MM v[56:59],a[228:231],v[188:191],v[56:59]
EX v36,v36
MM v[56:59],a[224:227],v[172:175],v[56:59]
s_waitcnt vmcnt(12) lgkmcnt(0)
s_barrier
MM a[0:3],a[128:131],v[64:67],a[0:3]
s_add_u32 m0, s65, 49152
CV v24,v24,v25
CV v25,v26,v27
MM a[0:3],a[132:135],v[68:71],a[0:3]
RD a[192:195],v224 offset:16384
CV v26,v28,v29
MM a[8:11],a[132:135],v[76:79],a[8:11]
RD a[196:199],v225 offset:16384
CV v27,v30,v31
MM a[8:11],a[128:131],v[72:75],a[8:11]
buffer_load_dwordx4 v228, s[8:11], s64 offen offset:0 lds
MM a[16:19],a[128:131],v[80:83],a[16:19]
RD a[200:203],v226 offset:16384
CV v4,v32,v33
MM a[16:19],a[132:135],v[84:87],a[16:19]
RD a[204:207],v227 offset:16384
CV v5,v34,v35
MM a[24:27],a[132:135],v[92:95],a[24:27]
buffer_load_dwordx4 v229, s[8:11], s64 offen offset:1024 lds
MM a[24:27],a[128:131],v[88:91],a[24:27]
RD a[208:211],v224 offset:20480
RD a[212:215],v225 offset:20480
MM a[28:31],a[136:139],v[88:91],a[28:31]
buffer_load_dwordx4 v230, s[8:11], s64 offen offset:2048 lds
MM a[28:31],a[140:143],v[92:95],a[28:31]
RD a[216:219],v226 offset:20480
RD a[220:223],v227 offset:20480
MM a[20:23],a[140:143],v[84:87],a[20:23]
EX v37,v37
MM a[20:23],a[136:139],v[80:83],a[20:23]
RD a[224:227],v224 offset:24576
CV v6,v36,v37
MM a[12:15],a[136:139],v[72:75],a[12:15]
EX v38,v38
MM a[12:15],a[140:143],v[76:79],a[12:15]
buffer_load_dwordx4 v231, s[8:11], s64 offen offset:3072 lds
MM a[4:7],a[140:143],v[68:71],a[4:7]
s_add_u32 m0, s65, 114688
EX v39,v39
MM a[4:7],a[136:139],v[64:67],a[4:7]
RD a[228:231],v225 offset:24576
CV v7,v38,v39
MM a[32:35],a[144:147],v[64:67],a[32:35]
EX v40,v40
MM a[32:35],a[148:151],v[68:71],a[32:35]
EX v41,v41
MM a[40:43],a[148:151],v[76:79],a[40:43]
RD a[232:235],v226 offset:24576
CV v12,v40,v41
MM a[40:43],a[144:147],v[72:75],a[40:43]
EX v42,v42
MM a[48:51],a[144:147],v[80:83],a[48:51]
buffer_load_dwordx4 v235, s[12:15], s81 offen offset:0 lds
MM a[48:51],a[148:151],v[84:87],a[48:51]
EX v43,v43
MM a[56:59],a[148:151],v[92:95],a[56:59]
RD a[236:239],v227 offset:24576
CV v13,v42,v43
MM a[56:59],a[144:147],v[88:91],a[56:59]
EX v44,v44
MM a[60:63],a[152:155],v[88:91],a[60:63]
EX v45,v45
MM a[60:63],a[156:159],v[92:95],a[60:63]
RD a[240:243],v224 offset:28672
CV v14,v44,v45
MM a[52:55],a[156:159],v[84:87],a[52:55]
EX v46,v46
MM a[52:55],a[152:155],v[80:83],a[52:55]
buffer_load_dwordx4 v235, s[12:15], s64 offen offset:1024 lds
MM a[44:47],a[152:155],v[72:75],a[44:47]
EX v47,v47
MM a[44:47],a[156:159],v[76:79],a[44:47]
RD a[244:247],v225 offset:28672
CV v15,v46,v47
MM a[36:39],a[156:159],v[68:71],a[36:39]
EX v48,v48
MM a[36:39],a[152:155],v[64:67],a[36:39]
EX v49,v49
MM a[64:67],a[160:163],v[64:67],a[64:67]
RD a[248:251],v226 offset:28672
CV v20,v48,v49
MM a[64:67],a[164:167],v[68:71],a[64:67]
EX v50,v50
MM a[72:75],a[164:167],v[76:79],a[72:75]
buffer_load_dwordx4 v235, s[12:15], s81 offen offset:2048 lds
MM a[72:75],a[160:163],v[72:75],a[72:75]
EX v51,v51
MM a[80:83],a[160:163],v[80:83],a[80:83]
RD a[252:255],v227 offset:28672
CV v21,v50,v51
MM a[80:83],a[164:167],v[84:87],a[80:83]
EX v52,v52
MM a[88:91],a[164:167],v[92:95],a[88:91]
EX v53,v53
MM a[88:91],a[160:163],v[88:91],a[88:91]
buffer_load_dwordx4 v235, s[12:15], s64 offen offset:3072 lds
MM a[92:95],a[168:171],v[88:91],a[92:95]
EX v54,v54
MM a[92:95],a[172:175],v[92:95],a[92:95]
EX v55,v55
MM a[84:87],a[172:175],v[84:87],a[84:87]
EX v56,v56
MM a[84:87],a[168:171],v[80:83],a[84:87]
EX v57,v57
MM a[76:79],a[168:171],v[72:75],a[76:79]
EX v58,v58
MM a[76:79],a[172:175],v[76:79],a[76:79]
EX v59,v59
MM a[68:71],a[172:175],v[68:71],a[68:71]
EX v60,v60
MM a[68:71],a[168:171],v[64:67],a[68:71]
EX v61,v61
MM a[96:99],a[176:179],v[64:67],a[96:99]
EX v62,v62
MM a[96:99],a[180:183],v[68:71],a[96:99]
EX v63,v63
MM a[104:107],a[180:183],v[76:79],a[104:107]
CV v22,v52,v53
CV v23,v54,v55
MM a[104:107],a[176:179],v[72:75],a[104:107]
CV v28,v56,v57
CV v29,v58,v59
MM a[112:115],a[176:179],v[80:83],a[112:115]
CV v30,v60,v61
CV v31,v62,v63
MM a[112:115],a[180:183],v[84:87],a[112:115]
MM a[120:123],a[180:183],v[92:95],a[120:123]
MM a[120:123],a[176:179],v[88:91],a[120:123]
MM a[124:127],a[184:187],v[88:91],a[124:127]
v_add_f32 v244, v144, v145
v_add_f32 v245, v148, v149
MM a[124:127],a[188:191],v[92:95],a[124:127]
v_add_f32 v246, v152, v153
v_add_f32 v247, v156, v157
MM a[116:119],a[188:191],v[84:87],a[116:119]
s_cmp_eq_u32 s70, 2
s_cbranch_scc1 LIP_%=
LLALIP_%=:
MM a[116:119],a[184:187],v[80:83],a[116:119]
MM a[108:111],a[184:187],v[72:75],a[108:111]
MM a[108:111],a[188:191],v[76:79],a[108:111]
MM a[100:103],a[188:191],v[68:71],a[100:103]
MM a[100:103],a[184:187],v[64:67],a[100:103]
v_smfmac_f32_16x16x64_bf16 v[144:147], v[236:239], v[0:7], v234
v_smfmac_f32_16x16x64_bf16 v[148:151], v[236:239], v[8:15], v234
v_smfmac_f32_16x16x64_bf16 v[152:155], v[236:239], v[16:23], v234
v_smfmac_f32_16x16x64_bf16 v[156:159], v[236:239], v[24:31], v234
s_waitcnt lgkmcnt(0)
MM v[64:67],a[192:195],v[160:163],v[128:131]
s_add_u32 s64, s64, 16384
TR a[128:129],v232 offset:0
s_and_b32 s64, s64, 524287
MM v[64:67],a[196:199],v[176:179],v[64:67]
s_add_u32 s81, s64, 896
TR a[130:131],v232 offset:4096
v_max3_f32 v249, v144, v145, v148
MM v[64:67],a[200:203],v[192:195],v[64:67]
TR a[132:133],v232 offset:8192
v_max3_f32 v249, v249, v149, v152
MM v[64:67],a[204:207],v[208:211],v[64:67]
TR a[134:135],v232 offset:12288
v_max3_f32 v249, v249, v153, v156
MM v[68:71],a[220:223],v[208:211],v[128:131]
TR a[136:137],v233 offset:0
v_max_f32 v249, v249, v157
MM v[68:71],a[216:219],v[192:195],v[68:71]
TR a[138:139],v233 offset:4096
v_cmp_lt_f32 vcc, s71, v249
MM v[68:71],a[212:215],v[176:179],v[68:71]
TR a[140:141],v233 offset:8192
TR a[142:143],v233 offset:12288
MM v[68:71],a[208:211],v[160:163],v[68:71]
s_cbranch_vccnz LSITE8_%=
LBACK8_%=:
MM v[76:79],a[208:211],v[164:167],v[132:135]
EX v64,v64
MM v[76:79],a[212:215],v[180:183],v[76:79]
EX v65,v65
MM v[76:79],a[216:219],v[196:199],v[76:79]
TR a[144:145],v232 offset:512
CV v64,v64,v65
MM v[76:79],a[220:223],v[212:215],v[76:79]
EX v66,v66
MM v[72:75],a[204:207],v[212:215],v[132:135]
EX v67,v67
MM v[72:75],a[200:203],v[196:199],v[72:75]
TR a[146:147],v232 offset:4608
CV v65,v66,v67
MM v[72:75],a[196:199],v[180:183],v[72:75]
EX v68,v68
MM v[72:75],a[192:195],v[164:167],v[72:75]
TR a[148:149],v232 offset:8704
TR a[150:151],v232 offset:12800
MM v[80:83],a[192:195],v[168:171],v[136:139]
EX v69,v69
MM v[80:83],a[196:199],v[184:187],v[80:83]
TR a[152:153],v233 offset:512
CV v66,v68,v69
MM v[80:83],a[200:203],v[200:203],v[80:83]
EX v70,v70
MM v[80:83],a[204:207],v[216:219],v[80:83]
TR a[154:155],v233 offset:4608
TR a[156:157],v233 offset:8704
MM v[84:87],a[220:223],v[216:219],v[136:139]
EX v71,v71
MM v[84:87],a[216:219],v[200:203],v[84:87]
TR a[158:159],v233 offset:12800
CV v67,v70,v71
MM v[84:87],a[212:215],v[184:187],v[84:87]
EX v72,v72
MM v[84:87],a[208:211],v[168:171],v[84:87]
EX v73,v73
MM v[92:95],a[208:211],v[172:175],v[140:143]
TR a[160:161],v232 offset:1024
CV v72,v72,v73
MM v[92:95],a[212:215],v[188:191],v[92:95]
EX v74,v74
MM v[92:95],a[216:219],v[204:207],v[92:95]
TR a[162:163],v232 offset:5120
TR a[164:165],v232 offset:9216
MM v[92:95],a[220:223],v[220:223],v[92:95]
EX v75,v75
MM v[88:91],a[204:207],v[220:223],v[140:143]
TR a[166:167],v232 offset:13312
CV v73,v74,v75
MM v[88:91],a[200:203],v[204:207],v[88:91]
EX v76,v76
MM v[88:91],a[196:199],v[188:191],v[88:91]
EX v77,v77
MM v[88:91],a[192:195],v[172:175],v[88:91]
TR a[168:169],v233 offset:1024
CV v74,v76,v77
MM v[96:99],a[224:227],v[160:163],v[128:131]
EX v78,v78
MM v[96:99],a[228:231],v[176:179],v[96:99]
TR a[170:171],v233 offset:5120
TR a[172:173],v233 offset:9216
MM v[96:99],a[232:235],v[192:195],v[96:99]
EX v79,v79
MM v[96:99],a[236:239],v[208:211],v[96:99]
TR a[174:175],v233 offset:13312
CV v75,v78,v79
MM v[100:103],a[252:255],v[208:211],v[128:131]
EX v80,v80
MM v[100:103],a[248:251],v[192:195],v[100:103]
EX v81,v81
MM v[100:103],a[244:247],v[176:179],v[100:103]
TR a[176:177],v232 offset:1536
CV v80,v80,v81
MM v[100:103],a[240:243],v[160:163],v[100:103]
EX v82,v82
MM v[108:111],a[240:243],v[164:167],v[132:135]
TR a[178:179],v232 offset:5632
TR a[180:181],v232 offset:9728
MM v[108:111],a[244:247],v[180:183],v[108:111]
EX v83,v83
MM v[108:111],a[248:251],v[196:199],v[108:111]
TR a[182:183],v232 offset:13824
CV v81,v82,v83
MM v[108:111],a[252:255],v[212:215],v[108:111]
EX v84,v84
MM v[104:107],a[236:239],v[212:215],v[132:135]
TR a[184:185],v233 offset:1536
TR a[186:187],v233 offset:5632
MM v[104:107],a[232:235],v[196:199],v[104:107]
EX v85,v85
MM v[104:107],a[228:231],v[180:183],v[104:107]
TR a[188:189],v233 offset:9728
CV v82,v84,v85
MM v[104:107],a[224:227],v[164:167],v[104:107]
EX v86,v86
MM v[112:115],a[224:227],v[168:171],v[136:139]
EX v87,v87
MM v[112:115],a[228:231],v[184:187],v[112:115]
TR a[190:191],v233 offset:13824
CV v83,v86,v87
MM v[112:115],a[232:235],v[200:203],v[112:115]
EX v88,v88
MM v[112:115],a[236:239],v[216:219],v[112:115]
EX v89,v89
MM v[116:119],a[252:255],v[216:219],v[136:139]
EX v90,v90
MM v[116:119],a[248:251],v[200:203],v[116:119]
EX v91,v91
MM v[116:119],a[244:247],v[184:187],v[116:119]
EX v92,v92
MM v[116:119],a[240:243],v[168:171],v[116:119]
EX v93,v93
MM v[124:127],a[240:243],v[172:175],v[140:143]
EX v94,v94
MM v[124:127],a[244:247],v[188:191],v[124:127]
EX v95,v95
MM v[124:127],a[248:251],v[204:207],v[124:127]
EX v96,v96
MM v[124:127],a[252:255],v[220:223],v[124:127]
EX v97,v97
MM v[120:123],a[236:239],v[220:223],v[140:143]
EX v98,v98
MM v[120:123],a[232:235],v[204:207],v[120:123]
EX v99,v99
MM v[120:123],a[228:231],v[188:191],v[120:123]
s_cmp_eq_u32 s70, 1
s_cbranch_scc1 LTP_%=
LLALTP_%=:
MM v[120:123],a[224:227],v[172:175],v[120:123]
s_waitcnt vmcnt(12) lgkmcnt(0)
s_barrier
MM a[0:3],a[128:131],v[0:3],a[0:3]
s_add_u32 m0, s65, 0
CV v88,v88,v89
CV v89,v90,v91
MM a[0:3],a[132:135],v[4:7],a[0:3]
RD a[192:195],v224 offset:32768
CV v90,v92,v93
MM a[8:11],a[132:135],v[12:15],a[8:11]
RD a[196:199],v225 offset:32768
CV v91,v94,v95
MM a[8:11],a[128:131],v[8:11],a[8:11]
buffer_load_dwordx4 v228, s[8:11], s64 offen offset:0 lds
MM a[16:19],a[128:131],v[16:19],a[16:19]
RD a[200:203],v226 offset:32768
CV v68,v96,v97
MM a[16:19],a[132:135],v[20:23],a[16:19]
RD a[204:207],v227 offset:32768
CV v69,v98,v99
MM a[24:27],a[132:135],v[28:31],a[24:27]
buffer_load_dwordx4 v229, s[8:11], s64 offen offset:1024 lds
MM a[24:27],a[128:131],v[24:27],a[24:27]
RD a[208:211],v224 offset:36864
RD a[212:215],v225 offset:36864
MM a[28:31],a[136:139],v[24:27],a[28:31]
buffer_load_dwordx4 v230, s[8:11], s64 offen offset:2048 lds
MM a[28:31],a[140:143],v[28:31],a[28:31]
EX v100,v100
MM a[20:23],a[140:143],v[20:23],a[20:23]
EX v101,v101
MM a[20:23],a[136:139],v[16:19],a[20:23]
RD a[216:219],v226 offset:36864
CV v70,v100,v101
MM a[12:15],a[136:139],v[8:11],a[12:15]
EX v102,v102
MM a[12:15],a[140:143],v[12:15],a[12:15]
RD a[220:223],v227 offset:36864
RD a[224:227],v224 offset:40960
MM a[4:7],a[140:143],v[4:7],a[4:7]
EX v103,v103
MM a[4:7],a[136:139],v[0:3],a[4:7]
RD a[228:231],v225 offset:40960
CV v71,v102,v103
MM a[32:35],a[144:147],v[0:3],a[32:35]
EX v104,v104
MM a[32:35],a[148:151],v[4:7],a[32:35]
buffer_load_dwordx4 v231, s[8:11], s64 offen offset:3072 lds
MM a[40:43],a[148:151],v[12:15],a[40:43]
s_add_u32 m0, s65, 65536
EX v105,v105
)ASM"
R"ASM(
MM a[40:43],a[144:147],v[8:11],a[40:43]
RD a[232:235],v226 offset:40960
CV v76,v104,v105
MM a[48:51],a[144:147],v[16:19],a[48:51]
EX v106,v106
MM a[48:51],a[148:151],v[20:23],a[48:51]
EX v107,v107
MM a[56:59],a[148:151],v[28:31],a[56:59]
RD a[236:239],v227 offset:40960
CV v77,v106,v107
MM a[56:59],a[144:147],v[24:27],a[56:59]
EX v108,v108
MM a[60:63],a[152:155],v[24:27],a[60:63]
buffer_load_dwordx4 v235, s[12:15], s81 offen offset:0 lds
MM a[60:63],a[156:159],v[28:31],a[60:63]
EX v109,v109
s_nop 0
LPG9_%=:
s_cbranch_execz LTR0_%=
MM a[52:55],a[156:159],v[20:23],a[52:55]
RD a[240:243],v224 offset:45056
CV v78,v108,v109
MM a[52:55],a[152:155],v[16:19],a[52:55]
EX v110,v110
MM a[44:47],a[152:155],v[8:11],a[44:47]
EX v111,v111
MM a[44:47],a[156:159],v[12:15],a[44:47]
RD a[244:247],v225 offset:45056
CV v79,v110,v111
MM a[36:39],a[156:159],v[4:7],a[36:39]
EX v112,v112
MM a[36:39],a[152:155],v[0:3],a[36:39]
buffer_load_dwordx4 v235, s[12:15], s64 offen offset:1024 lds
MM a[64:67],a[160:163],v[0:3],a[64:67]
EX v113,v113
MM a[64:67],a[164:167],v[4:7],a[64:67]
RD a[248:251],v226 offset:45056
CV v84,v112,v113
MM a[72:75],a[164:167],v[12:15],a[72:75]
EX v114,v114
MM a[72:75],a[160:163],v[8:11],a[72:75]
EX v115,v115
MM a[80:83],a[160:163],v[16:19],a[80:83]
RD a[252:255],v227 offset:45056
CV v85,v114,v115
MM a[80:83],a[164:167],v[20:23],a[80:83]
buffer_load_dwordx4 v235, s[12:15], s81 offen offset:2048 lds
MM a[88:91],a[164:167],v[28:31],a[88:91]
EX v116,v116
MM a[88:91],a[160:163],v[24:27],a[88:91]
EX v117,v117
MM a[92:95],a[168:171],v[24:27],a[92:95]
EX v118,v118
MM a[92:95],a[172:175],v[28:31],a[92:95]
buffer_load_dwordx4 v235, s[12:15], s64 offen offset:3072 lds
MM a[84:87],a[172:175],v[20:23],a[84:87]
EX v119,v119
MM a[84:87],a[168:171],v[16:19],a[84:87]
EX v120,v120
MM a[76:79],a[168:171],v[8:11],a[76:79]
EX v121,v121
MM a[76:79],a[172:175],v[12:15],a[76:79]
EX v122,v122
MM a[68:71],a[172:175],v[4:7],a[68:71]
EX v123,v123
MM a[68:71],a[168:171],v[0:3],a[68:71]
EX v124,v124
MM a[96:99],a[176:179],v[0:3],a[96:99]
EX v125,v125
MM a[96:99],a[180:183],v[4:7],a[96:99]
EX v126,v126
MM a[104:107],a[180:183],v[12:15],a[104:107]
EX v127,v127
MM a[104:107],a[176:179],v[8:11],a[104:107]
CV v86,v116,v117
CV v87,v118,v119
MM a[112:115],a[176:179],v[16:19],a[112:115]
CV v92,v120,v121
CV v93,v122,v123
MM a[112:115],a[180:183],v[20:23],a[112:115]
CV v94,v124,v125
CV v95,v126,v127
MM a[120:123],a[180:183],v[28:31],a[120:123]
MM a[120:123],a[176:179],v[24:27],a[120:123]
MM a[124:127],a[184:187],v[24:27],a[124:127]
v_add_f32 v244, v144, v145
v_add_f32 v245, v148, v149
MM a[124:127],a[188:191],v[28:31],a[124:127]
v_add_f32 v246, v152, v153
v_add_f32 v247, v156, v157
MM a[116:119],a[188:191],v[20:23],a[116:119]
s_cmp_eq_u32 s70, s82
s_cbranch_scc1 LWV_%=
LLALWV_%=:
MM a[116:119],a[184:187],v[16:19],a[116:119]
MM a[108:111],a[184:187],v[8:11],a[108:111]
MM a[108:111],a[188:191],v[12:15],a[108:111]
MM a[100:103],a[188:191],v[4:7],a[100:103]
MM a[100:103],a[184:187],v[0:3],a[100:103]
v_smfmac_f32_16x16x64_bf16 v[144:147], v[236:239], v[64:71], v234
v_smfmac_f32_16x16x64_bf16 v[148:151], v[236:239], v[72:79], v234
v_smfmac_f32_16x16x64_bf16 v[152:155], v[236:239], v[80:87], v234
v_smfmac_f32_16x16x64_bf16 v[156:159], v[236:239], v[88:95], v234
s_waitcnt lgkmcnt(0)
s_sub_u32 s70, s70, 1
s_cmp_lg_u32 s70, 0
s_cbranch_scc1 LLAP_%=
s_branch LLAJ_%=
LWW_%=:
v_lshlrev_b32 v250, 3, v248
v_min_u32 v250, 4992, v250
s_sub_u32 s91, 5242880, s26
s_sub_u32 s60, s24, s91
s_subb_u32 s61, s25, 0
s_mov_b32 s62, 5242880
s_mov_b32 s63, 131072
s_lshl_b32 s90, s16, 2
s_add_u32 s90, s90, %[swave]
s_mul_i32 s90, s90, 10240
s_add_u32 s90, s90, 0
s_add_u32 s91, %[slds], 135168
s_mov_b32 m0, s91
s_nop 0
buffer_load_dword v250, s[60:63], s90 offen offset:0 lds
s_branch LLALWW_%=
LIP_%=:
s_lshr_b32 s77, %[sb], 3
s_lshl_b32 s77, s77, 2
s_add_u32 s77, s77, %[swave]
s_lshl_b32 s77, s77, 9
s_add_u32 s77, s77, LLAJ_%=-LPDP_%=
v_lshrrev_b32 v253, 1, v248
v_add_u32 v253, s77, v253
s_mov_b32 s77, LKEND_ka-LPDP_%=-16
v_min_u32 v253, s77, v253
s_getpc_b64 s[98:99]
LPDP_%=:
global_load_dword v253, v253, s[98:99]
s_branch LLALIP_%=
LTP_%=:
s_getpc_b64 s[98:99]
LTA1_%=:
s_min_u32 s73, %[swave], 3
s_lshl_b32 s73, s73, 12
s_add_u32 s73, s73, LPG23_%=-LTA1_%=
s_add_u32 s98, s98, s73
s_addc_u32 s99, s99, 0
s_mov_b64 exec, 0
s_setpc_b64 s[98:99]
LTR1_%=:
s_mov_b64 exec, -1
s_branch LLALTP_%=
LWV_%=:
v_lshlrev_b32 v250, 3, v248
v_min_u32 v250, 4992, v250
s_sub_u32 s91, 5242880, s26
s_sub_u32 s60, s24, s91
s_subb_u32 s61, s25, 0
s_mov_b32 s62, 5242880
s_mov_b32 s63, 131072
s_lshl_b32 s90, s16, 2
s_add_u32 s90, s90, %[swave]
s_mul_i32 s90, s90, 10240
s_add_u32 s90, s90, 5120
s_add_u32 s91, %[slds], 135168
s_mov_b32 m0, s91
s_nop 0
buffer_load_dword v250, s[60:63], s90 offen offset:0 lds
s_branch LLALWV_%=
s_nop 0
s_nop 0
s_nop 0
s_nop 0
s_nop 0
s_nop 0
s_nop 0
s_nop 0
s_nop 0
s_nop 0
s_nop 0
s_nop 0
s_nop 0
s_nop 0
s_nop 0
s_nop 0
s_nop 0
s_nop 0
s_nop 0
s_nop 0
s_nop 0
s_nop 0
s_nop 0
s_nop 0
s_nop 0
s_nop 0
s_nop 0
s_nop 0
s_nop 0
s_nop 0
s_nop 0
s_nop 0
s_nop 0
s_nop 0
s_nop 0
s_nop 0
s_nop 0
s_nop 0
s_nop 0
s_nop 0
s_nop 0
s_nop 0
s_nop 0
s_nop 0
s_nop 0
s_nop 0
s_nop 0
s_nop 0
s_nop 0
s_nop 0
s_nop 0
s_nop 0
s_nop 0
s_nop 0
s_nop 0
s_nop 0
s_nop 0
s_nop 0
s_nop 0
s_nop 0
s_nop 0
s_nop 0
s_nop 0
s_nop 0
s_nop 0
s_nop 0
s_nop 0
s_nop 0
s_nop 0
s_nop 0
s_nop 0
s_nop 0
s_nop 0
s_nop 0
s_nop 0
s_nop 0
s_nop 0
s_nop 0
s_nop 0
s_nop 0
s_nop 0
s_nop 0
s_nop 0
s_nop 0
s_nop 0
s_nop 0
s_nop 0
s_nop 0
s_nop 0
s_nop 0
s_nop 0
s_nop 0
s_nop 0
s_nop 0
s_nop 0
s_nop 0
s_nop 0
s_nop 0
s_nop 0
s_nop 0
s_nop 0
s_nop 0
s_nop 0
s_nop 0
s_nop 0
s_nop 0
s_nop 0
s_nop 0
s_nop 0
s_nop 0
s_nop 0
s_nop 0
s_nop 0
s_nop 0
s_nop 0
s_nop 0
s_nop 0
s_nop 0
s_nop 0
s_nop 0
s_nop 0
s_nop 0
s_nop 0
s_nop 0
s_nop 0
s_nop 0
s_nop 0
s_nop 0
s_nop 0
s_nop 0
s_nop 0
s_nop 0
s_nop 0
s_nop 0
s_nop 0
s_nop 0
s_nop 0
s_nop 0
s_nop 0
s_nop 0
s_nop 0
s_nop 0
s_nop 0
s_nop 0
s_nop 0
s_nop 0
s_nop 0
s_nop 0
s_nop 0
s_nop 0
s_nop 0
s_nop 0
s_nop 0
s_nop 0
s_nop 0
s_nop 0
s_nop 0
s_nop 0
s_nop 0
s_nop 0
s_nop 0
s_nop 0
s_nop 0
s_nop 0
s_nop 0
s_nop 0
s_nop 0
s_nop 0
s_nop 0
s_nop 0
s_nop 0
s_nop 0
s_nop 0
s_nop 0
s_nop 0
s_nop 0
s_nop 0
s_nop 0
s_nop 0
s_nop 0
s_nop 0
s_nop 0
s_nop 0
s_nop 0
s_nop 0
s_nop 0
s_nop 0
s_nop 0
s_nop 0
s_nop 0
s_nop 0
s_nop 0
s_nop 0
s_nop 0
s_nop 0
s_nop 0
s_nop 0
s_nop 0
s_nop 0
s_nop 0
s_nop 0
s_nop 0
s_nop 0
s_nop 0
s_nop 0
s_nop 0
s_nop 0
s_nop 0
s_nop 0
s_nop 0
s_nop 0
s_nop 0
s_nop 0
s_nop 0
s_nop 0
s_nop 0
s_nop 0
s_nop 0
s_nop 0
s_nop 0
s_nop 0
s_nop 0
s_nop 0
s_nop 0
s_nop 0
s_nop 0
s_nop 0
s_nop 0
s_nop 0
s_nop 0
s_nop 0
s_nop 0
s_nop 0
s_nop 0
s_nop 0
s_nop 0
s_nop 0
s_nop 0
s_nop 0
s_nop 0
s_nop 0
s_nop 0
s_nop 0
s_nop 0
s_nop 0
s_nop 0
s_nop 0
s_nop 0
s_nop 0
s_nop 0
s_nop 0
s_nop 0
s_nop 0
s_nop 0
s_nop 0
s_nop 0
s_nop 0
s_nop 0
s_nop 0
s_nop 0
s_nop 0
s_nop 0
s_nop 0
s_nop 0
s_nop 0
s_nop 0
s_nop 0
s_nop 0
s_nop 0
s_nop 0
s_nop 0
s_nop 0
s_nop 0
s_nop 0
s_nop 0
s_nop 0
s_nop 0
s_nop 0
s_nop 0
s_nop 0
s_nop 0
s_nop 0
s_nop 0
s_nop 0
s_nop 0
s_nop 0
s_nop 0
s_nop 0
s_nop 0
s_nop 0
s_nop 0
s_nop 0
s_nop 0
s_nop 0
s_nop 0
s_nop 0
s_nop 0
s_nop 0
s_nop 0
s_nop 0
s_nop 0
s_nop 0
s_nop 0
s_nop 0
s_nop 0
s_nop 0
s_nop 0
s_nop 0
s_nop 0
s_nop 0
s_nop 0
s_nop 0
s_nop 0
s_nop 0
s_nop 0
s_nop 0
s_nop 0
s_nop 0
s_nop 0
s_nop 0
s_nop 0
s_nop 0
s_nop 0
s_nop 0
s_nop 0
s_nop 0
s_nop 0
s_nop 0
s_nop 0
s_nop 0
s_nop 0
s_nop 0
s_nop 0
s_nop 0
s_nop 0
s_nop 0
s_nop 0
s_nop 0
s_nop 0
s_nop 0
s_nop 0
s_nop 0
s_nop 0
s_nop 0
s_nop 0
s_nop 0
s_nop 0
s_nop 0
s_nop 0
s_nop 0
s_nop 0
s_nop 0
s_nop 0
s_nop 0
s_nop 0
s_nop 0
s_nop 0
s_nop 0
s_nop 0
s_nop 0
s_nop 0
s_nop 0
s_nop 0
s_nop 0
s_nop 0
s_nop 0
s_nop 0
s_nop 0
s_nop 0
s_nop 0
s_nop 0
s_nop 0
s_nop 0
s_nop 0
s_nop 0
s_nop 0
LLAR_%=:
MM v[0:3],a[192:195],v[160:163],v[128:131]
s_add_u32 s64, s64, 16384
TR a[128:129],v232 offset:16384
s_add_u32 s81, s64, 896
MM v[0:3],a[196:199],v[176:179],v[0:3]
TR a[130:131],v232 offset:20480
v_max3_f32 v249, v144, v145, v148
MM v[0:3],a[200:203],v[192:195],v[0:3]
TR a[132:133],v232 offset:24576
v_max3_f32 v249, v249, v149, v152
MM v[0:3],a[204:207],v[208:211],v[0:3]
TR a[134:135],v232 offset:28672
v_max3_f32 v249, v249, v153, v156
MM v[4:7],a[220:223],v[208:211],v[128:131]
TR a[136:137],v233 offset:16384
v_max_f32 v249, v249, v157
MM v[4:7],a[216:219],v[192:195],v[4:7]
TR a[138:139],v233 offset:20480
v_cmp_lt_f32 vcc, s71, v249
MM v[4:7],a[212:215],v[176:179],v[4:7]
TR a[140:141],v233 offset:24576
TR a[142:143],v233 offset:28672
MM v[4:7],a[208:211],v[160:163],v[4:7]
s_cbranch_vccnz LSITE9_%=
LBACK9_%=:
MM v[12:15],a[208:211],v[164:167],v[132:135]
s_lshl_b32 s17, s16, 2
EX v0,v0
MM v[12:15],a[212:215],v[180:183],v[12:15]
s_add_u32 s17, s17, %[swave]
EX v1,v1
MM v[12:15],a[216:219],v[196:199],v[12:15]
s_sub_u32 s90, s16, 108
TR a[144:145],v232 offset:16896
CV v0,v0,v1
MM v[12:15],a[220:223],v[212:215],v[12:15]
s_min_u32 s90, s90, s16
EX v2,v2
MM v[8:11],a[204:207],v[212:215],v[132:135]
s_lshl_b32 s48, s90, 5
EX v3,v3
MM v[8:11],a[200:203],v[196:199],v[8:11]
s_add_u32 s76, %[slds], 155648
v_add_u32 v250, s76, v248
CV v1,v2,v3
MM v[8:11],a[196:199],v[180:183],v[8:11]
s_lshl_b32 s77, s17, 4
TR a[146:147],v232 offset:20992
TR a[148:149],v232 offset:25088
MM v[8:11],a[192:195],v[164:167],v[8:11]
s_add_u32 s76, s76, s77
TR a[150:151],v232 offset:29184
v_mov_b32 v251, s76
MM v[16:19],a[192:195],v[168:171],v[136:139]
RD v[124:127],v251 offset:0
RD v[96:99],v250 offset:0
MM v[16:19],a[196:199],v[184:187],v[16:19]
RD v[100:103],v250 offset:1024
RD v[104:107],v250 offset:2048
MM v[16:19],a[200:203],v[200:203],v[16:19]
RD v[108:111],v250 offset:3072
RD v[112:115],v250 offset:4096
MM v[16:19],a[204:207],v[216:219],v[16:19]
RD v[116:119],v250 offset:5120
RD v[120:123],v250 offset:6144
MM v[20:23],a[220:223],v[216:219],v[136:139]
EX v4,v4
MM v[20:23],a[216:219],v[200:203],v[20:23]
EX v5,v5
MM v[20:23],a[212:215],v[184:187],v[20:23]
s_waitcnt lgkmcnt(7)
v_readfirstlane_b32 s52, v124
v_readfirstlane_b32 s53, v125
MM v[20:23],a[208:211],v[168:171],v[20:23]
v_readfirstlane_b32 s54, v126
v_readfirstlane_b32 s55, v127
EX v6,v6
MM v[28:31],a[208:211],v[172:175],v[140:143]
RD v[124:127],v250 offset:7168
TR a[152:153],v233 offset:16896
MM v[28:31],a[212:215],v[188:191],v[28:31]
s_waitcnt lgkmcnt(2)
v_max3_u32 v253, v96, v97, v98
v_max3_u32 v251, v99, v100, v101
EX v7,v7
MM v[28:31],a[216:219],v[204:207],v[28:31]
v_max3_u32 v253, v253, v102, v103
v_max3_u32 v251, v251, v104, v105
MM v[28:31],a[220:223],v[220:223],v[28:31]
v_max3_u32 v253, v253, v106, v107
v_max3_u32 v251, v251, v108, v109
EX v8,v8
MM v[24:27],a[204:207],v[220:223],v[140:143]
v_max3_u32 v253, v253, v110, v111
v_max3_u32 v251, v251, v112, v113
MM v[24:27],a[200:203],v[204:207],v[24:27]
v_max3_u32 v253, v253, v114, v115
v_max3_u32 v251, v251, v116, v117
EX v9,v9
MM v[24:27],a[196:199],v[188:191],v[24:27]
TR a[154:155],v233 offset:20992
v_max3_u32 v253, v253, v118, v119
v_max3_u32 v251, v251, v120, v121
MM v[24:27],a[192:195],v[172:175],v[24:27]
v_max3_u32 v253, v253, v122, v123
CV v2,v4,v5
EX v10,v10
MM v[32:35],a[224:227],v[160:163],v[128:131]
s_waitcnt lgkmcnt(2)
TR a[156:157],v233 offset:25088
v_max3_u32 v251, v251, v124, v125
v_max3_u32 v253, v253, v126, v127
EX v11,v11
MM v[32:35],a[228:231],v[176:179],v[32:35]
v_max_u32 v253, v253, v251
v_cmp_lt_u32 s[60:61], s52, v253
MM v[32:35],a[232:235],v[192:195],v[32:35]
TR a[158:159],v233 offset:29184
v_cmp_lt_u32 s[62:63], s53, v253
v_cmp_lt_u32 s[56:57], s54, v253
EX v12,v12
MM v[32:35],a[236:239],v[208:211],v[32:35]
v_cmp_lt_u32 s[58:59], s55, v253
CV v3,v6,v7
MM v[36:39],a[252:255],v[208:211],v[128:131]
TR a[160:161],v232 offset:17408
EX v13,v13
MM v[36:39],a[248:251],v[192:195],v[36:39]
s_bcnt1_i32_b64 s60, s[60:61]
TR a[162:163],v232 offset:21504
CV v8,v8,v9
MM v[36:39],a[244:247],v[176:179],v[36:39]
s_bcnt1_i32_b64 s62, s[62:63]
EX v14,v14
MM v[36:39],a[240:243],v[160:163],v[36:39]
s_bcnt1_i32_b64 s56, s[56:57]
)ASM"
R"ASM(
TR a[164:165],v232 offset:25600
TR a[166:167],v232 offset:29696
MM v[44:47],a[240:243],v[164:167],v[132:135]
s_bcnt1_i32_b64 s58, s[58:59]
EX v15,v15
MM v[44:47],a[244:247],v[180:183],v[44:47]
s_min_u32 s76, s60, s62
EX v16,v16
MM v[44:47],a[248:251],v[196:199],v[44:47]
s_min_u32 s77, s56, s58
TR a[168:169],v233 offset:17408
CV v9,v10,v11
MM v[44:47],a[252:255],v[212:215],v[44:47]
s_min_u32 s76, s76, s77
s_cmp_lt_u32 s76, 8
s_cbranch_scc1 LRK_%=
LRKb_%=:
EX v17,v17
MM v[40:43],a[236:239],v[212:215],v[132:135]
v_lshrrev_b32 v252, 6, v248
v_mul_u32_u24 v252, 3456, v252
MM v[40:43],a[232:235],v[196:199],v[40:43]
TR a[170:171],v233 offset:21504
v_and_b32 v251, 48, v248
v_add_u32 v252, v252, v251
EX v18,v18
MM v[40:43],a[228:231],v[180:183],v[40:43]
s_and_b32 s90, %[swave], 1
TR a[172:173],v233 offset:25600
CV v10,v12,v13
MM v[40:43],a[224:227],v[164:167],v[40:43]
s_mul_i32 s90, s90, 276480
EX v19,v19
MM v[48:51],a[224:227],v[168:171],v[136:139]
s_bfe_u32 s91, s48, 131077
TR a[174:175],v233 offset:29696
CV v11,v14,v15
MM v[48:51],a[228:231],v[184:187],v[48:51]
s_mul_i32 s91, s91, 552960
EX v20,v20
MM v[48:51],a[232:235],v[200:203],v[48:51]
s_add_u32 s90, s90, s91
TR a[176:177],v232 offset:17920
TR a[178:179],v232 offset:22016
MM v[48:51],a[236:239],v[216:219],v[48:51]
s_and_b32 s91, s48, 0xffffff80
EX v21,v21
MM v[52:55],a[252:255],v[216:219],v[136:139]
s_add_u32 s90, s90, s91
EX v22,v22
MM v[52:55],a[248:251],v[200:203],v[52:55]
s_and_b32 s91, s48, 16
TR a[180:181],v232 offset:26112
CV v16,v16,v17
MM v[52:55],a[244:247],v[184:187],v[52:55]
s_lshl_b32 s91, s91, 2
EX v23,v23
MM v[52:55],a[240:243],v[168:171],v[52:55]
s_add_u32 s66, s90, s91
TR a[182:183],v232 offset:30208
CV v17,v18,v19
MM v[60:63],a[240:243],v[172:175],v[140:143]
s_mul_i32 s91, %[swave], 5120
EX v24,v24
MM v[60:63],a[244:247],v[188:191],v[60:63]
s_add_u32 s91, s91, %[slds]
TR a[184:185],v233 offset:17920
TR a[186:187],v233 offset:22016
MM v[60:63],a[248:251],v[204:207],v[60:63]
s_add_u32 s67, s91, 131072
TR a[188:189],v233 offset:26112
TR a[190:191],v233 offset:30208
EX v25,v25
MM v[60:63],a[252:255],v[220:223],v[60:63]
EX v26,v26
MM v[56:59],a[236:239],v[220:223],v[140:143]
EX v27,v27
MM v[56:59],a[232:235],v[204:207],v[56:59]
EX v28,v28
MM v[56:59],a[228:231],v[188:191],v[56:59]
EX v29,v29
CV v18,v20,v21
MM v[56:59],a[224:227],v[172:175],v[56:59]
s_waitcnt lgkmcnt(0)
s_barrier
MM a[0:3],a[128:131],v[64:67],a[0:3]
s_add_u32 m0, s65, 16384
CV v19,v22,v23
CV v24,v24,v25
MM a[0:3],a[132:135],v[68:71],a[0:3]
CV v25,v26,v27
CV v26,v28,v29
MM a[8:11],a[132:135],v[76:79],a[8:11]
EX v30,v30
MM a[8:11],a[128:131],v[72:75],a[8:11]
EX v31,v31
MM a[16:19],a[128:131],v[80:83],a[16:19]
RD a[192:195],v224 offset:49152
CV v27,v30,v31
MM a[16:19],a[132:135],v[84:87],a[16:19]
EX v32,v32
MM a[24:27],a[132:135],v[92:95],a[24:27]
buffer_load_dwordx4 v228, s[8:11], s64 offen offset:0 lds
MM a[24:27],a[128:131],v[88:91],a[24:27]
EX v33,v33
MM a[28:31],a[136:139],v[88:91],a[28:31]
RD a[196:199],v225 offset:49152
CV v4,v32,v33
MM a[28:31],a[140:143],v[92:95],a[28:31]
EX v34,v34
MM a[20:23],a[140:143],v[84:87],a[20:23]
EX v35,v35
MM a[20:23],a[136:139],v[80:83],a[20:23]
RD a[200:203],v226 offset:49152
CV v5,v34,v35
MM a[12:15],a[136:139],v[72:75],a[12:15]
EX v36,v36
MM a[12:15],a[140:143],v[76:79],a[12:15]
LPG10_%=:
s_cbranch_execz LTR0_%=
buffer_load_dwordx4 v229, s[8:11], s64 offen offset:1024 lds
MM a[4:7],a[140:143],v[68:71],a[4:7]
EX v37,v37
MM a[4:7],a[136:139],v[64:67],a[4:7]
RD a[204:207],v227 offset:49152
CV v6,v36,v37
MM a[32:35],a[144:147],v[64:67],a[32:35]
EX v38,v38
MM a[32:35],a[148:151],v[68:71],a[32:35]
EX v39,v39
MM a[40:43],a[148:151],v[76:79],a[40:43]
RD a[208:211],v224 offset:53248
CV v7,v38,v39
MM a[40:43],a[144:147],v[72:75],a[40:43]
EX v40,v40
MM a[48:51],a[144:147],v[80:83],a[48:51]
buffer_load_dwordx4 v230, s[8:11], s64 offen offset:2048 lds
MM a[48:51],a[148:151],v[84:87],a[48:51]
EX v41,v41
MM a[56:59],a[148:151],v[92:95],a[56:59]
RD a[212:215],v225 offset:53248
CV v12,v40,v41
MM a[56:59],a[144:147],v[88:91],a[56:59]
EX v42,v42
MM a[60:63],a[152:155],v[88:91],a[60:63]
EX v43,v43
MM a[60:63],a[156:159],v[92:95],a[60:63]
RD a[216:219],v226 offset:53248
CV v13,v42,v43
MM a[52:55],a[156:159],v[84:87],a[52:55]
EX v44,v44
MM a[52:55],a[152:155],v[80:83],a[52:55]
buffer_load_dwordx4 v231, s[8:11], s64 offen offset:3072 lds
MM a[44:47],a[152:155],v[72:75],a[44:47]
s_add_u32 m0, s65, 81920
EX v45,v45
MM a[44:47],a[156:159],v[76:79],a[44:47]
RD a[220:223],v227 offset:53248
CV v14,v44,v45
MM a[36:39],a[156:159],v[68:71],a[36:39]
EX v46,v46
MM a[36:39],a[152:155],v[64:67],a[36:39]
RD a[224:227],v224 offset:57344
RD a[228:231],v225 offset:57344
MM a[64:67],a[160:163],v[64:67],a[64:67]
EX v47,v47
MM a[64:67],a[164:167],v[68:71],a[64:67]
RD a[232:235],v226 offset:57344
CV v15,v46,v47
MM a[72:75],a[164:167],v[76:79],a[72:75]
EX v48,v48
MM a[72:75],a[160:163],v[72:75],a[72:75]
buffer_load_dwordx4 v235, s[12:15], s81 offen offset:0 lds
MM a[80:83],a[160:163],v[80:83],a[80:83]
s_add_u32 m0, s67, 0
s_add_u32 s85, s66, 0
buffer_load_dwordx4 v252, s[36:39], s85 offen offset:0 lds
MM a[80:83],a[164:167],v[84:87],a[80:83]
EX v49,v49
MM a[88:91],a[164:167],v[92:95],a[88:91]
EX v50,v50
MM a[88:91],a[160:163],v[88:91],a[88:91]
RD a[236:239],v227 offset:57344
CV v20,v48,v49
MM a[92:95],a[168:171],v[88:91],a[92:95]
EX v51,v51
MM a[92:95],a[172:175],v[92:95],a[92:95]
RD a[240:243],v224 offset:61440
CV v21,v50,v51
MM a[84:87],a[172:175],v[84:87],a[84:87]
EX v52,v52
MM a[84:87],a[168:171],v[80:83],a[84:87]
EX v53,v53
MM a[76:79],a[168:171],v[72:75],a[76:79]
RD a[244:247],v225 offset:61440
CV v22,v52,v53
MM a[76:79],a[172:175],v[76:79],a[76:79]
EX v54,v54
MM a[68:71],a[172:175],v[68:71],a[68:71]
s_add_u32 m0, s65, 81920
s_nop 0
buffer_load_dwordx4 v235, s[12:15], s64 offen offset:1024 lds
MM a[68:71],a[168:171],v[64:67],a[68:71]
EX v55,v55
MM a[96:99],a[176:179],v[64:67],a[96:99]
RD a[248:251],v226 offset:61440
CV v23,v54,v55
MM a[96:99],a[180:183],v[68:71],a[96:99]
EX v56,v56
MM a[104:107],a[180:183],v[76:79],a[104:107]
EX v57,v57
MM a[104:107],a[176:179],v[72:75],a[104:107]
RD a[252:255],v227 offset:61440
CV v28,v56,v57
MM a[112:115],a[176:179],v[80:83],a[112:115]
buffer_load_dwordx4 v235, s[12:15], s81 offen offset:2048 lds
MM a[112:115],a[180:183],v[84:87],a[112:115]
EX v58,v58
MM a[120:123],a[180:183],v[92:95],a[120:123]
EX v59,v59
MM a[120:123],a[176:179],v[88:91],a[120:123]
EX v60,v60
MM a[124:127],a[184:187],v[88:91],a[124:127]
v_add_f32 v244, v144, v145
v_add_f32 v245, v148, v149
MM a[124:127],a[188:191],v[92:95],a[124:127]
v_add_f32 v246, v152, v153
v_add_f32 v247, v156, v157
MM a[116:119],a[188:191],v[84:87],a[116:119]
EX v61,v61
MM a[116:119],a[184:187],v[80:83],a[116:119]
buffer_load_dwordx4 v235, s[12:15], s64 offen offset:3072 lds
MM a[108:111],a[184:187],v[72:75],a[108:111]
EX v62,v62
MM a[108:111],a[188:191],v[76:79],a[108:111]
EX v63,v63
MM a[100:103],a[188:191],v[68:71],a[100:103]
CV v29,v58,v59
CV v30,v60,v61
MM a[100:103],a[184:187],v[64:67],a[100:103]
CV v31,v62,v63
v_smfmac_f32_16x16x64_bf16 v[144:147], v[236:239], v[0:7], v234
v_smfmac_f32_16x16x64_bf16 v[148:151], v[236:239], v[8:15], v234
v_smfmac_f32_16x16x64_bf16 v[152:155], v[236:239], v[16:23], v234
v_smfmac_f32_16x16x64_bf16 v[156:159], v[236:239], v[24:31], v234
s_waitcnt lgkmcnt(0)
MM v[64:67],a[192:195],v[160:163],v[128:131]
s_add_u32 s64, s64, 16384
TR a[128:129],v232 offset:32768
s_add_u32 s81, s64, 896
MM v[64:67],a[196:199],v[176:179],v[64:67]
TR a[130:131],v232 offset:36864
v_max3_f32 v249, v144, v145, v148
MM v[64:67],a[200:203],v[192:195],v[64:67]
TR a[132:133],v232 offset:40960
v_max3_f32 v249, v249, v149, v152
MM v[64:67],a[204:207],v[208:211],v[64:67]
TR a[134:135],v232 offset:45056
v_max3_f32 v249, v249, v153, v156
MM v[68:71],a[220:223],v[208:211],v[128:131]
TR a[136:137],v233 offset:32768
v_max_f32 v249, v249, v157
MM v[68:71],a[216:219],v[192:195],v[68:71]
TR a[138:139],v233 offset:36864
v_cmp_lt_f32 vcc, s71, v249
MM v[68:71],a[212:215],v[176:179],v[68:71]
TR a[140:141],v233 offset:40960
TR a[142:143],v233 offset:45056
MM v[68:71],a[208:211],v[160:163],v[68:71]
s_cbranch_vccnz LSITE10_%=
LBACK10_%=:
MM v[76:79],a[208:211],v[164:167],v[132:135]
s_add_u32 m0, s67, 1024
s_add_u32 s85, s66, 55296
buffer_load_dwordx4 v252, s[36:39], s85 offen offset:0 lds
MM v[76:79],a[212:215],v[180:183],v[76:79]
EX v64,v64
MM v[76:79],a[216:219],v[196:199],v[76:79]
EX v65,v65
MM v[76:79],a[220:223],v[212:215],v[76:79]
TR a[144:145],v232 offset:33280
CV v64,v64,v65
MM v[72:75],a[204:207],v[212:215],v[132:135]
EX v66,v66
MM v[72:75],a[200:203],v[196:199],v[72:75]
EX v67,v67
MM v[72:75],a[196:199],v[180:183],v[72:75]
TR a[146:147],v232 offset:37376
CV v65,v66,v67
MM v[72:75],a[192:195],v[164:167],v[72:75]
EX v68,v68
MM v[80:83],a[192:195],v[168:171],v[136:139]
TR a[148:149],v232 offset:41472
TR a[150:151],v232 offset:45568
MM v[80:83],a[196:199],v[184:187],v[80:83]
EX v69,v69
MM v[80:83],a[200:203],v[200:203],v[80:83]
TR a[152:153],v233 offset:33280
CV v66,v68,v69
MM v[80:83],a[204:207],v[216:219],v[80:83]
EX v70,v70
MM v[84:87],a[220:223],v[216:219],v[136:139]
EX v71,v71
MM v[84:87],a[216:219],v[200:203],v[84:87]
TR a[154:155],v233 offset:37376
CV v67,v70,v71
MM v[84:87],a[212:215],v[184:187],v[84:87]
EX v72,v72
MM v[84:87],a[208:211],v[168:171],v[84:87]
TR a[156:157],v233 offset:41472
TR a[158:159],v233 offset:45568
MM v[92:95],a[208:211],v[172:175],v[140:143]
EX v73,v73
MM v[92:95],a[212:215],v[188:191],v[92:95]
TR a[160:161],v232 offset:33792
CV v72,v72,v73
MM v[92:95],a[216:219],v[204:207],v[92:95]
EX v74,v74
MM v[92:95],a[220:223],v[220:223],v[92:95]
EX v75,v75
MM v[88:91],a[204:207],v[220:223],v[140:143]
TR a[162:163],v232 offset:37888
CV v73,v74,v75
MM v[88:91],a[200:203],v[204:207],v[88:91]
TR a[164:165],v232 offset:41984
TR a[166:167],v232 offset:46080
MM v[88:91],a[196:199],v[188:191],v[88:91]
EX v76,v76
MM v[88:91],a[192:195],v[172:175],v[88:91]
EX v77,v77
MM v[96:99],a[224:227],v[160:163],v[128:131]
TR a[168:169],v233 offset:33792
CV v74,v76,v77
MM v[96:99],a[228:231],v[176:179],v[96:99]
EX v78,v78
MM v[96:99],a[232:235],v[192:195],v[96:99]
TR a[170:171],v233 offset:37888
TR a[172:173],v233 offset:41984
MM v[96:99],a[236:239],v[208:211],v[96:99]
EX v79,v79
MM v[100:103],a[252:255],v[208:211],v[128:131]
TR a[174:175],v233 offset:46080
CV v75,v78,v79
MM v[100:103],a[248:251],v[192:195],v[100:103]
EX v80,v80
MM v[100:103],a[244:247],v[176:179],v[100:103]
EX v81,v81
MM v[100:103],a[240:243],v[160:163],v[100:103]
TR a[176:177],v232 offset:34304
CV v80,v80,v81
MM v[108:111],a[240:243],v[164:167],v[132:135]
s_add_u32 m0, s67, 2048
s_add_u32 s85, s66, 110592
buffer_load_dwordx4 v252, s[36:39], s85 offen offset:0 lds
MM v[108:111],a[244:247],v[180:183],v[108:111]
EX v82,v82
MM v[108:111],a[248:251],v[196:199],v[108:111]
TR a[178:179],v232 offset:38400
TR a[180:181],v232 offset:42496
MM v[108:111],a[252:255],v[212:215],v[108:111]
EX v83,v83
MM v[104:107],a[236:239],v[212:215],v[132:135]
TR a[182:183],v232 offset:46592
CV v81,v82,v83
MM v[104:107],a[232:235],v[196:199],v[104:107]
EX v84,v84
MM v[104:107],a[228:231],v[180:183],v[104:107]
EX v85,v85
MM v[104:107],a[224:227],v[164:167],v[104:107]
TR a[184:185],v233 offset:34304
CV v82,v84,v85
MM v[112:115],a[224:227],v[168:171],v[136:139]
EX v86,v86
MM v[112:115],a[228:231],v[184:187],v[112:115]
TR a[186:187],v233 offset:38400
TR a[188:189],v233 offset:42496
MM v[112:115],a[232:235],v[200:203],v[112:115]
EX v87,v87
MM v[112:115],a[236:239],v[216:219],v[112:115]
TR a[190:191],v233 offset:46592
CV v83,v86,v87
MM v[116:119],a[252:255],v[216:219],v[136:139]
EX v88,v88
MM v[116:119],a[248:251],v[200:203],v[116:119]
EX v89,v89
MM v[116:119],a[244:247],v[184:187],v[116:119]
EX v90,v90
MM v[116:119],a[240:243],v[168:171],v[116:119]
EX v91,v91
MM v[124:127],a[240:243],v[172:175],v[140:143]
EX v92,v92
MM v[124:127],a[244:247],v[188:191],v[124:127]
EX v93,v93
MM v[124:127],a[248:251],v[204:207],v[124:127]
EX v94,v94
MM v[124:127],a[252:255],v[220:223],v[124:127]
)ASM"
R"ASM(
EX v95,v95
MM v[120:123],a[236:239],v[220:223],v[140:143]
EX v96,v96
MM v[120:123],a[232:235],v[204:207],v[120:123]
EX v97,v97
MM v[120:123],a[228:231],v[188:191],v[120:123]
EX v98,v98
MM v[120:123],a[224:227],v[172:175],v[120:123]
s_waitcnt vmcnt(15) lgkmcnt(0)
s_barrier
MM a[0:3],a[128:131],v[0:3],a[0:3]
s_add_u32 m0, s65, 32768
CV v88,v88,v89
CV v89,v90,v91
MM a[0:3],a[132:135],v[4:7],a[0:3]
RD a[192:195],v224 offset:0
CV v90,v92,v93
MM a[8:11],a[132:135],v[12:15],a[8:11]
RD a[196:199],v225 offset:0
CV v91,v94,v95
MM a[8:11],a[128:131],v[8:11],a[8:11]
buffer_load_dwordx4 v228, s[8:11], s64 offen offset:0 lds
MM a[16:19],a[128:131],v[16:19],a[16:19]
RD a[200:203],v226 offset:0
CV v68,v96,v97
MM a[16:19],a[132:135],v[20:23],a[16:19]
buffer_load_dwordx4 v229, s[8:11], s64 offen offset:1024 lds
MM a[24:27],a[132:135],v[28:31],a[24:27]
RD a[204:207],v227 offset:0
RD a[208:211],v224 offset:4096
MM a[24:27],a[128:131],v[24:27],a[24:27]
EX v99,v99
MM a[28:31],a[136:139],v[24:27],a[28:31]
RD a[212:215],v225 offset:4096
CV v69,v98,v99
MM a[28:31],a[140:143],v[28:31],a[28:31]
EX v100,v100
MM a[20:23],a[140:143],v[20:23],a[20:23]
buffer_load_dwordx4 v230, s[8:11], s64 offen offset:2048 lds
MM a[20:23],a[136:139],v[16:19],a[20:23]
EX v101,v101
MM a[12:15],a[136:139],v[8:11],a[12:15]
RD a[216:219],v226 offset:4096
CV v70,v100,v101
MM a[12:15],a[140:143],v[12:15],a[12:15]
EX v102,v102
MM a[4:7],a[140:143],v[4:7],a[4:7]
EX v103,v103
MM a[4:7],a[136:139],v[0:3],a[4:7]
RD a[220:223],v227 offset:4096
CV v71,v102,v103
MM a[32:35],a[144:147],v[0:3],a[32:35]
EX v104,v104
MM a[32:35],a[148:151],v[4:7],a[32:35]
buffer_load_dwordx4 v231, s[8:11], s64 offen offset:3072 lds
MM a[40:43],a[148:151],v[12:15],a[40:43]
s_add_u32 m0, s65, 98304
EX v105,v105
MM a[40:43],a[144:147],v[8:11],a[40:43]
RD a[224:227],v224 offset:8192
CV v76,v104,v105
MM a[48:51],a[144:147],v[16:19],a[48:51]
EX v106,v106
MM a[48:51],a[148:151],v[20:23],a[48:51]
EX v107,v107
MM a[56:59],a[148:151],v[28:31],a[56:59]
RD a[228:231],v225 offset:8192
CV v77,v106,v107
MM a[56:59],a[144:147],v[24:27],a[56:59]
EX v108,v108
MM a[60:63],a[152:155],v[24:27],a[60:63]
buffer_load_dwordx4 v235, s[12:15], s81 offen offset:0 lds
MM a[60:63],a[156:159],v[28:31],a[60:63]
EX v109,v109
MM a[52:55],a[156:159],v[20:23],a[52:55]
RD a[232:235],v226 offset:8192
CV v78,v108,v109
MM a[52:55],a[152:155],v[16:19],a[52:55]
EX v110,v110
MM a[44:47],a[152:155],v[8:11],a[44:47]
EX v111,v111
MM a[44:47],a[156:159],v[12:15],a[44:47]
RD a[236:239],v227 offset:8192
CV v79,v110,v111
MM a[36:39],a[156:159],v[4:7],a[36:39]
buffer_load_dwordx4 v235, s[12:15], s64 offen offset:1024 lds
MM a[36:39],a[152:155],v[0:3],a[36:39]
EX v112,v112
MM a[64:67],a[160:163],v[0:3],a[64:67]
EX v113,v113
MM a[64:67],a[164:167],v[4:7],a[64:67]
RD a[240:243],v224 offset:12288
CV v84,v112,v113
MM a[72:75],a[164:167],v[12:15],a[72:75]
EX v114,v114
MM a[72:75],a[160:163],v[8:11],a[72:75]
RD a[244:247],v225 offset:12288
RD a[248:251],v226 offset:12288
MM a[80:83],a[160:163],v[16:19],a[80:83]
s_add_u32 m0, s67, 3072
s_add_u32 s85, s66, 165888
buffer_load_dwordx4 v252, s[36:39], s85 offen offset:0 lds
MM a[80:83],a[164:167],v[20:23],a[80:83]
EX v115,v115
MM a[88:91],a[164:167],v[28:31],a[88:91]
RD a[252:255],v227 offset:12288
CV v85,v114,v115
MM a[88:91],a[160:163],v[24:27],a[88:91]
EX v116,v116
MM a[92:95],a[168:171],v[24:27],a[92:95]
EX v117,v117
MM a[92:95],a[172:175],v[28:31],a[92:95]
s_add_u32 m0, s65, 98304
s_nop 0
buffer_load_dwordx4 v235, s[12:15], s81 offen offset:2048 lds
MM a[84:87],a[172:175],v[20:23],a[84:87]
EX v118,v118
MM a[84:87],a[168:171],v[16:19],a[84:87]
EX v119,v119
MM a[76:79],a[168:171],v[8:11],a[76:79]
buffer_load_dwordx4 v235, s[12:15], s64 offen offset:3072 lds
MM a[76:79],a[172:175],v[12:15],a[76:79]
EX v120,v120
MM a[68:71],a[172:175],v[4:7],a[68:71]
EX v121,v121
MM a[68:71],a[168:171],v[0:3],a[68:71]
EX v122,v122
MM a[96:99],a[176:179],v[0:3],a[96:99]
EX v123,v123
MM a[96:99],a[180:183],v[4:7],a[96:99]
EX v124,v124
MM a[104:107],a[180:183],v[12:15],a[104:107]
EX v125,v125
MM a[104:107],a[176:179],v[8:11],a[104:107]
EX v126,v126
MM a[112:115],a[176:179],v[16:19],a[112:115]
EX v127,v127
MM a[112:115],a[180:183],v[20:23],a[112:115]
CV v86,v116,v117
CV v87,v118,v119
MM a[120:123],a[180:183],v[28:31],a[120:123]
CV v92,v120,v121
CV v93,v122,v123
MM a[120:123],a[176:179],v[24:27],a[120:123]
CV v94,v124,v125
CV v95,v126,v127
MM a[124:127],a[184:187],v[24:27],a[124:127]
v_add_f32 v244, v144, v145
v_add_f32 v245, v148, v149
MM a[124:127],a[188:191],v[28:31],a[124:127]
v_add_f32 v246, v152, v153
v_add_f32 v247, v156, v157
MM a[116:119],a[188:191],v[20:23],a[116:119]
MM a[116:119],a[184:187],v[16:19],a[116:119]
MM a[108:111],a[184:187],v[8:11],a[108:111]
MM a[108:111],a[188:191],v[12:15],a[108:111]
MM a[100:103],a[188:191],v[4:7],a[100:103]
MM a[100:103],a[184:187],v[0:3],a[100:103]
v_smfmac_f32_16x16x64_bf16 v[144:147], v[236:239], v[64:71], v234
v_smfmac_f32_16x16x64_bf16 v[148:151], v[236:239], v[72:79], v234
v_smfmac_f32_16x16x64_bf16 v[152:155], v[236:239], v[80:87], v234
v_smfmac_f32_16x16x64_bf16 v[156:159], v[236:239], v[88:95], v234
s_waitcnt lgkmcnt(0)
MM v[0:3],a[192:195],v[160:163],v[128:131]
s_add_u32 s64, s64, 16384
TR a[128:129],v232 offset:49152
s_add_u32 s81, s64, 896
MM v[0:3],a[196:199],v[176:179],v[0:3]
TR a[130:131],v232 offset:53248
v_max3_f32 v249, v144, v145, v148
MM v[0:3],a[200:203],v[192:195],v[0:3]
TR a[132:133],v232 offset:57344
v_max3_f32 v249, v249, v149, v152
MM v[0:3],a[204:207],v[208:211],v[0:3]
TR a[134:135],v232 offset:61440
v_max3_f32 v249, v249, v153, v156
MM v[4:7],a[220:223],v[208:211],v[128:131]
TR a[136:137],v233 offset:49152
v_max_f32 v249, v249, v157
MM v[4:7],a[216:219],v[192:195],v[4:7]
TR a[138:139],v233 offset:53248
v_cmp_lt_f32 vcc, s71, v249
MM v[4:7],a[212:215],v[176:179],v[4:7]
TR a[140:141],v233 offset:57344
TR a[142:143],v233 offset:61440
MM v[4:7],a[208:211],v[160:163],v[4:7]
s_cbranch_vccnz LSITE11_%=
LBACK11_%=:
MM v[12:15],a[208:211],v[164:167],v[132:135]
s_add_u32 m0, s67, 4096
s_add_u32 s85, s66, 221184
buffer_load_dwordx4 v252, s[36:39], s85 offen offset:0 lds
MM v[12:15],a[212:215],v[180:183],v[12:15]
EX v0,v0
MM v[12:15],a[216:219],v[196:199],v[12:15]
EX v1,v1
MM v[12:15],a[220:223],v[212:215],v[12:15]
TR a[144:145],v232 offset:49664
CV v0,v0,v1
MM v[8:11],a[204:207],v[212:215],v[132:135]
EX v2,v2
MM v[8:11],a[200:203],v[196:199],v[8:11]
EX v3,v3
MM v[8:11],a[196:199],v[180:183],v[8:11]
TR a[146:147],v232 offset:53760
CV v1,v2,v3
MM v[8:11],a[192:195],v[164:167],v[8:11]
EX v4,v4
MM v[16:19],a[192:195],v[168:171],v[136:139]
TR a[148:149],v232 offset:57856
TR a[150:151],v232 offset:61952
MM v[16:19],a[196:199],v[184:187],v[16:19]
EX v5,v5
MM v[16:19],a[200:203],v[200:203],v[16:19]
TR a[152:153],v233 offset:49664
CV v2,v4,v5
MM v[16:19],a[204:207],v[216:219],v[16:19]
EX v6,v6
MM v[20:23],a[220:223],v[216:219],v[136:139]
EX v7,v7
MM v[20:23],a[216:219],v[200:203],v[20:23]
TR a[154:155],v233 offset:53760
CV v3,v6,v7
MM v[20:23],a[212:215],v[184:187],v[20:23]
EX v8,v8
MM v[20:23],a[208:211],v[168:171],v[20:23]
TR a[156:157],v233 offset:57856
TR a[158:159],v233 offset:61952
MM v[28:31],a[208:211],v[172:175],v[140:143]
EX v9,v9
MM v[28:31],a[212:215],v[188:191],v[28:31]
TR a[160:161],v232 offset:50176
CV v8,v8,v9
MM v[28:31],a[216:219],v[204:207],v[28:31]
EX v10,v10
MM v[28:31],a[220:223],v[220:223],v[28:31]
EX v11,v11
MM v[24:27],a[204:207],v[220:223],v[140:143]
TR a[162:163],v232 offset:54272
CV v9,v10,v11
MM v[24:27],a[200:203],v[204:207],v[24:27]
TR a[164:165],v232 offset:58368
TR a[166:167],v232 offset:62464
MM v[24:27],a[196:199],v[188:191],v[24:27]
EX v12,v12
MM v[24:27],a[192:195],v[172:175],v[24:27]
EX v13,v13
MM v[32:35],a[224:227],v[160:163],v[128:131]
TR a[168:169],v233 offset:50176
CV v10,v12,v13
MM v[32:35],a[228:231],v[176:179],v[32:35]
EX v14,v14
MM v[32:35],a[232:235],v[192:195],v[32:35]
TR a[170:171],v233 offset:54272
TR a[172:173],v233 offset:58368
MM v[32:35],a[236:239],v[208:211],v[32:35]
EX v15,v15
MM v[36:39],a[252:255],v[208:211],v[128:131]
TR a[174:175],v233 offset:62464
CV v11,v14,v15
MM v[36:39],a[248:251],v[192:195],v[36:39]
EX v16,v16
MM v[36:39],a[244:247],v[176:179],v[36:39]
EX v17,v17
MM v[36:39],a[240:243],v[160:163],v[36:39]
TR a[176:177],v232 offset:50688
CV v16,v16,v17
MM v[44:47],a[240:243],v[164:167],v[132:135]
EX v18,v18
MM v[44:47],a[244:247],v[180:183],v[44:47]
TR a[178:179],v232 offset:54784
TR a[180:181],v232 offset:58880
MM v[44:47],a[248:251],v[196:199],v[44:47]
EX v19,v19
MM v[44:47],a[252:255],v[212:215],v[44:47]
TR a[182:183],v232 offset:62976
CV v17,v18,v19
MM v[40:43],a[236:239],v[212:215],v[132:135]
EX v20,v20
MM v[40:43],a[232:235],v[196:199],v[40:43]
EX v21,v21
MM v[40:43],a[228:231],v[180:183],v[40:43]
TR a[184:185],v233 offset:50688
CV v18,v20,v21
MM v[40:43],a[224:227],v[164:167],v[40:43]
EX v22,v22
MM v[48:51],a[224:227],v[168:171],v[136:139]
TR a[186:187],v233 offset:54784
TR a[188:189],v233 offset:58880
MM v[48:51],a[228:231],v[184:187],v[48:51]
EX v23,v23
MM v[48:51],a[232:235],v[200:203],v[48:51]
TR a[190:191],v233 offset:62976
CV v19,v22,v23
MM v[48:51],a[236:239],v[216:219],v[48:51]
EX v24,v24
MM v[52:55],a[252:255],v[216:219],v[136:139]
EX v25,v25
MM v[52:55],a[248:251],v[200:203],v[52:55]
EX v26,v26
MM v[52:55],a[244:247],v[184:187],v[52:55]
EX v27,v27
MM v[52:55],a[240:243],v[168:171],v[52:55]
EX v28,v28
MM v[60:63],a[240:243],v[172:175],v[140:143]
EX v29,v29
MM v[60:63],a[244:247],v[188:191],v[60:63]
EX v30,v30
MM v[60:63],a[248:251],v[204:207],v[60:63]
EX v31,v31
LPG11_%=:
s_cbranch_execz LTR0_%=
MM v[60:63],a[252:255],v[220:223],v[60:63]
EX v32,v32
MM v[56:59],a[236:239],v[220:223],v[140:143]
EX v33,v33
MM v[56:59],a[232:235],v[204:207],v[56:59]
EX v34,v34
MM v[56:59],a[228:231],v[188:191],v[56:59]
EX v35,v35
MM v[56:59],a[224:227],v[172:175],v[56:59]
s_waitcnt vmcnt(17) lgkmcnt(0)
s_barrier
MM a[0:3],a[128:131],v[64:67],a[0:3]
s_mov_b32 s86, 94208
v_bfe_u32 v251, v248, 4, 3
v_lshlrev_b32 v251, 3, v251
buffer_load_dwordx2 v[252:253], v251, s[20:23], s86 offen offset:0 sc1
MM a[0:3],a[132:135],v[68:71],a[0:3]
s_add_u32 m0, s65, 49152
CV v24,v24,v25
CV v25,v26,v27
MM a[8:11],a[132:135],v[76:79],a[8:11]
RD a[192:195],v224 offset:16384
CV v26,v28,v29
MM a[8:11],a[128:131],v[72:75],a[8:11]
RD a[196:199],v225 offset:16384
CV v27,v30,v31
MM a[16:19],a[128:131],v[80:83],a[16:19]
buffer_load_dwordx4 v228, s[8:11], s64 offen offset:0 lds
MM a[16:19],a[132:135],v[84:87],a[16:19]
RD a[200:203],v226 offset:16384
CV v4,v32,v33
MM a[24:27],a[132:135],v[92:95],a[24:27]
RD a[204:207],v227 offset:16384
CV v5,v34,v35
MM a[24:27],a[128:131],v[88:91],a[24:27]
buffer_load_dwordx4 v229, s[8:11], s64 offen offset:1024 lds
MM a[28:31],a[136:139],v[88:91],a[28:31]
RD a[208:211],v224 offset:20480
RD a[212:215],v225 offset:20480
MM a[28:31],a[140:143],v[92:95],a[28:31]
EX v36,v36
MM a[20:23],a[140:143],v[84:87],a[20:23]
buffer_load_dwordx4 v230, s[8:11], s64 offen offset:2048 lds
MM a[20:23],a[136:139],v[80:83],a[20:23]
EX v37,v37
MM a[12:15],a[136:139],v[72:75],a[12:15]
RD a[216:219],v226 offset:20480
CV v6,v36,v37
MM a[12:15],a[140:143],v[76:79],a[12:15]
EX v38,v38
MM a[4:7],a[140:143],v[68:71],a[4:7]
EX v39,v39
MM a[4:7],a[136:139],v[64:67],a[4:7]
RD a[220:223],v227 offset:20480
CV v7,v38,v39
MM a[32:35],a[144:147],v[64:67],a[32:35]
EX v40,v40
MM a[32:35],a[148:151],v[68:71],a[32:35]
buffer_load_dwordx4 v231, s[8:11], s64 offen offset:3072 lds
MM a[40:43],a[148:151],v[76:79],a[40:43]
s_add_u32 m0, s65, 114688
EX v41,v41
MM a[40:43],a[144:147],v[72:75],a[40:43]
RD a[224:227],v224 offset:24576
CV v12,v40,v41
MM a[48:51],a[144:147],v[80:83],a[48:51]
EX v42,v42
MM a[48:51],a[148:151],v[84:87],a[48:51]
EX v43,v43
MM a[56:59],a[148:151],v[92:95],a[56:59]
RD a[228:231],v225 offset:24576
CV v13,v42,v43
MM a[56:59],a[144:147],v[88:91],a[56:59]
EX v44,v44
MM a[60:63],a[152:155],v[88:91],a[60:63]
buffer_load_dwordx4 v235, s[12:15], s81 offen offset:0 lds
MM a[60:63],a[156:159],v[92:95],a[60:63]
)ASM"
R"ASM(
EX v45,v45
MM a[52:55],a[156:159],v[84:87],a[52:55]
RD a[232:235],v226 offset:24576
CV v14,v44,v45
MM a[52:55],a[152:155],v[80:83],a[52:55]
EX v46,v46
MM a[44:47],a[152:155],v[72:75],a[44:47]
EX v47,v47
MM a[44:47],a[156:159],v[76:79],a[44:47]
RD a[236:239],v227 offset:24576
CV v15,v46,v47
MM a[36:39],a[156:159],v[68:71],a[36:39]
buffer_load_dwordx4 v235, s[12:15], s64 offen offset:1024 lds
MM a[36:39],a[152:155],v[64:67],a[36:39]
EX v48,v48
MM a[64:67],a[160:163],v[64:67],a[64:67]
EX v49,v49
MM a[64:67],a[164:167],v[68:71],a[64:67]
RD a[240:243],v224 offset:28672
CV v20,v48,v49
MM a[72:75],a[164:167],v[76:79],a[72:75]
EX v50,v50
MM a[72:75],a[160:163],v[72:75],a[72:75]
RD a[244:247],v225 offset:28672
RD a[248:251],v226 offset:28672
MM a[80:83],a[160:163],v[80:83],a[80:83]
EX v51,v51
MM a[80:83],a[164:167],v[84:87],a[80:83]
RD a[252:255],v227 offset:28672
CV v21,v50,v51
MM a[88:91],a[164:167],v[92:95],a[88:91]
EX v52,v52
MM a[88:91],a[160:163],v[88:91],a[88:91]
buffer_load_dwordx4 v235, s[12:15], s81 offen offset:2048 lds
MM a[92:95],a[168:171],v[88:91],a[92:95]
EX v53,v53
MM a[92:95],a[172:175],v[92:95],a[92:95]
EX v54,v54
MM a[84:87],a[172:175],v[84:87],a[84:87]
buffer_load_dwordx4 v235, s[12:15], s64 offen offset:3072 lds
MM a[84:87],a[168:171],v[80:83],a[84:87]
EX v55,v55
MM a[76:79],a[168:171],v[72:75],a[76:79]
EX v56,v56
MM a[76:79],a[172:175],v[76:79],a[76:79]
EX v57,v57
MM a[68:71],a[172:175],v[68:71],a[68:71]
EX v58,v58
MM a[68:71],a[168:171],v[64:67],a[68:71]
EX v59,v59
MM a[96:99],a[176:179],v[64:67],a[96:99]
EX v60,v60
MM a[96:99],a[180:183],v[68:71],a[96:99]
EX v61,v61
MM a[104:107],a[180:183],v[76:79],a[104:107]
EX v62,v62
MM a[104:107],a[176:179],v[72:75],a[104:107]
EX v63,v63
MM a[112:115],a[176:179],v[80:83],a[112:115]
CV v22,v52,v53
CV v23,v54,v55
MM a[112:115],a[180:183],v[84:87],a[112:115]
CV v28,v56,v57
CV v29,v58,v59
MM a[120:123],a[180:183],v[92:95],a[120:123]
CV v30,v60,v61
CV v31,v62,v63
MM a[120:123],a[176:179],v[88:91],a[120:123]
MM a[124:127],a[184:187],v[88:91],a[124:127]
v_add_f32 v244, v144, v145
v_add_f32 v245, v148, v149
MM a[124:127],a[188:191],v[92:95],a[124:127]
v_add_f32 v246, v152, v153
v_add_f32 v247, v156, v157
MM a[116:119],a[188:191],v[84:87],a[116:119]
MM a[116:119],a[184:187],v[80:83],a[116:119]
MM a[108:111],a[184:187],v[72:75],a[108:111]
MM a[108:111],a[188:191],v[76:79],a[108:111]
MM a[100:103],a[188:191],v[68:71],a[100:103]
MM a[100:103],a[184:187],v[64:67],a[100:103]
v_smfmac_f32_16x16x64_bf16 v[144:147], v[236:239], v[0:7], v234
v_smfmac_f32_16x16x64_bf16 v[148:151], v[236:239], v[8:15], v234
v_smfmac_f32_16x16x64_bf16 v[152:155], v[236:239], v[16:23], v234
v_smfmac_f32_16x16x64_bf16 v[156:159], v[236:239], v[24:31], v234
s_waitcnt lgkmcnt(0)
MM v[64:67],a[192:195],v[160:163],v[128:131]
s_add_u32 s64, s64, 16384
TR a[128:129],v232 offset:0
s_add_u32 s81, s64, 896
MM v[64:67],a[196:199],v[176:179],v[64:67]
TR a[130:131],v232 offset:4096
v_max3_f32 v249, v144, v145, v148
MM v[64:67],a[200:203],v[192:195],v[64:67]
TR a[132:133],v232 offset:8192
v_max3_f32 v249, v249, v149, v152
MM v[64:67],a[204:207],v[208:211],v[64:67]
TR a[134:135],v232 offset:12288
v_max3_f32 v249, v249, v153, v156
MM v[68:71],a[220:223],v[208:211],v[128:131]
TR a[136:137],v233 offset:0
v_max_f32 v249, v249, v157
MM v[68:71],a[216:219],v[192:195],v[68:71]
TR a[138:139],v233 offset:4096
v_cmp_lt_f32 vcc, s71, v249
MM v[68:71],a[212:215],v[176:179],v[68:71]
TR a[140:141],v233 offset:8192
TR a[142:143],v233 offset:12288
MM v[68:71],a[208:211],v[160:163],v[68:71]
s_cbranch_vccnz LSITE12_%=
LBACK12_%=:
MM v[76:79],a[208:211],v[164:167],v[132:135]
s_waitcnt vmcnt(8)
v_cmp_ne_u32 vcc, s69, v253
s_cbranch_vccnz LRIX_%=
LRIXb_%=:
MM v[76:79],a[212:215],v[180:183],v[76:79]
s_and_b32 s90, s17, 7
v_and_b32 v253, 65535, v252
v_readlane_b32 s19, v252, s90
MM v[76:79],a[216:219],v[196:199],v[76:79]
v_sub_u32 v253, 65535, v253
v_mul_u32_u24 v253, 1280, v253
MM v[76:79],a[220:223],v[212:215],v[76:79]
v_lshrrev_b32 v252, 7, v248
v_lshl_add_u32 v253, v252, 4, v253
MM v[72:75],a[204:207],v[212:215],v[132:135]
s_mul_i32 s91, %[swave], 3072
EX v64,v64
MM v[72:75],a[200:203],v[196:199],v[72:75]
s_add_u32 s91, s91, %[slds]
EX v65,v65
MM v[72:75],a[196:199],v[180:183],v[72:75]
s_bfe_u32 s90, s48, 131077
EX v66,v66
MM v[72:75],a[192:195],v[164:167],v[72:75]
s_mul_i32 s90, s90, 320
EX v67,v67
MM v[80:83],a[192:195],v[168:171],v[136:139]
s_add_u32 s90, s90, 131072
s_add_u32 m0, s91, 151552
s_nop 0
buffer_load_dwordx4 v253, s[20:23], s90 offen offset:0 sc1 lds
MM v[80:83],a[196:199],v[184:187],v[80:83]
TR a[144:145],v232 offset:512
CV v64,v64,v65
MM v[80:83],a[200:203],v[200:203],v[80:83]
s_add_u32 m0, s91, 152448
s_nop 0
buffer_load_dwordx4 v253, s[20:23], s90 offen offset:128 sc1 lds
MM v[80:83],a[204:207],v[216:219],v[80:83]
EX v68,v68
MM v[84:87],a[220:223],v[216:219],v[136:139]
s_add_u32 m0, s91, 153344
s_nop 0
buffer_load_dwordx4 v253, s[20:23], s90 offen offset:256 sc1 lds
MM v[84:87],a[216:219],v[200:203],v[84:87]
EX v69,v69
MM v[84:87],a[212:215],v[184:187],v[84:87]
s_lshr_b32 s77, %[sb], 3
TR a[146:147],v232 offset:4608
TR a[148:149],v232 offset:8704
MM v[84:87],a[208:211],v[168:171],v[84:87]
s_lshl_b32 s77, s77, 2
EX v70,v70
MM v[92:95],a[208:211],v[172:175],v[140:143]
s_add_u32 s77, s77, %[swave]
EX v71,v71
MM v[92:95],a[212:215],v[188:191],v[92:95]
s_lshl_b32 s77, s77, 9
TR a[150:151],v232 offset:12800
CV v65,v66,v67
MM v[92:95],a[216:219],v[204:207],v[92:95]
s_add_u32 s77, s77, 0
v_lshrrev_b32 v253, 1, v248
v_add_u32 v253, s77, v253
MM v[92:95],a[220:223],v[220:223],v[92:95]
s_mov_b32 s77, LKEND_ka-LPD0_%=-16
TR a[152:153],v233 offset:512
v_min_u32 v253, s77, v253
EX v72,v72
MM v[88:91],a[204:207],v[220:223],v[140:143]
s_getpc_b64 s[98:99]
LPD0_%=:
global_load_dword v253, v253, s[98:99]
MM v[88:91],a[200:203],v[204:207],v[88:91]
s_add_u32 s67, s95, 1024
EX v73,v73
MM v[88:91],a[196:199],v[188:191],v[88:91]
s_mov_b32 s70, 5
TR a[154:155],v233 offset:4608
CV v66,v68,v69
MM v[88:91],a[192:195],v[172:175],v[88:91]
EX v74,v74
MM v[96:99],a[224:227],v[160:163],v[128:131]
TR a[156:157],v233 offset:8704
TR a[158:159],v233 offset:12800
EX v75,v75
MM v[96:99],a[228:231],v[176:179],v[96:99]
TR a[160:161],v232 offset:1024
CV v67,v70,v71
MM v[96:99],a[232:235],v[192:195],v[96:99]
EX v76,v76
MM v[96:99],a[236:239],v[208:211],v[96:99]
EX v77,v77
MM v[100:103],a[252:255],v[208:211],v[128:131]
TR a[162:163],v232 offset:5120
CV v72,v72,v73
MM v[100:103],a[248:251],v[192:195],v[100:103]
EX v78,v78
MM v[100:103],a[244:247],v[176:179],v[100:103]
TR a[164:165],v232 offset:9216
CV v73,v74,v75
MM v[100:103],a[240:243],v[160:163],v[100:103]
EX v79,v79
MM v[108:111],a[240:243],v[164:167],v[132:135]
TR a[166:167],v232 offset:13312
TR a[168:169],v233 offset:1024
MM v[108:111],a[244:247],v[180:183],v[108:111]
EX v80,v80
MM v[108:111],a[248:251],v[196:199],v[108:111]
EX v81,v81
MM v[108:111],a[252:255],v[212:215],v[108:111]
TR a[170:171],v233 offset:5120
CV v74,v76,v77
MM v[104:107],a[236:239],v[212:215],v[132:135]
EX v82,v82
MM v[104:107],a[232:235],v[196:199],v[104:107]
TR a[172:173],v233 offset:9216
CV v75,v78,v79
MM v[104:107],a[228:231],v[180:183],v[104:107]
EX v83,v83
MM v[104:107],a[224:227],v[164:167],v[104:107]
TR a[174:175],v233 offset:13312
TR a[176:177],v232 offset:1536
MM v[112:115],a[224:227],v[168:171],v[136:139]
EX v84,v84
MM v[112:115],a[228:231],v[184:187],v[112:115]
TR a[178:179],v232 offset:5632
CV v80,v80,v81
MM v[112:115],a[232:235],v[200:203],v[112:115]
EX v85,v85
MM v[112:115],a[236:239],v[216:219],v[112:115]
TR a[180:181],v232 offset:9728
CV v81,v82,v83
MM v[116:119],a[252:255],v[216:219],v[136:139]
EX v86,v86
MM v[116:119],a[248:251],v[200:203],v[116:119]
TR a[182:183],v232 offset:13824
TR a[184:185],v233 offset:1536
MM v[116:119],a[244:247],v[184:187],v[116:119]
EX v87,v87
MM v[116:119],a[240:243],v[168:171],v[116:119]
EX v88,v88
MM v[124:127],a[240:243],v[172:175],v[140:143]
TR a[186:187],v233 offset:5632
CV v82,v84,v85
MM v[124:127],a[244:247],v[188:191],v[124:127]
EX v89,v89
MM v[124:127],a[248:251],v[204:207],v[124:127]
TR a[188:189],v233 offset:9728
TR a[190:191],v233 offset:13824
MM v[124:127],a[252:255],v[220:223],v[124:127]
EX v90,v90
MM v[120:123],a[236:239],v[220:223],v[140:143]
CV v83,v86,v87
CV v88,v88,v89
MM v[120:123],a[232:235],v[204:207],v[120:123]
EX v91,v91
MM v[120:123],a[228:231],v[188:191],v[120:123]
EX v92,v92
MM v[120:123],a[224:227],v[172:175],v[120:123]
s_waitcnt lgkmcnt(0)
s_barrier
MM a[0:3],a[128:131],v[0:3],a[0:3]
s_add_u32 m0, s65, 0
EX v93,v93
MM a[0:3],a[132:135],v[4:7],a[0:3]
CV v89,v90,v91
CV v90,v92,v93
MM a[8:11],a[132:135],v[12:15],a[8:11]
EX v94,v94
MM a[8:11],a[128:131],v[8:11],a[8:11]
EX v95,v95
MM a[16:19],a[128:131],v[16:19],a[16:19]
RD a[192:195],v224 offset:32768
CV v91,v94,v95
MM a[16:19],a[132:135],v[20:23],a[16:19]
EX v96,v96
MM a[24:27],a[132:135],v[28:31],a[24:27]
buffer_load_dwordx4 v228, s[8:11], s64 offen offset:0 lds
MM a[24:27],a[128:131],v[24:27],a[24:27]
EX v97,v97
MM a[28:31],a[136:139],v[24:27],a[28:31]
RD a[196:199],v225 offset:32768
CV v68,v96,v97
MM a[28:31],a[140:143],v[28:31],a[28:31]
EX v98,v98
MM a[20:23],a[140:143],v[20:23],a[20:23]
EX v99,v99
MM a[20:23],a[136:139],v[16:19],a[20:23]
RD a[200:203],v226 offset:32768
CV v69,v98,v99
MM a[12:15],a[136:139],v[8:11],a[12:15]
EX v100,v100
MM a[12:15],a[140:143],v[12:15],a[12:15]
buffer_load_dwordx4 v229, s[8:11], s64 offen offset:1024 lds
MM a[4:7],a[140:143],v[4:7],a[4:7]
EX v101,v101
MM a[4:7],a[136:139],v[0:3],a[4:7]
RD a[204:207],v227 offset:32768
CV v70,v100,v101
MM a[32:35],a[144:147],v[0:3],a[32:35]
EX v102,v102
MM a[32:35],a[148:151],v[4:7],a[32:35]
EX v103,v103
MM a[40:43],a[148:151],v[12:15],a[40:43]
RD a[208:211],v224 offset:36864
CV v71,v102,v103
MM a[40:43],a[144:147],v[8:11],a[40:43]
EX v104,v104
MM a[48:51],a[144:147],v[16:19],a[48:51]
buffer_load_dwordx4 v230, s[8:11], s64 offen offset:2048 lds
MM a[48:51],a[148:151],v[20:23],a[48:51]
EX v105,v105
MM a[56:59],a[148:151],v[28:31],a[56:59]
RD a[212:215],v225 offset:36864
CV v76,v104,v105
MM a[56:59],a[144:147],v[24:27],a[56:59]
EX v106,v106
MM a[60:63],a[152:155],v[24:27],a[60:63]
EX v107,v107
MM a[60:63],a[156:159],v[28:31],a[60:63]
RD a[216:219],v226 offset:36864
CV v77,v106,v107
MM a[52:55],a[156:159],v[20:23],a[52:55]
EX v108,v108
MM a[52:55],a[152:155],v[16:19],a[52:55]
buffer_load_dwordx4 v231, s[8:11], s64 offen offset:3072 lds
MM a[44:47],a[152:155],v[8:11],a[44:47]
s_add_u32 m0, s65, 65536
EX v109,v109
MM a[44:47],a[156:159],v[12:15],a[44:47]
RD a[220:223],v227 offset:36864
CV v78,v108,v109
MM a[36:39],a[156:159],v[4:7],a[36:39]
EX v110,v110
MM a[36:39],a[152:155],v[0:3],a[36:39]
RD a[224:227],v224 offset:40960
RD a[228:231],v225 offset:40960
MM a[64:67],a[160:163],v[0:3],a[64:67]
EX v111,v111
MM a[64:67],a[164:167],v[4:7],a[64:67]
RD a[232:235],v226 offset:40960
CV v79,v110,v111
MM a[72:75],a[164:167],v[12:15],a[72:75]
EX v112,v112
MM a[72:75],a[160:163],v[8:11],a[72:75]
buffer_load_dwordx4 v235, s[12:15], s81 offen offset:0 lds
MM a[80:83],a[160:163],v[16:19],a[80:83]
EX v113,v113
MM a[80:83],a[164:167],v[20:23],a[80:83]
RD a[236:239],v227 offset:40960
CV v84,v112,v113
MM a[88:91],a[164:167],v[28:31],a[88:91]
EX v114,v114
MM a[88:91],a[160:163],v[24:27],a[88:91]
EX v115,v115
MM a[92:95],a[168:171],v[24:27],a[92:95]
RD a[240:243],v224 offset:45056
CV v85,v114,v115
MM a[92:95],a[172:175],v[28:31],a[92:95]
EX v116,v116
MM a[84:87],a[172:175],v[20:23],a[84:87]
buffer_load_dwordx4 v235, s[12:15], s64 offen offset:1024 lds
MM a[84:87],a[168:171],v[16:19],a[84:87]
EX v117,v117
MM a[76:79],a[168:171],v[8:11],a[76:79]
RD a[244:247],v225 offset:45056
CV v86,v116,v117
MM a[76:79],a[172:175],v[12:15],a[76:79]
EX v118,v118
MM a[68:71],a[172:175],v[4:7],a[68:71]
EX v119,v119
MM a[68:71],a[168:171],v[0:3],a[68:71]
RD a[248:251],v226 offset:45056
CV v87,v118,v119
MM a[96:99],a[176:179],v[0:3],a[96:99]
EX v120,v120
MM a[96:99],a[180:183],v[4:7],a[96:99]
buffer_load_dwordx4 v235, s[12:15], s81 offen offset:2048 lds
)ASM"
R"ASM(
MM a[104:107],a[180:183],v[12:15],a[104:107]
EX v121,v121
MM a[104:107],a[176:179],v[8:11],a[104:107]
RD a[252:255],v227 offset:45056
CV v92,v120,v121
MM a[112:115],a[176:179],v[16:19],a[112:115]
EX v122,v122
MM a[112:115],a[180:183],v[20:23],a[112:115]
EX v123,v123
MM a[120:123],a[180:183],v[28:31],a[120:123]
s_add_u32 s87, s95, 0
buffer_store_dword v243, v248, s[20:23], s87 offen offset:12 sc1
MM a[120:123],a[176:179],v[24:27],a[120:123]
EX v124,v124
MM a[124:127],a[184:187],v[24:27],a[124:127]
v_add_f32 v244, v144, v145
v_add_f32 v245, v148, v149
MM a[124:127],a[188:191],v[28:31],a[124:127]
v_add_f32 v246, v152, v153
v_add_f32 v247, v156, v157
MM a[116:119],a[188:191],v[20:23],a[116:119]
EX v125,v125
MM a[116:119],a[184:187],v[16:19],a[116:119]
buffer_load_dwordx4 v235, s[12:15], s64 offen offset:3072 lds
MM a[108:111],a[184:187],v[8:11],a[108:111]
EX v126,v126
MM a[108:111],a[188:191],v[12:15],a[108:111]
EX v127,v127
MM a[100:103],a[188:191],v[4:7],a[100:103]
CV v93,v122,v123
CV v94,v124,v125
MM a[100:103],a[184:187],v[0:3],a[100:103]
CV v95,v126,v127
v_smfmac_f32_16x16x64_bf16 v[144:147], v[236:239], v[64:71], v234
v_smfmac_f32_16x16x64_bf16 v[148:151], v[236:239], v[72:79], v234
v_smfmac_f32_16x16x64_bf16 v[152:155], v[236:239], v[80:87], v234
v_smfmac_f32_16x16x64_bf16 v[156:159], v[236:239], v[88:95], v234
s_waitcnt lgkmcnt(0)
s_cmp_ge_u32 s16, 108
s_cbranch_scc1 LLAP_%=
MM v[0:3],a[192:195],v[160:163],v[128:131]
s_add_u32 s64, s64, 16384
TR a[128:129],v232 offset:16384
s_add_u32 s81, s64, 896
MM v[0:3],a[196:199],v[176:179],v[0:3]
TR a[130:131],v232 offset:20480
v_max3_f32 v249, v144, v145, v148
MM v[0:3],a[200:203],v[192:195],v[0:3]
TR a[132:133],v232 offset:24576
v_max3_f32 v249, v249, v149, v152
MM v[0:3],a[204:207],v[208:211],v[0:3]
TR a[134:135],v232 offset:28672
v_max3_f32 v249, v249, v153, v156
MM v[4:7],a[220:223],v[208:211],v[128:131]
TR a[136:137],v233 offset:16384
v_max_f32 v249, v249, v157
MM v[4:7],a[216:219],v[192:195],v[4:7]
TR a[138:139],v233 offset:20480
v_cmp_lt_f32 vcc, s71, v249
MM v[4:7],a[212:215],v[176:179],v[4:7]
TR a[140:141],v233 offset:24576
TR a[142:143],v233 offset:28672
MM v[4:7],a[208:211],v[160:163],v[4:7]
s_cbranch_vccnz LSITE13_%=
LBACK13_%=:
MM v[12:15],a[208:211],v[164:167],v[132:135]
v_lshrrev_b32 v127, 8, v248
v_lshlrev_b32 v124, 9, v127
MM v[12:15],a[212:215],v[180:183],v[12:15]
v_and_b32 v126, 192, v248
v_add_u32 v124, v124, v126
MM v[12:15],a[216:219],v[196:199],v[12:15]
v_bfe_u32 v126, v248, 4, 2
v_lshl_add_u32 v124, v126, 3, v124
MM v[12:15],a[220:223],v[212:215],v[12:15]
s_lshr_b32 s76, %[swave], 1
EX v0,v0
MM v[8:11],a[204:207],v[212:215],v[132:135]
s_mul_i32 s76, s76, 10240
EX v1,v1
MM v[8:11],a[200:203],v[196:199],v[8:11]
s_and_b32 s77, %[swave], 1
EX v2,v2
MM v[8:11],a[196:199],v[180:183],v[8:11]
s_lshl_b32 s77, s77, 5
TR a[144:145],v232 offset:16896
CV v0,v0,v1
MM v[8:11],a[192:195],v[164:167],v[8:11]
s_add_u32 s76, s76, s77
EX v3,v3
MM v[16:19],a[192:195],v[168:171],v[136:139]
s_add_u32 s76, s76, %[slds]
EX v4,v4
MM v[16:19],a[196:199],v[184:187],v[16:19]
s_add_u32 s76, s76, 131072
v_add_u32 v124, s76, v124
v_bfe_u32 v126, v248, 4, 3
MM v[16:19],a[200:203],v[200:203],v[16:19]
v_lshl_add_u32 v126, v127, 3, v126
v_lshlrev_b32 v126, 4, v126
MM v[16:19],a[204:207],v[216:219],v[16:19]
s_mul_i32 s76, %[swave], 3072
EX v5,v5
MM v[20:23],a[220:223],v[216:219],v[136:139]
s_add_u32 s76, s76, %[slds]
TR a[146:147],v232 offset:20992
CV v1,v2,v3
MM v[20:23],a[216:219],v[200:203],v[20:23]
s_add_u32 s76, s76, 151552
TR a[148:149],v232 offset:25088
v_add_u32 v125, s76, v126
MM v[20:23],a[212:215],v[184:187],v[20:23]
s_lshr_b32 s76, s48, 7
EX v6,v6
MM v[20:23],a[208:211],v[168:171],v[20:23]
s_lshl_b32 s76, s76, 5
EX v7,v7
MM v[28:31],a[208:211],v[172:175],v[140:143]
s_and_b32 s77, s48, 16
TR a[150:151],v232 offset:29184
TR a[152:153],v233 offset:16896
MM v[28:31],a[212:215],v[188:191],v[28:31]
s_add_u32 s76, s76, s77
EX v8,v8
MM v[28:31],a[216:219],v[204:207],v[28:31]
s_bfe_u32 s77, s48, 131077
TR a[154:155],v233 offset:20992
CV v2,v4,v5
MM v[28:31],a[220:223],v[220:223],v[28:31]
s_lshl_b32 s77, s77, 2
EX v9,v9
s_nop 0
LPG12_%=:
s_cbranch_execz LTR0_%=
MM v[24:27],a[204:207],v[220:223],v[140:143]
s_add_u32 s76, s76, s77
TR a[156:157],v233 offset:25088
CV v3,v6,v7
MM v[24:27],a[200:203],v[204:207],v[24:27]
s_add_u32 s76, s76, %[swave]
EX v10,v10
MM v[24:27],a[196:199],v[188:191],v[24:27]
s_lshl_b32 s97, s76, 2
TR a[158:159],v233 offset:29184
CV v8,v8,v9
MM v[24:27],a[192:195],v[172:175],v[24:27]
s_add_u32 s97, s97, 77824
EX v11,v11
MM v[32:35],a[224:227],v[160:163],v[128:131]
s_lshl_b32 s76, s76, 9
TR a[160:161],v232 offset:17408
TR a[162:163],v232 offset:21504
MM v[32:35],a[228:231],v[176:179],v[32:35]
s_add_u32 s76, s76, 22183936
EX v12,v12
MM v[32:35],a[232:235],v[192:195],v[32:35]
EX v13,v13
MM v[32:35],a[236:239],v[208:211],v[32:35]
TR a[164:165],v232 offset:25600
CV v9,v10,v11
MM v[36:39],a[252:255],v[208:211],v[128:131]
EX v14,v14
MM v[36:39],a[248:251],v[192:195],v[36:39]
TR a[166:167],v232 offset:29696
CV v10,v12,v13
MM v[36:39],a[244:247],v[176:179],v[36:39]
EX v15,v15
MM v[36:39],a[240:243],v[160:163],v[36:39]
TR a[168:169],v233 offset:17408
TR a[170:171],v233 offset:21504
MM v[44:47],a[240:243],v[164:167],v[132:135]
EX v16,v16
MM v[44:47],a[244:247],v[180:183],v[44:47]
EX v17,v17
MM v[44:47],a[248:251],v[196:199],v[44:47]
TR a[172:173],v233 offset:25600
CV v11,v14,v15
MM v[44:47],a[252:255],v[212:215],v[44:47]
EX v18,v18
MM v[40:43],a[236:239],v[212:215],v[132:135]
TR a[174:175],v233 offset:29696
CV v16,v16,v17
MM v[40:43],a[232:235],v[196:199],v[40:43]
EX v19,v19
MM v[40:43],a[228:231],v[180:183],v[40:43]
TR a[176:177],v232 offset:17920
TR a[178:179],v232 offset:22016
MM v[40:43],a[224:227],v[164:167],v[40:43]
EX v20,v20
MM v[48:51],a[224:227],v[168:171],v[136:139]
EX v21,v21
MM v[48:51],a[228:231],v[184:187],v[48:51]
TR a[180:181],v232 offset:26112
CV v17,v18,v19
MM v[48:51],a[232:235],v[200:203],v[48:51]
EX v22,v22
MM v[48:51],a[236:239],v[216:219],v[48:51]
TR a[182:183],v232 offset:30208
CV v18,v20,v21
MM v[52:55],a[252:255],v[216:219],v[136:139]
EX v23,v23
MM v[52:55],a[248:251],v[200:203],v[52:55]
TR a[184:185],v233 offset:17920
TR a[186:187],v233 offset:22016
MM v[52:55],a[244:247],v[184:187],v[52:55]
EX v24,v24
MM v[52:55],a[240:243],v[168:171],v[52:55]
TR a[188:189],v233 offset:26112
CV v19,v22,v23
MM v[60:63],a[240:243],v[172:175],v[140:143]
EX v25,v25
MM v[60:63],a[244:247],v[188:191],v[60:63]
TR a[190:191],v233 offset:30208
CV v24,v24,v25
MM v[60:63],a[248:251],v[204:207],v[60:63]
EX v26,v26
MM v[60:63],a[252:255],v[220:223],v[60:63]
EX v27,v27
MM v[56:59],a[236:239],v[220:223],v[140:143]
EX v28,v28
MM v[56:59],a[232:235],v[204:207],v[56:59]
EX v29,v29
MM v[56:59],a[228:231],v[188:191],v[56:59]
s_getpc_b64 s[98:99]
LTA2_%=:
s_min_u32 s73, %[swave], 3
s_lshl_b32 s73, s73, 12
s_add_u32 s73, s73, LPG13_%=-LTA2_%=
s_add_u32 s98, s98, s73
s_addc_u32 s99, s99, 0
s_mov_b64 exec, 0
s_setpc_b64 s[98:99]
LTR2_%=:
s_mov_b64 exec, -1
CV v25,v26,v27
MM v[56:59],a[224:227],v[172:175],v[56:59]
s_waitcnt vmcnt(17) lgkmcnt(0)
s_barrier
MM a[0:3],a[128:131],v[64:67],a[0:3]
s_add_u32 m0, s65, 16384
RD a[192:195],v224 offset:49152
CV v26,v28,v29
MM a[0:3],a[132:135],v[68:71],a[0:3]
EX v30,v30
MM a[8:11],a[132:135],v[76:79],a[8:11]
EX v31,v31
MM a[8:11],a[128:131],v[72:75],a[8:11]
RD a[196:199],v225 offset:49152
CV v27,v30,v31
MM a[16:19],a[128:131],v[80:83],a[16:19]
s_waitcnt vmcnt(10)
TR v[96:97],v124 offset:0
TR v[98:99],v124 offset:256
MM a[16:19],a[132:135],v[84:87],a[16:19]
RD v[108:111],v125 offset:0
TR v[100:101],v124 offset:2048
MM a[24:27],a[132:135],v[92:95],a[24:27]
TR v[102:103],v124 offset:2304
RD v[112:115],v125 offset:512
MM a[24:27],a[128:131],v[88:91],a[24:27]
TR v[104:105],v124 offset:4096
TR v[106:107],v124 offset:4352
MM a[28:31],a[136:139],v[88:91],a[28:31]
RD v[116:119],v125 offset:1024
RD a[200:203],v226 offset:49152
EX v32,v32
MM a[28:31],a[140:143],v[92:95],a[28:31]
EX v33,v33
MM a[20:23],a[140:143],v[84:87],a[20:23]
EX v34,v34
MM a[20:23],a[136:139],v[80:83],a[20:23]
buffer_load_dwordx4 v228, s[8:11], s64 offen offset:0 lds
MM a[12:15],a[136:139],v[72:75],a[12:15]
EX v35,v35
MM a[12:15],a[140:143],v[76:79],a[12:15]
EX v36,v36
MM a[4:7],a[140:143],v[68:71],a[4:7]
CV v4,v32,v33
CV v5,v34,v35
MM a[4:7],a[136:139],v[64:67],a[4:7]
EX v37,v37
MM a[32:35],a[144:147],v[64:67],a[32:35]
EX v38,v38
MM a[32:35],a[148:151],v[68:71],a[32:35]
s_waitcnt lgkmcnt(1)
buffer_load_dwordx4 v229, s[8:11], s64 offen offset:1024 lds
MM a[40:43],a[148:151],v[76:79],a[40:43]
EX v39,v39
MM v[120:123],v[96:99],v[108:111],0
MM v[120:123],v[100:103],v[112:115],v[120:123]
MM v[120:123],v[104:107],v[116:119],v[120:123]
MM a[40:43],a[144:147],v[72:75],a[40:43]
TR v[96:97],v124 offset:6144
TR v[98:99],v124 offset:6400
MM a[48:51],a[144:147],v[80:83],a[48:51]
RD v[108:111],v125 offset:1536
TR v[100:101],v124 offset:8192
MM a[48:51],a[148:151],v[84:87],a[48:51]
TR v[102:103],v124 offset:8448
RD v[112:115],v125 offset:2048
MM a[56:59],a[148:151],v[92:95],a[56:59]
EX v40,v40
MM a[56:59],a[144:147],v[88:91],a[56:59]
EX v41,v41
MM a[60:63],a[152:155],v[88:91],a[60:63]
RD a[204:207],v227 offset:49152
CV v6,v36,v37
MM a[60:63],a[156:159],v[92:95],a[60:63]
EX v42,v42
MM a[52:55],a[156:159],v[84:87],a[52:55]
RD a[208:211],v224 offset:53248
CV v7,v38,v39
MM a[52:55],a[152:155],v[80:83],a[52:55]
EX v43,v43
MM a[44:47],a[152:155],v[72:75],a[44:47]
buffer_load_dwordx4 v230, s[8:11], s64 offen offset:2048 lds
MM a[44:47],a[156:159],v[76:79],a[44:47]
EX v44,v44
MM a[36:39],a[156:159],v[68:71],a[36:39]
s_waitcnt lgkmcnt(2)
EX v45,v45
MM a[36:39],a[152:155],v[64:67],a[36:39]
RD a[212:215],v225 offset:53248
CV v12,v40,v41
MM v[120:123],v[96:99],v[108:111],v[120:123]
MM v[120:123],v[100:103],v[112:115],v[120:123]
MM a[64:67],a[160:163],v[64:67],a[64:67]
EX v46,v46
MM a[64:67],a[164:167],v[68:71],a[64:67]
RD a[216:219],v226 offset:53248
CV v13,v42,v43
MM a[72:75],a[164:167],v[76:79],a[72:75]
EX v47,v47
MM a[72:75],a[160:163],v[72:75],a[72:75]
buffer_load_dwordx4 v231, s[8:11], s64 offen offset:3072 lds
MM a[80:83],a[160:163],v[80:83],a[80:83]
s_add_u32 m0, s65, 81920
EX v48,v48
MM a[80:83],a[164:167],v[84:87],a[80:83]
RD a[220:223],v227 offset:53248
CV v14,v44,v45
MM a[88:91],a[164:167],v[92:95],a[88:91]
s_mov_b32 exec_lo, 0x00ff00ff
s_mov_b32 exec_hi, 0x00ff00ff
buffer_store_dwordx4 v[120:123], v126, s[20:23], s76 offen offset:0 sc1
s_mov_b64 exec, -1
s_nop 0
MM a[88:91],a[160:163],v[88:91],a[88:91]
EX v49,v49
MM a[92:95],a[168:171],v[88:91],a[92:95]
EX v50,v50
MM a[92:95],a[172:175],v[92:95],a[92:95]
RD a[224:227],v224 offset:57344
CV v15,v46,v47
MM a[84:87],a[172:175],v[84:87],a[84:87]
EX v51,v51
MM a[84:87],a[168:171],v[80:83],a[84:87]
EX v52,v52
MM a[76:79],a[168:171],v[72:75],a[76:79]
buffer_load_dwordx4 v235, s[12:15], s81 offen offset:0 lds
MM a[76:79],a[172:175],v[76:79],a[76:79]
EX v53,v53
MM a[68:71],a[172:175],v[68:71],a[68:71]
RD a[228:231],v225 offset:57344
CV v20,v48,v49
MM a[68:71],a[168:171],v[64:67],a[68:71]
EX v54,v54
MM a[96:99],a[176:179],v[64:67],a[96:99]
RD a[232:235],v226 offset:57344
CV v21,v50,v51
MM a[96:99],a[180:183],v[68:71],a[96:99]
EX v55,v55
MM a[104:107],a[180:183],v[76:79],a[104:107]
RD a[236:239],v227 offset:57344
CV v22,v52,v53
MM a[104:107],a[176:179],v[72:75],a[104:107]
EX v56,v56
MM a[112:115],a[176:179],v[80:83],a[112:115]
buffer_load_dwordx4 v235, s[12:15], s64 offen offset:1024 lds
MM a[112:115],a[180:183],v[84:87],a[112:115]
EX v57,v57
MM a[120:123],a[180:183],v[92:95],a[120:123]
EX v58,v58
MM a[120:123],a[176:179],v[88:91],a[120:123]
RD a[240:243],v224 offset:61440
CV v23,v54,v55
MM a[124:127],a[184:187],v[88:91],a[124:127]
v_add_f32 v244, v144, v145
v_add_f32 v245, v148, v149
MM a[124:127],a[188:191],v[92:95],a[124:127]
v_add_f32 v246, v152, v153
v_add_f32 v247, v156, v157
MM a[116:119],a[188:191],v[84:87],a[116:119]
EX v59,v59
MM a[116:119],a[184:187],v[80:83],a[116:119]
EX v60,v60
MM a[108:111],a[184:187],v[72:75],a[108:111]
)ASM"
R"ASM(
EX v61,v61
MM a[108:111],a[188:191],v[76:79],a[108:111]
RD a[244:247],v225 offset:61440
CV v28,v56,v57
MM a[100:103],a[188:191],v[68:71],a[100:103]
buffer_load_dwordx4 v235, s[12:15], s81 offen offset:2048 lds
MM a[100:103],a[184:187],v[64:67],a[100:103]
EX v62,v62
v_smfmac_f32_16x16x64_bf16 v[144:147], v[236:239], v[0:7], v234
RD a[248:251],v226 offset:61440
RD a[252:255],v227 offset:61440
EX v63,v63
v_smfmac_f32_16x16x64_bf16 v[148:151], v[236:239], v[8:15], v234
CV v29,v58,v59
v_smfmac_f32_16x16x64_bf16 v[152:155], v[236:239], v[16:23], v234
CV v30,v60,v61
CV v31,v62,v63
s_nop 1
v_smfmac_f32_16x16x64_bf16 v[156:159], v[236:239], v[24:31], v234
s_waitcnt lgkmcnt(0)
buffer_load_dwordx4 v235, s[12:15], s64 offen offset:3072 lds
MM v[64:67],a[192:195],v[160:163],v[128:131]
s_add_u32 s64, s64, 16384
TR a[128:129],v232 offset:32768
s_add_u32 s81, s64, 896
MM v[64:67],a[196:199],v[176:179],v[64:67]
TR a[130:131],v232 offset:36864
v_max3_f32 v249, v144, v145, v148
MM v[64:67],a[200:203],v[192:195],v[64:67]
TR a[132:133],v232 offset:40960
v_max3_f32 v249, v249, v149, v152
MM v[64:67],a[204:207],v[208:211],v[64:67]
TR a[134:135],v232 offset:45056
v_max3_f32 v249, v249, v153, v156
MM v[68:71],a[220:223],v[208:211],v[128:131]
TR a[136:137],v233 offset:32768
v_max_f32 v249, v249, v157
MM v[68:71],a[216:219],v[192:195],v[68:71]
TR a[138:139],v233 offset:36864
v_cmp_lt_f32 vcc, s71, v249
MM v[68:71],a[212:215],v[176:179],v[68:71]
TR a[140:141],v233 offset:40960
TR a[142:143],v233 offset:45056
MM v[68:71],a[208:211],v[160:163],v[68:71]
s_cbranch_vccnz LSITE14_%=
LBACK14_%=:
MM v[76:79],a[208:211],v[164:167],v[132:135]
s_add_u32 s48, s48, 16
v_lshrrev_b32 v252, 6, v248
v_mul_u32_u24 v252, 3456, v252
MM v[76:79],a[212:215],v[180:183],v[76:79]
v_and_b32 v251, 48, v248
v_add_u32 v252, v252, v251
MM v[76:79],a[216:219],v[196:199],v[76:79]
s_and_b32 s90, %[swave], 1
EX v64,v64
MM v[76:79],a[220:223],v[212:215],v[76:79]
s_mul_i32 s90, s90, 276480
EX v65,v65
MM v[72:75],a[204:207],v[212:215],v[132:135]
s_bfe_u32 s91, s48, 131077
EX v66,v66
MM v[72:75],a[200:203],v[196:199],v[72:75]
s_mul_i32 s91, s91, 552960
TR a[144:145],v232 offset:33280
CV v64,v64,v65
MM v[72:75],a[196:199],v[180:183],v[72:75]
s_add_u32 s90, s90, s91
EX v67,v67
MM v[72:75],a[192:195],v[164:167],v[72:75]
s_and_b32 s91, s48, 0xffffff80
TR a[146:147],v232 offset:37376
CV v65,v66,v67
MM v[80:83],a[192:195],v[168:171],v[136:139]
s_add_u32 s90, s90, s91
EX v68,v68
MM v[80:83],a[196:199],v[184:187],v[80:83]
s_and_b32 s91, s48, 16
TR a[148:149],v232 offset:41472
TR a[150:151],v232 offset:45568
MM v[80:83],a[200:203],v[200:203],v[80:83]
s_lshl_b32 s91, s91, 2
EX v69,v69
MM v[80:83],a[204:207],v[216:219],v[80:83]
s_add_u32 s66, s90, s91
TR a[152:153],v233 offset:33280
CV v66,v68,v69
MM v[84:87],a[220:223],v[216:219],v[136:139]
s_mul_i32 s91, %[swave], 5120
EX v70,v70
MM v[84:87],a[216:219],v[200:203],v[84:87]
s_add_u32 s91, s91, %[slds]
EX v71,v71
MM v[84:87],a[212:215],v[184:187],v[84:87]
s_add_u32 s67, s91, 131072
TR a[154:155],v233 offset:37376
CV v67,v70,v71
MM v[84:87],a[208:211],v[168:171],v[84:87]
s_and_b32 s18, s17, 255
EX v72,v72
MM v[92:95],a[208:211],v[172:175],v[140:143]
s_sub_u32 s90, s18, 40
s_cmp_ge_u32 s18, 216
s_cselect_b32 s18, s90, s18
MM v[92:95],a[212:215],v[188:191],v[92:95]
s_lshl_b32 s18, s18, 4
EX v73,v73
MM v[92:95],a[216:219],v[204:207],v[92:95]
s_sub_u32 s90, s16, 90
TR a[156:157],v233 offset:41472
CV v72,v72,v73
MM v[92:95],a[220:223],v[220:223],v[92:95]
s_min_u32 s47, s90, s16
EX v74,v74
MM v[88:91],a[204:207],v[220:223],v[140:143]
s_mul_i32 s90, s47, 205
TR a[158:159],v233 offset:45568
TR a[160:161],v232 offset:33792
MM v[88:91],a[200:203],v[204:207],v[88:91]
s_lshr_b32 s46, s90, 11
EX v75,v75
MM v[88:91],a[196:199],v[188:191],v[88:91]
s_mul_i32 s90, s46, 244480
TR a[162:163],v232 offset:37888
CV v73,v74,v75
MM v[88:91],a[192:195],v[172:175],v[88:91]
s_lshl_b32 s91, s47, 7
EX v76,v76
MM v[96:99],a[224:227],v[160:163],v[128:131]
s_add_u32 s90, s90, s91
s_add_u32 s28, s28, s90
s_addc_u32 s29, s29, 0
MM v[96:99],a[228:231],v[176:179],v[96:99]
s_sub_u32 s30, 2211840, s90
EX v77,v77
MM v[96:99],a[232:235],v[192:195],v[96:99]
s_mov_b32 s31, 131072
TR a[164:165],v232 offset:41984
CV v74,v76,v77
MM v[96:99],a[236:239],v[208:211],v[96:99]
EX v78,v78
MM v[100:103],a[252:255],v[208:211],v[128:131]
TR a[166:167],v232 offset:46080
TR a[168:169],v233 offset:33792
MM v[100:103],a[248:251],v[192:195],v[100:103]
EX v79,v79
MM v[100:103],a[244:247],v[176:179],v[100:103]
TR a[170:171],v233 offset:37888
CV v75,v78,v79
MM v[100:103],a[240:243],v[160:163],v[100:103]
EX v80,v80
MM v[108:111],a[240:243],v[164:167],v[132:135]
TR a[172:173],v233 offset:41984
TR a[174:175],v233 offset:46080
MM v[108:111],a[244:247],v[180:183],v[108:111]
EX v81,v81
MM v[108:111],a[248:251],v[196:199],v[108:111]
EX v82,v82
MM v[108:111],a[252:255],v[212:215],v[108:111]
TR a[176:177],v232 offset:34304
CV v80,v80,v81
MM v[104:107],a[236:239],v[212:215],v[132:135]
EX v83,v83
MM v[104:107],a[232:235],v[196:199],v[104:107]
TR a[178:179],v232 offset:38400
CV v81,v82,v83
MM v[104:107],a[228:231],v[180:183],v[104:107]
EX v84,v84
MM v[104:107],a[224:227],v[164:167],v[104:107]
TR a[180:181],v232 offset:42496
TR a[182:183],v232 offset:46592
MM v[112:115],a[224:227],v[168:171],v[136:139]
EX v85,v85
MM v[112:115],a[228:231],v[184:187],v[112:115]
TR a[184:185],v233 offset:34304
CV v82,v84,v85
MM v[112:115],a[232:235],v[200:203],v[112:115]
EX v86,v86
MM v[112:115],a[236:239],v[216:219],v[112:115]
EX v87,v87
MM v[116:119],a[252:255],v[216:219],v[136:139]
TR a[186:187],v233 offset:38400
CV v83,v86,v87
MM v[116:119],a[248:251],v[200:203],v[116:119]
EX v88,v88
MM v[116:119],a[244:247],v[184:187],v[116:119]
TR a[188:189],v233 offset:42496
TR a[190:191],v233 offset:46592
MM v[116:119],a[240:243],v[168:171],v[116:119]
EX v89,v89
MM v[124:127],a[240:243],v[172:175],v[140:143]
EX v90,v90
MM v[124:127],a[244:247],v[188:191],v[124:127]
EX v91,v91
MM v[124:127],a[248:251],v[204:207],v[124:127]
EX v92,v92
MM v[124:127],a[252:255],v[220:223],v[124:127]
EX v93,v93
MM v[120:123],a[236:239],v[220:223],v[140:143]
EX v94,v94
MM v[120:123],a[232:235],v[204:207],v[120:123]
EX v95,v95
MM v[120:123],a[228:231],v[188:191],v[120:123]
EX v96,v96
MM v[120:123],a[224:227],v[172:175],v[120:123]
s_waitcnt vmcnt(14) lgkmcnt(0)
s_barrier
MM a[0:3],a[128:131],v[0:3],a[0:3]
s_add_u32 m0, s65, 32768
CV v88,v88,v89
CV v89,v90,v91
MM a[0:3],a[132:135],v[4:7],a[0:3]
RD a[192:195],v224 offset:0
CV v90,v92,v93
MM a[8:11],a[132:135],v[12:15],a[8:11]
RD a[196:199],v225 offset:0
CV v91,v94,v95
MM a[8:11],a[128:131],v[8:11],a[8:11]
buffer_load_dwordx4 v228, s[8:11], s64 offen offset:0 lds
MM a[16:19],a[128:131],v[16:19],a[16:19]
RD a[200:203],v226 offset:0
RD a[204:207],v227 offset:0
MM a[16:19],a[132:135],v[20:23],a[16:19]
EX v97,v97
MM a[24:27],a[132:135],v[28:31],a[24:27]
RD a[208:211],v224 offset:4096
CV v68,v96,v97
MM a[24:27],a[128:131],v[24:27],a[24:27]
EX v98,v98
MM a[28:31],a[136:139],v[24:27],a[28:31]
buffer_load_dwordx4 v229, s[8:11], s64 offen offset:1024 lds
MM a[28:31],a[140:143],v[28:31],a[28:31]
EX v99,v99
MM a[20:23],a[140:143],v[20:23],a[20:23]
RD a[212:215],v225 offset:4096
CV v69,v98,v99
MM a[20:23],a[136:139],v[16:19],a[20:23]
EX v100,v100
MM a[12:15],a[136:139],v[8:11],a[12:15]
EX v101,v101
MM a[12:15],a[140:143],v[12:15],a[12:15]
RD a[216:219],v226 offset:4096
CV v70,v100,v101
MM a[4:7],a[140:143],v[4:7],a[4:7]
EX v102,v102
MM a[4:7],a[136:139],v[0:3],a[4:7]
buffer_load_dwordx4 v230, s[8:11], s64 offen offset:2048 lds
MM a[32:35],a[144:147],v[0:3],a[32:35]
EX v103,v103
MM a[32:35],a[148:151],v[4:7],a[32:35]
RD a[220:223],v227 offset:4096
CV v71,v102,v103
MM a[40:43],a[148:151],v[12:15],a[40:43]
EX v104,v104
MM a[40:43],a[144:147],v[8:11],a[40:43]
EX v105,v105
MM a[48:51],a[144:147],v[16:19],a[48:51]
RD a[224:227],v224 offset:8192
CV v76,v104,v105
MM a[48:51],a[148:151],v[20:23],a[48:51]
EX v106,v106
MM a[56:59],a[148:151],v[28:31],a[56:59]
buffer_load_dwordx4 v231, s[8:11], s64 offen offset:3072 lds
MM a[56:59],a[144:147],v[24:27],a[56:59]
s_add_u32 m0, s65, 98304
EX v107,v107
MM a[60:63],a[152:155],v[24:27],a[60:63]
RD a[228:231],v225 offset:8192
CV v77,v106,v107
MM a[60:63],a[156:159],v[28:31],a[60:63]
EX v108,v108
MM a[52:55],a[156:159],v[20:23],a[52:55]
EX v109,v109
MM a[52:55],a[152:155],v[16:19],a[52:55]
RD a[232:235],v226 offset:8192
CV v78,v108,v109
MM a[44:47],a[152:155],v[8:11],a[44:47]
EX v110,v110
MM a[44:47],a[156:159],v[12:15],a[44:47]
buffer_load_dwordx4 v235, s[12:15], s81 offen offset:0 lds
MM a[36:39],a[156:159],v[4:7],a[36:39]
EX v111,v111
MM a[36:39],a[152:155],v[0:3],a[36:39]
RD a[236:239],v227 offset:8192
CV v79,v110,v111
MM a[64:67],a[160:163],v[0:3],a[64:67]
EX v112,v112
MM a[64:67],a[164:167],v[4:7],a[64:67]
EX v113,v113
MM a[72:75],a[164:167],v[12:15],a[72:75]
RD a[240:243],v224 offset:12288
CV v84,v112,v113
MM a[72:75],a[160:163],v[8:11],a[72:75]
buffer_load_dwordx4 v235, s[12:15], s64 offen offset:1024 lds
MM a[80:83],a[160:163],v[16:19],a[80:83]
s_waitcnt vmcnt(10)
v_mov_b32 v250, s69
s_mov_b64 exec, 1
buffer_store_dword v250, off, s[20:23], s97 offset:0 sc1
s_mov_b64 exec, -1
MM a[80:83],a[164:167],v[20:23],a[80:83]
s_add_u32 m0, s67, 0
s_add_u32 s85, s66, 0
buffer_load_dwordx4 v252, s[36:39], s85 offen offset:0 lds
MM a[88:91],a[164:167],v[28:31],a[88:91]
EX v114,v114
MM a[88:91],a[160:163],v[24:27],a[88:91]
EX v115,v115
MM a[92:95],a[168:171],v[24:27],a[92:95]
RD a[244:247],v225 offset:12288
CV v85,v114,v115
MM a[92:95],a[172:175],v[28:31],a[92:95]
EX v116,v116
MM a[84:87],a[172:175],v[20:23],a[84:87]
EX v117,v117
MM a[84:87],a[168:171],v[16:19],a[84:87]
RD a[248:251],v226 offset:12288
CV v86,v116,v117
MM a[76:79],a[168:171],v[8:11],a[76:79]
EX v118,v118
MM a[76:79],a[172:175],v[12:15],a[76:79]
EX v119,v119
MM a[68:71],a[172:175],v[4:7],a[68:71]
RD a[252:255],v227 offset:12288
CV v87,v118,v119
MM a[68:71],a[168:171],v[0:3],a[68:71]
s_add_u32 m0, s65, 98304
s_nop 0
buffer_load_dwordx4 v235, s[12:15], s81 offen offset:2048 lds
MM a[96:99],a[176:179],v[0:3],a[96:99]
EX v120,v120
MM a[96:99],a[180:183],v[4:7],a[96:99]
EX v121,v121
MM a[104:107],a[180:183],v[12:15],a[104:107]
EX v122,v122
MM a[104:107],a[176:179],v[8:11],a[104:107]
s_nop 0
LPG13_%=:
s_cbranch_execz LTR2_%=
buffer_load_dwordx4 v235, s[12:15], s64 offen offset:3072 lds
MM a[112:115],a[176:179],v[16:19],a[112:115]
EX v123,v123
MM a[112:115],a[180:183],v[20:23],a[112:115]
EX v124,v124
MM a[120:123],a[180:183],v[28:31],a[120:123]
EX v125,v125
MM a[120:123],a[176:179],v[24:27],a[120:123]
EX v126,v126
MM a[124:127],a[184:187],v[24:27],a[124:127]
v_add_f32 v244, v144, v145
v_add_f32 v245, v148, v149
MM a[124:127],a[188:191],v[28:31],a[124:127]
v_add_f32 v246, v152, v153
v_add_f32 v247, v156, v157
MM a[116:119],a[188:191],v[20:23],a[116:119]
EX v127,v127
MM a[116:119],a[184:187],v[16:19],a[116:119]
CV v92,v120,v121
CV v93,v122,v123
MM a[108:111],a[184:187],v[8:11],a[108:111]
CV v94,v124,v125
CV v95,v126,v127
MM a[108:111],a[188:191],v[12:15],a[108:111]
MM a[100:103],a[188:191],v[4:7],a[100:103]
MM a[100:103],a[184:187],v[0:3],a[100:103]
v_smfmac_f32_16x16x64_bf16 v[144:147], v[236:239], v[64:71], v234
v_smfmac_f32_16x16x64_bf16 v[148:151], v[236:239], v[72:79], v234
v_smfmac_f32_16x16x64_bf16 v[152:155], v[236:239], v[80:87], v234
v_smfmac_f32_16x16x64_bf16 v[156:159], v[236:239], v[88:95], v234
s_waitcnt lgkmcnt(0)
MM v[0:3],a[192:195],v[160:163],v[128:131]
s_add_u32 s64, s64, 16384
TR a[128:129],v232 offset:49152
s_add_u32 s81, s64, 896
MM v[0:3],a[196:199],v[176:179],v[0:3]
TR a[130:131],v232 offset:53248
v_max3_f32 v249, v144, v145, v148
MM v[0:3],a[200:203],v[192:195],v[0:3]
TR a[132:133],v232 offset:57344
v_max3_f32 v249, v249, v149, v152
MM v[0:3],a[204:207],v[208:211],v[0:3]
TR a[134:135],v232 offset:61440
v_max3_f32 v249, v249, v153, v156
MM v[4:7],a[220:223],v[208:211],v[128:131]
TR a[136:137],v233 offset:49152
v_max_f32 v249, v249, v157
MM v[4:7],a[216:219],v[192:195],v[4:7]
TR a[138:139],v233 offset:53248
)ASM"
R"ASM(
v_cmp_lt_f32 vcc, s71, v249
MM v[4:7],a[212:215],v[176:179],v[4:7]
TR a[140:141],v233 offset:57344
TR a[142:143],v233 offset:61440
MM v[4:7],a[208:211],v[160:163],v[4:7]
s_cbranch_vccnz LSITE15_%=
LBACK15_%=:
MM v[12:15],a[208:211],v[164:167],v[132:135]
s_add_u32 m0, s67, 1024
s_add_u32 s85, s66, 55296
buffer_load_dwordx4 v252, s[36:39], s85 offen offset:0 lds
MM v[12:15],a[212:215],v[180:183],v[12:15]
EX v0,v0
MM v[12:15],a[216:219],v[196:199],v[12:15]
EX v1,v1
MM v[12:15],a[220:223],v[212:215],v[12:15]
TR a[144:145],v232 offset:49664
CV v0,v0,v1
MM v[8:11],a[204:207],v[212:215],v[132:135]
EX v2,v2
MM v[8:11],a[200:203],v[196:199],v[8:11]
EX v3,v3
MM v[8:11],a[196:199],v[180:183],v[8:11]
TR a[146:147],v232 offset:53760
CV v1,v2,v3
MM v[8:11],a[192:195],v[164:167],v[8:11]
EX v4,v4
MM v[16:19],a[192:195],v[168:171],v[136:139]
TR a[148:149],v232 offset:57856
TR a[150:151],v232 offset:61952
MM v[16:19],a[196:199],v[184:187],v[16:19]
EX v5,v5
MM v[16:19],a[200:203],v[200:203],v[16:19]
TR a[152:153],v233 offset:49664
CV v2,v4,v5
MM v[16:19],a[204:207],v[216:219],v[16:19]
EX v6,v6
MM v[20:23],a[220:223],v[216:219],v[136:139]
EX v7,v7
MM v[20:23],a[216:219],v[200:203],v[20:23]
TR a[154:155],v233 offset:53760
CV v3,v6,v7
MM v[20:23],a[212:215],v[184:187],v[20:23]
EX v8,v8
MM v[20:23],a[208:211],v[168:171],v[20:23]
TR a[156:157],v233 offset:57856
TR a[158:159],v233 offset:61952
MM v[28:31],a[208:211],v[172:175],v[140:143]
EX v9,v9
MM v[28:31],a[212:215],v[188:191],v[28:31]
TR a[160:161],v232 offset:50176
CV v8,v8,v9
MM v[28:31],a[216:219],v[204:207],v[28:31]
EX v10,v10
MM v[28:31],a[220:223],v[220:223],v[28:31]
EX v11,v11
MM v[24:27],a[204:207],v[220:223],v[140:143]
TR a[162:163],v232 offset:54272
CV v9,v10,v11
MM v[24:27],a[200:203],v[204:207],v[24:27]
TR a[164:165],v232 offset:58368
TR a[166:167],v232 offset:62464
MM v[24:27],a[196:199],v[188:191],v[24:27]
EX v12,v12
MM v[24:27],a[192:195],v[172:175],v[24:27]
EX v13,v13
MM v[32:35],a[224:227],v[160:163],v[128:131]
TR a[168:169],v233 offset:50176
CV v10,v12,v13
MM v[32:35],a[228:231],v[176:179],v[32:35]
EX v14,v14
MM v[32:35],a[232:235],v[192:195],v[32:35]
TR a[170:171],v233 offset:54272
TR a[172:173],v233 offset:58368
MM v[32:35],a[236:239],v[208:211],v[32:35]
EX v15,v15
MM v[36:39],a[252:255],v[208:211],v[128:131]
TR a[174:175],v233 offset:62464
CV v11,v14,v15
MM v[36:39],a[248:251],v[192:195],v[36:39]
EX v16,v16
MM v[36:39],a[244:247],v[176:179],v[36:39]
EX v17,v17
MM v[36:39],a[240:243],v[160:163],v[36:39]
TR a[176:177],v232 offset:50688
CV v16,v16,v17
MM v[44:47],a[240:243],v[164:167],v[132:135]
s_add_u32 m0, s67, 2048
s_add_u32 s85, s66, 110592
buffer_load_dwordx4 v252, s[36:39], s85 offen offset:0 lds
MM v[44:47],a[244:247],v[180:183],v[44:47]
EX v18,v18
MM v[44:47],a[248:251],v[196:199],v[44:47]
TR a[178:179],v232 offset:54784
TR a[180:181],v232 offset:58880
MM v[44:47],a[252:255],v[212:215],v[44:47]
EX v19,v19
MM v[40:43],a[236:239],v[212:215],v[132:135]
TR a[182:183],v232 offset:62976
CV v17,v18,v19
MM v[40:43],a[232:235],v[196:199],v[40:43]
EX v20,v20
MM v[40:43],a[228:231],v[180:183],v[40:43]
EX v21,v21
MM v[40:43],a[224:227],v[164:167],v[40:43]
TR a[184:185],v233 offset:50688
CV v18,v20,v21
MM v[48:51],a[224:227],v[168:171],v[136:139]
EX v22,v22
MM v[48:51],a[228:231],v[184:187],v[48:51]
TR a[186:187],v233 offset:54784
TR a[188:189],v233 offset:58880
MM v[48:51],a[232:235],v[200:203],v[48:51]
EX v23,v23
MM v[48:51],a[236:239],v[216:219],v[48:51]
TR a[190:191],v233 offset:62976
CV v19,v22,v23
MM v[52:55],a[252:255],v[216:219],v[136:139]
EX v24,v24
MM v[52:55],a[248:251],v[200:203],v[52:55]
EX v25,v25
MM v[52:55],a[244:247],v[184:187],v[52:55]
EX v26,v26
MM v[52:55],a[240:243],v[168:171],v[52:55]
EX v27,v27
MM v[60:63],a[240:243],v[172:175],v[140:143]
EX v28,v28
MM v[60:63],a[244:247],v[188:191],v[60:63]
EX v29,v29
MM v[60:63],a[248:251],v[204:207],v[60:63]
EX v30,v30
MM v[60:63],a[252:255],v[220:223],v[60:63]
EX v31,v31
MM v[56:59],a[236:239],v[220:223],v[140:143]
EX v32,v32
MM v[56:59],a[232:235],v[204:207],v[56:59]
EX v33,v33
MM v[56:59],a[228:231],v[188:191],v[56:59]
EX v34,v34
MM v[56:59],a[224:227],v[172:175],v[56:59]
s_waitcnt lgkmcnt(0)
s_barrier
MM a[0:3],a[128:131],v[64:67],a[0:3]
s_add_u32 m0, s65, 49152
CV v24,v24,v25
CV v25,v26,v27
MM a[0:3],a[132:135],v[68:71],a[0:3]
RD a[192:195],v224 offset:16384
CV v26,v28,v29
MM a[8:11],a[132:135],v[76:79],a[8:11]
RD a[196:199],v225 offset:16384
CV v27,v30,v31
MM a[8:11],a[128:131],v[72:75],a[8:11]
buffer_load_dwordx4 v228, s[8:11], s64 offen offset:0 lds
MM a[16:19],a[128:131],v[80:83],a[16:19]
s_add_u32 m0, s67, 3072
s_add_u32 s85, s66, 165888
buffer_load_dwordx4 v252, s[36:39], s85 offen offset:0 lds
MM a[16:19],a[132:135],v[84:87],a[16:19]
RD a[200:203],v226 offset:16384
CV v4,v32,v33
MM a[24:27],a[132:135],v[92:95],a[24:27]
RD a[204:207],v227 offset:16384
RD a[208:211],v224 offset:20480
MM a[24:27],a[128:131],v[88:91],a[24:27]
s_add_u32 m0, s65, 49152
s_nop 0
buffer_load_dwordx4 v229, s[8:11], s64 offen offset:1024 lds
MM a[28:31],a[136:139],v[88:91],a[28:31]
EX v35,v35
MM a[28:31],a[140:143],v[92:95],a[28:31]
RD a[212:215],v225 offset:20480
CV v5,v34,v35
MM a[20:23],a[140:143],v[84:87],a[20:23]
EX v36,v36
MM a[20:23],a[136:139],v[80:83],a[20:23]
EX v37,v37
MM a[12:15],a[136:139],v[72:75],a[12:15]
RD a[216:219],v226 offset:20480
CV v6,v36,v37
MM a[12:15],a[140:143],v[76:79],a[12:15]
buffer_load_dwordx4 v230, s[8:11], s64 offen offset:2048 lds
MM a[4:7],a[140:143],v[68:71],a[4:7]
EX v38,v38
MM a[4:7],a[136:139],v[64:67],a[4:7]
EX v39,v39
MM a[32:35],a[144:147],v[64:67],a[32:35]
RD a[220:223],v227 offset:20480
CV v7,v38,v39
MM a[32:35],a[148:151],v[68:71],a[32:35]
EX v40,v40
MM a[40:43],a[148:151],v[76:79],a[40:43]
EX v41,v41
MM a[40:43],a[144:147],v[72:75],a[40:43]
RD a[224:227],v224 offset:24576
CV v12,v40,v41
MM a[48:51],a[144:147],v[80:83],a[48:51]
buffer_load_dwordx4 v231, s[8:11], s64 offen offset:3072 lds
MM a[48:51],a[148:151],v[84:87],a[48:51]
s_add_u32 m0, s65, 114688
EX v42,v42
MM a[56:59],a[148:151],v[92:95],a[56:59]
EX v43,v43
MM a[56:59],a[144:147],v[88:91],a[56:59]
RD a[228:231],v225 offset:24576
CV v13,v42,v43
MM a[60:63],a[152:155],v[88:91],a[60:63]
EX v44,v44
MM a[60:63],a[156:159],v[92:95],a[60:63]
buffer_load_dwordx4 v235, s[12:15], s81 offen offset:0 lds
MM a[52:55],a[156:159],v[84:87],a[52:55]
EX v45,v45
MM a[52:55],a[152:155],v[80:83],a[52:55]
RD a[232:235],v226 offset:24576
CV v14,v44,v45
MM a[44:47],a[152:155],v[72:75],a[44:47]
EX v46,v46
MM a[44:47],a[156:159],v[76:79],a[44:47]
EX v47,v47
MM a[36:39],a[156:159],v[68:71],a[36:39]
RD a[236:239],v227 offset:24576
CV v15,v46,v47
MM a[36:39],a[152:155],v[64:67],a[36:39]
EX v48,v48
MM a[64:67],a[160:163],v[64:67],a[64:67]
buffer_load_dwordx4 v235, s[12:15], s64 offen offset:1024 lds
MM a[64:67],a[164:167],v[68:71],a[64:67]
EX v49,v49
MM a[72:75],a[164:167],v[76:79],a[72:75]
RD a[240:243],v224 offset:28672
CV v20,v48,v49
MM a[72:75],a[160:163],v[72:75],a[72:75]
EX v50,v50
MM a[80:83],a[160:163],v[80:83],a[80:83]
EX v51,v51
MM a[80:83],a[164:167],v[84:87],a[80:83]
RD a[244:247],v225 offset:28672
CV v21,v50,v51
MM a[88:91],a[164:167],v[92:95],a[88:91]
EX v52,v52
MM a[88:91],a[160:163],v[88:91],a[88:91]
buffer_load_dwordx4 v235, s[12:15], s81 offen offset:2048 lds
MM a[92:95],a[168:171],v[88:91],a[92:95]
EX v53,v53
MM a[92:95],a[172:175],v[92:95],a[92:95]
RD a[248:251],v226 offset:28672
CV v22,v52,v53
MM a[84:87],a[172:175],v[84:87],a[84:87]
EX v54,v54
MM a[84:87],a[168:171],v[80:83],a[84:87]
EX v55,v55
MM a[76:79],a[168:171],v[72:75],a[76:79]
RD a[252:255],v227 offset:28672
CV v23,v54,v55
MM a[76:79],a[172:175],v[76:79],a[76:79]
EX v56,v56
MM a[68:71],a[172:175],v[68:71],a[68:71]
buffer_load_dwordx4 v235, s[12:15], s64 offen offset:3072 lds
MM a[68:71],a[168:171],v[64:67],a[68:71]
EX v57,v57
MM a[96:99],a[176:179],v[64:67],a[96:99]
EX v58,v58
MM a[96:99],a[180:183],v[68:71],a[96:99]
EX v59,v59
MM a[104:107],a[180:183],v[76:79],a[104:107]
EX v60,v60
MM a[104:107],a[176:179],v[72:75],a[104:107]
EX v61,v61
MM a[112:115],a[176:179],v[80:83],a[112:115]
EX v62,v62
MM a[112:115],a[180:183],v[84:87],a[112:115]
EX v63,v63
MM a[120:123],a[180:183],v[92:95],a[120:123]
s_add_u32 m0, s67, 4096
s_add_u32 s85, s66, 221184
buffer_load_dwordx4 v252, s[36:39], s85 offen offset:0 lds
MM a[120:123],a[176:179],v[88:91],a[120:123]
CV v28,v56,v57
CV v29,v58,v59
MM a[124:127],a[184:187],v[88:91],a[124:127]
v_add_f32 v244, v144, v145
v_add_f32 v245, v148, v149
MM a[124:127],a[188:191],v[92:95],a[124:127]
v_add_f32 v246, v152, v153
v_add_f32 v247, v156, v157
MM a[116:119],a[188:191],v[84:87],a[116:119]
CV v30,v60,v61
CV v31,v62,v63
MM a[116:119],a[184:187],v[80:83],a[116:119]
MM a[108:111],a[184:187],v[72:75],a[108:111]
MM a[108:111],a[188:191],v[76:79],a[108:111]
MM a[100:103],a[188:191],v[68:71],a[100:103]
MM a[100:103],a[184:187],v[64:67],a[100:103]
v_smfmac_f32_16x16x64_bf16 v[144:147], v[236:239], v[0:7], v234
v_smfmac_f32_16x16x64_bf16 v[148:151], v[236:239], v[8:15], v234
v_smfmac_f32_16x16x64_bf16 v[152:155], v[236:239], v[16:23], v234
v_smfmac_f32_16x16x64_bf16 v[156:159], v[236:239], v[24:31], v234
s_waitcnt lgkmcnt(0)
MM v[64:67],a[192:195],v[160:163],v[128:131]
s_add_u32 s64, s64, 16384
TR a[128:129],v232 offset:0
s_add_u32 s81, s64, 896
MM v[64:67],a[196:199],v[176:179],v[64:67]
TR a[130:131],v232 offset:4096
v_max3_f32 v249, v144, v145, v148
MM v[64:67],a[200:203],v[192:195],v[64:67]
TR a[132:133],v232 offset:8192
v_max3_f32 v249, v249, v149, v152
MM v[64:67],a[204:207],v[208:211],v[64:67]
TR a[134:135],v232 offset:12288
v_max3_f32 v249, v249, v153, v156
MM v[68:71],a[220:223],v[208:211],v[128:131]
TR a[136:137],v233 offset:0
v_max_f32 v249, v249, v157
MM v[68:71],a[216:219],v[192:195],v[68:71]
TR a[138:139],v233 offset:4096
v_cmp_lt_f32 vcc, s71, v249
MM v[68:71],a[212:215],v[176:179],v[68:71]
TR a[140:141],v233 offset:8192
TR a[142:143],v233 offset:12288
MM v[68:71],a[208:211],v[160:163],v[68:71]
s_cbranch_vccnz LSITE16_%=
LBACK16_%=:
MM v[76:79],a[208:211],v[164:167],v[132:135]
EX v64,v64
MM v[76:79],a[212:215],v[180:183],v[76:79]
EX v65,v65
MM v[76:79],a[216:219],v[196:199],v[76:79]
TR a[144:145],v232 offset:512
CV v64,v64,v65
MM v[76:79],a[220:223],v[212:215],v[76:79]
EX v66,v66
MM v[72:75],a[204:207],v[212:215],v[132:135]
EX v67,v67
MM v[72:75],a[200:203],v[196:199],v[72:75]
TR a[146:147],v232 offset:4608
CV v65,v66,v67
MM v[72:75],a[196:199],v[180:183],v[72:75]
EX v68,v68
MM v[72:75],a[192:195],v[164:167],v[72:75]
TR a[148:149],v232 offset:8704
TR a[150:151],v232 offset:12800
MM v[80:83],a[192:195],v[168:171],v[136:139]
EX v69,v69
MM v[80:83],a[196:199],v[184:187],v[80:83]
TR a[152:153],v233 offset:512
CV v66,v68,v69
MM v[80:83],a[200:203],v[200:203],v[80:83]
EX v70,v70
MM v[80:83],a[204:207],v[216:219],v[80:83]
TR a[154:155],v233 offset:4608
TR a[156:157],v233 offset:8704
MM v[84:87],a[220:223],v[216:219],v[136:139]
s_add_u32 s87, s95, 1024
buffer_store_dword v243, v248, s[20:23], s87 offen offset:12 sc1
MM v[84:87],a[216:219],v[200:203],v[84:87]
EX v71,v71
MM v[84:87],a[212:215],v[184:187],v[84:87]
TR a[158:159],v233 offset:12800
CV v67,v70,v71
MM v[84:87],a[208:211],v[168:171],v[84:87]
EX v72,v72
MM v[92:95],a[208:211],v[172:175],v[140:143]
EX v73,v73
MM v[92:95],a[212:215],v[188:191],v[92:95]
TR a[160:161],v232 offset:1024
CV v72,v72,v73
MM v[92:95],a[216:219],v[204:207],v[92:95]
EX v74,v74
MM v[92:95],a[220:223],v[220:223],v[92:95]
EX v75,v75
MM v[88:91],a[204:207],v[220:223],v[140:143]
TR a[162:163],v232 offset:5120
CV v73,v74,v75
MM v[88:91],a[200:203],v[204:207],v[88:91]
TR a[164:165],v232 offset:9216
TR a[166:167],v232 offset:13312
MM v[88:91],a[196:199],v[188:191],v[88:91]
EX v76,v76
MM v[88:91],a[192:195],v[172:175],v[88:91]
EX v77,v77
MM v[96:99],a[224:227],v[160:163],v[128:131]
TR a[168:169],v233 offset:1024
CV v74,v76,v77
MM v[96:99],a[228:231],v[176:179],v[96:99]
EX v78,v78
MM v[96:99],a[232:235],v[192:195],v[96:99]
TR a[170:171],v233 offset:5120
TR a[172:173],v233 offset:9216
MM v[96:99],a[236:239],v[208:211],v[96:99]
)ASM"
R"ASM(
EX v79,v79
MM v[100:103],a[252:255],v[208:211],v[128:131]
TR a[174:175],v233 offset:13312
CV v75,v78,v79
MM v[100:103],a[248:251],v[192:195],v[100:103]
EX v80,v80
MM v[100:103],a[244:247],v[176:179],v[100:103]
EX v81,v81
MM v[100:103],a[240:243],v[160:163],v[100:103]
TR a[176:177],v232 offset:1536
CV v80,v80,v81
MM v[108:111],a[240:243],v[164:167],v[132:135]
EX v82,v82
MM v[108:111],a[244:247],v[180:183],v[108:111]
TR a[178:179],v232 offset:5632
TR a[180:181],v232 offset:9728
MM v[108:111],a[248:251],v[196:199],v[108:111]
EX v83,v83
MM v[108:111],a[252:255],v[212:215],v[108:111]
TR a[182:183],v232 offset:13824
CV v81,v82,v83
MM v[104:107],a[236:239],v[212:215],v[132:135]
EX v84,v84
MM v[104:107],a[232:235],v[196:199],v[104:107]
EX v85,v85
MM v[104:107],a[228:231],v[180:183],v[104:107]
TR a[184:185],v233 offset:1536
CV v82,v84,v85
MM v[104:107],a[224:227],v[164:167],v[104:107]
EX v86,v86
MM v[112:115],a[224:227],v[168:171],v[136:139]
TR a[186:187],v233 offset:5632
TR a[188:189],v233 offset:9728
MM v[112:115],a[228:231],v[184:187],v[112:115]
EX v87,v87
MM v[112:115],a[232:235],v[200:203],v[112:115]
TR a[190:191],v233 offset:13824
CV v83,v86,v87
MM v[112:115],a[236:239],v[216:219],v[112:115]
EX v88,v88
MM v[116:119],a[252:255],v[216:219],v[136:139]
EX v89,v89
MM v[116:119],a[248:251],v[200:203],v[116:119]
EX v90,v90
MM v[116:119],a[244:247],v[184:187],v[116:119]
EX v91,v91
MM v[116:119],a[240:243],v[168:171],v[116:119]
EX v92,v92
MM v[124:127],a[240:243],v[172:175],v[140:143]
EX v93,v93
MM v[124:127],a[244:247],v[188:191],v[124:127]
EX v94,v94
MM v[124:127],a[248:251],v[204:207],v[124:127]
EX v95,v95
MM v[124:127],a[252:255],v[220:223],v[124:127]
EX v96,v96
MM v[120:123],a[236:239],v[220:223],v[140:143]
EX v97,v97
MM v[120:123],a[232:235],v[204:207],v[120:123]
EX v98,v98
MM v[120:123],a[228:231],v[188:191],v[120:123]
EX v99,v99
MM v[120:123],a[224:227],v[172:175],v[120:123]
s_waitcnt vmcnt(19) lgkmcnt(0)
s_barrier
MM a[0:3],a[128:131],v[0:3],a[0:3]
s_add_u32 m0, s65, 0
CV v88,v88,v89
CV v89,v90,v91
MM a[0:3],a[132:135],v[4:7],a[0:3]
RD a[192:195],v224 offset:32768
CV v90,v92,v93
MM a[8:11],a[132:135],v[12:15],a[8:11]
RD a[196:199],v225 offset:32768
CV v91,v94,v95
MM a[8:11],a[128:131],v[8:11],a[8:11]
buffer_load_dwordx4 v228, s[8:11], s64 offen offset:0 lds
MM a[16:19],a[128:131],v[16:19],a[16:19]
RD a[200:203],v226 offset:32768
CV v68,v96,v97
MM a[16:19],a[132:135],v[20:23],a[16:19]
RD a[204:207],v227 offset:32768
CV v69,v98,v99
MM a[24:27],a[132:135],v[28:31],a[24:27]
buffer_load_dwordx4 v229, s[8:11], s64 offen offset:1024 lds
MM a[24:27],a[128:131],v[24:27],a[24:27]
RD a[208:211],v224 offset:36864
RD a[212:215],v225 offset:36864
MM a[28:31],a[136:139],v[24:27],a[28:31]
buffer_load_dwordx4 v230, s[8:11], s64 offen offset:2048 lds
MM a[28:31],a[140:143],v[28:31],a[28:31]
EX v100,v100
MM a[20:23],a[140:143],v[20:23],a[20:23]
EX v101,v101
MM a[20:23],a[136:139],v[16:19],a[20:23]
RD a[216:219],v226 offset:36864
CV v70,v100,v101
MM a[12:15],a[136:139],v[8:11],a[12:15]
EX v102,v102
MM a[12:15],a[140:143],v[12:15],a[12:15]
RD a[220:223],v227 offset:36864
RD a[224:227],v224 offset:40960
MM a[4:7],a[140:143],v[4:7],a[4:7]
EX v103,v103
MM a[4:7],a[136:139],v[0:3],a[4:7]
RD a[228:231],v225 offset:40960
CV v71,v102,v103
MM a[32:35],a[144:147],v[0:3],a[32:35]
EX v104,v104
MM a[32:35],a[148:151],v[4:7],a[32:35]
buffer_load_dwordx4 v231, s[8:11], s64 offen offset:3072 lds
MM a[40:43],a[148:151],v[12:15],a[40:43]
s_add_u32 m0, s65, 65536
EX v105,v105
MM a[40:43],a[144:147],v[8:11],a[40:43]
RD a[232:235],v226 offset:40960
CV v76,v104,v105
MM a[48:51],a[144:147],v[16:19],a[48:51]
EX v106,v106
MM a[48:51],a[148:151],v[20:23],a[48:51]
EX v107,v107
MM a[56:59],a[148:151],v[28:31],a[56:59]
RD a[236:239],v227 offset:40960
CV v77,v106,v107
MM a[56:59],a[144:147],v[24:27],a[56:59]
EX v108,v108
MM a[60:63],a[152:155],v[24:27],a[60:63]
buffer_load_dwordx4 v235, s[12:15], s81 offen offset:0 lds
MM a[60:63],a[156:159],v[28:31],a[60:63]
EX v109,v109
MM a[52:55],a[156:159],v[20:23],a[52:55]
s_add_u32 s87, s95, 2048
buffer_store_dword v243, v248, s[20:23], s87 offen offset:12 sc1
MM a[52:55],a[152:155],v[16:19],a[52:55]
RD a[240:243],v224 offset:45056
CV v78,v108,v109
MM a[44:47],a[152:155],v[8:11],a[44:47]
EX v110,v110
MM a[44:47],a[156:159],v[12:15],a[44:47]
EX v111,v111
MM a[36:39],a[156:159],v[4:7],a[36:39]
RD a[244:247],v225 offset:45056
CV v79,v110,v111
MM a[36:39],a[152:155],v[0:3],a[36:39]
LPG14_%=:
s_cbranch_execz LTR2_%=
EX v112,v112
MM a[64:67],a[160:163],v[0:3],a[64:67]
buffer_load_dwordx4 v235, s[12:15], s64 offen offset:1024 lds
MM a[64:67],a[164:167],v[4:7],a[64:67]
EX v113,v113
MM a[72:75],a[164:167],v[12:15],a[72:75]
RD a[248:251],v226 offset:45056
CV v84,v112,v113
MM a[72:75],a[160:163],v[8:11],a[72:75]
EX v114,v114
MM a[80:83],a[160:163],v[16:19],a[80:83]
EX v115,v115
MM a[80:83],a[164:167],v[20:23],a[80:83]
RD a[252:255],v227 offset:45056
CV v85,v114,v115
MM a[88:91],a[164:167],v[28:31],a[88:91]
EX v116,v116
MM a[88:91],a[160:163],v[24:27],a[88:91]
buffer_load_dwordx4 v235, s[12:15], s81 offen offset:2048 lds
MM a[92:95],a[168:171],v[24:27],a[92:95]
EX v117,v117
MM a[92:95],a[172:175],v[28:31],a[92:95]
EX v118,v118
MM a[84:87],a[172:175],v[20:23],a[84:87]
buffer_load_dwordx4 v235, s[12:15], s64 offen offset:3072 lds
MM a[84:87],a[168:171],v[16:19],a[84:87]
EX v119,v119
MM a[76:79],a[168:171],v[8:11],a[76:79]
EX v120,v120
MM a[76:79],a[172:175],v[12:15],a[76:79]
EX v121,v121
MM a[68:71],a[172:175],v[4:7],a[68:71]
EX v122,v122
MM a[68:71],a[168:171],v[0:3],a[68:71]
EX v123,v123
MM a[96:99],a[176:179],v[0:3],a[96:99]
EX v124,v124
MM a[96:99],a[180:183],v[4:7],a[96:99]
EX v125,v125
MM a[104:107],a[180:183],v[12:15],a[104:107]
EX v126,v126
MM a[104:107],a[176:179],v[8:11],a[104:107]
EX v127,v127
MM a[112:115],a[176:179],v[16:19],a[112:115]
CV v86,v116,v117
CV v87,v118,v119
MM a[112:115],a[180:183],v[20:23],a[112:115]
CV v92,v120,v121
CV v93,v122,v123
MM a[120:123],a[180:183],v[28:31],a[120:123]
CV v94,v124,v125
CV v95,v126,v127
MM a[120:123],a[176:179],v[24:27],a[120:123]
MM a[124:127],a[184:187],v[24:27],a[124:127]
v_add_f32 v244, v144, v145
v_add_f32 v245, v148, v149
MM a[124:127],a[188:191],v[28:31],a[124:127]
v_add_f32 v246, v152, v153
v_add_f32 v247, v156, v157
MM a[116:119],a[188:191],v[20:23],a[116:119]
MM a[116:119],a[184:187],v[16:19],a[116:119]
MM a[108:111],a[184:187],v[8:11],a[108:111]
MM a[108:111],a[188:191],v[12:15],a[108:111]
MM a[100:103],a[188:191],v[4:7],a[100:103]
MM a[100:103],a[184:187],v[0:3],a[100:103]
v_smfmac_f32_16x16x64_bf16 v[144:147], v[236:239], v[64:71], v234
v_smfmac_f32_16x16x64_bf16 v[148:151], v[236:239], v[72:79], v234
v_smfmac_f32_16x16x64_bf16 v[152:155], v[236:239], v[80:87], v234
v_smfmac_f32_16x16x64_bf16 v[156:159], v[236:239], v[88:95], v234
s_waitcnt lgkmcnt(0)
MM v[0:3],a[192:195],v[160:163],v[128:131]
s_add_u32 s64, s64, 16384
TR a[128:129],v232 offset:16384
s_add_u32 s81, s64, 896
MM v[0:3],a[196:199],v[176:179],v[0:3]
TR a[130:131],v232 offset:20480
v_max3_f32 v249, v144, v145, v148
MM v[0:3],a[200:203],v[192:195],v[0:3]
TR a[132:133],v232 offset:24576
v_max3_f32 v249, v249, v149, v152
MM v[0:3],a[204:207],v[208:211],v[0:3]
TR a[134:135],v232 offset:28672
v_max3_f32 v249, v249, v153, v156
MM v[4:7],a[220:223],v[208:211],v[128:131]
TR a[136:137],v233 offset:16384
v_max_f32 v249, v249, v157
MM v[4:7],a[216:219],v[192:195],v[4:7]
TR a[138:139],v233 offset:20480
v_cmp_lt_f32 vcc, s71, v249
MM v[4:7],a[212:215],v[176:179],v[4:7]
TR a[140:141],v233 offset:24576
TR a[142:143],v233 offset:28672
MM v[4:7],a[208:211],v[160:163],v[4:7]
s_cbranch_vccnz LSITE17_%=
LBACK17_%=:
MM v[12:15],a[208:211],v[164:167],v[132:135]
v_lshrrev_b32 v127, 8, v248
v_lshlrev_b32 v124, 9, v127
MM v[12:15],a[212:215],v[180:183],v[12:15]
v_and_b32 v126, 192, v248
v_add_u32 v124, v124, v126
MM v[12:15],a[216:219],v[196:199],v[12:15]
v_bfe_u32 v126, v248, 4, 2
v_lshl_add_u32 v124, v126, 3, v124
MM v[12:15],a[220:223],v[212:215],v[12:15]
s_lshr_b32 s76, %[swave], 1
EX v0,v0
MM v[8:11],a[204:207],v[212:215],v[132:135]
s_mul_i32 s76, s76, 10240
EX v1,v1
MM v[8:11],a[200:203],v[196:199],v[8:11]
s_and_b32 s77, %[swave], 1
EX v2,v2
MM v[8:11],a[196:199],v[180:183],v[8:11]
s_lshl_b32 s77, s77, 5
TR a[144:145],v232 offset:16896
CV v0,v0,v1
MM v[8:11],a[192:195],v[164:167],v[8:11]
s_add_u32 s76, s76, s77
EX v3,v3
MM v[16:19],a[192:195],v[168:171],v[136:139]
s_add_u32 s76, s76, %[slds]
EX v4,v4
MM v[16:19],a[196:199],v[184:187],v[16:19]
s_add_u32 s76, s76, 131072
v_add_u32 v124, s76, v124
v_bfe_u32 v126, v248, 4, 3
MM v[16:19],a[200:203],v[200:203],v[16:19]
v_lshl_add_u32 v126, v127, 3, v126
v_lshlrev_b32 v126, 4, v126
MM v[16:19],a[204:207],v[216:219],v[16:19]
s_mul_i32 s76, %[swave], 3072
EX v5,v5
MM v[20:23],a[220:223],v[216:219],v[136:139]
s_add_u32 s76, s76, %[slds]
TR a[146:147],v232 offset:20992
CV v1,v2,v3
MM v[20:23],a[216:219],v[200:203],v[20:23]
s_add_u32 s76, s76, 151552
TR a[148:149],v232 offset:25088
v_add_u32 v125, s76, v126
MM v[20:23],a[212:215],v[184:187],v[20:23]
s_lshr_b32 s76, s48, 7
EX v6,v6
MM v[20:23],a[208:211],v[168:171],v[20:23]
s_lshl_b32 s76, s76, 5
EX v7,v7
MM v[28:31],a[208:211],v[172:175],v[140:143]
s_and_b32 s77, s48, 16
TR a[150:151],v232 offset:29184
TR a[152:153],v233 offset:16896
MM v[28:31],a[212:215],v[188:191],v[28:31]
s_add_u32 s76, s76, s77
EX v8,v8
MM v[28:31],a[216:219],v[204:207],v[28:31]
s_bfe_u32 s77, s48, 131077
TR a[154:155],v233 offset:20992
CV v2,v4,v5
MM v[28:31],a[220:223],v[220:223],v[28:31]
s_lshl_b32 s77, s77, 2
EX v9,v9
MM v[24:27],a[204:207],v[220:223],v[140:143]
s_add_u32 s76, s76, s77
TR a[156:157],v233 offset:25088
CV v3,v6,v7
MM v[24:27],a[200:203],v[204:207],v[24:27]
s_add_u32 s76, s76, %[swave]
EX v10,v10
MM v[24:27],a[196:199],v[188:191],v[24:27]
s_lshl_b32 s97, s76, 2
TR a[158:159],v233 offset:29184
CV v8,v8,v9
MM v[24:27],a[192:195],v[172:175],v[24:27]
s_add_u32 s97, s97, 77824
EX v11,v11
MM v[32:35],a[224:227],v[160:163],v[128:131]
s_lshl_b32 s76, s76, 9
TR a[160:161],v232 offset:17408
TR a[162:163],v232 offset:21504
MM v[32:35],a[228:231],v[176:179],v[32:35]
s_add_u32 s76, s76, 22183936
EX v12,v12
MM v[32:35],a[232:235],v[192:195],v[32:35]
EX v13,v13
MM v[32:35],a[236:239],v[208:211],v[32:35]
TR a[164:165],v232 offset:25600
CV v9,v10,v11
MM v[36:39],a[252:255],v[208:211],v[128:131]
EX v14,v14
MM v[36:39],a[248:251],v[192:195],v[36:39]
TR a[166:167],v232 offset:29696
CV v10,v12,v13
MM v[36:39],a[244:247],v[176:179],v[36:39]
EX v15,v15
MM v[36:39],a[240:243],v[160:163],v[36:39]
TR a[168:169],v233 offset:17408
TR a[170:171],v233 offset:21504
MM v[44:47],a[240:243],v[164:167],v[132:135]
EX v16,v16
MM v[44:47],a[244:247],v[180:183],v[44:47]
EX v17,v17
MM v[44:47],a[248:251],v[196:199],v[44:47]
TR a[172:173],v233 offset:25600
CV v11,v14,v15
MM v[44:47],a[252:255],v[212:215],v[44:47]
EX v18,v18
MM v[40:43],a[236:239],v[212:215],v[132:135]
TR a[174:175],v233 offset:29696
CV v16,v16,v17
MM v[40:43],a[232:235],v[196:199],v[40:43]
EX v19,v19
MM v[40:43],a[228:231],v[180:183],v[40:43]
TR a[176:177],v232 offset:17920
TR a[178:179],v232 offset:22016
MM v[40:43],a[224:227],v[164:167],v[40:43]
EX v20,v20
MM v[48:51],a[224:227],v[168:171],v[136:139]
EX v21,v21
MM v[48:51],a[228:231],v[184:187],v[48:51]
TR a[180:181],v232 offset:26112
CV v17,v18,v19
MM v[48:51],a[232:235],v[200:203],v[48:51]
EX v22,v22
MM v[48:51],a[236:239],v[216:219],v[48:51]
TR a[182:183],v232 offset:30208
CV v18,v20,v21
MM v[52:55],a[252:255],v[216:219],v[136:139]
EX v23,v23
MM v[52:55],a[248:251],v[200:203],v[52:55]
TR a[184:185],v233 offset:17920
TR a[186:187],v233 offset:22016
MM v[52:55],a[244:247],v[184:187],v[52:55]
EX v24,v24
MM v[52:55],a[240:243],v[168:171],v[52:55]
TR a[188:189],v233 offset:26112
CV v19,v22,v23
MM v[60:63],a[240:243],v[172:175],v[140:143]
EX v25,v25
MM v[60:63],a[244:247],v[188:191],v[60:63]
TR a[190:191],v233 offset:30208
CV v24,v24,v25
MM v[60:63],a[248:251],v[204:207],v[60:63]
EX v26,v26
MM v[60:63],a[252:255],v[220:223],v[60:63]
EX v27,v27
)ASM"
R"ASM(
MM v[56:59],a[236:239],v[220:223],v[140:143]
EX v28,v28
MM v[56:59],a[232:235],v[204:207],v[56:59]
EX v29,v29
MM v[56:59],a[228:231],v[188:191],v[56:59]
EX v30,v30
MM v[56:59],a[224:227],v[172:175],v[56:59]
s_waitcnt vmcnt(10) lgkmcnt(0)
s_barrier
MM a[0:3],a[128:131],v[64:67],a[0:3]
s_add_u32 m0, s65, 16384
CV v25,v26,v27
CV v26,v28,v29
MM a[0:3],a[132:135],v[68:71],a[0:3]
RD a[192:195],v224 offset:49152
RD a[196:199],v225 offset:49152
MM a[8:11],a[132:135],v[76:79],a[8:11]
EX v31,v31
MM a[8:11],a[128:131],v[72:75],a[8:11]
RD a[200:203],v226 offset:49152
CV v27,v30,v31
MM a[16:19],a[128:131],v[80:83],a[16:19]
TR v[96:97],v124 offset:0
TR v[98:99],v124 offset:256
MM a[16:19],a[132:135],v[84:87],a[16:19]
RD v[108:111],v125 offset:0
TR v[100:101],v124 offset:2048
MM a[24:27],a[132:135],v[92:95],a[24:27]
TR v[102:103],v124 offset:2304
RD v[112:115],v125 offset:512
MM a[24:27],a[128:131],v[88:91],a[24:27]
TR v[104:105],v124 offset:4096
TR v[106:107],v124 offset:4352
MM a[28:31],a[136:139],v[88:91],a[28:31]
RD v[116:119],v125 offset:1024
RD a[204:207],v227 offset:49152
EX v32,v32
MM a[28:31],a[140:143],v[92:95],a[28:31]
EX v33,v33
MM a[20:23],a[140:143],v[84:87],a[20:23]
EX v34,v34
MM a[20:23],a[136:139],v[80:83],a[20:23]
buffer_load_dwordx4 v228, s[8:11], s64 offen offset:0 lds
MM a[12:15],a[136:139],v[72:75],a[12:15]
EX v35,v35
MM a[12:15],a[140:143],v[76:79],a[12:15]
EX v36,v36
MM a[4:7],a[140:143],v[68:71],a[4:7]
CV v4,v32,v33
CV v5,v34,v35
MM a[4:7],a[136:139],v[64:67],a[4:7]
EX v37,v37
MM a[32:35],a[144:147],v[64:67],a[32:35]
EX v38,v38
MM a[32:35],a[148:151],v[68:71],a[32:35]
s_waitcnt lgkmcnt(1)
buffer_load_dwordx4 v229, s[8:11], s64 offen offset:1024 lds
MM a[40:43],a[148:151],v[76:79],a[40:43]
EX v39,v39
MM v[120:123],v[96:99],v[108:111],0
MM v[120:123],v[100:103],v[112:115],v[120:123]
MM v[120:123],v[104:107],v[116:119],v[120:123]
MM a[40:43],a[144:147],v[72:75],a[40:43]
TR v[96:97],v124 offset:6144
TR v[98:99],v124 offset:6400
MM a[48:51],a[144:147],v[80:83],a[48:51]
RD v[108:111],v125 offset:1536
TR v[100:101],v124 offset:8192
MM a[48:51],a[148:151],v[84:87],a[48:51]
TR v[102:103],v124 offset:8448
RD v[112:115],v125 offset:2048
MM a[56:59],a[148:151],v[92:95],a[56:59]
EX v40,v40
MM a[56:59],a[144:147],v[88:91],a[56:59]
EX v41,v41
MM a[60:63],a[152:155],v[88:91],a[60:63]
CV v6,v36,v37
CV v7,v38,v39
MM a[60:63],a[156:159],v[92:95],a[60:63]
EX v42,v42
MM a[52:55],a[156:159],v[84:87],a[52:55]
RD a[208:211],v224 offset:53248
RD a[212:215],v225 offset:53248
MM a[52:55],a[152:155],v[80:83],a[52:55]
EX v43,v43
MM a[44:47],a[152:155],v[72:75],a[44:47]
buffer_load_dwordx4 v230, s[8:11], s64 offen offset:2048 lds
MM a[44:47],a[156:159],v[76:79],a[44:47]
EX v44,v44
MM a[36:39],a[156:159],v[68:71],a[36:39]
s_waitcnt lgkmcnt(2)
EX v45,v45
MM a[36:39],a[152:155],v[64:67],a[36:39]
CV v12,v40,v41
CV v13,v42,v43
MM v[120:123],v[96:99],v[108:111],v[120:123]
MM v[120:123],v[100:103],v[112:115],v[120:123]
MM a[64:67],a[160:163],v[64:67],a[64:67]
EX v46,v46
MM a[64:67],a[164:167],v[68:71],a[64:67]
RD a[216:219],v226 offset:53248
CV v14,v44,v45
MM a[72:75],a[164:167],v[76:79],a[72:75]
EX v47,v47
MM a[72:75],a[160:163],v[72:75],a[72:75]
buffer_load_dwordx4 v231, s[8:11], s64 offen offset:3072 lds
MM a[80:83],a[160:163],v[80:83],a[80:83]
s_add_u32 m0, s65, 81920
EX v48,v48
MM a[80:83],a[164:167],v[84:87],a[80:83]
RD a[220:223],v227 offset:53248
CV v15,v46,v47
MM a[88:91],a[164:167],v[92:95],a[88:91]
s_mov_b32 exec_lo, 0x00ff00ff
s_mov_b32 exec_hi, 0x00ff00ff
buffer_store_dwordx4 v[120:123], v126, s[20:23], s76 offen offset:0 sc1
s_mov_b64 exec, -1
s_nop 0
MM a[88:91],a[160:163],v[88:91],a[88:91]
EX v49,v49
MM a[92:95],a[168:171],v[88:91],a[92:95]
EX v50,v50
MM a[92:95],a[172:175],v[92:95],a[92:95]
EX v51,v51
MM a[84:87],a[172:175],v[84:87],a[84:87]
RD a[224:227],v224 offset:57344
CV v20,v48,v49
MM a[84:87],a[168:171],v[80:83],a[84:87]
EX v52,v52
MM a[76:79],a[168:171],v[72:75],a[76:79]
buffer_load_dwordx4 v235, s[12:15], s81 offen offset:0 lds
MM a[76:79],a[172:175],v[76:79],a[76:79]
EX v53,v53
MM a[68:71],a[172:175],v[68:71],a[68:71]
RD a[228:231],v225 offset:57344
CV v21,v50,v51
MM a[68:71],a[168:171],v[64:67],a[68:71]
EX v54,v54
MM a[96:99],a[176:179],v[64:67],a[96:99]
RD a[232:235],v226 offset:57344
CV v22,v52,v53
MM a[96:99],a[180:183],v[68:71],a[96:99]
EX v55,v55
MM a[104:107],a[180:183],v[76:79],a[104:107]
EX v56,v56
MM a[104:107],a[176:179],v[72:75],a[104:107]
buffer_load_dwordx4 v235, s[12:15], s64 offen offset:1024 lds
MM a[112:115],a[176:179],v[80:83],a[112:115]
EX v57,v57
MM a[112:115],a[180:183],v[84:87],a[112:115]
RD a[236:239],v227 offset:57344
CV v23,v54,v55
MM a[120:123],a[180:183],v[92:95],a[120:123]
EX v58,v58
MM a[120:123],a[176:179],v[88:91],a[120:123]
RD a[240:243],v224 offset:61440
CV v28,v56,v57
MM a[124:127],a[184:187],v[88:91],a[124:127]
v_add_f32 v244, v144, v145
v_add_f32 v245, v148, v149
MM a[124:127],a[188:191],v[92:95],a[124:127]
v_add_f32 v246, v152, v153
v_add_f32 v247, v156, v157
MM a[116:119],a[188:191],v[84:87],a[116:119]
EX v59,v59
MM a[116:119],a[184:187],v[80:83],a[116:119]
EX v60,v60
MM a[108:111],a[184:187],v[72:75],a[108:111]
EX v61,v61
MM a[108:111],a[188:191],v[76:79],a[108:111]
buffer_load_dwordx4 v235, s[12:15], s81 offen offset:2048 lds
MM a[100:103],a[188:191],v[68:71],a[100:103]
EX v62,v62
MM a[100:103],a[184:187],v[64:67],a[100:103]
RD a[244:247],v225 offset:61440
CV v29,v58,v59
v_smfmac_f32_16x16x64_bf16 v[144:147], v[236:239], v[0:7], v234
RD a[248:251],v226 offset:61440
RD a[252:255],v227 offset:61440
EX v63,v63
v_smfmac_f32_16x16x64_bf16 v[148:151], v[236:239], v[8:15], v234
CV v30,v60,v61
v_smfmac_f32_16x16x64_bf16 v[152:155], v[236:239], v[16:23], v234
CV v31,v62,v63
s_nop 1
v_smfmac_f32_16x16x64_bf16 v[156:159], v[236:239], v[24:31], v234
s_waitcnt lgkmcnt(0)
buffer_load_dwordx4 v235, s[12:15], s64 offen offset:3072 lds
MM v[64:67],a[192:195],v[160:163],v[128:131]
s_add_u32 s64, s64, 16384
TR a[128:129],v232 offset:32768
s_add_u32 s81, s64, 896
MM v[64:67],a[196:199],v[176:179],v[64:67]
TR a[130:131],v232 offset:36864
v_max3_f32 v249, v144, v145, v148
MM v[64:67],a[200:203],v[192:195],v[64:67]
TR a[132:133],v232 offset:40960
v_max3_f32 v249, v249, v149, v152
MM v[64:67],a[204:207],v[208:211],v[64:67]
TR a[134:135],v232 offset:45056
v_max3_f32 v249, v249, v153, v156
MM v[68:71],a[220:223],v[208:211],v[128:131]
TR a[136:137],v233 offset:32768
v_max_f32 v249, v249, v157
MM v[68:71],a[216:219],v[192:195],v[68:71]
TR a[138:139],v233 offset:36864
v_cmp_lt_f32 vcc, s71, v249
MM v[68:71],a[212:215],v[176:179],v[68:71]
TR a[140:141],v233 offset:40960
TR a[142:143],v233 offset:45056
MM v[68:71],a[208:211],v[160:163],v[68:71]
s_cbranch_vccnz LSITE18_%=
LBACK18_%=:
MM v[76:79],a[208:211],v[164:167],v[132:135]
EX v64,v64
MM v[76:79],a[212:215],v[180:183],v[76:79]
EX v65,v65
MM v[76:79],a[216:219],v[196:199],v[76:79]
TR a[144:145],v232 offset:33280
CV v64,v64,v65
MM v[76:79],a[220:223],v[212:215],v[76:79]
EX v66,v66
MM v[72:75],a[204:207],v[212:215],v[132:135]
EX v67,v67
MM v[72:75],a[200:203],v[196:199],v[72:75]
TR a[146:147],v232 offset:37376
CV v65,v66,v67
MM v[72:75],a[196:199],v[180:183],v[72:75]
EX v68,v68
MM v[72:75],a[192:195],v[164:167],v[72:75]
TR a[148:149],v232 offset:41472
TR a[150:151],v232 offset:45568
MM v[80:83],a[192:195],v[168:171],v[136:139]
EX v69,v69
MM v[80:83],a[196:199],v[184:187],v[80:83]
TR a[152:153],v233 offset:33280
CV v66,v68,v69
MM v[80:83],a[200:203],v[200:203],v[80:83]
EX v70,v70
MM v[80:83],a[204:207],v[216:219],v[80:83]
TR a[154:155],v233 offset:37376
TR a[156:157],v233 offset:41472
MM v[84:87],a[220:223],v[216:219],v[136:139]
EX v71,v71
MM v[84:87],a[216:219],v[200:203],v[84:87]
TR a[158:159],v233 offset:45568
CV v67,v70,v71
MM v[84:87],a[212:215],v[184:187],v[84:87]
EX v72,v72
MM v[84:87],a[208:211],v[168:171],v[84:87]
EX v73,v73
MM v[92:95],a[208:211],v[172:175],v[140:143]
TR a[160:161],v232 offset:33792
CV v72,v72,v73
MM v[92:95],a[212:215],v[188:191],v[92:95]
EX v74,v74
MM v[92:95],a[216:219],v[204:207],v[92:95]
TR a[162:163],v232 offset:37888
TR a[164:165],v232 offset:41984
MM v[92:95],a[220:223],v[220:223],v[92:95]
EX v75,v75
MM v[88:91],a[204:207],v[220:223],v[140:143]
TR a[166:167],v232 offset:46080
CV v73,v74,v75
MM v[88:91],a[200:203],v[204:207],v[88:91]
EX v76,v76
MM v[88:91],a[196:199],v[188:191],v[88:91]
EX v77,v77
MM v[88:91],a[192:195],v[172:175],v[88:91]
TR a[168:169],v233 offset:33792
CV v74,v76,v77
MM v[96:99],a[224:227],v[160:163],v[128:131]
EX v78,v78
MM v[96:99],a[228:231],v[176:179],v[96:99]
TR a[170:171],v233 offset:37888
TR a[172:173],v233 offset:41984
MM v[96:99],a[232:235],v[192:195],v[96:99]
EX v79,v79
MM v[96:99],a[236:239],v[208:211],v[96:99]
TR a[174:175],v233 offset:46080
CV v75,v78,v79
MM v[100:103],a[252:255],v[208:211],v[128:131]
EX v80,v80
MM v[100:103],a[248:251],v[192:195],v[100:103]
EX v81,v81
MM v[100:103],a[244:247],v[176:179],v[100:103]
TR a[176:177],v232 offset:34304
CV v80,v80,v81
MM v[100:103],a[240:243],v[160:163],v[100:103]
EX v82,v82
MM v[108:111],a[240:243],v[164:167],v[132:135]
v_lshrrev_b32 v253, 7, v248
v_mul_u32_u24 v253, 1280, v253
MM v[108:111],a[244:247],v[180:183],v[108:111]
v_and_b32 v252, 112, v248
v_add_u32 v253, v253, v252
MM v[108:111],a[248:251],v[196:199],v[108:111]
s_mul_i32 s90, %[swave], 61440
TR a[178:179],v232 offset:38400
TR a[180:181],v232 offset:42496
MM v[108:111],a[252:255],v[212:215],v[108:111]
s_mul_i32 s91, %[swave], 6144
EX v83,v83
MM v[104:107],a[236:239],v[212:215],v[132:135]
s_add_u32 s91, s91, %[slds]
TR a[182:183],v232 offset:46592
CV v81,v82,v83
MM v[104:107],a[232:235],v[196:199],v[104:107]
EX v84,v84
MM v[104:107],a[228:231],v[180:183],v[104:107]
EX v85,v85
MM v[104:107],a[224:227],v[164:167],v[104:107]
TR a[184:185],v233 offset:34304
CV v82,v84,v85
MM v[112:115],a[224:227],v[168:171],v[136:139]
EX v86,v86
MM v[112:115],a[228:231],v[184:187],v[112:115]
TR a[186:187],v233 offset:38400
TR a[188:189],v233 offset:42496
MM v[112:115],a[232:235],v[200:203],v[112:115]
EX v87,v87
MM v[112:115],a[236:239],v[216:219],v[112:115]
TR a[190:191],v233 offset:46592
CV v83,v86,v87
MM v[116:119],a[252:255],v[216:219],v[136:139]
EX v88,v88
MM v[116:119],a[248:251],v[200:203],v[116:119]
EX v89,v89
MM v[116:119],a[244:247],v[184:187],v[116:119]
EX v90,v90
MM v[116:119],a[240:243],v[168:171],v[116:119]
EX v91,v91
MM v[124:127],a[240:243],v[172:175],v[140:143]
EX v92,v92
MM v[124:127],a[244:247],v[188:191],v[124:127]
EX v93,v93
MM v[124:127],a[248:251],v[204:207],v[124:127]
EX v94,v94
MM v[124:127],a[252:255],v[220:223],v[124:127]
EX v95,v95
MM v[120:123],a[236:239],v[220:223],v[140:143]
EX v96,v96
MM v[120:123],a[232:235],v[204:207],v[120:123]
EX v97,v97
MM v[120:123],a[228:231],v[188:191],v[120:123]
EX v98,v98
MM v[120:123],a[224:227],v[172:175],v[120:123]
s_waitcnt vmcnt(14) lgkmcnt(0)
s_barrier
MM a[0:3],a[128:131],v[0:3],a[0:3]
s_nop 0
LPG15_%=:
s_cbranch_execz LTR2_%=
s_add_u32 m0, s65, 32768
CV v88,v88,v89
CV v89,v90,v91
MM a[0:3],a[132:135],v[4:7],a[0:3]
RD a[192:195],v224 offset:0
CV v90,v92,v93
MM a[8:11],a[132:135],v[12:15],a[8:11]
RD a[196:199],v225 offset:0
CV v91,v94,v95
MM a[8:11],a[128:131],v[8:11],a[8:11]
buffer_load_dwordx4 v228, s[8:11], s64 offen offset:0 lds
MM a[16:19],a[128:131],v[16:19],a[16:19]
RD a[200:203],v226 offset:0
CV v68,v96,v97
MM a[16:19],a[132:135],v[20:23],a[16:19]
buffer_load_dwordx4 v229, s[8:11], s64 offen offset:1024 lds
MM a[24:27],a[132:135],v[28:31],a[24:27]
RD a[204:207],v227 offset:0
RD a[208:211],v224 offset:4096
MM a[24:27],a[128:131],v[24:27],a[24:27]
EX v99,v99
MM a[28:31],a[136:139],v[24:27],a[28:31]
RD a[212:215],v225 offset:4096
CV v69,v98,v99
MM a[28:31],a[140:143],v[28:31],a[28:31]
EX v100,v100
MM a[20:23],a[140:143],v[20:23],a[20:23]
buffer_load_dwordx4 v230, s[8:11], s64 offen offset:2048 lds
MM a[20:23],a[136:139],v[16:19],a[20:23]
EX v101,v101
MM a[12:15],a[136:139],v[8:11],a[12:15]
RD a[216:219],v226 offset:4096
CV v70,v100,v101
MM a[12:15],a[140:143],v[12:15],a[12:15]
EX v102,v102
MM a[4:7],a[140:143],v[4:7],a[4:7]
)ASM"
R"ASM(
EX v103,v103
MM a[4:7],a[136:139],v[0:3],a[4:7]
RD a[220:223],v227 offset:4096
CV v71,v102,v103
MM a[32:35],a[144:147],v[0:3],a[32:35]
EX v104,v104
MM a[32:35],a[148:151],v[4:7],a[32:35]
buffer_load_dwordx4 v231, s[8:11], s64 offen offset:3072 lds
MM a[40:43],a[148:151],v[12:15],a[40:43]
s_add_u32 m0, s65, 98304
EX v105,v105
MM a[40:43],a[144:147],v[8:11],a[40:43]
RD a[224:227],v224 offset:8192
CV v76,v104,v105
MM a[48:51],a[144:147],v[16:19],a[48:51]
EX v106,v106
MM a[48:51],a[148:151],v[20:23],a[48:51]
EX v107,v107
MM a[56:59],a[148:151],v[28:31],a[56:59]
RD a[228:231],v225 offset:8192
CV v77,v106,v107
MM a[56:59],a[144:147],v[24:27],a[56:59]
EX v108,v108
MM a[60:63],a[152:155],v[24:27],a[60:63]
buffer_load_dwordx4 v235, s[12:15], s81 offen offset:0 lds
MM a[60:63],a[156:159],v[28:31],a[60:63]
EX v109,v109
MM a[52:55],a[156:159],v[20:23],a[52:55]
RD a[232:235],v226 offset:8192
CV v78,v108,v109
MM a[52:55],a[152:155],v[16:19],a[52:55]
EX v110,v110
MM a[44:47],a[152:155],v[8:11],a[44:47]
EX v111,v111
MM a[44:47],a[156:159],v[12:15],a[44:47]
RD a[236:239],v227 offset:8192
CV v79,v110,v111
MM a[36:39],a[156:159],v[4:7],a[36:39]
buffer_load_dwordx4 v235, s[12:15], s64 offen offset:1024 lds
MM a[36:39],a[152:155],v[0:3],a[36:39]
EX v112,v112
MM a[64:67],a[160:163],v[0:3],a[64:67]
EX v113,v113
MM a[64:67],a[164:167],v[4:7],a[64:67]
RD a[240:243],v224 offset:12288
CV v84,v112,v113
MM a[72:75],a[164:167],v[12:15],a[72:75]
EX v114,v114
MM a[72:75],a[160:163],v[8:11],a[72:75]
RD a[244:247],v225 offset:12288
RD a[248:251],v226 offset:12288
MM a[80:83],a[160:163],v[16:19],a[80:83]
s_waitcnt vmcnt(10)
v_mov_b32 v250, s69
s_mov_b64 exec, 1
buffer_store_dword v250, off, s[20:23], s97 offset:0 sc1
s_mov_b64 exec, -1
MM a[80:83],a[164:167],v[20:23],a[80:83]
s_add_u32 m0, s91, 139264
s_add_u32 s77, s90, 0
buffer_load_dwordx4 v253, s[28:31], s77 offen offset:0 lds
MM a[88:91],a[164:167],v[28:31],a[88:91]
EX v115,v115
MM a[88:91],a[160:163],v[24:27],a[88:91]
RD a[252:255],v227 offset:12288
CV v85,v114,v115
MM a[92:95],a[168:171],v[24:27],a[92:95]
EX v116,v116
MM a[92:95],a[172:175],v[28:31],a[92:95]
EX v117,v117
MM a[84:87],a[172:175],v[20:23],a[84:87]
s_add_u32 m0, s65, 98304
s_nop 0
buffer_load_dwordx4 v235, s[12:15], s81 offen offset:2048 lds
MM a[84:87],a[168:171],v[16:19],a[84:87]
EX v118,v118
MM a[76:79],a[168:171],v[8:11],a[76:79]
EX v119,v119
MM a[76:79],a[172:175],v[12:15],a[76:79]
EX v120,v120
MM a[68:71],a[172:175],v[4:7],a[68:71]
buffer_load_dwordx4 v235, s[12:15], s64 offen offset:3072 lds
MM a[68:71],a[168:171],v[0:3],a[68:71]
EX v121,v121
MM a[96:99],a[176:179],v[0:3],a[96:99]
EX v122,v122
MM a[96:99],a[180:183],v[4:7],a[96:99]
EX v123,v123
MM a[104:107],a[180:183],v[12:15],a[104:107]
EX v124,v124
MM a[104:107],a[176:179],v[8:11],a[104:107]
EX v125,v125
MM a[112:115],a[176:179],v[16:19],a[112:115]
EX v126,v126
MM a[112:115],a[180:183],v[20:23],a[112:115]
EX v127,v127
MM a[120:123],a[180:183],v[28:31],a[120:123]
CV v86,v116,v117
CV v87,v118,v119
MM a[120:123],a[176:179],v[24:27],a[120:123]
CV v92,v120,v121
CV v93,v122,v123
MM a[124:127],a[184:187],v[24:27],a[124:127]
v_add_f32 v244, v144, v145
v_add_f32 v245, v148, v149
MM a[124:127],a[188:191],v[28:31],a[124:127]
v_add_f32 v246, v152, v153
v_add_f32 v247, v156, v157
MM a[116:119],a[188:191],v[20:23],a[116:119]
CV v94,v124,v125
CV v95,v126,v127
MM a[116:119],a[184:187],v[16:19],a[116:119]
MM a[108:111],a[184:187],v[8:11],a[108:111]
MM a[108:111],a[188:191],v[12:15],a[108:111]
MM a[100:103],a[188:191],v[4:7],a[100:103]
MM a[100:103],a[184:187],v[0:3],a[100:103]
v_smfmac_f32_16x16x64_bf16 v[144:147], v[236:239], v[64:71], v234
v_smfmac_f32_16x16x64_bf16 v[148:151], v[236:239], v[72:79], v234
v_smfmac_f32_16x16x64_bf16 v[152:155], v[236:239], v[80:87], v234
v_smfmac_f32_16x16x64_bf16 v[156:159], v[236:239], v[88:95], v234
s_waitcnt lgkmcnt(0)
MM v[0:3],a[192:195],v[160:163],v[128:131]
s_add_u32 s64, s64, 16384
TR a[128:129],v232 offset:49152
s_add_u32 s81, s64, 896
MM v[0:3],a[196:199],v[176:179],v[0:3]
TR a[130:131],v232 offset:53248
v_max3_f32 v249, v144, v145, v148
MM v[0:3],a[200:203],v[192:195],v[0:3]
TR a[132:133],v232 offset:57344
v_max3_f32 v249, v249, v149, v152
MM v[0:3],a[204:207],v[208:211],v[0:3]
TR a[134:135],v232 offset:61440
v_max3_f32 v249, v249, v153, v156
MM v[4:7],a[220:223],v[208:211],v[128:131]
TR a[136:137],v233 offset:49152
v_max_f32 v249, v249, v157
MM v[4:7],a[216:219],v[192:195],v[4:7]
TR a[138:139],v233 offset:53248
v_cmp_lt_f32 vcc, s71, v249
MM v[4:7],a[212:215],v[176:179],v[4:7]
TR a[140:141],v233 offset:57344
TR a[142:143],v233 offset:61440
MM v[4:7],a[208:211],v[160:163],v[4:7]
s_cbranch_vccnz LSITE19_%=
LBACK19_%=:
MM v[12:15],a[208:211],v[164:167],v[132:135]
s_add_u32 m0, s91, 140288
s_add_u32 s77, s90, 10240
buffer_load_dwordx4 v253, s[28:31], s77 offen offset:0 lds
MM v[12:15],a[212:215],v[180:183],v[12:15]
EX v0,v0
MM v[12:15],a[216:219],v[196:199],v[12:15]
EX v1,v1
MM v[12:15],a[220:223],v[212:215],v[12:15]
TR a[144:145],v232 offset:49664
CV v0,v0,v1
MM v[8:11],a[204:207],v[212:215],v[132:135]
EX v2,v2
MM v[8:11],a[200:203],v[196:199],v[8:11]
EX v3,v3
MM v[8:11],a[196:199],v[180:183],v[8:11]
TR a[146:147],v232 offset:53760
CV v1,v2,v3
MM v[8:11],a[192:195],v[164:167],v[8:11]
EX v4,v4
MM v[16:19],a[192:195],v[168:171],v[136:139]
TR a[148:149],v232 offset:57856
TR a[150:151],v232 offset:61952
MM v[16:19],a[196:199],v[184:187],v[16:19]
EX v5,v5
MM v[16:19],a[200:203],v[200:203],v[16:19]
TR a[152:153],v233 offset:49664
CV v2,v4,v5
MM v[16:19],a[204:207],v[216:219],v[16:19]
EX v6,v6
MM v[20:23],a[220:223],v[216:219],v[136:139]
EX v7,v7
MM v[20:23],a[216:219],v[200:203],v[20:23]
TR a[154:155],v233 offset:53760
CV v3,v6,v7
MM v[20:23],a[212:215],v[184:187],v[20:23]
EX v8,v8
MM v[20:23],a[208:211],v[168:171],v[20:23]
TR a[156:157],v233 offset:57856
TR a[158:159],v233 offset:61952
MM v[28:31],a[208:211],v[172:175],v[140:143]
EX v9,v9
MM v[28:31],a[212:215],v[188:191],v[28:31]
TR a[160:161],v232 offset:50176
CV v8,v8,v9
MM v[28:31],a[216:219],v[204:207],v[28:31]
EX v10,v10
MM v[28:31],a[220:223],v[220:223],v[28:31]
EX v11,v11
MM v[24:27],a[204:207],v[220:223],v[140:143]
TR a[162:163],v232 offset:54272
CV v9,v10,v11
MM v[24:27],a[200:203],v[204:207],v[24:27]
TR a[164:165],v232 offset:58368
TR a[166:167],v232 offset:62464
MM v[24:27],a[196:199],v[188:191],v[24:27]
EX v12,v12
MM v[24:27],a[192:195],v[172:175],v[24:27]
EX v13,v13
MM v[32:35],a[224:227],v[160:163],v[128:131]
TR a[168:169],v233 offset:50176
CV v10,v12,v13
MM v[32:35],a[228:231],v[176:179],v[32:35]
EX v14,v14
MM v[32:35],a[232:235],v[192:195],v[32:35]
TR a[170:171],v233 offset:54272
TR a[172:173],v233 offset:58368
MM v[32:35],a[236:239],v[208:211],v[32:35]
EX v15,v15
MM v[36:39],a[252:255],v[208:211],v[128:131]
TR a[174:175],v233 offset:62464
CV v11,v14,v15
MM v[36:39],a[248:251],v[192:195],v[36:39]
EX v16,v16
MM v[36:39],a[244:247],v[176:179],v[36:39]
EX v17,v17
MM v[36:39],a[240:243],v[160:163],v[36:39]
TR a[176:177],v232 offset:50688
CV v16,v16,v17
MM v[44:47],a[240:243],v[164:167],v[132:135]
s_add_u32 m0, s91, 141312
s_add_u32 s77, s90, 20480
buffer_load_dwordx4 v253, s[28:31], s77 offen offset:0 lds
MM v[44:47],a[244:247],v[180:183],v[44:47]
EX v18,v18
MM v[44:47],a[248:251],v[196:199],v[44:47]
TR a[178:179],v232 offset:54784
TR a[180:181],v232 offset:58880
MM v[44:47],a[252:255],v[212:215],v[44:47]
EX v19,v19
MM v[40:43],a[236:239],v[212:215],v[132:135]
TR a[182:183],v232 offset:62976
CV v17,v18,v19
MM v[40:43],a[232:235],v[196:199],v[40:43]
EX v20,v20
MM v[40:43],a[228:231],v[180:183],v[40:43]
EX v21,v21
MM v[40:43],a[224:227],v[164:167],v[40:43]
TR a[184:185],v233 offset:50688
CV v18,v20,v21
MM v[48:51],a[224:227],v[168:171],v[136:139]
EX v22,v22
MM v[48:51],a[228:231],v[184:187],v[48:51]
TR a[186:187],v233 offset:54784
TR a[188:189],v233 offset:58880
MM v[48:51],a[232:235],v[200:203],v[48:51]
EX v23,v23
MM v[48:51],a[236:239],v[216:219],v[48:51]
TR a[190:191],v233 offset:62976
CV v19,v22,v23
MM v[52:55],a[252:255],v[216:219],v[136:139]
EX v24,v24
MM v[52:55],a[248:251],v[200:203],v[52:55]
EX v25,v25
MM v[52:55],a[244:247],v[184:187],v[52:55]
EX v26,v26
MM v[52:55],a[240:243],v[168:171],v[52:55]
EX v27,v27
MM v[60:63],a[240:243],v[172:175],v[140:143]
EX v28,v28
MM v[60:63],a[244:247],v[188:191],v[60:63]
EX v29,v29
MM v[60:63],a[248:251],v[204:207],v[60:63]
EX v30,v30
MM v[60:63],a[252:255],v[220:223],v[60:63]
EX v31,v31
MM v[56:59],a[236:239],v[220:223],v[140:143]
EX v32,v32
MM v[56:59],a[232:235],v[204:207],v[56:59]
EX v33,v33
MM v[56:59],a[228:231],v[188:191],v[56:59]
EX v34,v34
MM v[56:59],a[224:227],v[172:175],v[56:59]
s_waitcnt lgkmcnt(0)
s_barrier
MM a[0:3],a[128:131],v[64:67],a[0:3]
s_add_u32 m0, s65, 49152
CV v24,v24,v25
CV v25,v26,v27
MM a[0:3],a[132:135],v[68:71],a[0:3]
RD a[192:195],v224 offset:16384
CV v26,v28,v29
MM a[8:11],a[132:135],v[76:79],a[8:11]
RD a[196:199],v225 offset:16384
CV v27,v30,v31
MM a[8:11],a[128:131],v[72:75],a[8:11]
buffer_load_dwordx4 v228, s[8:11], s64 offen offset:0 lds
MM a[16:19],a[128:131],v[80:83],a[16:19]
s_add_u32 m0, s91, 142336
s_add_u32 s77, s90, 30720
buffer_load_dwordx4 v253, s[28:31], s77 offen offset:0 lds
MM a[16:19],a[132:135],v[84:87],a[16:19]
RD a[200:203],v226 offset:16384
CV v4,v32,v33
MM a[24:27],a[132:135],v[92:95],a[24:27]
RD a[204:207],v227 offset:16384
RD a[208:211],v224 offset:20480
MM a[24:27],a[128:131],v[88:91],a[24:27]
s_add_u32 m0, s65, 49152
s_nop 0
buffer_load_dwordx4 v229, s[8:11], s64 offen offset:1024 lds
MM a[28:31],a[136:139],v[88:91],a[28:31]
EX v35,v35
MM a[28:31],a[140:143],v[92:95],a[28:31]
RD a[212:215],v225 offset:20480
CV v5,v34,v35
MM a[20:23],a[140:143],v[84:87],a[20:23]
EX v36,v36
MM a[20:23],a[136:139],v[80:83],a[20:23]
EX v37,v37
MM a[12:15],a[136:139],v[72:75],a[12:15]
RD a[216:219],v226 offset:20480
CV v6,v36,v37
MM a[12:15],a[140:143],v[76:79],a[12:15]
buffer_load_dwordx4 v230, s[8:11], s64 offen offset:2048 lds
MM a[4:7],a[140:143],v[68:71],a[4:7]
EX v38,v38
MM a[4:7],a[136:139],v[64:67],a[4:7]
EX v39,v39
MM a[32:35],a[144:147],v[64:67],a[32:35]
RD a[220:223],v227 offset:20480
CV v7,v38,v39
MM a[32:35],a[148:151],v[68:71],a[32:35]
EX v40,v40
MM a[40:43],a[148:151],v[76:79],a[40:43]
EX v41,v41
MM a[40:43],a[144:147],v[72:75],a[40:43]
RD a[224:227],v224 offset:24576
CV v12,v40,v41
MM a[48:51],a[144:147],v[80:83],a[48:51]
buffer_load_dwordx4 v231, s[8:11], s64 offen offset:3072 lds
MM a[48:51],a[148:151],v[84:87],a[48:51]
s_add_u32 m0, s65, 114688
EX v42,v42
MM a[56:59],a[148:151],v[92:95],a[56:59]
EX v43,v43
MM a[56:59],a[144:147],v[88:91],a[56:59]
RD a[228:231],v225 offset:24576
CV v13,v42,v43
MM a[60:63],a[152:155],v[88:91],a[60:63]
EX v44,v44
MM a[60:63],a[156:159],v[92:95],a[60:63]
buffer_load_dwordx4 v235, s[12:15], s81 offen offset:0 lds
MM a[52:55],a[156:159],v[84:87],a[52:55]
EX v45,v45
MM a[52:55],a[152:155],v[80:83],a[52:55]
RD a[232:235],v226 offset:24576
CV v14,v44,v45
MM a[44:47],a[152:155],v[72:75],a[44:47]
EX v46,v46
MM a[44:47],a[156:159],v[76:79],a[44:47]
EX v47,v47
MM a[36:39],a[156:159],v[68:71],a[36:39]
RD a[236:239],v227 offset:24576
CV v15,v46,v47
MM a[36:39],a[152:155],v[64:67],a[36:39]
EX v48,v48
MM a[64:67],a[160:163],v[64:67],a[64:67]
buffer_load_dwordx4 v235, s[12:15], s64 offen offset:1024 lds
MM a[64:67],a[164:167],v[68:71],a[64:67]
EX v49,v49
MM a[72:75],a[164:167],v[76:79],a[72:75]
RD a[240:243],v224 offset:28672
CV v20,v48,v49
MM a[72:75],a[160:163],v[72:75],a[72:75]
EX v50,v50
MM a[80:83],a[160:163],v[80:83],a[80:83]
s_add_u32 m0, s91, 143360
s_add_u32 s77, s90, 40960
buffer_load_dwordx4 v253, s[28:31], s77 offen offset:0 lds
MM a[80:83],a[164:167],v[84:87],a[80:83]
EX v51,v51
MM a[88:91],a[164:167],v[92:95],a[88:91]
RD a[244:247],v225 offset:28672
CV v21,v50,v51
MM a[88:91],a[160:163],v[88:91],a[88:91]
EX v52,v52
MM a[92:95],a[168:171],v[88:91],a[92:95]
EX v53,v53
MM a[92:95],a[172:175],v[92:95],a[92:95]
RD a[248:251],v226 offset:28672
CV v22,v52,v53
MM a[84:87],a[172:175],v[84:87],a[84:87]
s_add_u32 m0, s65, 114688
)ASM"
R"ASM(
s_nop 0
buffer_load_dwordx4 v235, s[12:15], s81 offen offset:2048 lds
MM a[84:87],a[168:171],v[80:83],a[84:87]
EX v54,v54
MM a[76:79],a[168:171],v[72:75],a[76:79]
EX v55,v55
MM a[76:79],a[172:175],v[76:79],a[76:79]
RD a[252:255],v227 offset:28672
CV v23,v54,v55
MM a[68:71],a[172:175],v[68:71],a[68:71]
EX v56,v56
MM a[68:71],a[168:171],v[64:67],a[68:71]
EX v57,v57
MM a[96:99],a[176:179],v[64:67],a[96:99]
buffer_load_dwordx4 v235, s[12:15], s64 offen offset:3072 lds
MM a[96:99],a[180:183],v[68:71],a[96:99]
EX v58,v58
MM a[104:107],a[180:183],v[76:79],a[104:107]
EX v59,v59
MM a[104:107],a[176:179],v[72:75],a[104:107]
EX v60,v60
MM a[112:115],a[176:179],v[80:83],a[112:115]
EX v61,v61
MM a[112:115],a[180:183],v[84:87],a[112:115]
EX v62,v62
MM a[120:123],a[180:183],v[92:95],a[120:123]
s_add_u32 m0, s91, 144384
s_add_u32 s77, s90, 51200
buffer_load_dwordx4 v253, s[28:31], s77 offen offset:0 lds
MM a[120:123],a[176:179],v[88:91],a[120:123]
EX v63,v63
MM a[124:127],a[184:187],v[88:91],a[124:127]
v_add_f32 v244, v144, v145
v_add_f32 v245, v148, v149
MM a[124:127],a[188:191],v[92:95],a[124:127]
v_add_f32 v246, v152, v153
v_add_f32 v247, v156, v157
MM a[116:119],a[188:191],v[84:87],a[116:119]
CV v28,v56,v57
CV v29,v58,v59
MM a[116:119],a[184:187],v[80:83],a[116:119]
CV v30,v60,v61
CV v31,v62,v63
MM a[108:111],a[184:187],v[72:75],a[108:111]
MM a[108:111],a[188:191],v[76:79],a[108:111]
MM a[100:103],a[188:191],v[68:71],a[100:103]
MM a[100:103],a[184:187],v[64:67],a[100:103]
v_smfmac_f32_16x16x64_bf16 v[144:147], v[236:239], v[0:7], v234
v_smfmac_f32_16x16x64_bf16 v[148:151], v[236:239], v[8:15], v234
v_smfmac_f32_16x16x64_bf16 v[152:155], v[236:239], v[16:23], v234
v_smfmac_f32_16x16x64_bf16 v[156:159], v[236:239], v[24:31], v234
s_waitcnt lgkmcnt(0)
MM v[64:67],a[192:195],v[160:163],v[128:131]
s_add_u32 s64, s64, 16384
TR a[128:129],v232 offset:0
s_add_u32 s81, s64, 896
MM v[64:67],a[196:199],v[176:179],v[64:67]
TR a[130:131],v232 offset:4096
v_max3_f32 v249, v144, v145, v148
MM v[64:67],a[200:203],v[192:195],v[64:67]
TR a[132:133],v232 offset:8192
v_max3_f32 v249, v249, v149, v152
MM v[64:67],a[204:207],v[208:211],v[64:67]
TR a[134:135],v232 offset:12288
v_max3_f32 v249, v249, v153, v156
MM v[68:71],a[220:223],v[208:211],v[128:131]
TR a[136:137],v233 offset:0
v_max_f32 v249, v249, v157
MM v[68:71],a[216:219],v[192:195],v[68:71]
TR a[138:139],v233 offset:4096
v_cmp_lt_f32 vcc, s71, v249
MM v[68:71],a[212:215],v[176:179],v[68:71]
TR a[140:141],v233 offset:8192
TR a[142:143],v233 offset:12288
MM v[68:71],a[208:211],v[160:163],v[68:71]
s_cbranch_vccnz LSITE20_%=
LBACK20_%=:
MM v[76:79],a[208:211],v[164:167],v[132:135]
v_bfe_u32 v253, v248, 4, 4
v_lshlrev_b32 v253, 2, v253
MM v[76:79],a[212:215],v[180:183],v[76:79]
s_and_b32 s76, s18, 0xffffffc0
EX v64,v64
MM v[76:79],a[216:219],v[196:199],v[76:79]
s_add_u32 s76, s76, 77824
buffer_load_dword v252, v253, s[20:23], s76 offen offset:0 sc1
MM v[76:79],a[220:223],v[212:215],v[76:79]
EX v65,v65
MM v[72:75],a[204:207],v[212:215],v[132:135]
EX v66,v66
MM v[72:75],a[200:203],v[196:199],v[72:75]
TR a[144:145],v232 offset:512
CV v64,v64,v65
MM v[72:75],a[196:199],v[180:183],v[72:75]
EX v67,v67
MM v[72:75],a[192:195],v[164:167],v[72:75]
TR a[146:147],v232 offset:4608
CV v65,v66,v67
MM v[80:83],a[192:195],v[168:171],v[136:139]
EX v68,v68
MM v[80:83],a[196:199],v[184:187],v[80:83]
TR a[148:149],v232 offset:8704
TR a[150:151],v232 offset:12800
MM v[80:83],a[200:203],v[200:203],v[80:83]
EX v69,v69
MM v[80:83],a[204:207],v[216:219],v[80:83]
TR a[152:153],v233 offset:512
CV v66,v68,v69
MM v[84:87],a[220:223],v[216:219],v[136:139]
EX v70,v70
MM v[84:87],a[216:219],v[200:203],v[84:87]
EX v71,v71
MM v[84:87],a[212:215],v[184:187],v[84:87]
TR a[154:155],v233 offset:4608
CV v67,v70,v71
MM v[84:87],a[208:211],v[168:171],v[84:87]
EX v72,v72
MM v[92:95],a[208:211],v[172:175],v[140:143]
TR a[156:157],v233 offset:8704
TR a[158:159],v233 offset:12800
MM v[92:95],a[212:215],v[188:191],v[92:95]
EX v73,v73
MM v[92:95],a[216:219],v[204:207],v[92:95]
TR a[160:161],v232 offset:1024
CV v72,v72,v73
MM v[92:95],a[220:223],v[220:223],v[92:95]
EX v74,v74
MM v[88:91],a[204:207],v[220:223],v[140:143]
EX v75,v75
MM v[88:91],a[200:203],v[204:207],v[88:91]
TR a[162:163],v232 offset:5120
CV v73,v74,v75
MM v[88:91],a[196:199],v[188:191],v[88:91]
EX v76,v76
MM v[88:91],a[192:195],v[172:175],v[88:91]
TR a[164:165],v232 offset:9216
TR a[166:167],v232 offset:13312
MM v[96:99],a[224:227],v[160:163],v[128:131]
EX v77,v77
MM v[96:99],a[228:231],v[176:179],v[96:99]
TR a[168:169],v233 offset:1024
CV v74,v76,v77
MM v[96:99],a[232:235],v[192:195],v[96:99]
EX v78,v78
MM v[96:99],a[236:239],v[208:211],v[96:99]
EX v79,v79
MM v[100:103],a[252:255],v[208:211],v[128:131]
TR a[170:171],v233 offset:5120
CV v75,v78,v79
MM v[100:103],a[248:251],v[192:195],v[100:103]
EX v80,v80
MM v[100:103],a[244:247],v[176:179],v[100:103]
TR a[172:173],v233 offset:9216
TR a[174:175],v233 offset:13312
MM v[100:103],a[240:243],v[160:163],v[100:103]
EX v81,v81
s_nop 0
LPG16_%=:
s_cbranch_execz LTR2_%=
MM v[108:111],a[240:243],v[164:167],v[132:135]
TR a[176:177],v232 offset:1536
CV v80,v80,v81
MM v[108:111],a[244:247],v[180:183],v[108:111]
EX v82,v82
MM v[108:111],a[248:251],v[196:199],v[108:111]
TR a[178:179],v232 offset:5632
TR a[180:181],v232 offset:9728
MM v[108:111],a[252:255],v[212:215],v[108:111]
EX v83,v83
MM v[104:107],a[236:239],v[212:215],v[132:135]
TR a[182:183],v232 offset:13824
CV v81,v82,v83
MM v[104:107],a[232:235],v[196:199],v[104:107]
EX v84,v84
MM v[104:107],a[228:231],v[180:183],v[104:107]
EX v85,v85
MM v[104:107],a[224:227],v[164:167],v[104:107]
TR a[184:185],v233 offset:1536
CV v82,v84,v85
MM v[112:115],a[224:227],v[168:171],v[136:139]
EX v86,v86
MM v[112:115],a[228:231],v[184:187],v[112:115]
TR a[186:187],v233 offset:5632
TR a[188:189],v233 offset:9728
MM v[112:115],a[232:235],v[200:203],v[112:115]
EX v87,v87
MM v[112:115],a[236:239],v[216:219],v[112:115]
TR a[190:191],v233 offset:13824
CV v83,v86,v87
MM v[116:119],a[252:255],v[216:219],v[136:139]
EX v88,v88
MM v[116:119],a[248:251],v[200:203],v[116:119]
EX v89,v89
MM v[116:119],a[244:247],v[184:187],v[116:119]
EX v90,v90
MM v[116:119],a[240:243],v[168:171],v[116:119]
EX v91,v91
MM v[124:127],a[240:243],v[172:175],v[140:143]
EX v92,v92
MM v[124:127],a[244:247],v[188:191],v[124:127]
EX v93,v93
MM v[124:127],a[248:251],v[204:207],v[124:127]
EX v94,v94
MM v[124:127],a[252:255],v[220:223],v[124:127]
EX v95,v95
MM v[120:123],a[236:239],v[220:223],v[140:143]
EX v96,v96
MM v[120:123],a[232:235],v[204:207],v[120:123]
EX v97,v97
MM v[120:123],a[228:231],v[188:191],v[120:123]
s_getpc_b64 s[98:99]
LTA3_%=:
s_min_u32 s73, %[swave], 3
s_lshl_b32 s73, s73, 12
s_add_u32 s73, s73, LPG17_%=-LTA3_%=
s_add_u32 s98, s98, s73
s_addc_u32 s99, s99, 0
s_mov_b64 exec, 0
s_setpc_b64 s[98:99]
LTR3_%=:
s_mov_b64 exec, -1
CV v88,v88,v89
MM v[120:123],a[224:227],v[172:175],v[120:123]
s_waitcnt vmcnt(20) lgkmcnt(0)
s_barrier
MM a[0:3],a[128:131],v[0:3],a[0:3]
s_add_u32 m0, s65, 0
CV v89,v90,v91
CV v90,v92,v93
MM a[0:3],a[132:135],v[4:7],a[0:3]
RD a[192:195],v224 offset:32768
CV v91,v94,v95
MM a[8:11],a[132:135],v[12:15],a[8:11]
buffer_load_dwordx4 v228, s[8:11], s64 offen offset:0 lds
MM a[8:11],a[128:131],v[8:11],a[8:11]
RD a[196:199],v225 offset:32768
CV v68,v96,v97
MM a[16:19],a[128:131],v[16:19],a[16:19]
RD a[200:203],v226 offset:32768
RD a[204:207],v227 offset:32768
MM a[16:19],a[132:135],v[20:23],a[16:19]
buffer_load_dwordx4 v229, s[8:11], s64 offen offset:1024 lds
MM a[24:27],a[132:135],v[28:31],a[24:27]
EX v98,v98
MM a[24:27],a[128:131],v[24:27],a[24:27]
RD a[208:211],v224 offset:36864
RD a[212:215],v225 offset:36864
MM a[28:31],a[136:139],v[24:27],a[28:31]
EX v99,v99
MM a[28:31],a[140:143],v[28:31],a[28:31]
RD a[216:219],v226 offset:36864
CV v69,v98,v99
MM a[20:23],a[140:143],v[20:23],a[20:23]
EX v100,v100
MM a[20:23],a[136:139],v[16:19],a[20:23]
EX v101,v101
MM a[12:15],a[136:139],v[8:11],a[12:15]
RD a[220:223],v227 offset:36864
CV v70,v100,v101
MM a[12:15],a[140:143],v[12:15],a[12:15]
buffer_load_dwordx4 v230, s[8:11], s64 offen offset:2048 lds
MM a[4:7],a[140:143],v[4:7],a[4:7]
EX v102,v102
MM a[4:7],a[136:139],v[0:3],a[4:7]
EX v103,v103
MM a[32:35],a[144:147],v[0:3],a[32:35]
RD a[224:227],v224 offset:40960
CV v71,v102,v103
MM a[32:35],a[148:151],v[4:7],a[32:35]
EX v104,v104
MM a[40:43],a[148:151],v[12:15],a[40:43]
EX v105,v105
MM a[40:43],a[144:147],v[8:11],a[40:43]
RD a[228:231],v225 offset:40960
CV v76,v104,v105
MM a[48:51],a[144:147],v[16:19],a[48:51]
buffer_load_dwordx4 v231, s[8:11], s64 offen offset:3072 lds
MM a[48:51],a[148:151],v[20:23],a[48:51]
s_add_u32 m0, s65, 65536
EX v106,v106
MM a[56:59],a[148:151],v[28:31],a[56:59]
EX v107,v107
MM a[56:59],a[144:147],v[24:27],a[56:59]
RD a[232:235],v226 offset:40960
CV v77,v106,v107
MM a[60:63],a[152:155],v[24:27],a[60:63]
EX v108,v108
MM a[60:63],a[156:159],v[28:31],a[60:63]
buffer_load_dwordx4 v235, s[12:15], s81 offen offset:0 lds
MM a[52:55],a[156:159],v[20:23],a[52:55]
EX v109,v109
MM a[52:55],a[152:155],v[16:19],a[52:55]
RD a[236:239],v227 offset:40960
CV v78,v108,v109
MM a[44:47],a[152:155],v[8:11],a[44:47]
EX v110,v110
MM a[44:47],a[156:159],v[12:15],a[44:47]
EX v111,v111
MM a[36:39],a[156:159],v[4:7],a[36:39]
RD a[240:243],v224 offset:45056
CV v79,v110,v111
MM a[36:39],a[152:155],v[0:3],a[36:39]
EX v112,v112
MM a[64:67],a[160:163],v[0:3],a[64:67]
buffer_load_dwordx4 v235, s[12:15], s64 offen offset:1024 lds
MM a[64:67],a[164:167],v[4:7],a[64:67]
EX v113,v113
MM a[72:75],a[164:167],v[12:15],a[72:75]
RD a[244:247],v225 offset:45056
CV v84,v112,v113
MM a[72:75],a[160:163],v[8:11],a[72:75]
EX v114,v114
MM a[80:83],a[160:163],v[16:19],a[80:83]
EX v115,v115
MM a[80:83],a[164:167],v[20:23],a[80:83]
RD a[248:251],v226 offset:45056
CV v85,v114,v115
MM a[88:91],a[164:167],v[28:31],a[88:91]
EX v116,v116
MM a[88:91],a[160:163],v[24:27],a[88:91]
buffer_load_dwordx4 v235, s[12:15], s81 offen offset:2048 lds
MM a[92:95],a[168:171],v[24:27],a[92:95]
EX v117,v117
MM a[92:95],a[172:175],v[28:31],a[92:95]
RD a[252:255],v227 offset:45056
CV v86,v116,v117
MM a[84:87],a[172:175],v[20:23],a[84:87]
EX v118,v118
MM a[84:87],a[168:171],v[16:19],a[84:87]
EX v119,v119
MM a[76:79],a[168:171],v[8:11],a[76:79]
CV v87,v118,v119
MM a[76:79],a[172:175],v[12:15],a[76:79]
EX v120,v120
MM a[68:71],a[172:175],v[4:7],a[68:71]
buffer_load_dwordx4 v235, s[12:15], s64 offen offset:3072 lds
MM a[68:71],a[168:171],v[0:3],a[68:71]
EX v121,v121
MM a[96:99],a[176:179],v[0:3],a[96:99]
EX v122,v122
MM a[96:99],a[180:183],v[4:7],a[96:99]
EX v123,v123
MM a[104:107],a[180:183],v[12:15],a[104:107]
EX v124,v124
MM a[104:107],a[176:179],v[8:11],a[104:107]
EX v125,v125
MM a[112:115],a[176:179],v[16:19],a[112:115]
EX v126,v126
MM a[112:115],a[180:183],v[20:23],a[112:115]
EX v127,v127
MM a[120:123],a[180:183],v[28:31],a[120:123]
s_waitcnt vmcnt(8)
v_cmp_ne_u32 vcc, s69, v252
s_cbranch_vccnz LRFG_%=
LRFGb_%=:
MM a[120:123],a[176:179],v[24:27],a[120:123]
v_lshrrev_b32 v253, 8, v248
v_lshlrev_b32 v253, 10, v253
MM a[124:127],a[184:187],v[24:27],a[124:127]
v_add_f32 v244, v144, v145
v_add_f32 v245, v148, v149
MM a[124:127],a[188:191],v[28:31],a[124:127]
v_add_f32 v246, v152, v153
v_add_f32 v247, v156, v157
v_and_b32 v252, 240, v248
MM a[116:119],a[188:191],v[20:23],a[116:119]
v_add_u32 v253, v253, v252
CV v92,v120,v121
MM a[116:119],a[184:187],v[16:19],a[116:119]
s_lshr_b32 s76, s18, 6
CV v93,v122,v123
CV v94,v124,v125
MM a[108:111],a[184:187],v[8:11],a[108:111]
s_lshl_b32 s76, s76, 13
s_bfe_u32 s90, s18, 131076
CV v95,v126,v127
MM a[108:111],a[188:191],v[12:15],a[108:111]
s_lshl_b32 s90, s90, 8
s_add_u32 s76, s76, s90
s_add_u32 s76, s76, 22183936
MM a[100:103],a[188:191],v[4:7],a[100:103]
s_lshl_b32 s90, %[swave], 11
s_add_u32 s90, s90, %[slds]
MM a[100:103],a[184:187],v[0:3],a[100:103]
s_add_u32 m0, s90, 131072
s_nop 0
buffer_load_dwordx4 v253, s[20:23], s76 offen offset:0 sc1 lds
v_smfmac_f32_16x16x64_bf16 v[144:147], v[236:239], v[64:71], v234
v_smfmac_f32_16x16x64_bf16 v[148:151], v[236:239], v[72:79], v234
)ASM"
R"ASM(
v_smfmac_f32_16x16x64_bf16 v[152:155], v[236:239], v[80:87], v234
s_add_u32 m0, s90, 132096
s_add_u32 s76, s76, 4096
buffer_load_dwordx4 v253, s[20:23], s76 offen offset:0 sc1 lds
v_smfmac_f32_16x16x64_bf16 v[156:159], v[236:239], v[88:95], v234
s_waitcnt lgkmcnt(0)
s_add_u32 s67, s95, 3072
s_mov_b32 s70, 3
s_cmp_ge_u32 s16, 90
s_cbranch_scc1 LLAP_%=
MM v[0:3],a[192:195],v[160:163],v[128:131]
s_add_u32 s64, s64, 16384
TR a[128:129],v232 offset:16384
s_add_u32 s81, s64, 896
MM v[0:3],a[196:199],v[176:179],v[0:3]
TR a[130:131],v232 offset:20480
v_max3_f32 v249, v144, v145, v148
MM v[0:3],a[200:203],v[192:195],v[0:3]
TR a[132:133],v232 offset:24576
v_max3_f32 v249, v249, v149, v152
MM v[0:3],a[204:207],v[208:211],v[0:3]
TR a[134:135],v232 offset:28672
v_max3_f32 v249, v249, v153, v156
MM v[4:7],a[220:223],v[208:211],v[128:131]
TR a[136:137],v233 offset:16384
v_max_f32 v249, v249, v157
MM v[4:7],a[216:219],v[192:195],v[4:7]
TR a[138:139],v233 offset:20480
v_cmp_lt_f32 vcc, s71, v249
MM v[4:7],a[212:215],v[176:179],v[4:7]
TR a[140:141],v233 offset:24576
TR a[142:143],v233 offset:28672
MM v[4:7],a[208:211],v[160:163],v[4:7]
s_cbranch_vccnz LSITE21_%=
LBACK21_%=:
MM v[12:15],a[208:211],v[164:167],v[132:135]
EX v0,v0
MM v[12:15],a[212:215],v[180:183],v[12:15]
EX v1,v1
MM v[12:15],a[216:219],v[196:199],v[12:15]
TR a[144:145],v232 offset:16896
CV v0,v0,v1
MM v[12:15],a[220:223],v[212:215],v[12:15]
EX v2,v2
MM v[8:11],a[204:207],v[212:215],v[132:135]
EX v3,v3
MM v[8:11],a[200:203],v[196:199],v[8:11]
TR a[146:147],v232 offset:20992
CV v1,v2,v3
MM v[8:11],a[196:199],v[180:183],v[8:11]
EX v4,v4
MM v[8:11],a[192:195],v[164:167],v[8:11]
TR a[148:149],v232 offset:25088
TR a[150:151],v232 offset:29184
MM v[16:19],a[192:195],v[168:171],v[136:139]
EX v5,v5
MM v[16:19],a[196:199],v[184:187],v[16:19]
TR a[152:153],v233 offset:16896
CV v2,v4,v5
MM v[16:19],a[200:203],v[200:203],v[16:19]
EX v6,v6
MM v[16:19],a[204:207],v[216:219],v[16:19]
TR a[154:155],v233 offset:20992
TR a[156:157],v233 offset:25088
MM v[20:23],a[220:223],v[216:219],v[136:139]
s_add_u32 s87, s95, 3072
buffer_store_dword v243, v248, s[20:23], s87 offen offset:12 sc1
MM v[20:23],a[216:219],v[200:203],v[20:23]
EX v7,v7
MM v[20:23],a[212:215],v[184:187],v[20:23]
TR a[158:159],v233 offset:29184
CV v3,v6,v7
MM v[20:23],a[208:211],v[168:171],v[20:23]
EX v8,v8
MM v[28:31],a[208:211],v[172:175],v[140:143]
EX v9,v9
MM v[28:31],a[212:215],v[188:191],v[28:31]
TR a[160:161],v232 offset:17408
CV v8,v8,v9
MM v[28:31],a[216:219],v[204:207],v[28:31]
EX v10,v10
MM v[28:31],a[220:223],v[220:223],v[28:31]
EX v11,v11
MM v[24:27],a[204:207],v[220:223],v[140:143]
TR a[162:163],v232 offset:21504
CV v9,v10,v11
MM v[24:27],a[200:203],v[204:207],v[24:27]
TR a[164:165],v232 offset:25600
TR a[166:167],v232 offset:29696
MM v[24:27],a[196:199],v[188:191],v[24:27]
EX v12,v12
MM v[24:27],a[192:195],v[172:175],v[24:27]
EX v13,v13
MM v[32:35],a[224:227],v[160:163],v[128:131]
TR a[168:169],v233 offset:17408
CV v10,v12,v13
MM v[32:35],a[228:231],v[176:179],v[32:35]
EX v14,v14
MM v[32:35],a[232:235],v[192:195],v[32:35]
TR a[170:171],v233 offset:21504
TR a[172:173],v233 offset:25600
MM v[32:35],a[236:239],v[208:211],v[32:35]
EX v15,v15
MM v[36:39],a[252:255],v[208:211],v[128:131]
TR a[174:175],v233 offset:29696
CV v11,v14,v15
MM v[36:39],a[248:251],v[192:195],v[36:39]
EX v16,v16
MM v[36:39],a[244:247],v[176:179],v[36:39]
EX v17,v17
MM v[36:39],a[240:243],v[160:163],v[36:39]
TR a[176:177],v232 offset:17920
CV v16,v16,v17
MM v[44:47],a[240:243],v[164:167],v[132:135]
EX v18,v18
MM v[44:47],a[244:247],v[180:183],v[44:47]
TR a[178:179],v232 offset:22016
TR a[180:181],v232 offset:26112
MM v[44:47],a[248:251],v[196:199],v[44:47]
EX v19,v19
MM v[44:47],a[252:255],v[212:215],v[44:47]
TR a[182:183],v232 offset:30208
CV v17,v18,v19
MM v[40:43],a[236:239],v[212:215],v[132:135]
EX v20,v20
MM v[40:43],a[232:235],v[196:199],v[40:43]
EX v21,v21
MM v[40:43],a[228:231],v[180:183],v[40:43]
TR a[184:185],v233 offset:17920
CV v18,v20,v21
MM v[40:43],a[224:227],v[164:167],v[40:43]
EX v22,v22
MM v[48:51],a[224:227],v[168:171],v[136:139]
TR a[186:187],v233 offset:22016
TR a[188:189],v233 offset:26112
MM v[48:51],a[228:231],v[184:187],v[48:51]
EX v23,v23
MM v[48:51],a[232:235],v[200:203],v[48:51]
TR a[190:191],v233 offset:30208
CV v19,v22,v23
MM v[48:51],a[236:239],v[216:219],v[48:51]
EX v24,v24
MM v[52:55],a[252:255],v[216:219],v[136:139]
EX v25,v25
MM v[52:55],a[248:251],v[200:203],v[52:55]
EX v26,v26
MM v[52:55],a[244:247],v[184:187],v[52:55]
EX v27,v27
MM v[52:55],a[240:243],v[168:171],v[52:55]
EX v28,v28
MM v[60:63],a[240:243],v[172:175],v[140:143]
EX v29,v29
MM v[60:63],a[244:247],v[188:191],v[60:63]
EX v30,v30
MM v[60:63],a[248:251],v[204:207],v[60:63]
EX v31,v31
MM v[60:63],a[252:255],v[220:223],v[60:63]
EX v32,v32
MM v[56:59],a[236:239],v[220:223],v[140:143]
EX v33,v33
MM v[56:59],a[232:235],v[204:207],v[56:59]
EX v34,v34
MM v[56:59],a[228:231],v[188:191],v[56:59]
EX v35,v35
MM v[56:59],a[224:227],v[172:175],v[56:59]
s_waitcnt lgkmcnt(0)
s_barrier
MM a[0:3],a[128:131],v[64:67],a[0:3]
s_add_u32 m0, s65, 16384
CV v24,v24,v25
CV v25,v26,v27
MM a[0:3],a[132:135],v[68:71],a[0:3]
RD a[192:195],v224 offset:49152
CV v26,v28,v29
MM a[8:11],a[132:135],v[76:79],a[8:11]
RD a[196:199],v225 offset:49152
CV v27,v30,v31
MM a[8:11],a[128:131],v[72:75],a[8:11]
buffer_load_dwordx4 v228, s[8:11], s64 offen offset:0 lds
MM a[16:19],a[128:131],v[80:83],a[16:19]
RD a[200:203],v226 offset:49152
CV v4,v32,v33
MM a[16:19],a[132:135],v[84:87],a[16:19]
RD a[204:207],v227 offset:49152
CV v5,v34,v35
MM a[24:27],a[132:135],v[92:95],a[24:27]
buffer_load_dwordx4 v229, s[8:11], s64 offen offset:1024 lds
MM a[24:27],a[128:131],v[88:91],a[24:27]
RD a[208:211],v224 offset:53248
RD a[212:215],v225 offset:53248
MM a[28:31],a[136:139],v[88:91],a[28:31]
buffer_load_dwordx4 v230, s[8:11], s64 offen offset:2048 lds
MM a[28:31],a[140:143],v[92:95],a[28:31]
EX v36,v36
MM a[20:23],a[140:143],v[84:87],a[20:23]
EX v37,v37
MM a[20:23],a[136:139],v[80:83],a[20:23]
RD a[216:219],v226 offset:53248
CV v6,v36,v37
MM a[12:15],a[136:139],v[72:75],a[12:15]
EX v38,v38
MM a[12:15],a[140:143],v[76:79],a[12:15]
RD a[220:223],v227 offset:53248
RD a[224:227],v224 offset:57344
MM a[4:7],a[140:143],v[68:71],a[4:7]
EX v39,v39
MM a[4:7],a[136:139],v[64:67],a[4:7]
RD a[228:231],v225 offset:57344
CV v7,v38,v39
MM a[32:35],a[144:147],v[64:67],a[32:35]
EX v40,v40
MM a[32:35],a[148:151],v[68:71],a[32:35]
buffer_load_dwordx4 v231, s[8:11], s64 offen offset:3072 lds
MM a[40:43],a[148:151],v[76:79],a[40:43]
s_add_u32 m0, s65, 81920
EX v41,v41
MM a[40:43],a[144:147],v[72:75],a[40:43]
RD a[232:235],v226 offset:57344
CV v12,v40,v41
MM a[48:51],a[144:147],v[80:83],a[48:51]
EX v42,v42
MM a[48:51],a[148:151],v[84:87],a[48:51]
EX v43,v43
MM a[56:59],a[148:151],v[92:95],a[56:59]
RD a[236:239],v227 offset:57344
CV v13,v42,v43
MM a[56:59],a[144:147],v[88:91],a[56:59]
EX v44,v44
MM a[60:63],a[152:155],v[88:91],a[60:63]
buffer_load_dwordx4 v235, s[12:15], s81 offen offset:0 lds
MM a[60:63],a[156:159],v[92:95],a[60:63]
EX v45,v45
MM a[52:55],a[156:159],v[84:87],a[52:55]
RD a[240:243],v224 offset:61440
CV v14,v44,v45
MM a[52:55],a[152:155],v[80:83],a[52:55]
EX v46,v46
MM a[44:47],a[152:155],v[72:75],a[44:47]
EX v47,v47
MM a[44:47],a[156:159],v[76:79],a[44:47]
RD a[244:247],v225 offset:61440
CV v15,v46,v47
MM a[36:39],a[156:159],v[68:71],a[36:39]
buffer_load_dwordx4 v235, s[12:15], s64 offen offset:1024 lds
MM a[36:39],a[152:155],v[64:67],a[36:39]
EX v48,v48
MM a[64:67],a[160:163],v[64:67],a[64:67]
EX v49,v49
MM a[64:67],a[164:167],v[68:71],a[64:67]
RD a[248:251],v226 offset:61440
CV v20,v48,v49
MM a[72:75],a[164:167],v[76:79],a[72:75]
EX v50,v50
MM a[72:75],a[160:163],v[72:75],a[72:75]
EX v51,v51
MM a[80:83],a[160:163],v[80:83],a[80:83]
RD a[252:255],v227 offset:61440
CV v21,v50,v51
MM a[80:83],a[164:167],v[84:87],a[80:83]
buffer_load_dwordx4 v235, s[12:15], s81 offen offset:2048 lds
MM a[88:91],a[164:167],v[92:95],a[88:91]
EX v52,v52
MM a[88:91],a[160:163],v[88:91],a[88:91]
EX v53,v53
MM a[92:95],a[168:171],v[88:91],a[92:95]
EX v54,v54
MM a[92:95],a[172:175],v[92:95],a[92:95]
buffer_load_dwordx4 v235, s[12:15], s64 offen offset:3072 lds
MM a[84:87],a[172:175],v[84:87],a[84:87]
EX v55,v55
MM a[84:87],a[168:171],v[80:83],a[84:87]
EX v56,v56
MM a[76:79],a[168:171],v[72:75],a[76:79]
EX v57,v57
MM a[76:79],a[172:175],v[76:79],a[76:79]
EX v58,v58
MM a[68:71],a[172:175],v[68:71],a[68:71]
EX v59,v59
MM a[68:71],a[168:171],v[64:67],a[68:71]
EX v60,v60
MM a[96:99],a[176:179],v[64:67],a[96:99]
EX v61,v61
MM a[96:99],a[180:183],v[68:71],a[96:99]
EX v62,v62
MM a[104:107],a[180:183],v[76:79],a[104:107]
EX v63,v63
MM a[104:107],a[176:179],v[72:75],a[104:107]
CV v22,v52,v53
CV v23,v54,v55
MM a[112:115],a[176:179],v[80:83],a[112:115]
CV v28,v56,v57
CV v29,v58,v59
MM a[112:115],a[180:183],v[84:87],a[112:115]
CV v30,v60,v61
CV v31,v62,v63
MM a[120:123],a[180:183],v[92:95],a[120:123]
MM a[120:123],a[176:179],v[88:91],a[120:123]
MM a[124:127],a[184:187],v[88:91],a[124:127]
v_add_f32 v244, v144, v145
v_add_f32 v245, v148, v149
MM a[124:127],a[188:191],v[92:95],a[124:127]
v_add_f32 v246, v152, v153
v_add_f32 v247, v156, v157
MM a[116:119],a[188:191],v[84:87],a[116:119]
MM a[116:119],a[184:187],v[80:83],a[116:119]
MM a[108:111],a[184:187],v[72:75],a[108:111]
MM a[108:111],a[188:191],v[76:79],a[108:111]
MM a[100:103],a[188:191],v[68:71],a[100:103]
MM a[100:103],a[184:187],v[64:67],a[100:103]
v_smfmac_f32_16x16x64_bf16 v[144:147], v[236:239], v[0:7], v234
v_smfmac_f32_16x16x64_bf16 v[148:151], v[236:239], v[8:15], v234
v_smfmac_f32_16x16x64_bf16 v[152:155], v[236:239], v[16:23], v234
v_smfmac_f32_16x16x64_bf16 v[156:159], v[236:239], v[24:31], v234
s_waitcnt lgkmcnt(0)
MM v[64:67],a[192:195],v[160:163],v[128:131]
s_add_u32 s64, s64, 16384
TR a[128:129],v232 offset:32768
s_add_u32 s81, s64, 896
MM v[64:67],a[196:199],v[176:179],v[64:67]
TR a[130:131],v232 offset:36864
v_max3_f32 v249, v144, v145, v148
MM v[64:67],a[200:203],v[192:195],v[64:67]
TR a[132:133],v232 offset:40960
v_max3_f32 v249, v249, v149, v152
MM v[64:67],a[204:207],v[208:211],v[64:67]
TR a[134:135],v232 offset:45056
v_max3_f32 v249, v249, v153, v156
MM v[68:71],a[220:223],v[208:211],v[128:131]
TR a[136:137],v233 offset:32768
v_max_f32 v249, v249, v157
MM v[68:71],a[216:219],v[192:195],v[68:71]
TR a[138:139],v233 offset:36864
v_cmp_lt_f32 vcc, s71, v249
MM v[68:71],a[212:215],v[176:179],v[68:71]
TR a[140:141],v233 offset:40960
TR a[142:143],v233 offset:45056
MM v[68:71],a[208:211],v[160:163],v[68:71]
s_cbranch_vccnz LSITE22_%=
LBACK22_%=:
MM v[76:79],a[208:211],v[164:167],v[132:135]
v_lshrrev_b32 v43, 7, v248
v_bfe_u32 v42, v248, 6, 1
MM v[76:79],a[212:215],v[180:183],v[76:79]
v_lshl_add_u32 v40, v42, 3, v43
v_lshlrev_b32 v40, 4, v40
MM v[76:79],a[216:219],v[196:199],v[76:79]
v_bfe_u32 v42, v248, 4, 2
v_lshl_add_u32 v40, v42, 2, v40
MM v[76:79],a[220:223],v[212:215],v[76:79]
s_lshl_b32 s76, %[swave], 11
EX v64,v64
MM v[72:75],a[204:207],v[212:215],v[132:135]
s_add_u32 s76, s76, %[slds]
EX v65,v65
MM v[72:75],a[200:203],v[196:199],v[72:75]
s_add_u32 s76, s76, 131072
v_add_u32 v40, s76, v40
CV v64,v64,v65
MM v[72:75],a[196:199],v[180:183],v[72:75]
s_waitcnt vmcnt(9)
ds_read_b32 v32, v40 offset:0
ds_read_b32 v36, v40 offset:256
MM v[72:75],a[192:195],v[164:167],v[72:75]
ds_read_b32 v33, v40 offset:512
ds_read_b32 v37, v40 offset:768
MM v[80:83],a[192:195],v[168:171],v[136:139]
ds_read_b32 v34, v40 offset:1024
s_nop 0
LPG17_%=:
s_cbranch_execz LTR3_%=
ds_read_b32 v38, v40 offset:1280
EX v66,v66
MM v[80:83],a[196:199],v[184:187],v[80:83]
ds_read_b32 v35, v40 offset:1536
ds_read_b32 v39, v40 offset:1792
EX v67,v67
MM v[80:83],a[200:203],v[200:203],v[80:83]
v_mul_u32_u24 v41, 3456, v43
v_bfe_u32 v42, v248, 4, 3
MM v[80:83],a[204:207],v[216:219],v[80:83]
TR a[144:145],v232 offset:33280
v_lshl_add_u32 v41, v42, 1, v41
EX v68,v68
MM v[84:87],a[220:223],v[216:219],v[136:139]
s_add_u32 s76, s18, 21626880
EX v69,v69
MM v[84:87],a[216:219],v[200:203],v[84:87]
)ASM"
R"ASM(
TR a[146:147],v232 offset:37376
TR a[148:149],v232 offset:41472
MM v[84:87],a[212:215],v[184:187],v[84:87]
EX v70,v70
MM v[84:87],a[208:211],v[168:171],v[84:87]
TR a[150:151],v232 offset:45568
CV v65,v66,v67
MM v[92:95],a[208:211],v[172:175],v[140:143]
EX v71,v71
MM v[92:95],a[212:215],v[188:191],v[92:95]
EX v72,v72
MM v[92:95],a[216:219],v[204:207],v[92:95]
TR a[152:153],v233 offset:33280
CV v66,v68,v69
MM v[92:95],a[220:223],v[220:223],v[92:95]
EX v73,v73
MM v[88:91],a[204:207],v[220:223],v[140:143]
s_waitcnt lgkmcnt(5)
v_add_f32 v32, v32, v33
v_add_f32 v36, v36, v37
MM v[88:91],a[200:203],v[204:207],v[88:91]
v_add_f32 v34, v34, v35
v_add_f32 v38, v38, v39
MM v[88:91],a[196:199],v[188:191],v[88:91]
TR a[154:155],v233 offset:37376
v_add_f32 v32, v32, v34
v_add_f32 v36, v36, v38
MM v[88:91],a[192:195],v[172:175],v[88:91]
CV v42,v32,v36
v_lshlrev_b32 v32, 16, v42
EX v74,v74
MM v[96:99],a[224:227],v[160:163],v[128:131]
TR a[156:157],v233 offset:41472
v_and_b32 v36, 0xffff0000, v42
v_mul_f32 v42, 0xbfb8aa3b, v32
EX v75,v75
MM v[96:99],a[228:231],v[176:179],v[96:99]
EX v42,v42
MM v[96:99],a[232:235],v[192:195],v[96:99]
TR a[158:159],v233 offset:45568
v_add_f32 v42, 1.0, v42
EX v76,v76
MM v[96:99],a[236:239],v[208:211],v[96:99]
v_rcp_f32 v42, v42
MM v[100:103],a[252:255],v[208:211],v[128:131]
TR a[160:161],v232 offset:33792
v_mul_f32 v42, v32, v42
CV v42,v42,v42
EX v77,v77
CV v67,v70,v71
MM v[100:103],a[248:251],v[192:195],v[100:103]
v_lshlrev_b32 v42, 16, v42
v_mul_f32 v32, v42, v36
MM v[100:103],a[244:247],v[176:179],v[100:103]
TR a[162:163],v232 offset:37888
CV v43,v32,v32
EX v78,v78
MM v[100:103],a[240:243],v[160:163],v[100:103]
buffer_store_short v43, v41, s[20:23], s76 offen offset:0 sc1
s_nop 1
TR a[164:165],v232 offset:41984
MM v[108:111],a[240:243],v[164:167],v[132:135]
EX v79,v79
CV v72,v72,v73
MM v[108:111],a[244:247],v[180:183],v[108:111]
TR a[166:167],v232 offset:46080
CV v73,v74,v75
MM v[108:111],a[248:251],v[196:199],v[108:111]
EX v80,v80
MM v[108:111],a[252:255],v[212:215],v[108:111]
TR a[168:169],v233 offset:33792
TR a[170:171],v233 offset:37888
EX v81,v81
MM v[104:107],a[236:239],v[212:215],v[132:135]
TR a[172:173],v233 offset:41984
CV v74,v76,v77
MM v[104:107],a[232:235],v[196:199],v[104:107]
EX v82,v82
MM v[104:107],a[228:231],v[180:183],v[104:107]
EX v83,v83
MM v[104:107],a[224:227],v[164:167],v[104:107]
TR a[174:175],v233 offset:46080
CV v75,v78,v79
MM v[112:115],a[224:227],v[168:171],v[136:139]
EX v84,v84
MM v[112:115],a[228:231],v[184:187],v[112:115]
TR a[176:177],v232 offset:34304
TR a[178:179],v232 offset:38400
MM v[112:115],a[232:235],v[200:203],v[112:115]
EX v85,v85
MM v[112:115],a[236:239],v[216:219],v[112:115]
TR a[180:181],v232 offset:42496
CV v80,v80,v81
MM v[116:119],a[252:255],v[216:219],v[136:139]
EX v86,v86
MM v[116:119],a[248:251],v[200:203],v[116:119]
TR a[182:183],v232 offset:46592
CV v81,v82,v83
MM v[116:119],a[244:247],v[184:187],v[116:119]
EX v87,v87
MM v[116:119],a[240:243],v[168:171],v[116:119]
TR a[184:185],v233 offset:34304
CV v82,v84,v85
MM v[124:127],a[240:243],v[172:175],v[140:143]
EX v88,v88
MM v[124:127],a[244:247],v[188:191],v[124:127]
TR a[186:187],v233 offset:38400
TR a[188:189],v233 offset:42496
MM v[124:127],a[248:251],v[204:207],v[124:127]
TR a[190:191],v233 offset:46592
CV v83,v86,v87
EX v89,v89
MM v[124:127],a[252:255],v[220:223],v[124:127]
EX v90,v90
MM v[120:123],a[236:239],v[220:223],v[140:143]
EX v91,v91
MM v[120:123],a[232:235],v[204:207],v[120:123]
EX v92,v92
MM v[120:123],a[228:231],v[188:191],v[120:123]
EX v93,v93
MM v[120:123],a[224:227],v[172:175],v[120:123]
s_waitcnt lgkmcnt(0)
s_barrier
MM a[0:3],a[128:131],v[0:3],a[0:3]
s_add_u32 m0, s65, 32768
CV v88,v88,v89
CV v89,v90,v91
MM a[0:3],a[132:135],v[4:7],a[0:3]
EX v94,v94
MM a[8:11],a[132:135],v[12:15],a[8:11]
RD a[192:195],v224 offset:0
CV v90,v92,v93
MM a[8:11],a[128:131],v[8:11],a[8:11]
EX v95,v95
MM a[16:19],a[128:131],v[16:19],a[16:19]
RD a[196:199],v225 offset:0
CV v91,v94,v95
MM a[16:19],a[132:135],v[20:23],a[16:19]
EX v96,v96
MM a[24:27],a[132:135],v[28:31],a[24:27]
buffer_load_dwordx4 v228, s[8:11], s64 offen offset:0 lds
MM a[24:27],a[128:131],v[24:27],a[24:27]
EX v97,v97
MM a[28:31],a[136:139],v[24:27],a[28:31]
EX v98,v98
MM a[28:31],a[140:143],v[28:31],a[28:31]
RD a[200:203],v226 offset:0
CV v68,v96,v97
MM a[20:23],a[140:143],v[20:23],a[20:23]
EX v99,v99
MM a[20:23],a[136:139],v[16:19],a[20:23]
RD a[204:207],v227 offset:0
CV v69,v98,v99
MM a[12:15],a[136:139],v[8:11],a[12:15]
EX v100,v100
MM a[12:15],a[140:143],v[12:15],a[12:15]
buffer_load_dwordx4 v229, s[8:11], s64 offen offset:1024 lds
MM a[4:7],a[140:143],v[4:7],a[4:7]
EX v101,v101
MM a[4:7],a[136:139],v[0:3],a[4:7]
RD a[208:211],v224 offset:4096
CV v70,v100,v101
MM a[32:35],a[144:147],v[0:3],a[32:35]
EX v102,v102
MM a[32:35],a[148:151],v[4:7],a[32:35]
EX v103,v103
MM a[40:43],a[148:151],v[12:15],a[40:43]
RD a[212:215],v225 offset:4096
CV v71,v102,v103
MM a[40:43],a[144:147],v[8:11],a[40:43]
EX v104,v104
MM a[48:51],a[144:147],v[16:19],a[48:51]
buffer_load_dwordx4 v230, s[8:11], s64 offen offset:2048 lds
MM a[48:51],a[148:151],v[20:23],a[48:51]
EX v105,v105
MM a[56:59],a[148:151],v[28:31],a[56:59]
RD a[216:219],v226 offset:4096
CV v76,v104,v105
MM a[56:59],a[144:147],v[24:27],a[56:59]
EX v106,v106
MM a[60:63],a[152:155],v[24:27],a[60:63]
EX v107,v107
MM a[60:63],a[156:159],v[28:31],a[60:63]
RD a[220:223],v227 offset:4096
CV v77,v106,v107
MM a[52:55],a[156:159],v[20:23],a[52:55]
EX v108,v108
MM a[52:55],a[152:155],v[16:19],a[52:55]
buffer_load_dwordx4 v231, s[8:11], s64 offen offset:3072 lds
MM a[44:47],a[152:155],v[8:11],a[44:47]
s_add_u32 m0, s65, 98304
EX v109,v109
MM a[44:47],a[156:159],v[12:15],a[44:47]
RD a[224:227],v224 offset:8192
CV v78,v108,v109
MM a[36:39],a[156:159],v[4:7],a[36:39]
EX v110,v110
MM a[36:39],a[152:155],v[0:3],a[36:39]
EX v111,v111
MM a[64:67],a[160:163],v[0:3],a[64:67]
RD a[228:231],v225 offset:8192
CV v79,v110,v111
MM a[64:67],a[164:167],v[4:7],a[64:67]
EX v112,v112
MM a[72:75],a[164:167],v[12:15],a[72:75]
buffer_load_dwordx4 v235, s[12:15], s81 offen offset:0 lds
MM a[72:75],a[160:163],v[8:11],a[72:75]
EX v113,v113
MM a[80:83],a[160:163],v[16:19],a[80:83]
RD a[232:235],v226 offset:8192
CV v84,v112,v113
MM a[80:83],a[164:167],v[20:23],a[80:83]
EX v114,v114
MM a[88:91],a[164:167],v[28:31],a[88:91]
EX v115,v115
MM a[88:91],a[160:163],v[24:27],a[88:91]
RD a[236:239],v227 offset:8192
CV v85,v114,v115
MM a[92:95],a[168:171],v[24:27],a[92:95]
EX v116,v116
MM a[92:95],a[172:175],v[28:31],a[92:95]
buffer_load_dwordx4 v235, s[12:15], s64 offen offset:1024 lds
MM a[84:87],a[172:175],v[20:23],a[84:87]
EX v117,v117
MM a[84:87],a[168:171],v[16:19],a[84:87]
RD a[240:243],v224 offset:12288
CV v86,v116,v117
MM a[76:79],a[168:171],v[8:11],a[76:79]
EX v118,v118
MM a[76:79],a[172:175],v[12:15],a[76:79]
EX v119,v119
MM a[68:71],a[172:175],v[4:7],a[68:71]
RD a[244:247],v225 offset:12288
CV v87,v118,v119
MM a[68:71],a[168:171],v[0:3],a[68:71]
buffer_load_dwordx4 v235, s[12:15], s81 offen offset:2048 lds
MM a[96:99],a[176:179],v[0:3],a[96:99]
EX v120,v120
MM a[96:99],a[180:183],v[4:7],a[96:99]
EX v121,v121
MM a[104:107],a[180:183],v[12:15],a[104:107]
RD a[248:251],v226 offset:12288
CV v92,v120,v121
MM a[104:107],a[176:179],v[8:11],a[104:107]
EX v122,v122
MM a[112:115],a[176:179],v[16:19],a[112:115]
EX v123,v123
MM a[112:115],a[180:183],v[20:23],a[112:115]
RD a[252:255],v227 offset:12288
CV v93,v122,v123
MM a[120:123],a[180:183],v[28:31],a[120:123]
buffer_load_dwordx4 v235, s[12:15], s64 offen offset:3072 lds
MM a[120:123],a[176:179],v[24:27],a[120:123]
EX v124,v124
MM a[124:127],a[184:187],v[24:27],a[124:127]
v_add_f32 v244, v144, v145
v_add_f32 v245, v148, v149
MM a[124:127],a[188:191],v[28:31],a[124:127]
v_add_f32 v246, v152, v153
v_add_f32 v247, v156, v157
MM a[116:119],a[188:191],v[20:23],a[116:119]
EX v125,v125
MM a[116:119],a[184:187],v[16:19],a[116:119]
EX v126,v126
MM a[108:111],a[184:187],v[8:11],a[108:111]
EX v127,v127
MM a[108:111],a[188:191],v[12:15],a[108:111]
CV v94,v124,v125
CV v95,v126,v127
MM a[100:103],a[188:191],v[4:7],a[100:103]
MM a[100:103],a[184:187],v[0:3],a[100:103]
v_smfmac_f32_16x16x64_bf16 v[144:147], v[236:239], v[64:71], v234
v_smfmac_f32_16x16x64_bf16 v[148:151], v[236:239], v[72:79], v234
v_smfmac_f32_16x16x64_bf16 v[152:155], v[236:239], v[80:87], v234
v_smfmac_f32_16x16x64_bf16 v[156:159], v[236:239], v[88:95], v234
s_waitcnt lgkmcnt(0)
MM v[0:3],a[192:195],v[160:163],v[128:131]
s_add_u32 s64, s64, 16384
TR a[128:129],v232 offset:49152
s_add_u32 s81, s64, 896
MM v[0:3],a[196:199],v[176:179],v[0:3]
TR a[130:131],v232 offset:53248
v_max3_f32 v249, v144, v145, v148
MM v[0:3],a[200:203],v[192:195],v[0:3]
TR a[132:133],v232 offset:57344
v_max3_f32 v249, v249, v149, v152
MM v[0:3],a[204:207],v[208:211],v[0:3]
TR a[134:135],v232 offset:61440
v_max3_f32 v249, v249, v153, v156
MM v[4:7],a[220:223],v[208:211],v[128:131]
TR a[136:137],v233 offset:49152
v_max_f32 v249, v249, v157
MM v[4:7],a[216:219],v[192:195],v[4:7]
TR a[138:139],v233 offset:53248
v_cmp_lt_f32 vcc, s71, v249
MM v[4:7],a[212:215],v[176:179],v[4:7]
TR a[140:141],v233 offset:57344
TR a[142:143],v233 offset:61440
MM v[4:7],a[208:211],v[160:163],v[4:7]
s_cbranch_vccnz LSITE23_%=
LBACK23_%=:
MM v[12:15],a[208:211],v[164:167],v[132:135]
EX v0,v0
MM v[12:15],a[212:215],v[180:183],v[12:15]
EX v1,v1
MM v[12:15],a[216:219],v[196:199],v[12:15]
TR a[144:145],v232 offset:49664
CV v0,v0,v1
MM v[12:15],a[220:223],v[212:215],v[12:15]
EX v2,v2
MM v[8:11],a[204:207],v[212:215],v[132:135]
EX v3,v3
MM v[8:11],a[200:203],v[196:199],v[8:11]
TR a[146:147],v232 offset:53760
CV v1,v2,v3
MM v[8:11],a[196:199],v[180:183],v[8:11]
EX v4,v4
MM v[8:11],a[192:195],v[164:167],v[8:11]
TR a[148:149],v232 offset:57856
TR a[150:151],v232 offset:61952
MM v[16:19],a[192:195],v[168:171],v[136:139]
EX v5,v5
MM v[16:19],a[196:199],v[184:187],v[16:19]
TR a[152:153],v233 offset:49664
CV v2,v4,v5
MM v[16:19],a[200:203],v[200:203],v[16:19]
EX v6,v6
MM v[16:19],a[204:207],v[216:219],v[16:19]
TR a[154:155],v233 offset:53760
TR a[156:157],v233 offset:57856
MM v[20:23],a[220:223],v[216:219],v[136:139]
EX v7,v7
MM v[20:23],a[216:219],v[200:203],v[20:23]
TR a[158:159],v233 offset:61952
CV v3,v6,v7
MM v[20:23],a[212:215],v[184:187],v[20:23]
EX v8,v8
MM v[20:23],a[208:211],v[168:171],v[20:23]
EX v9,v9
MM v[28:31],a[208:211],v[172:175],v[140:143]
TR a[160:161],v232 offset:50176
CV v8,v8,v9
MM v[28:31],a[212:215],v[188:191],v[28:31]
EX v10,v10
MM v[28:31],a[216:219],v[204:207],v[28:31]
TR a[162:163],v232 offset:54272
TR a[164:165],v232 offset:58368
MM v[28:31],a[220:223],v[220:223],v[28:31]
EX v11,v11
MM v[24:27],a[204:207],v[220:223],v[140:143]
TR a[166:167],v232 offset:62464
CV v9,v10,v11
MM v[24:27],a[200:203],v[204:207],v[24:27]
EX v12,v12
MM v[24:27],a[196:199],v[188:191],v[24:27]
EX v13,v13
MM v[24:27],a[192:195],v[172:175],v[24:27]
TR a[168:169],v233 offset:50176
CV v10,v12,v13
MM v[32:35],a[224:227],v[160:163],v[128:131]
EX v14,v14
MM v[32:35],a[228:231],v[176:179],v[32:35]
TR a[170:171],v233 offset:54272
TR a[172:173],v233 offset:58368
MM v[32:35],a[232:235],v[192:195],v[32:35]
EX v15,v15
MM v[32:35],a[236:239],v[208:211],v[32:35]
TR a[174:175],v233 offset:62464
CV v11,v14,v15
MM v[36:39],a[252:255],v[208:211],v[128:131]
EX v16,v16
MM v[36:39],a[248:251],v[192:195],v[36:39]
EX v17,v17
MM v[36:39],a[244:247],v[176:179],v[36:39]
TR a[176:177],v232 offset:50688
CV v16,v16,v17
MM v[36:39],a[240:243],v[160:163],v[36:39]
EX v18,v18
MM v[44:47],a[240:243],v[164:167],v[132:135]
TR a[178:179],v232 offset:54784
TR a[180:181],v232 offset:58880
MM v[44:47],a[244:247],v[180:183],v[44:47]
EX v19,v19
MM v[44:47],a[248:251],v[196:199],v[44:47]
TR a[182:183],v232 offset:62976
CV v17,v18,v19
MM v[44:47],a[252:255],v[212:215],v[44:47]
EX v20,v20
MM v[40:43],a[236:239],v[212:215],v[132:135]
TR a[184:185],v233 offset:50688
TR a[186:187],v233 offset:54784
MM v[40:43],a[232:235],v[196:199],v[40:43]
EX v21,v21
MM v[40:43],a[228:231],v[180:183],v[40:43]
TR a[188:189],v233 offset:58880
)ASM"
R"ASM(
CV v18,v20,v21
MM v[40:43],a[224:227],v[164:167],v[40:43]
EX v22,v22
MM v[48:51],a[224:227],v[168:171],v[136:139]
EX v23,v23
MM v[48:51],a[228:231],v[184:187],v[48:51]
TR a[190:191],v233 offset:62976
CV v19,v22,v23
MM v[48:51],a[232:235],v[200:203],v[48:51]
EX v24,v24
MM v[48:51],a[236:239],v[216:219],v[48:51]
EX v25,v25
MM v[52:55],a[252:255],v[216:219],v[136:139]
EX v26,v26
MM v[52:55],a[248:251],v[200:203],v[52:55]
EX v27,v27
MM v[52:55],a[244:247],v[184:187],v[52:55]
EX v28,v28
MM v[52:55],a[240:243],v[168:171],v[52:55]
EX v29,v29
MM v[60:63],a[240:243],v[172:175],v[140:143]
EX v30,v30
MM v[60:63],a[244:247],v[188:191],v[60:63]
EX v31,v31
MM v[60:63],a[248:251],v[204:207],v[60:63]
EX v32,v32
MM v[60:63],a[252:255],v[220:223],v[60:63]
EX v33,v33
MM v[56:59],a[236:239],v[220:223],v[140:143]
EX v34,v34
MM v[56:59],a[232:235],v[204:207],v[56:59]
EX v35,v35
MM v[56:59],a[228:231],v[188:191],v[56:59]
EX v36,v36
MM v[56:59],a[224:227],v[172:175],v[56:59]
s_waitcnt vmcnt(13) lgkmcnt(0)
s_barrier
MM a[0:3],a[128:131],v[64:67],a[0:3]
s_add_u32 m0, s65, 49152
CV v24,v24,v25
CV v25,v26,v27
MM a[0:3],a[132:135],v[68:71],a[0:3]
RD a[192:195],v224 offset:16384
CV v26,v28,v29
MM a[8:11],a[132:135],v[76:79],a[8:11]
RD a[196:199],v225 offset:16384
CV v27,v30,v31
MM a[8:11],a[128:131],v[72:75],a[8:11]
buffer_load_dwordx4 v228, s[8:11], s64 offen offset:0 lds
MM a[16:19],a[128:131],v[80:83],a[16:19]
RD a[200:203],v226 offset:16384
CV v4,v32,v33
MM a[16:19],a[132:135],v[84:87],a[16:19]
RD a[204:207],v227 offset:16384
CV v5,v34,v35
MM a[24:27],a[132:135],v[92:95],a[24:27]
buffer_load_dwordx4 v229, s[8:11], s64 offen offset:1024 lds
MM a[24:27],a[128:131],v[88:91],a[24:27]
RD a[208:211],v224 offset:20480
RD a[212:215],v225 offset:20480
MM a[28:31],a[136:139],v[88:91],a[28:31]
buffer_load_dwordx4 v230, s[8:11], s64 offen offset:2048 lds
MM a[28:31],a[140:143],v[92:95],a[28:31]
RD a[216:219],v226 offset:20480
RD a[220:223],v227 offset:20480
MM a[20:23],a[140:143],v[84:87],a[20:23]
EX v37,v37
MM a[20:23],a[136:139],v[80:83],a[20:23]
RD a[224:227],v224 offset:24576
CV v6,v36,v37
MM a[12:15],a[136:139],v[72:75],a[12:15]
EX v38,v38
MM a[12:15],a[140:143],v[76:79],a[12:15]
buffer_load_dwordx4 v231, s[8:11], s64 offen offset:3072 lds
MM a[4:7],a[140:143],v[68:71],a[4:7]
s_add_u32 m0, s65, 114688
EX v39,v39
MM a[4:7],a[136:139],v[64:67],a[4:7]
RD a[228:231],v225 offset:24576
CV v7,v38,v39
MM a[32:35],a[144:147],v[64:67],a[32:35]
EX v40,v40
MM a[32:35],a[148:151],v[68:71],a[32:35]
EX v41,v41
MM a[40:43],a[148:151],v[76:79],a[40:43]
RD a[232:235],v226 offset:24576
CV v12,v40,v41
MM a[40:43],a[144:147],v[72:75],a[40:43]
EX v42,v42
MM a[48:51],a[144:147],v[80:83],a[48:51]
buffer_load_dwordx4 v235, s[12:15], s81 offen offset:0 lds
MM a[48:51],a[148:151],v[84:87],a[48:51]
EX v43,v43
MM a[56:59],a[148:151],v[92:95],a[56:59]
RD a[236:239],v227 offset:24576
CV v13,v42,v43
MM a[56:59],a[144:147],v[88:91],a[56:59]
EX v44,v44
MM a[60:63],a[152:155],v[88:91],a[60:63]
EX v45,v45
MM a[60:63],a[156:159],v[92:95],a[60:63]
RD a[240:243],v224 offset:28672
CV v14,v44,v45
MM a[52:55],a[156:159],v[84:87],a[52:55]
EX v46,v46
MM a[52:55],a[152:155],v[80:83],a[52:55]
buffer_load_dwordx4 v235, s[12:15], s64 offen offset:1024 lds
MM a[44:47],a[152:155],v[72:75],a[44:47]
EX v47,v47
MM a[44:47],a[156:159],v[76:79],a[44:47]
RD a[244:247],v225 offset:28672
CV v15,v46,v47
MM a[36:39],a[156:159],v[68:71],a[36:39]
EX v48,v48
MM a[36:39],a[152:155],v[64:67],a[36:39]
EX v49,v49
MM a[64:67],a[160:163],v[64:67],a[64:67]
RD a[248:251],v226 offset:28672
CV v20,v48,v49
MM a[64:67],a[164:167],v[68:71],a[64:67]
EX v50,v50
MM a[72:75],a[164:167],v[76:79],a[72:75]
buffer_load_dwordx4 v235, s[12:15], s81 offen offset:2048 lds
MM a[72:75],a[160:163],v[72:75],a[72:75]
EX v51,v51
MM a[80:83],a[160:163],v[80:83],a[80:83]
RD a[252:255],v227 offset:28672
CV v21,v50,v51
MM a[80:83],a[164:167],v[84:87],a[80:83]
EX v52,v52
MM a[88:91],a[164:167],v[92:95],a[88:91]
EX v53,v53
MM a[88:91],a[160:163],v[88:91],a[88:91]
buffer_load_dwordx4 v235, s[12:15], s64 offen offset:3072 lds
MM a[92:95],a[168:171],v[88:91],a[92:95]
EX v54,v54
MM a[92:95],a[172:175],v[92:95],a[92:95]
EX v55,v55
MM a[84:87],a[172:175],v[84:87],a[84:87]
EX v56,v56
MM a[84:87],a[168:171],v[80:83],a[84:87]
EX v57,v57
MM a[76:79],a[168:171],v[72:75],a[76:79]
EX v58,v58
MM a[76:79],a[172:175],v[76:79],a[76:79]
EX v59,v59
MM a[68:71],a[172:175],v[68:71],a[68:71]
EX v60,v60
MM a[68:71],a[168:171],v[64:67],a[68:71]
EX v61,v61
MM a[96:99],a[176:179],v[64:67],a[96:99]
EX v62,v62
MM a[96:99],a[180:183],v[68:71],a[96:99]
EX v63,v63
MM a[104:107],a[180:183],v[76:79],a[104:107]
CV v22,v52,v53
CV v23,v54,v55
MM a[104:107],a[176:179],v[72:75],a[104:107]
CV v28,v56,v57
CV v29,v58,v59
MM a[112:115],a[176:179],v[80:83],a[112:115]
CV v30,v60,v61
CV v31,v62,v63
MM a[112:115],a[180:183],v[84:87],a[112:115]
MM a[120:123],a[180:183],v[92:95],a[120:123]
s_lshr_b32 s91, s18, 2
s_add_u32 s91, s91, 69632
s_waitcnt vmcnt(16)
MM a[120:123],a[176:179],v[88:91],a[120:123]
v_mov_b32 v250, s69
s_mov_b64 exec, 1
buffer_store_dword v250, off, s[20:23], s91 offset:0 sc1
s_mov_b64 exec, -1
MM a[124:127],a[184:187],v[88:91],a[124:127]
v_add_f32 v244, v144, v145
v_add_f32 v245, v148, v149
MM a[124:127],a[188:191],v[92:95],a[124:127]
v_add_f32 v246, v152, v153
v_add_f32 v247, v156, v157
v_lshlrev_b32 v96, 3, v248
MM a[116:119],a[188:191],v[84:87],a[116:119]
v_min_u32 v96, 4992, v96
LPG18_%=:
s_cbranch_execz LTR3_%=
s_sub_u32 s91, 5242880, s26
MM a[116:119],a[184:187],v[80:83],a[116:119]
s_sub_u32 s60, s24, s91
s_subb_u32 s61, s25, 0
s_mov_b32 s62, 5242880
MM a[108:111],a[184:187],v[72:75],a[108:111]
s_mov_b32 s63, 131072
s_lshl_b32 s90, s16, 2
s_add_u32 s90, s90, %[swave]
MM a[108:111],a[188:191],v[76:79],a[108:111]
s_mul_i32 s90, s90, 10240
s_add_u32 s90, s90, 0
s_add_u32 s91, %[slds], 135168
MM a[100:103],a[188:191],v[68:71],a[100:103]
s_mov_b32 m0, s91
s_nop 0
buffer_load_dword v96, s[60:63], s90 offen offset:0 lds
MM a[100:103],a[184:187],v[64:67],a[100:103]
s_lshr_b32 s77, %[sb], 3
s_lshl_b32 s77, s77, 2
s_add_u32 s77, s77, %[swave]
v_smfmac_f32_16x16x64_bf16 v[144:147], v[236:239], v[0:7], v234
s_lshl_b32 s77, s77, 9
s_add_u32 s77, s77, 0
v_smfmac_f32_16x16x64_bf16 v[148:151], v[236:239], v[8:15], v234
v_lshrrev_b32 v253, 1, v248
v_smfmac_f32_16x16x64_bf16 v[152:155], v[236:239], v[16:23], v234
v_add_u32 v253, s77, v253
v_smfmac_f32_16x16x64_bf16 v[156:159], v[236:239], v[24:31], v234
s_waitcnt lgkmcnt(0)
s_mov_b32 s77, LKEND_ka-LPD1_%=-16
v_min_u32 v253, s77, v253
s_getpc_b64 s[98:99]
LPD1_%=:
global_load_dword v253, v253, s[98:99]
MM v[64:67],a[192:195],v[160:163],v[128:131]
s_add_u32 s64, s64, 16384
TR a[128:129],v232 offset:0
s_add_u32 s81, s64, 896
MM v[64:67],a[196:199],v[176:179],v[64:67]
TR a[130:131],v232 offset:4096
v_max3_f32 v249, v144, v145, v148
MM v[64:67],a[200:203],v[192:195],v[64:67]
TR a[132:133],v232 offset:8192
v_max3_f32 v249, v249, v149, v152
MM v[64:67],a[204:207],v[208:211],v[64:67]
TR a[134:135],v232 offset:12288
v_max3_f32 v249, v249, v153, v156
MM v[68:71],a[220:223],v[208:211],v[128:131]
TR a[136:137],v233 offset:0
v_max_f32 v249, v249, v157
MM v[68:71],a[216:219],v[192:195],v[68:71]
TR a[138:139],v233 offset:4096
v_cmp_lt_f32 vcc, s71, v249
MM v[68:71],a[212:215],v[176:179],v[68:71]
TR a[140:141],v233 offset:8192
TR a[142:143],v233 offset:12288
MM v[68:71],a[208:211],v[160:163],v[68:71]
s_cbranch_vccnz LSITE24_%=
LBACK24_%=:
MM v[76:79],a[208:211],v[164:167],v[132:135]
EX v64,v64
MM v[76:79],a[212:215],v[180:183],v[76:79]
EX v65,v65
MM v[76:79],a[216:219],v[196:199],v[76:79]
TR a[144:145],v232 offset:512
CV v64,v64,v65
MM v[76:79],a[220:223],v[212:215],v[76:79]
EX v66,v66
MM v[72:75],a[204:207],v[212:215],v[132:135]
EX v67,v67
MM v[72:75],a[200:203],v[196:199],v[72:75]
TR a[146:147],v232 offset:4608
CV v65,v66,v67
MM v[72:75],a[196:199],v[180:183],v[72:75]
EX v68,v68
MM v[72:75],a[192:195],v[164:167],v[72:75]
TR a[148:149],v232 offset:8704
TR a[150:151],v232 offset:12800
MM v[80:83],a[192:195],v[168:171],v[136:139]
EX v69,v69
MM v[80:83],a[196:199],v[184:187],v[80:83]
TR a[152:153],v233 offset:512
CV v66,v68,v69
MM v[80:83],a[200:203],v[200:203],v[80:83]
EX v70,v70
MM v[80:83],a[204:207],v[216:219],v[80:83]
TR a[154:155],v233 offset:4608
TR a[156:157],v233 offset:8704
MM v[84:87],a[220:223],v[216:219],v[136:139]
s_add_u32 s87, s95, 4096
buffer_store_dword v243, v248, s[20:23], s87 offen offset:12 sc1
MM v[84:87],a[216:219],v[200:203],v[84:87]
EX v71,v71
MM v[84:87],a[212:215],v[184:187],v[84:87]
TR a[158:159],v233 offset:12800
CV v67,v70,v71
MM v[84:87],a[208:211],v[168:171],v[84:87]
EX v72,v72
MM v[92:95],a[208:211],v[172:175],v[140:143]
EX v73,v73
MM v[92:95],a[212:215],v[188:191],v[92:95]
TR a[160:161],v232 offset:1024
CV v72,v72,v73
MM v[92:95],a[216:219],v[204:207],v[92:95]
EX v74,v74
MM v[92:95],a[220:223],v[220:223],v[92:95]
EX v75,v75
MM v[88:91],a[204:207],v[220:223],v[140:143]
TR a[162:163],v232 offset:5120
CV v73,v74,v75
MM v[88:91],a[200:203],v[204:207],v[88:91]
TR a[164:165],v232 offset:9216
TR a[166:167],v232 offset:13312
MM v[88:91],a[196:199],v[188:191],v[88:91]
EX v76,v76
MM v[88:91],a[192:195],v[172:175],v[88:91]
EX v77,v77
MM v[96:99],a[224:227],v[160:163],v[128:131]
TR a[168:169],v233 offset:1024
CV v74,v76,v77
MM v[96:99],a[228:231],v[176:179],v[96:99]
EX v78,v78
MM v[96:99],a[232:235],v[192:195],v[96:99]
TR a[170:171],v233 offset:5120
TR a[172:173],v233 offset:9216
MM v[96:99],a[236:239],v[208:211],v[96:99]
EX v79,v79
MM v[100:103],a[252:255],v[208:211],v[128:131]
TR a[174:175],v233 offset:13312
CV v75,v78,v79
MM v[100:103],a[248:251],v[192:195],v[100:103]
EX v80,v80
MM v[100:103],a[244:247],v[176:179],v[100:103]
EX v81,v81
MM v[100:103],a[240:243],v[160:163],v[100:103]
TR a[176:177],v232 offset:1536
CV v80,v80,v81
MM v[108:111],a[240:243],v[164:167],v[132:135]
EX v82,v82
MM v[108:111],a[244:247],v[180:183],v[108:111]
TR a[178:179],v232 offset:5632
TR a[180:181],v232 offset:9728
MM v[108:111],a[248:251],v[196:199],v[108:111]
EX v83,v83
MM v[108:111],a[252:255],v[212:215],v[108:111]
TR a[182:183],v232 offset:13824
CV v81,v82,v83
MM v[104:107],a[236:239],v[212:215],v[132:135]
EX v84,v84
MM v[104:107],a[232:235],v[196:199],v[104:107]
EX v85,v85
MM v[104:107],a[228:231],v[180:183],v[104:107]
TR a[184:185],v233 offset:1536
CV v82,v84,v85
MM v[104:107],a[224:227],v[164:167],v[104:107]
EX v86,v86
MM v[112:115],a[224:227],v[168:171],v[136:139]
TR a[186:187],v233 offset:5632
TR a[188:189],v233 offset:9728
MM v[112:115],a[228:231],v[184:187],v[112:115]
EX v87,v87
MM v[112:115],a[232:235],v[200:203],v[112:115]
TR a[190:191],v233 offset:13824
CV v83,v86,v87
MM v[112:115],a[236:239],v[216:219],v[112:115]
EX v88,v88
MM v[116:119],a[252:255],v[216:219],v[136:139]
EX v89,v89
MM v[116:119],a[248:251],v[200:203],v[116:119]
EX v90,v90
MM v[116:119],a[244:247],v[184:187],v[116:119]
EX v91,v91
MM v[116:119],a[240:243],v[168:171],v[116:119]
EX v92,v92
MM v[124:127],a[240:243],v[172:175],v[140:143]
EX v93,v93
MM v[124:127],a[244:247],v[188:191],v[124:127]
EX v94,v94
MM v[124:127],a[248:251],v[204:207],v[124:127]
EX v95,v95
MM v[124:127],a[252:255],v[220:223],v[124:127]
EX v96,v96
MM v[120:123],a[236:239],v[220:223],v[140:143]
EX v97,v97
MM v[120:123],a[232:235],v[204:207],v[120:123]
EX v98,v98
MM v[120:123],a[228:231],v[188:191],v[120:123]
EX v99,v99
MM v[120:123],a[224:227],v[172:175],v[120:123]
s_waitcnt vmcnt(16) lgkmcnt(0)
s_barrier
MM a[0:3],a[128:131],v[0:3],a[0:3]
s_add_u32 m0, s65, 0
CV v88,v88,v89
CV v89,v90,v91
MM a[0:3],a[132:135],v[4:7],a[0:3]
RD a[192:195],v224 offset:32768
CV v90,v92,v93
MM a[8:11],a[132:135],v[12:15],a[8:11]
RD a[196:199],v225 offset:32768
CV v91,v94,v95
MM a[8:11],a[128:131],v[8:11],a[8:11]
buffer_load_dwordx4 v228, s[8:11], s64 offen offset:0 lds
MM a[16:19],a[128:131],v[16:19],a[16:19]
RD a[200:203],v226 offset:32768
CV v68,v96,v97
MM a[16:19],a[132:135],v[20:23],a[16:19]
RD a[204:207],v227 offset:32768
CV v69,v98,v99
MM a[24:27],a[132:135],v[28:31],a[24:27]
)ASM"
R"ASM(
buffer_load_dwordx4 v229, s[8:11], s64 offen offset:1024 lds
MM a[24:27],a[128:131],v[24:27],a[24:27]
RD a[208:211],v224 offset:36864
RD a[212:215],v225 offset:36864
MM a[28:31],a[136:139],v[24:27],a[28:31]
buffer_load_dwordx4 v230, s[8:11], s64 offen offset:2048 lds
MM a[28:31],a[140:143],v[28:31],a[28:31]
EX v100,v100
MM a[20:23],a[140:143],v[20:23],a[20:23]
EX v101,v101
MM a[20:23],a[136:139],v[16:19],a[20:23]
RD a[216:219],v226 offset:36864
CV v70,v100,v101
MM a[12:15],a[136:139],v[8:11],a[12:15]
EX v102,v102
MM a[12:15],a[140:143],v[12:15],a[12:15]
RD a[220:223],v227 offset:36864
RD a[224:227],v224 offset:40960
MM a[4:7],a[140:143],v[4:7],a[4:7]
EX v103,v103
MM a[4:7],a[136:139],v[0:3],a[4:7]
RD a[228:231],v225 offset:40960
CV v71,v102,v103
MM a[32:35],a[144:147],v[0:3],a[32:35]
EX v104,v104
MM a[32:35],a[148:151],v[4:7],a[32:35]
buffer_load_dwordx4 v231, s[8:11], s64 offen offset:3072 lds
MM a[40:43],a[148:151],v[12:15],a[40:43]
s_add_u32 m0, s65, 65536
EX v105,v105
MM a[40:43],a[144:147],v[8:11],a[40:43]
RD a[232:235],v226 offset:40960
CV v76,v104,v105
MM a[48:51],a[144:147],v[16:19],a[48:51]
EX v106,v106
MM a[48:51],a[148:151],v[20:23],a[48:51]
EX v107,v107
MM a[56:59],a[148:151],v[28:31],a[56:59]
RD a[236:239],v227 offset:40960
CV v77,v106,v107
MM a[56:59],a[144:147],v[24:27],a[56:59]
EX v108,v108
MM a[60:63],a[152:155],v[24:27],a[60:63]
buffer_load_dwordx4 v235, s[12:15], s81 offen offset:0 lds
MM a[60:63],a[156:159],v[28:31],a[60:63]
EX v109,v109
MM a[52:55],a[156:159],v[20:23],a[52:55]
RD a[240:243],v224 offset:45056
CV v78,v108,v109
MM a[52:55],a[152:155],v[16:19],a[52:55]
EX v110,v110
MM a[44:47],a[152:155],v[8:11],a[44:47]
EX v111,v111
MM a[44:47],a[156:159],v[12:15],a[44:47]
RD a[244:247],v225 offset:45056
CV v79,v110,v111
MM a[36:39],a[156:159],v[4:7],a[36:39]
buffer_load_dwordx4 v235, s[12:15], s64 offen offset:1024 lds
MM a[36:39],a[152:155],v[0:3],a[36:39]
EX v112,v112
MM a[64:67],a[160:163],v[0:3],a[64:67]
EX v113,v113
MM a[64:67],a[164:167],v[4:7],a[64:67]
RD a[248:251],v226 offset:45056
CV v84,v112,v113
MM a[72:75],a[164:167],v[12:15],a[72:75]
EX v114,v114
MM a[72:75],a[160:163],v[8:11],a[72:75]
EX v115,v115
MM a[80:83],a[160:163],v[16:19],a[80:83]
RD a[252:255],v227 offset:45056
CV v85,v114,v115
MM a[80:83],a[164:167],v[20:23],a[80:83]
buffer_load_dwordx4 v235, s[12:15], s81 offen offset:2048 lds
MM a[88:91],a[164:167],v[28:31],a[88:91]
EX v116,v116
MM a[88:91],a[160:163],v[24:27],a[88:91]
EX v117,v117
MM a[92:95],a[168:171],v[24:27],a[92:95]
EX v118,v118
MM a[92:95],a[172:175],v[28:31],a[92:95]
buffer_load_dwordx4 v235, s[12:15], s64 offen offset:3072 lds
MM a[84:87],a[172:175],v[20:23],a[84:87]
EX v119,v119
MM a[84:87],a[168:171],v[16:19],a[84:87]
EX v120,v120
MM a[76:79],a[168:171],v[8:11],a[76:79]
EX v121,v121
MM a[76:79],a[172:175],v[12:15],a[76:79]
EX v122,v122
MM a[68:71],a[172:175],v[4:7],a[68:71]
EX v123,v123
MM a[68:71],a[168:171],v[0:3],a[68:71]
EX v124,v124
MM a[96:99],a[176:179],v[0:3],a[96:99]
EX v125,v125
MM a[96:99],a[180:183],v[4:7],a[96:99]
EX v126,v126
MM a[104:107],a[180:183],v[12:15],a[104:107]
EX v127,v127
MM a[104:107],a[176:179],v[8:11],a[104:107]
CV v86,v116,v117
CV v87,v118,v119
MM a[112:115],a[176:179],v[16:19],a[112:115]
CV v92,v120,v121
CV v93,v122,v123
MM a[112:115],a[180:183],v[20:23],a[112:115]
CV v94,v124,v125
CV v95,v126,v127
MM a[120:123],a[180:183],v[28:31],a[120:123]
MM a[120:123],a[176:179],v[24:27],a[120:123]
MM a[124:127],a[184:187],v[24:27],a[124:127]
v_add_f32 v244, v144, v145
v_add_f32 v245, v148, v149
MM a[124:127],a[188:191],v[28:31],a[124:127]
v_add_f32 v246, v152, v153
v_add_f32 v247, v156, v157
MM a[116:119],a[188:191],v[20:23],a[116:119]
MM a[116:119],a[184:187],v[16:19],a[116:119]
MM a[108:111],a[184:187],v[8:11],a[108:111]
MM a[108:111],a[188:191],v[12:15],a[108:111]
MM a[100:103],a[188:191],v[4:7],a[100:103]
MM a[100:103],a[184:187],v[0:3],a[100:103]
v_smfmac_f32_16x16x64_bf16 v[144:147], v[236:239], v[64:71], v234
v_smfmac_f32_16x16x64_bf16 v[148:151], v[236:239], v[72:79], v234
v_smfmac_f32_16x16x64_bf16 v[152:155], v[236:239], v[80:87], v234
v_smfmac_f32_16x16x64_bf16 v[156:159], v[236:239], v[88:95], v234
s_waitcnt lgkmcnt(0)
MM v[0:3],a[192:195],v[160:163],v[128:131]
s_add_u32 s64, s64, 16384
TR a[128:129],v232 offset:16384
s_add_u32 s81, s64, 896
MM v[0:3],a[196:199],v[176:179],v[0:3]
TR a[130:131],v232 offset:20480
v_max3_f32 v249, v144, v145, v148
MM v[0:3],a[200:203],v[192:195],v[0:3]
TR a[132:133],v232 offset:24576
v_max3_f32 v249, v249, v149, v152
MM v[0:3],a[204:207],v[208:211],v[0:3]
TR a[134:135],v232 offset:28672
v_max3_f32 v249, v249, v153, v156
MM v[4:7],a[220:223],v[208:211],v[128:131]
TR a[136:137],v233 offset:16384
v_max_f32 v249, v249, v157
MM v[4:7],a[216:219],v[192:195],v[4:7]
TR a[138:139],v233 offset:20480
v_cmp_lt_f32 vcc, s71, v249
MM v[4:7],a[212:215],v[176:179],v[4:7]
TR a[140:141],v233 offset:24576
TR a[142:143],v233 offset:28672
MM v[4:7],a[208:211],v[160:163],v[4:7]
s_cbranch_vccnz LSITE25_%=
LBACK25_%=:
MM v[12:15],a[208:211],v[164:167],v[132:135]
s_waitcnt vmcnt(9)
EX v0,v0
MM v[12:15],a[212:215],v[180:183],v[12:15]
s_mul_i32 s76, s46, 96
EX v1,v1
MM v[12:15],a[216:219],v[196:199],v[12:15]
s_add_u32 s76, s76, 69632
v_lshrrev_b32 v253, 2, v248
v_min_u32 v253, 92, v253
MM v[12:15],a[220:223],v[212:215],v[12:15]
buffer_load_dword v252, v253, s[20:23], s76 offen offset:0 sc1
MM v[8:11],a[204:207],v[212:215],v[132:135]
EX v2,v2
MM v[8:11],a[200:203],v[196:199],v[8:11]
TR a[144:145],v232 offset:16896
CV v0,v0,v1
MM v[8:11],a[196:199],v[180:183],v[8:11]
EX v3,v3
MM v[8:11],a[192:195],v[164:167],v[8:11]
TR a[146:147],v232 offset:20992
CV v1,v2,v3
MM v[16:19],a[192:195],v[168:171],v[136:139]
EX v4,v4
MM v[16:19],a[196:199],v[184:187],v[16:19]
TR a[148:149],v232 offset:25088
TR a[150:151],v232 offset:29184
MM v[16:19],a[200:203],v[200:203],v[16:19]
EX v5,v5
MM v[16:19],a[204:207],v[216:219],v[16:19]
TR a[152:153],v233 offset:16896
CV v2,v4,v5
MM v[20:23],a[220:223],v[216:219],v[136:139]
EX v6,v6
MM v[20:23],a[216:219],v[200:203],v[20:23]
EX v7,v7
MM v[20:23],a[212:215],v[184:187],v[20:23]
TR a[154:155],v233 offset:20992
CV v3,v6,v7
MM v[20:23],a[208:211],v[168:171],v[20:23]
EX v8,v8
MM v[28:31],a[208:211],v[172:175],v[140:143]
TR a[156:157],v233 offset:25088
TR a[158:159],v233 offset:29184
MM v[28:31],a[212:215],v[188:191],v[28:31]
EX v9,v9
MM v[28:31],a[216:219],v[204:207],v[28:31]
TR a[160:161],v232 offset:17408
CV v8,v8,v9
MM v[28:31],a[220:223],v[220:223],v[28:31]
EX v10,v10
MM v[24:27],a[204:207],v[220:223],v[140:143]
EX v11,v11
MM v[24:27],a[200:203],v[204:207],v[24:27]
TR a[162:163],v232 offset:21504
CV v9,v10,v11
MM v[24:27],a[196:199],v[188:191],v[24:27]
EX v12,v12
MM v[24:27],a[192:195],v[172:175],v[24:27]
TR a[164:165],v232 offset:25600
TR a[166:167],v232 offset:29696
MM v[32:35],a[224:227],v[160:163],v[128:131]
EX v13,v13
MM v[32:35],a[228:231],v[176:179],v[32:35]
TR a[168:169],v233 offset:17408
CV v10,v12,v13
MM v[32:35],a[232:235],v[192:195],v[32:35]
EX v14,v14
MM v[32:35],a[236:239],v[208:211],v[32:35]
EX v15,v15
MM v[36:39],a[252:255],v[208:211],v[128:131]
TR a[170:171],v233 offset:21504
CV v11,v14,v15
MM v[36:39],a[248:251],v[192:195],v[36:39]
EX v16,v16
MM v[36:39],a[244:247],v[176:179],v[36:39]
TR a[172:173],v233 offset:25600
TR a[174:175],v233 offset:29696
MM v[36:39],a[240:243],v[160:163],v[36:39]
EX v17,v17
MM v[44:47],a[240:243],v[164:167],v[132:135]
TR a[176:177],v232 offset:17920
CV v16,v16,v17
MM v[44:47],a[244:247],v[180:183],v[44:47]
EX v18,v18
MM v[44:47],a[248:251],v[196:199],v[44:47]
TR a[178:179],v232 offset:22016
TR a[180:181],v232 offset:26112
MM v[44:47],a[252:255],v[212:215],v[44:47]
EX v19,v19
MM v[40:43],a[236:239],v[212:215],v[132:135]
TR a[182:183],v232 offset:30208
CV v17,v18,v19
MM v[40:43],a[232:235],v[196:199],v[40:43]
EX v20,v20
MM v[40:43],a[228:231],v[180:183],v[40:43]
EX v21,v21
MM v[40:43],a[224:227],v[164:167],v[40:43]
TR a[184:185],v233 offset:17920
CV v18,v20,v21
MM v[48:51],a[224:227],v[168:171],v[136:139]
EX v22,v22
MM v[48:51],a[228:231],v[184:187],v[48:51]
TR a[186:187],v233 offset:22016
TR a[188:189],v233 offset:26112
MM v[48:51],a[232:235],v[200:203],v[48:51]
EX v23,v23
MM v[48:51],a[236:239],v[216:219],v[48:51]
TR a[190:191],v233 offset:30208
CV v19,v22,v23
MM v[52:55],a[252:255],v[216:219],v[136:139]
EX v24,v24
MM v[52:55],a[248:251],v[200:203],v[52:55]
EX v25,v25
MM v[52:55],a[244:247],v[184:187],v[52:55]
EX v26,v26
MM v[52:55],a[240:243],v[168:171],v[52:55]
EX v27,v27
MM v[60:63],a[240:243],v[172:175],v[140:143]
EX v28,v28
MM v[60:63],a[244:247],v[188:191],v[60:63]
EX v29,v29
MM v[60:63],a[248:251],v[204:207],v[60:63]
EX v30,v30
MM v[60:63],a[252:255],v[220:223],v[60:63]
EX v31,v31
MM v[56:59],a[236:239],v[220:223],v[140:143]
EX v32,v32
MM v[56:59],a[232:235],v[204:207],v[56:59]
EX v33,v33
MM v[56:59],a[228:231],v[188:191],v[56:59]
EX v34,v34
MM v[56:59],a[224:227],v[172:175],v[56:59]
s_waitcnt lgkmcnt(0)
s_barrier
MM a[0:3],a[128:131],v[64:67],a[0:3]
s_add_u32 m0, s65, 16384
CV v24,v24,v25
CV v25,v26,v27
MM a[0:3],a[132:135],v[68:71],a[0:3]
RD a[192:195],v224 offset:49152
CV v26,v28,v29
MM a[8:11],a[132:135],v[76:79],a[8:11]
RD a[196:199],v225 offset:49152
CV v27,v30,v31
MM a[8:11],a[128:131],v[72:75],a[8:11]
buffer_load_dwordx4 v228, s[8:11], s64 offen offset:0 lds
MM a[16:19],a[128:131],v[80:83],a[16:19]
RD a[200:203],v226 offset:49152
CV v4,v32,v33
MM a[16:19],a[132:135],v[84:87],a[16:19]
buffer_load_dwordx4 v229, s[8:11], s64 offen offset:1024 lds
MM a[24:27],a[132:135],v[92:95],a[24:27]
RD a[204:207],v227 offset:49152
RD a[208:211],v224 offset:53248
MM a[24:27],a[128:131],v[88:91],a[24:27]
EX v35,v35
MM a[28:31],a[136:139],v[88:91],a[28:31]
RD a[212:215],v225 offset:53248
CV v5,v34,v35
MM a[28:31],a[140:143],v[92:95],a[28:31]
EX v36,v36
MM a[20:23],a[140:143],v[84:87],a[20:23]
buffer_load_dwordx4 v230, s[8:11], s64 offen offset:2048 lds
MM a[20:23],a[136:139],v[80:83],a[20:23]
EX v37,v37
MM a[12:15],a[136:139],v[72:75],a[12:15]
RD a[216:219],v226 offset:53248
CV v6,v36,v37
MM a[12:15],a[140:143],v[76:79],a[12:15]
EX v38,v38
MM a[4:7],a[140:143],v[68:71],a[4:7]
EX v39,v39
MM a[4:7],a[136:139],v[64:67],a[4:7]
RD a[220:223],v227 offset:53248
CV v7,v38,v39
MM a[32:35],a[144:147],v[64:67],a[32:35]
EX v40,v40
MM a[32:35],a[148:151],v[68:71],a[32:35]
buffer_load_dwordx4 v231, s[8:11], s64 offen offset:3072 lds
MM a[40:43],a[148:151],v[76:79],a[40:43]
s_add_u32 m0, s65, 81920
EX v41,v41
MM a[40:43],a[144:147],v[72:75],a[40:43]
RD a[224:227],v224 offset:57344
CV v12,v40,v41
MM a[48:51],a[144:147],v[80:83],a[48:51]
EX v42,v42
MM a[48:51],a[148:151],v[84:87],a[48:51]
EX v43,v43
MM a[56:59],a[148:151],v[92:95],a[56:59]
RD a[228:231],v225 offset:57344
CV v13,v42,v43
MM a[56:59],a[144:147],v[88:91],a[56:59]
EX v44,v44
MM a[60:63],a[152:155],v[88:91],a[60:63]
buffer_load_dwordx4 v235, s[12:15], s81 offen offset:0 lds
MM a[60:63],a[156:159],v[92:95],a[60:63]
EX v45,v45
MM a[52:55],a[156:159],v[84:87],a[52:55]
RD a[232:235],v226 offset:57344
CV v14,v44,v45
MM a[52:55],a[152:155],v[80:83],a[52:55]
EX v46,v46
MM a[44:47],a[152:155],v[72:75],a[44:47]
EX v47,v47
MM a[44:47],a[156:159],v[76:79],a[44:47]
RD a[236:239],v227 offset:57344
CV v15,v46,v47
MM a[36:39],a[156:159],v[68:71],a[36:39]
buffer_load_dwordx4 v235, s[12:15], s64 offen offset:1024 lds
MM a[36:39],a[152:155],v[64:67],a[36:39]
EX v48,v48
MM a[64:67],a[160:163],v[64:67],a[64:67]
EX v49,v49
MM a[64:67],a[164:167],v[68:71],a[64:67]
RD a[240:243],v224 offset:61440
CV v20,v48,v49
MM a[72:75],a[164:167],v[76:79],a[72:75]
EX v50,v50
MM a[72:75],a[160:163],v[72:75],a[72:75]
RD a[244:247],v225 offset:61440
RD a[248:251],v226 offset:61440
MM a[80:83],a[160:163],v[80:83],a[80:83]
EX v51,v51
MM a[80:83],a[164:167],v[84:87],a[80:83]
LPG19_%=:
s_cbranch_execz LTR3_%=
RD a[252:255],v227 offset:61440
CV v21,v50,v51
MM a[88:91],a[164:167],v[92:95],a[88:91]
EX v52,v52
MM a[88:91],a[160:163],v[88:91],a[88:91]
buffer_load_dwordx4 v235, s[12:15], s81 offen offset:2048 lds
MM a[92:95],a[168:171],v[88:91],a[92:95]
)ASM"
R"ASM(
EX v53,v53
MM a[92:95],a[172:175],v[92:95],a[92:95]
EX v54,v54
MM a[84:87],a[172:175],v[84:87],a[84:87]
buffer_load_dwordx4 v235, s[12:15], s64 offen offset:3072 lds
MM a[84:87],a[168:171],v[80:83],a[84:87]
EX v55,v55
MM a[76:79],a[168:171],v[72:75],a[76:79]
EX v56,v56
MM a[76:79],a[172:175],v[76:79],a[76:79]
EX v57,v57
MM a[68:71],a[172:175],v[68:71],a[68:71]
EX v58,v58
MM a[68:71],a[168:171],v[64:67],a[68:71]
EX v59,v59
MM a[96:99],a[176:179],v[64:67],a[96:99]
EX v60,v60
MM a[96:99],a[180:183],v[68:71],a[96:99]
EX v61,v61
MM a[104:107],a[180:183],v[76:79],a[104:107]
EX v62,v62
MM a[104:107],a[176:179],v[72:75],a[104:107]
EX v63,v63
MM a[112:115],a[176:179],v[80:83],a[112:115]
CV v22,v52,v53
CV v23,v54,v55
MM a[112:115],a[180:183],v[84:87],a[112:115]
CV v28,v56,v57
CV v29,v58,v59
MM a[120:123],a[180:183],v[92:95],a[120:123]
s_waitcnt vmcnt(8)
v_cmp_ne_u32 vcc, s69, v252
s_cbranch_vccnz LRFH_%=
LRFHb_%=:
MM a[120:123],a[176:179],v[88:91],a[120:123]
v_bfe_u32 v253, v248, 4, 3
v_mul_u32_u24 v253, 3456, v253
MM a[124:127],a[184:187],v[88:91],a[124:127]
v_add_f32 v244, v144, v145
v_add_f32 v245, v148, v149
MM a[124:127],a[188:191],v[92:95],a[124:127]
v_add_f32 v246, v152, v153
v_add_f32 v247, v156, v157
v_lshrrev_b32 v252, 7, v248
MM a[116:119],a[188:191],v[84:87],a[116:119]
v_lshl_add_u32 v253, v252, 4, v253
CV v30,v60,v61
MM a[116:119],a[184:187],v[80:83],a[116:119]
s_min_u32 s90, %[swave], 2
s_mul_i32 s76, s46, 384
CV v31,v62,v63
MM a[108:111],a[184:187],v[72:75],a[108:111]
s_lshl_b32 s91, s90, 7
s_add_u32 s76, s76, s91
s_add_u32 s76, s76, 21626880
MM a[108:111],a[188:191],v[76:79],a[108:111]
s_lshl_b32 s90, s90, 10
s_add_u32 s90, s90, %[slds]
MM a[100:103],a[188:191],v[68:71],a[100:103]
s_add_u32 m0, s90, 131072
s_nop 0
buffer_load_dwordx4 v253, s[20:23], s76 offen offset:0 sc1 lds
MM a[100:103],a[184:187],v[64:67],a[100:103]
v_smfmac_f32_16x16x64_bf16 v[144:147], v[236:239], v[0:7], v234
v_smfmac_f32_16x16x64_bf16 v[148:151], v[236:239], v[8:15], v234
v_smfmac_f32_16x16x64_bf16 v[152:155], v[236:239], v[16:23], v234
v_smfmac_f32_16x16x64_bf16 v[156:159], v[236:239], v[24:31], v234
s_waitcnt lgkmcnt(0)
MM v[64:67],a[192:195],v[160:163],v[128:131]
s_add_u32 s64, s64, 16384
TR a[128:129],v232 offset:32768
s_add_u32 s81, s64, 896
MM v[64:67],a[196:199],v[176:179],v[64:67]
TR a[130:131],v232 offset:36864
v_max3_f32 v249, v144, v145, v148
MM v[64:67],a[200:203],v[192:195],v[64:67]
TR a[132:133],v232 offset:40960
v_max3_f32 v249, v249, v149, v152
MM v[64:67],a[204:207],v[208:211],v[64:67]
TR a[134:135],v232 offset:45056
v_max3_f32 v249, v249, v153, v156
MM v[68:71],a[220:223],v[208:211],v[128:131]
TR a[136:137],v233 offset:32768
v_max_f32 v249, v249, v157
MM v[68:71],a[216:219],v[192:195],v[68:71]
TR a[138:139],v233 offset:36864
v_cmp_lt_f32 vcc, s71, v249
MM v[68:71],a[212:215],v[176:179],v[68:71]
TR a[140:141],v233 offset:40960
TR a[142:143],v233 offset:45056
MM v[68:71],a[208:211],v[160:163],v[68:71]
s_cbranch_vccnz LSITE26_%=
LBACK26_%=:
MM v[76:79],a[208:211],v[164:167],v[132:135]
EX v64,v64
MM v[76:79],a[212:215],v[180:183],v[76:79]
EX v65,v65
MM v[76:79],a[216:219],v[196:199],v[76:79]
TR a[144:145],v232 offset:33280
CV v64,v64,v65
MM v[76:79],a[220:223],v[212:215],v[76:79]
EX v66,v66
MM v[72:75],a[204:207],v[212:215],v[132:135]
EX v67,v67
MM v[72:75],a[200:203],v[196:199],v[72:75]
TR a[146:147],v232 offset:37376
CV v65,v66,v67
MM v[72:75],a[196:199],v[180:183],v[72:75]
EX v68,v68
MM v[72:75],a[192:195],v[164:167],v[72:75]
TR a[148:149],v232 offset:41472
TR a[150:151],v232 offset:45568
MM v[80:83],a[192:195],v[168:171],v[136:139]
EX v69,v69
MM v[80:83],a[196:199],v[184:187],v[80:83]
TR a[152:153],v233 offset:33280
CV v66,v68,v69
MM v[80:83],a[200:203],v[200:203],v[80:83]
EX v70,v70
MM v[80:83],a[204:207],v[216:219],v[80:83]
TR a[154:155],v233 offset:37376
TR a[156:157],v233 offset:41472
MM v[84:87],a[220:223],v[216:219],v[136:139]
EX v71,v71
MM v[84:87],a[216:219],v[200:203],v[84:87]
TR a[158:159],v233 offset:45568
CV v67,v70,v71
MM v[84:87],a[212:215],v[184:187],v[84:87]
EX v72,v72
MM v[84:87],a[208:211],v[168:171],v[84:87]
EX v73,v73
MM v[92:95],a[208:211],v[172:175],v[140:143]
TR a[160:161],v232 offset:33792
CV v72,v72,v73
MM v[92:95],a[212:215],v[188:191],v[92:95]
EX v74,v74
MM v[92:95],a[216:219],v[204:207],v[92:95]
TR a[162:163],v232 offset:37888
TR a[164:165],v232 offset:41984
MM v[92:95],a[220:223],v[220:223],v[92:95]
EX v75,v75
MM v[88:91],a[204:207],v[220:223],v[140:143]
TR a[166:167],v232 offset:46080
CV v73,v74,v75
MM v[88:91],a[200:203],v[204:207],v[88:91]
EX v76,v76
MM v[88:91],a[196:199],v[188:191],v[88:91]
EX v77,v77
MM v[88:91],a[192:195],v[172:175],v[88:91]
TR a[168:169],v233 offset:33792
CV v74,v76,v77
MM v[96:99],a[224:227],v[160:163],v[128:131]
EX v78,v78
MM v[96:99],a[228:231],v[176:179],v[96:99]
TR a[170:171],v233 offset:37888
TR a[172:173],v233 offset:41984
MM v[96:99],a[232:235],v[192:195],v[96:99]
EX v79,v79
MM v[96:99],a[236:239],v[208:211],v[96:99]
TR a[174:175],v233 offset:46080
CV v75,v78,v79
MM v[100:103],a[252:255],v[208:211],v[128:131]
EX v80,v80
MM v[100:103],a[248:251],v[192:195],v[100:103]
EX v81,v81
MM v[100:103],a[244:247],v[176:179],v[100:103]
TR a[176:177],v232 offset:34304
CV v80,v80,v81
MM v[100:103],a[240:243],v[160:163],v[100:103]
EX v82,v82
MM v[108:111],a[240:243],v[164:167],v[132:135]
TR a[178:179],v232 offset:38400
TR a[180:181],v232 offset:42496
MM v[108:111],a[244:247],v[180:183],v[108:111]
EX v83,v83
MM v[108:111],a[248:251],v[196:199],v[108:111]
TR a[182:183],v232 offset:46592
CV v81,v82,v83
MM v[108:111],a[252:255],v[212:215],v[108:111]
EX v84,v84
MM v[104:107],a[236:239],v[212:215],v[132:135]
TR a[184:185],v233 offset:34304
TR a[186:187],v233 offset:38400
MM v[104:107],a[232:235],v[196:199],v[104:107]
EX v85,v85
MM v[104:107],a[228:231],v[180:183],v[104:107]
TR a[188:189],v233 offset:42496
CV v82,v84,v85
MM v[104:107],a[224:227],v[164:167],v[104:107]
EX v86,v86
MM v[112:115],a[224:227],v[168:171],v[136:139]
EX v87,v87
MM v[112:115],a[228:231],v[184:187],v[112:115]
TR a[190:191],v233 offset:46592
CV v83,v86,v87
MM v[112:115],a[232:235],v[200:203],v[112:115]
EX v88,v88
MM v[112:115],a[236:239],v[216:219],v[112:115]
EX v89,v89
MM v[116:119],a[252:255],v[216:219],v[136:139]
EX v90,v90
MM v[116:119],a[248:251],v[200:203],v[116:119]
EX v91,v91
MM v[116:119],a[244:247],v[184:187],v[116:119]
EX v92,v92
MM v[116:119],a[240:243],v[168:171],v[116:119]
EX v93,v93
MM v[124:127],a[240:243],v[172:175],v[140:143]
EX v94,v94
MM v[124:127],a[244:247],v[188:191],v[124:127]
EX v95,v95
MM v[124:127],a[248:251],v[204:207],v[124:127]
EX v96,v96
MM v[124:127],a[252:255],v[220:223],v[124:127]
EX v97,v97
MM v[120:123],a[236:239],v[220:223],v[140:143]
EX v98,v98
MM v[120:123],a[232:235],v[204:207],v[120:123]
EX v99,v99
MM v[120:123],a[228:231],v[188:191],v[120:123]
s_getpc_b64 s[98:99]
LTA4_%=:
s_min_u32 s73, %[swave], 1
s_lshl_b32 s73, s73, 12
s_add_u32 s73, s73, LPG21_%=-LTA4_%=
s_add_u32 s98, s98, s73
s_addc_u32 s99, s99, 0
s_mov_b64 exec, 0
s_setpc_b64 s[98:99]
LTR4_%=:
s_mov_b64 exec, -1
CV v88,v88,v89
MM v[120:123],a[224:227],v[172:175],v[120:123]
s_waitcnt lgkmcnt(0)
s_barrier
MM a[0:3],a[128:131],v[0:3],a[0:3]
s_add_u32 m0, s65, 32768
CV v89,v90,v91
CV v90,v92,v93
MM a[0:3],a[132:135],v[4:7],a[0:3]
RD a[192:195],v224 offset:0
CV v91,v94,v95
MM a[8:11],a[132:135],v[12:15],a[8:11]
buffer_load_dwordx4 v228, s[8:11], s64 offen offset:0 lds
MM a[8:11],a[128:131],v[8:11],a[8:11]
RD a[196:199],v225 offset:0
CV v68,v96,v97
MM a[16:19],a[128:131],v[16:19],a[16:19]
RD a[200:203],v226 offset:0
CV v69,v98,v99
MM a[16:19],a[132:135],v[20:23],a[16:19]
buffer_load_dwordx4 v229, s[8:11], s64 offen offset:1024 lds
MM a[24:27],a[132:135],v[28:31],a[24:27]
RD a[204:207],v227 offset:0
RD a[208:211],v224 offset:4096
MM a[24:27],a[128:131],v[24:27],a[24:27]
RD a[212:215],v225 offset:4096
RD a[216:219],v226 offset:4096
MM a[28:31],a[136:139],v[24:27],a[28:31]
buffer_load_dwordx4 v230, s[8:11], s64 offen offset:2048 lds
MM a[28:31],a[140:143],v[28:31],a[28:31]
EX v100,v100
MM a[20:23],a[140:143],v[20:23],a[20:23]
EX v101,v101
MM a[20:23],a[136:139],v[16:19],a[20:23]
RD a[220:223],v227 offset:4096
CV v70,v100,v101
MM a[12:15],a[136:139],v[8:11],a[12:15]
EX v102,v102
MM a[12:15],a[140:143],v[12:15],a[12:15]
buffer_load_dwordx4 v231, s[8:11], s64 offen offset:3072 lds
MM a[4:7],a[140:143],v[4:7],a[4:7]
s_add_u32 m0, s65, 98304
EX v103,v103
MM a[4:7],a[136:139],v[0:3],a[4:7]
RD a[224:227],v224 offset:8192
CV v71,v102,v103
MM a[32:35],a[144:147],v[0:3],a[32:35]
EX v104,v104
MM a[32:35],a[148:151],v[4:7],a[32:35]
EX v105,v105
MM a[40:43],a[148:151],v[12:15],a[40:43]
RD a[228:231],v225 offset:8192
CV v76,v104,v105
MM a[40:43],a[144:147],v[8:11],a[40:43]
EX v106,v106
MM a[48:51],a[144:147],v[16:19],a[48:51]
buffer_load_dwordx4 v235, s[12:15], s81 offen offset:0 lds
MM a[48:51],a[148:151],v[20:23],a[48:51]
EX v107,v107
MM a[56:59],a[148:151],v[28:31],a[56:59]
RD a[232:235],v226 offset:8192
CV v77,v106,v107
MM a[56:59],a[144:147],v[24:27],a[56:59]
EX v108,v108
MM a[60:63],a[152:155],v[24:27],a[60:63]
EX v109,v109
MM a[60:63],a[156:159],v[28:31],a[60:63]
RD a[236:239],v227 offset:8192
CV v78,v108,v109
MM a[52:55],a[156:159],v[20:23],a[52:55]
EX v110,v110
MM a[52:55],a[152:155],v[16:19],a[52:55]
buffer_load_dwordx4 v235, s[12:15], s64 offen offset:1024 lds
MM a[44:47],a[152:155],v[8:11],a[44:47]
EX v111,v111
MM a[44:47],a[156:159],v[12:15],a[44:47]
RD a[240:243],v224 offset:12288
CV v79,v110,v111
MM a[36:39],a[156:159],v[4:7],a[36:39]
EX v112,v112
MM a[36:39],a[152:155],v[0:3],a[36:39]
EX v113,v113
MM a[64:67],a[160:163],v[0:3],a[64:67]
RD a[244:247],v225 offset:12288
CV v84,v112,v113
MM a[64:67],a[164:167],v[4:7],a[64:67]
EX v114,v114
MM a[72:75],a[164:167],v[12:15],a[72:75]
buffer_load_dwordx4 v235, s[12:15], s81 offen offset:2048 lds
MM a[72:75],a[160:163],v[8:11],a[72:75]
EX v115,v115
MM a[80:83],a[160:163],v[16:19],a[80:83]
RD a[248:251],v226 offset:12288
CV v85,v114,v115
MM a[80:83],a[164:167],v[20:23],a[80:83]
EX v116,v116
MM a[88:91],a[164:167],v[28:31],a[88:91]
EX v117,v117
MM a[88:91],a[160:163],v[24:27],a[88:91]
RD a[252:255],v227 offset:12288
CV v86,v116,v117
MM a[92:95],a[168:171],v[24:27],a[92:95]
EX v118,v118
MM a[92:95],a[172:175],v[28:31],a[92:95]
buffer_load_dwordx4 v235, s[12:15], s64 offen offset:3072 lds
MM a[84:87],a[172:175],v[20:23],a[84:87]
EX v119,v119
MM a[84:87],a[168:171],v[16:19],a[84:87]
CV v87,v118,v119
MM a[76:79],a[168:171],v[8:11],a[76:79]
EX v120,v120
MM a[76:79],a[172:175],v[12:15],a[76:79]
EX v121,v121
MM a[68:71],a[172:175],v[4:7],a[68:71]
EX v122,v122
MM a[68:71],a[168:171],v[0:3],a[68:71]
EX v123,v123
MM a[96:99],a[176:179],v[0:3],a[96:99]
EX v124,v124
MM a[96:99],a[180:183],v[4:7],a[96:99]
EX v125,v125
MM a[104:107],a[180:183],v[12:15],a[104:107]
EX v126,v126
MM a[104:107],a[176:179],v[8:11],a[104:107]
EX v127,v127
MM a[112:115],a[176:179],v[16:19],a[112:115]
CV v92,v120,v121
CV v93,v122,v123
MM a[112:115],a[180:183],v[20:23],a[112:115]
CV v94,v124,v125
CV v95,v126,v127
MM a[120:123],a[180:183],v[28:31],a[120:123]
MM a[120:123],a[176:179],v[24:27],a[120:123]
MM a[124:127],a[184:187],v[24:27],a[124:127]
v_add_f32 v244, v144, v145
v_add_f32 v245, v148, v149
MM a[124:127],a[188:191],v[28:31],a[124:127]
v_add_f32 v246, v152, v153
v_add_f32 v247, v156, v157
MM a[116:119],a[188:191],v[20:23],a[116:119]
MM a[116:119],a[184:187],v[16:19],a[116:119]
MM a[108:111],a[184:187],v[8:11],a[108:111]
MM a[108:111],a[188:191],v[12:15],a[108:111]
MM a[100:103],a[188:191],v[4:7],a[100:103]
MM a[100:103],a[184:187],v[0:3],a[100:103]
v_smfmac_f32_16x16x64_bf16 v[144:147], v[236:239], v[64:71], v234
v_smfmac_f32_16x16x64_bf16 v[148:151], v[236:239], v[72:79], v234
v_smfmac_f32_16x16x64_bf16 v[152:155], v[236:239], v[80:87], v234
v_smfmac_f32_16x16x64_bf16 v[156:159], v[236:239], v[88:95], v234
s_waitcnt lgkmcnt(0)
MM v[0:3],a[192:195],v[160:163],v[128:131]
s_add_u32 s64, s64, 16384
TR a[128:129],v232 offset:49152
s_add_u32 s81, s64, 896
MM v[0:3],a[196:199],v[176:179],v[0:3]
)ASM"
R"ASM(
TR a[130:131],v232 offset:53248
v_max3_f32 v249, v144, v145, v148
MM v[0:3],a[200:203],v[192:195],v[0:3]
TR a[132:133],v232 offset:57344
v_max3_f32 v249, v249, v149, v152
MM v[0:3],a[204:207],v[208:211],v[0:3]
TR a[134:135],v232 offset:61440
v_max3_f32 v249, v249, v153, v156
MM v[4:7],a[220:223],v[208:211],v[128:131]
TR a[136:137],v233 offset:49152
v_max_f32 v249, v249, v157
MM v[4:7],a[216:219],v[192:195],v[4:7]
TR a[138:139],v233 offset:53248
v_cmp_lt_f32 vcc, s71, v249
MM v[4:7],a[212:215],v[176:179],v[4:7]
TR a[140:141],v233 offset:57344
TR a[142:143],v233 offset:61440
MM v[4:7],a[208:211],v[160:163],v[4:7]
s_cbranch_vccnz LSITE27_%=
LBACK27_%=:
MM v[12:15],a[208:211],v[164:167],v[132:135]
v_lshrrev_b32 v125, 8, v248
v_lshlrev_b32 v124, 10, v125
MM v[12:15],a[212:215],v[180:183],v[12:15]
v_bfe_u32 v126, v248, 6, 2
v_lshl_add_u32 v124, v126, 7, v124
MM v[12:15],a[216:219],v[196:199],v[12:15]
v_bfe_u32 v126, v248, 4, 2
v_lshl_add_u32 v124, v126, 3, v124
MM v[12:15],a[220:223],v[212:215],v[12:15]
s_lshl_b32 s91, %[swave], 5
EX v0,v0
MM v[8:11],a[204:207],v[212:215],v[132:135]
s_add_u32 s91, s91, %[slds]
EX v1,v1
MM v[8:11],a[200:203],v[196:199],v[8:11]
s_add_u32 s91, s91, 139264
v_add_u32 v124, s91, v124
v_and_b32 v126, 112, v248
MM v[8:11],a[196:199],v[180:183],v[8:11]
s_add_u32 s91, %[slds], 131072
v_lshl_add_u32 v126, v125, 7, v126
v_add_u32 v126, s91, v126
MM v[8:11],a[192:195],v[164:167],v[8:11]
v_bfe_u32 v127, v248, 4, 3
v_mul_u32_u24 v127, 2560, v127
MM v[16:19],a[192:195],v[168:171],v[136:139]
v_lshl_add_u32 v127, v125, 4, v127
CV v0,v0,v1
EX v2,v2
MM v[16:19],a[196:199],v[184:187],v[16:19]
s_lshl_b32 s90, %[swave], 6
EX v3,v3
MM v[16:19],a[200:203],v[200:203],v[16:19]
s_lshl_b32 s91, s47, 8
EX v4,v4
MM v[16:19],a[204:207],v[216:219],v[16:19]
s_add_u32 s90, s90, s91
TR a[144:145],v232 offset:49664
TR a[146:147],v232 offset:53760
MM v[20:23],a[220:223],v[216:219],v[136:139]
s_mul_i32 s91, s46, 17920
EX v5,v5
MM v[20:23],a[216:219],v[200:203],v[20:23]
s_add_u32 s90, s90, s91
TR a[148:149],v232 offset:57856
CV v1,v2,v3
MM v[20:23],a[212:215],v[184:187],v[20:23]
s_add_u32 s90, s90, 21659648
EX v6,v6
MM v[20:23],a[208:211],v[168:171],v[20:23]
EX v7,v7
MM v[28:31],a[208:211],v[172:175],v[140:143]
TR a[150:151],v232 offset:61952
TR a[152:153],v233 offset:49664
MM v[28:31],a[212:215],v[188:191],v[28:31]
EX v8,v8
MM v[28:31],a[216:219],v[204:207],v[28:31]
TR a[154:155],v233 offset:53760
CV v2,v4,v5
MM v[28:31],a[220:223],v[220:223],v[28:31]
EX v9,v9
MM v[24:27],a[204:207],v[220:223],v[140:143]
TR a[156:157],v233 offset:57856
CV v3,v6,v7
MM v[24:27],a[200:203],v[204:207],v[24:27]
EX v10,v10
MM v[24:27],a[196:199],v[188:191],v[24:27]
TR a[158:159],v233 offset:61952
CV v8,v8,v9
MM v[24:27],a[192:195],v[172:175],v[24:27]
EX v11,v11
MM v[32:35],a[224:227],v[160:163],v[128:131]
TR a[160:161],v232 offset:50176
TR a[162:163],v232 offset:54272
MM v[32:35],a[228:231],v[176:179],v[32:35]
EX v12,v12
MM v[32:35],a[232:235],v[192:195],v[32:35]
EX v13,v13
MM v[32:35],a[236:239],v[208:211],v[32:35]
TR a[164:165],v232 offset:58368
CV v9,v10,v11
MM v[36:39],a[252:255],v[208:211],v[128:131]
EX v14,v14
MM v[36:39],a[248:251],v[192:195],v[36:39]
TR a[166:167],v232 offset:62464
CV v10,v12,v13
MM v[36:39],a[244:247],v[176:179],v[36:39]
EX v15,v15
MM v[36:39],a[240:243],v[160:163],v[36:39]
TR a[168:169],v233 offset:50176
TR a[170:171],v233 offset:54272
MM v[44:47],a[240:243],v[164:167],v[132:135]
EX v16,v16
MM v[44:47],a[244:247],v[180:183],v[44:47]
EX v17,v17
MM v[44:47],a[248:251],v[196:199],v[44:47]
TR a[172:173],v233 offset:58368
CV v11,v14,v15
MM v[44:47],a[252:255],v[212:215],v[44:47]
EX v18,v18
MM v[40:43],a[236:239],v[212:215],v[132:135]
TR a[174:175],v233 offset:62464
CV v16,v16,v17
MM v[40:43],a[232:235],v[196:199],v[40:43]
EX v19,v19
MM v[40:43],a[228:231],v[180:183],v[40:43]
TR a[176:177],v232 offset:50688
TR a[178:179],v232 offset:54784
MM v[40:43],a[224:227],v[164:167],v[40:43]
EX v20,v20
MM v[48:51],a[224:227],v[168:171],v[136:139]
EX v21,v21
MM v[48:51],a[228:231],v[184:187],v[48:51]
TR a[180:181],v232 offset:58880
CV v17,v18,v19
MM v[48:51],a[232:235],v[200:203],v[48:51]
EX v22,v22
MM v[48:51],a[236:239],v[216:219],v[48:51]
TR a[182:183],v232 offset:62976
CV v18,v20,v21
MM v[52:55],a[252:255],v[216:219],v[136:139]
EX v23,v23
MM v[52:55],a[248:251],v[200:203],v[52:55]
TR a[184:185],v233 offset:50688
TR a[186:187],v233 offset:54784
MM v[52:55],a[244:247],v[184:187],v[52:55]
EX v24,v24
MM v[52:55],a[240:243],v[168:171],v[52:55]
TR a[188:189],v233 offset:58880
CV v19,v22,v23
MM v[60:63],a[240:243],v[172:175],v[140:143]
EX v25,v25
MM v[60:63],a[244:247],v[188:191],v[60:63]
TR a[190:191],v233 offset:62976
CV v24,v24,v25
MM v[60:63],a[248:251],v[204:207],v[60:63]
EX v26,v26
MM v[60:63],a[252:255],v[220:223],v[60:63]
EX v27,v27
MM v[56:59],a[236:239],v[220:223],v[140:143]
EX v28,v28
MM v[56:59],a[232:235],v[204:207],v[56:59]
EX v29,v29
MM v[56:59],a[228:231],v[188:191],v[56:59]
EX v30,v30
MM v[56:59],a[224:227],v[172:175],v[56:59]
s_waitcnt vmcnt(8) lgkmcnt(0)
s_barrier
MM a[0:3],a[128:131],v[64:67],a[0:3]
s_add_u32 m0, s65, 49152
CV v25,v26,v27
CV v26,v28,v29
MM a[0:3],a[132:135],v[68:71],a[0:3]
RD a[192:195],v224 offset:16384
RD a[196:199],v225 offset:16384
MM a[8:11],a[132:135],v[76:79],a[8:11]
EX v31,v31
MM a[8:11],a[128:131],v[72:75],a[8:11]
RD a[200:203],v226 offset:16384
CV v27,v30,v31
MM a[16:19],a[128:131],v[80:83],a[16:19]
TR v[96:97],v124 offset:0
TR v[98:99],v124 offset:512
MM a[16:19],a[132:135],v[84:87],a[16:19]
RD v[108:111],v126 offset:0
TR v[100:101],v124 offset:4096
MM a[24:27],a[132:135],v[92:95],a[24:27]
TR v[102:103],v124 offset:4608
RD v[112:115],v126 offset:512
MM a[24:27],a[128:131],v[88:91],a[24:27]
TR v[104:105],v124 offset:8192
TR v[106:107],v124 offset:8704
MM a[28:31],a[136:139],v[88:91],a[28:31]
RD v[116:119],v126 offset:1024
RD a[204:207],v227 offset:16384
EX v32,v32
MM a[28:31],a[140:143],v[92:95],a[28:31]
EX v33,v33
MM a[20:23],a[140:143],v[84:87],a[20:23]
EX v34,v34
MM a[20:23],a[136:139],v[80:83],a[20:23]
buffer_load_dwordx4 v228, s[8:11], s64 offen offset:0 lds
LPG20_%=:
s_cbranch_execz LTR3_%=
MM a[12:15],a[136:139],v[72:75],a[12:15]
EX v35,v35
MM a[12:15],a[140:143],v[76:79],a[12:15]
EX v36,v36
MM a[4:7],a[140:143],v[68:71],a[4:7]
CV v4,v32,v33
CV v5,v34,v35
MM a[4:7],a[136:139],v[64:67],a[4:7]
EX v37,v37
MM a[32:35],a[144:147],v[64:67],a[32:35]
EX v38,v38
MM a[32:35],a[148:151],v[68:71],a[32:35]
buffer_load_dwordx4 v229, s[8:11], s64 offen offset:1024 lds
MM a[40:43],a[148:151],v[76:79],a[40:43]
EX v39,v39
MM a[40:43],a[144:147],v[72:75],a[40:43]
RD a[208:211],v224 offset:20480
CV v6,v36,v37
MM a[48:51],a[144:147],v[80:83],a[48:51]
s_waitcnt lgkmcnt(2)
EX v40,v40
MM a[48:51],a[148:151],v[84:87],a[48:51]
RD a[212:215],v225 offset:20480
CV v7,v38,v39
MM v[120:123],v[96:99],v[108:111],0
MM v[120:123],v[100:103],v[112:115],v[120:123]
MM v[120:123],v[104:107],v[116:119],v[120:123]
MM a[56:59],a[148:151],v[92:95],a[56:59]
TR v[96:97],v124 offset:12288
TR v[98:99],v124 offset:12800
MM a[56:59],a[144:147],v[88:91],a[56:59]
RD v[108:111],v126 offset:1536
TR v[100:101],v124 offset:16384
MM a[60:63],a[152:155],v[88:91],a[60:63]
TR v[102:103],v124 offset:16896
RD v[112:115],v126 offset:2048
EX v41,v41
MM a[60:63],a[156:159],v[92:95],a[60:63]
TR v[104:105],v124 offset:20480
TR v[106:107],v124 offset:20992
MM a[52:55],a[156:159],v[84:87],a[52:55]
RD v[116:119],v126 offset:2560
CV v12,v40,v41
EX v42,v42
MM a[52:55],a[152:155],v[80:83],a[52:55]
EX v43,v43
MM a[44:47],a[152:155],v[72:75],a[44:47]
buffer_load_dwordx4 v230, s[8:11], s64 offen offset:2048 lds
MM a[44:47],a[156:159],v[76:79],a[44:47]
EX v44,v44
MM a[36:39],a[156:159],v[68:71],a[36:39]
EX v45,v45
MM a[36:39],a[152:155],v[64:67],a[36:39]
RD a[216:219],v226 offset:20480
CV v13,v42,v43
MM a[64:67],a[160:163],v[64:67],a[64:67]
EX v46,v46
MM a[64:67],a[164:167],v[68:71],a[64:67]
EX v47,v47
MM a[72:75],a[164:167],v[76:79],a[72:75]
buffer_load_dwordx4 v231, s[8:11], s64 offen offset:3072 lds
MM a[72:75],a[160:163],v[72:75],a[72:75]
s_add_u32 m0, s65, 114688
RD a[220:223],v227 offset:20480
CV v14,v44,v45
MM a[80:83],a[160:163],v[80:83],a[80:83]
EX v48,v48
MM a[80:83],a[164:167],v[84:87],a[80:83]
EX v49,v49
MM a[88:91],a[164:167],v[92:95],a[88:91]
s_waitcnt lgkmcnt(2)
RD a[224:227],v224 offset:24576
CV v15,v46,v47
MM a[88:91],a[160:163],v[88:91],a[88:91]
EX v50,v50
MM v[120:123],v[96:99],v[108:111],v[120:123]
MM v[120:123],v[100:103],v[112:115],v[120:123]
MM v[120:123],v[104:107],v[116:119],v[120:123]
MM a[92:95],a[168:171],v[88:91],a[92:95]
EX v51,v51
MM a[92:95],a[172:175],v[92:95],a[92:95]
buffer_load_dwordx4 v235, s[12:15], s81 offen offset:0 lds
MM a[84:87],a[172:175],v[84:87],a[84:87]
RD a[228:231],v225 offset:24576
CV v20,v48,v49
MM a[84:87],a[168:171],v[80:83],a[84:87]
EX v52,v52
MM a[76:79],a[168:171],v[72:75],a[76:79]
EX v53,v53
MM a[76:79],a[172:175],v[76:79],a[76:79]
RD a[232:235],v226 offset:24576
CV v21,v50,v51
MM a[68:71],a[172:175],v[68:71],a[68:71]
s_mov_b32 exec_lo, 0x00ff00ff
s_mov_b32 exec_hi, 0x00ff00ff
buffer_store_dwordx4 v[120:123], v127, s[20:23], s90 offen offset:0 sc1
s_mov_b64 exec, -1
s_nop 0
MM a[68:71],a[168:171],v[64:67],a[68:71]
EX v54,v54
MM a[96:99],a[176:179],v[64:67],a[96:99]
EX v55,v55
MM a[96:99],a[180:183],v[68:71],a[96:99]
RD a[236:239],v227 offset:24576
CV v22,v52,v53
MM a[104:107],a[180:183],v[76:79],a[104:107]
EX v56,v56
MM a[104:107],a[176:179],v[72:75],a[104:107]
buffer_load_dwordx4 v235, s[12:15], s64 offen offset:1024 lds
MM a[112:115],a[176:179],v[80:83],a[112:115]
EX v57,v57
MM a[112:115],a[180:183],v[84:87],a[112:115]
RD a[240:243],v224 offset:28672
CV v23,v54,v55
MM a[120:123],a[180:183],v[92:95],a[120:123]
EX v58,v58
MM a[120:123],a[176:179],v[88:91],a[120:123]
EX v59,v59
MM a[124:127],a[184:187],v[88:91],a[124:127]
v_add_f32 v244, v144, v145
v_add_f32 v245, v148, v149
MM a[124:127],a[188:191],v[92:95],a[124:127]
v_add_f32 v246, v152, v153
v_add_f32 v247, v156, v157
MM a[116:119],a[188:191],v[84:87],a[116:119]
EX v60,v60
MM a[116:119],a[184:187],v[80:83],a[116:119]
RD a[244:247],v225 offset:28672
CV v28,v56,v57
MM a[108:111],a[184:187],v[72:75],a[108:111]
EX v61,v61
MM a[108:111],a[188:191],v[76:79],a[108:111]
buffer_load_dwordx4 v235, s[12:15], s81 offen offset:2048 lds
MM a[100:103],a[188:191],v[68:71],a[100:103]
EX v62,v62
MM a[100:103],a[184:187],v[64:67],a[100:103]
RD a[248:251],v226 offset:28672
CV v29,v58,v59
v_smfmac_f32_16x16x64_bf16 v[144:147], v[236:239], v[0:7], v234
RD a[252:255],v227 offset:28672
EX v63,v63
v_smfmac_f32_16x16x64_bf16 v[148:151], v[236:239], v[8:15], v234
CV v30,v60,v61
v_smfmac_f32_16x16x64_bf16 v[152:155], v[236:239], v[16:23], v234
CV v31,v62,v63
s_nop 1
v_smfmac_f32_16x16x64_bf16 v[156:159], v[236:239], v[24:31], v234
s_waitcnt lgkmcnt(0)
buffer_load_dwordx4 v235, s[12:15], s64 offen offset:3072 lds
MM v[64:67],a[192:195],v[160:163],v[128:131]
s_add_u32 s64, s64, 16384
TR a[128:129],v232 offset:0
s_add_u32 s81, s64, 896
MM v[64:67],a[196:199],v[176:179],v[64:67]
TR a[130:131],v232 offset:4096
v_max3_f32 v249, v144, v145, v148
MM v[64:67],a[200:203],v[192:195],v[64:67]
TR a[132:133],v232 offset:8192
v_max3_f32 v249, v249, v149, v152
MM v[64:67],a[204:207],v[208:211],v[64:67]
TR a[134:135],v232 offset:12288
v_max3_f32 v249, v249, v153, v156
MM v[68:71],a[220:223],v[208:211],v[128:131]
TR a[136:137],v233 offset:0
v_max_f32 v249, v249, v157
MM v[68:71],a[216:219],v[192:195],v[68:71]
TR a[138:139],v233 offset:4096
v_cmp_lt_f32 vcc, s71, v249
MM v[68:71],a[212:215],v[176:179],v[68:71]
TR a[140:141],v233 offset:8192
TR a[142:143],v233 offset:12288
MM v[68:71],a[208:211],v[160:163],v[68:71]
s_cbranch_vccnz LSITE28_%=
LBACK28_%=:
MM v[76:79],a[208:211],v[164:167],v[132:135]
EX v64,v64
MM v[76:79],a[212:215],v[180:183],v[76:79]
EX v65,v65
MM v[76:79],a[216:219],v[196:199],v[76:79]
TR a[144:145],v232 offset:512
CV v64,v64,v65
MM v[76:79],a[220:223],v[212:215],v[76:79]
EX v66,v66
MM v[72:75],a[204:207],v[212:215],v[132:135]
EX v67,v67
MM v[72:75],a[200:203],v[196:199],v[72:75]
)ASM"
R"ASM(
TR a[146:147],v232 offset:4608
CV v65,v66,v67
MM v[72:75],a[196:199],v[180:183],v[72:75]
EX v68,v68
MM v[72:75],a[192:195],v[164:167],v[72:75]
TR a[148:149],v232 offset:8704
TR a[150:151],v232 offset:12800
MM v[80:83],a[192:195],v[168:171],v[136:139]
EX v69,v69
MM v[80:83],a[196:199],v[184:187],v[80:83]
TR a[152:153],v233 offset:512
CV v66,v68,v69
MM v[80:83],a[200:203],v[200:203],v[80:83]
EX v70,v70
MM v[80:83],a[204:207],v[216:219],v[80:83]
TR a[154:155],v233 offset:4608
TR a[156:157],v233 offset:8704
MM v[84:87],a[220:223],v[216:219],v[136:139]
v_lshlrev_b32 v32, 3, v248
v_min_u32 v32, 4992, v32
MM v[84:87],a[216:219],v[200:203],v[84:87]
s_sub_u32 s91, 5242880, s26
s_sub_u32 s60, s24, s91
s_subb_u32 s61, s25, 0
MM v[84:87],a[212:215],v[184:187],v[84:87]
s_mov_b32 s62, 5242880
EX v71,v71
MM v[84:87],a[208:211],v[168:171],v[84:87]
s_mov_b32 s63, 131072
TR a[158:159],v233 offset:12800
CV v67,v70,v71
MM v[92:95],a[208:211],v[172:175],v[140:143]
s_lshl_b32 s90, s16, 2
EX v72,v72
MM v[92:95],a[212:215],v[188:191],v[92:95]
s_add_u32 s90, s90, %[swave]
EX v73,v73
MM v[92:95],a[216:219],v[204:207],v[92:95]
s_mul_i32 s90, s90, 10240
TR a[160:161],v232 offset:1024
CV v72,v72,v73
MM v[92:95],a[220:223],v[220:223],v[92:95]
s_add_u32 s90, s90, 5120
EX v74,v74
MM v[88:91],a[204:207],v[220:223],v[140:143]
s_add_u32 s91, %[slds], 135168
s_mov_b32 m0, s91
s_nop 0
buffer_load_dword v32, s[60:63], s90 offen offset:0 lds
MM v[88:91],a[200:203],v[204:207],v[88:91]
EX v75,v75
MM v[88:91],a[196:199],v[188:191],v[88:91]
TR a[162:163],v232 offset:5120
CV v73,v74,v75
MM v[88:91],a[192:195],v[172:175],v[88:91]
EX v76,v76
MM v[96:99],a[224:227],v[160:163],v[128:131]
TR a[164:165],v232 offset:9216
TR a[166:167],v232 offset:13312
MM v[96:99],a[228:231],v[176:179],v[96:99]
EX v77,v77
MM v[96:99],a[232:235],v[192:195],v[96:99]
TR a[168:169],v233 offset:1024
CV v74,v76,v77
MM v[96:99],a[236:239],v[208:211],v[96:99]
EX v78,v78
MM v[100:103],a[252:255],v[208:211],v[128:131]
EX v79,v79
MM v[100:103],a[248:251],v[192:195],v[100:103]
TR a[170:171],v233 offset:5120
CV v75,v78,v79
MM v[100:103],a[244:247],v[176:179],v[100:103]
EX v80,v80
MM v[100:103],a[240:243],v[160:163],v[100:103]
TR a[172:173],v233 offset:9216
TR a[174:175],v233 offset:13312
MM v[108:111],a[240:243],v[164:167],v[132:135]
s_add_u32 s67, s95, 5120
EX v81,v81
MM v[108:111],a[244:247],v[180:183],v[108:111]
s_mov_b32 s70, 1
TR a[176:177],v232 offset:1536
CV v80,v80,v81
MM v[108:111],a[248:251],v[196:199],v[108:111]
EX v82,v82
MM v[108:111],a[252:255],v[212:215],v[108:111]
EX v83,v83
MM v[104:107],a[236:239],v[212:215],v[132:135]
TR a[178:179],v232 offset:5632
CV v81,v82,v83
MM v[104:107],a[232:235],v[196:199],v[104:107]
EX v84,v84
MM v[104:107],a[228:231],v[180:183],v[104:107]
TR a[180:181],v232 offset:9728
TR a[182:183],v232 offset:13824
MM v[104:107],a[224:227],v[164:167],v[104:107]
EX v85,v85
MM v[112:115],a[224:227],v[168:171],v[136:139]
TR a[184:185],v233 offset:1536
CV v82,v84,v85
MM v[112:115],a[228:231],v[184:187],v[112:115]
EX v86,v86
MM v[112:115],a[232:235],v[200:203],v[112:115]
EX v87,v87
MM v[112:115],a[236:239],v[216:219],v[112:115]
TR a[186:187],v233 offset:5632
CV v83,v86,v87
MM v[116:119],a[252:255],v[216:219],v[136:139]
EX v88,v88
MM v[116:119],a[248:251],v[200:203],v[116:119]
TR a[188:189],v233 offset:9728
TR a[190:191],v233 offset:13824
MM v[116:119],a[244:247],v[184:187],v[116:119]
EX v89,v89
MM v[116:119],a[240:243],v[168:171],v[116:119]
EX v90,v90
MM v[124:127],a[240:243],v[172:175],v[140:143]
EX v91,v91
MM v[124:127],a[244:247],v[188:191],v[124:127]
EX v92,v92
MM v[124:127],a[248:251],v[204:207],v[124:127]
EX v93,v93
MM v[124:127],a[252:255],v[220:223],v[124:127]
EX v94,v94
MM v[120:123],a[236:239],v[220:223],v[140:143]
EX v95,v95
MM v[120:123],a[232:235],v[204:207],v[120:123]
EX v96,v96
MM v[120:123],a[228:231],v[188:191],v[120:123]
EX v97,v97
MM v[120:123],a[224:227],v[172:175],v[120:123]
s_waitcnt vmcnt(14) lgkmcnt(0)
s_barrier
MM a[0:3],a[128:131],v[0:3],a[0:3]
s_add_u32 m0, s65, 0
CV v88,v88,v89
CV v89,v90,v91
MM a[0:3],a[132:135],v[4:7],a[0:3]
RD a[192:195],v224 offset:32768
CV v90,v92,v93
MM a[8:11],a[132:135],v[12:15],a[8:11]
RD a[196:199],v225 offset:32768
CV v91,v94,v95
MM a[8:11],a[128:131],v[8:11],a[8:11]
buffer_load_dwordx4 v228, s[8:11], s64 offen offset:0 lds
MM a[16:19],a[128:131],v[16:19],a[16:19]
RD a[200:203],v226 offset:32768
CV v68,v96,v97
MM a[16:19],a[132:135],v[20:23],a[16:19]
buffer_load_dwordx4 v229, s[8:11], s64 offen offset:1024 lds
MM a[24:27],a[132:135],v[28:31],a[24:27]
EX v98,v98
MM a[24:27],a[128:131],v[24:27],a[24:27]
RD a[204:207],v227 offset:32768
RD a[208:211],v224 offset:36864
MM a[28:31],a[136:139],v[24:27],a[28:31]
EX v99,v99
MM a[28:31],a[140:143],v[28:31],a[28:31]
RD a[212:215],v225 offset:36864
CV v69,v98,v99
MM a[20:23],a[140:143],v[20:23],a[20:23]
EX v100,v100
MM a[20:23],a[136:139],v[16:19],a[20:23]
EX v101,v101
MM a[12:15],a[136:139],v[8:11],a[12:15]
RD a[216:219],v226 offset:36864
CV v70,v100,v101
MM a[12:15],a[140:143],v[12:15],a[12:15]
buffer_load_dwordx4 v230, s[8:11], s64 offen offset:2048 lds
MM a[4:7],a[140:143],v[4:7],a[4:7]
EX v102,v102
MM a[4:7],a[136:139],v[0:3],a[4:7]
EX v103,v103
MM a[32:35],a[144:147],v[0:3],a[32:35]
RD a[220:223],v227 offset:36864
CV v71,v102,v103
MM a[32:35],a[148:151],v[4:7],a[32:35]
EX v104,v104
MM a[40:43],a[148:151],v[12:15],a[40:43]
EX v105,v105
MM a[40:43],a[144:147],v[8:11],a[40:43]
RD a[224:227],v224 offset:40960
CV v76,v104,v105
MM a[48:51],a[144:147],v[16:19],a[48:51]
buffer_load_dwordx4 v231, s[8:11], s64 offen offset:3072 lds
MM a[48:51],a[148:151],v[20:23],a[48:51]
s_add_u32 m0, s65, 65536
EX v106,v106
MM a[56:59],a[148:151],v[28:31],a[56:59]
EX v107,v107
MM a[56:59],a[144:147],v[24:27],a[56:59]
RD a[228:231],v225 offset:40960
CV v77,v106,v107
MM a[60:63],a[152:155],v[24:27],a[60:63]
EX v108,v108
MM a[60:63],a[156:159],v[28:31],a[60:63]
buffer_load_dwordx4 v235, s[12:15], s81 offen offset:0 lds
MM a[52:55],a[156:159],v[20:23],a[52:55]
EX v109,v109
MM a[52:55],a[152:155],v[16:19],a[52:55]
RD a[232:235],v226 offset:40960
CV v78,v108,v109
MM a[44:47],a[152:155],v[8:11],a[44:47]
EX v110,v110
MM a[44:47],a[156:159],v[12:15],a[44:47]
EX v111,v111
MM a[36:39],a[156:159],v[4:7],a[36:39]
RD a[236:239],v227 offset:40960
CV v79,v110,v111
MM a[36:39],a[152:155],v[0:3],a[36:39]
EX v112,v112
MM a[64:67],a[160:163],v[0:3],a[64:67]
buffer_load_dwordx4 v235, s[12:15], s64 offen offset:1024 lds
MM a[64:67],a[164:167],v[4:7],a[64:67]
EX v113,v113
MM a[72:75],a[164:167],v[12:15],a[72:75]
RD a[240:243],v224 offset:45056
CV v84,v112,v113
MM a[72:75],a[160:163],v[8:11],a[72:75]
EX v114,v114
MM a[80:83],a[160:163],v[16:19],a[80:83]
EX v115,v115
MM a[80:83],a[164:167],v[20:23],a[80:83]
RD a[244:247],v225 offset:45056
CV v85,v114,v115
MM a[88:91],a[164:167],v[28:31],a[88:91]
EX v116,v116
MM a[88:91],a[160:163],v[24:27],a[88:91]
buffer_load_dwordx4 v235, s[12:15], s81 offen offset:2048 lds
MM a[92:95],a[168:171],v[24:27],a[92:95]
EX v117,v117
MM a[92:95],a[172:175],v[28:31],a[92:95]
RD a[248:251],v226 offset:45056
CV v86,v116,v117
MM a[84:87],a[172:175],v[20:23],a[84:87]
EX v118,v118
MM a[84:87],a[168:171],v[16:19],a[84:87]
EX v119,v119
MM a[76:79],a[168:171],v[8:11],a[76:79]
RD a[252:255],v227 offset:45056
CV v87,v118,v119
MM a[76:79],a[172:175],v[12:15],a[76:79]
EX v120,v120
MM a[68:71],a[172:175],v[4:7],a[68:71]
buffer_load_dwordx4 v235, s[12:15], s64 offen offset:3072 lds
MM a[68:71],a[168:171],v[0:3],a[68:71]
EX v121,v121
MM a[96:99],a[176:179],v[0:3],a[96:99]
EX v122,v122
MM a[96:99],a[180:183],v[4:7],a[96:99]
EX v123,v123
MM a[104:107],a[180:183],v[12:15],a[104:107]
EX v124,v124
MM a[104:107],a[176:179],v[8:11],a[104:107]
EX v125,v125
MM a[112:115],a[176:179],v[16:19],a[112:115]
EX v126,v126
MM a[112:115],a[180:183],v[20:23],a[112:115]
EX v127,v127
MM a[120:123],a[180:183],v[28:31],a[120:123]
s_lshl_b32 s91, s47, 2
CV v92,v120,v121
CV v93,v122,v123
MM a[120:123],a[176:179],v[24:27],a[120:123]
s_add_u32 s91, s91, %[swave]
CV v94,v124,v125
CV v95,v126,v127
MM a[124:127],a[184:187],v[24:27],a[124:127]
s_lshl_b32 s91, s91, 2
v_add_f32 v244, v144, v145
v_add_f32 v245, v148, v149
MM a[124:127],a[188:191],v[28:31],a[124:127]
s_add_u32 s91, s91, 73728
v_add_f32 v246, v152, v153
v_add_f32 v247, v156, v157
MM a[116:119],a[188:191],v[20:23],a[116:119]
s_waitcnt vmcnt(12)
v_mov_b32 v250, s69
s_mov_b64 exec, 1
buffer_store_dword v250, off, s[20:23], s91 offset:0 sc1
s_mov_b64 exec, -1
MM a[116:119],a[184:187],v[16:19],a[116:119]
MM a[108:111],a[184:187],v[8:11],a[108:111]
MM a[108:111],a[188:191],v[12:15],a[108:111]
MM a[100:103],a[188:191],v[4:7],a[100:103]
MM a[100:103],a[184:187],v[0:3],a[100:103]
v_smfmac_f32_16x16x64_bf16 v[144:147], v[236:239], v[64:71], v234
v_smfmac_f32_16x16x64_bf16 v[148:151], v[236:239], v[72:79], v234
v_smfmac_f32_16x16x64_bf16 v[152:155], v[236:239], v[80:87], v234
v_smfmac_f32_16x16x64_bf16 v[156:159], v[236:239], v[88:95], v234
s_waitcnt lgkmcnt(0)
s_cmp_ge_u32 s16, 20
s_cbranch_scc1 LLAP_%=
MM v[0:3],a[192:195],v[160:163],v[128:131]
s_add_u32 s64, s64, 16384
TR a[128:129],v232 offset:16384
s_add_u32 s81, s64, 896
MM v[0:3],a[196:199],v[176:179],v[0:3]
TR a[130:131],v232 offset:20480
v_max3_f32 v249, v144, v145, v148
MM v[0:3],a[200:203],v[192:195],v[0:3]
TR a[132:133],v232 offset:24576
v_max3_f32 v249, v249, v149, v152
MM v[0:3],a[204:207],v[208:211],v[0:3]
TR a[134:135],v232 offset:28672
v_max3_f32 v249, v249, v153, v156
MM v[4:7],a[220:223],v[208:211],v[128:131]
TR a[136:137],v233 offset:16384
v_max_f32 v249, v249, v157
MM v[4:7],a[216:219],v[192:195],v[4:7]
TR a[138:139],v233 offset:20480
v_cmp_lt_f32 vcc, s71, v249
MM v[4:7],a[212:215],v[176:179],v[4:7]
TR a[140:141],v233 offset:24576
TR a[142:143],v233 offset:28672
MM v[4:7],a[208:211],v[160:163],v[4:7]
s_cbranch_vccnz LSITE29_%=
LBACK29_%=:
MM v[12:15],a[208:211],v[164:167],v[132:135]
EX v0,v0
MM v[12:15],a[212:215],v[180:183],v[12:15]
EX v1,v1
MM v[12:15],a[216:219],v[196:199],v[12:15]
TR a[144:145],v232 offset:16896
CV v0,v0,v1
MM v[12:15],a[220:223],v[212:215],v[12:15]
EX v2,v2
MM v[8:11],a[204:207],v[212:215],v[132:135]
EX v3,v3
MM v[8:11],a[200:203],v[196:199],v[8:11]
TR a[146:147],v232 offset:20992
CV v1,v2,v3
MM v[8:11],a[196:199],v[180:183],v[8:11]
EX v4,v4
MM v[8:11],a[192:195],v[164:167],v[8:11]
TR a[148:149],v232 offset:25088
TR a[150:151],v232 offset:29184
MM v[16:19],a[192:195],v[168:171],v[136:139]
EX v5,v5
MM v[16:19],a[196:199],v[184:187],v[16:19]
TR a[152:153],v233 offset:16896
CV v2,v4,v5
MM v[16:19],a[200:203],v[200:203],v[16:19]
EX v6,v6
MM v[16:19],a[204:207],v[216:219],v[16:19]
TR a[154:155],v233 offset:20992
TR a[156:157],v233 offset:25088
MM v[20:23],a[220:223],v[216:219],v[136:139]
s_add_u32 s87, s95, 5120
buffer_store_dword v243, v248, s[20:23], s87 offen offset:12 sc1
MM v[20:23],a[216:219],v[200:203],v[20:23]
EX v7,v7
MM v[20:23],a[212:215],v[184:187],v[20:23]
TR a[158:159],v233 offset:29184
CV v3,v6,v7
MM v[20:23],a[208:211],v[168:171],v[20:23]
EX v8,v8
MM v[28:31],a[208:211],v[172:175],v[140:143]
EX v9,v9
MM v[28:31],a[212:215],v[188:191],v[28:31]
TR a[160:161],v232 offset:17408
CV v8,v8,v9
MM v[28:31],a[216:219],v[204:207],v[28:31]
EX v10,v10
MM v[28:31],a[220:223],v[220:223],v[28:31]
EX v11,v11
MM v[24:27],a[204:207],v[220:223],v[140:143]
TR a[162:163],v232 offset:21504
CV v9,v10,v11
MM v[24:27],a[200:203],v[204:207],v[24:27]
TR a[164:165],v232 offset:25600
TR a[166:167],v232 offset:29696
MM v[24:27],a[196:199],v[188:191],v[24:27]
EX v12,v12
MM v[24:27],a[192:195],v[172:175],v[24:27]
EX v13,v13
MM v[32:35],a[224:227],v[160:163],v[128:131]
TR a[168:169],v233 offset:17408
CV v10,v12,v13
MM v[32:35],a[228:231],v[176:179],v[32:35]
EX v14,v14
MM v[32:35],a[232:235],v[192:195],v[32:35]
TR a[170:171],v233 offset:21504
TR a[172:173],v233 offset:25600
MM v[32:35],a[236:239],v[208:211],v[32:35]
EX v15,v15
MM v[36:39],a[252:255],v[208:211],v[128:131]
TR a[174:175],v233 offset:29696
CV v11,v14,v15
MM v[36:39],a[248:251],v[192:195],v[36:39]
EX v16,v16
MM v[36:39],a[244:247],v[176:179],v[36:39]
)ASM"
R"ASM(
EX v17,v17
MM v[36:39],a[240:243],v[160:163],v[36:39]
TR a[176:177],v232 offset:17920
CV v16,v16,v17
MM v[44:47],a[240:243],v[164:167],v[132:135]
EX v18,v18
MM v[44:47],a[244:247],v[180:183],v[44:47]
TR a[178:179],v232 offset:22016
TR a[180:181],v232 offset:26112
MM v[44:47],a[248:251],v[196:199],v[44:47]
EX v19,v19
MM v[44:47],a[252:255],v[212:215],v[44:47]
s_nop 0
LPG21_%=:
s_cbranch_execz LTR4_%=
TR a[182:183],v232 offset:30208
CV v17,v18,v19
MM v[40:43],a[236:239],v[212:215],v[132:135]
EX v20,v20
MM v[40:43],a[232:235],v[196:199],v[40:43]
EX v21,v21
MM v[40:43],a[228:231],v[180:183],v[40:43]
TR a[184:185],v233 offset:17920
CV v18,v20,v21
MM v[40:43],a[224:227],v[164:167],v[40:43]
EX v22,v22
MM v[48:51],a[224:227],v[168:171],v[136:139]
TR a[186:187],v233 offset:22016
TR a[188:189],v233 offset:26112
MM v[48:51],a[228:231],v[184:187],v[48:51]
EX v23,v23
MM v[48:51],a[232:235],v[200:203],v[48:51]
TR a[190:191],v233 offset:30208
CV v19,v22,v23
MM v[48:51],a[236:239],v[216:219],v[48:51]
EX v24,v24
MM v[52:55],a[252:255],v[216:219],v[136:139]
EX v25,v25
MM v[52:55],a[248:251],v[200:203],v[52:55]
EX v26,v26
MM v[52:55],a[244:247],v[184:187],v[52:55]
EX v27,v27
MM v[52:55],a[240:243],v[168:171],v[52:55]
EX v28,v28
MM v[60:63],a[240:243],v[172:175],v[140:143]
EX v29,v29
MM v[60:63],a[244:247],v[188:191],v[60:63]
EX v30,v30
MM v[60:63],a[248:251],v[204:207],v[60:63]
EX v31,v31
MM v[60:63],a[252:255],v[220:223],v[60:63]
EX v32,v32
MM v[56:59],a[236:239],v[220:223],v[140:143]
EX v33,v33
MM v[56:59],a[232:235],v[204:207],v[56:59]
EX v34,v34
MM v[56:59],a[228:231],v[188:191],v[56:59]
EX v35,v35
MM v[56:59],a[224:227],v[172:175],v[56:59]
s_waitcnt lgkmcnt(0)
s_barrier
MM a[0:3],a[128:131],v[64:67],a[0:3]
s_add_u32 m0, s65, 16384
CV v24,v24,v25
CV v25,v26,v27
MM a[0:3],a[132:135],v[68:71],a[0:3]
RD a[192:195],v224 offset:49152
CV v26,v28,v29
MM a[8:11],a[132:135],v[76:79],a[8:11]
RD a[196:199],v225 offset:49152
CV v27,v30,v31
MM a[8:11],a[128:131],v[72:75],a[8:11]
buffer_load_dwordx4 v228, s[8:11], s64 offen offset:0 lds
MM a[16:19],a[128:131],v[80:83],a[16:19]
RD a[200:203],v226 offset:49152
CV v4,v32,v33
MM a[16:19],a[132:135],v[84:87],a[16:19]
RD a[204:207],v227 offset:49152
CV v5,v34,v35
MM a[24:27],a[132:135],v[92:95],a[24:27]
buffer_load_dwordx4 v229, s[8:11], s64 offen offset:1024 lds
MM a[24:27],a[128:131],v[88:91],a[24:27]
RD a[208:211],v224 offset:53248
RD a[212:215],v225 offset:53248
MM a[28:31],a[136:139],v[88:91],a[28:31]
buffer_load_dwordx4 v230, s[8:11], s64 offen offset:2048 lds
MM a[28:31],a[140:143],v[92:95],a[28:31]
EX v36,v36
MM a[20:23],a[140:143],v[84:87],a[20:23]
EX v37,v37
MM a[20:23],a[136:139],v[80:83],a[20:23]
RD a[216:219],v226 offset:53248
CV v6,v36,v37
MM a[12:15],a[136:139],v[72:75],a[12:15]
EX v38,v38
MM a[12:15],a[140:143],v[76:79],a[12:15]
RD a[220:223],v227 offset:53248
RD a[224:227],v224 offset:57344
MM a[4:7],a[140:143],v[68:71],a[4:7]
EX v39,v39
MM a[4:7],a[136:139],v[64:67],a[4:7]
RD a[228:231],v225 offset:57344
CV v7,v38,v39
MM a[32:35],a[144:147],v[64:67],a[32:35]
EX v40,v40
MM a[32:35],a[148:151],v[68:71],a[32:35]
buffer_load_dwordx4 v231, s[8:11], s64 offen offset:3072 lds
MM a[40:43],a[148:151],v[76:79],a[40:43]
s_add_u32 m0, s65, 81920
EX v41,v41
MM a[40:43],a[144:147],v[72:75],a[40:43]
RD a[232:235],v226 offset:57344
CV v12,v40,v41
MM a[48:51],a[144:147],v[80:83],a[48:51]
EX v42,v42
MM a[48:51],a[148:151],v[84:87],a[48:51]
EX v43,v43
MM a[56:59],a[148:151],v[92:95],a[56:59]
RD a[236:239],v227 offset:57344
CV v13,v42,v43
MM a[56:59],a[144:147],v[88:91],a[56:59]
EX v44,v44
MM a[60:63],a[152:155],v[88:91],a[60:63]
buffer_load_dwordx4 v235, s[12:15], s81 offen offset:0 lds
MM a[60:63],a[156:159],v[92:95],a[60:63]
EX v45,v45
MM a[52:55],a[156:159],v[84:87],a[52:55]
RD a[240:243],v224 offset:61440
CV v14,v44,v45
MM a[52:55],a[152:155],v[80:83],a[52:55]
EX v46,v46
MM a[44:47],a[152:155],v[72:75],a[44:47]
EX v47,v47
MM a[44:47],a[156:159],v[76:79],a[44:47]
RD a[244:247],v225 offset:61440
CV v15,v46,v47
MM a[36:39],a[156:159],v[68:71],a[36:39]
buffer_load_dwordx4 v235, s[12:15], s64 offen offset:1024 lds
MM a[36:39],a[152:155],v[64:67],a[36:39]
EX v48,v48
MM a[64:67],a[160:163],v[64:67],a[64:67]
EX v49,v49
MM a[64:67],a[164:167],v[68:71],a[64:67]
RD a[248:251],v226 offset:61440
CV v20,v48,v49
MM a[72:75],a[164:167],v[76:79],a[72:75]
EX v50,v50
MM a[72:75],a[160:163],v[72:75],a[72:75]
EX v51,v51
MM a[80:83],a[160:163],v[80:83],a[80:83]
RD a[252:255],v227 offset:61440
CV v21,v50,v51
MM a[80:83],a[164:167],v[84:87],a[80:83]
buffer_load_dwordx4 v235, s[12:15], s81 offen offset:2048 lds
MM a[88:91],a[164:167],v[92:95],a[88:91]
EX v52,v52
MM a[88:91],a[160:163],v[88:91],a[88:91]
EX v53,v53
MM a[92:95],a[168:171],v[88:91],a[92:95]
EX v54,v54
MM a[92:95],a[172:175],v[92:95],a[92:95]
buffer_load_dwordx4 v235, s[12:15], s64 offen offset:3072 lds
MM a[84:87],a[172:175],v[84:87],a[84:87]
EX v55,v55
MM a[84:87],a[168:171],v[80:83],a[84:87]
EX v56,v56
MM a[76:79],a[168:171],v[72:75],a[76:79]
EX v57,v57
MM a[76:79],a[172:175],v[76:79],a[76:79]
EX v58,v58
MM a[68:71],a[172:175],v[68:71],a[68:71]
EX v59,v59
MM a[68:71],a[168:171],v[64:67],a[68:71]
EX v60,v60
MM a[96:99],a[176:179],v[64:67],a[96:99]
EX v61,v61
MM a[96:99],a[180:183],v[68:71],a[96:99]
EX v62,v62
MM a[104:107],a[180:183],v[76:79],a[104:107]
EX v63,v63
MM a[104:107],a[176:179],v[72:75],a[104:107]
CV v22,v52,v53
CV v23,v54,v55
MM a[112:115],a[176:179],v[80:83],a[112:115]
CV v28,v56,v57
CV v29,v58,v59
MM a[112:115],a[180:183],v[84:87],a[112:115]
CV v30,v60,v61
CV v31,v62,v63
MM a[120:123],a[180:183],v[92:95],a[120:123]
MM a[120:123],a[176:179],v[88:91],a[120:123]
MM a[124:127],a[184:187],v[88:91],a[124:127]
v_add_f32 v244, v144, v145
v_add_f32 v245, v148, v149
MM a[124:127],a[188:191],v[92:95],a[124:127]
v_add_f32 v246, v152, v153
v_add_f32 v247, v156, v157
MM a[116:119],a[188:191],v[84:87],a[116:119]
MM a[116:119],a[184:187],v[80:83],a[116:119]
MM a[108:111],a[184:187],v[72:75],a[108:111]
MM a[108:111],a[188:191],v[76:79],a[108:111]
MM a[100:103],a[188:191],v[68:71],a[100:103]
MM a[100:103],a[184:187],v[64:67],a[100:103]
v_smfmac_f32_16x16x64_bf16 v[144:147], v[236:239], v[0:7], v234
v_smfmac_f32_16x16x64_bf16 v[148:151], v[236:239], v[8:15], v234
v_smfmac_f32_16x16x64_bf16 v[152:155], v[236:239], v[16:23], v234
v_smfmac_f32_16x16x64_bf16 v[156:159], v[236:239], v[24:31], v234
s_waitcnt lgkmcnt(0)
MM v[64:67],a[192:195],v[160:163],v[128:131]
s_add_u32 s64, s64, 16384
TR a[128:129],v232 offset:32768
s_add_u32 s81, s64, 896
MM v[64:67],a[196:199],v[176:179],v[64:67]
TR a[130:131],v232 offset:36864
v_max3_f32 v249, v144, v145, v148
MM v[64:67],a[200:203],v[192:195],v[64:67]
TR a[132:133],v232 offset:40960
v_max3_f32 v249, v249, v149, v152
MM v[64:67],a[204:207],v[208:211],v[64:67]
TR a[134:135],v232 offset:45056
v_max3_f32 v249, v249, v153, v156
MM v[68:71],a[220:223],v[208:211],v[128:131]
TR a[136:137],v233 offset:32768
v_max_f32 v249, v249, v157
MM v[68:71],a[216:219],v[192:195],v[68:71]
TR a[138:139],v233 offset:36864
v_cmp_lt_f32 vcc, s71, v249
MM v[68:71],a[212:215],v[176:179],v[68:71]
TR a[140:141],v233 offset:40960
TR a[142:143],v233 offset:45056
MM v[68:71],a[208:211],v[160:163],v[68:71]
s_cbranch_vccnz LSITE30_%=
LBACK30_%=:
MM v[76:79],a[208:211],v[164:167],v[132:135]
s_and_b32 s90, s17, 63
s_cmp_lt_u32 s17, 80
s_cselect_b32 s17, s17, s90
s_cselect_b64 s[44:45], -1, 0
MM v[76:79],a[212:215],v[180:183],v[76:79]
v_lshrrev_b32 v253, 4, v248
v_min_u32 v253, 35, v253
MM v[76:79],a[216:219],v[196:199],v[76:79]
v_lshrrev_b32 v252, 2, v253
v_mul_u32_u24 v252, 144, v252
MM v[76:79],a[220:223],v[212:215],v[76:79]
TR a[144:145],v232 offset:33280
v_lshl_add_u32 v253, v253, 2, v252
MM v[72:75],a[204:207],v[212:215],v[132:135]
s_lshr_b32 s90, s17, 3
EX v64,v64
MM v[72:75],a[200:203],v[196:199],v[72:75]
s_lshl_b32 s76, s90, 4
EX v65,v65
MM v[72:75],a[196:199],v[180:183],v[72:75]
s_add_u32 s76, s76, 73728
buffer_load_dword v252, v253, s[20:23], s76 offen offset:0 sc1
MM v[72:75],a[192:195],v[164:167],v[72:75]
EX v66,v66
MM v[80:83],a[192:195],v[168:171],v[136:139]
EX v67,v67
MM v[80:83],a[196:199],v[184:187],v[80:83]
EX v68,v68
MM v[80:83],a[200:203],v[200:203],v[80:83]
TR a[146:147],v232 offset:37376
CV v64,v64,v65
MM v[80:83],a[204:207],v[216:219],v[80:83]
EX v69,v69
MM v[84:87],a[220:223],v[216:219],v[136:139]
TR a[148:149],v232 offset:41472
CV v65,v66,v67
MM v[84:87],a[216:219],v[200:203],v[84:87]
EX v70,v70
MM v[84:87],a[212:215],v[184:187],v[84:87]
TR a[150:151],v232 offset:45568
CV v66,v68,v69
MM v[84:87],a[208:211],v[168:171],v[84:87]
EX v71,v71
MM v[92:95],a[208:211],v[172:175],v[140:143]
TR a[152:153],v233 offset:33280
TR a[154:155],v233 offset:37376
MM v[92:95],a[212:215],v[188:191],v[92:95]
EX v72,v72
MM v[92:95],a[216:219],v[204:207],v[92:95]
EX v73,v73
MM v[92:95],a[220:223],v[220:223],v[92:95]
TR a[156:157],v233 offset:41472
CV v67,v70,v71
MM v[88:91],a[204:207],v[220:223],v[140:143]
EX v74,v74
MM v[88:91],a[200:203],v[204:207],v[88:91]
TR a[158:159],v233 offset:45568
CV v72,v72,v73
MM v[88:91],a[196:199],v[188:191],v[88:91]
EX v75,v75
MM v[88:91],a[192:195],v[172:175],v[88:91]
TR a[160:161],v232 offset:33792
TR a[162:163],v232 offset:37888
MM v[96:99],a[224:227],v[160:163],v[128:131]
EX v76,v76
MM v[96:99],a[228:231],v[176:179],v[96:99]
TR a[164:165],v232 offset:41984
CV v73,v74,v75
MM v[96:99],a[232:235],v[192:195],v[96:99]
EX v77,v77
MM v[96:99],a[236:239],v[208:211],v[96:99]
EX v78,v78
MM v[100:103],a[252:255],v[208:211],v[128:131]
TR a[166:167],v232 offset:46080
CV v74,v76,v77
MM v[100:103],a[248:251],v[192:195],v[100:103]
EX v79,v79
MM v[100:103],a[244:247],v[176:179],v[100:103]
TR a[168:169],v233 offset:33792
CV v75,v78,v79
MM v[100:103],a[240:243],v[160:163],v[100:103]
EX v80,v80
MM v[108:111],a[240:243],v[164:167],v[132:135]
TR a[170:171],v233 offset:37888
TR a[172:173],v233 offset:41984
MM v[108:111],a[244:247],v[180:183],v[108:111]
EX v81,v81
MM v[108:111],a[248:251],v[196:199],v[108:111]
TR a[174:175],v233 offset:46080
CV v80,v80,v81
MM v[108:111],a[252:255],v[212:215],v[108:111]
EX v82,v82
MM v[104:107],a[236:239],v[212:215],v[132:135]
EX v83,v83
MM v[104:107],a[232:235],v[196:199],v[104:107]
TR a[176:177],v232 offset:34304
TR a[178:179],v232 offset:38400
MM v[104:107],a[228:231],v[180:183],v[104:107]
EX v84,v84
MM v[104:107],a[224:227],v[164:167],v[104:107]
TR a[180:181],v232 offset:42496
CV v81,v82,v83
MM v[112:115],a[224:227],v[168:171],v[136:139]
EX v85,v85
MM v[112:115],a[228:231],v[184:187],v[112:115]
TR a[182:183],v232 offset:46592
CV v82,v84,v85
MM v[112:115],a[232:235],v[200:203],v[112:115]
EX v86,v86
MM v[112:115],a[236:239],v[216:219],v[112:115]
TR a[184:185],v233 offset:34304
TR a[186:187],v233 offset:38400
MM v[116:119],a[252:255],v[216:219],v[136:139]
EX v87,v87
MM v[116:119],a[248:251],v[200:203],v[116:119]
TR a[188:189],v233 offset:42496
CV v83,v86,v87
MM v[116:119],a[244:247],v[184:187],v[116:119]
EX v88,v88
MM v[116:119],a[240:243],v[168:171],v[116:119]
EX v89,v89
MM v[124:127],a[240:243],v[172:175],v[140:143]
TR a[190:191],v233 offset:46592
CV v88,v88,v89
MM v[124:127],a[244:247],v[188:191],v[124:127]
EX v90,v90
MM v[124:127],a[248:251],v[204:207],v[124:127]
EX v91,v91
MM v[124:127],a[252:255],v[220:223],v[124:127]
EX v92,v92
MM v[120:123],a[236:239],v[220:223],v[140:143]
EX v93,v93
MM v[120:123],a[232:235],v[204:207],v[120:123]
EX v94,v94
MM v[120:123],a[228:231],v[188:191],v[120:123]
EX v95,v95
MM v[120:123],a[224:227],v[172:175],v[120:123]
s_waitcnt vmcnt(15) lgkmcnt(0)
s_barrier
MM a[0:3],a[128:131],v[0:3],a[0:3]
s_add_u32 m0, s65, 32768
CV v89,v90,v91
CV v90,v92,v93
MM a[0:3],a[132:135],v[4:7],a[0:3]
RD a[192:195],v224 offset:0
CV v91,v94,v95
MM a[8:11],a[132:135],v[12:15],a[8:11]
buffer_load_dwordx4 v228, s[8:11], s64 offen offset:0 lds
MM a[8:11],a[128:131],v[8:11],a[8:11]
RD a[196:199],v225 offset:0
RD a[200:203],v226 offset:0
MM a[16:19],a[128:131],v[16:19],a[16:19]
EX v96,v96
MM a[16:19],a[132:135],v[20:23],a[16:19]
EX v97,v97
MM a[24:27],a[132:135],v[28:31],a[24:27]
)ASM"
R"ASM(
RD a[204:207],v227 offset:0
CV v68,v96,v97
MM a[24:27],a[128:131],v[24:27],a[24:27]
EX v98,v98
MM a[28:31],a[136:139],v[24:27],a[28:31]
buffer_load_dwordx4 v229, s[8:11], s64 offen offset:1024 lds
MM a[28:31],a[140:143],v[28:31],a[28:31]
EX v99,v99
MM a[20:23],a[140:143],v[20:23],a[20:23]
RD a[208:211],v224 offset:4096
CV v69,v98,v99
MM a[20:23],a[136:139],v[16:19],a[20:23]
EX v100,v100
MM a[12:15],a[136:139],v[8:11],a[12:15]
EX v101,v101
MM a[12:15],a[140:143],v[12:15],a[12:15]
RD a[212:215],v225 offset:4096
CV v70,v100,v101
MM a[4:7],a[140:143],v[4:7],a[4:7]
EX v102,v102
MM a[4:7],a[136:139],v[0:3],a[4:7]
buffer_load_dwordx4 v230, s[8:11], s64 offen offset:2048 lds
MM a[32:35],a[144:147],v[0:3],a[32:35]
EX v103,v103
MM a[32:35],a[148:151],v[4:7],a[32:35]
RD a[216:219],v226 offset:4096
CV v71,v102,v103
MM a[40:43],a[148:151],v[12:15],a[40:43]
EX v104,v104
MM a[40:43],a[144:147],v[8:11],a[40:43]
EX v105,v105
MM a[48:51],a[144:147],v[16:19],a[48:51]
RD a[220:223],v227 offset:4096
CV v76,v104,v105
MM a[48:51],a[148:151],v[20:23],a[48:51]
EX v106,v106
MM a[56:59],a[148:151],v[28:31],a[56:59]
buffer_load_dwordx4 v231, s[8:11], s64 offen offset:3072 lds
MM a[56:59],a[144:147],v[24:27],a[56:59]
s_add_u32 m0, s65, 98304
EX v107,v107
MM a[60:63],a[152:155],v[24:27],a[60:63]
RD a[224:227],v224 offset:8192
CV v77,v106,v107
MM a[60:63],a[156:159],v[28:31],a[60:63]
EX v108,v108
MM a[52:55],a[156:159],v[20:23],a[52:55]
EX v109,v109
MM a[52:55],a[152:155],v[16:19],a[52:55]
RD a[228:231],v225 offset:8192
CV v78,v108,v109
MM a[44:47],a[152:155],v[8:11],a[44:47]
EX v110,v110
MM a[44:47],a[156:159],v[12:15],a[44:47]
buffer_load_dwordx4 v235, s[12:15], s81 offen offset:0 lds
MM a[36:39],a[156:159],v[4:7],a[36:39]
EX v111,v111
MM a[36:39],a[152:155],v[0:3],a[36:39]
RD a[232:235],v226 offset:8192
CV v79,v110,v111
MM a[64:67],a[160:163],v[0:3],a[64:67]
EX v112,v112
MM a[64:67],a[164:167],v[4:7],a[64:67]
EX v113,v113
MM a[72:75],a[164:167],v[12:15],a[72:75]
RD a[236:239],v227 offset:8192
CV v84,v112,v113
MM a[72:75],a[160:163],v[8:11],a[72:75]
buffer_load_dwordx4 v235, s[12:15], s64 offen offset:1024 lds
MM a[80:83],a[160:163],v[16:19],a[80:83]
EX v114,v114
MM a[80:83],a[164:167],v[20:23],a[80:83]
EX v115,v115
MM a[88:91],a[164:167],v[28:31],a[88:91]
RD a[240:243],v224 offset:12288
CV v85,v114,v115
MM a[88:91],a[160:163],v[24:27],a[88:91]
EX v116,v116
MM a[92:95],a[168:171],v[24:27],a[92:95]
EX v117,v117
MM a[92:95],a[172:175],v[28:31],a[92:95]
RD a[244:247],v225 offset:12288
CV v86,v116,v117
MM a[84:87],a[172:175],v[20:23],a[84:87]
buffer_load_dwordx4 v235, s[12:15], s81 offen offset:2048 lds
MM a[84:87],a[168:171],v[16:19],a[84:87]
EX v118,v118
MM a[76:79],a[168:171],v[8:11],a[76:79]
EX v119,v119
MM a[76:79],a[172:175],v[12:15],a[76:79]
RD a[248:251],v226 offset:12288
CV v87,v118,v119
MM a[68:71],a[172:175],v[4:7],a[68:71]
EX v120,v120
MM a[68:71],a[168:171],v[0:3],a[68:71]
EX v121,v121
MM a[96:99],a[176:179],v[0:3],a[96:99]
RD a[252:255],v227 offset:12288
CV v92,v120,v121
MM a[96:99],a[180:183],v[4:7],a[96:99]
buffer_load_dwordx4 v235, s[12:15], s64 offen offset:3072 lds
MM a[104:107],a[180:183],v[12:15],a[104:107]
EX v122,v122
MM a[104:107],a[176:179],v[8:11],a[104:107]
EX v123,v123
MM a[112:115],a[176:179],v[16:19],a[112:115]
EX v124,v124
MM a[112:115],a[180:183],v[20:23],a[112:115]
EX v125,v125
MM a[120:123],a[180:183],v[28:31],a[120:123]
EX v126,v126
MM a[120:123],a[176:179],v[24:27],a[120:123]
EX v127,v127
MM a[124:127],a[184:187],v[24:27],a[124:127]
v_add_f32 v244, v144, v145
v_add_f32 v245, v148, v149
MM a[124:127],a[188:191],v[28:31],a[124:127]
v_add_f32 v246, v152, v153
v_add_f32 v247, v156, v157
MM a[116:119],a[188:191],v[20:23],a[116:119]
CV v93,v122,v123
CV v94,v124,v125
MM a[116:119],a[184:187],v[16:19],a[116:119]
CV v95,v126,v127
MM a[108:111],a[184:187],v[8:11],a[108:111]
MM a[108:111],a[188:191],v[12:15],a[108:111]
MM a[100:103],a[188:191],v[4:7],a[100:103]
MM a[100:103],a[184:187],v[0:3],a[100:103]
v_smfmac_f32_16x16x64_bf16 v[144:147], v[236:239], v[64:71], v234
v_smfmac_f32_16x16x64_bf16 v[148:151], v[236:239], v[72:79], v234
v_smfmac_f32_16x16x64_bf16 v[152:155], v[236:239], v[80:87], v234
v_smfmac_f32_16x16x64_bf16 v[156:159], v[236:239], v[88:95], v234
s_waitcnt lgkmcnt(0)
MM v[0:3],a[192:195],v[160:163],v[128:131]
s_add_u32 s64, s64, 16384
TR a[128:129],v232 offset:49152
s_add_u32 s81, s64, 896
MM v[0:3],a[196:199],v[176:179],v[0:3]
TR a[130:131],v232 offset:53248
v_max3_f32 v249, v144, v145, v148
MM v[0:3],a[200:203],v[192:195],v[0:3]
TR a[132:133],v232 offset:57344
v_max3_f32 v249, v249, v149, v152
MM v[0:3],a[204:207],v[208:211],v[0:3]
TR a[134:135],v232 offset:61440
v_max3_f32 v249, v249, v153, v156
MM v[4:7],a[220:223],v[208:211],v[128:131]
TR a[136:137],v233 offset:49152
v_max_f32 v249, v249, v157
MM v[4:7],a[216:219],v[192:195],v[4:7]
TR a[138:139],v233 offset:53248
v_cmp_lt_f32 vcc, s71, v249
MM v[4:7],a[212:215],v[176:179],v[4:7]
TR a[140:141],v233 offset:57344
TR a[142:143],v233 offset:61440
MM v[4:7],a[208:211],v[160:163],v[4:7]
s_cbranch_vccnz LSITE31_%=
LBACK31_%=:
MM v[12:15],a[208:211],v[164:167],v[132:135]
s_waitcnt vmcnt(8)
v_cmp_ne_u32 vcc, s69, v252
s_cbranch_vccnz LRFY_%=
LRFYb_%=:
MM v[12:15],a[212:215],v[180:183],v[12:15]
v_lshrrev_b32 v109, 2, v248
v_lshrrev_b32 v110, 3, v248
MM v[12:15],a[216:219],v[196:199],v[12:15]
s_and_b32 s76, s17, 7
EX v0,v0
MM v[12:15],a[220:223],v[212:215],v[12:15]
s_mul_i32 s76, s76, 2560
EX v1,v1
MM v[8:11],a[204:207],v[212:215],v[132:135]
s_lshl_b32 s91, s90, 8
EX v2,v2
MM v[8:11],a[200:203],v[196:199],v[8:11]
s_add_u32 s76, s76, s91
TR a[144:145],v232 offset:49664
CV v0,v0,v1
MM v[8:11],a[196:199],v[180:183],v[8:11]
s_add_u32 s76, s76, 21659648
buffer_load_dword v96, v109, s[20:23], s76 offen offset:0 sc1
MM v[8:11],a[192:195],v[164:167],v[8:11]
s_add_u32 s76, s76, 20480
EX v3,v3
MM v[16:19],a[192:195],v[168:171],v[136:139]
buffer_load_dword v97, v109, s[20:23], s76 offen offset:0 sc1
MM v[16:19],a[196:199],v[184:187],v[16:19]
s_add_u32 s76, s76, 20480
EX v4,v4
MM v[16:19],a[200:203],v[200:203],v[16:19]
buffer_load_dword v98, v109, s[20:23], s76 offen offset:0 sc1
MM v[16:19],a[204:207],v[216:219],v[16:19]
s_add_u32 s76, s76, 20480
EX v5,v5
MM v[20:23],a[220:223],v[216:219],v[136:139]
buffer_load_dword v99, v109, s[20:23], s76 offen offset:0 sc1
MM v[20:23],a[216:219],v[200:203],v[20:23]
s_add_u32 s76, s76, 20480
TR a[146:147],v232 offset:53760
TR a[148:149],v232 offset:57856
MM v[20:23],a[212:215],v[184:187],v[20:23]
buffer_load_dword v100, v109, s[20:23], s76 offen offset:0 sc1
MM v[20:23],a[208:211],v[168:171],v[20:23]
s_add_u32 s76, s76, 20480
EX v6,v6
MM v[28:31],a[208:211],v[172:175],v[140:143]
buffer_load_dword v101, v109, s[20:23], s76 offen offset:0 sc1
MM v[28:31],a[212:215],v[188:191],v[28:31]
s_add_u32 s76, s76, 20480
EX v7,v7
MM v[28:31],a[216:219],v[204:207],v[28:31]
buffer_load_dword v102, v109, s[20:23], s76 offen offset:0 sc1
MM v[28:31],a[220:223],v[220:223],v[28:31]
s_add_u32 s76, s76, 20480
s_nop 0
LPG22_%=:
s_cbranch_execz LTR4_%=
TR a[150:151],v232 offset:61952
EX v8,v8
MM v[24:27],a[204:207],v[220:223],v[140:143]
buffer_load_dword v103, v109, s[20:23], s76 offen offset:0 sc1
MM v[24:27],a[200:203],v[204:207],v[24:27]
s_add_u32 s76, s76, 20480
TR a[152:153],v233 offset:49664
EX v9,v9
CV v1,v2,v3
MM v[24:27],a[196:199],v[188:191],v[24:27]
buffer_load_dword v104, v109, s[20:23], s76 offen offset:0 sc1
TR a[154:155],v233 offset:53760
MM v[24:27],a[192:195],v[172:175],v[24:27]
s_and_b32 s76, s19, 65535
EX v10,v10
MM v[32:35],a[224:227],v[160:163],v[128:131]
s_sub_u32 s76, 65535, s76
TR a[156:157],v233 offset:57856
CV v2,v4,v5
EX v11,v11
MM v[32:35],a[228:231],v[176:179],v[32:35]
s_mul_i32 s76, s76, 1280
TR a[158:159],v233 offset:61952
CV v3,v6,v7
MM v[32:35],a[232:235],v[192:195],v[32:35]
s_lshl_b32 s91, s90, 7
EX v12,v12
MM v[32:35],a[236:239],v[208:211],v[32:35]
s_add_u32 s76, s76, s91
TR a[160:161],v232 offset:50176
TR a[162:163],v232 offset:54272
MM v[36:39],a[252:255],v[208:211],v[128:131]
s_add_u32 s76, s76, 131072
buffer_load_ushort v108, v110, s[20:23], s76 offen sc1
EX v13,v13
MM v[36:39],a[248:251],v[192:195],v[36:39]
s_ashr_i32 s91, s19, 31
EX v14,v14
MM v[36:39],a[244:247],v[176:179],v[36:39]
s_not_b32 s91, s91
TR a[164:165],v232 offset:58368
CV v8,v8,v9
MM v[36:39],a[240:243],v[160:163],v[36:39]
s_or_b32 s91, s91, 0x80000000
EX v15,v15
MM v[44:47],a[240:243],v[164:167],v[132:135]
s_xor_b32 s19, s19, s91
TR a[166:167],v232 offset:62464
CV v9,v10,v11
MM v[44:47],a[244:247],v[180:183],v[44:47]
s_and_b32 s19, s19, 0xffff0000
v_mov_b32 v111, 0xbfb8aa3b
v_mul_f32 v111, s19, v111
MM v[44:47],a[248:251],v[196:199],v[44:47]
EX v111,v111
EX v16,v16
MM v[44:47],a[252:255],v[212:215],v[44:47]
TR a[168:169],v233 offset:50176
v_add_f32 v111, 1.0, v111
EX v17,v17
MM v[40:43],a[236:239],v[212:215],v[132:135]
v_rcp_f32 v111, v111
MM v[40:43],a[232:235],v[196:199],v[40:43]
TR a[170:171],v233 offset:54272
CV v111,v111,v111
v_and_b32 v111, 0xffff0000, v111
EX v18,v18
MM v[40:43],a[228:231],v[180:183],v[40:43]
TR a[172:173],v233 offset:58368
CV v10,v12,v13
MM v[40:43],a[224:227],v[164:167],v[40:43]
EX v19,v19
MM v[48:51],a[224:227],v[168:171],v[136:139]
TR a[174:175],v233 offset:62464
CV v11,v14,v15
MM v[48:51],a[228:231],v[184:187],v[48:51]
EX v20,v20
MM v[48:51],a[232:235],v[200:203],v[48:51]
TR a[176:177],v232 offset:50688
TR a[178:179],v232 offset:54784
MM v[48:51],a[236:239],v[216:219],v[48:51]
EX v21,v21
MM v[52:55],a[252:255],v[216:219],v[136:139]
EX v22,v22
MM v[52:55],a[248:251],v[200:203],v[52:55]
TR a[180:181],v232 offset:58880
CV v16,v16,v17
MM v[52:55],a[244:247],v[184:187],v[52:55]
EX v23,v23
MM v[52:55],a[240:243],v[168:171],v[52:55]
TR a[182:183],v232 offset:62976
CV v17,v18,v19
MM v[60:63],a[240:243],v[172:175],v[140:143]
EX v24,v24
MM v[60:63],a[244:247],v[188:191],v[60:63]
TR a[184:185],v233 offset:50688
TR a[186:187],v233 offset:54784
MM v[60:63],a[248:251],v[204:207],v[60:63]
TR a[188:189],v233 offset:58880
TR a[190:191],v233 offset:62976
EX v25,v25
MM v[60:63],a[252:255],v[220:223],v[60:63]
EX v26,v26
MM v[56:59],a[236:239],v[220:223],v[140:143]
EX v27,v27
MM v[56:59],a[232:235],v[204:207],v[56:59]
EX v28,v28
MM v[56:59],a[228:231],v[188:191],v[56:59]
EX v29,v29
CV v18,v20,v21
MM v[56:59],a[224:227],v[172:175],v[56:59]
s_waitcnt lgkmcnt(0)
s_barrier
MM a[0:3],a[128:131],v[64:67],a[0:3]
s_add_u32 m0, s65, 49152
CV v19,v22,v23
CV v24,v24,v25
MM a[0:3],a[132:135],v[68:71],a[0:3]
CV v25,v26,v27
CV v26,v28,v29
MM a[8:11],a[132:135],v[76:79],a[8:11]
EX v30,v30
MM a[8:11],a[128:131],v[72:75],a[8:11]
EX v31,v31
MM a[16:19],a[128:131],v[80:83],a[16:19]
RD a[192:195],v224 offset:16384
CV v27,v30,v31
MM a[16:19],a[132:135],v[84:87],a[16:19]
EX v32,v32
MM a[24:27],a[132:135],v[92:95],a[24:27]
buffer_load_dwordx4 v228, s[8:11], s64 offen offset:0 lds
MM a[24:27],a[128:131],v[88:91],a[24:27]
EX v33,v33
MM a[28:31],a[136:139],v[88:91],a[28:31]
RD a[196:199],v225 offset:16384
CV v4,v32,v33
MM a[28:31],a[140:143],v[92:95],a[28:31]
EX v34,v34
MM a[20:23],a[140:143],v[84:87],a[20:23]
EX v35,v35
MM a[20:23],a[136:139],v[80:83],a[20:23]
RD a[200:203],v226 offset:16384
CV v5,v34,v35
MM a[12:15],a[136:139],v[72:75],a[12:15]
EX v36,v36
MM a[12:15],a[140:143],v[76:79],a[12:15]
buffer_load_dwordx4 v229, s[8:11], s64 offen offset:1024 lds
MM a[4:7],a[140:143],v[68:71],a[4:7]
EX v37,v37
MM a[4:7],a[136:139],v[64:67],a[4:7]
RD a[204:207],v227 offset:16384
CV v6,v36,v37
MM a[32:35],a[144:147],v[64:67],a[32:35]
EX v38,v38
MM a[32:35],a[148:151],v[68:71],a[32:35]
EX v39,v39
MM a[40:43],a[148:151],v[76:79],a[40:43]
RD a[208:211],v224 offset:20480
CV v7,v38,v39
MM a[40:43],a[144:147],v[72:75],a[40:43]
EX v40,v40
MM a[48:51],a[144:147],v[80:83],a[48:51]
buffer_load_dwordx4 v230, s[8:11], s64 offen offset:2048 lds
MM a[48:51],a[148:151],v[84:87],a[48:51]
EX v41,v41
MM a[56:59],a[148:151],v[92:95],a[56:59]
RD a[212:215],v225 offset:20480
CV v12,v40,v41
MM a[56:59],a[144:147],v[88:91],a[56:59]
EX v42,v42
MM a[60:63],a[152:155],v[88:91],a[60:63]
)ASM"
R"ASM(
EX v43,v43
MM a[60:63],a[156:159],v[92:95],a[60:63]
RD a[216:219],v226 offset:20480
CV v13,v42,v43
MM a[52:55],a[156:159],v[84:87],a[52:55]
EX v44,v44
MM a[52:55],a[152:155],v[80:83],a[52:55]
buffer_load_dwordx4 v231, s[8:11], s64 offen offset:3072 lds
MM a[44:47],a[152:155],v[72:75],a[44:47]
s_add_u32 m0, s65, 114688
EX v45,v45
MM a[44:47],a[156:159],v[76:79],a[44:47]
RD a[220:223],v227 offset:20480
CV v14,v44,v45
MM a[36:39],a[156:159],v[68:71],a[36:39]
EX v46,v46
MM a[36:39],a[152:155],v[64:67],a[36:39]
RD a[224:227],v224 offset:24576
RD a[228:231],v225 offset:24576
MM a[64:67],a[160:163],v[64:67],a[64:67]
EX v47,v47
MM a[64:67],a[164:167],v[68:71],a[64:67]
RD a[232:235],v226 offset:24576
CV v15,v46,v47
MM a[72:75],a[164:167],v[76:79],a[72:75]
EX v48,v48
MM a[72:75],a[160:163],v[72:75],a[72:75]
buffer_load_dwordx4 v235, s[12:15], s81 offen offset:0 lds
MM a[80:83],a[160:163],v[80:83],a[80:83]
EX v49,v49
MM a[80:83],a[164:167],v[84:87],a[80:83]
RD a[236:239],v227 offset:24576
CV v20,v48,v49
MM a[88:91],a[164:167],v[92:95],a[88:91]
EX v50,v50
MM a[88:91],a[160:163],v[88:91],a[88:91]
EX v51,v51
MM a[92:95],a[168:171],v[88:91],a[92:95]
RD a[240:243],v224 offset:28672
CV v21,v50,v51
MM a[92:95],a[172:175],v[92:95],a[92:95]
EX v52,v52
MM a[84:87],a[172:175],v[84:87],a[84:87]
buffer_load_dwordx4 v235, s[12:15], s64 offen offset:1024 lds
MM a[84:87],a[168:171],v[80:83],a[84:87]
EX v53,v53
MM a[76:79],a[168:171],v[72:75],a[76:79]
RD a[244:247],v225 offset:28672
CV v22,v52,v53
MM a[76:79],a[172:175],v[76:79],a[76:79]
EX v54,v54
MM a[68:71],a[172:175],v[68:71],a[68:71]
s_waitcnt vmcnt(6)
v_add_f32 v96, v96, v100
v_add_f32 v97, v97, v101
MM a[68:71],a[168:171],v[64:67],a[68:71]
v_add_f32 v98, v98, v102
v_add_f32 v99, v99, v103
MM a[96:99],a[176:179],v[64:67],a[96:99]
v_add_f32 v96, v96, v98
v_add_f32 v97, v97, v99
MM a[96:99],a[180:183],v[68:71],a[96:99]
v_add_f32 v96, v96, v104
v_lshlrev_b32 v108, 16, v108
MM a[104:107],a[180:183],v[76:79],a[104:107]
v_add_f32 v96, v96, v97
CV v96,v96,v96
EX v55,v55
MM a[104:107],a[176:179],v[72:75],a[104:107]
v_and_b32 v96, 0xffff0000, v96
v_mul_f32 v96, v96, v111
MM a[112:115],a[176:179],v[80:83],a[112:115]
CV v96,v96,v96
v_and_b32 v96, 0xffff0000, v96
EX v56,v56
MM a[112:115],a[180:183],v[84:87],a[112:115]
v_add_f32 v96, v96, v108
CV v96,v96,v96
MM a[120:123],a[180:183],v[92:95],a[120:123]
s_mov_b64 exec, s[44:45]
buffer_store_short v96, v110, s[20:23], s76 offen offset:0 sc1
s_mov_b64 exec, -1
s_nop 0
EX v57,v57
MM a[120:123],a[176:179],v[88:91],a[120:123]
EX v58,v58
MM a[124:127],a[184:187],v[88:91],a[124:127]
v_add_f32 v244, v144, v145
v_add_f32 v245, v148, v149
MM a[124:127],a[188:191],v[92:95],a[124:127]
v_add_f32 v246, v152, v153
v_add_f32 v247, v156, v157
MM a[116:119],a[188:191],v[84:87],a[116:119]
EX v59,v59
MM a[116:119],a[184:187],v[80:83],a[116:119]
EX v60,v60
MM a[108:111],a[184:187],v[72:75],a[108:111]
CV v23,v54,v55
CV v28,v56,v57
MM a[108:111],a[188:191],v[76:79],a[108:111]
EX v61,v61
MM a[100:103],a[188:191],v[68:71],a[100:103]
buffer_load_dwordx4 v235, s[12:15], s81 offen offset:2048 lds
MM a[100:103],a[184:187],v[64:67],a[100:103]
EX v62,v62
v_smfmac_f32_16x16x64_bf16 v[144:147], v[236:239], v[0:7], v234
RD a[248:251],v226 offset:28672
RD a[252:255],v227 offset:28672
EX v63,v63
v_smfmac_f32_16x16x64_bf16 v[148:151], v[236:239], v[8:15], v234
CV v29,v58,v59
v_smfmac_f32_16x16x64_bf16 v[152:155], v[236:239], v[16:23], v234
CV v30,v60,v61
CV v31,v62,v63
s_nop 1
v_smfmac_f32_16x16x64_bf16 v[156:159], v[236:239], v[24:31], v234
s_waitcnt lgkmcnt(0)
buffer_load_dwordx4 v235, s[12:15], s64 offen offset:3072 lds
MM v[64:67],a[192:195],v[160:163],v[128:131]
s_sub_u32 s64, s64, 507904
TR a[128:129],v232 offset:0
s_add_u32 s81, s64, 896
MM v[64:67],a[196:199],v[176:179],v[64:67]
TR a[130:131],v232 offset:4096
v_max3_f32 v249, v144, v145, v148
MM v[64:67],a[200:203],v[192:195],v[64:67]
TR a[132:133],v232 offset:8192
v_max3_f32 v249, v249, v149, v152
MM v[64:67],a[204:207],v[208:211],v[64:67]
TR a[134:135],v232 offset:12288
v_max3_f32 v249, v249, v153, v156
MM v[68:71],a[220:223],v[208:211],v[128:131]
TR a[136:137],v233 offset:0
v_max_f32 v249, v249, v157
MM v[68:71],a[216:219],v[192:195],v[68:71]
TR a[138:139],v233 offset:4096
v_cmp_lt_f32 vcc, s71, v249
MM v[68:71],a[212:215],v[176:179],v[68:71]
TR a[140:141],v233 offset:8192
TR a[142:143],v233 offset:12288
MM v[68:71],a[208:211],v[160:163],v[68:71]
s_cbranch_vccnz LSITE32_%=
LBACK32_%=:
MM v[76:79],a[208:211],v[164:167],v[132:135]
EX v64,v64
MM v[76:79],a[212:215],v[180:183],v[76:79]
EX v65,v65
MM v[76:79],a[216:219],v[196:199],v[76:79]
TR a[144:145],v232 offset:512
CV v64,v64,v65
MM v[76:79],a[220:223],v[212:215],v[76:79]
EX v66,v66
MM v[72:75],a[204:207],v[212:215],v[132:135]
EX v67,v67
MM v[72:75],a[200:203],v[196:199],v[72:75]
TR a[146:147],v232 offset:4608
CV v65,v66,v67
MM v[72:75],a[196:199],v[180:183],v[72:75]
EX v68,v68
MM v[72:75],a[192:195],v[164:167],v[72:75]
TR a[148:149],v232 offset:8704
TR a[150:151],v232 offset:12800
MM v[80:83],a[192:195],v[168:171],v[136:139]
EX v69,v69
MM v[80:83],a[196:199],v[184:187],v[80:83]
TR a[152:153],v233 offset:512
CV v66,v68,v69
MM v[80:83],a[200:203],v[200:203],v[80:83]
EX v70,v70
MM v[80:83],a[204:207],v[216:219],v[80:83]
TR a[154:155],v233 offset:4608
TR a[156:157],v233 offset:8704
MM v[84:87],a[220:223],v[216:219],v[136:139]
EX v71,v71
MM v[84:87],a[216:219],v[200:203],v[84:87]
TR a[158:159],v233 offset:12800
CV v67,v70,v71
MM v[84:87],a[212:215],v[184:187],v[84:87]
EX v72,v72
MM v[84:87],a[208:211],v[168:171],v[84:87]
EX v73,v73
MM v[92:95],a[208:211],v[172:175],v[140:143]
TR a[160:161],v232 offset:1024
CV v72,v72,v73
MM v[92:95],a[212:215],v[188:191],v[92:95]
EX v74,v74
MM v[92:95],a[216:219],v[204:207],v[92:95]
TR a[162:163],v232 offset:5120
TR a[164:165],v232 offset:9216
MM v[92:95],a[220:223],v[220:223],v[92:95]
EX v75,v75
MM v[88:91],a[204:207],v[220:223],v[140:143]
TR a[166:167],v232 offset:13312
CV v73,v74,v75
MM v[88:91],a[200:203],v[204:207],v[88:91]
EX v76,v76
MM v[88:91],a[196:199],v[188:191],v[88:91]
EX v77,v77
MM v[88:91],a[192:195],v[172:175],v[88:91]
TR a[168:169],v233 offset:1024
CV v74,v76,v77
MM v[96:99],a[224:227],v[160:163],v[128:131]
EX v78,v78
MM v[96:99],a[228:231],v[176:179],v[96:99]
TR a[170:171],v233 offset:5120
TR a[172:173],v233 offset:9216
MM v[96:99],a[232:235],v[192:195],v[96:99]
EX v79,v79
MM v[96:99],a[236:239],v[208:211],v[96:99]
TR a[174:175],v233 offset:13312
CV v75,v78,v79
MM v[100:103],a[252:255],v[208:211],v[128:131]
EX v80,v80
MM v[100:103],a[248:251],v[192:195],v[100:103]
EX v81,v81
MM v[100:103],a[244:247],v[176:179],v[100:103]
TR a[176:177],v232 offset:1536
CV v80,v80,v81
MM v[100:103],a[240:243],v[160:163],v[100:103]
EX v82,v82
MM v[108:111],a[240:243],v[164:167],v[132:135]
TR a[178:179],v232 offset:5632
TR a[180:181],v232 offset:9728
MM v[108:111],a[244:247],v[180:183],v[108:111]
EX v83,v83
MM v[108:111],a[248:251],v[196:199],v[108:111]
TR a[182:183],v232 offset:13824
CV v81,v82,v83
MM v[108:111],a[252:255],v[212:215],v[108:111]
EX v84,v84
MM v[104:107],a[236:239],v[212:215],v[132:135]
TR a[184:185],v233 offset:1536
TR a[186:187],v233 offset:5632
MM v[104:107],a[232:235],v[196:199],v[104:107]
EX v85,v85
MM v[104:107],a[228:231],v[180:183],v[104:107]
TR a[188:189],v233 offset:9728
CV v82,v84,v85
MM v[104:107],a[224:227],v[164:167],v[104:107]
EX v86,v86
MM v[112:115],a[224:227],v[168:171],v[136:139]
EX v87,v87
MM v[112:115],a[228:231],v[184:187],v[112:115]
TR a[190:191],v233 offset:13824
CV v83,v86,v87
MM v[112:115],a[232:235],v[200:203],v[112:115]
EX v88,v88
MM v[112:115],a[236:239],v[216:219],v[112:115]
EX v89,v89
MM v[116:119],a[252:255],v[216:219],v[136:139]
EX v90,v90
MM v[116:119],a[248:251],v[200:203],v[116:119]
EX v91,v91
MM v[116:119],a[244:247],v[184:187],v[116:119]
EX v92,v92
MM v[116:119],a[240:243],v[168:171],v[116:119]
EX v93,v93
MM v[124:127],a[240:243],v[172:175],v[140:143]
EX v94,v94
MM v[124:127],a[244:247],v[188:191],v[124:127]
EX v95,v95
MM v[124:127],a[248:251],v[204:207],v[124:127]
EX v96,v96
MM v[124:127],a[252:255],v[220:223],v[124:127]
EX v97,v97
MM v[120:123],a[236:239],v[220:223],v[140:143]
EX v98,v98
MM v[120:123],a[232:235],v[204:207],v[120:123]
EX v99,v99
MM v[120:123],a[228:231],v[188:191],v[120:123]
EX v100,v100
MM v[120:123],a[224:227],v[172:175],v[120:123]
s_waitcnt lgkmcnt(0)
s_barrier
MM a[0:3],a[128:131],v[0:3],a[0:3]
s_add_u32 m0, s65, 0
CV v88,v88,v89
CV v89,v90,v91
MM a[0:3],a[132:135],v[4:7],a[0:3]
RD a[192:195],v224 offset:32768
CV v90,v92,v93
MM a[8:11],a[132:135],v[12:15],a[8:11]
RD a[196:199],v225 offset:32768
CV v91,v94,v95
MM a[8:11],a[128:131],v[8:11],a[8:11]
buffer_load_dwordx4 v228, s[8:11], s64 offen offset:0 lds
MM a[16:19],a[128:131],v[16:19],a[16:19]
RD a[200:203],v226 offset:32768
CV v68,v96,v97
MM a[16:19],a[132:135],v[20:23],a[16:19]
RD a[204:207],v227 offset:32768
CV v69,v98,v99
MM a[24:27],a[132:135],v[28:31],a[24:27]
buffer_load_dwordx4 v229, s[8:11], s64 offen offset:1024 lds
MM a[24:27],a[128:131],v[24:27],a[24:27]
RD a[208:211],v224 offset:36864
RD a[212:215],v225 offset:36864
MM a[28:31],a[136:139],v[24:27],a[28:31]
buffer_load_dwordx4 v230, s[8:11], s64 offen offset:2048 lds
MM a[28:31],a[140:143],v[28:31],a[28:31]
RD a[216:219],v226 offset:36864
RD a[220:223],v227 offset:36864
MM a[20:23],a[140:143],v[20:23],a[20:23]
EX v101,v101
MM a[20:23],a[136:139],v[16:19],a[20:23]
RD a[224:227],v224 offset:40960
CV v70,v100,v101
MM a[12:15],a[136:139],v[8:11],a[12:15]
EX v102,v102
MM a[12:15],a[140:143],v[12:15],a[12:15]
buffer_load_dwordx4 v231, s[8:11], s64 offen offset:3072 lds
MM a[4:7],a[140:143],v[4:7],a[4:7]
s_add_u32 m0, s65, 65536
EX v103,v103
MM a[4:7],a[136:139],v[0:3],a[4:7]
RD a[228:231],v225 offset:40960
CV v71,v102,v103
MM a[32:35],a[144:147],v[0:3],a[32:35]
EX v104,v104
MM a[32:35],a[148:151],v[4:7],a[32:35]
EX v105,v105
MM a[40:43],a[148:151],v[12:15],a[40:43]
RD a[232:235],v226 offset:40960
CV v76,v104,v105
MM a[40:43],a[144:147],v[8:11],a[40:43]
EX v106,v106
MM a[48:51],a[144:147],v[16:19],a[48:51]
buffer_load_dwordx4 v235, s[12:15], s81 offen offset:0 lds
MM a[48:51],a[148:151],v[20:23],a[48:51]
EX v107,v107
MM a[56:59],a[148:151],v[28:31],a[56:59]
RD a[236:239],v227 offset:40960
CV v77,v106,v107
MM a[56:59],a[144:147],v[24:27],a[56:59]
EX v108,v108
MM a[60:63],a[152:155],v[24:27],a[60:63]
EX v109,v109
MM a[60:63],a[156:159],v[28:31],a[60:63]
RD a[240:243],v224 offset:45056
CV v78,v108,v109
MM a[52:55],a[156:159],v[20:23],a[52:55]
EX v110,v110
MM a[52:55],a[152:155],v[16:19],a[52:55]
buffer_load_dwordx4 v235, s[12:15], s64 offen offset:1024 lds
MM a[44:47],a[152:155],v[8:11],a[44:47]
EX v111,v111
MM a[44:47],a[156:159],v[12:15],a[44:47]
RD a[244:247],v225 offset:45056
CV v79,v110,v111
MM a[36:39],a[156:159],v[4:7],a[36:39]
EX v112,v112
MM a[36:39],a[152:155],v[0:3],a[36:39]
EX v113,v113
MM a[64:67],a[160:163],v[0:3],a[64:67]
RD a[248:251],v226 offset:45056
CV v84,v112,v113
MM a[64:67],a[164:167],v[4:7],a[64:67]
EX v114,v114
MM a[72:75],a[164:167],v[12:15],a[72:75]
buffer_load_dwordx4 v235, s[12:15], s81 offen offset:2048 lds
MM a[72:75],a[160:163],v[8:11],a[72:75]
EX v115,v115
MM a[80:83],a[160:163],v[16:19],a[80:83]
RD a[252:255],v227 offset:45056
CV v85,v114,v115
MM a[80:83],a[164:167],v[20:23],a[80:83]
EX v116,v116
MM a[88:91],a[164:167],v[28:31],a[88:91]
EX v117,v117
MM a[88:91],a[160:163],v[24:27],a[88:91]
buffer_load_dwordx4 v235, s[12:15], s64 offen offset:3072 lds
MM a[92:95],a[168:171],v[24:27],a[92:95]
EX v118,v118
MM a[92:95],a[172:175],v[28:31],a[92:95]
EX v119,v119
MM a[84:87],a[172:175],v[20:23],a[84:87]
EX v120,v120
MM a[84:87],a[168:171],v[16:19],a[84:87]
EX v121,v121
MM a[76:79],a[168:171],v[8:11],a[76:79]
EX v122,v122
MM a[76:79],a[172:175],v[12:15],a[76:79]
EX v123,v123
MM a[68:71],a[172:175],v[4:7],a[68:71]
EX v124,v124
MM a[68:71],a[168:171],v[0:3],a[68:71]
EX v125,v125
MM a[96:99],a[176:179],v[0:3],a[96:99]
EX v126,v126
MM a[96:99],a[180:183],v[4:7],a[96:99]
EX v127,v127
MM a[104:107],a[180:183],v[12:15],a[104:107]
)ASM"
R"ASM(
CV v86,v116,v117
CV v87,v118,v119
MM a[104:107],a[176:179],v[8:11],a[104:107]
CV v92,v120,v121
CV v93,v122,v123
MM a[112:115],a[176:179],v[16:19],a[112:115]
CV v94,v124,v125
CV v95,v126,v127
MM a[112:115],a[180:183],v[20:23],a[112:115]
MM a[120:123],a[180:183],v[28:31],a[120:123]
s_lshl_b32 s91, s17, 2
s_add_u32 s91, s91, 94464
s_and_b64 s[44:45], s[44:45], 1
MM a[120:123],a[176:179],v[24:27],a[120:123]
s_waitcnt vmcnt(10)
v_mov_b32 v250, s69
s_mov_b64 exec, s[44:45]
buffer_store_dword v250, off, s[20:23], s91 sc1
s_mov_b64 exec, -1
MM a[124:127],a[184:187],v[24:27],a[124:127]
v_add_f32 v244, v144, v145
v_add_f32 v245, v148, v149
MM a[124:127],a[188:191],v[28:31],a[124:127]
v_add_f32 v246, v152, v153
v_add_f32 v247, v156, v157
MM a[116:119],a[188:191],v[20:23],a[116:119]
MM a[116:119],a[184:187],v[16:19],a[116:119]
MM a[108:111],a[184:187],v[8:11],a[108:111]
MM a[108:111],a[188:191],v[12:15],a[108:111]
MM a[100:103],a[188:191],v[4:7],a[100:103]
MM a[100:103],a[184:187],v[0:3],a[100:103]
v_smfmac_f32_16x16x64_bf16 v[144:147], v[236:239], v[64:71], v234
v_smfmac_f32_16x16x64_bf16 v[148:151], v[236:239], v[72:79], v234
s_nop 0
LPG23_%=:
s_cbranch_execz LPG27_%=
v_smfmac_f32_16x16x64_bf16 v[152:155], v[236:239], v[80:87], v234
v_smfmac_f32_16x16x64_bf16 v[156:159], v[236:239], v[88:95], v234
s_waitcnt lgkmcnt(0)
LLAJ_%=:
MM v[0:3],a[192:195],v[160:163],v[128:131]
s_add_u32 s64, s64, 16384
TR a[128:129],v232 offset:16384
s_add_u32 s81, s64, 896
MM v[0:3],a[196:199],v[176:179],v[0:3]
TR a[130:131],v232 offset:20480
v_max3_f32 v249, v144, v145, v148
MM v[0:3],a[200:203],v[192:195],v[0:3]
TR a[132:133],v232 offset:24576
v_max3_f32 v249, v249, v149, v152
MM v[0:3],a[204:207],v[208:211],v[0:3]
TR a[134:135],v232 offset:28672
v_max3_f32 v249, v249, v153, v156
MM v[4:7],a[220:223],v[208:211],v[128:131]
TR a[136:137],v233 offset:16384
v_max_f32 v249, v249, v157
MM v[4:7],a[216:219],v[192:195],v[4:7]
TR a[138:139],v233 offset:20480
v_cmp_lt_f32 vcc, s71, v249
MM v[4:7],a[212:215],v[176:179],v[4:7]
TR a[140:141],v233 offset:24576
TR a[142:143],v233 offset:28672
MM v[4:7],a[208:211],v[160:163],v[4:7]
s_cbranch_vccnz LSITE33_%=
LBACK33_%=:
MM v[12:15],a[208:211],v[164:167],v[132:135]
v_bfe_u32 v242, v248, 6, 4
v_mul_u32_u24 v242, 1280, v242
MM v[12:15],a[212:215],v[180:183],v[12:15]
v_and_b32 v252, 48, v248
v_bfe_u32 v243, v248, 8, 1
MM v[12:15],a[216:219],v[196:199],v[12:15]
v_lshlrev_b32 v243, 5, v243
v_xor_b32 v252, v252, v243
MM v[12:15],a[220:223],v[212:215],v[12:15]
v_add_u32 v252, v252, v242
v_lshrrev_b32 v242, 3, v248
MM v[8:11],a[204:207],v[212:215],v[132:135]
v_xor_b32 v243, v242, v248
v_and_b32 v243, 96, v243
MM v[8:11],a[200:203],v[196:199],v[8:11]
TR a[144:145],v232 offset:16896
v_and_or_b32 v243, v248, 16, v243
MM v[8:11],a[196:199],v[180:183],v[8:11]
s_and_b32 s66, s49, 8
EX v0,v0
MM v[8:11],a[192:195],v[164:167],v[8:11]
s_lshl_b32 s66, s66, 3
v_lshrrev_b32 v242, 7, v248
v_xor_b32 v243, s66, v243
EX v1,v1
MM v[16:19],a[192:195],v[168:171],v[136:139]
v_mul_u32_u24 v242, 1280, v242
v_add_u32 v240, v242, v243
EX v2,v2
MM v[16:19],a[196:199],v[184:187],v[16:19]
s_mul_i32 s88, %[swave], 40960
EX v3,v3
MM v[16:19],a[200:203],v[200:203],v[16:19]
s_and_b32 s66, s84, 1
EX v4,v4
MM v[16:19],a[204:207],v[216:219],v[16:19]
s_lshl_b32 s86, s66, 6
TR a[146:147],v232 offset:20992
CV v0,v0,v1
MM v[20:23],a[220:223],v[216:219],v[136:139]
s_add_u32 s86, s86, s88
EX v5,v5
MM v[20:23],a[216:219],v[200:203],v[20:23]
s_xor_b32 s66, s66, 1
TR a[148:149],v232 offset:25088
CV v1,v2,v3
MM v[20:23],a[212:215],v[184:187],v[20:23]
s_lshl_b32 s66, s66, 8
EX v6,v6
MM v[20:23],a[208:211],v[168:171],v[20:23]
s_add_u32 s88, s88, s66
EX v7,v7
MM v[28:31],a[208:211],v[172:175],v[140:143]
s_and_b32 s66, s49, 16
TR a[150:151],v232 offset:29184
TR a[152:153],v233 offset:16896
MM v[28:31],a[212:215],v[188:191],v[28:31]
s_lshl_b32 s66, s66, 3
EX v8,v8
MM v[28:31],a[216:219],v[204:207],v[28:31]
s_sub_u32 s87, s86, s66
TR a[154:155],v233 offset:20992
CV v2,v4,v5
MM v[28:31],a[220:223],v[220:223],v[28:31]
s_add_u32 s87, s87, 128
EX v9,v9
MM v[24:27],a[204:207],v[220:223],v[140:143]
s_add_u32 s86, s86, s66
TR a[156:157],v233 offset:25088
CV v3,v6,v7
MM v[24:27],a[200:203],v[204:207],v[24:27]
s_and_b32 s94, s49, 7
EX v10,v10
MM v[24:27],a[196:199],v[188:191],v[24:27]
s_lshl_b32 s89, %[swave], 11
TR a[158:159],v233 offset:29184
CV v8,v8,v9
MM v[24:27],a[192:195],v[172:175],v[24:27]
s_add_u32 s89, s89, %[slds]
EX v11,v11
MM v[32:35],a[224:227],v[160:163],v[128:131]
s_xor_b32 s66, s94, 4
TR a[160:161],v232 offset:17408
TR a[162:163],v232 offset:21504
MM v[32:35],a[228:231],v[176:179],v[32:35]
s_mul_i32 s66, s66, 163840
EX v12,v12
MM v[32:35],a[232:235],v[192:195],v[32:35]
s_add_u32 s66, s66, s88
s_add_u32 m0, s89, 131072
s_nop 0
buffer_load_dwordx4 v252, s[24:27], s66 offen offset:0 lds
MM v[32:35],a[236:239],v[208:211],v[32:35]
s_add_u32 s66, s66, 19456
EX v13,v13
MM v[36:39],a[252:255],v[208:211],v[128:131]
TR a[164:165],v232 offset:25600
CV v9,v10,v11
MM v[36:39],a[248:251],v[192:195],v[36:39]
EX v14,v14
MM v[36:39],a[244:247],v[176:179],v[36:39]
EX v15,v15
MM v[36:39],a[240:243],v[160:163],v[36:39]
TR a[166:167],v232 offset:29696
TR a[168:169],v233 offset:17408
MM v[44:47],a[240:243],v[164:167],v[132:135]
EX v16,v16
MM v[44:47],a[244:247],v[180:183],v[44:47]
TR a[170:171],v233 offset:21504
CV v10,v12,v13
MM v[44:47],a[248:251],v[196:199],v[44:47]
EX v17,v17
MM v[44:47],a[252:255],v[212:215],v[44:47]
s_add_u32 m0, s89, 131072
s_nop 0
buffer_load_dwordx4 v252, s[24:27], s66 offen offset:1024 lds
MM v[40:43],a[236:239],v[212:215],v[132:135]
EX v18,v18
MM v[40:43],a[232:235],v[196:199],v[40:43]
TR a[172:173],v233 offset:25600
CV v11,v14,v15
MM v[40:43],a[228:231],v[180:183],v[40:43]
EX v19,v19
MM v[40:43],a[224:227],v[164:167],v[40:43]
TR a[174:175],v233 offset:29696
TR a[176:177],v232 offset:17920
MM v[48:51],a[224:227],v[168:171],v[136:139]
EX v20,v20
MM v[48:51],a[228:231],v[184:187],v[48:51]
TR a[178:179],v232 offset:22016
CV v16,v16,v17
MM v[48:51],a[232:235],v[200:203],v[48:51]
EX v21,v21
MM v[48:51],a[236:239],v[216:219],v[48:51]
s_lshl_b32 s60, s50, 2
TR a[180:181],v232 offset:26112
CV v17,v18,v19
MM v[52:55],a[252:255],v[216:219],v[136:139]
s_add_u32 s60, s60, %[swave]
EX v22,v22
MM v[52:55],a[248:251],v[200:203],v[52:55]
s_lshl_b32 s60, s60, 1
TR a[182:183],v232 offset:30208
TR a[184:185],v233 offset:17920
MM v[52:55],a[244:247],v[184:187],v[52:55]
s_bfe_u32 s61, s49, 65538
EX v23,v23
MM v[52:55],a[240:243],v[168:171],v[52:55]
s_xor_b32 s61, s61, 1
EX v24,v24
MM v[60:63],a[240:243],v[172:175],v[140:143]
s_add_u32 s60, s60, s61
TR a[186:187],v233 offset:22016
CV v18,v20,v21
MM v[60:63],a[244:247],v[188:191],v[60:63]
s_mul_i32 s18, s60, 40960
EX v25,v25
MM v[60:63],a[248:251],v[204:207],v[60:63]
s_mul_i32 s60, s84, 320
TR a[188:189],v233 offset:26112
TR a[190:191],v233 offset:30208
MM v[60:63],a[252:255],v[220:223],v[60:63]
s_add_u32 s18, s18, s60
EX v26,v26
MM v[56:59],a[236:239],v[220:223],v[140:143]
s_lshr_b32 s62, s49, 3
CV v19,v22,v23
CV v24,v24,v25
MM v[56:59],a[232:235],v[204:207],v[56:59]
s_mul_i32 s63, s62, 10240
EX v27,v27
MM v[56:59],a[228:231],v[188:191],v[56:59]
s_add_u32 s63, s63, s18
EX v28,v28
MM v[56:59],a[224:227],v[172:175],v[56:59]
s_waitcnt vmcnt(14) lgkmcnt(0)
s_barrier
MM a[0:3],a[128:131],v[64:67],a[0:3]
s_and_b32 s60, s84, 1
EX v29,v29
MM a[0:3],a[132:135],v[68:71],a[0:3]
s_add_u32 s61, s60, s62
CV v25,v26,v27
CV v26,v28,v29
MM a[8:11],a[132:135],v[76:79],a[8:11]
s_lshl_b32 s61, s61, 6
EX v30,v30
MM a[8:11],a[128:131],v[72:75],a[8:11]
s_add_u32 s18, s18, s61
EX v31,v31
MM a[16:19],a[128:131],v[80:83],a[16:19]
s_xor_b32 s60, s60, 1
RD a[192:195],v224 offset:49152
CV v27,v30,v31
MM a[16:19],a[132:135],v[84:87],a[16:19]
s_lshl_b32 s60, s60, 8
EX v32,v32
MM a[24:27],a[132:135],v[92:95],a[24:27]
s_add_u32 s63, s63, s60
s_add_u32 m0, s65, 16384
RD a[196:199],v225 offset:49152
MM a[24:27],a[128:131],v[88:91],a[24:27]
s_mul_i32 s60, s62, 33792
EX v33,v33
MM a[28:31],a[136:139],v[88:91],a[28:31]
s_sub_u32 s60, s95, s60
buffer_load_dwordx4 v228, s[8:11], s64 offen offset:0 lds
MM a[28:31],a[140:143],v[92:95],a[28:31]
s_xor_b32 s61, s62, 1
EX v34,v34
MM a[20:23],a[140:143],v[84:87],a[20:23]
s_mul_i32 s61, s61, 33792
EX v35,v35
MM a[20:23],a[136:139],v[80:83],a[20:23]
s_add_u32 s61, s61, s60
RD a[200:203],v226 offset:49152
CV v4,v32,v33
MM a[12:15],a[136:139],v[72:75],a[12:15]
s_add_u32 s36, s61, 2048
EX v36,v36
MM a[12:15],a[140:143],v[76:79],a[12:15]
s_xor_b32 s61, s62, 2
CV v5,v34,v35
MM a[4:7],a[140:143],v[68:71],a[4:7]
s_mul_i32 s61, s61, 33792
EX v37,v37
MM a[4:7],a[136:139],v[64:67],a[4:7]
s_add_u32 s61, s61, s60
buffer_load_dwordx4 v229, s[8:11], s64 offen offset:1024 lds
MM a[32:35],a[144:147],v[64:67],a[32:35]
s_add_u32 s37, s61, 4096
EX v38,v38
MM a[32:35],a[148:151],v[68:71],a[32:35]
s_xor_b32 s61, s62, 3
RD a[204:207],v227 offset:49152
CV v6,v36,v37
MM a[40:43],a[148:151],v[76:79],a[40:43]
s_mul_i32 s61, s61, 33792
EX v39,v39
MM a[40:43],a[144:147],v[72:75],a[40:43]
s_add_u32 s61, s61, s60
RD a[208:211],v224 offset:53248
CV v7,v38,v39
MM a[48:51],a[144:147],v[80:83],a[48:51]
s_add_u32 s38, s61, 6144
EX v40,v40
MM a[48:51],a[148:151],v[84:87],a[48:51]
s_lshl_b32 s61, s62, 9
EX v41,v41
MM a[56:59],a[148:151],v[92:95],a[56:59]
s_add_u32 s39, s60, s61
RD a[212:215],v225 offset:53248
MM a[56:59],a[144:147],v[88:91],a[56:59]
s_mov_b32 s17, 15
buffer_load_dwordx4 v230, s[8:11], s64 offen offset:2048 lds
MM a[60:63],a[152:155],v[88:91],a[60:63]
s_mov_b32 s58, 0
EX v42,v42
MM a[60:63],a[156:159],v[92:95],a[60:63]
s_lshl_b32 s60, s50, 1
EX v43,v43
MM a[52:55],a[156:159],v[84:87],a[52:55]
s_bfe_u32 s61, s49, 65538
RD a[216:219],v226 offset:53248
CV v12,v40,v41
MM a[52:55],a[152:155],v[80:83],a[52:55]
s_add_u32 s60, s60, s61
EX v44,v44
MM a[44:47],a[152:155],v[72:75],a[44:47]
s_lshl_b32 s60, s60, 2
CV v13,v42,v43
MM a[44:47],a[156:159],v[76:79],a[44:47]
s_add_u32 s60, s60, s84
EX v45,v45
MM a[36:39],a[156:159],v[68:71],a[36:39]
s_lshl_b32 s60, s60, 2
buffer_load_dwordx4 v231, s[8:11], s64 offen offset:3072 lds
MM a[36:39],a[152:155],v[64:67],a[36:39]
s_add_u32 s68, s60, %[swave]
EX v46,v46
MM a[64:67],a[160:163],v[64:67],a[64:67]
s_lshl_b32 s68, s68, 6
RD a[220:223],v227 offset:53248
CV v14,v44,v45
MM a[64:67],a[164:167],v[68:71],a[64:67]
s_add_u32 s68, s68, 16384
EX v47,v47
MM a[72:75],a[164:167],v[76:79],a[72:75]
s_lshr_b32 s61, s49, 3
EX v48,v48
MM a[72:75],a[160:163],v[72:75],a[72:75]
s_lshl_b32 s19, s61, 4
RD a[224:227],v224 offset:57344
CV v15,v46,v47
MM a[80:83],a[160:163],v[80:83],a[80:83]
s_add_u32 s19, s19, s68
EX v49,v49
MM a[80:83],a[164:167],v[84:87],a[80:83]
s_lshr_b32 s96, s49, 2
s_add_u32 m0, s65, 81920
CV v20,v48,v49
MM a[88:91],a[164:167],v[92:95],a[88:91]
s_lshl_b32 s97, s50, 3
EX v50,v50
MM a[88:91],a[160:163],v[88:91],a[88:91]
s_add_u32 s96, s96, s97
buffer_load_dwordx4 v235, s[12:15], s81 offen offset:0 lds
MM a[92:95],a[168:171],v[88:91],a[92:95]
s_lshl_b32 s96, s96, 2
EX v51,v51
MM a[92:95],a[172:175],v[92:95],a[92:95]
s_add_u32 s96, s96, %[swave]
RD a[228:231],v225 offset:57344
CV v21,v50,v51
MM a[84:87],a[172:175],v[84:87],a[84:87]
s_lshl_b32 s96, s96, 6
EX v52,v52
MM a[84:87],a[168:171],v[80:83],a[84:87]
s_add_u32 s96, s96, 0
RD a[232:235],v226 offset:57344
MM a[76:79],a[168:171],v[72:75],a[76:79]
s_xor_b32 s44, s96, 256
EX v53,v53
MM a[76:79],a[172:175],v[76:79],a[76:79]
s_xor_b32 s66, s94, 5
RD a[236:239],v227 offset:57344
CV v22,v52,v53
MM a[68:71],a[172:175],v[68:71],a[68:71]
s_mul_i32 s66, s66, 163840
EX v54,v54
MM a[68:71],a[168:171],v[64:67],a[68:71]
s_add_u32 s66, s66, s88
s_add_u32 m0, s89, 139264
s_nop 0
buffer_load_dwordx4 v252, s[24:27], s66 offen offset:0 lds
MM a[96:99],a[176:179],v[64:67],a[96:99]
s_add_u32 s66, s66, 19456
EX v55,v55
MM a[96:99],a[180:183],v[68:71],a[96:99]
s_add_u32 m0, s65, 81920
s_nop 0
buffer_load_dwordx4 v235, s[12:15], s64 offen offset:1024 lds
MM a[104:107],a[180:183],v[76:79],a[104:107]
EX v56,v56
MM a[104:107],a[176:179],v[72:75],a[104:107]
)ASM"
R"ASM(
RD a[240:243],v224 offset:61440
CV v23,v54,v55
MM a[112:115],a[176:179],v[80:83],a[112:115]
EX v57,v57
MM a[112:115],a[180:183],v[84:87],a[112:115]
EX v58,v58
MM a[120:123],a[180:183],v[92:95],a[120:123]
RD a[244:247],v225 offset:61440
CV v28,v56,v57
MM a[120:123],a[176:179],v[88:91],a[120:123]
EX v59,v59
MM a[124:127],a[184:187],v[88:91],a[124:127]
v_add_f32 v244, v144, v145
v_add_f32 v245, v148, v149
MM a[124:127],a[188:191],v[92:95],a[124:127]
v_add_f32 v246, v152, v153
v_add_f32 v247, v156, v157
MM a[116:119],a[188:191],v[84:87],a[116:119]
s_add_u32 m0, s89, 139264
s_nop 0
buffer_load_dwordx4 v252, s[24:27], s66 offen offset:1024 lds
MM a[116:119],a[184:187],v[80:83],a[116:119]
EX v60,v60
MM a[108:111],a[184:187],v[72:75],a[108:111]
EX v61,v61
MM a[108:111],a[188:191],v[76:79],a[108:111]
s_add_u32 m0, s65, 81920
s_nop 0
buffer_load_dwordx4 v235, s[12:15], s81 offen offset:2048 lds
MM a[100:103],a[188:191],v[68:71],a[100:103]
EX v62,v62
MM a[100:103],a[184:187],v[64:67],a[100:103]
RD a[248:251],v226 offset:61440
CV v29,v58,v59
v_smfmac_f32_16x16x64_bf16 v[144:147], v[236:239], v[0:7], v234
RD a[252:255],v227 offset:61440
EX v63,v63
v_smfmac_f32_16x16x64_bf16 v[148:151], v[236:239], v[8:15], v234
CV v30,v60,v61
v_smfmac_f32_16x16x64_bf16 v[152:155], v[236:239], v[16:23], v234
CV v31,v62,v63
s_nop 1
v_smfmac_f32_16x16x64_bf16 v[156:159], v[236:239], v[24:31], v234
s_waitcnt lgkmcnt(0)
buffer_load_dwordx4 v235, s[12:15], s64 offen offset:3072 lds
MM v[64:67],a[192:195],v[160:163],v[128:131]
s_add_u32 s64, s64, 16384
TR a[128:129],v232 offset:32768
s_add_u32 s81, s64, 896
MM v[64:67],a[196:199],v[176:179],v[64:67]
TR a[130:131],v232 offset:36864
v_max3_f32 v249, v144, v145, v148
MM v[64:67],a[200:203],v[192:195],v[64:67]
TR a[132:133],v232 offset:40960
v_max3_f32 v249, v249, v149, v152
MM v[64:67],a[204:207],v[208:211],v[64:67]
TR a[134:135],v232 offset:45056
v_max3_f32 v249, v249, v153, v156
MM v[68:71],a[220:223],v[208:211],v[128:131]
TR a[136:137],v233 offset:32768
v_max_f32 v249, v249, v157
MM v[68:71],a[216:219],v[192:195],v[68:71]
TR a[138:139],v233 offset:36864
v_cmp_lt_f32 vcc, s71, v249
MM v[68:71],a[212:215],v[176:179],v[68:71]
TR a[140:141],v233 offset:40960
TR a[142:143],v233 offset:45056
MM v[68:71],a[208:211],v[160:163],v[68:71]
s_cbranch_vccnz LSITE34_%=
LBACK34_%=:
MM v[76:79],a[208:211],v[164:167],v[132:135]
buffer_load_dword v242, off, s[20:23], s96 sc1
MM v[76:79],a[212:215],v[180:183],v[76:79]
buffer_load_dword v243, off, s[20:23], s19 sc1
MM v[76:79],a[216:219],v[196:199],v[76:79]
s_xor_b32 s66, s94, 6
v_and_b32 v32, 496, v248
v_mul_u32_u24 v32, 80, v32
MM v[76:79],a[220:223],v[212:215],v[76:79]
s_mul_i32 s66, s66, 163840
EX v64,v64
MM v[72:75],a[204:207],v[212:215],v[132:135]
s_add_u32 s66, s66, s88
s_add_u32 m0, s89, 147456
s_nop 0
buffer_load_dwordx4 v252, s[24:27], s66 offen offset:0 lds
MM v[72:75],a[200:203],v[196:199],v[72:75]
s_add_u32 s66, s66, 19456
EX v65,v65
MM v[72:75],a[196:199],v[180:183],v[72:75]
s_sub_u32 s60, s63, s18
v_add_u32 v32, s18, v32
CV v64,v64,v65
MM v[72:75],a[192:195],v[164:167],v[72:75]
s_mov_b32 s61, 131072
s_mov_b32 exec_lo, 0
v_add_u32 v32, s60, v32
s_mov_b64 exec, -1
MM v[80:83],a[192:195],v[168:171],v[136:139]
s_mov_b32 exec_hi, 0xff
buffer_load_dword v36, v32, s[32:35], 0 offen offset:0
s_mov_b64 exec, -1
EX v66,v66
MM v[80:83],a[196:199],v[184:187],v[80:83]
EX v67,v67
MM v[80:83],a[200:203],v[200:203],v[80:83]
EX v68,v68
MM v[80:83],a[204:207],v[216:219],v[80:83]
s_mov_b32 exec_hi, 0xff
buffer_load_dword v37, v32, s[20:23], s61 offen offset:0 sc1
s_mov_b64 exec, -1
MM v[84:87],a[220:223],v[216:219],v[136:139]
TR a[144:145],v232 offset:33280
TR a[146:147],v232 offset:37376
MM v[84:87],a[216:219],v[200:203],v[84:87]
s_add_u32 m0, s89, 147456
s_nop 0
buffer_load_dwordx4 v252, s[24:27], s66 offen offset:1024 lds
EX v69,v69
MM v[84:87],a[212:215],v[184:187],v[84:87]
EX v70,v70
MM v[84:87],a[208:211],v[168:171],v[84:87]
TR a[148:149],v232 offset:41472
CV v65,v66,v67
MM v[92:95],a[208:211],v[172:175],v[140:143]
EX v71,v71
MM v[92:95],a[212:215],v[188:191],v[92:95]
TR a[150:151],v232 offset:45568
TR a[152:153],v233 offset:33280
MM v[92:95],a[216:219],v[204:207],v[92:95]
EX v72,v72
MM v[92:95],a[220:223],v[220:223],v[92:95]
EX v73,v73
MM v[88:91],a[204:207],v[220:223],v[140:143]
TR a[154:155],v233 offset:37376
CV v66,v68,v69
MM v[88:91],a[200:203],v[204:207],v[88:91]
s_xor_b32 s66, s94, 7
EX v74,v74
MM v[88:91],a[196:199],v[188:191],v[88:91]
s_mul_i32 s66, s66, 163840
TR a[156:157],v233 offset:41472
CV v67,v70,v71
MM v[88:91],a[192:195],v[172:175],v[88:91]
s_add_u32 s66, s66, s88
s_add_u32 m0, s89, 155648
s_nop 0
buffer_load_dwordx4 v252, s[24:27], s66 offen offset:0 lds
MM v[96:99],a[224:227],v[160:163],v[128:131]
s_add_u32 s66, s66, 19456
EX v75,v75
MM v[96:99],a[228:231],v[176:179],v[96:99]
TR a[158:159],v233 offset:45568
TR a[160:161],v232 offset:33792
MM v[96:99],a[232:235],v[192:195],v[96:99]
EX v76,v76
MM v[96:99],a[236:239],v[208:211],v[96:99]
EX v77,v77
MM v[100:103],a[252:255],v[208:211],v[128:131]
TR a[162:163],v232 offset:37888
CV v72,v72,v73
MM v[100:103],a[248:251],v[192:195],v[100:103]
EX v78,v78
MM v[100:103],a[244:247],v[176:179],v[100:103]
TR a[164:165],v232 offset:41984
CV v73,v74,v75
MM v[100:103],a[240:243],v[160:163],v[100:103]
EX v79,v79
MM v[108:111],a[240:243],v[164:167],v[132:135]
s_add_u32 m0, s89, 155648
s_nop 0
buffer_load_dwordx4 v252, s[24:27], s66 offen offset:1024 lds
MM v[108:111],a[244:247],v[180:183],v[108:111]
TR a[166:167],v232 offset:46080
TR a[168:169],v233 offset:33792
MM v[108:111],a[248:251],v[196:199],v[108:111]
EX v80,v80
MM v[108:111],a[252:255],v[212:215],v[108:111]
EX v81,v81
MM v[104:107],a[236:239],v[212:215],v[132:135]
TR a[170:171],v233 offset:37888
CV v74,v76,v77
MM v[104:107],a[232:235],v[196:199],v[104:107]
EX v82,v82
MM v[104:107],a[228:231],v[180:183],v[104:107]
TR a[172:173],v233 offset:41984
TR a[174:175],v233 offset:46080
MM v[104:107],a[224:227],v[164:167],v[104:107]
EX v83,v83
MM v[112:115],a[224:227],v[168:171],v[136:139]
TR a[176:177],v232 offset:34304
CV v75,v78,v79
MM v[112:115],a[228:231],v[184:187],v[112:115]
EX v84,v84
MM v[112:115],a[232:235],v[200:203],v[112:115]
EX v85,v85
MM v[112:115],a[236:239],v[216:219],v[112:115]
TR a[178:179],v232 offset:38400
CV v80,v80,v81
MM v[116:119],a[252:255],v[216:219],v[136:139]
EX v86,v86
MM v[116:119],a[248:251],v[200:203],v[116:119]
TR a[180:181],v232 offset:42496
TR a[182:183],v232 offset:46592
MM v[116:119],a[244:247],v[184:187],v[116:119]
EX v87,v87
MM v[116:119],a[240:243],v[168:171],v[116:119]
TR a[184:185],v233 offset:34304
CV v81,v82,v83
MM v[124:127],a[240:243],v[172:175],v[140:143]
EX v88,v88
MM v[124:127],a[244:247],v[188:191],v[124:127]
TR a[186:187],v233 offset:38400
CV v82,v84,v85
MM v[124:127],a[248:251],v[204:207],v[124:127]
TR a[188:189],v233 offset:42496
TR a[190:191],v233 offset:46592
EX v89,v89
MM v[124:127],a[252:255],v[220:223],v[124:127]
EX v90,v90
MM v[120:123],a[236:239],v[220:223],v[140:143]
CV v83,v86,v87
CV v88,v88,v89
MM v[120:123],a[232:235],v[204:207],v[120:123]
EX v91,v91
MM v[120:123],a[228:231],v[188:191],v[120:123]
EX v92,v92
MM v[120:123],a[224:227],v[172:175],v[120:123]
s_waitcnt vmcnt(24) lgkmcnt(0)
s_barrier
MM a[0:3],a[128:131],v[0:3],a[0:3]
s_add_u32 m0, s65, 32768
EX v93,v93
MM a[0:3],a[132:135],v[4:7],a[0:3]
CV v89,v90,v91
CV v90,v92,v93
MM a[8:11],a[132:135],v[12:15],a[8:11]
EX v94,v94
MM a[8:11],a[128:131],v[8:11],a[8:11]
EX v95,v95
MM a[16:19],a[128:131],v[16:19],a[16:19]
RD a[192:195],v224 offset:0
LPG24_%=:
s_cbranch_execz LPG28_%=
CV v91,v94,v95
MM a[16:19],a[132:135],v[20:23],a[16:19]
EX v96,v96
MM a[24:27],a[132:135],v[28:31],a[24:27]
buffer_load_dwordx4 v228, s[8:11], s64 offen offset:0 lds
MM a[24:27],a[128:131],v[24:27],a[24:27]
EX v97,v97
MM a[28:31],a[136:139],v[24:27],a[28:31]
RD a[196:199],v225 offset:0
CV v68,v96,v97
MM a[28:31],a[140:143],v[28:31],a[28:31]
EX v98,v98
MM a[20:23],a[140:143],v[20:23],a[20:23]
EX v99,v99
MM a[20:23],a[136:139],v[16:19],a[20:23]
TR a[128:129],v232 offset:49152
RD a[200:203],v226 offset:0
MM a[12:15],a[136:139],v[8:11],a[12:15]
EX v100,v100
MM a[12:15],a[140:143],v[12:15],a[12:15]
TR a[130:131],v232 offset:53248
CV v69,v98,v99
MM a[4:7],a[140:143],v[4:7],a[4:7]
EX v101,v101
MM a[4:7],a[136:139],v[0:3],a[4:7]
TR a[132:133],v232 offset:57344
RD a[204:207],v227 offset:0
MM a[32:35],a[144:147],v[0:3],a[32:35]
EX v102,v102
MM a[32:35],a[148:151],v[4:7],a[32:35]
buffer_load_dwordx4 v229, s[8:11], s64 offen offset:1024 lds
MM a[40:43],a[148:151],v[12:15],a[40:43]
TR a[134:135],v232 offset:61440
CV v70,v100,v101
MM a[40:43],a[144:147],v[8:11],a[40:43]
EX v103,v103
MM a[48:51],a[144:147],v[16:19],a[48:51]
TR a[136:137],v233 offset:49152
TR a[138:139],v233 offset:53248
MM a[48:51],a[148:151],v[20:23],a[48:51]
EX v104,v104
MM a[56:59],a[148:151],v[28:31],a[56:59]
EX v105,v105
MM a[56:59],a[144:147],v[24:27],a[56:59]
TR a[140:141],v233 offset:57344
CV v71,v102,v103
MM a[60:63],a[152:155],v[24:27],a[60:63]
EX v106,v106
MM a[60:63],a[156:159],v[28:31],a[60:63]
RD a[208:211],v224 offset:4096
TR a[142:143],v233 offset:61440
MM a[52:55],a[156:159],v[20:23],a[52:55]
EX v107,v107
MM a[52:55],a[152:155],v[16:19],a[52:55]
buffer_load_dwordx4 v230, s[8:11], s64 offen offset:2048 lds
MM a[44:47],a[152:155],v[8:11],a[44:47]
TR a[144:145],v232 offset:49664
RD a[212:215],v225 offset:4096
MM a[44:47],a[156:159],v[12:15],a[44:47]
EX v108,v108
MM a[36:39],a[156:159],v[4:7],a[36:39]
TR a[146:147],v232 offset:53760
CV v76,v104,v105
MM a[36:39],a[152:155],v[0:3],a[36:39]
EX v109,v109
MM a[64:67],a[160:163],v[0:3],a[64:67]
TR a[148:149],v232 offset:57856
CV v77,v106,v107
MM a[64:67],a[164:167],v[4:7],a[64:67]
EX v110,v110
MM a[72:75],a[164:167],v[12:15],a[72:75]
EX v111,v111
MM a[72:75],a[160:163],v[8:11],a[72:75]
TR a[150:151],v232 offset:61952
RD a[216:219],v226 offset:4096
MM a[80:83],a[160:163],v[16:19],a[80:83]
TR a[152:153],v233 offset:49664
CV v78,v108,v109
MM a[80:83],a[164:167],v[20:23],a[80:83]
EX v112,v112
MM a[88:91],a[164:167],v[28:31],a[88:91]
EX v113,v113
MM a[88:91],a[160:163],v[24:27],a[88:91]
TR a[154:155],v233 offset:53760
RD a[220:223],v227 offset:4096
MM a[92:95],a[168:171],v[24:27],a[92:95]
buffer_load_dwordx4 v231, s[8:11], s64 offen offset:3072 lds
MM a[92:95],a[172:175],v[28:31],a[92:95]
s_add_u32 m0, s65, 98304
EX v114,v114
MM a[84:87],a[172:175],v[20:23],a[84:87]
TR a[156:157],v233 offset:57856
TR a[158:159],v233 offset:61952
MM a[84:87],a[168:171],v[16:19],a[84:87]
EX v115,v115
MM a[76:79],a[168:171],v[8:11],a[76:79]
EX v116,v116
MM a[76:79],a[172:175],v[12:15],a[76:79]
TR a[160:161],v232 offset:50176
CV v79,v110,v111
MM a[68:71],a[172:175],v[4:7],a[68:71]
EX v117,v117
MM a[68:71],a[168:171],v[0:3],a[68:71]
TR a[162:163],v232 offset:54272
RD a[224:227],v224 offset:8192
MM a[96:99],a[176:179],v[0:3],a[96:99]
EX v118,v118
MM a[96:99],a[180:183],v[4:7],a[96:99]
TR a[164:165],v232 offset:58368
CV v84,v112,v113
MM a[104:107],a[180:183],v[12:15],a[104:107]
buffer_load_dwordx4 v235, s[12:15], s81 offen offset:0 lds
TR a[166:167],v232 offset:62464
EX v119,v119
MM a[104:107],a[176:179],v[8:11],a[104:107]
RD a[228:231],v225 offset:8192
TR a[168:169],v233 offset:50176
MM a[112:115],a[176:179],v[16:19],a[112:115]
EX v120,v120
MM a[112:115],a[180:183],v[20:23],a[112:115]
TR a[170:171],v233 offset:54272
CV v85,v114,v115
MM a[120:123],a[180:183],v[28:31],a[120:123]
s_waitcnt vmcnt(8)
EX v121,v121
MM a[120:123],a[176:179],v[24:27],a[120:123]
s_lshr_b32 s77, %[sb], 3
RD a[232:235],v226 offset:8192
TR a[172:173],v233 offset:58368
MM a[124:127],a[184:187],v[24:27],a[124:127]
s_lshl_b32 s77, s77, 2
v_add_f32 v244, v144, v145
v_add_f32 v245, v148, v149
EX v122,v122
MM a[124:127],a[188:191],v[28:31],a[124:127]
s_add_u32 s77, s77, %[swave]
v_add_f32 v246, v152, v153
v_add_f32 v247, v156, v157
CV v86,v116,v117
MM a[116:119],a[188:191],v[20:23],a[116:119]
s_lshl_b32 s77, s77, 9
TR a[174:175],v233 offset:62464
buffer_load_dwordx4 v235, s[12:15], s64 offen offset:1024 lds
)ASM"
R"ASM(
EX v123,v123
MM a[116:119],a[184:187],v[16:19],a[116:119]
s_add_u32 s77, s77, 0
TR a[176:177],v232 offset:50688
v_lshrrev_b32 v253, 1, v248
v_add_u32 v253, s77, v253
MM a[108:111],a[184:187],v[8:11],a[108:111]
s_mov_b32 s77, LKEND_ka-LPD2_%=-16
RD a[236:239],v227 offset:8192
v_min_u32 v253, s77, v253
EX v124,v124
CV v87,v118,v119
MM a[108:111],a[188:191],v[12:15],a[108:111]
s_getpc_b64 s[98:99]
LPD2_%=:
global_load_dword v253, v253, s[98:99]
TR a[178:179],v232 offset:54784
EX v125,v125
MM a[100:103],a[188:191],v[4:7],a[100:103]
TR a[180:181],v232 offset:58880
RD a[240:243],v224 offset:12288
MM a[100:103],a[184:187],v[0:3],a[100:103]
EX v126,v126
v_smfmac_f32_16x16x64_bf16 v[144:147], v[236:239], v[64:71], v234
RD a[244:247],v225 offset:12288
RD a[248:251],v226 offset:12288
RD a[252:255],v227 offset:12288
EX v127,v127
v_smfmac_f32_16x16x64_bf16 v[148:151], v[236:239], v[72:79], v234
TR a[182:183],v232 offset:62976
CV v92,v120,v121
v_smfmac_f32_16x16x64_bf16 v[152:155], v[236:239], v[80:87], v234
buffer_load_dwordx4 v235, s[12:15], s81 offen offset:2048 lds
CV v93,v122,v123
CV v94,v124,v125
CV v95,v126,v127
s_nop 1
v_smfmac_f32_16x16x64_bf16 v[156:159], v[236:239], v[88:95], v234
TR a[184:185],v233 offset:50688
TR a[186:187],v233 offset:54784
TR a[188:189],v233 offset:58880
TR a[190:191],v233 offset:62976
s_waitcnt lgkmcnt(5)
buffer_load_dwordx4 v235, s[12:15], s64 offen offset:3072 lds
MM v[16:19],a[192:195],v[168:171],v[136:139]
MM v[16:19],a[196:199],v[184:187],v[16:19]
MM v[16:19],a[200:203],v[200:203],v[16:19]
v_max3_f32 v249, v144, v145, v148
v_max3_f32 v249, v249, v149, v152
MM v[16:19],a[204:207],v[216:219],v[16:19]
v_max3_f32 v249, v249, v153, v156
v_max_f32 v249, v249, v157
MM v[20:23],a[220:223],v[216:219],v[136:139]
v_cmp_lt_f32 vcc, s71, v249
MM v[20:23],a[216:219],v[200:203],v[20:23]
s_cbranch_vccnz LSITE35_%=
LBACK35_%=:
v_add_f32 v244, v144, v145
MM v[20:23],a[212:215],v[184:187],v[20:23]
v_add_f32 v245, v148, v149
v_add_f32 v246, v152, v153
MM v[20:23],a[208:211],v[168:171],v[20:23]
s_waitcnt lgkmcnt(0)
v_add_f32 v247, v156, v157
MM a[0:3],a[128:131],v[64:67],a[0:3]
EX v16,v16
MM a[0:3],a[132:135],v[68:71],a[0:3]
v_rcp_f32 v145, v244
MM a[8:11],a[132:135],v[76:79],a[8:11]
EX v17,v17
MM a[8:11],a[128:131],v[72:75],a[8:11]
EX v18,v18
MM a[12:15],a[136:139],v[72:75],a[12:15]
v_rcp_f32 v149, v245
MM a[12:15],a[140:143],v[76:79],a[12:15]
CV v16,v16,v17
v_bfe_u32 v144, v241, 0, 8
MM a[4:7],a[140:143],v[68:71],a[4:7]
EX v19,v19
MM a[4:7],a[136:139],v[64:67],a[4:7]
CV v17,v18,v19
v_lshl_or_b32 v144, v144, 1, 1
MM a[32:35],a[144:147],v[64:67],a[32:35]
EX v20,v20
MM a[32:35],a[148:151],v[68:71],a[32:35]
v_lshlrev_b32 v144, 22, v144
v_mul_f32 v144, -v144, v145
MM a[40:43],a[148:151],v[76:79],a[40:43]
v_bfe_u32 v148, v241, 8, 8
v_lshl_or_b32 v148, v148, 1, 1
MM a[40:43],a[144:147],v[72:75],a[40:43]
v_lshlrev_b32 v148, 22, v148
v_mul_f32 v148, -v148, v149
MM a[44:47],a[152:155],v[72:75],a[44:47]
s_mul_i32 s78, s49, 17408
s_add_u32 s78, s83, s78
s_add_u32 s78, s78, 0
s_add_u32 s79, s78, 4096
AR v0,a0
AR v1,a1
AR v2,a2
MM a[44:47],a[156:159],v[76:79],a[44:47]
AR v3,a3
AR v4,a4
AR v5,a5
MM a[36:39],a[156:159],v[68:71],a[36:39]
AR v6,a6
AR v7,a7
v_fma_f32 v0, v0, v145, v144
v_fma_f32 v1, v1, v145, v144
MM a[36:39],a[152:155],v[64:67],a[36:39]
v_fma_f32 v2, v2, v145, v144
v_fma_f32 v3, v3, v145, v144
v_fma_f32 v4, v4, v145, v144
v_fma_f32 v5, v5, v145, v144
MM a[64:67],a[160:163],v[64:67],a[64:67]
v_fma_f32 v6, v6, v145, v144
v_fma_f32 v7, v7, v145, v144
CV v160,v0,v1
CV v161,v2,v3
MM a[64:67],a[164:167],v[68:71],a[64:67]
CV v162,v4,v5
CV v163,v6,v7
MM a[72:75],a[164:167],v[76:79],a[72:75]
buffer_store_dwordx4 v[160:163], v248, s[20:23], s78 offen offset:0 sc1
AR v8,a8
AR v9,a9
AR v10,a10
MM a[72:75],a[160:163],v[72:75],a[72:75]
AR v11,a11
AR v12,a12
AR v13,a13
EX v21,v21
AR v14,a14
MM a[76:79],a[168:171],v[72:75],a[76:79]
AR v15,a15
v_fma_f32 v8, v8, v149, v148
v_fma_f32 v9, v9, v149, v148
v_fma_f32 v10, v10, v149, v148
MM a[76:79],a[172:175],v[76:79],a[76:79]
v_fma_f32 v11, v11, v149, v148
v_fma_f32 v12, v12, v149, v148
v_fma_f32 v13, v13, v149, v148
v_fma_f32 v14, v14, v149, v148
EX v22,v22
MM a[68:71],a[172:175],v[68:71],a[68:71]
v_fma_f32 v15, v15, v149, v148
CV v164,v8,v9
CV v165,v10,v11
CV v166,v12,v13
MM a[68:71],a[168:171],v[64:67],a[68:71]
CV v167,v14,v15
CV v18,v20,v21
MM a[96:99],a[176:179],v[64:67],a[96:99]
buffer_store_dwordx4 v[164:167], v248, s[20:23], s79 offen offset:0 sc1
AR v32,a32
AR v33,a33
AR v34,a34
EX v23,v23
AR v35,a35
MM a[96:99],a[180:183],v[68:71],a[96:99]
AR v36,a36
AR v37,a37
AR v38,a38
AR v39,a39
MM a[104:107],a[180:183],v[76:79],a[104:107]
v_fma_f32 v32, v32, v145, v144
v_fma_f32 v33, v33, v145, v144
v_fma_f32 v34, v34, v145, v144
v_fma_f32 v35, v35, v145, v144
MM a[104:107],a[176:179],v[72:75],a[104:107]
v_fma_f32 v36, v36, v145, v144
v_fma_f32 v37, v37, v145, v144
v_fma_f32 v38, v38, v145, v144
MM a[108:111],a[184:187],v[72:75],a[108:111]
v_fma_f32 v39, v39, v145, v144
CV v176,v32,v33
CV v177,v34,v35
CV v178,v36,v37
MM a[108:111],a[188:191],v[76:79],a[108:111]
CV v179,v38,v39
CV v19,v22,v23
MM a[100:103],a[188:191],v[68:71],a[100:103]
buffer_store_dwordx4 v[176:179], v248, s[20:23], s78 offen offset:1024 sc1
AR v40,a40
AR v41,a41
AR v42,a42
AR v43,a43
AR v44,a44
MM a[100:103],a[184:187],v[64:67],a[100:103]
AR v45,a45
AR v46,a46
AR v47,a47
v_fma_f32 v40, v40, v149, v148
MM v[28:31],a[208:211],v[172:175],v[140:143]
v_fma_f32 v41, v41, v149, v148
v_fma_f32 v42, v42, v149, v148
v_fma_f32 v43, v43, v149, v148
MM v[28:31],a[212:215],v[188:191],v[28:31]
v_fma_f32 v44, v44, v149, v148
v_fma_f32 v45, v45, v149, v148
v_fma_f32 v46, v46, v149, v148
v_fma_f32 v47, v47, v149, v148
MM v[28:31],a[216:219],v[204:207],v[28:31]
CV v180,v40,v41
CV v181,v42,v43
CV v182,v44,v45
CV v183,v46,v47
MM v[28:31],a[220:223],v[220:223],v[28:31]
buffer_store_dwordx4 v[180:183], v248, s[20:23], s79 offen offset:1024 sc1
AR v0,a64
AR v1,a65
MM v[24:27],a[204:207],v[220:223],v[140:143]
AR v2,a66
AR v3,a67
AR v4,a68
MM v[24:27],a[200:203],v[204:207],v[24:27]
AR v5,a69
AR v6,a70
AR v7,a71
v_fma_f32 v0, v0, v145, v144
MM v[24:27],a[196:199],v[188:191],v[24:27]
v_fma_f32 v1, v1, v145, v144
v_fma_f32 v2, v2, v145, v144
v_fma_f32 v3, v3, v145, v144
v_fma_f32 v4, v4, v145, v144
MM v[24:27],a[192:195],v[172:175],v[24:27]
v_fma_f32 v5, v5, v145, v144
v_fma_f32 v6, v6, v145, v144
v_fma_f32 v7, v7, v145, v144
CV v192,v0,v1
MM v[48:51],a[224:227],v[168:171],v[136:139]
CV v193,v2,v3
CV v194,v4,v5
CV v195,v6,v7
MM v[48:51],a[228:231],v[184:187],v[48:51]
buffer_store_dwordx4 v[192:195], v248, s[20:23], s78 offen offset:2048 sc1
AR v8,a72
AR v9,a73
MM v[48:51],a[232:235],v[200:203],v[48:51]
AR v10,a74
AR v11,a75
AR v12,a76
AR v13,a77
MM v[48:51],a[236:239],v[216:219],v[48:51]
AR v14,a78
AR v15,a79
v_fma_f32 v8, v8, v149, v148
v_fma_f32 v9, v9, v149, v148
MM v[52:55],a[252:255],v[216:219],v[136:139]
EX v24,v24
EX v25,v25
EX v26,v26
EX v27,v27
EX v28,v28
EX v29,v29
v_fma_f32 v10, v10, v149, v148
v_fma_f32 v11, v11, v149, v148
EX v30,v30
v_fma_f32 v12, v12, v149, v148
v_fma_f32 v13, v13, v149, v148
MM v[52:55],a[248:251],v[200:203],v[52:55]
v_fma_f32 v14, v14, v149, v148
v_fma_f32 v15, v15, v149, v148
CV v196,v8,v9
EX v31,v31
CV v24,v24,v25
CV v197,v10,v11
MM v[52:55],a[244:247],v[184:187],v[52:55]
CV v198,v12,v13
CV v199,v14,v15
MM v[52:55],a[240:243],v[168:171],v[52:55]
buffer_store_dwordx4 v[196:199], v248, s[20:23], s79 offen offset:2048 sc1
AR v32,a96
AR v33,a97
AR v34,a98
MM v[60:63],a[240:243],v[172:175],v[140:143]
AR v35,a99
AR v36,a100
AR v37,a101
EX v48,v48
AR v38,a102
MM v[60:63],a[244:247],v[188:191],v[60:63]
AR v39,a103
v_fma_f32 v32, v32, v145, v144
v_fma_f32 v33, v33, v145, v144
v_fma_f32 v34, v34, v145, v144
CV v25,v26,v27
MM v[60:63],a[248:251],v[204:207],v[60:63]
v_fma_f32 v35, v35, v145, v144
v_fma_f32 v36, v36, v145, v144
v_fma_f32 v37, v37, v145, v144
MM v[60:63],a[252:255],v[220:223],v[60:63]
v_fma_f32 v38, v38, v145, v144
v_fma_f32 v39, v39, v145, v144
CV v208,v32,v33
EX v49,v49
CV v209,v34,v35
MM v[56:59],a[236:239],v[220:223],v[140:143]
CV v210,v36,v37
CV v211,v38,v39
MM v[56:59],a[232:235],v[204:207],v[56:59]
buffer_store_dwordx4 v[208:211], v248, s[20:23], s78 offen offset:3072 sc1
AR v40,a104
AR v41,a105
AR v42,a106
AR v43,a107
EX v50,v50
MM v[56:59],a[228:231],v[188:191],v[56:59]
AR v44,a108
AR v45,a109
AR v46,a110
AR v47,a111
CV v26,v28,v29
MM v[56:59],a[224:227],v[172:175],v[56:59]
s_waitcnt lgkmcnt(0)
s_barrier
v_fma_f32 v40, v40, v149, v148
v_fma_f32 v41, v41, v149, v148
v_fma_f32 v42, v42, v149, v148
MM a[16:19],a[128:131],v[80:83],a[16:19]
s_xor_b32 s66, s94, 4
v_fma_f32 v43, v43, v149, v148
v_fma_f32 v44, v44, v149, v148
v_fma_f32 v45, v45, v149, v148
v_fma_f32 v46, v46, v149, v148
EX v51,v51
MM a[16:19],a[132:135],v[84:87],a[16:19]
s_mul_i32 s66, s66, 163840
v_fma_f32 v47, v47, v149, v148
CV v212,v40,v41
CV v213,v42,v43
CV v214,v44,v45
MM a[24:27],a[132:135],v[92:95],a[24:27]
s_add_u32 s66, s66, s87
CV v215,v46,v47
CV v27,v30,v31
MM a[24:27],a[128:131],v[88:91],a[24:27]
s_add_u32 m0, s65, 114688
buffer_store_dwordx4 v[212:215], v248, s[20:23], s79 offen offset:3072 sc1
EX v52,v52
MM a[28:31],a[136:139],v[88:91],a[28:31]
EX v53,v53
MM a[28:31],a[140:143],v[92:95],a[28:31]
RD a[192:195],v224 offset:16384
CV v20,v48,v49
MM a[20:23],a[140:143],v[84:87],a[20:23]
buffer_load_dwordx4 v240, s[24:27], s66 offen offset:0 lds
MM a[20:23],a[136:139],v[80:83],a[20:23]
s_add_u32 s66, s66, 9216
EX v54,v54
MM a[48:51],a[144:147],v[80:83],a[48:51]
RD a[196:199],v225 offset:16384
CV v21,v50,v51
MM a[48:51],a[148:151],v[84:87],a[48:51]
EX v55,v55
MM a[56:59],a[148:151],v[92:95],a[56:59]
RD a[200:203],v226 offset:16384
RD a[204:207],v227 offset:16384
MM a[56:59],a[144:147],v[88:91],a[56:59]
buffer_load_dwordx4 v240, s[24:27], s66 offen offset:1024 lds
MM a[60:63],a[152:155],v[88:91],a[60:63]
s_add_u32 s66, s66, 9216
EX v56,v56
MM a[60:63],a[156:159],v[92:95],a[60:63]
RD a[208:211],v224 offset:20480
CV v22,v52,v53
MM a[52:55],a[156:159],v[84:87],a[52:55]
buffer_load_dwordx4 v240, s[24:27], s66 offen offset:2048 lds
MM a[52:55],a[152:155],v[80:83],a[52:55]
s_add_u32 s66, s66, 9216
EX v57,v57
MM a[80:83],a[160:163],v[80:83],a[80:83]
RD a[212:215],v225 offset:20480
RD a[216:219],v226 offset:20480
MM a[80:83],a[164:167],v[84:87],a[80:83]
buffer_load_dwordx4 v240, s[24:27], s66 offen offset:3072 lds
MM a[88:91],a[164:167],v[92:95],a[88:91]
s_xor_b32 s66, s94, 4
EX v58,v58
MM a[88:91],a[160:163],v[88:91],a[88:91]
s_mul_i32 s66, s66, 163840
RD a[220:223],v227 offset:20480
CV v23,v54,v55
MM a[92:95],a[168:171],v[88:91],a[92:95]
s_add_u32 s66, s66, s86
EX v59,v59
MM a[92:95],a[172:175],v[92:95],a[92:95]
s_add_u32 m0, s65, 49152
RD a[224:227],v224 offset:24576
RD a[228:231],v225 offset:24576
MM a[84:87],a[172:175],v[84:87],a[84:87]
buffer_load_dwordx4 v240, s[24:27], s66 offen offset:0 lds
MM a[84:87],a[168:171],v[80:83],a[84:87]
s_add_u32 s66, s66, 9216
EX v60,v60
MM a[112:115],a[176:179],v[80:83],a[112:115]
RD a[232:235],v226 offset:24576
CV v28,v56,v57
MM a[112:115],a[180:183],v[84:87],a[112:115]
EX v61,v61
MM a[120:123],a[180:183],v[92:95],a[120:123]
buffer_load_dwordx4 v240, s[24:27], s66 offen offset:1024 lds
MM a[120:123],a[176:179],v[88:91],a[120:123]
s_add_u32 s66, s66, 9216
RD a[236:239],v227 offset:24576
RD a[240:243],v224 offset:28672
MM a[124:127],a[184:187],v[88:91],a[124:127]
EX v62,v62
MM a[124:127],a[188:191],v[92:95],a[124:127]
buffer_load_dwordx4 v240, s[24:27], s66 offen offset:2048 lds
MM a[116:119],a[188:191],v[84:87],a[116:119]
s_add_u32 s66, s66, 9216
RD a[244:247],v225 offset:28672
RD a[248:251],v226 offset:28672
RD a[252:255],v227 offset:28672
EX v63,v63
MM a[116:119],a[184:187],v[80:83],a[116:119]
buffer_load_dwordx4 v240, s[24:27], s66 offen offset:3072 lds
v_smfmac_f32_16x16x64_bf16 v[152:155], v[236:239], v[16:23], v234
s_nop 0
LPG25_%=:
s_cbranch_execz LPG29_%=
CV v29,v58,v59
CV v30,v60,v61
CV v31,v62,v63
s_nop 1
v_smfmac_f32_16x16x64_bf16 v[156:159], v[236:239], v[24:31], v234
)ASM"
R"ASM(
s_waitcnt lgkmcnt(0)
MM v[80:83],a[192:195],v[168:171],v[136:139]
TR a[128:129],v232 offset:0
TR a[130:131],v232 offset:4096
MM v[80:83],a[196:199],v[184:187],v[80:83]
TR a[132:133],v232 offset:8192
TR a[134:135],v232 offset:12288
MM v[80:83],a[200:203],v[200:203],v[80:83]
TR a[136:137],v233 offset:0
TR a[138:139],v233 offset:4096
MM v[80:83],a[204:207],v[216:219],v[80:83]
v_max3_f32 v249, v152, v153, v156
v_max_f32 v249, v249, v157
MM v[84:87],a[220:223],v[216:219],v[136:139]
TR a[140:141],v233 offset:8192
v_cmp_lt_f32 vcc, s71, v249
MM v[84:87],a[216:219],v[200:203],v[84:87]
s_cbranch_vccnz LSITE36_%=
LBACK36_%=:
TR a[142:143],v233 offset:12288
v_add_f32 v246, v152, v153
MM v[84:87],a[212:215],v[184:187],v[84:87]
TR a[144:145],v232 offset:512
v_add_f32 v247, v156, v157
MM v[84:87],a[208:211],v[168:171],v[84:87]
TR a[146:147],v232 offset:4608
TR a[148:149],v232 offset:8704
MM v[92:95],a[208:211],v[172:175],v[140:143]
s_lshl_b32 s97, s84, 2
s_add_u32 s97, s97, s96
s_add_u32 s97, s97, 0
MM v[92:95],a[212:215],v[188:191],v[92:95]
s_waitcnt vmcnt(8)
v_mov_b32 v242, s69
s_mov_b64 exec, 1
buffer_store_dword v242, off, s[20:23], s97 offset:0 sc1
s_mov_b64 exec, -1
MM v[92:95],a[216:219],v[204:207],v[92:95]
v_lshrrev_b32 v98, 1, v248
v_min_u32 v98, 312, v98
MM v[92:95],a[220:223],v[220:223],v[92:95]
s_mov_b32 s62, 94464
buffer_load_dwordx2 v[96:97], v98, s[20:23], s62 offen offset:0 sc1
MM v[88:91],a[204:207],v[220:223],v[140:143]
TR a[150:151],v232 offset:12800
EX v80,v80
MM v[88:91],a[200:203],v[204:207],v[88:91]
TR a[152:153],v233 offset:512
TR a[154:155],v233 offset:4608
EX v81,v81
MM v[88:91],a[196:199],v[188:191],v[88:91]
TR a[156:157],v233 offset:8704
TR a[158:159],v233 offset:12800
MM v[88:91],a[192:195],v[172:175],v[88:91]
EX v82,v82
MM v[112:115],a[224:227],v[168:171],v[136:139]
TR a[160:161],v232 offset:1024
TR a[162:163],v232 offset:5120
EX v83,v83
MM v[112:115],a[228:231],v[184:187],v[112:115]
TR a[164:165],v232 offset:9216
TR a[166:167],v232 offset:13312
MM v[112:115],a[232:235],v[200:203],v[112:115]
EX v84,v84
MM v[112:115],a[236:239],v[216:219],v[112:115]
TR a[168:169],v233 offset:1024
TR a[170:171],v233 offset:5120
MM v[116:119],a[252:255],v[216:219],v[136:139]
EX v85,v85
MM v[116:119],a[248:251],v[200:203],v[116:119]
TR a[172:173],v233 offset:9216
TR a[174:175],v233 offset:13312
MM v[116:119],a[244:247],v[184:187],v[116:119]
TR a[176:177],v232 offset:1536
EX v86,v86
MM v[116:119],a[240:243],v[168:171],v[116:119]
TR a[178:179],v232 offset:5632
CV v80,v80,v81
EX v87,v87
MM v[124:127],a[240:243],v[172:175],v[140:143]
TR a[180:181],v232 offset:9728
TR a[182:183],v232 offset:13824
MM v[124:127],a[244:247],v[188:191],v[124:127]
EX v88,v88
MM v[124:127],a[248:251],v[204:207],v[124:127]
TR a[184:185],v233 offset:1536
TR a[186:187],v233 offset:5632
TR a[188:189],v233 offset:9728
TR a[190:191],v233 offset:13824
EX v89,v89
MM v[124:127],a[252:255],v[220:223],v[124:127]
CV v81,v82,v83
CV v82,v84,v85
MM v[120:123],a[236:239],v[220:223],v[140:143]
EX v90,v90
MM v[120:123],a[232:235],v[204:207],v[120:123]
EX v91,v91
MM v[120:123],a[228:231],v[188:191],v[120:123]
CV v83,v86,v87
CV v88,v88,v89
MM v[120:123],a[224:227],v[172:175],v[120:123]
s_waitcnt lgkmcnt(0)
s_barrier
CV v89,v90,v91
EX v92,v92
MM a[16:19],a[128:131],v[16:19],a[16:19]
s_xor_b32 s66, s94, 5
EX v93,v93
MM a[16:19],a[132:135],v[20:23],a[16:19]
s_mul_i32 s66, s66, 163840
EX v94,v94
MM a[24:27],a[132:135],v[28:31],a[24:27]
s_add_u32 s66, s66, s87
EX v95,v95
MM a[24:27],a[128:131],v[24:27],a[24:27]
s_add_u32 m0, s65, 65536
RD a[192:195],v224 offset:32768
CV v90,v92,v93
MM a[28:31],a[136:139],v[24:27],a[28:31]
EX v112,v112
MM a[28:31],a[140:143],v[28:31],a[28:31]
buffer_load_dwordx4 v240, s[24:27], s66 offen offset:0 lds
MM a[20:23],a[140:143],v[20:23],a[20:23]
s_add_u32 s66, s66, 9216
EX v113,v113
MM a[20:23],a[136:139],v[16:19],a[20:23]
RD a[196:199],v225 offset:32768
CV v91,v94,v95
MM a[48:51],a[144:147],v[16:19],a[48:51]
v_lshrrev_b32 v243, 2, v248
v_min_u32 v243, 12, v243
MM a[48:51],a[148:151],v[20:23],a[48:51]
buffer_load_dword v242, v243, s[20:23], s44 offen offset:0 sc1
EX v114,v114
MM a[56:59],a[148:151],v[28:31],a[56:59]
RD a[200:203],v226 offset:32768
RD a[204:207],v227 offset:32768
MM a[56:59],a[144:147],v[24:27],a[56:59]
EX v115,v115
MM a[60:63],a[152:155],v[24:27],a[60:63]
buffer_load_dwordx4 v240, s[24:27], s66 offen offset:1024 lds
MM a[60:63],a[156:159],v[28:31],a[60:63]
s_add_u32 s66, s66, 9216
EX v116,v116
MM a[52:55],a[156:159],v[20:23],a[52:55]
EX v117,v117
MM a[52:55],a[152:155],v[16:19],a[52:55]
RD a[208:211],v224 offset:36864
RD a[212:215],v225 offset:36864
buffer_load_dwordx4 v240, s[24:27], s66 offen offset:2048 lds
MM a[80:83],a[160:163],v[16:19],a[80:83]
s_add_u32 s66, s66, 9216
EX v118,v118
MM a[80:83],a[164:167],v[20:23],a[80:83]
RD a[216:219],v226 offset:36864
CV v84,v112,v113
MM a[88:91],a[164:167],v[28:31],a[88:91]
EX v119,v119
MM a[88:91],a[160:163],v[24:27],a[88:91]
buffer_load_dwordx4 v240, s[24:27], s66 offen offset:3072 lds
EX v120,v120
MM a[92:95],a[168:171],v[24:27],a[92:95]
s_xor_b32 s66, s94, 5
RD a[220:223],v227 offset:36864
CV v85,v114,v115
MM a[92:95],a[172:175],v[28:31],a[92:95]
s_mul_i32 s66, s66, 163840
EX v121,v121
MM a[84:87],a[172:175],v[20:23],a[84:87]
s_add_u32 s66, s66, s86
RD a[224:227],v224 offset:40960
CV v86,v116,v117
MM a[84:87],a[168:171],v[16:19],a[84:87]
s_add_u32 m0, s65, 0
EX v122,v122
MM a[112:115],a[176:179],v[16:19],a[112:115]
buffer_load_dwordx4 v240, s[24:27], s66 offen offset:0 lds
RD a[228:231],v225 offset:40960
EX v123,v123
MM a[112:115],a[180:183],v[20:23],a[112:115]
s_add_u32 s66, s66, 9216
RD a[232:235],v226 offset:40960
CV v87,v118,v119
MM a[120:123],a[180:183],v[28:31],a[120:123]
EX v124,v124
MM a[120:123],a[176:179],v[24:27],a[120:123]
buffer_load_dwordx4 v240, s[24:27], s66 offen offset:1024 lds
MM a[124:127],a[184:187],v[24:27],a[124:127]
s_add_u32 s66, s66, 9216
RD a[236:239],v227 offset:40960
EX v125,v125
MM a[124:127],a[188:191],v[28:31],a[124:127]
EX v126,v126
MM a[116:119],a[188:191],v[20:23],a[116:119]
RD a[240:243],v224 offset:45056
RD a[244:247],v225 offset:45056
RD a[248:251],v226 offset:45056
RD a[252:255],v227 offset:45056
EX v127,v127
MM a[116:119],a[184:187],v[16:19],a[116:119]
buffer_load_dwordx4 v240, s[24:27], s66 offen offset:2048 lds
CV v92,v120,v121
v_smfmac_f32_16x16x64_bf16 v[152:155], v[236:239], v[80:87], v234
s_add_u32 s66, s66, 9216
CV v93,v122,v123
CV v94,v124,v125
CV v95,v126,v127
s_nop 1
v_smfmac_f32_16x16x64_bf16 v[156:159], v[236:239], v[88:95], v234
s_waitcnt lgkmcnt(0)
buffer_load_dwordx4 v240, s[24:27], s66 offen offset:3072 lds
v_lshrrev_b32 v145, 9, v248
v_mul_u32_u24 v145, 1536, v145
v_sub_u32 v244, v232, v145
v_sub_u32 v235, v233, v145
v_add_u32 v244, 65536, v244
v_add_u32 v235, 65536, v235
v_and_b32 v145, 960, v248
v_lshlrev_b32 v145, 1, v145
v_bfe_u32 v146, v248, 7, 2
v_lshl_add_u32 v145, v146, 5, v145
v_bfe_u32 v146, v248, 4, 2
v_lshl_add_u32 v145, v146, 3, v145
v_add_u32 v228, %[slds], v145
v_xor_b32 v229, 32, v228
v_xor_b32 v230, 64, v228
v_xor_b32 v231, 96, v228
v_add_u32 v144, 65536, v228
v_add_u32 v145, 65536, v229
v_add_u32 v146, 65536, v230
v_add_u32 v147, 65536, v231
MM v[16:19],a[192:195],v[168:171],v[136:139]
TR a[128:129],v232 offset:16384
TR a[130:131],v232 offset:20480
MM v[16:19],a[196:199],v[184:187],v[16:19]
TR a[132:133],v232 offset:24576
TR a[134:135],v232 offset:28672
MM v[16:19],a[200:203],v[200:203],v[16:19]
TR a[136:137],v233 offset:16384
TR a[138:139],v233 offset:20480
MM v[16:19],a[204:207],v[216:219],v[16:19]
v_max3_f32 v249, v152, v153, v156
v_max_f32 v249, v249, v157
MM v[20:23],a[220:223],v[216:219],v[136:139]
TR a[140:141],v233 offset:24576
v_cmp_lt_f32 vcc, s71, v249
MM v[20:23],a[216:219],v[200:203],v[20:23]
s_cbranch_vccnz LSITE37_%=
LBACK37_%=:
TR a[142:143],v233 offset:28672
v_add_f32 v246, v152, v153
MM v[20:23],a[212:215],v[184:187],v[20:23]
TR a[144:145],v232 offset:16896
v_add_f32 v247, v156, v157
MM v[20:23],a[208:211],v[168:171],v[20:23]
TR a[146:147],v232 offset:20992
TR a[148:149],v232 offset:25088
MM v[28:31],a[208:211],v[172:175],v[140:143]
s_waitcnt vmcnt(7)
v_cmp_ne_u32 vcc, s69, v242
s_cbranch_vccnz LRPO0_%=
LRPO0b_%=:
MM v[28:31],a[212:215],v[188:191],v[28:31]
s_xor_b32 s92, s49, 4
s_mul_i32 s92, s92, 17408
s_add_u32 s92, s83, s92
MM v[28:31],a[216:219],v[204:207],v[28:31]
buffer_load_dwordx4 a[0:3], v248, s[20:23], s92 offen offset:0 sc1
MM v[28:31],a[220:223],v[220:223],v[28:31]
buffer_load_dwordx4 a[32:35], v248, s[20:23], s92 offen offset:1024 sc1
MM v[24:27],a[204:207],v[220:223],v[140:143]
TR a[150:151],v232 offset:29184
v_xor_b32 v96, s69, v96
v_xor_b32 v97, s69, v97
EX v16,v16
MM v[24:27],a[200:203],v[204:207],v[24:27]
TR a[152:153],v233 offset:16896
TR a[154:155],v233 offset:20992
v_or_b32 v99, v96, v97
EX v17,v17
MM v[24:27],a[196:199],v[188:191],v[24:27]
buffer_load_dwordx4 a[64:67], v248, s[20:23], s92 offen offset:2048 sc1
TR a[156:157],v233 offset:25088
MM v[24:27],a[192:195],v[172:175],v[24:27]
TR a[158:159],v233 offset:29184
v_cmp_ne_u32 vcc, 0, v99
s_cbranch_vccnz LRPW_%=
LRPWb_%=:
EX v18,v18
MM v[48:51],a[224:227],v[168:171],v[136:139]
TR a[160:161],v232 offset:17408
TR a[162:163],v232 offset:21504
EX v19,v19
MM v[48:51],a[228:231],v[184:187],v[48:51]
buffer_load_dwordx4 a[96:99], v248, s[20:23], s92 offen offset:3072 sc1
TR a[164:165],v232 offset:25600
MM v[48:51],a[232:235],v[200:203],v[48:51]
s_add_u32 s92, s92, 4096
TR a[166:167],v232 offset:29696
TR a[168:169],v233 offset:17408
EX v20,v20
MM v[48:51],a[236:239],v[216:219],v[48:51]
TR a[170:171],v233 offset:21504
TR a[172:173],v233 offset:25600
MM v[52:55],a[252:255],v[216:219],v[136:139]
buffer_load_dwordx4 a[4:7], v248, s[20:23], s92 offen offset:0 sc1
EX v21,v21
MM v[52:55],a[248:251],v[200:203],v[52:55]
TR a[174:175],v233 offset:29696
CV v16,v16,v17
MM v[52:55],a[244:247],v[184:187],v[52:55]
TR a[176:177],v232 offset:17920
EX v22,v22
MM v[52:55],a[240:243],v[168:171],v[52:55]
buffer_load_dwordx4 a[36:39], v248, s[20:23], s92 offen offset:1024 sc1
TR a[178:179],v232 offset:22016
EX v23,v23
MM v[60:63],a[240:243],v[172:175],v[140:143]
TR a[180:181],v232 offset:26112
TR a[182:183],v232 offset:30208
MM v[60:63],a[244:247],v[188:191],v[60:63]
EX v24,v24
MM v[60:63],a[248:251],v[204:207],v[60:63]
buffer_load_dwordx4 a[68:71], v248, s[20:23], s92 offen offset:2048 sc1
TR a[184:185],v233 offset:17920
TR a[186:187],v233 offset:22016
TR a[188:189],v233 offset:26112
TR a[190:191],v233 offset:30208
EX v25,v25
MM v[60:63],a[252:255],v[220:223],v[60:63]
EX v26,v26
CV v17,v18,v19
MM v[56:59],a[236:239],v[220:223],v[140:143]
EX v27,v27
MM v[56:59],a[232:235],v[204:207],v[56:59]
buffer_load_dwordx4 a[100:103], v248, s[20:23], s92 offen offset:3072 sc1
MM v[56:59],a[228:231],v[188:191],v[56:59]
EX v28,v28
CV v18,v20,v21
MM v[56:59],a[224:227],v[172:175],v[56:59]
s_waitcnt lgkmcnt(0)
s_barrier
CV v19,v22,v23
MM a[16:19],a[128:131],v[80:83],a[16:19]
s_xor_b32 s66, s94, 6
EX v29,v29
MM a[16:19],a[132:135],v[84:87],a[16:19]
s_mul_i32 s66, s66, 163840
CV v24,v24,v25
CV v25,v26,v27
MM a[24:27],a[132:135],v[92:95],a[24:27]
s_add_u32 s66, s66, s87
EX v30,v30
MM a[24:27],a[128:131],v[88:91],a[24:27]
s_add_u32 m0, s65, 81920
EX v31,v31
MM a[28:31],a[136:139],v[88:91],a[28:31]
EX v48,v48
MM a[28:31],a[140:143],v[92:95],a[28:31]
CV v26,v28,v29
CV v27,v30,v31
MM a[20:23],a[140:143],v[84:87],a[20:23]
buffer_load_dwordx4 v240, s[24:27], s66 offen offset:0 lds
MM a[20:23],a[136:139],v[80:83],a[20:23]
s_add_u32 s66, s66, 9216
EX v49,v49
MM a[48:51],a[144:147],v[80:83],a[48:51]
EX v50,v50
MM a[48:51],a[148:151],v[84:87],a[48:51]
buffer_load_dwordx4 v240, s[24:27], s66 offen offset:1024 lds
MM a[56:59],a[148:151],v[92:95],a[56:59]
s_add_u32 s66, s66, 9216
EX v51,v51
MM a[56:59],a[144:147],v[88:91],a[56:59]
TR a[128:129],v232 offset:32768
CV v20,v48,v49
MM a[60:63],a[152:155],v[88:91],a[60:63]
EX v52,v52
MM a[60:63],a[156:159],v[92:95],a[60:63]
TR a[130:131],v232 offset:36864
TR a[132:133],v232 offset:40960
MM a[52:55],a[156:159],v[84:87],a[52:55]
)ASM"
R"ASM(
EX v53,v53
MM a[52:55],a[152:155],v[80:83],a[52:55]
TR a[134:135],v232 offset:45056
TR a[136:137],v233 offset:32768
buffer_load_dwordx4 v240, s[24:27], s66 offen offset:2048 lds
MM a[80:83],a[160:163],v[80:83],a[80:83]
s_add_u32 s66, s66, 9216
TR a[138:139],v233 offset:36864
TR a[140:141],v233 offset:40960
EX v54,v54
MM a[80:83],a[164:167],v[84:87],a[80:83]
TR a[142:143],v233 offset:45056
TR a[144:145],v232 offset:33280
MM a[88:91],a[164:167],v[92:95],a[88:91]
s_xor_b32 s92, s49, 5
s_mul_i32 s92, s92, 17408
s_add_u32 s92, s83, s92
EX v55,v55
MM a[88:91],a[160:163],v[88:91],a[88:91]
buffer_load_dwordx4 a[8:11], v248, s[20:23], s92 offen offset:0 sc1
TR a[146:147],v232 offset:37376
EX v56,v56
MM a[92:95],a[168:171],v[88:91],a[92:95]
TR a[148:149],v232 offset:41472
TR a[150:151],v232 offset:45568
buffer_load_dwordx4 v240, s[24:27], s66 offen offset:3072 lds
MM a[92:95],a[172:175],v[92:95],a[92:95]
s_xor_b32 s66, s94, 6
TR a[152:153],v233 offset:33280
EX v57,v57
CV v21,v50,v51
MM a[84:87],a[172:175],v[84:87],a[84:87]
s_mul_i32 s66, s66, 163840
TR a[154:155],v233 offset:37376
TR a[156:157],v233 offset:41472
MM a[84:87],a[168:171],v[80:83],a[84:87]
s_add_u32 s66, s66, s86
buffer_load_dwordx4 a[40:43], v248, s[20:23], s92 offen offset:1024 sc1
TR a[158:159],v233 offset:45568
s_add_u32 m0, s65, 16384
EX v58,v58
MM a[112:115],a[176:179],v[80:83],a[112:115]
TR a[160:161],v232 offset:33792
CV v22,v52,v53
EX v59,v59
MM a[112:115],a[180:183],v[84:87],a[112:115]
TR a[162:163],v232 offset:37888
TR a[164:165],v232 offset:41984
MM a[120:123],a[180:183],v[92:95],a[120:123]
buffer_load_dwordx4 a[72:75], v248, s[20:23], s92 offen offset:2048 sc1
TR a[166:167],v232 offset:46080
EX v60,v60
MM a[120:123],a[176:179],v[88:91],a[120:123]
TR a[168:169],v233 offset:33792
TR a[170:171],v233 offset:37888
CV v23,v54,v55
MM a[124:127],a[184:187],v[88:91],a[124:127]
TR a[172:173],v233 offset:41984
EX v61,v61
MM a[124:127],a[188:191],v[92:95],a[124:127]
buffer_load_dwordx4 a[104:107], v248, s[20:23], s92 offen offset:3072 sc1
TR a[174:175],v233 offset:46080
EX v62,v62
MM a[116:119],a[188:191],v[84:87],a[116:119]
TR a[176:177],v232 offset:34304
TR a[178:179],v232 offset:38400
EX v63,v63
MM a[116:119],a[184:187],v[80:83],a[116:119]
s_add_u32 s92, s92, 4096
TR a[180:181],v232 offset:42496
CV v28,v56,v57
v_smfmac_f32_16x16x64_bf16 v[152:155], v[236:239], v[16:23], v234
buffer_load_dwordx4 v240, s[24:27], s66 offen offset:0 lds
s_add_u32 s66, s66, 9216
TR a[182:183],v232 offset:46592
CV v29,v58,v59
CV v30,v60,v61
CV v31,v62,v63
s_nop 1
v_smfmac_f32_16x16x64_bf16 v[156:159], v[236:239], v[24:31], v234
TR a[184:185],v233 offset:34304
TR a[186:187],v233 offset:38400
TR a[188:189],v233 offset:42496
TR a[190:191],v233 offset:46592
s_waitcnt lgkmcnt(0)
buffer_load_dwordx4 v240, s[24:27], s66 offen offset:1024 lds
s_add_u32 s66, s66, 9216
buffer_load_dwordx4 v240, s[24:27], s66 offen offset:2048 lds
s_add_u32 s66, s66, 9216
buffer_load_dwordx4 v240, s[24:27], s66 offen offset:3072 lds
s_nop 6
v_max3_f32 v249, v152, v153, v156
v_max_f32 v249, v249, v157
v_cmp_lt_f32 vcc, s71, v249
s_cbranch_vccnz LSITE38_%=
LBACK38_%=:
v_add_f32 v246, v152, v153
v_add_f32 v247, v156, v157
s_waitcnt lgkmcnt(0)
MM a[16:19],a[128:131],v[16:19],a[16:19]
s_barrier
MM a[16:19],a[132:135],v[20:23],a[16:19]
buffer_load_dwordx4 a[12:15], v248, s[20:23], s92 offen offset:0 sc1
MM a[24:27],a[132:135],v[28:31],a[24:27]
v_rcp_f32 v153, v246
MM a[24:27],a[128:131],v[24:27],a[24:27]
v_rcp_f32 v157, v247
MM a[28:31],a[136:139],v[24:27],a[28:31]
v_bfe_u32 v152, v241, 16, 8
v_lshl_or_b32 v152, v152, 1, 1
MM a[28:31],a[140:143],v[28:31],a[28:31]
buffer_load_dwordx4 a[44:47], v248, s[20:23], s92 offen offset:1024 sc1
MM a[20:23],a[140:143],v[20:23],a[20:23]
v_lshlrev_b32 v152, 22, v152
v_mul_f32 v152, -v152, v153
MM a[20:23],a[136:139],v[16:19],a[20:23]
v_bfe_u32 v156, v241, 24, 8
v_lshl_or_b32 v156, v156, 1, 1
MM a[48:51],a[144:147],v[16:19],a[48:51]
TR a[128:129],v244 offset:0
TR a[130:131],v244 offset:1024
MM a[48:51],a[148:151],v[20:23],a[48:51]
buffer_load_dwordx4 a[76:79], v248, s[20:23], s92 offen offset:2048 sc1
MM a[56:59],a[148:151],v[28:31],a[56:59]
TR a[132:133],v244 offset:2048
TR a[134:135],v244 offset:3072
MM a[56:59],a[144:147],v[24:27],a[56:59]
v_lshlrev_b32 v156, 22, v156
v_mul_f32 v156, -v156, v157
MM a[60:63],a[152:155],v[24:27],a[60:63]
s_mul_i32 s78, s49, 17408
TR a[136:137],v244 offset:4096
TR a[138:139],v244 offset:5120
MM a[60:63],a[156:159],v[28:31],a[60:63]
s_add_u32 s78, s83, s78
buffer_load_dwordx4 a[108:111], v248, s[20:23], s92 offen offset:3072 sc1
MM a[52:55],a[156:159],v[20:23],a[52:55]
s_add_u32 s78, s78, 8192
TR a[140:141],v244 offset:6144
TR a[142:143],v244 offset:7168
MM a[52:55],a[152:155],v[16:19],a[52:55]
s_add_u32 s79, s78, 4096
AR v80,a16
AR v81,a17
MM a[80:83],a[160:163],v[16:19],a[80:83]
TR a[144:145],v235 offset:0
TR a[146:147],v235 offset:1024
MM a[80:83],a[164:167],v[20:23],a[80:83]
AR v82,a18
AR v83,a19
MM a[88:91],a[164:167],v[28:31],a[88:91]
TR a[148:149],v235 offset:2048
TR a[150:151],v235 offset:3072
MM a[88:91],a[160:163],v[24:27],a[88:91]
AR v84,a20
AR v85,a21
MM a[92:95],a[168:171],v[24:27],a[92:95]
TR a[152:153],v235 offset:4096
TR a[154:155],v235 offset:5120
MM a[92:95],a[172:175],v[28:31],a[92:95]
s_xor_b32 s66, s94, 7
AR v86,a22
AR v87,a23
MM a[84:87],a[172:175],v[20:23],a[84:87]
s_mul_i32 s66, s66, 163840
TR a[156:157],v235 offset:6144
TR a[158:159],v235 offset:7168
v_fma_f32 v80, v80, v153, v152
MM a[84:87],a[168:171],v[16:19],a[84:87]
s_add_u32 s66, s66, s87
v_fma_f32 v81, v81, v153, v152
v_fma_f32 v82, v82, v153, v152
MM a[112:115],a[176:179],v[16:19],a[112:115]
s_add_u32 m0, s65, 98304
TR a[160:161],v144 offset:49152
TR a[162:163],v144 offset:51200
v_fma_f32 v83, v83, v153, v152
MM a[112:115],a[180:183],v[20:23],a[112:115]
buffer_load_dwordx4 v240, s[24:27], s66 offen offset:0 lds
v_fma_f32 v84, v84, v153, v152
v_fma_f32 v85, v85, v153, v152
MM a[120:123],a[180:183],v[28:31],a[120:123]
s_add_u32 s66, s66, 9216
TR a[164:165],v144 offset:53248
TR a[166:167],v144 offset:55296
v_fma_f32 v86, v86, v153, v152
v_fma_f32 v87, v87, v153, v152
MM a[120:123],a[176:179],v[24:27],a[120:123]
CV v168,v80,v81
CV v169,v82,v83
MM a[124:127],a[184:187],v[24:27],a[124:127]
TR a[168:169],v144 offset:57344
TR a[170:171],v144 offset:59392
CV v170,v84,v85
MM a[124:127],a[188:191],v[28:31],a[124:127]
buffer_load_dwordx4 v240, s[24:27], s66 offen offset:1024 lds
CV v171,v86,v87
MM a[116:119],a[188:191],v[20:23],a[116:119]
LPG26_%=:
s_cbranch_execz LPG30_%=
s_add_u32 s66, s66, 9216
TR a[172:173],v144 offset:61440
TR a[174:175],v144 offset:63488
MM a[116:119],a[184:187],v[16:19],a[116:119]
buffer_store_dwordx4 v[168:171], v248, s[20:23], s78 offen offset:0 sc1
AR v88,a24
AR v89,a25
s_waitcnt vmcnt(23)
s_waitcnt lgkmcnt(15)
MM v[128:131],a[128:131],a[0:3],0
TR a[176:177],v145 offset:49152
TR a[178:179],v145 offset:51200
AR v90,a26
AR v91,a27
MM v[128:131],a[132:135],a[32:35],v[128:131]
v_bfe_u32 v249, v248, 6, 2
v_lshlrev_b32 v249, 8, v249
AR v92,a28
AR v93,a29
MM v[128:131],a[136:139],a[64:67],v[128:131]
TR a[180:181],v145 offset:53248
TR a[182:183],v145 offset:55296
AR v94,a30
MM v[128:131],a[140:143],a[96:99],v[128:131]
s_waitcnt vmcnt(19)
v_lshrrev_b32 v250, 8, v248
v_lshl_add_u32 v249, v250, 6, v249
AR v95,a31
v_fma_f32 v88, v88, v157, v156
MM v[136:139],a[140:143],a[100:103],0
TR a[184:185],v145 offset:57344
TR a[186:187],v145 offset:59392
v_fma_f32 v89, v89, v157, v156
v_fma_f32 v90, v90, v157, v156
MM v[136:139],a[136:139],a[68:71],v[136:139]
buffer_load_dwordx4 v240, s[24:27], s66 offen offset:2048 lds
v_and_b32 v251, 48, v248
v_add_u32 v249, v249, v251
v_fma_f32 v91, v91, v157, v156
MM v[136:139],a[132:135],a[36:39],v[136:139]
s_add_u32 s66, s66, 9216
TR a[188:189],v145 offset:61440
TR a[190:191],v145 offset:63488
v_fma_f32 v92, v92, v157, v156
v_fma_f32 v93, v93, v157, v156
MM v[136:139],a[128:131],a[4:7],v[136:139]
s_waitcnt lgkmcnt(15)
v_fma_f32 v94, v94, v157, v156
v_fma_f32 v95, v95, v157, v156
MM v[140:143],a[144:147],a[4:7],0
s_lshr_b32 s60, s49, 3
TR a[128:129],v146 offset:49152
TR a[130:131],v146 offset:51200
CV v172,v88,v89
CV v173,v90,v91
MM v[140:143],a[148:151],a[36:39],v[140:143]
buffer_load_dwordx4 v240, s[24:27], s66 offen offset:3072 lds
CV v174,v92,v93
MM v[140:143],a[152:155],a[68:71],v[140:143]
TR a[132:133],v146 offset:53248
TR a[134:135],v146 offset:55296
CV v175,v94,v95
MM v[140:143],a[156:159],a[100:103],v[140:143]
s_xor_b32 s66, s94, 7
buffer_store_dwordx4 v[172:175], v248, s[20:23], s79 offen offset:0 sc1
MM v[132:135],a[156:159],a[96:99],0
s_mul_i32 s66, s66, 163840
TR a[136:137],v146 offset:57344
TR a[138:139],v146 offset:59392
AR v112,a48
AR v113,a49
MM v[132:135],a[152:155],a[64:67],v[132:135]
s_add_u32 s66, s66, s86
AR v114,a50
AR v115,a51
MM v[132:135],a[148:151],a[32:35],v[132:135]
s_add_u32 m0, s65, 32768
TR a[140:141],v146 offset:61440
TR a[142:143],v146 offset:63488
AR v116,a52
MM v[132:135],a[144:147],a[0:3],v[132:135]
s_waitcnt lgkmcnt(15)
buffer_load_dwordx4 v240, s[24:27], s66 offen offset:0 lds
AR v117,a53
AR v118,a54
MM v[64:67],a[160:163],a[0:3],0
s_xor_b32 s92, s49, 6
TR a[144:145],v147 offset:49152
TR a[146:147],v147 offset:51200
AR v119,a55
v_fma_f32 v112, v112, v153, v152
MM v[64:67],a[164:167],a[32:35],v[64:67]
s_mul_i32 s92, s92, 17408
s_add_u32 s92, s83, s92
v_fma_f32 v113, v113, v153, v152
v_fma_f32 v114, v114, v153, v152
MM v[64:67],a[168:171],a[64:67],v[64:67]
s_add_u32 s66, s66, 9216
TR a[148:149],v147 offset:53248
TR a[150:151],v147 offset:55296
v_fma_f32 v115, v115, v153, v152
MM v[64:67],a[172:175],a[96:99],v[64:67]
buffer_load_dwordx4 v[160:163], v248, s[20:23], s92 offen offset:0 sc1
v_fma_f32 v116, v116, v153, v152
v_fma_f32 v117, v117, v153, v152
MM v[72:75],a[172:175],a[100:103],0
TR a[152:153],v147 offset:57344
TR a[154:155],v147 offset:59392
v_fma_f32 v118, v118, v153, v152
v_fma_f32 v119, v119, v153, v152
MM v[72:75],a[168:171],a[68:71],v[72:75]
CV v184,v112,v113
CV v185,v114,v115
MM v[72:75],a[164:167],a[36:39],v[72:75]
TR a[156:157],v147 offset:61440
TR a[158:159],v147 offset:63488
CV v186,v116,v117
MM v[72:75],a[160:163],a[4:7],v[72:75]
s_waitcnt lgkmcnt(15)
buffer_load_dwordx4 v[176:179], v248, s[20:23], s92 offen offset:1024 sc1
CV v187,v118,v119
MM v[76:79],a[176:179],a[4:7],0
TR a[160:161],v228 offset:49152
TR a[162:163],v228 offset:51200
MM v[76:79],a[180:183],a[36:39],v[76:79]
v_xor_b32 v253, s60, v250
v_and_b32 v238, 240, v248
MM v[76:79],a[184:187],a[68:71],v[76:79]
TR a[164:165],v228 offset:53248
TR a[166:167],v228 offset:55296
MM v[76:79],a[188:191],a[100:103],v[76:79]
buffer_store_dwordx4 v[184:187], v248, s[20:23], s78 offen offset:1024 sc1
AR v120,a56
AR v121,a57
AR v122,a58
AR v123,a59
AR v124,a60
AR v125,a61
MM v[68:71],a[188:191],a[96:99],0
TR a[168:169],v228 offset:57344
TR a[170:171],v228 offset:59392
AR v126,a62
MM v[68:71],a[184:187],a[64:67],v[68:71]
AR v127,a63
v_fma_f32 v120, v120, v157, v156
MM v[68:71],a[180:183],a[32:35],v[68:71]
TR a[172:173],v228 offset:61440
TR a[174:175],v228 offset:63488
v_fma_f32 v121, v121, v157, v156
v_fma_f32 v122, v122, v157, v156
MM v[68:71],a[176:179],a[0:3],v[68:71]
s_waitcnt lgkmcnt(15)
buffer_load_dwordx4 v[192:195], v248, s[20:23], s92 offen offset:2048 sc1
v_fma_f32 v123, v123, v157, v156
MM v[96:99],a[128:131],a[0:3],0
TR a[176:177],v229 offset:49152
TR a[178:179],v229 offset:51200
v_fma_f32 v124, v124, v157, v156
v_fma_f32 v125, v125, v157, v156
MM v[96:99],a[132:135],a[32:35],v[96:99]
v_fma_f32 v126, v126, v157, v156
v_fma_f32 v127, v127, v157, v156
MM v[96:99],a[136:139],a[64:67],v[96:99]
TR a[180:181],v229 offset:53248
TR a[182:183],v229 offset:55296
CV v188,v120,v121
MM v[96:99],a[140:143],a[96:99],v[96:99]
buffer_load_dwordx4 v[208:211], v248, s[20:23], s92 offen offset:3072 sc1
CV v189,v122,v123
CV v190,v124,v125
MM v[104:107],a[140:143],a[100:103],0
TR a[184:185],v229 offset:57344
TR a[186:187],v229 offset:59392
CV v191,v126,v127
MM v[104:107],a[136:139],a[68:71],v[104:107]
)ASM"
R"ASM(
s_add_u32 s92, s92, 4096
v_lshl_add_u32 v232, v250, 2, v238
s_add_u32 s61, s65, 131072
MM v[104:107],a[132:135],a[36:39],v[104:107]
TR a[188:189],v229 offset:61440
TR a[190:191],v229 offset:63488
MM v[104:107],a[128:131],a[4:7],v[104:107]
s_waitcnt lgkmcnt(15)
buffer_store_dwordx4 v[188:191], v248, s[20:23], s79 offen offset:1024 sc1
AR v80,a80
AR v81,a81
AR v82,a82
AR v83,a83
MM v[108:111],a[144:147],a[4:7],0
TR a[128:129],v230 offset:49152
TR a[130:131],v230 offset:51200
AR v84,a84
MM v[108:111],a[148:151],a[36:39],v[108:111]
AR v85,a85
AR v86,a86
MM v[108:111],a[152:155],a[68:71],v[108:111]
TR a[132:133],v230 offset:53248
TR a[134:135],v230 offset:55296
AR v87,a87
v_fma_f32 v80, v80, v153, v152
MM v[108:111],a[156:159],a[100:103],v[108:111]
buffer_load_dwordx4 v[164:167], v248, s[20:23], s92 offen offset:0 sc1
v_fma_f32 v81, v81, v153, v152
v_fma_f32 v82, v82, v153, v152
MM v[100:103],a[156:159],a[96:99],0
TR a[136:137],v230 offset:57344
TR a[138:139],v230 offset:59392
v_fma_f32 v83, v83, v153, v152
MM v[100:103],a[152:155],a[64:67],v[100:103]
v_fma_f32 v84, v84, v153, v152
v_fma_f32 v85, v85, v153, v152
MM v[100:103],a[148:151],a[32:35],v[100:103]
TR a[140:141],v230 offset:61440
TR a[142:143],v230 offset:63488
v_fma_f32 v86, v86, v153, v152
v_fma_f32 v87, v87, v153, v152
MM v[100:103],a[144:147],a[0:3],v[100:103]
s_waitcnt lgkmcnt(15)
buffer_load_dwordx4 v[180:183], v248, s[20:23], s92 offen offset:1024 sc1
CV v200,v80,v81
MM v[0:3],a[160:163],a[0:3],0
TR a[144:145],v231 offset:49152
TR a[146:147],v231 offset:51200
CV v201,v82,v83
CV v202,v84,v85
MM v[0:3],a[164:167],a[32:35],v[0:3]
CV v203,v86,v87
v_mul_u32_u24 v224, 5, v238
MM v[0:3],a[168:171],a[64:67],v[0:3]
TR a[148:149],v231 offset:53248
TR a[150:151],v231 offset:55296
MM v[0:3],a[172:175],a[96:99],v[0:3]
buffer_store_dwordx4 v[200:203], v248, s[20:23], s78 offen offset:2048 sc1
AR v88,a88
AR v89,a89
MM v[8:11],a[172:175],a[100:103],0
TR a[152:153],v231 offset:57344
TR a[154:155],v231 offset:59392
AR v90,a90
AR v91,a91
MM v[8:11],a[168:171],a[68:71],v[8:11]
AR v92,a92
AR v93,a93
MM v[8:11],a[164:167],a[36:39],v[8:11]
TR a[156:157],v231 offset:61440
TR a[158:159],v231 offset:63488
AR v94,a94
MM v[8:11],a[160:163],a[4:7],v[8:11]
s_waitcnt lgkmcnt(15)
buffer_load_dwordx4 v[196:199], v248, s[20:23], s92 offen offset:2048 sc1
AR v95,a95
v_fma_f32 v88, v88, v157, v156
s_barrier
MM v[12:15],a[176:179],a[4:7],0
TR a[160:161],v244 offset:8192
TR a[162:163],v244 offset:9216
v_fma_f32 v89, v89, v157, v156
v_fma_f32 v90, v90, v157, v156
MM v[12:15],a[180:183],a[36:39],v[12:15]
v_fma_f32 v91, v91, v157, v156
v_fma_f32 v92, v92, v157, v156
MM v[12:15],a[184:187],a[68:71],v[12:15]
TR a[164:165],v244 offset:10240
TR a[166:167],v244 offset:11264
v_fma_f32 v93, v93, v157, v156
MM v[12:15],a[188:191],a[100:103],v[12:15]
buffer_load_dwordx4 v[212:215], v248, s[20:23], s92 offen offset:3072 sc1
v_fma_f32 v94, v94, v157, v156
v_fma_f32 v95, v95, v157, v156
MM v[4:7],a[188:191],a[96:99],0
TR a[168:169],v244 offset:12288
TR a[170:171],v244 offset:13312
CV v204,v88,v89
MM v[4:7],a[184:187],a[64:67],v[4:7]
CV v205,v90,v91
CV v206,v92,v93
MM v[4:7],a[180:183],a[32:35],v[4:7]
TR a[172:173],v244 offset:14336
TR a[174:175],v244 offset:15360
CV v207,v94,v95
MM v[4:7],a[176:179],a[0:3],v[4:7]
s_waitcnt lgkmcnt(15)
buffer_store_dwordx4 v[204:207], v248, s[20:23], s79 offen offset:2048 sc1
MM v[32:35],a[128:131],a[0:3],0
TR a[176:177],v235 offset:8192
TR a[178:179],v235 offset:9216
AR v112,a112
AR v113,a113
MM v[32:35],a[132:135],a[32:35],v[32:35]
AR v114,a114
AR v115,a115
MM v[32:35],a[136:139],a[64:67],v[32:35]
TR a[180:181],v235 offset:10240
TR a[182:183],v235 offset:11264
AR v116,a116
MM v[32:35],a[140:143],a[96:99],v[32:35]
buffer_load_dwordx4 v240, s[24:27], s66 offen offset:1024 lds
AR v117,a117
AR v118,a118
MM v[40:43],a[140:143],a[100:103],0
s_add_u32 s66, s66, 9216
TR a[184:185],v235 offset:12288
TR a[186:187],v235 offset:13312
AR v119,a119
v_fma_f32 v112, v112, v153, v152
MM v[40:43],a[136:139],a[68:71],v[40:43]
v_fma_f32 v113, v113, v153, v152
v_fma_f32 v114, v114, v153, v152
MM v[40:43],a[132:135],a[36:39],v[40:43]
TR a[188:189],v235 offset:14336
TR a[190:191],v235 offset:15360
v_fma_f32 v115, v115, v153, v152
MM v[40:43],a[128:131],a[4:7],v[40:43]
s_waitcnt lgkmcnt(15)
buffer_load_dwordx4 v240, s[24:27], s66 offen offset:2048 lds
v_fma_f32 v116, v116, v153, v152
v_fma_f32 v117, v117, v153, v152
MM v[44:47],a[144:147],a[4:7],0
s_add_u32 s66, s66, 9216
TR a[128:129],v144 offset:0
TR a[130:131],v144 offset:2048
v_fma_f32 v118, v118, v153, v152
v_fma_f32 v119, v119, v153, v152
MM v[44:47],a[148:151],a[36:39],v[44:47]
CV v216,v112,v113
CV v217,v114,v115
MM v[44:47],a[152:155],a[68:71],v[44:47]
TR a[132:133],v144 offset:4096
TR a[134:135],v144 offset:6144
CV v218,v116,v117
MM v[44:47],a[156:159],a[100:103],v[44:47]
buffer_load_dwordx4 v240, s[24:27], s66 offen offset:3072 lds
CV v219,v118,v119
MM v[36:39],a[156:159],a[96:99],0
TR a[136:137],v144 offset:8192
TR a[138:139],v144 offset:10240
MM v[36:39],a[152:155],a[64:67],v[36:39]
s_xor_b32 s66, s94, 0
v_lshl_add_u32 v224, v250, 3, v224
v_add_u32 v224, s61, v224
MM v[36:39],a[148:151],a[32:35],v[36:39]
s_mul_i32 s66, s66, 163840
TR a[140:141],v144 offset:12288
TR a[142:143],v144 offset:14336
MM v[36:39],a[144:147],a[0:3],v[36:39]
s_waitcnt lgkmcnt(15)
buffer_store_dwordx4 v[216:219], v248, s[20:23], s78 offen offset:3072 sc1
AR v120,a120
AR v121,a121
AR v122,a122
AR v123,a123
AR v124,a124
s_waitcnt vmcnt(31)
MM v[128:131],a[160:163],a[8:11],v[128:131]
s_add_u32 s66, s66, s88
TR a[144:145],v145 offset:0
TR a[146:147],v145 offset:2048
AR v125,a125
AR v126,a126
MM v[128:131],a[164:167],a[40:43],v[128:131]
s_add_u32 m0, s89, 131072
AR v127,a127
v_fma_f32 v120, v120, v157, v156
MM v[128:131],a[168:171],a[72:75],v[128:131]
TR a[148:149],v145 offset:4096
TR a[150:151],v145 offset:6144
v_fma_f32 v121, v121, v157, v156
v_fma_f32 v122, v122, v157, v156
MM v[128:131],a[172:175],a[104:107],v[128:131]
s_waitcnt vmcnt(23)
buffer_load_dwordx4 v252, s[24:27], s66 offen offset:0 lds
v_fma_f32 v123, v123, v157, v156
MM v[136:139],a[172:175],a[108:111],v[136:139]
s_add_u32 s66, s66, 19456
TR a[152:153],v145 offset:8192
TR a[154:155],v145 offset:10240
v_fma_f32 v124, v124, v157, v156
v_fma_f32 v125, v125, v157, v156
MM v[136:139],a[168:171],a[76:79],v[136:139]
v_fma_f32 v126, v126, v157, v156
v_fma_f32 v127, v127, v157, v156
MM v[136:139],a[164:167],a[44:47],v[136:139]
TR a[156:157],v145 offset:12288
TR a[158:159],v145 offset:14336
CV v220,v120,v121
MM v[136:139],a[160:163],a[12:15],v[136:139]
s_waitcnt lgkmcnt(15)
buffer_load_dwordx4 v252, s[24:27], s66 offen offset:1024 lds
CV v221,v122,v123
CV v222,v124,v125
MM v[140:143],a[176:179],a[12:15],v[140:143]
TR a[160:161],v146 offset:0
TR a[162:163],v146 offset:2048
CV v223,v126,v127
MM v[140:143],a[180:183],a[44:47],v[140:143]
s_xor_b32 s66, s94, 0
v_mul_u32_u24 v254, 1280, v250
v_mul_u32_u24 v250, 33792, v253
MM v[140:143],a[184:187],a[76:79],v[140:143]
s_mul_i32 s66, s66, 163840
TR a[164:165],v146 offset:4096
TR a[166:167],v146 offset:6144
MM v[140:143],a[188:191],a[108:111],v[140:143]
s_add_u32 s66, s66, s87
buffer_store_dwordx4 v[220:223], v248, s[20:23], s79 offen offset:3072 sc1
MM v[132:135],a[188:191],a[104:107],v[132:135]
s_add_u32 m0, s65, 114688
TR a[168:169],v146 offset:8192
TR a[170:171],v146 offset:10240
MM v[132:135],a[184:187],a[72:75],v[132:135]
v_add_u32 v250, v250, v238
v_lshlrev_b32 v253, 4, v253
MM v[132:135],a[180:183],a[40:43],v[132:135]
TR a[172:173],v146 offset:12288
TR a[174:175],v146 offset:14336
MM v[132:135],a[176:179],a[8:11],v[132:135]
s_waitcnt lgkmcnt(15)
buffer_load_dwordx4 v240, s[24:27], s66 offen offset:0 lds
MM v[64:67],a[128:131],a[8:11],v[64:67]
s_xor_b32 s92, s49, 7
TR a[176:177],v147 offset:0
TR a[178:179],v147 offset:2048
MM v[64:67],a[132:135],a[40:43],v[64:67]
s_mul_i32 s92, s92, 17408
s_add_u32 s92, s83, s92
v_bfe_u32 v238, v248, 4, 3
MM v[64:67],a[136:139],a[72:75],v[64:67]
s_add_u32 s66, s66, 9216
TR a[180:181],v147 offset:4096
TR a[182:183],v147 offset:6144
MM v[64:67],a[140:143],a[104:107],v[64:67]
buffer_load_dwordx4 a[192:195], v248, s[20:23], s92 offen offset:0 sc1
MM v[72:75],a[140:143],a[108:111],v[72:75]
TR a[184:185],v147 offset:8192
TR a[186:187],v147 offset:10240
MM v[72:75],a[136:139],a[76:79],v[72:75]
v_lshl_add_u32 v254, v238, 3, v254
v_bfe_u32 v238, v248, 7, 1
MM v[72:75],a[132:135],a[44:47],v[72:75]
TR a[188:189],v147 offset:12288
TR a[190:191],v147 offset:14336
MM v[72:75],a[128:131],a[12:15],v[72:75]
s_waitcnt lgkmcnt(15)
buffer_load_dwordx4 a[208:211], v248, s[20:23], s92 offen offset:1024 sc1
MM v[76:79],a[144:147],a[12:15],v[76:79]
TR a[128:129],v228 offset:0
TR a[130:131],v228 offset:2048
MM v[76:79],a[148:151],a[44:47],v[76:79]
v_mul_u32_u24 v238, 5120, v238
v_add_u32 v254, v254, v238
MM v[76:79],a[152:155],a[76:79],v[76:79]
TR a[132:133],v228 offset:4096
TR a[134:135],v228 offset:6144
MM v[76:79],a[156:159],a[108:111],v[76:79]
buffer_load_dwordx4 a[224:227], v248, s[20:23], s92 offen offset:2048 sc1
MM v[68:71],a[156:159],a[104:107],v[68:71]
TR a[136:137],v228 offset:8192
TR a[138:139],v228 offset:10240
MM v[68:71],a[152:155],a[72:75],v[68:71]
v_lshrrev_b32 v238, 6, v248
v_mul_u32_u24 v225, 80, v238
MM v[68:71],a[148:151],a[40:43],v[68:71]
TR a[140:141],v228 offset:12288
TR a[142:143],v228 offset:14336
MM v[68:71],a[144:147],a[8:11],v[68:71]
s_waitcnt lgkmcnt(15)
buffer_load_dwordx4 a[240:243], v248, s[20:23], s92 offen offset:3072 sc1
MM v[96:99],a[160:163],a[8:11],v[96:99]
TR a[144:145],v229 offset:0
TR a[146:147],v229 offset:2048
MM v[96:99],a[164:167],a[40:43],v[96:99]
s_add_u32 s92, s92, 4096
v_add3_u32 v225, v225, v251, s61
v_mul_u32_u24 v238, 1280, v238
MM v[96:99],a[168:171],a[72:75],v[96:99]
TR a[148:149],v229 offset:4096
TR a[150:151],v229 offset:6144
MM v[96:99],a[172:175],a[104:107],v[96:99]
buffer_load_dwordx4 a[196:199], v248, s[20:23], s92 offen offset:0 sc1
MM v[104:107],a[172:175],a[108:111],v[104:107]
TR a[152:153],v229 offset:8192
TR a[154:155],v229 offset:10240
MM v[104:107],a[168:171],a[76:79],v[104:107]
v_add_u32 v251, v251, v238
MM v[104:107],a[164:167],a[44:47],v[104:107]
TR a[156:157],v229 offset:12288
TR a[158:159],v229 offset:14336
MM v[104:107],a[160:163],a[12:15],v[104:107]
s_waitcnt lgkmcnt(15)
buffer_load_dwordx4 a[212:215], v248, s[20:23], s92 offen offset:1024 sc1
MM v[108:111],a[176:179],a[12:15],v[108:111]
TR a[160:161],v230 offset:0
TR a[162:163],v230 offset:2048
MM v[108:111],a[180:183],a[44:47],v[108:111]
MM v[108:111],a[184:187],a[76:79],v[108:111]
TR a[164:165],v230 offset:4096
TR a[166:167],v230 offset:6144
MM v[108:111],a[188:191],a[108:111],v[108:111]
buffer_load_dwordx4 a[228:231], v248, s[20:23], s92 offen offset:2048 sc1
MM v[100:103],a[188:191],a[104:107],v[100:103]
TR a[168:169],v230 offset:8192
TR a[170:171],v230 offset:10240
MM v[100:103],a[184:187],a[72:75],v[100:103]
MM v[100:103],a[180:183],a[40:43],v[100:103]
TR a[172:173],v230 offset:12288
TR a[174:175],v230 offset:14336
MM v[100:103],a[176:179],a[8:11],v[100:103]
s_waitcnt lgkmcnt(15)
buffer_load_dwordx4 a[244:247], v248, s[20:23], s92 offen offset:3072 sc1
MM v[0:3],a[128:131],a[8:11],v[0:3]
TR a[176:177],v231 offset:0
TR a[178:179],v231 offset:2048
MM v[0:3],a[132:135],a[40:43],v[0:3]
MM v[0:3],a[136:139],a[72:75],v[0:3]
TR a[180:181],v231 offset:4096
TR a[182:183],v231 offset:6144
MM v[0:3],a[140:143],a[104:107],v[0:3]
buffer_load_dwordx4 v240, s[24:27], s66 offen offset:1024 lds
MM v[8:11],a[140:143],a[108:111],v[8:11]
s_add_u32 s66, s66, 9216
TR a[184:185],v231 offset:8192
TR a[186:187],v231 offset:10240
MM v[8:11],a[136:139],a[76:79],v[8:11]
MM v[8:11],a[132:135],a[44:47],v[8:11]
TR a[188:189],v231 offset:12288
TR a[190:191],v231 offset:14336
MM v[8:11],a[128:131],a[12:15],v[8:11]
s_waitcnt lgkmcnt(15)
buffer_load_dwordx4 v240, s[24:27], s66 offen offset:2048 lds
s_barrier
MM v[12:15],a[144:147],a[12:15],v[12:15]
s_add_u32 s66, s66, 9216
TR a[128:129],v244 offset:16384
TR a[130:131],v244 offset:17408
)ASM"
R"ASM(
MM v[12:15],a[148:151],a[44:47],v[12:15]
LPG27_%=:
s_cbranch_execz LTR1_%=
MM v[12:15],a[152:155],a[76:79],v[12:15]
TR a[132:133],v244 offset:18432
TR a[134:135],v244 offset:19456
MM v[12:15],a[156:159],a[108:111],v[12:15]
buffer_load_dwordx4 v240, s[24:27], s66 offen offset:3072 lds
MM v[4:7],a[156:159],a[104:107],v[4:7]
TR a[136:137],v244 offset:20480
TR a[138:139],v244 offset:21504
MM v[4:7],a[152:155],a[72:75],v[4:7]
s_xor_b32 s66, s94, 0
s_mul_i32 s66, s66, 163840
s_add_u32 s66, s66, s86
MM v[4:7],a[148:151],a[40:43],v[4:7]
s_add_u32 m0, s65, 49152
TR a[140:141],v244 offset:22528
TR a[142:143],v244 offset:23552
MM v[4:7],a[144:147],a[8:11],v[4:7]
s_waitcnt lgkmcnt(15)
buffer_load_dwordx4 v240, s[24:27], s66 offen offset:0 lds
MM v[32:35],a[160:163],a[8:11],v[32:35]
s_add_u32 s66, s66, 9216
TR a[144:145],v235 offset:16384
TR a[146:147],v235 offset:17408
MM v[32:35],a[164:167],a[40:43],v[32:35]
MM v[32:35],a[168:171],a[72:75],v[32:35]
TR a[148:149],v235 offset:18432
TR a[150:151],v235 offset:19456
MM v[32:35],a[172:175],a[104:107],v[32:35]
buffer_load_dwordx4 v240, s[24:27], s66 offen offset:1024 lds
MM v[40:43],a[172:175],a[108:111],v[40:43]
s_add_u32 s66, s66, 9216
TR a[152:153],v235 offset:20480
TR a[154:155],v235 offset:21504
MM v[40:43],a[168:171],a[76:79],v[40:43]
MM v[40:43],a[164:167],a[44:47],v[40:43]
TR a[156:157],v235 offset:22528
TR a[158:159],v235 offset:23552
MM v[40:43],a[160:163],a[12:15],v[40:43]
s_waitcnt lgkmcnt(15)
buffer_load_dwordx4 v240, s[24:27], s66 offen offset:2048 lds
MM v[44:47],a[176:179],a[12:15],v[44:47]
s_lshl_b32 s97, s84, 2
TR a[160:161],v144 offset:16384
TR a[162:163],v144 offset:18432
MM v[44:47],a[180:183],a[44:47],v[44:47]
s_add_u32 s97, s97, s96
s_add_u32 s97, s97, 16
s_waitcnt vmcnt(15)
MM v[44:47],a[184:187],a[76:79],v[44:47]
s_add_u32 s66, s66, 9216
TR a[164:165],v144 offset:20480
TR a[166:167],v144 offset:22528
MM v[44:47],a[188:191],a[108:111],v[44:47]
v_mov_b32 v242, s69
s_mov_b64 exec, 1
buffer_store_dword v242, off, s[20:23], s97 offset:0 sc1
s_mov_b64 exec, -1
MM v[36:39],a[188:191],a[104:107],v[36:39]
TR a[168:169],v144 offset:24576
TR a[170:171],v144 offset:26624
MM v[36:39],a[184:187],a[72:75],v[36:39]
buffer_load_dwordx4 v240, s[24:27], s66 offen offset:3072 lds
MM v[36:39],a[180:183],a[40:43],v[36:39]
TR a[172:173],v144 offset:28672
TR a[174:175],v144 offset:30720
MM v[36:39],a[176:179],a[8:11],v[36:39]
s_waitcnt lgkmcnt(15)
s_xor_b32 s66, s94, 1
s_mul_i32 s66, s66, 163840
MM v[128:131],a[128:131],v[160:163],v[128:131]
s_add_u32 s66, s66, s88
TR a[176:177],v145 offset:16384
TR a[178:179],v145 offset:18432
MM v[128:131],a[132:135],v[176:179],v[128:131]
s_add_u32 m0, s89, 139264
MM v[128:131],a[136:139],v[192:195],v[128:131]
TR a[180:181],v145 offset:20480
TR a[182:183],v145 offset:22528
MM v[128:131],a[140:143],v[208:211],v[128:131]
buffer_load_dwordx4 v252, s[24:27], s66 offen offset:0 lds
MM v[136:139],a[140:143],v[212:215],v[136:139]
s_add_u32 s66, s66, 19456
TR a[184:185],v145 offset:24576
TR a[186:187],v145 offset:26624
MM v[136:139],a[136:139],v[196:199],v[136:139]
MM v[136:139],a[132:135],v[180:183],v[136:139]
TR a[188:189],v145 offset:28672
TR a[190:191],v145 offset:30720
MM v[136:139],a[128:131],v[164:167],v[136:139]
s_waitcnt lgkmcnt(15)
buffer_load_dwordx4 v252, s[24:27], s66 offen offset:1024 lds
MM v[140:143],a[144:147],v[164:167],v[140:143]
TR a[128:129],v146 offset:16384
TR a[130:131],v146 offset:18432
MM v[140:143],a[148:151],v[180:183],v[140:143]
s_xor_b32 s66, s94, 1
s_mul_i32 s66, s66, 163840
s_add_u32 s66, s66, s87
MM v[140:143],a[152:155],v[196:199],v[140:143]
s_add_u32 m0, s65, 65536
TR a[132:133],v146 offset:20480
TR a[134:135],v146 offset:22528
MM v[140:143],a[156:159],v[212:215],v[140:143]
buffer_load_dwordx4 v240, s[24:27], s66 offen offset:0 lds
MM v[132:135],a[156:159],v[208:211],v[132:135]
s_add_u32 s66, s66, 9216
TR a[136:137],v146 offset:24576
TR a[138:139],v146 offset:26624
MM v[132:135],a[152:155],v[192:195],v[132:135]
MM v[132:135],a[148:151],v[176:179],v[132:135]
TR a[140:141],v146 offset:28672
TR a[142:143],v146 offset:30720
MM v[132:135],a[144:147],v[160:163],v[132:135]
s_waitcnt lgkmcnt(15)
buffer_load_dwordx4 v240, s[24:27], s66 offen offset:1024 lds
MM v[64:67],a[160:163],v[160:163],v[64:67]
s_add_u32 s66, s66, 9216
TR a[144:145],v147 offset:16384
TR a[146:147],v147 offset:18432
MM v[64:67],a[164:167],v[176:179],v[64:67]
MM v[64:67],a[168:171],v[192:195],v[64:67]
TR a[148:149],v147 offset:20480
TR a[150:151],v147 offset:22528
MM v[64:67],a[172:175],v[208:211],v[64:67]
buffer_load_dwordx4 v240, s[24:27], s66 offen offset:2048 lds
MM v[72:75],a[172:175],v[212:215],v[72:75]
s_add_u32 s66, s66, 9216
TR a[152:153],v147 offset:24576
TR a[154:155],v147 offset:26624
MM v[72:75],a[168:171],v[196:199],v[72:75]
MM v[72:75],a[164:167],v[180:183],v[72:75]
TR a[156:157],v147 offset:28672
TR a[158:159],v147 offset:30720
MM v[72:75],a[160:163],v[164:167],v[72:75]
s_waitcnt lgkmcnt(15)
buffer_load_dwordx4 v240, s[24:27], s66 offen offset:3072 lds
MM v[76:79],a[176:179],v[164:167],v[76:79]
TR a[160:161],v228 offset:16384
TR a[162:163],v228 offset:18432
MM v[76:79],a[180:183],v[180:183],v[76:79]
MM v[76:79],a[184:187],v[196:199],v[76:79]
TR a[164:165],v228 offset:20480
TR a[166:167],v228 offset:22528
MM v[76:79],a[188:191],v[212:215],v[76:79]
MM v[68:71],a[188:191],v[208:211],v[68:71]
TR a[168:169],v228 offset:24576
TR a[170:171],v228 offset:26624
MM v[68:71],a[184:187],v[192:195],v[68:71]
MM v[68:71],a[180:183],v[176:179],v[68:71]
TR a[172:173],v228 offset:28672
TR a[174:175],v228 offset:30720
MM v[68:71],a[176:179],v[160:163],v[68:71]
s_waitcnt lgkmcnt(15)
MM v[96:99],a[128:131],v[160:163],v[96:99]
s_add_u32 s80, s18, 131072
TR a[176:177],v229 offset:16384
TR a[178:179],v229 offset:18432
MM v[96:99],a[132:135],v[176:179],v[96:99]
buffer_load_dwordx4 v[16:19], v251, s[20:23], s80 offen offset:0 sc1
MM v[96:99],a[136:139],v[192:195],v[96:99]
TR a[180:181],v229 offset:20480
TR a[182:183],v229 offset:22528
MM v[96:99],a[140:143],v[208:211],v[96:99]
v_mov_b32 v237, 0x3f80
v_and_b32 v236, 16, v248
MM v[104:107],a[140:143],v[212:215],v[104:107]
TR a[184:185],v229 offset:24576
TR a[186:187],v229 offset:26624
MM v[104:107],a[136:139],v[196:199],v[104:107]
s_add_u32 s80, s18, 151552
buffer_load_dwordx4 v[20:23], v251, s[20:23], s80 offen offset:0 sc1
MM v[104:107],a[132:135],v[180:183],v[104:107]
TR a[188:189],v229 offset:28672
TR a[190:191],v229 offset:30720
MM v[104:107],a[128:131],v[164:167],v[104:107]
s_waitcnt lgkmcnt(15)
v_lshlrev_b32 v236, v236, v237
s_mov_b32 s28, 0x30003
MM v[108:111],a[144:147],v[164:167],v[108:111]
s_mov_b32 s29, s28
TR a[128:129],v230 offset:16384
TR a[130:131],v230 offset:18432
MM v[108:111],a[148:151],v[180:183],v[108:111]
s_add_u32 s80, s63, 131072
v_lshrrev_b32 v243, 2, v248
v_min_u32 v243, 12, v243
MM v[108:111],a[152:155],v[196:199],v[108:111]
TR a[132:133],v230 offset:20480
TR a[134:135],v230 offset:22528
MM v[108:111],a[156:159],v[212:215],v[108:111]
buffer_load_dword v242, v243, s[20:23], s96 offen offset:16 sc1
buffer_load_dwordx2 v[150:151], v254, s[20:23], s80 offen offset:0 sc1
MM v[100:103],a[156:159],v[208:211],v[100:103]
TR a[136:137],v230 offset:24576
TR a[138:139],v230 offset:26624
MM v[100:103],a[152:155],v[192:195],v[100:103]
v_cndmask_b32 v48, 0, v236, s[28:29]
s_lshl_b64 s[28:29], s[28:29], 2
MM v[100:103],a[148:151],v[176:179],v[100:103]
TR a[140:141],v230 offset:28672
TR a[142:143],v230 offset:30720
MM v[100:103],a[144:147],v[160:163],v[100:103]
s_waitcnt lgkmcnt(15)
v_cndmask_b32 v49, 0, v236, s[28:29]
s_lshl_b64 s[28:29], s[28:29], 2
MM v[0:3],a[160:163],v[160:163],v[0:3]
TR a[144:145],v231 offset:16384
TR a[146:147],v231 offset:18432
MM v[0:3],a[164:167],v[176:179],v[0:3]
v_cndmask_b32 v50, 0, v236, s[28:29]
s_lshl_b64 s[28:29], s[28:29], 2
MM v[0:3],a[168:171],v[192:195],v[0:3]
TR a[148:149],v231 offset:20480
TR a[150:151],v231 offset:22528
MM v[0:3],a[172:175],v[208:211],v[0:3]
v_cndmask_b32 v51, 0, v236, s[28:29]
s_lshl_b64 s[28:29], s[28:29], 2
MM v[8:11],a[172:175],v[212:215],v[8:11]
TR a[152:153],v231 offset:24576
TR a[154:155],v231 offset:26624
MM v[8:11],a[168:171],v[196:199],v[8:11]
v_cndmask_b32 v52, 0, v236, s[28:29]
s_lshl_b64 s[28:29], s[28:29], 2
MM v[8:11],a[164:167],v[180:183],v[8:11]
TR a[156:157],v231 offset:28672
TR a[158:159],v231 offset:30720
MM v[8:11],a[160:163],v[164:167],v[8:11]
s_waitcnt lgkmcnt(15)
v_cndmask_b32 v53, 0, v236, s[28:29]
s_lshl_b64 s[28:29], s[28:29], 2
s_barrier
MM v[12:15],a[176:179],v[164:167],v[12:15]
s_xor_b32 s66, s94, 2
TR a[160:161],v244 offset:24576
TR a[162:163],v244 offset:25600
MM v[12:15],a[180:183],v[180:183],v[12:15]
s_mul_i32 s66, s66, 163840
v_cndmask_b32 v54, 0, v236, s[28:29]
s_lshl_b64 s[28:29], s[28:29], 2
MM v[12:15],a[184:187],v[196:199],v[12:15]
s_add_u32 s66, s66, s88
TR a[164:165],v244 offset:26624
TR a[166:167],v244 offset:27648
MM v[12:15],a[188:191],v[212:215],v[12:15]
s_add_u32 m0, s89, 147456
v_cndmask_b32 v55, 0, v236, s[28:29]
s_mov_b32 s28, 0x300003
MM v[4:7],a[188:191],v[208:211],v[4:7]
s_mov_b32 s29, 0x30000300
TR a[168:169],v244 offset:28672
TR a[170:171],v244 offset:29696
MM v[4:7],a[184:187],v[192:195],v[4:7]
buffer_load_dwordx4 v252, s[24:27], s66 offen offset:0 lds
MM v[4:7],a[180:183],v[176:179],v[4:7]
s_add_u32 s66, s66, 19456
TR a[172:173],v244 offset:30720
TR a[174:175],v244 offset:31744
MM v[4:7],a[176:179],v[160:163],v[4:7]
s_waitcnt lgkmcnt(15)
v_mov_b32 v56, 0
v_mov_b32 v57, 0
MM v[32:35],a[128:131],v[160:163],v[32:35]
TR a[176:177],v235 offset:24576
TR a[178:179],v235 offset:25600
MM v[32:35],a[132:135],v[176:179],v[32:35]
buffer_load_dwordx4 v252, s[24:27], s66 offen offset:1024 lds
MM v[32:35],a[136:139],v[192:195],v[32:35]
TR a[180:181],v235 offset:26624
TR a[182:183],v235 offset:27648
MM v[32:35],a[140:143],v[208:211],v[32:35]
s_xor_b32 s66, s94, 2
v_mov_b32 v60, 0
v_mov_b32 v61, 0
MM v[40:43],a[140:143],v[212:215],v[40:43]
s_mul_i32 s66, s66, 163840
TR a[184:185],v235 offset:28672
TR a[186:187],v235 offset:29696
MM v[40:43],a[136:139],v[196:199],v[40:43]
s_add_u32 s66, s66, s87
v_cndmask_b32 v58, 0, v236, s[28:29]
s_lshl_b64 s[28:29], s[28:29], 2
MM v[40:43],a[132:135],v[180:183],v[40:43]
s_add_u32 m0, s65, 81920
TR a[188:189],v235 offset:30720
TR a[190:191],v235 offset:31744
MM v[40:43],a[128:131],v[164:167],v[40:43]
s_waitcnt lgkmcnt(15)
buffer_load_dwordx4 v240, s[24:27], s66 offen offset:0 lds
MM v[44:47],a[144:147],v[164:167],v[44:47]
s_add_u32 s66, s66, 9216
TR a[128:129],v144 offset:32768
TR a[130:131],v144 offset:34816
MM v[44:47],a[148:151],v[180:183],v[44:47]
v_cndmask_b32 v59, 0, v236, s[28:29]
MM v[44:47],a[152:155],v[196:199],v[44:47]
TR a[132:133],v144 offset:36864
TR a[134:135],v144 offset:38912
MM v[44:47],a[156:159],v[212:215],v[44:47]
buffer_load_dwordx4 v240, s[24:27], s66 offen offset:1024 lds
MM v[36:39],a[156:159],v[208:211],v[36:39]
s_add_u32 s66, s66, 9216
TR a[136:137],v144 offset:40960
TR a[138:139],v144 offset:43008
MM v[36:39],a[152:155],v[192:195],v[36:39]
MM v[36:39],a[148:151],v[176:179],v[36:39]
TR a[140:141],v144 offset:45056
TR a[142:143],v144 offset:47104
MM v[36:39],a[144:147],v[160:163],v[36:39]
s_waitcnt lgkmcnt(15)
buffer_load_dwordx4 v240, s[24:27], s66 offen offset:2048 lds
s_waitcnt vmcnt(27)
MM v[128:131],a[160:163],a[192:195],v[128:131]
s_add_u32 s80, s18, 0
TR a[144:145],v145 offset:32768
TR a[146:147],v145 offset:34816
MM v[128:131],a[164:167],a[208:211],v[128:131]
s_add_u32 s66, s66, 9216
buffer_load_dwordx4 v[24:27], v251, s[32:35], s80 offen offset:0
MM v[128:131],a[168:171],a[224:227],v[128:131]
TR a[148:149],v145 offset:36864
TR a[150:151],v145 offset:38912
MM v[128:131],a[172:175],a[240:243],v[128:131]
s_waitcnt vmcnt(24)
MM v[136:139],a[172:175],a[244:247],v[136:139]
TR a[152:153],v145 offset:40960
TR a[154:155],v145 offset:43008
MM v[136:139],a[168:171],a[228:231],v[136:139]
s_add_u32 s80, s18, 20480
buffer_load_dwordx4 v[28:31], v251, s[32:35], s80 offen offset:0
MM v[136:139],a[164:167],a[212:215],v[136:139]
)ASM"
R"ASM(
TR a[156:157],v145 offset:45056
TR a[158:159],v145 offset:47104
MM v[136:139],a[160:163],a[196:199],v[136:139]
s_waitcnt lgkmcnt(15)
MM v[140:143],a[176:179],a[196:199],v[140:143]
TR a[160:161],v146 offset:32768
TR a[162:163],v146 offset:34816
MM v[140:143],a[180:183],a[212:215],v[140:143]
s_add_u32 s80, s63, 0
buffer_load_dwordx2 v[62:63], v254, s[32:35], s80 offen offset:0
MM v[140:143],a[184:187],a[228:231],v[140:143]
TR a[164:165],v146 offset:36864
TR a[166:167],v146 offset:38912
MM v[140:143],a[188:191],a[244:247],v[140:143]
buffer_load_dwordx4 v240, s[24:27], s66 offen offset:3072 lds
MM v[132:135],a[188:191],a[240:243],v[132:135]
TR a[168:169],v146 offset:40960
TR a[170:171],v146 offset:43008
MM v[132:135],a[184:187],a[224:227],v[132:135]
MM v[132:135],a[180:183],a[208:211],v[132:135]
TR a[172:173],v146 offset:45056
TR a[174:175],v146 offset:47104
MM v[132:135],a[176:179],a[192:195],v[132:135]
s_waitcnt lgkmcnt(15)
MM v[64:67],a[128:131],a[192:195],v[64:67]
s_waitcnt vmcnt(10)
TR a[176:177],v147 offset:32768
TR a[178:179],v147 offset:34816
MM v[64:67],a[132:135],a[208:211],v[64:67]
v_cmp_ne_u32 vcc, s69, v242
s_cbranch_vccnz LRPO1_%=
LRPO1b_%=:
MM v[64:67],a[136:139],a[224:227],v[64:67]
s_xor_b32 s92, s49, 1
TR a[180:181],v147 offset:36864
TR a[182:183],v147 offset:38912
MM v[64:67],a[140:143],a[240:243],v[64:67]
s_mul_i32 s92, s92, 17408
s_add_u32 s92, s83, s92
s_add_u32 s92, s92, 8192
MM v[72:75],a[140:143],a[244:247],v[72:75]
TR a[184:185],v147 offset:40960
TR a[186:187],v147 offset:43008
MM v[72:75],a[136:139],a[228:231],v[72:75]
buffer_load_dwordx4 a[16:19], v248, s[20:23], s92 offen offset:0 sc1
MM v[72:75],a[132:135],a[212:215],v[72:75]
TR a[188:189],v147 offset:45056
TR a[190:191],v147 offset:47104
MM v[72:75],a[128:131],a[196:199],v[72:75]
s_waitcnt lgkmcnt(15)
buffer_load_dwordx4 a[48:51], v248, s[20:23], s92 offen offset:1024 sc1
MM v[76:79],a[144:147],a[196:199],v[76:79]
TR a[128:129],v228 offset:32768
TR a[130:131],v228 offset:34816
MM v[76:79],a[148:151],a[212:215],v[76:79]
MM v[76:79],a[152:155],a[228:231],v[76:79]
TR a[132:133],v228 offset:36864
TR a[134:135],v228 offset:38912
MM v[76:79],a[156:159],a[244:247],v[76:79]
buffer_load_dwordx4 a[80:83], v248, s[20:23], s92 offen offset:2048 sc1
MM v[68:71],a[156:159],a[240:243],v[68:71]
TR a[136:137],v228 offset:40960
TR a[138:139],v228 offset:43008
MM v[68:71],a[152:155],a[224:227],v[68:71]
MM v[68:71],a[148:151],a[208:211],v[68:71]
TR a[140:141],v228 offset:45056
TR a[142:143],v228 offset:47104
MM v[68:71],a[144:147],a[192:195],v[68:71]
s_waitcnt lgkmcnt(15)
buffer_load_dwordx4 a[112:115], v248, s[20:23], s92 offen offset:3072 sc1
MM v[96:99],a[160:163],a[192:195],v[96:99]
s_add_u32 s92, s92, 4096
TR a[144:145],v229 offset:32768
TR a[146:147],v229 offset:34816
MM v[96:99],a[164:167],a[208:211],v[96:99]
MM v[96:99],a[168:171],a[224:227],v[96:99]
TR a[148:149],v229 offset:36864
TR a[150:151],v229 offset:38912
MM v[96:99],a[172:175],a[240:243],v[96:99]
buffer_load_dwordx4 a[20:23], v248, s[20:23], s92 offen offset:0 sc1
MM v[104:107],a[172:175],a[244:247],v[104:107]
TR a[152:153],v229 offset:40960
TR a[154:155],v229 offset:43008
MM v[104:107],a[168:171],a[228:231],v[104:107]
MM v[104:107],a[164:167],a[212:215],v[104:107]
TR a[156:157],v229 offset:45056
TR a[158:159],v229 offset:47104
MM v[104:107],a[160:163],a[196:199],v[104:107]
s_waitcnt lgkmcnt(15)
buffer_load_dwordx4 a[52:55], v248, s[20:23], s92 offen offset:1024 sc1
MM v[108:111],a[176:179],a[196:199],v[108:111]
TR a[160:161],v230 offset:32768
TR a[162:163],v230 offset:34816
MM v[108:111],a[180:183],a[212:215],v[108:111]
MM v[108:111],a[184:187],a[228:231],v[108:111]
TR a[164:165],v230 offset:36864
TR a[166:167],v230 offset:38912
MM v[108:111],a[188:191],a[244:247],v[108:111]
buffer_load_dwordx4 a[84:87], v248, s[20:23], s92 offen offset:2048 sc1
MM v[100:103],a[188:191],a[240:243],v[100:103]
TR a[168:169],v230 offset:40960
TR a[170:171],v230 offset:43008
MM v[100:103],a[184:187],a[224:227],v[100:103]
MM v[100:103],a[180:183],a[208:211],v[100:103]
TR a[172:173],v230 offset:45056
TR a[174:175],v230 offset:47104
MM v[100:103],a[176:179],a[192:195],v[100:103]
s_waitcnt lgkmcnt(15)
buffer_load_dwordx4 a[116:119], v248, s[20:23], s92 offen offset:3072 sc1
MM v[0:3],a[128:131],a[192:195],v[0:3]
TR a[176:177],v231 offset:32768
TR a[178:179],v231 offset:34816
MM v[0:3],a[132:135],a[208:211],v[0:3]
MM v[0:3],a[136:139],a[224:227],v[0:3]
TR a[180:181],v231 offset:36864
TR a[182:183],v231 offset:38912
MM v[0:3],a[140:143],a[240:243],v[0:3]
MM v[8:11],a[140:143],a[244:247],v[8:11]
TR a[184:185],v231 offset:40960
TR a[186:187],v231 offset:43008
MM v[8:11],a[136:139],a[228:231],v[8:11]
MM v[8:11],a[132:135],a[212:215],v[8:11]
TR a[188:189],v231 offset:45056
TR a[190:191],v231 offset:47104
MM v[8:11],a[128:131],a[196:199],v[8:11]
s_waitcnt lgkmcnt(15)
s_barrier
MM v[12:15],a[144:147],a[196:199],v[12:15]
s_xor_b32 s66, s94, 3
TR a[128:129],v244 offset:0
TR a[130:131],v244 offset:1024
MM v[12:15],a[148:151],a[212:215],v[12:15]
s_mul_i32 s66, s66, 163840
s_add_u32 s66, s66, s88
s_add_u32 m0, s89, 155648
MM v[12:15],a[152:155],a[228:231],v[12:15]
TR a[132:133],v244 offset:2048
TR a[134:135],v244 offset:3072
MM v[12:15],a[156:159],a[244:247],v[12:15]
buffer_load_dwordx4 v252, s[24:27], s66 offen offset:0 lds
MM v[4:7],a[156:159],a[240:243],v[4:7]
s_add_u32 s66, s66, 19456
TR a[136:137],v244 offset:4096
TR a[138:139],v244 offset:5120
MM v[4:7],a[152:155],a[224:227],v[4:7]
MM v[4:7],a[148:151],a[208:211],v[4:7]
TR a[140:141],v244 offset:6144
TR a[142:143],v244 offset:7168
MM v[4:7],a[144:147],a[192:195],v[4:7]
s_waitcnt lgkmcnt(15)
buffer_load_dwordx4 v252, s[24:27], s66 offen offset:1024 lds
MM v[32:35],a[160:163],a[192:195],v[32:35]
TR a[144:145],v235 offset:0
TR a[146:147],v235 offset:1024
MM v[32:35],a[164:167],a[208:211],v[32:35]
s_xor_b32 s66, s94, 3
s_mul_i32 s66, s66, 163840
s_add_u32 s66, s66, s87
MM v[32:35],a[168:171],a[224:227],v[32:35]
s_add_u32 m0, s65, 98304
TR a[148:149],v235 offset:2048
TR a[150:151],v235 offset:3072
MM v[32:35],a[172:175],a[240:243],v[32:35]
buffer_load_dwordx4 v240, s[24:27], s66 offen offset:0 lds
MM v[40:43],a[172:175],a[244:247],v[40:43]
s_add_u32 s66, s66, 9216
TR a[152:153],v235 offset:4096
TR a[154:155],v235 offset:5120
MM v[40:43],a[168:171],a[228:231],v[40:43]
MM v[40:43],a[164:167],a[212:215],v[40:43]
TR a[156:157],v235 offset:6144
TR a[158:159],v235 offset:7168
MM v[40:43],a[160:163],a[196:199],v[40:43]
s_waitcnt lgkmcnt(15)
buffer_load_dwordx4 v240, s[24:27], s66 offen offset:1024 lds
MM v[44:47],a[176:179],a[196:199],v[44:47]
s_add_u32 s66, s66, 9216
TR a[160:161],v144 offset:49152
TR a[162:163],v144 offset:51200
MM v[44:47],a[180:183],a[212:215],v[44:47]
MM v[44:47],a[184:187],a[228:231],v[44:47]
TR a[164:165],v144 offset:53248
TR a[166:167],v144 offset:55296
MM v[44:47],a[188:191],a[244:247],v[44:47]
buffer_load_dwordx4 v240, s[24:27], s66 offen offset:2048 lds
MM v[36:39],a[188:191],a[240:243],v[36:39]
s_add_u32 s66, s66, 9216
TR a[168:169],v144 offset:57344
TR a[170:171],v144 offset:59392
MM v[36:39],a[184:187],a[224:227],v[36:39]
MM v[36:39],a[180:183],a[208:211],v[36:39]
TR a[172:173],v144 offset:61440
TR a[174:175],v144 offset:63488
MM v[36:39],a[176:179],a[192:195],v[36:39]
s_waitcnt lgkmcnt(15)
buffer_load_dwordx4 v240, s[24:27], s66 offen offset:3072 lds
MM v[128:131],a[128:131],v[168:171],v[128:131]
TR a[176:177],v145 offset:49152
TR a[178:179],v145 offset:51200
MM v[128:131],a[132:135],v[184:187],v[128:131]
MM v[128:131],a[136:139],v[200:203],v[128:131]
TR a[180:181],v145 offset:53248
TR a[182:183],v145 offset:55296
MM v[128:131],a[140:143],v[216:219],v[128:131]
MM v[136:139],a[140:143],v[220:223],v[136:139]
TR a[184:185],v145 offset:57344
TR a[186:187],v145 offset:59392
MM v[136:139],a[136:139],v[204:207],v[136:139]
MM v[136:139],a[132:135],v[188:191],v[136:139]
TR a[188:189],v145 offset:61440
TR a[190:191],v145 offset:63488
MM v[136:139],a[128:131],v[172:175],v[136:139]
s_waitcnt lgkmcnt(15)
MM v[140:143],a[144:147],v[172:175],v[140:143]
s_xor_b32 s92, s49, 2
TR a[128:129],v146 offset:49152
TR a[130:131],v146 offset:51200
MM v[140:143],a[148:151],v[188:191],v[140:143]
s_mul_i32 s92, s92, 17408
s_add_u32 s92, s83, s92
LPG28_%=:
s_cbranch_execz LTR1_%=
s_add_u32 s92, s92, 8192
MM v[140:143],a[152:155],v[204:207],v[140:143]
TR a[132:133],v146 offset:53248
TR a[134:135],v146 offset:55296
MM v[140:143],a[156:159],v[220:223],v[140:143]
buffer_load_dwordx4 a[24:27], v248, s[20:23], s92 offen offset:0 sc1
MM v[132:135],a[156:159],v[216:219],v[132:135]
TR a[136:137],v146 offset:57344
TR a[138:139],v146 offset:59392
MM v[132:135],a[152:155],v[200:203],v[132:135]
buffer_load_dwordx4 a[56:59], v248, s[20:23], s92 offen offset:1024 sc1
MM v[132:135],a[148:151],v[184:187],v[132:135]
TR a[140:141],v146 offset:61440
TR a[142:143],v146 offset:63488
MM v[132:135],a[144:147],v[168:171],v[132:135]
s_waitcnt lgkmcnt(15)
MM v[64:67],a[160:163],v[168:171],v[64:67]
TR a[144:145],v147 offset:49152
TR a[146:147],v147 offset:51200
MM v[64:67],a[164:167],v[184:187],v[64:67]
buffer_load_dwordx4 a[88:91], v248, s[20:23], s92 offen offset:2048 sc1
MM v[64:67],a[168:171],v[200:203],v[64:67]
TR a[148:149],v147 offset:53248
TR a[150:151],v147 offset:55296
MM v[64:67],a[172:175],v[216:219],v[64:67]
MM v[72:75],a[172:175],v[220:223],v[72:75]
TR a[152:153],v147 offset:57344
TR a[154:155],v147 offset:59392
MM v[72:75],a[168:171],v[204:207],v[72:75]
buffer_load_dwordx4 a[120:123], v248, s[20:23], s92 offen offset:3072 sc1
MM v[72:75],a[164:167],v[188:191],v[72:75]
s_add_u32 s92, s92, 4096
TR a[156:157],v147 offset:61440
TR a[158:159],v147 offset:63488
MM v[72:75],a[160:163],v[172:175],v[72:75]
s_waitcnt lgkmcnt(15)
MM v[76:79],a[176:179],v[172:175],v[76:79]
TR a[160:161],v230 offset:49152
TR a[162:163],v230 offset:51200
MM v[76:79],a[180:183],v[188:191],v[76:79]
buffer_load_dwordx4 a[28:31], v248, s[20:23], s92 offen offset:0 sc1
MM v[76:79],a[184:187],v[204:207],v[76:79]
TR a[164:165],v230 offset:53248
TR a[166:167],v230 offset:55296
MM v[76:79],a[188:191],v[220:223],v[76:79]
MM v[68:71],a[188:191],v[216:219],v[68:71]
TR a[168:169],v230 offset:57344
TR a[170:171],v230 offset:59392
MM v[68:71],a[184:187],v[200:203],v[68:71]
buffer_load_dwordx4 a[60:63], v248, s[20:23], s92 offen offset:1024 sc1
MM v[68:71],a[180:183],v[184:187],v[68:71]
TR a[172:173],v230 offset:61440
TR a[174:175],v230 offset:63488
MM v[68:71],a[176:179],v[168:171],v[68:71]
s_waitcnt lgkmcnt(15)
MM v[96:99],a[128:131],v[168:171],v[96:99]
TR a[176:177],v231 offset:49152
TR a[178:179],v231 offset:51200
MM v[96:99],a[132:135],v[184:187],v[96:99]
buffer_load_dwordx4 a[92:95], v248, s[20:23], s92 offen offset:2048 sc1
MM v[96:99],a[136:139],v[200:203],v[96:99]
TR a[180:181],v231 offset:53248
TR a[182:183],v231 offset:55296
MM v[96:99],a[140:143],v[216:219],v[96:99]
MM v[104:107],a[140:143],v[220:223],v[104:107]
TR a[184:185],v231 offset:57344
TR a[186:187],v231 offset:59392
MM v[104:107],a[136:139],v[204:207],v[104:107]
buffer_load_dwordx4 a[124:127], v248, s[20:23], s92 offen offset:3072 sc1
MM v[104:107],a[132:135],v[188:191],v[104:107]
s_xor_b32 s92, s49, 3
TR a[188:189],v231 offset:61440
TR a[190:191],v231 offset:63488
MM v[104:107],a[128:131],v[172:175],v[104:107]
s_waitcnt lgkmcnt(15)
s_mul_i32 s92, s92, 17408
s_add_u32 s92, s83, s92
s_barrier
MM v[108:111],a[144:147],v[172:175],v[108:111]
s_add_u32 s92, s92, 8192
TR a[128:129],v244 offset:8192
TR a[130:131],v244 offset:9216
MM v[108:111],a[148:151],v[188:191],v[108:111]
buffer_load_dwordx4 a[200:203], v248, s[20:23], s92 offen offset:0 sc1
MM v[108:111],a[152:155],v[204:207],v[108:111]
TR a[132:133],v244 offset:10240
TR a[134:135],v244 offset:11264
MM v[108:111],a[156:159],v[220:223],v[108:111]
MM v[100:103],a[156:159],v[216:219],v[100:103]
TR a[136:137],v244 offset:12288
TR a[138:139],v244 offset:13312
MM v[100:103],a[152:155],v[200:203],v[100:103]
buffer_load_dwordx4 a[216:219], v248, s[20:23], s92 offen offset:1024 sc1
MM v[100:103],a[148:151],v[184:187],v[100:103]
)ASM"
R"ASM(
TR a[140:141],v244 offset:14336
TR a[142:143],v244 offset:15360
MM v[100:103],a[144:147],v[168:171],v[100:103]
s_waitcnt lgkmcnt(15)
MM v[32:35],a[160:163],v[168:171],v[32:35]
TR a[144:145],v235 offset:8192
TR a[146:147],v235 offset:9216
MM v[32:35],a[164:167],v[184:187],v[32:35]
buffer_load_dwordx4 a[232:235], v248, s[20:23], s92 offen offset:2048 sc1
MM v[32:35],a[168:171],v[200:203],v[32:35]
TR a[148:149],v235 offset:10240
TR a[150:151],v235 offset:11264
MM v[32:35],a[172:175],v[216:219],v[32:35]
MM v[40:43],a[172:175],v[220:223],v[40:43]
TR a[152:153],v235 offset:12288
TR a[154:155],v235 offset:13312
MM v[40:43],a[168:171],v[204:207],v[40:43]
buffer_load_dwordx4 a[248:251], v248, s[20:23], s92 offen offset:3072 sc1
MM v[40:43],a[164:167],v[188:191],v[40:43]
s_add_u32 s92, s92, 4096
TR a[156:157],v235 offset:14336
TR a[158:159],v235 offset:15360
MM v[40:43],a[160:163],v[172:175],v[40:43]
s_waitcnt lgkmcnt(15)
MM v[44:47],a[176:179],v[172:175],v[44:47]
TR a[160:161],v144 offset:0
TR a[162:163],v144 offset:2048
MM v[44:47],a[180:183],v[188:191],v[44:47]
buffer_load_dwordx4 a[204:207], v248, s[20:23], s92 offen offset:0 sc1
MM v[44:47],a[184:187],v[204:207],v[44:47]
TR a[164:165],v144 offset:4096
TR a[166:167],v144 offset:6144
MM v[44:47],a[188:191],v[220:223],v[44:47]
MM v[36:39],a[188:191],v[216:219],v[36:39]
TR a[168:169],v144 offset:8192
TR a[170:171],v144 offset:10240
MM v[36:39],a[184:187],v[200:203],v[36:39]
buffer_load_dwordx4 a[220:223], v248, s[20:23], s92 offen offset:1024 sc1
MM v[36:39],a[180:183],v[184:187],v[36:39]
TR a[172:173],v144 offset:12288
TR a[174:175],v144 offset:14336
MM v[36:39],a[176:179],v[168:171],v[36:39]
s_waitcnt lgkmcnt(15)
s_waitcnt vmcnt(24)
MM v[128:131],a[128:131],a[16:19],v[128:131]
TR a[176:177],v145 offset:0
TR a[178:179],v145 offset:2048
MM v[128:131],a[132:135],a[48:51],v[128:131]
buffer_load_dwordx4 a[236:239], v248, s[20:23], s92 offen offset:2048 sc1
MM v[128:131],a[136:139],a[80:83],v[128:131]
TR a[180:181],v145 offset:4096
TR a[182:183],v145 offset:6144
MM v[128:131],a[140:143],a[112:115],v[128:131]
s_waitcnt vmcnt(21)
MM v[136:139],a[140:143],a[116:119],v[136:139]
TR a[184:185],v145 offset:8192
TR a[186:187],v145 offset:10240
MM v[136:139],a[136:139],a[84:87],v[136:139]
buffer_load_dwordx4 a[252:255], v248, s[20:23], s92 offen offset:3072 sc1
MM v[136:139],a[132:135],a[52:55],v[136:139]
TR a[188:189],v145 offset:12288
TR a[190:191],v145 offset:14336
MM v[136:139],a[128:131],a[20:23],v[136:139]
s_waitcnt lgkmcnt(15)
MM v[140:143],a[144:147],a[20:23],v[140:143]
TR a[128:129],v146 offset:0
TR a[130:131],v146 offset:2048
MM v[140:143],a[148:151],a[52:55],v[140:143]
MM v[140:143],a[152:155],a[84:87],v[140:143]
TR a[132:133],v146 offset:4096
TR a[134:135],v146 offset:6144
MM v[140:143],a[156:159],a[116:119],v[140:143]
MM v[132:135],a[156:159],a[112:115],v[132:135]
TR a[136:137],v146 offset:8192
TR a[138:139],v146 offset:10240
MM v[132:135],a[152:155],a[80:83],v[132:135]
MM v[132:135],a[148:151],a[48:51],v[132:135]
TR a[140:141],v146 offset:12288
TR a[142:143],v146 offset:14336
MM v[132:135],a[144:147],a[16:19],v[132:135]
s_waitcnt lgkmcnt(15)
MM v[64:67],a[160:163],a[16:19],v[64:67]
TR a[144:145],v147 offset:0
TR a[146:147],v147 offset:2048
MM v[64:67],a[164:167],a[48:51],v[64:67]
MM v[64:67],a[168:171],a[80:83],v[64:67]
TR a[148:149],v147 offset:4096
TR a[150:151],v147 offset:6144
MM v[64:67],a[172:175],a[112:115],v[64:67]
MM v[72:75],a[172:175],a[116:119],v[72:75]
TR a[152:153],v147 offset:8192
TR a[154:155],v147 offset:10240
MM v[72:75],a[168:171],a[84:87],v[72:75]
MM v[72:75],a[164:167],a[52:55],v[72:75]
TR a[156:157],v147 offset:12288
TR a[158:159],v147 offset:14336
MM v[72:75],a[160:163],a[20:23],v[72:75]
s_waitcnt lgkmcnt(15)
s_waitcnt vmcnt(16)
s_barrier
MM v[76:79],a[176:179],a[20:23],v[76:79]
s_xor_b32 s66, s94, 1
TR a[160:161],v244 offset:16384
TR a[162:163],v244 offset:17408
MM v[76:79],a[180:183],a[52:55],v[76:79]
s_mul_i32 s66, s66, 163840
s_add_u32 s66, s66, s86
s_add_u32 m0, s65, 0
MM v[76:79],a[184:187],a[84:87],v[76:79]
TR a[164:165],v244 offset:18432
TR a[166:167],v244 offset:19456
MM v[76:79],a[188:191],a[116:119],v[76:79]
buffer_load_dwordx4 v240, s[24:27], s66 offen offset:0 lds
MM v[68:71],a[188:191],a[112:115],v[68:71]
s_add_u32 s66, s66, 9216
TR a[168:169],v244 offset:20480
TR a[170:171],v244 offset:21504
MM v[68:71],a[184:187],a[80:83],v[68:71]
MM v[68:71],a[180:183],a[48:51],v[68:71]
TR a[172:173],v244 offset:22528
TR a[174:175],v244 offset:23552
MM v[68:71],a[176:179],a[16:19],v[68:71]
s_waitcnt lgkmcnt(15)
buffer_load_dwordx4 v240, s[24:27], s66 offen offset:1024 lds
MM v[96:99],a[128:131],a[16:19],v[96:99]
s_add_u32 s66, s66, 9216
TR a[176:177],v244 offset:24576
TR a[178:179],v244 offset:25600
MM v[96:99],a[132:135],a[48:51],v[96:99]
MM v[96:99],a[136:139],a[80:83],v[96:99]
TR a[180:181],v244 offset:26624
TR a[182:183],v244 offset:27648
MM v[96:99],a[140:143],a[112:115],v[96:99]
buffer_load_dwordx4 v240, s[24:27], s66 offen offset:2048 lds
MM v[104:107],a[140:143],a[116:119],v[104:107]
s_add_u32 s66, s66, 9216
TR a[184:185],v244 offset:28672
TR a[186:187],v244 offset:29696
MM v[104:107],a[136:139],a[84:87],v[104:107]
MM v[104:107],a[132:135],a[52:55],v[104:107]
TR a[188:189],v244 offset:30720
TR a[190:191],v244 offset:31744
MM v[104:107],a[128:131],a[20:23],v[104:107]
s_waitcnt lgkmcnt(15)
buffer_load_dwordx4 v240, s[24:27], s66 offen offset:3072 lds
MM v[108:111],a[144:147],a[20:23],v[108:111]
TR a[128:129],v235 offset:16384
TR a[130:131],v235 offset:17408
MM v[108:111],a[148:151],a[52:55],v[108:111]
s_xor_b32 s66, s94, 2
s_mul_i32 s66, s66, 163840
s_add_u32 s66, s66, s86
MM v[108:111],a[152:155],a[84:87],v[108:111]
s_add_u32 m0, s65, 16384
TR a[132:133],v235 offset:18432
TR a[134:135],v235 offset:19456
MM v[108:111],a[156:159],a[116:119],v[108:111]
buffer_load_dwordx4 v240, s[24:27], s66 offen offset:0 lds
MM v[100:103],a[156:159],a[112:115],v[100:103]
s_add_u32 s66, s66, 9216
TR a[136:137],v235 offset:20480
TR a[138:139],v235 offset:21504
MM v[100:103],a[152:155],a[80:83],v[100:103]
MM v[100:103],a[148:151],a[48:51],v[100:103]
TR a[140:141],v235 offset:22528
TR a[142:143],v235 offset:23552
MM v[100:103],a[144:147],a[16:19],v[100:103]
s_waitcnt lgkmcnt(15)
buffer_load_dwordx4 v240, s[24:27], s66 offen offset:1024 lds
s_waitcnt vmcnt(18)
MM v[128:131],a[160:163],a[24:27],v[128:131]
s_add_u32 s66, s66, 9216
TR a[144:145],v235 offset:24576
TR a[146:147],v235 offset:25600
MM v[128:131],a[164:167],a[56:59],v[128:131]
MM v[128:131],a[168:171],a[88:91],v[128:131]
TR a[148:149],v235 offset:26624
TR a[150:151],v235 offset:27648
MM v[128:131],a[172:175],a[120:123],v[128:131]
s_waitcnt vmcnt(14)
buffer_load_dwordx4 v240, s[24:27], s66 offen offset:2048 lds
MM v[136:139],a[172:175],a[124:127],v[136:139]
s_add_u32 s66, s66, 9216
TR a[152:153],v235 offset:28672
TR a[154:155],v235 offset:29696
MM v[136:139],a[168:171],a[92:95],v[136:139]
MM v[136:139],a[164:167],a[60:63],v[136:139]
TR a[156:157],v235 offset:30720
TR a[158:159],v235 offset:31744
MM v[136:139],a[160:163],a[28:31],v[136:139]
s_waitcnt lgkmcnt(15)
buffer_load_dwordx4 v240, s[24:27], s66 offen offset:3072 lds
s_waitcnt vmcnt(12)
MM v[128:131],a[176:179],a[200:203],v[128:131]
s_memrealtime s[56:57]
TR a[160:161],v144 offset:16384
TR a[162:163],v144 offset:18432
MM v[128:131],a[180:183],a[216:219],v[128:131]
s_xor_b32 s66, s94, 3
s_mul_i32 s66, s66, 163840
s_add_u32 s66, s66, s86
MM v[128:131],a[184:187],a[232:235],v[128:131]
s_add_u32 m0, s65, 32768
TR a[164:165],v144 offset:20480
TR a[166:167],v144 offset:22528
MM v[128:131],a[188:191],a[248:251],v[128:131]
s_waitcnt vmcnt(8)
buffer_load_dwordx4 v240, s[24:27], s66 offen offset:0 lds
MM v[136:139],a[188:191],a[252:255],v[136:139]
s_add_u32 s66, s66, 9216
TR a[168:169],v144 offset:24576
TR a[170:171],v144 offset:26624
MM v[136:139],a[184:187],a[236:239],v[136:139]
MM v[136:139],a[180:183],a[220:223],v[136:139]
TR a[172:173],v144 offset:28672
TR a[174:175],v144 offset:30720
MM v[136:139],a[176:179],a[204:207],v[136:139]
s_waitcnt lgkmcnt(0)
buffer_load_dwordx4 v240, s[24:27], s66 offen offset:1024 lds
MM v[140:143],a[128:131],a[28:31],v[140:143]
s_add_u32 s66, s66, 9216
TR a[176:177],v144 offset:32768
TR a[178:179],v144 offset:34816
MM v[140:143],a[132:135],a[60:63],v[140:143]
v_mov_b32 v158, s56
v_mov_b32 v159, s69
MM v[140:143],a[136:139],a[92:95],v[140:143]
TR a[180:181],v144 offset:36864
TR a[182:183],v144 offset:38912
MM v[140:143],a[140:143],a[124:127],v[140:143]
s_add_u32 s67, s95, 0
CV v128,v128,v129
CV v129,v130,v131
MM v[132:135],a[140:143],a[120:123],v[132:135]
buffer_load_dwordx4 v240, s[24:27], s66 offen offset:2048 lds
s_mov_b64 exec, 1
buffer_store_dwordx2 v[158:159], off, s[20:23], s19 offset:8 sc1
s_mov_b64 exec, -1
s_nop 0
TR a[184:185],v144 offset:40960
TR a[186:187],v144 offset:43008
MM v[132:135],a[136:139],a[88:91],v[132:135]
s_add_u32 s66, s66, 9216
CV v136,v136,v137
CV v137,v138,v139
MM v[132:135],a[132:135],a[56:59],v[132:135]
TR a[188:189],v144 offset:45056
TR a[190:191],v144 offset:47104
MM v[132:135],a[128:131],a[24:27],v[132:135]
buffer_load_dwordx4 v240, s[24:27], s66 offen offset:3072 lds
MM v[132:135],a[144:147],a[200:203],v[132:135]
TR a[128:129],v145 offset:16384
TR a[130:131],v145 offset:18432
MM v[132:135],a[148:151],a[216:219],v[132:135]
MM v[132:135],a[152:155],a[232:235],v[132:135]
TR a[132:133],v145 offset:20480
TR a[134:135],v145 offset:22528
MM v[132:135],a[156:159],a[248:251],v[132:135]
MM v[140:143],a[156:159],a[252:255],v[140:143]
TR a[136:137],v145 offset:24576
TR a[138:139],v145 offset:26624
MM v[140:143],a[152:155],a[236:239],v[140:143]
MM v[140:143],a[148:151],a[220:223],v[140:143]
TR a[140:141],v145 offset:28672
TR a[142:143],v145 offset:30720
MM v[140:143],a[144:147],a[204:207],v[140:143]
CV v130,v132,v133
CV v131,v134,v135
MM v[72:75],a[160:163],a[28:31],v[72:75]
TR a[144:145],v145 offset:32768
TR a[146:147],v145 offset:34816
MM v[72:75],a[164:167],a[60:63],v[72:75]
v_min_u32 v131, -2, v131
MM v[72:75],a[168:171],a[92:95],v[72:75]
TR a[148:149],v145 offset:36864
TR a[150:151],v145 offset:38912
MM v[72:75],a[172:175],a[124:127],v[72:75]
buffer_store_dwordx4 v[128:131], v249, s[20:23], s67 offen offset:0 sc1
MM v[64:67],a[172:175],a[120:123],v[64:67]
TR a[152:153],v145 offset:40960
TR a[154:155],v145 offset:43008
MM v[64:67],a[168:171],a[88:91],v[64:67]
CV v138,v140,v141
CV v139,v142,v143
MM v[64:67],a[164:167],a[56:59],v[64:67]
TR a[156:157],v145 offset:45056
TR a[158:159],v145 offset:47104
MM v[64:67],a[160:163],a[24:27],v[64:67]
s_waitcnt lgkmcnt(15)
v_min_u32 v139, -2, v139
MM v[64:67],a[176:179],a[200:203],v[64:67]
TR a[160:161],v146 offset:16384
TR a[162:163],v146 offset:18432
MM v[64:67],a[180:183],a[216:219],v[64:67]
buffer_store_dwordx4 v[136:139], v249, s[20:23], s67 offen offset:1024 sc1
MM v[64:67],a[184:187],a[232:235],v[64:67]
TR a[164:165],v146 offset:20480
TR a[166:167],v146 offset:22528
MM v[64:67],a[188:191],a[248:251],v[64:67]
MM v[72:75],a[188:191],a[252:255],v[72:75]
TR a[168:169],v146 offset:24576
TR a[170:171],v146 offset:26624
MM v[72:75],a[184:187],a[236:239],v[72:75]
MM v[72:75],a[180:183],a[220:223],v[72:75]
TR a[172:173],v146 offset:28672
TR a[174:175],v146 offset:30720
MM v[72:75],a[176:179],a[204:207],v[72:75]
s_waitcnt lgkmcnt(15)
MM v[76:79],a[128:131],a[28:31],v[76:79]
TR a[176:177],v146 offset:32768
TR a[178:179],v146 offset:34816
MM v[76:79],a[132:135],a[60:63],v[76:79]
v_lshlrev_b32 v136, 16, v150
v_and_b32 v137, 0xffff0000, v150
MM v[76:79],a[136:139],a[92:95],v[76:79]
TR a[180:181],v146 offset:36864
TR a[182:183],v146 offset:38912
MM v[76:79],a[140:143],a[124:127],v[76:79]
s_add_u32 s67, s95, 4096
CV v64,v64,v65
CV v65,v66,v67
MM v[68:71],a[140:143],a[120:123],v[68:71]
TR a[184:185],v146 offset:40960
TR a[186:187],v146 offset:43008
MM v[68:71],a[136:139],a[88:91],v[68:71]
CV v72,v72,v73
CV v73,v74,v75
MM v[68:71],a[132:135],a[56:59],v[68:71]
TR a[188:189],v146 offset:45056
TR a[190:191],v146 offset:47104
MM v[68:71],a[128:131],a[24:27],v[68:71]
)ASM"
R"ASM(
s_waitcnt lgkmcnt(15)
v_lshlrev_b32 v138, 16, v151
v_and_b32 v139, 0xffff0000, v151
MM v[68:71],a[144:147],a[200:203],v[68:71]
TR a[128:129],v147 offset:16384
TR a[130:131],v147 offset:18432
MM v[68:71],a[148:151],a[216:219],v[68:71]
buffer_load_dwordx2 v[148:149], v253, s[20:23], s68 offen offset:8 sc1
MM v[68:71],a[152:155],a[232:235],v[68:71]
TR a[132:133],v147 offset:20480
TR a[134:135],v147 offset:22528
MM v[68:71],a[156:159],a[248:251],v[68:71]
v_mul_f32 v140, 0xbfb8aa3b, v136
v_mul_f32 v141, 0xbfb8aa3b, v137
MM v[76:79],a[156:159],a[252:255],v[76:79]
TR a[136:137],v147 offset:24576
TR a[138:139],v147 offset:26624
MM v[76:79],a[152:155],a[236:239],v[76:79]
v_mul_f32 v142, 0xbfb8aa3b, v138
v_mul_f32 v143, 0xbfb8aa3b, v139
MM v[76:79],a[148:151],a[220:223],v[76:79]
TR a[140:141],v147 offset:28672
TR a[142:143],v147 offset:30720
MM v[76:79],a[144:147],a[204:207],v[76:79]
s_waitcnt lgkmcnt(15)
CV v66,v68,v69
CV v67,v70,v71
s_waitcnt vmcnt(3)
s_barrier
MM v[104:107],a[160:163],a[28:31],v[104:107]
TR a[144:145],v147 offset:32768
TR a[146:147],v147 offset:34816
MM v[104:107],a[164:167],a[60:63],v[104:107]
EX v140,v140
v_min_u32 v67, -2, v67
MM v[104:107],a[168:171],a[92:95],v[104:107]
TR a[148:149],v147 offset:36864
TR a[150:151],v147 offset:38912
MM v[104:107],a[172:175],a[124:127],v[104:107]
buffer_store_dwordx4 v[64:67], v248, s[20:23], s67 offen offset:0 sc1
MM v[96:99],a[172:175],a[120:123],v[96:99]
TR a[152:153],v147 offset:40960
TR a[154:155],v147 offset:43008
MM v[96:99],a[168:171],a[88:91],v[96:99]
CV v74,v76,v77
CV v75,v78,v79
MM v[96:99],a[164:167],a[56:59],v[96:99]
TR a[156:157],v147 offset:45056
TR a[158:159],v147 offset:47104
MM v[96:99],a[160:163],a[24:27],v[96:99]
s_waitcnt lgkmcnt(15)
EX v141,v141
v_min_u32 v75, -2, v75
MM v[96:99],a[176:179],a[200:203],v[96:99]
TR a[160:161],v230 offset:0
TR a[162:163],v230 offset:2048
MM v[96:99],a[180:183],a[216:219],v[96:99]
buffer_store_dwordx4 v[72:75], v248, s[20:23], s67 offen offset:1024 sc1
MM v[96:99],a[184:187],a[232:235],v[96:99]
TR a[164:165],v230 offset:4096
TR a[166:167],v230 offset:6144
MM v[96:99],a[188:191],a[248:251],v[96:99]
EX v142,v142
MM v[104:107],a[188:191],a[252:255],v[104:107]
TR a[168:169],v230 offset:8192
TR a[170:171],v230 offset:10240
MM v[104:107],a[184:187],a[236:239],v[104:107]
EX v143,v143
MM v[104:107],a[180:183],a[220:223],v[104:107]
TR a[172:173],v230 offset:12288
TR a[174:175],v230 offset:14336
MM v[104:107],a[176:179],a[204:207],v[104:107]
s_waitcnt lgkmcnt(15)
v_add_f32 v140, 1.0, v140
v_add_f32 v141, 1.0, v141
MM v[108:111],a[128:131],a[28:31],v[108:111]
TR a[176:177],v230 offset:16384
TR a[178:179],v230 offset:18432
MM v[108:111],a[132:135],a[60:63],v[108:111]
v_add_f32 v142, 1.0, v142
v_add_f32 v143, 1.0, v143
MM v[108:111],a[136:139],a[92:95],v[108:111]
TR a[180:181],v230 offset:20480
TR a[182:183],v230 offset:22528
MM v[108:111],a[140:143],a[124:127],v[108:111]
s_add_u32 s67, s95, 4096
CV v96,v96,v97
CV v97,v98,v99
MM v[100:103],a[140:143],a[120:123],v[100:103]
TR a[184:185],v230 offset:24576
TR a[186:187],v230 offset:26624
MM v[100:103],a[136:139],a[88:91],v[100:103]
CV v104,v104,v105
CV v105,v106,v107
MM v[100:103],a[132:135],a[56:59],v[100:103]
TR a[188:189],v230 offset:28672
TR a[190:191],v230 offset:30720
MM v[100:103],a[128:131],a[24:27],v[100:103]
s_waitcnt lgkmcnt(15)
v_rcp_f32 v140, v140
MM v[100:103],a[144:147],a[200:203],v[100:103]
TR a[128:129],v230 offset:32768
TR a[130:131],v230 offset:34816
MM v[100:103],a[148:151],a[216:219],v[100:103]
v_rcp_f32 v141, v141
MM v[100:103],a[152:155],a[232:235],v[100:103]
TR a[132:133],v230 offset:36864
TR a[134:135],v230 offset:38912
MM v[100:103],a[156:159],a[248:251],v[100:103]
v_rcp_f32 v142, v142
MM v[108:111],a[156:159],a[252:255],v[108:111]
TR a[136:137],v230 offset:40960
TR a[138:139],v230 offset:43008
MM v[108:111],a[152:155],a[236:239],v[108:111]
v_rcp_f32 v143, v143
MM v[108:111],a[148:151],a[220:223],v[108:111]
TR a[140:141],v230 offset:45056
TR a[142:143],v230 offset:47104
MM v[108:111],a[144:147],a[204:207],v[108:111]
s_waitcnt lgkmcnt(15)
LPG29_%=:
s_cbranch_execz LTR1_%=
CV v98,v100,v101
CV v99,v102,v103
MM v[40:43],a[160:163],a[20:23],v[40:43]
TR a[144:145],v231 offset:0
TR a[146:147],v231 offset:2048
MM v[40:43],a[164:167],a[52:55],v[40:43]
v_min_u32 v99, -2, v99
v_mul_f32 v140, v136, v140
MM v[40:43],a[168:171],a[84:87],v[40:43]
TR a[148:149],v231 offset:4096
TR a[150:151],v231 offset:6144
MM v[40:43],a[172:175],a[116:119],v[40:43]
buffer_store_dwordx4 v[96:99], v248, s[20:23], s67 offen offset:2048 sc1
MM v[32:35],a[172:175],a[112:115],v[32:35]
TR a[152:153],v231 offset:8192
TR a[154:155],v231 offset:10240
MM v[32:35],a[168:171],a[80:83],v[32:35]
CV v106,v108,v109
CV v107,v110,v111
MM v[32:35],a[164:167],a[48:51],v[32:35]
TR a[156:157],v231 offset:12288
TR a[158:159],v231 offset:14336
MM v[32:35],a[160:163],a[16:19],v[32:35]
s_waitcnt lgkmcnt(15)
v_min_u32 v107, -2, v107
v_mul_f32 v141, v137, v141
MM v[32:35],a[176:179],a[24:27],v[32:35]
TR a[160:161],v231 offset:16384
TR a[162:163],v231 offset:18432
MM v[32:35],a[180:183],a[56:59],v[32:35]
buffer_store_dwordx4 v[104:107], v248, s[20:23], s67 offen offset:3072 sc1
MM v[32:35],a[184:187],a[88:91],v[32:35]
TR a[164:165],v231 offset:20480
TR a[166:167],v231 offset:22528
MM v[32:35],a[188:191],a[120:123],v[32:35]
v_mul_f32 v142, v138, v142
v_mul_f32 v143, v139, v143
MM v[40:43],a[188:191],a[124:127],v[40:43]
TR a[168:169],v231 offset:24576
TR a[170:171],v231 offset:26624
MM v[40:43],a[184:187],a[92:95],v[40:43]
CV v150,v140,v141
CV v151,v142,v143
MM v[40:43],a[180:183],a[60:63],v[40:43]
TR a[172:173],v231 offset:28672
TR a[174:175],v231 offset:30720
MM v[40:43],a[176:179],a[28:31],v[40:43]
s_waitcnt lgkmcnt(15)
v_lshlrev_b32 v160, 16, v16
v_and_b32 v161, 0xffff0000, v16
MM v[40:43],a[128:131],a[204:207],v[40:43]
TR a[176:177],v231 offset:32768
TR a[178:179],v231 offset:34816
MM v[40:43],a[132:135],a[220:223],v[40:43]
v_lshlrev_b32 v162, 16, v17
v_and_b32 v163, 0xffff0000, v17
MM v[40:43],a[136:139],a[236:239],v[40:43]
TR a[180:181],v231 offset:36864
TR a[182:183],v231 offset:38912
MM v[40:43],a[140:143],a[252:255],v[40:43]
v_lshlrev_b32 v164, 16, v18
v_and_b32 v165, 0xffff0000, v18
MM v[32:35],a[140:143],a[248:251],v[32:35]
TR a[184:185],v231 offset:40960
TR a[186:187],v231 offset:43008
MM v[32:35],a[136:139],a[232:235],v[32:35]
v_lshlrev_b32 v166, 16, v19
v_and_b32 v167, 0xffff0000, v19
MM v[32:35],a[132:135],a[216:219],v[32:35]
TR a[188:189],v231 offset:45056
TR a[190:191],v231 offset:47104
MM v[32:35],a[128:131],a[200:203],v[32:35]
s_waitcnt lgkmcnt(15)
v_mul_f32 v136, 0xbfb8aa3b, v160
v_mul_f32 v137, 0xbfb8aa3b, v161
MM v[36:39],a[144:147],a[16:19],v[36:39]
TR a[128:129],v228 offset:49152
TR a[130:131],v228 offset:51200
MM v[36:39],a[148:151],a[48:51],v[36:39]
v_mul_f32 v138, 0xbfb8aa3b, v162
v_mul_f32 v139, 0xbfb8aa3b, v163
MM v[36:39],a[152:155],a[80:83],v[36:39]
TR a[132:133],v228 offset:53248
TR a[134:135],v228 offset:55296
MM v[36:39],a[156:159],a[112:115],v[36:39]
s_add_u32 s67, s95, 0
CV v32,v32,v33
CV v33,v34,v35
MM v[44:47],a[156:159],a[116:119],v[44:47]
TR a[136:137],v228 offset:57344
TR a[138:139],v228 offset:59392
MM v[44:47],a[152:155],a[84:87],v[44:47]
CV v40,v40,v41
CV v41,v42,v43
MM v[44:47],a[148:151],a[52:55],v[44:47]
TR a[140:141],v228 offset:61440
TR a[142:143],v228 offset:63488
MM v[44:47],a[144:147],a[20:23],v[44:47]
s_waitcnt lgkmcnt(15)
v_mul_f32 v140, 0xbfb8aa3b, v164
v_mul_f32 v141, 0xbfb8aa3b, v165
MM v[44:47],a[160:163],a[28:31],v[44:47]
s_waitcnt vmcnt(4)
TR a[144:145],v228 offset:0
TR a[146:147],v228 offset:2048
MM v[44:47],a[164:167],a[60:63],v[44:47]
v_cmp_eq_u32 s[8:9], s69, v149
v_subrev_u32 v226, s56, v148
MM v[44:47],a[168:171],a[92:95],v[44:47]
TR a[148:149],v228 offset:4096
TR a[150:151],v228 offset:6144
MM v[44:47],a[172:175],a[124:127],v[44:47]
v_cndmask_b32 v226, 4.0, v226, s[8:9]
v_mul_f32 v142, 0xbfb8aa3b, v166
MM v[36:39],a[172:175],a[120:123],v[36:39]
TR a[152:153],v228 offset:8192
TR a[154:155],v228 offset:10240
MM v[36:39],a[168:171],a[88:91],v[36:39]
v_readlane_b32 s13, v226, 16
v_readlane_b32 s14, v226, 32
MM v[36:39],a[164:167],a[56:59],v[36:39]
TR a[156:157],v228 offset:12288
TR a[158:159],v228 offset:14336
MM v[36:39],a[160:163],a[24:27],v[36:39]
s_waitcnt lgkmcnt(15)
v_readlane_b32 s15, v226, 48
v_mul_f32 v143, 0xbfb8aa3b, v167
MM v[36:39],a[176:179],a[200:203],v[36:39]
s_max_i32 s12, s13, s14
TR a[160:161],v228 offset:16384
TR a[162:163],v228 offset:18432
MM v[36:39],a[180:183],a[216:219],v[36:39]
s_max_i32 s12, s12, s15
EX v136,v136
MM v[36:39],a[184:187],a[232:235],v[36:39]
s_max_i32 s12, s12, 0
TR a[164:165],v228 offset:20480
TR a[166:167],v228 offset:22528
MM v[36:39],a[188:191],a[248:251],v[36:39]
buffer_load_dwordx2 v[148:149], v253, s[20:23], s68 offen offset:8 sc1
MM v[44:47],a[188:191],a[252:255],v[44:47]
TR a[168:169],v228 offset:24576
TR a[170:171],v228 offset:26624
MM v[44:47],a[184:187],a[236:239],v[44:47]
EX v137,v137
MM v[44:47],a[180:183],a[220:223],v[44:47]
TR a[172:173],v228 offset:28672
TR a[174:175],v228 offset:30720
MM v[44:47],a[176:179],a[204:207],v[44:47]
s_waitcnt lgkmcnt(15)
CV v34,v36,v37
CV v35,v38,v39
v_min_u32 v35, -2, v35
buffer_store_dwordx4 v[32:35], v248, s[20:23], s67 offen offset:2048 sc1
MM v[8:11],a[128:131],v[172:175],v[8:11]
TR a[176:177],v228 offset:32768
TR a[178:179],v228 offset:34816
MM v[8:11],a[132:135],v[188:191],v[8:11]
EX v138,v138
MM v[8:11],a[136:139],v[204:207],v[8:11]
TR a[180:181],v228 offset:36864
TR a[182:183],v228 offset:38912
MM v[8:11],a[140:143],v[220:223],v[8:11]
CV v42,v44,v45
CV v43,v46,v47
v_min_u32 v43, -2, v43
buffer_store_dwordx4 v[40:43], v248, s[20:23], s67 offen offset:3072 sc1
MM v[0:3],a[140:143],v[216:219],v[0:3]
TR a[184:185],v228 offset:40960
TR a[186:187],v228 offset:43008
MM v[0:3],a[136:139],v[200:203],v[0:3]
EX v139,v139
MM v[0:3],a[132:135],v[184:187],v[0:3]
TR a[188:189],v228 offset:45056
TR a[190:191],v228 offset:47104
MM v[0:3],a[128:131],v[168:171],v[0:3]
s_waitcnt lgkmcnt(15)
EX v140,v140
MM v[0:3],a[144:147],a[16:19],v[0:3]
TR a[128:129],v229 offset:49152
TR a[130:131],v229 offset:51200
MM v[0:3],a[148:151],a[48:51],v[0:3]
s_cmp_le_i32 s13, -42
s_cselect_b32 s60, 2, 0
s_cselect_b64 s[8:9], -1, 0
MM v[0:3],a[152:155],a[80:83],v[0:3]
s_or_b32 s58, s58, s60
TR a[132:133],v229 offset:53248
TR a[134:135],v229 offset:55296
MM v[0:3],a[156:159],a[112:115],v[0:3]
s_mov_b64 exec, s[8:9]
buffer_load_dwordx4 v[80:83], v248, s[20:23], s36 offen offset:0 sc1
s_mov_b64 exec, -1
MM v[8:11],a[156:159],a[116:119],v[8:11]
TR a[136:137],v229 offset:57344
TR a[138:139],v229 offset:59392
MM v[8:11],a[152:155],a[84:87],v[8:11]
s_mov_b64 exec, s[8:9]
buffer_load_dwordx4 v[84:87], v248, s[20:23], s36 offen offset:1024 sc1
s_mov_b64 exec, -1
MM v[8:11],a[148:151],a[52:55],v[8:11]
TR a[140:141],v229 offset:61440
TR a[142:143],v229 offset:63488
MM v[8:11],a[144:147],a[20:23],v[8:11]
s_waitcnt lgkmcnt(15)
EX v141,v141
MM v[8:11],a[160:163],a[28:31],v[8:11]
TR a[144:145],v229 offset:0
TR a[146:147],v229 offset:2048
MM v[8:11],a[164:167],a[60:63],v[8:11]
s_cmp_le_i32 s15, 17
s_cselect_b32 s60, 8, 0
s_cselect_b64 s[8:9], -1, 0
MM v[8:11],a[168:171],a[92:95],v[8:11]
s_or_b32 s58, s58, s60
TR a[148:149],v229 offset:4096
TR a[150:151],v229 offset:6144
MM v[8:11],a[172:175],a[124:127],v[8:11]
s_mov_b64 exec, s[8:9]
buffer_load_dwordx4 v[112:115], v248, s[20:23], s38 offen offset:0 sc1
s_mov_b64 exec, -1
MM v[0:3],a[172:175],a[120:123],v[0:3]
TR a[152:153],v229 offset:8192
TR a[154:155],v229 offset:10240
MM v[0:3],a[168:171],a[88:91],v[0:3]
s_mov_b64 exec, s[8:9]
buffer_load_dwordx4 v[116:119], v248, s[20:23], s38 offen offset:1024 sc1
s_mov_b64 exec, -1
MM v[0:3],a[164:167],a[56:59],v[0:3]
TR a[156:157],v229 offset:12288
TR a[158:159],v229 offset:14336
MM v[0:3],a[160:163],a[24:27],v[0:3]
s_waitcnt lgkmcnt(15)
EX v142,v142
MM v[0:3],a[176:179],a[200:203],v[0:3]
TR a[160:161],v229 offset:16384
TR a[162:163],v229 offset:18432
MM v[0:3],a[180:183],a[216:219],v[0:3]
)ASM"
R"ASM(
s_cmp_le_i32 s14, 59
s_cselect_b32 s60, 4, 0
s_cselect_b64 s[8:9], -1, 0
MM v[0:3],a[184:187],a[232:235],v[0:3]
s_or_b32 s58, s58, s60
TR a[164:165],v229 offset:20480
TR a[166:167],v229 offset:22528
MM v[0:3],a[188:191],a[248:251],v[0:3]
s_mov_b64 exec, s[8:9]
buffer_load_dwordx4 v[88:91], v248, s[20:23], s37 offen offset:0 sc1
s_mov_b64 exec, -1
MM v[8:11],a[188:191],a[252:255],v[8:11]
TR a[168:169],v229 offset:24576
TR a[170:171],v229 offset:26624
MM v[8:11],a[184:187],a[236:239],v[8:11]
s_mov_b64 exec, s[8:9]
buffer_load_dwordx4 v[92:95], v248, s[20:23], s37 offen offset:1024 sc1
s_mov_b64 exec, -1
MM v[8:11],a[180:183],a[220:223],v[8:11]
TR a[172:173],v229 offset:28672
TR a[174:175],v229 offset:30720
MM v[8:11],a[176:179],a[204:207],v[8:11]
s_waitcnt lgkmcnt(15)
EX v143,v143
MM v[12:15],a[128:131],v[172:175],v[12:15]
TR a[176:177],v229 offset:32768
TR a[178:179],v229 offset:34816
MM v[12:15],a[132:135],v[188:191],v[12:15]
s_cmp_le_i32 s12, 102
s_cselect_b32 s60, 1, 0
s_cselect_b64 s[8:9], -1, 0
MM v[12:15],a[136:139],v[204:207],v[12:15]
s_or_b32 s58, s58, s60
TR a[180:181],v229 offset:36864
TR a[182:183],v229 offset:38912
MM v[12:15],a[140:143],v[220:223],v[12:15]
s_mov_b64 exec, s[8:9]
buffer_load_dwordx4 v[120:123], v250, s[20:23], s39 offen offset:0 sc1
s_mov_b64 exec, -1
MM v[4:7],a[140:143],v[216:219],v[4:7]
TR a[184:185],v229 offset:40960
TR a[186:187],v229 offset:43008
MM v[4:7],a[136:139],v[200:203],v[4:7]
s_mov_b64 exec, s[8:9]
buffer_load_dwordx4 v[124:127], v250, s[20:23], s39 offen offset:256 sc1
s_mov_b64 exec, -1
MM v[4:7],a[132:135],v[184:187],v[4:7]
TR a[188:189],v229 offset:45056
TR a[190:191],v229 offset:47104
MM v[4:7],a[128:131],v[168:171],v[4:7]
s_waitcnt lgkmcnt(15)
v_add_f32 v136, 1.0, v136
v_add_f32 v137, 1.0, v137
MM v[4:7],a[144:147],a[16:19],v[4:7]
v_add_f32 v138, 1.0, v138
v_add_f32 v139, 1.0, v139
MM v[4:7],a[148:151],a[48:51],v[4:7]
v_add_f32 v140, 1.0, v140
v_add_f32 v141, 1.0, v141
MM v[4:7],a[152:155],a[80:83],v[4:7]
v_add_f32 v142, 1.0, v142
v_add_f32 v143, 1.0, v143
MM v[4:7],a[156:159],a[112:115],v[4:7]
v_rcp_f32 v136, v136
MM v[12:15],a[156:159],a[116:119],v[12:15]
v_rcp_f32 v137, v137
MM v[12:15],a[152:155],a[84:87],v[12:15]
v_rcp_f32 v138, v138
MM v[12:15],a[148:151],a[52:55],v[12:15]
v_rcp_f32 v139, v139
MM v[12:15],a[144:147],a[20:23],v[12:15]
s_waitcnt lgkmcnt(8)
v_rcp_f32 v140, v140
MM v[12:15],a[160:163],a[28:31],v[12:15]
s_cmp_le_i32 s13, 0
s_cselect_b32 s60, 2, 0
s_andn2_b32 s60, s60, s58
s_cselect_b64 s[8:9], -1, 0
MM v[12:15],a[164:167],a[60:63],v[12:15]
s_or_b32 s58, s58, s60
s_mov_b64 exec, s[8:9]
buffer_load_dwordx4 v[80:83], v248, s[20:23], s36 offen offset:0 sc1
s_mov_b64 exec, -1
MM v[12:15],a[168:171],a[92:95],v[12:15]
v_rcp_f32 v141, v141
MM v[12:15],a[172:175],a[124:127],v[12:15]
s_mov_b64 exec, s[8:9]
buffer_load_dwordx4 v[84:87], v248, s[20:23], s36 offen offset:1024 sc1
s_mov_b64 exec, -1
MM v[4:7],a[172:175],a[120:123],v[4:7]
v_rcp_f32 v142, v142
MM v[4:7],a[168:171],a[88:91],v[4:7]
v_rcp_f32 v143, v143
MM v[4:7],a[164:167],a[56:59],v[4:7]
v_mul_f32 v136, v160, v136
v_mul_f32 v137, v161, v137
MM v[4:7],a[160:163],a[24:27],v[4:7]
s_waitcnt lgkmcnt(0)
v_mul_f32 v138, v162, v138
v_mul_f32 v139, v163, v139
MM v[4:7],a[176:179],a[200:203],v[4:7]
v_mul_f32 v140, v164, v140
v_mul_f32 v141, v165, v141
MM v[4:7],a[180:183],a[216:219],v[4:7]
v_mul_f32 v142, v166, v142
v_mul_f32 v143, v167, v143
MM v[4:7],a[184:187],a[232:235],v[4:7]
CV v16,v136,v137
CV v17,v138,v139
MM v[4:7],a[188:191],a[248:251],v[4:7]
CV v18,v140,v141
CV v19,v142,v143
MM v[12:15],a[188:191],a[252:255],v[12:15]
v_lshlrev_b32 v160, 16, v16
v_and_b32 v161, 0xffff0000, v16
MM v[12:15],a[184:187],a[236:239],v[12:15]
v_lshlrev_b32 v162, 16, v17
v_and_b32 v163, 0xffff0000, v17
MM v[12:15],a[180:183],a[220:223],v[12:15]
v_lshlrev_b32 v164, 16, v18
v_and_b32 v165, 0xffff0000, v18
MM v[12:15],a[176:179],a[204:207],v[12:15]
v_lshlrev_b32 v166, 16, v19
v_and_b32 v167, 0xffff0000, v19
s_memrealtime s[54:55]
s_cmp_eq_u32 s58, 15
s_cbranch_scc1 LSK1_%=
s_memrealtime s[98:99]
s_andn2_b32 s91, s17, s58
s_mov_b32 s77, 0x7fffffff
s_waitcnt lgkmcnt(0)
s_sub_u32 s76, s98, s54
s_bitcmp0_b32 s91, 1
s_cbranch_scc1 LSM11_%=
s_sub_u32 s90, s13, s76
s_sub_u32 s90, s90, 12
s_cmp_gt_i32 s90, 0
s_cbranch_scc1 LSM11_%=
buffer_load_dwordx4 v[80:83], v248, s[20:23], s36 offen offset:0 sc1
buffer_load_dwordx4 v[84:87], v248, s[20:23], s36 offen offset:1024 sc1
s_bitset1_b32 s58, 1
LSM11_%=:
s_bitcmp0_b32 s91, 3
s_cbranch_scc1 LSM13_%=
s_sub_u32 s90, s15, s76
s_sub_u32 s90, s90, 63
s_cmp_gt_i32 s90, 0
s_cbranch_scc1 LSM13_%=
buffer_load_dwordx4 v[112:115], v248, s[20:23], s38 offen offset:0 sc1
buffer_load_dwordx4 v[116:119], v248, s[20:23], s38 offen offset:1024 sc1
s_bitset1_b32 s58, 3
LSM13_%=:
s_bitcmp0_b32 s91, 2
s_cbranch_scc1 LSM12_%=
s_sub_u32 s90, s14, s76
s_sub_u32 s90, s90, 97
s_cmp_gt_i32 s90, 0
s_cbranch_scc1 LSM12_%=
buffer_load_dwordx4 v[88:91], v248, s[20:23], s37 offen offset:0 sc1
buffer_load_dwordx4 v[92:95], v248, s[20:23], s37 offen offset:1024 sc1
s_bitset1_b32 s58, 2
LSM12_%=:
s_bitcmp0_b32 s91, 0
s_cbranch_scc1 LSM10_%=
s_sub_u32 s90, s12, s76
s_sub_u32 s90, s90, 131
s_cmp_gt_i32 s90, 0
s_cbranch_scc1 LSM10_%=
buffer_load_dwordx4 v[120:123], v250, s[20:23], s39 offen offset:0 sc1
buffer_load_dwordx4 v[124:127], v250, s[20:23], s39 offen offset:256 sc1
s_bitset1_b32 s58, 0
LSM10_%=:
LSK1_%=:
v_lshlrev_b32 v176, 16, v20
v_and_b32 v177, 0xffff0000, v20
v_lshlrev_b32 v178, 16, v21
v_and_b32 v179, 0xffff0000, v21
v_lshlrev_b32 v180, 16, v22
v_and_b32 v181, 0xffff0000, v22
v_lshlrev_b32 v182, 16, v23
v_and_b32 v183, 0xffff0000, v23
v_mul_f32 v136, 0xbfb8aa3b, v176
v_mul_f32 v137, 0xbfb8aa3b, v177
v_mul_f32 v138, 0xbfb8aa3b, v178
v_mul_f32 v139, 0xbfb8aa3b, v179
v_mul_f32 v140, 0xbfb8aa3b, v180
v_mul_f32 v141, 0xbfb8aa3b, v181
v_mul_f32 v142, 0xbfb8aa3b, v182
v_mul_f32 v143, 0xbfb8aa3b, v183
EX v136,v136
EX v137,v137
EX v138,v138
EX v139,v139
EX v140,v140
EX v141,v141
EX v142,v142
EX v143,v143
v_add_f32 v136, 1.0, v136
v_add_f32 v137, 1.0, v137
v_add_f32 v138, 1.0, v138
v_add_f32 v139, 1.0, v139
v_add_f32 v140, 1.0, v140
v_add_f32 v141, 1.0, v141
v_add_f32 v142, 1.0, v142
v_add_f32 v143, 1.0, v143
v_rcp_f32 v136, v136
v_rcp_f32 v137, v137
v_rcp_f32 v138, v138
v_rcp_f32 v139, v139
s_waitcnt vmcnt(12)
v_cmp_eq_u32 s[8:9], s69, v149
v_subrev_u32 v226, s56, v148
s_nop 0
v_cndmask_b32 v226, 4.0, v226, s[8:9]
s_nop 0
v_readlane_b32 s13, v226, 16
v_readlane_b32 s14, v226, 32
v_readlane_b32 s15, v226, 48
s_max_i32 s12, s13, s14
s_max_i32 s12, s12, s15
s_max_i32 s12, s12, 0
s_cmp_eq_u32 s58, 15
s_cbranch_scc1 LSK2_%=
s_memrealtime s[98:99]
s_andn2_b32 s91, s17, s58
s_mov_b32 s77, 0x7fffffff
s_waitcnt lgkmcnt(0)
s_sub_u32 s76, s98, s54
s_bitcmp0_b32 s91, 1
s_cbranch_scc1 LSM21_%=
s_sub_u32 s90, s13, s76
s_sub_u32 s90, s90, 12
s_cmp_gt_i32 s90, 0
s_cbranch_scc1 LSM21_%=
buffer_load_dwordx4 v[80:83], v248, s[20:23], s36 offen offset:0 sc1
buffer_load_dwordx4 v[84:87], v248, s[20:23], s36 offen offset:1024 sc1
s_bitset1_b32 s58, 1
LSM21_%=:
s_bitcmp0_b32 s91, 3
s_cbranch_scc1 LSM23_%=
s_sub_u32 s90, s15, s76
s_sub_u32 s90, s90, 63
s_cmp_gt_i32 s90, 0
s_cbranch_scc1 LSM23_%=
buffer_load_dwordx4 v[112:115], v248, s[20:23], s38 offen offset:0 sc1
buffer_load_dwordx4 v[116:119], v248, s[20:23], s38 offen offset:1024 sc1
s_bitset1_b32 s58, 3
LSM23_%=:
s_bitcmp0_b32 s91, 2
s_cbranch_scc1 LSM22_%=
s_sub_u32 s90, s14, s76
s_sub_u32 s90, s90, 97
s_cmp_gt_i32 s90, 0
s_cbranch_scc1 LSM22_%=
buffer_load_dwordx4 v[88:91], v248, s[20:23], s37 offen offset:0 sc1
buffer_load_dwordx4 v[92:95], v248, s[20:23], s37 offen offset:1024 sc1
s_bitset1_b32 s58, 2
LSM22_%=:
s_bitcmp0_b32 s91, 0
s_cbranch_scc1 LSM20_%=
s_sub_u32 s90, s12, s76
s_sub_u32 s90, s90, 131
s_cmp_gt_i32 s90, 0
s_cbranch_scc1 LSM20_%=
buffer_load_dwordx4 v[120:123], v250, s[20:23], s39 offen offset:0 sc1
buffer_load_dwordx4 v[124:127], v250, s[20:23], s39 offen offset:256 sc1
s_bitset1_b32 s58, 0
LSM20_%=:
LSK2_%=:
v_rcp_f32 v140, v140
v_rcp_f32 v141, v141
v_rcp_f32 v142, v142
v_rcp_f32 v143, v143
v_mul_f32 v136, v176, v136
v_mul_f32 v137, v177, v137
v_mul_f32 v138, v178, v138
v_mul_f32 v139, v179, v139
v_mul_f32 v140, v180, v140
v_mul_f32 v141, v181, v141
v_mul_f32 v142, v182, v142
v_mul_f32 v143, v183, v143
CV v20,v136,v137
CV v21,v138,v139
CV v22,v140,v141
CV v23,v142,v143
v_lshlrev_b32 v176, 16, v20
v_and_b32 v177, 0xffff0000, v20
v_lshlrev_b32 v178, 16, v21
v_and_b32 v179, 0xffff0000, v21
v_lshlrev_b32 v180, 16, v22
v_and_b32 v181, 0xffff0000, v22
v_lshlrev_b32 v182, 16, v23
v_and_b32 v183, 0xffff0000, v23
v_lshlrev_b32 v192, 16, v24
v_and_b32 v193, 0xffff0000, v24
v_lshlrev_b32 v194, 16, v25
v_and_b32 v195, 0xffff0000, v25
v_lshlrev_b32 v196, 16, v26
v_and_b32 v197, 0xffff0000, v26
v_lshlrev_b32 v198, 16, v27
v_and_b32 v199, 0xffff0000, v27
v_lshlrev_b32 v208, 16, v28
v_and_b32 v209, 0xffff0000, v28
v_lshlrev_b32 v210, 16, v29
v_and_b32 v211, 0xffff0000, v29
v_lshlrev_b32 v212, 16, v30
v_and_b32 v213, 0xffff0000, v30
v_lshlrev_b32 v214, 16, v31
v_and_b32 v215, 0xffff0000, v31
s_mov_b32 s73, 30000
s_cmp_lg_u32 s58, 15
s_cbranch_scc1 LSI_%=
s_waitcnt vmcnt(0)
v_max3_u32 v236, v123, v127, v83
v_max3_u32 v237, v87, v91, v95
v_max3_u32 v236, v236, v115, v119
v_max_u32 v236, v236, v237
v_cmp_eq_u32 vcc, -1, v236
s_cbranch_vccnz LSI_%=
LSF_%=:
s_waitcnt lgkmcnt(0)
MM v[152:155],v[48:51],v[120:123],0
MM v[152:155],v[52:55],v[124:127],v[152:155]
MM v[0:3],v[58:61],v[88:91],v[0:3]
v_lshlrev_b32 v128, 16, v150
v_and_b32 v129, 0xffff0000, v150
MM v[0:3],v[58:61],v[112:115],v[0:3]
v_lshlrev_b32 v130, 16, v151
v_and_b32 v131, 0xffff0000, v151
MM v[0:3],v[58:61],v[80:83],v[0:3]
v_lshlrev_b32 v132, 16, v62
v_and_b32 v133, 0xffff0000, v62
MM v[4:7],v[56:59],v[88:91],v[4:7]
v_lshlrev_b32 v134, 16, v63
v_and_b32 v135, 0xffff0000, v63
MM v[4:7],v[56:59],v[112:115],v[4:7]
CV v236,v152,v153
CV v237,v154,v155
MM v[4:7],v[56:59],v[80:83],v[4:7]
ds_bpermute_b32 v236, v232, v236
ds_bpermute_b32 v237, v232, v237
MM v[8:11],v[58:61],v[92:95],v[8:11]
MM v[8:11],v[58:61],v[116:119],v[8:11]
MM v[8:11],v[58:61],v[84:87],v[8:11]
s_add_u32 s80, s18, 20480
MM v[12:15],v[56:59],v[92:95],v[12:15]
MM v[12:15],v[56:59],v[116:119],v[12:15]
CV v32,v0,v1
CV v33,v2,v3
MM v[12:15],v[56:59],v[84:87],v[12:15]
CV v34,v4,v5
CV v35,v6,v7
ds_write2_b64 v224, v[32:33], v[34:35] offset0:0 offset1:4
RD v[40:43],v225 offset:0
CV v36,v8,v9
CV v37,v10,v11
s_nop 1
CV v38,v12,v13
CV v39,v14,v15
ds_write2_b64 v224, v[36:37], v[38:39] offset0:160 offset1:164
RD v[44:47],v225 offset:1280
s_waitcnt lgkmcnt(4)
v_lshlrev_b32 v152, 16, v236
v_and_b32 v153, 0xffff0000, v236
v_lshlrev_b32 v154, 16, v237
v_and_b32 v155, 0xffff0000, v237
v_pk_mul_f32 v[152:153], v[152:153], v[128:129]
v_pk_mul_f32 v[154:155], v[154:155], v[130:131]
CV v236,v152,v153
CV v237,v154,v155
v_lshlrev_b32 v152, 16, v236
v_and_b32 v153, 0xffff0000, v236
v_lshlrev_b32 v154, 16, v237
v_and_b32 v155, 0xffff0000, v237
v_pk_add_f32 v[152:153], v[152:153], v[132:133]
v_pk_add_f32 v[154:155], v[154:155], v[134:135]
CV v238,v152,v153
CV v239,v154,v155
buffer_store_dwordx2 v[238:239], v254, s[40:43], s63 offen offset:0 sc1
s_nop 0
s_waitcnt lgkmcnt(2)
v_lshlrev_b32 v0, 16, v40
v_and_b32 v1, 0xffff0000, v40
v_lshlrev_b32 v2, 16, v41
v_and_b32 v3, 0xffff0000, v41
v_lshlrev_b32 v4, 16, v42
v_and_b32 v5, 0xffff0000, v42
v_lshlrev_b32 v6, 16, v43
v_and_b32 v7, 0xffff0000, v43
v_pk_mul_f32 v[0:1], v[0:1], v[160:161]
v_pk_mul_f32 v[2:3], v[2:3], v[162:163]
v_pk_mul_f32 v[4:5], v[4:5], v[164:165]
v_pk_mul_f32 v[6:7], v[6:7], v[166:167]
CV v40,v0,v1
CV v41,v2,v3
CV v42,v4,v5
CV v43,v6,v7
v_lshlrev_b32 v0, 16, v40
v_and_b32 v1, 0xffff0000, v40
v_lshlrev_b32 v2, 16, v41
v_and_b32 v3, 0xffff0000, v41
v_lshlrev_b32 v4, 16, v42
v_and_b32 v5, 0xffff0000, v42
v_lshlrev_b32 v6, 16, v43
v_and_b32 v7, 0xffff0000, v43
v_pk_add_f32 v[0:1], v[0:1], v[192:193]
v_pk_add_f32 v[2:3], v[2:3], v[194:195]
v_pk_add_f32 v[4:5], v[4:5], v[196:197]
v_pk_add_f32 v[6:7], v[6:7], v[198:199]
)ASM"
R"ASM(
s_nop 0
LPG30_%=:
s_cbranch_execz LTR1_%=
CV v40,v0,v1
CV v41,v2,v3
CV v42,v4,v5
CV v43,v6,v7
buffer_store_dwordx4 v[40:43], v251, s[40:43], s18 offen offset:0 sc1
s_waitcnt lgkmcnt(0)
v_lshlrev_b32 v8, 16, v44
v_and_b32 v9, 0xffff0000, v44
v_lshlrev_b32 v10, 16, v45
v_and_b32 v11, 0xffff0000, v45
v_lshlrev_b32 v12, 16, v46
v_and_b32 v13, 0xffff0000, v46
v_lshlrev_b32 v14, 16, v47
v_and_b32 v15, 0xffff0000, v47
v_pk_mul_f32 v[8:9], v[8:9], v[176:177]
v_pk_mul_f32 v[10:11], v[10:11], v[178:179]
v_pk_mul_f32 v[12:13], v[12:13], v[180:181]
v_pk_mul_f32 v[14:15], v[14:15], v[182:183]
CV v44,v8,v9
CV v45,v10,v11
CV v46,v12,v13
CV v47,v14,v15
v_lshlrev_b32 v8, 16, v44
v_and_b32 v9, 0xffff0000, v44
v_lshlrev_b32 v10, 16, v45
v_and_b32 v11, 0xffff0000, v45
v_lshlrev_b32 v12, 16, v46
v_and_b32 v13, 0xffff0000, v46
v_lshlrev_b32 v14, 16, v47
v_and_b32 v15, 0xffff0000, v47
v_pk_add_f32 v[8:9], v[8:9], v[208:209]
v_pk_add_f32 v[10:11], v[10:11], v[210:211]
v_pk_add_f32 v[12:13], v[12:13], v[212:213]
v_pk_add_f32 v[14:15], v[14:15], v[214:215]
CV v44,v8,v9
CV v45,v10,v11
CV v46,v12,v13
CV v47,v14,v15
buffer_store_dwordx4 v[44:47], v251, s[40:43], s80 offen offset:0 sc1
s_waitcnt vmcnt(0)
s_endpgm
LSI_%=:
s_memrealtime s[98:99]
s_andn2_b32 s91, s17, s58
s_mov_b32 s77, 0x7fffffff
s_waitcnt lgkmcnt(0)
s_sub_u32 s76, s98, s54
s_bitcmp0_b32 s91, 1
s_cbranch_scc1 LSM1_%=
s_sub_u32 s90, s13, s76
s_sub_u32 s90, s90, 12
s_cmp_gt_i32 s90, 0
s_cbranch_scc1 LSN1_%=
buffer_load_dwordx4 v[80:83], v248, s[20:23], s36 offen offset:0 sc1
buffer_load_dwordx4 v[84:87], v248, s[20:23], s36 offen offset:1024 sc1
s_bitset1_b32 s58, 1
s_branch LSM1_%=
LSN1_%=:
s_min_i32 s77, s77, s90
LSM1_%=:
s_bitcmp0_b32 s91, 3
s_cbranch_scc1 LSM3_%=
s_sub_u32 s90, s15, s76
s_sub_u32 s90, s90, 63
s_cmp_gt_i32 s90, 0
s_cbranch_scc1 LSN3_%=
buffer_load_dwordx4 v[112:115], v248, s[20:23], s38 offen offset:0 sc1
buffer_load_dwordx4 v[116:119], v248, s[20:23], s38 offen offset:1024 sc1
s_bitset1_b32 s58, 3
s_branch LSM3_%=
LSN3_%=:
s_min_i32 s77, s77, s90
LSM3_%=:
s_bitcmp0_b32 s91, 2
s_cbranch_scc1 LSM2_%=
s_sub_u32 s90, s14, s76
s_sub_u32 s90, s90, 97
s_cmp_gt_i32 s90, 0
s_cbranch_scc1 LSN2_%=
buffer_load_dwordx4 v[88:91], v248, s[20:23], s37 offen offset:0 sc1
buffer_load_dwordx4 v[92:95], v248, s[20:23], s37 offen offset:1024 sc1
s_bitset1_b32 s58, 2
s_branch LSM2_%=
LSN2_%=:
s_min_i32 s77, s77, s90
LSM2_%=:
s_bitcmp0_b32 s91, 0
s_cbranch_scc1 LSM0_%=
s_sub_u32 s90, s12, s76
s_sub_u32 s90, s90, 131
s_cmp_gt_i32 s90, 0
s_cbranch_scc1 LSN0_%=
buffer_load_dwordx4 v[120:123], v250, s[20:23], s39 offen offset:0 sc1
buffer_load_dwordx4 v[124:127], v250, s[20:23], s39 offen offset:256 sc1
s_bitset1_b32 s58, 0
s_branch LSM0_%=
LSN0_%=:
s_min_i32 s77, s77, s90
LSM0_%=:
s_cmp_eq_u32 s12, 4.0
s_cselect_b32 s90, 30, 45
s_cmp_lt_i32 s77, s90
s_cbranch_scc1 LSI_%=
buffer_load_dwordx2 v[148:149], v253, s[20:23], s68 offen offset:8 sc1
s_waitcnt vmcnt(1)
s_bitcmp0_b32 s58, 1
s_cbranch_scc1 LSB1_%=
v_max_u32 v237, v83, v87
v_cmp_eq_u32 vcc, -1, v237
s_cbranch_vccnz LSB1_%=
s_bitset0_b32 s17, 1
LSB1_%=:
s_bitcmp0_b32 s58, 3
s_cbranch_scc1 LSB3_%=
v_max_u32 v239, v115, v119
v_cmp_eq_u32 vcc, -1, v239
s_cbranch_vccnz LSB3_%=
s_bitset0_b32 s17, 3
LSB3_%=:
s_bitcmp0_b32 s58, 2
s_cbranch_scc1 LSB2_%=
v_max_u32 v238, v91, v95
v_cmp_eq_u32 vcc, -1, v238
s_cbranch_vccnz LSB2_%=
s_bitset0_b32 s17, 2
LSB2_%=:
s_bitcmp0_b32 s58, 0
s_cbranch_scc1 LSB0_%=
v_max_u32 v236, v123, v127
v_cmp_eq_u32 vcc, -1, v236
s_cbranch_vccnz LSB0_%=
s_bitset0_b32 s17, 0
LSB0_%=:
s_cmp_eq_u32 s17, 0
s_cbranch_scc1 LSF_%=
s_mov_b32 s58, 0
s_waitcnt vmcnt(0)
v_cmp_eq_u32 s[8:9], s69, v149
v_subrev_u32 v226, s56, v148
s_nop 0
v_cndmask_b32 v226, 4.0, v226, s[8:9]
s_nop 0
v_readlane_b32 s13, v226, 16
v_readlane_b32 s14, v226, 32
v_readlane_b32 s15, v226, 48
s_max_i32 s12, s13, s14
s_max_i32 s12, s12, s15
s_max_i32 s12, s12, 0
s_sub_u32 s73, s73, 1
s_cbranch_scc0 LSI_%=
LSE_%=:
s_waitcnt vmcnt(0)
s_endpgm
LRG1o_%=:
s_mov_b32 s70, 30000
LRG1ol_%=:
s_sleep 2
buffer_load_dwordx4 v[200:203], v208, s[20:23], s62 offen offset:0 sc1
buffer_load_dwordx4 v[204:207], v208, s[20:23], s93 offen offset:0 sc1
s_waitcnt vmcnt(0)
v_xor_b32 v201, s69, v201
v_xor_b32 v203, s69, v203
v_xor_b32 v205, s69, v205
v_xor_b32 v207, s69, v207
v_or3_b32 v209, v201, v203, v205
v_or_b32 v209, v209, v207
v_cmp_ne_u32 vcc, 0, v209
s_cbranch_vccz LRG1ob_%=
s_sub_u32 s70, s70, 1
s_cbranch_scc0 LRG1ol_%=
s_branch LRG1ob_%=
LRFR_%=:
s_mov_b32 s91, 30000
LRFRl_%=:
s_sleep 2
buffer_load_dwordx4 v[56:59], v248, s[20:23], s90 offen offset:0 sc1
s_waitcnt vmcnt(0)
v_xor_b32 v56, s69, v56
v_xor_b32 v57, s69, v57
v_xor_b32 v58, s69, v58
v_xor_b32 v59, s69, v59
v_or3_b32 v252, v56, v57, v58
v_or_b32 v252, v252, v59
v_cmp_ne_u32 vcc, 0, v252
s_cbranch_vccz LRFRb_%=
s_sub_u32 s91, s91, 1
s_cbranch_scc0 LRFRl_%=
s_branch LRFRb_%=
LRRK_%=:
s_mov_b32 s91, 30000
LRRKl_%=:
s_sleep 2
buffer_load_dwordx2 v[32:33], v34, s[20:23], s90 offen offset:0 sc1
s_waitcnt vmcnt(0)
v_cmp_ne_u32 vcc, s69, v33
s_cbranch_vccz LRRKb_%=
s_sub_u32 s91, s91, 1
s_cbranch_scc0 LRRKl_%=
s_branch LRRKb_%=
LRK_%=:
s_cmp_ge_u32 s60, 8
s_cbranch_scc1 LRK0_%=
v_mov_b32 v253, 0
v_cmp_lt_u32 vcc, s52, v96
v_cmp_lt_u32 s[98:99], s52, v97
s_nop 0
v_addc_co_u32 v253, vcc, 0, v253, vcc
v_addc_co_u32 v253, s[98:99], 0, v253, s[98:99]
v_cmp_lt_u32 vcc, s52, v98
v_cmp_lt_u32 s[98:99], s52, v99
s_nop 0
v_addc_co_u32 v253, vcc, 0, v253, vcc
v_addc_co_u32 v253, s[98:99], 0, v253, s[98:99]
v_cmp_lt_u32 vcc, s52, v100
v_cmp_lt_u32 s[98:99], s52, v101
s_nop 0
v_addc_co_u32 v253, vcc, 0, v253, vcc
v_addc_co_u32 v253, s[98:99], 0, v253, s[98:99]
v_cmp_lt_u32 vcc, s52, v102
v_cmp_lt_u32 s[98:99], s52, v103
s_nop 0
v_addc_co_u32 v253, vcc, 0, v253, vcc
v_addc_co_u32 v253, s[98:99], 0, v253, s[98:99]
v_cmp_lt_u32 vcc, s52, v104
v_cmp_lt_u32 s[98:99], s52, v105
s_nop 0
v_addc_co_u32 v253, vcc, 0, v253, vcc
v_addc_co_u32 v253, s[98:99], 0, v253, s[98:99]
v_cmp_lt_u32 vcc, s52, v106
v_cmp_lt_u32 s[98:99], s52, v107
s_nop 0
v_addc_co_u32 v253, vcc, 0, v253, vcc
v_addc_co_u32 v253, s[98:99], 0, v253, s[98:99]
v_cmp_lt_u32 vcc, s52, v108
v_cmp_lt_u32 s[98:99], s52, v109
s_nop 0
v_addc_co_u32 v253, vcc, 0, v253, vcc
v_addc_co_u32 v253, s[98:99], 0, v253, s[98:99]
v_cmp_lt_u32 vcc, s52, v110
v_cmp_lt_u32 s[98:99], s52, v111
s_nop 0
v_addc_co_u32 v253, vcc, 0, v253, vcc
v_addc_co_u32 v253, s[98:99], 0, v253, s[98:99]
v_cmp_lt_u32 vcc, s52, v112
v_cmp_lt_u32 s[98:99], s52, v113
s_nop 0
v_addc_co_u32 v253, vcc, 0, v253, vcc
v_addc_co_u32 v253, s[98:99], 0, v253, s[98:99]
v_cmp_lt_u32 vcc, s52, v114
v_cmp_lt_u32 s[98:99], s52, v115
s_nop 0
v_addc_co_u32 v253, vcc, 0, v253, vcc
v_addc_co_u32 v253, s[98:99], 0, v253, s[98:99]
v_cmp_lt_u32 vcc, s52, v116
v_cmp_lt_u32 s[98:99], s52, v117
s_nop 0
v_addc_co_u32 v253, vcc, 0, v253, vcc
v_addc_co_u32 v253, s[98:99], 0, v253, s[98:99]
v_cmp_lt_u32 vcc, s52, v118
v_cmp_lt_u32 s[98:99], s52, v119
s_nop 0
v_addc_co_u32 v253, vcc, 0, v253, vcc
v_addc_co_u32 v253, s[98:99], 0, v253, s[98:99]
v_cmp_lt_u32 vcc, s52, v120
v_cmp_lt_u32 s[98:99], s52, v121
s_nop 0
v_addc_co_u32 v253, vcc, 0, v253, vcc
v_addc_co_u32 v253, s[98:99], 0, v253, s[98:99]
v_cmp_lt_u32 vcc, s52, v122
v_cmp_lt_u32 s[98:99], s52, v123
s_nop 0
v_addc_co_u32 v253, vcc, 0, v253, vcc
v_addc_co_u32 v253, s[98:99], 0, v253, s[98:99]
v_cmp_lt_u32 vcc, s52, v124
v_cmp_lt_u32 s[98:99], s52, v125
s_nop 0
v_addc_co_u32 v253, vcc, 0, v253, vcc
v_addc_co_u32 v253, s[98:99], 0, v253, s[98:99]
v_cmp_lt_u32 vcc, s52, v126
v_cmp_lt_u32 s[98:99], s52, v127
s_nop 0
v_addc_co_u32 v253, vcc, 0, v253, vcc
v_addc_co_u32 v253, s[98:99], 0, v253, s[98:99]
s_nop 1
v_add_u32_dpp v253, v253, v253 row_ror:8 row_mask:0xf bank_mask:0xf
s_nop 1
v_add_u32_dpp v253, v253, v253 row_ror:4 row_mask:0xf bank_mask:0xf
s_nop 1
v_add_u32_dpp v253, v253, v253 row_ror:2 row_mask:0xf bank_mask:0xf
s_nop 1
v_add_u32_dpp v253, v253, v253 row_ror:1 row_mask:0xf bank_mask:0xf
v_mov_b32 v250, v253
s_nop 1
v_permlane16_swap_b32 v253, v250
v_add_u32 v253, v253, v250
v_mov_b32 v250, v253
s_nop 1
v_permlane32_swap_b32 v253, v250
v_add_u32 v253, v253, v250
v_cmp_gt_u32 vcc, 8, v253
v_mov_b32 v250, s52
v_mov_b32 v251, s69
v_lshlrev_b32 v253, 3, v253
s_mov_b32 s76, 94208
s_cbranch_vccz LRK0_%=
s_mov_b64 exec, 1
buffer_store_dwordx2 v[250:251], v253, s[20:23], s76 offen offset:0 sc1
s_mov_b64 exec, -1
s_nop 1
LRK0_%=:
s_cmp_ge_u32 s62, 8
s_cbranch_scc1 LRK1_%=
v_mov_b32 v253, 0
v_cmp_lt_u32 vcc, s53, v96
v_cmp_lt_u32 s[98:99], s53, v97
s_nop 0
v_addc_co_u32 v253, vcc, 0, v253, vcc
v_addc_co_u32 v253, s[98:99], 0, v253, s[98:99]
v_cmp_lt_u32 vcc, s53, v98
v_cmp_lt_u32 s[98:99], s53, v99
s_nop 0
v_addc_co_u32 v253, vcc, 0, v253, vcc
v_addc_co_u32 v253, s[98:99], 0, v253, s[98:99]
v_cmp_lt_u32 vcc, s53, v100
v_cmp_lt_u32 s[98:99], s53, v101
s_nop 0
v_addc_co_u32 v253, vcc, 0, v253, vcc
v_addc_co_u32 v253, s[98:99], 0, v253, s[98:99]
v_cmp_lt_u32 vcc, s53, v102
v_cmp_lt_u32 s[98:99], s53, v103
s_nop 0
v_addc_co_u32 v253, vcc, 0, v253, vcc
v_addc_co_u32 v253, s[98:99], 0, v253, s[98:99]
v_cmp_lt_u32 vcc, s53, v104
v_cmp_lt_u32 s[98:99], s53, v105
s_nop 0
v_addc_co_u32 v253, vcc, 0, v253, vcc
v_addc_co_u32 v253, s[98:99], 0, v253, s[98:99]
v_cmp_lt_u32 vcc, s53, v106
v_cmp_lt_u32 s[98:99], s53, v107
s_nop 0
v_addc_co_u32 v253, vcc, 0, v253, vcc
v_addc_co_u32 v253, s[98:99], 0, v253, s[98:99]
v_cmp_lt_u32 vcc, s53, v108
v_cmp_lt_u32 s[98:99], s53, v109
s_nop 0
v_addc_co_u32 v253, vcc, 0, v253, vcc
v_addc_co_u32 v253, s[98:99], 0, v253, s[98:99]
v_cmp_lt_u32 vcc, s53, v110
v_cmp_lt_u32 s[98:99], s53, v111
s_nop 0
v_addc_co_u32 v253, vcc, 0, v253, vcc
v_addc_co_u32 v253, s[98:99], 0, v253, s[98:99]
v_cmp_lt_u32 vcc, s53, v112
v_cmp_lt_u32 s[98:99], s53, v113
s_nop 0
v_addc_co_u32 v253, vcc, 0, v253, vcc
v_addc_co_u32 v253, s[98:99], 0, v253, s[98:99]
v_cmp_lt_u32 vcc, s53, v114
v_cmp_lt_u32 s[98:99], s53, v115
s_nop 0
v_addc_co_u32 v253, vcc, 0, v253, vcc
v_addc_co_u32 v253, s[98:99], 0, v253, s[98:99]
v_cmp_lt_u32 vcc, s53, v116
v_cmp_lt_u32 s[98:99], s53, v117
s_nop 0
v_addc_co_u32 v253, vcc, 0, v253, vcc
v_addc_co_u32 v253, s[98:99], 0, v253, s[98:99]
v_cmp_lt_u32 vcc, s53, v118
v_cmp_lt_u32 s[98:99], s53, v119
s_nop 0
v_addc_co_u32 v253, vcc, 0, v253, vcc
v_addc_co_u32 v253, s[98:99], 0, v253, s[98:99]
v_cmp_lt_u32 vcc, s53, v120
v_cmp_lt_u32 s[98:99], s53, v121
s_nop 0
v_addc_co_u32 v253, vcc, 0, v253, vcc
v_addc_co_u32 v253, s[98:99], 0, v253, s[98:99]
v_cmp_lt_u32 vcc, s53, v122
v_cmp_lt_u32 s[98:99], s53, v123
s_nop 0
v_addc_co_u32 v253, vcc, 0, v253, vcc
v_addc_co_u32 v253, s[98:99], 0, v253, s[98:99]
v_cmp_lt_u32 vcc, s53, v124
v_cmp_lt_u32 s[98:99], s53, v125
s_nop 0
v_addc_co_u32 v253, vcc, 0, v253, vcc
v_addc_co_u32 v253, s[98:99], 0, v253, s[98:99]
v_cmp_lt_u32 vcc, s53, v126
v_cmp_lt_u32 s[98:99], s53, v127
s_nop 0
v_addc_co_u32 v253, vcc, 0, v253, vcc
v_addc_co_u32 v253, s[98:99], 0, v253, s[98:99]
s_nop 1
v_add_u32_dpp v253, v253, v253 row_ror:8 row_mask:0xf bank_mask:0xf
s_nop 1
v_add_u32_dpp v253, v253, v253 row_ror:4 row_mask:0xf bank_mask:0xf
s_nop 1
v_add_u32_dpp v253, v253, v253 row_ror:2 row_mask:0xf bank_mask:0xf
s_nop 1
v_add_u32_dpp v253, v253, v253 row_ror:1 row_mask:0xf bank_mask:0xf
v_mov_b32 v250, v253
s_nop 1
v_permlane16_swap_b32 v253, v250
v_add_u32 v253, v253, v250
v_mov_b32 v250, v253
s_nop 1
v_permlane32_swap_b32 v253, v250
v_add_u32 v253, v253, v250
v_cmp_gt_u32 vcc, 8, v253
v_mov_b32 v250, s53
v_mov_b32 v251, s69
v_lshlrev_b32 v253, 3, v253
s_mov_b32 s76, 94208
s_cbranch_vccz LRK1_%=
s_mov_b64 exec, 1
buffer_store_dwordx2 v[250:251], v253, s[20:23], s76 offen offset:0 sc1
s_mov_b64 exec, -1
s_nop 1
LRK1_%=:
s_cmp_ge_u32 s56, 8
s_cbranch_scc1 LRK2_%=
v_mov_b32 v253, 0
v_cmp_lt_u32 vcc, s54, v96
v_cmp_lt_u32 s[98:99], s54, v97
s_nop 0
v_addc_co_u32 v253, vcc, 0, v253, vcc
v_addc_co_u32 v253, s[98:99], 0, v253, s[98:99]
v_cmp_lt_u32 vcc, s54, v98
v_cmp_lt_u32 s[98:99], s54, v99
s_nop 0
v_addc_co_u32 v253, vcc, 0, v253, vcc
)ASM"
R"ASM(
v_addc_co_u32 v253, s[98:99], 0, v253, s[98:99]
v_cmp_lt_u32 vcc, s54, v100
v_cmp_lt_u32 s[98:99], s54, v101
s_nop 0
v_addc_co_u32 v253, vcc, 0, v253, vcc
v_addc_co_u32 v253, s[98:99], 0, v253, s[98:99]
v_cmp_lt_u32 vcc, s54, v102
v_cmp_lt_u32 s[98:99], s54, v103
s_nop 0
v_addc_co_u32 v253, vcc, 0, v253, vcc
v_addc_co_u32 v253, s[98:99], 0, v253, s[98:99]
v_cmp_lt_u32 vcc, s54, v104
v_cmp_lt_u32 s[98:99], s54, v105
s_nop 0
v_addc_co_u32 v253, vcc, 0, v253, vcc
v_addc_co_u32 v253, s[98:99], 0, v253, s[98:99]
v_cmp_lt_u32 vcc, s54, v106
v_cmp_lt_u32 s[98:99], s54, v107
s_nop 0
v_addc_co_u32 v253, vcc, 0, v253, vcc
v_addc_co_u32 v253, s[98:99], 0, v253, s[98:99]
v_cmp_lt_u32 vcc, s54, v108
v_cmp_lt_u32 s[98:99], s54, v109
s_nop 0
v_addc_co_u32 v253, vcc, 0, v253, vcc
v_addc_co_u32 v253, s[98:99], 0, v253, s[98:99]
v_cmp_lt_u32 vcc, s54, v110
v_cmp_lt_u32 s[98:99], s54, v111
s_nop 0
v_addc_co_u32 v253, vcc, 0, v253, vcc
v_addc_co_u32 v253, s[98:99], 0, v253, s[98:99]
v_cmp_lt_u32 vcc, s54, v112
v_cmp_lt_u32 s[98:99], s54, v113
s_nop 0
v_addc_co_u32 v253, vcc, 0, v253, vcc
v_addc_co_u32 v253, s[98:99], 0, v253, s[98:99]
v_cmp_lt_u32 vcc, s54, v114
v_cmp_lt_u32 s[98:99], s54, v115
s_nop 0
v_addc_co_u32 v253, vcc, 0, v253, vcc
v_addc_co_u32 v253, s[98:99], 0, v253, s[98:99]
v_cmp_lt_u32 vcc, s54, v116
v_cmp_lt_u32 s[98:99], s54, v117
s_nop 0
v_addc_co_u32 v253, vcc, 0, v253, vcc
v_addc_co_u32 v253, s[98:99], 0, v253, s[98:99]
v_cmp_lt_u32 vcc, s54, v118
v_cmp_lt_u32 s[98:99], s54, v119
s_nop 0
v_addc_co_u32 v253, vcc, 0, v253, vcc
v_addc_co_u32 v253, s[98:99], 0, v253, s[98:99]
v_cmp_lt_u32 vcc, s54, v120
v_cmp_lt_u32 s[98:99], s54, v121
s_nop 0
v_addc_co_u32 v253, vcc, 0, v253, vcc
v_addc_co_u32 v253, s[98:99], 0, v253, s[98:99]
v_cmp_lt_u32 vcc, s54, v122
v_cmp_lt_u32 s[98:99], s54, v123
s_nop 0
v_addc_co_u32 v253, vcc, 0, v253, vcc
v_addc_co_u32 v253, s[98:99], 0, v253, s[98:99]
v_cmp_lt_u32 vcc, s54, v124
v_cmp_lt_u32 s[98:99], s54, v125
s_nop 0
v_addc_co_u32 v253, vcc, 0, v253, vcc
v_addc_co_u32 v253, s[98:99], 0, v253, s[98:99]
v_cmp_lt_u32 vcc, s54, v126
v_cmp_lt_u32 s[98:99], s54, v127
s_nop 0
v_addc_co_u32 v253, vcc, 0, v253, vcc
v_addc_co_u32 v253, s[98:99], 0, v253, s[98:99]
s_nop 1
v_add_u32_dpp v253, v253, v253 row_ror:8 row_mask:0xf bank_mask:0xf
s_nop 1
v_add_u32_dpp v253, v253, v253 row_ror:4 row_mask:0xf bank_mask:0xf
s_nop 1
v_add_u32_dpp v253, v253, v253 row_ror:2 row_mask:0xf bank_mask:0xf
s_nop 1
v_add_u32_dpp v253, v253, v253 row_ror:1 row_mask:0xf bank_mask:0xf
v_mov_b32 v250, v253
s_nop 1
v_permlane16_swap_b32 v253, v250
v_add_u32 v253, v253, v250
v_mov_b32 v250, v253
s_nop 1
v_permlane32_swap_b32 v253, v250
v_add_u32 v253, v253, v250
v_cmp_gt_u32 vcc, 8, v253
v_mov_b32 v250, s54
v_mov_b32 v251, s69
v_lshlrev_b32 v253, 3, v253
s_mov_b32 s76, 94208
s_cbranch_vccz LRK2_%=
s_mov_b64 exec, 1
buffer_store_dwordx2 v[250:251], v253, s[20:23], s76 offen offset:0 sc1
s_mov_b64 exec, -1
s_nop 1
LRK2_%=:
s_cmp_ge_u32 s58, 8
s_cbranch_scc1 LRK3_%=
v_mov_b32 v253, 0
v_cmp_lt_u32 vcc, s55, v96
v_cmp_lt_u32 s[98:99], s55, v97
s_nop 0
v_addc_co_u32 v253, vcc, 0, v253, vcc
v_addc_co_u32 v253, s[98:99], 0, v253, s[98:99]
v_cmp_lt_u32 vcc, s55, v98
v_cmp_lt_u32 s[98:99], s55, v99
s_nop 0
v_addc_co_u32 v253, vcc, 0, v253, vcc
v_addc_co_u32 v253, s[98:99], 0, v253, s[98:99]
v_cmp_lt_u32 vcc, s55, v100
v_cmp_lt_u32 s[98:99], s55, v101
s_nop 0
v_addc_co_u32 v253, vcc, 0, v253, vcc
v_addc_co_u32 v253, s[98:99], 0, v253, s[98:99]
v_cmp_lt_u32 vcc, s55, v102
v_cmp_lt_u32 s[98:99], s55, v103
s_nop 0
v_addc_co_u32 v253, vcc, 0, v253, vcc
v_addc_co_u32 v253, s[98:99], 0, v253, s[98:99]
v_cmp_lt_u32 vcc, s55, v104
v_cmp_lt_u32 s[98:99], s55, v105
s_nop 0
v_addc_co_u32 v253, vcc, 0, v253, vcc
v_addc_co_u32 v253, s[98:99], 0, v253, s[98:99]
v_cmp_lt_u32 vcc, s55, v106
v_cmp_lt_u32 s[98:99], s55, v107
s_nop 0
v_addc_co_u32 v253, vcc, 0, v253, vcc
v_addc_co_u32 v253, s[98:99], 0, v253, s[98:99]
v_cmp_lt_u32 vcc, s55, v108
v_cmp_lt_u32 s[98:99], s55, v109
s_nop 0
v_addc_co_u32 v253, vcc, 0, v253, vcc
v_addc_co_u32 v253, s[98:99], 0, v253, s[98:99]
v_cmp_lt_u32 vcc, s55, v110
v_cmp_lt_u32 s[98:99], s55, v111
s_nop 0
v_addc_co_u32 v253, vcc, 0, v253, vcc
v_addc_co_u32 v253, s[98:99], 0, v253, s[98:99]
v_cmp_lt_u32 vcc, s55, v112
v_cmp_lt_u32 s[98:99], s55, v113
s_nop 0
v_addc_co_u32 v253, vcc, 0, v253, vcc
v_addc_co_u32 v253, s[98:99], 0, v253, s[98:99]
v_cmp_lt_u32 vcc, s55, v114
v_cmp_lt_u32 s[98:99], s55, v115
s_nop 0
v_addc_co_u32 v253, vcc, 0, v253, vcc
v_addc_co_u32 v253, s[98:99], 0, v253, s[98:99]
v_cmp_lt_u32 vcc, s55, v116
v_cmp_lt_u32 s[98:99], s55, v117
s_nop 0
v_addc_co_u32 v253, vcc, 0, v253, vcc
v_addc_co_u32 v253, s[98:99], 0, v253, s[98:99]
v_cmp_lt_u32 vcc, s55, v118
v_cmp_lt_u32 s[98:99], s55, v119
s_nop 0
v_addc_co_u32 v253, vcc, 0, v253, vcc
v_addc_co_u32 v253, s[98:99], 0, v253, s[98:99]
v_cmp_lt_u32 vcc, s55, v120
v_cmp_lt_u32 s[98:99], s55, v121
s_nop 0
v_addc_co_u32 v253, vcc, 0, v253, vcc
v_addc_co_u32 v253, s[98:99], 0, v253, s[98:99]
v_cmp_lt_u32 vcc, s55, v122
v_cmp_lt_u32 s[98:99], s55, v123
s_nop 0
v_addc_co_u32 v253, vcc, 0, v253, vcc
v_addc_co_u32 v253, s[98:99], 0, v253, s[98:99]
v_cmp_lt_u32 vcc, s55, v124
v_cmp_lt_u32 s[98:99], s55, v125
s_nop 0
v_addc_co_u32 v253, vcc, 0, v253, vcc
v_addc_co_u32 v253, s[98:99], 0, v253, s[98:99]
v_cmp_lt_u32 vcc, s55, v126
v_cmp_lt_u32 s[98:99], s55, v127
s_nop 0
v_addc_co_u32 v253, vcc, 0, v253, vcc
v_addc_co_u32 v253, s[98:99], 0, v253, s[98:99]
s_nop 1
v_add_u32_dpp v253, v253, v253 row_ror:8 row_mask:0xf bank_mask:0xf
s_nop 1
v_add_u32_dpp v253, v253, v253 row_ror:4 row_mask:0xf bank_mask:0xf
s_nop 1
v_add_u32_dpp v253, v253, v253 row_ror:2 row_mask:0xf bank_mask:0xf
s_nop 1
v_add_u32_dpp v253, v253, v253 row_ror:1 row_mask:0xf bank_mask:0xf
v_mov_b32 v250, v253
s_nop 1
v_permlane16_swap_b32 v253, v250
v_add_u32 v253, v253, v250
v_mov_b32 v250, v253
s_nop 1
v_permlane32_swap_b32 v253, v250
v_add_u32 v253, v253, v250
v_cmp_gt_u32 vcc, 8, v253
v_mov_b32 v250, s55
v_mov_b32 v251, s69
v_lshlrev_b32 v253, 3, v253
s_mov_b32 s76, 94208
s_cbranch_vccz LRK3_%=
s_mov_b64 exec, 1
buffer_store_dwordx2 v[250:251], v253, s[20:23], s76 offen offset:0 sc1
s_mov_b64 exec, -1
s_nop 1
LRK3_%=:
s_branch LRKb_%=
LRIX_%=:
s_mov_b32 s91, 30000
LRIXl_%=:
s_sleep 2
v_bfe_u32 v251, v248, 4, 3
v_lshlrev_b32 v251, 3, v251
buffer_load_dwordx2 v[252:253], v251, s[20:23], s86 offen offset:0 sc1
s_waitcnt vmcnt(0)
v_cmp_ne_u32 vcc, s69, v253
s_cbranch_vccz LRIXb_%=
s_sub_u32 s91, s91, 1
s_cbranch_scc0 LRIXl_%=
s_branch LRIXb_%=
LRFG_%=:
s_mov_b32 s91, 30000
LRFGl_%=:
s_sleep 2
buffer_load_dword v252, v253, s[20:23], s76 offen offset:0 sc1
s_waitcnt vmcnt(0)
v_cmp_ne_u32 vcc, s69, v252
s_cbranch_vccz LRFGb_%=
s_sub_u32 s91, s91, 1
s_cbranch_scc0 LRFGl_%=
s_branch LRFGb_%=
LRFH_%=:
s_mov_b32 s91, 30000
LRFHl_%=:
s_sleep 2
buffer_load_dword v252, v253, s[20:23], s76 offen offset:0 sc1
s_waitcnt vmcnt(0)
v_cmp_ne_u32 vcc, s69, v252
s_cbranch_vccz LRFHb_%=
s_sub_u32 s91, s91, 1
s_cbranch_scc0 LRFHl_%=
s_branch LRFHb_%=
LRFY_%=:
s_mov_b32 s91, 30000
LRFYl_%=:
s_sleep 2
buffer_load_dword v252, v253, s[20:23], s76 offen offset:0 sc1
s_waitcnt vmcnt(0)
v_cmp_ne_u32 vcc, s69, v252
s_cbranch_vccz LRFYb_%=
s_sub_u32 s91, s91, 1
s_cbranch_scc0 LRFYl_%=
s_branch LRFYb_%=
LRPO0_%=:
s_mov_b32 s97, 30000
buffer_load_dword v242, v243, s[20:23], s44 offen offset:0 sc1
s_sleep 4
LRPO0l_%=:
buffer_load_dword v245, v243, s[20:23], s44 offen offset:0 sc1
s_waitcnt vmcnt(1)
v_cmp_ne_u32 vcc, s69, v242
s_cbranch_vccz LRPO0b_%=
buffer_load_dword v242, v243, s[20:23], s44 offen offset:0 sc1
s_waitcnt vmcnt(1)
v_cmp_ne_u32 vcc, s69, v245
s_cbranch_vccz LRPO0b_%=
s_sub_u32 s97, s97, 1
s_cbranch_scc0 LRPO0l_%=
s_branch LRPO0b_%=
LRPW_%=:
s_mov_b32 s61, 30000
LRPWl_%=:
s_sleep 2
buffer_load_dwordx2 v[96:97], v98, s[20:23], s62 offen offset:0 sc1
s_waitcnt vmcnt(0)
v_xor_b32 v96, s69, v96
v_xor_b32 v97, s69, v97
v_or_b32 v99, v96, v97
v_cmp_ne_u32 vcc, 0, v99
s_cbranch_vccz LRPWb_%=
s_sub_u32 s61, s61, 1
s_cbranch_scc0 LRPWl_%=
s_branch LRPWb_%=
LRPO1_%=:
s_mov_b32 s97, 30000
LRPO1l_%=:
s_sleep 2
buffer_load_dword v242, v243, s[20:23], s96 offen offset:16 sc1
s_waitcnt vmcnt(0)
v_cmp_ne_u32 vcc, s69, v242
s_cbranch_vccz LRPO1b_%=
s_sub_u32 s97, s97, 1
s_cbranch_scc0 LRPO1l_%=
s_branch LRPO1b_%=
LKEND_ka:
LRET_%=:
s_cmp_eq_u32 s73, 0
s_cbranch_scc1 LBACK0_%=
s_cmp_eq_u32 s73, 2
s_cbranch_scc1 LBACK2_%=
s_cmp_eq_u32 s73, 1
s_cbranch_scc1 LBACK1_%=
s_cmp_eq_u32 s73, 3
s_cbranch_scc1 LBACK3_%=
s_cmp_eq_u32 s73, 5
s_cbranch_scc1 LBACK5_%=
s_cmp_eq_u32 s73, 7
s_cbranch_scc1 LBACK7_%=
s_cmp_eq_u32 s73, 9
s_cbranch_scc1 LBACK9_%=
s_cmp_eq_u32 s73, 11
s_cbranch_scc1 LBACK11_%=
s_cmp_eq_u32 s73, 13
s_cbranch_scc1 LBACK13_%=
s_cmp_eq_u32 s73, 15
s_cbranch_scc1 LBACK15_%=
s_cmp_eq_u32 s73, 17
s_cbranch_scc1 LBACK17_%=
s_cmp_eq_u32 s73, 19
s_cbranch_scc1 LBACK19_%=
s_cmp_eq_u32 s73, 21
s_cbranch_scc1 LBACK21_%=
s_cmp_eq_u32 s73, 23
s_cbranch_scc1 LBACK23_%=
s_cmp_eq_u32 s73, 25
s_cbranch_scc1 LBACK25_%=
s_cmp_eq_u32 s73, 27
s_cbranch_scc1 LBACK27_%=
s_cmp_eq_u32 s73, 29
s_cbranch_scc1 LBACK29_%=
s_cmp_eq_u32 s73, 31
s_cbranch_scc1 LBACK31_%=
s_cmp_eq_u32 s73, 33
s_cbranch_scc1 LBACK33_%=
s_cmp_eq_u32 s73, 35
s_cbranch_scc1 LBACK35_%=
s_cmp_eq_u32 s73, 4
s_cbranch_scc1 LBACK4_%=
s_cmp_eq_u32 s73, 6
s_cbranch_scc1 LBACK6_%=
s_cmp_eq_u32 s73, 8
s_cbranch_scc1 LBACK8_%=
s_cmp_eq_u32 s73, 10
s_cbranch_scc1 LBACK10_%=
s_cmp_eq_u32 s73, 12
s_cbranch_scc1 LBACK12_%=
s_cmp_eq_u32 s73, 14
s_cbranch_scc1 LBACK14_%=
s_cmp_eq_u32 s73, 16
s_cbranch_scc1 LBACK16_%=
s_cmp_eq_u32 s73, 18
s_cbranch_scc1 LBACK18_%=
s_cmp_eq_u32 s73, 20
s_cbranch_scc1 LBACK20_%=
s_cmp_eq_u32 s73, 22
s_cbranch_scc1 LBACK22_%=
s_cmp_eq_u32 s73, 24
s_cbranch_scc1 LBACK24_%=
s_cmp_eq_u32 s73, 26
s_cbranch_scc1 LBACK26_%=
s_cmp_eq_u32 s73, 28
s_cbranch_scc1 LBACK28_%=
s_cmp_eq_u32 s73, 30
s_cbranch_scc1 LBACK30_%=
s_cmp_eq_u32 s73, 32
s_cbranch_scc1 LBACK32_%=
s_cmp_eq_u32 s73, 34
s_cbranch_scc1 LBACK34_%=
s_cmp_eq_u32 s73, 36
s_cbranch_scc1 LBACK36_%=
s_cmp_eq_u32 s73, 38
s_cbranch_scc1 LBACK38_%=
s_cmp_eq_u32 s73, 37
s_cbranch_scc1 LBACK37_%=
s_endpgm
LSITE0_%=:
s_mov_b32 s73, 0
s_mov_b32 s74, 0
s_mov_b32 s75, 16384
s_branch LRARE002_%=
LSITE2_%=:
s_mov_b32 s73, 2
s_mov_b32 s74, 32768
s_mov_b32 s75, 49152
s_branch LRARE002_%=
LSITE1_%=:
s_mov_b32 s73, 1
s_mov_b32 s74, 16384
s_mov_b32 s75, 32768
s_branch LRARE102_%=
LSITE3_%=:
s_mov_b32 s73, 3
s_mov_b32 s74, 49152
s_mov_b32 s75, 0
s_branch LRARE104_%=
LSITE5_%=:
s_mov_b32 s73, 5
s_mov_b32 s74, 16384
s_mov_b32 s75, 32768
s_branch LRARE104_%=
LSITE7_%=:
s_mov_b32 s73, 7
s_mov_b32 s74, 49152
s_mov_b32 s75, 0
s_branch LRARE104_%=
LSITE9_%=:
s_mov_b32 s73, 9
s_mov_b32 s74, 16384
s_mov_b32 s75, 32768
s_branch LRARE104_%=
LSITE11_%=:
s_mov_b32 s73, 11
s_mov_b32 s74, 49152
s_mov_b32 s75, 0
s_branch LRARE104_%=
LSITE13_%=:
s_mov_b32 s73, 13
s_mov_b32 s74, 16384
s_mov_b32 s75, 32768
s_branch LRARE104_%=
LSITE15_%=:
s_mov_b32 s73, 15
s_mov_b32 s74, 49152
s_mov_b32 s75, 0
s_branch LRARE104_%=
LSITE17_%=:
s_mov_b32 s73, 17
s_mov_b32 s74, 16384
s_mov_b32 s75, 32768
s_branch LRARE104_%=
LSITE19_%=:
s_mov_b32 s73, 19
s_mov_b32 s74, 49152
s_mov_b32 s75, 0
s_branch LRARE104_%=
LSITE21_%=:
s_mov_b32 s73, 21
s_mov_b32 s74, 16384
s_mov_b32 s75, 32768
s_branch LRARE104_%=
LSITE23_%=:
s_mov_b32 s73, 23
s_mov_b32 s74, 49152
s_mov_b32 s75, 0
s_branch LRARE104_%=
LSITE25_%=:
s_mov_b32 s73, 25
s_mov_b32 s74, 16384
s_mov_b32 s75, 32768
s_branch LRARE104_%=
LSITE27_%=:
s_mov_b32 s73, 27
s_mov_b32 s74, 49152
s_mov_b32 s75, 0
s_branch LRARE104_%=
LSITE29_%=:
s_mov_b32 s73, 29
s_mov_b32 s74, 16384
s_mov_b32 s75, 32768
s_branch LRARE104_%=
LSITE31_%=:
s_mov_b32 s73, 31
s_mov_b32 s74, 49152
s_mov_b32 s75, 0
s_branch LRARE104_%=
LSITE33_%=:
s_mov_b32 s73, 33
s_mov_b32 s74, 16384
s_mov_b32 s75, 32768
s_branch LRARE104_%=
LSITE35_%=:
s_mov_b32 s73, 35
s_mov_b32 s74, 49152
s_mov_b32 s75, 0
s_branch LRARE104_%=
LSITE4_%=:
)ASM"
R"ASM(
s_mov_b32 s73, 4
s_mov_b32 s74, 0
s_mov_b32 s75, 16384
s_branch LRARE004_%=
LSITE6_%=:
s_mov_b32 s73, 6
s_mov_b32 s74, 32768
s_mov_b32 s75, 49152
s_branch LRARE004_%=
LSITE8_%=:
s_mov_b32 s73, 8
s_mov_b32 s74, 0
s_mov_b32 s75, 16384
s_branch LRARE004_%=
LSITE10_%=:
s_mov_b32 s73, 10
s_mov_b32 s74, 32768
s_mov_b32 s75, 49152
s_branch LRARE004_%=
LSITE12_%=:
s_mov_b32 s73, 12
s_mov_b32 s74, 0
s_mov_b32 s75, 16384
s_branch LRARE004_%=
LSITE14_%=:
s_mov_b32 s73, 14
s_mov_b32 s74, 32768
s_mov_b32 s75, 49152
s_branch LRARE004_%=
LSITE16_%=:
s_mov_b32 s73, 16
s_mov_b32 s74, 0
s_mov_b32 s75, 16384
s_branch LRARE004_%=
LSITE18_%=:
s_mov_b32 s73, 18
s_mov_b32 s74, 32768
s_mov_b32 s75, 49152
s_branch LRARE004_%=
LSITE20_%=:
s_mov_b32 s73, 20
s_mov_b32 s74, 0
s_mov_b32 s75, 16384
s_branch LRARE004_%=
LSITE22_%=:
s_mov_b32 s73, 22
s_mov_b32 s74, 32768
s_mov_b32 s75, 49152
s_branch LRARE004_%=
LSITE24_%=:
s_mov_b32 s73, 24
s_mov_b32 s74, 0
s_mov_b32 s75, 16384
s_branch LRARE004_%=
LSITE26_%=:
s_mov_b32 s73, 26
s_mov_b32 s74, 32768
s_mov_b32 s75, 49152
s_branch LRARE004_%=
LSITE28_%=:
s_mov_b32 s73, 28
s_mov_b32 s74, 0
s_mov_b32 s75, 16384
s_branch LRARE004_%=
LSITE30_%=:
s_mov_b32 s73, 30
s_mov_b32 s74, 32768
s_mov_b32 s75, 49152
s_branch LRARE004_%=
LSITE32_%=:
s_mov_b32 s73, 32
s_mov_b32 s74, 0
s_mov_b32 s75, 16384
s_branch LRARE004_%=
LSITE34_%=:
s_mov_b32 s73, 34
s_mov_b32 s74, 32768
s_mov_b32 s75, 49152
s_branch LRARE004_%=
LSITE36_%=:
s_mov_b32 s73, 36
s_mov_b32 s74, 0
s_mov_b32 s75, 16384
s_branch LRARE022_%=
LSITE38_%=:
s_mov_b32 s73, 38
s_mov_b32 s74, 32768
s_mov_b32 s75, 49152
s_branch LRARE022_%=
LSITE37_%=:
s_mov_b32 s73, 37
s_mov_b32 s74, 16384
s_mov_b32 s75, 32768
s_branch LRARE122_%=
LRARE002_%=:
s_nop 15
s_waitcnt vmcnt(0) lgkmcnt(0)
v_add_u32 v249, s74, v224
RD a[192:195],v249 offset:0
RD a[208:211],v249 offset:4096
RD a[224:227],v249 offset:8192
RD a[240:243],v249 offset:12288
v_add_u32 v250, s74, v225
RD a[196:199],v250 offset:0
RD a[212:215],v250 offset:4096
RD a[228:231],v250 offset:8192
RD a[244:247],v250 offset:12288
v_add_u32 v251, s74, v226
RD a[200:203],v251 offset:0
RD a[216:219],v251 offset:4096
RD a[232:235],v251 offset:8192
RD a[248:251],v251 offset:12288
v_add_u32 v254, s74, v227
RD a[204:207],v254 offset:0
RD a[220:223],v254 offset:4096
RD a[236:239],v254 offset:8192
RD a[252:255],v254 offset:12288
s_waitcnt lgkmcnt(0)
MM v[0:3],a[192:195],v[160:163],v[128:131]
MM v[0:3],a[196:199],v[176:179],v[0:3]
MM v[0:3],a[200:203],v[192:195],v[0:3]
MM v[0:3],a[204:207],v[208:211],v[0:3]
MM v[4:7],a[220:223],v[208:211],v[128:131]
MM v[4:7],a[216:219],v[192:195],v[4:7]
MM v[4:7],a[212:215],v[176:179],v[4:7]
MM v[4:7],a[208:211],v[160:163],v[4:7]
MM v[12:15],a[208:211],v[164:167],v[132:135]
MM v[12:15],a[212:215],v[180:183],v[12:15]
MM v[12:15],a[216:219],v[196:199],v[12:15]
MM v[12:15],a[220:223],v[212:215],v[12:15]
MM v[8:11],a[204:207],v[212:215],v[132:135]
MM v[8:11],a[200:203],v[196:199],v[8:11]
MM v[8:11],a[196:199],v[180:183],v[8:11]
MM v[8:11],a[192:195],v[164:167],v[8:11]
MM v[32:35],a[224:227],v[160:163],v[128:131]
MM v[32:35],a[228:231],v[176:179],v[32:35]
MM v[32:35],a[232:235],v[192:195],v[32:35]
MM v[32:35],a[236:239],v[208:211],v[32:35]
MM v[36:39],a[252:255],v[208:211],v[128:131]
MM v[36:39],a[248:251],v[192:195],v[36:39]
MM v[36:39],a[244:247],v[176:179],v[36:39]
MM v[36:39],a[240:243],v[160:163],v[36:39]
MM v[44:47],a[240:243],v[164:167],v[132:135]
MM v[44:47],a[244:247],v[180:183],v[44:47]
MM v[44:47],a[248:251],v[196:199],v[44:47]
MM v[44:47],a[252:255],v[212:215],v[44:47]
MM v[40:43],a[236:239],v[212:215],v[132:135]
MM v[40:43],a[232:235],v[196:199],v[40:43]
MM v[40:43],a[228:231],v[180:183],v[40:43]
MM v[40:43],a[224:227],v[164:167],v[40:43]
s_nop 15
v_add_u32 v249, s75, v224
RD a[192:195],v249 offset:0
RD a[208:211],v249 offset:4096
RD a[224:227],v249 offset:8192
RD a[240:243],v249 offset:12288
v_add_u32 v250, s75, v225
RD a[196:199],v250 offset:0
RD a[212:215],v250 offset:4096
RD a[228:231],v250 offset:8192
RD a[244:247],v250 offset:12288
v_add_u32 v251, s75, v226
RD a[200:203],v251 offset:0
RD a[216:219],v251 offset:4096
RD a[232:235],v251 offset:8192
RD a[248:251],v251 offset:12288
v_add_u32 v254, s75, v227
RD a[204:207],v254 offset:0
RD a[220:223],v254 offset:4096
RD a[236:239],v254 offset:8192
RD a[252:255],v254 offset:12288
s_waitcnt lgkmcnt(0)
v_max3_f32 v249, v0, v1, v2
v_max3_f32 v251, v32, v33, v34
v_max3_f32 v249, v249, v3, v4
v_max3_f32 v251, v251, v35, v36
v_max3_f32 v249, v249, v5, v6
v_max3_f32 v251, v251, v37, v38
v_max3_f32 v249, v249, v7, v39
v_max_f32 v249, v249, v251
v_mov_b32 v251, v249
s_nop 1
v_permlane16_swap_b32 v249, v251
v_max_f32 v249, v249, v251
v_mov_b32 v251, v249
s_nop 1
v_permlane32_swap_b32 v249, v251
v_max_f32 v249, v249, v251
v_log_f32 v250, v244
s_nop 0
v_max3_f32 v249, 0, v249, v250
v_sub_f32 v250, 0, v249
EX v250,v250
v_sub_f32 v128, v128, v249
v_sub_f32 v129, v129, v249
v_sub_f32 v130, v130, v249
v_sub_f32 v131, v131, v249
v_sub_f32 v64, v64, v249
v_sub_f32 v0, v0, v249
v_sub_f32 v65, v65, v249
v_sub_f32 v1, v1, v249
v_sub_f32 v66, v66, v249
v_sub_f32 v2, v2, v249
v_sub_f32 v67, v67, v249
v_sub_f32 v3, v3, v249
v_sub_f32 v68, v68, v249
v_sub_f32 v4, v4, v249
v_sub_f32 v69, v69, v249
v_sub_f32 v5, v5, v249
v_sub_f32 v70, v70, v249
v_sub_f32 v6, v6, v249
v_sub_f32 v71, v71, v249
v_sub_f32 v7, v7, v249
v_sub_f32 v96, v96, v249
v_sub_f32 v32, v32, v249
v_sub_f32 v97, v97, v249
v_sub_f32 v33, v33, v249
v_sub_f32 v98, v98, v249
v_sub_f32 v34, v34, v249
v_sub_f32 v99, v99, v249
v_sub_f32 v35, v35, v249
v_sub_f32 v100, v100, v249
v_sub_f32 v36, v36, v249
v_sub_f32 v101, v101, v249
v_sub_f32 v37, v37, v249
v_sub_f32 v102, v102, v249
v_sub_f32 v38, v38, v249
v_sub_f32 v103, v103, v249
v_sub_f32 v39, v39, v249
v_mul_f32 v144, v244, v250
v_mov_b32 v145, 0
v_bfe_u32 v249, v241, 0, 8
v_lshl_or_b32 v249, v249, 1, 1
v_lshlrev_b32 v249, 22, v249
v_and_b32 v241, 0xffffff00, v241
AR v254,a0
v_sub_f32 v254, v254, v249
v_mul_f32 v254, v254, v250
AW a0,v254
AR v251,a1
v_sub_f32 v251, v251, v249
v_mul_f32 v251, v251, v250
AW a1,v251
AR v254,a2
v_sub_f32 v254, v254, v249
v_mul_f32 v254, v254, v250
AW a2,v254
AR v251,a3
v_sub_f32 v251, v251, v249
v_mul_f32 v251, v251, v250
AW a3,v251
AR v254,a4
v_sub_f32 v254, v254, v249
v_mul_f32 v254, v254, v250
AW a4,v254
AR v251,a5
v_sub_f32 v251, v251, v249
v_mul_f32 v251, v251, v250
AW a5,v251
AR v254,a6
v_sub_f32 v254, v254, v249
v_mul_f32 v254, v254, v250
AW a6,v254
AR v251,a7
v_sub_f32 v251, v251, v249
v_mul_f32 v251, v251, v250
AW a7,v251
AR v254,a32
v_sub_f32 v254, v254, v249
v_mul_f32 v254, v254, v250
AW a32,v254
AR v251,a33
v_sub_f32 v251, v251, v249
v_mul_f32 v251, v251, v250
AW a33,v251
AR v254,a34
v_sub_f32 v254, v254, v249
v_mul_f32 v254, v254, v250
AW a34,v254
AR v251,a35
v_sub_f32 v251, v251, v249
v_mul_f32 v251, v251, v250
AW a35,v251
AR v254,a36
v_sub_f32 v254, v254, v249
v_mul_f32 v254, v254, v250
AW a36,v254
AR v251,a37
v_sub_f32 v251, v251, v249
v_mul_f32 v251, v251, v250
AW a37,v251
AR v254,a38
v_sub_f32 v254, v254, v249
v_mul_f32 v254, v254, v250
AW a38,v254
AR v251,a39
v_sub_f32 v251, v251, v249
v_mul_f32 v251, v251, v250
AW a39,v251
AR v254,a64
v_sub_f32 v254, v254, v249
v_mul_f32 v254, v254, v250
AW a64,v254
AR v251,a65
v_sub_f32 v251, v251, v249
v_mul_f32 v251, v251, v250
AW a65,v251
AR v254,a66
v_sub_f32 v254, v254, v249
v_mul_f32 v254, v254, v250
AW a66,v254
AR v251,a67
v_sub_f32 v251, v251, v249
v_mul_f32 v251, v251, v250
AW a67,v251
AR v254,a68
v_sub_f32 v254, v254, v249
v_mul_f32 v254, v254, v250
AW a68,v254
AR v251,a69
v_sub_f32 v251, v251, v249
v_mul_f32 v251, v251, v250
AW a69,v251
AR v254,a70
v_sub_f32 v254, v254, v249
v_mul_f32 v254, v254, v250
AW a70,v254
AR v251,a71
v_sub_f32 v251, v251, v249
v_mul_f32 v251, v251, v250
AW a71,v251
AR v254,a96
v_sub_f32 v254, v254, v249
v_mul_f32 v254, v254, v250
AW a96,v254
AR v251,a97
v_sub_f32 v251, v251, v249
v_mul_f32 v251, v251, v250
AW a97,v251
AR v254,a98
v_sub_f32 v254, v254, v249
v_mul_f32 v254, v254, v250
AW a98,v254
AR v251,a99
v_sub_f32 v251, v251, v249
v_mul_f32 v251, v251, v250
AW a99,v251
AR v254,a100
v_sub_f32 v254, v254, v249
v_mul_f32 v254, v254, v250
AW a100,v254
AR v251,a101
v_sub_f32 v251, v251, v249
v_mul_f32 v251, v251, v250
AW a101,v251
AR v254,a102
v_sub_f32 v254, v254, v249
v_mul_f32 v254, v254, v250
AW a102,v254
AR v251,a103
v_sub_f32 v251, v251, v249
v_mul_f32 v251, v251, v250
AW a103,v251
v_max3_f32 v249, v8, v9, v10
v_max3_f32 v251, v40, v41, v42
v_max3_f32 v249, v249, v11, v12
v_max3_f32 v251, v251, v43, v44
v_max3_f32 v249, v249, v13, v14
v_max3_f32 v251, v251, v45, v46
v_max3_f32 v249, v249, v15, v47
v_max_f32 v249, v249, v251
v_mov_b32 v251, v249
s_nop 1
v_permlane16_swap_b32 v249, v251
v_max_f32 v249, v249, v251
v_mov_b32 v251, v249
s_nop 1
v_permlane32_swap_b32 v249, v251
v_max_f32 v249, v249, v251
v_log_f32 v250, v245
s_nop 0
v_max3_f32 v249, 0, v249, v250
v_sub_f32 v250, 0, v249
EX v250,v250
v_sub_f32 v132, v132, v249
v_sub_f32 v133, v133, v249
v_sub_f32 v134, v134, v249
v_sub_f32 v135, v135, v249
v_sub_f32 v72, v72, v249
v_sub_f32 v8, v8, v249
v_sub_f32 v73, v73, v249
v_sub_f32 v9, v9, v249
v_sub_f32 v74, v74, v249
v_sub_f32 v10, v10, v249
v_sub_f32 v75, v75, v249
v_sub_f32 v11, v11, v249
v_sub_f32 v76, v76, v249
v_sub_f32 v12, v12, v249
v_sub_f32 v77, v77, v249
v_sub_f32 v13, v13, v249
v_sub_f32 v78, v78, v249
v_sub_f32 v14, v14, v249
v_sub_f32 v79, v79, v249
v_sub_f32 v15, v15, v249
v_sub_f32 v104, v104, v249
v_sub_f32 v40, v40, v249
v_sub_f32 v105, v105, v249
v_sub_f32 v41, v41, v249
v_sub_f32 v106, v106, v249
v_sub_f32 v42, v42, v249
v_sub_f32 v107, v107, v249
v_sub_f32 v43, v43, v249
v_sub_f32 v108, v108, v249
v_sub_f32 v44, v44, v249
v_sub_f32 v109, v109, v249
v_sub_f32 v45, v45, v249
v_sub_f32 v110, v110, v249
v_sub_f32 v46, v46, v249
v_sub_f32 v111, v111, v249
v_sub_f32 v47, v47, v249
v_mul_f32 v148, v245, v250
v_mov_b32 v149, 0
v_bfe_u32 v249, v241, 8, 8
v_lshl_or_b32 v249, v249, 1, 1
v_lshlrev_b32 v249, 22, v249
v_and_b32 v241, 0xffff00ff, v241
AR v254,a8
v_sub_f32 v254, v254, v249
v_mul_f32 v254, v254, v250
AW a8,v254
AR v251,a9
v_sub_f32 v251, v251, v249
v_mul_f32 v251, v251, v250
AW a9,v251
AR v254,a10
v_sub_f32 v254, v254, v249
v_mul_f32 v254, v254, v250
AW a10,v254
AR v251,a11
v_sub_f32 v251, v251, v249
v_mul_f32 v251, v251, v250
AW a11,v251
AR v254,a12
v_sub_f32 v254, v254, v249
v_mul_f32 v254, v254, v250
AW a12,v254
AR v251,a13
v_sub_f32 v251, v251, v249
v_mul_f32 v251, v251, v250
AW a13,v251
AR v254,a14
v_sub_f32 v254, v254, v249
v_mul_f32 v254, v254, v250
AW a14,v254
AR v251,a15
v_sub_f32 v251, v251, v249
v_mul_f32 v251, v251, v250
AW a15,v251
AR v254,a40
v_sub_f32 v254, v254, v249
v_mul_f32 v254, v254, v250
AW a40,v254
AR v251,a41
v_sub_f32 v251, v251, v249
v_mul_f32 v251, v251, v250
AW a41,v251
AR v254,a42
v_sub_f32 v254, v254, v249
v_mul_f32 v254, v254, v250
AW a42,v254
AR v251,a43
v_sub_f32 v251, v251, v249
v_mul_f32 v251, v251, v250
AW a43,v251
AR v254,a44
v_sub_f32 v254, v254, v249
v_mul_f32 v254, v254, v250
AW a44,v254
AR v251,a45
v_sub_f32 v251, v251, v249
v_mul_f32 v251, v251, v250
AW a45,v251
AR v254,a46
v_sub_f32 v254, v254, v249
v_mul_f32 v254, v254, v250
AW a46,v254
AR v251,a47
v_sub_f32 v251, v251, v249
v_mul_f32 v251, v251, v250
AW a47,v251
AR v254,a72
v_sub_f32 v254, v254, v249
v_mul_f32 v254, v254, v250
AW a72,v254
AR v251,a73
v_sub_f32 v251, v251, v249
v_mul_f32 v251, v251, v250
AW a73,v251
AR v254,a74
v_sub_f32 v254, v254, v249
v_mul_f32 v254, v254, v250
AW a74,v254
AR v251,a75
v_sub_f32 v251, v251, v249
v_mul_f32 v251, v251, v250
AW a75,v251
AR v254,a76
v_sub_f32 v254, v254, v249
v_mul_f32 v254, v254, v250
AW a76,v254
AR v251,a77
v_sub_f32 v251, v251, v249
v_mul_f32 v251, v251, v250
AW a77,v251
AR v254,a78
v_sub_f32 v254, v254, v249
v_mul_f32 v254, v254, v250
AW a78,v254
AR v251,a79
v_sub_f32 v251, v251, v249
v_mul_f32 v251, v251, v250
AW a79,v251
AR v254,a104
v_sub_f32 v254, v254, v249
v_mul_f32 v254, v254, v250
AW a104,v254
AR v251,a105
v_sub_f32 v251, v251, v249
)ASM"
R"ASM(
v_mul_f32 v251, v251, v250
AW a105,v251
AR v254,a106
v_sub_f32 v254, v254, v249
v_mul_f32 v254, v254, v250
AW a106,v254
AR v251,a107
v_sub_f32 v251, v251, v249
v_mul_f32 v251, v251, v250
AW a107,v251
AR v254,a108
v_sub_f32 v254, v254, v249
v_mul_f32 v254, v254, v250
AW a108,v254
AR v251,a109
v_sub_f32 v251, v251, v249
v_mul_f32 v251, v251, v250
AW a109,v251
AR v254,a110
v_sub_f32 v254, v254, v249
v_mul_f32 v254, v254, v250
AW a110,v254
AR v251,a111
v_sub_f32 v251, v251, v249
v_mul_f32 v251, v251, v250
AW a111,v251
EX v0,v0
EX v1,v1
EX v2,v2
EX v3,v3
EX v4,v4
EX v5,v5
EX v6,v6
EX v7,v7
EX v8,v8
EX v9,v9
EX v10,v10
EX v11,v11
EX v12,v12
EX v13,v13
EX v14,v14
EX v15,v15
EX v32,v32
EX v33,v33
EX v34,v34
EX v35,v35
EX v36,v36
EX v37,v37
EX v38,v38
EX v39,v39
EX v40,v40
EX v41,v41
EX v42,v42
EX v43,v43
EX v44,v44
EX v45,v45
EX v46,v46
EX v47,v47
CV v0,v0,v1
CV v1,v2,v3
CV v2,v4,v5
CV v3,v6,v7
CV v8,v8,v9
CV v9,v10,v11
CV v10,v12,v13
CV v11,v14,v15
CV v4,v32,v33
CV v5,v34,v35
CV v6,v36,v37
CV v7,v38,v39
CV v12,v40,v41
CV v13,v42,v43
CV v14,v44,v45
CV v15,v46,v47
v_smfmac_f32_16x16x64_bf16 v[144:147], v[236:239], v[0:7], v234
s_nop 0
v_smfmac_f32_16x16x64_bf16 v[148:151], v[236:239], v[8:15], v234
s_nop 9
s_branch LRET_%=
LRARE102_%=:
s_nop 15
s_waitcnt vmcnt(0) lgkmcnt(0)
v_add_u32 v249, s74, v224
RD a[192:195],v249 offset:0
RD a[208:211],v249 offset:4096
RD a[224:227],v249 offset:8192
RD a[240:243],v249 offset:12288
v_add_u32 v250, s74, v225
RD a[196:199],v250 offset:0
RD a[212:215],v250 offset:4096
RD a[228:231],v250 offset:8192
RD a[244:247],v250 offset:12288
v_add_u32 v251, s74, v226
RD a[200:203],v251 offset:0
RD a[216:219],v251 offset:4096
RD a[232:235],v251 offset:8192
RD a[248:251],v251 offset:12288
v_add_u32 v254, s74, v227
RD a[204:207],v254 offset:0
RD a[220:223],v254 offset:4096
RD a[236:239],v254 offset:8192
RD a[252:255],v254 offset:12288
s_waitcnt lgkmcnt(0)
MM v[64:67],a[192:195],v[160:163],v[128:131]
MM v[64:67],a[196:199],v[176:179],v[64:67]
MM v[64:67],a[200:203],v[192:195],v[64:67]
MM v[64:67],a[204:207],v[208:211],v[64:67]
MM v[68:71],a[220:223],v[208:211],v[128:131]
MM v[68:71],a[216:219],v[192:195],v[68:71]
MM v[68:71],a[212:215],v[176:179],v[68:71]
MM v[68:71],a[208:211],v[160:163],v[68:71]
MM v[76:79],a[208:211],v[164:167],v[132:135]
MM v[76:79],a[212:215],v[180:183],v[76:79]
MM v[76:79],a[216:219],v[196:199],v[76:79]
MM v[76:79],a[220:223],v[212:215],v[76:79]
MM v[72:75],a[204:207],v[212:215],v[132:135]
MM v[72:75],a[200:203],v[196:199],v[72:75]
MM v[72:75],a[196:199],v[180:183],v[72:75]
MM v[72:75],a[192:195],v[164:167],v[72:75]
MM v[96:99],a[224:227],v[160:163],v[128:131]
MM v[96:99],a[228:231],v[176:179],v[96:99]
MM v[96:99],a[232:235],v[192:195],v[96:99]
MM v[96:99],a[236:239],v[208:211],v[96:99]
MM v[100:103],a[252:255],v[208:211],v[128:131]
MM v[100:103],a[248:251],v[192:195],v[100:103]
MM v[100:103],a[244:247],v[176:179],v[100:103]
MM v[100:103],a[240:243],v[160:163],v[100:103]
MM v[108:111],a[240:243],v[164:167],v[132:135]
MM v[108:111],a[244:247],v[180:183],v[108:111]
MM v[108:111],a[248:251],v[196:199],v[108:111]
MM v[108:111],a[252:255],v[212:215],v[108:111]
MM v[104:107],a[236:239],v[212:215],v[132:135]
MM v[104:107],a[232:235],v[196:199],v[104:107]
MM v[104:107],a[228:231],v[180:183],v[104:107]
MM v[104:107],a[224:227],v[164:167],v[104:107]
s_nop 15
v_add_u32 v249, s75, v224
RD a[192:195],v249 offset:0
RD a[208:211],v249 offset:4096
RD a[224:227],v249 offset:8192
RD a[240:243],v249 offset:12288
v_add_u32 v250, s75, v225
RD a[196:199],v250 offset:0
RD a[212:215],v250 offset:4096
RD a[228:231],v250 offset:8192
RD a[244:247],v250 offset:12288
v_add_u32 v251, s75, v226
RD a[200:203],v251 offset:0
RD a[216:219],v251 offset:4096
RD a[232:235],v251 offset:8192
RD a[248:251],v251 offset:12288
v_add_u32 v254, s75, v227
RD a[204:207],v254 offset:0
RD a[220:223],v254 offset:4096
RD a[236:239],v254 offset:8192
RD a[252:255],v254 offset:12288
s_waitcnt lgkmcnt(0)
v_max3_f32 v249, v64, v65, v66
v_max3_f32 v251, v96, v97, v98
v_max3_f32 v249, v249, v67, v68
v_max3_f32 v251, v251, v99, v100
v_max3_f32 v249, v249, v69, v70
v_max3_f32 v251, v251, v101, v102
v_max3_f32 v249, v249, v71, v103
v_max_f32 v249, v249, v251
v_mov_b32 v251, v249
s_nop 1
v_permlane16_swap_b32 v249, v251
v_max_f32 v249, v249, v251
v_mov_b32 v251, v249
s_nop 1
v_permlane32_swap_b32 v249, v251
v_max_f32 v249, v249, v251
v_log_f32 v250, v244
s_nop 0
v_max3_f32 v249, 0, v249, v250
v_sub_f32 v250, 0, v249
EX v250,v250
v_sub_f32 v128, v128, v249
v_sub_f32 v129, v129, v249
v_sub_f32 v130, v130, v249
v_sub_f32 v131, v131, v249
v_sub_f32 v0, v0, v249
v_sub_f32 v64, v64, v249
v_sub_f32 v1, v1, v249
v_sub_f32 v65, v65, v249
v_sub_f32 v2, v2, v249
v_sub_f32 v66, v66, v249
v_sub_f32 v3, v3, v249
v_sub_f32 v67, v67, v249
v_sub_f32 v4, v4, v249
v_sub_f32 v68, v68, v249
v_sub_f32 v5, v5, v249
v_sub_f32 v69, v69, v249
v_sub_f32 v6, v6, v249
v_sub_f32 v70, v70, v249
v_sub_f32 v7, v7, v249
v_sub_f32 v71, v71, v249
v_sub_f32 v32, v32, v249
v_sub_f32 v96, v96, v249
v_sub_f32 v33, v33, v249
v_sub_f32 v97, v97, v249
v_sub_f32 v34, v34, v249
v_sub_f32 v98, v98, v249
v_sub_f32 v35, v35, v249
v_sub_f32 v99, v99, v249
v_sub_f32 v36, v36, v249
v_sub_f32 v100, v100, v249
v_sub_f32 v37, v37, v249
v_sub_f32 v101, v101, v249
v_sub_f32 v38, v38, v249
v_sub_f32 v102, v102, v249
v_sub_f32 v39, v39, v249
v_sub_f32 v103, v103, v249
v_mul_f32 v144, v244, v250
v_mov_b32 v145, 0
v_bfe_u32 v249, v241, 0, 8
v_lshl_or_b32 v249, v249, 1, 1
v_lshlrev_b32 v249, 22, v249
v_and_b32 v241, 0xffffff00, v241
AR v254,a0
v_sub_f32 v254, v254, v249
v_mul_f32 v254, v254, v250
AW a0,v254
AR v251,a1
v_sub_f32 v251, v251, v249
v_mul_f32 v251, v251, v250
AW a1,v251
AR v254,a2
v_sub_f32 v254, v254, v249
v_mul_f32 v254, v254, v250
AW a2,v254
AR v251,a3
v_sub_f32 v251, v251, v249
v_mul_f32 v251, v251, v250
AW a3,v251
AR v254,a4
v_sub_f32 v254, v254, v249
v_mul_f32 v254, v254, v250
AW a4,v254
AR v251,a5
v_sub_f32 v251, v251, v249
v_mul_f32 v251, v251, v250
AW a5,v251
AR v254,a6
v_sub_f32 v254, v254, v249
v_mul_f32 v254, v254, v250
AW a6,v254
AR v251,a7
v_sub_f32 v251, v251, v249
v_mul_f32 v251, v251, v250
AW a7,v251
AR v254,a32
v_sub_f32 v254, v254, v249
v_mul_f32 v254, v254, v250
AW a32,v254
AR v251,a33
v_sub_f32 v251, v251, v249
v_mul_f32 v251, v251, v250
AW a33,v251
AR v254,a34
v_sub_f32 v254, v254, v249
v_mul_f32 v254, v254, v250
AW a34,v254
AR v251,a35
v_sub_f32 v251, v251, v249
v_mul_f32 v251, v251, v250
AW a35,v251
AR v254,a36
v_sub_f32 v254, v254, v249
v_mul_f32 v254, v254, v250
AW a36,v254
AR v251,a37
v_sub_f32 v251, v251, v249
v_mul_f32 v251, v251, v250
AW a37,v251
AR v254,a38
v_sub_f32 v254, v254, v249
v_mul_f32 v254, v254, v250
AW a38,v254
AR v251,a39
v_sub_f32 v251, v251, v249
v_mul_f32 v251, v251, v250
AW a39,v251
AR v254,a64
v_sub_f32 v254, v254, v249
v_mul_f32 v254, v254, v250
AW a64,v254
AR v251,a65
v_sub_f32 v251, v251, v249
v_mul_f32 v251, v251, v250
AW a65,v251
AR v254,a66
v_sub_f32 v254, v254, v249
v_mul_f32 v254, v254, v250
AW a66,v254
AR v251,a67
v_sub_f32 v251, v251, v249
v_mul_f32 v251, v251, v250
AW a67,v251
AR v254,a68
v_sub_f32 v254, v254, v249
v_mul_f32 v254, v254, v250
AW a68,v254
AR v251,a69
v_sub_f32 v251, v251, v249
v_mul_f32 v251, v251, v250
AW a69,v251
AR v254,a70
v_sub_f32 v254, v254, v249
v_mul_f32 v254, v254, v250
AW a70,v254
AR v251,a71
v_sub_f32 v251, v251, v249
v_mul_f32 v251, v251, v250
AW a71,v251
AR v254,a96
v_sub_f32 v254, v254, v249
v_mul_f32 v254, v254, v250
AW a96,v254
AR v251,a97
v_sub_f32 v251, v251, v249
v_mul_f32 v251, v251, v250
AW a97,v251
AR v254,a98
v_sub_f32 v254, v254, v249
v_mul_f32 v254, v254, v250
AW a98,v254
AR v251,a99
v_sub_f32 v251, v251, v249
v_mul_f32 v251, v251, v250
AW a99,v251
AR v254,a100
v_sub_f32 v254, v254, v249
v_mul_f32 v254, v254, v250
AW a100,v254
AR v251,a101
v_sub_f32 v251, v251, v249
v_mul_f32 v251, v251, v250
AW a101,v251
AR v254,a102
v_sub_f32 v254, v254, v249
v_mul_f32 v254, v254, v250
AW a102,v254
AR v251,a103
v_sub_f32 v251, v251, v249
v_mul_f32 v251, v251, v250
AW a103,v251
v_max3_f32 v249, v72, v73, v74
v_max3_f32 v251, v104, v105, v106
v_max3_f32 v249, v249, v75, v76
v_max3_f32 v251, v251, v107, v108
v_max3_f32 v249, v249, v77, v78
v_max3_f32 v251, v251, v109, v110
v_max3_f32 v249, v249, v79, v111
v_max_f32 v249, v249, v251
v_mov_b32 v251, v249
s_nop 1
v_permlane16_swap_b32 v249, v251
v_max_f32 v249, v249, v251
v_mov_b32 v251, v249
s_nop 1
v_permlane32_swap_b32 v249, v251
v_max_f32 v249, v249, v251
v_log_f32 v250, v245
s_nop 0
v_max3_f32 v249, 0, v249, v250
v_sub_f32 v250, 0, v249
EX v250,v250
v_sub_f32 v132, v132, v249
v_sub_f32 v133, v133, v249
v_sub_f32 v134, v134, v249
v_sub_f32 v135, v135, v249
v_sub_f32 v8, v8, v249
v_sub_f32 v72, v72, v249
v_sub_f32 v9, v9, v249
v_sub_f32 v73, v73, v249
v_sub_f32 v10, v10, v249
v_sub_f32 v74, v74, v249
v_sub_f32 v11, v11, v249
v_sub_f32 v75, v75, v249
v_sub_f32 v12, v12, v249
v_sub_f32 v76, v76, v249
v_sub_f32 v13, v13, v249
v_sub_f32 v77, v77, v249
v_sub_f32 v14, v14, v249
v_sub_f32 v78, v78, v249
v_sub_f32 v15, v15, v249
v_sub_f32 v79, v79, v249
v_sub_f32 v40, v40, v249
v_sub_f32 v104, v104, v249
v_sub_f32 v41, v41, v249
v_sub_f32 v105, v105, v249
v_sub_f32 v42, v42, v249
v_sub_f32 v106, v106, v249
v_sub_f32 v43, v43, v249
v_sub_f32 v107, v107, v249
v_sub_f32 v44, v44, v249
v_sub_f32 v108, v108, v249
v_sub_f32 v45, v45, v249
v_sub_f32 v109, v109, v249
v_sub_f32 v46, v46, v249
v_sub_f32 v110, v110, v249
v_sub_f32 v47, v47, v249
v_sub_f32 v111, v111, v249
v_mul_f32 v148, v245, v250
v_mov_b32 v149, 0
v_bfe_u32 v249, v241, 8, 8
v_lshl_or_b32 v249, v249, 1, 1
v_lshlrev_b32 v249, 22, v249
v_and_b32 v241, 0xffff00ff, v241
AR v254,a8
v_sub_f32 v254, v254, v249
v_mul_f32 v254, v254, v250
AW a8,v254
AR v251,a9
v_sub_f32 v251, v251, v249
v_mul_f32 v251, v251, v250
AW a9,v251
AR v254,a10
v_sub_f32 v254, v254, v249
v_mul_f32 v254, v254, v250
AW a10,v254
AR v251,a11
v_sub_f32 v251, v251, v249
v_mul_f32 v251, v251, v250
AW a11,v251
AR v254,a12
v_sub_f32 v254, v254, v249
v_mul_f32 v254, v254, v250
AW a12,v254
AR v251,a13
v_sub_f32 v251, v251, v249
v_mul_f32 v251, v251, v250
AW a13,v251
AR v254,a14
v_sub_f32 v254, v254, v249
v_mul_f32 v254, v254, v250
AW a14,v254
AR v251,a15
v_sub_f32 v251, v251, v249
v_mul_f32 v251, v251, v250
AW a15,v251
AR v254,a40
v_sub_f32 v254, v254, v249
v_mul_f32 v254, v254, v250
AW a40,v254
AR v251,a41
v_sub_f32 v251, v251, v249
v_mul_f32 v251, v251, v250
AW a41,v251
AR v254,a42
v_sub_f32 v254, v254, v249
v_mul_f32 v254, v254, v250
AW a42,v254
AR v251,a43
v_sub_f32 v251, v251, v249
v_mul_f32 v251, v251, v250
AW a43,v251
AR v254,a44
v_sub_f32 v254, v254, v249
v_mul_f32 v254, v254, v250
AW a44,v254
AR v251,a45
v_sub_f32 v251, v251, v249
v_mul_f32 v251, v251, v250
AW a45,v251
AR v254,a46
v_sub_f32 v254, v254, v249
v_mul_f32 v254, v254, v250
AW a46,v254
AR v251,a47
v_sub_f32 v251, v251, v249
v_mul_f32 v251, v251, v250
AW a47,v251
AR v254,a72
v_sub_f32 v254, v254, v249
v_mul_f32 v254, v254, v250
AW a72,v254
AR v251,a73
v_sub_f32 v251, v251, v249
v_mul_f32 v251, v251, v250
AW a73,v251
AR v254,a74
v_sub_f32 v254, v254, v249
v_mul_f32 v254, v254, v250
AW a74,v254
AR v251,a75
v_sub_f32 v251, v251, v249
v_mul_f32 v251, v251, v250
AW a75,v251
AR v254,a76
v_sub_f32 v254, v254, v249
v_mul_f32 v254, v254, v250
AW a76,v254
AR v251,a77
v_sub_f32 v251, v251, v249
v_mul_f32 v251, v251, v250
AW a77,v251
AR v254,a78
v_sub_f32 v254, v254, v249
v_mul_f32 v254, v254, v250
AW a78,v254
AR v251,a79
v_sub_f32 v251, v251, v249
v_mul_f32 v251, v251, v250
AW a79,v251
AR v254,a104
v_sub_f32 v254, v254, v249
v_mul_f32 v254, v254, v250
AW a104,v254
AR v251,a105
v_sub_f32 v251, v251, v249
v_mul_f32 v251, v251, v250
AW a105,v251
AR v254,a106
v_sub_f32 v254, v254, v249
v_mul_f32 v254, v254, v250
AW a106,v254
AR v251,a107
v_sub_f32 v251, v251, v249
v_mul_f32 v251, v251, v250
AW a107,v251
AR v254,a108
v_sub_f32 v254, v254, v249
v_mul_f32 v254, v254, v250
AW a108,v254
AR v251,a109
v_sub_f32 v251, v251, v249
v_mul_f32 v251, v251, v250
AW a109,v251
)ASM"
R"ASM(
AR v254,a110
v_sub_f32 v254, v254, v249
v_mul_f32 v254, v254, v250
AW a110,v254
AR v251,a111
v_sub_f32 v251, v251, v249
v_mul_f32 v251, v251, v250
AW a111,v251
EX v64,v64
EX v65,v65
EX v66,v66
EX v67,v67
EX v68,v68
EX v69,v69
EX v70,v70
EX v71,v71
EX v72,v72
EX v73,v73
EX v74,v74
EX v75,v75
EX v76,v76
EX v77,v77
EX v78,v78
EX v79,v79
EX v96,v96
EX v97,v97
EX v98,v98
EX v99,v99
EX v100,v100
EX v101,v101
EX v102,v102
EX v103,v103
EX v104,v104
EX v105,v105
EX v106,v106
EX v107,v107
EX v108,v108
EX v109,v109
EX v110,v110
EX v111,v111
CV v64,v64,v65
CV v65,v66,v67
CV v66,v68,v69
CV v67,v70,v71
CV v72,v72,v73
CV v73,v74,v75
CV v74,v76,v77
CV v75,v78,v79
CV v68,v96,v97
CV v69,v98,v99
CV v70,v100,v101
CV v71,v102,v103
CV v76,v104,v105
CV v77,v106,v107
CV v78,v108,v109
CV v79,v110,v111
v_smfmac_f32_16x16x64_bf16 v[144:147], v[236:239], v[64:71], v234
s_nop 0
v_smfmac_f32_16x16x64_bf16 v[148:151], v[236:239], v[72:79], v234
s_nop 9
s_branch LRET_%=
LRARE104_%=:
s_nop 15
s_waitcnt vmcnt(0) lgkmcnt(0)
v_add_u32 v249, s74, v224
RD a[192:195],v249 offset:0
RD a[208:211],v249 offset:4096
RD a[224:227],v249 offset:8192
RD a[240:243],v249 offset:12288
v_add_u32 v250, s74, v225
RD a[196:199],v250 offset:0
RD a[212:215],v250 offset:4096
RD a[228:231],v250 offset:8192
RD a[244:247],v250 offset:12288
v_add_u32 v251, s74, v226
RD a[200:203],v251 offset:0
RD a[216:219],v251 offset:4096
RD a[232:235],v251 offset:8192
RD a[248:251],v251 offset:12288
v_add_u32 v254, s74, v227
RD a[204:207],v254 offset:0
RD a[220:223],v254 offset:4096
RD a[236:239],v254 offset:8192
RD a[252:255],v254 offset:12288
s_waitcnt lgkmcnt(0)
MM v[64:67],a[192:195],v[160:163],v[128:131]
MM v[64:67],a[196:199],v[176:179],v[64:67]
MM v[64:67],a[200:203],v[192:195],v[64:67]
MM v[64:67],a[204:207],v[208:211],v[64:67]
MM v[68:71],a[220:223],v[208:211],v[128:131]
MM v[68:71],a[216:219],v[192:195],v[68:71]
MM v[68:71],a[212:215],v[176:179],v[68:71]
MM v[68:71],a[208:211],v[160:163],v[68:71]
MM v[76:79],a[208:211],v[164:167],v[132:135]
MM v[76:79],a[212:215],v[180:183],v[76:79]
MM v[76:79],a[216:219],v[196:199],v[76:79]
MM v[76:79],a[220:223],v[212:215],v[76:79]
MM v[72:75],a[204:207],v[212:215],v[132:135]
MM v[72:75],a[200:203],v[196:199],v[72:75]
MM v[72:75],a[196:199],v[180:183],v[72:75]
MM v[72:75],a[192:195],v[164:167],v[72:75]
MM v[80:83],a[192:195],v[168:171],v[136:139]
MM v[80:83],a[196:199],v[184:187],v[80:83]
MM v[80:83],a[200:203],v[200:203],v[80:83]
MM v[80:83],a[204:207],v[216:219],v[80:83]
MM v[84:87],a[220:223],v[216:219],v[136:139]
MM v[84:87],a[216:219],v[200:203],v[84:87]
MM v[84:87],a[212:215],v[184:187],v[84:87]
MM v[84:87],a[208:211],v[168:171],v[84:87]
MM v[92:95],a[208:211],v[172:175],v[140:143]
MM v[92:95],a[212:215],v[188:191],v[92:95]
MM v[92:95],a[216:219],v[204:207],v[92:95]
MM v[92:95],a[220:223],v[220:223],v[92:95]
MM v[88:91],a[204:207],v[220:223],v[140:143]
MM v[88:91],a[200:203],v[204:207],v[88:91]
MM v[88:91],a[196:199],v[188:191],v[88:91]
MM v[88:91],a[192:195],v[172:175],v[88:91]
MM v[96:99],a[224:227],v[160:163],v[128:131]
MM v[96:99],a[228:231],v[176:179],v[96:99]
MM v[96:99],a[232:235],v[192:195],v[96:99]
MM v[96:99],a[236:239],v[208:211],v[96:99]
MM v[100:103],a[252:255],v[208:211],v[128:131]
MM v[100:103],a[248:251],v[192:195],v[100:103]
MM v[100:103],a[244:247],v[176:179],v[100:103]
MM v[100:103],a[240:243],v[160:163],v[100:103]
MM v[108:111],a[240:243],v[164:167],v[132:135]
MM v[108:111],a[244:247],v[180:183],v[108:111]
MM v[108:111],a[248:251],v[196:199],v[108:111]
MM v[108:111],a[252:255],v[212:215],v[108:111]
MM v[104:107],a[236:239],v[212:215],v[132:135]
MM v[104:107],a[232:235],v[196:199],v[104:107]
MM v[104:107],a[228:231],v[180:183],v[104:107]
MM v[104:107],a[224:227],v[164:167],v[104:107]
MM v[112:115],a[224:227],v[168:171],v[136:139]
MM v[112:115],a[228:231],v[184:187],v[112:115]
MM v[112:115],a[232:235],v[200:203],v[112:115]
MM v[112:115],a[236:239],v[216:219],v[112:115]
MM v[116:119],a[252:255],v[216:219],v[136:139]
MM v[116:119],a[248:251],v[200:203],v[116:119]
MM v[116:119],a[244:247],v[184:187],v[116:119]
MM v[116:119],a[240:243],v[168:171],v[116:119]
MM v[124:127],a[240:243],v[172:175],v[140:143]
MM v[124:127],a[244:247],v[188:191],v[124:127]
MM v[124:127],a[248:251],v[204:207],v[124:127]
MM v[124:127],a[252:255],v[220:223],v[124:127]
MM v[120:123],a[236:239],v[220:223],v[140:143]
MM v[120:123],a[232:235],v[204:207],v[120:123]
MM v[120:123],a[228:231],v[188:191],v[120:123]
MM v[120:123],a[224:227],v[172:175],v[120:123]
s_nop 15
v_add_u32 v249, s75, v224
RD a[192:195],v249 offset:0
RD a[208:211],v249 offset:4096
RD a[224:227],v249 offset:8192
RD a[240:243],v249 offset:12288
v_add_u32 v250, s75, v225
RD a[196:199],v250 offset:0
RD a[212:215],v250 offset:4096
RD a[228:231],v250 offset:8192
RD a[244:247],v250 offset:12288
v_add_u32 v251, s75, v226
RD a[200:203],v251 offset:0
RD a[216:219],v251 offset:4096
RD a[232:235],v251 offset:8192
RD a[248:251],v251 offset:12288
v_add_u32 v254, s75, v227
RD a[204:207],v254 offset:0
RD a[220:223],v254 offset:4096
RD a[236:239],v254 offset:8192
RD a[252:255],v254 offset:12288
s_waitcnt lgkmcnt(0)
v_max3_f32 v249, v64, v65, v66
v_max3_f32 v251, v96, v97, v98
v_max3_f32 v249, v249, v67, v68
v_max3_f32 v251, v251, v99, v100
v_max3_f32 v249, v249, v69, v70
v_max3_f32 v251, v251, v101, v102
v_max3_f32 v249, v249, v71, v103
v_max_f32 v249, v249, v251
v_mov_b32 v251, v249
s_nop 1
v_permlane16_swap_b32 v249, v251
v_max_f32 v249, v249, v251
v_mov_b32 v251, v249
s_nop 1
v_permlane32_swap_b32 v249, v251
v_max_f32 v249, v249, v251
v_log_f32 v250, v244
s_nop 0
v_max3_f32 v249, 0, v249, v250
v_sub_f32 v250, 0, v249
EX v250,v250
v_sub_f32 v128, v128, v249
v_sub_f32 v129, v129, v249
v_sub_f32 v130, v130, v249
v_sub_f32 v131, v131, v249
v_sub_f32 v0, v0, v249
v_sub_f32 v64, v64, v249
v_sub_f32 v1, v1, v249
v_sub_f32 v65, v65, v249
v_sub_f32 v2, v2, v249
v_sub_f32 v66, v66, v249
v_sub_f32 v3, v3, v249
v_sub_f32 v67, v67, v249
v_sub_f32 v4, v4, v249
v_sub_f32 v68, v68, v249
v_sub_f32 v5, v5, v249
v_sub_f32 v69, v69, v249
v_sub_f32 v6, v6, v249
v_sub_f32 v70, v70, v249
v_sub_f32 v7, v7, v249
v_sub_f32 v71, v71, v249
v_sub_f32 v32, v32, v249
v_sub_f32 v96, v96, v249
v_sub_f32 v33, v33, v249
v_sub_f32 v97, v97, v249
v_sub_f32 v34, v34, v249
v_sub_f32 v98, v98, v249
v_sub_f32 v35, v35, v249
v_sub_f32 v99, v99, v249
v_sub_f32 v36, v36, v249
v_sub_f32 v100, v100, v249
v_sub_f32 v37, v37, v249
v_sub_f32 v101, v101, v249
v_sub_f32 v38, v38, v249
v_sub_f32 v102, v102, v249
v_sub_f32 v39, v39, v249
v_sub_f32 v103, v103, v249
v_mul_f32 v144, v244, v250
v_mov_b32 v145, 0
v_bfe_u32 v249, v241, 0, 8
v_lshl_or_b32 v249, v249, 1, 1
v_lshlrev_b32 v249, 22, v249
v_and_b32 v241, 0xffffff00, v241
AR v254,a0
v_sub_f32 v254, v254, v249
v_mul_f32 v254, v254, v250
AW a0,v254
AR v251,a1
v_sub_f32 v251, v251, v249
v_mul_f32 v251, v251, v250
AW a1,v251
AR v254,a2
v_sub_f32 v254, v254, v249
v_mul_f32 v254, v254, v250
AW a2,v254
AR v251,a3
v_sub_f32 v251, v251, v249
v_mul_f32 v251, v251, v250
AW a3,v251
AR v254,a4
v_sub_f32 v254, v254, v249
v_mul_f32 v254, v254, v250
AW a4,v254
AR v251,a5
v_sub_f32 v251, v251, v249
v_mul_f32 v251, v251, v250
AW a5,v251
AR v254,a6
v_sub_f32 v254, v254, v249
v_mul_f32 v254, v254, v250
AW a6,v254
AR v251,a7
v_sub_f32 v251, v251, v249
v_mul_f32 v251, v251, v250
AW a7,v251
AR v254,a32
v_sub_f32 v254, v254, v249
v_mul_f32 v254, v254, v250
AW a32,v254
AR v251,a33
v_sub_f32 v251, v251, v249
v_mul_f32 v251, v251, v250
AW a33,v251
AR v254,a34
v_sub_f32 v254, v254, v249
v_mul_f32 v254, v254, v250
AW a34,v254
AR v251,a35
v_sub_f32 v251, v251, v249
v_mul_f32 v251, v251, v250
AW a35,v251
AR v254,a36
v_sub_f32 v254, v254, v249
v_mul_f32 v254, v254, v250
AW a36,v254
AR v251,a37
v_sub_f32 v251, v251, v249
v_mul_f32 v251, v251, v250
AW a37,v251
AR v254,a38
v_sub_f32 v254, v254, v249
v_mul_f32 v254, v254, v250
AW a38,v254
AR v251,a39
v_sub_f32 v251, v251, v249
v_mul_f32 v251, v251, v250
AW a39,v251
AR v254,a64
v_sub_f32 v254, v254, v249
v_mul_f32 v254, v254, v250
AW a64,v254
AR v251,a65
v_sub_f32 v251, v251, v249
v_mul_f32 v251, v251, v250
AW a65,v251
AR v254,a66
v_sub_f32 v254, v254, v249
v_mul_f32 v254, v254, v250
AW a66,v254
AR v251,a67
v_sub_f32 v251, v251, v249
v_mul_f32 v251, v251, v250
AW a67,v251
AR v254,a68
v_sub_f32 v254, v254, v249
v_mul_f32 v254, v254, v250
AW a68,v254
AR v251,a69
v_sub_f32 v251, v251, v249
v_mul_f32 v251, v251, v250
AW a69,v251
AR v254,a70
v_sub_f32 v254, v254, v249
v_mul_f32 v254, v254, v250
AW a70,v254
AR v251,a71
v_sub_f32 v251, v251, v249
v_mul_f32 v251, v251, v250
AW a71,v251
AR v254,a96
v_sub_f32 v254, v254, v249
v_mul_f32 v254, v254, v250
AW a96,v254
AR v251,a97
v_sub_f32 v251, v251, v249
v_mul_f32 v251, v251, v250
AW a97,v251
AR v254,a98
v_sub_f32 v254, v254, v249
v_mul_f32 v254, v254, v250
AW a98,v254
AR v251,a99
v_sub_f32 v251, v251, v249
v_mul_f32 v251, v251, v250
AW a99,v251
AR v254,a100
v_sub_f32 v254, v254, v249
v_mul_f32 v254, v254, v250
AW a100,v254
AR v251,a101
v_sub_f32 v251, v251, v249
v_mul_f32 v251, v251, v250
AW a101,v251
AR v254,a102
v_sub_f32 v254, v254, v249
v_mul_f32 v254, v254, v250
AW a102,v254
AR v251,a103
v_sub_f32 v251, v251, v249
v_mul_f32 v251, v251, v250
AW a103,v251
v_max3_f32 v249, v72, v73, v74
v_max3_f32 v251, v104, v105, v106
v_max3_f32 v249, v249, v75, v76
v_max3_f32 v251, v251, v107, v108
v_max3_f32 v249, v249, v77, v78
v_max3_f32 v251, v251, v109, v110
v_max3_f32 v249, v249, v79, v111
v_max_f32 v249, v249, v251
v_mov_b32 v251, v249
s_nop 1
v_permlane16_swap_b32 v249, v251
v_max_f32 v249, v249, v251
v_mov_b32 v251, v249
s_nop 1
v_permlane32_swap_b32 v249, v251
v_max_f32 v249, v249, v251
v_log_f32 v250, v245
s_nop 0
v_max3_f32 v249, 0, v249, v250
v_sub_f32 v250, 0, v249
EX v250,v250
v_sub_f32 v132, v132, v249
v_sub_f32 v133, v133, v249
v_sub_f32 v134, v134, v249
v_sub_f32 v135, v135, v249
v_sub_f32 v8, v8, v249
v_sub_f32 v72, v72, v249
v_sub_f32 v9, v9, v249
v_sub_f32 v73, v73, v249
v_sub_f32 v10, v10, v249
v_sub_f32 v74, v74, v249
v_sub_f32 v11, v11, v249
v_sub_f32 v75, v75, v249
v_sub_f32 v12, v12, v249
v_sub_f32 v76, v76, v249
v_sub_f32 v13, v13, v249
v_sub_f32 v77, v77, v249
v_sub_f32 v14, v14, v249
v_sub_f32 v78, v78, v249
v_sub_f32 v15, v15, v249
v_sub_f32 v79, v79, v249
v_sub_f32 v40, v40, v249
v_sub_f32 v104, v104, v249
v_sub_f32 v41, v41, v249
v_sub_f32 v105, v105, v249
v_sub_f32 v42, v42, v249
v_sub_f32 v106, v106, v249
v_sub_f32 v43, v43, v249
v_sub_f32 v107, v107, v249
v_sub_f32 v44, v44, v249
v_sub_f32 v108, v108, v249
v_sub_f32 v45, v45, v249
v_sub_f32 v109, v109, v249
v_sub_f32 v46, v46, v249
v_sub_f32 v110, v110, v249
v_sub_f32 v47, v47, v249
v_sub_f32 v111, v111, v249
v_mul_f32 v148, v245, v250
v_mov_b32 v149, 0
v_bfe_u32 v249, v241, 8, 8
v_lshl_or_b32 v249, v249, 1, 1
v_lshlrev_b32 v249, 22, v249
v_and_b32 v241, 0xffff00ff, v241
AR v254,a8
v_sub_f32 v254, v254, v249
v_mul_f32 v254, v254, v250
AW a8,v254
AR v251,a9
v_sub_f32 v251, v251, v249
v_mul_f32 v251, v251, v250
AW a9,v251
AR v254,a10
v_sub_f32 v254, v254, v249
v_mul_f32 v254, v254, v250
AW a10,v254
AR v251,a11
v_sub_f32 v251, v251, v249
v_mul_f32 v251, v251, v250
AW a11,v251
AR v254,a12
v_sub_f32 v254, v254, v249
v_mul_f32 v254, v254, v250
AW a12,v254
AR v251,a13
v_sub_f32 v251, v251, v249
v_mul_f32 v251, v251, v250
AW a13,v251
AR v254,a14
v_sub_f32 v254, v254, v249
v_mul_f32 v254, v254, v250
AW a14,v254
AR v251,a15
v_sub_f32 v251, v251, v249
v_mul_f32 v251, v251, v250
AW a15,v251
AR v254,a40
v_sub_f32 v254, v254, v249
v_mul_f32 v254, v254, v250
AW a40,v254
AR v251,a41
v_sub_f32 v251, v251, v249
v_mul_f32 v251, v251, v250
AW a41,v251
AR v254,a42
v_sub_f32 v254, v254, v249
v_mul_f32 v254, v254, v250
AW a42,v254
AR v251,a43
v_sub_f32 v251, v251, v249
v_mul_f32 v251, v251, v250
AW a43,v251
AR v254,a44
v_sub_f32 v254, v254, v249
v_mul_f32 v254, v254, v250
AW a44,v254
AR v251,a45
v_sub_f32 v251, v251, v249
v_mul_f32 v251, v251, v250
AW a45,v251
AR v254,a46
v_sub_f32 v254, v254, v249
v_mul_f32 v254, v254, v250
AW a46,v254
AR v251,a47
v_sub_f32 v251, v251, v249
)ASM"
R"ASM(
v_mul_f32 v251, v251, v250
AW a47,v251
AR v254,a72
v_sub_f32 v254, v254, v249
v_mul_f32 v254, v254, v250
AW a72,v254
AR v251,a73
v_sub_f32 v251, v251, v249
v_mul_f32 v251, v251, v250
AW a73,v251
AR v254,a74
v_sub_f32 v254, v254, v249
v_mul_f32 v254, v254, v250
AW a74,v254
AR v251,a75
v_sub_f32 v251, v251, v249
v_mul_f32 v251, v251, v250
AW a75,v251
AR v254,a76
v_sub_f32 v254, v254, v249
v_mul_f32 v254, v254, v250
AW a76,v254
AR v251,a77
v_sub_f32 v251, v251, v249
v_mul_f32 v251, v251, v250
AW a77,v251
AR v254,a78
v_sub_f32 v254, v254, v249
v_mul_f32 v254, v254, v250
AW a78,v254
AR v251,a79
v_sub_f32 v251, v251, v249
v_mul_f32 v251, v251, v250
AW a79,v251
AR v254,a104
v_sub_f32 v254, v254, v249
v_mul_f32 v254, v254, v250
AW a104,v254
AR v251,a105
v_sub_f32 v251, v251, v249
v_mul_f32 v251, v251, v250
AW a105,v251
AR v254,a106
v_sub_f32 v254, v254, v249
v_mul_f32 v254, v254, v250
AW a106,v254
AR v251,a107
v_sub_f32 v251, v251, v249
v_mul_f32 v251, v251, v250
AW a107,v251
AR v254,a108
v_sub_f32 v254, v254, v249
v_mul_f32 v254, v254, v250
AW a108,v254
AR v251,a109
v_sub_f32 v251, v251, v249
v_mul_f32 v251, v251, v250
AW a109,v251
AR v254,a110
v_sub_f32 v254, v254, v249
v_mul_f32 v254, v254, v250
AW a110,v254
AR v251,a111
v_sub_f32 v251, v251, v249
v_mul_f32 v251, v251, v250
AW a111,v251
v_max3_f32 v249, v80, v81, v82
v_max3_f32 v251, v112, v113, v114
v_max3_f32 v249, v249, v83, v84
v_max3_f32 v251, v251, v115, v116
v_max3_f32 v249, v249, v85, v86
v_max3_f32 v251, v251, v117, v118
v_max3_f32 v249, v249, v87, v119
v_max_f32 v249, v249, v251
v_mov_b32 v251, v249
s_nop 1
v_permlane16_swap_b32 v249, v251
v_max_f32 v249, v249, v251
v_mov_b32 v251, v249
s_nop 1
v_permlane32_swap_b32 v249, v251
v_max_f32 v249, v249, v251
v_log_f32 v250, v246
s_nop 0
v_max3_f32 v249, 0, v249, v250
v_sub_f32 v250, 0, v249
EX v250,v250
v_sub_f32 v136, v136, v249
v_sub_f32 v137, v137, v249
v_sub_f32 v138, v138, v249
v_sub_f32 v139, v139, v249
v_sub_f32 v16, v16, v249
v_sub_f32 v80, v80, v249
v_sub_f32 v17, v17, v249
v_sub_f32 v81, v81, v249
v_sub_f32 v18, v18, v249
v_sub_f32 v82, v82, v249
v_sub_f32 v19, v19, v249
v_sub_f32 v83, v83, v249
v_sub_f32 v20, v20, v249
v_sub_f32 v84, v84, v249
v_sub_f32 v21, v21, v249
v_sub_f32 v85, v85, v249
v_sub_f32 v22, v22, v249
v_sub_f32 v86, v86, v249
v_sub_f32 v23, v23, v249
v_sub_f32 v87, v87, v249
v_sub_f32 v48, v48, v249
v_sub_f32 v112, v112, v249
v_sub_f32 v49, v49, v249
v_sub_f32 v113, v113, v249
v_sub_f32 v50, v50, v249
v_sub_f32 v114, v114, v249
v_sub_f32 v51, v51, v249
v_sub_f32 v115, v115, v249
v_sub_f32 v52, v52, v249
v_sub_f32 v116, v116, v249
v_sub_f32 v53, v53, v249
v_sub_f32 v117, v117, v249
v_sub_f32 v54, v54, v249
v_sub_f32 v118, v118, v249
v_sub_f32 v55, v55, v249
v_sub_f32 v119, v119, v249
v_mul_f32 v152, v246, v250
v_mov_b32 v153, 0
v_bfe_u32 v249, v241, 16, 8
v_lshl_or_b32 v249, v249, 1, 1
v_lshlrev_b32 v249, 22, v249
v_and_b32 v241, 0xff00ffff, v241
AR v254,a16
v_sub_f32 v254, v254, v249
v_mul_f32 v254, v254, v250
AW a16,v254
AR v251,a17
v_sub_f32 v251, v251, v249
v_mul_f32 v251, v251, v250
AW a17,v251
AR v254,a18
v_sub_f32 v254, v254, v249
v_mul_f32 v254, v254, v250
AW a18,v254
AR v251,a19
v_sub_f32 v251, v251, v249
v_mul_f32 v251, v251, v250
AW a19,v251
AR v254,a20
v_sub_f32 v254, v254, v249
v_mul_f32 v254, v254, v250
AW a20,v254
AR v251,a21
v_sub_f32 v251, v251, v249
v_mul_f32 v251, v251, v250
AW a21,v251
AR v254,a22
v_sub_f32 v254, v254, v249
v_mul_f32 v254, v254, v250
AW a22,v254
AR v251,a23
v_sub_f32 v251, v251, v249
v_mul_f32 v251, v251, v250
AW a23,v251
AR v254,a48
v_sub_f32 v254, v254, v249
v_mul_f32 v254, v254, v250
AW a48,v254
AR v251,a49
v_sub_f32 v251, v251, v249
v_mul_f32 v251, v251, v250
AW a49,v251
AR v254,a50
v_sub_f32 v254, v254, v249
v_mul_f32 v254, v254, v250
AW a50,v254
AR v251,a51
v_sub_f32 v251, v251, v249
v_mul_f32 v251, v251, v250
AW a51,v251
AR v254,a52
v_sub_f32 v254, v254, v249
v_mul_f32 v254, v254, v250
AW a52,v254
AR v251,a53
v_sub_f32 v251, v251, v249
v_mul_f32 v251, v251, v250
AW a53,v251
AR v254,a54
v_sub_f32 v254, v254, v249
v_mul_f32 v254, v254, v250
AW a54,v254
AR v251,a55
v_sub_f32 v251, v251, v249
v_mul_f32 v251, v251, v250
AW a55,v251
AR v254,a80
v_sub_f32 v254, v254, v249
v_mul_f32 v254, v254, v250
AW a80,v254
AR v251,a81
v_sub_f32 v251, v251, v249
v_mul_f32 v251, v251, v250
AW a81,v251
AR v254,a82
v_sub_f32 v254, v254, v249
v_mul_f32 v254, v254, v250
AW a82,v254
AR v251,a83
v_sub_f32 v251, v251, v249
v_mul_f32 v251, v251, v250
AW a83,v251
AR v254,a84
v_sub_f32 v254, v254, v249
v_mul_f32 v254, v254, v250
AW a84,v254
AR v251,a85
v_sub_f32 v251, v251, v249
v_mul_f32 v251, v251, v250
AW a85,v251
AR v254,a86
v_sub_f32 v254, v254, v249
v_mul_f32 v254, v254, v250
AW a86,v254
AR v251,a87
v_sub_f32 v251, v251, v249
v_mul_f32 v251, v251, v250
AW a87,v251
AR v254,a112
v_sub_f32 v254, v254, v249
v_mul_f32 v254, v254, v250
AW a112,v254
AR v251,a113
v_sub_f32 v251, v251, v249
v_mul_f32 v251, v251, v250
AW a113,v251
AR v254,a114
v_sub_f32 v254, v254, v249
v_mul_f32 v254, v254, v250
AW a114,v254
AR v251,a115
v_sub_f32 v251, v251, v249
v_mul_f32 v251, v251, v250
AW a115,v251
AR v254,a116
v_sub_f32 v254, v254, v249
v_mul_f32 v254, v254, v250
AW a116,v254
AR v251,a117
v_sub_f32 v251, v251, v249
v_mul_f32 v251, v251, v250
AW a117,v251
AR v254,a118
v_sub_f32 v254, v254, v249
v_mul_f32 v254, v254, v250
AW a118,v254
AR v251,a119
v_sub_f32 v251, v251, v249
v_mul_f32 v251, v251, v250
AW a119,v251
v_max3_f32 v249, v88, v89, v90
v_max3_f32 v251, v120, v121, v122
v_max3_f32 v249, v249, v91, v92
v_max3_f32 v251, v251, v123, v124
v_max3_f32 v249, v249, v93, v94
v_max3_f32 v251, v251, v125, v126
v_max3_f32 v249, v249, v95, v127
v_max_f32 v249, v249, v251
v_mov_b32 v251, v249
s_nop 1
v_permlane16_swap_b32 v249, v251
v_max_f32 v249, v249, v251
v_mov_b32 v251, v249
s_nop 1
v_permlane32_swap_b32 v249, v251
v_max_f32 v249, v249, v251
v_log_f32 v250, v247
s_nop 0
v_max3_f32 v249, 0, v249, v250
v_sub_f32 v250, 0, v249
EX v250,v250
v_sub_f32 v140, v140, v249
v_sub_f32 v141, v141, v249
v_sub_f32 v142, v142, v249
v_sub_f32 v143, v143, v249
v_sub_f32 v24, v24, v249
v_sub_f32 v88, v88, v249
v_sub_f32 v25, v25, v249
v_sub_f32 v89, v89, v249
v_sub_f32 v26, v26, v249
v_sub_f32 v90, v90, v249
v_sub_f32 v27, v27, v249
v_sub_f32 v91, v91, v249
v_sub_f32 v28, v28, v249
v_sub_f32 v92, v92, v249
v_sub_f32 v29, v29, v249
v_sub_f32 v93, v93, v249
v_sub_f32 v30, v30, v249
v_sub_f32 v94, v94, v249
v_sub_f32 v31, v31, v249
v_sub_f32 v95, v95, v249
v_sub_f32 v56, v56, v249
v_sub_f32 v120, v120, v249
v_sub_f32 v57, v57, v249
v_sub_f32 v121, v121, v249
v_sub_f32 v58, v58, v249
v_sub_f32 v122, v122, v249
v_sub_f32 v59, v59, v249
v_sub_f32 v123, v123, v249
v_sub_f32 v60, v60, v249
v_sub_f32 v124, v124, v249
v_sub_f32 v61, v61, v249
v_sub_f32 v125, v125, v249
v_sub_f32 v62, v62, v249
v_sub_f32 v126, v126, v249
v_sub_f32 v63, v63, v249
v_sub_f32 v127, v127, v249
v_mul_f32 v156, v247, v250
v_mov_b32 v157, 0
v_bfe_u32 v249, v241, 24, 8
v_lshl_or_b32 v249, v249, 1, 1
v_lshlrev_b32 v249, 22, v249
v_and_b32 v241, 0xffffff, v241
AR v254,a24
v_sub_f32 v254, v254, v249
v_mul_f32 v254, v254, v250
AW a24,v254
AR v251,a25
v_sub_f32 v251, v251, v249
v_mul_f32 v251, v251, v250
AW a25,v251
AR v254,a26
v_sub_f32 v254, v254, v249
v_mul_f32 v254, v254, v250
AW a26,v254
AR v251,a27
v_sub_f32 v251, v251, v249
v_mul_f32 v251, v251, v250
AW a27,v251
AR v254,a28
v_sub_f32 v254, v254, v249
v_mul_f32 v254, v254, v250
AW a28,v254
AR v251,a29
v_sub_f32 v251, v251, v249
v_mul_f32 v251, v251, v250
AW a29,v251
AR v254,a30
v_sub_f32 v254, v254, v249
v_mul_f32 v254, v254, v250
AW a30,v254
AR v251,a31
v_sub_f32 v251, v251, v249
v_mul_f32 v251, v251, v250
AW a31,v251
AR v254,a56
v_sub_f32 v254, v254, v249
v_mul_f32 v254, v254, v250
AW a56,v254
AR v251,a57
v_sub_f32 v251, v251, v249
v_mul_f32 v251, v251, v250
AW a57,v251
AR v254,a58
v_sub_f32 v254, v254, v249
v_mul_f32 v254, v254, v250
AW a58,v254
AR v251,a59
v_sub_f32 v251, v251, v249
v_mul_f32 v251, v251, v250
AW a59,v251
AR v254,a60
v_sub_f32 v254, v254, v249
v_mul_f32 v254, v254, v250
AW a60,v254
AR v251,a61
v_sub_f32 v251, v251, v249
v_mul_f32 v251, v251, v250
AW a61,v251
AR v254,a62
v_sub_f32 v254, v254, v249
v_mul_f32 v254, v254, v250
AW a62,v254
AR v251,a63
v_sub_f32 v251, v251, v249
v_mul_f32 v251, v251, v250
AW a63,v251
AR v254,a88
v_sub_f32 v254, v254, v249
v_mul_f32 v254, v254, v250
AW a88,v254
AR v251,a89
v_sub_f32 v251, v251, v249
v_mul_f32 v251, v251, v250
AW a89,v251
AR v254,a90
v_sub_f32 v254, v254, v249
v_mul_f32 v254, v254, v250
AW a90,v254
AR v251,a91
v_sub_f32 v251, v251, v249
v_mul_f32 v251, v251, v250
AW a91,v251
AR v254,a92
v_sub_f32 v254, v254, v249
v_mul_f32 v254, v254, v250
AW a92,v254
AR v251,a93
v_sub_f32 v251, v251, v249
v_mul_f32 v251, v251, v250
AW a93,v251
AR v254,a94
v_sub_f32 v254, v254, v249
v_mul_f32 v254, v254, v250
AW a94,v254
AR v251,a95
v_sub_f32 v251, v251, v249
v_mul_f32 v251, v251, v250
AW a95,v251
AR v254,a120
v_sub_f32 v254, v254, v249
v_mul_f32 v254, v254, v250
AW a120,v254
AR v251,a121
v_sub_f32 v251, v251, v249
v_mul_f32 v251, v251, v250
AW a121,v251
AR v254,a122
v_sub_f32 v254, v254, v249
v_mul_f32 v254, v254, v250
AW a122,v254
AR v251,a123
v_sub_f32 v251, v251, v249
v_mul_f32 v251, v251, v250
AW a123,v251
AR v254,a124
v_sub_f32 v254, v254, v249
v_mul_f32 v254, v254, v250
AW a124,v254
AR v251,a125
v_sub_f32 v251, v251, v249
v_mul_f32 v251, v251, v250
AW a125,v251
AR v254,a126
v_sub_f32 v254, v254, v249
v_mul_f32 v254, v254, v250
AW a126,v254
AR v251,a127
v_sub_f32 v251, v251, v249
v_mul_f32 v251, v251, v250
AW a127,v251
EX v64,v64
EX v65,v65
EX v66,v66
EX v67,v67
EX v68,v68
EX v69,v69
EX v70,v70
EX v71,v71
EX v72,v72
EX v73,v73
EX v74,v74
EX v75,v75
EX v76,v76
EX v77,v77
EX v78,v78
EX v79,v79
EX v80,v80
EX v81,v81
EX v82,v82
EX v83,v83
EX v84,v84
EX v85,v85
EX v86,v86
EX v87,v87
EX v88,v88
EX v89,v89
EX v90,v90
EX v91,v91
EX v92,v92
EX v93,v93
EX v94,v94
EX v95,v95
EX v96,v96
EX v97,v97
EX v98,v98
EX v99,v99
EX v100,v100
EX v101,v101
EX v102,v102
EX v103,v103
EX v104,v104
EX v105,v105
EX v106,v106
EX v107,v107
EX v108,v108
EX v109,v109
EX v110,v110
EX v111,v111
EX v112,v112
EX v113,v113
EX v114,v114
EX v115,v115
EX v116,v116
EX v117,v117
EX v118,v118
EX v119,v119
EX v120,v120
EX v121,v121
EX v122,v122
EX v123,v123
EX v124,v124
EX v125,v125
EX v126,v126
EX v127,v127
CV v64,v64,v65
CV v65,v66,v67
CV v66,v68,v69
CV v67,v70,v71
CV v72,v72,v73
CV v73,v74,v75
CV v74,v76,v77
CV v75,v78,v79
CV v80,v80,v81
CV v81,v82,v83
CV v82,v84,v85
CV v83,v86,v87
CV v88,v88,v89
CV v89,v90,v91
CV v90,v92,v93
CV v91,v94,v95
CV v68,v96,v97
CV v69,v98,v99
CV v70,v100,v101
CV v71,v102,v103
CV v76,v104,v105
CV v77,v106,v107
CV v78,v108,v109
CV v79,v110,v111
CV v84,v112,v113
CV v85,v114,v115
CV v86,v116,v117
CV v87,v118,v119
CV v92,v120,v121
CV v93,v122,v123
CV v94,v124,v125
CV v95,v126,v127
v_smfmac_f32_16x16x64_bf16 v[144:147], v[236:239], v[64:71], v234
v_smfmac_f32_16x16x64_bf16 v[148:151], v[236:239], v[72:79], v234
v_smfmac_f32_16x16x64_bf16 v[152:155], v[236:239], v[80:87], v234
v_smfmac_f32_16x16x64_bf16 v[156:159], v[236:239], v[88:95], v234
s_nop 9
s_branch LRET_%=
LRARE004_%=:
s_nop 15
s_waitcnt vmcnt(0) lgkmcnt(0)
v_add_u32 v249, s74, v224
RD a[192:195],v249 offset:0
RD a[208:211],v249 offset:4096
RD a[224:227],v249 offset:8192
RD a[240:243],v249 offset:12288
v_add_u32 v250, s74, v225
RD a[196:199],v250 offset:0
RD a[212:215],v250 offset:4096
RD a[228:231],v250 offset:8192
RD a[244:247],v250 offset:12288
v_add_u32 v251, s74, v226
RD a[200:203],v251 offset:0
RD a[216:219],v251 offset:4096
RD a[232:235],v251 offset:8192
RD a[248:251],v251 offset:12288
v_add_u32 v254, s74, v227
RD a[204:207],v254 offset:0
RD a[220:223],v254 offset:4096
RD a[236:239],v254 offset:8192
RD a[252:255],v254 offset:12288
s_waitcnt lgkmcnt(0)
MM v[0:3],a[192:195],v[160:163],v[128:131]
MM v[0:3],a[196:199],v[176:179],v[0:3]
MM v[0:3],a[200:203],v[192:195],v[0:3]
MM v[0:3],a[204:207],v[208:211],v[0:3]
MM v[4:7],a[220:223],v[208:211],v[128:131]
MM v[4:7],a[216:219],v[192:195],v[4:7]
)ASM"
R"ASM(
MM v[4:7],a[212:215],v[176:179],v[4:7]
MM v[4:7],a[208:211],v[160:163],v[4:7]
MM v[12:15],a[208:211],v[164:167],v[132:135]
MM v[12:15],a[212:215],v[180:183],v[12:15]
MM v[12:15],a[216:219],v[196:199],v[12:15]
MM v[12:15],a[220:223],v[212:215],v[12:15]
MM v[8:11],a[204:207],v[212:215],v[132:135]
MM v[8:11],a[200:203],v[196:199],v[8:11]
MM v[8:11],a[196:199],v[180:183],v[8:11]
MM v[8:11],a[192:195],v[164:167],v[8:11]
MM v[16:19],a[192:195],v[168:171],v[136:139]
MM v[16:19],a[196:199],v[184:187],v[16:19]
MM v[16:19],a[200:203],v[200:203],v[16:19]
MM v[16:19],a[204:207],v[216:219],v[16:19]
MM v[20:23],a[220:223],v[216:219],v[136:139]
MM v[20:23],a[216:219],v[200:203],v[20:23]
MM v[20:23],a[212:215],v[184:187],v[20:23]
MM v[20:23],a[208:211],v[168:171],v[20:23]
MM v[28:31],a[208:211],v[172:175],v[140:143]
MM v[28:31],a[212:215],v[188:191],v[28:31]
MM v[28:31],a[216:219],v[204:207],v[28:31]
MM v[28:31],a[220:223],v[220:223],v[28:31]
MM v[24:27],a[204:207],v[220:223],v[140:143]
MM v[24:27],a[200:203],v[204:207],v[24:27]
MM v[24:27],a[196:199],v[188:191],v[24:27]
MM v[24:27],a[192:195],v[172:175],v[24:27]
MM v[32:35],a[224:227],v[160:163],v[128:131]
MM v[32:35],a[228:231],v[176:179],v[32:35]
MM v[32:35],a[232:235],v[192:195],v[32:35]
MM v[32:35],a[236:239],v[208:211],v[32:35]
MM v[36:39],a[252:255],v[208:211],v[128:131]
MM v[36:39],a[248:251],v[192:195],v[36:39]
MM v[36:39],a[244:247],v[176:179],v[36:39]
MM v[36:39],a[240:243],v[160:163],v[36:39]
MM v[44:47],a[240:243],v[164:167],v[132:135]
MM v[44:47],a[244:247],v[180:183],v[44:47]
MM v[44:47],a[248:251],v[196:199],v[44:47]
MM v[44:47],a[252:255],v[212:215],v[44:47]
MM v[40:43],a[236:239],v[212:215],v[132:135]
MM v[40:43],a[232:235],v[196:199],v[40:43]
MM v[40:43],a[228:231],v[180:183],v[40:43]
MM v[40:43],a[224:227],v[164:167],v[40:43]
MM v[48:51],a[224:227],v[168:171],v[136:139]
MM v[48:51],a[228:231],v[184:187],v[48:51]
MM v[48:51],a[232:235],v[200:203],v[48:51]
MM v[48:51],a[236:239],v[216:219],v[48:51]
MM v[52:55],a[252:255],v[216:219],v[136:139]
MM v[52:55],a[248:251],v[200:203],v[52:55]
MM v[52:55],a[244:247],v[184:187],v[52:55]
MM v[52:55],a[240:243],v[168:171],v[52:55]
MM v[60:63],a[240:243],v[172:175],v[140:143]
MM v[60:63],a[244:247],v[188:191],v[60:63]
MM v[60:63],a[248:251],v[204:207],v[60:63]
MM v[60:63],a[252:255],v[220:223],v[60:63]
MM v[56:59],a[236:239],v[220:223],v[140:143]
MM v[56:59],a[232:235],v[204:207],v[56:59]
MM v[56:59],a[228:231],v[188:191],v[56:59]
MM v[56:59],a[224:227],v[172:175],v[56:59]
s_nop 15
v_add_u32 v249, s75, v224
RD a[192:195],v249 offset:0
RD a[208:211],v249 offset:4096
RD a[224:227],v249 offset:8192
RD a[240:243],v249 offset:12288
v_add_u32 v250, s75, v225
RD a[196:199],v250 offset:0
RD a[212:215],v250 offset:4096
RD a[228:231],v250 offset:8192
RD a[244:247],v250 offset:12288
v_add_u32 v251, s75, v226
RD a[200:203],v251 offset:0
RD a[216:219],v251 offset:4096
RD a[232:235],v251 offset:8192
RD a[248:251],v251 offset:12288
v_add_u32 v254, s75, v227
RD a[204:207],v254 offset:0
RD a[220:223],v254 offset:4096
RD a[236:239],v254 offset:8192
RD a[252:255],v254 offset:12288
s_waitcnt lgkmcnt(0)
v_max3_f32 v249, v0, v1, v2
v_max3_f32 v251, v32, v33, v34
v_max3_f32 v249, v249, v3, v4
v_max3_f32 v251, v251, v35, v36
v_max3_f32 v249, v249, v5, v6
v_max3_f32 v251, v251, v37, v38
v_max3_f32 v249, v249, v7, v39
v_max_f32 v249, v249, v251
v_mov_b32 v251, v249
s_nop 1
v_permlane16_swap_b32 v249, v251
v_max_f32 v249, v249, v251
v_mov_b32 v251, v249
s_nop 1
v_permlane32_swap_b32 v249, v251
v_max_f32 v249, v249, v251
v_log_f32 v250, v244
s_nop 0
v_max3_f32 v249, 0, v249, v250
v_sub_f32 v250, 0, v249
EX v250,v250
v_sub_f32 v128, v128, v249
v_sub_f32 v129, v129, v249
v_sub_f32 v130, v130, v249
v_sub_f32 v131, v131, v249
v_sub_f32 v64, v64, v249
v_sub_f32 v0, v0, v249
v_sub_f32 v65, v65, v249
v_sub_f32 v1, v1, v249
v_sub_f32 v66, v66, v249
v_sub_f32 v2, v2, v249
v_sub_f32 v67, v67, v249
v_sub_f32 v3, v3, v249
v_sub_f32 v68, v68, v249
v_sub_f32 v4, v4, v249
v_sub_f32 v69, v69, v249
v_sub_f32 v5, v5, v249
v_sub_f32 v70, v70, v249
v_sub_f32 v6, v6, v249
v_sub_f32 v71, v71, v249
v_sub_f32 v7, v7, v249
v_sub_f32 v96, v96, v249
v_sub_f32 v32, v32, v249
v_sub_f32 v97, v97, v249
v_sub_f32 v33, v33, v249
v_sub_f32 v98, v98, v249
v_sub_f32 v34, v34, v249
v_sub_f32 v99, v99, v249
v_sub_f32 v35, v35, v249
v_sub_f32 v100, v100, v249
v_sub_f32 v36, v36, v249
v_sub_f32 v101, v101, v249
v_sub_f32 v37, v37, v249
v_sub_f32 v102, v102, v249
v_sub_f32 v38, v38, v249
v_sub_f32 v103, v103, v249
v_sub_f32 v39, v39, v249
v_mul_f32 v144, v244, v250
v_mov_b32 v145, 0
v_bfe_u32 v249, v241, 0, 8
v_lshl_or_b32 v249, v249, 1, 1
v_lshlrev_b32 v249, 22, v249
v_and_b32 v241, 0xffffff00, v241
AR v254,a0
v_sub_f32 v254, v254, v249
v_mul_f32 v254, v254, v250
AW a0,v254
AR v251,a1
v_sub_f32 v251, v251, v249
v_mul_f32 v251, v251, v250
AW a1,v251
AR v254,a2
v_sub_f32 v254, v254, v249
v_mul_f32 v254, v254, v250
AW a2,v254
AR v251,a3
v_sub_f32 v251, v251, v249
v_mul_f32 v251, v251, v250
AW a3,v251
AR v254,a4
v_sub_f32 v254, v254, v249
v_mul_f32 v254, v254, v250
AW a4,v254
AR v251,a5
v_sub_f32 v251, v251, v249
v_mul_f32 v251, v251, v250
AW a5,v251
AR v254,a6
v_sub_f32 v254, v254, v249
v_mul_f32 v254, v254, v250
AW a6,v254
AR v251,a7
v_sub_f32 v251, v251, v249
v_mul_f32 v251, v251, v250
AW a7,v251
AR v254,a32
v_sub_f32 v254, v254, v249
v_mul_f32 v254, v254, v250
AW a32,v254
AR v251,a33
v_sub_f32 v251, v251, v249
v_mul_f32 v251, v251, v250
AW a33,v251
AR v254,a34
v_sub_f32 v254, v254, v249
v_mul_f32 v254, v254, v250
AW a34,v254
AR v251,a35
v_sub_f32 v251, v251, v249
v_mul_f32 v251, v251, v250
AW a35,v251
AR v254,a36
v_sub_f32 v254, v254, v249
v_mul_f32 v254, v254, v250
AW a36,v254
AR v251,a37
v_sub_f32 v251, v251, v249
v_mul_f32 v251, v251, v250
AW a37,v251
AR v254,a38
v_sub_f32 v254, v254, v249
v_mul_f32 v254, v254, v250
AW a38,v254
AR v251,a39
v_sub_f32 v251, v251, v249
v_mul_f32 v251, v251, v250
AW a39,v251
AR v254,a64
v_sub_f32 v254, v254, v249
v_mul_f32 v254, v254, v250
AW a64,v254
AR v251,a65
v_sub_f32 v251, v251, v249
v_mul_f32 v251, v251, v250
AW a65,v251
AR v254,a66
v_sub_f32 v254, v254, v249
v_mul_f32 v254, v254, v250
AW a66,v254
AR v251,a67
v_sub_f32 v251, v251, v249
v_mul_f32 v251, v251, v250
AW a67,v251
AR v254,a68
v_sub_f32 v254, v254, v249
v_mul_f32 v254, v254, v250
AW a68,v254
AR v251,a69
v_sub_f32 v251, v251, v249
v_mul_f32 v251, v251, v250
AW a69,v251
AR v254,a70
v_sub_f32 v254, v254, v249
v_mul_f32 v254, v254, v250
AW a70,v254
AR v251,a71
v_sub_f32 v251, v251, v249
v_mul_f32 v251, v251, v250
AW a71,v251
AR v254,a96
v_sub_f32 v254, v254, v249
v_mul_f32 v254, v254, v250
AW a96,v254
AR v251,a97
v_sub_f32 v251, v251, v249
v_mul_f32 v251, v251, v250
AW a97,v251
AR v254,a98
v_sub_f32 v254, v254, v249
v_mul_f32 v254, v254, v250
AW a98,v254
AR v251,a99
v_sub_f32 v251, v251, v249
v_mul_f32 v251, v251, v250
AW a99,v251
AR v254,a100
v_sub_f32 v254, v254, v249
v_mul_f32 v254, v254, v250
AW a100,v254
AR v251,a101
v_sub_f32 v251, v251, v249
v_mul_f32 v251, v251, v250
AW a101,v251
AR v254,a102
v_sub_f32 v254, v254, v249
v_mul_f32 v254, v254, v250
AW a102,v254
AR v251,a103
v_sub_f32 v251, v251, v249
v_mul_f32 v251, v251, v250
AW a103,v251
v_max3_f32 v249, v8, v9, v10
v_max3_f32 v251, v40, v41, v42
v_max3_f32 v249, v249, v11, v12
v_max3_f32 v251, v251, v43, v44
v_max3_f32 v249, v249, v13, v14
v_max3_f32 v251, v251, v45, v46
v_max3_f32 v249, v249, v15, v47
v_max_f32 v249, v249, v251
v_mov_b32 v251, v249
s_nop 1
v_permlane16_swap_b32 v249, v251
v_max_f32 v249, v249, v251
v_mov_b32 v251, v249
s_nop 1
v_permlane32_swap_b32 v249, v251
v_max_f32 v249, v249, v251
v_log_f32 v250, v245
s_nop 0
v_max3_f32 v249, 0, v249, v250
v_sub_f32 v250, 0, v249
EX v250,v250
v_sub_f32 v132, v132, v249
v_sub_f32 v133, v133, v249
v_sub_f32 v134, v134, v249
v_sub_f32 v135, v135, v249
v_sub_f32 v72, v72, v249
v_sub_f32 v8, v8, v249
v_sub_f32 v73, v73, v249
v_sub_f32 v9, v9, v249
v_sub_f32 v74, v74, v249
v_sub_f32 v10, v10, v249
v_sub_f32 v75, v75, v249
v_sub_f32 v11, v11, v249
v_sub_f32 v76, v76, v249
v_sub_f32 v12, v12, v249
v_sub_f32 v77, v77, v249
v_sub_f32 v13, v13, v249
v_sub_f32 v78, v78, v249
v_sub_f32 v14, v14, v249
v_sub_f32 v79, v79, v249
v_sub_f32 v15, v15, v249
v_sub_f32 v104, v104, v249
v_sub_f32 v40, v40, v249
v_sub_f32 v105, v105, v249
v_sub_f32 v41, v41, v249
v_sub_f32 v106, v106, v249
v_sub_f32 v42, v42, v249
v_sub_f32 v107, v107, v249
v_sub_f32 v43, v43, v249
v_sub_f32 v108, v108, v249
v_sub_f32 v44, v44, v249
v_sub_f32 v109, v109, v249
v_sub_f32 v45, v45, v249
v_sub_f32 v110, v110, v249
v_sub_f32 v46, v46, v249
v_sub_f32 v111, v111, v249
v_sub_f32 v47, v47, v249
v_mul_f32 v148, v245, v250
v_mov_b32 v149, 0
v_bfe_u32 v249, v241, 8, 8
v_lshl_or_b32 v249, v249, 1, 1
v_lshlrev_b32 v249, 22, v249
v_and_b32 v241, 0xffff00ff, v241
AR v254,a8
v_sub_f32 v254, v254, v249
v_mul_f32 v254, v254, v250
AW a8,v254
AR v251,a9
v_sub_f32 v251, v251, v249
v_mul_f32 v251, v251, v250
AW a9,v251
AR v254,a10
v_sub_f32 v254, v254, v249
v_mul_f32 v254, v254, v250
AW a10,v254
AR v251,a11
v_sub_f32 v251, v251, v249
v_mul_f32 v251, v251, v250
AW a11,v251
AR v254,a12
v_sub_f32 v254, v254, v249
v_mul_f32 v254, v254, v250
AW a12,v254
AR v251,a13
v_sub_f32 v251, v251, v249
v_mul_f32 v251, v251, v250
AW a13,v251
AR v254,a14
v_sub_f32 v254, v254, v249
v_mul_f32 v254, v254, v250
AW a14,v254
AR v251,a15
v_sub_f32 v251, v251, v249
v_mul_f32 v251, v251, v250
AW a15,v251
AR v254,a40
v_sub_f32 v254, v254, v249
v_mul_f32 v254, v254, v250
AW a40,v254
AR v251,a41
v_sub_f32 v251, v251, v249
v_mul_f32 v251, v251, v250
AW a41,v251
AR v254,a42
v_sub_f32 v254, v254, v249
v_mul_f32 v254, v254, v250
AW a42,v254
AR v251,a43
v_sub_f32 v251, v251, v249
v_mul_f32 v251, v251, v250
AW a43,v251
AR v254,a44
v_sub_f32 v254, v254, v249
v_mul_f32 v254, v254, v250
AW a44,v254
AR v251,a45
v_sub_f32 v251, v251, v249
v_mul_f32 v251, v251, v250
AW a45,v251
AR v254,a46
v_sub_f32 v254, v254, v249
v_mul_f32 v254, v254, v250
AW a46,v254
AR v251,a47
v_sub_f32 v251, v251, v249
v_mul_f32 v251, v251, v250
AW a47,v251
AR v254,a72
v_sub_f32 v254, v254, v249
v_mul_f32 v254, v254, v250
AW a72,v254
AR v251,a73
v_sub_f32 v251, v251, v249
v_mul_f32 v251, v251, v250
AW a73,v251
AR v254,a74
v_sub_f32 v254, v254, v249
v_mul_f32 v254, v254, v250
AW a74,v254
AR v251,a75
v_sub_f32 v251, v251, v249
v_mul_f32 v251, v251, v250
AW a75,v251
AR v254,a76
v_sub_f32 v254, v254, v249
v_mul_f32 v254, v254, v250
AW a76,v254
AR v251,a77
v_sub_f32 v251, v251, v249
v_mul_f32 v251, v251, v250
AW a77,v251
AR v254,a78
v_sub_f32 v254, v254, v249
v_mul_f32 v254, v254, v250
AW a78,v254
AR v251,a79
v_sub_f32 v251, v251, v249
v_mul_f32 v251, v251, v250
AW a79,v251
AR v254,a104
v_sub_f32 v254, v254, v249
v_mul_f32 v254, v254, v250
AW a104,v254
AR v251,a105
v_sub_f32 v251, v251, v249
v_mul_f32 v251, v251, v250
AW a105,v251
AR v254,a106
v_sub_f32 v254, v254, v249
v_mul_f32 v254, v254, v250
AW a106,v254
AR v251,a107
v_sub_f32 v251, v251, v249
v_mul_f32 v251, v251, v250
AW a107,v251
AR v254,a108
v_sub_f32 v254, v254, v249
v_mul_f32 v254, v254, v250
AW a108,v254
AR v251,a109
v_sub_f32 v251, v251, v249
v_mul_f32 v251, v251, v250
AW a109,v251
AR v254,a110
v_sub_f32 v254, v254, v249
v_mul_f32 v254, v254, v250
AW a110,v254
AR v251,a111
v_sub_f32 v251, v251, v249
v_mul_f32 v251, v251, v250
AW a111,v251
v_max3_f32 v249, v16, v17, v18
v_max3_f32 v251, v48, v49, v50
v_max3_f32 v249, v249, v19, v20
v_max3_f32 v251, v251, v51, v52
v_max3_f32 v249, v249, v21, v22
v_max3_f32 v251, v251, v53, v54
v_max3_f32 v249, v249, v23, v55
v_max_f32 v249, v249, v251
v_mov_b32 v251, v249
s_nop 1
v_permlane16_swap_b32 v249, v251
v_max_f32 v249, v249, v251
v_mov_b32 v251, v249
s_nop 1
v_permlane32_swap_b32 v249, v251
v_max_f32 v249, v249, v251
v_log_f32 v250, v246
s_nop 0
v_max3_f32 v249, 0, v249, v250
v_sub_f32 v250, 0, v249
EX v250,v250
v_sub_f32 v136, v136, v249
v_sub_f32 v137, v137, v249
v_sub_f32 v138, v138, v249
v_sub_f32 v139, v139, v249
v_sub_f32 v80, v80, v249
v_sub_f32 v16, v16, v249
v_sub_f32 v81, v81, v249
)ASM"
R"ASM(
v_sub_f32 v17, v17, v249
v_sub_f32 v82, v82, v249
v_sub_f32 v18, v18, v249
v_sub_f32 v83, v83, v249
v_sub_f32 v19, v19, v249
v_sub_f32 v84, v84, v249
v_sub_f32 v20, v20, v249
v_sub_f32 v85, v85, v249
v_sub_f32 v21, v21, v249
v_sub_f32 v86, v86, v249
v_sub_f32 v22, v22, v249
v_sub_f32 v87, v87, v249
v_sub_f32 v23, v23, v249
v_sub_f32 v112, v112, v249
v_sub_f32 v48, v48, v249
v_sub_f32 v113, v113, v249
v_sub_f32 v49, v49, v249
v_sub_f32 v114, v114, v249
v_sub_f32 v50, v50, v249
v_sub_f32 v115, v115, v249
v_sub_f32 v51, v51, v249
v_sub_f32 v116, v116, v249
v_sub_f32 v52, v52, v249
v_sub_f32 v117, v117, v249
v_sub_f32 v53, v53, v249
v_sub_f32 v118, v118, v249
v_sub_f32 v54, v54, v249
v_sub_f32 v119, v119, v249
v_sub_f32 v55, v55, v249
v_mul_f32 v152, v246, v250
v_mov_b32 v153, 0
v_bfe_u32 v249, v241, 16, 8
v_lshl_or_b32 v249, v249, 1, 1
v_lshlrev_b32 v249, 22, v249
v_and_b32 v241, 0xff00ffff, v241
AR v254,a16
v_sub_f32 v254, v254, v249
v_mul_f32 v254, v254, v250
AW a16,v254
AR v251,a17
v_sub_f32 v251, v251, v249
v_mul_f32 v251, v251, v250
AW a17,v251
AR v254,a18
v_sub_f32 v254, v254, v249
v_mul_f32 v254, v254, v250
AW a18,v254
AR v251,a19
v_sub_f32 v251, v251, v249
v_mul_f32 v251, v251, v250
AW a19,v251
AR v254,a20
v_sub_f32 v254, v254, v249
v_mul_f32 v254, v254, v250
AW a20,v254
AR v251,a21
v_sub_f32 v251, v251, v249
v_mul_f32 v251, v251, v250
AW a21,v251
AR v254,a22
v_sub_f32 v254, v254, v249
v_mul_f32 v254, v254, v250
AW a22,v254
AR v251,a23
v_sub_f32 v251, v251, v249
v_mul_f32 v251, v251, v250
AW a23,v251
AR v254,a48
v_sub_f32 v254, v254, v249
v_mul_f32 v254, v254, v250
AW a48,v254
AR v251,a49
v_sub_f32 v251, v251, v249
v_mul_f32 v251, v251, v250
AW a49,v251
AR v254,a50
v_sub_f32 v254, v254, v249
v_mul_f32 v254, v254, v250
AW a50,v254
AR v251,a51
v_sub_f32 v251, v251, v249
v_mul_f32 v251, v251, v250
AW a51,v251
AR v254,a52
v_sub_f32 v254, v254, v249
v_mul_f32 v254, v254, v250
AW a52,v254
AR v251,a53
v_sub_f32 v251, v251, v249
v_mul_f32 v251, v251, v250
AW a53,v251
AR v254,a54
v_sub_f32 v254, v254, v249
v_mul_f32 v254, v254, v250
AW a54,v254
AR v251,a55
v_sub_f32 v251, v251, v249
v_mul_f32 v251, v251, v250
AW a55,v251
AR v254,a80
v_sub_f32 v254, v254, v249
v_mul_f32 v254, v254, v250
AW a80,v254
AR v251,a81
v_sub_f32 v251, v251, v249
v_mul_f32 v251, v251, v250
AW a81,v251
AR v254,a82
v_sub_f32 v254, v254, v249
v_mul_f32 v254, v254, v250
AW a82,v254
AR v251,a83
v_sub_f32 v251, v251, v249
v_mul_f32 v251, v251, v250
AW a83,v251
AR v254,a84
v_sub_f32 v254, v254, v249
v_mul_f32 v254, v254, v250
AW a84,v254
AR v251,a85
v_sub_f32 v251, v251, v249
v_mul_f32 v251, v251, v250
AW a85,v251
AR v254,a86
v_sub_f32 v254, v254, v249
v_mul_f32 v254, v254, v250
AW a86,v254
AR v251,a87
v_sub_f32 v251, v251, v249
v_mul_f32 v251, v251, v250
AW a87,v251
AR v254,a112
v_sub_f32 v254, v254, v249
v_mul_f32 v254, v254, v250
AW a112,v254
AR v251,a113
v_sub_f32 v251, v251, v249
v_mul_f32 v251, v251, v250
AW a113,v251
AR v254,a114
v_sub_f32 v254, v254, v249
v_mul_f32 v254, v254, v250
AW a114,v254
AR v251,a115
v_sub_f32 v251, v251, v249
v_mul_f32 v251, v251, v250
AW a115,v251
AR v254,a116
v_sub_f32 v254, v254, v249
v_mul_f32 v254, v254, v250
AW a116,v254
AR v251,a117
v_sub_f32 v251, v251, v249
v_mul_f32 v251, v251, v250
AW a117,v251
AR v254,a118
v_sub_f32 v254, v254, v249
v_mul_f32 v254, v254, v250
AW a118,v254
AR v251,a119
v_sub_f32 v251, v251, v249
v_mul_f32 v251, v251, v250
AW a119,v251
v_max3_f32 v249, v24, v25, v26
v_max3_f32 v251, v56, v57, v58
v_max3_f32 v249, v249, v27, v28
v_max3_f32 v251, v251, v59, v60
v_max3_f32 v249, v249, v29, v30
v_max3_f32 v251, v251, v61, v62
v_max3_f32 v249, v249, v31, v63
v_max_f32 v249, v249, v251
v_mov_b32 v251, v249
s_nop 1
v_permlane16_swap_b32 v249, v251
v_max_f32 v249, v249, v251
v_mov_b32 v251, v249
s_nop 1
v_permlane32_swap_b32 v249, v251
v_max_f32 v249, v249, v251
v_log_f32 v250, v247
s_nop 0
v_max3_f32 v249, 0, v249, v250
v_sub_f32 v250, 0, v249
EX v250,v250
v_sub_f32 v140, v140, v249
v_sub_f32 v141, v141, v249
v_sub_f32 v142, v142, v249
v_sub_f32 v143, v143, v249
v_sub_f32 v88, v88, v249
v_sub_f32 v24, v24, v249
v_sub_f32 v89, v89, v249
v_sub_f32 v25, v25, v249
v_sub_f32 v90, v90, v249
v_sub_f32 v26, v26, v249
v_sub_f32 v91, v91, v249
v_sub_f32 v27, v27, v249
v_sub_f32 v92, v92, v249
v_sub_f32 v28, v28, v249
v_sub_f32 v93, v93, v249
v_sub_f32 v29, v29, v249
v_sub_f32 v94, v94, v249
v_sub_f32 v30, v30, v249
v_sub_f32 v95, v95, v249
v_sub_f32 v31, v31, v249
v_sub_f32 v120, v120, v249
v_sub_f32 v56, v56, v249
v_sub_f32 v121, v121, v249
v_sub_f32 v57, v57, v249
v_sub_f32 v122, v122, v249
v_sub_f32 v58, v58, v249
v_sub_f32 v123, v123, v249
v_sub_f32 v59, v59, v249
v_sub_f32 v124, v124, v249
v_sub_f32 v60, v60, v249
v_sub_f32 v125, v125, v249
v_sub_f32 v61, v61, v249
v_sub_f32 v126, v126, v249
v_sub_f32 v62, v62, v249
v_sub_f32 v127, v127, v249
v_sub_f32 v63, v63, v249
v_mul_f32 v156, v247, v250
v_mov_b32 v157, 0
v_bfe_u32 v249, v241, 24, 8
v_lshl_or_b32 v249, v249, 1, 1
v_lshlrev_b32 v249, 22, v249
v_and_b32 v241, 0xffffff, v241
AR v254,a24
v_sub_f32 v254, v254, v249
v_mul_f32 v254, v254, v250
AW a24,v254
AR v251,a25
v_sub_f32 v251, v251, v249
v_mul_f32 v251, v251, v250
AW a25,v251
AR v254,a26
v_sub_f32 v254, v254, v249
v_mul_f32 v254, v254, v250
AW a26,v254
AR v251,a27
v_sub_f32 v251, v251, v249
v_mul_f32 v251, v251, v250
AW a27,v251
AR v254,a28
v_sub_f32 v254, v254, v249
v_mul_f32 v254, v254, v250
AW a28,v254
AR v251,a29
v_sub_f32 v251, v251, v249
v_mul_f32 v251, v251, v250
AW a29,v251
AR v254,a30
v_sub_f32 v254, v254, v249
v_mul_f32 v254, v254, v250
AW a30,v254
AR v251,a31
v_sub_f32 v251, v251, v249
v_mul_f32 v251, v251, v250
AW a31,v251
AR v254,a56
v_sub_f32 v254, v254, v249
v_mul_f32 v254, v254, v250
AW a56,v254
AR v251,a57
v_sub_f32 v251, v251, v249
v_mul_f32 v251, v251, v250
AW a57,v251
AR v254,a58
v_sub_f32 v254, v254, v249
v_mul_f32 v254, v254, v250
AW a58,v254
AR v251,a59
v_sub_f32 v251, v251, v249
v_mul_f32 v251, v251, v250
AW a59,v251
AR v254,a60
v_sub_f32 v254, v254, v249
v_mul_f32 v254, v254, v250
AW a60,v254
AR v251,a61
v_sub_f32 v251, v251, v249
v_mul_f32 v251, v251, v250
AW a61,v251
AR v254,a62
v_sub_f32 v254, v254, v249
v_mul_f32 v254, v254, v250
AW a62,v254
AR v251,a63
v_sub_f32 v251, v251, v249
v_mul_f32 v251, v251, v250
AW a63,v251
AR v254,a88
v_sub_f32 v254, v254, v249
v_mul_f32 v254, v254, v250
AW a88,v254
AR v251,a89
v_sub_f32 v251, v251, v249
v_mul_f32 v251, v251, v250
AW a89,v251
AR v254,a90
v_sub_f32 v254, v254, v249
v_mul_f32 v254, v254, v250
AW a90,v254
AR v251,a91
v_sub_f32 v251, v251, v249
v_mul_f32 v251, v251, v250
AW a91,v251
AR v254,a92
v_sub_f32 v254, v254, v249
v_mul_f32 v254, v254, v250
AW a92,v254
AR v251,a93
v_sub_f32 v251, v251, v249
v_mul_f32 v251, v251, v250
AW a93,v251
AR v254,a94
v_sub_f32 v254, v254, v249
v_mul_f32 v254, v254, v250
AW a94,v254
AR v251,a95
v_sub_f32 v251, v251, v249
v_mul_f32 v251, v251, v250
AW a95,v251
AR v254,a120
v_sub_f32 v254, v254, v249
v_mul_f32 v254, v254, v250
AW a120,v254
AR v251,a121
v_sub_f32 v251, v251, v249
v_mul_f32 v251, v251, v250
AW a121,v251
AR v254,a122
v_sub_f32 v254, v254, v249
v_mul_f32 v254, v254, v250
AW a122,v254
AR v251,a123
v_sub_f32 v251, v251, v249
v_mul_f32 v251, v251, v250
AW a123,v251
AR v254,a124
v_sub_f32 v254, v254, v249
v_mul_f32 v254, v254, v250
AW a124,v254
AR v251,a125
v_sub_f32 v251, v251, v249
v_mul_f32 v251, v251, v250
AW a125,v251
AR v254,a126
v_sub_f32 v254, v254, v249
v_mul_f32 v254, v254, v250
AW a126,v254
AR v251,a127
v_sub_f32 v251, v251, v249
v_mul_f32 v251, v251, v250
AW a127,v251
EX v0,v0
EX v1,v1
EX v2,v2
EX v3,v3
EX v4,v4
EX v5,v5
EX v6,v6
EX v7,v7
EX v8,v8
EX v9,v9
EX v10,v10
EX v11,v11
EX v12,v12
EX v13,v13
EX v14,v14
EX v15,v15
EX v16,v16
EX v17,v17
EX v18,v18
EX v19,v19
EX v20,v20
EX v21,v21
EX v22,v22
EX v23,v23
EX v24,v24
EX v25,v25
EX v26,v26
EX v27,v27
EX v28,v28
EX v29,v29
EX v30,v30
EX v31,v31
EX v32,v32
EX v33,v33
EX v34,v34
EX v35,v35
EX v36,v36
EX v37,v37
EX v38,v38
EX v39,v39
EX v40,v40
EX v41,v41
EX v42,v42
EX v43,v43
EX v44,v44
EX v45,v45
EX v46,v46
EX v47,v47
EX v48,v48
EX v49,v49
EX v50,v50
EX v51,v51
EX v52,v52
EX v53,v53
EX v54,v54
EX v55,v55
EX v56,v56
EX v57,v57
EX v58,v58
EX v59,v59
EX v60,v60
EX v61,v61
EX v62,v62
EX v63,v63
CV v0,v0,v1
CV v1,v2,v3
CV v2,v4,v5
CV v3,v6,v7
CV v8,v8,v9
CV v9,v10,v11
CV v10,v12,v13
CV v11,v14,v15
CV v16,v16,v17
CV v17,v18,v19
CV v18,v20,v21
CV v19,v22,v23
CV v24,v24,v25
CV v25,v26,v27
CV v26,v28,v29
CV v27,v30,v31
CV v4,v32,v33
CV v5,v34,v35
CV v6,v36,v37
CV v7,v38,v39
CV v12,v40,v41
CV v13,v42,v43
CV v14,v44,v45
CV v15,v46,v47
CV v20,v48,v49
CV v21,v50,v51
CV v22,v52,v53
CV v23,v54,v55
CV v28,v56,v57
CV v29,v58,v59
CV v30,v60,v61
CV v31,v62,v63
v_smfmac_f32_16x16x64_bf16 v[144:147], v[236:239], v[0:7], v234
v_smfmac_f32_16x16x64_bf16 v[148:151], v[236:239], v[8:15], v234
v_smfmac_f32_16x16x64_bf16 v[152:155], v[236:239], v[16:23], v234
v_smfmac_f32_16x16x64_bf16 v[156:159], v[236:239], v[24:31], v234
s_nop 9
s_branch LRET_%=
LRARE022_%=:
s_nop 15
s_waitcnt vmcnt(0) lgkmcnt(0)
v_add_u32 v249, s74, v224
RD a[192:195],v249 offset:0
RD a[208:211],v249 offset:4096
RD a[224:227],v249 offset:8192
RD a[240:243],v249 offset:12288
v_add_u32 v250, s74, v225
RD a[196:199],v250 offset:0
RD a[212:215],v250 offset:4096
RD a[228:231],v250 offset:8192
RD a[244:247],v250 offset:12288
v_add_u32 v251, s74, v226
RD a[200:203],v251 offset:0
RD a[216:219],v251 offset:4096
RD a[232:235],v251 offset:8192
RD a[248:251],v251 offset:12288
v_add_u32 v254, s74, v227
RD a[204:207],v254 offset:0
RD a[220:223],v254 offset:4096
RD a[236:239],v254 offset:8192
RD a[252:255],v254 offset:12288
s_waitcnt lgkmcnt(0)
MM v[16:19],a[192:195],v[168:171],v[136:139]
MM v[16:19],a[196:199],v[184:187],v[16:19]
MM v[16:19],a[200:203],v[200:203],v[16:19]
MM v[16:19],a[204:207],v[216:219],v[16:19]
MM v[20:23],a[220:223],v[216:219],v[136:139]
MM v[20:23],a[216:219],v[200:203],v[20:23]
MM v[20:23],a[212:215],v[184:187],v[20:23]
MM v[20:23],a[208:211],v[168:171],v[20:23]
MM v[28:31],a[208:211],v[172:175],v[140:143]
MM v[28:31],a[212:215],v[188:191],v[28:31]
MM v[28:31],a[216:219],v[204:207],v[28:31]
MM v[28:31],a[220:223],v[220:223],v[28:31]
MM v[24:27],a[204:207],v[220:223],v[140:143]
MM v[24:27],a[200:203],v[204:207],v[24:27]
MM v[24:27],a[196:199],v[188:191],v[24:27]
MM v[24:27],a[192:195],v[172:175],v[24:27]
MM v[48:51],a[224:227],v[168:171],v[136:139]
MM v[48:51],a[228:231],v[184:187],v[48:51]
MM v[48:51],a[232:235],v[200:203],v[48:51]
MM v[48:51],a[236:239],v[216:219],v[48:51]
MM v[52:55],a[252:255],v[216:219],v[136:139]
MM v[52:55],a[248:251],v[200:203],v[52:55]
MM v[52:55],a[244:247],v[184:187],v[52:55]
MM v[52:55],a[240:243],v[168:171],v[52:55]
MM v[60:63],a[240:243],v[172:175],v[140:143]
MM v[60:63],a[244:247],v[188:191],v[60:63]
MM v[60:63],a[248:251],v[204:207],v[60:63]
MM v[60:63],a[252:255],v[220:223],v[60:63]
MM v[56:59],a[236:239],v[220:223],v[140:143]
MM v[56:59],a[232:235],v[204:207],v[56:59]
MM v[56:59],a[228:231],v[188:191],v[56:59]
MM v[56:59],a[224:227],v[172:175],v[56:59]
s_nop 15
v_add_u32 v249, s75, v224
RD a[192:195],v249 offset:0
RD a[208:211],v249 offset:4096
RD a[224:227],v249 offset:8192
RD a[240:243],v249 offset:12288
v_add_u32 v250, s75, v225
RD a[196:199],v250 offset:0
RD a[212:215],v250 offset:4096
RD a[228:231],v250 offset:8192
RD a[244:247],v250 offset:12288
v_add_u32 v251, s75, v226
RD a[200:203],v251 offset:0
RD a[216:219],v251 offset:4096
RD a[232:235],v251 offset:8192
RD a[248:251],v251 offset:12288
v_add_u32 v254, s75, v227
RD a[204:207],v254 offset:0
RD a[220:223],v254 offset:4096
RD a[236:239],v254 offset:8192
RD a[252:255],v254 offset:12288
s_waitcnt lgkmcnt(0)
v_max3_f32 v249, v16, v17, v18
v_max3_f32 v251, v48, v49, v50
v_max3_f32 v249, v249, v19, v20
v_max3_f32 v251, v251, v51, v52
v_max3_f32 v249, v249, v21, v22
v_max3_f32 v251, v251, v53, v54
v_max3_f32 v249, v249, v23, v55
v_max_f32 v249, v249, v251
v_mov_b32 v251, v249
s_nop 1
v_permlane16_swap_b32 v249, v251
v_max_f32 v249, v249, v251
v_mov_b32 v251, v249
)ASM"
R"ASM(
s_nop 1
v_permlane32_swap_b32 v249, v251
v_max_f32 v249, v249, v251
v_log_f32 v250, v246
s_nop 0
v_max3_f32 v249, 0, v249, v250
v_sub_f32 v250, 0, v249
EX v250,v250
v_sub_f32 v136, v136, v249
v_sub_f32 v137, v137, v249
v_sub_f32 v138, v138, v249
v_sub_f32 v139, v139, v249
v_sub_f32 v80, v80, v249
v_sub_f32 v16, v16, v249
v_sub_f32 v81, v81, v249
v_sub_f32 v17, v17, v249
v_sub_f32 v82, v82, v249
v_sub_f32 v18, v18, v249
v_sub_f32 v83, v83, v249
v_sub_f32 v19, v19, v249
v_sub_f32 v84, v84, v249
v_sub_f32 v20, v20, v249
v_sub_f32 v85, v85, v249
v_sub_f32 v21, v21, v249
v_sub_f32 v86, v86, v249
v_sub_f32 v22, v22, v249
v_sub_f32 v87, v87, v249
v_sub_f32 v23, v23, v249
v_sub_f32 v112, v112, v249
v_sub_f32 v48, v48, v249
v_sub_f32 v113, v113, v249
v_sub_f32 v49, v49, v249
v_sub_f32 v114, v114, v249
v_sub_f32 v50, v50, v249
v_sub_f32 v115, v115, v249
v_sub_f32 v51, v51, v249
v_sub_f32 v116, v116, v249
v_sub_f32 v52, v52, v249
v_sub_f32 v117, v117, v249
v_sub_f32 v53, v53, v249
v_sub_f32 v118, v118, v249
v_sub_f32 v54, v54, v249
v_sub_f32 v119, v119, v249
v_sub_f32 v55, v55, v249
v_mul_f32 v152, v246, v250
v_mov_b32 v153, 0
v_bfe_u32 v249, v241, 16, 8
v_lshl_or_b32 v249, v249, 1, 1
v_lshlrev_b32 v249, 22, v249
v_and_b32 v241, 0xff00ffff, v241
AR v254,a16
v_sub_f32 v254, v254, v249
v_mul_f32 v254, v254, v250
AW a16,v254
AR v251,a17
v_sub_f32 v251, v251, v249
v_mul_f32 v251, v251, v250
AW a17,v251
AR v254,a18
v_sub_f32 v254, v254, v249
v_mul_f32 v254, v254, v250
AW a18,v254
AR v251,a19
v_sub_f32 v251, v251, v249
v_mul_f32 v251, v251, v250
AW a19,v251
AR v254,a20
v_sub_f32 v254, v254, v249
v_mul_f32 v254, v254, v250
AW a20,v254
AR v251,a21
v_sub_f32 v251, v251, v249
v_mul_f32 v251, v251, v250
AW a21,v251
AR v254,a22
v_sub_f32 v254, v254, v249
v_mul_f32 v254, v254, v250
AW a22,v254
AR v251,a23
v_sub_f32 v251, v251, v249
v_mul_f32 v251, v251, v250
AW a23,v251
AR v254,a48
v_sub_f32 v254, v254, v249
v_mul_f32 v254, v254, v250
AW a48,v254
AR v251,a49
v_sub_f32 v251, v251, v249
v_mul_f32 v251, v251, v250
AW a49,v251
AR v254,a50
v_sub_f32 v254, v254, v249
v_mul_f32 v254, v254, v250
AW a50,v254
AR v251,a51
v_sub_f32 v251, v251, v249
v_mul_f32 v251, v251, v250
AW a51,v251
AR v254,a52
v_sub_f32 v254, v254, v249
v_mul_f32 v254, v254, v250
AW a52,v254
AR v251,a53
v_sub_f32 v251, v251, v249
v_mul_f32 v251, v251, v250
AW a53,v251
AR v254,a54
v_sub_f32 v254, v254, v249
v_mul_f32 v254, v254, v250
AW a54,v254
AR v251,a55
v_sub_f32 v251, v251, v249
v_mul_f32 v251, v251, v250
AW a55,v251
AR v254,a80
v_sub_f32 v254, v254, v249
v_mul_f32 v254, v254, v250
AW a80,v254
AR v251,a81
v_sub_f32 v251, v251, v249
v_mul_f32 v251, v251, v250
AW a81,v251
AR v254,a82
v_sub_f32 v254, v254, v249
v_mul_f32 v254, v254, v250
AW a82,v254
AR v251,a83
v_sub_f32 v251, v251, v249
v_mul_f32 v251, v251, v250
AW a83,v251
AR v254,a84
v_sub_f32 v254, v254, v249
v_mul_f32 v254, v254, v250
AW a84,v254
AR v251,a85
v_sub_f32 v251, v251, v249
v_mul_f32 v251, v251, v250
AW a85,v251
AR v254,a86
v_sub_f32 v254, v254, v249
v_mul_f32 v254, v254, v250
AW a86,v254
AR v251,a87
v_sub_f32 v251, v251, v249
v_mul_f32 v251, v251, v250
AW a87,v251
AR v254,a112
v_sub_f32 v254, v254, v249
v_mul_f32 v254, v254, v250
AW a112,v254
AR v251,a113
v_sub_f32 v251, v251, v249
v_mul_f32 v251, v251, v250
AW a113,v251
AR v254,a114
v_sub_f32 v254, v254, v249
v_mul_f32 v254, v254, v250
AW a114,v254
AR v251,a115
v_sub_f32 v251, v251, v249
v_mul_f32 v251, v251, v250
AW a115,v251
AR v254,a116
v_sub_f32 v254, v254, v249
v_mul_f32 v254, v254, v250
AW a116,v254
AR v251,a117
v_sub_f32 v251, v251, v249
v_mul_f32 v251, v251, v250
AW a117,v251
AR v254,a118
v_sub_f32 v254, v254, v249
v_mul_f32 v254, v254, v250
AW a118,v254
AR v251,a119
v_sub_f32 v251, v251, v249
v_mul_f32 v251, v251, v250
AW a119,v251
v_max3_f32 v249, v24, v25, v26
v_max3_f32 v251, v56, v57, v58
v_max3_f32 v249, v249, v27, v28
v_max3_f32 v251, v251, v59, v60
v_max3_f32 v249, v249, v29, v30
v_max3_f32 v251, v251, v61, v62
v_max3_f32 v249, v249, v31, v63
v_max_f32 v249, v249, v251
v_mov_b32 v251, v249
s_nop 1
v_permlane16_swap_b32 v249, v251
v_max_f32 v249, v249, v251
v_mov_b32 v251, v249
s_nop 1
v_permlane32_swap_b32 v249, v251
v_max_f32 v249, v249, v251
v_log_f32 v250, v247
s_nop 0
v_max3_f32 v249, 0, v249, v250
v_sub_f32 v250, 0, v249
EX v250,v250
v_sub_f32 v140, v140, v249
v_sub_f32 v141, v141, v249
v_sub_f32 v142, v142, v249
v_sub_f32 v143, v143, v249
v_sub_f32 v88, v88, v249
v_sub_f32 v24, v24, v249
v_sub_f32 v89, v89, v249
v_sub_f32 v25, v25, v249
v_sub_f32 v90, v90, v249
v_sub_f32 v26, v26, v249
v_sub_f32 v91, v91, v249
v_sub_f32 v27, v27, v249
v_sub_f32 v92, v92, v249
v_sub_f32 v28, v28, v249
v_sub_f32 v93, v93, v249
v_sub_f32 v29, v29, v249
v_sub_f32 v94, v94, v249
v_sub_f32 v30, v30, v249
v_sub_f32 v95, v95, v249
v_sub_f32 v31, v31, v249
v_sub_f32 v120, v120, v249
v_sub_f32 v56, v56, v249
v_sub_f32 v121, v121, v249
v_sub_f32 v57, v57, v249
v_sub_f32 v122, v122, v249
v_sub_f32 v58, v58, v249
v_sub_f32 v123, v123, v249
v_sub_f32 v59, v59, v249
v_sub_f32 v124, v124, v249
v_sub_f32 v60, v60, v249
v_sub_f32 v125, v125, v249
v_sub_f32 v61, v61, v249
v_sub_f32 v126, v126, v249
v_sub_f32 v62, v62, v249
v_sub_f32 v127, v127, v249
v_sub_f32 v63, v63, v249
v_mul_f32 v156, v247, v250
v_mov_b32 v157, 0
v_bfe_u32 v249, v241, 24, 8
v_lshl_or_b32 v249, v249, 1, 1
v_lshlrev_b32 v249, 22, v249
v_and_b32 v241, 0xffffff, v241
AR v254,a24
v_sub_f32 v254, v254, v249
v_mul_f32 v254, v254, v250
AW a24,v254
AR v251,a25
v_sub_f32 v251, v251, v249
v_mul_f32 v251, v251, v250
AW a25,v251
AR v254,a26
v_sub_f32 v254, v254, v249
v_mul_f32 v254, v254, v250
AW a26,v254
AR v251,a27
v_sub_f32 v251, v251, v249
v_mul_f32 v251, v251, v250
AW a27,v251
AR v254,a28
v_sub_f32 v254, v254, v249
v_mul_f32 v254, v254, v250
AW a28,v254
AR v251,a29
v_sub_f32 v251, v251, v249
v_mul_f32 v251, v251, v250
AW a29,v251
AR v254,a30
v_sub_f32 v254, v254, v249
v_mul_f32 v254, v254, v250
AW a30,v254
AR v251,a31
v_sub_f32 v251, v251, v249
v_mul_f32 v251, v251, v250
AW a31,v251
AR v254,a56
v_sub_f32 v254, v254, v249
v_mul_f32 v254, v254, v250
AW a56,v254
AR v251,a57
v_sub_f32 v251, v251, v249
v_mul_f32 v251, v251, v250
AW a57,v251
AR v254,a58
v_sub_f32 v254, v254, v249
v_mul_f32 v254, v254, v250
AW a58,v254
AR v251,a59
v_sub_f32 v251, v251, v249
v_mul_f32 v251, v251, v250
AW a59,v251
AR v254,a60
v_sub_f32 v254, v254, v249
v_mul_f32 v254, v254, v250
AW a60,v254
AR v251,a61
v_sub_f32 v251, v251, v249
v_mul_f32 v251, v251, v250
AW a61,v251
AR v254,a62
v_sub_f32 v254, v254, v249
v_mul_f32 v254, v254, v250
AW a62,v254
AR v251,a63
v_sub_f32 v251, v251, v249
v_mul_f32 v251, v251, v250
AW a63,v251
AR v254,a88
v_sub_f32 v254, v254, v249
v_mul_f32 v254, v254, v250
AW a88,v254
AR v251,a89
v_sub_f32 v251, v251, v249
v_mul_f32 v251, v251, v250
AW a89,v251
AR v254,a90
v_sub_f32 v254, v254, v249
v_mul_f32 v254, v254, v250
AW a90,v254
AR v251,a91
v_sub_f32 v251, v251, v249
v_mul_f32 v251, v251, v250
AW a91,v251
AR v254,a92
v_sub_f32 v254, v254, v249
v_mul_f32 v254, v254, v250
AW a92,v254
AR v251,a93
v_sub_f32 v251, v251, v249
v_mul_f32 v251, v251, v250
AW a93,v251
AR v254,a94
v_sub_f32 v254, v254, v249
v_mul_f32 v254, v254, v250
AW a94,v254
AR v251,a95
v_sub_f32 v251, v251, v249
v_mul_f32 v251, v251, v250
AW a95,v251
AR v254,a120
v_sub_f32 v254, v254, v249
v_mul_f32 v254, v254, v250
AW a120,v254
AR v251,a121
v_sub_f32 v251, v251, v249
v_mul_f32 v251, v251, v250
AW a121,v251
AR v254,a122
v_sub_f32 v254, v254, v249
v_mul_f32 v254, v254, v250
AW a122,v254
AR v251,a123
v_sub_f32 v251, v251, v249
v_mul_f32 v251, v251, v250
AW a123,v251
AR v254,a124
v_sub_f32 v254, v254, v249
v_mul_f32 v254, v254, v250
AW a124,v254
AR v251,a125
v_sub_f32 v251, v251, v249
v_mul_f32 v251, v251, v250
AW a125,v251
AR v254,a126
v_sub_f32 v254, v254, v249
v_mul_f32 v254, v254, v250
AW a126,v254
AR v251,a127
v_sub_f32 v251, v251, v249
v_mul_f32 v251, v251, v250
AW a127,v251
EX v16,v16
EX v17,v17
EX v18,v18
EX v19,v19
EX v20,v20
EX v21,v21
EX v22,v22
EX v23,v23
EX v24,v24
EX v25,v25
EX v26,v26
EX v27,v27
EX v28,v28
EX v29,v29
EX v30,v30
EX v31,v31
EX v48,v48
EX v49,v49
EX v50,v50
EX v51,v51
EX v52,v52
EX v53,v53
EX v54,v54
EX v55,v55
EX v56,v56
EX v57,v57
EX v58,v58
EX v59,v59
EX v60,v60
EX v61,v61
EX v62,v62
EX v63,v63
CV v16,v16,v17
CV v17,v18,v19
CV v18,v20,v21
CV v19,v22,v23
CV v24,v24,v25
CV v25,v26,v27
CV v26,v28,v29
CV v27,v30,v31
CV v20,v48,v49
CV v21,v50,v51
CV v22,v52,v53
CV v23,v54,v55
CV v28,v56,v57
CV v29,v58,v59
CV v30,v60,v61
CV v31,v62,v63
v_smfmac_f32_16x16x64_bf16 v[152:155], v[236:239], v[16:23], v234
s_nop 0
v_smfmac_f32_16x16x64_bf16 v[156:159], v[236:239], v[24:31], v234
s_nop 9
s_branch LRET_%=
LRARE122_%=:
s_nop 15
s_waitcnt vmcnt(0) lgkmcnt(0)
v_add_u32 v249, s74, v224
RD a[192:195],v249 offset:0
RD a[208:211],v249 offset:4096
RD a[224:227],v249 offset:8192
RD a[240:243],v249 offset:12288
v_add_u32 v250, s74, v225
RD a[196:199],v250 offset:0
RD a[212:215],v250 offset:4096
RD a[228:231],v250 offset:8192
RD a[244:247],v250 offset:12288
v_add_u32 v251, s74, v226
RD a[200:203],v251 offset:0
RD a[216:219],v251 offset:4096
RD a[232:235],v251 offset:8192
RD a[248:251],v251 offset:12288
v_add_u32 v254, s74, v227
RD a[204:207],v254 offset:0
RD a[220:223],v254 offset:4096
RD a[236:239],v254 offset:8192
RD a[252:255],v254 offset:12288
s_waitcnt lgkmcnt(0)
MM v[80:83],a[192:195],v[168:171],v[136:139]
MM v[80:83],a[196:199],v[184:187],v[80:83]
MM v[80:83],a[200:203],v[200:203],v[80:83]
MM v[80:83],a[204:207],v[216:219],v[80:83]
MM v[84:87],a[220:223],v[216:219],v[136:139]
MM v[84:87],a[216:219],v[200:203],v[84:87]
MM v[84:87],a[212:215],v[184:187],v[84:87]
MM v[84:87],a[208:211],v[168:171],v[84:87]
MM v[92:95],a[208:211],v[172:175],v[140:143]
MM v[92:95],a[212:215],v[188:191],v[92:95]
MM v[92:95],a[216:219],v[204:207],v[92:95]
MM v[92:95],a[220:223],v[220:223],v[92:95]
MM v[88:91],a[204:207],v[220:223],v[140:143]
MM v[88:91],a[200:203],v[204:207],v[88:91]
MM v[88:91],a[196:199],v[188:191],v[88:91]
MM v[88:91],a[192:195],v[172:175],v[88:91]
MM v[112:115],a[224:227],v[168:171],v[136:139]
MM v[112:115],a[228:231],v[184:187],v[112:115]
MM v[112:115],a[232:235],v[200:203],v[112:115]
MM v[112:115],a[236:239],v[216:219],v[112:115]
MM v[116:119],a[252:255],v[216:219],v[136:139]
MM v[116:119],a[248:251],v[200:203],v[116:119]
MM v[116:119],a[244:247],v[184:187],v[116:119]
MM v[116:119],a[240:243],v[168:171],v[116:119]
MM v[124:127],a[240:243],v[172:175],v[140:143]
MM v[124:127],a[244:247],v[188:191],v[124:127]
MM v[124:127],a[248:251],v[204:207],v[124:127]
MM v[124:127],a[252:255],v[220:223],v[124:127]
MM v[120:123],a[236:239],v[220:223],v[140:143]
MM v[120:123],a[232:235],v[204:207],v[120:123]
MM v[120:123],a[228:231],v[188:191],v[120:123]
MM v[120:123],a[224:227],v[172:175],v[120:123]
s_nop 15
v_add_u32 v249, s75, v224
RD a[192:195],v249 offset:0
RD a[208:211],v249 offset:4096
RD a[224:227],v249 offset:8192
RD a[240:243],v249 offset:12288
v_add_u32 v250, s75, v225
RD a[196:199],v250 offset:0
RD a[212:215],v250 offset:4096
RD a[228:231],v250 offset:8192
RD a[244:247],v250 offset:12288
v_add_u32 v251, s75, v226
RD a[200:203],v251 offset:0
RD a[216:219],v251 offset:4096
RD a[232:235],v251 offset:8192
RD a[248:251],v251 offset:12288
v_add_u32 v254, s75, v227
RD a[204:207],v254 offset:0
RD a[220:223],v254 offset:4096
RD a[236:239],v254 offset:8192
RD a[252:255],v254 offset:12288
s_waitcnt lgkmcnt(0)
v_max3_f32 v249, v80, v81, v82
v_max3_f32 v251, v112, v113, v114
v_max3_f32 v249, v249, v83, v84
v_max3_f32 v251, v251, v115, v116
v_max3_f32 v249, v249, v85, v86
v_max3_f32 v251, v251, v117, v118
v_max3_f32 v249, v249, v87, v119
v_max_f32 v249, v249, v251
v_mov_b32 v251, v249
s_nop 1
v_permlane16_swap_b32 v249, v251
v_max_f32 v249, v249, v251
v_mov_b32 v251, v249
s_nop 1
v_permlane32_swap_b32 v249, v251
v_max_f32 v249, v249, v251
v_log_f32 v250, v246
s_nop 0
v_max3_f32 v249, 0, v249, v250
v_sub_f32 v250, 0, v249
EX v250,v250
v_sub_f32 v136, v136, v249
v_sub_f32 v137, v137, v249
v_sub_f32 v138, v138, v249
v_sub_f32 v139, v139, v249
)ASM"
R"ASM(
v_sub_f32 v16, v16, v249
v_sub_f32 v80, v80, v249
v_sub_f32 v17, v17, v249
v_sub_f32 v81, v81, v249
v_sub_f32 v18, v18, v249
v_sub_f32 v82, v82, v249
v_sub_f32 v19, v19, v249
v_sub_f32 v83, v83, v249
v_sub_f32 v20, v20, v249
v_sub_f32 v84, v84, v249
v_sub_f32 v21, v21, v249
v_sub_f32 v85, v85, v249
v_sub_f32 v22, v22, v249
v_sub_f32 v86, v86, v249
v_sub_f32 v23, v23, v249
v_sub_f32 v87, v87, v249
v_sub_f32 v48, v48, v249
v_sub_f32 v112, v112, v249
v_sub_f32 v49, v49, v249
v_sub_f32 v113, v113, v249
v_sub_f32 v50, v50, v249
v_sub_f32 v114, v114, v249
v_sub_f32 v51, v51, v249
v_sub_f32 v115, v115, v249
v_sub_f32 v52, v52, v249
v_sub_f32 v116, v116, v249
v_sub_f32 v53, v53, v249
v_sub_f32 v117, v117, v249
v_sub_f32 v54, v54, v249
v_sub_f32 v118, v118, v249
v_sub_f32 v55, v55, v249
v_sub_f32 v119, v119, v249
v_mul_f32 v152, v246, v250
v_mov_b32 v153, 0
v_bfe_u32 v249, v241, 16, 8
v_lshl_or_b32 v249, v249, 1, 1
v_lshlrev_b32 v249, 22, v249
v_and_b32 v241, 0xff00ffff, v241
AR v254,a16
v_sub_f32 v254, v254, v249
v_mul_f32 v254, v254, v250
AW a16,v254
AR v251,a17
v_sub_f32 v251, v251, v249
v_mul_f32 v251, v251, v250
AW a17,v251
AR v254,a18
v_sub_f32 v254, v254, v249
v_mul_f32 v254, v254, v250
AW a18,v254
AR v251,a19
v_sub_f32 v251, v251, v249
v_mul_f32 v251, v251, v250
AW a19,v251
AR v254,a20
v_sub_f32 v254, v254, v249
v_mul_f32 v254, v254, v250
AW a20,v254
AR v251,a21
v_sub_f32 v251, v251, v249
v_mul_f32 v251, v251, v250
AW a21,v251
AR v254,a22
v_sub_f32 v254, v254, v249
v_mul_f32 v254, v254, v250
AW a22,v254
AR v251,a23
v_sub_f32 v251, v251, v249
v_mul_f32 v251, v251, v250
AW a23,v251
AR v254,a48
v_sub_f32 v254, v254, v249
v_mul_f32 v254, v254, v250
AW a48,v254
AR v251,a49
v_sub_f32 v251, v251, v249
v_mul_f32 v251, v251, v250
AW a49,v251
AR v254,a50
v_sub_f32 v254, v254, v249
v_mul_f32 v254, v254, v250
AW a50,v254
AR v251,a51
v_sub_f32 v251, v251, v249
v_mul_f32 v251, v251, v250
AW a51,v251
AR v254,a52
v_sub_f32 v254, v254, v249
v_mul_f32 v254, v254, v250
AW a52,v254
AR v251,a53
v_sub_f32 v251, v251, v249
v_mul_f32 v251, v251, v250
AW a53,v251
AR v254,a54
v_sub_f32 v254, v254, v249
v_mul_f32 v254, v254, v250
AW a54,v254
AR v251,a55
v_sub_f32 v251, v251, v249
v_mul_f32 v251, v251, v250
AW a55,v251
AR v254,a80
v_sub_f32 v254, v254, v249
v_mul_f32 v254, v254, v250
AW a80,v254
AR v251,a81
v_sub_f32 v251, v251, v249
v_mul_f32 v251, v251, v250
AW a81,v251
AR v254,a82
v_sub_f32 v254, v254, v249
v_mul_f32 v254, v254, v250
AW a82,v254
AR v251,a83
v_sub_f32 v251, v251, v249
v_mul_f32 v251, v251, v250
AW a83,v251
AR v254,a84
v_sub_f32 v254, v254, v249
v_mul_f32 v254, v254, v250
AW a84,v254
AR v251,a85
v_sub_f32 v251, v251, v249
v_mul_f32 v251, v251, v250
AW a85,v251
AR v254,a86
v_sub_f32 v254, v254, v249
v_mul_f32 v254, v254, v250
AW a86,v254
AR v251,a87
v_sub_f32 v251, v251, v249
v_mul_f32 v251, v251, v250
AW a87,v251
AR v254,a112
v_sub_f32 v254, v254, v249
v_mul_f32 v254, v254, v250
AW a112,v254
AR v251,a113
v_sub_f32 v251, v251, v249
v_mul_f32 v251, v251, v250
AW a113,v251
AR v254,a114
v_sub_f32 v254, v254, v249
v_mul_f32 v254, v254, v250
AW a114,v254
AR v251,a115
v_sub_f32 v251, v251, v249
v_mul_f32 v251, v251, v250
AW a115,v251
AR v254,a116
v_sub_f32 v254, v254, v249
v_mul_f32 v254, v254, v250
AW a116,v254
AR v251,a117
v_sub_f32 v251, v251, v249
v_mul_f32 v251, v251, v250
AW a117,v251
AR v254,a118
v_sub_f32 v254, v254, v249
v_mul_f32 v254, v254, v250
AW a118,v254
AR v251,a119
v_sub_f32 v251, v251, v249
v_mul_f32 v251, v251, v250
AW a119,v251
v_max3_f32 v249, v88, v89, v90
v_max3_f32 v251, v120, v121, v122
v_max3_f32 v249, v249, v91, v92
v_max3_f32 v251, v251, v123, v124
v_max3_f32 v249, v249, v93, v94
v_max3_f32 v251, v251, v125, v126
v_max3_f32 v249, v249, v95, v127
v_max_f32 v249, v249, v251
v_mov_b32 v251, v249
s_nop 1
v_permlane16_swap_b32 v249, v251
v_max_f32 v249, v249, v251
v_mov_b32 v251, v249
s_nop 1
v_permlane32_swap_b32 v249, v251
v_max_f32 v249, v249, v251
v_log_f32 v250, v247
s_nop 0
v_max3_f32 v249, 0, v249, v250
v_sub_f32 v250, 0, v249
EX v250,v250
v_sub_f32 v140, v140, v249
v_sub_f32 v141, v141, v249
v_sub_f32 v142, v142, v249
v_sub_f32 v143, v143, v249
v_sub_f32 v24, v24, v249
v_sub_f32 v88, v88, v249
v_sub_f32 v25, v25, v249
v_sub_f32 v89, v89, v249
v_sub_f32 v26, v26, v249
v_sub_f32 v90, v90, v249
v_sub_f32 v27, v27, v249
v_sub_f32 v91, v91, v249
v_sub_f32 v28, v28, v249
v_sub_f32 v92, v92, v249
v_sub_f32 v29, v29, v249
v_sub_f32 v93, v93, v249
v_sub_f32 v30, v30, v249
v_sub_f32 v94, v94, v249
v_sub_f32 v31, v31, v249
v_sub_f32 v95, v95, v249
v_sub_f32 v56, v56, v249
v_sub_f32 v120, v120, v249
v_sub_f32 v57, v57, v249
v_sub_f32 v121, v121, v249
v_sub_f32 v58, v58, v249
v_sub_f32 v122, v122, v249
v_sub_f32 v59, v59, v249
v_sub_f32 v123, v123, v249
v_sub_f32 v60, v60, v249
v_sub_f32 v124, v124, v249
v_sub_f32 v61, v61, v249
v_sub_f32 v125, v125, v249
v_sub_f32 v62, v62, v249
v_sub_f32 v126, v126, v249
v_sub_f32 v63, v63, v249
v_sub_f32 v127, v127, v249
v_mul_f32 v156, v247, v250
v_mov_b32 v157, 0
v_bfe_u32 v249, v241, 24, 8
v_lshl_or_b32 v249, v249, 1, 1
v_lshlrev_b32 v249, 22, v249
v_and_b32 v241, 0xffffff, v241
AR v254,a24
v_sub_f32 v254, v254, v249
v_mul_f32 v254, v254, v250
AW a24,v254
AR v251,a25
v_sub_f32 v251, v251, v249
v_mul_f32 v251, v251, v250
AW a25,v251
AR v254,a26
v_sub_f32 v254, v254, v249
v_mul_f32 v254, v254, v250
AW a26,v254
AR v251,a27
v_sub_f32 v251, v251, v249
v_mul_f32 v251, v251, v250
AW a27,v251
AR v254,a28
v_sub_f32 v254, v254, v249
v_mul_f32 v254, v254, v250
AW a28,v254
AR v251,a29
v_sub_f32 v251, v251, v249
v_mul_f32 v251, v251, v250
AW a29,v251
AR v254,a30
v_sub_f32 v254, v254, v249
v_mul_f32 v254, v254, v250
AW a30,v254
AR v251,a31
v_sub_f32 v251, v251, v249
v_mul_f32 v251, v251, v250
AW a31,v251
AR v254,a56
v_sub_f32 v254, v254, v249
v_mul_f32 v254, v254, v250
AW a56,v254
AR v251,a57
v_sub_f32 v251, v251, v249
v_mul_f32 v251, v251, v250
AW a57,v251
AR v254,a58
v_sub_f32 v254, v254, v249
v_mul_f32 v254, v254, v250
AW a58,v254
AR v251,a59
v_sub_f32 v251, v251, v249
v_mul_f32 v251, v251, v250
AW a59,v251
AR v254,a60
v_sub_f32 v254, v254, v249
v_mul_f32 v254, v254, v250
AW a60,v254
AR v251,a61
v_sub_f32 v251, v251, v249
v_mul_f32 v251, v251, v250
AW a61,v251
AR v254,a62
v_sub_f32 v254, v254, v249
v_mul_f32 v254, v254, v250
AW a62,v254
AR v251,a63
v_sub_f32 v251, v251, v249
v_mul_f32 v251, v251, v250
AW a63,v251
AR v254,a88
v_sub_f32 v254, v254, v249
v_mul_f32 v254, v254, v250
AW a88,v254
AR v251,a89
v_sub_f32 v251, v251, v249
v_mul_f32 v251, v251, v250
AW a89,v251
AR v254,a90
v_sub_f32 v254, v254, v249
v_mul_f32 v254, v254, v250
AW a90,v254
AR v251,a91
v_sub_f32 v251, v251, v249
v_mul_f32 v251, v251, v250
AW a91,v251
AR v254,a92
v_sub_f32 v254, v254, v249
v_mul_f32 v254, v254, v250
AW a92,v254
AR v251,a93
v_sub_f32 v251, v251, v249
v_mul_f32 v251, v251, v250
AW a93,v251
AR v254,a94
v_sub_f32 v254, v254, v249
v_mul_f32 v254, v254, v250
AW a94,v254
AR v251,a95
v_sub_f32 v251, v251, v249
v_mul_f32 v251, v251, v250
AW a95,v251
AR v254,a120
v_sub_f32 v254, v254, v249
v_mul_f32 v254, v254, v250
AW a120,v254
AR v251,a121
v_sub_f32 v251, v251, v249
v_mul_f32 v251, v251, v250
AW a121,v251
AR v254,a122
v_sub_f32 v254, v254, v249
v_mul_f32 v254, v254, v250
AW a122,v254
AR v251,a123
v_sub_f32 v251, v251, v249
v_mul_f32 v251, v251, v250
AW a123,v251
AR v254,a124
v_sub_f32 v254, v254, v249
v_mul_f32 v254, v254, v250
AW a124,v254
AR v251,a125
v_sub_f32 v251, v251, v249
v_mul_f32 v251, v251, v250
AW a125,v251
AR v254,a126
v_sub_f32 v254, v254, v249
v_mul_f32 v254, v254, v250
AW a126,v254
AR v251,a127
v_sub_f32 v251, v251, v249
v_mul_f32 v251, v251, v250
AW a127,v251
EX v80,v80
EX v81,v81
EX v82,v82
EX v83,v83
EX v84,v84
EX v85,v85
EX v86,v86
EX v87,v87
EX v88,v88
EX v89,v89
EX v90,v90
EX v91,v91
EX v92,v92
EX v93,v93
EX v94,v94
EX v95,v95
EX v112,v112
EX v113,v113
EX v114,v114
EX v115,v115
EX v116,v116
EX v117,v117
EX v118,v118
EX v119,v119
EX v120,v120
EX v121,v121
EX v122,v122
EX v123,v123
EX v124,v124
EX v125,v125
EX v126,v126
EX v127,v127
CV v80,v80,v81
CV v81,v82,v83
CV v82,v84,v85
CV v83,v86,v87
CV v88,v88,v89
CV v89,v90,v91
CV v90,v92,v93
CV v91,v94,v95
CV v84,v112,v113
CV v85,v114,v115
CV v86,v116,v117
CV v87,v118,v119
CV v92,v120,v121
CV v93,v122,v123
CV v94,v124,v125
CV v95,v126,v127
v_smfmac_f32_16x16x64_bf16 v[152:155], v[236:239], v[80:87], v234
s_nop 0
v_smfmac_f32_16x16x64_bf16 v[156:159], v[236:239], v[88:95], v234
s_nop 9
s_branch LRET_%=
LEND_%=:
)ASM"

    :: [ka] "s"(__builtin_amdgcn_kernarg_segment_ptr()), [slds] "s"(slds), [swave] "s"(w), [sb] "s"(blockIdx.x)
    : "memory", "vcc", "scc", CLOB_V, CLOB_A, CLOB_S);
}
constexpr int DYN_LDS = 163840;

extern "C" void kernel_launch(void* const* d_in, const int* in_sizes, int n_in, void* d_out, int out_size, void* d_ws, size_t ws_size, hipStream_t stream) {
#if !defined(__HIP_DEVICE_COMPILE__)
  if (DYN_LDS) hipFuncSetAttribute((const void*)ka_fused, hipFuncAttributeMaxDynamicSharedMemorySize, DYN_LDS);
  ka_fused<<<256, 256, DYN_LDS, stream>>>(d_in[3], d_in[0], d_ws, d_in[1], d_in[6], d_in[2], d_in[4], d_in[7], d_in[8], d_in[9], d_out);
#endif
}
```

```cpp
#include <hip/hip_runtime.h>
#include <stdint.h>

using f32x4  = __attribute__((ext_vector_type(4))) float;
using u32x4  = __attribute__((ext_vector_type(4))) unsigned;
using u32x2  = __attribute__((ext_vector_type(2))) unsigned;

constexpr int SEQ = 2048, DM = 640, NH = 32, DH = 128, HD = NH * DH, FF = 1728, TOPC = 8;

constexpr unsigned WS_FO  = 0;
constexpr unsigned WS_EP  = 32768;
constexpr unsigned WS_G1  = 36864;
constexpr unsigned WS_G2  = 53248;
constexpr unsigned WS_FH  = 69632;
constexpr unsigned WS_FY  = 73728;
constexpr unsigned WS_FG  = 77824;
constexpr unsigned WS_FR  = 81920;
constexpr unsigned WS_RK  = 86016;
constexpr unsigned WS_XN  = 131072;
constexpr unsigned WS_OF  = WS_XN + SEQ * DM * 2;
constexpr unsigned WS_H   = WS_OF + (18u << 20);
constexpr unsigned WS_Y   = WS_H + 32768;
constexpr unsigned WS_P   = WS_Y + 1048576;
constexpr unsigned WS_SPAN = 64u << 20;

#define CLOB_V "v0","v1","v2","v3","v4","v5","v6","v7","v8","v9","v10","v11","v12","v13","v14","v15","v16","v17","v18","v19","v20","v21","v22","v23","v24","v25","v26","v27","v28","v29","v30","v31","v32","v33","v34","v35","v36","v37","v38","v39","v40","v41","v42","v43","v44","v45","v46","v47","v48","v49","v50","v51","v52","v53","v54","v55","v56","v57","v58","v59","v60","v61","v62","v63","v64","v65","v66","v67","v68","v69","v70","v71","v72","v73","v74","v75","v76","v77","v78","v79","v80","v81","v82","v83","v84","v85","v86","v87","v88","v89","v90","v91","v92","v93","v94","v95","v96","v97","v98","v99","v100","v101","v102","v103","v104","v105","v106","v107","v108","v109","v110","v111","v112","v113","v114","v115","v116","v117","v118","v119","v120","v121","v122","v123","v124","v125","v126","v127","v128","v129","v130","v131","v132","v133","v134","v135","v136","v137","v138","v139","v140","v141","v142","v143","v144","v145","v146","v147","v148","v149","v150","v151","v152","v153","v154","v155","v156","v157","v158","v159","v160","v161","v162","v163","v164","v165","v166","v167","v168","v169","v170","v171","v172","v173","v174","v175","v176","v177","v178","v179","v180","v181","v182","v183","v184","v185","v186","v187","v188","v189","v190","v191","v192","v193","v194","v195","v196","v197","v198","v199","v200","v201","v202","v203","v204","v205","v206","v207","v208","v209","v210","v211","v212","v213","v214","v215","v216","v217","v218","v219","v220","v221","v222","v223","v224","v225","v226","v227","v228","v229","v230","v231","v232","v233","v234","v235","v236","v237","v238","v239","v240","v241","v242","v243","v244","v245","v246","v247","v248","v249","v250","v251","v252","v253","v254","v255"
#define CLOB_A "a0","a1","a2","a3","a4","a5","a6","a7","a8","a9","a10","a11","a12","a13","a14","a15","a16","a17","a18","a19","a20","a21","a22","a23","a24","a25","a26","a27","a28","a29","a30","a31","a32","a33","a34","a35","a36","a37","a38","a39","a40","a41","a42","a43","a44","a45","a46","a47","a48","a49","a50","a51","a52","a53","a54","a55","a56","a57","a58","a59","a60","a61","a62","a63","a64","a65","a66","a67","a68","a69","a70","a71","a72","a73","a74","a75","a76","a77","a78","a79","a80","a81","a82","a83","a84","a85","a86","a87","a88","a89","a90","a91","a92","a93","a94","a95","a96","a97","a98","a99","a100","a101","a102","a103","a104","a105","a106","a107","a108","a109","a110","a111","a112","a113","a114","a115","a116","a117","a118","a119","a120","a121","a122","a123","a124","a125","a126","a127","a128","a129","a130","a131","a132","a133","a134","a135","a136","a137","a138","a139","a140","a141","a142","a143","a144","a145","a146","a147","a148","a149","a150","a151","a152","a153","a154","a155","a156","a157","a158","a159","a160","a161","a162","a163","a164","a165","a166","a167","a168","a169","a170","a171","a172","a173","a174","a175","a176","a177","a178","a179","a180","a181","a182","a183","a184","a185","a186","a187","a188","a189","a190","a191","a192","a193","a194","a195","a196","a197","a198","a199","a200","a201","a202","a203","a204","a205","a206","a207","a208","a209","a210","a211","a212","a213","a214","a215","a216","a217","a218","a219","a220","a221","a222","a223","a224","a225","a226","a227","a228","a229","a230","a231","a232","a233","a234","a235","a236","a237","a238","a239","a240","a241","a242","a243","a244","a245","a246","a247","a248","a249","a250","a251","a252","a253","a254","a255"
#define CLOB_S "s8","s9","s10","s11","s12","s13","s14","s15","s16","s17","s18","s19","s20","s21","s22","s23","s24","s25","s26","s27","s28","s29","s30","s31","s32","s33","s34","s35","s36","s37","s38","s39","s40","s41","s42","s43","s44","s45","s46","s47","s48","s49","s50","s51","s52","s53","s54","s55","s56","s57","s58","s59","s60","s61","s62","s63","s64","s65","s66","s67","s68","s69","s70","s71","s72","s73","s74","s75","s76","s77","s78","s79","s80","s81","s82","s83","s84","s85","s86","s87","s88","s89","s90","s91","s92","s93","s94","s95","s96","s97","s98","s99"
__global__ __launch_bounds__(256, 1) __attribute__((aligned(4096))) void ka_fused(const void* wq, const void* x, void* ws, const void* Kc, const void* wr, const void* Vc, const void* wo, const void* wg, const void* wu, const void* wd, void* out) {
  extern __shared__ __attribute__((aligned(16))) char lds[];
  const unsigned w = __builtin_amdgcn_readfirstlane(threadIdx.x >> 6);
  const unsigned slds = __builtin_amdgcn_readfirstlane((unsigned)(size_t)lds);
  asm volatile(
R"ASM(
.macro MM a, b, c, d:vararg
v_mfma_f32_16x16x32_bf16 \a, \b, \c, \d
.endm
.macro TR a, b:vararg
ds_read_b64_tr_b16 \a, \b
.endm
.macro CV a, b, c:vararg
v_cvt_pk_bf16_f32 \a, \b, \c
.endm
.macro AR a, b:vararg
v_accvgpr_read_b32 \a, \b
.endm
.macro AW a, b:vararg
v_accvgpr_write_b32 \a, \b
.endm
.macro EX a, b:vararg
v_exp_f32 \a, \b
.endm
.macro RD a, b:vararg
ds_read_b128 \a, \b
.endm
s_load_dwordx2 s[16:17], %[ka], 0
s_load_dwordx2 s[32:33], %[ka], 8
s_lshr_b32 s77, %[sb], 3
v_lshlrev_b32 v118, 4, v0
s_getpc_b64 s[98:99]
s_andn2_b32 s98, s98, 63
v_lshl_add_u32 v118, s77, 12, v118
global_load_dwordx4 a[252:255], v118, s[98:99]
.p2align 6
LPG0_%=:
s_load_dwordx2 s[20:21], %[ka], 16
v_mbcnt_lo_u32_b32 v127, -1, 0
v_mbcnt_hi_u32_b32 v127, -1, v127
v_lshlrev_b32 v248, 4, v127
s_and_b32 s77, %[sb], 7
s_lshl_b32 s77, s77, 2
s_bfe_u32 s84, %[sb], 131075
s_add_u32 s49, s77, s84
s_lshr_b32 s50, %[sb], 5
s_lshl_b32 s76, %[sb], 2
s_add_u32 s76, s76, %[swave]
v_lshrrev_b32 v180, 5, v127
s_mul_i32 s82, s76, 2560
v_and_b32 v182, 496, v248
v_mul_u32_u24 v181, 1280, v180
v_add_u32 v168, v181, v182
v_lshrrev_b32 v210, 1, v182
v_add_u32 v181, v181, v210
s_lshl_b32 s77, %[swave], 12
s_add_u32 s65, s77, %[slds]
s_lshl_b32 s89, %[swave], 17
v_bfe_u32 v210, v127, 2, 3
v_and_b32 v212, 3, v127
v_lshlrev_b32 v213, 4, v212
v_lshl_add_u32 v213, v180, 6, v213
v_lshrrev_b32 v253, 3, v127
v_lshlrev_b32 v253, 13, v253
v_and_b32 v254, 112, v248
v_or_b32 v253, v253, v254
v_and_b32 v254, 16, v127
v_lshlrev_b32 v254, 2, v254
v_xor_b32 v253, v253, v254
v_add_u32 v254, 128, v253
s_mul_i32 s96, %[swave], 4112
s_add_u32 s96, s96, %[slds]
s_lshl_b32 s77, s50, 2
s_add_u32 s77, s77, %[swave]
s_mul_i32 s68, s77, 81920
s_and_b32 s62, %[sb], 1
s_mul_i32 s63, s62, 40960
s_add_u32 s68, s68, s63
s_lshl_b32 s63, s63, 1
s_mul_i32 s83, s77, 560128
s_add_u32 s83, s83, 2752512
s_lshl_b32 s93, s77, 9
s_lshl_b32 s94, %[swave], 13
s_add_u32 s94, s94, %[slds]
v_lshrrev_b32 v211, 3, v127
v_mul_u32_u24 v220, 1280, v211
v_lshrrev_b32 v212, 4, v127
v_and_b32 v211, 7, v127
v_xor_b32 v211, v211, v212
v_lshl_add_u32 v220, v211, 4, v220
v_xor_b32 v221, 64, v220
s_bitcmp1_b32 s84, 0
s_mov_b32 s77, 0x77552200
s_cselect_b32 s77, 0x77552200, s77
s_lshl_b32 s91, s50, 2
s_lshr_b32 s77, s77, s91
s_and_b32 s77, s77, 15
s_lshl_b32 s86, s77, 7
s_lshl_b32 s85, s77, 19
s_lshl_b32 s90, s49, 8
s_sub_u32 s18, 5242880, s90
s_mov_b32 s19, 131072
s_mov_b32 s34, 2621440
s_mov_b32 s35, 131072
s_mov_b32 s22, 67108864
s_mov_b32 s23, 131072
s_add_u32 s87, s89, s85
s_add_u32 s85, s85, 524288
s_cmp_ge_u32 s85, 5242880
s_cselect_b32 s85, 0, s85
s_add_u32 m0, s96, 65536
s_waitcnt lgkmcnt(0)
s_add_u32 s16, s16, s90
s_addc_u32 s17, s17, 0
buffer_load_dwordx4 v253, s[16:19], s87 offen offset:0 lds
s_add_u32 m0, s96, 66560
s_nop 0
buffer_load_dwordx4 v254, s[16:19], s87 offen offset:0 lds
s_add_u32 s87, s87, 65536
s_add_u32 m0, s96, 67592
s_nop 0
buffer_load_dwordx4 v253, s[16:19], s87 offen offset:0 lds
s_add_u32 m0, s96, 68616
s_nop 0
buffer_load_dwordx4 v254, s[16:19], s87 offen offset:0 lds
s_add_u32 s88, s68, s86
s_add_u32 s86, s86, 128
s_cmp_ge_u32 s86, 1280
s_cselect_b32 s86, 0, s86
s_add_u32 m0, s94, 0
s_add_u32 s95, s88, 0
buffer_load_dwordx4 v220, s[32:35], s95 offen offset:0 lds
s_add_u32 s95, s88, 9216
buffer_load_dwordx4 v221, s[32:35], s95 offen offset:1024 lds
s_add_u32 s95, s88, 18432
buffer_load_dwordx4 v220, s[32:35], s95 offen offset:2048 lds
s_add_u32 s95, s88, 27648
buffer_load_dwordx4 v221, s[32:35], s95 offen offset:3072 lds
s_load_dwordx2 s[8:9], %[ka], 24
s_load_dwordx2 s[12:13], %[ka], 40
s_load_dwordx2 s[24:25], %[ka], 48
s_load_dwordx2 s[44:45], %[ka], 32
s_load_dwordx2 s[28:29], %[ka], 72
s_load_dwordx2 s[40:41], %[ka], 80
s_lshl_b32 s77, %[swave], 2
s_and_b32 s77, s77, 8
s_add_u32 s77, s77, 56
s_load_dwordx2 s[36:37], %[ka], s77
s_mov_b32 s66, 32768
buffer_load_dword v169, off, s[20:23], s66 sc1
buffer_load_dwordx4 v[128:131], v168, s[32:35], s82 offen offset:0
buffer_load_dwordx4 v[132:135], v168, s[32:35], s82 offen offset:512
buffer_load_dwordx2 v[136:137], v181, s[32:35], s82 offen offset:1024
s_add_u32 s82, s82, 131072
s_sub_u32 s88, s88, s63
s_add_u32 m0, s94, 4096
s_add_u32 s95, s88, 40960
buffer_load_dwordx4 v220, s[32:35], s95 offen offset:0 lds
s_add_u32 s95, s88, 50176
buffer_load_dwordx4 v221, s[32:35], s95 offen offset:1024 lds
s_add_u32 s95, s88, 59392
buffer_load_dwordx4 v220, s[32:35], s95 offen offset:2048 lds
s_add_u32 s95, s88, 68608
buffer_load_dwordx4 v221, s[32:35], s95 offen offset:3072 lds
v_and_b32 v211, 15, v127
v_bfe_u32 v222, v127, 1, 3
v_xor_b32 v222, v222, v212
v_lshlrev_b32 v222, 4, v222
v_lshl_add_u32 v222, v211, 7, v222
v_xor_b32 v223, 64, v222
v_add_u32 v222, s94, v222
v_add_u32 v223, s94, v223
v_and_b32 v214, 1, v212
v_lshlrev_b32 v215, 5, v214
v_xor_b32 v215, v215, v213
v_lshl_add_u32 v235, v210, 8, v215
v_add_u32 v235, 128, v235
v_and_b32 v234, 1, v127
v_mul_u32_u24 v234, 43690, v234
v_add_u32 v234, 17476, v234
v_bfe_u32 v215, v127, 2, 2
v_and_b32 v216, 3, v127
v_lshlrev_b32 v252, 7, v215
v_lshl_add_u32 v252, v216, 4, v252
v_and_b32 v251, 8, v127
v_lshl_add_u32 v252, v251, 3, v252
v_mul_u32_u24 v251, 2056, v212
s_xor_b32 s77, %[swave], 0
s_and_b32 s91, s77, 1
s_lshl_b32 s91, s91, 6
s_lshr_b32 s77, s77, 1
s_lshl_b32 s77, s77, 10
s_add_u32 s77, s77, %[slds]
s_add_u32 s77, s77, 65536
v_xor_b32 v192, s91, v252
v_add3_u32 v192, s77, v192, v251
v_add_u32 v196, 49344, v192
s_xor_b32 s77, %[swave], 1
s_and_b32 s91, s77, 1
s_lshl_b32 s91, s91, 6
s_lshr_b32 s77, s77, 1
s_lshl_b32 s77, s77, 10
s_add_u32 s77, s77, %[slds]
s_add_u32 s77, s77, 65536
v_xor_b32 v193, s91, v252
v_add3_u32 v193, s77, v193, v251
v_add_u32 v197, 49344, v193
s_xor_b32 s77, %[swave], 2
s_and_b32 s91, s77, 1
s_lshl_b32 s91, s91, 6
s_lshr_b32 s77, s77, 1
s_lshl_b32 s77, s77, 10
s_add_u32 s77, s77, %[slds]
s_add_u32 s77, s77, 65536
v_xor_b32 v194, s91, v252
v_add3_u32 v194, s77, v194, v251
v_add_u32 v198, 49344, v194
s_xor_b32 s77, %[swave], 3
s_and_b32 s91, s77, 1
s_lshl_b32 s91, s91, 6
s_lshr_b32 s77, s77, 1
s_lshl_b32 s77, s77, 10
s_add_u32 s77, s77, %[slds]
s_add_u32 s77, s77, 65536
v_xor_b32 v195, s91, v252
v_add3_u32 v195, s77, v195, v251
v_add_u32 v199, 49344, v195
v_mov_b32 v236, 0x3f803f80
v_mov_b32 v184, 0
v_mov_b32 v237, 0x3f803f80
v_mov_b32 v185, 0
v_mov_b32 v238, 0x3f803f80
v_mov_b32 v188, 0
v_mov_b32 v239, 0x3f803f80
v_mov_b32 v189, 0
s_add_u32 s87, s89, s85
s_add_u32 s85, s85, 524288
s_cmp_ge_u32 s85, 5242880
s_cselect_b32 s85, 0, s85
s_add_u32 m0, s96, 81984
s_nop 0
buffer_load_dwordx4 v253, s[16:19], s87 offen offset:0 lds
s_add_u32 m0, s96, 83008
s_nop 0
buffer_load_dwordx4 v254, s[16:19], s87 offen offset:0 lds
s_add_u32 s87, s87, 65536
s_add_u32 m0, s96, 84040
s_nop 0
buffer_load_dwordx4 v253, s[16:19], s87 offen offset:0 lds
s_add_u32 m0, s96, 85064
s_nop 0
buffer_load_dwordx4 v254, s[16:19], s87 offen offset:0 lds
s_getpc_b64 s[98:99]
LTA0_%=:
s_min_u32 s73, %[swave], 3
s_lshl_b32 s73, s73, 12
s_add_u32 s73, s73, LPG1_%=-LTA0_%=
s_add_u32 s98, s98, s73
s_addc_u32 s99, s99, 0
s_mov_b64 exec, 0
s_setpc_b64 s[98:99]
LTR0_%=:
s_mov_b64 exec, -1
s_waitcnt lgkmcnt(0)
s_add_u32 s88, s68, s86
s_add_u32 s86, s86, 128
s_cmp_ge_u32 s86, 1280
s_cselect_b32 s86, 0, s86
s_add_u32 m0, s94, 32768
s_add_u32 s95, s88, 0
buffer_load_dwordx4 v220, s[32:35], s95 offen offset:0 lds
s_add_u32 s95, s88, 9216
buffer_load_dwordx4 v221, s[32:35], s95 offen offset:1024 lds
s_add_u32 s95, s88, 18432
buffer_load_dwordx4 v220, s[32:35], s95 offen offset:2048 lds
s_add_u32 s95, s88, 27648
buffer_load_dwordx4 v221, s[32:35], s95 offen offset:3072 lds
s_sub_u32 s88, s88, s63
s_add_u32 m0, s94, 36864
s_add_u32 s95, s88, 40960
buffer_load_dwordx4 v220, s[32:35], s95 offen offset:0 lds
s_add_u32 s95, s88, 50176
buffer_load_dwordx4 v221, s[32:35], s95 offen offset:1024 lds
s_add_u32 s95, s88, 59392
buffer_load_dwordx4 v220, s[32:35], s95 offen offset:2048 lds
s_add_u32 s95, s88, 68608
buffer_load_dwordx4 v221, s[32:35], s95 offen offset:3072 lds
s_waitcnt vmcnt(20)
RD a[128:131],v222 offset:0
RD a[132:135],v223 offset:0
RD a[136:139],v222 offset:2048
RD a[140:143],v223 offset:2048
s_barrier
TR a[0:1],v192 offset:0
TR a[2:3],v192 offset:512
TR a[4:5],v192 offset:8224
TR a[6:7],v192 offset:8736
TR a[8:9],v192 offset:8
TR a[10:11],v192 offset:520
TR a[12:13],v192 offset:8232
TR a[14:15],v192 offset:8744
TR a[16:17],v193 offset:0
TR a[18:19],v193 offset:512
TR a[20:21],v193 offset:8224
TR a[22:23],v193 offset:8736
TR a[24:25],v193 offset:8
TR a[26:27],v193 offset:520
TR a[28:29],v193 offset:8232
TR a[30:31],v193 offset:8744
s_waitcnt lgkmcnt(15)
s_waitcnt lgkmcnt(12)
MM v[0:3],a[0:3],a[128:131],0
s_add_u32 s87, s89, s85
TR a[32:33],v194 offset:0
TR a[34:35],v194 offset:512
MM v[0:3],a[4:7],a[132:135],v[0:3]
s_add_u32 s85, s85, 524288
TR a[36:37],v194 offset:8224
TR a[38:39],v194 offset:8736
MM v[8:11],a[4:7],a[140:143],0
s_waitcnt vmcnt(16)
TR a[40:41],v194 offset:8
TR a[42:43],v194 offset:520
MM v[8:11],a[0:3],a[136:139],v[8:11]
s_waitcnt lgkmcnt(14)
TR a[44:45],v194 offset:8232
TR a[46:47],v194 offset:8744
s_cmp_ge_u32 s85, 5242880
s_cselect_b32 s85, 0, s85
MM v[4:7],a[8:11],a[128:131],0
s_add_u32 m0, s96, 98432
v_max3_f32 v200, |v0|, |v1|, |v2|
v_max_f32 v200, v200, |v3|
v_readfirstlane_b32 s69, v169
MM v[4:7],a[12:15],a[132:135],v[4:7]
s_lshl_b32 s90, s76, 1
TR a[48:49],v195 offset:0
TR a[50:51],v195 offset:512
MM v[12:15],a[12:15],a[140:143],0
s_and_b32 s77, s90, 15
buffer_load_dwordx4 v253, s[16:19], s87 offen offset:0 lds
v_max3_f32 v201, |v8|, |v9|, |v10|
v_max_f32 v201, v201, |v11|
MM v[12:15],a[8:11],a[136:139],v[12:15]
s_add_u32 m0, s96, 99456
TR a[52:53],v195 offset:8224
v_min_f32 v200, v200, v201
v_and_b32 v200, 0x7f800000, v200
v_lshlrev_b32 v148, 16, v128
v_smfmac_f32_16x16x64_bf16 v[184:187], v[236:239], a[0:7], v234
s_lshl_b32 s77, s77, 1
v_add_u32 v200, 0x2c00000, v200
v_smfmac_f32_16x16x64_bf16 v[188:191], v[236:239], a[8:15], v234
s_waitcnt lgkmcnt(15)
v_min_u32 v200, 0x78400000, v200
v_mov_b32 v201, v200
v_mov_b32 v202, v200
v_mov_b32 v203, v200
s_nop 1
MM v[32:35],a[16:19],a[128:131],v[200:203]
s_bfe_u32 s91, s90, 65540
TR a[54:55],v195 offset:8736
buffer_load_dwordx4 v254, s[16:19], s87 offen offset:0 lds
v_mov_b32 v250, v200
v_and_b32 v149, 0xffff0000, v128
MM v[32:35],a[20:23],a[132:135],v[32:35]
s_add_u32 s87, s87, 65536
TR a[56:57],v195 offset:8
TR a[58:59],v195 offset:520
MM v[40:43],a[20:23],a[140:143],v[200:203]
s_add_u32 m0, s96, 100488
TR a[60:61],v195 offset:8232
TR a[62:63],v195 offset:8744
MM v[40:43],a[16:19],a[136:139],v[40:43]
s_waitcnt lgkmcnt(15)
buffer_load_dwordx4 v253, s[16:19], s87 offen offset:0 lds
v_lshlrev_b32 v150, 16, v129
MM v[36:39],a[24:27],a[128:131],v[200:203]
s_add_u32 m0, s96, 101512
v_and_b32 v151, 0xffff0000, v129
v_lshlrev_b32 v152, 16, v130
MM v[36:39],a[28:31],a[132:135],v[36:39]
s_add_u32 s77, s77, s91
v_and_b32 v153, 0xffff0000, v130
v_lshlrev_b32 v154, 16, v131
MM v[44:47],a[28:31],a[140:143],v[200:203]
s_and_b32 s91, s90, 0xffffffe0
buffer_load_dwordx4 v254, s[16:19], s87 offen offset:0 lds
MM v[44:47],a[24:27],a[136:139],v[44:47]
s_waitcnt lgkmcnt(12)
s_waitcnt vmcnt(16)
RD a[144:147],v222 offset:4096
v_and_b32 v155, 0xffff0000, v131
MM v[64:67],a[32:35],a[128:131],v[200:203]
s_add_u32 s77, s77, s91
RD a[148:151],v223 offset:4096
RD a[152:155],v222 offset:6144
MM v[64:67],a[36:39],a[132:135],v[64:67]
s_lshl_b32 s77, s77, 3
RD a[156:159],v223 offset:6144
v_lshlrev_b32 v156, 16, v132
MM v[72:75],a[36:39],a[140:143],v[200:203]
s_add_u32 s97, s77, 36864
s_waitcnt vmcnt(12)
s_barrier
v_and_b32 v157, 0xffff0000, v132
MM v[72:75],a[32:35],a[136:139],v[72:75]
s_waitcnt lgkmcnt(12)
global_load_dwordx4 v[138:141], v182, s[44:45] offset:0
MM v[68:71],a[40:43],a[128:131],v[200:203]
s_sub_u32 s79, 65535, s90
v_lshlrev_b32 v158, 16, v133
v_and_b32 v159, 0xffff0000, v133
MM v[68:71],a[44:47],a[132:135],v[68:71]
s_lshl_b32 s78, s90, 2
v_lshlrev_b32 v160, 16, v134
v_and_b32 v161, 0xffff0000, v134
MM v[76:79],a[44:47],a[140:143],v[200:203]
s_add_u32 s78, s78, 53248
global_load_dwordx4 v[142:145], v182, s[44:45] offset:512
MM v[76:79],a[40:43],a[136:139],v[76:79]
s_waitcnt lgkmcnt(8)
v_lshrrev_b32 v182, 1, v182
v_lshlrev_b32 v162, 16, v135
MM v[96:99],a[48:51],a[128:131],v[200:203]
s_lshl_b32 s80, s76, 2
global_load_dwordx2 v[146:147], v182, s[44:45] offset:1024
MM v[96:99],a[52:55],a[132:135],v[96:99]
)ASM"
R"ASM(
s_waitcnt lgkmcnt(0)
s_add_u32 s88, s68, s86
s_add_u32 s86, s86, 128
v_and_b32 v163, 0xffff0000, v135
MM v[104:107],a[52:55],a[140:143],v[200:203]
s_add_u32 s80, s80, 81920
s_cmp_ge_u32 s86, 1280
s_cselect_b32 s86, 0, s86
MM v[104:107],a[48:51],a[136:139],v[104:107]
s_add_u32 m0, s94, 0
s_add_u32 s95, s88, 0
v_lshlrev_b32 v164, 16, v136
MM v[100:103],a[56:59],a[128:131],v[200:203]
s_mov_b32 s42, 2621440
buffer_load_dwordx4 v220, s[32:35], s95 offen offset:0 lds
MM v[100:103],a[60:63],a[132:135],v[100:103]
s_add_u32 s95, s88, 9216
v_and_b32 v165, 0xffff0000, v136
v_lshlrev_b32 v166, 16, v137
MM v[108:111],a[60:63],a[140:143],v[200:203]
s_mov_b32 s43, 131072
buffer_load_dwordx4 v221, s[32:35], s95 offen offset:1024 lds
MM v[108:111],a[56:59],a[136:139],v[108:111]
s_add_u32 s95, s88, 18432
v_and_b32 v167, 0xffff0000, v137
s_add_u32 s69, s69, 1
MM v[16:19],a[0:3],a[144:147],0
v_add_f32 v170, v148, v149
v_add_f32 v171, v150, v151
MM v[16:19],a[4:7],a[148:151],v[16:19]
v_add_f32 v170, v170, v152
v_add_f32 v171, v171, v153
MM v[24:27],a[4:7],a[156:159],0
buffer_load_dwordx4 v220, s[32:35], s95 offen offset:2048 lds
MM v[24:27],a[0:3],a[152:155],v[24:27]
s_add_u32 s95, s88, 27648
v_add_f32 v170, v170, v154
v_add_f32 v171, v171, v155
MM v[20:23],a[8:11],a[144:147],0
v_max3_f32 v204, |v16|, |v17|, |v18|
v_max_f32 v204, v204, |v19|
MM v[20:23],a[12:15],a[148:151],v[20:23]
TR a[0:1],v192 offset:16448
TR a[2:3],v192 offset:16960
v_add_f32 v170, v170, v156
MM v[28:31],a[12:15],a[156:159],0
v_max3_f32 v205, |v24|, |v25|, |v26|
v_max_f32 v205, v205, |v27|
MM v[28:31],a[8:11],a[152:155],v[28:31]
TR a[4:5],v192 offset:24672
v_min_f32 v204, v204, v205
v_and_b32 v204, 0x7f800000, v204
v_add_u32 v204, 0x2c00000, v204
v_min_u32 v204, 0x78400000, v204
v_mov_b32 v205, v204
v_mov_b32 v206, v204
v_mov_b32 v207, v204
s_nop 1
MM v[48:51],a[16:19],a[144:147],v[204:207]
TR a[6:7],v192 offset:25184
buffer_load_dwordx4 v221, s[32:35], s95 offen offset:3072 lds
v_mov_b32 v252, v204
v_add_f32 v171, v171, v157
MM v[48:51],a[20:23],a[148:151],v[48:51]
s_sub_u32 s88, s88, s63
TR a[8:9],v192 offset:16456
TR a[10:11],v192 offset:16968
MM v[56:59],a[20:23],a[156:159],v[204:207]
s_add_u32 m0, s94, 4096
TR a[12:13],v192 offset:24680
TR a[14:15],v192 offset:25192
MM v[56:59],a[16:19],a[152:155],v[56:59]
s_add_u32 s95, s88, 40960
buffer_load_dwordx4 v220, s[32:35], s95 offen offset:0 lds
v_add_f32 v170, v170, v158
MM v[52:55],a[24:27],a[144:147],v[204:207]
s_add_u32 s95, s88, 50176
TR a[16:17],v193 offset:16448
TR a[18:19],v193 offset:16960
MM v[52:55],a[28:31],a[148:151],v[52:55]
TR a[20:21],v193 offset:24672
TR a[22:23],v193 offset:25184
MM v[60:63],a[28:31],a[156:159],v[204:207]
buffer_load_dwordx4 v221, s[32:35], s95 offen offset:1024 lds
v_add_f32 v171, v171, v159
MM v[60:63],a[24:27],a[152:155],v[60:63]
s_add_u32 s95, s88, 59392
v_add_f32 v170, v170, v160
v_add_f32 v171, v171, v161
MM v[80:83],a[32:35],a[144:147],v[204:207]
TR a[24:25],v193 offset:16456
TR a[26:27],v193 offset:16968
MM v[80:83],a[36:39],a[148:151],v[80:83]
TR a[28:29],v193 offset:24680
TR a[30:31],v193 offset:25192
v_add_f32 v170, v170, v162
MM v[88:91],a[36:39],a[156:159],v[204:207]
buffer_load_dwordx4 v220, s[32:35], s95 offen offset:2048 lds
MM v[88:91],a[32:35],a[152:155],v[88:91]
s_add_u32 s95, s88, 68608
v_add_f32 v171, v171, v163
v_add_f32 v170, v170, v164
MM v[84:87],a[40:43],a[144:147],v[204:207]
TR a[32:33],v194 offset:16448
TR a[34:35],v194 offset:16960
MM v[84:87],a[44:47],a[148:151],v[84:87]
TR a[36:37],v194 offset:24672
TR a[38:39],v194 offset:25184
v_add_f32 v171, v171, v165
MM v[92:95],a[44:47],a[156:159],v[204:207]
buffer_load_dwordx4 v221, s[32:35], s95 offen offset:3072 lds
MM v[92:95],a[40:43],a[152:155],v[92:95]
s_add_u32 s87, s89, s85
s_add_u32 s85, s85, 524288
v_add_f32 v170, v170, v166
MM v[112:115],a[48:51],a[144:147],v[204:207]
TR a[40:41],v194 offset:16456
TR a[42:43],v194 offset:16968
MM v[112:115],a[52:55],a[148:151],v[112:115]
TR a[44:45],v194 offset:24680
TR a[46:47],v194 offset:25192
s_cmp_ge_u32 s85, 5242880
s_cselect_b32 s85, 0, s85
v_add_f32 v171, v171, v167
MM v[120:123],a[52:55],a[156:159],v[204:207]
s_add_u32 m0, s96, 114880
s_nop 0
buffer_load_dwordx4 v253, s[16:19], s87 offen offset:0 lds
MM v[120:123],a[48:51],a[152:155],v[120:123]
s_add_u32 m0, s96, 115904
v_add_f32 v172, v170, v171
v_lshlrev_b32 v232, 11, v180
MM v[116:119],a[56:59],a[144:147],v[204:207]
TR a[48:49],v195 offset:16448
TR a[50:51],v195 offset:16960
MM v[116:119],a[60:63],a[148:151],v[116:119]
TR a[52:53],v195 offset:24672
TR a[54:55],v195 offset:25184
MM v[124:127],a[60:63],a[156:159],v[204:207]
buffer_load_dwordx4 v254, s[16:19], s87 offen offset:0 lds
v_add_f32_dpp v172, v172, v172 row_ror:8 row_mask:0xf bank_mask:0xf
MM v[124:127],a[56:59],a[152:155],v[124:127]
s_add_u32 s87, s87, 65536
s_add_u32 m0, s96, 116936
v_add_f32_dpp v172, v172, v172 row_ror:4 row_mask:0xf bank_mask:0xf
buffer_load_dwordx4 v253, s[16:19], s87 offen offset:0 lds
s_add_u32 m0, s96, 117960
s_nop 0
buffer_load_dwordx4 v254, s[16:19], s87 offen offset:0 lds
s_waitcnt vmcnt(19)
RD a[160:163],v222 offset:32768
RD a[164:167],v223 offset:32768
RD a[168:171],v222 offset:34816
RD a[172:175],v223 offset:34816
RD a[176:179],v222 offset:36864
RD a[180:183],v223 offset:36864
RD a[184:187],v222 offset:38912
RD a[188:191],v223 offset:38912
s_waitcnt vmcnt(15) lgkmcnt(0)
s_barrier
MM v[0:3],a[0:3],a[160:163],v[0:3]
TR a[56:57],v195 offset:16456
TR a[58:59],v195 offset:16968
MM v[0:3],a[4:7],a[164:167],v[0:3]
TR a[60:61],v195 offset:24680
TR a[62:63],v195 offset:25192
MM v[8:11],a[4:7],a[172:175],v[8:11]
v_add_f32_dpp v172, v172, v172 row_ror:2 row_mask:0xf bank_mask:0xf
v_lshl_add_u32 v232, v214, 8, v232
MM v[8:11],a[0:3],a[168:171],v[8:11]
s_add_u32 s88, s68, s86
s_add_u32 s86, s86, 128
MM v[16:19],a[0:3],a[176:179],v[16:19]
s_cmp_ge_u32 s86, 1280
s_cselect_b32 s86, 0, s86
s_add_u32 m0, s94, 32768
MM v[16:19],a[4:7],a[180:183],v[16:19]
s_add_u32 s95, s88, 0
buffer_load_dwordx4 v220, s[32:35], s95 offen offset:0 lds
v_add_f32_dpp v172, v172, v172 row_ror:1 row_mask:0xf bank_mask:0xf
MM v[24:27],a[4:7],a[188:191],v[24:27]
s_add_u32 s95, s88, 9216
v_mov_b32 v173, v172
v_lshl_add_u32 v232, v215, 6, v232
MM v[24:27],a[0:3],a[184:187],v[24:27]
buffer_load_dwordx4 v221, s[32:35], s95 offen offset:1024 lds
MM v[4:7],a[8:11],a[160:163],v[4:7]
s_add_u32 s95, s88, 18432
v_permlane16_swap_b32 v172, v173
v_add_f32 v172, v172, v173
MM v[4:7],a[12:15],a[164:167],v[4:7]
v_mul_f32 v172, 0x3acccccd, v172
v_sub_f32 v176, v148, v172
MM v[12:15],a[12:15],a[172:175],v[12:15]
v_sub_f32 v177, v149, v172
v_mul_f32 v170, v176, v176
MM v[12:15],a[8:11],a[168:171],v[12:15]
buffer_load_dwordx4 v220, s[32:35], s95 offen offset:2048 lds
MM v[20:23],a[8:11],a[176:179],v[20:23]
s_add_u32 s95, s88, 27648
v_mul_f32 v171, v177, v177
v_sub_f32 v176, v150, v172
MM v[20:23],a[12:15],a[180:183],v[20:23]
v_sub_f32 v177, v151, v172
v_fma_f32 v170, v176, v176, v170
MM v[28:31],a[12:15],a[188:191],v[28:31]
v_fma_f32 v171, v177, v177, v171
v_sub_f32 v176, v152, v172
MM v[28:31],a[8:11],a[184:187],v[28:31]
buffer_load_dwordx4 v221, s[32:35], s95 offen offset:3072 lds
v_smfmac_f32_16x16x64_bf16 v[184:187], v[236:239], a[0:7], v234
s_sub_u32 s88, s88, s63
s_add_u32 m0, s94, 36864
v_sub_f32 v177, v153, v172
v_smfmac_f32_16x16x64_bf16 v[188:191], v[236:239], a[8:15], v234
s_add_u32 s95, s88, 40960
v_fma_f32 v170, v176, v176, v170
MM v[32:35],a[16:19],a[160:163],v[32:35]
TR a[0:1],v192 offset:32896
TR a[2:3],v192 offset:33408
MM v[32:35],a[20:23],a[164:167],v[32:35]
TR a[4:5],v192 offset:41120
TR a[6:7],v192 offset:41632
v_fma_f32 v171, v177, v177, v171
MM v[40:43],a[20:23],a[172:175],v[40:43]
LPG1_%=:
s_cbranch_execz LPG5_%=
TR a[8:9],v192 offset:32904
TR a[10:11],v192 offset:33416
MM v[40:43],a[16:19],a[168:171],v[40:43]
TR a[12:13],v192 offset:41128
TR a[14:15],v192 offset:41640
buffer_load_dwordx4 v220, s[32:35], s95 offen offset:0 lds
MM v[48:51],a[16:19],a[176:179],v[48:51]
s_add_u32 s95, s88, 50176
v_sub_f32 v176, v154, v172
v_sub_f32 v177, v155, v172
MM v[48:51],a[20:23],a[180:183],v[48:51]
buffer_load_dwordx4 v221, s[32:35], s95 offen offset:1024 lds
MM v[56:59],a[20:23],a[188:191],v[56:59]
s_add_u32 s95, s88, 59392
v_fma_f32 v170, v176, v176, v170
v_fma_f32 v171, v177, v177, v171
MM v[56:59],a[16:19],a[184:187],v[56:59]
v_sub_f32 v176, v156, v172
v_sub_f32 v177, v157, v172
MM v[36:39],a[24:27],a[160:163],v[36:39]
TR a[16:17],v193 offset:32896
TR a[18:19],v193 offset:33408
MM v[36:39],a[28:31],a[164:167],v[36:39]
TR a[20:21],v193 offset:41120
TR a[22:23],v193 offset:41632
v_fma_f32 v170, v176, v176, v170
MM v[44:47],a[28:31],a[172:175],v[44:47]
buffer_load_dwordx4 v220, s[32:35], s95 offen offset:2048 lds
MM v[44:47],a[24:27],a[168:171],v[44:47]
s_add_u32 s95, s88, 68608
v_fma_f32 v171, v177, v177, v171
v_sub_f32 v176, v158, v172
MM v[52:55],a[24:27],a[176:179],v[52:55]
v_sub_f32 v177, v159, v172
v_fma_f32 v170, v176, v176, v170
MM v[52:55],a[28:31],a[180:183],v[52:55]
buffer_load_dwordx4 v221, s[32:35], s95 offen offset:3072 lds
MM v[60:63],a[28:31],a[188:191],v[60:63]
s_add_u32 s87, s89, s85
s_add_u32 s85, s85, 524288
v_fma_f32 v171, v177, v177, v171
MM v[60:63],a[24:27],a[184:187],v[60:63]
s_cmp_ge_u32 s85, 5242880
s_cselect_b32 s85, 0, s85
s_add_u32 m0, s96, 131328
MM v[64:67],a[32:35],a[160:163],v[64:67]
TR a[24:25],v193 offset:32904
TR a[26:27],v193 offset:33416
v_sub_f32 v176, v160, v172
MM v[64:67],a[36:39],a[164:167],v[64:67]
TR a[28:29],v193 offset:41128
TR a[30:31],v193 offset:41640
MM v[72:75],a[36:39],a[172:175],v[72:75]
buffer_load_dwordx4 v253, s[16:19], s87 offen offset:0 lds
MM v[72:75],a[32:35],a[168:171],v[72:75]
s_add_u32 m0, s96, 132352
v_sub_f32 v177, v161, v172
v_fma_f32 v170, v176, v176, v170
MM v[80:83],a[32:35],a[176:179],v[80:83]
v_fma_f32 v171, v177, v177, v171
v_sub_f32 v176, v162, v172
MM v[80:83],a[36:39],a[180:183],v[80:83]
buffer_load_dwordx4 v254, s[16:19], s87 offen offset:0 lds
MM v[88:91],a[36:39],a[188:191],v[88:91]
s_add_u32 s87, s87, 65536
s_add_u32 m0, s96, 133384
v_sub_f32 v177, v163, v172
MM v[88:91],a[32:35],a[184:187],v[88:91]
v_fma_f32 v170, v176, v176, v170
v_fma_f32 v171, v177, v177, v171
MM v[68:71],a[40:43],a[160:163],v[68:71]
TR a[32:33],v194 offset:32896
TR a[34:35],v194 offset:33408
MM v[68:71],a[44:47],a[164:167],v[68:71]
TR a[36:37],v194 offset:41120
TR a[38:39],v194 offset:41632
v_sub_f32 v176, v164, v172
MM v[76:79],a[44:47],a[172:175],v[76:79]
buffer_load_dwordx4 v253, s[16:19], s87 offen offset:0 lds
MM v[76:79],a[40:43],a[168:171],v[76:79]
s_add_u32 m0, s96, 134408
v_sub_f32 v177, v165, v172
v_fma_f32 v170, v176, v176, v170
MM v[84:87],a[40:43],a[176:179],v[84:87]
v_fma_f32 v171, v177, v177, v171
v_sub_f32 v176, v166, v172
MM v[84:87],a[44:47],a[180:183],v[84:87]
buffer_load_dwordx4 v254, s[16:19], s87 offen offset:0 lds
MM v[92:95],a[44:47],a[188:191],v[92:95]
s_waitcnt vmcnt(16)
RD a[192:195],v222 offset:0
v_sub_f32 v177, v167, v172
MM v[92:95],a[40:43],a[184:187],v[92:95]
RD a[196:199],v223 offset:0
v_fma_f32 v170, v176, v176, v170
MM v[96:99],a[48:51],a[160:163],v[96:99]
TR a[40:41],v194 offset:32904
TR a[42:43],v194 offset:33416
MM v[96:99],a[52:55],a[164:167],v[96:99]
TR a[44:45],v194 offset:41128
TR a[46:47],v194 offset:41640
v_fma_f32 v171, v177, v177, v171
MM v[104:107],a[52:55],a[172:175],v[104:107]
RD a[200:203],v222 offset:2048
v_add_f32 v170, v170, v171
MM v[104:107],a[48:51],a[168:171],v[104:107]
RD a[204:207],v223 offset:2048
v_add_f32_dpp v170, v170, v170 row_ror:8 row_mask:0xf bank_mask:0xf
MM v[112:115],a[48:51],a[176:179],v[112:115]
RD a[208:211],v222 offset:4096
v_add_f32_dpp v170, v170, v170 row_ror:4 row_mask:0xf bank_mask:0xf
MM v[112:115],a[52:55],a[180:183],v[112:115]
RD a[212:215],v223 offset:4096
v_add_f32_dpp v170, v170, v170 row_ror:2 row_mask:0xf bank_mask:0xf
MM v[120:123],a[52:55],a[188:191],v[120:123]
RD a[216:219],v222 offset:6144
v_add_f32_dpp v170, v170, v170 row_ror:1 row_mask:0xf bank_mask:0xf
MM v[120:123],a[48:51],a[184:187],v[120:123]
)ASM"
R"ASM(
s_waitcnt lgkmcnt(15)
RD a[220:223],v223 offset:6144
v_mov_b32 v173, v170
MM v[100:103],a[56:59],a[160:163],v[100:103]
TR a[48:49],v195 offset:32896
TR a[50:51],v195 offset:33408
MM v[100:103],a[60:63],a[164:167],v[100:103]
TR a[52:53],v195 offset:41120
TR a[54:55],v195 offset:41632
MM v[108:111],a[60:63],a[172:175],v[108:111]
v_permlane16_swap_b32 v170, v173
v_add_f32 v170, v170, v173
MM v[108:111],a[56:59],a[168:171],v[108:111]
s_waitcnt lgkmcnt(4)
s_add_u32 s88, s68, s86
s_add_u32 s86, s86, 128
MM v[116:119],a[56:59],a[176:179],v[116:119]
s_cmp_ge_u32 s86, 1280
s_cselect_b32 s86, 0, s86
s_add_u32 m0, s94, 0
v_mul_f32 v170, 0x3acccccd, v170
MM v[116:119],a[60:63],a[180:183],v[116:119]
s_add_u32 s95, s88, 0
buffer_load_dwordx4 v220, s[32:35], s95 offen offset:0 lds
MM v[124:127],a[60:63],a[188:191],v[124:127]
s_add_u32 s95, s88, 9216
CV v178,v172,v170
v_lshlrev_b32 v174, 16, v178
MM v[124:127],a[56:59],a[184:187],v[124:127]
buffer_load_dwordx4 v221, s[32:35], s95 offen offset:1024 lds
s_waitcnt vmcnt(14)
s_barrier
MM v[0:3],a[0:3],a[192:195],v[0:3]
s_add_u32 s95, s88, 18432
TR a[56:57],v195 offset:32904
TR a[58:59],v195 offset:33416
v_and_b32 v170, 0xffff0000, v178
MM v[0:3],a[4:7],a[196:199],v[0:3]
TR a[60:61],v195 offset:41128
TR a[62:63],v195 offset:41640
MM v[8:11],a[4:7],a[204:207],v[8:11]
v_add_f32 v170, 0x3727c5ac, v170
CV v170,v170,v170
MM v[8:11],a[0:3],a[200:203],v[8:11]
buffer_load_dwordx4 v220, s[32:35], s95 offen offset:2048 lds
MM v[16:19],a[0:3],a[208:211],v[16:19]
s_add_u32 s95, s88, 27648
v_and_b32 v170, 0xffff0000, v170
v_lshl_add_u32 v232, v216, 3, v232
MM v[16:19],a[4:7],a[212:215],v[16:19]
v_sqrt_f32 v170, v170
MM v[24:27],a[4:7],a[220:223],v[24:27]
v_mov_b32 v179, s69
CV v178,v174,v170
MM v[24:27],a[0:3],a[216:219],v[24:27]
buffer_load_dwordx4 v221, s[32:35], s95 offen offset:3072 lds
MM v[4:7],a[8:11],a[192:195],v[4:7]
s_sub_u32 s88, s88, s63
s_add_u32 m0, s94, 4096
s_add_u32 s95, s88, 40960
v_and_b32 v170, 0xffff0000, v178
MM v[4:7],a[12:15],a[196:199],v[4:7]
v_rcp_f32 v175, v170
MM v[12:15],a[12:15],a[204:207],v[12:15]
v_lshlrev_b32 v173, 4, v180
v_lshl_add_u32 v232, v214, 5, v232
MM v[12:15],a[8:11],a[200:203],v[12:15]
buffer_load_dwordx4 v220, s[32:35], s95 offen offset:0 lds
MM v[20:23],a[8:11],a[208:211],v[20:23]
s_add_u32 s95, s88, 50176
s_mov_b32 exec_lo, 1
s_mov_b32 exec_hi, 1
buffer_store_dwordx2 v[178:179], v173, s[20:23], s97 offen offset:0 sc1
s_mov_b64 exec, -1
MM v[20:23],a[12:15],a[212:215],v[20:23]
v_sub_f32 v148, v148, v174
v_sub_f32 v149, v149, v174
MM v[28:31],a[12:15],a[220:223],v[28:31]
v_sub_f32 v150, v150, v174
v_sub_f32 v151, v151, v174
MM v[28:31],a[8:11],a[216:219],v[28:31]
buffer_load_dwordx4 v221, s[32:35], s95 offen offset:1024 lds
v_smfmac_f32_16x16x64_bf16 v[184:187], v[236:239], a[0:7], v234
s_add_u32 s95, s88, 59392
v_sub_f32 v152, v152, v174
v_smfmac_f32_16x16x64_bf16 v[188:191], v[236:239], a[8:15], v234
v_sub_f32 v153, v153, v174
MM v[32:35],a[16:19],a[192:195],v[32:35]
TR a[0:1],v196 offset:0
TR a[2:3],v196 offset:512
MM v[32:35],a[20:23],a[196:199],v[32:35]
TR a[4:5],v196 offset:8224
TR a[6:7],v196 offset:8736
v_sub_f32 v154, v154, v174
MM v[40:43],a[20:23],a[204:207],v[40:43]
TR a[8:9],v196 offset:8
TR a[10:11],v196 offset:520
MM v[40:43],a[16:19],a[200:203],v[40:43]
TR a[12:13],v196 offset:8232
TR a[14:15],v196 offset:8744
buffer_load_dwordx4 v220, s[32:35], s95 offen offset:2048 lds
MM v[48:51],a[16:19],a[208:211],v[48:51]
s_add_u32 s95, s88, 68608
v_sub_f32 v155, v155, v174
v_sub_f32 v156, v156, v174
MM v[48:51],a[20:23],a[212:215],v[48:51]
buffer_load_dwordx4 v221, s[32:35], s95 offen offset:3072 lds
MM v[56:59],a[20:23],a[220:223],v[56:59]
s_add_u32 s87, s89, s85
s_add_u32 s85, s85, 524288
v_sub_f32 v157, v157, v174
MM v[56:59],a[16:19],a[216:219],v[56:59]
s_cmp_ge_u32 s85, 5242880
s_cselect_b32 s85, 0, s85
s_add_u32 m0, s96, 65536
MM v[36:39],a[24:27],a[192:195],v[36:39]
TR a[16:17],v197 offset:0
TR a[18:19],v197 offset:512
v_sub_f32 v158, v158, v174
MM v[36:39],a[28:31],a[196:199],v[36:39]
TR a[20:21],v197 offset:8224
TR a[22:23],v197 offset:8736
MM v[44:47],a[28:31],a[204:207],v[44:47]
buffer_load_dwordx4 v253, s[16:19], s87 offen offset:0 lds
MM v[44:47],a[24:27],a[200:203],v[44:47]
s_add_u32 m0, s96, 66560
v_sub_f32 v159, v159, v174
v_sub_f32 v160, v160, v174
MM v[52:55],a[24:27],a[208:211],v[52:55]
v_sub_f32 v161, v161, v174
v_sub_f32 v162, v162, v174
MM v[52:55],a[28:31],a[212:215],v[52:55]
buffer_load_dwordx4 v254, s[16:19], s87 offen offset:0 lds
MM v[60:63],a[28:31],a[220:223],v[60:63]
s_add_u32 s87, s87, 65536
s_add_u32 m0, s96, 67592
v_sub_f32 v163, v163, v174
MM v[60:63],a[24:27],a[216:219],v[60:63]
v_sub_f32 v164, v164, v174
v_sub_f32 v165, v165, v174
MM v[64:67],a[32:35],a[192:195],v[64:67]
TR a[24:25],v197 offset:8
TR a[26:27],v197 offset:520
MM v[64:67],a[36:39],a[196:199],v[64:67]
TR a[28:29],v197 offset:8232
TR a[30:31],v197 offset:8744
v_sub_f32 v166, v166, v174
MM v[72:75],a[36:39],a[204:207],v[72:75]
buffer_load_dwordx4 v253, s[16:19], s87 offen offset:0 lds
MM v[72:75],a[32:35],a[200:203],v[72:75]
s_add_u32 m0, s96, 68616
v_sub_f32 v167, v167, v174
CV v128,v148,v149
MM v[80:83],a[32:35],a[208:211],v[80:83]
CV v129,v150,v151
CV v130,v152,v153
MM v[80:83],a[36:39],a[212:215],v[80:83]
buffer_load_dwordx4 v254, s[16:19], s87 offen offset:0 lds
MM v[88:91],a[36:39],a[220:223],v[88:91]
s_waitcnt vmcnt(17)
RD a[224:227],v222 offset:32768
CV v131,v154,v155
MM v[88:91],a[32:35],a[216:219],v[88:91]
RD a[228:231],v223 offset:32768
CV v132,v156,v157
MM v[68:71],a[40:43],a[192:195],v[68:71]
TR a[32:33],v198 offset:0
TR a[34:35],v198 offset:512
MM v[68:71],a[44:47],a[196:199],v[68:71]
TR a[36:37],v198 offset:8224
TR a[38:39],v198 offset:8736
CV v133,v158,v159
MM v[76:79],a[44:47],a[204:207],v[76:79]
RD a[232:235],v222 offset:34816
CV v134,v160,v161
MM v[76:79],a[40:43],a[200:203],v[76:79]
RD a[236:239],v223 offset:34816
CV v135,v162,v163
MM v[84:87],a[40:43],a[208:211],v[84:87]
RD a[240:243],v222 offset:36864
CV v136,v164,v165
MM v[84:87],a[44:47],a[212:215],v[84:87]
RD a[244:247],v223 offset:36864
CV v137,v166,v167
MM v[92:95],a[44:47],a[220:223],v[92:95]
RD a[248:251],v222 offset:38912
v_lshlrev_b32 v148, 16, v128
MM v[92:95],a[40:43],a[216:219],v[92:95]
s_waitcnt lgkmcnt(15)
RD a[252:255],v223 offset:38912
v_and_b32 v149, 0xffff0000, v128
MM v[96:99],a[48:51],a[192:195],v[96:99]
TR a[40:41],v198 offset:8
TR a[42:43],v198 offset:520
MM v[96:99],a[52:55],a[196:199],v[96:99]
TR a[44:45],v198 offset:8232
TR a[46:47],v198 offset:8744
v_lshlrev_b32 v150, 16, v129
MM v[104:107],a[52:55],a[204:207],v[104:107]
v_and_b32 v151, 0xffff0000, v129
v_lshlrev_b32 v152, 16, v130
MM v[104:107],a[48:51],a[200:203],v[104:107]
s_waitcnt lgkmcnt(4)
s_add_u32 s88, s68, s86
s_add_u32 s86, s86, 128
MM v[112:115],a[48:51],a[208:211],v[112:115]
s_cmp_ge_u32 s86, 1280
s_cselect_b32 s86, 0, s86
s_add_u32 m0, s94, 32768
v_and_b32 v153, 0xffff0000, v130
MM v[112:115],a[52:55],a[212:215],v[112:115]
s_add_u32 s95, s88, 0
buffer_load_dwordx4 v220, s[32:35], s95 offen offset:0 lds
MM v[120:123],a[52:55],a[220:223],v[120:123]
s_add_u32 s95, s88, 9216
v_lshlrev_b32 v154, 16, v131
v_and_b32 v155, 0xffff0000, v131
MM v[120:123],a[48:51],a[216:219],v[120:123]
buffer_load_dwordx4 v221, s[32:35], s95 offen offset:1024 lds
MM v[100:103],a[56:59],a[192:195],v[100:103]
s_add_u32 s95, s88, 18432
TR a[48:49],v199 offset:0
TR a[50:51],v199 offset:512
v_lshlrev_b32 v156, 16, v132
MM v[100:103],a[60:63],a[196:199],v[100:103]
TR a[52:53],v199 offset:8224
TR a[54:55],v199 offset:8736
MM v[108:111],a[60:63],a[204:207],v[108:111]
v_and_b32 v157, 0xffff0000, v132
v_lshlrev_b32 v158, 16, v133
MM v[108:111],a[56:59],a[200:203],v[108:111]
buffer_load_dwordx4 v220, s[32:35], s95 offen offset:2048 lds
MM v[116:119],a[56:59],a[208:211],v[116:119]
s_add_u32 s95, s88, 27648
v_and_b32 v159, 0xffff0000, v133
v_lshlrev_b32 v160, 16, v134
MM v[116:119],a[60:63],a[212:215],v[116:119]
v_and_b32 v161, 0xffff0000, v134
v_lshlrev_b32 v162, 16, v135
MM v[124:127],a[60:63],a[220:223],v[124:127]
v_and_b32 v163, 0xffff0000, v135
v_lshlrev_b32 v164, 16, v136
MM v[124:127],a[56:59],a[216:219],v[124:127]
buffer_load_dwordx4 v221, s[32:35], s95 offen offset:3072 lds
s_waitcnt vmcnt(17)
s_barrier
MM v[0:3],a[0:3],a[224:227],v[0:3]
s_sub_u32 s88, s88, s63
TR a[56:57],v199 offset:8
TR a[58:59],v199 offset:520
v_and_b32 v165, 0xffff0000, v136
MM v[0:3],a[4:7],a[228:231],v[0:3]
s_add_u32 m0, s94, 36864
TR a[60:61],v199 offset:8232
TR a[62:63],v199 offset:8744
MM v[8:11],a[4:7],a[236:239],v[8:11]
s_add_u32 s95, s88, 40960
v_lshlrev_b32 v166, 16, v137
v_and_b32 v167, 0xffff0000, v137
MM v[8:11],a[0:3],a[232:235],v[8:11]
buffer_load_dwordx4 v220, s[32:35], s95 offen offset:0 lds
MM v[16:19],a[0:3],a[240:243],v[16:19]
s_add_u32 s95, s88, 50176
v_mul_f32 v148, v148, v175
v_mul_f32 v149, v149, v175
MM v[16:19],a[4:7],a[244:247],v[16:19]
v_mul_f32 v150, v150, v175
v_mul_f32 v151, v151, v175
MM v[24:27],a[4:7],a[252:255],v[24:27]
v_mul_f32 v152, v152, v175
v_mul_f32 v153, v153, v175
MM v[24:27],a[0:3],a[248:251],v[24:27]
buffer_load_dwordx4 v221, s[32:35], s95 offen offset:1024 lds
MM v[4:7],a[8:11],a[224:227],v[4:7]
s_add_u32 s95, s88, 59392
v_mul_f32 v154, v154, v175
v_mul_f32 v155, v155, v175
MM v[4:7],a[12:15],a[228:231],v[4:7]
v_mul_f32 v156, v156, v175
v_mul_f32 v157, v157, v175
MM v[12:15],a[12:15],a[236:239],v[12:15]
v_mul_f32 v158, v158, v175
v_mul_f32 v159, v159, v175
MM v[12:15],a[8:11],a[232:235],v[12:15]
buffer_load_dwordx4 v220, s[32:35], s95 offen offset:2048 lds
MM v[20:23],a[8:11],a[240:243],v[20:23]
s_add_u32 s95, s88, 68608
v_mul_f32 v160, v160, v175
v_mul_f32 v161, v161, v175
MM v[20:23],a[12:15],a[244:247],v[20:23]
v_mul_f32 v162, v162, v175
v_mul_f32 v163, v163, v175
MM v[28:31],a[12:15],a[252:255],v[28:31]
v_mul_f32 v164, v164, v175
v_mul_f32 v165, v165, v175
MM v[28:31],a[8:11],a[248:251],v[28:31]
buffer_load_dwordx4 v221, s[32:35], s95 offen offset:3072 lds
v_smfmac_f32_16x16x64_bf16 v[184:187], v[236:239], a[0:7], v234
s_add_u32 s87, s89, s85
s_add_u32 s85, s85, 524288
v_mul_f32 v166, v166, v175
v_smfmac_f32_16x16x64_bf16 v[188:191], v[236:239], a[8:15], v234
s_cmp_ge_u32 s85, 5242880
s_cselect_b32 s85, 0, s85
MM v[32:35],a[16:19],a[224:227],v[32:35]
s_add_u32 m0, s96, 81984
TR a[0:1],v196 offset:16448
TR a[2:3],v196 offset:16960
MM v[32:35],a[20:23],a[228:231],v[32:35]
TR a[4:5],v196 offset:24672
TR a[6:7],v196 offset:25184
v_mul_f32 v167, v167, v175
MM v[40:43],a[20:23],a[236:239],v[40:43]
TR a[8:9],v196 offset:16456
TR a[10:11],v196 offset:16968
MM v[40:43],a[16:19],a[232:235],v[40:43]
TR a[12:13],v196 offset:24680
TR a[14:15],v196 offset:25192
buffer_load_dwordx4 v253, s[16:19], s87 offen offset:0 lds
MM v[48:51],a[16:19],a[240:243],v[48:51]
s_add_u32 m0, s96, 83008
CV v128,v148,v149
CV v129,v150,v151
MM v[48:51],a[20:23],a[244:247],v[48:51]
buffer_load_dwordx4 v254, s[16:19], s87 offen offset:0 lds
MM v[56:59],a[20:23],a[252:255],v[56:59]
s_add_u32 s87, s87, 65536
s_add_u32 m0, s96, 84040
CV v130,v152,v153
MM v[56:59],a[16:19],a[248:251],v[56:59]
CV v131,v154,v155
CV v132,v156,v157
MM v[36:39],a[24:27],a[224:227],v[36:39]
TR a[16:17],v197 offset:16448
TR a[18:19],v197 offset:16960
MM v[36:39],a[28:31],a[228:231],v[36:39]
TR a[20:21],v197 offset:24672
TR a[22:23],v197 offset:25184
CV v133,v158,v159
MM v[44:47],a[28:31],a[236:239],v[44:47]
buffer_load_dwordx4 v253, s[16:19], s87 offen offset:0 lds
MM v[44:47],a[24:27],a[232:235],v[44:47]
s_add_u32 m0, s96, 85064
CV v134,v160,v161
CV v135,v162,v163
MM v[52:55],a[24:27],a[240:243],v[52:55]
CV v136,v164,v165
CV v137,v166,v167
MM v[52:55],a[28:31],a[244:247],v[52:55]
buffer_load_dwordx4 v254, s[16:19], s87 offen offset:0 lds
MM v[60:63],a[28:31],a[252:255],v[60:63]
s_waitcnt vmcnt(16)
RD a[128:131],v222 offset:0
v_xor_b32 v233, 32, v232
MM v[60:63],a[24:27],a[248:251],v[60:63]
s_add_u32 s77, %[slds], 65536
RD a[132:135],v223 offset:0
v_add_u32 v232, s77, v232
MM v[64:67],a[32:35],a[224:227],v[64:67]
TR a[24:25],v197 offset:16456
)ASM"
R"ASM(
TR a[26:27],v197 offset:16968
MM v[64:67],a[36:39],a[228:231],v[64:67]
TR a[28:29],v197 offset:24680
TR a[30:31],v197 offset:25192
MM v[72:75],a[36:39],a[236:239],v[72:75]
RD a[136:139],v222 offset:2048
v_add_u32 v233, s77, v233
MM v[72:75],a[32:35],a[232:235],v[72:75]
RD a[140:143],v223 offset:2048
buffer_store_dwordx4 v[128:131], v168, s[20:23], s82 offen offset:0 sc1
v_lshlrev_b32 v214, 8, v211
MM v[80:83],a[32:35],a[240:243],v[80:83]
RD a[144:147],v222 offset:4096
v_add_u32 v214, %[slds], v214
MM v[80:83],a[36:39],a[244:247],v[80:83]
RD a[148:151],v223 offset:4096
v_xor_b32 v215, v211, v212
MM v[88:91],a[36:39],a[252:255],v[88:91]
s_lshl_b32 s77, %[swave], 2
RD a[152:155],v222 offset:6144
v_xor_b32 v213, s77, v215
MM v[88:91],a[32:35],a[248:251],v[88:91]
s_waitcnt lgkmcnt(15)
RD a[156:159],v223 offset:6144
buffer_store_dwordx4 v[132:135], v168, s[20:23], s82 offen offset:512 sc1
v_xor_b32 v216, 0, v213
MM v[68:71],a[40:43],a[224:227],v[68:71]
TR a[32:33],v198 offset:16448
TR a[34:35],v198 offset:16960
MM v[68:71],a[44:47],a[228:231],v[68:71]
TR a[36:37],v198 offset:24672
TR a[38:39],v198 offset:25184
MM v[76:79],a[44:47],a[236:239],v[76:79]
buffer_store_dwordx2 v[136:137], v181, s[20:23], s82 offen offset:1024 sc1
MM v[76:79],a[40:43],a[232:235],v[76:79]
s_waitcnt lgkmcnt(4)
s_add_u32 s88, s68, s86
s_add_u32 s86, s86, 128
MM v[84:87],a[40:43],a[240:243],v[84:87]
s_cmp_ge_u32 s86, 1280
s_cselect_b32 s86, 0, s86
MM v[84:87],a[44:47],a[244:247],v[84:87]
s_add_u32 m0, s94, 0
s_add_u32 s95, s88, 0
v_lshlrev_b32 v148, 16, v128
MM v[92:95],a[44:47],a[252:255],v[92:95]
buffer_load_dwordx4 v220, s[32:35], s95 offen offset:0 lds
MM v[92:95],a[40:43],a[248:251],v[92:95]
s_add_u32 s95, s88, 9216
v_and_b32 v149, 0xffff0000, v128
v_lshlrev_b32 v176, 16, v138
MM v[96:99],a[48:51],a[224:227],v[96:99]
TR a[40:41],v198 offset:16456
TR a[42:43],v198 offset:16968
MM v[96:99],a[52:55],a[228:231],v[96:99]
TR a[44:45],v198 offset:24680
TR a[46:47],v198 offset:25192
v_and_b32 v177, 0xffff0000, v138
MM v[104:107],a[52:55],a[236:239],v[104:107]
buffer_load_dwordx4 v221, s[32:35], s95 offen offset:1024 lds
MM v[104:107],a[48:51],a[232:235],v[104:107]
s_add_u32 s95, s88, 18432
v_mul_f32 v170, v148, v176
v_mul_f32 v171, v149, v177
MM v[112:115],a[48:51],a[240:243],v[112:115]
buffer_load_dwordx4 v220, s[32:35], s95 offen offset:2048 lds
MM v[112:115],a[52:55],a[244:247],v[112:115]
s_add_u32 s95, s88, 27648
v_lshlrev_b32 v148, 16, v129
v_and_b32 v149, 0xffff0000, v129
MM v[120:123],a[52:55],a[252:255],v[120:123]
v_lshlrev_b32 v176, 16, v139
v_and_b32 v177, 0xffff0000, v139
MM v[120:123],a[48:51],a[248:251],v[120:123]
v_fma_f32 v170, v148, v176, v170
v_fma_f32 v171, v149, v177, v171
MM v[100:103],a[56:59],a[224:227],v[100:103]
TR a[48:49],v199 offset:16448
TR a[50:51],v199 offset:16960
MM v[100:103],a[60:63],a[228:231],v[100:103]
TR a[52:53],v199 offset:24672
TR a[54:55],v199 offset:25184
v_lshlrev_b32 v148, 16, v130
MM v[108:111],a[60:63],a[236:239],v[108:111]
buffer_load_dwordx4 v221, s[32:35], s95 offen offset:3072 lds
MM v[108:111],a[56:59],a[232:235],v[108:111]
s_sub_u32 s88, s88, s63
s_add_u32 m0, s94, 4096
s_add_u32 s95, s88, 40960
MM v[116:119],a[56:59],a[240:243],v[116:119]
buffer_load_dwordx4 v220, s[32:35], s95 offen offset:0 lds
v_and_b32 v149, 0xffff0000, v130
MM v[116:119],a[60:63],a[244:247],v[116:119]
s_add_u32 s95, s88, 50176
v_lshlrev_b32 v176, 16, v140
v_and_b32 v177, 0xffff0000, v140
s_nop 0
LPG2_%=:
s_cbranch_execz LPG6_%=
MM v[124:127],a[60:63],a[252:255],v[124:127]
v_fma_f32 v170, v148, v176, v170
v_fma_f32 v171, v149, v177, v171
MM v[124:127],a[56:59],a[248:251],v[124:127]
v_lshlrev_b32 v148, 16, v131
v_and_b32 v149, 0xffff0000, v131
s_waitcnt vmcnt(20)
s_barrier
MM v[0:3],a[0:3],a[128:131],v[0:3]
TR a[56:57],v199 offset:16456
TR a[58:59],v199 offset:16968
MM v[0:3],a[4:7],a[132:135],v[0:3]
TR a[60:61],v199 offset:24680
TR a[62:63],v199 offset:25192
v_lshlrev_b32 v176, 16, v141
MM v[8:11],a[4:7],a[140:143],v[8:11]
buffer_load_dwordx4 v221, s[32:35], s95 offen offset:1024 lds
MM v[8:11],a[0:3],a[136:139],v[8:11]
s_add_u32 s95, s88, 59392
v_and_b32 v177, 0xffff0000, v141
v_fma_f32 v170, v148, v176, v170
MM v[16:19],a[0:3],a[144:147],v[16:19]
buffer_load_dwordx4 v220, s[32:35], s95 offen offset:2048 lds
MM v[16:19],a[4:7],a[148:151],v[16:19]
s_add_u32 s95, s88, 68608
v_fma_f32 v171, v149, v177, v171
v_lshlrev_b32 v148, 16, v132
MM v[24:27],a[4:7],a[156:159],v[24:27]
v_and_b32 v149, 0xffff0000, v132
v_lshlrev_b32 v176, 16, v142
MM v[24:27],a[0:3],a[152:155],v[24:27]
v_and_b32 v177, 0xffff0000, v142
v_fma_f32 v170, v148, v176, v170
MM v[4:7],a[8:11],a[128:131],v[4:7]
buffer_load_dwordx4 v221, s[32:35], s95 offen offset:3072 lds
MM v[4:7],a[12:15],a[132:135],v[4:7]
s_add_u32 s87, s89, s85
s_add_u32 s85, s85, 524288
v_fma_f32 v171, v149, v177, v171
MM v[12:15],a[12:15],a[140:143],v[12:15]
s_cmp_ge_u32 s85, 5242880
s_cselect_b32 s85, 0, s85
s_add_u32 m0, s96, 98432
MM v[12:15],a[8:11],a[136:139],v[12:15]
v_lshlrev_b32 v148, 16, v133
v_and_b32 v149, 0xffff0000, v133
MM v[20:23],a[8:11],a[144:147],v[20:23]
buffer_load_dwordx4 v253, s[16:19], s87 offen offset:0 lds
MM v[20:23],a[12:15],a[148:151],v[20:23]
s_add_u32 m0, s96, 99456
v_lshlrev_b32 v176, 16, v143
v_and_b32 v177, 0xffff0000, v143
MM v[28:31],a[12:15],a[156:159],v[28:31]
v_fma_f32 v170, v148, v176, v170
v_fma_f32 v171, v149, v177, v171
MM v[28:31],a[8:11],a[152:155],v[28:31]
v_lshlrev_b32 v148, 16, v134
v_and_b32 v149, 0xffff0000, v134
v_smfmac_f32_16x16x64_bf16 v[184:187], v[236:239], a[0:7], v234
v_lshlrev_b32 v176, 16, v144
v_smfmac_f32_16x16x64_bf16 v[188:191], v[236:239], a[8:15], v234
v_and_b32 v177, 0xffff0000, v144
MM v[32:35],a[16:19],a[128:131],v[32:35]
TR a[0:1],v192 offset:0
TR a[2:3],v192 offset:512
buffer_load_dwordx4 v254, s[16:19], s87 offen offset:0 lds
MM v[32:35],a[20:23],a[132:135],v[32:35]
s_add_u32 s87, s87, 65536
TR a[4:5],v192 offset:8224
TR a[6:7],v192 offset:8736
v_fma_f32 v170, v148, v176, v170
MM v[40:43],a[20:23],a[140:143],v[40:43]
s_add_u32 m0, s96, 100488
TR a[8:9],v192 offset:8
TR a[10:11],v192 offset:520
MM v[40:43],a[16:19],a[136:139],v[40:43]
TR a[12:13],v192 offset:8232
TR a[14:15],v192 offset:8744
MM v[48:51],a[16:19],a[144:147],v[48:51]
buffer_load_dwordx4 v253, s[16:19], s87 offen offset:0 lds
v_fma_f32 v171, v149, v177, v171
MM v[48:51],a[20:23],a[148:151],v[48:51]
s_add_u32 m0, s96, 101512
v_lshlrev_b32 v148, 16, v135
v_and_b32 v149, 0xffff0000, v135
MM v[56:59],a[20:23],a[156:159],v[56:59]
buffer_load_dwordx4 v254, s[16:19], s87 offen offset:0 lds
MM v[56:59],a[16:19],a[152:155],v[56:59]
s_waitcnt vmcnt(19)
RD a[160:163],v222 offset:32768
v_lshlrev_b32 v176, 16, v145
MM v[36:39],a[24:27],a[128:131],v[36:39]
TR a[16:17],v193 offset:0
TR a[18:19],v193 offset:512
MM v[36:39],a[28:31],a[132:135],v[36:39]
TR a[20:21],v193 offset:8224
TR a[22:23],v193 offset:8736
v_and_b32 v177, 0xffff0000, v145
MM v[44:47],a[28:31],a[140:143],v[44:47]
RD a[164:167],v223 offset:32768
v_fma_f32 v170, v148, v176, v170
MM v[44:47],a[24:27],a[136:139],v[44:47]
RD a[168:171],v222 offset:34816
v_fma_f32 v171, v149, v177, v171
MM v[52:55],a[24:27],a[144:147],v[52:55]
RD a[172:175],v223 offset:34816
v_lshlrev_b32 v148, 16, v136
MM v[52:55],a[28:31],a[148:151],v[52:55]
RD a[176:179],v222 offset:36864
v_and_b32 v149, 0xffff0000, v136
MM v[60:63],a[28:31],a[156:159],v[60:63]
RD a[180:183],v223 offset:36864
v_lshlrev_b32 v176, 16, v146
MM v[60:63],a[24:27],a[152:155],v[60:63]
s_waitcnt lgkmcnt(15)
RD a[184:187],v222 offset:38912
v_and_b32 v177, 0xffff0000, v146
MM v[64:67],a[32:35],a[128:131],v[64:67]
TR a[24:25],v193 offset:8
TR a[26:27],v193 offset:520
MM v[64:67],a[36:39],a[132:135],v[64:67]
TR a[28:29],v193 offset:8232
TR a[30:31],v193 offset:8744
v_fma_f32 v170, v148, v176, v170
MM v[72:75],a[36:39],a[140:143],v[72:75]
RD a[188:191],v223 offset:38912
v_fma_f32 v171, v149, v177, v171
MM v[72:75],a[32:35],a[136:139],v[72:75]
v_lshlrev_b32 v148, 16, v137
v_and_b32 v149, 0xffff0000, v137
MM v[80:83],a[32:35],a[144:147],v[80:83]
v_lshlrev_b32 v176, 16, v147
v_and_b32 v177, 0xffff0000, v147
MM v[80:83],a[36:39],a[148:151],v[80:83]
v_fma_f32 v170, v148, v176, v170
v_fma_f32 v171, v149, v177, v171
MM v[88:91],a[36:39],a[156:159],v[88:91]
s_waitcnt lgkmcnt(0)
s_add_u32 s88, s68, s86
s_add_u32 s86, s86, 128
MM v[88:91],a[32:35],a[152:155],v[88:91]
s_cmp_ge_u32 s86, 1280
s_cselect_b32 s86, 0, s86
s_add_u32 m0, s94, 32768
v_add_f32 v170, v170, v171
MM v[68:71],a[40:43],a[128:131],v[68:71]
s_add_u32 s95, s88, 0
TR a[32:33],v194 offset:0
TR a[34:35],v194 offset:512
MM v[68:71],a[44:47],a[132:135],v[68:71]
TR a[36:37],v194 offset:8224
TR a[38:39],v194 offset:8736
MM v[76:79],a[44:47],a[140:143],v[76:79]
buffer_load_dwordx4 v220, s[32:35], s95 offen offset:0 lds
v_add_f32_dpp v170, v170, v170 row_ror:8 row_mask:0xf bank_mask:0xf
MM v[76:79],a[40:43],a[136:139],v[76:79]
s_add_u32 s95, s88, 9216
v_add_f32_dpp v170, v170, v170 row_ror:4 row_mask:0xf bank_mask:0xf
v_lshl_add_u32 v224, v216, 4, v214
MM v[84:87],a[40:43],a[144:147],v[84:87]
buffer_load_dwordx4 v221, s[32:35], s95 offen offset:1024 lds
MM v[84:87],a[44:47],a[148:151],v[84:87]
s_add_u32 s95, s88, 18432
v_add_f32_dpp v170, v170, v170 row_ror:2 row_mask:0xf bank_mask:0xf
v_xor_b32 v216, 4, v213
MM v[92:95],a[44:47],a[156:159],v[92:95]
v_add_f32_dpp v170, v170, v170 row_ror:1 row_mask:0xf bank_mask:0xf
v_mov_b32 v173, v170
MM v[92:95],a[40:43],a[152:155],v[92:95]
s_nop 0
v_permlane16_swap_b32 v170, v173
v_add_f32 v170, v170, v173
MM v[96:99],a[48:51],a[128:131],v[96:99]
TR a[40:41],v194 offset:8
TR a[42:43],v194 offset:520
MM v[96:99],a[52:55],a[132:135],v[96:99]
TR a[44:45],v194 offset:8232
TR a[46:47],v194 offset:8744
CV v170,v170,v170
MM v[104:107],a[52:55],a[140:143],v[104:107]
buffer_load_dwordx4 v220, s[32:35], s95 offen offset:2048 lds
MM v[104:107],a[48:51],a[136:139],v[104:107]
s_add_u32 s95, s88, 27648
v_ashrrev_i32 v171, 31, v170
v_or_b32 v171, 0x80000000, v171
MM v[112:115],a[48:51],a[144:147],v[112:115]
buffer_load_dwordx4 v221, s[32:35], s95 offen offset:3072 lds
MM v[112:115],a[52:55],a[148:151],v[112:115]
s_sub_u32 s88, s88, s63
s_add_u32 m0, s94, 36864
s_add_u32 s95, s88, 40960
v_xor_b32 v170, v170, v171
MM v[120:123],a[52:55],a[156:159],v[120:123]
v_and_b32 v170, 0xffff0000, v170
v_sub_u32 v171, s79, v180
MM v[120:123],a[48:51],a[152:155],v[120:123]
v_or_b32 v170, v170, v171
v_lshlrev_b32 v173, 2, v180
MM v[100:103],a[56:59],a[128:131],v[100:103]
TR a[48:49],v195 offset:0
TR a[50:51],v195 offset:512
MM v[100:103],a[60:63],a[132:135],v[100:103]
TR a[52:53],v195 offset:8224
TR a[54:55],v195 offset:8736
MM v[108:111],a[60:63],a[140:143],v[108:111]
buffer_load_dwordx4 v220, s[32:35], s95 offen offset:0 lds
s_mov_b32 exec_lo, 1
s_mov_b32 exec_hi, 1
buffer_store_dword v170, v173, s[20:23], s78 offen offset:0 sc1
s_mov_b64 exec, -1
MM v[108:111],a[56:59],a[136:139],v[108:111]
s_add_u32 s95, s88, 50176
v_mov_b32 v171, s69
v_lshl_add_u32 v225, v216, 4, v214
MM v[116:119],a[56:59],a[144:147],v[116:119]
buffer_load_dwordx4 v221, s[32:35], s95 offen offset:1024 lds
MM v[116:119],a[60:63],a[148:151],v[116:119]
s_add_u32 s95, s88, 59392
v_xor_b32 v216, 8, v213
v_lshl_add_u32 v226, v216, 4, v214
MM v[124:127],a[60:63],a[156:159],v[124:127]
v_xor_b32 v216, 12, v213
v_lshl_add_u32 v227, v216, 4, v214
MM v[124:127],a[56:59],a[152:155],v[124:127]
v_lshlrev_b32 v214, 8, v212
v_xor_b32 v216, 0, v215
s_waitcnt vmcnt(22)
s_barrier
MM v[0:3],a[0:3],a[160:163],v[0:3]
TR a[56:57],v195 offset:8
TR a[58:59],v195 offset:520
MM v[0:3],a[4:7],a[164:167],v[0:3]
TR a[60:61],v195 offset:8232
TR a[62:63],v195 offset:8744
MM v[8:11],a[4:7],a[172:175],v[8:11]
buffer_load_dwordx4 v220, s[32:35], s95 offen offset:2048 lds
MM v[8:11],a[0:3],a[168:171],v[8:11]
s_add_u32 s95, s88, 68608
v_lshl_add_u32 v228, v216, 4, v214
v_xor_b32 v216, 4, v215
MM v[16:19],a[0:3],a[176:179],v[16:19]
buffer_load_dwordx4 v221, s[32:35], s95 offen offset:3072 lds
MM v[16:19],a[4:7],a[180:183],v[16:19]
s_add_u32 s87, s89, s85
)ASM"
R"ASM(
s_add_u32 s85, s85, 524288
v_lshl_add_u32 v229, v216, 4, v214
MM v[24:27],a[4:7],a[188:191],v[24:27]
s_cmp_ge_u32 s85, 5242880
s_cselect_b32 s85, 0, s85
s_add_u32 m0, s96, 114880
MM v[24:27],a[0:3],a[184:187],v[24:27]
v_xor_b32 v216, 8, v215
v_lshl_add_u32 v230, v216, 4, v214
MM v[4:7],a[8:11],a[160:163],v[4:7]
buffer_load_dwordx4 v253, s[16:19], s87 offen offset:0 lds
MM v[4:7],a[12:15],a[164:167],v[4:7]
s_add_u32 m0, s96, 115904
v_xor_b32 v216, 12, v215
v_lshl_add_u32 v231, v216, 4, v214
MM v[12:15],a[12:15],a[172:175],v[12:15]
s_lshl_b32 s90, s49, 19
s_add_u32 s8, s8, s90
s_addc_u32 s9, s9, 0
MM v[12:15],a[8:11],a[168:171],v[12:15]
s_mov_b32 s10, 524288
s_mov_b32 s11, 131072
MM v[20:23],a[8:11],a[176:179],v[20:23]
buffer_load_dwordx4 v254, s[16:19], s87 offen offset:0 lds
MM v[20:23],a[12:15],a[180:183],v[20:23]
s_add_u32 s87, s87, 65536
s_add_u32 s12, s12, s90
s_addc_u32 s13, s13, 0
MM v[28:31],a[12:15],a[188:191],v[28:31]
s_add_u32 m0, s96, 116936
s_mov_b32 s14, 525312
s_mov_b32 s15, 131072
MM v[28:31],a[8:11],a[184:187],v[28:31]
s_sub_u32 s12, s12, 1024
s_subb_u32 s13, s13, 0
s_lshr_b32 s90, s49, 3
v_smfmac_f32_16x16x64_bf16 v[184:187], v[236:239], a[0:7], v234
s_mul_i32 s90, s90, 1310720
s_mul_i32 s77, s84, 320
v_smfmac_f32_16x16x64_bf16 v[188:191], v[236:239], a[8:15], v234
s_add_u32 s90, s90, s77
MM v[32:35],a[16:19],a[160:163],v[32:35]
TR a[0:1],v192 offset:16448
TR a[2:3],v192 offset:16960
buffer_load_dwordx4 v253, s[16:19], s87 offen offset:0 lds
MM v[32:35],a[20:23],a[164:167],v[32:35]
s_add_u32 m0, s96, 117960
TR a[4:5],v192 offset:24672
TR a[6:7],v192 offset:25184
MM v[40:43],a[20:23],a[172:175],v[40:43]
TR a[8:9],v192 offset:16456
TR a[10:11],v192 offset:16968
MM v[40:43],a[16:19],a[168:171],v[40:43]
TR a[12:13],v192 offset:24680
TR a[14:15],v192 offset:25192
MM v[48:51],a[16:19],a[176:179],v[48:51]
buffer_load_dwordx4 v254, s[16:19], s87 offen offset:0 lds
MM v[48:51],a[20:23],a[180:183],v[48:51]
s_waitcnt vmcnt(17)
s_add_u32 s24, s24, s90
s_addc_u32 s25, s25, 0
MM v[56:59],a[20:23],a[188:191],v[56:59]
s_sub_u32 s26, 5242880, s90
RD a[192:195],v222 offset:0
s_mov_b32 s27, 131072
MM v[56:59],a[16:19],a[184:187],v[56:59]
s_mov_b32 s38, 2211840
RD a[196:199],v223 offset:0
s_mov_b32 s39, 131072
MM v[36:39],a[24:27],a[160:163],v[36:39]
s_mov_b32 s71, 0x4e800000
TR a[16:17],v193 offset:16448
TR a[18:19],v193 offset:16960
MM v[36:39],a[28:31],a[164:167],v[36:39]
s_mov_b32 s72, 0x3e0293ee
TR a[20:21],v193 offset:24672
TR a[22:23],v193 offset:25184
MM v[44:47],a[28:31],a[172:175],v[44:47]
s_lshl_b32 s64, %[swave], 12
RD a[200:203],v222 offset:2048
MM v[44:47],a[24:27],a[168:171],v[44:47]
RD a[204:207],v223 offset:2048
MM v[52:55],a[24:27],a[176:179],v[52:55]
RD a[208:211],v222 offset:4096
MM v[52:55],a[28:31],a[180:183],v[52:55]
RD a[212:215],v223 offset:4096
MM v[60:63],a[28:31],a[188:191],v[60:63]
RD a[216:219],v222 offset:6144
MM v[60:63],a[24:27],a[184:187],v[60:63]
s_waitcnt lgkmcnt(15)
RD a[220:223],v223 offset:6144
MM v[64:67],a[32:35],a[160:163],v[64:67]
TR a[24:25],v193 offset:16456
TR a[26:27],v193 offset:16968
MM v[64:67],a[36:39],a[164:167],v[64:67]
TR a[28:29],v193 offset:24680
TR a[30:31],v193 offset:25192
MM v[72:75],a[36:39],a[172:175],v[72:75]
MM v[72:75],a[32:35],a[168:171],v[72:75]
s_waitcnt lgkmcnt(4)
s_add_u32 s88, s68, s86
s_add_u32 s86, s86, 128
MM v[80:83],a[32:35],a[176:179],v[80:83]
s_cmp_ge_u32 s86, 1280
s_cselect_b32 s86, 0, s86
s_add_u32 m0, s94, 0
MM v[80:83],a[36:39],a[180:183],v[80:83]
s_add_u32 s95, s88, 0
buffer_load_dwordx4 v220, s[32:35], s95 offen offset:0 lds
MM v[88:91],a[36:39],a[188:191],v[88:91]
s_add_u32 s95, s88, 9216
MM v[88:91],a[32:35],a[184:187],v[88:91]
buffer_load_dwordx4 v221, s[32:35], s95 offen offset:1024 lds
MM v[68:71],a[40:43],a[160:163],v[68:71]
s_add_u32 s95, s88, 18432
TR a[32:33],v194 offset:16448
TR a[34:35],v194 offset:16960
MM v[68:71],a[44:47],a[164:167],v[68:71]
TR a[36:37],v194 offset:24672
TR a[38:39],v194 offset:25184
MM v[76:79],a[44:47],a[172:175],v[76:79]
MM v[76:79],a[40:43],a[168:171],v[76:79]
buffer_load_dwordx4 v220, s[32:35], s95 offen offset:2048 lds
MM v[84:87],a[40:43],a[176:179],v[84:87]
s_add_u32 s95, s88, 27648
MM v[84:87],a[44:47],a[180:183],v[84:87]
MM v[92:95],a[44:47],a[188:191],v[92:95]
MM v[92:95],a[40:43],a[184:187],v[92:95]
buffer_load_dwordx4 v221, s[32:35], s95 offen offset:3072 lds
MM v[96:99],a[48:51],a[160:163],v[96:99]
s_sub_u32 s88, s88, s63
TR a[40:41],v194 offset:16456
TR a[42:43],v194 offset:16968
MM v[96:99],a[52:55],a[164:167],v[96:99]
s_add_u32 m0, s94, 4096
TR a[44:45],v194 offset:24680
TR a[46:47],v194 offset:25192
MM v[104:107],a[52:55],a[172:175],v[104:107]
s_add_u32 s95, s88, 40960
MM v[104:107],a[48:51],a[168:171],v[104:107]
buffer_load_dwordx4 v220, s[32:35], s95 offen offset:0 lds
MM v[112:115],a[48:51],a[176:179],v[112:115]
s_add_u32 s95, s88, 50176
MM v[112:115],a[52:55],a[180:183],v[112:115]
MM v[120:123],a[52:55],a[188:191],v[120:123]
MM v[120:123],a[48:51],a[184:187],v[120:123]
buffer_load_dwordx4 v221, s[32:35], s95 offen offset:1024 lds
MM v[100:103],a[56:59],a[160:163],v[100:103]
s_add_u32 s95, s88, 59392
TR a[48:49],v195 offset:16448
TR a[50:51],v195 offset:16960
MM v[100:103],a[60:63],a[164:167],v[100:103]
TR a[52:53],v195 offset:24672
TR a[54:55],v195 offset:25184
MM v[108:111],a[60:63],a[172:175],v[108:111]
MM v[108:111],a[56:59],a[168:171],v[108:111]
buffer_load_dwordx4 v220, s[32:35], s95 offen offset:2048 lds
MM v[116:119],a[56:59],a[176:179],v[116:119]
s_add_u32 s95, s88, 68608
MM v[116:119],a[60:63],a[180:183],v[116:119]
MM v[124:127],a[60:63],a[188:191],v[124:127]
MM v[124:127],a[56:59],a[184:187],v[124:127]
buffer_load_dwordx4 v221, s[32:35], s95 offen offset:3072 lds
s_waitcnt vmcnt(21)
s_barrier
MM v[0:3],a[0:3],a[192:195],v[0:3]
s_add_u32 s87, s89, s85
TR a[56:57],v195 offset:16456
TR a[58:59],v195 offset:16968
MM v[0:3],a[4:7],a[196:199],v[0:3]
s_add_u32 s85, s85, 524288
TR a[60:61],v195 offset:24680
TR a[62:63],v195 offset:25192
MM v[8:11],a[4:7],a[204:207],v[8:11]
s_cmp_ge_u32 s85, 5242880
s_cselect_b32 s85, 0, s85
s_add_u32 m0, s96, 131328
v_and_b32 v208, 240, v248
MM v[8:11],a[0:3],a[200:203],v[8:11]
s_lshl_b32 s62, s62, 8
buffer_load_dwordx4 v253, s[16:19], s87 offen offset:0 lds
MM v[16:19],a[0:3],a[208:211],v[16:19]
s_add_u32 m0, s96, 132352
s_add_u32 s93, s93, 36864
s_add_u32 s62, s62, s93
MM v[16:19],a[4:7],a[212:215],v[16:19]
s_xor_b32 s93, s62, 256
MM v[24:27],a[4:7],a[220:223],v[24:27]
MM v[24:27],a[0:3],a[216:219],v[24:27]
buffer_load_dwordx4 v254, s[16:19], s87 offen offset:0 lds
MM v[4:7],a[8:11],a[192:195],v[4:7]
s_add_u32 s87, s87, 65536
s_add_u32 m0, s96, 133384
MM v[4:7],a[12:15],a[196:199],v[4:7]
MM v[12:15],a[12:15],a[204:207],v[12:15]
MM v[12:15],a[8:11],a[200:203],v[12:15]
buffer_load_dwordx4 v253, s[16:19], s87 offen offset:0 lds
MM v[20:23],a[8:11],a[208:211],v[20:23]
s_add_u32 m0, s96, 134408
MM v[20:23],a[12:15],a[212:215],v[20:23]
MM v[28:31],a[12:15],a[220:223],v[28:31]
MM v[28:31],a[8:11],a[216:219],v[28:31]
buffer_load_dwordx4 v254, s[16:19], s87 offen offset:0 lds
v_smfmac_f32_16x16x64_bf16 v[184:187], v[236:239], a[0:7], v234
s_waitcnt vmcnt(16)
RD a[224:227],v222 offset:32768
v_smfmac_f32_16x16x64_bf16 v[188:191], v[236:239], a[8:15], v234
RD a[228:231],v223 offset:32768
MM v[32:35],a[16:19],a[192:195],v[32:35]
TR a[0:1],v192 offset:32896
TR a[2:3],v192 offset:33408
MM v[32:35],a[20:23],a[196:199],v[32:35]
TR a[4:5],v192 offset:41120
TR a[6:7],v192 offset:41632
MM v[40:43],a[20:23],a[204:207],v[40:43]
TR a[8:9],v192 offset:32904
TR a[10:11],v192 offset:33416
RD a[232:235],v222 offset:34816
MM v[40:43],a[16:19],a[200:203],v[40:43]
TR a[12:13],v192 offset:41128
TR a[14:15],v192 offset:41640
buffer_load_dwordx4 v[200:203], v208, s[20:23], s62 offen offset:0 sc1
MM v[48:51],a[16:19],a[208:211],v[48:51]
RD a[236:239],v223 offset:34816
MM v[48:51],a[20:23],a[212:215],v[48:51]
buffer_load_dwordx4 v[204:207], v208, s[20:23], s93 offen offset:0 sc1
MM v[56:59],a[20:23],a[220:223],v[56:59]
RD a[240:243],v222 offset:36864
MM v[56:59],a[16:19],a[216:219],v[56:59]
s_waitcnt lgkmcnt(15)
RD a[244:247],v223 offset:36864
MM v[36:39],a[24:27],a[192:195],v[36:39]
TR a[16:17],v193 offset:32896
TR a[18:19],v193 offset:33408
MM v[36:39],a[28:31],a[196:199],v[36:39]
TR a[20:21],v193 offset:41120
TR a[22:23],v193 offset:41632
MM v[44:47],a[28:31],a[204:207],v[44:47]
RD a[248:251],v222 offset:38912
MM v[44:47],a[24:27],a[200:203],v[44:47]
RD a[252:255],v223 offset:38912
MM v[52:55],a[24:27],a[208:211],v[52:55]
MM v[52:55],a[28:31],a[212:215],v[52:55]
MM v[60:63],a[28:31],a[220:223],v[60:63]
MM v[60:63],a[24:27],a[216:219],v[60:63]
s_waitcnt lgkmcnt(0)
s_add_u32 s88, s68, s86
s_add_u32 s86, s86, 128
MM v[64:67],a[32:35],a[192:195],v[64:67]
TR a[24:25],v193 offset:32904
TR a[26:27],v193 offset:33416
MM v[64:67],a[36:39],a[196:199],v[64:67]
TR a[28:29],v193 offset:41128
TR a[30:31],v193 offset:41640
s_cmp_ge_u32 s86, 1280
s_cselect_b32 s86, 0, s86
MM v[72:75],a[36:39],a[204:207],v[72:75]
s_add_u32 m0, s94, 32768
s_add_u32 s95, s88, 0
MM v[72:75],a[32:35],a[200:203],v[72:75]
buffer_load_dwordx4 v220, s[32:35], s95 offen offset:0 lds
MM v[80:83],a[32:35],a[208:211],v[80:83]
s_add_u32 s95, s88, 9216
MM v[80:83],a[36:39],a[212:215],v[80:83]
buffer_load_dwordx4 v221, s[32:35], s95 offen offset:1024 lds
MM v[88:91],a[36:39],a[220:223],v[88:91]
s_add_u32 s95, s88, 18432
MM v[88:91],a[32:35],a[216:219],v[88:91]
MM v[68:71],a[40:43],a[192:195],v[68:71]
TR a[32:33],v194 offset:32896
TR a[34:35],v194 offset:33408
MM v[68:71],a[44:47],a[196:199],v[68:71]
TR a[36:37],v194 offset:41120
TR a[38:39],v194 offset:41632
MM v[76:79],a[44:47],a[204:207],v[76:79]
buffer_load_dwordx4 v220, s[32:35], s95 offen offset:2048 lds
MM v[76:79],a[40:43],a[200:203],v[76:79]
s_add_u32 s95, s88, 27648
MM v[84:87],a[40:43],a[208:211],v[84:87]
MM v[84:87],a[44:47],a[212:215],v[84:87]
buffer_load_dwordx4 v221, s[32:35], s95 offen offset:3072 lds
MM v[92:95],a[44:47],a[220:223],v[92:95]
s_waitcnt vmcnt(10)
RD a[128:131],v222 offset:0
MM v[92:95],a[40:43],a[216:219],v[92:95]
RD a[132:135],v223 offset:0
MM v[96:99],a[48:51],a[192:195],v[96:99]
TR a[40:41],v194 offset:32904
TR a[42:43],v194 offset:33416
MM v[96:99],a[52:55],a[196:199],v[96:99]
TR a[44:45],v194 offset:41128
TR a[46:47],v194 offset:41640
MM v[104:107],a[52:55],a[204:207],v[104:107]
RD a[136:139],v222 offset:2048
MM v[104:107],a[48:51],a[200:203],v[104:107]
RD a[140:143],v223 offset:2048
MM v[112:115],a[48:51],a[208:211],v[112:115]
RD a[144:147],v222 offset:4096
MM v[112:115],a[52:55],a[212:215],v[112:115]
RD a[148:151],v223 offset:4096
MM v[120:123],a[52:55],a[220:223],v[120:123]
RD a[152:155],v222 offset:6144
MM v[120:123],a[48:51],a[216:219],v[120:123]
RD a[156:159],v223 offset:6144
MM v[100:103],a[56:59],a[192:195],v[100:103]
s_sub_u32 s88, s88, s63
TR a[48:49],v195 offset:32896
TR a[50:51],v195 offset:33408
MM v[100:103],a[60:63],a[196:199],v[100:103]
s_nop 0
LPG3_%=:
s_cbranch_execz LPG7_%=
s_add_u32 m0, s94, 36864
TR a[52:53],v195 offset:41120
TR a[54:55],v195 offset:41632
MM v[108:111],a[60:63],a[204:207],v[108:111]
s_add_u32 s95, s88, 40960
buffer_load_dwordx4 v220, s[32:35], s95 offen offset:0 lds
MM v[108:111],a[56:59],a[200:203],v[108:111]
s_add_u32 s95, s88, 50176
MM v[116:119],a[56:59],a[208:211],v[116:119]
buffer_load_dwordx4 v221, s[32:35], s95 offen offset:1024 lds
MM v[116:119],a[60:63],a[212:215],v[116:119]
s_add_u32 s95, s88, 59392
MM v[124:127],a[60:63],a[220:223],v[124:127]
MM v[124:127],a[56:59],a[216:219],v[124:127]
buffer_load_dwordx4 v220, s[32:35], s95 offen offset:2048 lds
s_add_u32 s95, s88, 68608
buffer_load_dwordx4 v221, s[32:35], s95 offen offset:3072 lds
s_waitcnt lgkmcnt(4)
s_waitcnt vmcnt(10)
s_barrier
MM v[0:3],a[0:3],a[224:227],v[0:3]
s_add_u32 s77, s64, 0
TR a[56:57],v195 offset:32904
TR a[58:59],v195 offset:33416
MM v[0:3],a[4:7],a[228:231],v[0:3]
s_add_u32 m0, s65, 0
TR a[60:61],v195 offset:41128
TR a[62:63],v195 offset:41640
MM v[8:11],a[4:7],a[236:239],v[8:11]
)ASM"
R"ASM(
s_waitcnt vmcnt(8)
TR a[64:65],v196 offset:0
TR a[66:67],v196 offset:512
MM v[8:11],a[0:3],a[232:235],v[8:11]
s_lshl_b32 s76, %[swave], 7
buffer_load_dwordx4 v228, s[8:11], s77 offen offset:0 lds
v_lshrrev_b32 v253, 8, v248
v_lshlrev_b32 v253, 5, v253
MM v[16:19],a[0:3],a[240:243],v[16:19]
s_add_u32 s76, s76, %[slds]
TR a[68:69],v196 offset:8224
TR a[70:71],v196 offset:8736
v_xor_b32 v201, s69, v201
MM v[16:19],a[4:7],a[244:247],v[16:19]
s_mov_b64 exec, 1
buffer_store_dword v171, off, s[20:23], s80 sc1
s_mov_b64 exec, -1
v_add_u32 v253, s76, v253
v_add_u32 v253, 151552, v253
MM v[24:27],a[4:7],a[252:255],v[24:27]
TR a[72:73],v196 offset:16448
TR a[74:75],v196 offset:16960
MM v[24:27],a[0:3],a[248:251],v[24:27]
TR a[76:77],v196 offset:24672
TR a[78:79],v196 offset:25184
buffer_load_dwordx4 v229, s[8:11], s77 offen offset:1024 lds
v_xor_b32 v203, s69, v203
MM v[4:7],a[8:11],a[224:227],v[4:7]
TR a[80:81],v196 offset:8
TR a[82:83],v196 offset:520
MM v[4:7],a[12:15],a[228:231],v[4:7]
TR a[84:85],v196 offset:8232
TR a[86:87],v196 offset:8744
MM v[12:15],a[12:15],a[236:239],v[12:15]
TR a[88:89],v196 offset:16456
TR a[90:91],v196 offset:16968
v_xor_b32 v205, s69, v205
MM v[12:15],a[8:11],a[232:235],v[12:15]
TR a[92:93],v196 offset:24680
TR a[94:95],v196 offset:25192
buffer_load_dwordx4 v230, s[8:11], s77 offen offset:2048 lds
MM v[20:23],a[8:11],a[240:243],v[20:23]
TR a[96:97],v197 offset:0
TR a[98:99],v197 offset:512
MM v[20:23],a[12:15],a[244:247],v[20:23]
TR a[100:101],v197 offset:8224
TR a[102:103],v197 offset:8736
v_xor_b32 v207, s69, v207
MM v[28:31],a[12:15],a[252:255],v[28:31]
TR a[104:105],v197 offset:16448
TR a[106:107],v197 offset:16960
MM v[28:31],a[8:11],a[248:251],v[28:31]
TR a[108:109],v197 offset:24672
TR a[110:111],v197 offset:25184
buffer_load_dwordx4 v231, s[8:11], s77 offen offset:3072 lds
v_smfmac_f32_16x16x64_bf16 v[184:187], v[236:239], a[0:7], v234
s_add_u32 s77, s64, 16384
TR a[112:113],v197 offset:8
v_or3_b32 v209, v201, v203, v205
v_smfmac_f32_16x16x64_bf16 v[188:191], v[236:239], a[8:15], v234
s_add_u32 m0, s65, 16384
TR a[114:115],v197 offset:520
MM v[32:35],a[16:19],a[224:227],v[32:35]
TR a[116:117],v197 offset:8232
TR a[118:119],v197 offset:8744
MM v[32:35],a[20:23],a[228:231],v[32:35]
TR a[120:121],v197 offset:16456
TR a[122:123],v197 offset:16968
v_or_b32 v209, v209, v207
MM v[40:43],a[20:23],a[236:239],v[40:43]
TR a[124:125],v197 offset:24680
TR a[126:127],v197 offset:25192
MM v[40:43],a[16:19],a[232:235],v[40:43]
TR a[0:1],v198 offset:0
TR a[2:3],v198 offset:512
buffer_load_dwordx4 v228, s[8:11], s77 offen offset:0 lds
MM v[48:51],a[16:19],a[240:243],v[48:51]
TR a[4:5],v198 offset:8224
TR a[6:7],v198 offset:8736
v_cmp_ne_u32 vcc, 0, v209
s_cbranch_vccnz LRG1o_%=
LRG1ob_%=:
MM v[48:51],a[20:23],a[244:247],v[48:51]
TR a[8:9],v198 offset:16448
TR a[10:11],v198 offset:16960
MM v[56:59],a[20:23],a[252:255],v[56:59]
TR a[12:13],v198 offset:24672
TR a[14:15],v198 offset:25184
MM v[56:59],a[16:19],a[248:251],v[56:59]
v_lshlrev_b32 v240, 16, v200
v_and_b32 v200, 0xffff0000, v200
MM v[36:39],a[24:27],a[224:227],v[36:39]
v_rcp_f32 v244, v200
MM v[36:39],a[28:31],a[228:231],v[36:39]
v_xor_b32 v240, 0x80000000, v240
v_mul_f32 v244, s72, v244
MM v[44:47],a[28:31],a[236:239],v[44:47]
v_lshlrev_b32 v241, 16, v202
v_and_b32 v202, 0xffff0000, v202
MM v[44:47],a[24:27],a[232:235],v[44:47]
buffer_load_dwordx4 v229, s[8:11], s77 offen offset:1024 lds
MM v[52:55],a[24:27],a[240:243],v[52:55]
v_rcp_f32 v245, v202
MM v[52:55],a[28:31],a[244:247],v[52:55]
v_xor_b32 v241, 0x80000000, v241
v_mul_f32 v245, s72, v245
MM v[60:63],a[28:31],a[252:255],v[60:63]
v_lshlrev_b32 v242, 16, v204
v_and_b32 v204, 0xffff0000, v204
MM v[60:63],a[24:27],a[248:251],v[60:63]
v_rcp_f32 v246, v204
MM v[64:67],a[32:35],a[224:227],v[64:67]
TR a[16:17],v198 offset:8
TR a[18:19],v198 offset:520
MM v[64:67],a[36:39],a[228:231],v[64:67]
TR a[20:21],v198 offset:8232
TR a[22:23],v198 offset:8744
v_xor_b32 v242, 0x80000000, v242
MM v[72:75],a[36:39],a[236:239],v[72:75]
TR a[24:25],v198 offset:16456
TR a[26:27],v198 offset:16968
MM v[72:75],a[32:35],a[232:235],v[72:75]
TR a[28:29],v198 offset:24680
TR a[30:31],v198 offset:25192
MM v[80:83],a[32:35],a[240:243],v[80:83]
v_mul_f32 v246, s72, v246
v_lshlrev_b32 v243, 16, v206
MM v[80:83],a[36:39],a[244:247],v[80:83]
buffer_load_dwordx4 v230, s[8:11], s77 offen offset:2048 lds
MM v[88:91],a[36:39],a[252:255],v[88:91]
v_and_b32 v206, 0xffff0000, v206
MM v[88:91],a[32:35],a[248:251],v[88:91]
s_waitcnt lgkmcnt(15)
v_rcp_f32 v247, v206
v_smfmac_f32_16x16x64_bf16 v[184:187], v[236:239], a[64:71], v234
v_xor_b32 v243, 0x80000000, v243
v_smfmac_f32_16x16x64_bf16 v[184:187], v[236:239], a[72:79], v234
v_mul_f32 v247, s72, v247
v_smfmac_f32_16x16x64_bf16 v[188:191], v[236:239], a[80:87], v234
v_smfmac_f32_16x16x64_bf16 v[188:191], v[236:239], a[88:95], v234
MM v[68:71],a[40:43],a[224:227],v[68:71]
v_bfe_u32 v183, v248, 6, 2
v_lshlrev_b32 v183, 5, v183
MM v[68:71],a[44:47],a[228:231],v[68:71]
v_bfe_u32 v182, v248, 4, 2
v_lshl_add_u32 v183, v182, 2, v183
MM v[76:79],a[44:47],a[236:239],v[76:79]
v_add_u32 v183, s76, v183
v_add_u32 v183, 151552, v183
MM v[76:79],a[40:43],a[232:235],v[76:79]
buffer_load_dwordx4 v231, s[8:11], s77 offen offset:3072 lds
MM v[84:87],a[40:43],a[240:243],v[84:87]
s_waitcnt vmcnt(9)
RD a[160:163],v222 offset:32768
MM v[84:87],a[44:47],a[244:247],v[84:87]
RD a[164:167],v223 offset:32768
MM v[92:95],a[44:47],a[252:255],v[92:95]
RD a[168:171],v222 offset:34816
MM v[92:95],a[40:43],a[248:251],v[92:95]
v_add_f32 v184, v184, v185
v_add_f32 v188, v188, v189
MM v[96:99],a[48:51],a[224:227],v[96:99]
TR a[32:33],v199 offset:0
TR a[34:35],v199 offset:512
MM v[96:99],a[52:55],a[228:231],v[96:99]
RD a[172:175],v223 offset:34816
ds_write_b32 v183, v184 offset:0
ds_write_b32 v183, v188 offset:16
MM v[104:107],a[52:55],a[236:239],v[104:107]
TR a[36:37],v199 offset:8224
TR a[38:39],v199 offset:8736
MM v[104:107],a[48:51],a[232:235],v[104:107]
TR a[40:41],v199 offset:16448
TR a[42:43],v199 offset:16960
MM v[112:115],a[48:51],a[240:243],v[112:115]
TR a[44:45],v199 offset:24672
TR a[46:47],v199 offset:25184
RD a[176:179],v222 offset:36864
MM v[112:115],a[52:55],a[244:247],v[112:115]
RD a[180:183],v223 offset:36864
MM v[120:123],a[52:55],a[252:255],v[120:123]
RD a[184:187],v222 offset:38912
MM v[120:123],a[48:51],a[248:251],v[120:123]
RD a[188:191],v223 offset:38912
MM v[100:103],a[56:59],a[224:227],v[100:103]
s_add_u32 s77, s64, 0
s_add_u32 s81, s77, 896
s_add_u32 m0, s65, 65536
MM v[100:103],a[60:63],a[228:231],v[100:103]
buffer_load_dwordx4 v235, s[12:15], s81 offen offset:0 lds
MM v[108:111],a[60:63],a[236:239],v[108:111]
MM v[108:111],a[56:59],a[232:235],v[108:111]
MM v[116:119],a[56:59],a[240:243],v[116:119]
MM v[116:119],a[60:63],a[244:247],v[116:119]
MM v[124:127],a[60:63],a[252:255],v[124:127]
MM v[124:127],a[56:59],a[248:251],v[124:127]
buffer_load_dwordx4 v235, s[12:15], s77 offen offset:1024 lds
s_waitcnt lgkmcnt(0)
s_barrier
MM v[0:3],a[64:67],a[128:131],v[0:3]
s_memrealtime s[52:53]
TR a[48:49],v199 offset:8
TR a[50:51],v199 offset:520
MM v[0:3],a[68:71],a[132:135],v[0:3]
s_memtime s[54:55]
TR a[52:53],v199 offset:8232
TR a[54:55],v199 offset:8744
MM v[0:3],a[72:75],a[160:163],v[0:3]
TR a[56:57],v199 offset:16456
TR a[58:59],v199 offset:16968
MM v[0:3],a[76:79],a[164:167],v[0:3]
TR a[60:61],v199 offset:24680
TR a[62:63],v199 offset:25192
MM v[8:11],a[76:79],a[172:175],v[8:11]
v_xor_b32 v151, 0, v253
v_xor_b32 v143, 0, v253
MM v[8:11],a[72:75],a[168:171],v[8:11]
RD v[144:147],v151 offset:0
RD v[148:151],v151 offset:16
MM v[8:11],a[68:71],a[140:143],v[8:11]
RD v[136:139],v143 offset:0
RD v[140:143],v143 offset:16
MM v[8:11],a[64:67],a[136:139],v[8:11]
MM v[4:7],a[80:83],a[128:131],v[4:7]
MM v[4:7],a[84:87],a[132:135],v[4:7]
s_waitcnt lgkmcnt(2)
buffer_load_dwordx4 v235, s[12:15], s81 offen offset:2048 lds
MM v[4:7],a[88:91],a[160:163],v[4:7]
s_waitcnt lgkmcnt(0)
MM v[4:7],a[92:95],a[164:167],v[4:7]
MM v[12:15],a[92:95],a[172:175],v[12:15]
MM v[12:15],a[88:91],a[168:171],v[12:15]
MM v[12:15],a[84:87],a[140:143],v[12:15]
MM v[12:15],a[80:83],a[136:139],v[12:15]
MM v[32:35],a[96:99],a[128:131],v[32:35]
MM v[32:35],a[100:103],a[132:135],v[32:35]
MM v[32:35],a[104:107],a[160:163],v[32:35]
MM v[32:35],a[108:111],a[164:167],v[32:35]
buffer_load_dwordx4 v235, s[12:15], s77 offen offset:3072 lds
v_fmac_f32 v0, v240, v144
v_fmac_f32 v1, v240, v145
v_fmac_f32 v2, v240, v146
MM v[40:43],a[108:111],a[172:175],v[40:43]
s_add_u32 s77, s64, 32768
s_add_u32 m0, s65, 32768
v_fmac_f32 v3, v240, v147
v_fmac_f32 v4, v240, v148
MM v[40:43],a[104:107],a[168:171],v[40:43]
v_fmac_f32 v5, v240, v149
v_fmac_f32 v6, v240, v150
MM v[40:43],a[100:103],a[140:143],v[40:43]
v_fmac_f32 v7, v240, v151
v_mul_f32 v0, v244, v0
v_mul_f32 v1, v244, v1
MM v[40:43],a[96:99],a[136:139],v[40:43]
v_mul_f32 v2, v244, v2
v_mul_f32 v3, v244, v3
MM v[36:39],a[112:115],a[128:131],v[36:39]
v_mul_f32 v4, v244, v4
v_mul_f32 v5, v244, v5
MM v[36:39],a[116:119],a[132:135],v[36:39]
v_mul_f32 v6, v244, v6
v_mul_f32 v7, v244, v7
CV v160,v0,v1
MM v[36:39],a[120:123],a[160:163],v[36:39]
CV v161,v2,v3
CV v162,v4,v5
MM v[36:39],a[124:127],a[164:167],v[36:39]
CV v163,v6,v7
v_fmac_f32 v8, v241, v144
MM v[44:47],a[124:127],a[172:175],v[44:47]
v_fmac_f32 v9, v241, v145
v_fmac_f32 v10, v241, v146
v_fmac_f32 v11, v241, v147
MM v[44:47],a[120:123],a[168:171],v[44:47]
buffer_load_dwordx4 v228, s[8:11], s77 offen offset:0 lds
v_fmac_f32 v12, v241, v148
v_fmac_f32 v13, v241, v149
MM v[44:47],a[116:119],a[140:143],v[44:47]
v_fmac_f32 v14, v241, v150
v_fmac_f32 v15, v241, v151
v_mul_f32 v8, v245, v8
MM v[44:47],a[112:115],a[136:139],v[44:47]
v_mul_f32 v9, v245, v9
v_mul_f32 v10, v245, v10
MM v[64:67],a[0:3],a[128:131],v[64:67]
v_mul_f32 v11, v245, v11
v_mul_f32 v12, v245, v12
MM v[64:67],a[4:7],a[132:135],v[64:67]
v_mul_f32 v13, v245, v13
v_mul_f32 v14, v245, v14
v_mul_f32 v15, v245, v15
MM v[64:67],a[8:11],a[160:163],v[64:67]
CV v164,v8,v9
CV v165,v10,v11
MM v[64:67],a[12:15],a[164:167],v[64:67]
CV v166,v12,v13
CV v167,v14,v15
MM v[72:75],a[12:15],a[172:175],v[72:75]
v_xor_b32 v159, 128, v253
MM v[72:75],a[8:11],a[168:171],v[72:75]
RD v[152:155],v159 offset:0
RD v[156:159],v159 offset:16
MM v[72:75],a[4:7],a[140:143],v[72:75]
MM v[72:75],a[0:3],a[136:139],v[72:75]
buffer_load_dwordx4 v229, s[8:11], s77 offen offset:1024 lds
MM v[68:71],a[16:19],a[128:131],v[68:71]
MM v[68:71],a[20:23],a[132:135],v[68:71]
s_waitcnt lgkmcnt(0)
v_fmac_f32 v32, v240, v152
v_fmac_f32 v33, v240, v153
v_fmac_f32 v34, v240, v154
v_fmac_f32 v35, v240, v155
v_fmac_f32 v36, v240, v156
v_fmac_f32 v37, v240, v157
v_fmac_f32 v38, v240, v158
v_fmac_f32 v39, v240, v159
v_mul_f32 v179, -v250, v244
v_fma_f32 v32, v244, v32, v179
v_fma_f32 v33, v244, v33, v179
v_fma_f32 v34, v244, v34, v179
MM v[68:71],a[24:27],a[160:163],v[68:71]
v_fma_f32 v35, v244, v35, v179
v_fma_f32 v36, v244, v36, v179
MM v[68:71],a[28:31],a[164:167],v[68:71]
v_fma_f32 v37, v244, v37, v179
v_fma_f32 v38, v244, v38, v179
v_fma_f32 v39, v244, v39, v179
MM v[76:79],a[28:31],a[172:175],v[76:79]
CV v176,v32,v33
CV v177,v34,v35
MM v[76:79],a[24:27],a[168:171],v[76:79]
CV v178,v36,v37
CV v179,v38,v39
v_fmac_f32 v40, v241, v152
MM v[76:79],a[20:23],a[140:143],v[76:79]
v_fmac_f32 v41, v241, v153
v_fmac_f32 v42, v241, v154
v_fmac_f32 v43, v241, v155
MM v[76:79],a[16:19],a[136:139],v[76:79]
v_fmac_f32 v44, v241, v156
v_fmac_f32 v45, v241, v157
MM v[96:99],a[32:35],a[128:131],v[96:99]
v_fmac_f32 v46, v241, v158
v_fmac_f32 v47, v241, v159
MM v[96:99],a[36:39],a[132:135],v[96:99]
buffer_load_dwordx4 v230, s[8:11], s77 offen offset:2048 lds
v_mul_f32 v183, -v250, v245
v_fma_f32 v40, v245, v40, v183
v_fma_f32 v41, v245, v41, v183
MM v[96:99],a[40:43],a[160:163],v[96:99]
v_fma_f32 v42, v245, v42, v183
v_fma_f32 v43, v245, v43, v183
MM v[96:99],a[44:47],a[164:167],v[96:99]
v_fma_f32 v44, v245, v44, v183
v_fma_f32 v45, v245, v45, v183
MM v[104:107],a[44:47],a[172:175],v[104:107]
v_fma_f32 v46, v245, v46, v183
)ASM"
R"ASM(
v_fma_f32 v47, v245, v47, v183
CV v180,v40,v41
MM v[104:107],a[40:43],a[168:171],v[104:107]
CV v181,v42,v43
CV v182,v44,v45
MM v[104:107],a[36:39],a[140:143],v[104:107]
CV v183,v46,v47
v_xor_b32 v151, 256, v253
MM v[104:107],a[32:35],a[136:139],v[104:107]
RD v[144:147],v151 offset:0
RD v[148:151],v151 offset:16
MM v[100:103],a[48:51],a[128:131],v[100:103]
MM v[100:103],a[52:55],a[132:135],v[100:103]
MM v[100:103],a[56:59],a[160:163],v[100:103]
MM v[100:103],a[60:63],a[164:167],v[100:103]
s_waitcnt lgkmcnt(0)
buffer_load_dwordx4 v231, s[8:11], s77 offen offset:3072 lds
v_fmac_f32 v64, v240, v144
v_fmac_f32 v65, v240, v145
v_fmac_f32 v66, v240, v146
v_fmac_f32 v67, v240, v147
v_fmac_f32 v68, v240, v148
v_fmac_f32 v69, v240, v149
v_fmac_f32 v70, v240, v150
v_fmac_f32 v71, v240, v151
v_mul_f32 v195, -v250, v244
v_fma_f32 v64, v244, v64, v195
v_fma_f32 v65, v244, v65, v195
MM v[108:111],a[60:63],a[172:175],v[108:111]
s_add_u32 s77, s64, 16384
s_add_u32 s81, s77, 896
s_add_u32 m0, s65, 81920
v_fma_f32 v66, v244, v66, v195
v_fma_f32 v67, v244, v67, v195
MM v[108:111],a[56:59],a[168:171],v[108:111]
v_fma_f32 v68, v244, v68, v195
v_fma_f32 v69, v244, v69, v195
MM v[108:111],a[52:55],a[140:143],v[108:111]
v_fma_f32 v70, v244, v70, v195
v_fma_f32 v71, v244, v71, v195
CV v192,v64,v65
MM v[108:111],a[48:51],a[136:139],v[108:111]
CV v193,v66,v67
CV v194,v68,v69
MM v[16:19],a[64:67],a[144:147],v[16:19]
CV v195,v70,v71
v_fmac_f32 v72, v241, v144
v_fmac_f32 v73, v241, v145
MM v[16:19],a[68:71],a[148:151],v[16:19]
v_fmac_f32 v74, v241, v146
v_fmac_f32 v75, v241, v147
v_fmac_f32 v76, v241, v148
MM v[16:19],a[72:75],a[176:179],v[16:19]
v_fmac_f32 v77, v241, v149
v_fmac_f32 v78, v241, v150
MM v[16:19],a[76:79],a[180:183],v[16:19]
v_fmac_f32 v79, v241, v151
v_mul_f32 v199, -v250, v245
v_fma_f32 v72, v245, v72, v199
MM v[24:27],a[76:79],a[188:191],v[24:27]
v_fma_f32 v73, v245, v73, v199
v_fma_f32 v74, v245, v74, v199
MM v[24:27],a[72:75],a[184:187],v[24:27]
buffer_load_dwordx4 v235, s[12:15], s81 offen offset:0 lds
v_fma_f32 v75, v245, v75, v199
v_fma_f32 v76, v245, v76, v199
MM v[24:27],a[68:71],a[156:159],v[24:27]
s_waitcnt vmcnt(13)
s_barrier
RD a[192:195],v224 offset:0
v_fma_f32 v77, v245, v77, v199
v_fma_f32 v78, v245, v78, v199
v_fma_f32 v79, v245, v79, v199
MM v[24:27],a[64:67],a[152:155],v[24:27]
RD a[196:199],v225 offset:0
RD a[200:203],v226 offset:0
CV v196,v72,v73
CV v197,v74,v75
MM v[20:23],a[80:83],a[144:147],v[20:23]
RD a[204:207],v227 offset:0
RD a[208:211],v224 offset:4096
CV v198,v76,v77
CV v199,v78,v79
MM v[20:23],a[84:87],a[148:151],v[20:23]
RD a[212:215],v225 offset:4096
RD a[216:219],v226 offset:4096
v_xor_b32 v159, 384, v253
MM v[20:23],a[88:91],a[176:179],v[20:23]
RD a[220:223],v227 offset:4096
RD a[224:227],v224 offset:8192
RD v[152:155],v159 offset:0
RD v[156:159],v159 offset:16
MM v[20:23],a[92:95],a[180:183],v[20:23]
RD a[228:231],v225 offset:8192
RD a[232:235],v226 offset:8192
MM v[28:31],a[92:95],a[188:191],v[28:31]
RD a[236:239],v227 offset:8192
RD a[240:243],v224 offset:12288
MM v[28:31],a[88:91],a[184:187],v[28:31]
RD a[244:247],v225 offset:12288
RD a[248:251],v226 offset:12288
MM v[28:31],a[84:87],a[156:159],v[28:31]
s_waitcnt lgkmcnt(6)
RD a[252:255],v227 offset:12288
v_fmac_f32 v96, v240, v152
v_fmac_f32 v97, v240, v153
v_fmac_f32 v98, v240, v154
v_fmac_f32 v99, v240, v155
v_fmac_f32 v100, v240, v156
v_fmac_f32 v101, v240, v157
v_fmac_f32 v102, v240, v158
v_fmac_f32 v103, v240, v159
v_mul_f32 v211, -v250, v244
v_fma_f32 v96, v244, v96, v211
v_fma_f32 v97, v244, v97, v211
v_fma_f32 v98, v244, v98, v211
MM v[28:31],a[80:83],a[152:155],v[28:31]
buffer_load_dwordx4 v235, s[12:15], s77 offen offset:1024 lds
v_fma_f32 v99, v244, v99, v211
v_fma_f32 v100, v244, v100, v211
MM v[48:51],a[96:99],a[144:147],v[48:51]
v_fma_f32 v101, v244, v101, v211
v_fma_f32 v102, v244, v102, v211
v_fma_f32 v103, v244, v103, v211
MM v[48:51],a[100:103],a[148:151],v[48:51]
CV v208,v96,v97
CV v209,v98,v99
MM v[48:51],a[104:107],a[176:179],v[48:51]
CV v210,v100,v101
CV v211,v102,v103
v_fmac_f32 v104, v241, v152
MM v[48:51],a[108:111],a[180:183],v[48:51]
v_fmac_f32 v105, v241, v153
v_fmac_f32 v106, v241, v154
v_fmac_f32 v107, v241, v155
MM v[56:59],a[108:111],a[188:191],v[56:59]
v_fmac_f32 v108, v241, v156
v_fmac_f32 v109, v241, v157
MM v[56:59],a[104:107],a[184:187],v[56:59]
v_fmac_f32 v110, v241, v158
v_fmac_f32 v111, v241, v159
MM v[56:59],a[100:103],a[156:159],v[56:59]
v_mul_f32 v215, -v250, v245
v_fma_f32 v104, v245, v104, v215
v_fma_f32 v105, v245, v105, v215
MM v[56:59],a[96:99],a[152:155],v[56:59]
v_fma_f32 v106, v245, v106, v215
v_fma_f32 v107, v245, v107, v215
MM v[52:55],a[112:115],a[144:147],v[52:55]
v_fma_f32 v108, v245, v108, v215
v_fma_f32 v109, v245, v109, v215
MM v[52:55],a[116:119],a[148:151],v[52:55]
buffer_load_dwordx4 v235, s[12:15], s81 offen offset:2048 lds
v_fma_f32 v110, v245, v110, v215
v_fma_f32 v111, v245, v111, v215
CV v212,v104,v105
MM v[52:55],a[120:123],a[176:179],v[52:55]
CV v213,v106,v107
CV v214,v108,v109
CV v215,v110,v111
MM v[52:55],a[124:127],a[180:183],v[52:55]
v_mov_b32 v241, 0
v_mov_b32 v244, 0
s_waitcnt lgkmcnt(0)
MM v[0:3],a[192:195],v[160:163],0
v_mov_b32 v144, 0
v_mov_b32 v145, 0
MM v[0:3],a[196:199],v[176:179],v[0:3]
v_mov_b32 v245, 0
v_mov_b32 v148, 0
MM v[0:3],a[200:203],v[192:195],v[0:3]
v_mov_b32 v149, 0
v_fmac_f32 v16, v242, v136
MM v[0:3],a[204:207],v[208:211],v[0:3]
v_fmac_f32 v17, v242, v137
v_fmac_f32 v18, v242, v138
MM v[8:11],a[192:195],v[164:167],0
v_fmac_f32 v19, v242, v139
v_fmac_f32 v20, v242, v140
MM v[8:11],a[196:199],v[180:183],v[8:11]
v_fmac_f32 v21, v242, v141
v_fmac_f32 v22, v242, v142
MM v[8:11],a[200:203],v[196:199],v[8:11]
v_fmac_f32 v23, v242, v143
v_mul_f32 v16, v246, v16
MM v[8:11],a[204:207],v[212:215],v[8:11]
buffer_load_dwordx4 v235, s[12:15], s77 offen offset:3072 lds
MM v[4:7],a[208:211],v[160:163],0
s_add_u32 s77, s64, 32768
s_add_u32 s81, s77, 896
s_add_u32 m0, s65, 98304
MM v[4:7],a[212:215],v[176:179],v[4:7]
v_max3_f32 v146, v0, v1, v2
v_max_f32 v146, v146, v3
MM v[4:7],a[216:219],v[192:195],v[4:7]
v_mov_b32 v147, v146
v_mul_f32 v17, v246, v17
MM v[4:7],a[220:223],v[208:211],v[4:7]
v_permlane16_swap_b32 v146, v147
v_max_f32 v146, v146, v147
MM v[12:15],a[208:211],v[164:167],0
s_waitcnt vmcnt(8)
LPG4_%=:
s_cbranch_execz LPG8_%=
s_barrier
RD a[192:195],v224 offset:16384
MM v[12:15],a[212:215],v[180:183],v[12:15]
RD a[196:199],v225 offset:16384
RD a[200:203],v226 offset:16384
MM v[12:15],a[216:219],v[196:199],v[12:15]
RD a[204:207],v227 offset:16384
v_mov_b32 v147, v146
MM v[12:15],a[220:223],v[212:215],v[12:15]
s_nop 0
v_permlane32_swap_b32 v146, v147
v_max_f32 v146, v146, v147
v_sub_f32 v128, 0xc1600000, v146
v_sub_f32 v129, 0xc1600000, v146
MM v[32:35],a[224:227],v[160:163],0
RD a[208:211],v224 offset:20480
RD a[212:215],v225 offset:20480
v_sub_f32 v130, 0xc1600000, v146
v_sub_f32 v131, 0xc1600000, v146
v_max3_f32 v150, v8, v9, v10
MM v[32:35],a[228:231],v[176:179],v[32:35]
buffer_load_dwordx4 v235, s[12:15], s81 offen offset:0 lds
v_max_f32 v150, v150, v11
v_mov_b32 v151, v150
MM v[32:35],a[232:235],v[192:195],v[32:35]
RD a[216:219],v226 offset:20480
RD a[220:223],v227 offset:20480
v_permlane16_swap_b32 v150, v151
v_max_f32 v150, v150, v151
v_mov_b32 v151, v150
MM v[32:35],a[236:239],v[208:211],v[32:35]
s_nop 0
v_permlane32_swap_b32 v150, v151
v_max_f32 v150, v150, v151
v_add_f32 v0, v0, v128
v_sub_f32 v132, 0xc1600000, v150
MM v[40:43],a[224:227],v[164:167],0
v_sub_f32 v133, 0xc1600000, v150
v_sub_f32 v134, 0xc1600000, v150
v_sub_f32 v135, 0xc1600000, v150
v_add_f32 v1, v1, v128
EX v0,v0
MM v[40:43],a[228:231],v[180:183],v[40:43]
EX v1,v1
MM v[40:43],a[232:235],v[196:199],v[40:43]
v_add_f32 v2, v2, v128
v_add_f32 v3, v3, v128
MM v[40:43],a[236:239],v[212:215],v[40:43]
EX v2,v2
MM v[36:39],a[240:243],v[160:163],v[128:131]
RD a[224:227],v224 offset:24576
RD a[228:231],v225 offset:24576
EX v3,v3
MM v[36:39],a[244:247],v[176:179],v[36:39]
RD a[232:235],v226 offset:24576
RD a[236:239],v227 offset:24576
CV v0,v0,v1
v_add_f32 v4, v4, v128
MM v[36:39],a[248:251],v[192:195],v[36:39]
v_add_f32 v5, v5, v128
v_mul_f32 v18, v246, v18
EX v4,v4
MM v[36:39],a[252:255],v[208:211],v[36:39]
buffer_load_dwordx4 v235, s[12:15], s77 offen offset:1024 lds
EX v5,v5
MM v[44:47],a[240:243],v[164:167],v[132:135]
CV v1,v2,v3
v_add_f32 v6, v6, v128
MM v[44:47],a[244:247],v[180:183],v[44:47]
v_add_f32 v7, v7, v128
v_mul_f32 v19, v246, v19
MM v[44:47],a[248:251],v[196:199],v[44:47]
EX v6,v6
EX v7,v7
MM v[44:47],a[252:255],v[212:215],v[44:47]
CV v2,v4,v5
v_add_f32 v8, v8, v132
MM v[60:63],a[124:127],a[188:191],v[60:63]
RD a[240:243],v224 offset:28672
RD a[244:247],v225 offset:28672
v_add_f32 v9, v9, v132
MM v[60:63],a[120:123],a[184:187],v[60:63]
RD a[248:251],v226 offset:28672
RD a[252:255],v227 offset:28672
EX v8,v8
MM v[60:63],a[116:119],a[156:159],v[60:63]
EX v9,v9
MM v[60:63],a[112:115],a[152:155],v[60:63]
CV v3,v6,v7
v_add_f32 v10, v10, v132
MM v[80:83],a[0:3],a[144:147],v[80:83]
v_add_f32 v11, v11, v132
v_mul_f32 v20, v246, v20
EX v10,v10
MM v[80:83],a[4:7],a[148:151],v[80:83]
buffer_load_dwordx4 v235, s[12:15], s81 offen offset:2048 lds
EX v11,v11
MM v[80:83],a[8:11],a[176:179],v[80:83]
CV v8,v8,v9
v_add_f32 v12, v12, v132
MM v[80:83],a[12:15],a[180:183],v[80:83]
v_add_f32 v13, v13, v132
v_mul_f32 v21, v246, v21
EX v12,v12
MM v[88:91],a[12:15],a[188:191],v[88:91]
EX v13,v13
MM v[88:91],a[8:11],a[184:187],v[88:91]
CV v9,v10,v11
v_add_f32 v14, v14, v132
MM v[88:91],a[4:7],a[156:159],v[88:91]
v_add_f32 v15, v15, v132
v_mul_f32 v22, v246, v22
EX v14,v14
MM v[88:91],a[0:3],a[152:155],v[88:91]
EX v15,v15
MM v[84:87],a[16:19],a[144:147],v[84:87]
CV v10,v12,v13
v_add_f32 v32, v32, v128
MM v[84:87],a[20:23],a[148:151],v[84:87]
v_add_f32 v33, v33, v128
v_mul_f32 v23, v246, v23
MM v[84:87],a[24:27],a[176:179],v[84:87]
EX v32,v32
MM v[84:87],a[28:31],a[180:183],v[84:87]
buffer_load_dwordx4 v235, s[12:15], s77 offen offset:3072 lds
EX v33,v33
MM v[92:95],a[28:31],a[188:191],v[92:95]
s_add_u32 s77, s64, 49152
s_add_u32 m0, s65, 49152
CV v11,v14,v15
v_add_f32 v34, v34, v128
v_add_f32 v35, v35, v128
MM v[92:95],a[24:27],a[184:187],v[92:95]
buffer_load_dwordx4 v228, s[8:11], s77 offen offset:0 lds
EX v34,v34
MM v[92:95],a[20:23],a[156:159],v[92:95]
EX v35,v35
MM v[92:95],a[16:19],a[152:155],v[92:95]
CV v4,v32,v33
CV v168,v16,v17
EX v36,v36
MM v[112:115],a[32:35],a[144:147],v[112:115]
EX v37,v37
MM v[112:115],a[36:39],a[148:151],v[112:115]
buffer_load_dwordx4 v229, s[8:11], s77 offen offset:1024 lds
CV v5,v34,v35
MM v[112:115],a[40:43],a[176:179],v[112:115]
EX v38,v38
EX v39,v39
MM v[112:115],a[44:47],a[180:183],v[112:115]
CV v6,v36,v37
v_add_f32 v40, v40, v132
MM v[120:123],a[44:47],a[188:191],v[120:123]
v_add_f32 v41, v41, v132
CV v169,v18,v19
MM v[120:123],a[40:43],a[184:187],v[120:123]
buffer_load_dwordx4 v230, s[8:11], s77 offen offset:2048 lds
EX v40,v40
MM v[120:123],a[36:39],a[156:159],v[120:123]
EX v41,v41
MM v[120:123],a[32:35],a[152:155],v[120:123]
CV v7,v38,v39
v_add_f32 v42, v42, v132
MM v[116:119],a[48:51],a[144:147],v[116:119]
v_add_f32 v43, v43, v132
CV v170,v20,v21
EX v42,v42
MM v[116:119],a[52:55],a[148:151],v[116:119]
buffer_load_dwordx4 v231, s[8:11], s77 offen offset:3072 lds
EX v43,v43
MM v[116:119],a[56:59],a[176:179],v[116:119]
s_add_u32 s77, s64, 49152
s_add_u32 s81, s77, 896
s_add_u32 m0, s65, 114688
CV v12,v40,v41
EX v44,v44
MM v[116:119],a[60:63],a[180:183],v[116:119]
EX v45,v45
MM v[124:127],a[60:63],a[188:191],v[124:127]
CV v13,v42,v43
CV v171,v22,v23
MM v[124:127],a[56:59],a[184:187],v[124:127]
buffer_load_dwordx4 v235, s[12:15], s81 offen offset:0 lds
EX v46,v46
MM v[124:127],a[52:55],a[156:159],v[124:127]
EX v47,v47
CV v14,v44,v45
CV v15,v46,v47
MM v[124:127],a[48:51],a[152:155],v[124:127]
v_fmac_f32 v24, v243, v136
v_fmac_f32 v25, v243, v137
v_smfmac_f32_16x16x64_bf16 v[144:147], v[236:239], v[0:7], v234
v_fmac_f32 v26, v243, v138
v_smfmac_f32_16x16x64_bf16 v[148:151], v[236:239], v[8:15], v234
s_waitcnt lgkmcnt(0)
)ASM"
R"ASM(
v_fmac_f32 v27, v243, v139
buffer_load_dwordx4 v235, s[12:15], s77 offen offset:1024 lds
buffer_load_dwordx4 v235, s[12:15], s81 offen offset:2048 lds
buffer_load_dwordx4 v235, s[12:15], s77 offen offset:3072 lds
s_add_u32 s64, s64, 49152
MM v[64:67],a[192:195],v[160:163],v[128:131]
s_add_u32 s64, s64, 16384
v_fmac_f32 v28, v243, v140
v_fmac_f32 v29, v243, v141
MM v[64:67],a[196:199],v[176:179],v[64:67]
s_add_u32 s81, s64, 896
TR a[128:129],v232 offset:0
v_fmac_f32 v30, v243, v142
MM v[64:67],a[200:203],v[192:195],v[64:67]
v_fmac_f32 v31, v243, v143
v_mul_f32 v24, v247, v24
MM v[64:67],a[204:207],v[208:211],v[64:67]
v_max3_f32 v249, v144, v145, v148
v_max_f32 v249, v249, v149
v_mul_f32 v25, v247, v25
MM v[68:71],a[220:223],v[208:211],v[128:131]
TR a[130:131],v232 offset:4096
TR a[132:133],v232 offset:8192
v_cmp_lt_f32 vcc, s71, v249
v_mul_f32 v26, v247, v26
v_mul_f32 v27, v247, v27
MM v[68:71],a[216:219],v[192:195],v[68:71]
s_cbranch_vccnz LSITE0_%=
LBACK0_%=:
TR a[134:135],v232 offset:12288
v_add_f32 v244, v144, v145
v_mul_f32 v28, v247, v28
v_mul_f32 v29, v247, v29
v_mul_f32 v30, v247, v30
MM v[68:71],a[212:215],v[176:179],v[68:71]
TR a[136:137],v233 offset:0
v_add_f32 v245, v148, v149
v_mul_f32 v31, v247, v31
CV v172,v24,v25
MM v[68:71],a[208:211],v[160:163],v[68:71]
TR a[138:139],v233 offset:4096
CV v173,v26,v27
CV v174,v28,v29
MM v[76:79],a[208:211],v[164:167],v[132:135]
TR a[140:141],v233 offset:8192
TR a[142:143],v233 offset:12288
CV v175,v30,v31
v_xor_b32 v143, 128, v253
MM v[76:79],a[212:215],v[180:183],v[76:79]
RD v[136:139],v143 offset:0
RD v[140:143],v143 offset:16
TR a[144:145],v232 offset:512
MM v[76:79],a[216:219],v[196:199],v[76:79]
s_waitcnt lgkmcnt(9)
TR a[146:147],v232 offset:4608
TR a[148:149],v232 offset:8704
MM v[76:79],a[220:223],v[212:215],v[76:79]
EX v64,v64
MM v[32:35],a[128:131],v[0:3],0
MM v[36:39],a[128:131],v[8:11],0
MM v[72:75],a[204:207],v[212:215],v[132:135]
TR a[150:151],v232 offset:12800
TR a[152:153],v233 offset:512
MM v[72:75],a[200:203],v[196:199],v[72:75]
s_waitcnt lgkmcnt(5)
TR a[154:155],v233 offset:4608
v_fmac_f32 v48, v242, v136
v_fmac_f32 v49, v242, v137
v_fmac_f32 v50, v242, v138
v_fmac_f32 v51, v242, v139
v_fmac_f32 v52, v242, v140
v_fmac_f32 v53, v242, v141
v_fmac_f32 v54, v242, v142
v_fmac_f32 v55, v242, v143
v_mul_f32 v187, -v252, v246
v_fma_f32 v48, v246, v48, v187
v_fma_f32 v49, v246, v49, v187
EX v65,v65
MM v[72:75],a[196:199],v[180:183],v[72:75]
TR a[156:157],v233 offset:8704
v_fma_f32 v50, v246, v50, v187
v_fma_f32 v51, v246, v51, v187
v_fma_f32 v52, v246, v52, v187
MM v[72:75],a[192:195],v[164:167],v[72:75]
TR a[158:159],v233 offset:12800
v_fma_f32 v53, v246, v53, v187
EX v66,v66
v_fma_f32 v54, v246, v54, v187
MM v[96:99],a[224:227],v[160:163],v[128:131]
TR a[160:161],v232 offset:1024
v_max3_f32 v32, |v32|, |v33|, |v34|
v_max_f32 v32, v32, |v35|
v_fma_f32 v55, v246, v55, v187
CV v184,v48,v49
EX v67,v67
MM v[96:99],a[228:231],v[176:179],v[96:99]
TR a[162:163],v232 offset:5120
TR a[164:165],v232 offset:9216
v_bfe_u32 v32, v32, 23, 8
v_add_u32 v32, 7, v32
CV v185,v50,v51
CV v186,v52,v53
CV v187,v54,v55
v_fmac_f32 v56, v243, v136
MM v[96:99],a[232:235],v[192:195],v[96:99]
TR a[166:167],v232 offset:13312
v_min_u32 v32, 240, v32
v_lshl_or_b32 v241, v32, 0, v241
v_fmac_f32 v57, v243, v137
v_fmac_f32 v58, v243, v138
EX v68,v68
MM v[96:99],a[236:239],v[208:211],v[96:99]
TR a[168:169],v233 offset:1024
v_lshl_or_b32 v32, v32, 1, 1
v_lshlrev_b32 v32, 22, v32
v_fmac_f32 v59, v243, v139
v_fmac_f32 v60, v243, v140
MM v[100:103],a[252:255],v[208:211],v[128:131]
TR a[170:171],v233 offset:5120
AW a100,v32
AW a101,v32
v_fmac_f32 v61, v243, v141
v_fmac_f32 v62, v243, v142
EX v69,v69
v_fmac_f32 v63, v243, v143
MM v[100:103],a[248:251],v[192:195],v[100:103]
TR a[172:173],v233 offset:9216
TR a[174:175],v233 offset:13312
AW a102,v32
AW a103,v32
v_mul_f32 v191, -v252, v247
v_fma_f32 v56, v247, v56, v191
MM v[100:103],a[244:247],v[176:179],v[100:103]
TR a[176:177],v232 offset:1536
v_max3_f32 v36, |v36|, |v37|, |v38|
v_max_f32 v36, v36, |v39|
v_fma_f32 v57, v247, v57, v191
EX v70,v70
v_fma_f32 v58, v247, v58, v191
MM v[100:103],a[240:243],v[160:163],v[100:103]
TR a[178:179],v232 offset:5632
v_bfe_u32 v36, v36, 23, 8
v_add_u32 v36, 7, v36
v_fma_f32 v59, v247, v59, v191
v_fma_f32 v60, v247, v60, v191
EX v71,v71
v_fma_f32 v61, v247, v61, v191
MM v[108:111],a[240:243],v[164:167],v[132:135]
TR a[180:181],v232 offset:9728
v_min_u32 v36, 240, v36
v_lshl_or_b32 v241, v36, 8, v241
v_fma_f32 v62, v247, v62, v191
v_fma_f32 v63, v247, v63, v191
CV v64,v64,v65
MM v[108:111],a[244:247],v[180:183],v[108:111]
TR a[182:183],v232 offset:13824
v_lshl_or_b32 v36, v36, 1, 1
v_lshlrev_b32 v36, 22, v36
CV v188,v56,v57
CV v189,v58,v59
EX v72,v72
CV v190,v60,v61
MM v[108:111],a[248:251],v[196:199],v[108:111]
TR a[184:185],v233 offset:1536
TR a[186:187],v233 offset:5632
TR a[188:189],v233 offset:9728
TR a[190:191],v233 offset:13824
AW a108,v36
AW a109,v36
CV v191,v62,v63
v_xor_b32 v143, 256, v253
EX v73,v73
MM v[108:111],a[252:255],v[212:215],v[108:111]
RD v[136:139],v143 offset:0
RD v[140:143],v143 offset:16
AW a110,v36
AW a111,v36
CV v65,v66,v67
MM v[104:107],a[236:239],v[212:215],v[132:135]
EX v74,v74
MM v[104:107],a[232:235],v[196:199],v[104:107]
EX v75,v75
MM v[104:107],a[228:231],v[180:183],v[104:107]
EX v76,v76
CV v66,v68,v69
MM v[104:107],a[224:227],v[164:167],v[104:107]
s_waitcnt vmcnt(12) lgkmcnt(0)
s_barrier
v_fmac_f32 v80, v242, v136
v_fmac_f32 v81, v242, v137
v_fmac_f32 v82, v242, v138
v_fmac_f32 v83, v242, v139
v_fmac_f32 v84, v242, v140
v_fmac_f32 v85, v242, v141
v_fmac_f32 v86, v242, v142
v_fmac_f32 v87, v242, v143
v_mul_f32 v203, -v252, v246
v_fma_f32 v80, v246, v80, v203
MM a[0:3],a[128:131],v[0:3],a[100:103]
s_add_u32 m0, s65, 0
v_fma_f32 v81, v246, v81, v203
v_fma_f32 v82, v246, v82, v203
v_fma_f32 v83, v246, v83, v203
MM a[0:3],a[132:135],v[4:7],a[0:3]
v_fma_f32 v84, v246, v84, v203
v_fma_f32 v85, v246, v85, v203
EX v77,v77
CV v67,v70,v71
MM a[8:11],a[132:135],v[12:15],a[108:111]
v_fma_f32 v86, v246, v86, v203
v_fma_f32 v87, v246, v87, v203
MM a[8:11],a[128:131],v[8:11],a[8:11]
CV v200,v80,v81
CV v201,v82,v83
EX v78,v78
CV v202,v84,v85
MM a[12:15],a[136:139],v[8:11],a[108:111]
CV v203,v86,v87
v_fmac_f32 v88, v243, v136
v_fmac_f32 v89, v243, v137
EX v79,v79
MM a[12:15],a[140:143],v[12:15],a[12:15]
v_fmac_f32 v90, v243, v138
v_fmac_f32 v91, v243, v139
CV v72,v72,v73
MM a[4:7],a[140:143],v[4:7],a[100:103]
v_fmac_f32 v92, v243, v140
v_fmac_f32 v93, v243, v141
EX v96,v96
v_fmac_f32 v94, v243, v142
MM a[4:7],a[136:139],v[0:3],a[4:7]
v_fmac_f32 v95, v243, v143
v_mul_f32 v207, -v252, v247
MM a[32:35],a[144:147],v[0:3],a[100:103]
RD a[192:195],v224 offset:32768
v_fma_f32 v88, v247, v88, v207
v_fma_f32 v89, v247, v89, v207
EX v97,v97
CV v73,v74,v75
v_fma_f32 v90, v247, v90, v207
MM a[32:35],a[148:151],v[4:7],a[32:35]
buffer_load_dwordx4 v228, s[8:11], s64 offen offset:0 lds
v_fma_f32 v91, v247, v91, v207
v_fma_f32 v92, v247, v92, v207
EX v98,v98
MM a[40:43],a[148:151],v[12:15],a[108:111]
RD a[196:199],v225 offset:32768
v_fma_f32 v93, v247, v93, v207
v_fma_f32 v94, v247, v94, v207
MM a[40:43],a[144:147],v[8:11],a[40:43]
RD a[200:203],v226 offset:32768
v_fma_f32 v95, v247, v95, v207
CV v204,v88,v89
EX v99,v99
CV v74,v76,v77
CV v205,v90,v91
MM a[44:47],a[152:155],v[8:11],a[108:111]
CV v206,v92,v93
CV v207,v94,v95
MM a[44:47],a[156:159],v[12:15],a[44:47]
RD a[204:207],v227 offset:32768
buffer_load_dwordx4 v229, s[8:11], s64 offen offset:1024 lds
v_xor_b32 v143, 384, v253
EX v100,v100
MM a[36:39],a[156:159],v[4:7],a[100:103]
RD v[136:139],v143 offset:0
RD v[140:143],v143 offset:16
EX v101,v101
CV v75,v78,v79
MM a[36:39],a[152:155],v[0:3],a[36:39]
RD a[208:211],v224 offset:36864
RD a[212:215],v225 offset:36864
MM a[64:67],a[160:163],v[0:3],a[100:103]
EX v102,v102
MM a[64:67],a[164:167],v[4:7],a[64:67]
buffer_load_dwordx4 v230, s[8:11], s64 offen offset:2048 lds
MM a[72:75],a[164:167],v[12:15],a[108:111]
s_waitcnt lgkmcnt(2)
v_fmac_f32 v112, v242, v136
v_fmac_f32 v113, v242, v137
v_fmac_f32 v114, v242, v138
v_fmac_f32 v115, v242, v139
v_fmac_f32 v116, v242, v140
v_fmac_f32 v117, v242, v141
v_fmac_f32 v118, v242, v142
v_fmac_f32 v119, v242, v143
v_mul_f32 v219, -v252, v246
v_fma_f32 v112, v246, v112, v219
v_fma_f32 v113, v246, v113, v219
EX v103,v103
CV v68,v96,v97
v_fma_f32 v114, v246, v114, v219
MM a[72:75],a[160:163],v[8:11],a[72:75]
RD a[216:219],v226 offset:36864
v_fma_f32 v115, v246, v115, v219
v_fma_f32 v116, v246, v116, v219
EX v104,v104
MM a[76:79],a[168:171],v[8:11],a[108:111]
buffer_load_dwordx4 v231, s[8:11], s64 offen offset:3072 lds
RD a[220:223],v227 offset:36864
v_fma_f32 v117, v246, v117, v219
v_fma_f32 v118, v246, v118, v219
MM a[76:79],a[172:175],v[12:15],a[76:79]
s_add_u32 m0, s65, 65536
v_fma_f32 v119, v246, v119, v219
CV v216,v112,v113
EX v105,v105
CV v217,v114,v115
CV v69,v98,v99
MM a[68:71],a[172:175],v[4:7],a[100:103]
RD a[224:227],v224 offset:40960
CV v218,v116,v117
CV v219,v118,v119
v_fmac_f32 v120, v243, v136
MM a[68:71],a[168:171],v[0:3],a[68:71]
v_fmac_f32 v121, v243, v137
v_fmac_f32 v122, v243, v138
EX v106,v106
v_fmac_f32 v123, v243, v139
MM a[96:99],a[176:179],v[0:3],a[100:103]
RD a[228:231],v225 offset:40960
buffer_load_dwordx4 v235, s[12:15], s81 offen offset:0 lds
v_fmac_f32 v124, v243, v140
v_fmac_f32 v125, v243, v141
EX v107,v107
CV v70,v100,v101
MM a[96:99],a[180:183],v[4:7],a[96:99]
v_fmac_f32 v126, v243, v142
v_fmac_f32 v127, v243, v143
MM a[104:107],a[180:183],v[12:15],a[108:111]
RD a[232:235],v226 offset:40960
v_mul_f32 v223, -v252, v247
v_fma_f32 v120, v247, v120, v223
EX v108,v108
v_fma_f32 v121, v247, v121, v223
MM a[104:107],a[176:179],v[8:11],a[104:107]
v_fma_f32 v122, v247, v122, v223
v_fma_f32 v123, v247, v123, v223
CV v71,v102,v103
MM a[108:111],a[184:187],v[8:11],a[108:111]
RD a[236:239],v227 offset:40960
buffer_load_dwordx4 v235, s[12:15], s64 offen offset:1024 lds
v_fma_f32 v124, v247, v124, v223
v_fma_f32 v125, v247, v125, v223
EX v109,v109
MM a[108:111],a[188:191],v[12:15],a[108:111]
v_fma_f32 v126, v247, v126, v223
v_fma_f32 v127, v247, v127, v223
EX v110,v110
CV v220,v120,v121
MM a[100:103],a[188:191],v[4:7],a[100:103]
RD a[240:243],v224 offset:45056
RD a[244:247],v225 offset:45056
RD a[248:251],v226 offset:45056
RD a[252:255],v227 offset:45056
CV v221,v122,v123
EX v111,v111
CV v222,v124,v125
CV v223,v126,v127
MM a[100:103],a[184:187],v[0:3],a[100:103]
buffer_load_dwordx4 v235, s[12:15], s81 offen offset:2048 lds
CV v76,v104,v105
v_smfmac_f32_16x16x64_bf16 v[144:147], v[236:239], v[64:71], v234
CV v77,v106,v107
CV v78,v108,v109
CV v79,v110,v111
s_nop 1
v_smfmac_f32_16x16x64_bf16 v[148:151], v[236:239], v[72:79], v234
s_waitcnt lgkmcnt(0)
buffer_load_dwordx4 v235, s[12:15], s64 offen offset:3072 lds
MM v[0:3],a[192:195],v[160:163],v[128:131]
s_add_u32 s64, s64, 16384
TR a[128:129],v232 offset:16384
TR a[130:131],v232 offset:20480
MM v[0:3],a[196:199],v[176:179],v[0:3]
s_add_u32 s81, s64, 896
TR a[132:133],v232 offset:24576
TR a[134:135],v232 offset:28672
MM v[0:3],a[200:203],v[192:195],v[0:3]
TR a[136:137],v233 offset:16384
TR a[138:139],v233 offset:20480
MM v[0:3],a[204:207],v[208:211],v[0:3]
v_max3_f32 v249, v144, v145, v148
v_max_f32 v249, v249, v149
MM v[4:7],a[220:223],v[208:211],v[128:131]
TR a[140:141],v233 offset:24576
v_cmp_lt_f32 vcc, s71, v249
MM v[4:7],a[216:219],v[192:195],v[4:7]
s_cbranch_vccnz LSITE1_%=
LBACK1_%=:
TR a[142:143],v233 offset:28672
v_add_f32 v244, v144, v145
MM v[4:7],a[212:215],v[176:179],v[4:7]
TR a[144:145],v232 offset:16896
v_add_f32 v245, v148, v149
MM v[4:7],a[208:211],v[160:163],v[4:7]
TR a[146:147],v232 offset:20992
TR a[148:149],v232 offset:25088
MM v[12:15],a[208:211],v[164:167],v[132:135]
EX v0,v0
MM v[12:15],a[212:215],v[180:183],v[12:15]
EX v1,v1
MM v[12:15],a[216:219],v[196:199],v[12:15]
TR a[150:151],v232 offset:29184
CV v0,v0,v1
MM v[12:15],a[220:223],v[212:215],v[12:15]
TR a[152:153],v233 offset:16896
TR a[154:155],v233 offset:20992
MM v[8:11],a[204:207],v[212:215],v[132:135]
)ASM"
R"ASM(
EX v2,v2
MM v[8:11],a[200:203],v[196:199],v[8:11]
EX v3,v3
MM v[8:11],a[196:199],v[180:183],v[8:11]
TR a[156:157],v233 offset:25088
CV v1,v2,v3
MM v[8:11],a[192:195],v[164:167],v[8:11]
TR a[158:159],v233 offset:29184
s_nop 0
LPG5_%=:
s_cbranch_execz LPG9_%=
TR a[160:161],v232 offset:17408
MM v[32:35],a[224:227],v[160:163],v[128:131]
EX v4,v4
MM v[32:35],a[228:231],v[176:179],v[32:35]
TR a[162:163],v232 offset:21504
TR a[164:165],v232 offset:25600
MM v[32:35],a[232:235],v[192:195],v[32:35]
EX v5,v5
MM v[32:35],a[236:239],v[208:211],v[32:35]
TR a[166:167],v232 offset:29696
CV v2,v4,v5
MM v[36:39],a[252:255],v[208:211],v[128:131]
EX v6,v6
MM v[36:39],a[248:251],v[192:195],v[36:39]
TR a[168:169],v233 offset:17408
TR a[170:171],v233 offset:21504
MM v[36:39],a[244:247],v[176:179],v[36:39]
EX v7,v7
MM v[36:39],a[240:243],v[160:163],v[36:39]
TR a[172:173],v233 offset:25600
CV v3,v6,v7
MM v[44:47],a[240:243],v[164:167],v[132:135]
EX v8,v8
MM v[44:47],a[244:247],v[180:183],v[44:47]
TR a[174:175],v233 offset:29696
TR a[176:177],v232 offset:17920
MM v[44:47],a[248:251],v[196:199],v[44:47]
EX v9,v9
MM v[44:47],a[252:255],v[212:215],v[44:47]
TR a[178:179],v232 offset:22016
TR a[180:181],v232 offset:26112
MM v[40:43],a[236:239],v[212:215],v[132:135]
EX v10,v10
MM v[40:43],a[232:235],v[196:199],v[40:43]
TR a[182:183],v232 offset:30208
CV v8,v8,v9
MM v[40:43],a[228:231],v[180:183],v[40:43]
EX v11,v11
MM v[40:43],a[224:227],v[164:167],v[40:43]
TR a[184:185],v233 offset:17920
TR a[186:187],v233 offset:22016
MM v[16:19],a[192:195],v[168:171],0
EX v12,v12
MM v[16:19],a[196:199],v[184:187],v[16:19]
TR a[188:189],v233 offset:26112
TR a[190:191],v233 offset:30208
MM v[16:19],a[200:203],v[200:203],v[16:19]
EX v13,v13
MM v[16:19],a[204:207],v[216:219],v[16:19]
EX v14,v14
MM v[24:27],a[192:195],v[172:175],0
EX v15,v15
MM v[24:27],a[196:199],v[188:191],v[24:27]
EX v32,v32
MM v[24:27],a[200:203],v[204:207],v[24:27]
EX v33,v33
MM v[24:27],a[204:207],v[220:223],v[24:27]
s_waitcnt vmcnt(12) lgkmcnt(0)
s_barrier
CV v9,v10,v11
MM a[0:3],a[128:131],v[64:67],a[0:3]
s_add_u32 m0, s65, 16384
CV v10,v12,v13
CV v11,v14,v15
MM a[0:3],a[132:135],v[68:71],a[0:3]
RD a[192:195],v224 offset:49152
CV v4,v32,v33
MM a[8:11],a[132:135],v[76:79],a[8:11]
v_max3_f32 v154, v16, v17, v18
v_max_f32 v154, v154, v19
MM a[8:11],a[128:131],v[72:75],a[8:11]
buffer_load_dwordx4 v228, s[8:11], s64 offen offset:0 lds
MM a[12:15],a[136:139],v[72:75],a[12:15]
EX v34,v34
MM a[12:15],a[140:143],v[76:79],a[12:15]
RD a[196:199],v225 offset:49152
v_mov_b32 v155, v154
MM a[4:7],a[140:143],v[68:71],a[4:7]
s_nop 0
v_permlane16_swap_b32 v154, v155
v_max_f32 v154, v154, v155
MM a[4:7],a[136:139],v[64:67],a[4:7]
EX v35,v35
MM a[32:35],a[144:147],v[64:67],a[32:35]
RD a[200:203],v226 offset:49152
v_mov_b32 v155, v154
MM a[32:35],a[148:151],v[68:71],a[32:35]
s_nop 0
v_permlane32_swap_b32 v154, v155
v_max_f32 v154, v154, v155
MM a[40:43],a[148:151],v[76:79],a[40:43]
EX v36,v36
MM a[40:43],a[144:147],v[72:75],a[40:43]
RD a[204:207],v227 offset:49152
v_sub_f32 v136, 0xc1600000, v154
MM a[44:47],a[152:155],v[72:75],a[44:47]
EX v37,v37
MM a[44:47],a[156:159],v[76:79],a[44:47]
buffer_load_dwordx4 v229, s[8:11], s64 offen offset:1024 lds
v_sub_f32 v137, 0xc1600000, v154
v_sub_f32 v138, 0xc1600000, v154
MM a[36:39],a[156:159],v[68:71],a[36:39]
EX v38,v38
MM a[36:39],a[152:155],v[64:67],a[36:39]
RD a[208:211],v224 offset:53248
v_sub_f32 v139, 0xc1600000, v154
v_mov_b32 v246, 0
MM a[64:67],a[160:163],v[64:67],a[64:67]
EX v39,v39
v_mov_b32 v152, 0
MM a[64:67],a[164:167],v[68:71],a[64:67]
buffer_load_dwordx4 v230, s[8:11], s64 offen offset:2048 lds
RD a[212:215],v225 offset:53248
v_mov_b32 v153, 0
MM a[72:75],a[164:167],v[76:79],a[72:75]
EX v40,v40
v_max3_f32 v158, v24, v25, v26
MM a[72:75],a[160:163],v[72:75],a[72:75]
RD a[216:219],v226 offset:53248
v_max_f32 v158, v158, v27
CV v5,v34,v35
MM a[76:79],a[168:171],v[72:75],a[76:79]
buffer_load_dwordx4 v231, s[8:11], s64 offen offset:3072 lds
RD a[220:223],v227 offset:53248
EX v41,v41
v_mov_b32 v159, v158
MM a[76:79],a[172:175],v[76:79],a[76:79]
s_add_u32 m0, s65, 81920
v_permlane16_swap_b32 v158, v159
v_max_f32 v158, v158, v159
MM a[68:71],a[172:175],v[68:71],a[68:71]
RD a[224:227],v224 offset:57344
EX v42,v42
v_mov_b32 v159, v158
MM a[68:71],a[168:171],v[64:67],a[68:71]
s_nop 0
v_permlane32_swap_b32 v158, v159
CV v6,v36,v37
EX v43,v43
MM a[96:99],a[176:179],v[64:67],a[96:99]
RD a[228:231],v225 offset:57344
buffer_load_dwordx4 v235, s[12:15], s81 offen offset:0 lds
v_max_f32 v158, v158, v159
MM a[96:99],a[180:183],v[68:71],a[96:99]
v_sub_f32 v140, 0xc1600000, v158
CV v7,v38,v39
EX v44,v44
MM a[104:107],a[180:183],v[76:79],a[104:107]
RD a[232:235],v226 offset:57344
v_sub_f32 v141, 0xc1600000, v158
MM a[104:107],a[176:179],v[72:75],a[104:107]
v_sub_f32 v142, 0xc1600000, v158
v_sub_f32 v143, 0xc1600000, v158
EX v45,v45
MM a[108:111],a[184:187],v[72:75],a[108:111]
RD a[236:239],v227 offset:57344
buffer_load_dwordx4 v235, s[12:15], s64 offen offset:1024 lds
v_mov_b32 v247, 0
v_mov_b32 v156, 0
v_mov_b32 v157, 0
MM a[108:111],a[188:191],v[76:79],a[108:111]
EX v46,v46
MM a[100:103],a[188:191],v[68:71],a[100:103]
RD a[240:243],v224 offset:61440
RD a[244:247],v225 offset:61440
RD a[248:251],v226 offset:61440
RD a[252:255],v227 offset:61440
EX v47,v47
MM a[100:103],a[184:187],v[64:67],a[100:103]
buffer_load_dwordx4 v235, s[12:15], s81 offen offset:2048 lds
CV v12,v40,v41
v_smfmac_f32_16x16x64_bf16 v[144:147], v[236:239], v[0:7], v234
CV v13,v42,v43
CV v14,v44,v45
CV v15,v46,v47
s_nop 1
v_smfmac_f32_16x16x64_bf16 v[148:151], v[236:239], v[8:15], v234
s_waitcnt lgkmcnt(0)
buffer_load_dwordx4 v235, s[12:15], s64 offen offset:3072 lds
MM v[64:67],a[192:195],v[160:163],v[128:131]
s_add_u32 s64, s64, 16384
TR a[128:129],v232 offset:32768
s_add_u32 s81, s64, 896
MM v[64:67],a[196:199],v[176:179],v[64:67]
TR a[130:131],v232 offset:36864
TR a[132:133],v232 offset:40960
MM v[64:67],a[200:203],v[192:195],v[64:67]
TR a[134:135],v232 offset:45056
TR a[136:137],v233 offset:32768
MM v[64:67],a[204:207],v[208:211],v[64:67]
v_max3_f32 v249, v144, v145, v148
v_max_f32 v249, v249, v149
MM v[68:71],a[220:223],v[208:211],v[128:131]
TR a[138:139],v233 offset:36864
v_cmp_lt_f32 vcc, s71, v249
MM v[68:71],a[216:219],v[192:195],v[68:71]
s_cbranch_vccnz LSITE2_%=
LBACK2_%=:
TR a[140:141],v233 offset:40960
v_add_f32 v244, v144, v145
MM v[68:71],a[212:215],v[176:179],v[68:71]
TR a[142:143],v233 offset:45056
v_add_f32 v245, v148, v149
MM v[68:71],a[208:211],v[160:163],v[68:71]
TR a[144:145],v232 offset:33280
TR a[146:147],v232 offset:37376
MM v[76:79],a[208:211],v[164:167],v[132:135]
s_lshl_b32 s90, %[swave], 10
s_add_u32 s90, s90, 81920
TR a[148:149],v232 offset:41472
MM v[76:79],a[212:215],v[180:183],v[76:79]
buffer_load_dwordx4 v[56:59], v248, s[20:23], s90 offen offset:0 sc1
MM v[76:79],a[216:219],v[196:199],v[76:79]
EX v64,v64
MM v[76:79],a[220:223],v[212:215],v[76:79]
EX v65,v65
MM v[72:75],a[204:207],v[212:215],v[132:135]
EX v66,v66
MM v[72:75],a[200:203],v[196:199],v[72:75]
EX v67,v67
MM v[72:75],a[196:199],v[180:183],v[72:75]
CV v64,v64,v65
CV v65,v66,v67
MM v[72:75],a[192:195],v[164:167],v[72:75]
EX v68,v68
MM v[80:83],a[192:195],v[168:171],v[136:139]
EX v69,v69
MM v[80:83],a[196:199],v[184:187],v[80:83]
EX v70,v70
MM v[80:83],a[200:203],v[200:203],v[80:83]
EX v71,v71
MM v[80:83],a[204:207],v[216:219],v[80:83]
CV v66,v68,v69
CV v67,v70,v71
MM v[84:87],a[220:223],v[216:219],v[136:139]
EX v72,v72
MM v[84:87],a[216:219],v[200:203],v[84:87]
EX v73,v73
MM v[84:87],a[212:215],v[184:187],v[84:87]
TR a[150:151],v232 offset:45568
TR a[152:153],v233 offset:33280
MM v[84:87],a[208:211],v[168:171],v[84:87]
EX v74,v74
MM v[92:95],a[208:211],v[172:175],v[140:143]
EX v75,v75
MM v[92:95],a[212:215],v[188:191],v[92:95]
TR a[154:155],v233 offset:37376
CV v72,v72,v73
MM v[92:95],a[216:219],v[204:207],v[92:95]
EX v76,v76
MM v[92:95],a[220:223],v[220:223],v[92:95]
EX v77,v77
MM v[88:91],a[204:207],v[220:223],v[140:143]
TR a[156:157],v233 offset:41472
CV v73,v74,v75
MM v[88:91],a[200:203],v[204:207],v[88:91]
EX v78,v78
MM v[88:91],a[196:199],v[188:191],v[88:91]
EX v79,v79
MM v[88:91],a[192:195],v[172:175],v[88:91]
TR a[158:159],v233 offset:45568
CV v74,v76,v77
MM v[96:99],a[224:227],v[160:163],v[128:131]
EX v80,v80
MM v[96:99],a[228:231],v[176:179],v[96:99]
EX v81,v81
MM v[96:99],a[232:235],v[192:195],v[96:99]
TR a[160:161],v232 offset:33792
TR a[162:163],v232 offset:37888
MM v[96:99],a[236:239],v[208:211],v[96:99]
EX v82,v82
MM v[100:103],a[252:255],v[208:211],v[128:131]
EX v83,v83
MM v[100:103],a[248:251],v[192:195],v[100:103]
TR a[164:165],v232 offset:41984
CV v75,v78,v79
MM v[100:103],a[244:247],v[176:179],v[100:103]
EX v84,v84
MM v[100:103],a[240:243],v[160:163],v[100:103]
EX v85,v85
MM v[108:111],a[240:243],v[164:167],v[132:135]
TR a[166:167],v232 offset:46080
CV v80,v80,v81
MM v[108:111],a[244:247],v[180:183],v[108:111]
EX v86,v86
MM v[108:111],a[248:251],v[196:199],v[108:111]
EX v87,v87
MM v[108:111],a[252:255],v[212:215],v[108:111]
TR a[168:169],v233 offset:33792
TR a[170:171],v233 offset:37888
MM v[104:107],a[236:239],v[212:215],v[132:135]
EX v88,v88
MM v[104:107],a[232:235],v[196:199],v[104:107]
EX v89,v89
MM v[104:107],a[228:231],v[180:183],v[104:107]
TR a[172:173],v233 offset:41984
CV v81,v82,v83
EX v90,v90
MM v[104:107],a[224:227],v[164:167],v[104:107]
TR a[174:175],v233 offset:46080
CV v82,v84,v85
MM v[112:115],a[224:227],v[168:171],v[136:139]
EX v91,v91
MM v[112:115],a[228:231],v[184:187],v[112:115]
EX v92,v92
MM v[112:115],a[232:235],v[200:203],v[112:115]
TR a[176:177],v232 offset:34304
EX v93,v93
MM v[112:115],a[236:239],v[216:219],v[112:115]
TR a[178:179],v232 offset:38400
CV v83,v86,v87
MM v[116:119],a[252:255],v[216:219],v[136:139]
EX v94,v94
MM v[116:119],a[248:251],v[200:203],v[116:119]
EX v95,v95
MM v[116:119],a[244:247],v[184:187],v[116:119]
TR a[180:181],v232 offset:42496
CV v88,v88,v89
MM v[116:119],a[240:243],v[168:171],v[116:119]
s_memrealtime s[56:57]
EX v96,v96
MM v[124:127],a[240:243],v[172:175],v[140:143]
s_memtime s[58:59]
TR a[182:183],v232 offset:46592
EX v97,v97
CV v89,v90,v91
MM v[124:127],a[244:247],v[188:191],v[124:127]
EX v98,v98
MM v[124:127],a[248:251],v[204:207],v[124:127]
TR a[184:185],v233 offset:34304
TR a[186:187],v233 offset:38400
TR a[188:189],v233 offset:42496
TR a[190:191],v233 offset:46592
MM v[124:127],a[252:255],v[220:223],v[124:127]
EX v99,v99
CV v90,v92,v93
MM v[120:123],a[236:239],v[220:223],v[140:143]
EX v100,v100
MM v[120:123],a[232:235],v[204:207],v[120:123]
EX v101,v101
CV v91,v94,v95
MM v[120:123],a[228:231],v[188:191],v[120:123]
EX v102,v102
MM v[120:123],a[224:227],v[172:175],v[120:123]
s_waitcnt vmcnt(13) lgkmcnt(0)
s_barrier
CV v68,v96,v97
MM a[0:3],a[128:131],v[0:3],a[0:3]
s_sub_u32 s52, s56, s52
s_sub_u32 s54, s58, s54
v_cvt_f32_u32 v250, s52
EX v103,v103
MM a[0:3],a[132:135],v[4:7],a[0:3]
s_add_u32 m0, s65, 32768
v_cvt_f32_u32 v251, s54
CV v69,v98,v99
EX v104,v104
MM a[8:11],a[132:135],v[12:15],a[8:11]
v_rcp_f32 v251, v251
MM a[8:11],a[128:131],v[8:11],a[8:11]
v_mul_f32 v250, 0x47abe000, v250
s_lshl_b32 s52, %[swave], 11
EX v105,v105
MM a[12:15],a[136:139],v[8:11],a[12:15]
v_mul_f32 v250, v250, v251
s_lshl_b32 s54, %[sb], 3
EX v106,v106
MM a[12:15],a[140:143],v[12:15],a[12:15]
v_cvt_u32_f32 v250, v250
s_add_u32 s52, s52, s54
EX v107,v107
MM a[4:7],a[140:143],v[4:7],a[4:7]
s_waitcnt vmcnt(0)
v_add_u32 v250, s56, v250
v_mov_b32 v251, s69
CV v70,v100,v101
MM a[4:7],a[136:139],v[0:3],a[4:7]
s_add_u32 s52, s52, 86016
s_mov_b64 exec, 1
buffer_store_dwordx2 v[250:251], off, s[20:23], s52 sc1
s_mov_b64 exec, -1
s_nop 0
EX v108,v108
MM a[32:35],a[144:147],v[0:3],a[32:35]
RD a[192:195],v224 offset:0
v_xor_b32 v56, s69, v56
v_xor_b32 v57, s69, v57
EX v109,v109
CV v71,v102,v103
MM a[32:35],a[148:151],v[4:7],a[32:35]
buffer_load_dwordx4 v228, s[8:11], s64 offen offset:0 lds
v_xor_b32 v58, s69, v58
v_xor_b32 v59, s69, v59
MM a[40:43],a[148:151],v[12:15],a[40:43]
RD a[196:199],v225 offset:0
v_or3_b32 v252, v56, v57, v58
)ASM"
R"ASM(
v_or_b32 v252, v252, v59
EX v110,v110
MM a[40:43],a[144:147],v[8:11],a[40:43]
v_cmp_ne_u32 vcc, 0, v252
s_cbranch_vccnz LRFR_%=
LRFRb_%=:
EX v111,v111
CV v76,v104,v105
MM a[44:47],a[152:155],v[8:11],a[44:47]
s_lshl_b32 s90, %[swave], 11
s_add_u32 s78, s90, %[slds]
s_add_u32 s78, s78, 155648
RD a[200:203],v226 offset:0
EX v112,v112
MM a[44:47],a[156:159],v[12:15],a[44:47]
s_add_u32 s90, s90, 53248
buffer_load_dwordx4 v229, s[8:11], s64 offen offset:1024 lds
MM a[36:39],a[156:159],v[4:7],a[36:39]
RD a[204:207],v227 offset:0
EX v113,v113
CV v77,v106,v107
MM a[36:39],a[152:155],v[0:3],a[36:39]
EX v114,v114
MM a[64:67],a[160:163],v[0:3],a[64:67]
RD a[208:211],v224 offset:4096
EX v115,v115
CV v78,v108,v109
MM a[64:67],a[164:167],v[4:7],a[64:67]
buffer_load_dwordx4 v230, s[8:11], s64 offen offset:2048 lds
MM a[72:75],a[164:167],v[12:15],a[72:75]
RD a[212:215],v225 offset:4096
EX v116,v116
MM a[72:75],a[160:163],v[8:11],a[72:75]
EX v117,v117
CV v79,v110,v111
MM a[76:79],a[168:171],v[8:11],a[76:79]
RD a[216:219],v226 offset:4096
CV v84,v112,v113
EX v118,v118
MM a[76:79],a[172:175],v[12:15],a[76:79]
buffer_load_dwordx4 v231, s[8:11], s64 offen offset:3072 lds
MM a[68:71],a[172:175],v[4:7],a[68:71]
s_add_u32 m0, s65, 98304
RD a[220:223],v227 offset:4096
EX v119,v119
MM a[68:71],a[168:171],v[0:3],a[68:71]
EX v120,v120
MM a[96:99],a[176:179],v[0:3],a[96:99]
RD a[224:227],v224 offset:8192
CV v85,v114,v115
EX v121,v121
MM a[96:99],a[180:183],v[4:7],a[96:99]
RD a[228:231],v225 offset:8192
CV v86,v116,v117
MM a[104:107],a[180:183],v[12:15],a[104:107]
buffer_load_dwordx4 v235, s[12:15], s81 offen offset:0 lds
EX v122,v122
MM a[104:107],a[176:179],v[8:11],a[104:107]
EX v123,v123
MM a[108:111],a[184:187],v[8:11],a[108:111]
RD a[232:235],v226 offset:8192
CV v87,v118,v119
MM a[108:111],a[188:191],v[12:15],a[108:111]
EX v124,v124
MM a[100:103],a[188:191],v[4:7],a[100:103]
RD a[236:239],v227 offset:8192
buffer_load_dwordx4 v235, s[12:15], s64 offen offset:1024 lds
EX v125,v125
MM a[100:103],a[184:187],v[0:3],a[100:103]
EX v126,v126
v_smfmac_f32_16x16x64_bf16 v[144:147], v[236:239], v[64:71], v234
RD a[240:243],v224 offset:12288
RD a[244:247],v225 offset:12288
RD a[248:251],v226 offset:12288
RD a[252:255],v227 offset:12288
EX v127,v127
v_smfmac_f32_16x16x64_bf16 v[148:151], v[236:239], v[72:79], v234
CV v92,v120,v121
v_smfmac_f32_16x16x64_bf16 v[152:155], v[236:239], v[80:87], v234
buffer_load_dwordx4 v235, s[12:15], s81 offen offset:2048 lds
CV v93,v122,v123
CV v94,v124,v125
CV v95,v126,v127
s_nop 1
v_smfmac_f32_16x16x64_bf16 v[156:159], v[236:239], v[88:95], v234
s_waitcnt lgkmcnt(0)
buffer_load_dwordx4 v235, s[12:15], s64 offen offset:3072 lds
MM v[0:3],a[192:195],v[160:163],v[128:131]
s_add_u32 s64, s64, 16384
TR a[128:129],v232 offset:49152
s_add_u32 s81, s64, 896
MM v[0:3],a[196:199],v[176:179],v[0:3]
TR a[130:131],v232 offset:53248
v_max3_f32 v249, v144, v145, v148
MM v[0:3],a[200:203],v[192:195],v[0:3]
TR a[132:133],v232 offset:57344
v_max3_f32 v249, v249, v149, v152
MM v[0:3],a[204:207],v[208:211],v[0:3]
TR a[134:135],v232 offset:61440
v_max3_f32 v249, v249, v153, v156
MM v[4:7],a[220:223],v[208:211],v[128:131]
TR a[136:137],v233 offset:49152
v_max_f32 v249, v249, v157
MM v[4:7],a[216:219],v[192:195],v[4:7]
TR a[138:139],v233 offset:53248
v_cmp_lt_f32 vcc, s71, v249
MM v[4:7],a[212:215],v[176:179],v[4:7]
TR a[140:141],v233 offset:57344
TR a[142:143],v233 offset:61440
MM v[4:7],a[208:211],v[160:163],v[4:7]
s_cbranch_vccnz LSITE3_%=
LBACK3_%=:
MM v[12:15],a[208:211],v[164:167],v[132:135]
s_mov_b32 m0, s78
EX v0,v0
MM v[12:15],a[212:215],v[180:183],v[12:15]
s_nop 0
buffer_load_dwordx4 v248, s[20:23], s90 offen offset:0 sc1 lds
MM v[12:15],a[216:219],v[196:199],v[12:15]
s_waitcnt lgkmcnt(6)
buffer_load_dwordx4 v248, s[20:23], s90 offen offset:1024 sc1 lds
MM v[12:15],a[220:223],v[212:215],v[12:15]
EX v1,v1
MM v[112:115],a[128:131],v[80:83],0
MM v[116:119],a[128:131],v[88:91],0
MM v[8:11],a[204:207],v[212:215],v[132:135]
EX v2,v2
MM v[8:11],a[200:203],v[196:199],v[8:11]
TR a[144:145],v232 offset:49664
CV v0,v0,v1
MM v[8:11],a[196:199],v[180:183],v[8:11]
EX v3,v3
MM v[8:11],a[192:195],v[164:167],v[8:11]
TR a[146:147],v232 offset:53760
CV v1,v2,v3
MM v[16:19],a[192:195],v[168:171],v[136:139]
v_max3_f32 v112, |v112|, |v113|, |v114|
v_max_f32 v112, v112, |v115|
MM v[16:19],a[196:199],v[184:187],v[16:19]
v_bfe_u32 v112, v112, 23, 8
v_add_u32 v112, 7, v112
MM v[16:19],a[200:203],v[200:203],v[16:19]
v_min_u32 v112, 240, v112
v_lshl_or_b32 v241, v112, 16, v241
MM v[16:19],a[204:207],v[216:219],v[16:19]
v_lshl_or_b32 v112, v112, 1, 1
v_lshlrev_b32 v112, 22, v112
EX v4,v4
MM v[20:23],a[220:223],v[216:219],v[136:139]
AW a116,v112
AW a117,v112
MM v[20:23],a[216:219],v[200:203],v[20:23]
AW a118,v112
AW a119,v112
EX v5,v5
MM v[20:23],a[212:215],v[184:187],v[20:23]
v_max3_f32 v116, |v116|, |v117|, |v118|
v_max_f32 v116, v116, |v119|
MM v[20:23],a[208:211],v[168:171],v[20:23]
v_bfe_u32 v116, v116, 23, 8
v_add_u32 v116, 7, v116
EX v6,v6
MM v[28:31],a[208:211],v[172:175],v[140:143]
v_min_u32 v116, 240, v116
v_lshl_or_b32 v241, v116, 24, v241
MM v[28:31],a[212:215],v[188:191],v[28:31]
TR a[148:149],v232 offset:57856
v_lshl_or_b32 v116, v116, 1, 1
v_lshlrev_b32 v116, 22, v116
EX v7,v7
MM v[28:31],a[216:219],v[204:207],v[28:31]
AW a124,v116
AW a125,v116
MM v[28:31],a[220:223],v[220:223],v[28:31]
TR a[150:151],v232 offset:61952
AW a126,v116
AW a127,v116
EX v8,v8
MM v[24:27],a[204:207],v[220:223],v[140:143]
TR a[152:153],v233 offset:49664
TR a[154:155],v233 offset:53760
MM v[24:27],a[200:203],v[204:207],v[24:27]
EX v9,v9
MM v[24:27],a[196:199],v[188:191],v[24:27]
EX v10,v10
MM v[24:27],a[192:195],v[172:175],v[24:27]
TR a[156:157],v233 offset:57856
CV v2,v4,v5
MM v[32:35],a[224:227],v[160:163],v[128:131]
EX v11,v11
MM v[32:35],a[228:231],v[176:179],v[32:35]
TR a[158:159],v233 offset:61952
CV v3,v6,v7
MM v[32:35],a[232:235],v[192:195],v[32:35]
EX v12,v12
MM v[32:35],a[236:239],v[208:211],v[32:35]
TR a[160:161],v232 offset:50176
TR a[162:163],v232 offset:54272
MM v[36:39],a[252:255],v[208:211],v[128:131]
EX v13,v13
MM v[36:39],a[248:251],v[192:195],v[36:39]
EX v14,v14
MM v[36:39],a[244:247],v[176:179],v[36:39]
TR a[164:165],v232 offset:58368
CV v8,v8,v9
MM v[36:39],a[240:243],v[160:163],v[36:39]
EX v15,v15
MM v[44:47],a[240:243],v[164:167],v[132:135]
s_lshl_b32 s76, s50, 1
LPG6_%=:
s_cbranch_execz LPG10_%=
TR a[166:167],v232 offset:62464
CV v9,v10,v11
MM v[44:47],a[244:247],v[180:183],v[44:47]
s_bfe_u32 s77, s49, 65538
EX v16,v16
MM v[44:47],a[248:251],v[196:199],v[44:47]
s_add_u32 s76, s76, s77
TR a[168:169],v233 offset:50176
TR a[170:171],v233 offset:54272
MM v[44:47],a[252:255],v[212:215],v[44:47]
s_lshl_b32 s76, s76, 2
EX v17,v17
MM v[40:43],a[236:239],v[212:215],v[132:135]
s_add_u32 s76, s76, s84
TR a[172:173],v233 offset:58368
CV v10,v12,v13
MM v[40:43],a[232:235],v[196:199],v[40:43]
s_lshl_b32 s76, s76, 2
EX v18,v18
MM v[40:43],a[228:231],v[180:183],v[40:43]
s_lshr_b32 s77, s49, 3
EX v19,v19
MM v[40:43],a[224:227],v[164:167],v[40:43]
s_add_u32 s76, s76, s77
TR a[174:175],v233 offset:62464
CV v11,v14,v15
MM v[48:51],a[224:227],v[168:171],v[136:139]
s_mul_i32 s95, s76, 33792
EX v20,v20
MM v[48:51],a[228:231],v[184:187],v[48:51]
s_lshl_b32 s76, %[swave], 13
TR a[176:177],v232 offset:50688
TR a[178:179],v232 offset:54784
MM v[48:51],a[232:235],v[200:203],v[48:51]
s_add_u32 s95, s95, s76
EX v21,v21
MM v[48:51],a[236:239],v[216:219],v[48:51]
s_add_u32 s95, s95, 22708224
v_mov_b32 v243, -1
CV v16,v16,v17
MM v[52:55],a[252:255],v[216:219],v[136:139]
s_mov_b32 s67, s95
EX v22,v22
MM v[52:55],a[248:251],v[200:203],v[52:55]
s_add_u32 s87, s95, 6144
buffer_store_dword v243, v248, s[20:23], s87 offen offset:12 sc1
MM v[52:55],a[244:247],v[184:187],v[52:55]
TR a[180:181],v232 offset:58880
TR a[182:183],v232 offset:62976
MM v[52:55],a[240:243],v[168:171],v[52:55]
EX v23,v23
MM v[60:63],a[240:243],v[172:175],v[140:143]
EX v24,v24
MM v[60:63],a[244:247],v[188:191],v[60:63]
TR a[184:185],v233 offset:50688
CV v17,v18,v19
MM v[60:63],a[248:251],v[204:207],v[60:63]
TR a[186:187],v233 offset:54784
TR a[188:189],v233 offset:58880
TR a[190:191],v233 offset:62976
EX v25,v25
MM v[60:63],a[252:255],v[220:223],v[60:63]
s_add_u32 s87, s95, 7168
buffer_store_dword v243, v248, s[20:23], s87 offen offset:12 sc1
MM v[56:59],a[236:239],v[220:223],v[140:143]
EX v26,v26
MM v[56:59],a[232:235],v[204:207],v[56:59]
EX v27,v27
MM v[56:59],a[228:231],v[188:191],v[56:59]
EX v28,v28
CV v18,v20,v21
MM v[56:59],a[224:227],v[172:175],v[56:59]
s_waitcnt lgkmcnt(0)
s_barrier
MM a[0:3],a[128:131],v[64:67],a[0:3]
s_add_u32 m0, s65, 49152
CV v19,v22,v23
CV v24,v24,v25
MM a[0:3],a[132:135],v[68:71],a[0:3]
EX v29,v29
MM a[8:11],a[132:135],v[76:79],a[8:11]
EX v30,v30
MM a[8:11],a[128:131],v[72:75],a[8:11]
CV v25,v26,v27
CV v26,v28,v29
MM a[16:19],a[128:131],v[80:83],a[116:119]
EX v31,v31
MM a[16:19],a[132:135],v[84:87],a[16:19]
EX v32,v32
MM a[24:27],a[132:135],v[92:95],a[124:127]
RD a[192:195],v224 offset:16384
CV v27,v30,v31
MM a[24:27],a[128:131],v[88:91],a[24:27]
EX v33,v33
MM a[28:31],a[136:139],v[88:91],a[124:127]
buffer_load_dwordx4 v228, s[8:11], s64 offen offset:0 lds
MM a[28:31],a[140:143],v[92:95],a[28:31]
EX v34,v34
MM a[20:23],a[140:143],v[84:87],a[116:119]
RD a[196:199],v225 offset:16384
CV v4,v32,v33
MM a[20:23],a[136:139],v[80:83],a[20:23]
EX v35,v35
MM a[12:15],a[136:139],v[72:75],a[12:15]
EX v36,v36
MM a[12:15],a[140:143],v[76:79],a[12:15]
RD a[200:203],v226 offset:16384
CV v5,v34,v35
MM a[4:7],a[140:143],v[68:71],a[4:7]
EX v37,v37
MM a[4:7],a[136:139],v[64:67],a[4:7]
buffer_load_dwordx4 v229, s[8:11], s64 offen offset:1024 lds
MM a[32:35],a[144:147],v[64:67],a[32:35]
EX v38,v38
MM a[32:35],a[148:151],v[68:71],a[32:35]
RD a[204:207],v227 offset:16384
CV v6,v36,v37
MM a[40:43],a[148:151],v[76:79],a[40:43]
EX v39,v39
MM a[40:43],a[144:147],v[72:75],a[40:43]
RD a[208:211],v224 offset:20480
CV v7,v38,v39
MM a[48:51],a[144:147],v[80:83],a[116:119]
EX v40,v40
MM a[48:51],a[148:151],v[84:87],a[48:51]
EX v41,v41
MM a[56:59],a[148:151],v[92:95],a[124:127]
buffer_load_dwordx4 v230, s[8:11], s64 offen offset:2048 lds
MM a[56:59],a[144:147],v[88:91],a[56:59]
EX v42,v42
MM a[60:63],a[152:155],v[88:91],a[124:127]
RD a[212:215],v225 offset:20480
CV v12,v40,v41
MM a[60:63],a[156:159],v[92:95],a[60:63]
EX v43,v43
MM a[52:55],a[156:159],v[84:87],a[116:119]
RD a[216:219],v226 offset:20480
CV v13,v42,v43
MM a[52:55],a[152:155],v[80:83],a[52:55]
EX v44,v44
MM a[44:47],a[152:155],v[72:75],a[44:47]
EX v45,v45
MM a[44:47],a[156:159],v[76:79],a[44:47]
buffer_load_dwordx4 v231, s[8:11], s64 offen offset:3072 lds
MM a[36:39],a[156:159],v[68:71],a[36:39]
s_add_u32 m0, s65, 114688
RD a[220:223],v227 offset:20480
CV v14,v44,v45
MM a[36:39],a[152:155],v[64:67],a[36:39]
EX v46,v46
MM a[64:67],a[160:163],v[64:67],a[64:67]
EX v47,v47
MM a[64:67],a[164:167],v[68:71],a[64:67]
RD a[224:227],v224 offset:24576
CV v15,v46,v47
MM a[72:75],a[164:167],v[76:79],a[72:75]
EX v48,v48
MM a[72:75],a[160:163],v[72:75],a[72:75]
buffer_load_dwordx4 v235, s[12:15], s81 offen offset:0 lds
MM a[80:83],a[160:163],v[80:83],a[116:119]
EX v49,v49
MM a[80:83],a[164:167],v[84:87],a[80:83]
RD a[228:231],v225 offset:24576
CV v20,v48,v49
MM a[88:91],a[164:167],v[92:95],a[124:127]
EX v50,v50
MM a[88:91],a[160:163],v[88:91],a[88:91]
EX v51,v51
MM a[92:95],a[168:171],v[88:91],a[124:127]
RD a[232:235],v226 offset:24576
CV v21,v50,v51
MM a[92:95],a[172:175],v[92:95],a[92:95]
EX v52,v52
MM a[84:87],a[172:175],v[84:87],a[116:119]
buffer_load_dwordx4 v235, s[12:15], s64 offen offset:1024 lds
MM a[84:87],a[168:171],v[80:83],a[84:87]
EX v53,v53
MM a[76:79],a[168:171],v[72:75],a[76:79]
RD a[236:239],v227 offset:24576
CV v22,v52,v53
MM a[76:79],a[172:175],v[76:79],a[76:79]
EX v54,v54
MM a[68:71],a[172:175],v[68:71],a[68:71]
EX v55,v55
MM a[68:71],a[168:171],v[64:67],a[68:71]
RD a[240:243],v224 offset:28672
CV v23,v54,v55
MM a[96:99],a[176:179],v[64:67],a[96:99]
EX v56,v56
MM a[96:99],a[180:183],v[68:71],a[96:99]
buffer_load_dwordx4 v235, s[12:15], s81 offen offset:2048 lds
)ASM"
R"ASM(
MM a[104:107],a[180:183],v[76:79],a[104:107]
EX v57,v57
MM a[104:107],a[176:179],v[72:75],a[104:107]
RD a[244:247],v225 offset:28672
CV v28,v56,v57
MM a[112:115],a[176:179],v[80:83],a[116:119]
EX v58,v58
MM a[112:115],a[180:183],v[84:87],a[112:115]
RD a[248:251],v226 offset:28672
RD a[252:255],v227 offset:28672
MM a[120:123],a[180:183],v[92:95],a[124:127]
EX v59,v59
MM a[120:123],a[176:179],v[88:91],a[120:123]
EX v60,v60
MM a[124:127],a[184:187],v[88:91],a[124:127]
v_add_f32 v244, v144, v145
v_add_f32 v245, v148, v149
MM a[124:127],a[188:191],v[92:95],a[124:127]
v_add_f32 v246, v152, v153
v_add_f32 v247, v156, v157
MM a[116:119],a[188:191],v[84:87],a[116:119]
EX v61,v61
MM a[116:119],a[184:187],v[80:83],a[116:119]
buffer_load_dwordx4 v235, s[12:15], s64 offen offset:3072 lds
MM a[108:111],a[184:187],v[72:75],a[108:111]
EX v62,v62
MM a[108:111],a[188:191],v[76:79],a[108:111]
EX v63,v63
MM a[100:103],a[188:191],v[68:71],a[100:103]
CV v29,v58,v59
CV v30,v60,v61
MM a[100:103],a[184:187],v[64:67],a[100:103]
CV v31,v62,v63
v_smfmac_f32_16x16x64_bf16 v[144:147], v[236:239], v[0:7], v234
v_smfmac_f32_16x16x64_bf16 v[148:151], v[236:239], v[8:15], v234
v_smfmac_f32_16x16x64_bf16 v[152:155], v[236:239], v[16:23], v234
v_smfmac_f32_16x16x64_bf16 v[156:159], v[236:239], v[24:31], v234
s_waitcnt lgkmcnt(0)
MM v[64:67],a[192:195],v[160:163],v[128:131]
s_add_u32 s64, s64, 16384
TR a[128:129],v232 offset:0
s_add_u32 s81, s64, 896
MM v[64:67],a[196:199],v[176:179],v[64:67]
TR a[130:131],v232 offset:4096
v_max3_f32 v249, v144, v145, v148
MM v[64:67],a[200:203],v[192:195],v[64:67]
TR a[132:133],v232 offset:8192
v_max3_f32 v249, v249, v149, v152
MM v[64:67],a[204:207],v[208:211],v[64:67]
TR a[134:135],v232 offset:12288
v_max3_f32 v249, v249, v153, v156
MM v[68:71],a[220:223],v[208:211],v[128:131]
TR a[136:137],v233 offset:0
v_max_f32 v249, v249, v157
MM v[68:71],a[216:219],v[192:195],v[68:71]
TR a[138:139],v233 offset:4096
v_cmp_lt_f32 vcc, s71, v249
MM v[68:71],a[212:215],v[176:179],v[68:71]
TR a[140:141],v233 offset:8192
TR a[142:143],v233 offset:12288
MM v[68:71],a[208:211],v[160:163],v[68:71]
s_cbranch_vccnz LSITE4_%=
LBACK4_%=:
MM v[76:79],a[208:211],v[164:167],v[132:135]
s_cmp_lg_u32 %[sb], 0
s_cbranch_scc1 LEP_%=
v_mov_b32 v250, s69
s_mov_b32 s76, 32768
s_mov_b64 exec, 1
buffer_store_dword v250, off, s[20:23], s76 sc1
s_mov_b64 exec, -1
s_nop 1
LEP_%=:
MM v[76:79],a[212:215],v[180:183],v[76:79]
EX v64,v64
MM v[76:79],a[216:219],v[196:199],v[76:79]
EX v65,v65
MM v[76:79],a[220:223],v[212:215],v[76:79]
TR a[144:145],v232 offset:512
CV v64,v64,v65
MM v[72:75],a[204:207],v[212:215],v[132:135]
TR a[146:147],v232 offset:4608
v_lshrrev_b32 v34, 1, v248
MM v[72:75],a[200:203],v[196:199],v[72:75]
s_mov_b32 s90, 86016
buffer_load_dwordx2 v[32:33], v34, s[20:23], s90 offen offset:0 sc1
MM v[72:75],a[196:199],v[180:183],v[72:75]
EX v66,v66
MM v[72:75],a[192:195],v[164:167],v[72:75]
EX v67,v67
MM v[80:83],a[192:195],v[168:171],v[136:139]
EX v68,v68
MM v[80:83],a[196:199],v[184:187],v[80:83]
TR a[148:149],v232 offset:8704
CV v65,v66,v67
MM v[80:83],a[200:203],v[200:203],v[80:83]
EX v69,v69
MM v[80:83],a[204:207],v[216:219],v[80:83]
TR a[150:151],v232 offset:12800
CV v66,v68,v69
MM v[84:87],a[220:223],v[216:219],v[136:139]
EX v70,v70
MM v[84:87],a[216:219],v[200:203],v[84:87]
TR a[152:153],v233 offset:512
TR a[154:155],v233 offset:4608
MM v[84:87],a[212:215],v[184:187],v[84:87]
EX v71,v71
MM v[84:87],a[208:211],v[168:171],v[84:87]
TR a[156:157],v233 offset:8704
CV v67,v70,v71
MM v[92:95],a[208:211],v[172:175],v[140:143]
EX v72,v72
MM v[92:95],a[212:215],v[188:191],v[92:95]
EX v73,v73
MM v[92:95],a[216:219],v[204:207],v[92:95]
TR a[158:159],v233 offset:12800
CV v72,v72,v73
MM v[92:95],a[220:223],v[220:223],v[92:95]
EX v74,v74
MM v[88:91],a[204:207],v[220:223],v[140:143]
TR a[160:161],v232 offset:1024
TR a[162:163],v232 offset:5120
MM v[88:91],a[200:203],v[204:207],v[88:91]
EX v75,v75
MM v[88:91],a[196:199],v[188:191],v[88:91]
TR a[164:165],v232 offset:9216
CV v73,v74,v75
MM v[88:91],a[192:195],v[172:175],v[88:91]
EX v76,v76
MM v[96:99],a[224:227],v[160:163],v[128:131]
EX v77,v77
MM v[96:99],a[228:231],v[176:179],v[96:99]
TR a[166:167],v232 offset:13312
CV v74,v76,v77
MM v[96:99],a[232:235],v[192:195],v[96:99]
EX v78,v78
MM v[96:99],a[236:239],v[208:211],v[96:99]
TR a[168:169],v233 offset:1024
TR a[170:171],v233 offset:5120
MM v[100:103],a[252:255],v[208:211],v[128:131]
EX v79,v79
MM v[100:103],a[248:251],v[192:195],v[100:103]
TR a[172:173],v233 offset:9216
CV v75,v78,v79
MM v[100:103],a[244:247],v[176:179],v[100:103]
EX v80,v80
MM v[100:103],a[240:243],v[160:163],v[100:103]
EX v81,v81
MM v[108:111],a[240:243],v[164:167],v[132:135]
TR a[174:175],v233 offset:13312
CV v80,v80,v81
MM v[108:111],a[244:247],v[180:183],v[108:111]
EX v82,v82
MM v[108:111],a[248:251],v[196:199],v[108:111]
TR a[176:177],v232 offset:1536
TR a[178:179],v232 offset:5632
MM v[108:111],a[252:255],v[212:215],v[108:111]
EX v83,v83
MM v[104:107],a[236:239],v[212:215],v[132:135]
TR a[180:181],v232 offset:9728
CV v81,v82,v83
MM v[104:107],a[232:235],v[196:199],v[104:107]
EX v84,v84
MM v[104:107],a[228:231],v[180:183],v[104:107]
TR a[182:183],v232 offset:13824
TR a[184:185],v233 offset:1536
MM v[104:107],a[224:227],v[164:167],v[104:107]
EX v85,v85
MM v[112:115],a[224:227],v[168:171],v[136:139]
TR a[186:187],v233 offset:5632
CV v82,v84,v85
MM v[112:115],a[228:231],v[184:187],v[112:115]
EX v86,v86
MM v[112:115],a[232:235],v[200:203],v[112:115]
EX v87,v87
MM v[112:115],a[236:239],v[216:219],v[112:115]
TR a[188:189],v233 offset:9728
CV v83,v86,v87
MM v[116:119],a[252:255],v[216:219],v[136:139]
EX v88,v88
MM v[116:119],a[248:251],v[200:203],v[116:119]
EX v89,v89
MM v[116:119],a[244:247],v[184:187],v[116:119]
TR a[190:191],v233 offset:13824
CV v88,v88,v89
MM v[116:119],a[240:243],v[168:171],v[116:119]
EX v90,v90
MM v[124:127],a[240:243],v[172:175],v[140:143]
EX v91,v91
MM v[124:127],a[244:247],v[188:191],v[124:127]
EX v92,v92
MM v[124:127],a[248:251],v[204:207],v[124:127]
EX v93,v93
MM v[124:127],a[252:255],v[220:223],v[124:127]
EX v94,v94
MM v[120:123],a[236:239],v[220:223],v[140:143]
EX v95,v95
MM v[120:123],a[232:235],v[204:207],v[120:123]
EX v96,v96
MM v[120:123],a[228:231],v[188:191],v[120:123]
EX v97,v97
MM v[120:123],a[224:227],v[172:175],v[120:123]
s_waitcnt vmcnt(11) lgkmcnt(0)
s_barrier
MM a[0:3],a[128:131],v[0:3],a[0:3]
s_add_u32 m0, s65, 0
CV v89,v90,v91
CV v90,v92,v93
MM a[0:3],a[132:135],v[4:7],a[0:3]
RD a[192:195],v224 offset:32768
CV v91,v94,v95
MM a[8:11],a[132:135],v[12:15],a[8:11]
buffer_load_dwordx4 v228, s[8:11], s64 offen offset:0 lds
MM a[8:11],a[128:131],v[8:11],a[8:11]
RD a[196:199],v225 offset:32768
CV v68,v96,v97
MM a[16:19],a[128:131],v[16:19],a[16:19]
RD a[200:203],v226 offset:32768
RD a[204:207],v227 offset:32768
MM a[16:19],a[132:135],v[20:23],a[16:19]
buffer_load_dwordx4 v229, s[8:11], s64 offen offset:1024 lds
MM a[24:27],a[132:135],v[28:31],a[24:27]
EX v98,v98
MM a[24:27],a[128:131],v[24:27],a[24:27]
RD a[208:211],v224 offset:36864
RD a[212:215],v225 offset:36864
MM a[28:31],a[136:139],v[24:27],a[28:31]
EX v99,v99
MM a[28:31],a[140:143],v[28:31],a[28:31]
RD a[216:219],v226 offset:36864
CV v69,v98,v99
MM a[20:23],a[140:143],v[20:23],a[20:23]
EX v100,v100
MM a[20:23],a[136:139],v[16:19],a[20:23]
EX v101,v101
MM a[12:15],a[136:139],v[8:11],a[12:15]
s_waitcnt vmcnt(2)
v_cmp_ne_u32 vcc, s69, v33
s_cbranch_vccnz LRRK_%=
LRRKb_%=:
MM a[12:15],a[140:143],v[12:15],a[12:15]
v_add_u32_dpp v32, v32, v32 row_ror:8 row_mask:0xf bank_mask:0xf
CV v70,v100,v101
MM a[4:7],a[140:143],v[4:7],a[4:7]
s_and_b32 s76, %[sb], 7
RD a[220:223],v227 offset:36864
v_mov_b32 v35, v32
MM a[4:7],a[136:139],v[0:3],a[4:7]
s_lshl_b32 s54, s50, 2
v_permlane16_swap_b32 v32, v35
v_add_u32 v32, v32, v35
MM a[32:35],a[144:147],v[0:3],a[32:35]
s_add_u32 s54, s54, %[swave]
RD a[224:227],v224 offset:40960
v_mov_b32 v35, v32
MM a[32:35],a[148:151],v[4:7],a[32:35]
s_lshl_b32 s54, s54, 1
v_permlane32_swap_b32 v32, v35
v_add_u32 v32, v32, v35
MM a[40:43],a[148:151],v[12:15],a[40:43]
s_bfe_u32 s55, s49, 65538
RD a[228:231],v225 offset:40960
v_readlane_b32 s77, v32, s76
MM a[40:43],a[144:147],v[8:11],a[40:43]
s_bfm_b32 s76, s76, 0
EX v102,v102
MM a[48:51],a[144:147],v[16:19],a[48:51]
s_xor_b32 s55, s55, 1
v_cmp_gt_u32 s[60:61], s77, v32
v_cmp_eq_u32 s[62:63], s77, v32
MM a[48:51],a[148:151],v[20:23],a[48:51]
s_and_b32 s62, s62, s76
EX v103,v103
MM a[56:59],a[148:151],v[28:31],a[56:59]
s_or_b32 s60, s60, s62
EX v104,v104
MM a[56:59],a[144:147],v[24:27],a[56:59]
s_and_b32 s60, s60, 255
EX v105,v105
MM a[60:63],a[152:155],v[24:27],a[60:63]
s_bcnt1_i32_b32 s51, s60
s_cmp_lt_u32 s51, 4
s_cselect_b32 s82, 3, 99
MM a[60:63],a[156:159],v[28:31],a[60:63]
s_lshr_b32 s16, %[sb], 3
EX v106,v106
MM a[52:55],a[156:159],v[20:23],a[52:55]
s_lshl_b32 s76, s51, 5
CV v71,v102,v103
CV v76,v104,v105
MM a[52:55],a[152:155],v[16:19],a[52:55]
s_add_u32 s16, s16, s76
EX v107,v107
MM a[44:47],a[152:155],v[8:11],a[44:47]
s_add_u32 s54, s54, s55
buffer_load_dwordx4 v230, s[8:11], s64 offen offset:2048 lds
MM a[44:47],a[156:159],v[12:15],a[44:47]
s_mul_i32 s52, s54, 40960
EX v108,v108
MM a[36:39],a[156:159],v[4:7],a[36:39]
s_lshr_b32 s54, s84, 1
EX v109,v109
MM a[36:39],a[152:155],v[0:3],a[36:39]
s_mul_i32 s54, s54, 640
CV v77,v106,v107
MM a[64:67],a[160:163],v[0:3],a[64:67]
s_add_u32 s52, s52, s54
EX v110,v110
MM a[64:67],a[164:167],v[4:7],a[64:67]
s_lshr_b32 s56, s49, 3
EX v111,v111
MM a[72:75],a[164:167],v[12:15],a[72:75]
s_and_b32 s54, s84, 1
buffer_load_dwordx4 v231, s[8:11], s64 offen offset:3072 lds
MM a[72:75],a[160:163],v[8:11],a[72:75]
s_lshl_b32 s55, s56, 1
CV v78,v108,v109
CV v79,v110,v111
MM a[80:83],a[160:163],v[16:19],a[80:83]
s_add_u32 s55, s55, s54
EX v112,v112
MM a[80:83],a[164:167],v[20:23],a[80:83]
s_mul_i32 s53, s55, 5120
EX v113,v113
MM a[88:91],a[164:167],v[28:31],a[88:91]
s_add_u32 s53, s53, s52
EX v114,v114
MM a[88:91],a[160:163],v[24:27],a[88:91]
s_mul_i32 s54, s54, 384
s_add_u32 m0, s65, 65536
CV v84,v112,v113
MM a[92:95],a[168:171],v[24:27],a[92:95]
s_add_u32 s52, s52, s54
EX v115,v115
MM a[92:95],a[172:175],v[28:31],a[92:95]
s_and_b32 s55, s56, 2
buffer_load_dwordx4 v235, s[12:15], s81 offen offset:0 lds
MM a[84:87],a[172:175],v[20:23],a[84:87]
s_lshl_b32 s55, s55, 6
EX v116,v116
MM a[84:87],a[168:171],v[16:19],a[84:87]
s_add_u32 s52, s52, s55
RD a[232:235],v226 offset:40960
CV v85,v114,v115
MM a[76:79],a[168:171],v[8:11],a[76:79]
s_and_b32 s55, s56, 1
EX v117,v117
MM a[76:79],a[172:175],v[12:15],a[76:79]
s_mul_i32 s55, s55, 20480
CV v86,v116,v117
MM a[68:71],a[172:175],v[4:7],a[68:71]
s_add_u32 s52, s52, s55
v_lshrrev_b32 v40, 7, v248
v_mul_u32_u24 v40, 1280, v40
MM a[68:71],a[168:171],v[0:3],a[68:71]
v_and_b32 v41, 112, v248
v_add_u32 v40, v40, v41
MM a[96:99],a[176:179],v[0:3],a[96:99]
buffer_store_dwordx4 v[236:239], v40, s[40:43], s52 offen offset:0 sc1
EX v118,v118
MM a[96:99],a[180:183],v[4:7],a[96:99]
s_add_u32 s52, s52, 10240
EX v119,v119
MM a[104:107],a[180:183],v[12:15],a[104:107]
EX v120,v120
MM a[104:107],a[176:179],v[8:11],a[104:107]
buffer_load_dwordx4 v235, s[12:15], s64 offen offset:1024 lds
MM a[112:115],a[176:179],v[16:19],a[112:115]
EX v121,v121
MM a[112:115],a[180:183],v[20:23],a[112:115]
RD a[236:239],v227 offset:40960
CV v87,v118,v119
MM a[120:123],a[180:183],v[28:31],a[120:123]
EX v122,v122
MM a[120:123],a[176:179],v[24:27],a[120:123]
buffer_store_dwordx4 v[236:239], v40, s[40:43], s52 offen offset:0 sc1
MM a[124:127],a[184:187],v[24:27],a[124:127]
v_add_f32 v244, v144, v145
v_add_f32 v245, v148, v149
MM a[124:127],a[188:191],v[28:31],a[124:127]
v_add_f32 v246, v152, v153
v_add_f32 v247, v156, v157
MM a[116:119],a[188:191],v[20:23],a[116:119]
EX v123,v123
MM a[116:119],a[184:187],v[16:19],a[116:119]
EX v124,v124
MM a[108:111],a[184:187],v[8:11],a[108:111]
RD a[240:243],v224 offset:45056
CV v92,v120,v121
MM a[108:111],a[188:191],v[12:15],a[108:111]
s_mov_b32 exec_hi, 0
buffer_store_dwordx4 v[236:239], v40, s[40:43], s53 offen offset:256 sc1
)ASM"
R"ASM(
s_mov_b64 exec, -1
EX v125,v125
MM a[100:103],a[188:191],v[4:7],a[100:103]
s_nop 0
buffer_load_dwordx4 v235, s[12:15], s81 offen offset:2048 lds
MM a[100:103],a[184:187],v[0:3],a[100:103]
EX v126,v126
v_smfmac_f32_16x16x64_bf16 v[144:147], v[236:239], v[64:71], v234
RD a[244:247],v225 offset:45056
RD a[248:251],v226 offset:45056
RD a[252:255],v227 offset:45056
EX v127,v127
v_smfmac_f32_16x16x64_bf16 v[148:151], v[236:239], v[72:79], v234
CV v93,v122,v123
v_smfmac_f32_16x16x64_bf16 v[152:155], v[236:239], v[80:87], v234
CV v94,v124,v125
CV v95,v126,v127
s_nop 1
v_smfmac_f32_16x16x64_bf16 v[156:159], v[236:239], v[88:95], v234
s_waitcnt lgkmcnt(0)
buffer_load_dwordx4 v235, s[12:15], s64 offen offset:3072 lds
s_cmp_lt_u32 s51, 4
s_cbranch_scc1 LLAR_%=
s_mov_b32 s70, 6
s_branch LLAP_%=
s_nop 0
s_nop 0
s_nop 0
s_nop 0
s_nop 0
s_nop 0
LPG7_%=:
s_cbranch_execz LPG11_%=
s_nop 0
s_nop 0
s_nop 0
s_nop 0
s_nop 0
s_nop 0
s_nop 0
s_nop 0
s_nop 0
s_nop 0
s_nop 0
s_nop 0
s_nop 0
s_nop 0
s_nop 0
s_nop 0
s_nop 0
s_nop 0
s_nop 0
s_nop 0
s_nop 0
s_nop 0
s_nop 0
s_nop 0
s_nop 0
s_nop 0
s_nop 0
s_nop 0
s_nop 0
s_nop 0
s_nop 0
s_nop 0
s_nop 0
s_nop 0
s_nop 0
s_nop 0
s_nop 0
s_nop 0
s_nop 0
s_nop 0
s_nop 0
s_nop 0
s_nop 0
s_nop 0
s_nop 0
s_nop 0
s_nop 0
s_nop 0
s_nop 0
s_nop 0
s_nop 0
s_nop 0
s_nop 0
s_nop 0
s_nop 0
s_nop 0
s_nop 0
s_nop 0
s_nop 0
s_nop 0
s_nop 0
s_nop 0
s_nop 0
LLAP_%=:
MM v[0:3],a[192:195],v[160:163],v[128:131]
s_add_u32 s64, s64, 16384
TR a[128:129],v232 offset:16384
s_add_u32 s81, s64, 896
MM v[0:3],a[196:199],v[176:179],v[0:3]
TR a[130:131],v232 offset:20480
v_max3_f32 v249, v144, v145, v148
MM v[0:3],a[200:203],v[192:195],v[0:3]
TR a[132:133],v232 offset:24576
v_max3_f32 v249, v249, v149, v152
MM v[0:3],a[204:207],v[208:211],v[0:3]
TR a[134:135],v232 offset:28672
v_max3_f32 v249, v249, v153, v156
MM v[4:7],a[220:223],v[208:211],v[128:131]
TR a[136:137],v233 offset:16384
v_max_f32 v249, v249, v157
MM v[4:7],a[216:219],v[192:195],v[4:7]
TR a[138:139],v233 offset:20480
v_cmp_lt_f32 vcc, s71, v249
MM v[4:7],a[212:215],v[176:179],v[4:7]
TR a[140:141],v233 offset:24576
TR a[142:143],v233 offset:28672
MM v[4:7],a[208:211],v[160:163],v[4:7]
s_cbranch_vccnz LSITE5_%=
LBACK5_%=:
MM v[12:15],a[208:211],v[164:167],v[132:135]
EX v0,v0
MM v[12:15],a[212:215],v[180:183],v[12:15]
EX v1,v1
MM v[12:15],a[216:219],v[196:199],v[12:15]
TR a[144:145],v232 offset:16896
CV v0,v0,v1
MM v[12:15],a[220:223],v[212:215],v[12:15]
EX v2,v2
MM v[8:11],a[204:207],v[212:215],v[132:135]
EX v3,v3
MM v[8:11],a[200:203],v[196:199],v[8:11]
TR a[146:147],v232 offset:20992
CV v1,v2,v3
MM v[8:11],a[196:199],v[180:183],v[8:11]
EX v4,v4
MM v[8:11],a[192:195],v[164:167],v[8:11]
TR a[148:149],v232 offset:25088
TR a[150:151],v232 offset:29184
MM v[16:19],a[192:195],v[168:171],v[136:139]
EX v5,v5
MM v[16:19],a[196:199],v[184:187],v[16:19]
TR a[152:153],v233 offset:16896
CV v2,v4,v5
MM v[16:19],a[200:203],v[200:203],v[16:19]
EX v6,v6
MM v[16:19],a[204:207],v[216:219],v[16:19]
TR a[154:155],v233 offset:20992
TR a[156:157],v233 offset:25088
MM v[20:23],a[220:223],v[216:219],v[136:139]
EX v7,v7
MM v[20:23],a[216:219],v[200:203],v[20:23]
TR a[158:159],v233 offset:29184
CV v3,v6,v7
MM v[20:23],a[212:215],v[184:187],v[20:23]
EX v8,v8
MM v[20:23],a[208:211],v[168:171],v[20:23]
EX v9,v9
MM v[28:31],a[208:211],v[172:175],v[140:143]
TR a[160:161],v232 offset:17408
CV v8,v8,v9
MM v[28:31],a[212:215],v[188:191],v[28:31]
EX v10,v10
MM v[28:31],a[216:219],v[204:207],v[28:31]
TR a[162:163],v232 offset:21504
TR a[164:165],v232 offset:25600
MM v[28:31],a[220:223],v[220:223],v[28:31]
EX v11,v11
MM v[24:27],a[204:207],v[220:223],v[140:143]
TR a[166:167],v232 offset:29696
CV v9,v10,v11
MM v[24:27],a[200:203],v[204:207],v[24:27]
EX v12,v12
MM v[24:27],a[196:199],v[188:191],v[24:27]
EX v13,v13
MM v[24:27],a[192:195],v[172:175],v[24:27]
TR a[168:169],v233 offset:17408
CV v10,v12,v13
MM v[32:35],a[224:227],v[160:163],v[128:131]
EX v14,v14
MM v[32:35],a[228:231],v[176:179],v[32:35]
TR a[170:171],v233 offset:21504
TR a[172:173],v233 offset:25600
MM v[32:35],a[232:235],v[192:195],v[32:35]
EX v15,v15
MM v[32:35],a[236:239],v[208:211],v[32:35]
TR a[174:175],v233 offset:29696
CV v11,v14,v15
MM v[36:39],a[252:255],v[208:211],v[128:131]
EX v16,v16
MM v[36:39],a[248:251],v[192:195],v[36:39]
EX v17,v17
MM v[36:39],a[244:247],v[176:179],v[36:39]
TR a[176:177],v232 offset:17920
CV v16,v16,v17
MM v[36:39],a[240:243],v[160:163],v[36:39]
EX v18,v18
MM v[44:47],a[240:243],v[164:167],v[132:135]
TR a[178:179],v232 offset:22016
TR a[180:181],v232 offset:26112
MM v[44:47],a[244:247],v[180:183],v[44:47]
EX v19,v19
MM v[44:47],a[248:251],v[196:199],v[44:47]
TR a[182:183],v232 offset:30208
CV v17,v18,v19
MM v[44:47],a[252:255],v[212:215],v[44:47]
EX v20,v20
MM v[40:43],a[236:239],v[212:215],v[132:135]
TR a[184:185],v233 offset:17920
TR a[186:187],v233 offset:22016
MM v[40:43],a[232:235],v[196:199],v[40:43]
EX v21,v21
MM v[40:43],a[228:231],v[180:183],v[40:43]
TR a[188:189],v233 offset:26112
CV v18,v20,v21
MM v[40:43],a[224:227],v[164:167],v[40:43]
EX v22,v22
MM v[48:51],a[224:227],v[168:171],v[136:139]
EX v23,v23
MM v[48:51],a[228:231],v[184:187],v[48:51]
TR a[190:191],v233 offset:30208
CV v19,v22,v23
MM v[48:51],a[232:235],v[200:203],v[48:51]
EX v24,v24
MM v[48:51],a[236:239],v[216:219],v[48:51]
EX v25,v25
MM v[52:55],a[252:255],v[216:219],v[136:139]
EX v26,v26
MM v[52:55],a[248:251],v[200:203],v[52:55]
EX v27,v27
MM v[52:55],a[244:247],v[184:187],v[52:55]
EX v28,v28
MM v[52:55],a[240:243],v[168:171],v[52:55]
EX v29,v29
MM v[60:63],a[240:243],v[172:175],v[140:143]
EX v30,v30
MM v[60:63],a[244:247],v[188:191],v[60:63]
EX v31,v31
MM v[60:63],a[248:251],v[204:207],v[60:63]
EX v32,v32
MM v[60:63],a[252:255],v[220:223],v[60:63]
EX v33,v33
MM v[56:59],a[236:239],v[220:223],v[140:143]
EX v34,v34
MM v[56:59],a[232:235],v[204:207],v[56:59]
EX v35,v35
MM v[56:59],a[228:231],v[188:191],v[56:59]
EX v36,v36
MM v[56:59],a[224:227],v[172:175],v[56:59]
s_waitcnt vmcnt(12) lgkmcnt(0)
s_barrier
MM a[0:3],a[128:131],v[64:67],a[0:3]
s_add_u32 m0, s65, 16384
CV v24,v24,v25
CV v25,v26,v27
MM a[0:3],a[132:135],v[68:71],a[0:3]
RD a[192:195],v224 offset:49152
CV v26,v28,v29
MM a[8:11],a[132:135],v[76:79],a[8:11]
RD a[196:199],v225 offset:49152
CV v27,v30,v31
MM a[8:11],a[128:131],v[72:75],a[8:11]
buffer_load_dwordx4 v228, s[8:11], s64 offen offset:0 lds
MM a[16:19],a[128:131],v[80:83],a[16:19]
RD a[200:203],v226 offset:49152
CV v4,v32,v33
MM a[16:19],a[132:135],v[84:87],a[16:19]
RD a[204:207],v227 offset:49152
CV v5,v34,v35
MM a[24:27],a[132:135],v[92:95],a[24:27]
buffer_load_dwordx4 v229, s[8:11], s64 offen offset:1024 lds
MM a[24:27],a[128:131],v[88:91],a[24:27]
RD a[208:211],v224 offset:53248
RD a[212:215],v225 offset:53248
MM a[28:31],a[136:139],v[88:91],a[28:31]
buffer_load_dwordx4 v230, s[8:11], s64 offen offset:2048 lds
MM a[28:31],a[140:143],v[92:95],a[28:31]
RD a[216:219],v226 offset:53248
RD a[220:223],v227 offset:53248
MM a[20:23],a[140:143],v[84:87],a[20:23]
EX v37,v37
MM a[20:23],a[136:139],v[80:83],a[20:23]
RD a[224:227],v224 offset:57344
CV v6,v36,v37
MM a[12:15],a[136:139],v[72:75],a[12:15]
EX v38,v38
MM a[12:15],a[140:143],v[76:79],a[12:15]
buffer_load_dwordx4 v231, s[8:11], s64 offen offset:3072 lds
MM a[4:7],a[140:143],v[68:71],a[4:7]
s_add_u32 m0, s65, 81920
EX v39,v39
MM a[4:7],a[136:139],v[64:67],a[4:7]
RD a[228:231],v225 offset:57344
CV v7,v38,v39
MM a[32:35],a[144:147],v[64:67],a[32:35]
EX v40,v40
MM a[32:35],a[148:151],v[68:71],a[32:35]
EX v41,v41
MM a[40:43],a[148:151],v[76:79],a[40:43]
RD a[232:235],v226 offset:57344
CV v12,v40,v41
MM a[40:43],a[144:147],v[72:75],a[40:43]
EX v42,v42
MM a[48:51],a[144:147],v[80:83],a[48:51]
buffer_load_dwordx4 v235, s[12:15], s81 offen offset:0 lds
MM a[48:51],a[148:151],v[84:87],a[48:51]
EX v43,v43
MM a[56:59],a[148:151],v[92:95],a[56:59]
RD a[236:239],v227 offset:57344
CV v13,v42,v43
MM a[56:59],a[144:147],v[88:91],a[56:59]
EX v44,v44
MM a[60:63],a[152:155],v[88:91],a[60:63]
EX v45,v45
MM a[60:63],a[156:159],v[92:95],a[60:63]
RD a[240:243],v224 offset:61440
CV v14,v44,v45
MM a[52:55],a[156:159],v[84:87],a[52:55]
EX v46,v46
MM a[52:55],a[152:155],v[80:83],a[52:55]
buffer_load_dwordx4 v235, s[12:15], s64 offen offset:1024 lds
MM a[44:47],a[152:155],v[72:75],a[44:47]
EX v47,v47
MM a[44:47],a[156:159],v[76:79],a[44:47]
RD a[244:247],v225 offset:61440
CV v15,v46,v47
MM a[36:39],a[156:159],v[68:71],a[36:39]
EX v48,v48
MM a[36:39],a[152:155],v[64:67],a[36:39]
EX v49,v49
MM a[64:67],a[160:163],v[64:67],a[64:67]
RD a[248:251],v226 offset:61440
CV v20,v48,v49
MM a[64:67],a[164:167],v[68:71],a[64:67]
EX v50,v50
MM a[72:75],a[164:167],v[76:79],a[72:75]
buffer_load_dwordx4 v235, s[12:15], s81 offen offset:2048 lds
MM a[72:75],a[160:163],v[72:75],a[72:75]
EX v51,v51
MM a[80:83],a[160:163],v[80:83],a[80:83]
RD a[252:255],v227 offset:61440
CV v21,v50,v51
MM a[80:83],a[164:167],v[84:87],a[80:83]
EX v52,v52
MM a[88:91],a[164:167],v[92:95],a[88:91]
EX v53,v53
MM a[88:91],a[160:163],v[88:91],a[88:91]
buffer_load_dwordx4 v235, s[12:15], s64 offen offset:3072 lds
MM a[92:95],a[168:171],v[88:91],a[92:95]
EX v54,v54
MM a[92:95],a[172:175],v[92:95],a[92:95]
EX v55,v55
MM a[84:87],a[172:175],v[84:87],a[84:87]
EX v56,v56
MM a[84:87],a[168:171],v[80:83],a[84:87]
EX v57,v57
MM a[76:79],a[168:171],v[72:75],a[76:79]
EX v58,v58
MM a[76:79],a[172:175],v[76:79],a[76:79]
EX v59,v59
MM a[68:71],a[172:175],v[68:71],a[68:71]
EX v60,v60
MM a[68:71],a[168:171],v[64:67],a[68:71]
EX v61,v61
MM a[96:99],a[176:179],v[64:67],a[96:99]
EX v62,v62
MM a[96:99],a[180:183],v[68:71],a[96:99]
EX v63,v63
MM a[104:107],a[180:183],v[76:79],a[104:107]
CV v22,v52,v53
CV v23,v54,v55
MM a[104:107],a[176:179],v[72:75],a[104:107]
CV v28,v56,v57
CV v29,v58,v59
MM a[112:115],a[176:179],v[80:83],a[112:115]
CV v30,v60,v61
CV v31,v62,v63
MM a[112:115],a[180:183],v[84:87],a[112:115]
MM a[120:123],a[180:183],v[92:95],a[120:123]
MM a[120:123],a[176:179],v[88:91],a[120:123]
MM a[124:127],a[184:187],v[88:91],a[124:127]
v_add_f32 v244, v144, v145
v_add_f32 v245, v148, v149
MM a[124:127],a[188:191],v[92:95],a[124:127]
v_add_f32 v246, v152, v153
v_add_f32 v247, v156, v157
MM a[116:119],a[188:191],v[84:87],a[116:119]
s_cmp_eq_u32 s70, s82
s_cbranch_scc1 LWW_%=
LLALWW_%=:
MM a[116:119],a[184:187],v[80:83],a[116:119]
MM a[108:111],a[184:187],v[72:75],a[108:111]
MM a[108:111],a[188:191],v[76:79],a[108:111]
MM a[100:103],a[188:191],v[68:71],a[100:103]
MM a[100:103],a[184:187],v[64:67],a[100:103]
v_smfmac_f32_16x16x64_bf16 v[144:147], v[236:239], v[0:7], v234
v_smfmac_f32_16x16x64_bf16 v[148:151], v[236:239], v[8:15], v234
v_smfmac_f32_16x16x64_bf16 v[152:155], v[236:239], v[16:23], v234
v_smfmac_f32_16x16x64_bf16 v[156:159], v[236:239], v[24:31], v234
s_waitcnt lgkmcnt(0)
MM v[64:67],a[192:195],v[160:163],v[128:131]
s_add_u32 s64, s64, 16384
TR a[128:129],v232 offset:32768
s_add_u32 s81, s64, 896
MM v[64:67],a[196:199],v[176:179],v[64:67]
TR a[130:131],v232 offset:36864
v_max3_f32 v249, v144, v145, v148
MM v[64:67],a[200:203],v[192:195],v[64:67]
TR a[132:133],v232 offset:40960
v_max3_f32 v249, v249, v149, v152
MM v[64:67],a[204:207],v[208:211],v[64:67]
TR a[134:135],v232 offset:45056
v_max3_f32 v249, v249, v153, v156
MM v[68:71],a[220:223],v[208:211],v[128:131]
TR a[136:137],v233 offset:32768
v_max_f32 v249, v249, v157
MM v[68:71],a[216:219],v[192:195],v[68:71]
TR a[138:139],v233 offset:36864
v_cmp_lt_f32 vcc, s71, v249
MM v[68:71],a[212:215],v[176:179],v[68:71]
TR a[140:141],v233 offset:40960
TR a[142:143],v233 offset:45056
MM v[68:71],a[208:211],v[160:163],v[68:71]
s_cbranch_vccnz LSITE6_%=
LBACK6_%=:
MM v[76:79],a[208:211],v[164:167],v[132:135]
EX v64,v64
MM v[76:79],a[212:215],v[180:183],v[76:79]
EX v65,v65
MM v[76:79],a[216:219],v[196:199],v[76:79]
TR a[144:145],v232 offset:33280
CV v64,v64,v65
MM v[76:79],a[220:223],v[212:215],v[76:79]
EX v66,v66
MM v[72:75],a[204:207],v[212:215],v[132:135]
EX v67,v67
MM v[72:75],a[200:203],v[196:199],v[72:75]
TR a[146:147],v232 offset:37376
CV v65,v66,v67
MM v[72:75],a[196:199],v[180:183],v[72:75]
)ASM"
R"ASM(
EX v68,v68
MM v[72:75],a[192:195],v[164:167],v[72:75]
TR a[148:149],v232 offset:41472
TR a[150:151],v232 offset:45568
MM v[80:83],a[192:195],v[168:171],v[136:139]
EX v69,v69
MM v[80:83],a[196:199],v[184:187],v[80:83]
TR a[152:153],v233 offset:33280
CV v66,v68,v69
MM v[80:83],a[200:203],v[200:203],v[80:83]
EX v70,v70
MM v[80:83],a[204:207],v[216:219],v[80:83]
TR a[154:155],v233 offset:37376
TR a[156:157],v233 offset:41472
MM v[84:87],a[220:223],v[216:219],v[136:139]
EX v71,v71
MM v[84:87],a[216:219],v[200:203],v[84:87]
TR a[158:159],v233 offset:45568
CV v67,v70,v71
MM v[84:87],a[212:215],v[184:187],v[84:87]
EX v72,v72
MM v[84:87],a[208:211],v[168:171],v[84:87]
EX v73,v73
MM v[92:95],a[208:211],v[172:175],v[140:143]
TR a[160:161],v232 offset:33792
CV v72,v72,v73
MM v[92:95],a[212:215],v[188:191],v[92:95]
EX v74,v74
MM v[92:95],a[216:219],v[204:207],v[92:95]
TR a[162:163],v232 offset:37888
TR a[164:165],v232 offset:41984
MM v[92:95],a[220:223],v[220:223],v[92:95]
EX v75,v75
MM v[88:91],a[204:207],v[220:223],v[140:143]
TR a[166:167],v232 offset:46080
CV v73,v74,v75
MM v[88:91],a[200:203],v[204:207],v[88:91]
EX v76,v76
MM v[88:91],a[196:199],v[188:191],v[88:91]
EX v77,v77
MM v[88:91],a[192:195],v[172:175],v[88:91]
TR a[168:169],v233 offset:33792
CV v74,v76,v77
MM v[96:99],a[224:227],v[160:163],v[128:131]
EX v78,v78
MM v[96:99],a[228:231],v[176:179],v[96:99]
TR a[170:171],v233 offset:37888
TR a[172:173],v233 offset:41984
MM v[96:99],a[232:235],v[192:195],v[96:99]
EX v79,v79
MM v[96:99],a[236:239],v[208:211],v[96:99]
TR a[174:175],v233 offset:46080
CV v75,v78,v79
MM v[100:103],a[252:255],v[208:211],v[128:131]
EX v80,v80
MM v[100:103],a[248:251],v[192:195],v[100:103]
EX v81,v81
MM v[100:103],a[244:247],v[176:179],v[100:103]
TR a[176:177],v232 offset:34304
CV v80,v80,v81
MM v[100:103],a[240:243],v[160:163],v[100:103]
EX v82,v82
MM v[108:111],a[240:243],v[164:167],v[132:135]
buffer_store_dword v243, v248, s[20:23], s67 offen offset:12 sc1
MM v[108:111],a[244:247],v[180:183],v[108:111]
s_add_u32 s67, s67, 1024
TR a[178:179],v232 offset:38400
TR a[180:181],v232 offset:42496
MM v[108:111],a[248:251],v[196:199],v[108:111]
EX v83,v83
MM v[108:111],a[252:255],v[212:215],v[108:111]
TR a[182:183],v232 offset:46592
CV v81,v82,v83
MM v[104:107],a[236:239],v[212:215],v[132:135]
EX v84,v84
MM v[104:107],a[232:235],v[196:199],v[104:107]
EX v85,v85
MM v[104:107],a[228:231],v[180:183],v[104:107]
TR a[184:185],v233 offset:34304
CV v82,v84,v85
MM v[104:107],a[224:227],v[164:167],v[104:107]
EX v86,v86
MM v[112:115],a[224:227],v[168:171],v[136:139]
TR a[186:187],v233 offset:38400
TR a[188:189],v233 offset:42496
MM v[112:115],a[228:231],v[184:187],v[112:115]
EX v87,v87
MM v[112:115],a[232:235],v[200:203],v[112:115]
TR a[190:191],v233 offset:46592
CV v83,v86,v87
MM v[112:115],a[236:239],v[216:219],v[112:115]
EX v88,v88
MM v[116:119],a[252:255],v[216:219],v[136:139]
EX v89,v89
MM v[116:119],a[248:251],v[200:203],v[116:119]
EX v90,v90
MM v[116:119],a[244:247],v[184:187],v[116:119]
EX v91,v91
MM v[116:119],a[240:243],v[168:171],v[116:119]
EX v92,v92
MM v[124:127],a[240:243],v[172:175],v[140:143]
EX v93,v93
MM v[124:127],a[244:247],v[188:191],v[124:127]
EX v94,v94
MM v[124:127],a[248:251],v[204:207],v[124:127]
EX v95,v95
MM v[124:127],a[252:255],v[220:223],v[124:127]
EX v96,v96
MM v[120:123],a[236:239],v[220:223],v[140:143]
EX v97,v97
MM v[120:123],a[232:235],v[204:207],v[120:123]
EX v98,v98
MM v[120:123],a[228:231],v[188:191],v[120:123]
EX v99,v99
MM v[120:123],a[224:227],v[172:175],v[120:123]
s_waitcnt vmcnt(12) lgkmcnt(0)
s_barrier
MM a[0:3],a[128:131],v[0:3],a[0:3]
s_add_u32 m0, s65, 32768
CV v88,v88,v89
CV v89,v90,v91
MM a[0:3],a[132:135],v[4:7],a[0:3]
RD a[192:195],v224 offset:0
CV v90,v92,v93
MM a[8:11],a[132:135],v[12:15],a[8:11]
RD a[196:199],v225 offset:0
CV v91,v94,v95
MM a[8:11],a[128:131],v[8:11],a[8:11]
buffer_load_dwordx4 v228, s[8:11], s64 offen offset:0 lds
MM a[16:19],a[128:131],v[16:19],a[16:19]
RD a[200:203],v226 offset:0
CV v68,v96,v97
MM a[16:19],a[132:135],v[20:23],a[16:19]
RD a[204:207],v227 offset:0
CV v69,v98,v99
MM a[24:27],a[132:135],v[28:31],a[24:27]
buffer_load_dwordx4 v229, s[8:11], s64 offen offset:1024 lds
MM a[24:27],a[128:131],v[24:27],a[24:27]
RD a[208:211],v224 offset:4096
RD a[212:215],v225 offset:4096
MM a[28:31],a[136:139],v[24:27],a[28:31]
buffer_load_dwordx4 v230, s[8:11], s64 offen offset:2048 lds
MM a[28:31],a[140:143],v[28:31],a[28:31]
EX v100,v100
MM a[20:23],a[140:143],v[20:23],a[20:23]
EX v101,v101
MM a[20:23],a[136:139],v[16:19],a[20:23]
RD a[216:219],v226 offset:4096
CV v70,v100,v101
MM a[12:15],a[136:139],v[8:11],a[12:15]
EX v102,v102
MM a[12:15],a[140:143],v[12:15],a[12:15]
RD a[220:223],v227 offset:4096
RD a[224:227],v224 offset:8192
MM a[4:7],a[140:143],v[4:7],a[4:7]
EX v103,v103
MM a[4:7],a[136:139],v[0:3],a[4:7]
RD a[228:231],v225 offset:8192
CV v71,v102,v103
MM a[32:35],a[144:147],v[0:3],a[32:35]
EX v104,v104
MM a[32:35],a[148:151],v[4:7],a[32:35]
buffer_load_dwordx4 v231, s[8:11], s64 offen offset:3072 lds
MM a[40:43],a[148:151],v[12:15],a[40:43]
s_add_u32 m0, s65, 98304
EX v105,v105
MM a[40:43],a[144:147],v[8:11],a[40:43]
RD a[232:235],v226 offset:8192
CV v76,v104,v105
MM a[48:51],a[144:147],v[16:19],a[48:51]
EX v106,v106
MM a[48:51],a[148:151],v[20:23],a[48:51]
EX v107,v107
MM a[56:59],a[148:151],v[28:31],a[56:59]
RD a[236:239],v227 offset:8192
CV v77,v106,v107
MM a[56:59],a[144:147],v[24:27],a[56:59]
EX v108,v108
MM a[60:63],a[152:155],v[24:27],a[60:63]
buffer_load_dwordx4 v235, s[12:15], s81 offen offset:0 lds
MM a[60:63],a[156:159],v[28:31],a[60:63]
EX v109,v109
MM a[52:55],a[156:159],v[20:23],a[52:55]
RD a[240:243],v224 offset:12288
CV v78,v108,v109
MM a[52:55],a[152:155],v[16:19],a[52:55]
EX v110,v110
MM a[44:47],a[152:155],v[8:11],a[44:47]
EX v111,v111
MM a[44:47],a[156:159],v[12:15],a[44:47]
RD a[244:247],v225 offset:12288
CV v79,v110,v111
MM a[36:39],a[156:159],v[4:7],a[36:39]
buffer_load_dwordx4 v235, s[12:15], s64 offen offset:1024 lds
MM a[36:39],a[152:155],v[0:3],a[36:39]
EX v112,v112
MM a[64:67],a[160:163],v[0:3],a[64:67]
EX v113,v113
MM a[64:67],a[164:167],v[4:7],a[64:67]
RD a[248:251],v226 offset:12288
CV v84,v112,v113
MM a[72:75],a[164:167],v[12:15],a[72:75]
EX v114,v114
MM a[72:75],a[160:163],v[8:11],a[72:75]
LPG8_%=:
s_cbranch_execz LPG12_%=
EX v115,v115
MM a[80:83],a[160:163],v[16:19],a[80:83]
RD a[252:255],v227 offset:12288
CV v85,v114,v115
MM a[80:83],a[164:167],v[20:23],a[80:83]
buffer_load_dwordx4 v235, s[12:15], s81 offen offset:2048 lds
MM a[88:91],a[164:167],v[28:31],a[88:91]
EX v116,v116
MM a[88:91],a[160:163],v[24:27],a[88:91]
EX v117,v117
MM a[92:95],a[168:171],v[24:27],a[92:95]
EX v118,v118
MM a[92:95],a[172:175],v[28:31],a[92:95]
buffer_load_dwordx4 v235, s[12:15], s64 offen offset:3072 lds
MM a[84:87],a[172:175],v[20:23],a[84:87]
EX v119,v119
MM a[84:87],a[168:171],v[16:19],a[84:87]
EX v120,v120
MM a[76:79],a[168:171],v[8:11],a[76:79]
EX v121,v121
MM a[76:79],a[172:175],v[12:15],a[76:79]
EX v122,v122
MM a[68:71],a[172:175],v[4:7],a[68:71]
EX v123,v123
MM a[68:71],a[168:171],v[0:3],a[68:71]
EX v124,v124
MM a[96:99],a[176:179],v[0:3],a[96:99]
EX v125,v125
MM a[96:99],a[180:183],v[4:7],a[96:99]
EX v126,v126
MM a[104:107],a[180:183],v[12:15],a[104:107]
EX v127,v127
MM a[104:107],a[176:179],v[8:11],a[104:107]
CV v86,v116,v117
CV v87,v118,v119
MM a[112:115],a[176:179],v[16:19],a[112:115]
CV v92,v120,v121
CV v93,v122,v123
MM a[112:115],a[180:183],v[20:23],a[112:115]
CV v94,v124,v125
CV v95,v126,v127
MM a[120:123],a[180:183],v[28:31],a[120:123]
MM a[120:123],a[176:179],v[24:27],a[120:123]
MM a[124:127],a[184:187],v[24:27],a[124:127]
v_add_f32 v244, v144, v145
v_add_f32 v245, v148, v149
MM a[124:127],a[188:191],v[28:31],a[124:127]
v_add_f32 v246, v152, v153
v_add_f32 v247, v156, v157
MM a[116:119],a[188:191],v[20:23],a[116:119]
MM a[116:119],a[184:187],v[16:19],a[116:119]
MM a[108:111],a[184:187],v[8:11],a[108:111]
MM a[108:111],a[188:191],v[12:15],a[108:111]
MM a[100:103],a[188:191],v[4:7],a[100:103]
MM a[100:103],a[184:187],v[0:3],a[100:103]
v_smfmac_f32_16x16x64_bf16 v[144:147], v[236:239], v[64:71], v234
v_smfmac_f32_16x16x64_bf16 v[148:151], v[236:239], v[72:79], v234
v_smfmac_f32_16x16x64_bf16 v[152:155], v[236:239], v[80:87], v234
v_smfmac_f32_16x16x64_bf16 v[156:159], v[236:239], v[88:95], v234
s_waitcnt lgkmcnt(0)
MM v[0:3],a[192:195],v[160:163],v[128:131]
s_add_u32 s64, s64, 16384
TR a[128:129],v232 offset:49152
s_add_u32 s81, s64, 896
MM v[0:3],a[196:199],v[176:179],v[0:3]
TR a[130:131],v232 offset:53248
v_max3_f32 v249, v144, v145, v148
MM v[0:3],a[200:203],v[192:195],v[0:3]
TR a[132:133],v232 offset:57344
v_max3_f32 v249, v249, v149, v152
MM v[0:3],a[204:207],v[208:211],v[0:3]
TR a[134:135],v232 offset:61440
v_max3_f32 v249, v249, v153, v156
MM v[4:7],a[220:223],v[208:211],v[128:131]
TR a[136:137],v233 offset:49152
v_max_f32 v249, v249, v157
MM v[4:7],a[216:219],v[192:195],v[4:7]
TR a[138:139],v233 offset:53248
v_cmp_lt_f32 vcc, s71, v249
MM v[4:7],a[212:215],v[176:179],v[4:7]
TR a[140:141],v233 offset:57344
TR a[142:143],v233 offset:61440
MM v[4:7],a[208:211],v[160:163],v[4:7]
s_cbranch_vccnz LSITE7_%=
LBACK7_%=:
MM v[12:15],a[208:211],v[164:167],v[132:135]
EX v0,v0
MM v[12:15],a[212:215],v[180:183],v[12:15]
EX v1,v1
MM v[12:15],a[216:219],v[196:199],v[12:15]
TR a[144:145],v232 offset:49664
CV v0,v0,v1
MM v[12:15],a[220:223],v[212:215],v[12:15]
EX v2,v2
MM v[8:11],a[204:207],v[212:215],v[132:135]
EX v3,v3
MM v[8:11],a[200:203],v[196:199],v[8:11]
TR a[146:147],v232 offset:53760
CV v1,v2,v3
MM v[8:11],a[196:199],v[180:183],v[8:11]
EX v4,v4
MM v[8:11],a[192:195],v[164:167],v[8:11]
TR a[148:149],v232 offset:57856
TR a[150:151],v232 offset:61952
MM v[16:19],a[192:195],v[168:171],v[136:139]
EX v5,v5
MM v[16:19],a[196:199],v[184:187],v[16:19]
TR a[152:153],v233 offset:49664
CV v2,v4,v5
MM v[16:19],a[200:203],v[200:203],v[16:19]
EX v6,v6
MM v[16:19],a[204:207],v[216:219],v[16:19]
TR a[154:155],v233 offset:53760
TR a[156:157],v233 offset:57856
MM v[20:23],a[220:223],v[216:219],v[136:139]
EX v7,v7
MM v[20:23],a[216:219],v[200:203],v[20:23]
TR a[158:159],v233 offset:61952
CV v3,v6,v7
MM v[20:23],a[212:215],v[184:187],v[20:23]
EX v8,v8
MM v[20:23],a[208:211],v[168:171],v[20:23]
EX v9,v9
MM v[28:31],a[208:211],v[172:175],v[140:143]
TR a[160:161],v232 offset:50176
CV v8,v8,v9
MM v[28:31],a[212:215],v[188:191],v[28:31]
EX v10,v10
MM v[28:31],a[216:219],v[204:207],v[28:31]
TR a[162:163],v232 offset:54272
TR a[164:165],v232 offset:58368
MM v[28:31],a[220:223],v[220:223],v[28:31]
EX v11,v11
MM v[24:27],a[204:207],v[220:223],v[140:143]
TR a[166:167],v232 offset:62464
CV v9,v10,v11
MM v[24:27],a[200:203],v[204:207],v[24:27]
EX v12,v12
MM v[24:27],a[196:199],v[188:191],v[24:27]
EX v13,v13
MM v[24:27],a[192:195],v[172:175],v[24:27]
TR a[168:169],v233 offset:50176
CV v10,v12,v13
MM v[32:35],a[224:227],v[160:163],v[128:131]
EX v14,v14
MM v[32:35],a[228:231],v[176:179],v[32:35]
TR a[170:171],v233 offset:54272
TR a[172:173],v233 offset:58368
MM v[32:35],a[232:235],v[192:195],v[32:35]
EX v15,v15
MM v[32:35],a[236:239],v[208:211],v[32:35]
TR a[174:175],v233 offset:62464
CV v11,v14,v15
MM v[36:39],a[252:255],v[208:211],v[128:131]
EX v16,v16
MM v[36:39],a[248:251],v[192:195],v[36:39]
EX v17,v17
MM v[36:39],a[244:247],v[176:179],v[36:39]
TR a[176:177],v232 offset:50688
CV v16,v16,v17
MM v[36:39],a[240:243],v[160:163],v[36:39]
EX v18,v18
MM v[44:47],a[240:243],v[164:167],v[132:135]
TR a[178:179],v232 offset:54784
TR a[180:181],v232 offset:58880
MM v[44:47],a[244:247],v[180:183],v[44:47]
EX v19,v19
MM v[44:47],a[248:251],v[196:199],v[44:47]
TR a[182:183],v232 offset:62976
CV v17,v18,v19
MM v[44:47],a[252:255],v[212:215],v[44:47]
EX v20,v20
MM v[40:43],a[236:239],v[212:215],v[132:135]
TR a[184:185],v233 offset:50688
TR a[186:187],v233 offset:54784
MM v[40:43],a[232:235],v[196:199],v[40:43]
EX v21,v21
MM v[40:43],a[228:231],v[180:183],v[40:43]
TR a[188:189],v233 offset:58880
CV v18,v20,v21
MM v[40:43],a[224:227],v[164:167],v[40:43]
EX v22,v22
MM v[48:51],a[224:227],v[168:171],v[136:139]
)ASM"
R"ASM(
EX v23,v23
MM v[48:51],a[228:231],v[184:187],v[48:51]
TR a[190:191],v233 offset:62976
CV v19,v22,v23
MM v[48:51],a[232:235],v[200:203],v[48:51]
EX v24,v24
MM v[48:51],a[236:239],v[216:219],v[48:51]
EX v25,v25
MM v[52:55],a[252:255],v[216:219],v[136:139]
EX v26,v26
MM v[52:55],a[248:251],v[200:203],v[52:55]
EX v27,v27
MM v[52:55],a[244:247],v[184:187],v[52:55]
EX v28,v28
MM v[52:55],a[240:243],v[168:171],v[52:55]
EX v29,v29
MM v[60:63],a[240:243],v[172:175],v[140:143]
EX v30,v30
MM v[60:63],a[244:247],v[188:191],v[60:63]
EX v31,v31
MM v[60:63],a[248:251],v[204:207],v[60:63]
EX v32,v32
MM v[60:63],a[252:255],v[220:223],v[60:63]
EX v33,v33
MM v[56:59],a[236:239],v[220:223],v[140:143]
EX v34,v34
MM v[56:59],a[232:235],v[204:207],v[56:59]
EX v35,v35
MM v[56:59],a[228:231],v[188:191],v[56:59]
EX v36,v36
MM v[56:59],a[224:227],v[172:175],v[56:59]
s_waitcnt vmcnt(12) lgkmcnt(0)
s_barrier
MM a[0:3],a[128:131],v[64:67],a[0:3]
s_add_u32 m0, s65, 49152
CV v24,v24,v25
CV v25,v26,v27
MM a[0:3],a[132:135],v[68:71],a[0:3]
RD a[192:195],v224 offset:16384
CV v26,v28,v29
MM a[8:11],a[132:135],v[76:79],a[8:11]
RD a[196:199],v225 offset:16384
CV v27,v30,v31
MM a[8:11],a[128:131],v[72:75],a[8:11]
buffer_load_dwordx4 v228, s[8:11], s64 offen offset:0 lds
MM a[16:19],a[128:131],v[80:83],a[16:19]
RD a[200:203],v226 offset:16384
CV v4,v32,v33
MM a[16:19],a[132:135],v[84:87],a[16:19]
RD a[204:207],v227 offset:16384
CV v5,v34,v35
MM a[24:27],a[132:135],v[92:95],a[24:27]
buffer_load_dwordx4 v229, s[8:11], s64 offen offset:1024 lds
MM a[24:27],a[128:131],v[88:91],a[24:27]
RD a[208:211],v224 offset:20480
RD a[212:215],v225 offset:20480
MM a[28:31],a[136:139],v[88:91],a[28:31]
buffer_load_dwordx4 v230, s[8:11], s64 offen offset:2048 lds
MM a[28:31],a[140:143],v[92:95],a[28:31]
RD a[216:219],v226 offset:20480
RD a[220:223],v227 offset:20480
MM a[20:23],a[140:143],v[84:87],a[20:23]
EX v37,v37
MM a[20:23],a[136:139],v[80:83],a[20:23]
RD a[224:227],v224 offset:24576
CV v6,v36,v37
MM a[12:15],a[136:139],v[72:75],a[12:15]
EX v38,v38
MM a[12:15],a[140:143],v[76:79],a[12:15]
buffer_load_dwordx4 v231, s[8:11], s64 offen offset:3072 lds
MM a[4:7],a[140:143],v[68:71],a[4:7]
s_add_u32 m0, s65, 114688
EX v39,v39
MM a[4:7],a[136:139],v[64:67],a[4:7]
RD a[228:231],v225 offset:24576
CV v7,v38,v39
MM a[32:35],a[144:147],v[64:67],a[32:35]
EX v40,v40
MM a[32:35],a[148:151],v[68:71],a[32:35]
EX v41,v41
MM a[40:43],a[148:151],v[76:79],a[40:43]
RD a[232:235],v226 offset:24576
CV v12,v40,v41
MM a[40:43],a[144:147],v[72:75],a[40:43]
EX v42,v42
MM a[48:51],a[144:147],v[80:83],a[48:51]
buffer_load_dwordx4 v235, s[12:15], s81 offen offset:0 lds
MM a[48:51],a[148:151],v[84:87],a[48:51]
EX v43,v43
MM a[56:59],a[148:151],v[92:95],a[56:59]
RD a[236:239],v227 offset:24576
CV v13,v42,v43
MM a[56:59],a[144:147],v[88:91],a[56:59]
EX v44,v44
MM a[60:63],a[152:155],v[88:91],a[60:63]
EX v45,v45
MM a[60:63],a[156:159],v[92:95],a[60:63]
RD a[240:243],v224 offset:28672
CV v14,v44,v45
MM a[52:55],a[156:159],v[84:87],a[52:55]
EX v46,v46
MM a[52:55],a[152:155],v[80:83],a[52:55]
buffer_load_dwordx4 v235, s[12:15], s64 offen offset:1024 lds
MM a[44:47],a[152:155],v[72:75],a[44:47]
EX v47,v47
MM a[44:47],a[156:159],v[76:79],a[44:47]
RD a[244:247],v225 offset:28672
CV v15,v46,v47
MM a[36:39],a[156:159],v[68:71],a[36:39]
EX v48,v48
MM a[36:39],a[152:155],v[64:67],a[36:39]
EX v49,v49
MM a[64:67],a[160:163],v[64:67],a[64:67]
RD a[248:251],v226 offset:28672
CV v20,v48,v49
MM a[64:67],a[164:167],v[68:71],a[64:67]
EX v50,v50
MM a[72:75],a[164:167],v[76:79],a[72:75]
buffer_load_dwordx4 v235, s[12:15], s81 offen offset:2048 lds
MM a[72:75],a[160:163],v[72:75],a[72:75]
EX v51,v51
MM a[80:83],a[160:163],v[80:83],a[80:83]
RD a[252:255],v227 offset:28672
CV v21,v50,v51
MM a[80:83],a[164:167],v[84:87],a[80:83]
EX v52,v52
MM a[88:91],a[164:167],v[92:95],a[88:91]
EX v53,v53
MM a[88:91],a[160:163],v[88:91],a[88:91]
buffer_load_dwordx4 v235, s[12:15], s64 offen offset:3072 lds
MM a[92:95],a[168:171],v[88:91],a[92:95]
EX v54,v54
MM a[92:95],a[172:175],v[92:95],a[92:95]
EX v55,v55
MM a[84:87],a[172:175],v[84:87],a[84:87]
EX v56,v56
MM a[84:87],a[168:171],v[80:83],a[84:87]
EX v57,v57
MM a[76:79],a[168:171],v[72:75],a[76:79]
EX v58,v58
MM a[76:79],a[172:175],v[76:79],a[76:79]
EX v59,v59
MM a[68:71],a[172:175],v[68:71],a[68:71]
EX v60,v60
MM a[68:71],a[168:171],v[64:67],a[68:71]
EX v61,v61
MM a[96:99],a[176:179],v[64:67],a[96:99]
EX v62,v62
MM a[96:99],a[180:183],v[68:71],a[96:99]
EX v63,v63
MM a[104:107],a[180:183],v[76:79],a[104:107]
CV v22,v52,v53
CV v23,v54,v55
MM a[104:107],a[176:179],v[72:75],a[104:107]
CV v28,v56,v57
CV v29,v58,v59
MM a[112:115],a[176:179],v[80:83],a[112:115]
CV v30,v60,v61
CV v31,v62,v63
MM a[112:115],a[180:183],v[84:87],a[112:115]
MM a[120:123],a[180:183],v[92:95],a[120:123]
MM a[120:123],a[176:179],v[88:91],a[120:123]
MM a[124:127],a[184:187],v[88:91],a[124:127]
v_add_f32 v244, v144, v145
v_add_f32 v245, v148, v149
MM a[124:127],a[188:191],v[92:95],a[124:127]
v_add_f32 v246, v152, v153
v_add_f32 v247, v156, v157
MM a[116:119],a[188:191],v[84:87],a[116:119]
s_cmp_eq_u32 s70, 2
s_cbranch_scc1 LIP_%=
LLALIP_%=:
MM a[116:119],a[184:187],v[80:83],a[116:119]
MM a[108:111],a[184:187],v[72:75],a[108:111]
MM a[108:111],a[188:191],v[76:79],a[108:111]
MM a[100:103],a[188:191],v[68:71],a[100:103]
MM a[100:103],a[184:187],v[64:67],a[100:103]
v_smfmac_f32_16x16x64_bf16 v[144:147], v[236:239], v[0:7], v234
v_smfmac_f32_16x16x64_bf16 v[148:151], v[236:239], v[8:15], v234
v_smfmac_f32_16x16x64_bf16 v[152:155], v[236:239], v[16:23], v234
v_smfmac_f32_16x16x64_bf16 v[156:159], v[236:239], v[24:31], v234
s_waitcnt lgkmcnt(0)
MM v[64:67],a[192:195],v[160:163],v[128:131]
s_add_u32 s64, s64, 16384
TR a[128:129],v232 offset:0
s_and_b32 s64, s64, 524287
MM v[64:67],a[196:199],v[176:179],v[64:67]
s_add_u32 s81, s64, 896
TR a[130:131],v232 offset:4096
v_max3_f32 v249, v144, v145, v148
MM v[64:67],a[200:203],v[192:195],v[64:67]
TR a[132:133],v232 offset:8192
v_max3_f32 v249, v249, v149, v152
MM v[64:67],a[204:207],v[208:211],v[64:67]
TR a[134:135],v232 offset:12288
v_max3_f32 v249, v249, v153, v156
MM v[68:71],a[220:223],v[208:211],v[128:131]
TR a[136:137],v233 offset:0
v_max_f32 v249, v249, v157
MM v[68:71],a[216:219],v[192:195],v[68:71]
TR a[138:139],v233 offset:4096
v_cmp_lt_f32 vcc, s71, v249
MM v[68:71],a[212:215],v[176:179],v[68:71]
TR a[140:141],v233 offset:8192
TR a[142:143],v233 offset:12288
MM v[68:71],a[208:211],v[160:163],v[68:71]
s_cbranch_vccnz LSITE8_%=
LBACK8_%=:
MM v[76:79],a[208:211],v[164:167],v[132:135]
EX v64,v64
MM v[76:79],a[212:215],v[180:183],v[76:79]
EX v65,v65
MM v[76:79],a[216:219],v[196:199],v[76:79]
TR a[144:145],v232 offset:512
CV v64,v64,v65
MM v[76:79],a[220:223],v[212:215],v[76:79]
EX v66,v66
MM v[72:75],a[204:207],v[212:215],v[132:135]
EX v67,v67
MM v[72:75],a[200:203],v[196:199],v[72:75]
TR a[146:147],v232 offset:4608
CV v65,v66,v67
MM v[72:75],a[196:199],v[180:183],v[72:75]
EX v68,v68
MM v[72:75],a[192:195],v[164:167],v[72:75]
TR a[148:149],v232 offset:8704
TR a[150:151],v232 offset:12800
MM v[80:83],a[192:195],v[168:171],v[136:139]
EX v69,v69
MM v[80:83],a[196:199],v[184:187],v[80:83]
TR a[152:153],v233 offset:512
CV v66,v68,v69
MM v[80:83],a[200:203],v[200:203],v[80:83]
EX v70,v70
MM v[80:83],a[204:207],v[216:219],v[80:83]
TR a[154:155],v233 offset:4608
TR a[156:157],v233 offset:8704
MM v[84:87],a[220:223],v[216:219],v[136:139]
EX v71,v71
MM v[84:87],a[216:219],v[200:203],v[84:87]
TR a[158:159],v233 offset:12800
CV v67,v70,v71
MM v[84:87],a[212:215],v[184:187],v[84:87]
EX v72,v72
MM v[84:87],a[208:211],v[168:171],v[84:87]
EX v73,v73
MM v[92:95],a[208:211],v[172:175],v[140:143]
TR a[160:161],v232 offset:1024
CV v72,v72,v73
MM v[92:95],a[212:215],v[188:191],v[92:95]
EX v74,v74
MM v[92:95],a[216:219],v[204:207],v[92:95]
TR a[162:163],v232 offset:5120
TR a[164:165],v232 offset:9216
MM v[92:95],a[220:223],v[220:223],v[92:95]
EX v75,v75
MM v[88:91],a[204:207],v[220:223],v[140:143]
TR a[166:167],v232 offset:13312
CV v73,v74,v75
MM v[88:91],a[200:203],v[204:207],v[88:91]
EX v76,v76
MM v[88:91],a[196:199],v[188:191],v[88:91]
EX v77,v77
MM v[88:91],a[192:195],v[172:175],v[88:91]
TR a[168:169],v233 offset:1024
CV v74,v76,v77
MM v[96:99],a[224:227],v[160:163],v[128:131]
EX v78,v78
MM v[96:99],a[228:231],v[176:179],v[96:99]
TR a[170:171],v233 offset:5120
TR a[172:173],v233 offset:9216
MM v[96:99],a[232:235],v[192:195],v[96:99]
EX v79,v79
MM v[96:99],a[236:239],v[208:211],v[96:99]
TR a[174:175],v233 offset:13312
CV v75,v78,v79
MM v[100:103],a[252:255],v[208:211],v[128:131]
EX v80,v80
MM v[100:103],a[248:251],v[192:195],v[100:103]
EX v81,v81
MM v[100:103],a[244:247],v[176:179],v[100:103]
TR a[176:177],v232 offset:1536
CV v80,v80,v81
MM v[100:103],a[240:243],v[160:163],v[100:103]
EX v82,v82
MM v[108:111],a[240:243],v[164:167],v[132:135]
TR a[178:179],v232 offset:5632
TR a[180:181],v232 offset:9728
MM v[108:111],a[244:247],v[180:183],v[108:111]
EX v83,v83
MM v[108:111],a[248:251],v[196:199],v[108:111]
TR a[182:183],v232 offset:13824
CV v81,v82,v83
MM v[108:111],a[252:255],v[212:215],v[108:111]
EX v84,v84
MM v[104:107],a[236:239],v[212:215],v[132:135]
TR a[184:185],v233 offset:1536
TR a[186:187],v233 offset:5632
MM v[104:107],a[232:235],v[196:199],v[104:107]
EX v85,v85
MM v[104:107],a[228:231],v[180:183],v[104:107]
TR a[188:189],v233 offset:9728
CV v82,v84,v85
MM v[104:107],a[224:227],v[164:167],v[104:107]
EX v86,v86
MM v[112:115],a[224:227],v[168:171],v[136:139]
EX v87,v87
MM v[112:115],a[228:231],v[184:187],v[112:115]
TR a[190:191],v233 offset:13824
CV v83,v86,v87
MM v[112:115],a[232:235],v[200:203],v[112:115]
EX v88,v88
MM v[112:115],a[236:239],v[216:219],v[112:115]
EX v89,v89
MM v[116:119],a[252:255],v[216:219],v[136:139]
EX v90,v90
MM v[116:119],a[248:251],v[200:203],v[116:119]
EX v91,v91
MM v[116:119],a[244:247],v[184:187],v[116:119]
EX v92,v92
MM v[116:119],a[240:243],v[168:171],v[116:119]
EX v93,v93
MM v[124:127],a[240:243],v[172:175],v[140:143]
EX v94,v94
MM v[124:127],a[244:247],v[188:191],v[124:127]
EX v95,v95
MM v[124:127],a[248:251],v[204:207],v[124:127]
EX v96,v96
MM v[124:127],a[252:255],v[220:223],v[124:127]
EX v97,v97
MM v[120:123],a[236:239],v[220:223],v[140:143]
EX v98,v98
MM v[120:123],a[232:235],v[204:207],v[120:123]
EX v99,v99
MM v[120:123],a[228:231],v[188:191],v[120:123]
s_cmp_eq_u32 s70, 1
s_cbranch_scc1 LTP_%=
LLALTP_%=:
MM v[120:123],a[224:227],v[172:175],v[120:123]
s_waitcnt vmcnt(12) lgkmcnt(0)
s_barrier
MM a[0:3],a[128:131],v[0:3],a[0:3]
s_add_u32 m0, s65, 0
CV v88,v88,v89
CV v89,v90,v91
MM a[0:3],a[132:135],v[4:7],a[0:3]
RD a[192:195],v224 offset:32768
CV v90,v92,v93
MM a[8:11],a[132:135],v[12:15],a[8:11]
RD a[196:199],v225 offset:32768
CV v91,v94,v95
MM a[8:11],a[128:131],v[8:11],a[8:11]
buffer_load_dwordx4 v228, s[8:11], s64 offen offset:0 lds
MM a[16:19],a[128:131],v[16:19],a[16:19]
RD a[200:203],v226 offset:32768
CV v68,v96,v97
MM a[16:19],a[132:135],v[20:23],a[16:19]
RD a[204:207],v227 offset:32768
CV v69,v98,v99
MM a[24:27],a[132:135],v[28:31],a[24:27]
buffer_load_dwordx4 v229, s[8:11], s64 offen offset:1024 lds
MM a[24:27],a[128:131],v[24:27],a[24:27]
RD a[208:211],v224 offset:36864
RD a[212:215],v225 offset:36864
MM a[28:31],a[136:139],v[24:27],a[28:31]
buffer_load_dwordx4 v230, s[8:11], s64 offen offset:2048 lds
MM a[28:31],a[140:143],v[28:31],a[28:31]
EX v100,v100
MM a[20:23],a[140:143],v[20:23],a[20:23]
EX v101,v101
MM a[20:23],a[136:139],v[16:19],a[20:23]
RD a[216:219],v226 offset:36864
CV v70,v100,v101
MM a[12:15],a[136:139],v[8:11],a[12:15]
EX v102,v102
MM a[12:15],a[140:143],v[12:15],a[12:15]
RD a[220:223],v227 offset:36864
RD a[224:227],v224 offset:40960
MM a[4:7],a[140:143],v[4:7],a[4:7]
EX v103,v103
MM a[4:7],a[136:139],v[0:3],a[4:7]
RD a[228:231],v225 offset:40960
CV v71,v102,v103
MM a[32:35],a[144:147],v[0:3],a[32:35]
EX v104,v104
MM a[32:35],a[148:151],v[4:7],a[32:35]
buffer_load_dwordx4 v231, s[8:11], s64 offen offset:3072 lds
MM a[40:43],a[148:151],v[12:15],a[40:43]
s_add_u32 m0, s65, 65536
EX v105,v105
)ASM"
R"ASM(
MM a[40:43],a[144:147],v[8:11],a[40:43]
RD a[232:235],v226 offset:40960
CV v76,v104,v105
MM a[48:51],a[144:147],v[16:19],a[48:51]
EX v106,v106
MM a[48:51],a[148:151],v[20:23],a[48:51]
EX v107,v107
MM a[56:59],a[148:151],v[28:31],a[56:59]
RD a[236:239],v227 offset:40960
CV v77,v106,v107
MM a[56:59],a[144:147],v[24:27],a[56:59]
EX v108,v108
MM a[60:63],a[152:155],v[24:27],a[60:63]
buffer_load_dwordx4 v235, s[12:15], s81 offen offset:0 lds
MM a[60:63],a[156:159],v[28:31],a[60:63]
EX v109,v109
s_nop 0
LPG9_%=:
s_cbranch_execz LTR0_%=
MM a[52:55],a[156:159],v[20:23],a[52:55]
RD a[240:243],v224 offset:45056
CV v78,v108,v109
MM a[52:55],a[152:155],v[16:19],a[52:55]
EX v110,v110
MM a[44:47],a[152:155],v[8:11],a[44:47]
EX v111,v111
MM a[44:47],a[156:159],v[12:15],a[44:47]
RD a[244:247],v225 offset:45056
CV v79,v110,v111
MM a[36:39],a[156:159],v[4:7],a[36:39]
EX v112,v112
MM a[36:39],a[152:155],v[0:3],a[36:39]
buffer_load_dwordx4 v235, s[12:15], s64 offen offset:1024 lds
MM a[64:67],a[160:163],v[0:3],a[64:67]
EX v113,v113
MM a[64:67],a[164:167],v[4:7],a[64:67]
RD a[248:251],v226 offset:45056
CV v84,v112,v113
MM a[72:75],a[164:167],v[12:15],a[72:75]
EX v114,v114
MM a[72:75],a[160:163],v[8:11],a[72:75]
EX v115,v115
MM a[80:83],a[160:163],v[16:19],a[80:83]
RD a[252:255],v227 offset:45056
CV v85,v114,v115
MM a[80:83],a[164:167],v[20:23],a[80:83]
buffer_load_dwordx4 v235, s[12:15], s81 offen offset:2048 lds
MM a[88:91],a[164:167],v[28:31],a[88:91]
EX v116,v116
MM a[88:91],a[160:163],v[24:27],a[88:91]
EX v117,v117
MM a[92:95],a[168:171],v[24:27],a[92:95]
EX v118,v118
MM a[92:95],a[172:175],v[28:31],a[92:95]
buffer_load_dwordx4 v235, s[12:15], s64 offen offset:3072 lds
MM a[84:87],a[172:175],v[20:23],a[84:87]
EX v119,v119
MM a[84:87],a[168:171],v[16:19],a[84:87]
EX v120,v120
MM a[76:79],a[168:171],v[8:11],a[76:79]
EX v121,v121
MM a[76:79],a[172:175],v[12:15],a[76:79]
EX v122,v122
MM a[68:71],a[172:175],v[4:7],a[68:71]
EX v123,v123
MM a[68:71],a[168:171],v[0:3],a[68:71]
EX v124,v124
MM a[96:99],a[176:179],v[0:3],a[96:99]
EX v125,v125
MM a[96:99],a[180:183],v[4:7],a[96:99]
EX v126,v126
MM a[104:107],a[180:183],v[12:15],a[104:107]
EX v127,v127
MM a[104:107],a[176:179],v[8:11],a[104:107]
CV v86,v116,v117
CV v87,v118,v119
MM a[112:115],a[176:179],v[16:19],a[112:115]
CV v92,v120,v121
CV v93,v122,v123
MM a[112:115],a[180:183],v[20:23],a[112:115]
CV v94,v124,v125
CV v95,v126,v127
MM a[120:123],a[180:183],v[28:31],a[120:123]
MM a[120:123],a[176:179],v[24:27],a[120:123]
MM a[124:127],a[184:187],v[24:27],a[124:127]
v_add_f32 v244, v144, v145
v_add_f32 v245, v148, v149
MM a[124:127],a[188:191],v[28:31],a[124:127]
v_add_f32 v246, v152, v153
v_add_f32 v247, v156, v157
MM a[116:119],a[188:191],v[20:23],a[116:119]
s_cmp_eq_u32 s70, s82
s_cbranch_scc1 LWV_%=
LLALWV_%=:
MM a[116:119],a[184:187],v[16:19],a[116:119]
MM a[108:111],a[184:187],v[8:11],a[108:111]
MM a[108:111],a[188:191],v[12:15],a[108:111]
MM a[100:103],a[188:191],v[4:7],a[100:103]
MM a[100:103],a[184:187],v[0:3],a[100:103]
v_smfmac_f32_16x16x64_bf16 v[144:147], v[236:239], v[64:71], v234
v_smfmac_f32_16x16x64_bf16 v[148:151], v[236:239], v[72:79], v234
v_smfmac_f32_16x16x64_bf16 v[152:155], v[236:239], v[80:87], v234
v_smfmac_f32_16x16x64_bf16 v[156:159], v[236:239], v[88:95], v234
s_waitcnt lgkmcnt(0)
s_sub_u32 s70, s70, 1
s_cmp_lg_u32 s70, 0
s_cbranch_scc1 LLAP_%=
s_branch LLAJ_%=
LWW_%=:
v_lshlrev_b32 v250, 3, v248
v_min_u32 v250, 4992, v250
s_sub_u32 s91, 5242880, s26
s_sub_u32 s60, s24, s91
s_subb_u32 s61, s25, 0
s_mov_b32 s62, 5242880
s_mov_b32 s63, 131072
s_lshl_b32 s90, s16, 2
s_add_u32 s90, s90, %[swave]
s_mul_i32 s90, s90, 10240
s_add_u32 s90, s90, 0
s_add_u32 s91, %[slds], 135168
s_mov_b32 m0, s91
s_nop 0
buffer_load_dword v250, s[60:63], s90 offen offset:0 lds
s_branch LLALWW_%=
LIP_%=:
s_lshr_b32 s77, %[sb], 3
s_lshl_b32 s77, s77, 2
s_add_u32 s77, s77, %[swave]
s_lshl_b32 s77, s77, 9
s_add_u32 s77, s77, LLAJ_%=-LPDP_%=
v_lshrrev_b32 v253, 1, v248
v_add_u32 v253, s77, v253
s_mov_b32 s77, LKEND_ka-LPDP_%=-16
v_min_u32 v253, s77, v253
s_getpc_b64 s[98:99]
LPDP_%=:
global_load_dword v253, v253, s[98:99]
s_branch LLALIP_%=
LTP_%=:
s_getpc_b64 s[98:99]
LTA1_%=:
s_min_u32 s73, %[swave], 3
s_lshl_b32 s73, s73, 12
s_add_u32 s73, s73, LPG23_%=-LTA1_%=
s_add_u32 s98, s98, s73
s_addc_u32 s99, s99, 0
s_mov_b64 exec, 0
s_setpc_b64 s[98:99]
LTR1_%=:
s_mov_b64 exec, -1
s_branch LLALTP_%=
LWV_%=:
v_lshlrev_b32 v250, 3, v248
v_min_u32 v250, 4992, v250
s_sub_u32 s91, 5242880, s26
s_sub_u32 s60, s24, s91
s_subb_u32 s61, s25, 0
s_mov_b32 s62, 5242880
s_mov_b32 s63, 131072
s_lshl_b32 s90, s16, 2
s_add_u32 s90, s90, %[swave]
s_mul_i32 s90, s90, 10240
s_add_u32 s90, s90, 5120
s_add_u32 s91, %[slds], 135168
s_mov_b32 m0, s91
s_nop 0
buffer_load_dword v250, s[60:63], s90 offen offset:0 lds
s_branch LLALWV_%=
s_nop 0
s_nop 0
s_nop 0
s_nop 0
s_nop 0
s_nop 0
s_nop 0
s_nop 0
s_nop 0
s_nop 0
s_nop 0
s_nop 0
s_nop 0
s_nop 0
s_nop 0
s_nop 0
s_nop 0
s_nop 0
s_nop 0
s_nop 0
s_nop 0
s_nop 0
s_nop 0
s_nop 0
s_nop 0
s_nop 0
s_nop 0
s_nop 0
s_nop 0
s_nop 0
s_nop 0
s_nop 0
s_nop 0
s_nop 0
s_nop 0
s_nop 0
s_nop 0
s_nop 0
s_nop 0
s_nop 0
s_nop 0
s_nop 0
s_nop 0
s_nop 0
s_nop 0
s_nop 0
s_nop 0
s_nop 0
s_nop 0
s_nop 0
s_nop 0
s_nop 0
s_nop 0
s_nop 0
s_nop 0
s_nop 0
s_nop 0
s_nop 0
s_nop 0
s_nop 0
s_nop 0
s_nop 0
s_nop 0
s_nop 0
s_nop 0
s_nop 0
s_nop 0
s_nop 0
s_nop 0
s_nop 0
s_nop 0
s_nop 0
s_nop 0
s_nop 0
s_nop 0
s_nop 0
s_nop 0
s_nop 0
s_nop 0
s_nop 0
s_nop 0
s_nop 0
s_nop 0
s_nop 0
s_nop 0
s_nop 0
s_nop 0
s_nop 0
s_nop 0
s_nop 0
s_nop 0
s_nop 0
s_nop 0
s_nop 0
s_nop 0
s_nop 0
s_nop 0
s_nop 0
s_nop 0
s_nop 0
s_nop 0
s_nop 0
s_nop 0
s_nop 0
s_nop 0
s_nop 0
s_nop 0
s_nop 0
s_nop 0
s_nop 0
s_nop 0
s_nop 0
s_nop 0
s_nop 0
s_nop 0
s_nop 0
s_nop 0
s_nop 0
s_nop 0
s_nop 0
s_nop 0
s_nop 0
s_nop 0
s_nop 0
s_nop 0
s_nop 0
s_nop 0
s_nop 0
s_nop 0
s_nop 0
s_nop 0
s_nop 0
s_nop 0
s_nop 0
s_nop 0
s_nop 0
s_nop 0
s_nop 0
s_nop 0
s_nop 0
s_nop 0
s_nop 0
s_nop 0
s_nop 0
s_nop 0
s_nop 0
s_nop 0
s_nop 0
s_nop 0
s_nop 0
s_nop 0
s_nop 0
s_nop 0
s_nop 0
s_nop 0
s_nop 0
s_nop 0
s_nop 0
s_nop 0
s_nop 0
s_nop 0
s_nop 0
s_nop 0
s_nop 0
s_nop 0
s_nop 0
s_nop 0
s_nop 0
s_nop 0
s_nop 0
s_nop 0
s_nop 0
s_nop 0
s_nop 0
s_nop 0
s_nop 0
s_nop 0
s_nop 0
s_nop 0
s_nop 0
s_nop 0
s_nop 0
s_nop 0
s_nop 0
s_nop 0
s_nop 0
s_nop 0
s_nop 0
s_nop 0
s_nop 0
s_nop 0
s_nop 0
s_nop 0
s_nop 0
s_nop 0
s_nop 0
s_nop 0
s_nop 0
s_nop 0
s_nop 0
s_nop 0
s_nop 0
s_nop 0
s_nop 0
s_nop 0
s_nop 0
s_nop 0
s_nop 0
s_nop 0
s_nop 0
s_nop 0
s_nop 0
s_nop 0
s_nop 0
s_nop 0
s_nop 0
s_nop 0
s_nop 0
s_nop 0
s_nop 0
s_nop 0
s_nop 0
s_nop 0
s_nop 0
s_nop 0
s_nop 0
s_nop 0
s_nop 0
s_nop 0
s_nop 0
s_nop 0
s_nop 0
s_nop 0
s_nop 0
s_nop 0
s_nop 0
s_nop 0
s_nop 0
s_nop 0
s_nop 0
s_nop 0
s_nop 0
s_nop 0
s_nop 0
s_nop 0
s_nop 0
s_nop 0
s_nop 0
s_nop 0
s_nop 0
s_nop 0
s_nop 0
s_nop 0
s_nop 0
s_nop 0
s_nop 0
s_nop 0
s_nop 0
s_nop 0
s_nop 0
s_nop 0
s_nop 0
s_nop 0
s_nop 0
s_nop 0
s_nop 0
s_nop 0
s_nop 0
s_nop 0
s_nop 0
s_nop 0
s_nop 0
s_nop 0
s_nop 0
s_nop 0
s_nop 0
s_nop 0
s_nop 0
s_nop 0
s_nop 0
s_nop 0
s_nop 0
s_nop 0
s_nop 0
s_nop 0
s_nop 0
s_nop 0
s_nop 0
s_nop 0
s_nop 0
s_nop 0
s_nop 0
s_nop 0
s_nop 0
s_nop 0
s_nop 0
s_nop 0
s_nop 0
s_nop 0
s_nop 0
s_nop 0
s_nop 0
s_nop 0
s_nop 0
s_nop 0
s_nop 0
s_nop 0
s_nop 0
s_nop 0
s_nop 0
s_nop 0
s_nop 0
s_nop 0
s_nop 0
s_nop 0
s_nop 0
s_nop 0
s_nop 0
s_nop 0
s_nop 0
s_nop 0
s_nop 0
s_nop 0
s_nop 0
s_nop 0
s_nop 0
s_nop 0
s_nop 0
s_nop 0
s_nop 0
s_nop 0
s_nop 0
s_nop 0
s_nop 0
s_nop 0
s_nop 0
s_nop 0
s_nop 0
s_nop 0
s_nop 0
s_nop 0
s_nop 0
s_nop 0
s_nop 0
s_nop 0
s_nop 0
s_nop 0
s_nop 0
s_nop 0
s_nop 0
s_nop 0
s_nop 0
s_nop 0
s_nop 0
s_nop 0
s_nop 0
s_nop 0
s_nop 0
s_nop 0
s_nop 0
s_nop 0
s_nop 0
s_nop 0
s_nop 0
s_nop 0
s_nop 0
s_nop 0
s_nop 0
s_nop 0
s_nop 0
s_nop 0
s_nop 0
s_nop 0
s_nop 0
s_nop 0
s_nop 0
LLAR_%=:
MM v[0:3],a[192:195],v[160:163],v[128:131]
s_add_u32 s64, s64, 16384
TR a[128:129],v232 offset:16384
s_add_u32 s81, s64, 896
MM v[0:3],a[196:199],v[176:179],v[0:3]
TR a[130:131],v232 offset:20480
v_max3_f32 v249, v144, v145, v148
MM v[0:3],a[200:203],v[192:195],v[0:3]
TR a[132:133],v232 offset:24576
v_max3_f32 v249, v249, v149, v152
MM v[0:3],a[204:207],v[208:211],v[0:3]
TR a[134:135],v232 offset:28672
v_max3_f32 v249, v249, v153, v156
MM v[4:7],a[220:223],v[208:211],v[128:131]
TR a[136:137],v233 offset:16384
v_max_f32 v249, v249, v157
MM v[4:7],a[216:219],v[192:195],v[4:7]
TR a[138:139],v233 offset:20480
v_cmp_lt_f32 vcc, s71, v249
MM v[4:7],a[212:215],v[176:179],v[4:7]
TR a[140:141],v233 offset:24576
TR a[142:143],v233 offset:28672
MM v[4:7],a[208:211],v[160:163],v[4:7]
s_cbranch_vccnz LSITE9_%=
LBACK9_%=:
MM v[12:15],a[208:211],v[164:167],v[132:135]
s_lshl_b32 s17, s16, 2
EX v0,v0
MM v[12:15],a[212:215],v[180:183],v[12:15]
s_add_u32 s17, s17, %[swave]
EX v1,v1
MM v[12:15],a[216:219],v[196:199],v[12:15]
s_sub_u32 s90, s16, 108
TR a[144:145],v232 offset:16896
CV v0,v0,v1
MM v[12:15],a[220:223],v[212:215],v[12:15]
s_min_u32 s90, s90, s16
EX v2,v2
MM v[8:11],a[204:207],v[212:215],v[132:135]
s_lshl_b32 s48, s90, 5
EX v3,v3
MM v[8:11],a[200:203],v[196:199],v[8:11]
s_add_u32 s76, %[slds], 155648
v_add_u32 v250, s76, v248
CV v1,v2,v3
MM v[8:11],a[196:199],v[180:183],v[8:11]
s_lshl_b32 s77, s17, 4
TR a[146:147],v232 offset:20992
TR a[148:149],v232 offset:25088
MM v[8:11],a[192:195],v[164:167],v[8:11]
s_add_u32 s76, s76, s77
TR a[150:151],v232 offset:29184
v_mov_b32 v251, s76
MM v[16:19],a[192:195],v[168:171],v[136:139]
RD v[124:127],v251 offset:0
RD v[96:99],v250 offset:0
MM v[16:19],a[196:199],v[184:187],v[16:19]
RD v[100:103],v250 offset:1024
RD v[104:107],v250 offset:2048
MM v[16:19],a[200:203],v[200:203],v[16:19]
RD v[108:111],v250 offset:3072
RD v[112:115],v250 offset:4096
MM v[16:19],a[204:207],v[216:219],v[16:19]
RD v[116:119],v250 offset:5120
RD v[120:123],v250 offset:6144
MM v[20:23],a[220:223],v[216:219],v[136:139]
EX v4,v4
MM v[20:23],a[216:219],v[200:203],v[20:23]
EX v5,v5
MM v[20:23],a[212:215],v[184:187],v[20:23]
s_waitcnt lgkmcnt(7)
v_readfirstlane_b32 s52, v124
v_readfirstlane_b32 s53, v125
MM v[20:23],a[208:211],v[168:171],v[20:23]
v_readfirstlane_b32 s54, v126
v_readfirstlane_b32 s55, v127
EX v6,v6
MM v[28:31],a[208:211],v[172:175],v[140:143]
RD v[124:127],v250 offset:7168
TR a[152:153],v233 offset:16896
MM v[28:31],a[212:215],v[188:191],v[28:31]
s_waitcnt lgkmcnt(2)
v_max3_u32 v253, v96, v97, v98
v_max3_u32 v251, v99, v100, v101
EX v7,v7
MM v[28:31],a[216:219],v[204:207],v[28:31]
v_max3_u32 v253, v253, v102, v103
v_max3_u32 v251, v251, v104, v105
MM v[28:31],a[220:223],v[220:223],v[28:31]
v_max3_u32 v253, v253, v106, v107
v_max3_u32 v251, v251, v108, v109
EX v8,v8
MM v[24:27],a[204:207],v[220:223],v[140:143]
v_max3_u32 v253, v253, v110, v111
v_max3_u32 v251, v251, v112, v113
MM v[24:27],a[200:203],v[204:207],v[24:27]
v_max3_u32 v253, v253, v114, v115
v_max3_u32 v251, v251, v116, v117
EX v9,v9
MM v[24:27],a[196:199],v[188:191],v[24:27]
TR a[154:155],v233 offset:20992
v_max3_u32 v253, v253, v118, v119
v_max3_u32 v251, v251, v120, v121
MM v[24:27],a[192:195],v[172:175],v[24:27]
v_max3_u32 v253, v253, v122, v123
CV v2,v4,v5
EX v10,v10
MM v[32:35],a[224:227],v[160:163],v[128:131]
s_waitcnt lgkmcnt(2)
TR a[156:157],v233 offset:25088
v_max3_u32 v251, v251, v124, v125
v_max3_u32 v253, v253, v126, v127
EX v11,v11
MM v[32:35],a[228:231],v[176:179],v[32:35]
v_max_u32 v253, v253, v251
v_cmp_lt_u32 s[60:61], s52, v253
MM v[32:35],a[232:235],v[192:195],v[32:35]
TR a[158:159],v233 offset:29184
v_cmp_lt_u32 s[62:63], s53, v253
v_cmp_lt_u32 s[56:57], s54, v253
EX v12,v12
MM v[32:35],a[236:239],v[208:211],v[32:35]
v_cmp_lt_u32 s[58:59], s55, v253
CV v3,v6,v7
MM v[36:39],a[252:255],v[208:211],v[128:131]
TR a[160:161],v232 offset:17408
EX v13,v13
MM v[36:39],a[248:251],v[192:195],v[36:39]
s_bcnt1_i32_b64 s60, s[60:61]
TR a[162:163],v232 offset:21504
CV v8,v8,v9
MM v[36:39],a[244:247],v[176:179],v[36:39]
s_bcnt1_i32_b64 s62, s[62:63]
EX v14,v14
MM v[36:39],a[240:243],v[160:163],v[36:39]
s_bcnt1_i32_b64 s56, s[56:57]
)ASM"
R"ASM(
TR a[164:165],v232 offset:25600
TR a[166:167],v232 offset:29696
MM v[44:47],a[240:243],v[164:167],v[132:135]
s_bcnt1_i32_b64 s58, s[58:59]
EX v15,v15
MM v[44:47],a[244:247],v[180:183],v[44:47]
s_min_u32 s76, s60, s62
EX v16,v16
MM v[44:47],a[248:251],v[196:199],v[44:47]
s_min_u32 s77, s56, s58
TR a[168:169],v233 offset:17408
CV v9,v10,v11
MM v[44:47],a[252:255],v[212:215],v[44:47]
s_min_u32 s76, s76, s77
s_cmp_lt_u32 s76, 8
s_cbranch_scc1 LRK_%=
LRKb_%=:
EX v17,v17
MM v[40:43],a[236:239],v[212:215],v[132:135]
v_lshrrev_b32 v252, 6, v248
v_mul_u32_u24 v252, 3456, v252
MM v[40:43],a[232:235],v[196:199],v[40:43]
TR a[170:171],v233 offset:21504
v_and_b32 v251, 48, v248
v_add_u32 v252, v252, v251
EX v18,v18
MM v[40:43],a[228:231],v[180:183],v[40:43]
s_and_b32 s90, %[swave], 1
TR a[172:173],v233 offset:25600
CV v10,v12,v13
MM v[40:43],a[224:227],v[164:167],v[40:43]
s_mul_i32 s90, s90, 276480
EX v19,v19
MM v[48:51],a[224:227],v[168:171],v[136:139]
s_bfe_u32 s91, s48, 131077
TR a[174:175],v233 offset:29696
CV v11,v14,v15
MM v[48:51],a[228:231],v[184:187],v[48:51]
s_mul_i32 s91, s91, 552960
EX v20,v20
MM v[48:51],a[232:235],v[200:203],v[48:51]
s_add_u32 s90, s90, s91
TR a[176:177],v232 offset:17920
TR a[178:179],v232 offset:22016
MM v[48:51],a[236:239],v[216:219],v[48:51]
s_and_b32 s91, s48, 0xffffff80
EX v21,v21
MM v[52:55],a[252:255],v[216:219],v[136:139]
s_add_u32 s90, s90, s91
EX v22,v22
MM v[52:55],a[248:251],v[200:203],v[52:55]
s_and_b32 s91, s48, 16
TR a[180:181],v232 offset:26112
CV v16,v16,v17
MM v[52:55],a[244:247],v[184:187],v[52:55]
s_lshl_b32 s91, s91, 2
EX v23,v23
MM v[52:55],a[240:243],v[168:171],v[52:55]
s_add_u32 s66, s90, s91
TR a[182:183],v232 offset:30208
CV v17,v18,v19
MM v[60:63],a[240:243],v[172:175],v[140:143]
s_mul_i32 s91, %[swave], 5120
EX v24,v24
MM v[60:63],a[244:247],v[188:191],v[60:63]
s_add_u32 s91, s91, %[slds]
TR a[184:185],v233 offset:17920
TR a[186:187],v233 offset:22016
MM v[60:63],a[248:251],v[204:207],v[60:63]
s_add_u32 s67, s91, 131072
TR a[188:189],v233 offset:26112
TR a[190:191],v233 offset:30208
EX v25,v25
MM v[60:63],a[252:255],v[220:223],v[60:63]
EX v26,v26
MM v[56:59],a[236:239],v[220:223],v[140:143]
EX v27,v27
MM v[56:59],a[232:235],v[204:207],v[56:59]
EX v28,v28
MM v[56:59],a[228:231],v[188:191],v[56:59]
EX v29,v29
CV v18,v20,v21
MM v[56:59],a[224:227],v[172:175],v[56:59]
s_waitcnt lgkmcnt(0)
s_barrier
MM a[0:3],a[128:131],v[64:67],a[0:3]
s_add_u32 m0, s65, 16384
CV v19,v22,v23
CV v24,v24,v25
MM a[0:3],a[132:135],v[68:71],a[0:3]
CV v25,v26,v27
CV v26,v28,v29
MM a[8:11],a[132:135],v[76:79],a[8:11]
EX v30,v30
MM a[8:11],a[128:131],v[72:75],a[8:11]
EX v31,v31
MM a[16:19],a[128:131],v[80:83],a[16:19]
RD a[192:195],v224 offset:49152
CV v27,v30,v31
MM a[16:19],a[132:135],v[84:87],a[16:19]
EX v32,v32
MM a[24:27],a[132:135],v[92:95],a[24:27]
buffer_load_dwordx4 v228, s[8:11], s64 offen offset:0 lds
MM a[24:27],a[128:131],v[88:91],a[24:27]
EX v33,v33
MM a[28:31],a[136:139],v[88:91],a[28:31]
RD a[196:199],v225 offset:49152
CV v4,v32,v33
MM a[28:31],a[140:143],v[92:95],a[28:31]
EX v34,v34
MM a[20:23],a[140:143],v[84:87],a[20:23]
EX v35,v35
MM a[20:23],a[136:139],v[80:83],a[20:23]
RD a[200:203],v226 offset:49152
CV v5,v34,v35
MM a[12:15],a[136:139],v[72:75],a[12:15]
EX v36,v36
MM a[12:15],a[140:143],v[76:79],a[12:15]
LPG10_%=:
s_cbranch_execz LTR0_%=
buffer_load_dwordx4 v229, s[8:11], s64 offen offset:1024 lds
MM a[4:7],a[140:143],v[68:71],a[4:7]
EX v37,v37
MM a[4:7],a[136:139],v[64:67],a[4:7]
RD a[204:207],v227 offset:49152
CV v6,v36,v37
MM a[32:35],a[144:147],v[64:67],a[32:35]
EX v38,v38
MM a[32:35],a[148:151],v[68:71],a[32:35]
EX v39,v39
MM a[40:43],a[148:151],v[76:79],a[40:43]
RD a[208:211],v224 offset:53248
CV v7,v38,v39
MM a[40:43],a[144:147],v[72:75],a[40:43]
EX v40,v40
MM a[48:51],a[144:147],v[80:83],a[48:51]
buffer_load_dwordx4 v230, s[8:11], s64 offen offset:2048 lds
MM a[48:51],a[148:151],v[84:87],a[48:51]
EX v41,v41
MM a[56:59],a[148:151],v[92:95],a[56:59]
RD a[212:215],v225 offset:53248
CV v12,v40,v41
MM a[56:59],a[144:147],v[88:91],a[56:59]
EX v42,v42
MM a[60:63],a[152:155],v[88:91],a[60:63]
EX v43,v43
MM a[60:63],a[156:159],v[92:95],a[60:63]
RD a[216:219],v226 offset:53248
CV v13,v42,v43
MM a[52:55],a[156:159],v[84:87],a[52:55]
EX v44,v44
MM a[52:55],a[152:155],v[80:83],a[52:55]
buffer_load_dwordx4 v231, s[8:11], s64 offen offset:3072 lds
MM a[44:47],a[152:155],v[72:75],a[44:47]
s_add_u32 m0, s65, 81920
EX v45,v45
MM a[44:47],a[156:159],v[76:79],a[44:47]
RD a[220:223],v227 offset:53248
CV v14,v44,v45
MM a[36:39],a[156:159],v[68:71],a[36:39]
EX v46,v46
MM a[36:39],a[152:155],v[64:67],a[36:39]
RD a[224:227],v224 offset:57344
RD a[228:231],v225 offset:57344
MM a[64:67],a[160:163],v[64:67],a[64:67]
EX v47,v47
MM a[64:67],a[164:167],v[68:71],a[64:67]
RD a[232:235],v226 offset:57344
CV v15,v46,v47
MM a[72:75],a[164:167],v[76:79],a[72:75]
EX v48,v48
MM a[72:75],a[160:163],v[72:75],a[72:75]
buffer_load_dwordx4 v235, s[12:15], s81 offen offset:0 lds
MM a[80:83],a[160:163],v[80:83],a[80:83]
s_add_u32 m0, s67, 0
s_add_u32 s85, s66, 0
buffer_load_dwordx4 v252, s[36:39], s85 offen offset:0 lds
MM a[80:83],a[164:167],v[84:87],a[80:83]
EX v49,v49
MM a[88:91],a[164:167],v[92:95],a[88:91]
EX v50,v50
MM a[88:91],a[160:163],v[88:91],a[88:91]
RD a[236:239],v227 offset:57344
CV v20,v48,v49
MM a[92:95],a[168:171],v[88:91],a[92:95]
EX v51,v51
MM a[92:95],a[172:175],v[92:95],a[92:95]
RD a[240:243],v224 offset:61440
CV v21,v50,v51
MM a[84:87],a[172:175],v[84:87],a[84:87]
EX v52,v52
MM a[84:87],a[168:171],v[80:83],a[84:87]
EX v53,v53
MM a[76:79],a[168:171],v[72:75],a[76:79]
RD a[244:247],v225 offset:61440
CV v22,v52,v53
MM a[76:79],a[172:175],v[76:79],a[76:79]
EX v54,v54
MM a[68:71],a[172:175],v[68:71],a[68:71]
s_add_u32 m0, s65, 81920
s_nop 0
buffer_load_dwordx4 v235, s[12:15], s64 offen offset:1024 lds
MM a[68:71],a[168:171],v[64:67],a[68:71]
EX v55,v55
MM a[96:99],a[176:179],v[64:67],a[96:99]
RD a[248:251],v226 offset:61440
CV v23,v54,v55
MM a[96:99],a[180:183],v[68:71],a[96:99]
EX v56,v56
MM a[104:107],a[180:183],v[76:79],a[104:107]
EX v57,v57
MM a[104:107],a[176:179],v[72:75],a[104:107]
RD a[252:255],v227 offset:61440
CV v28,v56,v57
MM a[112:115],a[176:179],v[80:83],a[112:115]
buffer_load_dwordx4 v235, s[12:15], s81 offen offset:2048 lds
MM a[112:115],a[180:183],v[84:87],a[112:115]
EX v58,v58
MM a[120:123],a[180:183],v[92:95],a[120:123]
EX v59,v59
MM a[120:123],a[176:179],v[88:91],a[120:123]
EX v60,v60
MM a[124:127],a[184:187],v[88:91],a[124:127]
v_add_f32 v244, v144, v145
v_add_f32 v245, v148, v149
MM a[124:127],a[188:191],v[92:95],a[124:127]
v_add_f32 v246, v152, v153
v_add_f32 v247, v156, v157
MM a[116:119],a[188:191],v[84:87],a[116:119]
EX v61,v61
MM a[116:119],a[184:187],v[80:83],a[116:119]
buffer_load_dwordx4 v235, s[12:15], s64 offen offset:3072 lds
MM a[108:111],a[184:187],v[72:75],a[108:111]
EX v62,v62
MM a[108:111],a[188:191],v[76:79],a[108:111]
EX v63,v63
MM a[100:103],a[188:191],v[68:71],a[100:103]
CV v29,v58,v59
CV v30,v60,v61
MM a[100:103],a[184:187],v[64:67],a[100:103]
CV v31,v62,v63
v_smfmac_f32_16x16x64_bf16 v[144:147], v[236:239], v[0:7], v234
v_smfmac_f32_16x16x64_bf16 v[148:151], v[236:239], v[8:15], v234
v_smfmac_f32_16x16x64_bf16 v[152:155], v[236:239], v[16:23], v234
v_smfmac_f32_16x16x64_bf16 v[156:159], v[236:239], v[24:31], v234
s_waitcnt lgkmcnt(0)
MM v[64:67],a[192:195],v[160:163],v[128:131]
s_add_u32 s64, s64, 16384
TR a[128:129],v232 offset:32768
s_add_u32 s81, s64, 896
MM v[64:67],a[196:199],v[176:179],v[64:67]
TR a[130:131],v232 offset:36864
v_max3_f32 v249, v144, v145, v148
MM v[64:67],a[200:203],v[192:195],v[64:67]
TR a[132:133],v232 offset:40960
v_max3_f32 v249, v249, v149, v152
MM v[64:67],a[204:207],v[208:211],v[64:67]
TR a[134:135],v232 offset:45056
v_max3_f32 v249, v249, v153, v156
MM v[68:71],a[220:223],v[208:211],v[128:131]
TR a[136:137],v233 offset:32768
v_max_f32 v249, v249, v157
MM v[68:71],a[216:219],v[192:195],v[68:71]
TR a[138:139],v233 offset:36864
v_cmp_lt_f32 vcc, s71, v249
MM v[68:71],a[212:215],v[176:179],v[68:71]
TR a[140:141],v233 offset:40960
TR a[142:143],v233 offset:45056
MM v[68:71],a[208:211],v[160:163],v[68:71]
s_cbranch_vccnz LSITE10_%=
LBACK10_%=:
MM v[76:79],a[208:211],v[164:167],v[132:135]
s_add_u32 m0, s67, 1024
s_add_u32 s85, s66, 55296
buffer_load_dwordx4 v252, s[36:39], s85 offen offset:0 lds
MM v[76:79],a[212:215],v[180:183],v[76:79]
EX v64,v64
MM v[76:79],a[216:219],v[196:199],v[76:79]
EX v65,v65
MM v[76:79],a[220:223],v[212:215],v[76:79]
TR a[144:145],v232 offset:33280
CV v64,v64,v65
MM v[72:75],a[204:207],v[212:215],v[132:135]
EX v66,v66
MM v[72:75],a[200:203],v[196:199],v[72:75]
EX v67,v67
MM v[72:75],a[196:199],v[180:183],v[72:75]
TR a[146:147],v232 offset:37376
CV v65,v66,v67
MM v[72:75],a[192:195],v[164:167],v[72:75]
EX v68,v68
MM v[80:83],a[192:195],v[168:171],v[136:139]
TR a[148:149],v232 offset:41472
TR a[150:151],v232 offset:45568
MM v[80:83],a[196:199],v[184:187],v[80:83]
EX v69,v69
MM v[80:83],a[200:203],v[200:203],v[80:83]
TR a[152:153],v233 offset:33280
CV v66,v68,v69
MM v[80:83],a[204:207],v[216:219],v[80:83]
EX v70,v70
MM v[84:87],a[220:223],v[216:219],v[136:139]
EX v71,v71
MM v[84:87],a[216:219],v[200:203],v[84:87]
TR a[154:155],v233 offset:37376
CV v67,v70,v71
MM v[84:87],a[212:215],v[184:187],v[84:87]
EX v72,v72
MM v[84:87],a[208:211],v[168:171],v[84:87]
TR a[156:157],v233 offset:41472
TR a[158:159],v233 offset:45568
MM v[92:95],a[208:211],v[172:175],v[140:143]
EX v73,v73
MM v[92:95],a[212:215],v[188:191],v[92:95]
TR a[160:161],v232 offset:33792
CV v72,v72,v73
MM v[92:95],a[216:219],v[204:207],v[92:95]
EX v74,v74
MM v[92:95],a[220:223],v[220:223],v[92:95]
EX v75,v75
MM v[88:91],a[204:207],v[220:223],v[140:143]
TR a[162:163],v232 offset:37888
CV v73,v74,v75
MM v[88:91],a[200:203],v[204:207],v[88:91]
TR a[164:165],v232 offset:41984
TR a[166:167],v232 offset:46080
MM v[88:91],a[196:199],v[188:191],v[88:91]
EX v76,v76
MM v[88:91],a[192:195],v[172:175],v[88:91]
EX v77,v77
MM v[96:99],a[224:227],v[160:163],v[128:131]
TR a[168:169],v233 offset:33792
CV v74,v76,v77
MM v[96:99],a[228:231],v[176:179],v[96:99]
EX v78,v78
MM v[96:99],a[232:235],v[192:195],v[96:99]
TR a[170:171],v233 offset:37888
TR a[172:173],v233 offset:41984
MM v[96:99],a[236:239],v[208:211],v[96:99]
EX v79,v79
MM v[100:103],a[252:255],v[208:211],v[128:131]
TR a[174:175],v233 offset:46080
CV v75,v78,v79
MM v[100:103],a[248:251],v[192:195],v[100:103]
EX v80,v80
MM v[100:103],a[244:247],v[176:179],v[100:103]
EX v81,v81
MM v[100:103],a[240:243],v[160:163],v[100:103]
TR a[176:177],v232 offset:34304
CV v80,v80,v81
MM v[108:111],a[240:243],v[164:167],v[132:135]
s_add_u32 m0, s67, 2048
s_add_u32 s85, s66, 110592
buffer_load_dwordx4 v252, s[36:39], s85 offen offset:0 lds
MM v[108:111],a[244:247],v[180:183],v[108:111]
EX v82,v82
MM v[108:111],a[248:251],v[196:199],v[108:111]
TR a[178:179],v232 offset:38400
TR a[180:181],v232 offset:42496
MM v[108:111],a[252:255],v[212:215],v[108:111]
EX v83,v83
MM v[104:107],a[236:239],v[212:215],v[132:135]
TR a[182:183],v232 offset:46592
CV v81,v82,v83
MM v[104:107],a[232:235],v[196:199],v[104:107]
EX v84,v84
MM v[104:107],a[228:231],v[180:183],v[104:107]
EX v85,v85
MM v[104:107],a[224:227],v[164:167],v[104:107]
TR a[184:185],v233 offset:34304
CV v82,v84,v85
MM v[112:115],a[224:227],v[168:171],v[136:139]
EX v86,v86
MM v[112:115],a[228:231],v[184:187],v[112:115]
TR a[186:187],v233 offset:38400
TR a[188:189],v233 offset:42496
MM v[112:115],a[232:235],v[200:203],v[112:115]
EX v87,v87
MM v[112:115],a[236:239],v[216:219],v[112:115]
TR a[190:191],v233 offset:46592
CV v83,v86,v87
MM v[116:119],a[252:255],v[216:219],v[136:139]
EX v88,v88
MM v[116:119],a[248:251],v[200:203],v[116:119]
EX v89,v89
MM v[116:119],a[244:247],v[184:187],v[116:119]
EX v90,v90
MM v[116:119],a[240:243],v[168:171],v[116:119]
EX v91,v91
MM v[124:127],a[240:243],v[172:175],v[140:143]
EX v92,v92
MM v[124:127],a[244:247],v[188:191],v[124:127]
EX v93,v93
MM v[124:127],a[248:251],v[204:207],v[124:127]
EX v94,v94
MM v[124:127],a[252:255],v[220:223],v[124:127]
)ASM"
R"ASM(
EX v95,v95
MM v[120:123],a[236:239],v[220:223],v[140:143]
EX v96,v96
MM v[120:123],a[232:235],v[204:207],v[120:123]
EX v97,v97
MM v[120:123],a[228:231],v[188:191],v[120:123]
EX v98,v98
MM v[120:123],a[224:227],v[172:175],v[120:123]
s_waitcnt vmcnt(15) lgkmcnt(0)
s_barrier
MM a[0:3],a[128:131],v[0:3],a[0:3]
s_add_u32 m0, s65, 32768
CV v88,v88,v89
CV v89,v90,v91
MM a[0:3],a[132:135],v[4:7],a[0:3]
RD a[192:195],v224 offset:0
CV v90,v92,v93
MM a[8:11],a[132:135],v[12:15],a[8:11]
RD a[196:199],v225 offset:0
CV v91,v94,v95
MM a[8:11],a[128:131],v[8:11],a[8:11]
buffer_load_dwordx4 v228, s[8:11], s64 offen offset:0 lds
MM a[16:19],a[128:131],v[16:19],a[16:19]
RD a[200:203],v226 offset:0
CV v68,v96,v97
MM a[16:19],a[132:135],v[20:23],a[16:19]
buffer_load_dwordx4 v229, s[8:11], s64 offen offset:1024 lds
MM a[24:27],a[132:135],v[28:31],a[24:27]
RD a[204:207],v227 offset:0
RD a[208:211],v224 offset:4096
MM a[24:27],a[128:131],v[24:27],a[24:27]
EX v99,v99
MM a[28:31],a[136:139],v[24:27],a[28:31]
RD a[212:215],v225 offset:4096
CV v69,v98,v99
MM a[28:31],a[140:143],v[28:31],a[28:31]
EX v100,v100
MM a[20:23],a[140:143],v[20:23],a[20:23]
buffer_load_dwordx4 v230, s[8:11], s64 offen offset:2048 lds
MM a[20:23],a[136:139],v[16:19],a[20:23]
EX v101,v101
MM a[12:15],a[136:139],v[8:11],a[12:15]
RD a[216:219],v226 offset:4096
CV v70,v100,v101
MM a[12:15],a[140:143],v[12:15],a[12:15]
EX v102,v102
MM a[4:7],a[140:143],v[4:7],a[4:7]
EX v103,v103
MM a[4:7],a[136:139],v[0:3],a[4:7]
RD a[220:223],v227 offset:4096
CV v71,v102,v103
MM a[32:35],a[144:147],v[0:3],a[32:35]
EX v104,v104
MM a[32:35],a[148:151],v[4:7],a[32:35]
buffer_load_dwordx4 v231, s[8:11], s64 offen offset:3072 lds
MM a[40:43],a[148:151],v[12:15],a[40:43]
s_add_u32 m0, s65, 98304
EX v105,v105
MM a[40:43],a[144:147],v[8:11],a[40:43]
RD a[224:227],v224 offset:8192
CV v76,v104,v105
MM a[48:51],a[144:147],v[16:19],a[48:51]
EX v106,v106
MM a[48:51],a[148:151],v[20:23],a[48:51]
EX v107,v107
MM a[56:59],a[148:151],v[28:31],a[56:59]
RD a[228:231],v225 offset:8192
CV v77,v106,v107
MM a[56:59],a[144:147],v[24:27],a[56:59]
EX v108,v108
MM a[60:63],a[152:155],v[24:27],a[60:63]
buffer_load_dwordx4 v235, s[12:15], s81 offen offset:0 lds
MM a[60:63],a[156:159],v[28:31],a[60:63]
EX v109,v109
MM a[52:55],a[156:159],v[20:23],a[52:55]
RD a[232:235],v226 offset:8192
CV v78,v108,v109
MM a[52:55],a[152:155],v[16:19],a[52:55]
EX v110,v110
MM a[44:47],a[152:155],v[8:11],a[44:47]
EX v111,v111
MM a[44:47],a[156:159],v[12:15],a[44:47]
RD a[236:239],v227 offset:8192
CV v79,v110,v111
MM a[36:39],a[156:159],v[4:7],a[36:39]
buffer_load_dwordx4 v235, s[12:15], s64 offen offset:1024 lds
MM a[36:39],a[152:155],v[0:3],a[36:39]
EX v112,v112
MM a[64:67],a[160:163],v[0:3],a[64:67]
EX v113,v113
MM a[64:67],a[164:167],v[4:7],a[64:67]
RD a[240:243],v224 offset:12288
CV v84,v112,v113
MM a[72:75],a[164:167],v[12:15],a[72:75]
EX v114,v114
MM a[72:75],a[160:163],v[8:11],a[72:75]
RD a[244:247],v225 offset:12288
RD a[248:251],v226 offset:12288
MM a[80:83],a[160:163],v[16:19],a[80:83]
s_add_u32 m0, s67, 3072
s_add_u32 s85, s66, 165888
buffer_load_dwordx4 v252, s[36:39], s85 offen offset:0 lds
MM a[80:83],a[164:167],v[20:23],a[80:83]
EX v115,v115
MM a[88:91],a[164:167],v[28:31],a[88:91]
RD a[252:255],v227 offset:12288
CV v85,v114,v115
MM a[88:91],a[160:163],v[24:27],a[88:91]
EX v116,v116
MM a[92:95],a[168:171],v[24:27],a[92:95]
EX v117,v117
MM a[92:95],a[172:175],v[28:31],a[92:95]
s_add_u32 m0, s65, 98304
s_nop 0
buffer_load_dwordx4 v235, s[12:15], s81 offen offset:2048 lds
MM a[84:87],a[172:175],v[20:23],a[84:87]
EX v118,v118
MM a[84:87],a[168:171],v[16:19],a[84:87]
EX v119,v119
MM a[76:79],a[168:171],v[8:11],a[76:79]
buffer_load_dwordx4 v235, s[12:15], s64 offen offset:3072 lds
MM a[76:79],a[172:175],v[12:15],a[76:79]
EX v120,v120
MM a[68:71],a[172:175],v[4:7],a[68:71]
EX v121,v121
MM a[68:71],a[168:171],v[0:3],a[68:71]
EX v122,v122
MM a[96:99],a[176:179],v[0:3],a[96:99]
EX v123,v123
MM a[96:99],a[180:183],v[4:7],a[96:99]
EX v124,v124
MM a[104:107],a[180:183],v[12:15],a[104:107]
EX v125,v125
MM a[104:107],a[176:179],v[8:11],a[104:107]
EX v126,v126
MM a[112:115],a[176:179],v[16:19],a[112:115]
EX v127,v127
MM a[112:115],a[180:183],v[20:23],a[112:115]
CV v86,v116,v117
CV v87,v118,v119
MM a[120:123],a[180:183],v[28:31],a[120:123]
CV v92,v120,v121
CV v93,v122,v123
MM a[120:123],a[176:179],v[24:27],a[120:123]
CV v94,v124,v125
CV v95,v126,v127
MM a[124:127],a[184:187],v[24:27],a[124:127]
v_add_f32 v244, v144, v145
v_add_f32 v245, v148, v149
MM a[124:127],a[188:191],v[28:31],a[124:127]
v_add_f32 v246, v152, v153
v_add_f32 v247, v156, v157
MM a[116:119],a[188:191],v[20:23],a[116:119]
MM a[116:119],a[184:187],v[16:19],a[116:119]
MM a[108:111],a[184:187],v[8:11],a[108:111]
MM a[108:111],a[188:191],v[12:15],a[108:111]
MM a[100:103],a[188:191],v[4:7],a[100:103]
MM a[100:103],a[184:187],v[0:3],a[100:103]
v_smfmac_f32_16x16x64_bf16 v[144:147], v[236:239], v[64:71], v234
v_smfmac_f32_16x16x64_bf16 v[148:151], v[236:239], v[72:79], v234
v_smfmac_f32_16x16x64_bf16 v[152:155], v[236:239], v[80:87], v234
v_smfmac_f32_16x16x64_bf16 v[156:159], v[236:239], v[88:95], v234
s_waitcnt lgkmcnt(0)
MM v[0:3],a[192:195],v[160:163],v[128:131]
s_add_u32 s64, s64, 16384
TR a[128:129],v232 offset:49152
s_add_u32 s81, s64, 896
MM v[0:3],a[196:199],v[176:179],v[0:3]
TR a[130:131],v232 offset:53248
v_max3_f32 v249, v144, v145, v148
MM v[0:3],a[200:203],v[192:195],v[0:3]
TR a[132:133],v232 offset:57344
v_max3_f32 v249, v249, v149, v152
MM v[0:3],a[204:207],v[208:211],v[0:3]
TR a[134:135],v232 offset:61440
v_max3_f32 v249, v249, v153, v156
MM v[4:7],a[220:223],v[208:211],v[128:131]
TR a[136:137],v233 offset:49152
v_max_f32 v249, v249, v157
MM v[4:7],a[216:219],v[192:195],v[4:7]
TR a[138:139],v233 offset:53248
v_cmp_lt_f32 vcc, s71, v249
MM v[4:7],a[212:215],v[176:179],v[4:7]
TR a[140:141],v233 offset:57344
TR a[142:143],v233 offset:61440
MM v[4:7],a[208:211],v[160:163],v[4:7]
s_cbranch_vccnz LSITE11_%=
LBACK11_%=:
MM v[12:15],a[208:211],v[164:167],v[132:135]
s_add_u32 m0, s67, 4096
s_add_u32 s85, s66, 221184
buffer_load_dwordx4 v252, s[36:39], s85 offen offset:0 lds
MM v[12:15],a[212:215],v[180:183],v[12:15]
EX v0,v0
MM v[12:15],a[216:219],v[196:199],v[12:15]
EX v1,v1
MM v[12:15],a[220:223],v[212:215],v[12:15]
TR a[144:145],v232 offset:49664
CV v0,v0,v1
MM v[8:11],a[204:207],v[212:215],v[132:135]
EX v2,v2
MM v[8:11],a[200:203],v[196:199],v[8:11]
EX v3,v3
MM v[8:11],a[196:199],v[180:183],v[8:11]
TR a[146:147],v232 offset:53760
CV v1,v2,v3
MM v[8:11],a[192:195],v[164:167],v[8:11]
EX v4,v4
MM v[16:19],a[192:195],v[168:171],v[136:139]
TR a[148:149],v232 offset:57856
TR a[150:151],v232 offset:61952
MM v[16:19],a[196:199],v[184:187],v[16:19]
EX v5,v5
MM v[16:19],a[200:203],v[200:203],v[16:19]
TR a[152:153],v233 offset:49664
CV v2,v4,v5
MM v[16:19],a[204:207],v[216:219],v[16:19]
EX v6,v6
MM v[20:23],a[220:223],v[216:219],v[136:139]
EX v7,v7
MM v[20:23],a[216:219],v[200:203],v[20:23]
TR a[154:155],v233 offset:53760
CV v3,v6,v7
MM v[20:23],a[212:215],v[184:187],v[20:23]
EX v8,v8
MM v[20:23],a[208:211],v[168:171],v[20:23]
TR a[156:157],v233 offset:57856
TR a[158:159],v233 offset:61952
MM v[28:31],a[208:211],v[172:175],v[140:143]
EX v9,v9
MM v[28:31],a[212:215],v[188:191],v[28:31]
TR a[160:161],v232 offset:50176
CV v8,v8,v9
MM v[28:31],a[216:219],v[204:207],v[28:31]
EX v10,v10
MM v[28:31],a[220:223],v[220:223],v[28:31]
EX v11,v11
MM v[24:27],a[204:207],v[220:223],v[140:143]
TR a[162:163],v232 offset:54272
CV v9,v10,v11
MM v[24:27],a[200:203],v[204:207],v[24:27]
TR a[164:165],v232 offset:58368
TR a[166:167],v232 offset:62464
MM v[24:27],a[196:199],v[188:191],v[24:27]
EX v12,v12
MM v[24:27],a[192:195],v[172:175],v[24:27]
EX v13,v13
MM v[32:35],a[224:227],v[160:163],v[128:131]
TR a[168:169],v233 offset:50176
CV v10,v12,v13
MM v[32:35],a[228:231],v[176:179],v[32:35]
EX v14,v14
MM v[32:35],a[232:235],v[192:195],v[32:35]
TR a[170:171],v233 offset:54272
TR a[172:173],v233 offset:58368
MM v[32:35],a[236:239],v[208:211],v[32:35]
EX v15,v15
MM v[36:39],a[252:255],v[208:211],v[128:131]
TR a[174:175],v233 offset:62464
CV v11,v14,v15
MM v[36:39],a[248:251],v[192:195],v[36:39]
EX v16,v16
MM v[36:39],a[244:247],v[176:179],v[36:39]
EX v17,v17
MM v[36:39],a[240:243],v[160:163],v[36:39]
TR a[176:177],v232 offset:50688
CV v16,v16,v17
MM v[44:47],a[240:243],v[164:167],v[132:135]
EX v18,v18
MM v[44:47],a[244:247],v[180:183],v[44:47]
TR a[178:179],v232 offset:54784
TR a[180:181],v232 offset:58880
MM v[44:47],a[248:251],v[196:199],v[44:47]
EX v19,v19
MM v[44:47],a[252:255],v[212:215],v[44:47]
TR a[182:183],v232 offset:62976
CV v17,v18,v19
MM v[40:43],a[236:239],v[212:215],v[132:135]
EX v20,v20
MM v[40:43],a[232:235],v[196:199],v[40:43]
EX v21,v21
MM v[40:43],a[228:231],v[180:183],v[40:43]
TR a[184:185],v233 offset:50688
CV v18,v20,v21
MM v[40:43],a[224:227],v[164:167],v[40:43]
EX v22,v22
MM v[48:51],a[224:227],v[168:171],v[136:139]
TR a[186:187],v233 offset:54784
TR a[188:189],v233 offset:58880
MM v[48:51],a[228:231],v[184:187],v[48:51]
EX v23,v23
MM v[48:51],a[232:235],v[200:203],v[48:51]
TR a[190:191],v233 offset:62976
CV v19,v22,v23
MM v[48:51],a[236:239],v[216:219],v[48:51]
EX v24,v24
MM v[52:55],a[252:255],v[216:219],v[136:139]
EX v25,v25
MM v[52:55],a[248:251],v[200:203],v[52:55]
EX v26,v26
MM v[52:55],a[244:247],v[184:187],v[52:55]
EX v27,v27
MM v[52:55],a[240:243],v[168:171],v[52:55]
EX v28,v28
MM v[60:63],a[240:243],v[172:175],v[140:143]
EX v29,v29
MM v[60:63],a[244:247],v[188:191],v[60:63]
EX v30,v30
MM v[60:63],a[248:251],v[204:207],v[60:63]
EX v31,v31
LPG11_%=:
s_cbranch_execz LTR0_%=
MM v[60:63],a[252:255],v[220:223],v[60:63]
EX v32,v32
MM v[56:59],a[236:239],v[220:223],v[140:143]
EX v33,v33
MM v[56:59],a[232:235],v[204:207],v[56:59]
EX v34,v34
MM v[56:59],a[228:231],v[188:191],v[56:59]
EX v35,v35
MM v[56:59],a[224:227],v[172:175],v[56:59]
s_waitcnt vmcnt(17) lgkmcnt(0)
s_barrier
MM a[0:3],a[128:131],v[64:67],a[0:3]
s_mov_b32 s86, 94208
v_bfe_u32 v251, v248, 4, 3
v_lshlrev_b32 v251, 3, v251
buffer_load_dwordx2 v[252:253], v251, s[20:23], s86 offen offset:0 sc1
MM a[0:3],a[132:135],v[68:71],a[0:3]
s_add_u32 m0, s65, 49152
CV v24,v24,v25
CV v25,v26,v27
MM a[8:11],a[132:135],v[76:79],a[8:11]
RD a[192:195],v224 offset:16384
CV v26,v28,v29
MM a[8:11],a[128:131],v[72:75],a[8:11]
RD a[196:199],v225 offset:16384
CV v27,v30,v31
MM a[16:19],a[128:131],v[80:83],a[16:19]
buffer_load_dwordx4 v228, s[8:11], s64 offen offset:0 lds
MM a[16:19],a[132:135],v[84:87],a[16:19]
RD a[200:203],v226 offset:16384
CV v4,v32,v33
MM a[24:27],a[132:135],v[92:95],a[24:27]
RD a[204:207],v227 offset:16384
CV v5,v34,v35
MM a[24:27],a[128:131],v[88:91],a[24:27]
buffer_load_dwordx4 v229, s[8:11], s64 offen offset:1024 lds
MM a[28:31],a[136:139],v[88:91],a[28:31]
RD a[208:211],v224 offset:20480
RD a[212:215],v225 offset:20480
MM a[28:31],a[140:143],v[92:95],a[28:31]
EX v36,v36
MM a[20:23],a[140:143],v[84:87],a[20:23]
buffer_load_dwordx4 v230, s[8:11], s64 offen offset:2048 lds
MM a[20:23],a[136:139],v[80:83],a[20:23]
EX v37,v37
MM a[12:15],a[136:139],v[72:75],a[12:15]
RD a[216:219],v226 offset:20480
CV v6,v36,v37
MM a[12:15],a[140:143],v[76:79],a[12:15]
EX v38,v38
MM a[4:7],a[140:143],v[68:71],a[4:7]
EX v39,v39
MM a[4:7],a[136:139],v[64:67],a[4:7]
RD a[220:223],v227 offset:20480
CV v7,v38,v39
MM a[32:35],a[144:147],v[64:67],a[32:35]
EX v40,v40
MM a[32:35],a[148:151],v[68:71],a[32:35]
buffer_load_dwordx4 v231, s[8:11], s64 offen offset:3072 lds
MM a[40:43],a[148:151],v[76:79],a[40:43]
s_add_u32 m0, s65, 114688
EX v41,v41
MM a[40:43],a[144:147],v[72:75],a[40:43]
RD a[224:227],v224 offset:24576
CV v12,v40,v41
MM a[48:51],a[144:147],v[80:83],a[48:51]
EX v42,v42
MM a[48:51],a[148:151],v[84:87],a[48:51]
EX v43,v43
MM a[56:59],a[148:151],v[92:95],a[56:59]
RD a[228:231],v225 offset:24576
CV v13,v42,v43
MM a[56:59],a[144:147],v[88:91],a[56:59]
EX v44,v44
MM a[60:63],a[152:155],v[88:91],a[60:63]
buffer_load_dwordx4 v235, s[12:15], s81 offen offset:0 lds
MM a[60:63],a[156:159],v[92:95],a[60:63]
)ASM"
R"ASM(
EX v45,v45
MM a[52:55],a[156:159],v[84:87],a[52:55]
RD a[232:235],v226 offset:24576
CV v14,v44,v45
MM a[52:55],a[152:155],v[80:83],a[52:55]
EX v46,v46
MM a[44:47],a[152:155],v[72:75],a[44:47]
EX v47,v47
MM a[44:47],a[156:159],v[76:79],a[44:47]
RD a[236:239],v227 offset:24576
CV v15,v46,v47
MM a[36:39],a[156:159],v[68:71],a[36:39]
buffer_load_dwordx4 v235, s[12:15], s64 offen offset:1024 lds
MM a[36:39],a[152:155],v[64:67],a[36:39]
EX v48,v48
MM a[64:67],a[160:163],v[64:67],a[64:67]
EX v49,v49
MM a[64:67],a[164:167],v[68:71],a[64:67]
RD a[240:243],v224 offset:28672
CV v20,v48,v49
MM a[72:75],a[164:167],v[76:79],a[72:75]
EX v50,v50
MM a[72:75],a[160:163],v[72:75],a[72:75]
RD a[244:247],v225 offset:28672
RD a[248:251],v226 offset:28672
MM a[80:83],a[160:163],v[80:83],a[80:83]
EX v51,v51
MM a[80:83],a[164:167],v[84:87],a[80:83]
RD a[252:255],v227 offset:28672
CV v21,v50,v51
MM a[88:91],a[164:167],v[92:95],a[88:91]
EX v52,v52
MM a[88:91],a[160:163],v[88:91],a[88:91]
buffer_load_dwordx4 v235, s[12:15], s81 offen offset:2048 lds
MM a[92:95],a[168:171],v[88:91],a[92:95]
EX v53,v53
MM a[92:95],a[172:175],v[92:95],a[92:95]
EX v54,v54
MM a[84:87],a[172:175],v[84:87],a[84:87]
buffer_load_dwordx4 v235, s[12:15], s64 offen offset:3072 lds
MM a[84:87],a[168:171],v[80:83],a[84:87]
EX v55,v55
MM a[76:79],a[168:171],v[72:75],a[76:79]
EX v56,v56
MM a[76:79],a[172:175],v[76:79],a[76:79]
EX v57,v57
MM a[68:71],a[172:175],v[68:71],a[68:71]
EX v58,v58
MM a[68:71],a[168:171],v[64:67],a[68:71]
EX v59,v59
MM a[96:99],a[176:179],v[64:67],a[96:99]
EX v60,v60
MM a[96:99],a[180:183],v[68:71],a[96:99]
EX v61,v61
MM a[104:107],a[180:183],v[76:79],a[104:107]
EX v62,v62
MM a[104:107],a[176:179],v[72:75],a[104:107]
EX v63,v63
MM a[112:115],a[176:179],v[80:83],a[112:115]
CV v22,v52,v53
CV v23,v54,v55
MM a[112:115],a[180:183],v[84:87],a[112:115]
CV v28,v56,v57
CV v29,v58,v59
MM a[120:123],a[180:183],v[92:95],a[120:123]
CV v30,v60,v61
CV v31,v62,v63
MM a[120:123],a[176:179],v[88:91],a[120:123]
MM a[124:127],a[184:187],v[88:91],a[124:127]
v_add_f32 v244, v144, v145
v_add_f32 v245, v148, v149
MM a[124:127],a[188:191],v[92:95],a[124:127]
v_add_f32 v246, v152, v153
v_add_f32 v247, v156, v157
MM a[116:119],a[188:191],v[84:87],a[116:119]
MM a[116:119],a[184:187],v[80:83],a[116:119]
MM a[108:111],a[184:187],v[72:75],a[108:111]
MM a[108:111],a[188:191],v[76:79],a[108:111]
MM a[100:103],a[188:191],v[68:71],a[100:103]
MM a[100:103],a[184:187],v[64:67],a[100:103]
v_smfmac_f32_16x16x64_bf16 v[144:147], v[236:239], v[0:7], v234
v_smfmac_f32_16x16x64_bf16 v[148:151], v[236:239], v[8:15], v234
v_smfmac_f32_16x16x64_bf16 v[152:155], v[236:239], v[16:23], v234
v_smfmac_f32_16x16x64_bf16 v[156:159], v[236:239], v[24:31], v234
s_waitcnt lgkmcnt(0)
MM v[64:67],a[192:195],v[160:163],v[128:131]
s_add_u32 s64, s64, 16384
TR a[128:129],v232 offset:0
s_add_u32 s81, s64, 896
MM v[64:67],a[196:199],v[176:179],v[64:67]
TR a[130:131],v232 offset:4096
v_max3_f32 v249, v144, v145, v148
MM v[64:67],a[200:203],v[192:195],v[64:67]
TR a[132:133],v232 offset:8192
v_max3_f32 v249, v249, v149, v152
MM v[64:67],a[204:207],v[208:211],v[64:67]
TR a[134:135],v232 offset:12288
v_max3_f32 v249, v249, v153, v156
MM v[68:71],a[220:223],v[208:211],v[128:131]
TR a[136:137],v233 offset:0
v_max_f32 v249, v249, v157
MM v[68:71],a[216:219],v[192:195],v[68:71]
TR a[138:139],v233 offset:4096
v_cmp_lt_f32 vcc, s71, v249
MM v[68:71],a[212:215],v[176:179],v[68:71]
TR a[140:141],v233 offset:8192
TR a[142:143],v233 offset:12288
MM v[68:71],a[208:211],v[160:163],v[68:71]
s_cbranch_vccnz LSITE12_%=
LBACK12_%=:
MM v[76:79],a[208:211],v[164:167],v[132:135]
s_waitcnt vmcnt(8)
v_cmp_ne_u32 vcc, s69, v253
s_cbranch_vccnz LRIX_%=
LRIXb_%=:
MM v[76:79],a[212:215],v[180:183],v[76:79]
s_and_b32 s90, s17, 7
v_and_b32 v253, 65535, v252
v_readlane_b32 s19, v252, s90
MM v[76:79],a[216:219],v[196:199],v[76:79]
v_sub_u32 v253, 65535, v253
v_mul_u32_u24 v253, 1280, v253
MM v[76:79],a[220:223],v[212:215],v[76:79]
v_lshrrev_b32 v252, 7, v248
v_lshl_add_u32 v253, v252, 4, v253
MM v[72:75],a[204:207],v[212:215],v[132:135]
s_mul_i32 s91, %[swave], 3072
EX v64,v64
MM v[72:75],a[200:203],v[196:199],v[72:75]
s_add_u32 s91, s91, %[slds]
EX v65,v65
MM v[72:75],a[196:199],v[180:183],v[72:75]
s_bfe_u32 s90, s48, 131077
EX v66,v66
MM v[72:75],a[192:195],v[164:167],v[72:75]
s_mul_i32 s90, s90, 320
EX v67,v67
MM v[80:83],a[192:195],v[168:171],v[136:139]
s_add_u32 s90, s90, 131072
s_add_u32 m0, s91, 151552
s_nop 0
buffer_load_dwordx4 v253, s[20:23], s90 offen offset:0 sc1 lds
MM v[80:83],a[196:199],v[184:187],v[80:83]
TR a[144:145],v232 offset:512
CV v64,v64,v65
MM v[80:83],a[200:203],v[200:203],v[80:83]
s_add_u32 m0, s91, 152448
s_nop 0
buffer_load_dwordx4 v253, s[20:23], s90 offen offset:128 sc1 lds
MM v[80:83],a[204:207],v[216:219],v[80:83]
EX v68,v68
MM v[84:87],a[220:223],v[216:219],v[136:139]
s_add_u32 m0, s91, 153344
s_nop 0
buffer_load_dwordx4 v253, s[20:23], s90 offen offset:256 sc1 lds
MM v[84:87],a[216:219],v[200:203],v[84:87]
EX v69,v69
MM v[84:87],a[212:215],v[184:187],v[84:87]
s_lshr_b32 s77, %[sb], 3
TR a[146:147],v232 offset:4608
TR a[148:149],v232 offset:8704
MM v[84:87],a[208:211],v[168:171],v[84:87]
s_lshl_b32 s77, s77, 2
EX v70,v70
MM v[92:95],a[208:211],v[172:175],v[140:143]
s_add_u32 s77, s77, %[swave]
EX v71,v71
MM v[92:95],a[212:215],v[188:191],v[92:95]
s_lshl_b32 s77, s77, 9
TR a[150:151],v232 offset:12800
CV v65,v66,v67
MM v[92:95],a[216:219],v[204:207],v[92:95]
s_add_u32 s77, s77, 0
v_lshrrev_b32 v253, 1, v248
v_add_u32 v253, s77, v253
MM v[92:95],a[220:223],v[220:223],v[92:95]
s_mov_b32 s77, LKEND_ka-LPD0_%=-16
TR a[152:153],v233 offset:512
v_min_u32 v253, s77, v253
EX v72,v72
MM v[88:91],a[204:207],v[220:223],v[140:143]
s_getpc_b64 s[98:99]
LPD0_%=:
global_load_dword v253, v253, s[98:99]
MM v[88:91],a[200:203],v[204:207],v[88:91]
s_add_u32 s67, s95, 1024
EX v73,v73
MM v[88:91],a[196:199],v[188:191],v[88:91]
s_mov_b32 s70, 5
TR a[154:155],v233 offset:4608
CV v66,v68,v69
MM v[88:91],a[192:195],v[172:175],v[88:91]
EX v74,v74
MM v[96:99],a[224:227],v[160:163],v[128:131]
TR a[156:157],v233 offset:8704
TR a[158:159],v233 offset:12800
EX v75,v75
MM v[96:99],a[228:231],v[176:179],v[96:99]
TR a[160:161],v232 offset:1024
CV v67,v70,v71
MM v[96:99],a[232:235],v[192:195],v[96:99]
EX v76,v76
MM v[96:99],a[236:239],v[208:211],v[96:99]
EX v77,v77
MM v[100:103],a[252:255],v[208:211],v[128:131]
TR a[162:163],v232 offset:5120
CV v72,v72,v73
MM v[100:103],a[248:251],v[192:195],v[100:103]
EX v78,v78
MM v[100:103],a[244:247],v[176:179],v[100:103]
TR a[164:165],v232 offset:9216
CV v73,v74,v75
MM v[100:103],a[240:243],v[160:163],v[100:103]
EX v79,v79
MM v[108:111],a[240:243],v[164:167],v[132:135]
TR a[166:167],v232 offset:13312
TR a[168:169],v233 offset:1024
MM v[108:111],a[244:247],v[180:183],v[108:111]
EX v80,v80
MM v[108:111],a[248:251],v[196:199],v[108:111]
EX v81,v81
MM v[108:111],a[252:255],v[212:215],v[108:111]
TR a[170:171],v233 offset:5120
CV v74,v76,v77
MM v[104:107],a[236:239],v[212:215],v[132:135]
EX v82,v82
MM v[104:107],a[232:235],v[196:199],v[104:107]
TR a[172:173],v233 offset:9216
CV v75,v78,v79
MM v[104:107],a[228:231],v[180:183],v[104:107]
EX v83,v83
MM v[104:107],a[224:227],v[164:167],v[104:107]
TR a[174:175],v233 offset:13312
TR a[176:177],v232 offset:1536
MM v[112:115],a[224:227],v[168:171],v[136:139]
EX v84,v84
MM v[112:115],a[228:231],v[184:187],v[112:115]
TR a[178:179],v232 offset:5632
CV v80,v80,v81
MM v[112:115],a[232:235],v[200:203],v[112:115]
EX v85,v85
MM v[112:115],a[236:239],v[216:219],v[112:115]
TR a[180:181],v232 offset:9728
CV v81,v82,v83
MM v[116:119],a[252:255],v[216:219],v[136:139]
EX v86,v86
MM v[116:119],a[248:251],v[200:203],v[116:119]
TR a[182:183],v232 offset:13824
TR a[184:185],v233 offset:1536
MM v[116:119],a[244:247],v[184:187],v[116:119]
EX v87,v87
MM v[116:119],a[240:243],v[168:171],v[116:119]
EX v88,v88
MM v[124:127],a[240:243],v[172:175],v[140:143]
TR a[186:187],v233 offset:5632
CV v82,v84,v85
MM v[124:127],a[244:247],v[188:191],v[124:127]
EX v89,v89
MM v[124:127],a[248:251],v[204:207],v[124:127]
TR a[188:189],v233 offset:9728
TR a[190:191],v233 offset:13824
MM v[124:127],a[252:255],v[220:223],v[124:127]
EX v90,v90
MM v[120:123],a[236:239],v[220:223],v[140:143]
CV v83,v86,v87
CV v88,v88,v89
MM v[120:123],a[232:235],v[204:207],v[120:123]
EX v91,v91
MM v[120:123],a[228:231],v[188:191],v[120:123]
EX v92,v92
MM v[120:123],a[224:227],v[172:175],v[120:123]
s_waitcnt lgkmcnt(0)
s_barrier
MM a[0:3],a[128:131],v[0:3],a[0:3]
s_add_u32 m0, s65, 0
EX v93,v93
MM a[0:3],a[132:135],v[4:7],a[0:3]
CV v89,v90,v91
CV v90,v92,v93
MM a[8:11],a[132:135],v[12:15],a[8:11]
EX v94,v94
MM a[8:11],a[128:131],v[8:11],a[8:11]
EX v95,v95
MM a[16:19],a[128:131],v[16:19],a[16:19]
RD a[192:195],v224 offset:32768
CV v91,v94,v95
MM a[16:19],a[132:135],v[20:23],a[16:19]
EX v96,v96
MM a[24:27],a[132:135],v[28:31],a[24:27]
buffer_load_dwordx4 v228, s[8:11], s64 offen offset:0 lds
MM a[24:27],a[128:131],v[24:27],a[24:27]
EX v97,v97
MM a[28:31],a[136:139],v[24:27],a[28:31]
RD a[196:199],v225 offset:32768
CV v68,v96,v97
MM a[28:31],a[140:143],v[28:31],a[28:31]
EX v98,v98
MM a[20:23],a[140:143],v[20:23],a[20:23]
EX v99,v99
MM a[20:23],a[136:139],v[16:19],a[20:23]
RD a[200:203],v226 offset:32768
CV v69,v98,v99
MM a[12:15],a[136:139],v[8:11],a[12:15]
EX v100,v100
MM a[12:15],a[140:143],v[12:15],a[12:15]
buffer_load_dwordx4 v229, s[8:11], s64 offen offset:1024 lds
MM a[4:7],a[140:143],v[4:7],a[4:7]
EX v101,v101
MM a[4:7],a[136:139],v[0:3],a[4:7]
RD a[204:207],v227 offset:32768
CV v70,v100,v101
MM a[32:35],a[144:147],v[0:3],a[32:35]
EX v102,v102
MM a[32:35],a[148:151],v[4:7],a[32:35]
EX v103,v103
MM a[40:43],a[148:151],v[12:15],a[40:43]
RD a[208:211],v224 offset:36864
CV v71,v102,v103
MM a[40:43],a[144:147],v[8:11],a[40:43]
EX v104,v104
MM a[48:51],a[144:147],v[16:19],a[48:51]
buffer_load_dwordx4 v230, s[8:11], s64 offen offset:2048 lds
MM a[48:51],a[148:151],v[20:23],a[48:51]
EX v105,v105
MM a[56:59],a[148:151],v[28:31],a[56:59]
RD a[212:215],v225 offset:36864
CV v76,v104,v105
MM a[56:59],a[144:147],v[24:27],a[56:59]
EX v106,v106
MM a[60:63],a[152:155],v[24:27],a[60:63]
EX v107,v107
MM a[60:63],a[156:159],v[28:31],a[60:63]
RD a[216:219],v226 offset:36864
CV v77,v106,v107
MM a[52:55],a[156:159],v[20:23],a[52:55]
EX v108,v108
MM a[52:55],a[152:155],v[16:19],a[52:55]
buffer_load_dwordx4 v231, s[8:11], s64 offen offset:3072 lds
MM a[44:47],a[152:155],v[8:11],a[44:47]
s_add_u32 m0, s65, 65536
EX v109,v109
MM a[44:47],a[156:159],v[12:15],a[44:47]
RD a[220:223],v227 offset:36864
CV v78,v108,v109
MM a[36:39],a[156:159],v[4:7],a[36:39]
EX v110,v110
MM a[36:39],a[152:155],v[0:3],a[36:39]
RD a[224:227],v224 offset:40960
RD a[228:231],v225 offset:40960
MM a[64:67],a[160:163],v[0:3],a[64:67]
EX v111,v111
MM a[64:67],a[164:167],v[4:7],a[64:67]
RD a[232:235],v226 offset:40960
CV v79,v110,v111
MM a[72:75],a[164:167],v[12:15],a[72:75]
EX v112,v112
MM a[72:75],a[160:163],v[8:11],a[72:75]
buffer_load_dwordx4 v235, s[12:15], s81 offen offset:0 lds
MM a[80:83],a[160:163],v[16:19],a[80:83]
EX v113,v113
MM a[80:83],a[164:167],v[20:23],a[80:83]
RD a[236:239],v227 offset:40960
CV v84,v112,v113
MM a[88:91],a[164:167],v[28:31],a[88:91]
EX v114,v114
MM a[88:91],a[160:163],v[24:27],a[88:91]
EX v115,v115
MM a[92:95],a[168:171],v[24:27],a[92:95]
RD a[240:243],v224 offset:45056
CV v85,v114,v115
MM a[92:95],a[172:175],v[28:31],a[92:95]
EX v116,v116
MM a[84:87],a[172:175],v[20:23],a[84:87]
buffer_load_dwordx4 v235, s[12:15], s64 offen offset:1024 lds
MM a[84:87],a[168:171],v[16:19],a[84:87]
EX v117,v117
MM a[76:79],a[168:171],v[8:11],a[76:79]
RD a[244:247],v225 offset:45056
CV v86,v116,v117
MM a[76:79],a[172:175],v[12:15],a[76:79]
EX v118,v118
MM a[68:71],a[172:175],v[4:7],a[68:71]
EX v119,v119
MM a[68:71],a[168:171],v[0:3],a[68:71]
RD a[248:251],v226 offset:45056
CV v87,v118,v119
MM a[96:99],a[176:179],v[0:3],a[96:99]
EX v120,v120
MM a[96:99],a[180:183],v[4:7],a[96:99]
buffer_load_dwordx4 v235, s[12:15], s81 offen offset:2048 lds
)ASM"
R"ASM(
MM a[104:107],a[180:183],v[12:15],a[104:107]
EX v121,v121
MM a[104:107],a[176:179],v[8:11],a[104:107]
RD a[252:255],v227 offset:45056
CV v92,v120,v121
MM a[112:115],a[176:179],v[16:19],a[112:115]
EX v122,v122
MM a[112:115],a[180:183],v[20:23],a[112:115]
EX v123,v123
MM a[120:123],a[180:183],v[28:31],a[120:123]
s_add_u32 s87, s95, 0
buffer_store_dword v243, v248, s[20:23], s87 offen offset:12 sc1
MM a[120:123],a[176:179],v[24:27],a[120:123]
EX v124,v124
MM a[124:127],a[184:187],v[24:27],a[124:127]
v_add_f32 v244, v144, v145
v_add_f32 v245, v148, v149
MM a[124:127],a[188:191],v[28:31],a[124:127]
v_add_f32 v246, v152, v153
v_add_f32 v247, v156, v157
MM a[116:119],a[188:191],v[20:23],a[116:119]
EX v125,v125
MM a[116:119],a[184:187],v[16:19],a[116:119]
buffer_load_dwordx4 v235, s[12:15], s64 offen offset:3072 lds
MM a[108:111],a[184:187],v[8:11],a[108:111]
EX v126,v126
MM a[108:111],a[188:191],v[12:15],a[108:111]
EX v127,v127
MM a[100:103],a[188:191],v[4:7],a[100:103]
CV v93,v122,v123
CV v94,v124,v125
MM a[100:103],a[184:187],v[0:3],a[100:103]
CV v95,v126,v127
v_smfmac_f32_16x16x64_bf16 v[144:147], v[236:239], v[64:71], v234
v_smfmac_f32_16x16x64_bf16 v[148:151], v[236:239], v[72:79], v234
v_smfmac_f32_16x16x64_bf16 v[152:155], v[236:239], v[80:87], v234
v_smfmac_f32_16x16x64_bf16 v[156:159], v[236:239], v[88:95], v234
s_waitcnt lgkmcnt(0)
s_cmp_ge_u32 s16, 108
s_cbranch_scc1 LLAP_%=
MM v[0:3],a[192:195],v[160:163],v[128:131]
s_add_u32 s64, s64, 16384
TR a[128:129],v232 offset:16384
s_add_u32 s81, s64, 896
MM v[0:3],a[196:199],v[176:179],v[0:3]
TR a[130:131],v232 offset:20480
v_max3_f32 v249, v144, v145, v148
MM v[0:3],a[200:203],v[192:195],v[0:3]
TR a[132:133],v232 offset:24576
v_max3_f32 v249, v249, v149, v152
MM v[0:3],a[204:207],v[208:211],v[0:3]
TR a[134:135],v232 offset:28672
v_max3_f32 v249, v249, v153, v156
MM v[4:7],a[220:223],v[208:211],v[128:131]
TR a[136:137],v233 offset:16384
v_max_f32 v249, v249, v157
MM v[4:7],a[216:219],v[192:195],v[4:7]
TR a[138:139],v233 offset:20480
v_cmp_lt_f32 vcc, s71, v249
MM v[4:7],a[212:215],v[176:179],v[4:7]
TR a[140:141],v233 offset:24576
TR a[142:143],v233 offset:28672
MM v[4:7],a[208:211],v[160:163],v[4:7]
s_cbranch_vccnz LSITE13_%=
LBACK13_%=:
MM v[12:15],a[208:211],v[164:167],v[132:135]
v_lshrrev_b32 v127, 8, v248
v_lshlrev_b32 v124, 9, v127
MM v[12:15],a[212:215],v[180:183],v[12:15]
v_and_b32 v126, 192, v248
v_add_u32 v124, v124, v126
MM v[12:15],a[216:219],v[196:199],v[12:15]
v_bfe_u32 v126, v248, 4, 2
v_lshl_add_u32 v124, v126, 3, v124
MM v[12:15],a[220:223],v[212:215],v[12:15]
s_lshr_b32 s76, %[swave], 1
EX v0,v0
MM v[8:11],a[204:207],v[212:215],v[132:135]
s_mul_i32 s76, s76, 10240
EX v1,v1
MM v[8:11],a[200:203],v[196:199],v[8:11]
s_and_b32 s77, %[swave], 1
EX v2,v2
MM v[8:11],a[196:199],v[180:183],v[8:11]
s_lshl_b32 s77, s77, 5
TR a[144:145],v232 offset:16896
CV v0,v0,v1
MM v[8:11],a[192:195],v[164:167],v[8:11]
s_add_u32 s76, s76, s77
EX v3,v3
MM v[16:19],a[192:195],v[168:171],v[136:139]
s_add_u32 s76, s76, %[slds]
EX v4,v4
MM v[16:19],a[196:199],v[184:187],v[16:19]
s_add_u32 s76, s76, 131072
v_add_u32 v124, s76, v124
v_bfe_u32 v126, v248, 4, 3
MM v[16:19],a[200:203],v[200:203],v[16:19]
v_lshl_add_u32 v126, v127, 3, v126
v_lshlrev_b32 v126, 4, v126
MM v[16:19],a[204:207],v[216:219],v[16:19]
s_mul_i32 s76, %[swave], 3072
EX v5,v5
MM v[20:23],a[220:223],v[216:219],v[136:139]
s_add_u32 s76, s76, %[slds]
TR a[146:147],v232 offset:20992
CV v1,v2,v3
MM v[20:23],a[216:219],v[200:203],v[20:23]
s_add_u32 s76, s76, 151552
TR a[148:149],v232 offset:25088
v_add_u32 v125, s76, v126
MM v[20:23],a[212:215],v[184:187],v[20:23]
s_lshr_b32 s76, s48, 7
EX v6,v6
MM v[20:23],a[208:211],v[168:171],v[20:23]
s_lshl_b32 s76, s76, 5
EX v7,v7
MM v[28:31],a[208:211],v[172:175],v[140:143]
s_and_b32 s77, s48, 16
TR a[150:151],v232 offset:29184
TR a[152:153],v233 offset:16896
MM v[28:31],a[212:215],v[188:191],v[28:31]
s_add_u32 s76, s76, s77
EX v8,v8
MM v[28:31],a[216:219],v[204:207],v[28:31]
s_bfe_u32 s77, s48, 131077
TR a[154:155],v233 offset:20992
CV v2,v4,v5
MM v[28:31],a[220:223],v[220:223],v[28:31]
s_lshl_b32 s77, s77, 2
EX v9,v9
s_nop 0
LPG12_%=:
s_cbranch_execz LTR0_%=
MM v[24:27],a[204:207],v[220:223],v[140:143]
s_add_u32 s76, s76, s77
TR a[156:157],v233 offset:25088
CV v3,v6,v7
MM v[24:27],a[200:203],v[204:207],v[24:27]
s_add_u32 s76, s76, %[swave]
EX v10,v10
MM v[24:27],a[196:199],v[188:191],v[24:27]
s_lshl_b32 s97, s76, 2
TR a[158:159],v233 offset:29184
CV v8,v8,v9
MM v[24:27],a[192:195],v[172:175],v[24:27]
s_add_u32 s97, s97, 77824
EX v11,v11
MM v[32:35],a[224:227],v[160:163],v[128:131]
s_lshl_b32 s76, s76, 9
TR a[160:161],v232 offset:17408
TR a[162:163],v232 offset:21504
MM v[32:35],a[228:231],v[176:179],v[32:35]
s_add_u32 s76, s76, 22183936
EX v12,v12
MM v[32:35],a[232:235],v[192:195],v[32:35]
EX v13,v13
MM v[32:35],a[236:239],v[208:211],v[32:35]
TR a[164:165],v232 offset:25600
CV v9,v10,v11
MM v[36:39],a[252:255],v[208:211],v[128:131]
EX v14,v14
MM v[36:39],a[248:251],v[192:195],v[36:39]
TR a[166:167],v232 offset:29696
CV v10,v12,v13
MM v[36:39],a[244:247],v[176:179],v[36:39]
EX v15,v15
MM v[36:39],a[240:243],v[160:163],v[36:39]
TR a[168:169],v233 offset:17408
TR a[170:171],v233 offset:21504
MM v[44:47],a[240:243],v[164:167],v[132:135]
EX v16,v16
MM v[44:47],a[244:247],v[180:183],v[44:47]
EX v17,v17
MM v[44:47],a[248:251],v[196:199],v[44:47]
TR a[172:173],v233 offset:25600
CV v11,v14,v15
MM v[44:47],a[252:255],v[212:215],v[44:47]
EX v18,v18
MM v[40:43],a[236:239],v[212:215],v[132:135]
TR a[174:175],v233 offset:29696
CV v16,v16,v17
MM v[40:43],a[232:235],v[196:199],v[40:43]
EX v19,v19
MM v[40:43],a[228:231],v[180:183],v[40:43]
TR a[176:177],v232 offset:17920
TR a[178:179],v232 offset:22016
MM v[40:43],a[224:227],v[164:167],v[40:43]
EX v20,v20
MM v[48:51],a[224:227],v[168:171],v[136:139]
EX v21,v21
MM v[48:51],a[228:231],v[184:187],v[48:51]
TR a[180:181],v232 offset:26112
CV v17,v18,v19
MM v[48:51],a[232:235],v[200:203],v[48:51]
EX v22,v22
MM v[48:51],a[236:239],v[216:219],v[48:51]
TR a[182:183],v232 offset:30208
CV v18,v20,v21
MM v[52:55],a[252:255],v[216:219],v[136:139]
EX v23,v23
MM v[52:55],a[248:251],v[200:203],v[52:55]
TR a[184:185],v233 offset:17920
TR a[186:187],v233 offset:22016
MM v[52:55],a[244:247],v[184:187],v[52:55]
EX v24,v24
MM v[52:55],a[240:243],v[168:171],v[52:55]
TR a[188:189],v233 offset:26112
CV v19,v22,v23
MM v[60:63],a[240:243],v[172:175],v[140:143]
EX v25,v25
MM v[60:63],a[244:247],v[188:191],v[60:63]
TR a[190:191],v233 offset:30208
CV v24,v24,v25
MM v[60:63],a[248:251],v[204:207],v[60:63]
EX v26,v26
MM v[60:63],a[252:255],v[220:223],v[60:63]
EX v27,v27
MM v[56:59],a[236:239],v[220:223],v[140:143]
EX v28,v28
MM v[56:59],a[232:235],v[204:207],v[56:59]
EX v29,v29
MM v[56:59],a[228:231],v[188:191],v[56:59]
s_getpc_b64 s[98:99]
LTA2_%=:
s_min_u32 s73, %[swave], 3
s_lshl_b32 s73, s73, 12
s_add_u32 s73, s73, LPG13_%=-LTA2_%=
s_add_u32 s98, s98, s73
s_addc_u32 s99, s99, 0
s_mov_b64 exec, 0
s_setpc_b64 s[98:99]
LTR2_%=:
s_mov_b64 exec, -1
CV v25,v26,v27
MM v[56:59],a[224:227],v[172:175],v[56:59]
s_waitcnt vmcnt(17) lgkmcnt(0)
s_barrier
MM a[0:3],a[128:131],v[64:67],a[0:3]
s_add_u32 m0, s65, 16384
RD a[192:195],v224 offset:49152
CV v26,v28,v29
MM a[0:3],a[132:135],v[68:71],a[0:3]
EX v30,v30
MM a[8:11],a[132:135],v[76:79],a[8:11]
EX v31,v31
MM a[8:11],a[128:131],v[72:75],a[8:11]
RD a[196:199],v225 offset:49152
CV v27,v30,v31
MM a[16:19],a[128:131],v[80:83],a[16:19]
s_waitcnt vmcnt(10)
TR v[96:97],v124 offset:0
TR v[98:99],v124 offset:256
MM a[16:19],a[132:135],v[84:87],a[16:19]
RD v[108:111],v125 offset:0
TR v[100:101],v124 offset:2048
MM a[24:27],a[132:135],v[92:95],a[24:27]
TR v[102:103],v124 offset:2304
RD v[112:115],v125 offset:512
MM a[24:27],a[128:131],v[88:91],a[24:27]
TR v[104:105],v124 offset:4096
TR v[106:107],v124 offset:4352
MM a[28:31],a[136:139],v[88:91],a[28:31]
RD v[116:119],v125 offset:1024
RD a[200:203],v226 offset:49152
EX v32,v32
MM a[28:31],a[140:143],v[92:95],a[28:31]
EX v33,v33
MM a[20:23],a[140:143],v[84:87],a[20:23]
EX v34,v34
MM a[20:23],a[136:139],v[80:83],a[20:23]
buffer_load_dwordx4 v228, s[8:11], s64 offen offset:0 lds
MM a[12:15],a[136:139],v[72:75],a[12:15]
EX v35,v35
MM a[12:15],a[140:143],v[76:79],a[12:15]
EX v36,v36
MM a[4:7],a[140:143],v[68:71],a[4:7]
CV v4,v32,v33
CV v5,v34,v35
MM a[4:7],a[136:139],v[64:67],a[4:7]
EX v37,v37
MM a[32:35],a[144:147],v[64:67],a[32:35]
EX v38,v38
MM a[32:35],a[148:151],v[68:71],a[32:35]
s_waitcnt lgkmcnt(1)
buffer_load_dwordx4 v229, s[8:11], s64 offen offset:1024 lds
MM a[40:43],a[148:151],v[76:79],a[40:43]
EX v39,v39
MM v[120:123],v[96:99],v[108:111],0
MM v[120:123],v[100:103],v[112:115],v[120:123]
MM v[120:123],v[104:107],v[116:119],v[120:123]
MM a[40:43],a[144:147],v[72:75],a[40:43]
TR v[96:97],v124 offset:6144
TR v[98:99],v124 offset:6400
MM a[48:51],a[144:147],v[80:83],a[48:51]
RD v[108:111],v125 offset:1536
TR v[100:101],v124 offset:8192
MM a[48:51],a[148:151],v[84:87],a[48:51]
TR v[102:103],v124 offset:8448
RD v[112:115],v125 offset:2048
MM a[56:59],a[148:151],v[92:95],a[56:59]
EX v40,v40
MM a[56:59],a[144:147],v[88:91],a[56:59]
EX v41,v41
MM a[60:63],a[152:155],v[88:91],a[60:63]
RD a[204:207],v227 offset:49152
CV v6,v36,v37
MM a[60:63],a[156:159],v[92:95],a[60:63]
EX v42,v42
MM a[52:55],a[156:159],v[84:87],a[52:55]
RD a[208:211],v224 offset:53248
CV v7,v38,v39
MM a[52:55],a[152:155],v[80:83],a[52:55]
EX v43,v43
MM a[44:47],a[152:155],v[72:75],a[44:47]
buffer_load_dwordx4 v230, s[8:11], s64 offen offset:2048 lds
MM a[44:47],a[156:159],v[76:79],a[44:47]
EX v44,v44
MM a[36:39],a[156:159],v[68:71],a[36:39]
s_waitcnt lgkmcnt(2)
EX v45,v45
MM a[36:39],a[152:155],v[64:67],a[36:39]
RD a[212:215],v225 offset:53248
CV v12,v40,v41
MM v[120:123],v[96:99],v[108:111],v[120:123]
MM v[120:123],v[100:103],v[112:115],v[120:123]
MM a[64:67],a[160:163],v[64:67],a[64:67]
EX v46,v46
MM a[64:67],a[164:167],v[68:71],a[64:67]
RD a[216:219],v226 offset:53248
CV v13,v42,v43
MM a[72:75],a[164:167],v[76:79],a[72:75]
EX v47,v47
MM a[72:75],a[160:163],v[72:75],a[72:75]
buffer_load_dwordx4 v231, s[8:11], s64 offen offset:3072 lds
MM a[80:83],a[160:163],v[80:83],a[80:83]
s_add_u32 m0, s65, 81920
EX v48,v48
MM a[80:83],a[164:167],v[84:87],a[80:83]
RD a[220:223],v227 offset:53248
CV v14,v44,v45
MM a[88:91],a[164:167],v[92:95],a[88:91]
s_mov_b32 exec_lo, 0x00ff00ff
s_mov_b32 exec_hi, 0x00ff00ff
buffer_store_dwordx4 v[120:123], v126, s[20:23], s76 offen offset:0 sc1
s_mov_b64 exec, -1
s_nop 0
MM a[88:91],a[160:163],v[88:91],a[88:91]
EX v49,v49
MM a[92:95],a[168:171],v[88:91],a[92:95]
EX v50,v50
MM a[92:95],a[172:175],v[92:95],a[92:95]
RD a[224:227],v224 offset:57344
CV v15,v46,v47
MM a[84:87],a[172:175],v[84:87],a[84:87]
EX v51,v51
MM a[84:87],a[168:171],v[80:83],a[84:87]
EX v52,v52
MM a[76:79],a[168:171],v[72:75],a[76:79]
buffer_load_dwordx4 v235, s[12:15], s81 offen offset:0 lds
MM a[76:79],a[172:175],v[76:79],a[76:79]
EX v53,v53
MM a[68:71],a[172:175],v[68:71],a[68:71]
RD a[228:231],v225 offset:57344
CV v20,v48,v49
MM a[68:71],a[168:171],v[64:67],a[68:71]
EX v54,v54
MM a[96:99],a[176:179],v[64:67],a[96:99]
RD a[232:235],v226 offset:57344
CV v21,v50,v51
MM a[96:99],a[180:183],v[68:71],a[96:99]
EX v55,v55
MM a[104:107],a[180:183],v[76:79],a[104:107]
RD a[236:239],v227 offset:57344
CV v22,v52,v53
MM a[104:107],a[176:179],v[72:75],a[104:107]
EX v56,v56
MM a[112:115],a[176:179],v[80:83],a[112:115]
buffer_load_dwordx4 v235, s[12:15], s64 offen offset:1024 lds
MM a[112:115],a[180:183],v[84:87],a[112:115]
EX v57,v57
MM a[120:123],a[180:183],v[92:95],a[120:123]
EX v58,v58
MM a[120:123],a[176:179],v[88:91],a[120:123]
RD a[240:243],v224 offset:61440
CV v23,v54,v55
MM a[124:127],a[184:187],v[88:91],a[124:127]
v_add_f32 v244, v144, v145
v_add_f32 v245, v148, v149
MM a[124:127],a[188:191],v[92:95],a[124:127]
v_add_f32 v246, v152, v153
v_add_f32 v247, v156, v157
MM a[116:119],a[188:191],v[84:87],a[116:119]
EX v59,v59
MM a[116:119],a[184:187],v[80:83],a[116:119]
EX v60,v60
MM a[108:111],a[184:187],v[72:75],a[108:111]
)ASM"
R"ASM(
EX v61,v61
MM a[108:111],a[188:191],v[76:79],a[108:111]
RD a[244:247],v225 offset:61440
CV v28,v56,v57
MM a[100:103],a[188:191],v[68:71],a[100:103]
buffer_load_dwordx4 v235, s[12:15], s81 offen offset:2048 lds
MM a[100:103],a[184:187],v[64:67],a[100:103]
EX v62,v62
v_smfmac_f32_16x16x64_bf16 v[144:147], v[236:239], v[0:7], v234
RD a[248:251],v226 offset:61440
RD a[252:255],v227 offset:61440
EX v63,v63
v_smfmac_f32_16x16x64_bf16 v[148:151], v[236:239], v[8:15], v234
CV v29,v58,v59
v_smfmac_f32_16x16x64_bf16 v[152:155], v[236:239], v[16:23], v234
CV v30,v60,v61
CV v31,v62,v63
s_nop 1
v_smfmac_f32_16x16x64_bf16 v[156:159], v[236:239], v[24:31], v234
s_waitcnt lgkmcnt(0)
buffer_load_dwordx4 v235, s[12:15], s64 offen offset:3072 lds
MM v[64:67],a[192:195],v[160:163],v[128:131]
s_add_u32 s64, s64, 16384
TR a[128:129],v232 offset:32768
s_add_u32 s81, s64, 896
MM v[64:67],a[196:199],v[176:179],v[64:67]
TR a[130:131],v232 offset:36864
v_max3_f32 v249, v144, v145, v148
MM v[64:67],a[200:203],v[192:195],v[64:67]
TR a[132:133],v232 offset:40960
v_max3_f32 v249, v249, v149, v152
MM v[64:67],a[204:207],v[208:211],v[64:67]
TR a[134:135],v232 offset:45056
v_max3_f32 v249, v249, v153, v156
MM v[68:71],a[220:223],v[208:211],v[128:131]
TR a[136:137],v233 offset:32768
v_max_f32 v249, v249, v157
MM v[68:71],a[216:219],v[192:195],v[68:71]
TR a[138:139],v233 offset:36864
v_cmp_lt_f32 vcc, s71, v249
MM v[68:71],a[212:215],v[176:179],v[68:71]
TR a[140:141],v233 offset:40960
TR a[142:143],v233 offset:45056
MM v[68:71],a[208:211],v[160:163],v[68:71]
s_cbranch_vccnz LSITE14_%=
LBACK14_%=:
MM v[76:79],a[208:211],v[164:167],v[132:135]
s_add_u32 s48, s48, 16
v_lshrrev_b32 v252, 6, v248
v_mul_u32_u24 v252, 3456, v252
MM v[76:79],a[212:215],v[180:183],v[76:79]
v_and_b32 v251, 48, v248
v_add_u32 v252, v252, v251
MM v[76:79],a[216:219],v[196:199],v[76:79]
s_and_b32 s90, %[swave], 1
EX v64,v64
MM v[76:79],a[220:223],v[212:215],v[76:79]
s_mul_i32 s90, s90, 276480
EX v65,v65
MM v[72:75],a[204:207],v[212:215],v[132:135]
s_bfe_u32 s91, s48, 131077
EX v66,v66
MM v[72:75],a[200:203],v[196:199],v[72:75]
s_mul_i32 s91, s91, 552960
TR a[144:145],v232 offset:33280
CV v64,v64,v65
MM v[72:75],a[196:199],v[180:183],v[72:75]
s_add_u32 s90, s90, s91
EX v67,v67
MM v[72:75],a[192:195],v[164:167],v[72:75]
s_and_b32 s91, s48, 0xffffff80
TR a[146:147],v232 offset:37376
CV v65,v66,v67
MM v[80:83],a[192:195],v[168:171],v[136:139]
s_add_u32 s90, s90, s91
EX v68,v68
MM v[80:83],a[196:199],v[184:187],v[80:83]
s_and_b32 s91, s48, 16
TR a[148:149],v232 offset:41472
TR a[150:151],v232 offset:45568
MM v[80:83],a[200:203],v[200:203],v[80:83]
s_lshl_b32 s91, s91, 2
EX v69,v69
MM v[80:83],a[204:207],v[216:219],v[80:83]
s_add_u32 s66, s90, s91
TR a[152:153],v233 offset:33280
CV v66,v68,v69
MM v[84:87],a[220:223],v[216:219],v[136:139]
s_mul_i32 s91, %[swave], 5120
EX v70,v70
MM v[84:87],a[216:219],v[200:203],v[84:87]
s_add_u32 s91, s91, %[slds]
EX v71,v71
MM v[84:87],a[212:215],v[184:187],v[84:87]
s_add_u32 s67, s91, 131072
TR a[154:155],v233 offset:37376
CV v67,v70,v71
MM v[84:87],a[208:211],v[168:171],v[84:87]
s_and_b32 s18, s17, 255
EX v72,v72
MM v[92:95],a[208:211],v[172:175],v[140:143]
s_sub_u32 s90, s18, 40
s_cmp_ge_u32 s18, 216
s_cselect_b32 s18, s90, s18
MM v[92:95],a[212:215],v[188:191],v[92:95]
s_lshl_b32 s18, s18, 4
EX v73,v73
MM v[92:95],a[216:219],v[204:207],v[92:95]
s_sub_u32 s90, s16, 90
TR a[156:157],v233 offset:41472
CV v72,v72,v73
MM v[92:95],a[220:223],v[220:223],v[92:95]
s_min_u32 s47, s90, s16
EX v74,v74
MM v[88:91],a[204:207],v[220:223],v[140:143]
s_mul_i32 s90, s47, 205
TR a[158:159],v233 offset:45568
TR a[160:161],v232 offset:33792
MM v[88:91],a[200:203],v[204:207],v[88:91]
s_lshr_b32 s46, s90, 11
EX v75,v75
MM v[88:91],a[196:199],v[188:191],v[88:91]
s_mul_i32 s90, s46, 244480
TR a[162:163],v232 offset:37888
CV v73,v74,v75
MM v[88:91],a[192:195],v[172:175],v[88:91]
s_lshl_b32 s91, s47, 7
EX v76,v76
MM v[96:99],a[224:227],v[160:163],v[128:131]
s_add_u32 s90, s90, s91
s_add_u32 s28, s28, s90
s_addc_u32 s29, s29, 0
MM v[96:99],a[228:231],v[176:179],v[96:99]
s_sub_u32 s30, 2211840, s90
EX v77,v77
MM v[96:99],a[232:235],v[192:195],v[96:99]
s_mov_b32 s31, 131072
TR a[164:165],v232 offset:41984
CV v74,v76,v77
MM v[96:99],a[236:239],v[208:211],v[96:99]
EX v78,v78
MM v[100:103],a[252:255],v[208:211],v[128:131]
TR a[166:167],v232 offset:46080
TR a[168:169],v233 offset:33792
MM v[100:103],a[248:251],v[192:195],v[100:103]
EX v79,v79
MM v[100:103],a[244:247],v[176:179],v[100:103]
TR a[170:171],v233 offset:37888
CV v75,v78,v79
MM v[100:103],a[240:243],v[160:163],v[100:103]
EX v80,v80
MM v[108:111],a[240:243],v[164:167],v[132:135]
TR a[172:173],v233 offset:41984
TR a[174:175],v233 offset:46080
MM v[108:111],a[244:247],v[180:183],v[108:111]
EX v81,v81
MM v[108:111],a[248:251],v[196:199],v[108:111]
EX v82,v82
MM v[108:111],a[252:255],v[212:215],v[108:111]
TR a[176:177],v232 offset:34304
CV v80,v80,v81
MM v[104:107],a[236:239],v[212:215],v[132:135]
EX v83,v83
MM v[104:107],a[232:235],v[196:199],v[104:107]
TR a[178:179],v232 offset:38400
CV v81,v82,v83
MM v[104:107],a[228:231],v[180:183],v[104:107]
EX v84,v84
MM v[104:107],a[224:227],v[164:167],v[104:107]
TR a[180:181],v232 offset:42496
TR a[182:183],v232 offset:46592
MM v[112:115],a[224:227],v[168:171],v[136:139]
EX v85,v85
MM v[112:115],a[228:231],v[184:187],v[112:115]
TR a[184:185],v233 offset:34304
CV v82,v84,v85
MM v[112:115],a[232:235],v[200:203],v[112:115]
EX v86,v86
MM v[112:115],a[236:239],v[216:219],v[112:115]
EX v87,v87
MM v[116:119],a[252:255],v[216:219],v[136:139]
TR a[186:187],v233 offset:38400
CV v83,v86,v87
MM v[116:119],a[248:251],v[200:203],v[116:119]
EX v88,v88
MM v[116:119],a[244:247],v[184:187],v[116:119]
TR a[188:189],v233 offset:42496
TR a[190:191],v233 offset:46592
MM v[116:119],a[240:243],v[168:171],v[116:119]
EX v89,v89
MM v[124:127],a[240:243],v[172:175],v[140:143]
EX v90,v90
MM v[124:127],a[244:247],v[188:191],v[124:127]
EX v91,v91
MM v[124:127],a[248:251],v[204:207],v[124:127]
EX v92,v92
MM v[124:127],a[252:255],v[220:223],v[124:127]
EX v93,v93
MM v[120:123],a[236:239],v[220:223],v[140:143]
EX v94,v94
MM v[120:123],a[232:235],v[204:207],v[120:123]
EX v95,v95
MM v[120:123],a[228:231],v[188:191],v[120:123]
EX v96,v96
MM v[120:123],a[224:227],v[172:175],v[120:123]
s_waitcnt vmcnt(14) lgkmcnt(0)
s_barrier
MM a[0:3],a[128:131],v[0:3],a[0:3]
s_add_u32 m0, s65, 32768
CV v88,v88,v89
CV v89,v90,v91
MM a[0:3],a[132:135],v[4:7],a[0:3]
RD a[192:195],v224 offset:0
CV v90,v92,v93
MM a[8:11],a[132:135],v[12:15],a[8:11]
RD a[196:199],v225 offset:0
CV v91,v94,v95
MM a[8:11],a[128:131],v[8:11],a[8:11]
buffer_load_dwordx4 v228, s[8:11], s64 offen offset:0 lds
MM a[16:19],a[128:131],v[16:19],a[16:19]
RD a[200:203],v226 offset:0
RD a[204:207],v227 offset:0
MM a[16:19],a[132:135],v[20:23],a[16:19]
EX v97,v97
MM a[24:27],a[132:135],v[28:31],a[24:27]
RD a[208:211],v224 offset:4096
CV v68,v96,v97
MM a[24:27],a[128:131],v[24:27],a[24:27]
EX v98,v98
MM a[28:31],a[136:139],v[24:27],a[28:31]
buffer_load_dwordx4 v229, s[8:11], s64 offen offset:1024 lds
MM a[28:31],a[140:143],v[28:31],a[28:31]
EX v99,v99
MM a[20:23],a[140:143],v[20:23],a[20:23]
RD a[212:215],v225 offset:4096
CV v69,v98,v99
MM a[20:23],a[136:139],v[16:19],a[20:23]
EX v100,v100
MM a[12:15],a[136:139],v[8:11],a[12:15]
EX v101,v101
MM a[12:15],a[140:143],v[12:15],a[12:15]
RD a[216:219],v226 offset:4096
CV v70,v100,v101
MM a[4:7],a[140:143],v[4:7],a[4:7]
EX v102,v102
MM a[4:7],a[136:139],v[0:3],a[4:7]
buffer_load_dwordx4 v230, s[8:11], s64 offen offset:2048 lds
MM a[32:35],a[144:147],v[0:3],a[32:35]
EX v103,v103
MM a[32:35],a[148:151],v[4:7],a[32:35]
RD a[220:223],v227 offset:4096
CV v71,v102,v103
MM a[40:43],a[148:151],v[12:15],a[40:43]
EX v104,v104
MM a[40:43],a[144:147],v[8:11],a[40:43]
EX v105,v105
MM a[48:51],a[144:147],v[16:19],a[48:51]
RD a[224:227],v224 offset:8192
CV v76,v104,v105
MM a[48:51],a[148:151],v[20:23],a[48:51]
EX v106,v106
MM a[56:59],a[148:151],v[28:31],a[56:59]
buffer_load_dwordx4 v231, s[8:11], s64 offen offset:3072 lds
MM a[56:59],a[144:147],v[24:27],a[56:59]
s_add_u32 m0, s65, 98304
EX v107,v107
MM a[60:63],a[152:155],v[24:27],a[60:63]
RD a[228:231],v225 offset:8192
CV v77,v106,v107
MM a[60:63],a[156:159],v[28:31],a[60:63]
EX v108,v108
MM a[52:55],a[156:159],v[20:23],a[52:55]
EX v109,v109
MM a[52:55],a[152:155],v[16:19],a[52:55]
RD a[232:235],v226 offset:8192
CV v78,v108,v109
MM a[44:47],a[152:155],v[8:11],a[44:47]
EX v110,v110
MM a[44:47],a[156:159],v[12:15],a[44:47]
buffer_load_dwordx4 v235, s[12:15], s81 offen offset:0 lds
MM a[36:39],a[156:159],v[4:7],a[36:39]
EX v111,v111
MM a[36:39],a[152:155],v[0:3],a[36:39]
RD a[236:239],v227 offset:8192
CV v79,v110,v111
MM a[64:67],a[160:163],v[0:3],a[64:67]
EX v112,v112
MM a[64:67],a[164:167],v[4:7],a[64:67]
EX v113,v113
MM a[72:75],a[164:167],v[12:15],a[72:75]
RD a[240:243],v224 offset:12288
CV v84,v112,v113
MM a[72:75],a[160:163],v[8:11],a[72:75]
buffer_load_dwordx4 v235, s[12:15], s64 offen offset:1024 lds
MM a[80:83],a[160:163],v[16:19],a[80:83]
s_waitcnt vmcnt(10)
v_mov_b32 v250, s69
s_mov_b64 exec, 1
buffer_store_dword v250, off, s[20:23], s97 offset:0 sc1
s_mov_b64 exec, -1
MM a[80:83],a[164:167],v[20:23],a[80:83]
s_add_u32 m0, s67, 0
s_add_u32 s85, s66, 0
buffer_load_dwordx4 v252, s[36:39], s85 offen offset:0 lds
MM a[88:91],a[164:167],v[28:31],a[88:91]
EX v114,v114
MM a[88:91],a[160:163],v[24:27],a[88:91]
EX v115,v115
MM a[92:95],a[168:171],v[24:27],a[92:95]
RD a[244:247],v225 offset:12288
CV v85,v114,v115
MM a[92:95],a[172:175],v[28:31],a[92:95]
EX v116,v116
MM a[84:87],a[172:175],v[20:23],a[84:87]
EX v117,v117
MM a[84:87],a[168:171],v[16:19],a[84:87]
RD a[248:251],v226 offset:12288
CV v86,v116,v117
MM a[76:79],a[168:171],v[8:11],a[76:79]
EX v118,v118
MM a[76:79],a[172:175],v[12:15],a[76:79]
EX v119,v119
MM a[68:71],a[172:175],v[4:7],a[68:71]
RD a[252:255],v227 offset:12288
CV v87,v118,v119
MM a[68:71],a[168:171],v[0:3],a[68:71]
s_add_u32 m0, s65, 98304
s_nop 0
buffer_load_dwordx4 v235, s[12:15], s81 offen offset:2048 lds
MM a[96:99],a[176:179],v[0:3],a[96:99]
EX v120,v120
MM a[96:99],a[180:183],v[4:7],a[96:99]
EX v121,v121
MM a[104:107],a[180:183],v[12:15],a[104:107]
EX v122,v122
MM a[104:107],a[176:179],v[8:11],a[104:107]
s_nop 0
LPG13_%=:
s_cbranch_execz LTR2_%=
buffer_load_dwordx4 v235, s[12:15], s64 offen offset:3072 lds
MM a[112:115],a[176:179],v[16:19],a[112:115]
EX v123,v123
MM a[112:115],a[180:183],v[20:23],a[112:115]
EX v124,v124
MM a[120:123],a[180:183],v[28:31],a[120:123]
EX v125,v125
MM a[120:123],a[176:179],v[24:27],a[120:123]
EX v126,v126
MM a[124:127],a[184:187],v[24:27],a[124:127]
v_add_f32 v244, v144, v145
v_add_f32 v245, v148, v149
MM a[124:127],a[188:191],v[28:31],a[124:127]
v_add_f32 v246, v152, v153
v_add_f32 v247, v156, v157
MM a[116:119],a[188:191],v[20:23],a[116:119]
EX v127,v127
MM a[116:119],a[184:187],v[16:19],a[116:119]
CV v92,v120,v121
CV v93,v122,v123
MM a[108:111],a[184:187],v[8:11],a[108:111]
CV v94,v124,v125
CV v95,v126,v127
MM a[108:111],a[188:191],v[12:15],a[108:111]
MM a[100:103],a[188:191],v[4:7],a[100:103]
MM a[100:103],a[184:187],v[0:3],a[100:103]
v_smfmac_f32_16x16x64_bf16 v[144:147], v[236:239], v[64:71], v234
v_smfmac_f32_16x16x64_bf16 v[148:151], v[236:239], v[72:79], v234
v_smfmac_f32_16x16x64_bf16 v[152:155], v[236:239], v[80:87], v234
v_smfmac_f32_16x16x64_bf16 v[156:159], v[236:239], v[88:95], v234
s_waitcnt lgkmcnt(0)
MM v[0:3],a[192:195],v[160:163],v[128:131]
s_add_u32 s64, s64, 16384
TR a[128:129],v232 offset:49152
s_add_u32 s81, s64, 896
MM v[0:3],a[196:199],v[176:179],v[0:3]
TR a[130:131],v232 offset:53248
v_max3_f32 v249, v144, v145, v148
MM v[0:3],a[200:203],v[192:195],v[0:3]
TR a[132:133],v232 offset:57344
v_max3_f32 v249, v249, v149, v152
MM v[0:3],a[204:207],v[208:211],v[0:3]
TR a[134:135],v232 offset:61440
v_max3_f32 v249, v249, v153, v156
MM v[4:7],a[220:223],v[208:211],v[128:131]
TR a[136:137],v233 offset:49152
v_max_f32 v249, v249, v157
MM v[4:7],a[216:219],v[192:195],v[4:7]
TR a[138:139],v233 offset:53248
)ASM"
R"ASM(
v_cmp_lt_f32 vcc, s71, v249
MM v[4:7],a[212:215],v[176:179],v[4:7]
TR a[140:141],v233 offset:57344
TR a[142:143],v233 offset:61440
MM v[4:7],a[208:211],v[160:163],v[4:7]
s_cbranch_vccnz LSITE15_%=
LBACK15_%=:
MM v[12:15],a[208:211],v[164:167],v[132:135]
s_add_u32 m0, s67, 1024
s_add_u32 s85, s66, 55296
buffer_load_dwordx4 v252, s[36:39], s85 offen offset:0 lds
MM v[12:15],a[212:215],v[180:183],v[12:15]
EX v0,v0
MM v[12:15],a[216:219],v[196:199],v[12:15]
EX v1,v1
MM v[12:15],a[220:223],v[212:215],v[12:15]
TR a[144:145],v232 offset:49664
CV v0,v0,v1
MM v[8:11],a[204:207],v[212:215],v[132:135]
EX v2,v2
MM v[8:11],a[200:203],v[196:199],v[8:11]
EX v3,v3
MM v[8:11],a[196:199],v[180:183],v[8:11]
TR a[146:147],v232 offset:53760
CV v1,v2,v3
MM v[8:11],a[192:195],v[164:167],v[8:11]
EX v4,v4
MM v[16:19],a[192:195],v[168:171],v[136:139]
TR a[148:149],v232 offset:57856
TR a[150:151],v232 offset:61952
MM v[16:19],a[196:199],v[184:187],v[16:19]
EX v5,v5
MM v[16:19],a[200:203],v[200:203],v[16:19]
TR a[152:153],v233 offset:49664
CV v2,v4,v5
MM v[16:19],a[204:207],v[216:219],v[16:19]
EX v6,v6
MM v[20:23],a[220:223],v[216:219],v[136:139]
EX v7,v7
MM v[20:23],a[216:219],v[200:203],v[20:23]
TR a[154:155],v233 offset:53760
CV v3,v6,v7
MM v[20:23],a[212:215],v[184:187],v[20:23]
EX v8,v8
MM v[20:23],a[208:211],v[168:171],v[20:23]
TR a[156:157],v233 offset:57856
TR a[158:159],v233 offset:61952
MM v[28:31],a[208:211],v[172:175],v[140:143]
EX v9,v9
MM v[28:31],a[212:215],v[188:191],v[28:31]
TR a[160:161],v232 offset:50176
CV v8,v8,v9
MM v[28:31],a[216:219],v[204:207],v[28:31]
EX v10,v10
MM v[28:31],a[220:223],v[220:223],v[28:31]
EX v11,v11
MM v[24:27],a[204:207],v[220:223],v[140:143]
TR a[162:163],v232 offset:54272
CV v9,v10,v11
MM v[24:27],a[200:203],v[204:207],v[24:27]
TR a[164:165],v232 offset:58368
TR a[166:167],v232 offset:62464
MM v[24:27],a[196:199],v[188:191],v[24:27]
EX v12,v12
MM v[24:27],a[192:195],v[172:175],v[24:27]
EX v13,v13
MM v[32:35],a[224:227],v[160:163],v[128:131]
TR a[168:169],v233 offset:50176
CV v10,v12,v13
MM v[32:35],a[228:231],v[176:179],v[32:35]
EX v14,v14
MM v[32:35],a[232:235],v[192:195],v[32:35]
TR a[170:171],v233 offset:54272
TR a[172:173],v233 offset:58368
MM v[32:35],a[236:239],v[208:211],v[32:35]
EX v15,v15
MM v[36:39],a[252:255],v[208:211],v[128:131]
TR a[174:175],v233 offset:62464
CV v11,v14,v15
MM v[36:39],a[248:251],v[192:195],v[36:39]
EX v16,v16
MM v[36:39],a[244:247],v[176:179],v[36:39]
EX v17,v17
MM v[36:39],a[240:243],v[160:163],v[36:39]
TR a[176:177],v232 offset:50688
CV v16,v16,v17
MM v[44:47],a[240:243],v[164:167],v[132:135]
s_add_u32 m0, s67, 2048
s_add_u32 s85, s66, 110592
buffer_load_dwordx4 v252, s[36:39], s85 offen offset:0 lds
MM v[44:47],a[244:247],v[180:183],v[44:47]
EX v18,v18
MM v[44:47],a[248:251],v[196:199],v[44:47]
TR a[178:179],v232 offset:54784
TR a[180:181],v232 offset:58880
MM v[44:47],a[252:255],v[212:215],v[44:47]
EX v19,v19
MM v[40:43],a[236:239],v[212:215],v[132:135]
TR a[182:183],v232 offset:62976
CV v17,v18,v19
MM v[40:43],a[232:235],v[196:199],v[40:43]
EX v20,v20
MM v[40:43],a[228:231],v[180:183],v[40:43]
EX v21,v21
MM v[40:43],a[224:227],v[164:167],v[40:43]
TR a[184:185],v233 offset:50688
CV v18,v20,v21
MM v[48:51],a[224:227],v[168:171],v[136:139]
EX v22,v22
MM v[48:51],a[228:231],v[184:187],v[48:51]
TR a[186:187],v233 offset:54784
TR a[188:189],v233 offset:58880
MM v[48:51],a[232:235],v[200:203],v[48:51]
EX v23,v23
MM v[48:51],a[236:239],v[216:219],v[48:51]
TR a[190:191],v233 offset:62976
CV v19,v22,v23
MM v[52:55],a[252:255],v[216:219],v[136:139]
EX v24,v24
MM v[52:55],a[248:251],v[200:203],v[52:55]
EX v25,v25
MM v[52:55],a[244:247],v[184:187],v[52:55]
EX v26,v26
MM v[52:55],a[240:243],v[168:171],v[52:55]
EX v27,v27
MM v[60:63],a[240:243],v[172:175],v[140:143]
EX v28,v28
MM v[60:63],a[244:247],v[188:191],v[60:63]
EX v29,v29
MM v[60:63],a[248:251],v[204:207],v[60:63]
EX v30,v30
MM v[60:63],a[252:255],v[220:223],v[60:63]
EX v31,v31
MM v[56:59],a[236:239],v[220:223],v[140:143]
EX v32,v32
MM v[56:59],a[232:235],v[204:207],v[56:59]
EX v33,v33
MM v[56:59],a[228:231],v[188:191],v[56:59]
EX v34,v34
MM v[56:59],a[224:227],v[172:175],v[56:59]
s_waitcnt lgkmcnt(0)
s_barrier
MM a[0:3],a[128:131],v[64:67],a[0:3]
s_add_u32 m0, s65, 49152
CV v24,v24,v25
CV v25,v26,v27
MM a[0:3],a[132:135],v[68:71],a[0:3]
RD a[192:195],v224 offset:16384
CV v26,v28,v29
MM a[8:11],a[132:135],v[76:79],a[8:11]
RD a[196:199],v225 offset:16384
CV v27,v30,v31
MM a[8:11],a[128:131],v[72:75],a[8:11]
buffer_load_dwordx4 v228, s[8:11], s64 offen offset:0 lds
MM a[16:19],a[128:131],v[80:83],a[16:19]
s_add_u32 m0, s67, 3072
s_add_u32 s85, s66, 165888
buffer_load_dwordx4 v252, s[36:39], s85 offen offset:0 lds
MM a[16:19],a[132:135],v[84:87],a[16:19]
RD a[200:203],v226 offset:16384
CV v4,v32,v33
MM a[24:27],a[132:135],v[92:95],a[24:27]
RD a[204:207],v227 offset:16384
RD a[208:211],v224 offset:20480
MM a[24:27],a[128:131],v[88:91],a[24:27]
s_add_u32 m0, s65, 49152
s_nop 0
buffer_load_dwordx4 v229, s[8:11], s64 offen offset:1024 lds
MM a[28:31],a[136:139],v[88:91],a[28:31]
EX v35,v35
MM a[28:31],a[140:143],v[92:95],a[28:31]
RD a[212:215],v225 offset:20480
CV v5,v34,v35
MM a[20:23],a[140:143],v[84:87],a[20:23]
EX v36,v36
MM a[20:23],a[136:139],v[80:83],a[20:23]
EX v37,v37
MM a[12:15],a[136:139],v[72:75],a[12:15]
RD a[216:219],v226 offset:20480
CV v6,v36,v37
MM a[12:15],a[140:143],v[76:79],a[12:15]
buffer_load_dwordx4 v230, s[8:11], s64 offen offset:2048 lds
MM a[4:7],a[140:143],v[68:71],a[4:7]
EX v38,v38
MM a[4:7],a[136:139],v[64:67],a[4:7]
EX v39,v39
MM a[32:35],a[144:147],v[64:67],a[32:35]
RD a[220:223],v227 offset:20480
CV v7,v38,v39
MM a[32:35],a[148:151],v[68:71],a[32:35]
EX v40,v40
MM a[40:43],a[148:151],v[76:79],a[40:43]
EX v41,v41
MM a[40:43],a[144:147],v[72:75],a[40:43]
RD a[224:227],v224 offset:24576
CV v12,v40,v41
MM a[48:51],a[144:147],v[80:83],a[48:51]
buffer_load_dwordx4 v231, s[8:11], s64 offen offset:3072 lds
MM a[48:51],a[148:151],v[84:87],a[48:51]
s_add_u32 m0, s65, 114688
EX v42,v42
MM a[56:59],a[148:151],v[92:95],a[56:59]
EX v43,v43
MM a[56:59],a[144:147],v[88:91],a[56:59]
RD a[228:231],v225 offset:24576
CV v13,v42,v43
MM a[60:63],a[152:155],v[88:91],a[60:63]
EX v44,v44
MM a[60:63],a[156:159],v[92:95],a[60:63]
buffer_load_dwordx4 v235, s[12:15], s81 offen offset:0 lds
MM a[52:55],a[156:159],v[84:87],a[52:55]
EX v45,v45
MM a[52:55],a[152:155],v[80:83],a[52:55]
RD a[232:235],v226 offset:24576
CV v14,v44,v45
MM a[44:47],a[152:155],v[72:75],a[44:47]
EX v46,v46
MM a[44:47],a[156:159],v[76:79],a[44:47]
EX v47,v47
MM a[36:39],a[156:159],v[68:71],a[36:39]
RD a[236:239],v227 offset:24576
CV v15,v46,v47
MM a[36:39],a[152:155],v[64:67],a[36:39]
EX v48,v48
MM a[64:67],a[160:163],v[64:67],a[64:67]
buffer_load_dwordx4 v235, s[12:15], s64 offen offset:1024 lds
MM a[64:67],a[164:167],v[68:71],a[64:67]
EX v49,v49
MM a[72:75],a[164:167],v[76:79],a[72:75]
RD a[240:243],v224 offset:28672
CV v20,v48,v49
MM a[72:75],a[160:163],v[72:75],a[72:75]
EX v50,v50
MM a[80:83],a[160:163],v[80:83],a[80:83]
EX v51,v51
MM a[80:83],a[164:167],v[84:87],a[80:83]
RD a[244:247],v225 offset:28672
CV v21,v50,v51
MM a[88:91],a[164:167],v[92:95],a[88:91]
EX v52,v52
MM a[88:91],a[160:163],v[88:91],a[88:91]
buffer_load_dwordx4 v235, s[12:15], s81 offen offset:2048 lds
MM a[92:95],a[168:171],v[88:91],a[92:95]
EX v53,v53
MM a[92:95],a[172:175],v[92:95],a[92:95]
RD a[248:251],v226 offset:28672
CV v22,v52,v53
MM a[84:87],a[172:175],v[84:87],a[84:87]
EX v54,v54
MM a[84:87],a[168:171],v[80:83],a[84:87]
EX v55,v55
MM a[76:79],a[168:171],v[72:75],a[76:79]
RD a[252:255],v227 offset:28672
CV v23,v54,v55
MM a[76:79],a[172:175],v[76:79],a[76:79]
EX v56,v56
MM a[68:71],a[172:175],v[68:71],a[68:71]
buffer_load_dwordx4 v235, s[12:15], s64 offen offset:3072 lds
MM a[68:71],a[168:171],v[64:67],a[68:71]
EX v57,v57
MM a[96:99],a[176:179],v[64:67],a[96:99]
EX v58,v58
MM a[96:99],a[180:183],v[68:71],a[96:99]
EX v59,v59
MM a[104:107],a[180:183],v[76:79],a[104:107]
EX v60,v60
MM a[104:107],a[176:179],v[72:75],a[104:107]
EX v61,v61
MM a[112:115],a[176:179],v[80:83],a[112:115]
EX v62,v62
MM a[112:115],a[180:183],v[84:87],a[112:115]
EX v63,v63
MM a[120:123],a[180:183],v[92:95],a[120:123]
s_add_u32 m0, s67, 4096
s_add_u32 s85, s66, 221184
buffer_load_dwordx4 v252, s[36:39], s85 offen offset:0 lds
MM a[120:123],a[176:179],v[88:91],a[120:123]
CV v28,v56,v57
CV v29,v58,v59
MM a[124:127],a[184:187],v[88:91],a[124:127]
v_add_f32 v244, v144, v145
v_add_f32 v245, v148, v149
MM a[124:127],a[188:191],v[92:95],a[124:127]
v_add_f32 v246, v152, v153
v_add_f32 v247, v156, v157
MM a[116:119],a[188:191],v[84:87],a[116:119]
CV v30,v60,v61
CV v31,v62,v63
MM a[116:119],a[184:187],v[80:83],a[116:119]
MM a[108:111],a[184:187],v[72:75],a[108:111]
MM a[108:111],a[188:191],v[76:79],a[108:111]
MM a[100:103],a[188:191],v[68:71],a[100:103]
MM a[100:103],a[184:187],v[64:67],a[100:103]
v_smfmac_f32_16x16x64_bf16 v[144:147], v[236:239], v[0:7], v234
v_smfmac_f32_16x16x64_bf16 v[148:151], v[236:239], v[8:15], v234
v_smfmac_f32_16x16x64_bf16 v[152:155], v[236:239], v[16:23], v234
v_smfmac_f32_16x16x64_bf16 v[156:159], v[236:239], v[24:31], v234
s_waitcnt lgkmcnt(0)
MM v[64:67],a[192:195],v[160:163],v[128:131]
s_add_u32 s64, s64, 16384
TR a[128:129],v232 offset:0
s_add_u32 s81, s64, 896
MM v[64:67],a[196:199],v[176:179],v[64:67]
TR a[130:131],v232 offset:4096
v_max3_f32 v249, v144, v145, v148
MM v[64:67],a[200:203],v[192:195],v[64:67]
TR a[132:133],v232 offset:8192
v_max3_f32 v249, v249, v149, v152
MM v[64:67],a[204:207],v[208:211],v[64:67]
TR a[134:135],v232 offset:12288
v_max3_f32 v249, v249, v153, v156
MM v[68:71],a[220:223],v[208:211],v[128:131]
TR a[136:137],v233 offset:0
v_max_f32 v249, v249, v157
MM v[68:71],a[216:219],v[192:195],v[68:71]
TR a[138:139],v233 offset:4096
v_cmp_lt_f32 vcc, s71, v249
MM v[68:71],a[212:215],v[176:179],v[68:71]
TR a[140:141],v233 offset:8192
TR a[142:143],v233 offset:12288
MM v[68:71],a[208:211],v[160:163],v[68:71]
s_cbranch_vccnz LSITE16_%=
LBACK16_%=:
MM v[76:79],a[208:211],v[164:167],v[132:135]
EX v64,v64
MM v[76:79],a[212:215],v[180:183],v[76:79]
EX v65,v65
MM v[76:79],a[216:219],v[196:199],v[76:79]
TR a[144:145],v232 offset:512
CV v64,v64,v65
MM v[76:79],a[220:223],v[212:215],v[76:79]
EX v66,v66
MM v[72:75],a[204:207],v[212:215],v[132:135]
EX v67,v67
MM v[72:75],a[200:203],v[196:199],v[72:75]
TR a[146:147],v232 offset:4608
CV v65,v66,v67
MM v[72:75],a[196:199],v[180:183],v[72:75]
EX v68,v68
MM v[72:75],a[192:195],v[164:167],v[72:75]
TR a[148:149],v232 offset:8704
TR a[150:151],v232 offset:12800
MM v[80:83],a[192:195],v[168:171],v[136:139]
EX v69,v69
MM v[80:83],a[196:199],v[184:187],v[80:83]
TR a[152:153],v233 offset:512
CV v66,v68,v69
MM v[80:83],a[200:203],v[200:203],v[80:83]
EX v70,v70
MM v[80:83],a[204:207],v[216:219],v[80:83]
TR a[154:155],v233 offset:4608
TR a[156:157],v233 offset:8704
MM v[84:87],a[220:223],v[216:219],v[136:139]
s_add_u32 s87, s95, 1024
buffer_store_dword v243, v248, s[20:23], s87 offen offset:12 sc1
MM v[84:87],a[216:219],v[200:203],v[84:87]
EX v71,v71
MM v[84:87],a[212:215],v[184:187],v[84:87]
TR a[158:159],v233 offset:12800
CV v67,v70,v71
MM v[84:87],a[208:211],v[168:171],v[84:87]
EX v72,v72
MM v[92:95],a[208:211],v[172:175],v[140:143]
EX v73,v73
MM v[92:95],a[212:215],v[188:191],v[92:95]
TR a[160:161],v232 offset:1024
CV v72,v72,v73
MM v[92:95],a[216:219],v[204:207],v[92:95]
EX v74,v74
MM v[92:95],a[220:223],v[220:223],v[92:95]
EX v75,v75
MM v[88:91],a[204:207],v[220:223],v[140:143]
TR a[162:163],v232 offset:5120
CV v73,v74,v75
MM v[88:91],a[200:203],v[204:207],v[88:91]
TR a[164:165],v232 offset:9216
TR a[166:167],v232 offset:13312
MM v[88:91],a[196:199],v[188:191],v[88:91]
EX v76,v76
MM v[88:91],a[192:195],v[172:175],v[88:91]
EX v77,v77
MM v[96:99],a[224:227],v[160:163],v[128:131]
TR a[168:169],v233 offset:1024
CV v74,v76,v77
MM v[96:99],a[228:231],v[176:179],v[96:99]
EX v78,v78
MM v[96:99],a[232:235],v[192:195],v[96:99]
TR a[170:171],v233 offset:5120
TR a[172:173],v233 offset:9216
MM v[96:99],a[236:239],v[208:211],v[96:99]
)ASM"
R"ASM(
EX v79,v79
MM v[100:103],a[252:255],v[208:211],v[128:131]
TR a[174:175],v233 offset:13312
CV v75,v78,v79
MM v[100:103],a[248:251],v[192:195],v[100:103]
EX v80,v80
MM v[100:103],a[244:247],v[176:179],v[100:103]
EX v81,v81
MM v[100:103],a[240:243],v[160:163],v[100:103]
TR a[176:177],v232 offset:1536
CV v80,v80,v81
MM v[108:111],a[240:243],v[164:167],v[132:135]
EX v82,v82
MM v[108:111],a[244:247],v[180:183],v[108:111]
TR a[178:179],v232 offset:5632
TR a[180:181],v232 offset:9728
MM v[108:111],a[248:251],v[196:199],v[108:111]
EX v83,v83
MM v[108:111],a[252:255],v[212:215],v[108:111]
TR a[182:183],v232 offset:13824
CV v81,v82,v83
MM v[104:107],a[236:239],v[212:215],v[132:135]
EX v84,v84
MM v[104:107],a[232:235],v[196:199],v[104:107]
EX v85,v85
MM v[104:107],a[228:231],v[180:183],v[104:107]
TR a[184:185],v233 offset:1536
CV v82,v84,v85
MM v[104:107],a[224:227],v[164:167],v[104:107]
EX v86,v86
MM v[112:115],a[224:227],v[168:171],v[136:139]
TR a[186:187],v233 offset:5632
TR a[188:189],v233 offset:9728
MM v[112:115],a[228:231],v[184:187],v[112:115]
EX v87,v87
MM v[112:115],a[232:235],v[200:203],v[112:115]
TR a[190:191],v233 offset:13824
CV v83,v86,v87
MM v[112:115],a[236:239],v[216:219],v[112:115]
EX v88,v88
MM v[116:119],a[252:255],v[216:219],v[136:139]
EX v89,v89
MM v[116:119],a[248:251],v[200:203],v[116:119]
EX v90,v90
MM v[116:119],a[244:247],v[184:187],v[116:119]
EX v91,v91
MM v[116:119],a[240:243],v[168:171],v[116:119]
EX v92,v92
MM v[124:127],a[240:243],v[172:175],v[140:143]
EX v93,v93
MM v[124:127],a[244:247],v[188:191],v[124:127]
EX v94,v94
MM v[124:127],a[248:251],v[204:207],v[124:127]
EX v95,v95
MM v[124:127],a[252:255],v[220:223],v[124:127]
EX v96,v96
MM v[120:123],a[236:239],v[220:223],v[140:143]
EX v97,v97
MM v[120:123],a[232:235],v[204:207],v[120:123]
EX v98,v98
MM v[120:123],a[228:231],v[188:191],v[120:123]
EX v99,v99
MM v[120:123],a[224:227],v[172:175],v[120:123]
s_waitcnt vmcnt(19) lgkmcnt(0)
s_barrier
MM a[0:3],a[128:131],v[0:3],a[0:3]
s_add_u32 m0, s65, 0
CV v88,v88,v89
CV v89,v90,v91
MM a[0:3],a[132:135],v[4:7],a[0:3]
RD a[192:195],v224 offset:32768
CV v90,v92,v93
MM a[8:11],a[132:135],v[12:15],a[8:11]
RD a[196:199],v225 offset:32768
CV v91,v94,v95
MM a[8:11],a[128:131],v[8:11],a[8:11]
buffer_load_dwordx4 v228, s[8:11], s64 offen offset:0 lds
MM a[16:19],a[128:131],v[16:19],a[16:19]
RD a[200:203],v226 offset:32768
CV v68,v96,v97
MM a[16:19],a[132:135],v[20:23],a[16:19]
RD a[204:207],v227 offset:32768
CV v69,v98,v99
MM a[24:27],a[132:135],v[28:31],a[24:27]
buffer_load_dwordx4 v229, s[8:11], s64 offen offset:1024 lds
MM a[24:27],a[128:131],v[24:27],a[24:27]
RD a[208:211],v224 offset:36864
RD a[212:215],v225 offset:36864
MM a[28:31],a[136:139],v[24:27],a[28:31]
buffer_load_dwordx4 v230, s[8:11], s64 offen offset:2048 lds
MM a[28:31],a[140:143],v[28:31],a[28:31]
EX v100,v100
MM a[20:23],a[140:143],v[20:23],a[20:23]
EX v101,v101
MM a[20:23],a[136:139],v[16:19],a[20:23]
RD a[216:219],v226 offset:36864
CV v70,v100,v101
MM a[12:15],a[136:139],v[8:11],a[12:15]
EX v102,v102
MM a[12:15],a[140:143],v[12:15],a[12:15]
RD a[220:223],v227 offset:36864
RD a[224:227],v224 offset:40960
MM a[4:7],a[140:143],v[4:7],a[4:7]
EX v103,v103
MM a[4:7],a[136:139],v[0:3],a[4:7]
RD a[228:231],v225 offset:40960
CV v71,v102,v103
MM a[32:35],a[144:147],v[0:3],a[32:35]
EX v104,v104
MM a[32:35],a[148:151],v[4:7],a[32:35]
buffer_load_dwordx4 v231, s[8:11], s64 offen offset:3072 lds
MM a[40:43],a[148:151],v[12:15],a[40:43]
s_add_u32 m0, s65, 65536
EX v105,v105
MM a[40:43],a[144:147],v[8:11],a[40:43]
RD a[232:235],v226 offset:40960
CV v76,v104,v105
MM a[48:51],a[144:147],v[16:19],a[48:51]
EX v106,v106
MM a[48:51],a[148:151],v[20:23],a[48:51]
EX v107,v107
MM a[56:59],a[148:151],v[28:31],a[56:59]
RD a[236:239],v227 offset:40960
CV v77,v106,v107
MM a[56:59],a[144:147],v[24:27],a[56:59]
EX v108,v108
MM a[60:63],a[152:155],v[24:27],a[60:63]
buffer_load_dwordx4 v235, s[12:15], s81 offen offset:0 lds
MM a[60:63],a[156:159],v[28:31],a[60:63]
EX v109,v109
MM a[52:55],a[156:159],v[20:23],a[52:55]
s_add_u32 s87, s95, 2048
buffer_store_dword v243, v248, s[20:23], s87 offen offset:12 sc1
MM a[52:55],a[152:155],v[16:19],a[52:55]
RD a[240:243],v224 offset:45056
CV v78,v108,v109
MM a[44:47],a[152:155],v[8:11],a[44:47]
EX v110,v110
MM a[44:47],a[156:159],v[12:15],a[44:47]
EX v111,v111
MM a[36:39],a[156:159],v[4:7],a[36:39]
RD a[244:247],v225 offset:45056
CV v79,v110,v111
MM a[36:39],a[152:155],v[0:3],a[36:39]
LPG14_%=:
s_cbranch_execz LTR2_%=
EX v112,v112
MM a[64:67],a[160:163],v[0:3],a[64:67]
buffer_load_dwordx4 v235, s[12:15], s64 offen offset:1024 lds
MM a[64:67],a[164:167],v[4:7],a[64:67]
EX v113,v113
MM a[72:75],a[164:167],v[12:15],a[72:75]
RD a[248:251],v226 offset:45056
CV v84,v112,v113
MM a[72:75],a[160:163],v[8:11],a[72:75]
EX v114,v114
MM a[80:83],a[160:163],v[16:19],a[80:83]
EX v115,v115
MM a[80:83],a[164:167],v[20:23],a[80:83]
RD a[252:255],v227 offset:45056
CV v85,v114,v115
MM a[88:91],a[164:167],v[28:31],a[88:91]
EX v116,v116
MM a[88:91],a[160:163],v[24:27],a[88:91]
buffer_load_dwordx4 v235, s[12:15], s81 offen offset:2048 lds
MM a[92:95],a[168:171],v[24:27],a[92:95]
EX v117,v117
MM a[92:95],a[172:175],v[28:31],a[92:95]
EX v118,v118
MM a[84:87],a[172:175],v[20:23],a[84:87]
buffer_load_dwordx4 v235, s[12:15], s64 offen offset:3072 lds
MM a[84:87],a[168:171],v[16:19],a[84:87]
EX v119,v119
MM a[76:79],a[168:171],v[8:11],a[76:79]
EX v120,v120
MM a[76:79],a[172:175],v[12:15],a[76:79]
EX v121,v121
MM a[68:71],a[172:175],v[4:7],a[68:71]
EX v122,v122
MM a[68:71],a[168:171],v[0:3],a[68:71]
EX v123,v123
MM a[96:99],a[176:179],v[0:3],a[96:99]
EX v124,v124
MM a[96:99],a[180:183],v[4:7],a[96:99]
EX v125,v125
MM a[104:107],a[180:183],v[12:15],a[104:107]
EX v126,v126
MM a[104:107],a[176:179],v[8:11],a[104:107]
EX v127,v127
MM a[112:115],a[176:179],v[16:19],a[112:115]
CV v86,v116,v117
CV v87,v118,v119
MM a[112:115],a[180:183],v[20:23],a[112:115]
CV v92,v120,v121
CV v93,v122,v123
MM a[120:123],a[180:183],v[28:31],a[120:123]
CV v94,v124,v125
CV v95,v126,v127
MM a[120:123],a[176:179],v[24:27],a[120:123]
MM a[124:127],a[184:187],v[24:27],a[124:127]
v_add_f32 v244, v144, v145
v_add_f32 v245, v148, v149
MM a[124:127],a[188:191],v[28:31],a[124:127]
v_add_f32 v246, v152, v153
v_add_f32 v247, v156, v157
MM a[116:119],a[188:191],v[20:23],a[116:119]
MM a[116:119],a[184:187],v[16:19],a[116:119]
MM a[108:111],a[184:187],v[8:11],a[108:111]
MM a[108:111],a[188:191],v[12:15],a[108:111]
MM a[100:103],a[188:191],v[4:7],a[100:103]
MM a[100:103],a[184:187],v[0:3],a[100:103]
v_smfmac_f32_16x16x64_bf16 v[144:147], v[236:239], v[64:71], v234
v_smfmac_f32_16x16x64_bf16 v[148:151], v[236:239], v[72:79], v234
v_smfmac_f32_16x16x64_bf16 v[152:155], v[236:239], v[80:87], v234
v_smfmac_f32_16x16x64_bf16 v[156:159], v[236:239], v[88:95], v234
s_waitcnt lgkmcnt(0)
MM v[0:3],a[192:195],v[160:163],v[128:131]
s_add_u32 s64, s64, 16384
TR a[128:129],v232 offset:16384
s_add_u32 s81, s64, 896
MM v[0:3],a[196:199],v[176:179],v[0:3]
TR a[130:131],v232 offset:20480
v_max3_f32 v249, v144, v145, v148
MM v[0:3],a[200:203],v[192:195],v[0:3]
TR a[132:133],v232 offset:24576
v_max3_f32 v249, v249, v149, v152
MM v[0:3],a[204:207],v[208:211],v[0:3]
TR a[134:135],v232 offset:28672
v_max3_f32 v249, v249, v153, v156
MM v[4:7],a[220:223],v[208:211],v[128:131]
TR a[136:137],v233 offset:16384
v_max_f32 v249, v249, v157
MM v[4:7],a[216:219],v[192:195],v[4:7]
TR a[138:139],v233 offset:20480
v_cmp_lt_f32 vcc, s71, v249
MM v[4:7],a[212:215],v[176:179],v[4:7]
TR a[140:141],v233 offset:24576
TR a[142:143],v233 offset:28672
MM v[4:7],a[208:211],v[160:163],v[4:7]
s_cbranch_vccnz LSITE17_%=
LBACK17_%=:
MM v[12:15],a[208:211],v[164:167],v[132:135]
v_lshrrev_b32 v127, 8, v248
v_lshlrev_b32 v124, 9, v127
MM v[12:15],a[212:215],v[180:183],v[12:15]
v_and_b32 v126, 192, v248
v_add_u32 v124, v124, v126
MM v[12:15],a[216:219],v[196:199],v[12:15]
v_bfe_u32 v126, v248, 4, 2
v_lshl_add_u32 v124, v126, 3, v124
MM v[12:15],a[220:223],v[212:215],v[12:15]
s_lshr_b32 s76, %[swave], 1
EX v0,v0
MM v[8:11],a[204:207],v[212:215],v[132:135]
s_mul_i32 s76, s76, 10240
EX v1,v1
MM v[8:11],a[200:203],v[196:199],v[8:11]
s_and_b32 s77, %[swave], 1
EX v2,v2
MM v[8:11],a[196:199],v[180:183],v[8:11]
s_lshl_b32 s77, s77, 5
TR a[144:145],v232 offset:16896
CV v0,v0,v1
MM v[8:11],a[192:195],v[164:167],v[8:11]
s_add_u32 s76, s76, s77
EX v3,v3
MM v[16:19],a[192:195],v[168:171],v[136:139]
s_add_u32 s76, s76, %[slds]
EX v4,v4
MM v[16:19],a[196:199],v[184:187],v[16:19]
s_add_u32 s76, s76, 131072
v_add_u32 v124, s76, v124
v_bfe_u32 v126, v248, 4, 3
MM v[16:19],a[200:203],v[200:203],v[16:19]
v_lshl_add_u32 v126, v127, 3, v126
v_lshlrev_b32 v126, 4, v126
MM v[16:19],a[204:207],v[216:219],v[16:19]
s_mul_i32 s76, %[swave], 3072
EX v5,v5
MM v[20:23],a[220:223],v[216:219],v[136:139]
s_add_u32 s76, s76, %[slds]
TR a[146:147],v232 offset:20992
CV v1,v2,v3
MM v[20:23],a[216:219],v[200:203],v[20:23]
s_add_u32 s76, s76, 151552
TR a[148:149],v232 offset:25088
v_add_u32 v125, s76, v126
MM v[20:23],a[212:215],v[184:187],v[20:23]
s_lshr_b32 s76, s48, 7
EX v6,v6
MM v[20:23],a[208:211],v[168:171],v[20:23]
s_lshl_b32 s76, s76, 5
EX v7,v7
MM v[28:31],a[208:211],v[172:175],v[140:143]
s_and_b32 s77, s48, 16
TR a[150:151],v232 offset:29184
TR a[152:153],v233 offset:16896
MM v[28:31],a[212:215],v[188:191],v[28:31]
s_add_u32 s76, s76, s77
EX v8,v8
MM v[28:31],a[216:219],v[204:207],v[28:31]
s_bfe_u32 s77, s48, 131077
TR a[154:155],v233 offset:20992
CV v2,v4,v5
MM v[28:31],a[220:223],v[220:223],v[28:31]
s_lshl_b32 s77, s77, 2
EX v9,v9
MM v[24:27],a[204:207],v[220:223],v[140:143]
s_add_u32 s76, s76, s77
TR a[156:157],v233 offset:25088
CV v3,v6,v7
MM v[24:27],a[200:203],v[204:207],v[24:27]
s_add_u32 s76, s76, %[swave]
EX v10,v10
MM v[24:27],a[196:199],v[188:191],v[24:27]
s_lshl_b32 s97, s76, 2
TR a[158:159],v233 offset:29184
CV v8,v8,v9
MM v[24:27],a[192:195],v[172:175],v[24:27]
s_add_u32 s97, s97, 77824
EX v11,v11
MM v[32:35],a[224:227],v[160:163],v[128:131]
s_lshl_b32 s76, s76, 9
TR a[160:161],v232 offset:17408
TR a[162:163],v232 offset:21504
MM v[32:35],a[228:231],v[176:179],v[32:35]
s_add_u32 s76, s76, 22183936
EX v12,v12
MM v[32:35],a[232:235],v[192:195],v[32:35]
EX v13,v13
MM v[32:35],a[236:239],v[208:211],v[32:35]
TR a[164:165],v232 offset:25600
CV v9,v10,v11
MM v[36:39],a[252:255],v[208:211],v[128:131]
EX v14,v14
MM v[36:39],a[248:251],v[192:195],v[36:39]
TR a[166:167],v232 offset:29696
CV v10,v12,v13
MM v[36:39],a[244:247],v[176:179],v[36:39]
EX v15,v15
MM v[36:39],a[240:243],v[160:163],v[36:39]
TR a[168:169],v233 offset:17408
TR a[170:171],v233 offset:21504
MM v[44:47],a[240:243],v[164:167],v[132:135]
EX v16,v16
MM v[44:47],a[244:247],v[180:183],v[44:47]
EX v17,v17
MM v[44:47],a[248:251],v[196:199],v[44:47]
TR a[172:173],v233 offset:25600
CV v11,v14,v15
MM v[44:47],a[252:255],v[212:215],v[44:47]
EX v18,v18
MM v[40:43],a[236:239],v[212:215],v[132:135]
TR a[174:175],v233 offset:29696
CV v16,v16,v17
MM v[40:43],a[232:235],v[196:199],v[40:43]
EX v19,v19
MM v[40:43],a[228:231],v[180:183],v[40:43]
TR a[176:177],v232 offset:17920
TR a[178:179],v232 offset:22016
MM v[40:43],a[224:227],v[164:167],v[40:43]
EX v20,v20
MM v[48:51],a[224:227],v[168:171],v[136:139]
EX v21,v21
MM v[48:51],a[228:231],v[184:187],v[48:51]
TR a[180:181],v232 offset:26112
CV v17,v18,v19
MM v[48:51],a[232:235],v[200:203],v[48:51]
EX v22,v22
MM v[48:51],a[236:239],v[216:219],v[48:51]
TR a[182:183],v232 offset:30208
CV v18,v20,v21
MM v[52:55],a[252:255],v[216:219],v[136:139]
EX v23,v23
MM v[52:55],a[248:251],v[200:203],v[52:55]
TR a[184:185],v233 offset:17920
TR a[186:187],v233 offset:22016
MM v[52:55],a[244:247],v[184:187],v[52:55]
EX v24,v24
MM v[52:55],a[240:243],v[168:171],v[52:55]
TR a[188:189],v233 offset:26112
CV v19,v22,v23
MM v[60:63],a[240:243],v[172:175],v[140:143]
EX v25,v25
MM v[60:63],a[244:247],v[188:191],v[60:63]
TR a[190:191],v233 offset:30208
CV v24,v24,v25
MM v[60:63],a[248:251],v[204:207],v[60:63]
EX v26,v26
MM v[60:63],a[252:255],v[220:223],v[60:63]
EX v27,v27
)ASM"
R"ASM(
MM v[56:59],a[236:239],v[220:223],v[140:143]
EX v28,v28
MM v[56:59],a[232:235],v[204:207],v[56:59]
EX v29,v29
MM v[56:59],a[228:231],v[188:191],v[56:59]
EX v30,v30
MM v[56:59],a[224:227],v[172:175],v[56:59]
s_waitcnt vmcnt(10) lgkmcnt(0)
s_barrier
MM a[0:3],a[128:131],v[64:67],a[0:3]
s_add_u32 m0, s65, 16384
CV v25,v26,v27
CV v26,v28,v29
MM a[0:3],a[132:135],v[68:71],a[0:3]
RD a[192:195],v224 offset:49152
RD a[196:199],v225 offset:49152
MM a[8:11],a[132:135],v[76:79],a[8:11]
EX v31,v31
MM a[8:11],a[128:131],v[72:75],a[8:11]
RD a[200:203],v226 offset:49152
CV v27,v30,v31
MM a[16:19],a[128:131],v[80:83],a[16:19]
TR v[96:97],v124 offset:0
TR v[98:99],v124 offset:256
MM a[16:19],a[132:135],v[84:87],a[16:19]
RD v[108:111],v125 offset:0
TR v[100:101],v124 offset:2048
MM a[24:27],a[132:135],v[92:95],a[24:27]
TR v[102:103],v124 offset:2304
RD v[112:115],v125 offset:512
MM a[24:27],a[128:131],v[88:91],a[24:27]
TR v[104:105],v124 offset:4096
TR v[106:107],v124 offset:4352
MM a[28:31],a[136:139],v[88:91],a[28:31]
RD v[116:119],v125 offset:1024
RD a[204:207],v227 offset:49152
EX v32,v32
MM a[28:31],a[140:143],v[92:95],a[28:31]
EX v33,v33
MM a[20:23],a[140:143],v[84:87],a[20:23]
EX v34,v34
MM a[20:23],a[136:139],v[80:83],a[20:23]
buffer_load_dwordx4 v228, s[8:11], s64 offen offset:0 lds
MM a[12:15],a[136:139],v[72:75],a[12:15]
EX v35,v35
MM a[12:15],a[140:143],v[76:79],a[12:15]
EX v36,v36
MM a[4:7],a[140:143],v[68:71],a[4:7]
CV v4,v32,v33
CV v5,v34,v35
MM a[4:7],a[136:139],v[64:67],a[4:7]
EX v37,v37
MM a[32:35],a[144:147],v[64:67],a[32:35]
EX v38,v38
MM a[32:35],a[148:151],v[68:71],a[32:35]
s_waitcnt lgkmcnt(1)
buffer_load_dwordx4 v229, s[8:11], s64 offen offset:1024 lds
MM a[40:43],a[148:151],v[76:79],a[40:43]
EX v39,v39
MM v[120:123],v[96:99],v[108:111],0
MM v[120:123],v[100:103],v[112:115],v[120:123]
MM v[120:123],v[104:107],v[116:119],v[120:123]
MM a[40:43],a[144:147],v[72:75],a[40:43]
TR v[96:97],v124 offset:6144
TR v[98:99],v124 offset:6400
MM a[48:51],a[144:147],v[80:83],a[48:51]
RD v[108:111],v125 offset:1536
TR v[100:101],v124 offset:8192
MM a[48:51],a[148:151],v[84:87],a[48:51]
TR v[102:103],v124 offset:8448
RD v[112:115],v125 offset:2048
MM a[56:59],a[148:151],v[92:95],a[56:59]
EX v40,v40
MM a[56:59],a[144:147],v[88:91],a[56:59]
EX v41,v41
MM a[60:63],a[152:155],v[88:91],a[60:63]
CV v6,v36,v37
CV v7,v38,v39
MM a[60:63],a[156:159],v[92:95],a[60:63]
EX v42,v42
MM a[52:55],a[156:159],v[84:87],a[52:55]
RD a[208:211],v224 offset:53248
RD a[212:215],v225 offset:53248
MM a[52:55],a[152:155],v[80:83],a[52:55]
EX v43,v43
MM a[44:47],a[152:155],v[72:75],a[44:47]
buffer_load_dwordx4 v230, s[8:11], s64 offen offset:2048 lds
MM a[44:47],a[156:159],v[76:79],a[44:47]
EX v44,v44
MM a[36:39],a[156:159],v[68:71],a[36:39]
s_waitcnt lgkmcnt(2)
EX v45,v45
MM a[36:39],a[152:155],v[64:67],a[36:39]
CV v12,v40,v41
CV v13,v42,v43
MM v[120:123],v[96:99],v[108:111],v[120:123]
MM v[120:123],v[100:103],v[112:115],v[120:123]
MM a[64:67],a[160:163],v[64:67],a[64:67]
EX v46,v46
MM a[64:67],a[164:167],v[68:71],a[64:67]
RD a[216:219],v226 offset:53248
CV v14,v44,v45
MM a[72:75],a[164:167],v[76:79],a[72:75]
EX v47,v47
MM a[72:75],a[160:163],v[72:75],a[72:75]
buffer_load_dwordx4 v231, s[8:11], s64 offen offset:3072 lds
MM a[80:83],a[160:163],v[80:83],a[80:83]
s_add_u32 m0, s65, 81920
EX v48,v48
MM a[80:83],a[164:167],v[84:87],a[80:83]
RD a[220:223],v227 offset:53248
CV v15,v46,v47
MM a[88:91],a[164:167],v[92:95],a[88:91]
s_mov_b32 exec_lo, 0x00ff00ff
s_mov_b32 exec_hi, 0x00ff00ff
buffer_store_dwordx4 v[120:123], v126, s[20:23], s76 offen offset:0 sc1
s_mov_b64 exec, -1
s_nop 0
MM a[88:91],a[160:163],v[88:91],a[88:91]
EX v49,v49
MM a[92:95],a[168:171],v[88:91],a[92:95]
EX v50,v50
MM a[92:95],a[172:175],v[92:95],a[92:95]
EX v51,v51
MM a[84:87],a[172:175],v[84:87],a[84:87]
RD a[224:227],v224 offset:57344
CV v20,v48,v49
MM a[84:87],a[168:171],v[80:83],a[84:87]
EX v52,v52
MM a[76:79],a[168:171],v[72:75],a[76:79]
buffer_load_dwordx4 v235, s[12:15], s81 offen offset:0 lds
MM a[76:79],a[172:175],v[76:79],a[76:79]
EX v53,v53
MM a[68:71],a[172:175],v[68:71],a[68:71]
RD a[228:231],v225 offset:57344
CV v21,v50,v51
MM a[68:71],a[168:171],v[64:67],a[68:71]
EX v54,v54
MM a[96:99],a[176:179],v[64:67],a[96:99]
RD a[232:235],v226 offset:57344
CV v22,v52,v53
MM a[96:99],a[180:183],v[68:71],a[96:99]
EX v55,v55
MM a[104:107],a[180:183],v[76:79],a[104:107]
EX v56,v56
MM a[104:107],a[176:179],v[72:75],a[104:107]
buffer_load_dwordx4 v235, s[12:15], s64 offen offset:1024 lds
MM a[112:115],a[176:179],v[80:83],a[112:115]
EX v57,v57
MM a[112:115],a[180:183],v[84:87],a[112:115]
RD a[236:239],v227 offset:57344
CV v23,v54,v55
MM a[120:123],a[180:183],v[92:95],a[120:123]
EX v58,v58
MM a[120:123],a[176:179],v[88:91],a[120:123]
RD a[240:243],v224 offset:61440
CV v28,v56,v57
MM a[124:127],a[184:187],v[88:91],a[124:127]
v_add_f32 v244, v144, v145
v_add_f32 v245, v148, v149
MM a[124:127],a[188:191],v[92:95],a[124:127]
v_add_f32 v246, v152, v153
v_add_f32 v247, v156, v157
MM a[116:119],a[188:191],v[84:87],a[116:119]
EX v59,v59
MM a[116:119],a[184:187],v[80:83],a[116:119]
EX v60,v60
MM a[108:111],a[184:187],v[72:75],a[108:111]
EX v61,v61
MM a[108:111],a[188:191],v[76:79],a[108:111]
buffer_load_dwordx4 v235, s[12:15], s81 offen offset:2048 lds
MM a[100:103],a[188:191],v[68:71],a[100:103]
EX v62,v62
MM a[100:103],a[184:187],v[64:67],a[100:103]
RD a[244:247],v225 offset:61440
CV v29,v58,v59
v_smfmac_f32_16x16x64_bf16 v[144:147], v[236:239], v[0:7], v234
RD a[248:251],v226 offset:61440
RD a[252:255],v227 offset:61440
EX v63,v63
v_smfmac_f32_16x16x64_bf16 v[148:151], v[236:239], v[8:15], v234
CV v30,v60,v61
v_smfmac_f32_16x16x64_bf16 v[152:155], v[236:239], v[16:23], v234
CV v31,v62,v63
s_nop 1
v_smfmac_f32_16x16x64_bf16 v[156:159], v[236:239], v[24:31], v234
s_waitcnt lgkmcnt(0)
buffer_load_dwordx4 v235, s[12:15], s64 offen offset:3072 lds
MM v[64:67],a[192:195],v[160:163],v[128:131]
s_add_u32 s64, s64, 16384
TR a[128:129],v232 offset:32768
s_add_u32 s81, s64, 896
MM v[64:67],a[196:199],v[176:179],v[64:67]
TR a[130:131],v232 offset:36864
v_max3_f32 v249, v144, v145, v148
MM v[64:67],a[200:203],v[192:195],v[64:67]
TR a[132:133],v232 offset:40960
v_max3_f32 v249, v249, v149, v152
MM v[64:67],a[204:207],v[208:211],v[64:67]
TR a[134:135],v232 offset:45056
v_max3_f32 v249, v249, v153, v156
MM v[68:71],a[220:223],v[208:211],v[128:131]
TR a[136:137],v233 offset:32768
v_max_f32 v249, v249, v157
MM v[68:71],a[216:219],v[192:195],v[68:71]
TR a[138:139],v233 offset:36864
v_cmp_lt_f32 vcc, s71, v249
MM v[68:71],a[212:215],v[176:179],v[68:71]
TR a[140:141],v233 offset:40960
TR a[142:143],v233 offset:45056
MM v[68:71],a[208:211],v[160:163],v[68:71]
s_cbranch_vccnz LSITE18_%=
LBACK18_%=:
MM v[76:79],a[208:211],v[164:167],v[132:135]
EX v64,v64
MM v[76:79],a[212:215],v[180:183],v[76:79]
EX v65,v65
MM v[76:79],a[216:219],v[196:199],v[76:79]
TR a[144:145],v232 offset:33280
CV v64,v64,v65
MM v[76:79],a[220:223],v[212:215],v[76:79]
EX v66,v66
MM v[72:75],a[204:207],v[212:215],v[132:135]
EX v67,v67
MM v[72:75],a[200:203],v[196:199],v[72:75]
TR a[146:147],v232 offset:37376
CV v65,v66,v67
MM v[72:75],a[196:199],v[180:183],v[72:75]
EX v68,v68
MM v[72:75],a[192:195],v[164:167],v[72:75]
TR a[148:149],v232 offset:41472
TR a[150:151],v232 offset:45568
MM v[80:83],a[192:195],v[168:171],v[136:139]
EX v69,v69
MM v[80:83],a[196:199],v[184:187],v[80:83]
TR a[152:153],v233 offset:33280
CV v66,v68,v69
MM v[80:83],a[200:203],v[200:203],v[80:83]
EX v70,v70
MM v[80:83],a[204:207],v[216:219],v[80:83]
TR a[154:155],v233 offset:37376
TR a[156:157],v233 offset:41472
MM v[84:87],a[220:223],v[216:219],v[136:139]
EX v71,v71
MM v[84:87],a[216:219],v[200:203],v[84:87]
TR a[158:159],v233 offset:45568
CV v67,v70,v71
MM v[84:87],a[212:215],v[184:187],v[84:87]
EX v72,v72
MM v[84:87],a[208:211],v[168:171],v[84:87]
EX v73,v73
MM v[92:95],a[208:211],v[172:175],v[140:143]
TR a[160:161],v232 offset:33792
CV v72,v72,v73
MM v[92:95],a[212:215],v[188:191],v[92:95]
EX v74,v74
MM v[92:95],a[216:219],v[204:207],v[92:95]
TR a[162:163],v232 offset:37888
TR a[164:165],v232 offset:41984
MM v[92:95],a[220:223],v[220:223],v[92:95]
EX v75,v75
MM v[88:91],a[204:207],v[220:223],v[140:143]
TR a[166:167],v232 offset:46080
CV v73,v74,v75
MM v[88:91],a[200:203],v[204:207],v[88:91]
EX v76,v76
MM v[88:91],a[196:199],v[188:191],v[88:91]
EX v77,v77
MM v[88:91],a[192:195],v[172:175],v[88:91]
TR a[168:169],v233 offset:33792
CV v74,v76,v77
MM v[96:99],a[224:227],v[160:163],v[128:131]
EX v78,v78
MM v[96:99],a[228:231],v[176:179],v[96:99]
TR a[170:171],v233 offset:37888
TR a[172:173],v233 offset:41984
MM v[96:99],a[232:235],v[192:195],v[96:99]
EX v79,v79
MM v[96:99],a[236:239],v[208:211],v[96:99]
TR a[174:175],v233 offset:46080
CV v75,v78,v79
MM v[100:103],a[252:255],v[208:211],v[128:131]
EX v80,v80
MM v[100:103],a[248:251],v[192:195],v[100:103]
EX v81,v81
MM v[100:103],a[244:247],v[176:179],v[100:103]
TR a[176:177],v232 offset:34304
CV v80,v80,v81
MM v[100:103],a[240:243],v[160:163],v[100:103]
EX v82,v82
MM v[108:111],a[240:243],v[164:167],v[132:135]
v_lshrrev_b32 v253, 7, v248
v_mul_u32_u24 v253, 1280, v253
MM v[108:111],a[244:247],v[180:183],v[108:111]
v_and_b32 v252, 112, v248
v_add_u32 v253, v253, v252
MM v[108:111],a[248:251],v[196:199],v[108:111]
s_mul_i32 s90, %[swave], 61440
TR a[178:179],v232 offset:38400
TR a[180:181],v232 offset:42496
MM v[108:111],a[252:255],v[212:215],v[108:111]
s_mul_i32 s91, %[swave], 6144
EX v83,v83
MM v[104:107],a[236:239],v[212:215],v[132:135]
s_add_u32 s91, s91, %[slds]
TR a[182:183],v232 offset:46592
CV v81,v82,v83
MM v[104:107],a[232:235],v[196:199],v[104:107]
EX v84,v84
MM v[104:107],a[228:231],v[180:183],v[104:107]
EX v85,v85
MM v[104:107],a[224:227],v[164:167],v[104:107]
TR a[184:185],v233 offset:34304
CV v82,v84,v85
MM v[112:115],a[224:227],v[168:171],v[136:139]
EX v86,v86
MM v[112:115],a[228:231],v[184:187],v[112:115]
TR a[186:187],v233 offset:38400
TR a[188:189],v233 offset:42496
MM v[112:115],a[232:235],v[200:203],v[112:115]
EX v87,v87
MM v[112:115],a[236:239],v[216:219],v[112:115]
TR a[190:191],v233 offset:46592
CV v83,v86,v87
MM v[116:119],a[252:255],v[216:219],v[136:139]
EX v88,v88
MM v[116:119],a[248:251],v[200:203],v[116:119]
EX v89,v89
MM v[116:119],a[244:247],v[184:187],v[116:119]
EX v90,v90
MM v[116:119],a[240:243],v[168:171],v[116:119]
EX v91,v91
MM v[124:127],a[240:243],v[172:175],v[140:143]
EX v92,v92
MM v[124:127],a[244:247],v[188:191],v[124:127]
EX v93,v93
MM v[124:127],a[248:251],v[204:207],v[124:127]
EX v94,v94
MM v[124:127],a[252:255],v[220:223],v[124:127]
EX v95,v95
MM v[120:123],a[236:239],v[220:223],v[140:143]
EX v96,v96
MM v[120:123],a[232:235],v[204:207],v[120:123]
EX v97,v97
MM v[120:123],a[228:231],v[188:191],v[120:123]
EX v98,v98
MM v[120:123],a[224:227],v[172:175],v[120:123]
s_waitcnt vmcnt(14) lgkmcnt(0)
s_barrier
MM a[0:3],a[128:131],v[0:3],a[0:3]
s_nop 0
LPG15_%=:
s_cbranch_execz LTR2_%=
s_add_u32 m0, s65, 32768
CV v88,v88,v89
CV v89,v90,v91
MM a[0:3],a[132:135],v[4:7],a[0:3]
RD a[192:195],v224 offset:0
CV v90,v92,v93
MM a[8:11],a[132:135],v[12:15],a[8:11]
RD a[196:199],v225 offset:0
CV v91,v94,v95
MM a[8:11],a[128:131],v[8:11],a[8:11]
buffer_load_dwordx4 v228, s[8:11], s64 offen offset:0 lds
MM a[16:19],a[128:131],v[16:19],a[16:19]
RD a[200:203],v226 offset:0
CV v68,v96,v97
MM a[16:19],a[132:135],v[20:23],a[16:19]
buffer_load_dwordx4 v229, s[8:11], s64 offen offset:1024 lds
MM a[24:27],a[132:135],v[28:31],a[24:27]
RD a[204:207],v227 offset:0
RD a[208:211],v224 offset:4096
MM a[24:27],a[128:131],v[24:27],a[24:27]
EX v99,v99
MM a[28:31],a[136:139],v[24:27],a[28:31]
RD a[212:215],v225 offset:4096
CV v69,v98,v99
MM a[28:31],a[140:143],v[28:31],a[28:31]
EX v100,v100
MM a[20:23],a[140:143],v[20:23],a[20:23]
buffer_load_dwordx4 v230, s[8:11], s64 offen offset:2048 lds
MM a[20:23],a[136:139],v[16:19],a[20:23]
EX v101,v101
MM a[12:15],a[136:139],v[8:11],a[12:15]
RD a[216:219],v226 offset:4096
CV v70,v100,v101
MM a[12:15],a[140:143],v[12:15],a[12:15]
EX v102,v102
MM a[4:7],a[140:143],v[4:7],a[4:7]
)ASM"
R"ASM(
EX v103,v103
MM a[4:7],a[136:139],v[0:3],a[4:7]
RD a[220:223],v227 offset:4096
CV v71,v102,v103
MM a[32:35],a[144:147],v[0:3],a[32:35]
EX v104,v104
MM a[32:35],a[148:151],v[4:7],a[32:35]
buffer_load_dwordx4 v231, s[8:11], s64 offen offset:3072 lds
MM a[40:43],a[148:151],v[12:15],a[40:43]
s_add_u32 m0, s65, 98304
EX v105,v105
MM a[40:43],a[144:147],v[8:11],a[40:43]
RD a[224:227],v224 offset:8192
CV v76,v104,v105
MM a[48:51],a[144:147],v[16:19],a[48:51]
EX v106,v106
MM a[48:51],a[148:151],v[20:23],a[48:51]
EX v107,v107
MM a[56:59],a[148:151],v[28:31],a[56:59]
RD a[228:231],v225 offset:8192
CV v77,v106,v107
MM a[56:59],a[144:147],v[24:27],a[56:59]
EX v108,v108
MM a[60:63],a[152:155],v[24:27],a[60:63]
buffer_load_dwordx4 v235, s[12:15], s81 offen offset:0 lds
MM a[60:63],a[156:159],v[28:31],a[60:63]
EX v109,v109
MM a[52:55],a[156:159],v[20:23],a[52:55]
RD a[232:235],v226 offset:8192
CV v78,v108,v109
MM a[52:55],a[152:155],v[16:19],a[52:55]
EX v110,v110
MM a[44:47],a[152:155],v[8:11],a[44:47]
EX v111,v111
MM a[44:47],a[156:159],v[12:15],a[44:47]
RD a[236:239],v227 offset:8192
CV v79,v110,v111
MM a[36:39],a[156:159],v[4:7],a[36:39]
buffer_load_dwordx4 v235, s[12:15], s64 offen offset:1024 lds
MM a[36:39],a[152:155],v[0:3],a[36:39]
EX v112,v112
MM a[64:67],a[160:163],v[0:3],a[64:67]
EX v113,v113
MM a[64:67],a[164:167],v[4:7],a[64:67]
RD a[240:243],v224 offset:12288
CV v84,v112,v113
MM a[72:75],a[164:167],v[12:15],a[72:75]
EX v114,v114
MM a[72:75],a[160:163],v[8:11],a[72:75]
RD a[244:247],v225 offset:12288
RD a[248:251],v226 offset:12288
MM a[80:83],a[160:163],v[16:19],a[80:83]
s_waitcnt vmcnt(10)
v_mov_b32 v250, s69
s_mov_b64 exec, 1
buffer_store_dword v250, off, s[20:23], s97 offset:0 sc1
s_mov_b64 exec, -1
MM a[80:83],a[164:167],v[20:23],a[80:83]
s_add_u32 m0, s91, 139264
s_add_u32 s77, s90, 0
buffer_load_dwordx4 v253, s[28:31], s77 offen offset:0 lds
MM a[88:91],a[164:167],v[28:31],a[88:91]
EX v115,v115
MM a[88:91],a[160:163],v[24:27],a[88:91]
RD a[252:255],v227 offset:12288
CV v85,v114,v115
MM a[92:95],a[168:171],v[24:27],a[92:95]
EX v116,v116
MM a[92:95],a[172:175],v[28:31],a[92:95]
EX v117,v117
MM a[84:87],a[172:175],v[20:23],a[84:87]
s_add_u32 m0, s65, 98304
s_nop 0
buffer_load_dwordx4 v235, s[12:15], s81 offen offset:2048 lds
MM a[84:87],a[168:171],v[16:19],a[84:87]
EX v118,v118
MM a[76:79],a[168:171],v[8:11],a[76:79]
EX v119,v119
MM a[76:79],a[172:175],v[12:15],a[76:79]
EX v120,v120
MM a[68:71],a[172:175],v[4:7],a[68:71]
buffer_load_dwordx4 v235, s[12:15], s64 offen offset:3072 lds
MM a[68:71],a[168:171],v[0:3],a[68:71]
EX v121,v121
MM a[96:99],a[176:179],v[0:3],a[96:99]
EX v122,v122
MM a[96:99],a[180:183],v[4:7],a[96:99]
EX v123,v123
MM a[104:107],a[180:183],v[12:15],a[104:107]
EX v124,v124
MM a[104:107],a[176:179],v[8:11],a[104:107]
EX v125,v125
MM a[112:115],a[176:179],v[16:19],a[112:115]
EX v126,v126
MM a[112:115],a[180:183],v[20:23],a[112:115]
EX v127,v127
MM a[120:123],a[180:183],v[28:31],a[120:123]
CV v86,v116,v117
CV v87,v118,v119
MM a[120:123],a[176:179],v[24:27],a[120:123]
CV v92,v120,v121
CV v93,v122,v123
MM a[124:127],a[184:187],v[24:27],a[124:127]
v_add_f32 v244, v144, v145
v_add_f32 v245, v148, v149
MM a[124:127],a[188:191],v[28:31],a[124:127]
v_add_f32 v246, v152, v153
v_add_f32 v247, v156, v157
MM a[116:119],a[188:191],v[20:23],a[116:119]
CV v94,v124,v125
CV v95,v126,v127
MM a[116:119],a[184:187],v[16:19],a[116:119]
MM a[108:111],a[184:187],v[8:11],a[108:111]
MM a[108:111],a[188:191],v[12:15],a[108:111]
MM a[100:103],a[188:191],v[4:7],a[100:103]
MM a[100:103],a[184:187],v[0:3],a[100:103]
v_smfmac_f32_16x16x64_bf16 v[144:147], v[236:239], v[64:71], v234
v_smfmac_f32_16x16x64_bf16 v[148:151], v[236:239], v[72:79], v234
v_smfmac_f32_16x16x64_bf16 v[152:155], v[236:239], v[80:87], v234
v_smfmac_f32_16x16x64_bf16 v[156:159], v[236:239], v[88:95], v234
s_waitcnt lgkmcnt(0)
MM v[0:3],a[192:195],v[160:163],v[128:131]
s_add_u32 s64, s64, 16384
TR a[128:129],v232 offset:49152
s_add_u32 s81, s64, 896
MM v[0:3],a[196:199],v[176:179],v[0:3]
TR a[130:131],v232 offset:53248
v_max3_f32 v249, v144, v145, v148
MM v[0:3],a[200:203],v[192:195],v[0:3]
TR a[132:133],v232 offset:57344
v_max3_f32 v249, v249, v149, v152
MM v[0:3],a[204:207],v[208:211],v[0:3]
TR a[134:135],v232 offset:61440
v_max3_f32 v249, v249, v153, v156
MM v[4:7],a[220:223],v[208:211],v[128:131]
TR a[136:137],v233 offset:49152
v_max_f32 v249, v249, v157
MM v[4:7],a[216:219],v[192:195],v[4:7]
TR a[138:139],v233 offset:53248
v_cmp_lt_f32 vcc, s71, v249
MM v[4:7],a[212:215],v[176:179],v[4:7]
TR a[140:141],v233 offset:57344
TR a[142:143],v233 offset:61440
MM v[4:7],a[208:211],v[160:163],v[4:7]
s_cbranch_vccnz LSITE19_%=
LBACK19_%=:
MM v[12:15],a[208:211],v[164:167],v[132:135]
s_add_u32 m0, s91, 140288
s_add_u32 s77, s90, 10240
buffer_load_dwordx4 v253, s[28:31], s77 offen offset:0 lds
MM v[12:15],a[212:215],v[180:183],v[12:15]
EX v0,v0
MM v[12:15],a[216:219],v[196:199],v[12:15]
EX v1,v1
MM v[12:15],a[220:223],v[212:215],v[12:15]
TR a[144:145],v232 offset:49664
CV v0,v0,v1
MM v[8:11],a[204:207],v[212:215],v[132:135]
EX v2,v2
MM v[8:11],a[200:203],v[196:199],v[8:11]
EX v3,v3
MM v[8:11],a[196:199],v[180:183],v[8:11]
TR a[146:147],v232 offset:53760
CV v1,v2,v3
MM v[8:11],a[192:195],v[164:167],v[8:11]
EX v4,v4
MM v[16:19],a[192:195],v[168:171],v[136:139]
TR a[148:149],v232 offset:57856
TR a[150:151],v232 offset:61952
MM v[16:19],a[196:199],v[184:187],v[16:19]
EX v5,v5
MM v[16:19],a[200:203],v[200:203],v[16:19]
TR a[152:153],v233 offset:49664
CV v2,v4,v5
MM v[16:19],a[204:207],v[216:219],v[16:19]
EX v6,v6
MM v[20:23],a[220:223],v[216:219],v[136:139]
EX v7,v7
MM v[20:23],a[216:219],v[200:203],v[20:23]
TR a[154:155],v233 offset:53760
CV v3,v6,v7
MM v[20:23],a[212:215],v[184:187],v[20:23]
EX v8,v8
MM v[20:23],a[208:211],v[168:171],v[20:23]
TR a[156:157],v233 offset:57856
TR a[158:159],v233 offset:61952
MM v[28:31],a[208:211],v[172:175],v[140:143]
EX v9,v9
MM v[28:31],a[212:215],v[188:191],v[28:31]
TR a[160:161],v232 offset:50176
CV v8,v8,v9
MM v[28:31],a[216:219],v[204:207],v[28:31]
EX v10,v10
MM v[28:31],a[220:223],v[220:223],v[28:31]
EX v11,v11
MM v[24:27],a[204:207],v[220:223],v[140:143]
TR a[162:163],v232 offset:54272
CV v9,v10,v11
MM v[24:27],a[200:203],v[204:207],v[24:27]
TR a[164:165],v232 offset:58368
TR a[166:167],v232 offset:62464
MM v[24:27],a[196:199],v[188:191],v[24:27]
EX v12,v12
MM v[24:27],a[192:195],v[172:175],v[24:27]
EX v13,v13
MM v[32:35],a[224:227],v[160:163],v[128:131]
TR a[168:169],v233 offset:50176
CV v10,v12,v13
MM v[32:35],a[228:231],v[176:179],v[32:35]
EX v14,v14
MM v[32:35],a[232:235],v[192:195],v[32:35]
TR a[170:171],v233 offset:54272
TR a[172:173],v233 offset:58368
MM v[32:35],a[236:239],v[208:211],v[32:35]
EX v15,v15
MM v[36:39],a[252:255],v[208:211],v[128:131]
TR a[174:175],v233 offset:62464
CV v11,v14,v15
MM v[36:39],a[248:251],v[192:195],v[36:39]
EX v16,v16
MM v[36:39],a[244:247],v[176:179],v[36:39]
EX v17,v17
MM v[36:39],a[240:243],v[160:163],v[36:39]
TR a[176:177],v232 offset:50688
CV v16,v16,v17
MM v[44:47],a[240:243],v[164:167],v[132:135]
s_add_u32 m0, s91, 141312
s_add_u32 s77, s90, 20480
buffer_load_dwordx4 v253, s[28:31], s77 offen offset:0 lds
MM v[44:47],a[244:247],v[180:183],v[44:47]
EX v18,v18
MM v[44:47],a[248:251],v[196:199],v[44:47]
TR a[178:179],v232 offset:54784
TR a[180:181],v232 offset:58880
MM v[44:47],a[252:255],v[212:215],v[44:47]
EX v19,v19
MM v[40:43],a[236:239],v[212:215],v[132:135]
TR a[182:183],v232 offset:62976
CV v17,v18,v19
MM v[40:43],a[232:235],v[196:199],v[40:43]
EX v20,v20
MM v[40:43],a[228:231],v[180:183],v[40:43]
EX v21,v21
MM v[40:43],a[224:227],v[164:167],v[40:43]
TR a[184:185],v233 offset:50688
CV v18,v20,v21
MM v[48:51],a[224:227],v[168:171],v[136:139]
EX v22,v22
MM v[48:51],a[228:231],v[184:187],v[48:51]
TR a[186:187],v233 offset:54784
TR a[188:189],v233 offset:58880
MM v[48:51],a[232:235],v[200:203],v[48:51]
EX v23,v23
MM v[48:51],a[236:239],v[216:219],v[48:51]
TR a[190:191],v233 offset:62976
CV v19,v22,v23
MM v[52:55],a[252:255],v[216:219],v[136:139]
EX v24,v24
MM v[52:55],a[248:251],v[200:203],v[52:55]
EX v25,v25
MM v[52:55],a[244:247],v[184:187],v[52:55]
EX v26,v26
MM v[52:55],a[240:243],v[168:171],v[52:55]
EX v27,v27
MM v[60:63],a[240:243],v[172:175],v[140:143]
EX v28,v28
MM v[60:63],a[244:247],v[188:191],v[60:63]
EX v29,v29
MM v[60:63],a[248:251],v[204:207],v[60:63]
EX v30,v30
MM v[60:63],a[252:255],v[220:223],v[60:63]
EX v31,v31
MM v[56:59],a[236:239],v[220:223],v[140:143]
EX v32,v32
MM v[56:59],a[232:235],v[204:207],v[56:59]
EX v33,v33
MM v[56:59],a[228:231],v[188:191],v[56:59]
EX v34,v34
MM v[56:59],a[224:227],v[172:175],v[56:59]
s_waitcnt lgkmcnt(0)
s_barrier
MM a[0:3],a[128:131],v[64:67],a[0:3]
s_add_u32 m0, s65, 49152
CV v24,v24,v25
CV v25,v26,v27
MM a[0:3],a[132:135],v[68:71],a[0:3]
RD a[192:195],v224 offset:16384
CV v26,v28,v29
MM a[8:11],a[132:135],v[76:79],a[8:11]
RD a[196:199],v225 offset:16384
CV v27,v30,v31
MM a[8:11],a[128:131],v[72:75],a[8:11]
buffer_load_dwordx4 v228, s[8:11], s64 offen offset:0 lds
MM a[16:19],a[128:131],v[80:83],a[16:19]
s_add_u32 m0, s91, 142336
s_add_u32 s77, s90, 30720
buffer_load_dwordx4 v253, s[28:31], s77 offen offset:0 lds
MM a[16:19],a[132:135],v[84:87],a[16:19]
RD a[200:203],v226 offset:16384
CV v4,v32,v33
MM a[24:27],a[132:135],v[92:95],a[24:27]
RD a[204:207],v227 offset:16384
RD a[208:211],v224 offset:20480
MM a[24:27],a[128:131],v[88:91],a[24:27]
s_add_u32 m0, s65, 49152
s_nop 0
buffer_load_dwordx4 v229, s[8:11], s64 offen offset:1024 lds
MM a[28:31],a[136:139],v[88:91],a[28:31]
EX v35,v35
MM a[28:31],a[140:143],v[92:95],a[28:31]
RD a[212:215],v225 offset:20480
CV v5,v34,v35
MM a[20:23],a[140:143],v[84:87],a[20:23]
EX v36,v36
MM a[20:23],a[136:139],v[80:83],a[20:23]
EX v37,v37
MM a[12:15],a[136:139],v[72:75],a[12:15]
RD a[216:219],v226 offset:20480
CV v6,v36,v37
MM a[12:15],a[140:143],v[76:79],a[12:15]
buffer_load_dwordx4 v230, s[8:11], s64 offen offset:2048 lds
MM a[4:7],a[140:143],v[68:71],a[4:7]
EX v38,v38
MM a[4:7],a[136:139],v[64:67],a[4:7]
EX v39,v39
MM a[32:35],a[144:147],v[64:67],a[32:35]
RD a[220:223],v227 offset:20480
CV v7,v38,v39
MM a[32:35],a[148:151],v[68:71],a[32:35]
EX v40,v40
MM a[40:43],a[148:151],v[76:79],a[40:43]
EX v41,v41
MM a[40:43],a[144:147],v[72:75],a[40:43]
RD a[224:227],v224 offset:24576
CV v12,v40,v41
MM a[48:51],a[144:147],v[80:83],a[48:51]
buffer_load_dwordx4 v231, s[8:11], s64 offen offset:3072 lds
MM a[48:51],a[148:151],v[84:87],a[48:51]
s_add_u32 m0, s65, 114688
EX v42,v42
MM a[56:59],a[148:151],v[92:95],a[56:59]
EX v43,v43
MM a[56:59],a[144:147],v[88:91],a[56:59]
RD a[228:231],v225 offset:24576
CV v13,v42,v43
MM a[60:63],a[152:155],v[88:91],a[60:63]
EX v44,v44
MM a[60:63],a[156:159],v[92:95],a[60:63]
buffer_load_dwordx4 v235, s[12:15], s81 offen offset:0 lds
MM a[52:55],a[156:159],v[84:87],a[52:55]
EX v45,v45
MM a[52:55],a[152:155],v[80:83],a[52:55]
RD a[232:235],v226 offset:24576
CV v14,v44,v45
MM a[44:47],a[152:155],v[72:75],a[44:47]
EX v46,v46
MM a[44:47],a[156:159],v[76:79],a[44:47]
EX v47,v47
MM a[36:39],a[156:159],v[68:71],a[36:39]
RD a[236:239],v227 offset:24576
CV v15,v46,v47
MM a[36:39],a[152:155],v[64:67],a[36:39]
EX v48,v48
MM a[64:67],a[160:163],v[64:67],a[64:67]
buffer_load_dwordx4 v235, s[12:15], s64 offen offset:1024 lds
MM a[64:67],a[164:167],v[68:71],a[64:67]
EX v49,v49
MM a[72:75],a[164:167],v[76:79],a[72:75]
RD a[240:243],v224 offset:28672
CV v20,v48,v49
MM a[72:75],a[160:163],v[72:75],a[72:75]
EX v50,v50
MM a[80:83],a[160:163],v[80:83],a[80:83]
s_add_u32 m0, s91, 143360
s_add_u32 s77, s90, 40960
buffer_load_dwordx4 v253, s[28:31], s77 offen offset:0 lds
MM a[80:83],a[164:167],v[84:87],a[80:83]
EX v51,v51
MM a[88:91],a[164:167],v[92:95],a[88:91]
RD a[244:247],v225 offset:28672
CV v21,v50,v51
MM a[88:91],a[160:163],v[88:91],a[88:91]
EX v52,v52
MM a[92:95],a[168:171],v[88:91],a[92:95]
EX v53,v53
MM a[92:95],a[172:175],v[92:95],a[92:95]
RD a[248:251],v226 offset:28672
CV v22,v52,v53
MM a[84:87],a[172:175],v[84:87],a[84:87]
s_add_u32 m0, s65, 114688
)ASM"
R"ASM(
s_nop 0
buffer_load_dwordx4 v235, s[12:15], s81 offen offset:2048 lds
MM a[84:87],a[168:171],v[80:83],a[84:87]
EX v54,v54
MM a[76:79],a[168:171],v[72:75],a[76:79]
EX v55,v55
MM a[76:79],a[172:175],v[76:79],a[76:79]
RD a[252:255],v227 offset:28672
CV v23,v54,v55
MM a[68:71],a[172:175],v[68:71],a[68:71]
EX v56,v56
MM a[68:71],a[168:171],v[64:67],a[68:71]
EX v57,v57
MM a[96:99],a[176:179],v[64:67],a[96:99]
buffer_load_dwordx4 v235, s[12:15], s64 offen offset:3072 lds
MM a[96:99],a[180:183],v[68:71],a[96:99]
EX v58,v58
MM a[104:107],a[180:183],v[76:79],a[104:107]
EX v59,v59
MM a[104:107],a[176:179],v[72:75],a[104:107]
EX v60,v60
MM a[112:115],a[176:179],v[80:83],a[112:115]
EX v61,v61
MM a[112:115],a[180:183],v[84:87],a[112:115]
EX v62,v62
MM a[120:123],a[180:183],v[92:95],a[120:123]
s_add_u32 m0, s91, 144384
s_add_u32 s77, s90, 51200
buffer_load_dwordx4 v253, s[28:31], s77 offen offset:0 lds
MM a[120:123],a[176:179],v[88:91],a[120:123]
EX v63,v63
MM a[124:127],a[184:187],v[88:91],a[124:127]
v_add_f32 v244, v144, v145
v_add_f32 v245, v148, v149
MM a[124:127],a[188:191],v[92:95],a[124:127]
v_add_f32 v246, v152, v153
v_add_f32 v247, v156, v157
MM a[116:119],a[188:191],v[84:87],a[116:119]
CV v28,v56,v57
CV v29,v58,v59
MM a[116:119],a[184:187],v[80:83],a[116:119]
CV v30,v60,v61
CV v31,v62,v63
MM a[108:111],a[184:187],v[72:75],a[108:111]
MM a[108:111],a[188:191],v[76:79],a[108:111]
MM a[100:103],a[188:191],v[68:71],a[100:103]
MM a[100:103],a[184:187],v[64:67],a[100:103]
v_smfmac_f32_16x16x64_bf16 v[144:147], v[236:239], v[0:7], v234
v_smfmac_f32_16x16x64_bf16 v[148:151], v[236:239], v[8:15], v234
v_smfmac_f32_16x16x64_bf16 v[152:155], v[236:239], v[16:23], v234
v_smfmac_f32_16x16x64_bf16 v[156:159], v[236:239], v[24:31], v234
s_waitcnt lgkmcnt(0)
MM v[64:67],a[192:195],v[160:163],v[128:131]
s_add_u32 s64, s64, 16384
TR a[128:129],v232 offset:0
s_add_u32 s81, s64, 896
MM v[64:67],a[196:199],v[176:179],v[64:67]
TR a[130:131],v232 offset:4096
v_max3_f32 v249, v144, v145, v148
MM v[64:67],a[200:203],v[192:195],v[64:67]
TR a[132:133],v232 offset:8192
v_max3_f32 v249, v249, v149, v152
MM v[64:67],a[204:207],v[208:211],v[64:67]
TR a[134:135],v232 offset:12288
v_max3_f32 v249, v249, v153, v156
MM v[68:71],a[220:223],v[208:211],v[128:131]
TR a[136:137],v233 offset:0
v_max_f32 v249, v249, v157
MM v[68:71],a[216:219],v[192:195],v[68:71]
TR a[138:139],v233 offset:4096
v_cmp_lt_f32 vcc, s71, v249
MM v[68:71],a[212:215],v[176:179],v[68:71]
TR a[140:141],v233 offset:8192
TR a[142:143],v233 offset:12288
MM v[68:71],a[208:211],v[160:163],v[68:71]
s_cbranch_vccnz LSITE20_%=
LBACK20_%=:
MM v[76:79],a[208:211],v[164:167],v[132:135]
v_bfe_u32 v253, v248, 4, 4
v_lshlrev_b32 v253, 2, v253
MM v[76:79],a[212:215],v[180:183],v[76:79]
s_and_b32 s76, s18, 0xffffffc0
EX v64,v64
MM v[76:79],a[216:219],v[196:199],v[76:79]
s_add_u32 s76, s76, 77824
buffer_load_dword v252, v253, s[20:23], s76 offen offset:0 sc1
MM v[76:79],a[220:223],v[212:215],v[76:79]
EX v65,v65
MM v[72:75],a[204:207],v[212:215],v[132:135]
EX v66,v66
MM v[72:75],a[200:203],v[196:199],v[72:75]
TR a[144:145],v232 offset:512
CV v64,v64,v65
MM v[72:75],a[196:199],v[180:183],v[72:75]
EX v67,v67
MM v[72:75],a[192:195],v[164:167],v[72:75]
TR a[146:147],v232 offset:4608
CV v65,v66,v67
MM v[80:83],a[192:195],v[168:171],v[136:139]
EX v68,v68
MM v[80:83],a[196:199],v[184:187],v[80:83]
TR a[148:149],v232 offset:8704
TR a[150:151],v232 offset:12800
MM v[80:83],a[200:203],v[200:203],v[80:83]
EX v69,v69
MM v[80:83],a[204:207],v[216:219],v[80:83]
TR a[152:153],v233 offset:512
CV v66,v68,v69
MM v[84:87],a[220:223],v[216:219],v[136:139]
EX v70,v70
MM v[84:87],a[216:219],v[200:203],v[84:87]
EX v71,v71
MM v[84:87],a[212:215],v[184:187],v[84:87]
TR a[154:155],v233 offset:4608
CV v67,v70,v71
MM v[84:87],a[208:211],v[168:171],v[84:87]
EX v72,v72
MM v[92:95],a[208:211],v[172:175],v[140:143]
TR a[156:157],v233 offset:8704
TR a[158:159],v233 offset:12800
MM v[92:95],a[212:215],v[188:191],v[92:95]
EX v73,v73
MM v[92:95],a[216:219],v[204:207],v[92:95]
TR a[160:161],v232 offset:1024
CV v72,v72,v73
MM v[92:95],a[220:223],v[220:223],v[92:95]
EX v74,v74
MM v[88:91],a[204:207],v[220:223],v[140:143]
EX v75,v75
MM v[88:91],a[200:203],v[204:207],v[88:91]
TR a[162:163],v232 offset:5120
CV v73,v74,v75
MM v[88:91],a[196:199],v[188:191],v[88:91]
EX v76,v76
MM v[88:91],a[192:195],v[172:175],v[88:91]
TR a[164:165],v232 offset:9216
TR a[166:167],v232 offset:13312
MM v[96:99],a[224:227],v[160:163],v[128:131]
EX v77,v77
MM v[96:99],a[228:231],v[176:179],v[96:99]
TR a[168:169],v233 offset:1024
CV v74,v76,v77
MM v[96:99],a[232:235],v[192:195],v[96:99]
EX v78,v78
MM v[96:99],a[236:239],v[208:211],v[96:99]
EX v79,v79
MM v[100:103],a[252:255],v[208:211],v[128:131]
TR a[170:171],v233 offset:5120
CV v75,v78,v79
MM v[100:103],a[248:251],v[192:195],v[100:103]
EX v80,v80
MM v[100:103],a[244:247],v[176:179],v[100:103]
TR a[172:173],v233 offset:9216
TR a[174:175],v233 offset:13312
MM v[100:103],a[240:243],v[160:163],v[100:103]
EX v81,v81
s_nop 0
LPG16_%=:
s_cbranch_execz LTR2_%=
MM v[108:111],a[240:243],v[164:167],v[132:135]
TR a[176:177],v232 offset:1536
CV v80,v80,v81
MM v[108:111],a[244:247],v[180:183],v[108:111]
EX v82,v82
MM v[108:111],a[248:251],v[196:199],v[108:111]
TR a[178:179],v232 offset:5632
TR a[180:181],v232 offset:9728
MM v[108:111],a[252:255],v[212:215],v[108:111]
EX v83,v83
MM v[104:107],a[236:239],v[212:215],v[132:135]
TR a[182:183],v232 offset:13824
CV v81,v82,v83
MM v[104:107],a[232:235],v[196:199],v[104:107]
EX v84,v84
MM v[104:107],a[228:231],v[180:183],v[104:107]
EX v85,v85
MM v[104:107],a[224:227],v[164:167],v[104:107]
TR a[184:185],v233 offset:1536
CV v82,v84,v85
MM v[112:115],a[224:227],v[168:171],v[136:139]
EX v86,v86
MM v[112:115],a[228:231],v[184:187],v[112:115]
TR a[186:187],v233 offset:5632
TR a[188:189],v233 offset:9728
MM v[112:115],a[232:235],v[200:203],v[112:115]
EX v87,v87
MM v[112:115],a[236:239],v[216:219],v[112:115]
TR a[190:191],v233 offset:13824
CV v83,v86,v87
MM v[116:119],a[252:255],v[216:219],v[136:139]
EX v88,v88
MM v[116:119],a[248:251],v[200:203],v[116:119]
EX v89,v89
MM v[116:119],a[244:247],v[184:187],v[116:119]
EX v90,v90
MM v[116:119],a[240:243],v[168:171],v[116:119]
EX v91,v91
MM v[124:127],a[240:243],v[172:175],v[140:143]
EX v92,v92
MM v[124:127],a[244:247],v[188:191],v[124:127]
EX v93,v93
MM v[124:127],a[248:251],v[204:207],v[124:127]
EX v94,v94
MM v[124:127],a[252:255],v[220:223],v[124:127]
EX v95,v95
MM v[120:123],a[236:239],v[220:223],v[140:143]
EX v96,v96
MM v[120:123],a[232:235],v[204:207],v[120:123]
EX v97,v97
MM v[120:123],a[228:231],v[188:191],v[120:123]
s_getpc_b64 s[98:99]
LTA3_%=:
s_min_u32 s73, %[swave], 3
s_lshl_b32 s73, s73, 12
s_add_u32 s73, s73, LPG17_%=-LTA3_%=
s_add_u32 s98, s98, s73
s_addc_u32 s99, s99, 0
s_mov_b64 exec, 0
s_setpc_b64 s[98:99]
LTR3_%=:
s_mov_b64 exec, -1
CV v88,v88,v89
MM v[120:123],a[224:227],v[172:175],v[120:123]
s_waitcnt vmcnt(20) lgkmcnt(0)
s_barrier
MM a[0:3],a[128:131],v[0:3],a[0:3]
s_add_u32 m0, s65, 0
CV v89,v90,v91
CV v90,v92,v93
MM a[0:3],a[132:135],v[4:7],a[0:3]
RD a[192:195],v224 offset:32768
CV v91,v94,v95
MM a[8:11],a[132:135],v[12:15],a[8:11]
buffer_load_dwordx4 v228, s[8:11], s64 offen offset:0 lds
MM a[8:11],a[128:131],v[8:11],a[8:11]
RD a[196:199],v225 offset:32768
CV v68,v96,v97
MM a[16:19],a[128:131],v[16:19],a[16:19]
RD a[200:203],v226 offset:32768
RD a[204:207],v227 offset:32768
MM a[16:19],a[132:135],v[20:23],a[16:19]
buffer_load_dwordx4 v229, s[8:11], s64 offen offset:1024 lds
MM a[24:27],a[132:135],v[28:31],a[24:27]
EX v98,v98
MM a[24:27],a[128:131],v[24:27],a[24:27]
RD a[208:211],v224 offset:36864
RD a[212:215],v225 offset:36864
MM a[28:31],a[136:139],v[24:27],a[28:31]
EX v99,v99
MM a[28:31],a[140:143],v[28:31],a[28:31]
RD a[216:219],v226 offset:36864
CV v69,v98,v99
MM a[20:23],a[140:143],v[20:23],a[20:23]
EX v100,v100
MM a[20:23],a[136:139],v[16:19],a[20:23]
EX v101,v101
MM a[12:15],a[136:139],v[8:11],a[12:15]
RD a[220:223],v227 offset:36864
CV v70,v100,v101
MM a[12:15],a[140:143],v[12:15],a[12:15]
buffer_load_dwordx4 v230, s[8:11], s64 offen offset:2048 lds
MM a[4:7],a[140:143],v[4:7],a[4:7]
EX v102,v102
MM a[4:7],a[136:139],v[0:3],a[4:7]
EX v103,v103
MM a[32:35],a[144:147],v[0:3],a[32:35]
RD a[224:227],v224 offset:40960
CV v71,v102,v103
MM a[32:35],a[148:151],v[4:7],a[32:35]
EX v104,v104
MM a[40:43],a[148:151],v[12:15],a[40:43]
EX v105,v105
MM a[40:43],a[144:147],v[8:11],a[40:43]
RD a[228:231],v225 offset:40960
CV v76,v104,v105
MM a[48:51],a[144:147],v[16:19],a[48:51]
buffer_load_dwordx4 v231, s[8:11], s64 offen offset:3072 lds
MM a[48:51],a[148:151],v[20:23],a[48:51]
s_add_u32 m0, s65, 65536
EX v106,v106
MM a[56:59],a[148:151],v[28:31],a[56:59]
EX v107,v107
MM a[56:59],a[144:147],v[24:27],a[56:59]
RD a[232:235],v226 offset:40960
CV v77,v106,v107
MM a[60:63],a[152:155],v[24:27],a[60:63]
EX v108,v108
MM a[60:63],a[156:159],v[28:31],a[60:63]
buffer_load_dwordx4 v235, s[12:15], s81 offen offset:0 lds
MM a[52:55],a[156:159],v[20:23],a[52:55]
EX v109,v109
MM a[52:55],a[152:155],v[16:19],a[52:55]
RD a[236:239],v227 offset:40960
CV v78,v108,v109
MM a[44:47],a[152:155],v[8:11],a[44:47]
EX v110,v110
MM a[44:47],a[156:159],v[12:15],a[44:47]
EX v111,v111
MM a[36:39],a[156:159],v[4:7],a[36:39]
RD a[240:243],v224 offset:45056
CV v79,v110,v111
MM a[36:39],a[152:155],v[0:3],a[36:39]
EX v112,v112
MM a[64:67],a[160:163],v[0:3],a[64:67]
buffer_load_dwordx4 v235, s[12:15], s64 offen offset:1024 lds
MM a[64:67],a[164:167],v[4:7],a[64:67]
EX v113,v113
MM a[72:75],a[164:167],v[12:15],a[72:75]
RD a[244:247],v225 offset:45056
CV v84,v112,v113
MM a[72:75],a[160:163],v[8:11],a[72:75]
EX v114,v114
MM a[80:83],a[160:163],v[16:19],a[80:83]
EX v115,v115
MM a[80:83],a[164:167],v[20:23],a[80:83]
RD a[248:251],v226 offset:45056
CV v85,v114,v115
MM a[88:91],a[164:167],v[28:31],a[88:91]
EX v116,v116
MM a[88:91],a[160:163],v[24:27],a[88:91]
buffer_load_dwordx4 v235, s[12:15], s81 offen offset:2048 lds
MM a[92:95],a[168:171],v[24:27],a[92:95]
EX v117,v117
MM a[92:95],a[172:175],v[28:31],a[92:95]
RD a[252:255],v227 offset:45056
CV v86,v116,v117
MM a[84:87],a[172:175],v[20:23],a[84:87]
EX v118,v118
MM a[84:87],a[168:171],v[16:19],a[84:87]
EX v119,v119
MM a[76:79],a[168:171],v[8:11],a[76:79]
CV v87,v118,v119
MM a[76:79],a[172:175],v[12:15],a[76:79]
EX v120,v120
MM a[68:71],a[172:175],v[4:7],a[68:71]
buffer_load_dwordx4 v235, s[12:15], s64 offen offset:3072 lds
MM a[68:71],a[168:171],v[0:3],a[68:71]
EX v121,v121
MM a[96:99],a[176:179],v[0:3],a[96:99]
EX v122,v122
MM a[96:99],a[180:183],v[4:7],a[96:99]
EX v123,v123
MM a[104:107],a[180:183],v[12:15],a[104:107]
EX v124,v124
MM a[104:107],a[176:179],v[8:11],a[104:107]
EX v125,v125
MM a[112:115],a[176:179],v[16:19],a[112:115]
EX v126,v126
MM a[112:115],a[180:183],v[20:23],a[112:115]
EX v127,v127
MM a[120:123],a[180:183],v[28:31],a[120:123]
s_waitcnt vmcnt(8)
v_cmp_ne_u32 vcc, s69, v252
s_cbranch_vccnz LRFG_%=
LRFGb_%=:
MM a[120:123],a[176:179],v[24:27],a[120:123]
v_lshrrev_b32 v253, 8, v248
v_lshlrev_b32 v253, 10, v253
MM a[124:127],a[184:187],v[24:27],a[124:127]
v_add_f32 v244, v144, v145
v_add_f32 v245, v148, v149
MM a[124:127],a[188:191],v[28:31],a[124:127]
v_add_f32 v246, v152, v153
v_add_f32 v247, v156, v157
v_and_b32 v252, 240, v248
MM a[116:119],a[188:191],v[20:23],a[116:119]
v_add_u32 v253, v253, v252
CV v92,v120,v121
MM a[116:119],a[184:187],v[16:19],a[116:119]
s_lshr_b32 s76, s18, 6
CV v93,v122,v123
CV v94,v124,v125
MM a[108:111],a[184:187],v[8:11],a[108:111]
s_lshl_b32 s76, s76, 13
s_bfe_u32 s90, s18, 131076
CV v95,v126,v127
MM a[108:111],a[188:191],v[12:15],a[108:111]
s_lshl_b32 s90, s90, 8
s_add_u32 s76, s76, s90
s_add_u32 s76, s76, 22183936
MM a[100:103],a[188:191],v[4:7],a[100:103]
s_lshl_b32 s90, %[swave], 11
s_add_u32 s90, s90, %[slds]
MM a[100:103],a[184:187],v[0:3],a[100:103]
s_add_u32 m0, s90, 131072
s_nop 0
buffer_load_dwordx4 v253, s[20:23], s76 offen offset:0 sc1 lds
v_smfmac_f32_16x16x64_bf16 v[144:147], v[236:239], v[64:71], v234
v_smfmac_f32_16x16x64_bf16 v[148:151], v[236:239], v[72:79], v234
)ASM"
R"ASM(
v_smfmac_f32_16x16x64_bf16 v[152:155], v[236:239], v[80:87], v234
s_add_u32 m0, s90, 132096
s_add_u32 s76, s76, 4096
buffer_load_dwordx4 v253, s[20:23], s76 offen offset:0 sc1 lds
v_smfmac_f32_16x16x64_bf16 v[156:159], v[236:239], v[88:95], v234
s_waitcnt lgkmcnt(0)
s_add_u32 s67, s95, 3072
s_mov_b32 s70, 3
s_cmp_ge_u32 s16, 90
s_cbranch_scc1 LLAP_%=
MM v[0:3],a[192:195],v[160:163],v[128:131]
s_add_u32 s64, s64, 16384
TR a[128:129],v232 offset:16384
s_add_u32 s81, s64, 896
MM v[0:3],a[196:199],v[176:179],v[0:3]
TR a[130:131],v232 offset:20480
v_max3_f32 v249, v144, v145, v148
MM v[0:3],a[200:203],v[192:195],v[0:3]
TR a[132:133],v232 offset:24576
v_max3_f32 v249, v249, v149, v152
MM v[0:3],a[204:207],v[208:211],v[0:3]
TR a[134:135],v232 offset:28672
v_max3_f32 v249, v249, v153, v156
MM v[4:7],a[220:223],v[208:211],v[128:131]
TR a[136:137],v233 offset:16384
v_max_f32 v249, v249, v157
MM v[4:7],a[216:219],v[192:195],v[4:7]
TR a[138:139],v233 offset:20480
v_cmp_lt_f32 vcc, s71, v249
MM v[4:7],a[212:215],v[176:179],v[4:7]
TR a[140:141],v233 offset:24576
TR a[142:143],v233 offset:28672
MM v[4:7],a[208:211],v[160:163],v[4:7]
s_cbranch_vccnz LSITE21_%=
LBACK21_%=:
MM v[12:15],a[208:211],v[164:167],v[132:135]
EX v0,v0
MM v[12:15],a[212:215],v[180:183],v[12:15]
EX v1,v1
MM v[12:15],a[216:219],v[196:199],v[12:15]
TR a[144:145],v232 offset:16896
CV v0,v0,v1
MM v[12:15],a[220:223],v[212:215],v[12:15]
EX v2,v2
MM v[8:11],a[204:207],v[212:215],v[132:135]
EX v3,v3
MM v[8:11],a[200:203],v[196:199],v[8:11]
TR a[146:147],v232 offset:20992
CV v1,v2,v3
MM v[8:11],a[196:199],v[180:183],v[8:11]
EX v4,v4
MM v[8:11],a[192:195],v[164:167],v[8:11]
TR a[148:149],v232 offset:25088
TR a[150:151],v232 offset:29184
MM v[16:19],a[192:195],v[168:171],v[136:139]
EX v5,v5
MM v[16:19],a[196:199],v[184:187],v[16:19]
TR a[152:153],v233 offset:16896
CV v2,v4,v5
MM v[16:19],a[200:203],v[200:203],v[16:19]
EX v6,v6
MM v[16:19],a[204:207],v[216:219],v[16:19]
TR a[154:155],v233 offset:20992
TR a[156:157],v233 offset:25088
MM v[20:23],a[220:223],v[216:219],v[136:139]
s_add_u32 s87, s95, 3072
buffer_store_dword v243, v248, s[20:23], s87 offen offset:12 sc1
MM v[20:23],a[216:219],v[200:203],v[20:23]
EX v7,v7
MM v[20:23],a[212:215],v[184:187],v[20:23]
TR a[158:159],v233 offset:29184
CV v3,v6,v7
MM v[20:23],a[208:211],v[168:171],v[20:23]
EX v8,v8
MM v[28:31],a[208:211],v[172:175],v[140:143]
EX v9,v9
MM v[28:31],a[212:215],v[188:191],v[28:31]
TR a[160:161],v232 offset:17408
CV v8,v8,v9
MM v[28:31],a[216:219],v[204:207],v[28:31]
EX v10,v10
MM v[28:31],a[220:223],v[220:223],v[28:31]
EX v11,v11
MM v[24:27],a[204:207],v[220:223],v[140:143]
TR a[162:163],v232 offset:21504
CV v9,v10,v11
MM v[24:27],a[200:203],v[204:207],v[24:27]
TR a[164:165],v232 offset:25600
TR a[166:167],v232 offset:29696
MM v[24:27],a[196:199],v[188:191],v[24:27]
EX v12,v12
MM v[24:27],a[192:195],v[172:175],v[24:27]
EX v13,v13
MM v[32:35],a[224:227],v[160:163],v[128:131]
TR a[168:169],v233 offset:17408
CV v10,v12,v13
MM v[32:35],a[228:231],v[176:179],v[32:35]
EX v14,v14
MM v[32:35],a[232:235],v[192:195],v[32:35]
TR a[170:171],v233 offset:21504
TR a[172:173],v233 offset:25600
MM v[32:35],a[236:239],v[208:211],v[32:35]
EX v15,v15
MM v[36:39],a[252:255],v[208:211],v[128:131]
TR a[174:175],v233 offset:29696
CV v11,v14,v15
MM v[36:39],a[248:251],v[192:195],v[36:39]
EX v16,v16
MM v[36:39],a[244:247],v[176:179],v[36:39]
EX v17,v17
MM v[36:39],a[240:243],v[160:163],v[36:39]
TR a[176:177],v232 offset:17920
CV v16,v16,v17
MM v[44:47],a[240:243],v[164:167],v[132:135]
EX v18,v18
MM v[44:47],a[244:247],v[180:183],v[44:47]
TR a[178:179],v232 offset:22016
TR a[180:181],v232 offset:26112
MM v[44:47],a[248:251],v[196:199],v[44:47]
EX v19,v19
MM v[44:47],a[252:255],v[212:215],v[44:47]
TR a[182:183],v232 offset:30208
CV v17,v18,v19
MM v[40:43],a[236:239],v[212:215],v[132:135]
EX v20,v20
MM v[40:43],a[232:235],v[196:199],v[40:43]
EX v21,v21
MM v[40:43],a[228:231],v[180:183],v[40:43]
TR a[184:185],v233 offset:17920
CV v18,v20,v21
MM v[40:43],a[224:227],v[164:167],v[40:43]
EX v22,v22
MM v[48:51],a[224:227],v[168:171],v[136:139]
TR a[186:187],v233 offset:22016
TR a[188:189],v233 offset:26112
MM v[48:51],a[228:231],v[184:187],v[48:51]
EX v23,v23
MM v[48:51],a[232:235],v[200:203],v[48:51]
TR a[190:191],v233 offset:30208
CV v19,v22,v23
MM v[48:51],a[236:239],v[216:219],v[48:51]
EX v24,v24
MM v[52:55],a[252:255],v[216:219],v[136:139]
EX v25,v25
MM v[52:55],a[248:251],v[200:203],v[52:55]
EX v26,v26
MM v[52:55],a[244:247],v[184:187],v[52:55]
EX v27,v27
MM v[52:55],a[240:243],v[168:171],v[52:55]
EX v28,v28
MM v[60:63],a[240:243],v[172:175],v[140:143]
EX v29,v29
MM v[60:63],a[244:247],v[188:191],v[60:63]
EX v30,v30
MM v[60:63],a[248:251],v[204:207],v[60:63]
EX v31,v31
MM v[60:63],a[252:255],v[220:223],v[60:63]
EX v32,v32
MM v[56:59],a[236:239],v[220:223],v[140:143]
EX v33,v33
MM v[56:59],a[232:235],v[204:207],v[56:59]
EX v34,v34
MM v[56:59],a[228:231],v[188:191],v[56:59]
EX v35,v35
MM v[56:59],a[224:227],v[172:175],v[56:59]
s_waitcnt lgkmcnt(0)
s_barrier
MM a[0:3],a[128:131],v[64:67],a[0:3]
s_add_u32 m0, s65, 16384
CV v24,v24,v25
CV v25,v26,v27
MM a[0:3],a[132:135],v[68:71],a[0:3]
RD a[192:195],v224 offset:49152
CV v26,v28,v29
MM a[8:11],a[132:135],v[76:79],a[8:11]
RD a[196:199],v225 offset:49152
CV v27,v30,v31
MM a[8:11],a[128:131],v[72:75],a[8:11]
buffer_load_dwordx4 v228, s[8:11], s64 offen offset:0 lds
MM a[16:19],a[128:131],v[80:83],a[16:19]
RD a[200:203],v226 offset:49152
CV v4,v32,v33
MM a[16:19],a[132:135],v[84:87],a[16:19]
RD a[204:207],v227 offset:49152
CV v5,v34,v35
MM a[24:27],a[132:135],v[92:95],a[24:27]
buffer_load_dwordx4 v229, s[8:11], s64 offen offset:1024 lds
MM a[24:27],a[128:131],v[88:91],a[24:27]
RD a[208:211],v224 offset:53248
RD a[212:215],v225 offset:53248
MM a[28:31],a[136:139],v[88:91],a[28:31]
buffer_load_dwordx4 v230, s[8:11], s64 offen offset:2048 lds
MM a[28:31],a[140:143],v[92:95],a[28:31]
EX v36,v36
MM a[20:23],a[140:143],v[84:87],a[20:23]
EX v37,v37
MM a[20:23],a[136:139],v[80:83],a[20:23]
RD a[216:219],v226 offset:53248
CV v6,v36,v37
MM a[12:15],a[136:139],v[72:75],a[12:15]
EX v38,v38
MM a[12:15],a[140:143],v[76:79],a[12:15]
RD a[220:223],v227 offset:53248
RD a[224:227],v224 offset:57344
MM a[4:7],a[140:143],v[68:71],a[4:7]
EX v39,v39
MM a[4:7],a[136:139],v[64:67],a[4:7]
RD a[228:231],v225 offset:57344
CV v7,v38,v39
MM a[32:35],a[144:147],v[64:67],a[32:35]
EX v40,v40
MM a[32:35],a[148:151],v[68:71],a[32:35]
buffer_load_dwordx4 v231, s[8:11], s64 offen offset:3072 lds
MM a[40:43],a[148:151],v[76:79],a[40:43]
s_add_u32 m0, s65, 81920
EX v41,v41
MM a[40:43],a[144:147],v[72:75],a[40:43]
RD a[232:235],v226 offset:57344
CV v12,v40,v41
MM a[48:51],a[144:147],v[80:83],a[48:51]
EX v42,v42
MM a[48:51],a[148:151],v[84:87],a[48:51]
EX v43,v43
MM a[56:59],a[148:151],v[92:95],a[56:59]
RD a[236:239],v227 offset:57344
CV v13,v42,v43
MM a[56:59],a[144:147],v[88:91],a[56:59]
EX v44,v44
MM a[60:63],a[152:155],v[88:91],a[60:63]
buffer_load_dwordx4 v235, s[12:15], s81 offen offset:0 lds
MM a[60:63],a[156:159],v[92:95],a[60:63]
EX v45,v45
MM a[52:55],a[156:159],v[84:87],a[52:55]
RD a[240:243],v224 offset:61440
CV v14,v44,v45
MM a[52:55],a[152:155],v[80:83],a[52:55]
EX v46,v46
MM a[44:47],a[152:155],v[72:75],a[44:47]
EX v47,v47
MM a[44:47],a[156:159],v[76:79],a[44:47]
RD a[244:247],v225 offset:61440
CV v15,v46,v47
MM a[36:39],a[156:159],v[68:71],a[36:39]
buffer_load_dwordx4 v235, s[12:15], s64 offen offset:1024 lds
MM a[36:39],a[152:155],v[64:67],a[36:39]
EX v48,v48
MM a[64:67],a[160:163],v[64:67],a[64:67]
EX v49,v49
MM a[64:67],a[164:167],v[68:71],a[64:67]
RD a[248:251],v226 offset:61440
CV v20,v48,v49
MM a[72:75],a[164:167],v[76:79],a[72:75]
EX v50,v50
MM a[72:75],a[160:163],v[72:75],a[72:75]
EX v51,v51
MM a[80:83],a[160:163],v[80:83],a[80:83]
RD a[252:255],v227 offset:61440
CV v21,v50,v51
MM a[80:83],a[164:167],v[84:87],a[80:83]
buffer_load_dwordx4 v235, s[12:15], s81 offen offset:2048 lds
MM a[88:91],a[164:167],v[92:95],a[88:91]
EX v52,v52
MM a[88:91],a[160:163],v[88:91],a[88:91]
EX v53,v53
MM a[92:95],a[168:171],v[88:91],a[92:95]
EX v54,v54
MM a[92:95],a[172:175],v[92:95],a[92:95]
buffer_load_dwordx4 v235, s[12:15], s64 offen offset:3072 lds
MM a[84:87],a[172:175],v[84:87],a[84:87]
EX v55,v55
MM a[84:87],a[168:171],v[80:83],a[84:87]
EX v56,v56
MM a[76:79],a[168:171],v[72:75],a[76:79]
EX v57,v57
MM a[76:79],a[172:175],v[76:79],a[76:79]
EX v58,v58
MM a[68:71],a[172:175],v[68:71],a[68:71]
EX v59,v59
MM a[68:71],a[168:171],v[64:67],a[68:71]
EX v60,v60
MM a[96:99],a[176:179],v[64:67],a[96:99]
EX v61,v61
MM a[96:99],a[180:183],v[68:71],a[96:99]
EX v62,v62
MM a[104:107],a[180:183],v[76:79],a[104:107]
EX v63,v63
MM a[104:107],a[176:179],v[72:75],a[104:107]
CV v22,v52,v53
CV v23,v54,v55
MM a[112:115],a[176:179],v[80:83],a[112:115]
CV v28,v56,v57
CV v29,v58,v59
MM a[112:115],a[180:183],v[84:87],a[112:115]
CV v30,v60,v61
CV v31,v62,v63
MM a[120:123],a[180:183],v[92:95],a[120:123]
MM a[120:123],a[176:179],v[88:91],a[120:123]
MM a[124:127],a[184:187],v[88:91],a[124:127]
v_add_f32 v244, v144, v145
v_add_f32 v245, v148, v149
MM a[124:127],a[188:191],v[92:95],a[124:127]
v_add_f32 v246, v152, v153
v_add_f32 v247, v156, v157
MM a[116:119],a[188:191],v[84:87],a[116:119]
MM a[116:119],a[184:187],v[80:83],a[116:119]
MM a[108:111],a[184:187],v[72:75],a[108:111]
MM a[108:111],a[188:191],v[76:79],a[108:111]
MM a[100:103],a[188:191],v[68:71],a[100:103]
MM a[100:103],a[184:187],v[64:67],a[100:103]
v_smfmac_f32_16x16x64_bf16 v[144:147], v[236:239], v[0:7], v234
v_smfmac_f32_16x16x64_bf16 v[148:151], v[236:239], v[8:15], v234
v_smfmac_f32_16x16x64_bf16 v[152:155], v[236:239], v[16:23], v234
v_smfmac_f32_16x16x64_bf16 v[156:159], v[236:239], v[24:31], v234
s_waitcnt lgkmcnt(0)
MM v[64:67],a[192:195],v[160:163],v[128:131]
s_add_u32 s64, s64, 16384
TR a[128:129],v232 offset:32768
s_add_u32 s81, s64, 896
MM v[64:67],a[196:199],v[176:179],v[64:67]
TR a[130:131],v232 offset:36864
v_max3_f32 v249, v144, v145, v148
MM v[64:67],a[200:203],v[192:195],v[64:67]
TR a[132:133],v232 offset:40960
v_max3_f32 v249, v249, v149, v152
MM v[64:67],a[204:207],v[208:211],v[64:67]
TR a[134:135],v232 offset:45056
v_max3_f32 v249, v249, v153, v156
MM v[68:71],a[220:223],v[208:211],v[128:131]
TR a[136:137],v233 offset:32768
v_max_f32 v249, v249, v157
MM v[68:71],a[216:219],v[192:195],v[68:71]
TR a[138:139],v233 offset:36864
v_cmp_lt_f32 vcc, s71, v249
MM v[68:71],a[212:215],v[176:179],v[68:71]
TR a[140:141],v233 offset:40960
TR a[142:143],v233 offset:45056
MM v[68:71],a[208:211],v[160:163],v[68:71]
s_cbranch_vccnz LSITE22_%=
LBACK22_%=:
MM v[76:79],a[208:211],v[164:167],v[132:135]
v_lshrrev_b32 v43, 7, v248
v_bfe_u32 v42, v248, 6, 1
MM v[76:79],a[212:215],v[180:183],v[76:79]
v_lshl_add_u32 v40, v42, 3, v43
v_lshlrev_b32 v40, 4, v40
MM v[76:79],a[216:219],v[196:199],v[76:79]
v_bfe_u32 v42, v248, 4, 2
v_lshl_add_u32 v40, v42, 2, v40
MM v[76:79],a[220:223],v[212:215],v[76:79]
s_lshl_b32 s76, %[swave], 11
EX v64,v64
MM v[72:75],a[204:207],v[212:215],v[132:135]
s_add_u32 s76, s76, %[slds]
EX v65,v65
MM v[72:75],a[200:203],v[196:199],v[72:75]
s_add_u32 s76, s76, 131072
v_add_u32 v40, s76, v40
CV v64,v64,v65
MM v[72:75],a[196:199],v[180:183],v[72:75]
s_waitcnt vmcnt(9)
ds_read_b32 v32, v40 offset:0
ds_read_b32 v36, v40 offset:256
MM v[72:75],a[192:195],v[164:167],v[72:75]
ds_read_b32 v33, v40 offset:512
ds_read_b32 v37, v40 offset:768
MM v[80:83],a[192:195],v[168:171],v[136:139]
ds_read_b32 v34, v40 offset:1024
s_nop 0
LPG17_%=:
s_cbranch_execz LTR3_%=
ds_read_b32 v38, v40 offset:1280
EX v66,v66
MM v[80:83],a[196:199],v[184:187],v[80:83]
ds_read_b32 v35, v40 offset:1536
ds_read_b32 v39, v40 offset:1792
EX v67,v67
MM v[80:83],a[200:203],v[200:203],v[80:83]
v_mul_u32_u24 v41, 3456, v43
v_bfe_u32 v42, v248, 4, 3
MM v[80:83],a[204:207],v[216:219],v[80:83]
TR a[144:145],v232 offset:33280
v_lshl_add_u32 v41, v42, 1, v41
EX v68,v68
MM v[84:87],a[220:223],v[216:219],v[136:139]
s_add_u32 s76, s18, 21626880
EX v69,v69
MM v[84:87],a[216:219],v[200:203],v[84:87]
)ASM"
R"ASM(
TR a[146:147],v232 offset:37376
TR a[148:149],v232 offset:41472
MM v[84:87],a[212:215],v[184:187],v[84:87]
EX v70,v70
MM v[84:87],a[208:211],v[168:171],v[84:87]
TR a[150:151],v232 offset:45568
CV v65,v66,v67
MM v[92:95],a[208:211],v[172:175],v[140:143]
EX v71,v71
MM v[92:95],a[212:215],v[188:191],v[92:95]
EX v72,v72
MM v[92:95],a[216:219],v[204:207],v[92:95]
TR a[152:153],v233 offset:33280
CV v66,v68,v69
MM v[92:95],a[220:223],v[220:223],v[92:95]
EX v73,v73
MM v[88:91],a[204:207],v[220:223],v[140:143]
s_waitcnt lgkmcnt(5)
v_add_f32 v32, v32, v33
v_add_f32 v36, v36, v37
MM v[88:91],a[200:203],v[204:207],v[88:91]
v_add_f32 v34, v34, v35
v_add_f32 v38, v38, v39
MM v[88:91],a[196:199],v[188:191],v[88:91]
TR a[154:155],v233 offset:37376
v_add_f32 v32, v32, v34
v_add_f32 v36, v36, v38
MM v[88:91],a[192:195],v[172:175],v[88:91]
CV v42,v32,v36
v_lshlrev_b32 v32, 16, v42
EX v74,v74
MM v[96:99],a[224:227],v[160:163],v[128:131]
TR a[156:157],v233 offset:41472
v_and_b32 v36, 0xffff0000, v42
v_mul_f32 v42, 0xbfb8aa3b, v32
EX v75,v75
MM v[96:99],a[228:231],v[176:179],v[96:99]
EX v42,v42
MM v[96:99],a[232:235],v[192:195],v[96:99]
TR a[158:159],v233 offset:45568
v_add_f32 v42, 1.0, v42
EX v76,v76
MM v[96:99],a[236:239],v[208:211],v[96:99]
v_rcp_f32 v42, v42
MM v[100:103],a[252:255],v[208:211],v[128:131]
TR a[160:161],v232 offset:33792
v_mul_f32 v42, v32, v42
CV v42,v42,v42
EX v77,v77
CV v67,v70,v71
MM v[100:103],a[248:251],v[192:195],v[100:103]
v_lshlrev_b32 v42, 16, v42
v_mul_f32 v32, v42, v36
MM v[100:103],a[244:247],v[176:179],v[100:103]
TR a[162:163],v232 offset:37888
CV v43,v32,v32
EX v78,v78
MM v[100:103],a[240:243],v[160:163],v[100:103]
buffer_store_short v43, v41, s[20:23], s76 offen offset:0 sc1
s_nop 1
TR a[164:165],v232 offset:41984
MM v[108:111],a[240:243],v[164:167],v[132:135]
EX v79,v79
CV v72,v72,v73
MM v[108:111],a[244:247],v[180:183],v[108:111]
TR a[166:167],v232 offset:46080
CV v73,v74,v75
MM v[108:111],a[248:251],v[196:199],v[108:111]
EX v80,v80
MM v[108:111],a[252:255],v[212:215],v[108:111]
TR a[168:169],v233 offset:33792
TR a[170:171],v233 offset:37888
EX v81,v81
MM v[104:107],a[236:239],v[212:215],v[132:135]
TR a[172:173],v233 offset:41984
CV v74,v76,v77
MM v[104:107],a[232:235],v[196:199],v[104:107]
EX v82,v82
MM v[104:107],a[228:231],v[180:183],v[104:107]
EX v83,v83
MM v[104:107],a[224:227],v[164:167],v[104:107]
TR a[174:175],v233 offset:46080
CV v75,v78,v79
MM v[112:115],a[224:227],v[168:171],v[136:139]
EX v84,v84
MM v[112:115],a[228:231],v[184:187],v[112:115]
TR a[176:177],v232 offset:34304
TR a[178:179],v232 offset:38400
MM v[112:115],a[232:235],v[200:203],v[112:115]
EX v85,v85
MM v[112:115],a[236:239],v[216:219],v[112:115]
TR a[180:181],v232 offset:42496
CV v80,v80,v81
MM v[116:119],a[252:255],v[216:219],v[136:139]
EX v86,v86
MM v[116:119],a[248:251],v[200:203],v[116:119]
TR a[182:183],v232 offset:46592
CV v81,v82,v83
MM v[116:119],a[244:247],v[184:187],v[116:119]
EX v87,v87
MM v[116:119],a[240:243],v[168:171],v[116:119]
TR a[184:185],v233 offset:34304
CV v82,v84,v85
MM v[124:127],a[240:243],v[172:175],v[140:143]
EX v88,v88
MM v[124:127],a[244:247],v[188:191],v[124:127]
TR a[186:187],v233 offset:38400
TR a[188:189],v233 offset:42496
MM v[124:127],a[248:251],v[204:207],v[124:127]
TR a[190:191],v233 offset:46592
CV v83,v86,v87
EX v89,v89
MM v[124:127],a[252:255],v[220:223],v[124:127]
EX v90,v90
MM v[120:123],a[236:239],v[220:223],v[140:143]
EX v91,v91
MM v[120:123],a[232:235],v[204:207],v[120:123]
EX v92,v92
MM v[120:123],a[228:231],v[188:191],v[120:123]
EX v93,v93
MM v[120:123],a[224:227],v[172:175],v[120:123]
s_waitcnt lgkmcnt(0)
s_barrier
MM a[0:3],a[128:131],v[0:3],a[0:3]
s_add_u32 m0, s65, 32768
CV v88,v88,v89
CV v89,v90,v91
MM a[0:3],a[132:135],v[4:7],a[0:3]
EX v94,v94
MM a[8:11],a[132:135],v[12:15],a[8:11]
RD a[192:195],v224 offset:0
CV v90,v92,v93
MM a[8:11],a[128:131],v[8:11],a[8:11]
EX v95,v95
MM a[16:19],a[128:131],v[16:19],a[16:19]
RD a[196:199],v225 offset:0
CV v91,v94,v95
MM a[16:19],a[132:135],v[20:23],a[16:19]
EX v96,v96
MM a[24:27],a[132:135],v[28:31],a[24:27]
buffer_load_dwordx4 v228, s[8:11], s64 offen offset:0 lds
MM a[24:27],a[128:131],v[24:27],a[24:27]
EX v97,v97
MM a[28:31],a[136:139],v[24:27],a[28:31]
EX v98,v98
MM a[28:31],a[140:143],v[28:31],a[28:31]
RD a[200:203],v226 offset:0
CV v68,v96,v97
MM a[20:23],a[140:143],v[20:23],a[20:23]
EX v99,v99
MM a[20:23],a[136:139],v[16:19],a[20:23]
RD a[204:207],v227 offset:0
CV v69,v98,v99
MM a[12:15],a[136:139],v[8:11],a[12:15]
EX v100,v100
MM a[12:15],a[140:143],v[12:15],a[12:15]
buffer_load_dwordx4 v229, s[8:11], s64 offen offset:1024 lds
MM a[4:7],a[140:143],v[4:7],a[4:7]
EX v101,v101
MM a[4:7],a[136:139],v[0:3],a[4:7]
RD a[208:211],v224 offset:4096
CV v70,v100,v101
MM a[32:35],a[144:147],v[0:3],a[32:35]
EX v102,v102
MM a[32:35],a[148:151],v[4:7],a[32:35]
EX v103,v103
MM a[40:43],a[148:151],v[12:15],a[40:43]
RD a[212:215],v225 offset:4096
CV v71,v102,v103
MM a[40:43],a[144:147],v[8:11],a[40:43]
EX v104,v104
MM a[48:51],a[144:147],v[16:19],a[48:51]
buffer_load_dwordx4 v230, s[8:11], s64 offen offset:2048 lds
MM a[48:51],a[148:151],v[20:23],a[48:51]
EX v105,v105
MM a[56:59],a[148:151],v[28:31],a[56:59]
RD a[216:219],v226 offset:4096
CV v76,v104,v105
MM a[56:59],a[144:147],v[24:27],a[56:59]
EX v106,v106
MM a[60:63],a[152:155],v[24:27],a[60:63]
EX v107,v107
MM a[60:63],a[156:159],v[28:31],a[60:63]
RD a[220:223],v227 offset:4096
CV v77,v106,v107
MM a[52:55],a[156:159],v[20:23],a[52:55]
EX v108,v108
MM a[52:55],a[152:155],v[16:19],a[52:55]
buffer_load_dwordx4 v231, s[8:11], s64 offen offset:3072 lds
MM a[44:47],a[152:155],v[8:11],a[44:47]
s_add_u32 m0, s65, 98304
EX v109,v109
MM a[44:47],a[156:159],v[12:15],a[44:47]
RD a[224:227],v224 offset:8192
CV v78,v108,v109
MM a[36:39],a[156:159],v[4:7],a[36:39]
EX v110,v110
MM a[36:39],a[152:155],v[0:3],a[36:39]
EX v111,v111
MM a[64:67],a[160:163],v[0:3],a[64:67]
RD a[228:231],v225 offset:8192
CV v79,v110,v111
MM a[64:67],a[164:167],v[4:7],a[64:67]
EX v112,v112
MM a[72:75],a[164:167],v[12:15],a[72:75]
buffer_load_dwordx4 v235, s[12:15], s81 offen offset:0 lds
MM a[72:75],a[160:163],v[8:11],a[72:75]
EX v113,v113
MM a[80:83],a[160:163],v[16:19],a[80:83]
RD a[232:235],v226 offset:8192
CV v84,v112,v113
MM a[80:83],a[164:167],v[20:23],a[80:83]
EX v114,v114
MM a[88:91],a[164:167],v[28:31],a[88:91]
EX v115,v115
MM a[88:91],a[160:163],v[24:27],a[88:91]
RD a[236:239],v227 offset:8192
CV v85,v114,v115
MM a[92:95],a[168:171],v[24:27],a[92:95]
EX v116,v116
MM a[92:95],a[172:175],v[28:31],a[92:95]
buffer_load_dwordx4 v235, s[12:15], s64 offen offset:1024 lds
MM a[84:87],a[172:175],v[20:23],a[84:87]
EX v117,v117
MM a[84:87],a[168:171],v[16:19],a[84:87]
RD a[240:243],v224 offset:12288
CV v86,v116,v117
MM a[76:79],a[168:171],v[8:11],a[76:79]
EX v118,v118
MM a[76:79],a[172:175],v[12:15],a[76:79]
EX v119,v119
MM a[68:71],a[172:175],v[4:7],a[68:71]
RD a[244:247],v225 offset:12288
CV v87,v118,v119
MM a[68:71],a[168:171],v[0:3],a[68:71]
buffer_load_dwordx4 v235, s[12:15], s81 offen offset:2048 lds
MM a[96:99],a[176:179],v[0:3],a[96:99]
EX v120,v120
MM a[96:99],a[180:183],v[4:7],a[96:99]
EX v121,v121
MM a[104:107],a[180:183],v[12:15],a[104:107]
RD a[248:251],v226 offset:12288
CV v92,v120,v121
MM a[104:107],a[176:179],v[8:11],a[104:107]
EX v122,v122
MM a[112:115],a[176:179],v[16:19],a[112:115]
EX v123,v123
MM a[112:115],a[180:183],v[20:23],a[112:115]
RD a[252:255],v227 offset:12288
CV v93,v122,v123
MM a[120:123],a[180:183],v[28:31],a[120:123]
buffer_load_dwordx4 v235, s[12:15], s64 offen offset:3072 lds
MM a[120:123],a[176:179],v[24:27],a[120:123]
EX v124,v124
MM a[124:127],a[184:187],v[24:27],a[124:127]
v_add_f32 v244, v144, v145
v_add_f32 v245, v148, v149
MM a[124:127],a[188:191],v[28:31],a[124:127]
v_add_f32 v246, v152, v153
v_add_f32 v247, v156, v157
MM a[116:119],a[188:191],v[20:23],a[116:119]
EX v125,v125
MM a[116:119],a[184:187],v[16:19],a[116:119]
EX v126,v126
MM a[108:111],a[184:187],v[8:11],a[108:111]
EX v127,v127
MM a[108:111],a[188:191],v[12:15],a[108:111]
CV v94,v124,v125
CV v95,v126,v127
MM a[100:103],a[188:191],v[4:7],a[100:103]
MM a[100:103],a[184:187],v[0:3],a[100:103]
v_smfmac_f32_16x16x64_bf16 v[144:147], v[236:239], v[64:71], v234
v_smfmac_f32_16x16x64_bf16 v[148:151], v[236:239], v[72:79], v234
v_smfmac_f32_16x16x64_bf16 v[152:155], v[236:239], v[80:87], v234
v_smfmac_f32_16x16x64_bf16 v[156:159], v[236:239], v[88:95], v234
s_waitcnt lgkmcnt(0)
MM v[0:3],a[192:195],v[160:163],v[128:131]
s_add_u32 s64, s64, 16384
TR a[128:129],v232 offset:49152
s_add_u32 s81, s64, 896
MM v[0:3],a[196:199],v[176:179],v[0:3]
TR a[130:131],v232 offset:53248
v_max3_f32 v249, v144, v145, v148
MM v[0:3],a[200:203],v[192:195],v[0:3]
TR a[132:133],v232 offset:57344
v_max3_f32 v249, v249, v149, v152
MM v[0:3],a[204:207],v[208:211],v[0:3]
TR a[134:135],v232 offset:61440
v_max3_f32 v249, v249, v153, v156
MM v[4:7],a[220:223],v[208:211],v[128:131]
TR a[136:137],v233 offset:49152
v_max_f32 v249, v249, v157
MM v[4:7],a[216:219],v[192:195],v[4:7]
TR a[138:139],v233 offset:53248
v_cmp_lt_f32 vcc, s71, v249
MM v[4:7],a[212:215],v[176:179],v[4:7]
TR a[140:141],v233 offset:57344
TR a[142:143],v233 offset:61440
MM v[4:7],a[208:211],v[160:163],v[4:7]
s_cbranch_vccnz LSITE23_%=
LBACK23_%=:
MM v[12:15],a[208:211],v[164:167],v[132:135]
EX v0,v0
MM v[12:15],a[212:215],v[180:183],v[12:15]
EX v1,v1
MM v[12:15],a[216:219],v[196:199],v[12:15]
TR a[144:145],v232 offset:49664
CV v0,v0,v1
MM v[12:15],a[220:223],v[212:215],v[12:15]
EX v2,v2
MM v[8:11],a[204:207],v[212:215],v[132:135]
EX v3,v3
MM v[8:11],a[200:203],v[196:199],v[8:11]
TR a[146:147],v232 offset:53760
CV v1,v2,v3
MM v[8:11],a[196:199],v[180:183],v[8:11]
EX v4,v4
MM v[8:11],a[192:195],v[164:167],v[8:11]
TR a[148:149],v232 offset:57856
TR a[150:151],v232 offset:61952
MM v[16:19],a[192:195],v[168:171],v[136:139]
EX v5,v5
MM v[16:19],a[196:199],v[184:187],v[16:19]
TR a[152:153],v233 offset:49664
CV v2,v4,v5
MM v[16:19],a[200:203],v[200:203],v[16:19]
EX v6,v6
MM v[16:19],a[204:207],v[216:219],v[16:19]
TR a[154:155],v233 offset:53760
TR a[156:157],v233 offset:57856
MM v[20:23],a[220:223],v[216:219],v[136:139]
EX v7,v7
MM v[20:23],a[216:219],v[200:203],v[20:23]
TR a[158:159],v233 offset:61952
CV v3,v6,v7
MM v[20:23],a[212:215],v[184:187],v[20:23]
EX v8,v8
MM v[20:23],a[208:211],v[168:171],v[20:23]
EX v9,v9
MM v[28:31],a[208:211],v[172:175],v[140:143]
TR a[160:161],v232 offset:50176
CV v8,v8,v9
MM v[28:31],a[212:215],v[188:191],v[28:31]
EX v10,v10
MM v[28:31],a[216:219],v[204:207],v[28:31]
TR a[162:163],v232 offset:54272
TR a[164:165],v232 offset:58368
MM v[28:31],a[220:223],v[220:223],v[28:31]
EX v11,v11
MM v[24:27],a[204:207],v[220:223],v[140:143]
TR a[166:167],v232 offset:62464
CV v9,v10,v11
MM v[24:27],a[200:203],v[204:207],v[24:27]
EX v12,v12
MM v[24:27],a[196:199],v[188:191],v[24:27]
EX v13,v13
MM v[24:27],a[192:195],v[172:175],v[24:27]
TR a[168:169],v233 offset:50176
CV v10,v12,v13
MM v[32:35],a[224:227],v[160:163],v[128:131]
EX v14,v14
MM v[32:35],a[228:231],v[176:179],v[32:35]
TR a[170:171],v233 offset:54272
TR a[172:173],v233 offset:58368
MM v[32:35],a[232:235],v[192:195],v[32:35]
EX v15,v15
MM v[32:35],a[236:239],v[208:211],v[32:35]
TR a[174:175],v233 offset:62464
CV v11,v14,v15
MM v[36:39],a[252:255],v[208:211],v[128:131]
EX v16,v16
MM v[36:39],a[248:251],v[192:195],v[36:39]
EX v17,v17
MM v[36:39],a[244:247],v[176:179],v[36:39]
TR a[176:177],v232 offset:50688
CV v16,v16,v17
MM v[36:39],a[240:243],v[160:163],v[36:39]
EX v18,v18
MM v[44:47],a[240:243],v[164:167],v[132:135]
TR a[178:179],v232 offset:54784
TR a[180:181],v232 offset:58880
MM v[44:47],a[244:247],v[180:183],v[44:47]
EX v19,v19
MM v[44:47],a[248:251],v[196:199],v[44:47]
TR a[182:183],v232 offset:62976
CV v17,v18,v19
MM v[44:47],a[252:255],v[212:215],v[44:47]
EX v20,v20
MM v[40:43],a[236:239],v[212:215],v[132:135]
TR a[184:185],v233 offset:50688
TR a[186:187],v233 offset:54784
MM v[40:43],a[232:235],v[196:199],v[40:43]
EX v21,v21
MM v[40:43],a[228:231],v[180:183],v[40:43]
TR a[188:189],v233 offset:58880
)ASM"
R"ASM(
CV v18,v20,v21
MM v[40:43],a[224:227],v[164:167],v[40:43]
EX v22,v22
MM v[48:51],a[224:227],v[168:171],v[136:139]
EX v23,v23
MM v[48:51],a[228:231],v[184:187],v[48:51]
TR a[190:191],v233 offset:62976
CV v19,v22,v23
MM v[48:51],a[232:235],v[200:203],v[48:51]
EX v24,v24
MM v[48:51],a[236:239],v[216:219],v[48:51]
EX v25,v25
MM v[52:55],a[252:255],v[216:219],v[136:139]
EX v26,v26
MM v[52:55],a[248:251],v[200:203],v[52:55]
EX v27,v27
MM v[52:55],a[244:247],v[184:187],v[52:55]
EX v28,v28
MM v[52:55],a[240:243],v[168:171],v[52:55]
EX v29,v29
MM v[60:63],a[240:243],v[172:175],v[140:143]
EX v30,v30
MM v[60:63],a[244:247],v[188:191],v[60:63]
EX v31,v31
MM v[60:63],a[248:251],v[204:207],v[60:63]
EX v32,v32
MM v[60:63],a[252:255],v[220:223],v[60:63]
EX v33,v33
MM v[56:59],a[236:239],v[220:223],v[140:143]
EX v34,v34
MM v[56:59],a[232:235],v[204:207],v[56:59]
EX v35,v35
MM v[56:59],a[228:231],v[188:191],v[56:59]
EX v36,v36
MM v[56:59],a[224:227],v[172:175],v[56:59]
s_waitcnt vmcnt(13) lgkmcnt(0)
s_barrier
MM a[0:3],a[128:131],v[64:67],a[0:3]
s_add_u32 m0, s65, 49152
CV v24,v24,v25
CV v25,v26,v27
MM a[0:3],a[132:135],v[68:71],a[0:3]
RD a[192:195],v224 offset:16384
CV v26,v28,v29
MM a[8:11],a[132:135],v[76:79],a[8:11]
RD a[196:199],v225 offset:16384
CV v27,v30,v31
MM a[8:11],a[128:131],v[72:75],a[8:11]
buffer_load_dwordx4 v228, s[8:11], s64 offen offset:0 lds
MM a[16:19],a[128:131],v[80:83],a[16:19]
RD a[200:203],v226 offset:16384
CV v4,v32,v33
MM a[16:19],a[132:135],v[84:87],a[16:19]
RD a[204:207],v227 offset:16384
CV v5,v34,v35
MM a[24:27],a[132:135],v[92:95],a[24:27]
buffer_load_dwordx4 v229, s[8:11], s64 offen offset:1024 lds
MM a[24:27],a[128:131],v[88:91],a[24:27]
RD a[208:211],v224 offset:20480
RD a[212:215],v225 offset:20480
MM a[28:31],a[136:139],v[88:91],a[28:31]
buffer_load_dwordx4 v230, s[8:11], s64 offen offset:2048 lds
MM a[28:31],a[140:143],v[92:95],a[28:31]
RD a[216:219],v226 offset:20480
RD a[220:223],v227 offset:20480
MM a[20:23],a[140:143],v[84:87],a[20:23]
EX v37,v37
MM a[20:23],a[136:139],v[80:83],a[20:23]
RD a[224:227],v224 offset:24576
CV v6,v36,v37
MM a[12:15],a[136:139],v[72:75],a[12:15]
EX v38,v38
MM a[12:15],a[140:143],v[76:79],a[12:15]
buffer_load_dwordx4 v231, s[8:11], s64 offen offset:3072 lds
MM a[4:7],a[140:143],v[68:71],a[4:7]
s_add_u32 m0, s65, 114688
EX v39,v39
MM a[4:7],a[136:139],v[64:67],a[4:7]
RD a[228:231],v225 offset:24576
CV v7,v38,v39
MM a[32:35],a[144:147],v[64:67],a[32:35]
EX v40,v40
MM a[32:35],a[148:151],v[68:71],a[32:35]
EX v41,v41
MM a[40:43],a[148:151],v[76:79],a[40:43]
RD a[232:235],v226 offset:24576
CV v12,v40,v41
MM a[40:43],a[144:147],v[72:75],a[40:43]
EX v42,v42
MM a[48:51],a[144:147],v[80:83],a[48:51]
buffer_load_dwordx4 v235, s[12:15], s81 offen offset:0 lds
MM a[48:51],a[148:151],v[84:87],a[48:51]
EX v43,v43
MM a[56:59],a[148:151],v[92:95],a[56:59]
RD a[236:239],v227 offset:24576
CV v13,v42,v43
MM a[56:59],a[144:147],v[88:91],a[56:59]
EX v44,v44
MM a[60:63],a[152:155],v[88:91],a[60:63]
EX v45,v45
MM a[60:63],a[156:159],v[92:95],a[60:63]
RD a[240:243],v224 offset:28672
CV v14,v44,v45
MM a[52:55],a[156:159],v[84:87],a[52:55]
EX v46,v46
MM a[52:55],a[152:155],v[80:83],a[52:55]
buffer_load_dwordx4 v235, s[12:15], s64 offen offset:1024 lds
MM a[44:47],a[152:155],v[72:75],a[44:47]
EX v47,v47
MM a[44:47],a[156:159],v[76:79],a[44:47]
RD a[244:247],v225 offset:28672
CV v15,v46,v47
MM a[36:39],a[156:159],v[68:71],a[36:39]
EX v48,v48
MM a[36:39],a[152:155],v[64:67],a[36:39]
EX v49,v49
MM a[64:67],a[160:163],v[64:67],a[64:67]
RD a[248:251],v226 offset:28672
CV v20,v48,v49
MM a[64:67],a[164:167],v[68:71],a[64:67]
EX v50,v50
MM a[72:75],a[164:167],v[76:79],a[72:75]
buffer_load_dwordx4 v235, s[12:15], s81 offen offset:2048 lds
MM a[72:75],a[160:163],v[72:75],a[72:75]
EX v51,v51
MM a[80:83],a[160:163],v[80:83],a[80:83]
RD a[252:255],v227 offset:28672
CV v21,v50,v51
MM a[80:83],a[164:167],v[84:87],a[80:83]
EX v52,v52
MM a[88:91],a[164:167],v[92:95],a[88:91]
EX v53,v53
MM a[88:91],a[160:163],v[88:91],a[88:91]
buffer_load_dwordx4 v235, s[12:15], s64 offen offset:3072 lds
MM a[92:95],a[168:171],v[88:91],a[92:95]
EX v54,v54
MM a[92:95],a[172:175],v[92:95],a[92:95]
EX v55,v55
MM a[84:87],a[172:175],v[84:87],a[84:87]
EX v56,v56
MM a[84:87],a[168:171],v[80:83],a[84:87]
EX v57,v57
MM a[76:79],a[168:171],v[72:75],a[76:79]
EX v58,v58
MM a[76:79],a[172:175],v[76:79],a[76:79]
EX v59,v59
MM a[68:71],a[172:175],v[68:71],a[68:71]
EX v60,v60
MM a[68:71],a[168:171],v[64:67],a[68:71]
EX v61,v61
MM a[96:99],a[176:179],v[64:67],a[96:99]
EX v62,v62
MM a[96:99],a[180:183],v[68:71],a[96:99]
EX v63,v63
MM a[104:107],a[180:183],v[76:79],a[104:107]
CV v22,v52,v53
CV v23,v54,v55
MM a[104:107],a[176:179],v[72:75],a[104:107]
CV v28,v56,v57
CV v29,v58,v59
MM a[112:115],a[176:179],v[80:83],a[112:115]
CV v30,v60,v61
CV v31,v62,v63
MM a[112:115],a[180:183],v[84:87],a[112:115]
MM a[120:123],a[180:183],v[92:95],a[120:123]
s_lshr_b32 s91, s18, 2
s_add_u32 s91, s91, 69632
s_waitcnt vmcnt(16)
MM a[120:123],a[176:179],v[88:91],a[120:123]
v_mov_b32 v250, s69
s_mov_b64 exec, 1
buffer_store_dword v250, off, s[20:23], s91 offset:0 sc1
s_mov_b64 exec, -1
MM a[124:127],a[184:187],v[88:91],a[124:127]
v_add_f32 v244, v144, v145
v_add_f32 v245, v148, v149
MM a[124:127],a[188:191],v[92:95],a[124:127]
v_add_f32 v246, v152, v153
v_add_f32 v247, v156, v157
v_lshlrev_b32 v96, 3, v248
MM a[116:119],a[188:191],v[84:87],a[116:119]
v_min_u32 v96, 4992, v96
LPG18_%=:
s_cbranch_execz LTR3_%=
s_sub_u32 s91, 5242880, s26
MM a[116:119],a[184:187],v[80:83],a[116:119]
s_sub_u32 s60, s24, s91
s_subb_u32 s61, s25, 0
s_mov_b32 s62, 5242880
MM a[108:111],a[184:187],v[72:75],a[108:111]
s_mov_b32 s63, 131072
s_lshl_b32 s90, s16, 2
s_add_u32 s90, s90, %[swave]
MM a[108:111],a[188:191],v[76:79],a[108:111]
s_mul_i32 s90, s90, 10240
s_add_u32 s90, s90, 0
s_add_u32 s91, %[slds], 135168
MM a[100:103],a[188:191],v[68:71],a[100:103]
s_mov_b32 m0, s91
s_nop 0
buffer_load_dword v96, s[60:63], s90 offen offset:0 lds
MM a[100:103],a[184:187],v[64:67],a[100:103]
s_lshr_b32 s77, %[sb], 3
s_lshl_b32 s77, s77, 2
s_add_u32 s77, s77, %[swave]
v_smfmac_f32_16x16x64_bf16 v[144:147], v[236:239], v[0:7], v234
s_lshl_b32 s77, s77, 9
s_add_u32 s77, s77, 0
v_smfmac_f32_16x16x64_bf16 v[148:151], v[236:239], v[8:15], v234
v_lshrrev_b32 v253, 1, v248
v_smfmac_f32_16x16x64_bf16 v[152:155], v[236:239], v[16:23], v234
v_add_u32 v253, s77, v253
v_smfmac_f32_16x16x64_bf16 v[156:159], v[236:239], v[24:31], v234
s_waitcnt lgkmcnt(0)
s_mov_b32 s77, LKEND_ka-LPD1_%=-16
v_min_u32 v253, s77, v253
s_getpc_b64 s[98:99]
LPD1_%=:
global_load_dword v253, v253, s[98:99]
MM v[64:67],a[192:195],v[160:163],v[128:131]
s_add_u32 s64, s64, 16384
TR a[128:129],v232 offset:0
s_add_u32 s81, s64, 896
MM v[64:67],a[196:199],v[176:179],v[64:67]
TR a[130:131],v232 offset:4096
v_max3_f32 v249, v144, v145, v148
MM v[64:67],a[200:203],v[192:195],v[64:67]
TR a[132:133],v232 offset:8192
v_max3_f32 v249, v249, v149, v152
MM v[64:67],a[204:207],v[208:211],v[64:67]
TR a[134:135],v232 offset:12288
v_max3_f32 v249, v249, v153, v156
MM v[68:71],a[220:223],v[208:211],v[128:131]
TR a[136:137],v233 offset:0
v_max_f32 v249, v249, v157
MM v[68:71],a[216:219],v[192:195],v[68:71]
TR a[138:139],v233 offset:4096
v_cmp_lt_f32 vcc, s71, v249
MM v[68:71],a[212:215],v[176:179],v[68:71]
TR a[140:141],v233 offset:8192
TR a[142:143],v233 offset:12288
MM v[68:71],a[208:211],v[160:163],v[68:71]
s_cbranch_vccnz LSITE24_%=
LBACK24_%=:
MM v[76:79],a[208:211],v[164:167],v[132:135]
EX v64,v64
MM v[76:79],a[212:215],v[180:183],v[76:79]
EX v65,v65
MM v[76:79],a[216:219],v[196:199],v[76:79]
TR a[144:145],v232 offset:512
CV v64,v64,v65
MM v[76:79],a[220:223],v[212:215],v[76:79]
EX v66,v66
MM v[72:75],a[204:207],v[212:215],v[132:135]
EX v67,v67
MM v[72:75],a[200:203],v[196:199],v[72:75]
TR a[146:147],v232 offset:4608
CV v65,v66,v67
MM v[72:75],a[196:199],v[180:183],v[72:75]
EX v68,v68
MM v[72:75],a[192:195],v[164:167],v[72:75]
TR a[148:149],v232 offset:8704
TR a[150:151],v232 offset:12800
MM v[80:83],a[192:195],v[168:171],v[136:139]
EX v69,v69
MM v[80:83],a[196:199],v[184:187],v[80:83]
TR a[152:153],v233 offset:512
CV v66,v68,v69
MM v[80:83],a[200:203],v[200:203],v[80:83]
EX v70,v70
MM v[80:83],a[204:207],v[216:219],v[80:83]
TR a[154:155],v233 offset:4608
TR a[156:157],v233 offset:8704
MM v[84:87],a[220:223],v[216:219],v[136:139]
s_add_u32 s87, s95, 4096
buffer_store_dword v243, v248, s[20:23], s87 offen offset:12 sc1
MM v[84:87],a[216:219],v[200:203],v[84:87]
EX v71,v71
MM v[84:87],a[212:215],v[184:187],v[84:87]
TR a[158:159],v233 offset:12800
CV v67,v70,v71
MM v[84:87],a[208:211],v[168:171],v[84:87]
EX v72,v72
MM v[92:95],a[208:211],v[172:175],v[140:143]
EX v73,v73
MM v[92:95],a[212:215],v[188:191],v[92:95]
TR a[160:161],v232 offset:1024
CV v72,v72,v73
MM v[92:95],a[216:219],v[204:207],v[92:95]
EX v74,v74
MM v[92:95],a[220:223],v[220:223],v[92:95]
EX v75,v75
MM v[88:91],a[204:207],v[220:223],v[140:143]
TR a[162:163],v232 offset:5120
CV v73,v74,v75
MM v[88:91],a[200:203],v[204:207],v[88:91]
TR a[164:165],v232 offset:9216
TR a[166:167],v232 offset:13312
MM v[88:91],a[196:199],v[188:191],v[88:91]
EX v76,v76
MM v[88:91],a[192:195],v[172:175],v[88:91]
EX v77,v77
MM v[96:99],a[224:227],v[160:163],v[128:131]
TR a[168:169],v233 offset:1024
CV v74,v76,v77
MM v[96:99],a[228:231],v[176:179],v[96:99]
EX v78,v78
MM v[96:99],a[232:235],v[192:195],v[96:99]
TR a[170:171],v233 offset:5120
TR a[172:173],v233 offset:9216
MM v[96:99],a[236:239],v[208:211],v[96:99]
EX v79,v79
MM v[100:103],a[252:255],v[208:211],v[128:131]
TR a[174:175],v233 offset:13312
CV v75,v78,v79
MM v[100:103],a[248:251],v[192:195],v[100:103]
EX v80,v80
MM v[100:103],a[244:247],v[176:179],v[100:103]
EX v81,v81
MM v[100:103],a[240:243],v[160:163],v[100:103]
TR a[176:177],v232 offset:1536
CV v80,v80,v81
MM v[108:111],a[240:243],v[164:167],v[132:135]
EX v82,v82
MM v[108:111],a[244:247],v[180:183],v[108:111]
TR a[178:179],v232 offset:5632
TR a[180:181],v232 offset:9728
MM v[108:111],a[248:251],v[196:199],v[108:111]
EX v83,v83
MM v[108:111],a[252:255],v[212:215],v[108:111]
TR a[182:183],v232 offset:13824
CV v81,v82,v83
MM v[104:107],a[236:239],v[212:215],v[132:135]
EX v84,v84
MM v[104:107],a[232:235],v[196:199],v[104:107]
EX v85,v85
MM v[104:107],a[228:231],v[180:183],v[104:107]
TR a[184:185],v233 offset:1536
CV v82,v84,v85
MM v[104:107],a[224:227],v[164:167],v[104:107]
EX v86,v86
MM v[112:115],a[224:227],v[168:171],v[136:139]
TR a[186:187],v233 offset:5632
TR a[188:189],v233 offset:9728
MM v[112:115],a[228:231],v[184:187],v[112:115]
EX v87,v87
MM v[112:115],a[232:235],v[200:203],v[112:115]
TR a[190:191],v233 offset:13824
CV v83,v86,v87
MM v[112:115],a[236:239],v[216:219],v[112:115]
EX v88,v88
MM v[116:119],a[252:255],v[216:219],v[136:139]
EX v89,v89
MM v[116:119],a[248:251],v[200:203],v[116:119]
EX v90,v90
MM v[116:119],a[244:247],v[184:187],v[116:119]
EX v91,v91
MM v[116:119],a[240:243],v[168:171],v[116:119]
EX v92,v92
MM v[124:127],a[240:243],v[172:175],v[140:143]
EX v93,v93
MM v[124:127],a[244:247],v[188:191],v[124:127]
EX v94,v94
MM v[124:127],a[248:251],v[204:207],v[124:127]
EX v95,v95
MM v[124:127],a[252:255],v[220:223],v[124:127]
EX v96,v96
MM v[120:123],a[236:239],v[220:223],v[140:143]
EX v97,v97
MM v[120:123],a[232:235],v[204:207],v[120:123]
EX v98,v98
MM v[120:123],a[228:231],v[188:191],v[120:123]
EX v99,v99
MM v[120:123],a[224:227],v[172:175],v[120:123]
s_waitcnt vmcnt(16) lgkmcnt(0)
s_barrier
MM a[0:3],a[128:131],v[0:3],a[0:3]
s_add_u32 m0, s65, 0
CV v88,v88,v89
CV v89,v90,v91
MM a[0:3],a[132:135],v[4:7],a[0:3]
RD a[192:195],v224 offset:32768
CV v90,v92,v93
MM a[8:11],a[132:135],v[12:15],a[8:11]
RD a[196:199],v225 offset:32768
CV v91,v94,v95
MM a[8:11],a[128:131],v[8:11],a[8:11]
buffer_load_dwordx4 v228, s[8:11], s64 offen offset:0 lds
MM a[16:19],a[128:131],v[16:19],a[16:19]
RD a[200:203],v226 offset:32768
CV v68,v96,v97
MM a[16:19],a[132:135],v[20:23],a[16:19]
RD a[204:207],v227 offset:32768
CV v69,v98,v99
MM a[24:27],a[132:135],v[28:31],a[24:27]
)ASM"
R"ASM(
buffer_load_dwordx4 v229, s[8:11], s64 offen offset:1024 lds
MM a[24:27],a[128:131],v[24:27],a[24:27]
RD a[208:211],v224 offset:36864
RD a[212:215],v225 offset:36864
MM a[28:31],a[136:139],v[24:27],a[28:31]
buffer_load_dwordx4 v230, s[8:11], s64 offen offset:2048 lds
MM a[28:31],a[140:143],v[28:31],a[28:31]
EX v100,v100
MM a[20:23],a[140:143],v[20:23],a[20:23]
EX v101,v101
MM a[20:23],a[136:139],v[16:19],a[20:23]
RD a[216:219],v226 offset:36864
CV v70,v100,v101
MM a[12:15],a[136:139],v[8:11],a[12:15]
EX v102,v102
MM a[12:15],a[140:143],v[12:15],a[12:15]
RD a[220:223],v227 offset:36864
RD a[224:227],v224 offset:40960
MM a[4:7],a[140:143],v[4:7],a[4:7]
EX v103,v103
MM a[4:7],a[136:139],v[0:3],a[4:7]
RD a[228:231],v225 offset:40960
CV v71,v102,v103
MM a[32:35],a[144:147],v[0:3],a[32:35]
EX v104,v104
MM a[32:35],a[148:151],v[4:7],a[32:35]
buffer_load_dwordx4 v231, s[8:11], s64 offen offset:3072 lds
MM a[40:43],a[148:151],v[12:15],a[40:43]
s_add_u32 m0, s65, 65536
EX v105,v105
MM a[40:43],a[144:147],v[8:11],a[40:43]
RD a[232:235],v226 offset:40960
CV v76,v104,v105
MM a[48:51],a[144:147],v[16:19],a[48:51]
EX v106,v106
MM a[48:51],a[148:151],v[20:23],a[48:51]
EX v107,v107
MM a[56:59],a[148:151],v[28:31],a[56:59]
RD a[236:239],v227 offset:40960
CV v77,v106,v107
MM a[56:59],a[144:147],v[24:27],a[56:59]
EX v108,v108
MM a[60:63],a[152:155],v[24:27],a[60:63]
buffer_load_dwordx4 v235, s[12:15], s81 offen offset:0 lds
MM a[60:63],a[156:159],v[28:31],a[60:63]
EX v109,v109
MM a[52:55],a[156:159],v[20:23],a[52:55]
RD a[240:243],v224 offset:45056
CV v78,v108,v109
MM a[52:55],a[152:155],v[16:19],a[52:55]
EX v110,v110
MM a[44:47],a[152:155],v[8:11],a[44:47]
EX v111,v111
MM a[44:47],a[156:159],v[12:15],a[44:47]
RD a[244:247],v225 offset:45056
CV v79,v110,v111
MM a[36:39],a[156:159],v[4:7],a[36:39]
buffer_load_dwordx4 v235, s[12:15], s64 offen offset:1024 lds
MM a[36:39],a[152:155],v[0:3],a[36:39]
EX v112,v112
MM a[64:67],a[160:163],v[0:3],a[64:67]
EX v113,v113
MM a[64:67],a[164:167],v[4:7],a[64:67]
RD a[248:251],v226 offset:45056
CV v84,v112,v113
MM a[72:75],a[164:167],v[12:15],a[72:75]
EX v114,v114
MM a[72:75],a[160:163],v[8:11],a[72:75]
EX v115,v115
MM a[80:83],a[160:163],v[16:19],a[80:83]
RD a[252:255],v227 offset:45056
CV v85,v114,v115
MM a[80:83],a[164:167],v[20:23],a[80:83]
buffer_load_dwordx4 v235, s[12:15], s81 offen offset:2048 lds
MM a[88:91],a[164:167],v[28:31],a[88:91]
EX v116,v116
MM a[88:91],a[160:163],v[24:27],a[88:91]
EX v117,v117
MM a[92:95],a[168:171],v[24:27],a[92:95]
EX v118,v118
MM a[92:95],a[172:175],v[28:31],a[92:95]
buffer_load_dwordx4 v235, s[12:15], s64 offen offset:3072 lds
MM a[84:87],a[172:175],v[20:23],a[84:87]
EX v119,v119
MM a[84:87],a[168:171],v[16:19],a[84:87]
EX v120,v120
MM a[76:79],a[168:171],v[8:11],a[76:79]
EX v121,v121
MM a[76:79],a[172:175],v[12:15],a[76:79]
EX v122,v122
MM a[68:71],a[172:175],v[4:7],a[68:71]
EX v123,v123
MM a[68:71],a[168:171],v[0:3],a[68:71]
EX v124,v124
MM a[96:99],a[176:179],v[0:3],a[96:99]
EX v125,v125
MM a[96:99],a[180:183],v[4:7],a[96:99]
EX v126,v126
MM a[104:107],a[180:183],v[12:15],a[104:107]
EX v127,v127
MM a[104:107],a[176:179],v[8:11],a[104:107]
CV v86,v116,v117
CV v87,v118,v119
MM a[112:115],a[176:179],v[16:19],a[112:115]
CV v92,v120,v121
CV v93,v122,v123
MM a[112:115],a[180:183],v[20:23],a[112:115]
CV v94,v124,v125
CV v95,v126,v127
MM a[120:123],a[180:183],v[28:31],a[120:123]
MM a[120:123],a[176:179],v[24:27],a[120:123]
MM a[124:127],a[184:187],v[24:27],a[124:127]
v_add_f32 v244, v144, v145
v_add_f32 v245, v148, v149
MM a[124:127],a[188:191],v[28:31],a[124:127]
v_add_f32 v246, v152, v153
v_add_f32 v247, v156, v157
MM a[116:119],a[188:191],v[20:23],a[116:119]
MM a[116:119],a[184:187],v[16:19],a[116:119]
MM a[108:111],a[184:187],v[8:11],a[108:111]
MM a[108:111],a[188:191],v[12:15],a[108:111]
MM a[100:103],a[188:191],v[4:7],a[100:103]
MM a[100:103],a[184:187],v[0:3],a[100:103]
v_smfmac_f32_16x16x64_bf16 v[144:147], v[236:239], v[64:71], v234
v_smfmac_f32_16x16x64_bf16 v[148:151], v[236:239], v[72:79], v234
v_smfmac_f32_16x16x64_bf16 v[152:155], v[236:239], v[80:87], v234
v_smfmac_f32_16x16x64_bf16 v[156:159], v[236:239], v[88:95], v234
s_waitcnt lgkmcnt(0)
MM v[0:3],a[192:195],v[160:163],v[128:131]
s_add_u32 s64, s64, 16384
TR a[128:129],v232 offset:16384
s_add_u32 s81, s64, 896
MM v[0:3],a[196:199],v[176:179],v[0:3]
TR a[130:131],v232 offset:20480
v_max3_f32 v249, v144, v145, v148
MM v[0:3],a[200:203],v[192:195],v[0:3]
TR a[132:133],v232 offset:24576
v_max3_f32 v249, v249, v149, v152
MM v[0:3],a[204:207],v[208:211],v[0:3]
TR a[134:135],v232 offset:28672
v_max3_f32 v249, v249, v153, v156
MM v[4:7],a[220:223],v[208:211],v[128:131]
TR a[136:137],v233 offset:16384
v_max_f32 v249, v249, v157
MM v[4:7],a[216:219],v[192:195],v[4:7]
TR a[138:139],v233 offset:20480
v_cmp_lt_f32 vcc, s71, v249
MM v[4:7],a[212:215],v[176:179],v[4:7]
TR a[140:141],v233 offset:24576
TR a[142:143],v233 offset:28672
MM v[4:7],a[208:211],v[160:163],v[4:7]
s_cbranch_vccnz LSITE25_%=
LBACK25_%=:
MM v[12:15],a[208:211],v[164:167],v[132:135]
s_waitcnt vmcnt(9)
EX v0,v0
MM v[12:15],a[212:215],v[180:183],v[12:15]
s_mul_i32 s76, s46, 96
EX v1,v1
MM v[12:15],a[216:219],v[196:199],v[12:15]
s_add_u32 s76, s76, 69632
v_lshrrev_b32 v253, 2, v248
v_min_u32 v253, 92, v253
MM v[12:15],a[220:223],v[212:215],v[12:15]
buffer_load_dword v252, v253, s[20:23], s76 offen offset:0 sc1
MM v[8:11],a[204:207],v[212:215],v[132:135]
EX v2,v2
MM v[8:11],a[200:203],v[196:199],v[8:11]
TR a[144:145],v232 offset:16896
CV v0,v0,v1
MM v[8:11],a[196:199],v[180:183],v[8:11]
EX v3,v3
MM v[8:11],a[192:195],v[164:167],v[8:11]
TR a[146:147],v232 offset:20992
CV v1,v2,v3
MM v[16:19],a[192:195],v[168:171],v[136:139]
EX v4,v4
MM v[16:19],a[196:199],v[184:187],v[16:19]
TR a[148:149],v232 offset:25088
TR a[150:151],v232 offset:29184
MM v[16:19],a[200:203],v[200:203],v[16:19]
EX v5,v5
MM v[16:19],a[204:207],v[216:219],v[16:19]
TR a[152:153],v233 offset:16896
CV v2,v4,v5
MM v[20:23],a[220:223],v[216:219],v[136:139]
EX v6,v6
MM v[20:23],a[216:219],v[200:203],v[20:23]
EX v7,v7
MM v[20:23],a[212:215],v[184:187],v[20:23]
TR a[154:155],v233 offset:20992
CV v3,v6,v7
MM v[20:23],a[208:211],v[168:171],v[20:23]
EX v8,v8
MM v[28:31],a[208:211],v[172:175],v[140:143]
TR a[156:157],v233 offset:25088
TR a[158:159],v233 offset:29184
MM v[28:31],a[212:215],v[188:191],v[28:31]
EX v9,v9
MM v[28:31],a[216:219],v[204:207],v[28:31]
TR a[160:161],v232 offset:17408
CV v8,v8,v9
MM v[28:31],a[220:223],v[220:223],v[28:31]
EX v10,v10
MM v[24:27],a[204:207],v[220:223],v[140:143]
EX v11,v11
MM v[24:27],a[200:203],v[204:207],v[24:27]
TR a[162:163],v232 offset:21504
CV v9,v10,v11
MM v[24:27],a[196:199],v[188:191],v[24:27]
EX v12,v12
MM v[24:27],a[192:195],v[172:175],v[24:27]
TR a[164:165],v232 offset:25600
TR a[166:167],v232 offset:29696
MM v[32:35],a[224:227],v[160:163],v[128:131]
EX v13,v13
MM v[32:35],a[228:231],v[176:179],v[32:35]
TR a[168:169],v233 offset:17408
CV v10,v12,v13
MM v[32:35],a[232:235],v[192:195],v[32:35]
EX v14,v14
MM v[32:35],a[236:239],v[208:211],v[32:35]
EX v15,v15
MM v[36:39],a[252:255],v[208:211],v[128:131]
TR a[170:171],v233 offset:21504
CV v11,v14,v15
MM v[36:39],a[248:251],v[192:195],v[36:39]
EX v16,v16
MM v[36:39],a[244:247],v[176:179],v[36:39]
TR a[172:173],v233 offset:25600
TR a[174:175],v233 offset:29696
MM v[36:39],a[240:243],v[160:163],v[36:39]
EX v17,v17
MM v[44:47],a[240:243],v[164:167],v[132:135]
TR a[176:177],v232 offset:17920
CV v16,v16,v17
MM v[44:47],a[244:247],v[180:183],v[44:47]
EX v18,v18
MM v[44:47],a[248:251],v[196:199],v[44:47]
TR a[178:179],v232 offset:22016
TR a[180:181],v232 offset:26112
MM v[44:47],a[252:255],v[212:215],v[44:47]
EX v19,v19
MM v[40:43],a[236:239],v[212:215],v[132:135]
TR a[182:183],v232 offset:30208
CV v17,v18,v19
MM v[40:43],a[232:235],v[196:199],v[40:43]
EX v20,v20
MM v[40:43],a[228:231],v[180:183],v[40:43]
EX v21,v21
MM v[40:43],a[224:227],v[164:167],v[40:43]
TR a[184:185],v233 offset:17920
CV v18,v20,v21
MM v[48:51],a[224:227],v[168:171],v[136:139]
EX v22,v22
MM v[48:51],a[228:231],v[184:187],v[48:51]
TR a[186:187],v233 offset:22016
TR a[188:189],v233 offset:26112
MM v[48:51],a[232:235],v[200:203],v[48:51]
EX v23,v23
MM v[48:51],a[236:239],v[216:219],v[48:51]
TR a[190:191],v233 offset:30208
CV v19,v22,v23
MM v[52:55],a[252:255],v[216:219],v[136:139]
EX v24,v24
MM v[52:55],a[248:251],v[200:203],v[52:55]
EX v25,v25
MM v[52:55],a[244:247],v[184:187],v[52:55]
EX v26,v26
MM v[52:55],a[240:243],v[168:171],v[52:55]
EX v27,v27
MM v[60:63],a[240:243],v[172:175],v[140:143]
EX v28,v28
MM v[60:63],a[244:247],v[188:191],v[60:63]
EX v29,v29
MM v[60:63],a[248:251],v[204:207],v[60:63]
EX v30,v30
MM v[60:63],a[252:255],v[220:223],v[60:63]
EX v31,v31
MM v[56:59],a[236:239],v[220:223],v[140:143]
EX v32,v32
MM v[56:59],a[232:235],v[204:207],v[56:59]
EX v33,v33
MM v[56:59],a[228:231],v[188:191],v[56:59]
EX v34,v34
MM v[56:59],a[224:227],v[172:175],v[56:59]
s_waitcnt lgkmcnt(0)
s_barrier
MM a[0:3],a[128:131],v[64:67],a[0:3]
s_add_u32 m0, s65, 16384
CV v24,v24,v25
CV v25,v26,v27
MM a[0:3],a[132:135],v[68:71],a[0:3]
RD a[192:195],v224 offset:49152
CV v26,v28,v29
MM a[8:11],a[132:135],v[76:79],a[8:11]
RD a[196:199],v225 offset:49152
CV v27,v30,v31
MM a[8:11],a[128:131],v[72:75],a[8:11]
buffer_load_dwordx4 v228, s[8:11], s64 offen offset:0 lds
MM a[16:19],a[128:131],v[80:83],a[16:19]
RD a[200:203],v226 offset:49152
CV v4,v32,v33
MM a[16:19],a[132:135],v[84:87],a[16:19]
buffer_load_dwordx4 v229, s[8:11], s64 offen offset:1024 lds
MM a[24:27],a[132:135],v[92:95],a[24:27]
RD a[204:207],v227 offset:49152
RD a[208:211],v224 offset:53248
MM a[24:27],a[128:131],v[88:91],a[24:27]
EX v35,v35
MM a[28:31],a[136:139],v[88:91],a[28:31]
RD a[212:215],v225 offset:53248
CV v5,v34,v35
MM a[28:31],a[140:143],v[92:95],a[28:31]
EX v36,v36
MM a[20:23],a[140:143],v[84:87],a[20:23]
buffer_load_dwordx4 v230, s[8:11], s64 offen offset:2048 lds
MM a[20:23],a[136:139],v[80:83],a[20:23]
EX v37,v37
MM a[12:15],a[136:139],v[72:75],a[12:15]
RD a[216:219],v226 offset:53248
CV v6,v36,v37
MM a[12:15],a[140:143],v[76:79],a[12:15]
EX v38,v38
MM a[4:7],a[140:143],v[68:71],a[4:7]
EX v39,v39
MM a[4:7],a[136:139],v[64:67],a[4:7]
RD a[220:223],v227 offset:53248
CV v7,v38,v39
MM a[32:35],a[144:147],v[64:67],a[32:35]
EX v40,v40
MM a[32:35],a[148:151],v[68:71],a[32:35]
buffer_load_dwordx4 v231, s[8:11], s64 offen offset:3072 lds
MM a[40:43],a[148:151],v[76:79],a[40:43]
s_add_u32 m0, s65, 81920
EX v41,v41
MM a[40:43],a[144:147],v[72:75],a[40:43]
RD a[224:227],v224 offset:57344
CV v12,v40,v41
MM a[48:51],a[144:147],v[80:83],a[48:51]
EX v42,v42
MM a[48:51],a[148:151],v[84:87],a[48:51]
EX v43,v43
MM a[56:59],a[148:151],v[92:95],a[56:59]
RD a[228:231],v225 offset:57344
CV v13,v42,v43
MM a[56:59],a[144:147],v[88:91],a[56:59]
EX v44,v44
MM a[60:63],a[152:155],v[88:91],a[60:63]
buffer_load_dwordx4 v235, s[12:15], s81 offen offset:0 lds
MM a[60:63],a[156:159],v[92:95],a[60:63]
EX v45,v45
MM a[52:55],a[156:159],v[84:87],a[52:55]
RD a[232:235],v226 offset:57344
CV v14,v44,v45
MM a[52:55],a[152:155],v[80:83],a[52:55]
EX v46,v46
MM a[44:47],a[152:155],v[72:75],a[44:47]
EX v47,v47
MM a[44:47],a[156:159],v[76:79],a[44:47]
RD a[236:239],v227 offset:57344
CV v15,v46,v47
MM a[36:39],a[156:159],v[68:71],a[36:39]
buffer_load_dwordx4 v235, s[12:15], s64 offen offset:1024 lds
MM a[36:39],a[152:155],v[64:67],a[36:39]
EX v48,v48
MM a[64:67],a[160:163],v[64:67],a[64:67]
EX v49,v49
MM a[64:67],a[164:167],v[68:71],a[64:67]
RD a[240:243],v224 offset:61440
CV v20,v48,v49
MM a[72:75],a[164:167],v[76:79],a[72:75]
EX v50,v50
MM a[72:75],a[160:163],v[72:75],a[72:75]
RD a[244:247],v225 offset:61440
RD a[248:251],v226 offset:61440
MM a[80:83],a[160:163],v[80:83],a[80:83]
EX v51,v51
MM a[80:83],a[164:167],v[84:87],a[80:83]
LPG19_%=:
s_cbranch_execz LTR3_%=
RD a[252:255],v227 offset:61440
CV v21,v50,v51
MM a[88:91],a[164:167],v[92:95],a[88:91]
EX v52,v52
MM a[88:91],a[160:163],v[88:91],a[88:91]
buffer_load_dwordx4 v235, s[12:15], s81 offen offset:2048 lds
MM a[92:95],a[168:171],v[88:91],a[92:95]
)ASM"
R"ASM(
EX v53,v53
MM a[92:95],a[172:175],v[92:95],a[92:95]
EX v54,v54
MM a[84:87],a[172:175],v[84:87],a[84:87]
buffer_load_dwordx4 v235, s[12:15], s64 offen offset:3072 lds
MM a[84:87],a[168:171],v[80:83],a[84:87]
EX v55,v55
MM a[76:79],a[168:171],v[72:75],a[76:79]
EX v56,v56
MM a[76:79],a[172:175],v[76:79],a[76:79]
EX v57,v57
MM a[68:71],a[172:175],v[68:71],a[68:71]
EX v58,v58
MM a[68:71],a[168:171],v[64:67],a[68:71]
EX v59,v59
MM a[96:99],a[176:179],v[64:67],a[96:99]
EX v60,v60
MM a[96:99],a[180:183],v[68:71],a[96:99]
EX v61,v61
MM a[104:107],a[180:183],v[76:79],a[104:107]
EX v62,v62
MM a[104:107],a[176:179],v[72:75],a[104:107]
EX v63,v63
MM a[112:115],a[176:179],v[80:83],a[112:115]
CV v22,v52,v53
CV v23,v54,v55
MM a[112:115],a[180:183],v[84:87],a[112:115]
CV v28,v56,v57
CV v29,v58,v59
MM a[120:123],a[180:183],v[92:95],a[120:123]
s_waitcnt vmcnt(8)
v_cmp_ne_u32 vcc, s69, v252
s_cbranch_vccnz LRFH_%=
LRFHb_%=:
MM a[120:123],a[176:179],v[88:91],a[120:123]
v_bfe_u32 v253, v248, 4, 3
v_mul_u32_u24 v253, 3456, v253
MM a[124:127],a[184:187],v[88:91],a[124:127]
v_add_f32 v244, v144, v145
v_add_f32 v245, v148, v149
MM a[124:127],a[188:191],v[92:95],a[124:127]
v_add_f32 v246, v152, v153
v_add_f32 v247, v156, v157
v_lshrrev_b32 v252, 7, v248
MM a[116:119],a[188:191],v[84:87],a[116:119]
v_lshl_add_u32 v253, v252, 4, v253
CV v30,v60,v61
MM a[116:119],a[184:187],v[80:83],a[116:119]
s_min_u32 s90, %[swave], 2
s_mul_i32 s76, s46, 384
CV v31,v62,v63
MM a[108:111],a[184:187],v[72:75],a[108:111]
s_lshl_b32 s91, s90, 7
s_add_u32 s76, s76, s91
s_add_u32 s76, s76, 21626880
MM a[108:111],a[188:191],v[76:79],a[108:111]
s_lshl_b32 s90, s90, 10
s_add_u32 s90, s90, %[slds]
MM a[100:103],a[188:191],v[68:71],a[100:103]
s_add_u32 m0, s90, 131072
s_nop 0
buffer_load_dwordx4 v253, s[20:23], s76 offen offset:0 sc1 lds
MM a[100:103],a[184:187],v[64:67],a[100:103]
v_smfmac_f32_16x16x64_bf16 v[144:147], v[236:239], v[0:7], v234
v_smfmac_f32_16x16x64_bf16 v[148:151], v[236:239], v[8:15], v234
v_smfmac_f32_16x16x64_bf16 v[152:155], v[236:239], v[16:23], v234
v_smfmac_f32_16x16x64_bf16 v[156:159], v[236:239], v[24:31], v234
s_waitcnt lgkmcnt(0)
MM v[64:67],a[192:195],v[160:163],v[128:131]
s_add_u32 s64, s64, 16384
TR a[128:129],v232 offset:32768
s_add_u32 s81, s64, 896
MM v[64:67],a[196:199],v[176:179],v[64:67]
TR a[130:131],v232 offset:36864
v_max3_f32 v249, v144, v145, v148
MM v[64:67],a[200:203],v[192:195],v[64:67]
TR a[132:133],v232 offset:40960
v_max3_f32 v249, v249, v149, v152
MM v[64:67],a[204:207],v[208:211],v[64:67]
TR a[134:135],v232 offset:45056
v_max3_f32 v249, v249, v153, v156
MM v[68:71],a[220:223],v[208:211],v[128:131]
TR a[136:137],v233 offset:32768
v_max_f32 v249, v249, v157
MM v[68:71],a[216:219],v[192:195],v[68:71]
TR a[138:139],v233 offset:36864
v_cmp_lt_f32 vcc, s71, v249
MM v[68:71],a[212:215],v[176:179],v[68:71]
TR a[140:141],v233 offset:40960
TR a[142:143],v233 offset:45056
MM v[68:71],a[208:211],v[160:163],v[68:71]
s_cbranch_vccnz LSITE26_%=
LBACK26_%=:
MM v[76:79],a[208:211],v[164:167],v[132:135]
EX v64,v64
MM v[76:79],a[212:215],v[180:183],v[76:79]
EX v65,v65
MM v[76:79],a[216:219],v[196:199],v[76:79]
TR a[144:145],v232 offset:33280
CV v64,v64,v65
MM v[76:79],a[220:223],v[212:215],v[76:79]
EX v66,v66
MM v[72:75],a[204:207],v[212:215],v[132:135]
EX v67,v67
MM v[72:75],a[200:203],v[196:199],v[72:75]
TR a[146:147],v232 offset:37376
CV v65,v66,v67
MM v[72:75],a[196:199],v[180:183],v[72:75]
EX v68,v68
MM v[72:75],a[192:195],v[164:167],v[72:75]
TR a[148:149],v232 offset:41472
TR a[150:151],v232 offset:45568
MM v[80:83],a[192:195],v[168:171],v[136:139]
EX v69,v69
MM v[80:83],a[196:199],v[184:187],v[80:83]
TR a[152:153],v233 offset:33280
CV v66,v68,v69
MM v[80:83],a[200:203],v[200:203],v[80:83]
EX v70,v70
MM v[80:83],a[204:207],v[216:219],v[80:83]
TR a[154:155],v233 offset:37376
TR a[156:157],v233 offset:41472
MM v[84:87],a[220:223],v[216:219],v[136:139]
EX v71,v71
MM v[84:87],a[216:219],v[200:203],v[84:87]
TR a[158:159],v233 offset:45568
CV v67,v70,v71
MM v[84:87],a[212:215],v[184:187],v[84:87]
EX v72,v72
MM v[84:87],a[208:211],v[168:171],v[84:87]
EX v73,v73
MM v[92:95],a[208:211],v[172:175],v[140:143]
TR a[160:161],v232 offset:33792
CV v72,v72,v73
MM v[92:95],a[212:215],v[188:191],v[92:95]
EX v74,v74
MM v[92:95],a[216:219],v[204:207],v[92:95]
TR a[162:163],v232 offset:37888
TR a[164:165],v232 offset:41984
MM v[92:95],a[220:223],v[220:223],v[92:95]
EX v75,v75
MM v[88:91],a[204:207],v[220:223],v[140:143]
TR a[166:167],v232 offset:46080
CV v73,v74,v75
MM v[88:91],a[200:203],v[204:207],v[88:91]
EX v76,v76
MM v[88:91],a[196:199],v[188:191],v[88:91]
EX v77,v77
MM v[88:91],a[192:195],v[172:175],v[88:91]
TR a[168:169],v233 offset:33792
CV v74,v76,v77
MM v[96:99],a[224:227],v[160:163],v[128:131]
EX v78,v78
MM v[96:99],a[228:231],v[176:179],v[96:99]
TR a[170:171],v233 offset:37888
TR a[172:173],v233 offset:41984
MM v[96:99],a[232:235],v[192:195],v[96:99]
EX v79,v79
MM v[96:99],a[236:239],v[208:211],v[96:99]
TR a[174:175],v233 offset:46080
CV v75,v78,v79
MM v[100:103],a[252:255],v[208:211],v[128:131]
EX v80,v80
MM v[100:103],a[248:251],v[192:195],v[100:103]
EX v81,v81
MM v[100:103],a[244:247],v[176:179],v[100:103]
TR a[176:177],v232 offset:34304
CV v80,v80,v81
MM v[100:103],a[240:243],v[160:163],v[100:103]
EX v82,v82
MM v[108:111],a[240:243],v[164:167],v[132:135]
TR a[178:179],v232 offset:38400
TR a[180:181],v232 offset:42496
MM v[108:111],a[244:247],v[180:183],v[108:111]
EX v83,v83
MM v[108:111],a[248:251],v[196:199],v[108:111]
TR a[182:183],v232 offset:46592
CV v81,v82,v83
MM v[108:111],a[252:255],v[212:215],v[108:111]
EX v84,v84
MM v[104:107],a[236:239],v[212:215],v[132:135]
TR a[184:185],v233 offset:34304
TR a[186:187],v233 offset:38400
MM v[104:107],a[232:235],v[196:199],v[104:107]
EX v85,v85
MM v[104:107],a[228:231],v[180:183],v[104:107]
TR a[188:189],v233 offset:42496
CV v82,v84,v85
MM v[104:107],a[224:227],v[164:167],v[104:107]
EX v86,v86
MM v[112:115],a[224:227],v[168:171],v[136:139]
EX v87,v87
MM v[112:115],a[228:231],v[184:187],v[112:115]
TR a[190:191],v233 offset:46592
CV v83,v86,v87
MM v[112:115],a[232:235],v[200:203],v[112:115]
EX v88,v88
MM v[112:115],a[236:239],v[216:219],v[112:115]
EX v89,v89
MM v[116:119],a[252:255],v[216:219],v[136:139]
EX v90,v90
MM v[116:119],a[248:251],v[200:203],v[116:119]
EX v91,v91
MM v[116:119],a[244:247],v[184:187],v[116:119]
EX v92,v92
MM v[116:119],a[240:243],v[168:171],v[116:119]
EX v93,v93
MM v[124:127],a[240:243],v[172:175],v[140:143]
EX v94,v94
MM v[124:127],a[244:247],v[188:191],v[124:127]
EX v95,v95
MM v[124:127],a[248:251],v[204:207],v[124:127]
EX v96,v96
MM v[124:127],a[252:255],v[220:223],v[124:127]
EX v97,v97
MM v[120:123],a[236:239],v[220:223],v[140:143]
EX v98,v98
MM v[120:123],a[232:235],v[204:207],v[120:123]
EX v99,v99
MM v[120:123],a[228:231],v[188:191],v[120:123]
s_getpc_b64 s[98:99]
LTA4_%=:
s_min_u32 s73, %[swave], 1
s_lshl_b32 s73, s73, 12
s_add_u32 s73, s73, LPG21_%=-LTA4_%=
s_add_u32 s98, s98, s73
s_addc_u32 s99, s99, 0
s_mov_b64 exec, 0
s_setpc_b64 s[98:99]
LTR4_%=:
s_mov_b64 exec, -1
CV v88,v88,v89
MM v[120:123],a[224:227],v[172:175],v[120:123]
s_waitcnt lgkmcnt(0)
s_barrier
MM a[0:3],a[128:131],v[0:3],a[0:3]
s_add_u32 m0, s65, 32768
CV v89,v90,v91
CV v90,v92,v93
MM a[0:3],a[132:135],v[4:7],a[0:3]
RD a[192:195],v224 offset:0
CV v91,v94,v95
MM a[8:11],a[132:135],v[12:15],a[8:11]
buffer_load_dwordx4 v228, s[8:11], s64 offen offset:0 lds
MM a[8:11],a[128:131],v[8:11],a[8:11]
RD a[196:199],v225 offset:0
CV v68,v96,v97
MM a[16:19],a[128:131],v[16:19],a[16:19]
RD a[200:203],v226 offset:0
CV v69,v98,v99
MM a[16:19],a[132:135],v[20:23],a[16:19]
buffer_load_dwordx4 v229, s[8:11], s64 offen offset:1024 lds
MM a[24:27],a[132:135],v[28:31],a[24:27]
RD a[204:207],v227 offset:0
RD a[208:211],v224 offset:4096
MM a[24:27],a[128:131],v[24:27],a[24:27]
RD a[212:215],v225 offset:4096
RD a[216:219],v226 offset:4096
MM a[28:31],a[136:139],v[24:27],a[28:31]
buffer_load_dwordx4 v230, s[8:11], s64 offen offset:2048 lds
MM a[28:31],a[140:143],v[28:31],a[28:31]
EX v100,v100
MM a[20:23],a[140:143],v[20:23],a[20:23]
EX v101,v101
MM a[20:23],a[136:139],v[16:19],a[20:23]
RD a[220:223],v227 offset:4096
CV v70,v100,v101
MM a[12:15],a[136:139],v[8:11],a[12:15]
EX v102,v102
MM a[12:15],a[140:143],v[12:15],a[12:15]
buffer_load_dwordx4 v231, s[8:11], s64 offen offset:3072 lds
MM a[4:7],a[140:143],v[4:7],a[4:7]
s_add_u32 m0, s65, 98304
EX v103,v103
MM a[4:7],a[136:139],v[0:3],a[4:7]
RD a[224:227],v224 offset:8192
CV v71,v102,v103
MM a[32:35],a[144:147],v[0:3],a[32:35]
EX v104,v104
MM a[32:35],a[148:151],v[4:7],a[32:35]
EX v105,v105
MM a[40:43],a[148:151],v[12:15],a[40:43]
RD a[228:231],v225 offset:8192
CV v76,v104,v105
MM a[40:43],a[144:147],v[8:11],a[40:43]
EX v106,v106
MM a[48:51],a[144:147],v[16:19],a[48:51]
buffer_load_dwordx4 v235, s[12:15], s81 offen offset:0 lds
MM a[48:51],a[148:151],v[20:23],a[48:51]
EX v107,v107
MM a[56:59],a[148:151],v[28:31],a[56:59]
RD a[232:235],v226 offset:8192
CV v77,v106,v107
MM a[56:59],a[144:147],v[24:27],a[56:59]
EX v108,v108
MM a[60:63],a[152:155],v[24:27],a[60:63]
EX v109,v109
MM a[60:63],a[156:159],v[28:31],a[60:63]
RD a[236:239],v227 offset:8192
CV v78,v108,v109
MM a[52:55],a[156:159],v[20:23],a[52:55]
EX v110,v110
MM a[52:55],a[152:155],v[16:19],a[52:55]
buffer_load_dwordx4 v235, s[12:15], s64 offen offset:1024 lds
MM a[44:47],a[152:155],v[8:11],a[44:47]
EX v111,v111
MM a[44:47],a[156:159],v[12:15],a[44:47]
RD a[240:243],v224 offset:12288
CV v79,v110,v111
MM a[36:39],a[156:159],v[4:7],a[36:39]
EX v112,v112
MM a[36:39],a[152:155],v[0:3],a[36:39]
EX v113,v113
MM a[64:67],a[160:163],v[0:3],a[64:67]
RD a[244:247],v225 offset:12288
CV v84,v112,v113
MM a[64:67],a[164:167],v[4:7],a[64:67]
EX v114,v114
MM a[72:75],a[164:167],v[12:15],a[72:75]
buffer_load_dwordx4 v235, s[12:15], s81 offen offset:2048 lds
MM a[72:75],a[160:163],v[8:11],a[72:75]
EX v115,v115
MM a[80:83],a[160:163],v[16:19],a[80:83]
RD a[248:251],v226 offset:12288
CV v85,v114,v115
MM a[80:83],a[164:167],v[20:23],a[80:83]
EX v116,v116
MM a[88:91],a[164:167],v[28:31],a[88:91]
EX v117,v117
MM a[88:91],a[160:163],v[24:27],a[88:91]
RD a[252:255],v227 offset:12288
CV v86,v116,v117
MM a[92:95],a[168:171],v[24:27],a[92:95]
EX v118,v118
MM a[92:95],a[172:175],v[28:31],a[92:95]
buffer_load_dwordx4 v235, s[12:15], s64 offen offset:3072 lds
MM a[84:87],a[172:175],v[20:23],a[84:87]
EX v119,v119
MM a[84:87],a[168:171],v[16:19],a[84:87]
CV v87,v118,v119
MM a[76:79],a[168:171],v[8:11],a[76:79]
EX v120,v120
MM a[76:79],a[172:175],v[12:15],a[76:79]
EX v121,v121
MM a[68:71],a[172:175],v[4:7],a[68:71]
EX v122,v122
MM a[68:71],a[168:171],v[0:3],a[68:71]
EX v123,v123
MM a[96:99],a[176:179],v[0:3],a[96:99]
EX v124,v124
MM a[96:99],a[180:183],v[4:7],a[96:99]
EX v125,v125
MM a[104:107],a[180:183],v[12:15],a[104:107]
EX v126,v126
MM a[104:107],a[176:179],v[8:11],a[104:107]
EX v127,v127
MM a[112:115],a[176:179],v[16:19],a[112:115]
CV v92,v120,v121
CV v93,v122,v123
MM a[112:115],a[180:183],v[20:23],a[112:115]
CV v94,v124,v125
CV v95,v126,v127
MM a[120:123],a[180:183],v[28:31],a[120:123]
MM a[120:123],a[176:179],v[24:27],a[120:123]
MM a[124:127],a[184:187],v[24:27],a[124:127]
v_add_f32 v244, v144, v145
v_add_f32 v245, v148, v149
MM a[124:127],a[188:191],v[28:31],a[124:127]
v_add_f32 v246, v152, v153
v_add_f32 v247, v156, v157
MM a[116:119],a[188:191],v[20:23],a[116:119]
MM a[116:119],a[184:187],v[16:19],a[116:119]
MM a[108:111],a[184:187],v[8:11],a[108:111]
MM a[108:111],a[188:191],v[12:15],a[108:111]
MM a[100:103],a[188:191],v[4:7],a[100:103]
MM a[100:103],a[184:187],v[0:3],a[100:103]
v_smfmac_f32_16x16x64_bf16 v[144:147], v[236:239], v[64:71], v234
v_smfmac_f32_16x16x64_bf16 v[148:151], v[236:239], v[72:79], v234
v_smfmac_f32_16x16x64_bf16 v[152:155], v[236:239], v[80:87], v234
v_smfmac_f32_16x16x64_bf16 v[156:159], v[236:239], v[88:95], v234
s_waitcnt lgkmcnt(0)
MM v[0:3],a[192:195],v[160:163],v[128:131]
s_add_u32 s64, s64, 16384
TR a[128:129],v232 offset:49152
s_add_u32 s81, s64, 896
MM v[0:3],a[196:199],v[176:179],v[0:3]
)ASM"
R"ASM(
TR a[130:131],v232 offset:53248
v_max3_f32 v249, v144, v145, v148
MM v[0:3],a[200:203],v[192:195],v[0:3]
TR a[132:133],v232 offset:57344
v_max3_f32 v249, v249, v149, v152
MM v[0:3],a[204:207],v[208:211],v[0:3]
TR a[134:135],v232 offset:61440
v_max3_f32 v249, v249, v153, v156
MM v[4:7],a[220:223],v[208:211],v[128:131]
TR a[136:137],v233 offset:49152
v_max_f32 v249, v249, v157
MM v[4:7],a[216:219],v[192:195],v[4:7]
TR a[138:139],v233 offset:53248
v_cmp_lt_f32 vcc, s71, v249
MM v[4:7],a[212:215],v[176:179],v[4:7]
TR a[140:141],v233 offset:57344
TR a[142:143],v233 offset:61440
MM v[4:7],a[208:211],v[160:163],v[4:7]
s_cbranch_vccnz LSITE27_%=
LBACK27_%=:
MM v[12:15],a[208:211],v[164:167],v[132:135]
v_lshrrev_b32 v125, 8, v248
v_lshlrev_b32 v124, 10, v125
MM v[12:15],a[212:215],v[180:183],v[12:15]
v_bfe_u32 v126, v248, 6, 2
v_lshl_add_u32 v124, v126, 7, v124
MM v[12:15],a[216:219],v[196:199],v[12:15]
v_bfe_u32 v126, v248, 4, 2
v_lshl_add_u32 v124, v126, 3, v124
MM v[12:15],a[220:223],v[212:215],v[12:15]
s_lshl_b32 s91, %[swave], 5
EX v0,v0
MM v[8:11],a[204:207],v[212:215],v[132:135]
s_add_u32 s91, s91, %[slds]
EX v1,v1
MM v[8:11],a[200:203],v[196:199],v[8:11]
s_add_u32 s91, s91, 139264
v_add_u32 v124, s91, v124
v_and_b32 v126, 112, v248
MM v[8:11],a[196:199],v[180:183],v[8:11]
s_add_u32 s91, %[slds], 131072
v_lshl_add_u32 v126, v125, 7, v126
v_add_u32 v126, s91, v126
MM v[8:11],a[192:195],v[164:167],v[8:11]
v_bfe_u32 v127, v248, 4, 3
v_mul_u32_u24 v127, 2560, v127
MM v[16:19],a[192:195],v[168:171],v[136:139]
v_lshl_add_u32 v127, v125, 4, v127
CV v0,v0,v1
EX v2,v2
MM v[16:19],a[196:199],v[184:187],v[16:19]
s_lshl_b32 s90, %[swave], 6
EX v3,v3
MM v[16:19],a[200:203],v[200:203],v[16:19]
s_lshl_b32 s91, s47, 8
EX v4,v4
MM v[16:19],a[204:207],v[216:219],v[16:19]
s_add_u32 s90, s90, s91
TR a[144:145],v232 offset:49664
TR a[146:147],v232 offset:53760
MM v[20:23],a[220:223],v[216:219],v[136:139]
s_mul_i32 s91, s46, 17920
EX v5,v5
MM v[20:23],a[216:219],v[200:203],v[20:23]
s_add_u32 s90, s90, s91
TR a[148:149],v232 offset:57856
CV v1,v2,v3
MM v[20:23],a[212:215],v[184:187],v[20:23]
s_add_u32 s90, s90, 21659648
EX v6,v6
MM v[20:23],a[208:211],v[168:171],v[20:23]
EX v7,v7
MM v[28:31],a[208:211],v[172:175],v[140:143]
TR a[150:151],v232 offset:61952
TR a[152:153],v233 offset:49664
MM v[28:31],a[212:215],v[188:191],v[28:31]
EX v8,v8
MM v[28:31],a[216:219],v[204:207],v[28:31]
TR a[154:155],v233 offset:53760
CV v2,v4,v5
MM v[28:31],a[220:223],v[220:223],v[28:31]
EX v9,v9
MM v[24:27],a[204:207],v[220:223],v[140:143]
TR a[156:157],v233 offset:57856
CV v3,v6,v7
MM v[24:27],a[200:203],v[204:207],v[24:27]
EX v10,v10
MM v[24:27],a[196:199],v[188:191],v[24:27]
TR a[158:159],v233 offset:61952
CV v8,v8,v9
MM v[24:27],a[192:195],v[172:175],v[24:27]
EX v11,v11
MM v[32:35],a[224:227],v[160:163],v[128:131]
TR a[160:161],v232 offset:50176
TR a[162:163],v232 offset:54272
MM v[32:35],a[228:231],v[176:179],v[32:35]
EX v12,v12
MM v[32:35],a[232:235],v[192:195],v[32:35]
EX v13,v13
MM v[32:35],a[236:239],v[208:211],v[32:35]
TR a[164:165],v232 offset:58368
CV v9,v10,v11
MM v[36:39],a[252:255],v[208:211],v[128:131]
EX v14,v14
MM v[36:39],a[248:251],v[192:195],v[36:39]
TR a[166:167],v232 offset:62464
CV v10,v12,v13
MM v[36:39],a[244:247],v[176:179],v[36:39]
EX v15,v15
MM v[36:39],a[240:243],v[160:163],v[36:39]
TR a[168:169],v233 offset:50176
TR a[170:171],v233 offset:54272
MM v[44:47],a[240:243],v[164:167],v[132:135]
EX v16,v16
MM v[44:47],a[244:247],v[180:183],v[44:47]
EX v17,v17
MM v[44:47],a[248:251],v[196:199],v[44:47]
TR a[172:173],v233 offset:58368
CV v11,v14,v15
MM v[44:47],a[252:255],v[212:215],v[44:47]
EX v18,v18
MM v[40:43],a[236:239],v[212:215],v[132:135]
TR a[174:175],v233 offset:62464
CV v16,v16,v17
MM v[40:43],a[232:235],v[196:199],v[40:43]
EX v19,v19
MM v[40:43],a[228:231],v[180:183],v[40:43]
TR a[176:177],v232 offset:50688
TR a[178:179],v232 offset:54784
MM v[40:43],a[224:227],v[164:167],v[40:43]
EX v20,v20
MM v[48:51],a[224:227],v[168:171],v[136:139]
EX v21,v21
MM v[48:51],a[228:231],v[184:187],v[48:51]
TR a[180:181],v232 offset:58880
CV v17,v18,v19
MM v[48:51],a[232:235],v[200:203],v[48:51]
EX v22,v22
MM v[48:51],a[236:239],v[216:219],v[48:51]
TR a[182:183],v232 offset:62976
CV v18,v20,v21
MM v[52:55],a[252:255],v[216:219],v[136:139]
EX v23,v23
MM v[52:55],a[248:251],v[200:203],v[52:55]
TR a[184:185],v233 offset:50688
TR a[186:187],v233 offset:54784
MM v[52:55],a[244:247],v[184:187],v[52:55]
EX v24,v24
MM v[52:55],a[240:243],v[168:171],v[52:55]
TR a[188:189],v233 offset:58880
CV v19,v22,v23
MM v[60:63],a[240:243],v[172:175],v[140:143]
EX v25,v25
MM v[60:63],a[244:247],v[188:191],v[60:63]
TR a[190:191],v233 offset:62976
CV v24,v24,v25
MM v[60:63],a[248:251],v[204:207],v[60:63]
EX v26,v26
MM v[60:63],a[252:255],v[220:223],v[60:63]
EX v27,v27
MM v[56:59],a[236:239],v[220:223],v[140:143]
EX v28,v28
MM v[56:59],a[232:235],v[204:207],v[56:59]
EX v29,v29
MM v[56:59],a[228:231],v[188:191],v[56:59]
EX v30,v30
MM v[56:59],a[224:227],v[172:175],v[56:59]
s_waitcnt vmcnt(8) lgkmcnt(0)
s_barrier
MM a[0:3],a[128:131],v[64:67],a[0:3]
s_add_u32 m0, s65, 49152
CV v25,v26,v27
CV v26,v28,v29
MM a[0:3],a[132:135],v[68:71],a[0:3]
RD a[192:195],v224 offset:16384
RD a[196:199],v225 offset:16384
MM a[8:11],a[132:135],v[76:79],a[8:11]
EX v31,v31
MM a[8:11],a[128:131],v[72:75],a[8:11]
RD a[200:203],v226 offset:16384
CV v27,v30,v31
MM a[16:19],a[128:131],v[80:83],a[16:19]
TR v[96:97],v124 offset:0
TR v[98:99],v124 offset:512
MM a[16:19],a[132:135],v[84:87],a[16:19]
RD v[108:111],v126 offset:0
TR v[100:101],v124 offset:4096
MM a[24:27],a[132:135],v[92:95],a[24:27]
TR v[102:103],v124 offset:4608
RD v[112:115],v126 offset:512
MM a[24:27],a[128:131],v[88:91],a[24:27]
TR v[104:105],v124 offset:8192
TR v[106:107],v124 offset:8704
MM a[28:31],a[136:139],v[88:91],a[28:31]
RD v[116:119],v126 offset:1024
RD a[204:207],v227 offset:16384
EX v32,v32
MM a[28:31],a[140:143],v[92:95],a[28:31]
EX v33,v33
MM a[20:23],a[140:143],v[84:87],a[20:23]
EX v34,v34
MM a[20:23],a[136:139],v[80:83],a[20:23]
buffer_load_dwordx4 v228, s[8:11], s64 offen offset:0 lds
LPG20_%=:
s_cbranch_execz LTR3_%=
MM a[12:15],a[136:139],v[72:75],a[12:15]
EX v35,v35
MM a[12:15],a[140:143],v[76:79],a[12:15]
EX v36,v36
MM a[4:7],a[140:143],v[68:71],a[4:7]
CV v4,v32,v33
CV v5,v34,v35
MM a[4:7],a[136:139],v[64:67],a[4:7]
EX v37,v37
MM a[32:35],a[144:147],v[64:67],a[32:35]
EX v38,v38
MM a[32:35],a[148:151],v[68:71],a[32:35]
buffer_load_dwordx4 v229, s[8:11], s64 offen offset:1024 lds
MM a[40:43],a[148:151],v[76:79],a[40:43]
EX v39,v39
MM a[40:43],a[144:147],v[72:75],a[40:43]
RD a[208:211],v224 offset:20480
CV v6,v36,v37
MM a[48:51],a[144:147],v[80:83],a[48:51]
s_waitcnt lgkmcnt(2)
EX v40,v40
MM a[48:51],a[148:151],v[84:87],a[48:51]
RD a[212:215],v225 offset:20480
CV v7,v38,v39
MM v[120:123],v[96:99],v[108:111],0
MM v[120:123],v[100:103],v[112:115],v[120:123]
MM v[120:123],v[104:107],v[116:119],v[120:123]
MM a[56:59],a[148:151],v[92:95],a[56:59]
TR v[96:97],v124 offset:12288
TR v[98:99],v124 offset:12800
MM a[56:59],a[144:147],v[88:91],a[56:59]
RD v[108:111],v126 offset:1536
TR v[100:101],v124 offset:16384
MM a[60:63],a[152:155],v[88:91],a[60:63]
TR v[102:103],v124 offset:16896
RD v[112:115],v126 offset:2048
EX v41,v41
MM a[60:63],a[156:159],v[92:95],a[60:63]
TR v[104:105],v124 offset:20480
TR v[106:107],v124 offset:20992
MM a[52:55],a[156:159],v[84:87],a[52:55]
RD v[116:119],v126 offset:2560
CV v12,v40,v41
EX v42,v42
MM a[52:55],a[152:155],v[80:83],a[52:55]
EX v43,v43
MM a[44:47],a[152:155],v[72:75],a[44:47]
buffer_load_dwordx4 v230, s[8:11], s64 offen offset:2048 lds
MM a[44:47],a[156:159],v[76:79],a[44:47]
EX v44,v44
MM a[36:39],a[156:159],v[68:71],a[36:39]
EX v45,v45
MM a[36:39],a[152:155],v[64:67],a[36:39]
RD a[216:219],v226 offset:20480
CV v13,v42,v43
MM a[64:67],a[160:163],v[64:67],a[64:67]
EX v46,v46
MM a[64:67],a[164:167],v[68:71],a[64:67]
EX v47,v47
MM a[72:75],a[164:167],v[76:79],a[72:75]
buffer_load_dwordx4 v231, s[8:11], s64 offen offset:3072 lds
MM a[72:75],a[160:163],v[72:75],a[72:75]
s_add_u32 m0, s65, 114688
RD a[220:223],v227 offset:20480
CV v14,v44,v45
MM a[80:83],a[160:163],v[80:83],a[80:83]
EX v48,v48
MM a[80:83],a[164:167],v[84:87],a[80:83]
EX v49,v49
MM a[88:91],a[164:167],v[92:95],a[88:91]
s_waitcnt lgkmcnt(2)
RD a[224:227],v224 offset:24576
CV v15,v46,v47
MM a[88:91],a[160:163],v[88:91],a[88:91]
EX v50,v50
MM v[120:123],v[96:99],v[108:111],v[120:123]
MM v[120:123],v[100:103],v[112:115],v[120:123]
MM v[120:123],v[104:107],v[116:119],v[120:123]
MM a[92:95],a[168:171],v[88:91],a[92:95]
EX v51,v51
MM a[92:95],a[172:175],v[92:95],a[92:95]
buffer_load_dwordx4 v235, s[12:15], s81 offen offset:0 lds
MM a[84:87],a[172:175],v[84:87],a[84:87]
RD a[228:231],v225 offset:24576
CV v20,v48,v49
MM a[84:87],a[168:171],v[80:83],a[84:87]
EX v52,v52
MM a[76:79],a[168:171],v[72:75],a[76:79]
EX v53,v53
MM a[76:79],a[172:175],v[76:79],a[76:79]
RD a[232:235],v226 offset:24576
CV v21,v50,v51
MM a[68:71],a[172:175],v[68:71],a[68:71]
s_mov_b32 exec_lo, 0x00ff00ff
s_mov_b32 exec_hi, 0x00ff00ff
buffer_store_dwordx4 v[120:123], v127, s[20:23], s90 offen offset:0 sc1
s_mov_b64 exec, -1
s_nop 0
MM a[68:71],a[168:171],v[64:67],a[68:71]
EX v54,v54
MM a[96:99],a[176:179],v[64:67],a[96:99]
EX v55,v55
MM a[96:99],a[180:183],v[68:71],a[96:99]
RD a[236:239],v227 offset:24576
CV v22,v52,v53
MM a[104:107],a[180:183],v[76:79],a[104:107]
EX v56,v56
MM a[104:107],a[176:179],v[72:75],a[104:107]
buffer_load_dwordx4 v235, s[12:15], s64 offen offset:1024 lds
MM a[112:115],a[176:179],v[80:83],a[112:115]
EX v57,v57
MM a[112:115],a[180:183],v[84:87],a[112:115]
RD a[240:243],v224 offset:28672
CV v23,v54,v55
MM a[120:123],a[180:183],v[92:95],a[120:123]
EX v58,v58
MM a[120:123],a[176:179],v[88:91],a[120:123]
EX v59,v59
MM a[124:127],a[184:187],v[88:91],a[124:127]
v_add_f32 v244, v144, v145
v_add_f32 v245, v148, v149
MM a[124:127],a[188:191],v[92:95],a[124:127]
v_add_f32 v246, v152, v153
v_add_f32 v247, v156, v157
MM a[116:119],a[188:191],v[84:87],a[116:119]
EX v60,v60
MM a[116:119],a[184:187],v[80:83],a[116:119]
RD a[244:247],v225 offset:28672
CV v28,v56,v57
MM a[108:111],a[184:187],v[72:75],a[108:111]
EX v61,v61
MM a[108:111],a[188:191],v[76:79],a[108:111]
buffer_load_dwordx4 v235, s[12:15], s81 offen offset:2048 lds
MM a[100:103],a[188:191],v[68:71],a[100:103]
EX v62,v62
MM a[100:103],a[184:187],v[64:67],a[100:103]
RD a[248:251],v226 offset:28672
CV v29,v58,v59
v_smfmac_f32_16x16x64_bf16 v[144:147], v[236:239], v[0:7], v234
RD a[252:255],v227 offset:28672
EX v63,v63
v_smfmac_f32_16x16x64_bf16 v[148:151], v[236:239], v[8:15], v234
CV v30,v60,v61
v_smfmac_f32_16x16x64_bf16 v[152:155], v[236:239], v[16:23], v234
CV v31,v62,v63
s_nop 1
v_smfmac_f32_16x16x64_bf16 v[156:159], v[236:239], v[24:31], v234
s_waitcnt lgkmcnt(0)
buffer_load_dwordx4 v235, s[12:15], s64 offen offset:3072 lds
MM v[64:67],a[192:195],v[160:163],v[128:131]
s_add_u32 s64, s64, 16384
TR a[128:129],v232 offset:0
s_add_u32 s81, s64, 896
MM v[64:67],a[196:199],v[176:179],v[64:67]
TR a[130:131],v232 offset:4096
v_max3_f32 v249, v144, v145, v148
MM v[64:67],a[200:203],v[192:195],v[64:67]
TR a[132:133],v232 offset:8192
v_max3_f32 v249, v249, v149, v152
MM v[64:67],a[204:207],v[208:211],v[64:67]
TR a[134:135],v232 offset:12288
v_max3_f32 v249, v249, v153, v156
MM v[68:71],a[220:223],v[208:211],v[128:131]
TR a[136:137],v233 offset:0
v_max_f32 v249, v249, v157
MM v[68:71],a[216:219],v[192:195],v[68:71]
TR a[138:139],v233 offset:4096
v_cmp_lt_f32 vcc, s71, v249
MM v[68:71],a[212:215],v[176:179],v[68:71]
TR a[140:141],v233 offset:8192
TR a[142:143],v233 offset:12288
MM v[68:71],a[208:211],v[160:163],v[68:71]
s_cbranch_vccnz LSITE28_%=
LBACK28_%=:
MM v[76:79],a[208:211],v[164:167],v[132:135]
EX v64,v64
MM v[76:79],a[212:215],v[180:183],v[76:79]
EX v65,v65
MM v[76:79],a[216:219],v[196:199],v[76:79]
TR a[144:145],v232 offset:512
CV v64,v64,v65
MM v[76:79],a[220:223],v[212:215],v[76:79]
EX v66,v66
MM v[72:75],a[204:207],v[212:215],v[132:135]
EX v67,v67
MM v[72:75],a[200:203],v[196:199],v[72:75]
)ASM"
R"ASM(
TR a[146:147],v232 offset:4608
CV v65,v66,v67
MM v[72:75],a[196:199],v[180:183],v[72:75]
EX v68,v68
MM v[72:75],a[192:195],v[164:167],v[72:75]
TR a[148:149],v232 offset:8704
TR a[150:151],v232 offset:12800
MM v[80:83],a[192:195],v[168:171],v[136:139]
EX v69,v69
MM v[80:83],a[196:199],v[184:187],v[80:83]
TR a[152:153],v233 offset:512
CV v66,v68,v69
MM v[80:83],a[200:203],v[200:203],v[80:83]
EX v70,v70
MM v[80:83],a[204:207],v[216:219],v[80:83]
TR a[154:155],v233 offset:4608
TR a[156:157],v233 offset:8704
MM v[84:87],a[220:223],v[216:219],v[136:139]
v_lshlrev_b32 v32, 3, v248
v_min_u32 v32, 4992, v32
MM v[84:87],a[216:219],v[200:203],v[84:87]
s_sub_u32 s91, 5242880, s26
s_sub_u32 s60, s24, s91
s_subb_u32 s61, s25, 0
MM v[84:87],a[212:215],v[184:187],v[84:87]
s_mov_b32 s62, 5242880
EX v71,v71
MM v[84:87],a[208:211],v[168:171],v[84:87]
s_mov_b32 s63, 131072
TR a[158:159],v233 offset:12800
CV v67,v70,v71
MM v[92:95],a[208:211],v[172:175],v[140:143]
s_lshl_b32 s90, s16, 2
EX v72,v72
MM v[92:95],a[212:215],v[188:191],v[92:95]
s_add_u32 s90, s90, %[swave]
EX v73,v73
MM v[92:95],a[216:219],v[204:207],v[92:95]
s_mul_i32 s90, s90, 10240
TR a[160:161],v232 offset:1024
CV v72,v72,v73
MM v[92:95],a[220:223],v[220:223],v[92:95]
s_add_u32 s90, s90, 5120
EX v74,v74
MM v[88:91],a[204:207],v[220:223],v[140:143]
s_add_u32 s91, %[slds], 135168
s_mov_b32 m0, s91
s_nop 0
buffer_load_dword v32, s[60:63], s90 offen offset:0 lds
MM v[88:91],a[200:203],v[204:207],v[88:91]
EX v75,v75
MM v[88:91],a[196:199],v[188:191],v[88:91]
TR a[162:163],v232 offset:5120
CV v73,v74,v75
MM v[88:91],a[192:195],v[172:175],v[88:91]
EX v76,v76
MM v[96:99],a[224:227],v[160:163],v[128:131]
TR a[164:165],v232 offset:9216
TR a[166:167],v232 offset:13312
MM v[96:99],a[228:231],v[176:179],v[96:99]
EX v77,v77
MM v[96:99],a[232:235],v[192:195],v[96:99]
TR a[168:169],v233 offset:1024
CV v74,v76,v77
MM v[96:99],a[236:239],v[208:211],v[96:99]
EX v78,v78
MM v[100:103],a[252:255],v[208:211],v[128:131]
EX v79,v79
MM v[100:103],a[248:251],v[192:195],v[100:103]
TR a[170:171],v233 offset:5120
CV v75,v78,v79
MM v[100:103],a[244:247],v[176:179],v[100:103]
EX v80,v80
MM v[100:103],a[240:243],v[160:163],v[100:103]
TR a[172:173],v233 offset:9216
TR a[174:175],v233 offset:13312
MM v[108:111],a[240:243],v[164:167],v[132:135]
s_add_u32 s67, s95, 5120
EX v81,v81
MM v[108:111],a[244:247],v[180:183],v[108:111]
s_mov_b32 s70, 1
TR a[176:177],v232 offset:1536
CV v80,v80,v81
MM v[108:111],a[248:251],v[196:199],v[108:111]
EX v82,v82
MM v[108:111],a[252:255],v[212:215],v[108:111]
EX v83,v83
MM v[104:107],a[236:239],v[212:215],v[132:135]
TR a[178:179],v232 offset:5632
CV v81,v82,v83
MM v[104:107],a[232:235],v[196:199],v[104:107]
EX v84,v84
MM v[104:107],a[228:231],v[180:183],v[104:107]
TR a[180:181],v232 offset:9728
TR a[182:183],v232 offset:13824
MM v[104:107],a[224:227],v[164:167],v[104:107]
EX v85,v85
MM v[112:115],a[224:227],v[168:171],v[136:139]
TR a[184:185],v233 offset:1536
CV v82,v84,v85
MM v[112:115],a[228:231],v[184:187],v[112:115]
EX v86,v86
MM v[112:115],a[232:235],v[200:203],v[112:115]
EX v87,v87
MM v[112:115],a[236:239],v[216:219],v[112:115]
TR a[186:187],v233 offset:5632
CV v83,v86,v87
MM v[116:119],a[252:255],v[216:219],v[136:139]
EX v88,v88
MM v[116:119],a[248:251],v[200:203],v[116:119]
TR a[188:189],v233 offset:9728
TR a[190:191],v233 offset:13824
MM v[116:119],a[244:247],v[184:187],v[116:119]
EX v89,v89
MM v[116:119],a[240:243],v[168:171],v[116:119]
EX v90,v90
MM v[124:127],a[240:243],v[172:175],v[140:143]
EX v91,v91
MM v[124:127],a[244:247],v[188:191],v[124:127]
EX v92,v92
MM v[124:127],a[248:251],v[204:207],v[124:127]
EX v93,v93
MM v[124:127],a[252:255],v[220:223],v[124:127]
EX v94,v94
MM v[120:123],a[236:239],v[220:223],v[140:143]
EX v95,v95
MM v[120:123],a[232:235],v[204:207],v[120:123]
EX v96,v96
MM v[120:123],a[228:231],v[188:191],v[120:123]
EX v97,v97
MM v[120:123],a[224:227],v[172:175],v[120:123]
s_waitcnt vmcnt(14) lgkmcnt(0)
s_barrier
MM a[0:3],a[128:131],v[0:3],a[0:3]
s_add_u32 m0, s65, 0
CV v88,v88,v89
CV v89,v90,v91
MM a[0:3],a[132:135],v[4:7],a[0:3]
RD a[192:195],v224 offset:32768
CV v90,v92,v93
MM a[8:11],a[132:135],v[12:15],a[8:11]
RD a[196:199],v225 offset:32768
CV v91,v94,v95
MM a[8:11],a[128:131],v[8:11],a[8:11]
buffer_load_dwordx4 v228, s[8:11], s64 offen offset:0 lds
MM a[16:19],a[128:131],v[16:19],a[16:19]
RD a[200:203],v226 offset:32768
CV v68,v96,v97
MM a[16:19],a[132:135],v[20:23],a[16:19]
buffer_load_dwordx4 v229, s[8:11], s64 offen offset:1024 lds
MM a[24:27],a[132:135],v[28:31],a[24:27]
EX v98,v98
MM a[24:27],a[128:131],v[24:27],a[24:27]
RD a[204:207],v227 offset:32768
RD a[208:211],v224 offset:36864
MM a[28:31],a[136:139],v[24:27],a[28:31]
EX v99,v99
MM a[28:31],a[140:143],v[28:31],a[28:31]
RD a[212:215],v225 offset:36864
CV v69,v98,v99
MM a[20:23],a[140:143],v[20:23],a[20:23]
EX v100,v100
MM a[20:23],a[136:139],v[16:19],a[20:23]
EX v101,v101
MM a[12:15],a[136:139],v[8:11],a[12:15]
RD a[216:219],v226 offset:36864
CV v70,v100,v101
MM a[12:15],a[140:143],v[12:15],a[12:15]
buffer_load_dwordx4 v230, s[8:11], s64 offen offset:2048 lds
MM a[4:7],a[140:143],v[4:7],a[4:7]
EX v102,v102
MM a[4:7],a[136:139],v[0:3],a[4:7]
EX v103,v103
MM a[32:35],a[144:147],v[0:3],a[32:35]
RD a[220:223],v227 offset:36864
CV v71,v102,v103
MM a[32:35],a[148:151],v[4:7],a[32:35]
EX v104,v104
MM a[40:43],a[148:151],v[12:15],a[40:43]
EX v105,v105
MM a[40:43],a[144:147],v[8:11],a[40:43]
RD a[224:227],v224 offset:40960
CV v76,v104,v105
MM a[48:51],a[144:147],v[16:19],a[48:51]
buffer_load_dwordx4 v231, s[8:11], s64 offen offset:3072 lds
MM a[48:51],a[148:151],v[20:23],a[48:51]
s_add_u32 m0, s65, 65536
EX v106,v106
MM a[56:59],a[148:151],v[28:31],a[56:59]
EX v107,v107
MM a[56:59],a[144:147],v[24:27],a[56:59]
RD a[228:231],v225 offset:40960
CV v77,v106,v107
MM a[60:63],a[152:155],v[24:27],a[60:63]
EX v108,v108
MM a[60:63],a[156:159],v[28:31],a[60:63]
buffer_load_dwordx4 v235, s[12:15], s81 offen offset:0 lds
MM a[52:55],a[156:159],v[20:23],a[52:55]
EX v109,v109
MM a[52:55],a[152:155],v[16:19],a[52:55]
RD a[232:235],v226 offset:40960
CV v78,v108,v109
MM a[44:47],a[152:155],v[8:11],a[44:47]
EX v110,v110
MM a[44:47],a[156:159],v[12:15],a[44:47]
EX v111,v111
MM a[36:39],a[156:159],v[4:7],a[36:39]
RD a[236:239],v227 offset:40960
CV v79,v110,v111
MM a[36:39],a[152:155],v[0:3],a[36:39]
EX v112,v112
MM a[64:67],a[160:163],v[0:3],a[64:67]
buffer_load_dwordx4 v235, s[12:15], s64 offen offset:1024 lds
MM a[64:67],a[164:167],v[4:7],a[64:67]
EX v113,v113
MM a[72:75],a[164:167],v[12:15],a[72:75]
RD a[240:243],v224 offset:45056
CV v84,v112,v113
MM a[72:75],a[160:163],v[8:11],a[72:75]
EX v114,v114
MM a[80:83],a[160:163],v[16:19],a[80:83]
EX v115,v115
MM a[80:83],a[164:167],v[20:23],a[80:83]
RD a[244:247],v225 offset:45056
CV v85,v114,v115
MM a[88:91],a[164:167],v[28:31],a[88:91]
EX v116,v116
MM a[88:91],a[160:163],v[24:27],a[88:91]
buffer_load_dwordx4 v235, s[12:15], s81 offen offset:2048 lds
MM a[92:95],a[168:171],v[24:27],a[92:95]
EX v117,v117
MM a[92:95],a[172:175],v[28:31],a[92:95]
RD a[248:251],v226 offset:45056
CV v86,v116,v117
MM a[84:87],a[172:175],v[20:23],a[84:87]
EX v118,v118
MM a[84:87],a[168:171],v[16:19],a[84:87]
EX v119,v119
MM a[76:79],a[168:171],v[8:11],a[76:79]
RD a[252:255],v227 offset:45056
CV v87,v118,v119
MM a[76:79],a[172:175],v[12:15],a[76:79]
EX v120,v120
MM a[68:71],a[172:175],v[4:7],a[68:71]
buffer_load_dwordx4 v235, s[12:15], s64 offen offset:3072 lds
MM a[68:71],a[168:171],v[0:3],a[68:71]
EX v121,v121
MM a[96:99],a[176:179],v[0:3],a[96:99]
EX v122,v122
MM a[96:99],a[180:183],v[4:7],a[96:99]
EX v123,v123
MM a[104:107],a[180:183],v[12:15],a[104:107]
EX v124,v124
MM a[104:107],a[176:179],v[8:11],a[104:107]
EX v125,v125
MM a[112:115],a[176:179],v[16:19],a[112:115]
EX v126,v126
MM a[112:115],a[180:183],v[20:23],a[112:115]
EX v127,v127
MM a[120:123],a[180:183],v[28:31],a[120:123]
s_lshl_b32 s91, s47, 2
CV v92,v120,v121
CV v93,v122,v123
MM a[120:123],a[176:179],v[24:27],a[120:123]
s_add_u32 s91, s91, %[swave]
CV v94,v124,v125
CV v95,v126,v127
MM a[124:127],a[184:187],v[24:27],a[124:127]
s_lshl_b32 s91, s91, 2
v_add_f32 v244, v144, v145
v_add_f32 v245, v148, v149
MM a[124:127],a[188:191],v[28:31],a[124:127]
s_add_u32 s91, s91, 73728
v_add_f32 v246, v152, v153
v_add_f32 v247, v156, v157
MM a[116:119],a[188:191],v[20:23],a[116:119]
s_waitcnt vmcnt(12)
v_mov_b32 v250, s69
s_mov_b64 exec, 1
buffer_store_dword v250, off, s[20:23], s91 offset:0 sc1
s_mov_b64 exec, -1
MM a[116:119],a[184:187],v[16:19],a[116:119]
MM a[108:111],a[184:187],v[8:11],a[108:111]
MM a[108:111],a[188:191],v[12:15],a[108:111]
MM a[100:103],a[188:191],v[4:7],a[100:103]
MM a[100:103],a[184:187],v[0:3],a[100:103]
v_smfmac_f32_16x16x64_bf16 v[144:147], v[236:239], v[64:71], v234
v_smfmac_f32_16x16x64_bf16 v[148:151], v[236:239], v[72:79], v234
v_smfmac_f32_16x16x64_bf16 v[152:155], v[236:239], v[80:87], v234
v_smfmac_f32_16x16x64_bf16 v[156:159], v[236:239], v[88:95], v234
s_waitcnt lgkmcnt(0)
s_cmp_ge_u32 s16, 20
s_cbranch_scc1 LLAP_%=
MM v[0:3],a[192:195],v[160:163],v[128:131]
s_add_u32 s64, s64, 16384
TR a[128:129],v232 offset:16384
s_add_u32 s81, s64, 896
MM v[0:3],a[196:199],v[176:179],v[0:3]
TR a[130:131],v232 offset:20480
v_max3_f32 v249, v144, v145, v148
MM v[0:3],a[200:203],v[192:195],v[0:3]
TR a[132:133],v232 offset:24576
v_max3_f32 v249, v249, v149, v152
MM v[0:3],a[204:207],v[208:211],v[0:3]
TR a[134:135],v232 offset:28672
v_max3_f32 v249, v249, v153, v156
MM v[4:7],a[220:223],v[208:211],v[128:131]
TR a[136:137],v233 offset:16384
v_max_f32 v249, v249, v157
MM v[4:7],a[216:219],v[192:195],v[4:7]
TR a[138:139],v233 offset:20480
v_cmp_lt_f32 vcc, s71, v249
MM v[4:7],a[212:215],v[176:179],v[4:7]
TR a[140:141],v233 offset:24576
TR a[142:143],v233 offset:28672
MM v[4:7],a[208:211],v[160:163],v[4:7]
s_cbranch_vccnz LSITE29_%=
LBACK29_%=:
MM v[12:15],a[208:211],v[164:167],v[132:135]
EX v0,v0
MM v[12:15],a[212:215],v[180:183],v[12:15]
EX v1,v1
MM v[12:15],a[216:219],v[196:199],v[12:15]
TR a[144:145],v232 offset:16896
CV v0,v0,v1
MM v[12:15],a[220:223],v[212:215],v[12:15]
EX v2,v2
MM v[8:11],a[204:207],v[212:215],v[132:135]
EX v3,v3
MM v[8:11],a[200:203],v[196:199],v[8:11]
TR a[146:147],v232 offset:20992
CV v1,v2,v3
MM v[8:11],a[196:199],v[180:183],v[8:11]
EX v4,v4
MM v[8:11],a[192:195],v[164:167],v[8:11]
TR a[148:149],v232 offset:25088
TR a[150:151],v232 offset:29184
MM v[16:19],a[192:195],v[168:171],v[136:139]
EX v5,v5
MM v[16:19],a[196:199],v[184:187],v[16:19]
TR a[152:153],v233 offset:16896
CV v2,v4,v5
MM v[16:19],a[200:203],v[200:203],v[16:19]
EX v6,v6
MM v[16:19],a[204:207],v[216:219],v[16:19]
TR a[154:155],v233 offset:20992
TR a[156:157],v233 offset:25088
MM v[20:23],a[220:223],v[216:219],v[136:139]
s_add_u32 s87, s95, 5120
buffer_store_dword v243, v248, s[20:23], s87 offen offset:12 sc1
MM v[20:23],a[216:219],v[200:203],v[20:23]
EX v7,v7
MM v[20:23],a[212:215],v[184:187],v[20:23]
TR a[158:159],v233 offset:29184
CV v3,v6,v7
MM v[20:23],a[208:211],v[168:171],v[20:23]
EX v8,v8
MM v[28:31],a[208:211],v[172:175],v[140:143]
EX v9,v9
MM v[28:31],a[212:215],v[188:191],v[28:31]
TR a[160:161],v232 offset:17408
CV v8,v8,v9
MM v[28:31],a[216:219],v[204:207],v[28:31]
EX v10,v10
MM v[28:31],a[220:223],v[220:223],v[28:31]
EX v11,v11
MM v[24:27],a[204:207],v[220:223],v[140:143]
TR a[162:163],v232 offset:21504
CV v9,v10,v11
MM v[24:27],a[200:203],v[204:207],v[24:27]
TR a[164:165],v232 offset:25600
TR a[166:167],v232 offset:29696
MM v[24:27],a[196:199],v[188:191],v[24:27]
EX v12,v12
MM v[24:27],a[192:195],v[172:175],v[24:27]
EX v13,v13
MM v[32:35],a[224:227],v[160:163],v[128:131]
TR a[168:169],v233 offset:17408
CV v10,v12,v13
MM v[32:35],a[228:231],v[176:179],v[32:35]
EX v14,v14
MM v[32:35],a[232:235],v[192:195],v[32:35]
TR a[170:171],v233 offset:21504
TR a[172:173],v233 offset:25600
MM v[32:35],a[236:239],v[208:211],v[32:35]
EX v15,v15
MM v[36:39],a[252:255],v[208:211],v[128:131]
TR a[174:175],v233 offset:29696
CV v11,v14,v15
MM v[36:39],a[248:251],v[192:195],v[36:39]
EX v16,v16
MM v[36:39],a[244:247],v[176:179],v[36:39]
)ASM"
R"ASM(
EX v17,v17
MM v[36:39],a[240:243],v[160:163],v[36:39]
TR a[176:177],v232 offset:17920
CV v16,v16,v17
MM v[44:47],a[240:243],v[164:167],v[132:135]
EX v18,v18
MM v[44:47],a[244:247],v[180:183],v[44:47]
TR a[178:179],v232 offset:22016
TR a[180:181],v232 offset:26112
MM v[44:47],a[248:251],v[196:199],v[44:47]
EX v19,v19
MM v[44:47],a[252:255],v[212:215],v[44:47]
s_nop 0
LPG21_%=:
s_cbranch_execz LTR4_%=
TR a[182:183],v232 offset:30208
CV v17,v18,v19
MM v[40:43],a[236:239],v[212:215],v[132:135]
EX v20,v20
MM v[40:43],a[232:235],v[196:199],v[40:43]
EX v21,v21
MM v[40:43],a[228:231],v[180:183],v[40:43]
TR a[184:185],v233 offset:17920
CV v18,v20,v21
MM v[40:43],a[224:227],v[164:167],v[40:43]
EX v22,v22
MM v[48:51],a[224:227],v[168:171],v[136:139]
TR a[186:187],v233 offset:22016
TR a[188:189],v233 offset:26112
MM v[48:51],a[228:231],v[184:187],v[48:51]
EX v23,v23
MM v[48:51],a[232:235],v[200:203],v[48:51]
TR a[190:191],v233 offset:30208
CV v19,v22,v23
MM v[48:51],a[236:239],v[216:219],v[48:51]
EX v24,v24
MM v[52:55],a[252:255],v[216:219],v[136:139]
EX v25,v25
MM v[52:55],a[248:251],v[200:203],v[52:55]
EX v26,v26
MM v[52:55],a[244:247],v[184:187],v[52:55]
EX v27,v27
MM v[52:55],a[240:243],v[168:171],v[52:55]
EX v28,v28
MM v[60:63],a[240:243],v[172:175],v[140:143]
EX v29,v29
MM v[60:63],a[244:247],v[188:191],v[60:63]
EX v30,v30
MM v[60:63],a[248:251],v[204:207],v[60:63]
EX v31,v31
MM v[60:63],a[252:255],v[220:223],v[60:63]
EX v32,v32
MM v[56:59],a[236:239],v[220:223],v[140:143]
EX v33,v33
MM v[56:59],a[232:235],v[204:207],v[56:59]
EX v34,v34
MM v[56:59],a[228:231],v[188:191],v[56:59]
EX v35,v35
MM v[56:59],a[224:227],v[172:175],v[56:59]
s_waitcnt lgkmcnt(0)
s_barrier
MM a[0:3],a[128:131],v[64:67],a[0:3]
s_add_u32 m0, s65, 16384
CV v24,v24,v25
CV v25,v26,v27
MM a[0:3],a[132:135],v[68:71],a[0:3]
RD a[192:195],v224 offset:49152
CV v26,v28,v29
MM a[8:11],a[132:135],v[76:79],a[8:11]
RD a[196:199],v225 offset:49152
CV v27,v30,v31
MM a[8:11],a[128:131],v[72:75],a[8:11]
buffer_load_dwordx4 v228, s[8:11], s64 offen offset:0 lds
MM a[16:19],a[128:131],v[80:83],a[16:19]
RD a[200:203],v226 offset:49152
CV v4,v32,v33
MM a[16:19],a[132:135],v[84:87],a[16:19]
RD a[204:207],v227 offset:49152
CV v5,v34,v35
MM a[24:27],a[132:135],v[92:95],a[24:27]
buffer_load_dwordx4 v229, s[8:11], s64 offen offset:1024 lds
MM a[24:27],a[128:131],v[88:91],a[24:27]
RD a[208:211],v224 offset:53248
RD a[212:215],v225 offset:53248
MM a[28:31],a[136:139],v[88:91],a[28:31]
buffer_load_dwordx4 v230, s[8:11], s64 offen offset:2048 lds
MM a[28:31],a[140:143],v[92:95],a[28:31]
EX v36,v36
MM a[20:23],a[140:143],v[84:87],a[20:23]
EX v37,v37
MM a[20:23],a[136:139],v[80:83],a[20:23]
RD a[216:219],v226 offset:53248
CV v6,v36,v37
MM a[12:15],a[136:139],v[72:75],a[12:15]
EX v38,v38
MM a[12:15],a[140:143],v[76:79],a[12:15]
RD a[220:223],v227 offset:53248
RD a[224:227],v224 offset:57344
MM a[4:7],a[140:143],v[68:71],a[4:7]
EX v39,v39
MM a[4:7],a[136:139],v[64:67],a[4:7]
RD a[228:231],v225 offset:57344
CV v7,v38,v39
MM a[32:35],a[144:147],v[64:67],a[32:35]
EX v40,v40
MM a[32:35],a[148:151],v[68:71],a[32:35]
buffer_load_dwordx4 v231, s[8:11], s64 offen offset:3072 lds
MM a[40:43],a[148:151],v[76:79],a[40:43]
s_add_u32 m0, s65, 81920
EX v41,v41
MM a[40:43],a[144:147],v[72:75],a[40:43]
RD a[232:235],v226 offset:57344
CV v12,v40,v41
MM a[48:51],a[144:147],v[80:83],a[48:51]
EX v42,v42
MM a[48:51],a[148:151],v[84:87],a[48:51]
EX v43,v43
MM a[56:59],a[148:151],v[92:95],a[56:59]
RD a[236:239],v227 offset:57344
CV v13,v42,v43
MM a[56:59],a[144:147],v[88:91],a[56:59]
EX v44,v44
MM a[60:63],a[152:155],v[88:91],a[60:63]
buffer_load_dwordx4 v235, s[12:15], s81 offen offset:0 lds
MM a[60:63],a[156:159],v[92:95],a[60:63]
EX v45,v45
MM a[52:55],a[156:159],v[84:87],a[52:55]
RD a[240:243],v224 offset:61440
CV v14,v44,v45
MM a[52:55],a[152:155],v[80:83],a[52:55]
EX v46,v46
MM a[44:47],a[152:155],v[72:75],a[44:47]
EX v47,v47
MM a[44:47],a[156:159],v[76:79],a[44:47]
RD a[244:247],v225 offset:61440
CV v15,v46,v47
MM a[36:39],a[156:159],v[68:71],a[36:39]
buffer_load_dwordx4 v235, s[12:15], s64 offen offset:1024 lds
MM a[36:39],a[152:155],v[64:67],a[36:39]
EX v48,v48
MM a[64:67],a[160:163],v[64:67],a[64:67]
EX v49,v49
MM a[64:67],a[164:167],v[68:71],a[64:67]
RD a[248:251],v226 offset:61440
CV v20,v48,v49
MM a[72:75],a[164:167],v[76:79],a[72:75]
EX v50,v50
MM a[72:75],a[160:163],v[72:75],a[72:75]
EX v51,v51
MM a[80:83],a[160:163],v[80:83],a[80:83]
RD a[252:255],v227 offset:61440
CV v21,v50,v51
MM a[80:83],a[164:167],v[84:87],a[80:83]
buffer_load_dwordx4 v235, s[12:15], s81 offen offset:2048 lds
MM a[88:91],a[164:167],v[92:95],a[88:91]
EX v52,v52
MM a[88:91],a[160:163],v[88:91],a[88:91]
EX v53,v53
MM a[92:95],a[168:171],v[88:91],a[92:95]
EX v54,v54
MM a[92:95],a[172:175],v[92:95],a[92:95]
buffer_load_dwordx4 v235, s[12:15], s64 offen offset:3072 lds
MM a[84:87],a[172:175],v[84:87],a[84:87]
EX v55,v55
MM a[84:87],a[168:171],v[80:83],a[84:87]
EX v56,v56
MM a[76:79],a[168:171],v[72:75],a[76:79]
EX v57,v57
MM a[76:79],a[172:175],v[76:79],a[76:79]
EX v58,v58
MM a[68:71],a[172:175],v[68:71],a[68:71]
EX v59,v59
MM a[68:71],a[168:171],v[64:67],a[68:71]
EX v60,v60
MM a[96:99],a[176:179],v[64:67],a[96:99]
EX v61,v61
MM a[96:99],a[180:183],v[68:71],a[96:99]
EX v62,v62
MM a[104:107],a[180:183],v[76:79],a[104:107]
EX v63,v63
MM a[104:107],a[176:179],v[72:75],a[104:107]
CV v22,v52,v53
CV v23,v54,v55
MM a[112:115],a[176:179],v[80:83],a[112:115]
CV v28,v56,v57
CV v29,v58,v59
MM a[112:115],a[180:183],v[84:87],a[112:115]
CV v30,v60,v61
CV v31,v62,v63
MM a[120:123],a[180:183],v[92:95],a[120:123]
MM a[120:123],a[176:179],v[88:91],a[120:123]
MM a[124:127],a[184:187],v[88:91],a[124:127]
v_add_f32 v244, v144, v145
v_add_f32 v245, v148, v149
MM a[124:127],a[188:191],v[92:95],a[124:127]
v_add_f32 v246, v152, v153
v_add_f32 v247, v156, v157
MM a[116:119],a[188:191],v[84:87],a[116:119]
MM a[116:119],a[184:187],v[80:83],a[116:119]
MM a[108:111],a[184:187],v[72:75],a[108:111]
MM a[108:111],a[188:191],v[76:79],a[108:111]
MM a[100:103],a[188:191],v[68:71],a[100:103]
MM a[100:103],a[184:187],v[64:67],a[100:103]
v_smfmac_f32_16x16x64_bf16 v[144:147], v[236:239], v[0:7], v234
v_smfmac_f32_16x16x64_bf16 v[148:151], v[236:239], v[8:15], v234
v_smfmac_f32_16x16x64_bf16 v[152:155], v[236:239], v[16:23], v234
v_smfmac_f32_16x16x64_bf16 v[156:159], v[236:239], v[24:31], v234
s_waitcnt lgkmcnt(0)
MM v[64:67],a[192:195],v[160:163],v[128:131]
s_add_u32 s64, s64, 16384
TR a[128:129],v232 offset:32768
s_add_u32 s81, s64, 896
MM v[64:67],a[196:199],v[176:179],v[64:67]
TR a[130:131],v232 offset:36864
v_max3_f32 v249, v144, v145, v148
MM v[64:67],a[200:203],v[192:195],v[64:67]
TR a[132:133],v232 offset:40960
v_max3_f32 v249, v249, v149, v152
MM v[64:67],a[204:207],v[208:211],v[64:67]
TR a[134:135],v232 offset:45056
v_max3_f32 v249, v249, v153, v156
MM v[68:71],a[220:223],v[208:211],v[128:131]
TR a[136:137],v233 offset:32768
v_max_f32 v249, v249, v157
MM v[68:71],a[216:219],v[192:195],v[68:71]
TR a[138:139],v233 offset:36864
v_cmp_lt_f32 vcc, s71, v249
MM v[68:71],a[212:215],v[176:179],v[68:71]
TR a[140:141],v233 offset:40960
TR a[142:143],v233 offset:45056
MM v[68:71],a[208:211],v[160:163],v[68:71]
s_cbranch_vccnz LSITE30_%=
LBACK30_%=:
MM v[76:79],a[208:211],v[164:167],v[132:135]
s_and_b32 s90, s17, 63
s_cmp_lt_u32 s17, 80
s_cselect_b32 s17, s17, s90
s_cselect_b64 s[44:45], -1, 0
MM v[76:79],a[212:215],v[180:183],v[76:79]
v_lshrrev_b32 v253, 4, v248
v_min_u32 v253, 35, v253
MM v[76:79],a[216:219],v[196:199],v[76:79]
v_lshrrev_b32 v252, 2, v253
v_mul_u32_u24 v252, 144, v252
MM v[76:79],a[220:223],v[212:215],v[76:79]
TR a[144:145],v232 offset:33280
v_lshl_add_u32 v253, v253, 2, v252
MM v[72:75],a[204:207],v[212:215],v[132:135]
s_lshr_b32 s90, s17, 3
EX v64,v64
MM v[72:75],a[200:203],v[196:199],v[72:75]
s_lshl_b32 s76, s90, 4
EX v65,v65
MM v[72:75],a[196:199],v[180:183],v[72:75]
s_add_u32 s76, s76, 73728
buffer_load_dword v252, v253, s[20:23], s76 offen offset:0 sc1
MM v[72:75],a[192:195],v[164:167],v[72:75]
EX v66,v66
MM v[80:83],a[192:195],v[168:171],v[136:139]
EX v67,v67
MM v[80:83],a[196:199],v[184:187],v[80:83]
EX v68,v68
MM v[80:83],a[200:203],v[200:203],v[80:83]
TR a[146:147],v232 offset:37376
CV v64,v64,v65
MM v[80:83],a[204:207],v[216:219],v[80:83]
EX v69,v69
MM v[84:87],a[220:223],v[216:219],v[136:139]
TR a[148:149],v232 offset:41472
CV v65,v66,v67
MM v[84:87],a[216:219],v[200:203],v[84:87]
EX v70,v70
MM v[84:87],a[212:215],v[184:187],v[84:87]
TR a[150:151],v232 offset:45568
CV v66,v68,v69
MM v[84:87],a[208:211],v[168:171],v[84:87]
EX v71,v71
MM v[92:95],a[208:211],v[172:175],v[140:143]
TR a[152:153],v233 offset:33280
TR a[154:155],v233 offset:37376
MM v[92:95],a[212:215],v[188:191],v[92:95]
EX v72,v72
MM v[92:95],a[216:219],v[204:207],v[92:95]
EX v73,v73
MM v[92:95],a[220:223],v[220:223],v[92:95]
TR a[156:157],v233 offset:41472
CV v67,v70,v71
MM v[88:91],a[204:207],v[220:223],v[140:143]
EX v74,v74
MM v[88:91],a[200:203],v[204:207],v[88:91]
TR a[158:159],v233 offset:45568
CV v72,v72,v73
MM v[88:91],a[196:199],v[188:191],v[88:91]
EX v75,v75
MM v[88:91],a[192:195],v[172:175],v[88:91]
TR a[160:161],v232 offset:33792
TR a[162:163],v232 offset:37888
MM v[96:99],a[224:227],v[160:163],v[128:131]
EX v76,v76
MM v[96:99],a[228:231],v[176:179],v[96:99]
TR a[164:165],v232 offset:41984
CV v73,v74,v75
MM v[96:99],a[232:235],v[192:195],v[96:99]
EX v77,v77
MM v[96:99],a[236:239],v[208:211],v[96:99]
EX v78,v78
MM v[100:103],a[252:255],v[208:211],v[128:131]
TR a[166:167],v232 offset:46080
CV v74,v76,v77
MM v[100:103],a[248:251],v[192:195],v[100:103]
EX v79,v79
MM v[100:103],a[244:247],v[176:179],v[100:103]
TR a[168:169],v233 offset:33792
CV v75,v78,v79
MM v[100:103],a[240:243],v[160:163],v[100:103]
EX v80,v80
MM v[108:111],a[240:243],v[164:167],v[132:135]
TR a[170:171],v233 offset:37888
TR a[172:173],v233 offset:41984
MM v[108:111],a[244:247],v[180:183],v[108:111]
EX v81,v81
MM v[108:111],a[248:251],v[196:199],v[108:111]
TR a[174:175],v233 offset:46080
CV v80,v80,v81
MM v[108:111],a[252:255],v[212:215],v[108:111]
EX v82,v82
MM v[104:107],a[236:239],v[212:215],v[132:135]
EX v83,v83
MM v[104:107],a[232:235],v[196:199],v[104:107]
TR a[176:177],v232 offset:34304
TR a[178:179],v232 offset:38400
MM v[104:107],a[228:231],v[180:183],v[104:107]
EX v84,v84
MM v[104:107],a[224:227],v[164:167],v[104:107]
TR a[180:181],v232 offset:42496
CV v81,v82,v83
MM v[112:115],a[224:227],v[168:171],v[136:139]
EX v85,v85
MM v[112:115],a[228:231],v[184:187],v[112:115]
TR a[182:183],v232 offset:46592
CV v82,v84,v85
MM v[112:115],a[232:235],v[200:203],v[112:115]
EX v86,v86
MM v[112:115],a[236:239],v[216:219],v[112:115]
TR a[184:185],v233 offset:34304
TR a[186:187],v233 offset:38400
MM v[116:119],a[252:255],v[216:219],v[136:139]
EX v87,v87
MM v[116:119],a[248:251],v[200:203],v[116:119]
TR a[188:189],v233 offset:42496
CV v83,v86,v87
MM v[116:119],a[244:247],v[184:187],v[116:119]
EX v88,v88
MM v[116:119],a[240:243],v[168:171],v[116:119]
EX v89,v89
MM v[124:127],a[240:243],v[172:175],v[140:143]
TR a[190:191],v233 offset:46592
CV v88,v88,v89
MM v[124:127],a[244:247],v[188:191],v[124:127]
EX v90,v90
MM v[124:127],a[248:251],v[204:207],v[124:127]
EX v91,v91
MM v[124:127],a[252:255],v[220:223],v[124:127]
EX v92,v92
MM v[120:123],a[236:239],v[220:223],v[140:143]
EX v93,v93
MM v[120:123],a[232:235],v[204:207],v[120:123]
EX v94,v94
MM v[120:123],a[228:231],v[188:191],v[120:123]
EX v95,v95
MM v[120:123],a[224:227],v[172:175],v[120:123]
s_waitcnt vmcnt(15) lgkmcnt(0)
s_barrier
MM a[0:3],a[128:131],v[0:3],a[0:3]
s_add_u32 m0, s65, 32768
CV v89,v90,v91
CV v90,v92,v93
MM a[0:3],a[132:135],v[4:7],a[0:3]
RD a[192:195],v224 offset:0
CV v91,v94,v95
MM a[8:11],a[132:135],v[12:15],a[8:11]
buffer_load_dwordx4 v228, s[8:11], s64 offen offset:0 lds
MM a[8:11],a[128:131],v[8:11],a[8:11]
RD a[196:199],v225 offset:0
RD a[200:203],v226 offset:0
MM a[16:19],a[128:131],v[16:19],a[16:19]
EX v96,v96
MM a[16:19],a[132:135],v[20:23],a[16:19]
EX v97,v97
MM a[24:27],a[132:135],v[28:31],a[24:27]
)ASM"
R"ASM(
RD a[204:207],v227 offset:0
CV v68,v96,v97
MM a[24:27],a[128:131],v[24:27],a[24:27]
EX v98,v98
MM a[28:31],a[136:139],v[24:27],a[28:31]
buffer_load_dwordx4 v229, s[8:11], s64 offen offset:1024 lds
MM a[28:31],a[140:143],v[28:31],a[28:31]
EX v99,v99
MM a[20:23],a[140:143],v[20:23],a[20:23]
RD a[208:211],v224 offset:4096
CV v69,v98,v99
MM a[20:23],a[136:139],v[16:19],a[20:23]
EX v100,v100
MM a[12:15],a[136:139],v[8:11],a[12:15]
EX v101,v101
MM a[12:15],a[140:143],v[12:15],a[12:15]
RD a[212:215],v225 offset:4096
CV v70,v100,v101
MM a[4:7],a[140:143],v[4:7],a[4:7]
EX v102,v102
MM a[4:7],a[136:139],v[0:3],a[4:7]
buffer_load_dwordx4 v230, s[8:11], s64 offen offset:2048 lds
MM a[32:35],a[144:147],v[0:3],a[32:35]
EX v103,v103
MM a[32:35],a[148:151],v[4:7],a[32:35]
RD a[216:219],v226 offset:4096
CV v71,v102,v103
MM a[40:43],a[148:151],v[12:15],a[40:43]
EX v104,v104
MM a[40:43],a[144:147],v[8:11],a[40:43]
EX v105,v105
MM a[48:51],a[144:147],v[16:19],a[48:51]
RD a[220:223],v227 offset:4096
CV v76,v104,v105
MM a[48:51],a[148:151],v[20:23],a[48:51]
EX v106,v106
MM a[56:59],a[148:151],v[28:31],a[56:59]
buffer_load_dwordx4 v231, s[8:11], s64 offen offset:3072 lds
MM a[56:59],a[144:147],v[24:27],a[56:59]
s_add_u32 m0, s65, 98304
EX v107,v107
MM a[60:63],a[152:155],v[24:27],a[60:63]
RD a[224:227],v224 offset:8192
CV v77,v106,v107
MM a[60:63],a[156:159],v[28:31],a[60:63]
EX v108,v108
MM a[52:55],a[156:159],v[20:23],a[52:55]
EX v109,v109
MM a[52:55],a[152:155],v[16:19],a[52:55]
RD a[228:231],v225 offset:8192
CV v78,v108,v109
MM a[44:47],a[152:155],v[8:11],a[44:47]
EX v110,v110
MM a[44:47],a[156:159],v[12:15],a[44:47]
buffer_load_dwordx4 v235, s[12:15], s81 offen offset:0 lds
MM a[36:39],a[156:159],v[4:7],a[36:39]
EX v111,v111
MM a[36:39],a[152:155],v[0:3],a[36:39]
RD a[232:235],v226 offset:8192
CV v79,v110,v111
MM a[64:67],a[160:163],v[0:3],a[64:67]
EX v112,v112
MM a[64:67],a[164:167],v[4:7],a[64:67]
EX v113,v113
MM a[72:75],a[164:167],v[12:15],a[72:75]
RD a[236:239],v227 offset:8192
CV v84,v112,v113
MM a[72:75],a[160:163],v[8:11],a[72:75]
buffer_load_dwordx4 v235, s[12:15], s64 offen offset:1024 lds
MM a[80:83],a[160:163],v[16:19],a[80:83]
EX v114,v114
MM a[80:83],a[164:167],v[20:23],a[80:83]
EX v115,v115
MM a[88:91],a[164:167],v[28:31],a[88:91]
RD a[240:243],v224 offset:12288
CV v85,v114,v115
MM a[88:91],a[160:163],v[24:27],a[88:91]
EX v116,v116
MM a[92:95],a[168:171],v[24:27],a[92:95]
EX v117,v117
MM a[92:95],a[172:175],v[28:31],a[92:95]
RD a[244:247],v225 offset:12288
CV v86,v116,v117
MM a[84:87],a[172:175],v[20:23],a[84:87]
buffer_load_dwordx4 v235, s[12:15], s81 offen offset:2048 lds
MM a[84:87],a[168:171],v[16:19],a[84:87]
EX v118,v118
MM a[76:79],a[168:171],v[8:11],a[76:79]
EX v119,v119
MM a[76:79],a[172:175],v[12:15],a[76:79]
RD a[248:251],v226 offset:12288
CV v87,v118,v119
MM a[68:71],a[172:175],v[4:7],a[68:71]
EX v120,v120
MM a[68:71],a[168:171],v[0:3],a[68:71]
EX v121,v121
MM a[96:99],a[176:179],v[0:3],a[96:99]
RD a[252:255],v227 offset:12288
CV v92,v120,v121
MM a[96:99],a[180:183],v[4:7],a[96:99]
buffer_load_dwordx4 v235, s[12:15], s64 offen offset:3072 lds
MM a[104:107],a[180:183],v[12:15],a[104:107]
EX v122,v122
MM a[104:107],a[176:179],v[8:11],a[104:107]
EX v123,v123
MM a[112:115],a[176:179],v[16:19],a[112:115]
EX v124,v124
MM a[112:115],a[180:183],v[20:23],a[112:115]
EX v125,v125
MM a[120:123],a[180:183],v[28:31],a[120:123]
EX v126,v126
MM a[120:123],a[176:179],v[24:27],a[120:123]
EX v127,v127
MM a[124:127],a[184:187],v[24:27],a[124:127]
v_add_f32 v244, v144, v145
v_add_f32 v245, v148, v149
MM a[124:127],a[188:191],v[28:31],a[124:127]
v_add_f32 v246, v152, v153
v_add_f32 v247, v156, v157
MM a[116:119],a[188:191],v[20:23],a[116:119]
CV v93,v122,v123
CV v94,v124,v125
MM a[116:119],a[184:187],v[16:19],a[116:119]
CV v95,v126,v127
MM a[108:111],a[184:187],v[8:11],a[108:111]
MM a[108:111],a[188:191],v[12:15],a[108:111]
MM a[100:103],a[188:191],v[4:7],a[100:103]
MM a[100:103],a[184:187],v[0:3],a[100:103]
v_smfmac_f32_16x16x64_bf16 v[144:147], v[236:239], v[64:71], v234
v_smfmac_f32_16x16x64_bf16 v[148:151], v[236:239], v[72:79], v234
v_smfmac_f32_16x16x64_bf16 v[152:155], v[236:239], v[80:87], v234
v_smfmac_f32_16x16x64_bf16 v[156:159], v[236:239], v[88:95], v234
s_waitcnt lgkmcnt(0)
MM v[0:3],a[192:195],v[160:163],v[128:131]
s_add_u32 s64, s64, 16384
TR a[128:129],v232 offset:49152
s_add_u32 s81, s64, 896
MM v[0:3],a[196:199],v[176:179],v[0:3]
TR a[130:131],v232 offset:53248
v_max3_f32 v249, v144, v145, v148
MM v[0:3],a[200:203],v[192:195],v[0:3]
TR a[132:133],v232 offset:57344
v_max3_f32 v249, v249, v149, v152
MM v[0:3],a[204:207],v[208:211],v[0:3]
TR a[134:135],v232 offset:61440
v_max3_f32 v249, v249, v153, v156
MM v[4:7],a[220:223],v[208:211],v[128:131]
TR a[136:137],v233 offset:49152
v_max_f32 v249, v249, v157
MM v[4:7],a[216:219],v[192:195],v[4:7]
TR a[138:139],v233 offset:53248
v_cmp_lt_f32 vcc, s71, v249
MM v[4:7],a[212:215],v[176:179],v[4:7]
TR a[140:141],v233 offset:57344
TR a[142:143],v233 offset:61440
MM v[4:7],a[208:211],v[160:163],v[4:7]
s_cbranch_vccnz LSITE31_%=
LBACK31_%=:
MM v[12:15],a[208:211],v[164:167],v[132:135]
s_waitcnt vmcnt(8)
v_cmp_ne_u32 vcc, s69, v252
s_cbranch_vccnz LRFY_%=
LRFYb_%=:
MM v[12:15],a[212:215],v[180:183],v[12:15]
v_lshrrev_b32 v109, 2, v248
v_lshrrev_b32 v110, 3, v248
MM v[12:15],a[216:219],v[196:199],v[12:15]
s_and_b32 s76, s17, 7
EX v0,v0
MM v[12:15],a[220:223],v[212:215],v[12:15]
s_mul_i32 s76, s76, 2560
EX v1,v1
MM v[8:11],a[204:207],v[212:215],v[132:135]
s_lshl_b32 s91, s90, 8
EX v2,v2
MM v[8:11],a[200:203],v[196:199],v[8:11]
s_add_u32 s76, s76, s91
TR a[144:145],v232 offset:49664
CV v0,v0,v1
MM v[8:11],a[196:199],v[180:183],v[8:11]
s_add_u32 s76, s76, 21659648
buffer_load_dword v96, v109, s[20:23], s76 offen offset:0 sc1
MM v[8:11],a[192:195],v[164:167],v[8:11]
s_add_u32 s76, s76, 20480
EX v3,v3
MM v[16:19],a[192:195],v[168:171],v[136:139]
buffer_load_dword v97, v109, s[20:23], s76 offen offset:0 sc1
MM v[16:19],a[196:199],v[184:187],v[16:19]
s_add_u32 s76, s76, 20480
EX v4,v4
MM v[16:19],a[200:203],v[200:203],v[16:19]
buffer_load_dword v98, v109, s[20:23], s76 offen offset:0 sc1
MM v[16:19],a[204:207],v[216:219],v[16:19]
s_add_u32 s76, s76, 20480
EX v5,v5
MM v[20:23],a[220:223],v[216:219],v[136:139]
buffer_load_dword v99, v109, s[20:23], s76 offen offset:0 sc1
MM v[20:23],a[216:219],v[200:203],v[20:23]
s_add_u32 s76, s76, 20480
TR a[146:147],v232 offset:53760
TR a[148:149],v232 offset:57856
MM v[20:23],a[212:215],v[184:187],v[20:23]
buffer_load_dword v100, v109, s[20:23], s76 offen offset:0 sc1
MM v[20:23],a[208:211],v[168:171],v[20:23]
s_add_u32 s76, s76, 20480
EX v6,v6
MM v[28:31],a[208:211],v[172:175],v[140:143]
buffer_load_dword v101, v109, s[20:23], s76 offen offset:0 sc1
MM v[28:31],a[212:215],v[188:191],v[28:31]
s_add_u32 s76, s76, 20480
EX v7,v7
MM v[28:31],a[216:219],v[204:207],v[28:31]
buffer_load_dword v102, v109, s[20:23], s76 offen offset:0 sc1
MM v[28:31],a[220:223],v[220:223],v[28:31]
s_add_u32 s76, s76, 20480
s_nop 0
LPG22_%=:
s_cbranch_execz LTR4_%=
TR a[150:151],v232 offset:61952
EX v8,v8
MM v[24:27],a[204:207],v[220:223],v[140:143]
buffer_load_dword v103, v109, s[20:23], s76 offen offset:0 sc1
MM v[24:27],a[200:203],v[204:207],v[24:27]
s_add_u32 s76, s76, 20480
TR a[152:153],v233 offset:49664
EX v9,v9
CV v1,v2,v3
MM v[24:27],a[196:199],v[188:191],v[24:27]
buffer_load_dword v104, v109, s[20:23], s76 offen offset:0 sc1
TR a[154:155],v233 offset:53760
MM v[24:27],a[192:195],v[172:175],v[24:27]
s_and_b32 s76, s19, 65535
EX v10,v10
MM v[32:35],a[224:227],v[160:163],v[128:131]
s_sub_u32 s76, 65535, s76
TR a[156:157],v233 offset:57856
CV v2,v4,v5
EX v11,v11
MM v[32:35],a[228:231],v[176:179],v[32:35]
s_mul_i32 s76, s76, 1280
TR a[158:159],v233 offset:61952
CV v3,v6,v7
MM v[32:35],a[232:235],v[192:195],v[32:35]
s_lshl_b32 s91, s90, 7
EX v12,v12
MM v[32:35],a[236:239],v[208:211],v[32:35]
s_add_u32 s76, s76, s91
TR a[160:161],v232 offset:50176
TR a[162:163],v232 offset:54272
MM v[36:39],a[252:255],v[208:211],v[128:131]
s_add_u32 s76, s76, 131072
buffer_load_ushort v108, v110, s[20:23], s76 offen sc1
EX v13,v13
MM v[36:39],a[248:251],v[192:195],v[36:39]
s_ashr_i32 s91, s19, 31
EX v14,v14
MM v[36:39],a[244:247],v[176:179],v[36:39]
s_not_b32 s91, s91
TR a[164:165],v232 offset:58368
CV v8,v8,v9
MM v[36:39],a[240:243],v[160:163],v[36:39]
s_or_b32 s91, s91, 0x80000000
EX v15,v15
MM v[44:47],a[240:243],v[164:167],v[132:135]
s_xor_b32 s19, s19, s91
TR a[166:167],v232 offset:62464
CV v9,v10,v11
MM v[44:47],a[244:247],v[180:183],v[44:47]
s_and_b32 s19, s19, 0xffff0000
v_mov_b32 v111, 0xbfb8aa3b
v_mul_f32 v111, s19, v111
MM v[44:47],a[248:251],v[196:199],v[44:47]
EX v111,v111
EX v16,v16
MM v[44:47],a[252:255],v[212:215],v[44:47]
TR a[168:169],v233 offset:50176
v_add_f32 v111, 1.0, v111
EX v17,v17
MM v[40:43],a[236:239],v[212:215],v[132:135]
v_rcp_f32 v111, v111
MM v[40:43],a[232:235],v[196:199],v[40:43]
TR a[170:171],v233 offset:54272
CV v111,v111,v111
v_and_b32 v111, 0xffff0000, v111
EX v18,v18
MM v[40:43],a[228:231],v[180:183],v[40:43]
TR a[172:173],v233 offset:58368
CV v10,v12,v13
MM v[40:43],a[224:227],v[164:167],v[40:43]
EX v19,v19
MM v[48:51],a[224:227],v[168:171],v[136:139]
TR a[174:175],v233 offset:62464
CV v11,v14,v15
MM v[48:51],a[228:231],v[184:187],v[48:51]
EX v20,v20
MM v[48:51],a[232:235],v[200:203],v[48:51]
TR a[176:177],v232 offset:50688
TR a[178:179],v232 offset:54784
MM v[48:51],a[236:239],v[216:219],v[48:51]
EX v21,v21
MM v[52:55],a[252:255],v[216:219],v[136:139]
EX v22,v22
MM v[52:55],a[248:251],v[200:203],v[52:55]
TR a[180:181],v232 offset:58880
CV v16,v16,v17
MM v[52:55],a[244:247],v[184:187],v[52:55]
EX v23,v23
MM v[52:55],a[240:243],v[168:171],v[52:55]
TR a[182:183],v232 offset:62976
CV v17,v18,v19
MM v[60:63],a[240:243],v[172:175],v[140:143]
EX v24,v24
MM v[60:63],a[244:247],v[188:191],v[60:63]
TR a[184:185],v233 offset:50688
TR a[186:187],v233 offset:54784
MM v[60:63],a[248:251],v[204:207],v[60:63]
TR a[188:189],v233 offset:58880
TR a[190:191],v233 offset:62976
EX v25,v25
MM v[60:63],a[252:255],v[220:223],v[60:63]
EX v26,v26
MM v[56:59],a[236:239],v[220:223],v[140:143]
EX v27,v27
MM v[56:59],a[232:235],v[204:207],v[56:59]
EX v28,v28
MM v[56:59],a[228:231],v[188:191],v[56:59]
EX v29,v29
CV v18,v20,v21
MM v[56:59],a[224:227],v[172:175],v[56:59]
s_waitcnt lgkmcnt(0)
s_barrier
MM a[0:3],a[128:131],v[64:67],a[0:3]
s_add_u32 m0, s65, 49152
CV v19,v22,v23
CV v24,v24,v25
MM a[0:3],a[132:135],v[68:71],a[0:3]
CV v25,v26,v27
CV v26,v28,v29
MM a[8:11],a[132:135],v[76:79],a[8:11]
EX v30,v30
MM a[8:11],a[128:131],v[72:75],a[8:11]
EX v31,v31
MM a[16:19],a[128:131],v[80:83],a[16:19]
RD a[192:195],v224 offset:16384
CV v27,v30,v31
MM a[16:19],a[132:135],v[84:87],a[16:19]
EX v32,v32
MM a[24:27],a[132:135],v[92:95],a[24:27]
buffer_load_dwordx4 v228, s[8:11], s64 offen offset:0 lds
MM a[24:27],a[128:131],v[88:91],a[24:27]
EX v33,v33
MM a[28:31],a[136:139],v[88:91],a[28:31]
RD a[196:199],v225 offset:16384
CV v4,v32,v33
MM a[28:31],a[140:143],v[92:95],a[28:31]
EX v34,v34
MM a[20:23],a[140:143],v[84:87],a[20:23]
EX v35,v35
MM a[20:23],a[136:139],v[80:83],a[20:23]
RD a[200:203],v226 offset:16384
CV v5,v34,v35
MM a[12:15],a[136:139],v[72:75],a[12:15]
EX v36,v36
MM a[12:15],a[140:143],v[76:79],a[12:15]
buffer_load_dwordx4 v229, s[8:11], s64 offen offset:1024 lds
MM a[4:7],a[140:143],v[68:71],a[4:7]
EX v37,v37
MM a[4:7],a[136:139],v[64:67],a[4:7]
RD a[204:207],v227 offset:16384
CV v6,v36,v37
MM a[32:35],a[144:147],v[64:67],a[32:35]
EX v38,v38
MM a[32:35],a[148:151],v[68:71],a[32:35]
EX v39,v39
MM a[40:43],a[148:151],v[76:79],a[40:43]
RD a[208:211],v224 offset:20480
CV v7,v38,v39
MM a[40:43],a[144:147],v[72:75],a[40:43]
EX v40,v40
MM a[48:51],a[144:147],v[80:83],a[48:51]
buffer_load_dwordx4 v230, s[8:11], s64 offen offset:2048 lds
MM a[48:51],a[148:151],v[84:87],a[48:51]
EX v41,v41
MM a[56:59],a[148:151],v[92:95],a[56:59]
RD a[212:215],v225 offset:20480
CV v12,v40,v41
MM a[56:59],a[144:147],v[88:91],a[56:59]
EX v42,v42
MM a[60:63],a[152:155],v[88:91],a[60:63]
)ASM"
R"ASM(
EX v43,v43
MM a[60:63],a[156:159],v[92:95],a[60:63]
RD a[216:219],v226 offset:20480
CV v13,v42,v43
MM a[52:55],a[156:159],v[84:87],a[52:55]
EX v44,v44
MM a[52:55],a[152:155],v[80:83],a[52:55]
buffer_load_dwordx4 v231, s[8:11], s64 offen offset:3072 lds
MM a[44:47],a[152:155],v[72:75],a[44:47]
s_add_u32 m0, s65, 114688
EX v45,v45
MM a[44:47],a[156:159],v[76:79],a[44:47]
RD a[220:223],v227 offset:20480
CV v14,v44,v45
MM a[36:39],a[156:159],v[68:71],a[36:39]
EX v46,v46
MM a[36:39],a[152:155],v[64:67],a[36:39]
RD a[224:227],v224 offset:24576
RD a[228:231],v225 offset:24576
MM a[64:67],a[160:163],v[64:67],a[64:67]
EX v47,v47
MM a[64:67],a[164:167],v[68:71],a[64:67]
RD a[232:235],v226 offset:24576
CV v15,v46,v47
MM a[72:75],a[164:167],v[76:79],a[72:75]
EX v48,v48
MM a[72:75],a[160:163],v[72:75],a[72:75]
buffer_load_dwordx4 v235, s[12:15], s81 offen offset:0 lds
MM a[80:83],a[160:163],v[80:83],a[80:83]
EX v49,v49
MM a[80:83],a[164:167],v[84:87],a[80:83]
RD a[236:239],v227 offset:24576
CV v20,v48,v49
MM a[88:91],a[164:167],v[92:95],a[88:91]
EX v50,v50
MM a[88:91],a[160:163],v[88:91],a[88:91]
EX v51,v51
MM a[92:95],a[168:171],v[88:91],a[92:95]
RD a[240:243],v224 offset:28672
CV v21,v50,v51
MM a[92:95],a[172:175],v[92:95],a[92:95]
EX v52,v52
MM a[84:87],a[172:175],v[84:87],a[84:87]
buffer_load_dwordx4 v235, s[12:15], s64 offen offset:1024 lds
MM a[84:87],a[168:171],v[80:83],a[84:87]
EX v53,v53
MM a[76:79],a[168:171],v[72:75],a[76:79]
RD a[244:247],v225 offset:28672
CV v22,v52,v53
MM a[76:79],a[172:175],v[76:79],a[76:79]
EX v54,v54
MM a[68:71],a[172:175],v[68:71],a[68:71]
s_waitcnt vmcnt(6)
v_add_f32 v96, v96, v100
v_add_f32 v97, v97, v101
MM a[68:71],a[168:171],v[64:67],a[68:71]
v_add_f32 v98, v98, v102
v_add_f32 v99, v99, v103
MM a[96:99],a[176:179],v[64:67],a[96:99]
v_add_f32 v96, v96, v98
v_add_f32 v97, v97, v99
MM a[96:99],a[180:183],v[68:71],a[96:99]
v_add_f32 v96, v96, v104
v_lshlrev_b32 v108, 16, v108
MM a[104:107],a[180:183],v[76:79],a[104:107]
v_add_f32 v96, v96, v97
CV v96,v96,v96
EX v55,v55
MM a[104:107],a[176:179],v[72:75],a[104:107]
v_and_b32 v96, 0xffff0000, v96
v_mul_f32 v96, v96, v111
MM a[112:115],a[176:179],v[80:83],a[112:115]
CV v96,v96,v96
v_and_b32 v96, 0xffff0000, v96
EX v56,v56
MM a[112:115],a[180:183],v[84:87],a[112:115]
v_add_f32 v96, v96, v108
CV v96,v96,v96
MM a[120:123],a[180:183],v[92:95],a[120:123]
s_mov_b64 exec, s[44:45]
buffer_store_short v96, v110, s[20:23], s76 offen offset:0 sc1
s_mov_b64 exec, -1
s_nop 0
EX v57,v57
MM a[120:123],a[176:179],v[88:91],a[120:123]
EX v58,v58
MM a[124:127],a[184:187],v[88:91],a[124:127]
v_add_f32 v244, v144, v145
v_add_f32 v245, v148, v149
MM a[124:127],a[188:191],v[92:95],a[124:127]
v_add_f32 v246, v152, v153
v_add_f32 v247, v156, v157
MM a[116:119],a[188:191],v[84:87],a[116:119]
EX v59,v59
MM a[116:119],a[184:187],v[80:83],a[116:119]
EX v60,v60
MM a[108:111],a[184:187],v[72:75],a[108:111]
CV v23,v54,v55
CV v28,v56,v57
MM a[108:111],a[188:191],v[76:79],a[108:111]
EX v61,v61
MM a[100:103],a[188:191],v[68:71],a[100:103]
buffer_load_dwordx4 v235, s[12:15], s81 offen offset:2048 lds
MM a[100:103],a[184:187],v[64:67],a[100:103]
EX v62,v62
v_smfmac_f32_16x16x64_bf16 v[144:147], v[236:239], v[0:7], v234
RD a[248:251],v226 offset:28672
RD a[252:255],v227 offset:28672
EX v63,v63
v_smfmac_f32_16x16x64_bf16 v[148:151], v[236:239], v[8:15], v234
CV v29,v58,v59
v_smfmac_f32_16x16x64_bf16 v[152:155], v[236:239], v[16:23], v234
CV v30,v60,v61
CV v31,v62,v63
s_nop 1
v_smfmac_f32_16x16x64_bf16 v[156:159], v[236:239], v[24:31], v234
s_waitcnt lgkmcnt(0)
buffer_load_dwordx4 v235, s[12:15], s64 offen offset:3072 lds
MM v[64:67],a[192:195],v[160:163],v[128:131]
s_sub_u32 s64, s64, 507904
TR a[128:129],v232 offset:0
s_add_u32 s81, s64, 896
MM v[64:67],a[196:199],v[176:179],v[64:67]
TR a[130:131],v232 offset:4096
v_max3_f32 v249, v144, v145, v148
MM v[64:67],a[200:203],v[192:195],v[64:67]
TR a[132:133],v232 offset:8192
v_max3_f32 v249, v249, v149, v152
MM v[64:67],a[204:207],v[208:211],v[64:67]
TR a[134:135],v232 offset:12288
v_max3_f32 v249, v249, v153, v156
MM v[68:71],a[220:223],v[208:211],v[128:131]
TR a[136:137],v233 offset:0
v_max_f32 v249, v249, v157
MM v[68:71],a[216:219],v[192:195],v[68:71]
TR a[138:139],v233 offset:4096
v_cmp_lt_f32 vcc, s71, v249
MM v[68:71],a[212:215],v[176:179],v[68:71]
TR a[140:141],v233 offset:8192
TR a[142:143],v233 offset:12288
MM v[68:71],a[208:211],v[160:163],v[68:71]
s_cbranch_vccnz LSITE32_%=
LBACK32_%=:
MM v[76:79],a[208:211],v[164:167],v[132:135]
EX v64,v64
MM v[76:79],a[212:215],v[180:183],v[76:79]
EX v65,v65
MM v[76:79],a[216:219],v[196:199],v[76:79]
TR a[144:145],v232 offset:512
CV v64,v64,v65
MM v[76:79],a[220:223],v[212:215],v[76:79]
EX v66,v66
MM v[72:75],a[204:207],v[212:215],v[132:135]
EX v67,v67
MM v[72:75],a[200:203],v[196:199],v[72:75]
TR a[146:147],v232 offset:4608
CV v65,v66,v67
MM v[72:75],a[196:199],v[180:183],v[72:75]
EX v68,v68
MM v[72:75],a[192:195],v[164:167],v[72:75]
TR a[148:149],v232 offset:8704
TR a[150:151],v232 offset:12800
MM v[80:83],a[192:195],v[168:171],v[136:139]
EX v69,v69
MM v[80:83],a[196:199],v[184:187],v[80:83]
TR a[152:153],v233 offset:512
CV v66,v68,v69
MM v[80:83],a[200:203],v[200:203],v[80:83]
EX v70,v70
MM v[80:83],a[204:207],v[216:219],v[80:83]
TR a[154:155],v233 offset:4608
TR a[156:157],v233 offset:8704
MM v[84:87],a[220:223],v[216:219],v[136:139]
EX v71,v71
MM v[84:87],a[216:219],v[200:203],v[84:87]
TR a[158:159],v233 offset:12800
CV v67,v70,v71
MM v[84:87],a[212:215],v[184:187],v[84:87]
EX v72,v72
MM v[84:87],a[208:211],v[168:171],v[84:87]
EX v73,v73
MM v[92:95],a[208:211],v[172:175],v[140:143]
TR a[160:161],v232 offset:1024
CV v72,v72,v73
MM v[92:95],a[212:215],v[188:191],v[92:95]
EX v74,v74
MM v[92:95],a[216:219],v[204:207],v[92:95]
TR a[162:163],v232 offset:5120
TR a[164:165],v232 offset:9216
MM v[92:95],a[220:223],v[220:223],v[92:95]
EX v75,v75
MM v[88:91],a[204:207],v[220:223],v[140:143]
TR a[166:167],v232 offset:13312
CV v73,v74,v75
MM v[88:91],a[200:203],v[204:207],v[88:91]
EX v76,v76
MM v[88:91],a[196:199],v[188:191],v[88:91]
EX v77,v77
MM v[88:91],a[192:195],v[172:175],v[88:91]
TR a[168:169],v233 offset:1024
CV v74,v76,v77
MM v[96:99],a[224:227],v[160:163],v[128:131]
EX v78,v78
MM v[96:99],a[228:231],v[176:179],v[96:99]
TR a[170:171],v233 offset:5120
TR a[172:173],v233 offset:9216
MM v[96:99],a[232:235],v[192:195],v[96:99]
EX v79,v79
MM v[96:99],a[236:239],v[208:211],v[96:99]
TR a[174:175],v233 offset:13312
CV v75,v78,v79
MM v[100:103],a[252:255],v[208:211],v[128:131]
EX v80,v80
MM v[100:103],a[248:251],v[192:195],v[100:103]
EX v81,v81
MM v[100:103],a[244:247],v[176:179],v[100:103]
TR a[176:177],v232 offset:1536
CV v80,v80,v81
MM v[100:103],a[240:243],v[160:163],v[100:103]
EX v82,v82
MM v[108:111],a[240:243],v[164:167],v[132:135]
TR a[178:179],v232 offset:5632
TR a[180:181],v232 offset:9728
MM v[108:111],a[244:247],v[180:183],v[108:111]
EX v83,v83
MM v[108:111],a[248:251],v[196:199],v[108:111]
TR a[182:183],v232 offset:13824
CV v81,v82,v83
MM v[108:111],a[252:255],v[212:215],v[108:111]
EX v84,v84
MM v[104:107],a[236:239],v[212:215],v[132:135]
TR a[184:185],v233 offset:1536
TR a[186:187],v233 offset:5632
MM v[104:107],a[232:235],v[196:199],v[104:107]
EX v85,v85
MM v[104:107],a[228:231],v[180:183],v[104:107]
TR a[188:189],v233 offset:9728
CV v82,v84,v85
MM v[104:107],a[224:227],v[164:167],v[104:107]
EX v86,v86
MM v[112:115],a[224:227],v[168:171],v[136:139]
EX v87,v87
MM v[112:115],a[228:231],v[184:187],v[112:115]
TR a[190:191],v233 offset:13824
CV v83,v86,v87
MM v[112:115],a[232:235],v[200:203],v[112:115]
EX v88,v88
MM v[112:115],a[236:239],v[216:219],v[112:115]
EX v89,v89
MM v[116:119],a[252:255],v[216:219],v[136:139]
EX v90,v90
MM v[116:119],a[248:251],v[200:203],v[116:119]
EX v91,v91
MM v[116:119],a[244:247],v[184:187],v[116:119]
EX v92,v92
MM v[116:119],a[240:243],v[168:171],v[116:119]
EX v93,v93
MM v[124:127],a[240:243],v[172:175],v[140:143]
EX v94,v94
MM v[124:127],a[244:247],v[188:191],v[124:127]
EX v95,v95
MM v[124:127],a[248:251],v[204:207],v[124:127]
EX v96,v96
MM v[124:127],a[252:255],v[220:223],v[124:127]
EX v97,v97
MM v[120:123],a[236:239],v[220:223],v[140:143]
EX v98,v98
MM v[120:123],a[232:235],v[204:207],v[120:123]
EX v99,v99
MM v[120:123],a[228:231],v[188:191],v[120:123]
EX v100,v100
MM v[120:123],a[224:227],v[172:175],v[120:123]
s_waitcnt lgkmcnt(0)
s_barrier
MM a[0:3],a[128:131],v[0:3],a[0:3]
s_add_u32 m0, s65, 0
CV v88,v88,v89
CV v89,v90,v91
MM a[0:3],a[132:135],v[4:7],a[0:3]
RD a[192:195],v224 offset:32768
CV v90,v92,v93
MM a[8:11],a[132:135],v[12:15],a[8:11]
RD a[196:199],v225 offset:32768
CV v91,v94,v95
MM a[8:11],a[128:131],v[8:11],a[8:11]
buffer_load_dwordx4 v228, s[8:11], s64 offen offset:0 lds
MM a[16:19],a[128:131],v[16:19],a[16:19]
RD a[200:203],v226 offset:32768
CV v68,v96,v97
MM a[16:19],a[132:135],v[20:23],a[16:19]
RD a[204:207],v227 offset:32768
CV v69,v98,v99
MM a[24:27],a[132:135],v[28:31],a[24:27]
buffer_load_dwordx4 v229, s[8:11], s64 offen offset:1024 lds
MM a[24:27],a[128:131],v[24:27],a[24:27]
RD a[208:211],v224 offset:36864
RD a[212:215],v225 offset:36864
MM a[28:31],a[136:139],v[24:27],a[28:31]
buffer_load_dwordx4 v230, s[8:11], s64 offen offset:2048 lds
MM a[28:31],a[140:143],v[28:31],a[28:31]
RD a[216:219],v226 offset:36864
RD a[220:223],v227 offset:36864
MM a[20:23],a[140:143],v[20:23],a[20:23]
EX v101,v101
MM a[20:23],a[136:139],v[16:19],a[20:23]
RD a[224:227],v224 offset:40960
CV v70,v100,v101
MM a[12:15],a[136:139],v[8:11],a[12:15]
EX v102,v102
MM a[12:15],a[140:143],v[12:15],a[12:15]
buffer_load_dwordx4 v231, s[8:11], s64 offen offset:3072 lds
MM a[4:7],a[140:143],v[4:7],a[4:7]
s_add_u32 m0, s65, 65536
EX v103,v103
MM a[4:7],a[136:139],v[0:3],a[4:7]
RD a[228:231],v225 offset:40960
CV v71,v102,v103
MM a[32:35],a[144:147],v[0:3],a[32:35]
EX v104,v104
MM a[32:35],a[148:151],v[4:7],a[32:35]
EX v105,v105
MM a[40:43],a[148:151],v[12:15],a[40:43]
RD a[232:235],v226 offset:40960
CV v76,v104,v105
MM a[40:43],a[144:147],v[8:11],a[40:43]
EX v106,v106
MM a[48:51],a[144:147],v[16:19],a[48:51]
buffer_load_dwordx4 v235, s[12:15], s81 offen offset:0 lds
MM a[48:51],a[148:151],v[20:23],a[48:51]
EX v107,v107
MM a[56:59],a[148:151],v[28:31],a[56:59]
RD a[236:239],v227 offset:40960
CV v77,v106,v107
MM a[56:59],a[144:147],v[24:27],a[56:59]
EX v108,v108
MM a[60:63],a[152:155],v[24:27],a[60:63]
EX v109,v109
MM a[60:63],a[156:159],v[28:31],a[60:63]
RD a[240:243],v224 offset:45056
CV v78,v108,v109
MM a[52:55],a[156:159],v[20:23],a[52:55]
EX v110,v110
MM a[52:55],a[152:155],v[16:19],a[52:55]
buffer_load_dwordx4 v235, s[12:15], s64 offen offset:1024 lds
MM a[44:47],a[152:155],v[8:11],a[44:47]
EX v111,v111
MM a[44:47],a[156:159],v[12:15],a[44:47]
RD a[244:247],v225 offset:45056
CV v79,v110,v111
MM a[36:39],a[156:159],v[4:7],a[36:39]
EX v112,v112
MM a[36:39],a[152:155],v[0:3],a[36:39]
EX v113,v113
MM a[64:67],a[160:163],v[0:3],a[64:67]
RD a[248:251],v226 offset:45056
CV v84,v112,v113
MM a[64:67],a[164:167],v[4:7],a[64:67]
EX v114,v114
MM a[72:75],a[164:167],v[12:15],a[72:75]
buffer_load_dwordx4 v235, s[12:15], s81 offen offset:2048 lds
MM a[72:75],a[160:163],v[8:11],a[72:75]
EX v115,v115
MM a[80:83],a[160:163],v[16:19],a[80:83]
RD a[252:255],v227 offset:45056
CV v85,v114,v115
MM a[80:83],a[164:167],v[20:23],a[80:83]
EX v116,v116
MM a[88:91],a[164:167],v[28:31],a[88:91]
EX v117,v117
MM a[88:91],a[160:163],v[24:27],a[88:91]
buffer_load_dwordx4 v235, s[12:15], s64 offen offset:3072 lds
MM a[92:95],a[168:171],v[24:27],a[92:95]
EX v118,v118
MM a[92:95],a[172:175],v[28:31],a[92:95]
EX v119,v119
MM a[84:87],a[172:175],v[20:23],a[84:87]
EX v120,v120
MM a[84:87],a[168:171],v[16:19],a[84:87]
EX v121,v121
MM a[76:79],a[168:171],v[8:11],a[76:79]
EX v122,v122
MM a[76:79],a[172:175],v[12:15],a[76:79]
EX v123,v123
MM a[68:71],a[172:175],v[4:7],a[68:71]
EX v124,v124
MM a[68:71],a[168:171],v[0:3],a[68:71]
EX v125,v125
MM a[96:99],a[176:179],v[0:3],a[96:99]
EX v126,v126
MM a[96:99],a[180:183],v[4:7],a[96:99]
EX v127,v127
MM a[104:107],a[180:183],v[12:15],a[104:107]
)ASM"
R"ASM(
CV v86,v116,v117
CV v87,v118,v119
MM a[104:107],a[176:179],v[8:11],a[104:107]
CV v92,v120,v121
CV v93,v122,v123
MM a[112:115],a[176:179],v[16:19],a[112:115]
CV v94,v124,v125
CV v95,v126,v127
MM a[112:115],a[180:183],v[20:23],a[112:115]
MM a[120:123],a[180:183],v[28:31],a[120:123]
s_lshl_b32 s91, s17, 2
s_add_u32 s91, s91, 94464
s_and_b64 s[44:45], s[44:45], 1
MM a[120:123],a[176:179],v[24:27],a[120:123]
s_waitcnt vmcnt(10)
v_mov_b32 v250, s69
s_mov_b64 exec, s[44:45]
buffer_store_dword v250, off, s[20:23], s91 sc1
s_mov_b64 exec, -1
MM a[124:127],a[184:187],v[24:27],a[124:127]
v_add_f32 v244, v144, v145
v_add_f32 v245, v148, v149
MM a[124:127],a[188:191],v[28:31],a[124:127]
v_add_f32 v246, v152, v153
v_add_f32 v247, v156, v157
MM a[116:119],a[188:191],v[20:23],a[116:119]
MM a[116:119],a[184:187],v[16:19],a[116:119]
MM a[108:111],a[184:187],v[8:11],a[108:111]
MM a[108:111],a[188:191],v[12:15],a[108:111]
MM a[100:103],a[188:191],v[4:7],a[100:103]
MM a[100:103],a[184:187],v[0:3],a[100:103]
v_smfmac_f32_16x16x64_bf16 v[144:147], v[236:239], v[64:71], v234
v_smfmac_f32_16x16x64_bf16 v[148:151], v[236:239], v[72:79], v234
s_nop 0
LPG23_%=:
s_cbranch_execz LPG27_%=
v_smfmac_f32_16x16x64_bf16 v[152:155], v[236:239], v[80:87], v234
v_smfmac_f32_16x16x64_bf16 v[156:159], v[236:239], v[88:95], v234
s_waitcnt lgkmcnt(0)
LLAJ_%=:
MM v[0:3],a[192:195],v[160:163],v[128:131]
s_add_u32 s64, s64, 16384
TR a[128:129],v232 offset:16384
s_add_u32 s81, s64, 896
MM v[0:3],a[196:199],v[176:179],v[0:3]
TR a[130:131],v232 offset:20480
v_max3_f32 v249, v144, v145, v148
MM v[0:3],a[200:203],v[192:195],v[0:3]
TR a[132:133],v232 offset:24576
v_max3_f32 v249, v249, v149, v152
MM v[0:3],a[204:207],v[208:211],v[0:3]
TR a[134:135],v232 offset:28672
v_max3_f32 v249, v249, v153, v156
MM v[4:7],a[220:223],v[208:211],v[128:131]
TR a[136:137],v233 offset:16384
v_max_f32 v249, v249, v157
MM v[4:7],a[216:219],v[192:195],v[4:7]
TR a[138:139],v233 offset:20480
v_cmp_lt_f32 vcc, s71, v249
MM v[4:7],a[212:215],v[176:179],v[4:7]
TR a[140:141],v233 offset:24576
TR a[142:143],v233 offset:28672
MM v[4:7],a[208:211],v[160:163],v[4:7]
s_cbranch_vccnz LSITE33_%=
LBACK33_%=:
MM v[12:15],a[208:211],v[164:167],v[132:135]
v_bfe_u32 v242, v248, 6, 4
v_mul_u32_u24 v242, 1280, v242
MM v[12:15],a[212:215],v[180:183],v[12:15]
v_and_b32 v252, 48, v248
v_bfe_u32 v243, v248, 8, 1
MM v[12:15],a[216:219],v[196:199],v[12:15]
v_lshlrev_b32 v243, 5, v243
v_xor_b32 v252, v252, v243
MM v[12:15],a[220:223],v[212:215],v[12:15]
v_add_u32 v252, v252, v242
v_lshrrev_b32 v242, 3, v248
MM v[8:11],a[204:207],v[212:215],v[132:135]
v_xor_b32 v243, v242, v248
v_and_b32 v243, 96, v243
MM v[8:11],a[200:203],v[196:199],v[8:11]
TR a[144:145],v232 offset:16896
v_and_or_b32 v243, v248, 16, v243
MM v[8:11],a[196:199],v[180:183],v[8:11]
s_and_b32 s66, s49, 8
EX v0,v0
MM v[8:11],a[192:195],v[164:167],v[8:11]
s_lshl_b32 s66, s66, 3
v_lshrrev_b32 v242, 7, v248
v_xor_b32 v243, s66, v243
EX v1,v1
MM v[16:19],a[192:195],v[168:171],v[136:139]
v_mul_u32_u24 v242, 1280, v242
v_add_u32 v240, v242, v243
EX v2,v2
MM v[16:19],a[196:199],v[184:187],v[16:19]
s_mul_i32 s88, %[swave], 40960
EX v3,v3
MM v[16:19],a[200:203],v[200:203],v[16:19]
s_and_b32 s66, s84, 1
EX v4,v4
MM v[16:19],a[204:207],v[216:219],v[16:19]
s_lshl_b32 s86, s66, 6
TR a[146:147],v232 offset:20992
CV v0,v0,v1
MM v[20:23],a[220:223],v[216:219],v[136:139]
s_add_u32 s86, s86, s88
EX v5,v5
MM v[20:23],a[216:219],v[200:203],v[20:23]
s_xor_b32 s66, s66, 1
TR a[148:149],v232 offset:25088
CV v1,v2,v3
MM v[20:23],a[212:215],v[184:187],v[20:23]
s_lshl_b32 s66, s66, 8
EX v6,v6
MM v[20:23],a[208:211],v[168:171],v[20:23]
s_add_u32 s88, s88, s66
EX v7,v7
MM v[28:31],a[208:211],v[172:175],v[140:143]
s_and_b32 s66, s49, 16
TR a[150:151],v232 offset:29184
TR a[152:153],v233 offset:16896
MM v[28:31],a[212:215],v[188:191],v[28:31]
s_lshl_b32 s66, s66, 3
EX v8,v8
MM v[28:31],a[216:219],v[204:207],v[28:31]
s_sub_u32 s87, s86, s66
TR a[154:155],v233 offset:20992
CV v2,v4,v5
MM v[28:31],a[220:223],v[220:223],v[28:31]
s_add_u32 s87, s87, 128
EX v9,v9
MM v[24:27],a[204:207],v[220:223],v[140:143]
s_add_u32 s86, s86, s66
TR a[156:157],v233 offset:25088
CV v3,v6,v7
MM v[24:27],a[200:203],v[204:207],v[24:27]
s_and_b32 s94, s49, 7
EX v10,v10
MM v[24:27],a[196:199],v[188:191],v[24:27]
s_lshl_b32 s89, %[swave], 11
TR a[158:159],v233 offset:29184
CV v8,v8,v9
MM v[24:27],a[192:195],v[172:175],v[24:27]
s_add_u32 s89, s89, %[slds]
EX v11,v11
MM v[32:35],a[224:227],v[160:163],v[128:131]
s_xor_b32 s66, s94, 4
TR a[160:161],v232 offset:17408
TR a[162:163],v232 offset:21504
MM v[32:35],a[228:231],v[176:179],v[32:35]
s_mul_i32 s66, s66, 163840
EX v12,v12
MM v[32:35],a[232:235],v[192:195],v[32:35]
s_add_u32 s66, s66, s88
s_add_u32 m0, s89, 131072
s_nop 0
buffer_load_dwordx4 v252, s[24:27], s66 offen offset:0 lds
MM v[32:35],a[236:239],v[208:211],v[32:35]
s_add_u32 s66, s66, 19456
EX v13,v13
MM v[36:39],a[252:255],v[208:211],v[128:131]
TR a[164:165],v232 offset:25600
CV v9,v10,v11
MM v[36:39],a[248:251],v[192:195],v[36:39]
EX v14,v14
MM v[36:39],a[244:247],v[176:179],v[36:39]
EX v15,v15
MM v[36:39],a[240:243],v[160:163],v[36:39]
TR a[166:167],v232 offset:29696
TR a[168:169],v233 offset:17408
MM v[44:47],a[240:243],v[164:167],v[132:135]
EX v16,v16
MM v[44:47],a[244:247],v[180:183],v[44:47]
TR a[170:171],v233 offset:21504
CV v10,v12,v13
MM v[44:47],a[248:251],v[196:199],v[44:47]
EX v17,v17
MM v[44:47],a[252:255],v[212:215],v[44:47]
s_add_u32 m0, s89, 131072
s_nop 0
buffer_load_dwordx4 v252, s[24:27], s66 offen offset:1024 lds
MM v[40:43],a[236:239],v[212:215],v[132:135]
EX v18,v18
MM v[40:43],a[232:235],v[196:199],v[40:43]
TR a[172:173],v233 offset:25600
CV v11,v14,v15
MM v[40:43],a[228:231],v[180:183],v[40:43]
EX v19,v19
MM v[40:43],a[224:227],v[164:167],v[40:43]
TR a[174:175],v233 offset:29696
TR a[176:177],v232 offset:17920
MM v[48:51],a[224:227],v[168:171],v[136:139]
EX v20,v20
MM v[48:51],a[228:231],v[184:187],v[48:51]
TR a[178:179],v232 offset:22016
CV v16,v16,v17
MM v[48:51],a[232:235],v[200:203],v[48:51]
EX v21,v21
MM v[48:51],a[236:239],v[216:219],v[48:51]
s_lshl_b32 s60, s50, 2
TR a[180:181],v232 offset:26112
CV v17,v18,v19
MM v[52:55],a[252:255],v[216:219],v[136:139]
s_add_u32 s60, s60, %[swave]
EX v22,v22
MM v[52:55],a[248:251],v[200:203],v[52:55]
s_lshl_b32 s60, s60, 1
TR a[182:183],v232 offset:30208
TR a[184:185],v233 offset:17920
MM v[52:55],a[244:247],v[184:187],v[52:55]
s_bfe_u32 s61, s49, 65538
EX v23,v23
MM v[52:55],a[240:243],v[168:171],v[52:55]
s_xor_b32 s61, s61, 1
EX v24,v24
MM v[60:63],a[240:243],v[172:175],v[140:143]
s_add_u32 s60, s60, s61
TR a[186:187],v233 offset:22016
CV v18,v20,v21
MM v[60:63],a[244:247],v[188:191],v[60:63]
s_mul_i32 s18, s60, 40960
EX v25,v25
MM v[60:63],a[248:251],v[204:207],v[60:63]
s_mul_i32 s60, s84, 320
TR a[188:189],v233 offset:26112
TR a[190:191],v233 offset:30208
MM v[60:63],a[252:255],v[220:223],v[60:63]
s_add_u32 s18, s18, s60
EX v26,v26
MM v[56:59],a[236:239],v[220:223],v[140:143]
s_lshr_b32 s62, s49, 3
CV v19,v22,v23
CV v24,v24,v25
MM v[56:59],a[232:235],v[204:207],v[56:59]
s_mul_i32 s63, s62, 10240
EX v27,v27
MM v[56:59],a[228:231],v[188:191],v[56:59]
s_add_u32 s63, s63, s18
EX v28,v28
MM v[56:59],a[224:227],v[172:175],v[56:59]
s_waitcnt vmcnt(14) lgkmcnt(0)
s_barrier
MM a[0:3],a[128:131],v[64:67],a[0:3]
s_and_b32 s60, s84, 1
EX v29,v29
MM a[0:3],a[132:135],v[68:71],a[0:3]
s_add_u32 s61, s60, s62
CV v25,v26,v27
CV v26,v28,v29
MM a[8:11],a[132:135],v[76:79],a[8:11]
s_lshl_b32 s61, s61, 6
EX v30,v30
MM a[8:11],a[128:131],v[72:75],a[8:11]
s_add_u32 s18, s18, s61
EX v31,v31
MM a[16:19],a[128:131],v[80:83],a[16:19]
s_xor_b32 s60, s60, 1
RD a[192:195],v224 offset:49152
CV v27,v30,v31
MM a[16:19],a[132:135],v[84:87],a[16:19]
s_lshl_b32 s60, s60, 8
EX v32,v32
MM a[24:27],a[132:135],v[92:95],a[24:27]
s_add_u32 s63, s63, s60
s_add_u32 m0, s65, 16384
RD a[196:199],v225 offset:49152
MM a[24:27],a[128:131],v[88:91],a[24:27]
s_mul_i32 s60, s62, 33792
EX v33,v33
MM a[28:31],a[136:139],v[88:91],a[28:31]
s_sub_u32 s60, s95, s60
buffer_load_dwordx4 v228, s[8:11], s64 offen offset:0 lds
MM a[28:31],a[140:143],v[92:95],a[28:31]
s_xor_b32 s61, s62, 1
EX v34,v34
MM a[20:23],a[140:143],v[84:87],a[20:23]
s_mul_i32 s61, s61, 33792
EX v35,v35
MM a[20:23],a[136:139],v[80:83],a[20:23]
s_add_u32 s61, s61, s60
RD a[200:203],v226 offset:49152
CV v4,v32,v33
MM a[12:15],a[136:139],v[72:75],a[12:15]
s_add_u32 s36, s61, 2048
EX v36,v36
MM a[12:15],a[140:143],v[76:79],a[12:15]
s_xor_b32 s61, s62, 2
CV v5,v34,v35
MM a[4:7],a[140:143],v[68:71],a[4:7]
s_mul_i32 s61, s61, 33792
EX v37,v37
MM a[4:7],a[136:139],v[64:67],a[4:7]
s_add_u32 s61, s61, s60
buffer_load_dwordx4 v229, s[8:11], s64 offen offset:1024 lds
MM a[32:35],a[144:147],v[64:67],a[32:35]
s_add_u32 s37, s61, 4096
EX v38,v38
MM a[32:35],a[148:151],v[68:71],a[32:35]
s_xor_b32 s61, s62, 3
RD a[204:207],v227 offset:49152
CV v6,v36,v37
MM a[40:43],a[148:151],v[76:79],a[40:43]
s_mul_i32 s61, s61, 33792
EX v39,v39
MM a[40:43],a[144:147],v[72:75],a[40:43]
s_add_u32 s61, s61, s60
RD a[208:211],v224 offset:53248
CV v7,v38,v39
MM a[48:51],a[144:147],v[80:83],a[48:51]
s_add_u32 s38, s61, 6144
EX v40,v40
MM a[48:51],a[148:151],v[84:87],a[48:51]
s_lshl_b32 s61, s62, 9
EX v41,v41
MM a[56:59],a[148:151],v[92:95],a[56:59]
s_add_u32 s39, s60, s61
RD a[212:215],v225 offset:53248
MM a[56:59],a[144:147],v[88:91],a[56:59]
s_mov_b32 s17, 15
buffer_load_dwordx4 v230, s[8:11], s64 offen offset:2048 lds
MM a[60:63],a[152:155],v[88:91],a[60:63]
s_mov_b32 s58, 0
EX v42,v42
MM a[60:63],a[156:159],v[92:95],a[60:63]
s_lshl_b32 s60, s50, 1
EX v43,v43
MM a[52:55],a[156:159],v[84:87],a[52:55]
s_bfe_u32 s61, s49, 65538
RD a[216:219],v226 offset:53248
CV v12,v40,v41
MM a[52:55],a[152:155],v[80:83],a[52:55]
s_add_u32 s60, s60, s61
EX v44,v44
MM a[44:47],a[152:155],v[72:75],a[44:47]
s_lshl_b32 s60, s60, 2
CV v13,v42,v43
MM a[44:47],a[156:159],v[76:79],a[44:47]
s_add_u32 s60, s60, s84
EX v45,v45
MM a[36:39],a[156:159],v[68:71],a[36:39]
s_lshl_b32 s60, s60, 2
buffer_load_dwordx4 v231, s[8:11], s64 offen offset:3072 lds
MM a[36:39],a[152:155],v[64:67],a[36:39]
s_add_u32 s68, s60, %[swave]
EX v46,v46
MM a[64:67],a[160:163],v[64:67],a[64:67]
s_lshl_b32 s68, s68, 6
RD a[220:223],v227 offset:53248
CV v14,v44,v45
MM a[64:67],a[164:167],v[68:71],a[64:67]
s_add_u32 s68, s68, 16384
EX v47,v47
MM a[72:75],a[164:167],v[76:79],a[72:75]
s_lshr_b32 s61, s49, 3
EX v48,v48
MM a[72:75],a[160:163],v[72:75],a[72:75]
s_lshl_b32 s19, s61, 4
RD a[224:227],v224 offset:57344
CV v15,v46,v47
MM a[80:83],a[160:163],v[80:83],a[80:83]
s_add_u32 s19, s19, s68
EX v49,v49
MM a[80:83],a[164:167],v[84:87],a[80:83]
s_lshr_b32 s96, s49, 2
s_add_u32 m0, s65, 81920
CV v20,v48,v49
MM a[88:91],a[164:167],v[92:95],a[88:91]
s_lshl_b32 s97, s50, 3
EX v50,v50
MM a[88:91],a[160:163],v[88:91],a[88:91]
s_add_u32 s96, s96, s97
buffer_load_dwordx4 v235, s[12:15], s81 offen offset:0 lds
MM a[92:95],a[168:171],v[88:91],a[92:95]
s_lshl_b32 s96, s96, 2
EX v51,v51
MM a[92:95],a[172:175],v[92:95],a[92:95]
s_add_u32 s96, s96, %[swave]
RD a[228:231],v225 offset:57344
CV v21,v50,v51
MM a[84:87],a[172:175],v[84:87],a[84:87]
s_lshl_b32 s96, s96, 6
EX v52,v52
MM a[84:87],a[168:171],v[80:83],a[84:87]
s_add_u32 s96, s96, 0
RD a[232:235],v226 offset:57344
MM a[76:79],a[168:171],v[72:75],a[76:79]
s_xor_b32 s44, s96, 256
EX v53,v53
MM a[76:79],a[172:175],v[76:79],a[76:79]
s_xor_b32 s66, s94, 5
RD a[236:239],v227 offset:57344
CV v22,v52,v53
MM a[68:71],a[172:175],v[68:71],a[68:71]
s_mul_i32 s66, s66, 163840
EX v54,v54
MM a[68:71],a[168:171],v[64:67],a[68:71]
s_add_u32 s66, s66, s88
s_add_u32 m0, s89, 139264
s_nop 0
buffer_load_dwordx4 v252, s[24:27], s66 offen offset:0 lds
MM a[96:99],a[176:179],v[64:67],a[96:99]
s_add_u32 s66, s66, 19456
EX v55,v55
MM a[96:99],a[180:183],v[68:71],a[96:99]
s_add_u32 m0, s65, 81920
s_nop 0
buffer_load_dwordx4 v235, s[12:15], s64 offen offset:1024 lds
MM a[104:107],a[180:183],v[76:79],a[104:107]
EX v56,v56
MM a[104:107],a[176:179],v[72:75],a[104:107]
)ASM"
R"ASM(
RD a[240:243],v224 offset:61440
CV v23,v54,v55
MM a[112:115],a[176:179],v[80:83],a[112:115]
EX v57,v57
MM a[112:115],a[180:183],v[84:87],a[112:115]
EX v58,v58
MM a[120:123],a[180:183],v[92:95],a[120:123]
RD a[244:247],v225 offset:61440
CV v28,v56,v57
MM a[120:123],a[176:179],v[88:91],a[120:123]
EX v59,v59
MM a[124:127],a[184:187],v[88:91],a[124:127]
v_add_f32 v244, v144, v145
v_add_f32 v245, v148, v149
MM a[124:127],a[188:191],v[92:95],a[124:127]
v_add_f32 v246, v152, v153
v_add_f32 v247, v156, v157
MM a[116:119],a[188:191],v[84:87],a[116:119]
s_add_u32 m0, s89, 139264
s_nop 0
buffer_load_dwordx4 v252, s[24:27], s66 offen offset:1024 lds
MM a[116:119],a[184:187],v[80:83],a[116:119]
EX v60,v60
MM a[108:111],a[184:187],v[72:75],a[108:111]
EX v61,v61
MM a[108:111],a[188:191],v[76:79],a[108:111]
s_add_u32 m0, s65, 81920
s_nop 0
buffer_load_dwordx4 v235, s[12:15], s81 offen offset:2048 lds
MM a[100:103],a[188:191],v[68:71],a[100:103]
EX v62,v62
MM a[100:103],a[184:187],v[64:67],a[100:103]
RD a[248:251],v226 offset:61440
CV v29,v58,v59
v_smfmac_f32_16x16x64_bf16 v[144:147], v[236:239], v[0:7], v234
RD a[252:255],v227 offset:61440
EX v63,v63
v_smfmac_f32_16x16x64_bf16 v[148:151], v[236:239], v[8:15], v234
CV v30,v60,v61
v_smfmac_f32_16x16x64_bf16 v[152:155], v[236:239], v[16:23], v234
CV v31,v62,v63
s_nop 1
v_smfmac_f32_16x16x64_bf16 v[156:159], v[236:239], v[24:31], v234
s_waitcnt lgkmcnt(0)
buffer_load_dwordx4 v235, s[12:15], s64 offen offset:3072 lds
MM v[64:67],a[192:195],v[160:163],v[128:131]
s_add_u32 s64, s64, 16384
TR a[128:129],v232 offset:32768
s_add_u32 s81, s64, 896
MM v[64:67],a[196:199],v[176:179],v[64:67]
TR a[130:131],v232 offset:36864
v_max3_f32 v249, v144, v145, v148
MM v[64:67],a[200:203],v[192:195],v[64:67]
TR a[132:133],v232 offset:40960
v_max3_f32 v249, v249, v149, v152
MM v[64:67],a[204:207],v[208:211],v[64:67]
TR a[134:135],v232 offset:45056
v_max3_f32 v249, v249, v153, v156
MM v[68:71],a[220:223],v[208:211],v[128:131]
TR a[136:137],v233 offset:32768
v_max_f32 v249, v249, v157
MM v[68:71],a[216:219],v[192:195],v[68:71]
TR a[138:139],v233 offset:36864
v_cmp_lt_f32 vcc, s71, v249
MM v[68:71],a[212:215],v[176:179],v[68:71]
TR a[140:141],v233 offset:40960
TR a[142:143],v233 offset:45056
MM v[68:71],a[208:211],v[160:163],v[68:71]
s_cbranch_vccnz LSITE34_%=
LBACK34_%=:
MM v[76:79],a[208:211],v[164:167],v[132:135]
buffer_load_dword v242, off, s[20:23], s96 sc1
MM v[76:79],a[212:215],v[180:183],v[76:79]
buffer_load_dword v243, off, s[20:23], s19 sc1
MM v[76:79],a[216:219],v[196:199],v[76:79]
s_xor_b32 s66, s94, 6
v_and_b32 v32, 496, v248
v_mul_u32_u24 v32, 80, v32
MM v[76:79],a[220:223],v[212:215],v[76:79]
s_mul_i32 s66, s66, 163840
EX v64,v64
MM v[72:75],a[204:207],v[212:215],v[132:135]
s_add_u32 s66, s66, s88
s_add_u32 m0, s89, 147456
s_nop 0
buffer_load_dwordx4 v252, s[24:27], s66 offen offset:0 lds
MM v[72:75],a[200:203],v[196:199],v[72:75]
s_add_u32 s66, s66, 19456
EX v65,v65
MM v[72:75],a[196:199],v[180:183],v[72:75]
s_sub_u32 s60, s63, s18
v_add_u32 v32, s18, v32
CV v64,v64,v65
MM v[72:75],a[192:195],v[164:167],v[72:75]
s_mov_b32 s61, 131072
s_mov_b32 exec_lo, 0
v_add_u32 v32, s60, v32
s_mov_b64 exec, -1
MM v[80:83],a[192:195],v[168:171],v[136:139]
s_mov_b32 exec_hi, 0xff
buffer_load_dword v36, v32, s[32:35], 0 offen offset:0
s_mov_b64 exec, -1
EX v66,v66
MM v[80:83],a[196:199],v[184:187],v[80:83]
EX v67,v67
MM v[80:83],a[200:203],v[200:203],v[80:83]
EX v68,v68
MM v[80:83],a[204:207],v[216:219],v[80:83]
s_mov_b32 exec_hi, 0xff
buffer_load_dword v37, v32, s[20:23], s61 offen offset:0 sc1
s_mov_b64 exec, -1
MM v[84:87],a[220:223],v[216:219],v[136:139]
TR a[144:145],v232 offset:33280
TR a[146:147],v232 offset:37376
MM v[84:87],a[216:219],v[200:203],v[84:87]
s_add_u32 m0, s89, 147456
s_nop 0
buffer_load_dwordx4 v252, s[24:27], s66 offen offset:1024 lds
EX v69,v69
MM v[84:87],a[212:215],v[184:187],v[84:87]
EX v70,v70
MM v[84:87],a[208:211],v[168:171],v[84:87]
TR a[148:149],v232 offset:41472
CV v65,v66,v67
MM v[92:95],a[208:211],v[172:175],v[140:143]
EX v71,v71
MM v[92:95],a[212:215],v[188:191],v[92:95]
TR a[150:151],v232 offset:45568
TR a[152:153],v233 offset:33280
MM v[92:95],a[216:219],v[204:207],v[92:95]
EX v72,v72
MM v[92:95],a[220:223],v[220:223],v[92:95]
EX v73,v73
MM v[88:91],a[204:207],v[220:223],v[140:143]
TR a[154:155],v233 offset:37376
CV v66,v68,v69
MM v[88:91],a[200:203],v[204:207],v[88:91]
s_xor_b32 s66, s94, 7
EX v74,v74
MM v[88:91],a[196:199],v[188:191],v[88:91]
s_mul_i32 s66, s66, 163840
TR a[156:157],v233 offset:41472
CV v67,v70,v71
MM v[88:91],a[192:195],v[172:175],v[88:91]
s_add_u32 s66, s66, s88
s_add_u32 m0, s89, 155648
s_nop 0
buffer_load_dwordx4 v252, s[24:27], s66 offen offset:0 lds
MM v[96:99],a[224:227],v[160:163],v[128:131]
s_add_u32 s66, s66, 19456
EX v75,v75
MM v[96:99],a[228:231],v[176:179],v[96:99]
TR a[158:159],v233 offset:45568
TR a[160:161],v232 offset:33792
MM v[96:99],a[232:235],v[192:195],v[96:99]
EX v76,v76
MM v[96:99],a[236:239],v[208:211],v[96:99]
EX v77,v77
MM v[100:103],a[252:255],v[208:211],v[128:131]
TR a[162:163],v232 offset:37888
CV v72,v72,v73
MM v[100:103],a[248:251],v[192:195],v[100:103]
EX v78,v78
MM v[100:103],a[244:247],v[176:179],v[100:103]
TR a[164:165],v232 offset:41984
CV v73,v74,v75
MM v[100:103],a[240:243],v[160:163],v[100:103]
EX v79,v79
MM v[108:111],a[240:243],v[164:167],v[132:135]
s_add_u32 m0, s89, 155648
s_nop 0
buffer_load_dwordx4 v252, s[24:27], s66 offen offset:1024 lds
MM v[108:111],a[244:247],v[180:183],v[108:111]
TR a[166:167],v232 offset:46080
TR a[168:169],v233 offset:33792
MM v[108:111],a[248:251],v[196:199],v[108:111]
EX v80,v80
MM v[108:111],a[252:255],v[212:215],v[108:111]
EX v81,v81
MM v[104:107],a[236:239],v[212:215],v[132:135]
TR a[170:171],v233 offset:37888
CV v74,v76,v77
MM v[104:107],a[232:235],v[196:199],v[104:107]
EX v82,v82
MM v[104:107],a[228:231],v[180:183],v[104:107]
TR a[172:173],v233 offset:41984
TR a[174:175],v233 offset:46080
MM v[104:107],a[224:227],v[164:167],v[104:107]
EX v83,v83
MM v[112:115],a[224:227],v[168:171],v[136:139]
TR a[176:177],v232 offset:34304
CV v75,v78,v79
MM v[112:115],a[228:231],v[184:187],v[112:115]
EX v84,v84
MM v[112:115],a[232:235],v[200:203],v[112:115]
EX v85,v85
MM v[112:115],a[236:239],v[216:219],v[112:115]
TR a[178:179],v232 offset:38400
CV v80,v80,v81
MM v[116:119],a[252:255],v[216:219],v[136:139]
EX v86,v86
MM v[116:119],a[248:251],v[200:203],v[116:119]
TR a[180:181],v232 offset:42496
TR a[182:183],v232 offset:46592
MM v[116:119],a[244:247],v[184:187],v[116:119]
EX v87,v87
MM v[116:119],a[240:243],v[168:171],v[116:119]
TR a[184:185],v233 offset:34304
CV v81,v82,v83
MM v[124:127],a[240:243],v[172:175],v[140:143]
EX v88,v88
MM v[124:127],a[244:247],v[188:191],v[124:127]
TR a[186:187],v233 offset:38400
CV v82,v84,v85
MM v[124:127],a[248:251],v[204:207],v[124:127]
TR a[188:189],v233 offset:42496
TR a[190:191],v233 offset:46592
EX v89,v89
MM v[124:127],a[252:255],v[220:223],v[124:127]
EX v90,v90
MM v[120:123],a[236:239],v[220:223],v[140:143]
CV v83,v86,v87
CV v88,v88,v89
MM v[120:123],a[232:235],v[204:207],v[120:123]
EX v91,v91
MM v[120:123],a[228:231],v[188:191],v[120:123]
EX v92,v92
MM v[120:123],a[224:227],v[172:175],v[120:123]
s_waitcnt vmcnt(24) lgkmcnt(0)
s_barrier
MM a[0:3],a[128:131],v[0:3],a[0:3]
s_add_u32 m0, s65, 32768
EX v93,v93
MM a[0:3],a[132:135],v[4:7],a[0:3]
CV v89,v90,v91
CV v90,v92,v93
MM a[8:11],a[132:135],v[12:15],a[8:11]
EX v94,v94
MM a[8:11],a[128:131],v[8:11],a[8:11]
EX v95,v95
MM a[16:19],a[128:131],v[16:19],a[16:19]
RD a[192:195],v224 offset:0
LPG24_%=:
s_cbranch_execz LPG28_%=
CV v91,v94,v95
MM a[16:19],a[132:135],v[20:23],a[16:19]
EX v96,v96
MM a[24:27],a[132:135],v[28:31],a[24:27]
buffer_load_dwordx4 v228, s[8:11], s64 offen offset:0 lds
MM a[24:27],a[128:131],v[24:27],a[24:27]
EX v97,v97
MM a[28:31],a[136:139],v[24:27],a[28:31]
RD a[196:199],v225 offset:0
CV v68,v96,v97
MM a[28:31],a[140:143],v[28:31],a[28:31]
EX v98,v98
MM a[20:23],a[140:143],v[20:23],a[20:23]
EX v99,v99
MM a[20:23],a[136:139],v[16:19],a[20:23]
TR a[128:129],v232 offset:49152
RD a[200:203],v226 offset:0
MM a[12:15],a[136:139],v[8:11],a[12:15]
EX v100,v100
MM a[12:15],a[140:143],v[12:15],a[12:15]
TR a[130:131],v232 offset:53248
CV v69,v98,v99
MM a[4:7],a[140:143],v[4:7],a[4:7]
EX v101,v101
MM a[4:7],a[136:139],v[0:3],a[4:7]
TR a[132:133],v232 offset:57344
RD a[204:207],v227 offset:0
MM a[32:35],a[144:147],v[0:3],a[32:35]
EX v102,v102
MM a[32:35],a[148:151],v[4:7],a[32:35]
buffer_load_dwordx4 v229, s[8:11], s64 offen offset:1024 lds
MM a[40:43],a[148:151],v[12:15],a[40:43]
TR a[134:135],v232 offset:61440
CV v70,v100,v101
MM a[40:43],a[144:147],v[8:11],a[40:43]
EX v103,v103
MM a[48:51],a[144:147],v[16:19],a[48:51]
TR a[136:137],v233 offset:49152
TR a[138:139],v233 offset:53248
MM a[48:51],a[148:151],v[20:23],a[48:51]
EX v104,v104
MM a[56:59],a[148:151],v[28:31],a[56:59]
EX v105,v105
MM a[56:59],a[144:147],v[24:27],a[56:59]
TR a[140:141],v233 offset:57344
CV v71,v102,v103
MM a[60:63],a[152:155],v[24:27],a[60:63]
EX v106,v106
MM a[60:63],a[156:159],v[28:31],a[60:63]
RD a[208:211],v224 offset:4096
TR a[142:143],v233 offset:61440
MM a[52:55],a[156:159],v[20:23],a[52:55]
EX v107,v107
MM a[52:55],a[152:155],v[16:19],a[52:55]
buffer_load_dwordx4 v230, s[8:11], s64 offen offset:2048 lds
MM a[44:47],a[152:155],v[8:11],a[44:47]
TR a[144:145],v232 offset:49664
RD a[212:215],v225 offset:4096
MM a[44:47],a[156:159],v[12:15],a[44:47]
EX v108,v108
MM a[36:39],a[156:159],v[4:7],a[36:39]
TR a[146:147],v232 offset:53760
CV v76,v104,v105
MM a[36:39],a[152:155],v[0:3],a[36:39]
EX v109,v109
MM a[64:67],a[160:163],v[0:3],a[64:67]
TR a[148:149],v232 offset:57856
CV v77,v106,v107
MM a[64:67],a[164:167],v[4:7],a[64:67]
EX v110,v110
MM a[72:75],a[164:167],v[12:15],a[72:75]
EX v111,v111
MM a[72:75],a[160:163],v[8:11],a[72:75]
TR a[150:151],v232 offset:61952
RD a[216:219],v226 offset:4096
MM a[80:83],a[160:163],v[16:19],a[80:83]
TR a[152:153],v233 offset:49664
CV v78,v108,v109
MM a[80:83],a[164:167],v[20:23],a[80:83]
EX v112,v112
MM a[88:91],a[164:167],v[28:31],a[88:91]
EX v113,v113
MM a[88:91],a[160:163],v[24:27],a[88:91]
TR a[154:155],v233 offset:53760
RD a[220:223],v227 offset:4096
MM a[92:95],a[168:171],v[24:27],a[92:95]
buffer_load_dwordx4 v231, s[8:11], s64 offen offset:3072 lds
MM a[92:95],a[172:175],v[28:31],a[92:95]
s_add_u32 m0, s65, 98304
EX v114,v114
MM a[84:87],a[172:175],v[20:23],a[84:87]
TR a[156:157],v233 offset:57856
TR a[158:159],v233 offset:61952
MM a[84:87],a[168:171],v[16:19],a[84:87]
EX v115,v115
MM a[76:79],a[168:171],v[8:11],a[76:79]
EX v116,v116
MM a[76:79],a[172:175],v[12:15],a[76:79]
TR a[160:161],v232 offset:50176
CV v79,v110,v111
MM a[68:71],a[172:175],v[4:7],a[68:71]
EX v117,v117
MM a[68:71],a[168:171],v[0:3],a[68:71]
TR a[162:163],v232 offset:54272
RD a[224:227],v224 offset:8192
MM a[96:99],a[176:179],v[0:3],a[96:99]
EX v118,v118
MM a[96:99],a[180:183],v[4:7],a[96:99]
TR a[164:165],v232 offset:58368
CV v84,v112,v113
MM a[104:107],a[180:183],v[12:15],a[104:107]
buffer_load_dwordx4 v235, s[12:15], s81 offen offset:0 lds
TR a[166:167],v232 offset:62464
EX v119,v119
MM a[104:107],a[176:179],v[8:11],a[104:107]
RD a[228:231],v225 offset:8192
TR a[168:169],v233 offset:50176
MM a[112:115],a[176:179],v[16:19],a[112:115]
EX v120,v120
MM a[112:115],a[180:183],v[20:23],a[112:115]
TR a[170:171],v233 offset:54272
CV v85,v114,v115
MM a[120:123],a[180:183],v[28:31],a[120:123]
s_waitcnt vmcnt(8)
EX v121,v121
MM a[120:123],a[176:179],v[24:27],a[120:123]
s_lshr_b32 s77, %[sb], 3
RD a[232:235],v226 offset:8192
TR a[172:173],v233 offset:58368
MM a[124:127],a[184:187],v[24:27],a[124:127]
s_lshl_b32 s77, s77, 2
v_add_f32 v244, v144, v145
v_add_f32 v245, v148, v149
EX v122,v122
MM a[124:127],a[188:191],v[28:31],a[124:127]
s_add_u32 s77, s77, %[swave]
v_add_f32 v246, v152, v153
v_add_f32 v247, v156, v157
CV v86,v116,v117
MM a[116:119],a[188:191],v[20:23],a[116:119]
s_lshl_b32 s77, s77, 9
TR a[174:175],v233 offset:62464
buffer_load_dwordx4 v235, s[12:15], s64 offen offset:1024 lds
)ASM"
R"ASM(
EX v123,v123
MM a[116:119],a[184:187],v[16:19],a[116:119]
s_add_u32 s77, s77, 0
TR a[176:177],v232 offset:50688
v_lshrrev_b32 v253, 1, v248
v_add_u32 v253, s77, v253
MM a[108:111],a[184:187],v[8:11],a[108:111]
s_mov_b32 s77, LKEND_ka-LPD2_%=-16
RD a[236:239],v227 offset:8192
v_min_u32 v253, s77, v253
EX v124,v124
CV v87,v118,v119
MM a[108:111],a[188:191],v[12:15],a[108:111]
s_getpc_b64 s[98:99]
LPD2_%=:
global_load_dword v253, v253, s[98:99]
TR a[178:179],v232 offset:54784
EX v125,v125
MM a[100:103],a[188:191],v[4:7],a[100:103]
TR a[180:181],v232 offset:58880
RD a[240:243],v224 offset:12288
MM a[100:103],a[184:187],v[0:3],a[100:103]
EX v126,v126
v_smfmac_f32_16x16x64_bf16 v[144:147], v[236:239], v[64:71], v234
RD a[244:247],v225 offset:12288
RD a[248:251],v226 offset:12288
RD a[252:255],v227 offset:12288
EX v127,v127
v_smfmac_f32_16x16x64_bf16 v[148:151], v[236:239], v[72:79], v234
TR a[182:183],v232 offset:62976
CV v92,v120,v121
v_smfmac_f32_16x16x64_bf16 v[152:155], v[236:239], v[80:87], v234
buffer_load_dwordx4 v235, s[12:15], s81 offen offset:2048 lds
CV v93,v122,v123
CV v94,v124,v125
CV v95,v126,v127
s_nop 1
v_smfmac_f32_16x16x64_bf16 v[156:159], v[236:239], v[88:95], v234
TR a[184:185],v233 offset:50688
TR a[186:187],v233 offset:54784
TR a[188:189],v233 offset:58880
TR a[190:191],v233 offset:62976
s_waitcnt lgkmcnt(5)
buffer_load_dwordx4 v235, s[12:15], s64 offen offset:3072 lds
MM v[16:19],a[192:195],v[168:171],v[136:139]
MM v[16:19],a[196:199],v[184:187],v[16:19]
MM v[16:19],a[200:203],v[200:203],v[16:19]
v_max3_f32 v249, v144, v145, v148
v_max3_f32 v249, v249, v149, v152
MM v[16:19],a[204:207],v[216:219],v[16:19]
v_max3_f32 v249, v249, v153, v156
v_max_f32 v249, v249, v157
MM v[20:23],a[220:223],v[216:219],v[136:139]
v_cmp_lt_f32 vcc, s71, v249
MM v[20:23],a[216:219],v[200:203],v[20:23]
s_cbranch_vccnz LSITE35_%=
LBACK35_%=:
v_add_f32 v244, v144, v145
MM v[20:23],a[212:215],v[184:187],v[20:23]
v_add_f32 v245, v148, v149
v_add_f32 v246, v152, v153
MM v[20:23],a[208:211],v[168:171],v[20:23]
s_waitcnt lgkmcnt(0)
v_add_f32 v247, v156, v157
MM a[0:3],a[128:131],v[64:67],a[0:3]
EX v16,v16
MM a[0:3],a[132:135],v[68:71],a[0:3]
v_rcp_f32 v145, v244
MM a[8:11],a[132:135],v[76:79],a[8:11]
EX v17,v17
MM a[8:11],a[128:131],v[72:75],a[8:11]
EX v18,v18
MM a[12:15],a[136:139],v[72:75],a[12:15]
v_rcp_f32 v149, v245
MM a[12:15],a[140:143],v[76:79],a[12:15]
CV v16,v16,v17
v_bfe_u32 v144, v241, 0, 8
MM a[4:7],a[140:143],v[68:71],a[4:7]
EX v19,v19
MM a[4:7],a[136:139],v[64:67],a[4:7]
CV v17,v18,v19
v_lshl_or_b32 v144, v144, 1, 1
MM a[32:35],a[144:147],v[64:67],a[32:35]
EX v20,v20
MM a[32:35],a[148:151],v[68:71],a[32:35]
v_lshlrev_b32 v144, 22, v144
v_mul_f32 v144, -v144, v145
MM a[40:43],a[148:151],v[76:79],a[40:43]
v_bfe_u32 v148, v241, 8, 8
v_lshl_or_b32 v148, v148, 1, 1
MM a[40:43],a[144:147],v[72:75],a[40:43]
v_lshlrev_b32 v148, 22, v148
v_mul_f32 v148, -v148, v149
MM a[44:47],a[152:155],v[72:75],a[44:47]
s_mul_i32 s78, s49, 17408
s_add_u32 s78, s83, s78
s_add_u32 s78, s78, 0
s_add_u32 s79, s78, 4096
AR v0,a0
AR v1,a1
AR v2,a2
MM a[44:47],a[156:159],v[76:79],a[44:47]
AR v3,a3
AR v4,a4
AR v5,a5
MM a[36:39],a[156:159],v[68:71],a[36:39]
AR v6,a6
AR v7,a7
v_fma_f32 v0, v0, v145, v144
v_fma_f32 v1, v1, v145, v144
MM a[36:39],a[152:155],v[64:67],a[36:39]
v_fma_f32 v2, v2, v145, v144
v_fma_f32 v3, v3, v145, v144
v_fma_f32 v4, v4, v145, v144
v_fma_f32 v5, v5, v145, v144
MM a[64:67],a[160:163],v[64:67],a[64:67]
v_fma_f32 v6, v6, v145, v144
v_fma_f32 v7, v7, v145, v144
CV v160,v0,v1
CV v161,v2,v3
MM a[64:67],a[164:167],v[68:71],a[64:67]
CV v162,v4,v5
CV v163,v6,v7
MM a[72:75],a[164:167],v[76:79],a[72:75]
buffer_store_dwordx4 v[160:163], v248, s[20:23], s78 offen offset:0 sc1
AR v8,a8
AR v9,a9
AR v10,a10
MM a[72:75],a[160:163],v[72:75],a[72:75]
AR v11,a11
AR v12,a12
AR v13,a13
EX v21,v21
AR v14,a14
MM a[76:79],a[168:171],v[72:75],a[76:79]
AR v15,a15
v_fma_f32 v8, v8, v149, v148
v_fma_f32 v9, v9, v149, v148
v_fma_f32 v10, v10, v149, v148
MM a[76:79],a[172:175],v[76:79],a[76:79]
v_fma_f32 v11, v11, v149, v148
v_fma_f32 v12, v12, v149, v148
v_fma_f32 v13, v13, v149, v148
v_fma_f32 v14, v14, v149, v148
EX v22,v22
MM a[68:71],a[172:175],v[68:71],a[68:71]
v_fma_f32 v15, v15, v149, v148
CV v164,v8,v9
CV v165,v10,v11
CV v166,v12,v13
MM a[68:71],a[168:171],v[64:67],a[68:71]
CV v167,v14,v15
CV v18,v20,v21
MM a[96:99],a[176:179],v[64:67],a[96:99]
buffer_store_dwordx4 v[164:167], v248, s[20:23], s79 offen offset:0 sc1
AR v32,a32
AR v33,a33
AR v34,a34
EX v23,v23
AR v35,a35
MM a[96:99],a[180:183],v[68:71],a[96:99]
AR v36,a36
AR v37,a37
AR v38,a38
AR v39,a39
MM a[104:107],a[180:183],v[76:79],a[104:107]
v_fma_f32 v32, v32, v145, v144
v_fma_f32 v33, v33, v145, v144
v_fma_f32 v34, v34, v145, v144
v_fma_f32 v35, v35, v145, v144
MM a[104:107],a[176:179],v[72:75],a[104:107]
v_fma_f32 v36, v36, v145, v144
v_fma_f32 v37, v37, v145, v144
v_fma_f32 v38, v38, v145, v144
MM a[108:111],a[184:187],v[72:75],a[108:111]
v_fma_f32 v39, v39, v145, v144
CV v176,v32,v33
CV v177,v34,v35
CV v178,v36,v37
MM a[108:111],a[188:191],v[76:79],a[108:111]
CV v179,v38,v39
CV v19,v22,v23
MM a[100:103],a[188:191],v[68:71],a[100:103]
buffer_store_dwordx4 v[176:179], v248, s[20:23], s78 offen offset:1024 sc1
AR v40,a40
AR v41,a41
AR v42,a42
AR v43,a43
AR v44,a44
MM a[100:103],a[184:187],v[64:67],a[100:103]
AR v45,a45
AR v46,a46
AR v47,a47
v_fma_f32 v40, v40, v149, v148
MM v[28:31],a[208:211],v[172:175],v[140:143]
v_fma_f32 v41, v41, v149, v148
v_fma_f32 v42, v42, v149, v148
v_fma_f32 v43, v43, v149, v148
MM v[28:31],a[212:215],v[188:191],v[28:31]
v_fma_f32 v44, v44, v149, v148
v_fma_f32 v45, v45, v149, v148
v_fma_f32 v46, v46, v149, v148
v_fma_f32 v47, v47, v149, v148
MM v[28:31],a[216:219],v[204:207],v[28:31]
CV v180,v40,v41
CV v181,v42,v43
CV v182,v44,v45
CV v183,v46,v47
MM v[28:31],a[220:223],v[220:223],v[28:31]
buffer_store_dwordx4 v[180:183], v248, s[20:23], s79 offen offset:1024 sc1
AR v0,a64
AR v1,a65
MM v[24:27],a[204:207],v[220:223],v[140:143]
AR v2,a66
AR v3,a67
AR v4,a68
MM v[24:27],a[200:203],v[204:207],v[24:27]
AR v5,a69
AR v6,a70
AR v7,a71
v_fma_f32 v0, v0, v145, v144
MM v[24:27],a[196:199],v[188:191],v[24:27]
v_fma_f32 v1, v1, v145, v144
v_fma_f32 v2, v2, v145, v144
v_fma_f32 v3, v3, v145, v144
v_fma_f32 v4, v4, v145, v144
MM v[24:27],a[192:195],v[172:175],v[24:27]
v_fma_f32 v5, v5, v145, v144
v_fma_f32 v6, v6, v145, v144
v_fma_f32 v7, v7, v145, v144
CV v192,v0,v1
MM v[48:51],a[224:227],v[168:171],v[136:139]
CV v193,v2,v3
CV v194,v4,v5
CV v195,v6,v7
MM v[48:51],a[228:231],v[184:187],v[48:51]
buffer_store_dwordx4 v[192:195], v248, s[20:23], s78 offen offset:2048 sc1
AR v8,a72
AR v9,a73
MM v[48:51],a[232:235],v[200:203],v[48:51]
AR v10,a74
AR v11,a75
AR v12,a76
AR v13,a77
MM v[48:51],a[236:239],v[216:219],v[48:51]
AR v14,a78
AR v15,a79
v_fma_f32 v8, v8, v149, v148
v_fma_f32 v9, v9, v149, v148
MM v[52:55],a[252:255],v[216:219],v[136:139]
EX v24,v24
EX v25,v25
EX v26,v26
EX v27,v27
EX v28,v28
EX v29,v29
v_fma_f32 v10, v10, v149, v148
v_fma_f32 v11, v11, v149, v148
EX v30,v30
v_fma_f32 v12, v12, v149, v148
v_fma_f32 v13, v13, v149, v148
MM v[52:55],a[248:251],v[200:203],v[52:55]
v_fma_f32 v14, v14, v149, v148
v_fma_f32 v15, v15, v149, v148
CV v196,v8,v9
EX v31,v31
CV v24,v24,v25
CV v197,v10,v11
MM v[52:55],a[244:247],v[184:187],v[52:55]
CV v198,v12,v13
CV v199,v14,v15
MM v[52:55],a[240:243],v[168:171],v[52:55]
buffer_store_dwordx4 v[196:199], v248, s[20:23], s79 offen offset:2048 sc1
AR v32,a96
AR v33,a97
AR v34,a98
MM v[60:63],a[240:243],v[172:175],v[140:143]
AR v35,a99
AR v36,a100
AR v37,a101
EX v48,v48
AR v38,a102
MM v[60:63],a[244:247],v[188:191],v[60:63]
AR v39,a103
v_fma_f32 v32, v32, v145, v144
v_fma_f32 v33, v33, v145, v144
v_fma_f32 v34, v34, v145, v144
CV v25,v26,v27
MM v[60:63],a[248:251],v[204:207],v[60:63]
v_fma_f32 v35, v35, v145, v144
v_fma_f32 v36, v36, v145, v144
v_fma_f32 v37, v37, v145, v144
MM v[60:63],a[252:255],v[220:223],v[60:63]
v_fma_f32 v38, v38, v145, v144
v_fma_f32 v39, v39, v145, v144
CV v208,v32,v33
EX v49,v49
CV v209,v34,v35
MM v[56:59],a[236:239],v[220:223],v[140:143]
CV v210,v36,v37
CV v211,v38,v39
MM v[56:59],a[232:235],v[204:207],v[56:59]
buffer_store_dwordx4 v[208:211], v248, s[20:23], s78 offen offset:3072 sc1
AR v40,a104
AR v41,a105
AR v42,a106
AR v43,a107
EX v50,v50
MM v[56:59],a[228:231],v[188:191],v[56:59]
AR v44,a108
AR v45,a109
AR v46,a110
AR v47,a111
CV v26,v28,v29
MM v[56:59],a[224:227],v[172:175],v[56:59]
s_waitcnt lgkmcnt(0)
s_barrier
v_fma_f32 v40, v40, v149, v148
v_fma_f32 v41, v41, v149, v148
v_fma_f32 v42, v42, v149, v148
MM a[16:19],a[128:131],v[80:83],a[16:19]
s_xor_b32 s66, s94, 4
v_fma_f32 v43, v43, v149, v148
v_fma_f32 v44, v44, v149, v148
v_fma_f32 v45, v45, v149, v148
v_fma_f32 v46, v46, v149, v148
EX v51,v51
MM a[16:19],a[132:135],v[84:87],a[16:19]
s_mul_i32 s66, s66, 163840
v_fma_f32 v47, v47, v149, v148
CV v212,v40,v41
CV v213,v42,v43
CV v214,v44,v45
MM a[24:27],a[132:135],v[92:95],a[24:27]
s_add_u32 s66, s66, s87
CV v215,v46,v47
CV v27,v30,v31
MM a[24:27],a[128:131],v[88:91],a[24:27]
s_add_u32 m0, s65, 114688
buffer_store_dwordx4 v[212:215], v248, s[20:23], s79 offen offset:3072 sc1
EX v52,v52
MM a[28:31],a[136:139],v[88:91],a[28:31]
EX v53,v53
MM a[28:31],a[140:143],v[92:95],a[28:31]
RD a[192:195],v224 offset:16384
CV v20,v48,v49
MM a[20:23],a[140:143],v[84:87],a[20:23]
buffer_load_dwordx4 v240, s[24:27], s66 offen offset:0 lds
MM a[20:23],a[136:139],v[80:83],a[20:23]
s_add_u32 s66, s66, 9216
EX v54,v54
MM a[48:51],a[144:147],v[80:83],a[48:51]
RD a[196:199],v225 offset:16384
CV v21,v50,v51
MM a[48:51],a[148:151],v[84:87],a[48:51]
EX v55,v55
MM a[56:59],a[148:151],v[92:95],a[56:59]
RD a[200:203],v226 offset:16384
RD a[204:207],v227 offset:16384
MM a[56:59],a[144:147],v[88:91],a[56:59]
buffer_load_dwordx4 v240, s[24:27], s66 offen offset:1024 lds
MM a[60:63],a[152:155],v[88:91],a[60:63]
s_add_u32 s66, s66, 9216
EX v56,v56
MM a[60:63],a[156:159],v[92:95],a[60:63]
RD a[208:211],v224 offset:20480
CV v22,v52,v53
MM a[52:55],a[156:159],v[84:87],a[52:55]
buffer_load_dwordx4 v240, s[24:27], s66 offen offset:2048 lds
MM a[52:55],a[152:155],v[80:83],a[52:55]
s_add_u32 s66, s66, 9216
EX v57,v57
MM a[80:83],a[160:163],v[80:83],a[80:83]
RD a[212:215],v225 offset:20480
RD a[216:219],v226 offset:20480
MM a[80:83],a[164:167],v[84:87],a[80:83]
buffer_load_dwordx4 v240, s[24:27], s66 offen offset:3072 lds
MM a[88:91],a[164:167],v[92:95],a[88:91]
s_xor_b32 s66, s94, 4
EX v58,v58
MM a[88:91],a[160:163],v[88:91],a[88:91]
s_mul_i32 s66, s66, 163840
RD a[220:223],v227 offset:20480
CV v23,v54,v55
MM a[92:95],a[168:171],v[88:91],a[92:95]
s_add_u32 s66, s66, s86
EX v59,v59
MM a[92:95],a[172:175],v[92:95],a[92:95]
s_add_u32 m0, s65, 49152
RD a[224:227],v224 offset:24576
RD a[228:231],v225 offset:24576
MM a[84:87],a[172:175],v[84:87],a[84:87]
buffer_load_dwordx4 v240, s[24:27], s66 offen offset:0 lds
MM a[84:87],a[168:171],v[80:83],a[84:87]
s_add_u32 s66, s66, 9216
EX v60,v60
MM a[112:115],a[176:179],v[80:83],a[112:115]
RD a[232:235],v226 offset:24576
CV v28,v56,v57
MM a[112:115],a[180:183],v[84:87],a[112:115]
EX v61,v61
MM a[120:123],a[180:183],v[92:95],a[120:123]
buffer_load_dwordx4 v240, s[24:27], s66 offen offset:1024 lds
MM a[120:123],a[176:179],v[88:91],a[120:123]
s_add_u32 s66, s66, 9216
RD a[236:239],v227 offset:24576
RD a[240:243],v224 offset:28672
MM a[124:127],a[184:187],v[88:91],a[124:127]
EX v62,v62
MM a[124:127],a[188:191],v[92:95],a[124:127]
buffer_load_dwordx4 v240, s[24:27], s66 offen offset:2048 lds
MM a[116:119],a[188:191],v[84:87],a[116:119]
s_add_u32 s66, s66, 9216
RD a[244:247],v225 offset:28672
RD a[248:251],v226 offset:28672
RD a[252:255],v227 offset:28672
EX v63,v63
MM a[116:119],a[184:187],v[80:83],a[116:119]
buffer_load_dwordx4 v240, s[24:27], s66 offen offset:3072 lds
v_smfmac_f32_16x16x64_bf16 v[152:155], v[236:239], v[16:23], v234
s_nop 0
LPG25_%=:
s_cbranch_execz LPG29_%=
CV v29,v58,v59
CV v30,v60,v61
CV v31,v62,v63
s_nop 1
v_smfmac_f32_16x16x64_bf16 v[156:159], v[236:239], v[24:31], v234
)ASM"
R"ASM(
s_waitcnt lgkmcnt(0)
MM v[80:83],a[192:195],v[168:171],v[136:139]
TR a[128:129],v232 offset:0
TR a[130:131],v232 offset:4096
MM v[80:83],a[196:199],v[184:187],v[80:83]
TR a[132:133],v232 offset:8192
TR a[134:135],v232 offset:12288
MM v[80:83],a[200:203],v[200:203],v[80:83]
TR a[136:137],v233 offset:0
TR a[138:139],v233 offset:4096
MM v[80:83],a[204:207],v[216:219],v[80:83]
v_max3_f32 v249, v152, v153, v156
v_max_f32 v249, v249, v157
MM v[84:87],a[220:223],v[216:219],v[136:139]
TR a[140:141],v233 offset:8192
v_cmp_lt_f32 vcc, s71, v249
MM v[84:87],a[216:219],v[200:203],v[84:87]
s_cbranch_vccnz LSITE36_%=
LBACK36_%=:
TR a[142:143],v233 offset:12288
v_add_f32 v246, v152, v153
MM v[84:87],a[212:215],v[184:187],v[84:87]
TR a[144:145],v232 offset:512
v_add_f32 v247, v156, v157
MM v[84:87],a[208:211],v[168:171],v[84:87]
TR a[146:147],v232 offset:4608
TR a[148:149],v232 offset:8704
MM v[92:95],a[208:211],v[172:175],v[140:143]
s_lshl_b32 s97, s84, 2
s_add_u32 s97, s97, s96
s_add_u32 s97, s97, 0
MM v[92:95],a[212:215],v[188:191],v[92:95]
s_waitcnt vmcnt(8)
v_mov_b32 v242, s69
s_mov_b64 exec, 1
buffer_store_dword v242, off, s[20:23], s97 offset:0 sc1
s_mov_b64 exec, -1
MM v[92:95],a[216:219],v[204:207],v[92:95]
v_lshrrev_b32 v98, 1, v248
v_min_u32 v98, 312, v98
MM v[92:95],a[220:223],v[220:223],v[92:95]
s_mov_b32 s62, 94464
buffer_load_dwordx2 v[96:97], v98, s[20:23], s62 offen offset:0 sc1
MM v[88:91],a[204:207],v[220:223],v[140:143]
TR a[150:151],v232 offset:12800
EX v80,v80
MM v[88:91],a[200:203],v[204:207],v[88:91]
TR a[152:153],v233 offset:512
TR a[154:155],v233 offset:4608
EX v81,v81
MM v[88:91],a[196:199],v[188:191],v[88:91]
TR a[156:157],v233 offset:8704
TR a[158:159],v233 offset:12800
MM v[88:91],a[192:195],v[172:175],v[88:91]
EX v82,v82
MM v[112:115],a[224:227],v[168:171],v[136:139]
TR a[160:161],v232 offset:1024
TR a[162:163],v232 offset:5120
EX v83,v83
MM v[112:115],a[228:231],v[184:187],v[112:115]
TR a[164:165],v232 offset:9216
TR a[166:167],v232 offset:13312
MM v[112:115],a[232:235],v[200:203],v[112:115]
EX v84,v84
MM v[112:115],a[236:239],v[216:219],v[112:115]
TR a[168:169],v233 offset:1024
TR a[170:171],v233 offset:5120
MM v[116:119],a[252:255],v[216:219],v[136:139]
EX v85,v85
MM v[116:119],a[248:251],v[200:203],v[116:119]
TR a[172:173],v233 offset:9216
TR a[174:175],v233 offset:13312
MM v[116:119],a[244:247],v[184:187],v[116:119]
TR a[176:177],v232 offset:1536
EX v86,v86
MM v[116:119],a[240:243],v[168:171],v[116:119]
TR a[178:179],v232 offset:5632
CV v80,v80,v81
EX v87,v87
MM v[124:127],a[240:243],v[172:175],v[140:143]
TR a[180:181],v232 offset:9728
TR a[182:183],v232 offset:13824
MM v[124:127],a[244:247],v[188:191],v[124:127]
EX v88,v88
MM v[124:127],a[248:251],v[204:207],v[124:127]
TR a[184:185],v233 offset:1536
TR a[186:187],v233 offset:5632
TR a[188:189],v233 offset:9728
TR a[190:191],v233 offset:13824
EX v89,v89
MM v[124:127],a[252:255],v[220:223],v[124:127]
CV v81,v82,v83
CV v82,v84,v85
MM v[120:123],a[236:239],v[220:223],v[140:143]
EX v90,v90
MM v[120:123],a[232:235],v[204:207],v[120:123]
EX v91,v91
MM v[120:123],a[228:231],v[188:191],v[120:123]
CV v83,v86,v87
CV v88,v88,v89
MM v[120:123],a[224:227],v[172:175],v[120:123]
s_waitcnt lgkmcnt(0)
s_barrier
CV v89,v90,v91
EX v92,v92
MM a[16:19],a[128:131],v[16:19],a[16:19]
s_xor_b32 s66, s94, 5
EX v93,v93
MM a[16:19],a[132:135],v[20:23],a[16:19]
s_mul_i32 s66, s66, 163840
EX v94,v94
MM a[24:27],a[132:135],v[28:31],a[24:27]
s_add_u32 s66, s66, s87
EX v95,v95
MM a[24:27],a[128:131],v[24:27],a[24:27]
s_add_u32 m0, s65, 65536
RD a[192:195],v224 offset:32768
CV v90,v92,v93
MM a[28:31],a[136:139],v[24:27],a[28:31]
EX v112,v112
MM a[28:31],a[140:143],v[28:31],a[28:31]
buffer_load_dwordx4 v240, s[24:27], s66 offen offset:0 lds
MM a[20:23],a[140:143],v[20:23],a[20:23]
s_add_u32 s66, s66, 9216
EX v113,v113
MM a[20:23],a[136:139],v[16:19],a[20:23]
RD a[196:199],v225 offset:32768
CV v91,v94,v95
MM a[48:51],a[144:147],v[16:19],a[48:51]
v_lshrrev_b32 v243, 2, v248
v_min_u32 v243, 12, v243
MM a[48:51],a[148:151],v[20:23],a[48:51]
buffer_load_dword v242, v243, s[20:23], s44 offen offset:0 sc1
EX v114,v114
MM a[56:59],a[148:151],v[28:31],a[56:59]
RD a[200:203],v226 offset:32768
RD a[204:207],v227 offset:32768
MM a[56:59],a[144:147],v[24:27],a[56:59]
EX v115,v115
MM a[60:63],a[152:155],v[24:27],a[60:63]
buffer_load_dwordx4 v240, s[24:27], s66 offen offset:1024 lds
MM a[60:63],a[156:159],v[28:31],a[60:63]
s_add_u32 s66, s66, 9216
EX v116,v116
MM a[52:55],a[156:159],v[20:23],a[52:55]
EX v117,v117
MM a[52:55],a[152:155],v[16:19],a[52:55]
RD a[208:211],v224 offset:36864
RD a[212:215],v225 offset:36864
buffer_load_dwordx4 v240, s[24:27], s66 offen offset:2048 lds
MM a[80:83],a[160:163],v[16:19],a[80:83]
s_add_u32 s66, s66, 9216
EX v118,v118
MM a[80:83],a[164:167],v[20:23],a[80:83]
RD a[216:219],v226 offset:36864
CV v84,v112,v113
MM a[88:91],a[164:167],v[28:31],a[88:91]
EX v119,v119
MM a[88:91],a[160:163],v[24:27],a[88:91]
buffer_load_dwordx4 v240, s[24:27], s66 offen offset:3072 lds
EX v120,v120
MM a[92:95],a[168:171],v[24:27],a[92:95]
s_xor_b32 s66, s94, 5
RD a[220:223],v227 offset:36864
CV v85,v114,v115
MM a[92:95],a[172:175],v[28:31],a[92:95]
s_mul_i32 s66, s66, 163840
EX v121,v121
MM a[84:87],a[172:175],v[20:23],a[84:87]
s_add_u32 s66, s66, s86
RD a[224:227],v224 offset:40960
CV v86,v116,v117
MM a[84:87],a[168:171],v[16:19],a[84:87]
s_add_u32 m0, s65, 0
EX v122,v122
MM a[112:115],a[176:179],v[16:19],a[112:115]
buffer_load_dwordx4 v240, s[24:27], s66 offen offset:0 lds
RD a[228:231],v225 offset:40960
EX v123,v123
MM a[112:115],a[180:183],v[20:23],a[112:115]
s_add_u32 s66, s66, 9216
RD a[232:235],v226 offset:40960
CV v87,v118,v119
MM a[120:123],a[180:183],v[28:31],a[120:123]
EX v124,v124
MM a[120:123],a[176:179],v[24:27],a[120:123]
buffer_load_dwordx4 v240, s[24:27], s66 offen offset:1024 lds
MM a[124:127],a[184:187],v[24:27],a[124:127]
s_add_u32 s66, s66, 9216
RD a[236:239],v227 offset:40960
EX v125,v125
MM a[124:127],a[188:191],v[28:31],a[124:127]
EX v126,v126
MM a[116:119],a[188:191],v[20:23],a[116:119]
RD a[240:243],v224 offset:45056
RD a[244:247],v225 offset:45056
RD a[248:251],v226 offset:45056
RD a[252:255],v227 offset:45056
EX v127,v127
MM a[116:119],a[184:187],v[16:19],a[116:119]
buffer_load_dwordx4 v240, s[24:27], s66 offen offset:2048 lds
CV v92,v120,v121
v_smfmac_f32_16x16x64_bf16 v[152:155], v[236:239], v[80:87], v234
s_add_u32 s66, s66, 9216
CV v93,v122,v123
CV v94,v124,v125
CV v95,v126,v127
s_nop 1
v_smfmac_f32_16x16x64_bf16 v[156:159], v[236:239], v[88:95], v234
s_waitcnt lgkmcnt(0)
buffer_load_dwordx4 v240, s[24:27], s66 offen offset:3072 lds
v_lshrrev_b32 v145, 9, v248
v_mul_u32_u24 v145, 1536, v145
v_sub_u32 v244, v232, v145
v_sub_u32 v235, v233, v145
v_add_u32 v244, 65536, v244
v_add_u32 v235, 65536, v235
v_and_b32 v145, 960, v248
v_lshlrev_b32 v145, 1, v145
v_bfe_u32 v146, v248, 7, 2
v_lshl_add_u32 v145, v146, 5, v145
v_bfe_u32 v146, v248, 4, 2
v_lshl_add_u32 v145, v146, 3, v145
v_add_u32 v228, %[slds], v145
v_xor_b32 v229, 32, v228
v_xor_b32 v230, 64, v228
v_xor_b32 v231, 96, v228
v_add_u32 v144, 65536, v228
v_add_u32 v145, 65536, v229
v_add_u32 v146, 65536, v230
v_add_u32 v147, 65536, v231
MM v[16:19],a[192:195],v[168:171],v[136:139]
TR a[128:129],v232 offset:16384
TR a[130:131],v232 offset:20480
MM v[16:19],a[196:199],v[184:187],v[16:19]
TR a[132:133],v232 offset:24576
TR a[134:135],v232 offset:28672
MM v[16:19],a[200:203],v[200:203],v[16:19]
TR a[136:137],v233 offset:16384
TR a[138:139],v233 offset:20480
MM v[16:19],a[204:207],v[216:219],v[16:19]
v_max3_f32 v249, v152, v153, v156
v_max_f32 v249, v249, v157
MM v[20:23],a[220:223],v[216:219],v[136:139]
TR a[140:141],v233 offset:24576
v_cmp_lt_f32 vcc, s71, v249
MM v[20:23],a[216:219],v[200:203],v[20:23]
s_cbranch_vccnz LSITE37_%=
LBACK37_%=:
TR a[142:143],v233 offset:28672
v_add_f32 v246, v152, v153
MM v[20:23],a[212:215],v[184:187],v[20:23]
TR a[144:145],v232 offset:16896
v_add_f32 v247, v156, v157
MM v[20:23],a[208:211],v[168:171],v[20:23]
TR a[146:147],v232 offset:20992
TR a[148:149],v232 offset:25088
MM v[28:31],a[208:211],v[172:175],v[140:143]
s_waitcnt vmcnt(7)
v_cmp_ne_u32 vcc, s69, v242
s_cbranch_vccnz LRPO0_%=
LRPO0b_%=:
MM v[28:31],a[212:215],v[188:191],v[28:31]
s_xor_b32 s92, s49, 4
s_mul_i32 s92, s92, 17408
s_add_u32 s92, s83, s92
MM v[28:31],a[216:219],v[204:207],v[28:31]
buffer_load_dwordx4 a[0:3], v248, s[20:23], s92 offen offset:0 sc1
MM v[28:31],a[220:223],v[220:223],v[28:31]
buffer_load_dwordx4 a[32:35], v248, s[20:23], s92 offen offset:1024 sc1
MM v[24:27],a[204:207],v[220:223],v[140:143]
TR a[150:151],v232 offset:29184
v_xor_b32 v96, s69, v96
v_xor_b32 v97, s69, v97
EX v16,v16
MM v[24:27],a[200:203],v[204:207],v[24:27]
TR a[152:153],v233 offset:16896
TR a[154:155],v233 offset:20992
v_or_b32 v99, v96, v97
EX v17,v17
MM v[24:27],a[196:199],v[188:191],v[24:27]
buffer_load_dwordx4 a[64:67], v248, s[20:23], s92 offen offset:2048 sc1
TR a[156:157],v233 offset:25088
MM v[24:27],a[192:195],v[172:175],v[24:27]
TR a[158:159],v233 offset:29184
v_cmp_ne_u32 vcc, 0, v99
s_cbranch_vccnz LRPW_%=
LRPWb_%=:
EX v18,v18
MM v[48:51],a[224:227],v[168:171],v[136:139]
TR a[160:161],v232 offset:17408
TR a[162:163],v232 offset:21504
EX v19,v19
MM v[48:51],a[228:231],v[184:187],v[48:51]
buffer_load_dwordx4 a[96:99], v248, s[20:23], s92 offen offset:3072 sc1
TR a[164:165],v232 offset:25600
MM v[48:51],a[232:235],v[200:203],v[48:51]
s_add_u32 s92, s92, 4096
TR a[166:167],v232 offset:29696
TR a[168:169],v233 offset:17408
EX v20,v20
MM v[48:51],a[236:239],v[216:219],v[48:51]
TR a[170:171],v233 offset:21504
TR a[172:173],v233 offset:25600
MM v[52:55],a[252:255],v[216:219],v[136:139]
buffer_load_dwordx4 a[4:7], v248, s[20:23], s92 offen offset:0 sc1
EX v21,v21
MM v[52:55],a[248:251],v[200:203],v[52:55]
TR a[174:175],v233 offset:29696
CV v16,v16,v17
MM v[52:55],a[244:247],v[184:187],v[52:55]
TR a[176:177],v232 offset:17920
EX v22,v22
MM v[52:55],a[240:243],v[168:171],v[52:55]
buffer_load_dwordx4 a[36:39], v248, s[20:23], s92 offen offset:1024 sc1
TR a[178:179],v232 offset:22016
EX v23,v23
MM v[60:63],a[240:243],v[172:175],v[140:143]
TR a[180:181],v232 offset:26112
TR a[182:183],v232 offset:30208
MM v[60:63],a[244:247],v[188:191],v[60:63]
EX v24,v24
MM v[60:63],a[248:251],v[204:207],v[60:63]
buffer_load_dwordx4 a[68:71], v248, s[20:23], s92 offen offset:2048 sc1
TR a[184:185],v233 offset:17920
TR a[186:187],v233 offset:22016
TR a[188:189],v233 offset:26112
TR a[190:191],v233 offset:30208
EX v25,v25
MM v[60:63],a[252:255],v[220:223],v[60:63]
EX v26,v26
CV v17,v18,v19
MM v[56:59],a[236:239],v[220:223],v[140:143]
EX v27,v27
MM v[56:59],a[232:235],v[204:207],v[56:59]
buffer_load_dwordx4 a[100:103], v248, s[20:23], s92 offen offset:3072 sc1
MM v[56:59],a[228:231],v[188:191],v[56:59]
EX v28,v28
CV v18,v20,v21
MM v[56:59],a[224:227],v[172:175],v[56:59]
s_waitcnt lgkmcnt(0)
s_barrier
CV v19,v22,v23
MM a[16:19],a[128:131],v[80:83],a[16:19]
s_xor_b32 s66, s94, 6
EX v29,v29
MM a[16:19],a[132:135],v[84:87],a[16:19]
s_mul_i32 s66, s66, 163840
CV v24,v24,v25
CV v25,v26,v27
MM a[24:27],a[132:135],v[92:95],a[24:27]
s_add_u32 s66, s66, s87
EX v30,v30
MM a[24:27],a[128:131],v[88:91],a[24:27]
s_add_u32 m0, s65, 81920
EX v31,v31
MM a[28:31],a[136:139],v[88:91],a[28:31]
EX v48,v48
MM a[28:31],a[140:143],v[92:95],a[28:31]
CV v26,v28,v29
CV v27,v30,v31
MM a[20:23],a[140:143],v[84:87],a[20:23]
buffer_load_dwordx4 v240, s[24:27], s66 offen offset:0 lds
MM a[20:23],a[136:139],v[80:83],a[20:23]
s_add_u32 s66, s66, 9216
EX v49,v49
MM a[48:51],a[144:147],v[80:83],a[48:51]
EX v50,v50
MM a[48:51],a[148:151],v[84:87],a[48:51]
buffer_load_dwordx4 v240, s[24:27], s66 offen offset:1024 lds
MM a[56:59],a[148:151],v[92:95],a[56:59]
s_add_u32 s66, s66, 9216
EX v51,v51
MM a[56:59],a[144:147],v[88:91],a[56:59]
TR a[128:129],v232 offset:32768
CV v20,v48,v49
MM a[60:63],a[152:155],v[88:91],a[60:63]
EX v52,v52
MM a[60:63],a[156:159],v[92:95],a[60:63]
TR a[130:131],v232 offset:36864
TR a[132:133],v232 offset:40960
MM a[52:55],a[156:159],v[84:87],a[52:55]
)ASM"
R"ASM(
EX v53,v53
MM a[52:55],a[152:155],v[80:83],a[52:55]
TR a[134:135],v232 offset:45056
TR a[136:137],v233 offset:32768
buffer_load_dwordx4 v240, s[24:27], s66 offen offset:2048 lds
MM a[80:83],a[160:163],v[80:83],a[80:83]
s_add_u32 s66, s66, 9216
TR a[138:139],v233 offset:36864
TR a[140:141],v233 offset:40960
EX v54,v54
MM a[80:83],a[164:167],v[84:87],a[80:83]
TR a[142:143],v233 offset:45056
TR a[144:145],v232 offset:33280
MM a[88:91],a[164:167],v[92:95],a[88:91]
s_xor_b32 s92, s49, 5
s_mul_i32 s92, s92, 17408
s_add_u32 s92, s83, s92
EX v55,v55
MM a[88:91],a[160:163],v[88:91],a[88:91]
buffer_load_dwordx4 a[8:11], v248, s[20:23], s92 offen offset:0 sc1
TR a[146:147],v232 offset:37376
EX v56,v56
MM a[92:95],a[168:171],v[88:91],a[92:95]
TR a[148:149],v232 offset:41472
TR a[150:151],v232 offset:45568
buffer_load_dwordx4 v240, s[24:27], s66 offen offset:3072 lds
MM a[92:95],a[172:175],v[92:95],a[92:95]
s_xor_b32 s66, s94, 6
TR a[152:153],v233 offset:33280
EX v57,v57
CV v21,v50,v51
MM a[84:87],a[172:175],v[84:87],a[84:87]
s_mul_i32 s66, s66, 163840
TR a[154:155],v233 offset:37376
TR a[156:157],v233 offset:41472
MM a[84:87],a[168:171],v[80:83],a[84:87]
s_add_u32 s66, s66, s86
buffer_load_dwordx4 a[40:43], v248, s[20:23], s92 offen offset:1024 sc1
TR a[158:159],v233 offset:45568
s_add_u32 m0, s65, 16384
EX v58,v58
MM a[112:115],a[176:179],v[80:83],a[112:115]
TR a[160:161],v232 offset:33792
CV v22,v52,v53
EX v59,v59
MM a[112:115],a[180:183],v[84:87],a[112:115]
TR a[162:163],v232 offset:37888
TR a[164:165],v232 offset:41984
MM a[120:123],a[180:183],v[92:95],a[120:123]
buffer_load_dwordx4 a[72:75], v248, s[20:23], s92 offen offset:2048 sc1
TR a[166:167],v232 offset:46080
EX v60,v60
MM a[120:123],a[176:179],v[88:91],a[120:123]
TR a[168:169],v233 offset:33792
TR a[170:171],v233 offset:37888
CV v23,v54,v55
MM a[124:127],a[184:187],v[88:91],a[124:127]
TR a[172:173],v233 offset:41984
EX v61,v61
MM a[124:127],a[188:191],v[92:95],a[124:127]
buffer_load_dwordx4 a[104:107], v248, s[20:23], s92 offen offset:3072 sc1
TR a[174:175],v233 offset:46080
EX v62,v62
MM a[116:119],a[188:191],v[84:87],a[116:119]
TR a[176:177],v232 offset:34304
TR a[178:179],v232 offset:38400
EX v63,v63
MM a[116:119],a[184:187],v[80:83],a[116:119]
s_add_u32 s92, s92, 4096
TR a[180:181],v232 offset:42496
CV v28,v56,v57
v_smfmac_f32_16x16x64_bf16 v[152:155], v[236:239], v[16:23], v234
buffer_load_dwordx4 v240, s[24:27], s66 offen offset:0 lds
s_add_u32 s66, s66, 9216
TR a[182:183],v232 offset:46592
CV v29,v58,v59
CV v30,v60,v61
CV v31,v62,v63
s_nop 1
v_smfmac_f32_16x16x64_bf16 v[156:159], v[236:239], v[24:31], v234
TR a[184:185],v233 offset:34304
TR a[186:187],v233 offset:38400
TR a[188:189],v233 offset:42496
TR a[190:191],v233 offset:46592
s_waitcnt lgkmcnt(0)
buffer_load_dwordx4 v240, s[24:27], s66 offen offset:1024 lds
s_add_u32 s66, s66, 9216
buffer_load_dwordx4 v240, s[24:27], s66 offen offset:2048 lds
s_add_u32 s66, s66, 9216
buffer_load_dwordx4 v240, s[24:27], s66 offen offset:3072 lds
s_nop 6
v_max3_f32 v249, v152, v153, v156
v_max_f32 v249, v249, v157
v_cmp_lt_f32 vcc, s71, v249
s_cbranch_vccnz LSITE38_%=
LBACK38_%=:
v_add_f32 v246, v152, v153
v_add_f32 v247, v156, v157
s_waitcnt lgkmcnt(0)
MM a[16:19],a[128:131],v[16:19],a[16:19]
s_barrier
MM a[16:19],a[132:135],v[20:23],a[16:19]
buffer_load_dwordx4 a[12:15], v248, s[20:23], s92 offen offset:0 sc1
MM a[24:27],a[132:135],v[28:31],a[24:27]
v_rcp_f32 v153, v246
MM a[24:27],a[128:131],v[24:27],a[24:27]
v_rcp_f32 v157, v247
MM a[28:31],a[136:139],v[24:27],a[28:31]
v_bfe_u32 v152, v241, 16, 8
v_lshl_or_b32 v152, v152, 1, 1
MM a[28:31],a[140:143],v[28:31],a[28:31]
buffer_load_dwordx4 a[44:47], v248, s[20:23], s92 offen offset:1024 sc1
MM a[20:23],a[140:143],v[20:23],a[20:23]
v_lshlrev_b32 v152, 22, v152
v_mul_f32 v152, -v152, v153
MM a[20:23],a[136:139],v[16:19],a[20:23]
v_bfe_u32 v156, v241, 24, 8
v_lshl_or_b32 v156, v156, 1, 1
MM a[48:51],a[144:147],v[16:19],a[48:51]
TR a[128:129],v244 offset:0
TR a[130:131],v244 offset:1024
MM a[48:51],a[148:151],v[20:23],a[48:51]
buffer_load_dwordx4 a[76:79], v248, s[20:23], s92 offen offset:2048 sc1
MM a[56:59],a[148:151],v[28:31],a[56:59]
TR a[132:133],v244 offset:2048
TR a[134:135],v244 offset:3072
MM a[56:59],a[144:147],v[24:27],a[56:59]
v_lshlrev_b32 v156, 22, v156
v_mul_f32 v156, -v156, v157
MM a[60:63],a[152:155],v[24:27],a[60:63]
s_mul_i32 s78, s49, 17408
TR a[136:137],v244 offset:4096
TR a[138:139],v244 offset:5120
MM a[60:63],a[156:159],v[28:31],a[60:63]
s_add_u32 s78, s83, s78
buffer_load_dwordx4 a[108:111], v248, s[20:23], s92 offen offset:3072 sc1
MM a[52:55],a[156:159],v[20:23],a[52:55]
s_add_u32 s78, s78, 8192
TR a[140:141],v244 offset:6144
TR a[142:143],v244 offset:7168
MM a[52:55],a[152:155],v[16:19],a[52:55]
s_add_u32 s79, s78, 4096
AR v80,a16
AR v81,a17
MM a[80:83],a[160:163],v[16:19],a[80:83]
TR a[144:145],v235 offset:0
TR a[146:147],v235 offset:1024
MM a[80:83],a[164:167],v[20:23],a[80:83]
AR v82,a18
AR v83,a19
MM a[88:91],a[164:167],v[28:31],a[88:91]
TR a[148:149],v235 offset:2048
TR a[150:151],v235 offset:3072
MM a[88:91],a[160:163],v[24:27],a[88:91]
AR v84,a20
AR v85,a21
MM a[92:95],a[168:171],v[24:27],a[92:95]
TR a[152:153],v235 offset:4096
TR a[154:155],v235 offset:5120
MM a[92:95],a[172:175],v[28:31],a[92:95]
s_xor_b32 s66, s94, 7
AR v86,a22
AR v87,a23
MM a[84:87],a[172:175],v[20:23],a[84:87]
s_mul_i32 s66, s66, 163840
TR a[156:157],v235 offset:6144
TR a[158:159],v235 offset:7168
v_fma_f32 v80, v80, v153, v152
MM a[84:87],a[168:171],v[16:19],a[84:87]
s_add_u32 s66, s66, s87
v_fma_f32 v81, v81, v153, v152
v_fma_f32 v82, v82, v153, v152
MM a[112:115],a[176:179],v[16:19],a[112:115]
s_add_u32 m0, s65, 98304
TR a[160:161],v144 offset:49152
TR a[162:163],v144 offset:51200
v_fma_f32 v83, v83, v153, v152
MM a[112:115],a[180:183],v[20:23],a[112:115]
buffer_load_dwordx4 v240, s[24:27], s66 offen offset:0 lds
v_fma_f32 v84, v84, v153, v152
v_fma_f32 v85, v85, v153, v152
MM a[120:123],a[180:183],v[28:31],a[120:123]
s_add_u32 s66, s66, 9216
TR a[164:165],v144 offset:53248
TR a[166:167],v144 offset:55296
v_fma_f32 v86, v86, v153, v152
v_fma_f32 v87, v87, v153, v152
MM a[120:123],a[176:179],v[24:27],a[120:123]
CV v168,v80,v81
CV v169,v82,v83
MM a[124:127],a[184:187],v[24:27],a[124:127]
TR a[168:169],v144 offset:57344
TR a[170:171],v144 offset:59392
CV v170,v84,v85
MM a[124:127],a[188:191],v[28:31],a[124:127]
buffer_load_dwordx4 v240, s[24:27], s66 offen offset:1024 lds
CV v171,v86,v87
MM a[116:119],a[188:191],v[20:23],a[116:119]
LPG26_%=:
s_cbranch_execz LPG30_%=
s_add_u32 s66, s66, 9216
TR a[172:173],v144 offset:61440
TR a[174:175],v144 offset:63488
MM a[116:119],a[184:187],v[16:19],a[116:119]
buffer_store_dwordx4 v[168:171], v248, s[20:23], s78 offen offset:0 sc1
AR v88,a24
AR v89,a25
s_waitcnt vmcnt(23)
s_waitcnt lgkmcnt(15)
MM v[128:131],a[128:131],a[0:3],0
TR a[176:177],v145 offset:49152
TR a[178:179],v145 offset:51200
AR v90,a26
AR v91,a27
MM v[128:131],a[132:135],a[32:35],v[128:131]
v_bfe_u32 v249, v248, 6, 2
v_lshlrev_b32 v249, 8, v249
AR v92,a28
AR v93,a29
MM v[128:131],a[136:139],a[64:67],v[128:131]
TR a[180:181],v145 offset:53248
TR a[182:183],v145 offset:55296
AR v94,a30
MM v[128:131],a[140:143],a[96:99],v[128:131]
s_waitcnt vmcnt(19)
v_lshrrev_b32 v250, 8, v248
v_lshl_add_u32 v249, v250, 6, v249
AR v95,a31
v_fma_f32 v88, v88, v157, v156
MM v[136:139],a[140:143],a[100:103],0
TR a[184:185],v145 offset:57344
TR a[186:187],v145 offset:59392
v_fma_f32 v89, v89, v157, v156
v_fma_f32 v90, v90, v157, v156
MM v[136:139],a[136:139],a[68:71],v[136:139]
buffer_load_dwordx4 v240, s[24:27], s66 offen offset:2048 lds
v_and_b32 v251, 48, v248
v_add_u32 v249, v249, v251
v_fma_f32 v91, v91, v157, v156
MM v[136:139],a[132:135],a[36:39],v[136:139]
s_add_u32 s66, s66, 9216
TR a[188:189],v145 offset:61440
TR a[190:191],v145 offset:63488
v_fma_f32 v92, v92, v157, v156
v_fma_f32 v93, v93, v157, v156
MM v[136:139],a[128:131],a[4:7],v[136:139]
s_waitcnt lgkmcnt(15)
v_fma_f32 v94, v94, v157, v156
v_fma_f32 v95, v95, v157, v156
MM v[140:143],a[144:147],a[4:7],0
s_lshr_b32 s60, s49, 3
TR a[128:129],v146 offset:49152
TR a[130:131],v146 offset:51200
CV v172,v88,v89
CV v173,v90,v91
MM v[140:143],a[148:151],a[36:39],v[140:143]
buffer_load_dwordx4 v240, s[24:27], s66 offen offset:3072 lds
CV v174,v92,v93
MM v[140:143],a[152:155],a[68:71],v[140:143]
TR a[132:133],v146 offset:53248
TR a[134:135],v146 offset:55296
CV v175,v94,v95
MM v[140:143],a[156:159],a[100:103],v[140:143]
s_xor_b32 s66, s94, 7
buffer_store_dwordx4 v[172:175], v248, s[20:23], s79 offen offset:0 sc1
MM v[132:135],a[156:159],a[96:99],0
s_mul_i32 s66, s66, 163840
TR a[136:137],v146 offset:57344
TR a[138:139],v146 offset:59392
AR v112,a48
AR v113,a49
MM v[132:135],a[152:155],a[64:67],v[132:135]
s_add_u32 s66, s66, s86
AR v114,a50
AR v115,a51
MM v[132:135],a[148:151],a[32:35],v[132:135]
s_add_u32 m0, s65, 32768
TR a[140:141],v146 offset:61440
TR a[142:143],v146 offset:63488
AR v116,a52
MM v[132:135],a[144:147],a[0:3],v[132:135]
s_waitcnt lgkmcnt(15)
buffer_load_dwordx4 v240, s[24:27], s66 offen offset:0 lds
AR v117,a53
AR v118,a54
MM v[64:67],a[160:163],a[0:3],0
s_xor_b32 s92, s49, 6
TR a[144:145],v147 offset:49152
TR a[146:147],v147 offset:51200
AR v119,a55
v_fma_f32 v112, v112, v153, v152
MM v[64:67],a[164:167],a[32:35],v[64:67]
s_mul_i32 s92, s92, 17408
s_add_u32 s92, s83, s92
v_fma_f32 v113, v113, v153, v152
v_fma_f32 v114, v114, v153, v152
MM v[64:67],a[168:171],a[64:67],v[64:67]
s_add_u32 s66, s66, 9216
TR a[148:149],v147 offset:53248
TR a[150:151],v147 offset:55296
v_fma_f32 v115, v115, v153, v152
MM v[64:67],a[172:175],a[96:99],v[64:67]
buffer_load_dwordx4 v[160:163], v248, s[20:23], s92 offen offset:0 sc1
v_fma_f32 v116, v116, v153, v152
v_fma_f32 v117, v117, v153, v152
MM v[72:75],a[172:175],a[100:103],0
TR a[152:153],v147 offset:57344
TR a[154:155],v147 offset:59392
v_fma_f32 v118, v118, v153, v152
v_fma_f32 v119, v119, v153, v152
MM v[72:75],a[168:171],a[68:71],v[72:75]
CV v184,v112,v113
CV v185,v114,v115
MM v[72:75],a[164:167],a[36:39],v[72:75]
TR a[156:157],v147 offset:61440
TR a[158:159],v147 offset:63488
CV v186,v116,v117
MM v[72:75],a[160:163],a[4:7],v[72:75]
s_waitcnt lgkmcnt(15)
buffer_load_dwordx4 v[176:179], v248, s[20:23], s92 offen offset:1024 sc1
CV v187,v118,v119
MM v[76:79],a[176:179],a[4:7],0
TR a[160:161],v228 offset:49152
TR a[162:163],v228 offset:51200
MM v[76:79],a[180:183],a[36:39],v[76:79]
v_xor_b32 v253, s60, v250
v_and_b32 v238, 240, v248
MM v[76:79],a[184:187],a[68:71],v[76:79]
TR a[164:165],v228 offset:53248
TR a[166:167],v228 offset:55296
MM v[76:79],a[188:191],a[100:103],v[76:79]
buffer_store_dwordx4 v[184:187], v248, s[20:23], s78 offen offset:1024 sc1
AR v120,a56
AR v121,a57
AR v122,a58
AR v123,a59
AR v124,a60
AR v125,a61
MM v[68:71],a[188:191],a[96:99],0
TR a[168:169],v228 offset:57344
TR a[170:171],v228 offset:59392
AR v126,a62
MM v[68:71],a[184:187],a[64:67],v[68:71]
AR v127,a63
v_fma_f32 v120, v120, v157, v156
MM v[68:71],a[180:183],a[32:35],v[68:71]
TR a[172:173],v228 offset:61440
TR a[174:175],v228 offset:63488
v_fma_f32 v121, v121, v157, v156
v_fma_f32 v122, v122, v157, v156
MM v[68:71],a[176:179],a[0:3],v[68:71]
s_waitcnt lgkmcnt(15)
buffer_load_dwordx4 v[192:195], v248, s[20:23], s92 offen offset:2048 sc1
v_fma_f32 v123, v123, v157, v156
MM v[96:99],a[128:131],a[0:3],0
TR a[176:177],v229 offset:49152
TR a[178:179],v229 offset:51200
v_fma_f32 v124, v124, v157, v156
v_fma_f32 v125, v125, v157, v156
MM v[96:99],a[132:135],a[32:35],v[96:99]
v_fma_f32 v126, v126, v157, v156
v_fma_f32 v127, v127, v157, v156
MM v[96:99],a[136:139],a[64:67],v[96:99]
TR a[180:181],v229 offset:53248
TR a[182:183],v229 offset:55296
CV v188,v120,v121
MM v[96:99],a[140:143],a[96:99],v[96:99]
buffer_load_dwordx4 v[208:211], v248, s[20:23], s92 offen offset:3072 sc1
CV v189,v122,v123
CV v190,v124,v125
MM v[104:107],a[140:143],a[100:103],0
TR a[184:185],v229 offset:57344
TR a[186:187],v229 offset:59392
CV v191,v126,v127
MM v[104:107],a[136:139],a[68:71],v[104:107]
)ASM"
R"ASM(
s_add_u32 s92, s92, 4096
v_lshl_add_u32 v232, v250, 2, v238
s_add_u32 s61, s65, 131072
MM v[104:107],a[132:135],a[36:39],v[104:107]
TR a[188:189],v229 offset:61440
TR a[190:191],v229 offset:63488
MM v[104:107],a[128:131],a[4:7],v[104:107]
s_waitcnt lgkmcnt(15)
buffer_store_dwordx4 v[188:191], v248, s[20:23], s79 offen offset:1024 sc1
AR v80,a80
AR v81,a81
AR v82,a82
AR v83,a83
MM v[108:111],a[144:147],a[4:7],0
TR a[128:129],v230 offset:49152
TR a[130:131],v230 offset:51200
AR v84,a84
MM v[108:111],a[148:151],a[36:39],v[108:111]
AR v85,a85
AR v86,a86
MM v[108:111],a[152:155],a[68:71],v[108:111]
TR a[132:133],v230 offset:53248
TR a[134:135],v230 offset:55296
AR v87,a87
v_fma_f32 v80, v80, v153, v152
MM v[108:111],a[156:159],a[100:103],v[108:111]
buffer_load_dwordx4 v[164:167], v248, s[20:23], s92 offen offset:0 sc1
v_fma_f32 v81, v81, v153, v152
v_fma_f32 v82, v82, v153, v152
MM v[100:103],a[156:159],a[96:99],0
TR a[136:137],v230 offset:57344
TR a[138:139],v230 offset:59392
v_fma_f32 v83, v83, v153, v152
MM v[100:103],a[152:155],a[64:67],v[100:103]
v_fma_f32 v84, v84, v153, v152
v_fma_f32 v85, v85, v153, v152
MM v[100:103],a[148:151],a[32:35],v[100:103]
TR a[140:141],v230 offset:61440
TR a[142:143],v230 offset:63488
v_fma_f32 v86, v86, v153, v152
v_fma_f32 v87, v87, v153, v152
MM v[100:103],a[144:147],a[0:3],v[100:103]
s_waitcnt lgkmcnt(15)
buffer_load_dwordx4 v[180:183], v248, s[20:23], s92 offen offset:1024 sc1
CV v200,v80,v81
MM v[0:3],a[160:163],a[0:3],0
TR a[144:145],v231 offset:49152
TR a[146:147],v231 offset:51200
CV v201,v82,v83
CV v202,v84,v85
MM v[0:3],a[164:167],a[32:35],v[0:3]
CV v203,v86,v87
v_mul_u32_u24 v224, 5, v238
MM v[0:3],a[168:171],a[64:67],v[0:3]
TR a[148:149],v231 offset:53248
TR a[150:151],v231 offset:55296
MM v[0:3],a[172:175],a[96:99],v[0:3]
buffer_store_dwordx4 v[200:203], v248, s[20:23], s78 offen offset:2048 sc1
AR v88,a88
AR v89,a89
MM v[8:11],a[172:175],a[100:103],0
TR a[152:153],v231 offset:57344
TR a[154:155],v231 offset:59392
AR v90,a90
AR v91,a91
MM v[8:11],a[168:171],a[68:71],v[8:11]
AR v92,a92
AR v93,a93
MM v[8:11],a[164:167],a[36:39],v[8:11]
TR a[156:157],v231 offset:61440
TR a[158:159],v231 offset:63488
AR v94,a94
MM v[8:11],a[160:163],a[4:7],v[8:11]
s_waitcnt lgkmcnt(15)
buffer_load_dwordx4 v[196:199], v248, s[20:23], s92 offen offset:2048 sc1
AR v95,a95
v_fma_f32 v88, v88, v157, v156
s_barrier
MM v[12:15],a[176:179],a[4:7],0
TR a[160:161],v244 offset:8192
TR a[162:163],v244 offset:9216
v_fma_f32 v89, v89, v157, v156
v_fma_f32 v90, v90, v157, v156
MM v[12:15],a[180:183],a[36:39],v[12:15]
v_fma_f32 v91, v91, v157, v156
v_fma_f32 v92, v92, v157, v156
MM v[12:15],a[184:187],a[68:71],v[12:15]
TR a[164:165],v244 offset:10240
TR a[166:167],v244 offset:11264
v_fma_f32 v93, v93, v157, v156
MM v[12:15],a[188:191],a[100:103],v[12:15]
buffer_load_dwordx4 v[212:215], v248, s[20:23], s92 offen offset:3072 sc1
v_fma_f32 v94, v94, v157, v156
v_fma_f32 v95, v95, v157, v156
MM v[4:7],a[188:191],a[96:99],0
TR a[168:169],v244 offset:12288
TR a[170:171],v244 offset:13312
CV v204,v88,v89
MM v[4:7],a[184:187],a[64:67],v[4:7]
CV v205,v90,v91
CV v206,v92,v93
MM v[4:7],a[180:183],a[32:35],v[4:7]
TR a[172:173],v244 offset:14336
TR a[174:175],v244 offset:15360
CV v207,v94,v95
MM v[4:7],a[176:179],a[0:3],v[4:7]
s_waitcnt lgkmcnt(15)
buffer_store_dwordx4 v[204:207], v248, s[20:23], s79 offen offset:2048 sc1
MM v[32:35],a[128:131],a[0:3],0
TR a[176:177],v235 offset:8192
TR a[178:179],v235 offset:9216
AR v112,a112
AR v113,a113
MM v[32:35],a[132:135],a[32:35],v[32:35]
AR v114,a114
AR v115,a115
MM v[32:35],a[136:139],a[64:67],v[32:35]
TR a[180:181],v235 offset:10240
TR a[182:183],v235 offset:11264
AR v116,a116
MM v[32:35],a[140:143],a[96:99],v[32:35]
buffer_load_dwordx4 v240, s[24:27], s66 offen offset:1024 lds
AR v117,a117
AR v118,a118
MM v[40:43],a[140:143],a[100:103],0
s_add_u32 s66, s66, 9216
TR a[184:185],v235 offset:12288
TR a[186:187],v235 offset:13312
AR v119,a119
v_fma_f32 v112, v112, v153, v152
MM v[40:43],a[136:139],a[68:71],v[40:43]
v_fma_f32 v113, v113, v153, v152
v_fma_f32 v114, v114, v153, v152
MM v[40:43],a[132:135],a[36:39],v[40:43]
TR a[188:189],v235 offset:14336
TR a[190:191],v235 offset:15360
v_fma_f32 v115, v115, v153, v152
MM v[40:43],a[128:131],a[4:7],v[40:43]
s_waitcnt lgkmcnt(15)
buffer_load_dwordx4 v240, s[24:27], s66 offen offset:2048 lds
v_fma_f32 v116, v116, v153, v152
v_fma_f32 v117, v117, v153, v152
MM v[44:47],a[144:147],a[4:7],0
s_add_u32 s66, s66, 9216
TR a[128:129],v144 offset:0
TR a[130:131],v144 offset:2048
v_fma_f32 v118, v118, v153, v152
v_fma_f32 v119, v119, v153, v152
MM v[44:47],a[148:151],a[36:39],v[44:47]
CV v216,v112,v113
CV v217,v114,v115
MM v[44:47],a[152:155],a[68:71],v[44:47]
TR a[132:133],v144 offset:4096
TR a[134:135],v144 offset:6144
CV v218,v116,v117
MM v[44:47],a[156:159],a[100:103],v[44:47]
buffer_load_dwordx4 v240, s[24:27], s66 offen offset:3072 lds
CV v219,v118,v119
MM v[36:39],a[156:159],a[96:99],0
TR a[136:137],v144 offset:8192
TR a[138:139],v144 offset:10240
MM v[36:39],a[152:155],a[64:67],v[36:39]
s_xor_b32 s66, s94, 0
v_lshl_add_u32 v224, v250, 3, v224
v_add_u32 v224, s61, v224
MM v[36:39],a[148:151],a[32:35],v[36:39]
s_mul_i32 s66, s66, 163840
TR a[140:141],v144 offset:12288
TR a[142:143],v144 offset:14336
MM v[36:39],a[144:147],a[0:3],v[36:39]
s_waitcnt lgkmcnt(15)
buffer_store_dwordx4 v[216:219], v248, s[20:23], s78 offen offset:3072 sc1
AR v120,a120
AR v121,a121
AR v122,a122
AR v123,a123
AR v124,a124
s_waitcnt vmcnt(31)
MM v[128:131],a[160:163],a[8:11],v[128:131]
s_add_u32 s66, s66, s88
TR a[144:145],v145 offset:0
TR a[146:147],v145 offset:2048
AR v125,a125
AR v126,a126
MM v[128:131],a[164:167],a[40:43],v[128:131]
s_add_u32 m0, s89, 131072
AR v127,a127
v_fma_f32 v120, v120, v157, v156
MM v[128:131],a[168:171],a[72:75],v[128:131]
TR a[148:149],v145 offset:4096
TR a[150:151],v145 offset:6144
v_fma_f32 v121, v121, v157, v156
v_fma_f32 v122, v122, v157, v156
MM v[128:131],a[172:175],a[104:107],v[128:131]
s_waitcnt vmcnt(23)
buffer_load_dwordx4 v252, s[24:27], s66 offen offset:0 lds
v_fma_f32 v123, v123, v157, v156
MM v[136:139],a[172:175],a[108:111],v[136:139]
s_add_u32 s66, s66, 19456
TR a[152:153],v145 offset:8192
TR a[154:155],v145 offset:10240
v_fma_f32 v124, v124, v157, v156
v_fma_f32 v125, v125, v157, v156
MM v[136:139],a[168:171],a[76:79],v[136:139]
v_fma_f32 v126, v126, v157, v156
v_fma_f32 v127, v127, v157, v156
MM v[136:139],a[164:167],a[44:47],v[136:139]
TR a[156:157],v145 offset:12288
TR a[158:159],v145 offset:14336
CV v220,v120,v121
MM v[136:139],a[160:163],a[12:15],v[136:139]
s_waitcnt lgkmcnt(15)
buffer_load_dwordx4 v252, s[24:27], s66 offen offset:1024 lds
CV v221,v122,v123
CV v222,v124,v125
MM v[140:143],a[176:179],a[12:15],v[140:143]
TR a[160:161],v146 offset:0
TR a[162:163],v146 offset:2048
CV v223,v126,v127
MM v[140:143],a[180:183],a[44:47],v[140:143]
s_xor_b32 s66, s94, 0
v_mul_u32_u24 v254, 1280, v250
v_mul_u32_u24 v250, 33792, v253
MM v[140:143],a[184:187],a[76:79],v[140:143]
s_mul_i32 s66, s66, 163840
TR a[164:165],v146 offset:4096
TR a[166:167],v146 offset:6144
MM v[140:143],a[188:191],a[108:111],v[140:143]
s_add_u32 s66, s66, s87
buffer_store_dwordx4 v[220:223], v248, s[20:23], s79 offen offset:3072 sc1
MM v[132:135],a[188:191],a[104:107],v[132:135]
s_add_u32 m0, s65, 114688
TR a[168:169],v146 offset:8192
TR a[170:171],v146 offset:10240
MM v[132:135],a[184:187],a[72:75],v[132:135]
v_add_u32 v250, v250, v238
v_lshlrev_b32 v253, 4, v253
MM v[132:135],a[180:183],a[40:43],v[132:135]
TR a[172:173],v146 offset:12288
TR a[174:175],v146 offset:14336
MM v[132:135],a[176:179],a[8:11],v[132:135]
s_waitcnt lgkmcnt(15)
buffer_load_dwordx4 v240, s[24:27], s66 offen offset:0 lds
MM v[64:67],a[128:131],a[8:11],v[64:67]
s_xor_b32 s92, s49, 7
TR a[176:177],v147 offset:0
TR a[178:179],v147 offset:2048
MM v[64:67],a[132:135],a[40:43],v[64:67]
s_mul_i32 s92, s92, 17408
s_add_u32 s92, s83, s92
v_bfe_u32 v238, v248, 4, 3
MM v[64:67],a[136:139],a[72:75],v[64:67]
s_add_u32 s66, s66, 9216
TR a[180:181],v147 offset:4096
TR a[182:183],v147 offset:6144
MM v[64:67],a[140:143],a[104:107],v[64:67]
buffer_load_dwordx4 a[192:195], v248, s[20:23], s92 offen offset:0 sc1
MM v[72:75],a[140:143],a[108:111],v[72:75]
TR a[184:185],v147 offset:8192
TR a[186:187],v147 offset:10240
MM v[72:75],a[136:139],a[76:79],v[72:75]
v_lshl_add_u32 v254, v238, 3, v254
v_bfe_u32 v238, v248, 7, 1
MM v[72:75],a[132:135],a[44:47],v[72:75]
TR a[188:189],v147 offset:12288
TR a[190:191],v147 offset:14336
MM v[72:75],a[128:131],a[12:15],v[72:75]
s_waitcnt lgkmcnt(15)
buffer_load_dwordx4 a[208:211], v248, s[20:23], s92 offen offset:1024 sc1
MM v[76:79],a[144:147],a[12:15],v[76:79]
TR a[128:129],v228 offset:0
TR a[130:131],v228 offset:2048
MM v[76:79],a[148:151],a[44:47],v[76:79]
v_mul_u32_u24 v238, 5120, v238
v_add_u32 v254, v254, v238
MM v[76:79],a[152:155],a[76:79],v[76:79]
TR a[132:133],v228 offset:4096
TR a[134:135],v228 offset:6144
MM v[76:79],a[156:159],a[108:111],v[76:79]
buffer_load_dwordx4 a[224:227], v248, s[20:23], s92 offen offset:2048 sc1
MM v[68:71],a[156:159],a[104:107],v[68:71]
TR a[136:137],v228 offset:8192
TR a[138:139],v228 offset:10240
MM v[68:71],a[152:155],a[72:75],v[68:71]
v_lshrrev_b32 v238, 6, v248
v_mul_u32_u24 v225, 80, v238
MM v[68:71],a[148:151],a[40:43],v[68:71]
TR a[140:141],v228 offset:12288
TR a[142:143],v228 offset:14336
MM v[68:71],a[144:147],a[8:11],v[68:71]
s_waitcnt lgkmcnt(15)
buffer_load_dwordx4 a[240:243], v248, s[20:23], s92 offen offset:3072 sc1
MM v[96:99],a[160:163],a[8:11],v[96:99]
TR a[144:145],v229 offset:0
TR a[146:147],v229 offset:2048
MM v[96:99],a[164:167],a[40:43],v[96:99]
s_add_u32 s92, s92, 4096
v_add3_u32 v225, v225, v251, s61
v_mul_u32_u24 v238, 1280, v238
MM v[96:99],a[168:171],a[72:75],v[96:99]
TR a[148:149],v229 offset:4096
TR a[150:151],v229 offset:6144
MM v[96:99],a[172:175],a[104:107],v[96:99]
buffer_load_dwordx4 a[196:199], v248, s[20:23], s92 offen offset:0 sc1
MM v[104:107],a[172:175],a[108:111],v[104:107]
TR a[152:153],v229 offset:8192
TR a[154:155],v229 offset:10240
MM v[104:107],a[168:171],a[76:79],v[104:107]
v_add_u32 v251, v251, v238
MM v[104:107],a[164:167],a[44:47],v[104:107]
TR a[156:157],v229 offset:12288
TR a[158:159],v229 offset:14336
MM v[104:107],a[160:163],a[12:15],v[104:107]
s_waitcnt lgkmcnt(15)
buffer_load_dwordx4 a[212:215], v248, s[20:23], s92 offen offset:1024 sc1
MM v[108:111],a[176:179],a[12:15],v[108:111]
TR a[160:161],v230 offset:0
TR a[162:163],v230 offset:2048
MM v[108:111],a[180:183],a[44:47],v[108:111]
MM v[108:111],a[184:187],a[76:79],v[108:111]
TR a[164:165],v230 offset:4096
TR a[166:167],v230 offset:6144
MM v[108:111],a[188:191],a[108:111],v[108:111]
buffer_load_dwordx4 a[228:231], v248, s[20:23], s92 offen offset:2048 sc1
MM v[100:103],a[188:191],a[104:107],v[100:103]
TR a[168:169],v230 offset:8192
TR a[170:171],v230 offset:10240
MM v[100:103],a[184:187],a[72:75],v[100:103]
MM v[100:103],a[180:183],a[40:43],v[100:103]
TR a[172:173],v230 offset:12288
TR a[174:175],v230 offset:14336
MM v[100:103],a[176:179],a[8:11],v[100:103]
s_waitcnt lgkmcnt(15)
buffer_load_dwordx4 a[244:247], v248, s[20:23], s92 offen offset:3072 sc1
MM v[0:3],a[128:131],a[8:11],v[0:3]
TR a[176:177],v231 offset:0
TR a[178:179],v231 offset:2048
MM v[0:3],a[132:135],a[40:43],v[0:3]
MM v[0:3],a[136:139],a[72:75],v[0:3]
TR a[180:181],v231 offset:4096
TR a[182:183],v231 offset:6144
MM v[0:3],a[140:143],a[104:107],v[0:3]
buffer_load_dwordx4 v240, s[24:27], s66 offen offset:1024 lds
MM v[8:11],a[140:143],a[108:111],v[8:11]
s_add_u32 s66, s66, 9216
TR a[184:185],v231 offset:8192
TR a[186:187],v231 offset:10240
MM v[8:11],a[136:139],a[76:79],v[8:11]
MM v[8:11],a[132:135],a[44:47],v[8:11]
TR a[188:189],v231 offset:12288
TR a[190:191],v231 offset:14336
MM v[8:11],a[128:131],a[12:15],v[8:11]
s_waitcnt lgkmcnt(15)
buffer_load_dwordx4 v240, s[24:27], s66 offen offset:2048 lds
s_barrier
MM v[12:15],a[144:147],a[12:15],v[12:15]
s_add_u32 s66, s66, 9216
TR a[128:129],v244 offset:16384
TR a[130:131],v244 offset:17408
)ASM"
R"ASM(
MM v[12:15],a[148:151],a[44:47],v[12:15]
LPG27_%=:
s_cbranch_execz LTR1_%=
MM v[12:15],a[152:155],a[76:79],v[12:15]
TR a[132:133],v244 offset:18432
TR a[134:135],v244 offset:19456
MM v[12:15],a[156:159],a[108:111],v[12:15]
buffer_load_dwordx4 v240, s[24:27], s66 offen offset:3072 lds
MM v[4:7],a[156:159],a[104:107],v[4:7]
TR a[136:137],v244 offset:20480
TR a[138:139],v244 offset:21504
MM v[4:7],a[152:155],a[72:75],v[4:7]
s_xor_b32 s66, s94, 0
s_mul_i32 s66, s66, 163840
s_add_u32 s66, s66, s86
MM v[4:7],a[148:151],a[40:43],v[4:7]
s_add_u32 m0, s65, 49152
TR a[140:141],v244 offset:22528
TR a[142:143],v244 offset:23552
MM v[4:7],a[144:147],a[8:11],v[4:7]
s_waitcnt lgkmcnt(15)
buffer_load_dwordx4 v240, s[24:27], s66 offen offset:0 lds
MM v[32:35],a[160:163],a[8:11],v[32:35]
s_add_u32 s66, s66, 9216
TR a[144:145],v235 offset:16384
TR a[146:147],v235 offset:17408
MM v[32:35],a[164:167],a[40:43],v[32:35]
MM v[32:35],a[168:171],a[72:75],v[32:35]
TR a[148:149],v235 offset:18432
TR a[150:151],v235 offset:19456
MM v[32:35],a[172:175],a[104:107],v[32:35]
buffer_load_dwordx4 v240, s[24:27], s66 offen offset:1024 lds
MM v[40:43],a[172:175],a[108:111],v[40:43]
s_add_u32 s66, s66, 9216
TR a[152:153],v235 offset:20480
TR a[154:155],v235 offset:21504
MM v[40:43],a[168:171],a[76:79],v[40:43]
MM v[40:43],a[164:167],a[44:47],v[40:43]
TR a[156:157],v235 offset:22528
TR a[158:159],v235 offset:23552
MM v[40:43],a[160:163],a[12:15],v[40:43]
s_waitcnt lgkmcnt(15)
buffer_load_dwordx4 v240, s[24:27], s66 offen offset:2048 lds
MM v[44:47],a[176:179],a[12:15],v[44:47]
s_lshl_b32 s97, s84, 2
TR a[160:161],v144 offset:16384
TR a[162:163],v144 offset:18432
MM v[44:47],a[180:183],a[44:47],v[44:47]
s_add_u32 s97, s97, s96
s_add_u32 s97, s97, 16
s_waitcnt vmcnt(15)
MM v[44:47],a[184:187],a[76:79],v[44:47]
s_add_u32 s66, s66, 9216
TR a[164:165],v144 offset:20480
TR a[166:167],v144 offset:22528
MM v[44:47],a[188:191],a[108:111],v[44:47]
v_mov_b32 v242, s69
s_mov_b64 exec, 1
buffer_store_dword v242, off, s[20:23], s97 offset:0 sc1
s_mov_b64 exec, -1
MM v[36:39],a[188:191],a[104:107],v[36:39]
TR a[168:169],v144 offset:24576
TR a[170:171],v144 offset:26624
MM v[36:39],a[184:187],a[72:75],v[36:39]
buffer_load_dwordx4 v240, s[24:27], s66 offen offset:3072 lds
MM v[36:39],a[180:183],a[40:43],v[36:39]
TR a[172:173],v144 offset:28672
TR a[174:175],v144 offset:30720
MM v[36:39],a[176:179],a[8:11],v[36:39]
s_waitcnt lgkmcnt(15)
s_xor_b32 s66, s94, 1
s_mul_i32 s66, s66, 163840
MM v[128:131],a[128:131],v[160:163],v[128:131]
s_add_u32 s66, s66, s88
TR a[176:177],v145 offset:16384
TR a[178:179],v145 offset:18432
MM v[128:131],a[132:135],v[176:179],v[128:131]
s_add_u32 m0, s89, 139264
MM v[128:131],a[136:139],v[192:195],v[128:131]
TR a[180:181],v145 offset:20480
TR a[182:183],v145 offset:22528
MM v[128:131],a[140:143],v[208:211],v[128:131]
buffer_load_dwordx4 v252, s[24:27], s66 offen offset:0 lds
MM v[136:139],a[140:143],v[212:215],v[136:139]
s_add_u32 s66, s66, 19456
TR a[184:185],v145 offset:24576
TR a[186:187],v145 offset:26624
MM v[136:139],a[136:139],v[196:199],v[136:139]
MM v[136:139],a[132:135],v[180:183],v[136:139]
TR a[188:189],v145 offset:28672
TR a[190:191],v145 offset:30720
MM v[136:139],a[128:131],v[164:167],v[136:139]
s_waitcnt lgkmcnt(15)
buffer_load_dwordx4 v252, s[24:27], s66 offen offset:1024 lds
MM v[140:143],a[144:147],v[164:167],v[140:143]
TR a[128:129],v146 offset:16384
TR a[130:131],v146 offset:18432
MM v[140:143],a[148:151],v[180:183],v[140:143]
s_xor_b32 s66, s94, 1
s_mul_i32 s66, s66, 163840
s_add_u32 s66, s66, s87
MM v[140:143],a[152:155],v[196:199],v[140:143]
s_add_u32 m0, s65, 65536
TR a[132:133],v146 offset:20480
TR a[134:135],v146 offset:22528
MM v[140:143],a[156:159],v[212:215],v[140:143]
buffer_load_dwordx4 v240, s[24:27], s66 offen offset:0 lds
MM v[132:135],a[156:159],v[208:211],v[132:135]
s_add_u32 s66, s66, 9216
TR a[136:137],v146 offset:24576
TR a[138:139],v146 offset:26624
MM v[132:135],a[152:155],v[192:195],v[132:135]
MM v[132:135],a[148:151],v[176:179],v[132:135]
TR a[140:141],v146 offset:28672
TR a[142:143],v146 offset:30720
MM v[132:135],a[144:147],v[160:163],v[132:135]
s_waitcnt lgkmcnt(15)
buffer_load_dwordx4 v240, s[24:27], s66 offen offset:1024 lds
MM v[64:67],a[160:163],v[160:163],v[64:67]
s_add_u32 s66, s66, 9216
TR a[144:145],v147 offset:16384
TR a[146:147],v147 offset:18432
MM v[64:67],a[164:167],v[176:179],v[64:67]
MM v[64:67],a[168:171],v[192:195],v[64:67]
TR a[148:149],v147 offset:20480
TR a[150:151],v147 offset:22528
MM v[64:67],a[172:175],v[208:211],v[64:67]
buffer_load_dwordx4 v240, s[24:27], s66 offen offset:2048 lds
MM v[72:75],a[172:175],v[212:215],v[72:75]
s_add_u32 s66, s66, 9216
TR a[152:153],v147 offset:24576
TR a[154:155],v147 offset:26624
MM v[72:75],a[168:171],v[196:199],v[72:75]
MM v[72:75],a[164:167],v[180:183],v[72:75]
TR a[156:157],v147 offset:28672
TR a[158:159],v147 offset:30720
MM v[72:75],a[160:163],v[164:167],v[72:75]
s_waitcnt lgkmcnt(15)
buffer_load_dwordx4 v240, s[24:27], s66 offen offset:3072 lds
MM v[76:79],a[176:179],v[164:167],v[76:79]
TR a[160:161],v228 offset:16384
TR a[162:163],v228 offset:18432
MM v[76:79],a[180:183],v[180:183],v[76:79]
MM v[76:79],a[184:187],v[196:199],v[76:79]
TR a[164:165],v228 offset:20480
TR a[166:167],v228 offset:22528
MM v[76:79],a[188:191],v[212:215],v[76:79]
MM v[68:71],a[188:191],v[208:211],v[68:71]
TR a[168:169],v228 offset:24576
TR a[170:171],v228 offset:26624
MM v[68:71],a[184:187],v[192:195],v[68:71]
MM v[68:71],a[180:183],v[176:179],v[68:71]
TR a[172:173],v228 offset:28672
TR a[174:175],v228 offset:30720
MM v[68:71],a[176:179],v[160:163],v[68:71]
s_waitcnt lgkmcnt(15)
MM v[96:99],a[128:131],v[160:163],v[96:99]
s_add_u32 s80, s18, 131072
TR a[176:177],v229 offset:16384
TR a[178:179],v229 offset:18432
MM v[96:99],a[132:135],v[176:179],v[96:99]
buffer_load_dwordx4 v[16:19], v251, s[20:23], s80 offen offset:0 sc1
MM v[96:99],a[136:139],v[192:195],v[96:99]
TR a[180:181],v229 offset:20480
TR a[182:183],v229 offset:22528
MM v[96:99],a[140:143],v[208:211],v[96:99]
v_mov_b32 v237, 0x3f80
v_and_b32 v236, 16, v248
MM v[104:107],a[140:143],v[212:215],v[104:107]
TR a[184:185],v229 offset:24576
TR a[186:187],v229 offset:26624
MM v[104:107],a[136:139],v[196:199],v[104:107]
s_add_u32 s80, s18, 151552
buffer_load_dwordx4 v[20:23], v251, s[20:23], s80 offen offset:0 sc1
MM v[104:107],a[132:135],v[180:183],v[104:107]
TR a[188:189],v229 offset:28672
TR a[190:191],v229 offset:30720
MM v[104:107],a[128:131],v[164:167],v[104:107]
s_waitcnt lgkmcnt(15)
v_lshlrev_b32 v236, v236, v237
s_mov_b32 s28, 0x30003
MM v[108:111],a[144:147],v[164:167],v[108:111]
s_mov_b32 s29, s28
TR a[128:129],v230 offset:16384
TR a[130:131],v230 offset:18432
MM v[108:111],a[148:151],v[180:183],v[108:111]
s_add_u32 s80, s63, 131072
v_lshrrev_b32 v243, 2, v248
v_min_u32 v243, 12, v243
MM v[108:111],a[152:155],v[196:199],v[108:111]
TR a[132:133],v230 offset:20480
TR a[134:135],v230 offset:22528
MM v[108:111],a[156:159],v[212:215],v[108:111]
buffer_load_dword v242, v243, s[20:23], s96 offen offset:16 sc1
buffer_load_dwordx2 v[150:151], v254, s[20:23], s80 offen offset:0 sc1
MM v[100:103],a[156:159],v[208:211],v[100:103]
TR a[136:137],v230 offset:24576
TR a[138:139],v230 offset:26624
MM v[100:103],a[152:155],v[192:195],v[100:103]
v_cndmask_b32 v48, 0, v236, s[28:29]
s_lshl_b64 s[28:29], s[28:29], 2
MM v[100:103],a[148:151],v[176:179],v[100:103]
TR a[140:141],v230 offset:28672
TR a[142:143],v230 offset:30720
MM v[100:103],a[144:147],v[160:163],v[100:103]
s_waitcnt lgkmcnt(15)
v_cndmask_b32 v49, 0, v236, s[28:29]
s_lshl_b64 s[28:29], s[28:29], 2
MM v[0:3],a[160:163],v[160:163],v[0:3]
TR a[144:145],v231 offset:16384
TR a[146:147],v231 offset:18432
MM v[0:3],a[164:167],v[176:179],v[0:3]
v_cndmask_b32 v50, 0, v236, s[28:29]
s_lshl_b64 s[28:29], s[28:29], 2
MM v[0:3],a[168:171],v[192:195],v[0:3]
TR a[148:149],v231 offset:20480
TR a[150:151],v231 offset:22528
MM v[0:3],a[172:175],v[208:211],v[0:3]
v_cndmask_b32 v51, 0, v236, s[28:29]
s_lshl_b64 s[28:29], s[28:29], 2
MM v[8:11],a[172:175],v[212:215],v[8:11]
TR a[152:153],v231 offset:24576
TR a[154:155],v231 offset:26624
MM v[8:11],a[168:171],v[196:199],v[8:11]
v_cndmask_b32 v52, 0, v236, s[28:29]
s_lshl_b64 s[28:29], s[28:29], 2
MM v[8:11],a[164:167],v[180:183],v[8:11]
TR a[156:157],v231 offset:28672
TR a[158:159],v231 offset:30720
MM v[8:11],a[160:163],v[164:167],v[8:11]
s_waitcnt lgkmcnt(15)
v_cndmask_b32 v53, 0, v236, s[28:29]
s_lshl_b64 s[28:29], s[28:29], 2
s_barrier
MM v[12:15],a[176:179],v[164:167],v[12:15]
s_xor_b32 s66, s94, 2
TR a[160:161],v244 offset:24576
TR a[162:163],v244 offset:25600
MM v[12:15],a[180:183],v[180:183],v[12:15]
s_mul_i32 s66, s66, 163840
v_cndmask_b32 v54, 0, v236, s[28:29]
s_lshl_b64 s[28:29], s[28:29], 2
MM v[12:15],a[184:187],v[196:199],v[12:15]
s_add_u32 s66, s66, s88
TR a[164:165],v244 offset:26624
TR a[166:167],v244 offset:27648
MM v[12:15],a[188:191],v[212:215],v[12:15]
s_add_u32 m0, s89, 147456
v_cndmask_b32 v55, 0, v236, s[28:29]
s_mov_b32 s28, 0x300003
MM v[4:7],a[188:191],v[208:211],v[4:7]
s_mov_b32 s29, 0x30000300
TR a[168:169],v244 offset:28672
TR a[170:171],v244 offset:29696
MM v[4:7],a[184:187],v[192:195],v[4:7]
buffer_load_dwordx4 v252, s[24:27], s66 offen offset:0 lds
MM v[4:7],a[180:183],v[176:179],v[4:7]
s_add_u32 s66, s66, 19456
TR a[172:173],v244 offset:30720
TR a[174:175],v244 offset:31744
MM v[4:7],a[176:179],v[160:163],v[4:7]
s_waitcnt lgkmcnt(15)
v_mov_b32 v56, 0
v_mov_b32 v57, 0
MM v[32:35],a[128:131],v[160:163],v[32:35]
TR a[176:177],v235 offset:24576
TR a[178:179],v235 offset:25600
MM v[32:35],a[132:135],v[176:179],v[32:35]
buffer_load_dwordx4 v252, s[24:27], s66 offen offset:1024 lds
MM v[32:35],a[136:139],v[192:195],v[32:35]
TR a[180:181],v235 offset:26624
TR a[182:183],v235 offset:27648
MM v[32:35],a[140:143],v[208:211],v[32:35]
s_xor_b32 s66, s94, 2
v_mov_b32 v60, 0
v_mov_b32 v61, 0
MM v[40:43],a[140:143],v[212:215],v[40:43]
s_mul_i32 s66, s66, 163840
TR a[184:185],v235 offset:28672
TR a[186:187],v235 offset:29696
MM v[40:43],a[136:139],v[196:199],v[40:43]
s_add_u32 s66, s66, s87
v_cndmask_b32 v58, 0, v236, s[28:29]
s_lshl_b64 s[28:29], s[28:29], 2
MM v[40:43],a[132:135],v[180:183],v[40:43]
s_add_u32 m0, s65, 81920
TR a[188:189],v235 offset:30720
TR a[190:191],v235 offset:31744
MM v[40:43],a[128:131],v[164:167],v[40:43]
s_waitcnt lgkmcnt(15)
buffer_load_dwordx4 v240, s[24:27], s66 offen offset:0 lds
MM v[44:47],a[144:147],v[164:167],v[44:47]
s_add_u32 s66, s66, 9216
TR a[128:129],v144 offset:32768
TR a[130:131],v144 offset:34816
MM v[44:47],a[148:151],v[180:183],v[44:47]
v_cndmask_b32 v59, 0, v236, s[28:29]
MM v[44:47],a[152:155],v[196:199],v[44:47]
TR a[132:133],v144 offset:36864
TR a[134:135],v144 offset:38912
MM v[44:47],a[156:159],v[212:215],v[44:47]
buffer_load_dwordx4 v240, s[24:27], s66 offen offset:1024 lds
MM v[36:39],a[156:159],v[208:211],v[36:39]
s_add_u32 s66, s66, 9216
TR a[136:137],v144 offset:40960
TR a[138:139],v144 offset:43008
MM v[36:39],a[152:155],v[192:195],v[36:39]
MM v[36:39],a[148:151],v[176:179],v[36:39]
TR a[140:141],v144 offset:45056
TR a[142:143],v144 offset:47104
MM v[36:39],a[144:147],v[160:163],v[36:39]
s_waitcnt lgkmcnt(15)
buffer_load_dwordx4 v240, s[24:27], s66 offen offset:2048 lds
s_waitcnt vmcnt(27)
MM v[128:131],a[160:163],a[192:195],v[128:131]
s_add_u32 s80, s18, 0
TR a[144:145],v145 offset:32768
TR a[146:147],v145 offset:34816
MM v[128:131],a[164:167],a[208:211],v[128:131]
s_add_u32 s66, s66, 9216
buffer_load_dwordx4 v[24:27], v251, s[32:35], s80 offen offset:0
MM v[128:131],a[168:171],a[224:227],v[128:131]
TR a[148:149],v145 offset:36864
TR a[150:151],v145 offset:38912
MM v[128:131],a[172:175],a[240:243],v[128:131]
s_waitcnt vmcnt(24)
MM v[136:139],a[172:175],a[244:247],v[136:139]
TR a[152:153],v145 offset:40960
TR a[154:155],v145 offset:43008
MM v[136:139],a[168:171],a[228:231],v[136:139]
s_add_u32 s80, s18, 20480
buffer_load_dwordx4 v[28:31], v251, s[32:35], s80 offen offset:0
MM v[136:139],a[164:167],a[212:215],v[136:139]
)ASM"
R"ASM(
TR a[156:157],v145 offset:45056
TR a[158:159],v145 offset:47104
MM v[136:139],a[160:163],a[196:199],v[136:139]
s_waitcnt lgkmcnt(15)
MM v[140:143],a[176:179],a[196:199],v[140:143]
TR a[160:161],v146 offset:32768
TR a[162:163],v146 offset:34816
MM v[140:143],a[180:183],a[212:215],v[140:143]
s_add_u32 s80, s63, 0
buffer_load_dwordx2 v[62:63], v254, s[32:35], s80 offen offset:0
MM v[140:143],a[184:187],a[228:231],v[140:143]
TR a[164:165],v146 offset:36864
TR a[166:167],v146 offset:38912
MM v[140:143],a[188:191],a[244:247],v[140:143]
buffer_load_dwordx4 v240, s[24:27], s66 offen offset:3072 lds
MM v[132:135],a[188:191],a[240:243],v[132:135]
TR a[168:169],v146 offset:40960
TR a[170:171],v146 offset:43008
MM v[132:135],a[184:187],a[224:227],v[132:135]
MM v[132:135],a[180:183],a[208:211],v[132:135]
TR a[172:173],v146 offset:45056
TR a[174:175],v146 offset:47104
MM v[132:135],a[176:179],a[192:195],v[132:135]
s_waitcnt lgkmcnt(15)
MM v[64:67],a[128:131],a[192:195],v[64:67]
s_waitcnt vmcnt(10)
TR a[176:177],v147 offset:32768
TR a[178:179],v147 offset:34816
MM v[64:67],a[132:135],a[208:211],v[64:67]
v_cmp_ne_u32 vcc, s69, v242
s_cbranch_vccnz LRPO1_%=
LRPO1b_%=:
MM v[64:67],a[136:139],a[224:227],v[64:67]
s_xor_b32 s92, s49, 1
TR a[180:181],v147 offset:36864
TR a[182:183],v147 offset:38912
MM v[64:67],a[140:143],a[240:243],v[64:67]
s_mul_i32 s92, s92, 17408
s_add_u32 s92, s83, s92
s_add_u32 s92, s92, 8192
MM v[72:75],a[140:143],a[244:247],v[72:75]
TR a[184:185],v147 offset:40960
TR a[186:187],v147 offset:43008
MM v[72:75],a[136:139],a[228:231],v[72:75]
buffer_load_dwordx4 a[16:19], v248, s[20:23], s92 offen offset:0 sc1
MM v[72:75],a[132:135],a[212:215],v[72:75]
TR a[188:189],v147 offset:45056
TR a[190:191],v147 offset:47104
MM v[72:75],a[128:131],a[196:199],v[72:75]
s_waitcnt lgkmcnt(15)
buffer_load_dwordx4 a[48:51], v248, s[20:23], s92 offen offset:1024 sc1
MM v[76:79],a[144:147],a[196:199],v[76:79]
TR a[128:129],v228 offset:32768
TR a[130:131],v228 offset:34816
MM v[76:79],a[148:151],a[212:215],v[76:79]
MM v[76:79],a[152:155],a[228:231],v[76:79]
TR a[132:133],v228 offset:36864
TR a[134:135],v228 offset:38912
MM v[76:79],a[156:159],a[244:247],v[76:79]
buffer_load_dwordx4 a[80:83], v248, s[20:23], s92 offen offset:2048 sc1
MM v[68:71],a[156:159],a[240:243],v[68:71]
TR a[136:137],v228 offset:40960
TR a[138:139],v228 offset:43008
MM v[68:71],a[152:155],a[224:227],v[68:71]
MM v[68:71],a[148:151],a[208:211],v[68:71]
TR a[140:141],v228 offset:45056
TR a[142:143],v228 offset:47104
MM v[68:71],a[144:147],a[192:195],v[68:71]
s_waitcnt lgkmcnt(15)
buffer_load_dwordx4 a[112:115], v248, s[20:23], s92 offen offset:3072 sc1
MM v[96:99],a[160:163],a[192:195],v[96:99]
s_add_u32 s92, s92, 4096
TR a[144:145],v229 offset:32768
TR a[146:147],v229 offset:34816
MM v[96:99],a[164:167],a[208:211],v[96:99]
MM v[96:99],a[168:171],a[224:227],v[96:99]
TR a[148:149],v229 offset:36864
TR a[150:151],v229 offset:38912
MM v[96:99],a[172:175],a[240:243],v[96:99]
buffer_load_dwordx4 a[20:23], v248, s[20:23], s92 offen offset:0 sc1
MM v[104:107],a[172:175],a[244:247],v[104:107]
TR a[152:153],v229 offset:40960
TR a[154:155],v229 offset:43008
MM v[104:107],a[168:171],a[228:231],v[104:107]
MM v[104:107],a[164:167],a[212:215],v[104:107]
TR a[156:157],v229 offset:45056
TR a[158:159],v229 offset:47104
MM v[104:107],a[160:163],a[196:199],v[104:107]
s_waitcnt lgkmcnt(15)
buffer_load_dwordx4 a[52:55], v248, s[20:23], s92 offen offset:1024 sc1
MM v[108:111],a[176:179],a[196:199],v[108:111]
TR a[160:161],v230 offset:32768
TR a[162:163],v230 offset:34816
MM v[108:111],a[180:183],a[212:215],v[108:111]
MM v[108:111],a[184:187],a[228:231],v[108:111]
TR a[164:165],v230 offset:36864
TR a[166:167],v230 offset:38912
MM v[108:111],a[188:191],a[244:247],v[108:111]
buffer_load_dwordx4 a[84:87], v248, s[20:23], s92 offen offset:2048 sc1
MM v[100:103],a[188:191],a[240:243],v[100:103]
TR a[168:169],v230 offset:40960
TR a[170:171],v230 offset:43008
MM v[100:103],a[184:187],a[224:227],v[100:103]
MM v[100:103],a[180:183],a[208:211],v[100:103]
TR a[172:173],v230 offset:45056
TR a[174:175],v230 offset:47104
MM v[100:103],a[176:179],a[192:195],v[100:103]
s_waitcnt lgkmcnt(15)
buffer_load_dwordx4 a[116:119], v248, s[20:23], s92 offen offset:3072 sc1
MM v[0:3],a[128:131],a[192:195],v[0:3]
TR a[176:177],v231 offset:32768
TR a[178:179],v231 offset:34816
MM v[0:3],a[132:135],a[208:211],v[0:3]
MM v[0:3],a[136:139],a[224:227],v[0:3]
TR a[180:181],v231 offset:36864
TR a[182:183],v231 offset:38912
MM v[0:3],a[140:143],a[240:243],v[0:3]
MM v[8:11],a[140:143],a[244:247],v[8:11]
TR a[184:185],v231 offset:40960
TR a[186:187],v231 offset:43008
MM v[8:11],a[136:139],a[228:231],v[8:11]
MM v[8:11],a[132:135],a[212:215],v[8:11]
TR a[188:189],v231 offset:45056
TR a[190:191],v231 offset:47104
MM v[8:11],a[128:131],a[196:199],v[8:11]
s_waitcnt lgkmcnt(15)
s_barrier
MM v[12:15],a[144:147],a[196:199],v[12:15]
s_xor_b32 s66, s94, 3
TR a[128:129],v244 offset:0
TR a[130:131],v244 offset:1024
MM v[12:15],a[148:151],a[212:215],v[12:15]
s_mul_i32 s66, s66, 163840
s_add_u32 s66, s66, s88
s_add_u32 m0, s89, 155648
MM v[12:15],a[152:155],a[228:231],v[12:15]
TR a[132:133],v244 offset:2048
TR a[134:135],v244 offset:3072
MM v[12:15],a[156:159],a[244:247],v[12:15]
buffer_load_dwordx4 v252, s[24:27], s66 offen offset:0 lds
MM v[4:7],a[156:159],a[240:243],v[4:7]
s_add_u32 s66, s66, 19456
TR a[136:137],v244 offset:4096
TR a[138:139],v244 offset:5120
MM v[4:7],a[152:155],a[224:227],v[4:7]
MM v[4:7],a[148:151],a[208:211],v[4:7]
TR a[140:141],v244 offset:6144
TR a[142:143],v244 offset:7168
MM v[4:7],a[144:147],a[192:195],v[4:7]
s_waitcnt lgkmcnt(15)
buffer_load_dwordx4 v252, s[24:27], s66 offen offset:1024 lds
MM v[32:35],a[160:163],a[192:195],v[32:35]
TR a[144:145],v235 offset:0
TR a[146:147],v235 offset:1024
MM v[32:35],a[164:167],a[208:211],v[32:35]
s_xor_b32 s66, s94, 3
s_mul_i32 s66, s66, 163840
s_add_u32 s66, s66, s87
MM v[32:35],a[168:171],a[224:227],v[32:35]
s_add_u32 m0, s65, 98304
TR a[148:149],v235 offset:2048
TR a[150:151],v235 offset:3072
MM v[32:35],a[172:175],a[240:243],v[32:35]
buffer_load_dwordx4 v240, s[24:27], s66 offen offset:0 lds
MM v[40:43],a[172:175],a[244:247],v[40:43]
s_add_u32 s66, s66, 9216
TR a[152:153],v235 offset:4096
TR a[154:155],v235 offset:5120
MM v[40:43],a[168:171],a[228:231],v[40:43]
MM v[40:43],a[164:167],a[212:215],v[40:43]
TR a[156:157],v235 offset:6144
TR a[158:159],v235 offset:7168
MM v[40:43],a[160:163],a[196:199],v[40:43]
s_waitcnt lgkmcnt(15)
buffer_load_dwordx4 v240, s[24:27], s66 offen offset:1024 lds
MM v[44:47],a[176:179],a[196:199],v[44:47]
s_add_u32 s66, s66, 9216
TR a[160:161],v144 offset:49152
TR a[162:163],v144 offset:51200
MM v[44:47],a[180:183],a[212:215],v[44:47]
MM v[44:47],a[184:187],a[228:231],v[44:47]
TR a[164:165],v144 offset:53248
TR a[166:167],v144 offset:55296
MM v[44:47],a[188:191],a[244:247],v[44:47]
buffer_load_dwordx4 v240, s[24:27], s66 offen offset:2048 lds
MM v[36:39],a[188:191],a[240:243],v[36:39]
s_add_u32 s66, s66, 9216
TR a[168:169],v144 offset:57344
TR a[170:171],v144 offset:59392
MM v[36:39],a[184:187],a[224:227],v[36:39]
MM v[36:39],a[180:183],a[208:211],v[36:39]
TR a[172:173],v144 offset:61440
TR a[174:175],v144 offset:63488
MM v[36:39],a[176:179],a[192:195],v[36:39]
s_waitcnt lgkmcnt(15)
buffer_load_dwordx4 v240, s[24:27], s66 offen offset:3072 lds
MM v[128:131],a[128:131],v[168:171],v[128:131]
TR a[176:177],v145 offset:49152
TR a[178:179],v145 offset:51200
MM v[128:131],a[132:135],v[184:187],v[128:131]
MM v[128:131],a[136:139],v[200:203],v[128:131]
TR a[180:181],v145 offset:53248
TR a[182:183],v145 offset:55296
MM v[128:131],a[140:143],v[216:219],v[128:131]
MM v[136:139],a[140:143],v[220:223],v[136:139]
TR a[184:185],v145 offset:57344
TR a[186:187],v145 offset:59392
MM v[136:139],a[136:139],v[204:207],v[136:139]
MM v[136:139],a[132:135],v[188:191],v[136:139]
TR a[188:189],v145 offset:61440
TR a[190:191],v145 offset:63488
MM v[136:139],a[128:131],v[172:175],v[136:139]
s_waitcnt lgkmcnt(15)
MM v[140:143],a[144:147],v[172:175],v[140:143]
s_xor_b32 s92, s49, 2
TR a[128:129],v146 offset:49152
TR a[130:131],v146 offset:51200
MM v[140:143],a[148:151],v[188:191],v[140:143]
s_mul_i32 s92, s92, 17408
s_add_u32 s92, s83, s92
LPG28_%=:
s_cbranch_execz LTR1_%=
s_add_u32 s92, s92, 8192
MM v[140:143],a[152:155],v[204:207],v[140:143]
TR a[132:133],v146 offset:53248
TR a[134:135],v146 offset:55296
MM v[140:143],a[156:159],v[220:223],v[140:143]
buffer_load_dwordx4 a[24:27], v248, s[20:23], s92 offen offset:0 sc1
MM v[132:135],a[156:159],v[216:219],v[132:135]
TR a[136:137],v146 offset:57344
TR a[138:139],v146 offset:59392
MM v[132:135],a[152:155],v[200:203],v[132:135]
buffer_load_dwordx4 a[56:59], v248, s[20:23], s92 offen offset:1024 sc1
MM v[132:135],a[148:151],v[184:187],v[132:135]
TR a[140:141],v146 offset:61440
TR a[142:143],v146 offset:63488
MM v[132:135],a[144:147],v[168:171],v[132:135]
s_waitcnt lgkmcnt(15)
MM v[64:67],a[160:163],v[168:171],v[64:67]
TR a[144:145],v147 offset:49152
TR a[146:147],v147 offset:51200
MM v[64:67],a[164:167],v[184:187],v[64:67]
buffer_load_dwordx4 a[88:91], v248, s[20:23], s92 offen offset:2048 sc1
MM v[64:67],a[168:171],v[200:203],v[64:67]
TR a[148:149],v147 offset:53248
TR a[150:151],v147 offset:55296
MM v[64:67],a[172:175],v[216:219],v[64:67]
MM v[72:75],a[172:175],v[220:223],v[72:75]
TR a[152:153],v147 offset:57344
TR a[154:155],v147 offset:59392
MM v[72:75],a[168:171],v[204:207],v[72:75]
buffer_load_dwordx4 a[120:123], v248, s[20:23], s92 offen offset:3072 sc1
MM v[72:75],a[164:167],v[188:191],v[72:75]
s_add_u32 s92, s92, 4096
TR a[156:157],v147 offset:61440
TR a[158:159],v147 offset:63488
MM v[72:75],a[160:163],v[172:175],v[72:75]
s_waitcnt lgkmcnt(15)
MM v[76:79],a[176:179],v[172:175],v[76:79]
TR a[160:161],v230 offset:49152
TR a[162:163],v230 offset:51200
MM v[76:79],a[180:183],v[188:191],v[76:79]
buffer_load_dwordx4 a[28:31], v248, s[20:23], s92 offen offset:0 sc1
MM v[76:79],a[184:187],v[204:207],v[76:79]
TR a[164:165],v230 offset:53248
TR a[166:167],v230 offset:55296
MM v[76:79],a[188:191],v[220:223],v[76:79]
MM v[68:71],a[188:191],v[216:219],v[68:71]
TR a[168:169],v230 offset:57344
TR a[170:171],v230 offset:59392
MM v[68:71],a[184:187],v[200:203],v[68:71]
buffer_load_dwordx4 a[60:63], v248, s[20:23], s92 offen offset:1024 sc1
MM v[68:71],a[180:183],v[184:187],v[68:71]
TR a[172:173],v230 offset:61440
TR a[174:175],v230 offset:63488
MM v[68:71],a[176:179],v[168:171],v[68:71]
s_waitcnt lgkmcnt(15)
MM v[96:99],a[128:131],v[168:171],v[96:99]
TR a[176:177],v231 offset:49152
TR a[178:179],v231 offset:51200
MM v[96:99],a[132:135],v[184:187],v[96:99]
buffer_load_dwordx4 a[92:95], v248, s[20:23], s92 offen offset:2048 sc1
MM v[96:99],a[136:139],v[200:203],v[96:99]
TR a[180:181],v231 offset:53248
TR a[182:183],v231 offset:55296
MM v[96:99],a[140:143],v[216:219],v[96:99]
MM v[104:107],a[140:143],v[220:223],v[104:107]
TR a[184:185],v231 offset:57344
TR a[186:187],v231 offset:59392
MM v[104:107],a[136:139],v[204:207],v[104:107]
buffer_load_dwordx4 a[124:127], v248, s[20:23], s92 offen offset:3072 sc1
MM v[104:107],a[132:135],v[188:191],v[104:107]
s_xor_b32 s92, s49, 3
TR a[188:189],v231 offset:61440
TR a[190:191],v231 offset:63488
MM v[104:107],a[128:131],v[172:175],v[104:107]
s_waitcnt lgkmcnt(15)
s_mul_i32 s92, s92, 17408
s_add_u32 s92, s83, s92
s_barrier
MM v[108:111],a[144:147],v[172:175],v[108:111]
s_add_u32 s92, s92, 8192
TR a[128:129],v244 offset:8192
TR a[130:131],v244 offset:9216
MM v[108:111],a[148:151],v[188:191],v[108:111]
buffer_load_dwordx4 a[200:203], v248, s[20:23], s92 offen offset:0 sc1
MM v[108:111],a[152:155],v[204:207],v[108:111]
TR a[132:133],v244 offset:10240
TR a[134:135],v244 offset:11264
MM v[108:111],a[156:159],v[220:223],v[108:111]
MM v[100:103],a[156:159],v[216:219],v[100:103]
TR a[136:137],v244 offset:12288
TR a[138:139],v244 offset:13312
MM v[100:103],a[152:155],v[200:203],v[100:103]
buffer_load_dwordx4 a[216:219], v248, s[20:23], s92 offen offset:1024 sc1
MM v[100:103],a[148:151],v[184:187],v[100:103]
)ASM"
R"ASM(
TR a[140:141],v244 offset:14336
TR a[142:143],v244 offset:15360
MM v[100:103],a[144:147],v[168:171],v[100:103]
s_waitcnt lgkmcnt(15)
MM v[32:35],a[160:163],v[168:171],v[32:35]
TR a[144:145],v235 offset:8192
TR a[146:147],v235 offset:9216
MM v[32:35],a[164:167],v[184:187],v[32:35]
buffer_load_dwordx4 a[232:235], v248, s[20:23], s92 offen offset:2048 sc1
MM v[32:35],a[168:171],v[200:203],v[32:35]
TR a[148:149],v235 offset:10240
TR a[150:151],v235 offset:11264
MM v[32:35],a[172:175],v[216:219],v[32:35]
MM v[40:43],a[172:175],v[220:223],v[40:43]
TR a[152:153],v235 offset:12288
TR a[154:155],v235 offset:13312
MM v[40:43],a[168:171],v[204:207],v[40:43]
buffer_load_dwordx4 a[248:251], v248, s[20:23], s92 offen offset:3072 sc1
MM v[40:43],a[164:167],v[188:191],v[40:43]
s_add_u32 s92, s92, 4096
TR a[156:157],v235 offset:14336
TR a[158:159],v235 offset:15360
MM v[40:43],a[160:163],v[172:175],v[40:43]
s_waitcnt lgkmcnt(15)
MM v[44:47],a[176:179],v[172:175],v[44:47]
TR a[160:161],v144 offset:0
TR a[162:163],v144 offset:2048
MM v[44:47],a[180:183],v[188:191],v[44:47]
buffer_load_dwordx4 a[204:207], v248, s[20:23], s92 offen offset:0 sc1
MM v[44:47],a[184:187],v[204:207],v[44:47]
TR a[164:165],v144 offset:4096
TR a[166:167],v144 offset:6144
MM v[44:47],a[188:191],v[220:223],v[44:47]
MM v[36:39],a[188:191],v[216:219],v[36:39]
TR a[168:169],v144 offset:8192
TR a[170:171],v144 offset:10240
MM v[36:39],a[184:187],v[200:203],v[36:39]
buffer_load_dwordx4 a[220:223], v248, s[20:23], s92 offen offset:1024 sc1
MM v[36:39],a[180:183],v[184:187],v[36:39]
TR a[172:173],v144 offset:12288
TR a[174:175],v144 offset:14336
MM v[36:39],a[176:179],v[168:171],v[36:39]
s_waitcnt lgkmcnt(15)
s_waitcnt vmcnt(24)
MM v[128:131],a[128:131],a[16:19],v[128:131]
TR a[176:177],v145 offset:0
TR a[178:179],v145 offset:2048
MM v[128:131],a[132:135],a[48:51],v[128:131]
buffer_load_dwordx4 a[236:239], v248, s[20:23], s92 offen offset:2048 sc1
MM v[128:131],a[136:139],a[80:83],v[128:131]
TR a[180:181],v145 offset:4096
TR a[182:183],v145 offset:6144
MM v[128:131],a[140:143],a[112:115],v[128:131]
s_waitcnt vmcnt(21)
MM v[136:139],a[140:143],a[116:119],v[136:139]
TR a[184:185],v145 offset:8192
TR a[186:187],v145 offset:10240
MM v[136:139],a[136:139],a[84:87],v[136:139]
buffer_load_dwordx4 a[252:255], v248, s[20:23], s92 offen offset:3072 sc1
MM v[136:139],a[132:135],a[52:55],v[136:139]
TR a[188:189],v145 offset:12288
TR a[190:191],v145 offset:14336
MM v[136:139],a[128:131],a[20:23],v[136:139]
s_waitcnt lgkmcnt(15)
MM v[140:143],a[144:147],a[20:23],v[140:143]
TR a[128:129],v146 offset:0
TR a[130:131],v146 offset:2048
MM v[140:143],a[148:151],a[52:55],v[140:143]
MM v[140:143],a[152:155],a[84:87],v[140:143]
TR a[132:133],v146 offset:4096
TR a[134:135],v146 offset:6144
MM v[140:143],a[156:159],a[116:119],v[140:143]
MM v[132:135],a[156:159],a[112:115],v[132:135]
TR a[136:137],v146 offset:8192
TR a[138:139],v146 offset:10240
MM v[132:135],a[152:155],a[80:83],v[132:135]
MM v[132:135],a[148:151],a[48:51],v[132:135]
TR a[140:141],v146 offset:12288
TR a[142:143],v146 offset:14336
MM v[132:135],a[144:147],a[16:19],v[132:135]
s_waitcnt lgkmcnt(15)
MM v[64:67],a[160:163],a[16:19],v[64:67]
TR a[144:145],v147 offset:0
TR a[146:147],v147 offset:2048
MM v[64:67],a[164:167],a[48:51],v[64:67]
MM v[64:67],a[168:171],a[80:83],v[64:67]
TR a[148:149],v147 offset:4096
TR a[150:151],v147 offset:6144
MM v[64:67],a[172:175],a[112:115],v[64:67]
MM v[72:75],a[172:175],a[116:119],v[72:75]
TR a[152:153],v147 offset:8192
TR a[154:155],v147 offset:10240
MM v[72:75],a[168:171],a[84:87],v[72:75]
MM v[72:75],a[164:167],a[52:55],v[72:75]
TR a[156:157],v147 offset:12288
TR a[158:159],v147 offset:14336
MM v[72:75],a[160:163],a[20:23],v[72:75]
s_waitcnt lgkmcnt(15)
s_waitcnt vmcnt(16)
s_barrier
MM v[76:79],a[176:179],a[20:23],v[76:79]
s_xor_b32 s66, s94, 1
TR a[160:161],v244 offset:16384
TR a[162:163],v244 offset:17408
MM v[76:79],a[180:183],a[52:55],v[76:79]
s_mul_i32 s66, s66, 163840
s_add_u32 s66, s66, s86
s_add_u32 m0, s65, 0
MM v[76:79],a[184:187],a[84:87],v[76:79]
TR a[164:165],v244 offset:18432
TR a[166:167],v244 offset:19456
MM v[76:79],a[188:191],a[116:119],v[76:79]
buffer_load_dwordx4 v240, s[24:27], s66 offen offset:0 lds
MM v[68:71],a[188:191],a[112:115],v[68:71]
s_add_u32 s66, s66, 9216
TR a[168:169],v244 offset:20480
TR a[170:171],v244 offset:21504
MM v[68:71],a[184:187],a[80:83],v[68:71]
MM v[68:71],a[180:183],a[48:51],v[68:71]
TR a[172:173],v244 offset:22528
TR a[174:175],v244 offset:23552
MM v[68:71],a[176:179],a[16:19],v[68:71]
s_waitcnt lgkmcnt(15)
buffer_load_dwordx4 v240, s[24:27], s66 offen offset:1024 lds
MM v[96:99],a[128:131],a[16:19],v[96:99]
s_add_u32 s66, s66, 9216
TR a[176:177],v244 offset:24576
TR a[178:179],v244 offset:25600
MM v[96:99],a[132:135],a[48:51],v[96:99]
MM v[96:99],a[136:139],a[80:83],v[96:99]
TR a[180:181],v244 offset:26624
TR a[182:183],v244 offset:27648
MM v[96:99],a[140:143],a[112:115],v[96:99]
buffer_load_dwordx4 v240, s[24:27], s66 offen offset:2048 lds
MM v[104:107],a[140:143],a[116:119],v[104:107]
s_add_u32 s66, s66, 9216
TR a[184:185],v244 offset:28672
TR a[186:187],v244 offset:29696
MM v[104:107],a[136:139],a[84:87],v[104:107]
MM v[104:107],a[132:135],a[52:55],v[104:107]
TR a[188:189],v244 offset:30720
TR a[190:191],v244 offset:31744
MM v[104:107],a[128:131],a[20:23],v[104:107]
s_waitcnt lgkmcnt(15)
buffer_load_dwordx4 v240, s[24:27], s66 offen offset:3072 lds
MM v[108:111],a[144:147],a[20:23],v[108:111]
TR a[128:129],v235 offset:16384
TR a[130:131],v235 offset:17408
MM v[108:111],a[148:151],a[52:55],v[108:111]
s_xor_b32 s66, s94, 2
s_mul_i32 s66, s66, 163840
s_add_u32 s66, s66, s86
MM v[108:111],a[152:155],a[84:87],v[108:111]
s_add_u32 m0, s65, 16384
TR a[132:133],v235 offset:18432
TR a[134:135],v235 offset:19456
MM v[108:111],a[156:159],a[116:119],v[108:111]
buffer_load_dwordx4 v240, s[24:27], s66 offen offset:0 lds
MM v[100:103],a[156:159],a[112:115],v[100:103]
s_add_u32 s66, s66, 9216
TR a[136:137],v235 offset:20480
TR a[138:139],v235 offset:21504
MM v[100:103],a[152:155],a[80:83],v[100:103]
MM v[100:103],a[148:151],a[48:51],v[100:103]
TR a[140:141],v235 offset:22528
TR a[142:143],v235 offset:23552
MM v[100:103],a[144:147],a[16:19],v[100:103]
s_waitcnt lgkmcnt(15)
buffer_load_dwordx4 v240, s[24:27], s66 offen offset:1024 lds
s_waitcnt vmcnt(18)
MM v[128:131],a[160:163],a[24:27],v[128:131]
s_add_u32 s66, s66, 9216
TR a[144:145],v235 offset:24576
TR a[146:147],v235 offset:25600
MM v[128:131],a[164:167],a[56:59],v[128:131]
MM v[128:131],a[168:171],a[88:91],v[128:131]
TR a[148:149],v235 offset:26624
TR a[150:151],v235 offset:27648
MM v[128:131],a[172:175],a[120:123],v[128:131]
s_waitcnt vmcnt(14)
buffer_load_dwordx4 v240, s[24:27], s66 offen offset:2048 lds
MM v[136:139],a[172:175],a[124:127],v[136:139]
s_add_u32 s66, s66, 9216
TR a[152:153],v235 offset:28672
TR a[154:155],v235 offset:29696
MM v[136:139],a[168:171],a[92:95],v[136:139]
MM v[136:139],a[164:167],a[60:63],v[136:139]
TR a[156:157],v235 offset:30720
TR a[158:159],v235 offset:31744
MM v[136:139],a[160:163],a[28:31],v[136:139]
s_waitcnt lgkmcnt(15)
buffer_load_dwordx4 v240, s[24:27], s66 offen offset:3072 lds
s_waitcnt vmcnt(12)
MM v[128:131],a[176:179],a[200:203],v[128:131]
s_memrealtime s[56:57]
TR a[160:161],v144 offset:16384
TR a[162:163],v144 offset:18432
MM v[128:131],a[180:183],a[216:219],v[128:131]
s_xor_b32 s66, s94, 3
s_mul_i32 s66, s66, 163840
s_add_u32 s66, s66, s86
MM v[128:131],a[184:187],a[232:235],v[128:131]
s_add_u32 m0, s65, 32768
TR a[164:165],v144 offset:20480
TR a[166:167],v144 offset:22528
MM v[128:131],a[188:191],a[248:251],v[128:131]
s_waitcnt vmcnt(8)
buffer_load_dwordx4 v240, s[24:27], s66 offen offset:0 lds
MM v[136:139],a[188:191],a[252:255],v[136:139]
s_add_u32 s66, s66, 9216
TR a[168:169],v144 offset:24576
TR a[170:171],v144 offset:26624
MM v[136:139],a[184:187],a[236:239],v[136:139]
MM v[136:139],a[180:183],a[220:223],v[136:139]
TR a[172:173],v144 offset:28672
TR a[174:175],v144 offset:30720
MM v[136:139],a[176:179],a[204:207],v[136:139]
s_waitcnt lgkmcnt(0)
buffer_load_dwordx4 v240, s[24:27], s66 offen offset:1024 lds
MM v[140:143],a[128:131],a[28:31],v[140:143]
s_add_u32 s66, s66, 9216
TR a[176:177],v144 offset:32768
TR a[178:179],v144 offset:34816
MM v[140:143],a[132:135],a[60:63],v[140:143]
v_mov_b32 v158, s56
v_mov_b32 v159, s69
MM v[140:143],a[136:139],a[92:95],v[140:143]
TR a[180:181],v144 offset:36864
TR a[182:183],v144 offset:38912
MM v[140:143],a[140:143],a[124:127],v[140:143]
s_add_u32 s67, s95, 0
CV v128,v128,v129
CV v129,v130,v131
MM v[132:135],a[140:143],a[120:123],v[132:135]
buffer_load_dwordx4 v240, s[24:27], s66 offen offset:2048 lds
s_mov_b64 exec, 1
buffer_store_dwordx2 v[158:159], off, s[20:23], s19 offset:8 sc1
s_mov_b64 exec, -1
s_nop 0
TR a[184:185],v144 offset:40960
TR a[186:187],v144 offset:43008
MM v[132:135],a[136:139],a[88:91],v[132:135]
s_add_u32 s66, s66, 9216
CV v136,v136,v137
CV v137,v138,v139
MM v[132:135],a[132:135],a[56:59],v[132:135]
TR a[188:189],v144 offset:45056
TR a[190:191],v144 offset:47104
MM v[132:135],a[128:131],a[24:27],v[132:135]
buffer_load_dwordx4 v240, s[24:27], s66 offen offset:3072 lds
MM v[132:135],a[144:147],a[200:203],v[132:135]
TR a[128:129],v145 offset:16384
TR a[130:131],v145 offset:18432
MM v[132:135],a[148:151],a[216:219],v[132:135]
MM v[132:135],a[152:155],a[232:235],v[132:135]
TR a[132:133],v145 offset:20480
TR a[134:135],v145 offset:22528
MM v[132:135],a[156:159],a[248:251],v[132:135]
MM v[140:143],a[156:159],a[252:255],v[140:143]
TR a[136:137],v145 offset:24576
TR a[138:139],v145 offset:26624
MM v[140:143],a[152:155],a[236:239],v[140:143]
MM v[140:143],a[148:151],a[220:223],v[140:143]
TR a[140:141],v145 offset:28672
TR a[142:143],v145 offset:30720
MM v[140:143],a[144:147],a[204:207],v[140:143]
CV v130,v132,v133
CV v131,v134,v135
MM v[72:75],a[160:163],a[28:31],v[72:75]
TR a[144:145],v145 offset:32768
TR a[146:147],v145 offset:34816
MM v[72:75],a[164:167],a[60:63],v[72:75]
v_min_u32 v131, -2, v131
MM v[72:75],a[168:171],a[92:95],v[72:75]
TR a[148:149],v145 offset:36864
TR a[150:151],v145 offset:38912
MM v[72:75],a[172:175],a[124:127],v[72:75]
buffer_store_dwordx4 v[128:131], v249, s[20:23], s67 offen offset:0 sc1
MM v[64:67],a[172:175],a[120:123],v[64:67]
TR a[152:153],v145 offset:40960
TR a[154:155],v145 offset:43008
MM v[64:67],a[168:171],a[88:91],v[64:67]
CV v138,v140,v141
CV v139,v142,v143
MM v[64:67],a[164:167],a[56:59],v[64:67]
TR a[156:157],v145 offset:45056
TR a[158:159],v145 offset:47104
MM v[64:67],a[160:163],a[24:27],v[64:67]
s_waitcnt lgkmcnt(15)
v_min_u32 v139, -2, v139
MM v[64:67],a[176:179],a[200:203],v[64:67]
TR a[160:161],v146 offset:16384
TR a[162:163],v146 offset:18432
MM v[64:67],a[180:183],a[216:219],v[64:67]
buffer_store_dwordx4 v[136:139], v249, s[20:23], s67 offen offset:1024 sc1
MM v[64:67],a[184:187],a[232:235],v[64:67]
TR a[164:165],v146 offset:20480
TR a[166:167],v146 offset:22528
MM v[64:67],a[188:191],a[248:251],v[64:67]
MM v[72:75],a[188:191],a[252:255],v[72:75]
TR a[168:169],v146 offset:24576
TR a[170:171],v146 offset:26624
MM v[72:75],a[184:187],a[236:239],v[72:75]
MM v[72:75],a[180:183],a[220:223],v[72:75]
TR a[172:173],v146 offset:28672
TR a[174:175],v146 offset:30720
MM v[72:75],a[176:179],a[204:207],v[72:75]
s_waitcnt lgkmcnt(15)
MM v[76:79],a[128:131],a[28:31],v[76:79]
TR a[176:177],v146 offset:32768
TR a[178:179],v146 offset:34816
MM v[76:79],a[132:135],a[60:63],v[76:79]
v_lshlrev_b32 v136, 16, v150
v_and_b32 v137, 0xffff0000, v150
MM v[76:79],a[136:139],a[92:95],v[76:79]
TR a[180:181],v146 offset:36864
TR a[182:183],v146 offset:38912
MM v[76:79],a[140:143],a[124:127],v[76:79]
s_add_u32 s67, s95, 4096
CV v64,v64,v65
CV v65,v66,v67
MM v[68:71],a[140:143],a[120:123],v[68:71]
TR a[184:185],v146 offset:40960
TR a[186:187],v146 offset:43008
MM v[68:71],a[136:139],a[88:91],v[68:71]
CV v72,v72,v73
CV v73,v74,v75
MM v[68:71],a[132:135],a[56:59],v[68:71]
TR a[188:189],v146 offset:45056
TR a[190:191],v146 offset:47104
MM v[68:71],a[128:131],a[24:27],v[68:71]
)ASM"
R"ASM(
s_waitcnt lgkmcnt(15)
v_lshlrev_b32 v138, 16, v151
v_and_b32 v139, 0xffff0000, v151
MM v[68:71],a[144:147],a[200:203],v[68:71]
TR a[128:129],v147 offset:16384
TR a[130:131],v147 offset:18432
MM v[68:71],a[148:151],a[216:219],v[68:71]
buffer_load_dwordx2 v[148:149], v253, s[20:23], s68 offen offset:8 sc1
MM v[68:71],a[152:155],a[232:235],v[68:71]
TR a[132:133],v147 offset:20480
TR a[134:135],v147 offset:22528
MM v[68:71],a[156:159],a[248:251],v[68:71]
v_mul_f32 v140, 0xbfb8aa3b, v136
v_mul_f32 v141, 0xbfb8aa3b, v137
MM v[76:79],a[156:159],a[252:255],v[76:79]
TR a[136:137],v147 offset:24576
TR a[138:139],v147 offset:26624
MM v[76:79],a[152:155],a[236:239],v[76:79]
v_mul_f32 v142, 0xbfb8aa3b, v138
v_mul_f32 v143, 0xbfb8aa3b, v139
MM v[76:79],a[148:151],a[220:223],v[76:79]
TR a[140:141],v147 offset:28672
TR a[142:143],v147 offset:30720
MM v[76:79],a[144:147],a[204:207],v[76:79]
s_waitcnt lgkmcnt(15)
CV v66,v68,v69
CV v67,v70,v71
s_waitcnt vmcnt(3)
s_barrier
MM v[104:107],a[160:163],a[28:31],v[104:107]
TR a[144:145],v147 offset:32768
TR a[146:147],v147 offset:34816
MM v[104:107],a[164:167],a[60:63],v[104:107]
EX v140,v140
v_min_u32 v67, -2, v67
MM v[104:107],a[168:171],a[92:95],v[104:107]
TR a[148:149],v147 offset:36864
TR a[150:151],v147 offset:38912
MM v[104:107],a[172:175],a[124:127],v[104:107]
buffer_store_dwordx4 v[64:67], v248, s[20:23], s67 offen offset:0 sc1
MM v[96:99],a[172:175],a[120:123],v[96:99]
TR a[152:153],v147 offset:40960
TR a[154:155],v147 offset:43008
MM v[96:99],a[168:171],a[88:91],v[96:99]
CV v74,v76,v77
CV v75,v78,v79
MM v[96:99],a[164:167],a[56:59],v[96:99]
TR a[156:157],v147 offset:45056
TR a[158:159],v147 offset:47104
MM v[96:99],a[160:163],a[24:27],v[96:99]
s_waitcnt lgkmcnt(15)
EX v141,v141
v_min_u32 v75, -2, v75
MM v[96:99],a[176:179],a[200:203],v[96:99]
TR a[160:161],v230 offset:0
TR a[162:163],v230 offset:2048
MM v[96:99],a[180:183],a[216:219],v[96:99]
buffer_store_dwordx4 v[72:75], v248, s[20:23], s67 offen offset:1024 sc1
MM v[96:99],a[184:187],a[232:235],v[96:99]
TR a[164:165],v230 offset:4096
TR a[166:167],v230 offset:6144
MM v[96:99],a[188:191],a[248:251],v[96:99]
EX v142,v142
MM v[104:107],a[188:191],a[252:255],v[104:107]
TR a[168:169],v230 offset:8192
TR a[170:171],v230 offset:10240
MM v[104:107],a[184:187],a[236:239],v[104:107]
EX v143,v143
MM v[104:107],a[180:183],a[220:223],v[104:107]
TR a[172:173],v230 offset:12288
TR a[174:175],v230 offset:14336
MM v[104:107],a[176:179],a[204:207],v[104:107]
s_waitcnt lgkmcnt(15)
v_add_f32 v140, 1.0, v140
v_add_f32 v141, 1.0, v141
MM v[108:111],a[128:131],a[28:31],v[108:111]
TR a[176:177],v230 offset:16384
TR a[178:179],v230 offset:18432
MM v[108:111],a[132:135],a[60:63],v[108:111]
v_add_f32 v142, 1.0, v142
v_add_f32 v143, 1.0, v143
MM v[108:111],a[136:139],a[92:95],v[108:111]
TR a[180:181],v230 offset:20480
TR a[182:183],v230 offset:22528
MM v[108:111],a[140:143],a[124:127],v[108:111]
s_add_u32 s67, s95, 4096
CV v96,v96,v97
CV v97,v98,v99
MM v[100:103],a[140:143],a[120:123],v[100:103]
TR a[184:185],v230 offset:24576
TR a[186:187],v230 offset:26624
MM v[100:103],a[136:139],a[88:91],v[100:103]
CV v104,v104,v105
CV v105,v106,v107
MM v[100:103],a[132:135],a[56:59],v[100:103]
TR a[188:189],v230 offset:28672
TR a[190:191],v230 offset:30720
MM v[100:103],a[128:131],a[24:27],v[100:103]
s_waitcnt lgkmcnt(15)
v_rcp_f32 v140, v140
MM v[100:103],a[144:147],a[200:203],v[100:103]
TR a[128:129],v230 offset:32768
TR a[130:131],v230 offset:34816
MM v[100:103],a[148:151],a[216:219],v[100:103]
v_rcp_f32 v141, v141
MM v[100:103],a[152:155],a[232:235],v[100:103]
TR a[132:133],v230 offset:36864
TR a[134:135],v230 offset:38912
MM v[100:103],a[156:159],a[248:251],v[100:103]
v_rcp_f32 v142, v142
MM v[108:111],a[156:159],a[252:255],v[108:111]
TR a[136:137],v230 offset:40960
TR a[138:139],v230 offset:43008
MM v[108:111],a[152:155],a[236:239],v[108:111]
v_rcp_f32 v143, v143
MM v[108:111],a[148:151],a[220:223],v[108:111]
TR a[140:141],v230 offset:45056
TR a[142:143],v230 offset:47104
MM v[108:111],a[144:147],a[204:207],v[108:111]
s_waitcnt lgkmcnt(15)
LPG29_%=:
s_cbranch_execz LTR1_%=
CV v98,v100,v101
CV v99,v102,v103
MM v[40:43],a[160:163],a[20:23],v[40:43]
TR a[144:145],v231 offset:0
TR a[146:147],v231 offset:2048
MM v[40:43],a[164:167],a[52:55],v[40:43]
v_min_u32 v99, -2, v99
v_mul_f32 v140, v136, v140
MM v[40:43],a[168:171],a[84:87],v[40:43]
TR a[148:149],v231 offset:4096
TR a[150:151],v231 offset:6144
MM v[40:43],a[172:175],a[116:119],v[40:43]
buffer_store_dwordx4 v[96:99], v248, s[20:23], s67 offen offset:2048 sc1
MM v[32:35],a[172:175],a[112:115],v[32:35]
TR a[152:153],v231 offset:8192
TR a[154:155],v231 offset:10240
MM v[32:35],a[168:171],a[80:83],v[32:35]
CV v106,v108,v109
CV v107,v110,v111
MM v[32:35],a[164:167],a[48:51],v[32:35]
TR a[156:157],v231 offset:12288
TR a[158:159],v231 offset:14336
MM v[32:35],a[160:163],a[16:19],v[32:35]
s_waitcnt lgkmcnt(15)
v_min_u32 v107, -2, v107
v_mul_f32 v141, v137, v141
MM v[32:35],a[176:179],a[24:27],v[32:35]
TR a[160:161],v231 offset:16384
TR a[162:163],v231 offset:18432
MM v[32:35],a[180:183],a[56:59],v[32:35]
buffer_store_dwordx4 v[104:107], v248, s[20:23], s67 offen offset:3072 sc1
MM v[32:35],a[184:187],a[88:91],v[32:35]
TR a[164:165],v231 offset:20480
TR a[166:167],v231 offset:22528
MM v[32:35],a[188:191],a[120:123],v[32:35]
v_mul_f32 v142, v138, v142
v_mul_f32 v143, v139, v143
MM v[40:43],a[188:191],a[124:127],v[40:43]
TR a[168:169],v231 offset:24576
TR a[170:171],v231 offset:26624
MM v[40:43],a[184:187],a[92:95],v[40:43]
CV v150,v140,v141
CV v151,v142,v143
MM v[40:43],a[180:183],a[60:63],v[40:43]
TR a[172:173],v231 offset:28672
TR a[174:175],v231 offset:30720
MM v[40:43],a[176:179],a[28:31],v[40:43]
s_waitcnt lgkmcnt(15)
v_lshlrev_b32 v160, 16, v16
v_and_b32 v161, 0xffff0000, v16
MM v[40:43],a[128:131],a[204:207],v[40:43]
TR a[176:177],v231 offset:32768
TR a[178:179],v231 offset:34816
MM v[40:43],a[132:135],a[220:223],v[40:43]
v_lshlrev_b32 v162, 16, v17
v_and_b32 v163, 0xffff0000, v17
MM v[40:43],a[136:139],a[236:239],v[40:43]
TR a[180:181],v231 offset:36864
TR a[182:183],v231 offset:38912
MM v[40:43],a[140:143],a[252:255],v[40:43]
v_lshlrev_b32 v164, 16, v18
v_and_b32 v165, 0xffff0000, v18
MM v[32:35],a[140:143],a[248:251],v[32:35]
TR a[184:185],v231 offset:40960
TR a[186:187],v231 offset:43008
MM v[32:35],a[136:139],a[232:235],v[32:35]
v_lshlrev_b32 v166, 16, v19
v_and_b32 v167, 0xffff0000, v19
MM v[32:35],a[132:135],a[216:219],v[32:35]
TR a[188:189],v231 offset:45056
TR a[190:191],v231 offset:47104
MM v[32:35],a[128:131],a[200:203],v[32:35]
s_waitcnt lgkmcnt(15)
v_mul_f32 v136, 0xbfb8aa3b, v160
v_mul_f32 v137, 0xbfb8aa3b, v161
MM v[36:39],a[144:147],a[16:19],v[36:39]
TR a[128:129],v228 offset:49152
TR a[130:131],v228 offset:51200
MM v[36:39],a[148:151],a[48:51],v[36:39]
v_mul_f32 v138, 0xbfb8aa3b, v162
v_mul_f32 v139, 0xbfb8aa3b, v163
MM v[36:39],a[152:155],a[80:83],v[36:39]
TR a[132:133],v228 offset:53248
TR a[134:135],v228 offset:55296
MM v[36:39],a[156:159],a[112:115],v[36:39]
s_add_u32 s67, s95, 0
CV v32,v32,v33
CV v33,v34,v35
MM v[44:47],a[156:159],a[116:119],v[44:47]
TR a[136:137],v228 offset:57344
TR a[138:139],v228 offset:59392
MM v[44:47],a[152:155],a[84:87],v[44:47]
CV v40,v40,v41
CV v41,v42,v43
MM v[44:47],a[148:151],a[52:55],v[44:47]
TR a[140:141],v228 offset:61440
TR a[142:143],v228 offset:63488
MM v[44:47],a[144:147],a[20:23],v[44:47]
s_waitcnt lgkmcnt(15)
v_mul_f32 v140, 0xbfb8aa3b, v164
v_mul_f32 v141, 0xbfb8aa3b, v165
MM v[44:47],a[160:163],a[28:31],v[44:47]
s_waitcnt vmcnt(4)
TR a[144:145],v228 offset:0
TR a[146:147],v228 offset:2048
MM v[44:47],a[164:167],a[60:63],v[44:47]
v_cmp_eq_u32 s[8:9], s69, v149
v_subrev_u32 v226, s56, v148
MM v[44:47],a[168:171],a[92:95],v[44:47]
TR a[148:149],v228 offset:4096
TR a[150:151],v228 offset:6144
MM v[44:47],a[172:175],a[124:127],v[44:47]
v_cndmask_b32 v226, 4.0, v226, s[8:9]
v_mul_f32 v142, 0xbfb8aa3b, v166
MM v[36:39],a[172:175],a[120:123],v[36:39]
TR a[152:153],v228 offset:8192
TR a[154:155],v228 offset:10240
MM v[36:39],a[168:171],a[88:91],v[36:39]
v_readlane_b32 s13, v226, 16
v_readlane_b32 s14, v226, 32
MM v[36:39],a[164:167],a[56:59],v[36:39]
TR a[156:157],v228 offset:12288
TR a[158:159],v228 offset:14336
MM v[36:39],a[160:163],a[24:27],v[36:39]
s_waitcnt lgkmcnt(15)
v_readlane_b32 s15, v226, 48
v_mul_f32 v143, 0xbfb8aa3b, v167
MM v[36:39],a[176:179],a[200:203],v[36:39]
s_max_i32 s12, s13, s14
TR a[160:161],v228 offset:16384
TR a[162:163],v228 offset:18432
MM v[36:39],a[180:183],a[216:219],v[36:39]
s_max_i32 s12, s12, s15
EX v136,v136
MM v[36:39],a[184:187],a[232:235],v[36:39]
s_max_i32 s12, s12, 0
TR a[164:165],v228 offset:20480
TR a[166:167],v228 offset:22528
MM v[36:39],a[188:191],a[248:251],v[36:39]
buffer_load_dwordx2 v[148:149], v253, s[20:23], s68 offen offset:8 sc1
MM v[44:47],a[188:191],a[252:255],v[44:47]
TR a[168:169],v228 offset:24576
TR a[170:171],v228 offset:26624
MM v[44:47],a[184:187],a[236:239],v[44:47]
EX v137,v137
MM v[44:47],a[180:183],a[220:223],v[44:47]
TR a[172:173],v228 offset:28672
TR a[174:175],v228 offset:30720
MM v[44:47],a[176:179],a[204:207],v[44:47]
s_waitcnt lgkmcnt(15)
CV v34,v36,v37
CV v35,v38,v39
v_min_u32 v35, -2, v35
buffer_store_dwordx4 v[32:35], v248, s[20:23], s67 offen offset:2048 sc1
MM v[8:11],a[128:131],v[172:175],v[8:11]
TR a[176:177],v228 offset:32768
TR a[178:179],v228 offset:34816
MM v[8:11],a[132:135],v[188:191],v[8:11]
EX v138,v138
MM v[8:11],a[136:139],v[204:207],v[8:11]
TR a[180:181],v228 offset:36864
TR a[182:183],v228 offset:38912
MM v[8:11],a[140:143],v[220:223],v[8:11]
CV v42,v44,v45
CV v43,v46,v47
v_min_u32 v43, -2, v43
buffer_store_dwordx4 v[40:43], v248, s[20:23], s67 offen offset:3072 sc1
MM v[0:3],a[140:143],v[216:219],v[0:3]
TR a[184:185],v228 offset:40960
TR a[186:187],v228 offset:43008
MM v[0:3],a[136:139],v[200:203],v[0:3]
EX v139,v139
MM v[0:3],a[132:135],v[184:187],v[0:3]
TR a[188:189],v228 offset:45056
TR a[190:191],v228 offset:47104
MM v[0:3],a[128:131],v[168:171],v[0:3]
s_waitcnt lgkmcnt(15)
EX v140,v140
MM v[0:3],a[144:147],a[16:19],v[0:3]
TR a[128:129],v229 offset:49152
TR a[130:131],v229 offset:51200
MM v[0:3],a[148:151],a[48:51],v[0:3]
s_cmp_le_i32 s13, -42
s_cselect_b32 s60, 2, 0
s_cselect_b64 s[8:9], -1, 0
MM v[0:3],a[152:155],a[80:83],v[0:3]
s_or_b32 s58, s58, s60
TR a[132:133],v229 offset:53248
TR a[134:135],v229 offset:55296
MM v[0:3],a[156:159],a[112:115],v[0:3]
s_mov_b64 exec, s[8:9]
buffer_load_dwordx4 v[80:83], v248, s[20:23], s36 offen offset:0 sc1
s_mov_b64 exec, -1
MM v[8:11],a[156:159],a[116:119],v[8:11]
TR a[136:137],v229 offset:57344
TR a[138:139],v229 offset:59392
MM v[8:11],a[152:155],a[84:87],v[8:11]
s_mov_b64 exec, s[8:9]
buffer_load_dwordx4 v[84:87], v248, s[20:23], s36 offen offset:1024 sc1
s_mov_b64 exec, -1
MM v[8:11],a[148:151],a[52:55],v[8:11]
TR a[140:141],v229 offset:61440
TR a[142:143],v229 offset:63488
MM v[8:11],a[144:147],a[20:23],v[8:11]
s_waitcnt lgkmcnt(15)
EX v141,v141
MM v[8:11],a[160:163],a[28:31],v[8:11]
TR a[144:145],v229 offset:0
TR a[146:147],v229 offset:2048
MM v[8:11],a[164:167],a[60:63],v[8:11]
s_cmp_le_i32 s15, 17
s_cselect_b32 s60, 8, 0
s_cselect_b64 s[8:9], -1, 0
MM v[8:11],a[168:171],a[92:95],v[8:11]
s_or_b32 s58, s58, s60
TR a[148:149],v229 offset:4096
TR a[150:151],v229 offset:6144
MM v[8:11],a[172:175],a[124:127],v[8:11]
s_mov_b64 exec, s[8:9]
buffer_load_dwordx4 v[112:115], v248, s[20:23], s38 offen offset:0 sc1
s_mov_b64 exec, -1
MM v[0:3],a[172:175],a[120:123],v[0:3]
TR a[152:153],v229 offset:8192
TR a[154:155],v229 offset:10240
MM v[0:3],a[168:171],a[88:91],v[0:3]
s_mov_b64 exec, s[8:9]
buffer_load_dwordx4 v[116:119], v248, s[20:23], s38 offen offset:1024 sc1
s_mov_b64 exec, -1
MM v[0:3],a[164:167],a[56:59],v[0:3]
TR a[156:157],v229 offset:12288
TR a[158:159],v229 offset:14336
MM v[0:3],a[160:163],a[24:27],v[0:3]
s_waitcnt lgkmcnt(15)
EX v142,v142
MM v[0:3],a[176:179],a[200:203],v[0:3]
TR a[160:161],v229 offset:16384
TR a[162:163],v229 offset:18432
MM v[0:3],a[180:183],a[216:219],v[0:3]
)ASM"
R"ASM(
s_cmp_le_i32 s14, 59
s_cselect_b32 s60, 4, 0
s_cselect_b64 s[8:9], -1, 0
MM v[0:3],a[184:187],a[232:235],v[0:3]
s_or_b32 s58, s58, s60
TR a[164:165],v229 offset:20480
TR a[166:167],v229 offset:22528
MM v[0:3],a[188:191],a[248:251],v[0:3]
s_mov_b64 exec, s[8:9]
buffer_load_dwordx4 v[88:91], v248, s[20:23], s37 offen offset:0 sc1
s_mov_b64 exec, -1
MM v[8:11],a[188:191],a[252:255],v[8:11]
TR a[168:169],v229 offset:24576
TR a[170:171],v229 offset:26624
MM v[8:11],a[184:187],a[236:239],v[8:11]
s_mov_b64 exec, s[8:9]
buffer_load_dwordx4 v[92:95], v248, s[20:23], s37 offen offset:1024 sc1
s_mov_b64 exec, -1
MM v[8:11],a[180:183],a[220:223],v[8:11]
TR a[172:173],v229 offset:28672
TR a[174:175],v229 offset:30720
MM v[8:11],a[176:179],a[204:207],v[8:11]
s_waitcnt lgkmcnt(15)
EX v143,v143
MM v[12:15],a[128:131],v[172:175],v[12:15]
TR a[176:177],v229 offset:32768
TR a[178:179],v229 offset:34816
MM v[12:15],a[132:135],v[188:191],v[12:15]
s_cmp_le_i32 s12, 102
s_cselect_b32 s60, 1, 0
s_cselect_b64 s[8:9], -1, 0
MM v[12:15],a[136:139],v[204:207],v[12:15]
s_or_b32 s58, s58, s60
TR a[180:181],v229 offset:36864
TR a[182:183],v229 offset:38912
MM v[12:15],a[140:143],v[220:223],v[12:15]
s_mov_b64 exec, s[8:9]
buffer_load_dwordx4 v[120:123], v250, s[20:23], s39 offen offset:0 sc1
s_mov_b64 exec, -1
MM v[4:7],a[140:143],v[216:219],v[4:7]
TR a[184:185],v229 offset:40960
TR a[186:187],v229 offset:43008
MM v[4:7],a[136:139],v[200:203],v[4:7]
s_mov_b64 exec, s[8:9]
buffer_load_dwordx4 v[124:127], v250, s[20:23], s39 offen offset:256 sc1
s_mov_b64 exec, -1
MM v[4:7],a[132:135],v[184:187],v[4:7]
TR a[188:189],v229 offset:45056
TR a[190:191],v229 offset:47104
MM v[4:7],a[128:131],v[168:171],v[4:7]
s_waitcnt lgkmcnt(15)
v_add_f32 v136, 1.0, v136
v_add_f32 v137, 1.0, v137
MM v[4:7],a[144:147],a[16:19],v[4:7]
v_add_f32 v138, 1.0, v138
v_add_f32 v139, 1.0, v139
MM v[4:7],a[148:151],a[48:51],v[4:7]
v_add_f32 v140, 1.0, v140
v_add_f32 v141, 1.0, v141
MM v[4:7],a[152:155],a[80:83],v[4:7]
v_add_f32 v142, 1.0, v142
v_add_f32 v143, 1.0, v143
MM v[4:7],a[156:159],a[112:115],v[4:7]
v_rcp_f32 v136, v136
MM v[12:15],a[156:159],a[116:119],v[12:15]
v_rcp_f32 v137, v137
MM v[12:15],a[152:155],a[84:87],v[12:15]
v_rcp_f32 v138, v138
MM v[12:15],a[148:151],a[52:55],v[12:15]
v_rcp_f32 v139, v139
MM v[12:15],a[144:147],a[20:23],v[12:15]
s_waitcnt lgkmcnt(8)
v_rcp_f32 v140, v140
MM v[12:15],a[160:163],a[28:31],v[12:15]
s_cmp_le_i32 s13, 0
s_cselect_b32 s60, 2, 0
s_andn2_b32 s60, s60, s58
s_cselect_b64 s[8:9], -1, 0
MM v[12:15],a[164:167],a[60:63],v[12:15]
s_or_b32 s58, s58, s60
s_mov_b64 exec, s[8:9]
buffer_load_dwordx4 v[80:83], v248, s[20:23], s36 offen offset:0 sc1
s_mov_b64 exec, -1
MM v[12:15],a[168:171],a[92:95],v[12:15]
v_rcp_f32 v141, v141
MM v[12:15],a[172:175],a[124:127],v[12:15]
s_mov_b64 exec, s[8:9]
buffer_load_dwordx4 v[84:87], v248, s[20:23], s36 offen offset:1024 sc1
s_mov_b64 exec, -1
MM v[4:7],a[172:175],a[120:123],v[4:7]
v_rcp_f32 v142, v142
MM v[4:7],a[168:171],a[88:91],v[4:7]
v_rcp_f32 v143, v143
MM v[4:7],a[164:167],a[56:59],v[4:7]
v_mul_f32 v136, v160, v136
v_mul_f32 v137, v161, v137
MM v[4:7],a[160:163],a[24:27],v[4:7]
s_waitcnt lgkmcnt(0)
v_mul_f32 v138, v162, v138
v_mul_f32 v139, v163, v139
MM v[4:7],a[176:179],a[200:203],v[4:7]
v_mul_f32 v140, v164, v140
v_mul_f32 v141, v165, v141
MM v[4:7],a[180:183],a[216:219],v[4:7]
v_mul_f32 v142, v166, v142
v_mul_f32 v143, v167, v143
MM v[4:7],a[184:187],a[232:235],v[4:7]
CV v16,v136,v137
CV v17,v138,v139
MM v[4:7],a[188:191],a[248:251],v[4:7]
CV v18,v140,v141
CV v19,v142,v143
MM v[12:15],a[188:191],a[252:255],v[12:15]
v_lshlrev_b32 v160, 16, v16
v_and_b32 v161, 0xffff0000, v16
MM v[12:15],a[184:187],a[236:239],v[12:15]
v_lshlrev_b32 v162, 16, v17
v_and_b32 v163, 0xffff0000, v17
MM v[12:15],a[180:183],a[220:223],v[12:15]
v_lshlrev_b32 v164, 16, v18
v_and_b32 v165, 0xffff0000, v18
MM v[12:15],a[176:179],a[204:207],v[12:15]
v_lshlrev_b32 v166, 16, v19
v_and_b32 v167, 0xffff0000, v19
s_memrealtime s[54:55]
s_cmp_eq_u32 s58, 15
s_cbranch_scc1 LSK1_%=
s_memrealtime s[98:99]
s_andn2_b32 s91, s17, s58
s_mov_b32 s77, 0x7fffffff
s_waitcnt lgkmcnt(0)
s_sub_u32 s76, s98, s54
s_bitcmp0_b32 s91, 1
s_cbranch_scc1 LSM11_%=
s_sub_u32 s90, s13, s76
s_sub_u32 s90, s90, 12
s_cmp_gt_i32 s90, 0
s_cbranch_scc1 LSM11_%=
buffer_load_dwordx4 v[80:83], v248, s[20:23], s36 offen offset:0 sc1
buffer_load_dwordx4 v[84:87], v248, s[20:23], s36 offen offset:1024 sc1
s_bitset1_b32 s58, 1
LSM11_%=:
s_bitcmp0_b32 s91, 3
s_cbranch_scc1 LSM13_%=
s_sub_u32 s90, s15, s76
s_sub_u32 s90, s90, 63
s_cmp_gt_i32 s90, 0
s_cbranch_scc1 LSM13_%=
buffer_load_dwordx4 v[112:115], v248, s[20:23], s38 offen offset:0 sc1
buffer_load_dwordx4 v[116:119], v248, s[20:23], s38 offen offset:1024 sc1
s_bitset1_b32 s58, 3
LSM13_%=:
s_bitcmp0_b32 s91, 2
s_cbranch_scc1 LSM12_%=
s_sub_u32 s90, s14, s76
s_sub_u32 s90, s90, 97
s_cmp_gt_i32 s90, 0
s_cbranch_scc1 LSM12_%=
buffer_load_dwordx4 v[88:91], v248, s[20:23], s37 offen offset:0 sc1
buffer_load_dwordx4 v[92:95], v248, s[20:23], s37 offen offset:1024 sc1
s_bitset1_b32 s58, 2
LSM12_%=:
s_bitcmp0_b32 s91, 0
s_cbranch_scc1 LSM10_%=
s_sub_u32 s90, s12, s76
s_sub_u32 s90, s90, 131
s_cmp_gt_i32 s90, 0
s_cbranch_scc1 LSM10_%=
buffer_load_dwordx4 v[120:123], v250, s[20:23], s39 offen offset:0 sc1
buffer_load_dwordx4 v[124:127], v250, s[20:23], s39 offen offset:256 sc1
s_bitset1_b32 s58, 0
LSM10_%=:
LSK1_%=:
v_lshlrev_b32 v176, 16, v20
v_and_b32 v177, 0xffff0000, v20
v_lshlrev_b32 v178, 16, v21
v_and_b32 v179, 0xffff0000, v21
v_lshlrev_b32 v180, 16, v22
v_and_b32 v181, 0xffff0000, v22
v_lshlrev_b32 v182, 16, v23
v_and_b32 v183, 0xffff0000, v23
v_mul_f32 v136, 0xbfb8aa3b, v176
v_mul_f32 v137, 0xbfb8aa3b, v177
v_mul_f32 v138, 0xbfb8aa3b, v178
v_mul_f32 v139, 0xbfb8aa3b, v179
v_mul_f32 v140, 0xbfb8aa3b, v180
v_mul_f32 v141, 0xbfb8aa3b, v181
v_mul_f32 v142, 0xbfb8aa3b, v182
v_mul_f32 v143, 0xbfb8aa3b, v183
EX v136,v136
EX v137,v137
EX v138,v138
EX v139,v139
EX v140,v140
EX v141,v141
EX v142,v142
EX v143,v143
v_add_f32 v136, 1.0, v136
v_add_f32 v137, 1.0, v137
v_add_f32 v138, 1.0, v138
v_add_f32 v139, 1.0, v139
v_add_f32 v140, 1.0, v140
v_add_f32 v141, 1.0, v141
v_add_f32 v142, 1.0, v142
v_add_f32 v143, 1.0, v143
v_rcp_f32 v136, v136
v_rcp_f32 v137, v137
v_rcp_f32 v138, v138
v_rcp_f32 v139, v139
s_waitcnt vmcnt(12)
v_cmp_eq_u32 s[8:9], s69, v149
v_subrev_u32 v226, s56, v148
s_nop 0
v_cndmask_b32 v226, 4.0, v226, s[8:9]
s_nop 0
v_readlane_b32 s13, v226, 16
v_readlane_b32 s14, v226, 32
v_readlane_b32 s15, v226, 48
s_max_i32 s12, s13, s14
s_max_i32 s12, s12, s15
s_max_i32 s12, s12, 0
s_cmp_eq_u32 s58, 15
s_cbranch_scc1 LSK2_%=
s_memrealtime s[98:99]
s_andn2_b32 s91, s17, s58
s_mov_b32 s77, 0x7fffffff
s_waitcnt lgkmcnt(0)
s_sub_u32 s76, s98, s54
s_bitcmp0_b32 s91, 1
s_cbranch_scc1 LSM21_%=
s_sub_u32 s90, s13, s76
s_sub_u32 s90, s90, 12
s_cmp_gt_i32 s90, 0
s_cbranch_scc1 LSM21_%=
buffer_load_dwordx4 v[80:83], v248, s[20:23], s36 offen offset:0 sc1
buffer_load_dwordx4 v[84:87], v248, s[20:23], s36 offen offset:1024 sc1
s_bitset1_b32 s58, 1
LSM21_%=:
s_bitcmp0_b32 s91, 3
s_cbranch_scc1 LSM23_%=
s_sub_u32 s90, s15, s76
s_sub_u32 s90, s90, 63
s_cmp_gt_i32 s90, 0
s_cbranch_scc1 LSM23_%=
buffer_load_dwordx4 v[112:115], v248, s[20:23], s38 offen offset:0 sc1
buffer_load_dwordx4 v[116:119], v248, s[20:23], s38 offen offset:1024 sc1
s_bitset1_b32 s58, 3
LSM23_%=:
s_bitcmp0_b32 s91, 2
s_cbranch_scc1 LSM22_%=
s_sub_u32 s90, s14, s76
s_sub_u32 s90, s90, 97
s_cmp_gt_i32 s90, 0
s_cbranch_scc1 LSM22_%=
buffer_load_dwordx4 v[88:91], v248, s[20:23], s37 offen offset:0 sc1
buffer_load_dwordx4 v[92:95], v248, s[20:23], s37 offen offset:1024 sc1
s_bitset1_b32 s58, 2
LSM22_%=:
s_bitcmp0_b32 s91, 0
s_cbranch_scc1 LSM20_%=
s_sub_u32 s90, s12, s76
s_sub_u32 s90, s90, 131
s_cmp_gt_i32 s90, 0
s_cbranch_scc1 LSM20_%=
buffer_load_dwordx4 v[120:123], v250, s[20:23], s39 offen offset:0 sc1
buffer_load_dwordx4 v[124:127], v250, s[20:23], s39 offen offset:256 sc1
s_bitset1_b32 s58, 0
LSM20_%=:
LSK2_%=:
v_rcp_f32 v140, v140
v_rcp_f32 v141, v141
v_rcp_f32 v142, v142
v_rcp_f32 v143, v143
v_mul_f32 v136, v176, v136
v_mul_f32 v137, v177, v137
v_mul_f32 v138, v178, v138
v_mul_f32 v139, v179, v139
v_mul_f32 v140, v180, v140
v_mul_f32 v141, v181, v141
v_mul_f32 v142, v182, v142
v_mul_f32 v143, v183, v143
CV v20,v136,v137
CV v21,v138,v139
CV v22,v140,v141
CV v23,v142,v143
v_lshlrev_b32 v176, 16, v20
v_and_b32 v177, 0xffff0000, v20
v_lshlrev_b32 v178, 16, v21
v_and_b32 v179, 0xffff0000, v21
v_lshlrev_b32 v180, 16, v22
v_and_b32 v181, 0xffff0000, v22
v_lshlrev_b32 v182, 16, v23
v_and_b32 v183, 0xffff0000, v23
v_lshlrev_b32 v192, 16, v24
v_and_b32 v193, 0xffff0000, v24
v_lshlrev_b32 v194, 16, v25
v_and_b32 v195, 0xffff0000, v25
v_lshlrev_b32 v196, 16, v26
v_and_b32 v197, 0xffff0000, v26
v_lshlrev_b32 v198, 16, v27
v_and_b32 v199, 0xffff0000, v27
v_lshlrev_b32 v208, 16, v28
v_and_b32 v209, 0xffff0000, v28
v_lshlrev_b32 v210, 16, v29
v_and_b32 v211, 0xffff0000, v29
v_lshlrev_b32 v212, 16, v30
v_and_b32 v213, 0xffff0000, v30
v_lshlrev_b32 v214, 16, v31
v_and_b32 v215, 0xffff0000, v31
s_mov_b32 s73, 30000
s_cmp_lg_u32 s58, 15
s_cbranch_scc1 LSI_%=
s_waitcnt vmcnt(0)
v_max3_u32 v236, v123, v127, v83
v_max3_u32 v237, v87, v91, v95
v_max3_u32 v236, v236, v115, v119
v_max_u32 v236, v236, v237
v_cmp_eq_u32 vcc, -1, v236
s_cbranch_vccnz LSI_%=
LSF_%=:
s_waitcnt lgkmcnt(0)
MM v[152:155],v[48:51],v[120:123],0
MM v[152:155],v[52:55],v[124:127],v[152:155]
MM v[0:3],v[58:61],v[88:91],v[0:3]
v_lshlrev_b32 v128, 16, v150
v_and_b32 v129, 0xffff0000, v150
MM v[0:3],v[58:61],v[112:115],v[0:3]
v_lshlrev_b32 v130, 16, v151
v_and_b32 v131, 0xffff0000, v151
MM v[0:3],v[58:61],v[80:83],v[0:3]
v_lshlrev_b32 v132, 16, v62
v_and_b32 v133, 0xffff0000, v62
MM v[4:7],v[56:59],v[88:91],v[4:7]
v_lshlrev_b32 v134, 16, v63
v_and_b32 v135, 0xffff0000, v63
MM v[4:7],v[56:59],v[112:115],v[4:7]
CV v236,v152,v153
CV v237,v154,v155
MM v[4:7],v[56:59],v[80:83],v[4:7]
ds_bpermute_b32 v236, v232, v236
ds_bpermute_b32 v237, v232, v237
MM v[8:11],v[58:61],v[92:95],v[8:11]
MM v[8:11],v[58:61],v[116:119],v[8:11]
MM v[8:11],v[58:61],v[84:87],v[8:11]
s_add_u32 s80, s18, 20480
MM v[12:15],v[56:59],v[92:95],v[12:15]
MM v[12:15],v[56:59],v[116:119],v[12:15]
CV v32,v0,v1
CV v33,v2,v3
MM v[12:15],v[56:59],v[84:87],v[12:15]
CV v34,v4,v5
CV v35,v6,v7
ds_write2_b64 v224, v[32:33], v[34:35] offset0:0 offset1:4
RD v[40:43],v225 offset:0
CV v36,v8,v9
CV v37,v10,v11
s_nop 1
CV v38,v12,v13
CV v39,v14,v15
ds_write2_b64 v224, v[36:37], v[38:39] offset0:160 offset1:164
RD v[44:47],v225 offset:1280
s_waitcnt lgkmcnt(4)
v_lshlrev_b32 v152, 16, v236
v_and_b32 v153, 0xffff0000, v236
v_lshlrev_b32 v154, 16, v237
v_and_b32 v155, 0xffff0000, v237
v_pk_mul_f32 v[152:153], v[152:153], v[128:129]
v_pk_mul_f32 v[154:155], v[154:155], v[130:131]
CV v236,v152,v153
CV v237,v154,v155
v_lshlrev_b32 v152, 16, v236
v_and_b32 v153, 0xffff0000, v236
v_lshlrev_b32 v154, 16, v237
v_and_b32 v155, 0xffff0000, v237
v_pk_add_f32 v[152:153], v[152:153], v[132:133]
v_pk_add_f32 v[154:155], v[154:155], v[134:135]
CV v238,v152,v153
CV v239,v154,v155
buffer_store_dwordx2 v[238:239], v254, s[40:43], s63 offen offset:0 sc1
s_nop 0
s_waitcnt lgkmcnt(2)
v_lshlrev_b32 v0, 16, v40
v_and_b32 v1, 0xffff0000, v40
v_lshlrev_b32 v2, 16, v41
v_and_b32 v3, 0xffff0000, v41
v_lshlrev_b32 v4, 16, v42
v_and_b32 v5, 0xffff0000, v42
v_lshlrev_b32 v6, 16, v43
v_and_b32 v7, 0xffff0000, v43
v_pk_mul_f32 v[0:1], v[0:1], v[160:161]
v_pk_mul_f32 v[2:3], v[2:3], v[162:163]
v_pk_mul_f32 v[4:5], v[4:5], v[164:165]
v_pk_mul_f32 v[6:7], v[6:7], v[166:167]
CV v40,v0,v1
CV v41,v2,v3
CV v42,v4,v5
CV v43,v6,v7
v_lshlrev_b32 v0, 16, v40
v_and_b32 v1, 0xffff0000, v40
v_lshlrev_b32 v2, 16, v41
v_and_b32 v3, 0xffff0000, v41
v_lshlrev_b32 v4, 16, v42
v_and_b32 v5, 0xffff0000, v42
v_lshlrev_b32 v6, 16, v43
v_and_b32 v7, 0xffff0000, v43
v_pk_add_f32 v[0:1], v[0:1], v[192:193]
v_pk_add_f32 v[2:3], v[2:3], v[194:195]
v_pk_add_f32 v[4:5], v[4:5], v[196:197]
v_pk_add_f32 v[6:7], v[6:7], v[198:199]
)ASM"
R"ASM(
s_nop 0
LPG30_%=:
s_cbranch_execz LTR1_%=
CV v40,v0,v1
CV v41,v2,v3
CV v42,v4,v5
CV v43,v6,v7
buffer_store_dwordx4 v[40:43], v251, s[40:43], s18 offen offset:0 sc1
s_waitcnt lgkmcnt(0)
v_lshlrev_b32 v8, 16, v44
v_and_b32 v9, 0xffff0000, v44
v_lshlrev_b32 v10, 16, v45
v_and_b32 v11, 0xffff0000, v45
v_lshlrev_b32 v12, 16, v46
v_and_b32 v13, 0xffff0000, v46
v_lshlrev_b32 v14, 16, v47
v_and_b32 v15, 0xffff0000, v47
v_pk_mul_f32 v[8:9], v[8:9], v[176:177]
v_pk_mul_f32 v[10:11], v[10:11], v[178:179]
v_pk_mul_f32 v[12:13], v[12:13], v[180:181]
v_pk_mul_f32 v[14:15], v[14:15], v[182:183]
CV v44,v8,v9
CV v45,v10,v11
CV v46,v12,v13
CV v47,v14,v15
v_lshlrev_b32 v8, 16, v44
v_and_b32 v9, 0xffff0000, v44
v_lshlrev_b32 v10, 16, v45
v_and_b32 v11, 0xffff0000, v45
v_lshlrev_b32 v12, 16, v46
v_and_b32 v13, 0xffff0000, v46
v_lshlrev_b32 v14, 16, v47
v_and_b32 v15, 0xffff0000, v47
v_pk_add_f32 v[8:9], v[8:9], v[208:209]
v_pk_add_f32 v[10:11], v[10:11], v[210:211]
v_pk_add_f32 v[12:13], v[12:13], v[212:213]
v_pk_add_f32 v[14:15], v[14:15], v[214:215]
CV v44,v8,v9
CV v45,v10,v11
CV v46,v12,v13
CV v47,v14,v15
buffer_store_dwordx4 v[44:47], v251, s[40:43], s80 offen offset:0 sc1
s_waitcnt vmcnt(0)
s_endpgm
LSI_%=:
s_memrealtime s[98:99]
s_andn2_b32 s91, s17, s58
s_mov_b32 s77, 0x7fffffff
s_waitcnt lgkmcnt(0)
s_sub_u32 s76, s98, s54
s_bitcmp0_b32 s91, 1
s_cbranch_scc1 LSM1_%=
s_sub_u32 s90, s13, s76
s_sub_u32 s90, s90, 12
s_cmp_gt_i32 s90, 0
s_cbranch_scc1 LSN1_%=
buffer_load_dwordx4 v[80:83], v248, s[20:23], s36 offen offset:0 sc1
buffer_load_dwordx4 v[84:87], v248, s[20:23], s36 offen offset:1024 sc1
s_bitset1_b32 s58, 1
s_branch LSM1_%=
LSN1_%=:
s_min_i32 s77, s77, s90
LSM1_%=:
s_bitcmp0_b32 s91, 3
s_cbranch_scc1 LSM3_%=
s_sub_u32 s90, s15, s76
s_sub_u32 s90, s90, 63
s_cmp_gt_i32 s90, 0
s_cbranch_scc1 LSN3_%=
buffer_load_dwordx4 v[112:115], v248, s[20:23], s38 offen offset:0 sc1
buffer_load_dwordx4 v[116:119], v248, s[20:23], s38 offen offset:1024 sc1
s_bitset1_b32 s58, 3
s_branch LSM3_%=
LSN3_%=:
s_min_i32 s77, s77, s90
LSM3_%=:
s_bitcmp0_b32 s91, 2
s_cbranch_scc1 LSM2_%=
s_sub_u32 s90, s14, s76
s_sub_u32 s90, s90, 97
s_cmp_gt_i32 s90, 0
s_cbranch_scc1 LSN2_%=
buffer_load_dwordx4 v[88:91], v248, s[20:23], s37 offen offset:0 sc1
buffer_load_dwordx4 v[92:95], v248, s[20:23], s37 offen offset:1024 sc1
s_bitset1_b32 s58, 2
s_branch LSM2_%=
LSN2_%=:
s_min_i32 s77, s77, s90
LSM2_%=:
s_bitcmp0_b32 s91, 0
s_cbranch_scc1 LSM0_%=
s_sub_u32 s90, s12, s76
s_sub_u32 s90, s90, 131
s_cmp_gt_i32 s90, 0
s_cbranch_scc1 LSN0_%=
buffer_load_dwordx4 v[120:123], v250, s[20:23], s39 offen offset:0 sc1
buffer_load_dwordx4 v[124:127], v250, s[20:23], s39 offen offset:256 sc1
s_bitset1_b32 s58, 0
s_branch LSM0_%=
LSN0_%=:
s_min_i32 s77, s77, s90
LSM0_%=:
s_cmp_eq_u32 s12, 4.0
s_cselect_b32 s90, 30, 45
s_cmp_lt_i32 s77, s90
s_cbranch_scc1 LSI_%=
buffer_load_dwordx2 v[148:149], v253, s[20:23], s68 offen offset:8 sc1
s_waitcnt vmcnt(1)
s_bitcmp0_b32 s58, 1
s_cbranch_scc1 LSB1_%=
v_max_u32 v237, v83, v87
v_cmp_eq_u32 vcc, -1, v237
s_cbranch_vccnz LSB1_%=
s_bitset0_b32 s17, 1
LSB1_%=:
s_bitcmp0_b32 s58, 3
s_cbranch_scc1 LSB3_%=
v_max_u32 v239, v115, v119
v_cmp_eq_u32 vcc, -1, v239
s_cbranch_vccnz LSB3_%=
s_bitset0_b32 s17, 3
LSB3_%=:
s_bitcmp0_b32 s58, 2
s_cbranch_scc1 LSB2_%=
v_max_u32 v238, v91, v95
v_cmp_eq_u32 vcc, -1, v238
s_cbranch_vccnz LSB2_%=
s_bitset0_b32 s17, 2
LSB2_%=:
s_bitcmp0_b32 s58, 0
s_cbranch_scc1 LSB0_%=
v_max_u32 v236, v123, v127
v_cmp_eq_u32 vcc, -1, v236
s_cbranch_vccnz LSB0_%=
s_bitset0_b32 s17, 0
LSB0_%=:
s_cmp_eq_u32 s17, 0
s_cbranch_scc1 LSF_%=
s_mov_b32 s58, 0
s_waitcnt vmcnt(0)
v_cmp_eq_u32 s[8:9], s69, v149
v_subrev_u32 v226, s56, v148
s_nop 0
v_cndmask_b32 v226, 4.0, v226, s[8:9]
s_nop 0
v_readlane_b32 s13, v226, 16
v_readlane_b32 s14, v226, 32
v_readlane_b32 s15, v226, 48
s_max_i32 s12, s13, s14
s_max_i32 s12, s12, s15
s_max_i32 s12, s12, 0
s_sub_u32 s73, s73, 1
s_cbranch_scc0 LSI_%=
LSE_%=:
s_waitcnt vmcnt(0)
s_endpgm
LRG1o_%=:
s_mov_b32 s70, 30000
LRG1ol_%=:
s_sleep 2
buffer_load_dwordx4 v[200:203], v208, s[20:23], s62 offen offset:0 sc1
buffer_load_dwordx4 v[204:207], v208, s[20:23], s93 offen offset:0 sc1
s_waitcnt vmcnt(0)
v_xor_b32 v201, s69, v201
v_xor_b32 v203, s69, v203
v_xor_b32 v205, s69, v205
v_xor_b32 v207, s69, v207
v_or3_b32 v209, v201, v203, v205
v_or_b32 v209, v209, v207
v_cmp_ne_u32 vcc, 0, v209
s_cbranch_vccz LRG1ob_%=
s_sub_u32 s70, s70, 1
s_cbranch_scc0 LRG1ol_%=
s_branch LRG1ob_%=
LRFR_%=:
s_mov_b32 s91, 30000
LRFRl_%=:
s_sleep 2
buffer_load_dwordx4 v[56:59], v248, s[20:23], s90 offen offset:0 sc1
s_waitcnt vmcnt(0)
v_xor_b32 v56, s69, v56
v_xor_b32 v57, s69, v57
v_xor_b32 v58, s69, v58
v_xor_b32 v59, s69, v59
v_or3_b32 v252, v56, v57, v58
v_or_b32 v252, v252, v59
v_cmp_ne_u32 vcc, 0, v252
s_cbranch_vccz LRFRb_%=
s_sub_u32 s91, s91, 1
s_cbranch_scc0 LRFRl_%=
s_branch LRFRb_%=
LRRK_%=:
s_mov_b32 s91, 30000
LRRKl_%=:
s_sleep 2
buffer_load_dwordx2 v[32:33], v34, s[20:23], s90 offen offset:0 sc1
s_waitcnt vmcnt(0)
v_cmp_ne_u32 vcc, s69, v33
s_cbranch_vccz LRRKb_%=
s_sub_u32 s91, s91, 1
s_cbranch_scc0 LRRKl_%=
s_branch LRRKb_%=
LRK_%=:
s_cmp_ge_u32 s60, 8
s_cbranch_scc1 LRK0_%=
v_mov_b32 v253, 0
v_cmp_lt_u32 vcc, s52, v96
v_cmp_lt_u32 s[98:99], s52, v97
s_nop 0
v_addc_co_u32 v253, vcc, 0, v253, vcc
v_addc_co_u32 v253, s[98:99], 0, v253, s[98:99]
v_cmp_lt_u32 vcc, s52, v98
v_cmp_lt_u32 s[98:99], s52, v99
s_nop 0
v_addc_co_u32 v253, vcc, 0, v253, vcc
v_addc_co_u32 v253, s[98:99], 0, v253, s[98:99]
v_cmp_lt_u32 vcc, s52, v100
v_cmp_lt_u32 s[98:99], s52, v101
s_nop 0
v_addc_co_u32 v253, vcc, 0, v253, vcc
v_addc_co_u32 v253, s[98:99], 0, v253, s[98:99]
v_cmp_lt_u32 vcc, s52, v102
v_cmp_lt_u32 s[98:99], s52, v103
s_nop 0
v_addc_co_u32 v253, vcc, 0, v253, vcc
v_addc_co_u32 v253, s[98:99], 0, v253, s[98:99]
v_cmp_lt_u32 vcc, s52, v104
v_cmp_lt_u32 s[98:99], s52, v105
s_nop 0
v_addc_co_u32 v253, vcc, 0, v253, vcc
v_addc_co_u32 v253, s[98:99], 0, v253, s[98:99]
v_cmp_lt_u32 vcc, s52, v106
v_cmp_lt_u32 s[98:99], s52, v107
s_nop 0
v_addc_co_u32 v253, vcc, 0, v253, vcc
v_addc_co_u32 v253, s[98:99], 0, v253, s[98:99]
v_cmp_lt_u32 vcc, s52, v108
v_cmp_lt_u32 s[98:99], s52, v109
s_nop 0
v_addc_co_u32 v253, vcc, 0, v253, vcc
v_addc_co_u32 v253, s[98:99], 0, v253, s[98:99]
v_cmp_lt_u32 vcc, s52, v110
v_cmp_lt_u32 s[98:99], s52, v111
s_nop 0
v_addc_co_u32 v253, vcc, 0, v253, vcc
v_addc_co_u32 v253, s[98:99], 0, v253, s[98:99]
v_cmp_lt_u32 vcc, s52, v112
v_cmp_lt_u32 s[98:99], s52, v113
s_nop 0
v_addc_co_u32 v253, vcc, 0, v253, vcc
v_addc_co_u32 v253, s[98:99], 0, v253, s[98:99]
v_cmp_lt_u32 vcc, s52, v114
v_cmp_lt_u32 s[98:99], s52, v115
s_nop 0
v_addc_co_u32 v253, vcc, 0, v253, vcc
v_addc_co_u32 v253, s[98:99], 0, v253, s[98:99]
v_cmp_lt_u32 vcc, s52, v116
v_cmp_lt_u32 s[98:99], s52, v117
s_nop 0
v_addc_co_u32 v253, vcc, 0, v253, vcc
v_addc_co_u32 v253, s[98:99], 0, v253, s[98:99]
v_cmp_lt_u32 vcc, s52, v118
v_cmp_lt_u32 s[98:99], s52, v119
s_nop 0
v_addc_co_u32 v253, vcc, 0, v253, vcc
v_addc_co_u32 v253, s[98:99], 0, v253, s[98:99]
v_cmp_lt_u32 vcc, s52, v120
v_cmp_lt_u32 s[98:99], s52, v121
s_nop 0
v_addc_co_u32 v253, vcc, 0, v253, vcc
v_addc_co_u32 v253, s[98:99], 0, v253, s[98:99]
v_cmp_lt_u32 vcc, s52, v122
v_cmp_lt_u32 s[98:99], s52, v123
s_nop 0
v_addc_co_u32 v253, vcc, 0, v253, vcc
v_addc_co_u32 v253, s[98:99], 0, v253, s[98:99]
v_cmp_lt_u32 vcc, s52, v124
v_cmp_lt_u32 s[98:99], s52, v125
s_nop 0
v_addc_co_u32 v253, vcc, 0, v253, vcc
v_addc_co_u32 v253, s[98:99], 0, v253, s[98:99]
v_cmp_lt_u32 vcc, s52, v126
v_cmp_lt_u32 s[98:99], s52, v127
s_nop 0
v_addc_co_u32 v253, vcc, 0, v253, vcc
v_addc_co_u32 v253, s[98:99], 0, v253, s[98:99]
s_nop 1
v_add_u32_dpp v253, v253, v253 row_ror:8 row_mask:0xf bank_mask:0xf
s_nop 1
v_add_u32_dpp v253, v253, v253 row_ror:4 row_mask:0xf bank_mask:0xf
s_nop 1
v_add_u32_dpp v253, v253, v253 row_ror:2 row_mask:0xf bank_mask:0xf
s_nop 1
v_add_u32_dpp v253, v253, v253 row_ror:1 row_mask:0xf bank_mask:0xf
v_mov_b32 v250, v253
s_nop 1
v_permlane16_swap_b32 v253, v250
v_add_u32 v253, v253, v250
v_mov_b32 v250, v253
s_nop 1
v_permlane32_swap_b32 v253, v250
v_add_u32 v253, v253, v250
v_cmp_gt_u32 vcc, 8, v253
v_mov_b32 v250, s52
v_mov_b32 v251, s69
v_lshlrev_b32 v253, 3, v253
s_mov_b32 s76, 94208
s_cbranch_vccz LRK0_%=
s_mov_b64 exec, 1
buffer_store_dwordx2 v[250:251], v253, s[20:23], s76 offen offset:0 sc1
s_mov_b64 exec, -1
s_nop 1
LRK0_%=:
s_cmp_ge_u32 s62, 8
s_cbranch_scc1 LRK1_%=
v_mov_b32 v253, 0
v_cmp_lt_u32 vcc, s53, v96
v_cmp_lt_u32 s[98:99], s53, v97
s_nop 0
v_addc_co_u32 v253, vcc, 0, v253, vcc
v_addc_co_u32 v253, s[98:99], 0, v253, s[98:99]
v_cmp_lt_u32 vcc, s53, v98
v_cmp_lt_u32 s[98:99], s53, v99
s_nop 0
v_addc_co_u32 v253, vcc, 0, v253, vcc
v_addc_co_u32 v253, s[98:99], 0, v253, s[98:99]
v_cmp_lt_u32 vcc, s53, v100
v_cmp_lt_u32 s[98:99], s53, v101
s_nop 0
v_addc_co_u32 v253, vcc, 0, v253, vcc
v_addc_co_u32 v253, s[98:99], 0, v253, s[98:99]
v_cmp_lt_u32 vcc, s53, v102
v_cmp_lt_u32 s[98:99], s53, v103
s_nop 0
v_addc_co_u32 v253, vcc, 0, v253, vcc
v_addc_co_u32 v253, s[98:99], 0, v253, s[98:99]
v_cmp_lt_u32 vcc, s53, v104
v_cmp_lt_u32 s[98:99], s53, v105
s_nop 0
v_addc_co_u32 v253, vcc, 0, v253, vcc
v_addc_co_u32 v253, s[98:99], 0, v253, s[98:99]
v_cmp_lt_u32 vcc, s53, v106
v_cmp_lt_u32 s[98:99], s53, v107
s_nop 0
v_addc_co_u32 v253, vcc, 0, v253, vcc
v_addc_co_u32 v253, s[98:99], 0, v253, s[98:99]
v_cmp_lt_u32 vcc, s53, v108
v_cmp_lt_u32 s[98:99], s53, v109
s_nop 0
v_addc_co_u32 v253, vcc, 0, v253, vcc
v_addc_co_u32 v253, s[98:99], 0, v253, s[98:99]
v_cmp_lt_u32 vcc, s53, v110
v_cmp_lt_u32 s[98:99], s53, v111
s_nop 0
v_addc_co_u32 v253, vcc, 0, v253, vcc
v_addc_co_u32 v253, s[98:99], 0, v253, s[98:99]
v_cmp_lt_u32 vcc, s53, v112
v_cmp_lt_u32 s[98:99], s53, v113
s_nop 0
v_addc_co_u32 v253, vcc, 0, v253, vcc
v_addc_co_u32 v253, s[98:99], 0, v253, s[98:99]
v_cmp_lt_u32 vcc, s53, v114
v_cmp_lt_u32 s[98:99], s53, v115
s_nop 0
v_addc_co_u32 v253, vcc, 0, v253, vcc
v_addc_co_u32 v253, s[98:99], 0, v253, s[98:99]
v_cmp_lt_u32 vcc, s53, v116
v_cmp_lt_u32 s[98:99], s53, v117
s_nop 0
v_addc_co_u32 v253, vcc, 0, v253, vcc
v_addc_co_u32 v253, s[98:99], 0, v253, s[98:99]
v_cmp_lt_u32 vcc, s53, v118
v_cmp_lt_u32 s[98:99], s53, v119
s_nop 0
v_addc_co_u32 v253, vcc, 0, v253, vcc
v_addc_co_u32 v253, s[98:99], 0, v253, s[98:99]
v_cmp_lt_u32 vcc, s53, v120
v_cmp_lt_u32 s[98:99], s53, v121
s_nop 0
v_addc_co_u32 v253, vcc, 0, v253, vcc
v_addc_co_u32 v253, s[98:99], 0, v253, s[98:99]
v_cmp_lt_u32 vcc, s53, v122
v_cmp_lt_u32 s[98:99], s53, v123
s_nop 0
v_addc_co_u32 v253, vcc, 0, v253, vcc
v_addc_co_u32 v253, s[98:99], 0, v253, s[98:99]
v_cmp_lt_u32 vcc, s53, v124
v_cmp_lt_u32 s[98:99], s53, v125
s_nop 0
v_addc_co_u32 v253, vcc, 0, v253, vcc
v_addc_co_u32 v253, s[98:99], 0, v253, s[98:99]
v_cmp_lt_u32 vcc, s53, v126
v_cmp_lt_u32 s[98:99], s53, v127
s_nop 0
v_addc_co_u32 v253, vcc, 0, v253, vcc
v_addc_co_u32 v253, s[98:99], 0, v253, s[98:99]
s_nop 1
v_add_u32_dpp v253, v253, v253 row_ror:8 row_mask:0xf bank_mask:0xf
s_nop 1
v_add_u32_dpp v253, v253, v253 row_ror:4 row_mask:0xf bank_mask:0xf
s_nop 1
v_add_u32_dpp v253, v253, v253 row_ror:2 row_mask:0xf bank_mask:0xf
s_nop 1
v_add_u32_dpp v253, v253, v253 row_ror:1 row_mask:0xf bank_mask:0xf
v_mov_b32 v250, v253
s_nop 1
v_permlane16_swap_b32 v253, v250
v_add_u32 v253, v253, v250
v_mov_b32 v250, v253
s_nop 1
v_permlane32_swap_b32 v253, v250
v_add_u32 v253, v253, v250
v_cmp_gt_u32 vcc, 8, v253
v_mov_b32 v250, s53
v_mov_b32 v251, s69
v_lshlrev_b32 v253, 3, v253
s_mov_b32 s76, 94208
s_cbranch_vccz LRK1_%=
s_mov_b64 exec, 1
buffer_store_dwordx2 v[250:251], v253, s[20:23], s76 offen offset:0 sc1
s_mov_b64 exec, -1
s_nop 1
LRK1_%=:
s_cmp_ge_u32 s56, 8
s_cbranch_scc1 LRK2_%=
v_mov_b32 v253, 0
v_cmp_lt_u32 vcc, s54, v96
v_cmp_lt_u32 s[98:99], s54, v97
s_nop 0
v_addc_co_u32 v253, vcc, 0, v253, vcc
v_addc_co_u32 v253, s[98:99], 0, v253, s[98:99]
v_cmp_lt_u32 vcc, s54, v98
v_cmp_lt_u32 s[98:99], s54, v99
s_nop 0
v_addc_co_u32 v253, vcc, 0, v253, vcc
)ASM"
R"ASM(
v_addc_co_u32 v253, s[98:99], 0, v253, s[98:99]
v_cmp_lt_u32 vcc, s54, v100
v_cmp_lt_u32 s[98:99], s54, v101
s_nop 0
v_addc_co_u32 v253, vcc, 0, v253, vcc
v_addc_co_u32 v253, s[98:99], 0, v253, s[98:99]
v_cmp_lt_u32 vcc, s54, v102
v_cmp_lt_u32 s[98:99], s54, v103
s_nop 0
v_addc_co_u32 v253, vcc, 0, v253, vcc
v_addc_co_u32 v253, s[98:99], 0, v253, s[98:99]
v_cmp_lt_u32 vcc, s54, v104
v_cmp_lt_u32 s[98:99], s54, v105
s_nop 0
v_addc_co_u32 v253, vcc, 0, v253, vcc
v_addc_co_u32 v253, s[98:99], 0, v253, s[98:99]
v_cmp_lt_u32 vcc, s54, v106
v_cmp_lt_u32 s[98:99], s54, v107
s_nop 0
v_addc_co_u32 v253, vcc, 0, v253, vcc
v_addc_co_u32 v253, s[98:99], 0, v253, s[98:99]
v_cmp_lt_u32 vcc, s54, v108
v_cmp_lt_u32 s[98:99], s54, v109
s_nop 0
v_addc_co_u32 v253, vcc, 0, v253, vcc
v_addc_co_u32 v253, s[98:99], 0, v253, s[98:99]
v_cmp_lt_u32 vcc, s54, v110
v_cmp_lt_u32 s[98:99], s54, v111
s_nop 0
v_addc_co_u32 v253, vcc, 0, v253, vcc
v_addc_co_u32 v253, s[98:99], 0, v253, s[98:99]
v_cmp_lt_u32 vcc, s54, v112
v_cmp_lt_u32 s[98:99], s54, v113
s_nop 0
v_addc_co_u32 v253, vcc, 0, v253, vcc
v_addc_co_u32 v253, s[98:99], 0, v253, s[98:99]
v_cmp_lt_u32 vcc, s54, v114
v_cmp_lt_u32 s[98:99], s54, v115
s_nop 0
v_addc_co_u32 v253, vcc, 0, v253, vcc
v_addc_co_u32 v253, s[98:99], 0, v253, s[98:99]
v_cmp_lt_u32 vcc, s54, v116
v_cmp_lt_u32 s[98:99], s54, v117
s_nop 0
v_addc_co_u32 v253, vcc, 0, v253, vcc
v_addc_co_u32 v253, s[98:99], 0, v253, s[98:99]
v_cmp_lt_u32 vcc, s54, v118
v_cmp_lt_u32 s[98:99], s54, v119
s_nop 0
v_addc_co_u32 v253, vcc, 0, v253, vcc
v_addc_co_u32 v253, s[98:99], 0, v253, s[98:99]
v_cmp_lt_u32 vcc, s54, v120
v_cmp_lt_u32 s[98:99], s54, v121
s_nop 0
v_addc_co_u32 v253, vcc, 0, v253, vcc
v_addc_co_u32 v253, s[98:99], 0, v253, s[98:99]
v_cmp_lt_u32 vcc, s54, v122
v_cmp_lt_u32 s[98:99], s54, v123
s_nop 0
v_addc_co_u32 v253, vcc, 0, v253, vcc
v_addc_co_u32 v253, s[98:99], 0, v253, s[98:99]
v_cmp_lt_u32 vcc, s54, v124
v_cmp_lt_u32 s[98:99], s54, v125
s_nop 0
v_addc_co_u32 v253, vcc, 0, v253, vcc
v_addc_co_u32 v253, s[98:99], 0, v253, s[98:99]
v_cmp_lt_u32 vcc, s54, v126
v_cmp_lt_u32 s[98:99], s54, v127
s_nop 0
v_addc_co_u32 v253, vcc, 0, v253, vcc
v_addc_co_u32 v253, s[98:99], 0, v253, s[98:99]
s_nop 1
v_add_u32_dpp v253, v253, v253 row_ror:8 row_mask:0xf bank_mask:0xf
s_nop 1
v_add_u32_dpp v253, v253, v253 row_ror:4 row_mask:0xf bank_mask:0xf
s_nop 1
v_add_u32_dpp v253, v253, v253 row_ror:2 row_mask:0xf bank_mask:0xf
s_nop 1
v_add_u32_dpp v253, v253, v253 row_ror:1 row_mask:0xf bank_mask:0xf
v_mov_b32 v250, v253
s_nop 1
v_permlane16_swap_b32 v253, v250
v_add_u32 v253, v253, v250
v_mov_b32 v250, v253
s_nop 1
v_permlane32_swap_b32 v253, v250
v_add_u32 v253, v253, v250
v_cmp_gt_u32 vcc, 8, v253
v_mov_b32 v250, s54
v_mov_b32 v251, s69
v_lshlrev_b32 v253, 3, v253
s_mov_b32 s76, 94208
s_cbranch_vccz LRK2_%=
s_mov_b64 exec, 1
buffer_store_dwordx2 v[250:251], v253, s[20:23], s76 offen offset:0 sc1
s_mov_b64 exec, -1
s_nop 1
LRK2_%=:
s_cmp_ge_u32 s58, 8
s_cbranch_scc1 LRK3_%=
v_mov_b32 v253, 0
v_cmp_lt_u32 vcc, s55, v96
v_cmp_lt_u32 s[98:99], s55, v97
s_nop 0
v_addc_co_u32 v253, vcc, 0, v253, vcc
v_addc_co_u32 v253, s[98:99], 0, v253, s[98:99]
v_cmp_lt_u32 vcc, s55, v98
v_cmp_lt_u32 s[98:99], s55, v99
s_nop 0
v_addc_co_u32 v253, vcc, 0, v253, vcc
v_addc_co_u32 v253, s[98:99], 0, v253, s[98:99]
v_cmp_lt_u32 vcc, s55, v100
v_cmp_lt_u32 s[98:99], s55, v101
s_nop 0
v_addc_co_u32 v253, vcc, 0, v253, vcc
v_addc_co_u32 v253, s[98:99], 0, v253, s[98:99]
v_cmp_lt_u32 vcc, s55, v102
v_cmp_lt_u32 s[98:99], s55, v103
s_nop 0
v_addc_co_u32 v253, vcc, 0, v253, vcc
v_addc_co_u32 v253, s[98:99], 0, v253, s[98:99]
v_cmp_lt_u32 vcc, s55, v104
v_cmp_lt_u32 s[98:99], s55, v105
s_nop 0
v_addc_co_u32 v253, vcc, 0, v253, vcc
v_addc_co_u32 v253, s[98:99], 0, v253, s[98:99]
v_cmp_lt_u32 vcc, s55, v106
v_cmp_lt_u32 s[98:99], s55, v107
s_nop 0
v_addc_co_u32 v253, vcc, 0, v253, vcc
v_addc_co_u32 v253, s[98:99], 0, v253, s[98:99]
v_cmp_lt_u32 vcc, s55, v108
v_cmp_lt_u32 s[98:99], s55, v109
s_nop 0
v_addc_co_u32 v253, vcc, 0, v253, vcc
v_addc_co_u32 v253, s[98:99], 0, v253, s[98:99]
v_cmp_lt_u32 vcc, s55, v110
v_cmp_lt_u32 s[98:99], s55, v111
s_nop 0
v_addc_co_u32 v253, vcc, 0, v253, vcc
v_addc_co_u32 v253, s[98:99], 0, v253, s[98:99]
v_cmp_lt_u32 vcc, s55, v112
v_cmp_lt_u32 s[98:99], s55, v113
s_nop 0
v_addc_co_u32 v253, vcc, 0, v253, vcc
v_addc_co_u32 v253, s[98:99], 0, v253, s[98:99]
v_cmp_lt_u32 vcc, s55, v114
v_cmp_lt_u32 s[98:99], s55, v115
s_nop 0
v_addc_co_u32 v253, vcc, 0, v253, vcc
v_addc_co_u32 v253, s[98:99], 0, v253, s[98:99]
v_cmp_lt_u32 vcc, s55, v116
v_cmp_lt_u32 s[98:99], s55, v117
s_nop 0
v_addc_co_u32 v253, vcc, 0, v253, vcc
v_addc_co_u32 v253, s[98:99], 0, v253, s[98:99]
v_cmp_lt_u32 vcc, s55, v118
v_cmp_lt_u32 s[98:99], s55, v119
s_nop 0
v_addc_co_u32 v253, vcc, 0, v253, vcc
v_addc_co_u32 v253, s[98:99], 0, v253, s[98:99]
v_cmp_lt_u32 vcc, s55, v120
v_cmp_lt_u32 s[98:99], s55, v121
s_nop 0
v_addc_co_u32 v253, vcc, 0, v253, vcc
v_addc_co_u32 v253, s[98:99], 0, v253, s[98:99]
v_cmp_lt_u32 vcc, s55, v122
v_cmp_lt_u32 s[98:99], s55, v123
s_nop 0
v_addc_co_u32 v253, vcc, 0, v253, vcc
v_addc_co_u32 v253, s[98:99], 0, v253, s[98:99]
v_cmp_lt_u32 vcc, s55, v124
v_cmp_lt_u32 s[98:99], s55, v125
s_nop 0
v_addc_co_u32 v253, vcc, 0, v253, vcc
v_addc_co_u32 v253, s[98:99], 0, v253, s[98:99]
v_cmp_lt_u32 vcc, s55, v126
v_cmp_lt_u32 s[98:99], s55, v127
s_nop 0
v_addc_co_u32 v253, vcc, 0, v253, vcc
v_addc_co_u32 v253, s[98:99], 0, v253, s[98:99]
s_nop 1
v_add_u32_dpp v253, v253, v253 row_ror:8 row_mask:0xf bank_mask:0xf
s_nop 1
v_add_u32_dpp v253, v253, v253 row_ror:4 row_mask:0xf bank_mask:0xf
s_nop 1
v_add_u32_dpp v253, v253, v253 row_ror:2 row_mask:0xf bank_mask:0xf
s_nop 1
v_add_u32_dpp v253, v253, v253 row_ror:1 row_mask:0xf bank_mask:0xf
v_mov_b32 v250, v253
s_nop 1
v_permlane16_swap_b32 v253, v250
v_add_u32 v253, v253, v250
v_mov_b32 v250, v253
s_nop 1
v_permlane32_swap_b32 v253, v250
v_add_u32 v253, v253, v250
v_cmp_gt_u32 vcc, 8, v253
v_mov_b32 v250, s55
v_mov_b32 v251, s69
v_lshlrev_b32 v253, 3, v253
s_mov_b32 s76, 94208
s_cbranch_vccz LRK3_%=
s_mov_b64 exec, 1
buffer_store_dwordx2 v[250:251], v253, s[20:23], s76 offen offset:0 sc1
s_mov_b64 exec, -1
s_nop 1
LRK3_%=:
s_branch LRKb_%=
LRIX_%=:
s_mov_b32 s91, 30000
LRIXl_%=:
s_sleep 2
v_bfe_u32 v251, v248, 4, 3
v_lshlrev_b32 v251, 3, v251
buffer_load_dwordx2 v[252:253], v251, s[20:23], s86 offen offset:0 sc1
s_waitcnt vmcnt(0)
v_cmp_ne_u32 vcc, s69, v253
s_cbranch_vccz LRIXb_%=
s_sub_u32 s91, s91, 1
s_cbranch_scc0 LRIXl_%=
s_branch LRIXb_%=
LRFG_%=:
s_mov_b32 s91, 30000
LRFGl_%=:
s_sleep 2
buffer_load_dword v252, v253, s[20:23], s76 offen offset:0 sc1
s_waitcnt vmcnt(0)
v_cmp_ne_u32 vcc, s69, v252
s_cbranch_vccz LRFGb_%=
s_sub_u32 s91, s91, 1
s_cbranch_scc0 LRFGl_%=
s_branch LRFGb_%=
LRFH_%=:
s_mov_b32 s91, 30000
LRFHl_%=:
s_sleep 2
buffer_load_dword v252, v253, s[20:23], s76 offen offset:0 sc1
s_waitcnt vmcnt(0)
v_cmp_ne_u32 vcc, s69, v252
s_cbranch_vccz LRFHb_%=
s_sub_u32 s91, s91, 1
s_cbranch_scc0 LRFHl_%=
s_branch LRFHb_%=
LRFY_%=:
s_mov_b32 s91, 30000
LRFYl_%=:
s_sleep 2
buffer_load_dword v252, v253, s[20:23], s76 offen offset:0 sc1
s_waitcnt vmcnt(0)
v_cmp_ne_u32 vcc, s69, v252
s_cbranch_vccz LRFYb_%=
s_sub_u32 s91, s91, 1
s_cbranch_scc0 LRFYl_%=
s_branch LRFYb_%=
LRPO0_%=:
s_mov_b32 s97, 30000
buffer_load_dword v242, v243, s[20:23], s44 offen offset:0 sc1
s_sleep 4
LRPO0l_%=:
buffer_load_dword v245, v243, s[20:23], s44 offen offset:0 sc1
s_waitcnt vmcnt(1)
v_cmp_ne_u32 vcc, s69, v242
s_cbranch_vccz LRPO0b_%=
buffer_load_dword v242, v243, s[20:23], s44 offen offset:0 sc1
s_waitcnt vmcnt(1)
v_cmp_ne_u32 vcc, s69, v245
s_cbranch_vccz LRPO0b_%=
s_sub_u32 s97, s97, 1
s_cbranch_scc0 LRPO0l_%=
s_branch LRPO0b_%=
LRPW_%=:
s_mov_b32 s61, 30000
LRPWl_%=:
s_sleep 2
buffer_load_dwordx2 v[96:97], v98, s[20:23], s62 offen offset:0 sc1
s_waitcnt vmcnt(0)
v_xor_b32 v96, s69, v96
v_xor_b32 v97, s69, v97
v_or_b32 v99, v96, v97
v_cmp_ne_u32 vcc, 0, v99
s_cbranch_vccz LRPWb_%=
s_sub_u32 s61, s61, 1
s_cbranch_scc0 LRPWl_%=
s_branch LRPWb_%=
LRPO1_%=:
s_mov_b32 s97, 30000
LRPO1l_%=:
s_sleep 2
buffer_load_dword v242, v243, s[20:23], s96 offen offset:16 sc1
s_waitcnt vmcnt(0)
v_cmp_ne_u32 vcc, s69, v242
s_cbranch_vccz LRPO1b_%=
s_sub_u32 s97, s97, 1
s_cbranch_scc0 LRPO1l_%=
s_branch LRPO1b_%=
LKEND_ka:
LRET_%=:
s_cmp_eq_u32 s73, 0
s_cbranch_scc1 LBACK0_%=
s_cmp_eq_u32 s73, 2
s_cbranch_scc1 LBACK2_%=
s_cmp_eq_u32 s73, 1
s_cbranch_scc1 LBACK1_%=
s_cmp_eq_u32 s73, 3
s_cbranch_scc1 LBACK3_%=
s_cmp_eq_u32 s73, 5
s_cbranch_scc1 LBACK5_%=
s_cmp_eq_u32 s73, 7
s_cbranch_scc1 LBACK7_%=
s_cmp_eq_u32 s73, 9
s_cbranch_scc1 LBACK9_%=
s_cmp_eq_u32 s73, 11
s_cbranch_scc1 LBACK11_%=
s_cmp_eq_u32 s73, 13
s_cbranch_scc1 LBACK13_%=
s_cmp_eq_u32 s73, 15
s_cbranch_scc1 LBACK15_%=
s_cmp_eq_u32 s73, 17
s_cbranch_scc1 LBACK17_%=
s_cmp_eq_u32 s73, 19
s_cbranch_scc1 LBACK19_%=
s_cmp_eq_u32 s73, 21
s_cbranch_scc1 LBACK21_%=
s_cmp_eq_u32 s73, 23
s_cbranch_scc1 LBACK23_%=
s_cmp_eq_u32 s73, 25
s_cbranch_scc1 LBACK25_%=
s_cmp_eq_u32 s73, 27
s_cbranch_scc1 LBACK27_%=
s_cmp_eq_u32 s73, 29
s_cbranch_scc1 LBACK29_%=
s_cmp_eq_u32 s73, 31
s_cbranch_scc1 LBACK31_%=
s_cmp_eq_u32 s73, 33
s_cbranch_scc1 LBACK33_%=
s_cmp_eq_u32 s73, 35
s_cbranch_scc1 LBACK35_%=
s_cmp_eq_u32 s73, 4
s_cbranch_scc1 LBACK4_%=
s_cmp_eq_u32 s73, 6
s_cbranch_scc1 LBACK6_%=
s_cmp_eq_u32 s73, 8
s_cbranch_scc1 LBACK8_%=
s_cmp_eq_u32 s73, 10
s_cbranch_scc1 LBACK10_%=
s_cmp_eq_u32 s73, 12
s_cbranch_scc1 LBACK12_%=
s_cmp_eq_u32 s73, 14
s_cbranch_scc1 LBACK14_%=
s_cmp_eq_u32 s73, 16
s_cbranch_scc1 LBACK16_%=
s_cmp_eq_u32 s73, 18
s_cbranch_scc1 LBACK18_%=
s_cmp_eq_u32 s73, 20
s_cbranch_scc1 LBACK20_%=
s_cmp_eq_u32 s73, 22
s_cbranch_scc1 LBACK22_%=
s_cmp_eq_u32 s73, 24
s_cbranch_scc1 LBACK24_%=
s_cmp_eq_u32 s73, 26
s_cbranch_scc1 LBACK26_%=
s_cmp_eq_u32 s73, 28
s_cbranch_scc1 LBACK28_%=
s_cmp_eq_u32 s73, 30
s_cbranch_scc1 LBACK30_%=
s_cmp_eq_u32 s73, 32
s_cbranch_scc1 LBACK32_%=
s_cmp_eq_u32 s73, 34
s_cbranch_scc1 LBACK34_%=
s_cmp_eq_u32 s73, 36
s_cbranch_scc1 LBACK36_%=
s_cmp_eq_u32 s73, 38
s_cbranch_scc1 LBACK38_%=
s_cmp_eq_u32 s73, 37
s_cbranch_scc1 LBACK37_%=
s_endpgm
LSITE0_%=:
s_mov_b32 s73, 0
s_mov_b32 s74, 0
s_mov_b32 s75, 16384
s_branch LRARE002_%=
LSITE2_%=:
s_mov_b32 s73, 2
s_mov_b32 s74, 32768
s_mov_b32 s75, 49152
s_branch LRARE002_%=
LSITE1_%=:
s_mov_b32 s73, 1
s_mov_b32 s74, 16384
s_mov_b32 s75, 32768
s_branch LRARE102_%=
LSITE3_%=:
s_mov_b32 s73, 3
s_mov_b32 s74, 49152
s_mov_b32 s75, 0
s_branch LRARE104_%=
LSITE5_%=:
s_mov_b32 s73, 5
s_mov_b32 s74, 16384
s_mov_b32 s75, 32768
s_branch LRARE104_%=
LSITE7_%=:
s_mov_b32 s73, 7
s_mov_b32 s74, 49152
s_mov_b32 s75, 0
s_branch LRARE104_%=
LSITE9_%=:
s_mov_b32 s73, 9
s_mov_b32 s74, 16384
s_mov_b32 s75, 32768
s_branch LRARE104_%=
LSITE11_%=:
s_mov_b32 s73, 11
s_mov_b32 s74, 49152
s_mov_b32 s75, 0
s_branch LRARE104_%=
LSITE13_%=:
s_mov_b32 s73, 13
s_mov_b32 s74, 16384
s_mov_b32 s75, 32768
s_branch LRARE104_%=
LSITE15_%=:
s_mov_b32 s73, 15
s_mov_b32 s74, 49152
s_mov_b32 s75, 0
s_branch LRARE104_%=
LSITE17_%=:
s_mov_b32 s73, 17
s_mov_b32 s74, 16384
s_mov_b32 s75, 32768
s_branch LRARE104_%=
LSITE19_%=:
s_mov_b32 s73, 19
s_mov_b32 s74, 49152
s_mov_b32 s75, 0
s_branch LRARE104_%=
LSITE21_%=:
s_mov_b32 s73, 21
s_mov_b32 s74, 16384
s_mov_b32 s75, 32768
s_branch LRARE104_%=
LSITE23_%=:
s_mov_b32 s73, 23
s_mov_b32 s74, 49152
s_mov_b32 s75, 0
s_branch LRARE104_%=
LSITE25_%=:
s_mov_b32 s73, 25
s_mov_b32 s74, 16384
s_mov_b32 s75, 32768
s_branch LRARE104_%=
LSITE27_%=:
s_mov_b32 s73, 27
s_mov_b32 s74, 49152
s_mov_b32 s75, 0
s_branch LRARE104_%=
LSITE29_%=:
s_mov_b32 s73, 29
s_mov_b32 s74, 16384
s_mov_b32 s75, 32768
s_branch LRARE104_%=
LSITE31_%=:
s_mov_b32 s73, 31
s_mov_b32 s74, 49152
s_mov_b32 s75, 0
s_branch LRARE104_%=
LSITE33_%=:
s_mov_b32 s73, 33
s_mov_b32 s74, 16384
s_mov_b32 s75, 32768
s_branch LRARE104_%=
LSITE35_%=:
s_mov_b32 s73, 35
s_mov_b32 s74, 49152
s_mov_b32 s75, 0
s_branch LRARE104_%=
LSITE4_%=:
)ASM"
R"ASM(
s_mov_b32 s73, 4
s_mov_b32 s74, 0
s_mov_b32 s75, 16384
s_branch LRARE004_%=
LSITE6_%=:
s_mov_b32 s73, 6
s_mov_b32 s74, 32768
s_mov_b32 s75, 49152
s_branch LRARE004_%=
LSITE8_%=:
s_mov_b32 s73, 8
s_mov_b32 s74, 0
s_mov_b32 s75, 16384
s_branch LRARE004_%=
LSITE10_%=:
s_mov_b32 s73, 10
s_mov_b32 s74, 32768
s_mov_b32 s75, 49152
s_branch LRARE004_%=
LSITE12_%=:
s_mov_b32 s73, 12
s_mov_b32 s74, 0
s_mov_b32 s75, 16384
s_branch LRARE004_%=
LSITE14_%=:
s_mov_b32 s73, 14
s_mov_b32 s74, 32768
s_mov_b32 s75, 49152
s_branch LRARE004_%=
LSITE16_%=:
s_mov_b32 s73, 16
s_mov_b32 s74, 0
s_mov_b32 s75, 16384
s_branch LRARE004_%=
LSITE18_%=:
s_mov_b32 s73, 18
s_mov_b32 s74, 32768
s_mov_b32 s75, 49152
s_branch LRARE004_%=
LSITE20_%=:
s_mov_b32 s73, 20
s_mov_b32 s74, 0
s_mov_b32 s75, 16384
s_branch LRARE004_%=
LSITE22_%=:
s_mov_b32 s73, 22
s_mov_b32 s74, 32768
s_mov_b32 s75, 49152
s_branch LRARE004_%=
LSITE24_%=:
s_mov_b32 s73, 24
s_mov_b32 s74, 0
s_mov_b32 s75, 16384
s_branch LRARE004_%=
LSITE26_%=:
s_mov_b32 s73, 26
s_mov_b32 s74, 32768
s_mov_b32 s75, 49152
s_branch LRARE004_%=
LSITE28_%=:
s_mov_b32 s73, 28
s_mov_b32 s74, 0
s_mov_b32 s75, 16384
s_branch LRARE004_%=
LSITE30_%=:
s_mov_b32 s73, 30
s_mov_b32 s74, 32768
s_mov_b32 s75, 49152
s_branch LRARE004_%=
LSITE32_%=:
s_mov_b32 s73, 32
s_mov_b32 s74, 0
s_mov_b32 s75, 16384
s_branch LRARE004_%=
LSITE34_%=:
s_mov_b32 s73, 34
s_mov_b32 s74, 32768
s_mov_b32 s75, 49152
s_branch LRARE004_%=
LSITE36_%=:
s_mov_b32 s73, 36
s_mov_b32 s74, 0
s_mov_b32 s75, 16384
s_branch LRARE022_%=
LSITE38_%=:
s_mov_b32 s73, 38
s_mov_b32 s74, 32768
s_mov_b32 s75, 49152
s_branch LRARE022_%=
LSITE37_%=:
s_mov_b32 s73, 37
s_mov_b32 s74, 16384
s_mov_b32 s75, 32768
s_branch LRARE122_%=
LRARE002_%=:
s_nop 15
s_waitcnt vmcnt(0) lgkmcnt(0)
v_add_u32 v249, s74, v224
RD a[192:195],v249 offset:0
RD a[208:211],v249 offset:4096
RD a[224:227],v249 offset:8192
RD a[240:243],v249 offset:12288
v_add_u32 v250, s74, v225
RD a[196:199],v250 offset:0
RD a[212:215],v250 offset:4096
RD a[228:231],v250 offset:8192
RD a[244:247],v250 offset:12288
v_add_u32 v251, s74, v226
RD a[200:203],v251 offset:0
RD a[216:219],v251 offset:4096
RD a[232:235],v251 offset:8192
RD a[248:251],v251 offset:12288
v_add_u32 v254, s74, v227
RD a[204:207],v254 offset:0
RD a[220:223],v254 offset:4096
RD a[236:239],v254 offset:8192
RD a[252:255],v254 offset:12288
s_waitcnt lgkmcnt(0)
MM v[0:3],a[192:195],v[160:163],v[128:131]
MM v[0:3],a[196:199],v[176:179],v[0:3]
MM v[0:3],a[200:203],v[192:195],v[0:3]
MM v[0:3],a[204:207],v[208:211],v[0:3]
MM v[4:7],a[220:223],v[208:211],v[128:131]
MM v[4:7],a[216:219],v[192:195],v[4:7]
MM v[4:7],a[212:215],v[176:179],v[4:7]
MM v[4:7],a[208:211],v[160:163],v[4:7]
MM v[12:15],a[208:211],v[164:167],v[132:135]
MM v[12:15],a[212:215],v[180:183],v[12:15]
MM v[12:15],a[216:219],v[196:199],v[12:15]
MM v[12:15],a[220:223],v[212:215],v[12:15]
MM v[8:11],a[204:207],v[212:215],v[132:135]
MM v[8:11],a[200:203],v[196:199],v[8:11]
MM v[8:11],a[196:199],v[180:183],v[8:11]
MM v[8:11],a[192:195],v[164:167],v[8:11]
MM v[32:35],a[224:227],v[160:163],v[128:131]
MM v[32:35],a[228:231],v[176:179],v[32:35]
MM v[32:35],a[232:235],v[192:195],v[32:35]
MM v[32:35],a[236:239],v[208:211],v[32:35]
MM v[36:39],a[252:255],v[208:211],v[128:131]
MM v[36:39],a[248:251],v[192:195],v[36:39]
MM v[36:39],a[244:247],v[176:179],v[36:39]
MM v[36:39],a[240:243],v[160:163],v[36:39]
MM v[44:47],a[240:243],v[164:167],v[132:135]
MM v[44:47],a[244:247],v[180:183],v[44:47]
MM v[44:47],a[248:251],v[196:199],v[44:47]
MM v[44:47],a[252:255],v[212:215],v[44:47]
MM v[40:43],a[236:239],v[212:215],v[132:135]
MM v[40:43],a[232:235],v[196:199],v[40:43]
MM v[40:43],a[228:231],v[180:183],v[40:43]
MM v[40:43],a[224:227],v[164:167],v[40:43]
s_nop 15
v_add_u32 v249, s75, v224
RD a[192:195],v249 offset:0
RD a[208:211],v249 offset:4096
RD a[224:227],v249 offset:8192
RD a[240:243],v249 offset:12288
v_add_u32 v250, s75, v225
RD a[196:199],v250 offset:0
RD a[212:215],v250 offset:4096
RD a[228:231],v250 offset:8192
RD a[244:247],v250 offset:12288
v_add_u32 v251, s75, v226
RD a[200:203],v251 offset:0
RD a[216:219],v251 offset:4096
RD a[232:235],v251 offset:8192
RD a[248:251],v251 offset:12288
v_add_u32 v254, s75, v227
RD a[204:207],v254 offset:0
RD a[220:223],v254 offset:4096
RD a[236:239],v254 offset:8192
RD a[252:255],v254 offset:12288
s_waitcnt lgkmcnt(0)
v_max3_f32 v249, v0, v1, v2
v_max3_f32 v251, v32, v33, v34
v_max3_f32 v249, v249, v3, v4
v_max3_f32 v251, v251, v35, v36
v_max3_f32 v249, v249, v5, v6
v_max3_f32 v251, v251, v37, v38
v_max3_f32 v249, v249, v7, v39
v_max_f32 v249, v249, v251
v_mov_b32 v251, v249
s_nop 1
v_permlane16_swap_b32 v249, v251
v_max_f32 v249, v249, v251
v_mov_b32 v251, v249
s_nop 1
v_permlane32_swap_b32 v249, v251
v_max_f32 v249, v249, v251
v_log_f32 v250, v244
s_nop 0
v_max3_f32 v249, 0, v249, v250
v_sub_f32 v250, 0, v249
EX v250,v250
v_sub_f32 v128, v128, v249
v_sub_f32 v129, v129, v249
v_sub_f32 v130, v130, v249
v_sub_f32 v131, v131, v249
v_sub_f32 v64, v64, v249
v_sub_f32 v0, v0, v249
v_sub_f32 v65, v65, v249
v_sub_f32 v1, v1, v249
v_sub_f32 v66, v66, v249
v_sub_f32 v2, v2, v249
v_sub_f32 v67, v67, v249
v_sub_f32 v3, v3, v249
v_sub_f32 v68, v68, v249
v_sub_f32 v4, v4, v249
v_sub_f32 v69, v69, v249
v_sub_f32 v5, v5, v249
v_sub_f32 v70, v70, v249
v_sub_f32 v6, v6, v249
v_sub_f32 v71, v71, v249
v_sub_f32 v7, v7, v249
v_sub_f32 v96, v96, v249
v_sub_f32 v32, v32, v249
v_sub_f32 v97, v97, v249
v_sub_f32 v33, v33, v249
v_sub_f32 v98, v98, v249
v_sub_f32 v34, v34, v249
v_sub_f32 v99, v99, v249
v_sub_f32 v35, v35, v249
v_sub_f32 v100, v100, v249
v_sub_f32 v36, v36, v249
v_sub_f32 v101, v101, v249
v_sub_f32 v37, v37, v249
v_sub_f32 v102, v102, v249
v_sub_f32 v38, v38, v249
v_sub_f32 v103, v103, v249
v_sub_f32 v39, v39, v249
v_mul_f32 v144, v244, v250
v_mov_b32 v145, 0
v_bfe_u32 v249, v241, 0, 8
v_lshl_or_b32 v249, v249, 1, 1
v_lshlrev_b32 v249, 22, v249
v_and_b32 v241, 0xffffff00, v241
AR v254,a0
v_sub_f32 v254, v254, v249
v_mul_f32 v254, v254, v250
AW a0,v254
AR v251,a1
v_sub_f32 v251, v251, v249
v_mul_f32 v251, v251, v250
AW a1,v251
AR v254,a2
v_sub_f32 v254, v254, v249
v_mul_f32 v254, v254, v250
AW a2,v254
AR v251,a3
v_sub_f32 v251, v251, v249
v_mul_f32 v251, v251, v250
AW a3,v251
AR v254,a4
v_sub_f32 v254, v254, v249
v_mul_f32 v254, v254, v250
AW a4,v254
AR v251,a5
v_sub_f32 v251, v251, v249
v_mul_f32 v251, v251, v250
AW a5,v251
AR v254,a6
v_sub_f32 v254, v254, v249
v_mul_f32 v254, v254, v250
AW a6,v254
AR v251,a7
v_sub_f32 v251, v251, v249
v_mul_f32 v251, v251, v250
AW a7,v251
AR v254,a32
v_sub_f32 v254, v254, v249
v_mul_f32 v254, v254, v250
AW a32,v254
AR v251,a33
v_sub_f32 v251, v251, v249
v_mul_f32 v251, v251, v250
AW a33,v251
AR v254,a34
v_sub_f32 v254, v254, v249
v_mul_f32 v254, v254, v250
AW a34,v254
AR v251,a35
v_sub_f32 v251, v251, v249
v_mul_f32 v251, v251, v250
AW a35,v251
AR v254,a36
v_sub_f32 v254, v254, v249
v_mul_f32 v254, v254, v250
AW a36,v254
AR v251,a37
v_sub_f32 v251, v251, v249
v_mul_f32 v251, v251, v250
AW a37,v251
AR v254,a38
v_sub_f32 v254, v254, v249
v_mul_f32 v254, v254, v250
AW a38,v254
AR v251,a39
v_sub_f32 v251, v251, v249
v_mul_f32 v251, v251, v250
AW a39,v251
AR v254,a64
v_sub_f32 v254, v254, v249
v_mul_f32 v254, v254, v250
AW a64,v254
AR v251,a65
v_sub_f32 v251, v251, v249
v_mul_f32 v251, v251, v250
AW a65,v251
AR v254,a66
v_sub_f32 v254, v254, v249
v_mul_f32 v254, v254, v250
AW a66,v254
AR v251,a67
v_sub_f32 v251, v251, v249
v_mul_f32 v251, v251, v250
AW a67,v251
AR v254,a68
v_sub_f32 v254, v254, v249
v_mul_f32 v254, v254, v250
AW a68,v254
AR v251,a69
v_sub_f32 v251, v251, v249
v_mul_f32 v251, v251, v250
AW a69,v251
AR v254,a70
v_sub_f32 v254, v254, v249
v_mul_f32 v254, v254, v250
AW a70,v254
AR v251,a71
v_sub_f32 v251, v251, v249
v_mul_f32 v251, v251, v250
AW a71,v251
AR v254,a96
v_sub_f32 v254, v254, v249
v_mul_f32 v254, v254, v250
AW a96,v254
AR v251,a97
v_sub_f32 v251, v251, v249
v_mul_f32 v251, v251, v250
AW a97,v251
AR v254,a98
v_sub_f32 v254, v254, v249
v_mul_f32 v254, v254, v250
AW a98,v254
AR v251,a99
v_sub_f32 v251, v251, v249
v_mul_f32 v251, v251, v250
AW a99,v251
AR v254,a100
v_sub_f32 v254, v254, v249
v_mul_f32 v254, v254, v250
AW a100,v254
AR v251,a101
v_sub_f32 v251, v251, v249
v_mul_f32 v251, v251, v250
AW a101,v251
AR v254,a102
v_sub_f32 v254, v254, v249
v_mul_f32 v254, v254, v250
AW a102,v254
AR v251,a103
v_sub_f32 v251, v251, v249
v_mul_f32 v251, v251, v250
AW a103,v251
v_max3_f32 v249, v8, v9, v10
v_max3_f32 v251, v40, v41, v42
v_max3_f32 v249, v249, v11, v12
v_max3_f32 v251, v251, v43, v44
v_max3_f32 v249, v249, v13, v14
v_max3_f32 v251, v251, v45, v46
v_max3_f32 v249, v249, v15, v47
v_max_f32 v249, v249, v251
v_mov_b32 v251, v249
s_nop 1
v_permlane16_swap_b32 v249, v251
v_max_f32 v249, v249, v251
v_mov_b32 v251, v249
s_nop 1
v_permlane32_swap_b32 v249, v251
v_max_f32 v249, v249, v251
v_log_f32 v250, v245
s_nop 0
v_max3_f32 v249, 0, v249, v250
v_sub_f32 v250, 0, v249
EX v250,v250
v_sub_f32 v132, v132, v249
v_sub_f32 v133, v133, v249
v_sub_f32 v134, v134, v249
v_sub_f32 v135, v135, v249
v_sub_f32 v72, v72, v249
v_sub_f32 v8, v8, v249
v_sub_f32 v73, v73, v249
v_sub_f32 v9, v9, v249
v_sub_f32 v74, v74, v249
v_sub_f32 v10, v10, v249
v_sub_f32 v75, v75, v249
v_sub_f32 v11, v11, v249
v_sub_f32 v76, v76, v249
v_sub_f32 v12, v12, v249
v_sub_f32 v77, v77, v249
v_sub_f32 v13, v13, v249
v_sub_f32 v78, v78, v249
v_sub_f32 v14, v14, v249
v_sub_f32 v79, v79, v249
v_sub_f32 v15, v15, v249
v_sub_f32 v104, v104, v249
v_sub_f32 v40, v40, v249
v_sub_f32 v105, v105, v249
v_sub_f32 v41, v41, v249
v_sub_f32 v106, v106, v249
v_sub_f32 v42, v42, v249
v_sub_f32 v107, v107, v249
v_sub_f32 v43, v43, v249
v_sub_f32 v108, v108, v249
v_sub_f32 v44, v44, v249
v_sub_f32 v109, v109, v249
v_sub_f32 v45, v45, v249
v_sub_f32 v110, v110, v249
v_sub_f32 v46, v46, v249
v_sub_f32 v111, v111, v249
v_sub_f32 v47, v47, v249
v_mul_f32 v148, v245, v250
v_mov_b32 v149, 0
v_bfe_u32 v249, v241, 8, 8
v_lshl_or_b32 v249, v249, 1, 1
v_lshlrev_b32 v249, 22, v249
v_and_b32 v241, 0xffff00ff, v241
AR v254,a8
v_sub_f32 v254, v254, v249
v_mul_f32 v254, v254, v250
AW a8,v254
AR v251,a9
v_sub_f32 v251, v251, v249
v_mul_f32 v251, v251, v250
AW a9,v251
AR v254,a10
v_sub_f32 v254, v254, v249
v_mul_f32 v254, v254, v250
AW a10,v254
AR v251,a11
v_sub_f32 v251, v251, v249
v_mul_f32 v251, v251, v250
AW a11,v251
AR v254,a12
v_sub_f32 v254, v254, v249
v_mul_f32 v254, v254, v250
AW a12,v254
AR v251,a13
v_sub_f32 v251, v251, v249
v_mul_f32 v251, v251, v250
AW a13,v251
AR v254,a14
v_sub_f32 v254, v254, v249
v_mul_f32 v254, v254, v250
AW a14,v254
AR v251,a15
v_sub_f32 v251, v251, v249
v_mul_f32 v251, v251, v250
AW a15,v251
AR v254,a40
v_sub_f32 v254, v254, v249
v_mul_f32 v254, v254, v250
AW a40,v254
AR v251,a41
v_sub_f32 v251, v251, v249
v_mul_f32 v251, v251, v250
AW a41,v251
AR v254,a42
v_sub_f32 v254, v254, v249
v_mul_f32 v254, v254, v250
AW a42,v254
AR v251,a43
v_sub_f32 v251, v251, v249
v_mul_f32 v251, v251, v250
AW a43,v251
AR v254,a44
v_sub_f32 v254, v254, v249
v_mul_f32 v254, v254, v250
AW a44,v254
AR v251,a45
v_sub_f32 v251, v251, v249
v_mul_f32 v251, v251, v250
AW a45,v251
AR v254,a46
v_sub_f32 v254, v254, v249
v_mul_f32 v254, v254, v250
AW a46,v254
AR v251,a47
v_sub_f32 v251, v251, v249
v_mul_f32 v251, v251, v250
AW a47,v251
AR v254,a72
v_sub_f32 v254, v254, v249
v_mul_f32 v254, v254, v250
AW a72,v254
AR v251,a73
v_sub_f32 v251, v251, v249
v_mul_f32 v251, v251, v250
AW a73,v251
AR v254,a74
v_sub_f32 v254, v254, v249
v_mul_f32 v254, v254, v250
AW a74,v254
AR v251,a75
v_sub_f32 v251, v251, v249
v_mul_f32 v251, v251, v250
AW a75,v251
AR v254,a76
v_sub_f32 v254, v254, v249
v_mul_f32 v254, v254, v250
AW a76,v254
AR v251,a77
v_sub_f32 v251, v251, v249
v_mul_f32 v251, v251, v250
AW a77,v251
AR v254,a78
v_sub_f32 v254, v254, v249
v_mul_f32 v254, v254, v250
AW a78,v254
AR v251,a79
v_sub_f32 v251, v251, v249
v_mul_f32 v251, v251, v250
AW a79,v251
AR v254,a104
v_sub_f32 v254, v254, v249
v_mul_f32 v254, v254, v250
AW a104,v254
AR v251,a105
v_sub_f32 v251, v251, v249
)ASM"
R"ASM(
v_mul_f32 v251, v251, v250
AW a105,v251
AR v254,a106
v_sub_f32 v254, v254, v249
v_mul_f32 v254, v254, v250
AW a106,v254
AR v251,a107
v_sub_f32 v251, v251, v249
v_mul_f32 v251, v251, v250
AW a107,v251
AR v254,a108
v_sub_f32 v254, v254, v249
v_mul_f32 v254, v254, v250
AW a108,v254
AR v251,a109
v_sub_f32 v251, v251, v249
v_mul_f32 v251, v251, v250
AW a109,v251
AR v254,a110
v_sub_f32 v254, v254, v249
v_mul_f32 v254, v254, v250
AW a110,v254
AR v251,a111
v_sub_f32 v251, v251, v249
v_mul_f32 v251, v251, v250
AW a111,v251
EX v0,v0
EX v1,v1
EX v2,v2
EX v3,v3
EX v4,v4
EX v5,v5
EX v6,v6
EX v7,v7
EX v8,v8
EX v9,v9
EX v10,v10
EX v11,v11
EX v12,v12
EX v13,v13
EX v14,v14
EX v15,v15
EX v32,v32
EX v33,v33
EX v34,v34
EX v35,v35
EX v36,v36
EX v37,v37
EX v38,v38
EX v39,v39
EX v40,v40
EX v41,v41
EX v42,v42
EX v43,v43
EX v44,v44
EX v45,v45
EX v46,v46
EX v47,v47
CV v0,v0,v1
CV v1,v2,v3
CV v2,v4,v5
CV v3,v6,v7
CV v8,v8,v9
CV v9,v10,v11
CV v10,v12,v13
CV v11,v14,v15
CV v4,v32,v33
CV v5,v34,v35
CV v6,v36,v37
CV v7,v38,v39
CV v12,v40,v41
CV v13,v42,v43
CV v14,v44,v45
CV v15,v46,v47
v_smfmac_f32_16x16x64_bf16 v[144:147], v[236:239], v[0:7], v234
s_nop 0
v_smfmac_f32_16x16x64_bf16 v[148:151], v[236:239], v[8:15], v234
s_nop 9
s_branch LRET_%=
LRARE102_%=:
s_nop 15
s_waitcnt vmcnt(0) lgkmcnt(0)
v_add_u32 v249, s74, v224
RD a[192:195],v249 offset:0
RD a[208:211],v249 offset:4096
RD a[224:227],v249 offset:8192
RD a[240:243],v249 offset:12288
v_add_u32 v250, s74, v225
RD a[196:199],v250 offset:0
RD a[212:215],v250 offset:4096
RD a[228:231],v250 offset:8192
RD a[244:247],v250 offset:12288
v_add_u32 v251, s74, v226
RD a[200:203],v251 offset:0
RD a[216:219],v251 offset:4096
RD a[232:235],v251 offset:8192
RD a[248:251],v251 offset:12288
v_add_u32 v254, s74, v227
RD a[204:207],v254 offset:0
RD a[220:223],v254 offset:4096
RD a[236:239],v254 offset:8192
RD a[252:255],v254 offset:12288
s_waitcnt lgkmcnt(0)
MM v[64:67],a[192:195],v[160:163],v[128:131]
MM v[64:67],a[196:199],v[176:179],v[64:67]
MM v[64:67],a[200:203],v[192:195],v[64:67]
MM v[64:67],a[204:207],v[208:211],v[64:67]
MM v[68:71],a[220:223],v[208:211],v[128:131]
MM v[68:71],a[216:219],v[192:195],v[68:71]
MM v[68:71],a[212:215],v[176:179],v[68:71]
MM v[68:71],a[208:211],v[160:163],v[68:71]
MM v[76:79],a[208:211],v[164:167],v[132:135]
MM v[76:79],a[212:215],v[180:183],v[76:79]
MM v[76:79],a[216:219],v[196:199],v[76:79]
MM v[76:79],a[220:223],v[212:215],v[76:79]
MM v[72:75],a[204:207],v[212:215],v[132:135]
MM v[72:75],a[200:203],v[196:199],v[72:75]
MM v[72:75],a[196:199],v[180:183],v[72:75]
MM v[72:75],a[192:195],v[164:167],v[72:75]
MM v[96:99],a[224:227],v[160:163],v[128:131]
MM v[96:99],a[228:231],v[176:179],v[96:99]
MM v[96:99],a[232:235],v[192:195],v[96:99]
MM v[96:99],a[236:239],v[208:211],v[96:99]
MM v[100:103],a[252:255],v[208:211],v[128:131]
MM v[100:103],a[248:251],v[192:195],v[100:103]
MM v[100:103],a[244:247],v[176:179],v[100:103]
MM v[100:103],a[240:243],v[160:163],v[100:103]
MM v[108:111],a[240:243],v[164:167],v[132:135]
MM v[108:111],a[244:247],v[180:183],v[108:111]
MM v[108:111],a[248:251],v[196:199],v[108:111]
MM v[108:111],a[252:255],v[212:215],v[108:111]
MM v[104:107],a[236:239],v[212:215],v[132:135]
MM v[104:107],a[232:235],v[196:199],v[104:107]
MM v[104:107],a[228:231],v[180:183],v[104:107]
MM v[104:107],a[224:227],v[164:167],v[104:107]
s_nop 15
v_add_u32 v249, s75, v224
RD a[192:195],v249 offset:0
RD a[208:211],v249 offset:4096
RD a[224:227],v249 offset:8192
RD a[240:243],v249 offset:12288
v_add_u32 v250, s75, v225
RD a[196:199],v250 offset:0
RD a[212:215],v250 offset:4096
RD a[228:231],v250 offset:8192
RD a[244:247],v250 offset:12288
v_add_u32 v251, s75, v226
RD a[200:203],v251 offset:0
RD a[216:219],v251 offset:4096
RD a[232:235],v251 offset:8192
RD a[248:251],v251 offset:12288
v_add_u32 v254, s75, v227
RD a[204:207],v254 offset:0
RD a[220:223],v254 offset:4096
RD a[236:239],v254 offset:8192
RD a[252:255],v254 offset:12288
s_waitcnt lgkmcnt(0)
v_max3_f32 v249, v64, v65, v66
v_max3_f32 v251, v96, v97, v98
v_max3_f32 v249, v249, v67, v68
v_max3_f32 v251, v251, v99, v100
v_max3_f32 v249, v249, v69, v70
v_max3_f32 v251, v251, v101, v102
v_max3_f32 v249, v249, v71, v103
v_max_f32 v249, v249, v251
v_mov_b32 v251, v249
s_nop 1
v_permlane16_swap_b32 v249, v251
v_max_f32 v249, v249, v251
v_mov_b32 v251, v249
s_nop 1
v_permlane32_swap_b32 v249, v251
v_max_f32 v249, v249, v251
v_log_f32 v250, v244
s_nop 0
v_max3_f32 v249, 0, v249, v250
v_sub_f32 v250, 0, v249
EX v250,v250
v_sub_f32 v128, v128, v249
v_sub_f32 v129, v129, v249
v_sub_f32 v130, v130, v249
v_sub_f32 v131, v131, v249
v_sub_f32 v0, v0, v249
v_sub_f32 v64, v64, v249
v_sub_f32 v1, v1, v249
v_sub_f32 v65, v65, v249
v_sub_f32 v2, v2, v249
v_sub_f32 v66, v66, v249
v_sub_f32 v3, v3, v249
v_sub_f32 v67, v67, v249
v_sub_f32 v4, v4, v249
v_sub_f32 v68, v68, v249
v_sub_f32 v5, v5, v249
v_sub_f32 v69, v69, v249
v_sub_f32 v6, v6, v249
v_sub_f32 v70, v70, v249
v_sub_f32 v7, v7, v249
v_sub_f32 v71, v71, v249
v_sub_f32 v32, v32, v249
v_sub_f32 v96, v96, v249
v_sub_f32 v33, v33, v249
v_sub_f32 v97, v97, v249
v_sub_f32 v34, v34, v249
v_sub_f32 v98, v98, v249
v_sub_f32 v35, v35, v249
v_sub_f32 v99, v99, v249
v_sub_f32 v36, v36, v249
v_sub_f32 v100, v100, v249
v_sub_f32 v37, v37, v249
v_sub_f32 v101, v101, v249
v_sub_f32 v38, v38, v249
v_sub_f32 v102, v102, v249
v_sub_f32 v39, v39, v249
v_sub_f32 v103, v103, v249
v_mul_f32 v144, v244, v250
v_mov_b32 v145, 0
v_bfe_u32 v249, v241, 0, 8
v_lshl_or_b32 v249, v249, 1, 1
v_lshlrev_b32 v249, 22, v249
v_and_b32 v241, 0xffffff00, v241
AR v254,a0
v_sub_f32 v254, v254, v249
v_mul_f32 v254, v254, v250
AW a0,v254
AR v251,a1
v_sub_f32 v251, v251, v249
v_mul_f32 v251, v251, v250
AW a1,v251
AR v254,a2
v_sub_f32 v254, v254, v249
v_mul_f32 v254, v254, v250
AW a2,v254
AR v251,a3
v_sub_f32 v251, v251, v249
v_mul_f32 v251, v251, v250
AW a3,v251
AR v254,a4
v_sub_f32 v254, v254, v249
v_mul_f32 v254, v254, v250
AW a4,v254
AR v251,a5
v_sub_f32 v251, v251, v249
v_mul_f32 v251, v251, v250
AW a5,v251
AR v254,a6
v_sub_f32 v254, v254, v249
v_mul_f32 v254, v254, v250
AW a6,v254
AR v251,a7
v_sub_f32 v251, v251, v249
v_mul_f32 v251, v251, v250
AW a7,v251
AR v254,a32
v_sub_f32 v254, v254, v249
v_mul_f32 v254, v254, v250
AW a32,v254
AR v251,a33
v_sub_f32 v251, v251, v249
v_mul_f32 v251, v251, v250
AW a33,v251
AR v254,a34
v_sub_f32 v254, v254, v249
v_mul_f32 v254, v254, v250
AW a34,v254
AR v251,a35
v_sub_f32 v251, v251, v249
v_mul_f32 v251, v251, v250
AW a35,v251
AR v254,a36
v_sub_f32 v254, v254, v249
v_mul_f32 v254, v254, v250
AW a36,v254
AR v251,a37
v_sub_f32 v251, v251, v249
v_mul_f32 v251, v251, v250
AW a37,v251
AR v254,a38
v_sub_f32 v254, v254, v249
v_mul_f32 v254, v254, v250
AW a38,v254
AR v251,a39
v_sub_f32 v251, v251, v249
v_mul_f32 v251, v251, v250
AW a39,v251
AR v254,a64
v_sub_f32 v254, v254, v249
v_mul_f32 v254, v254, v250
AW a64,v254
AR v251,a65
v_sub_f32 v251, v251, v249
v_mul_f32 v251, v251, v250
AW a65,v251
AR v254,a66
v_sub_f32 v254, v254, v249
v_mul_f32 v254, v254, v250
AW a66,v254
AR v251,a67
v_sub_f32 v251, v251, v249
v_mul_f32 v251, v251, v250
AW a67,v251
AR v254,a68
v_sub_f32 v254, v254, v249
v_mul_f32 v254, v254, v250
AW a68,v254
AR v251,a69
v_sub_f32 v251, v251, v249
v_mul_f32 v251, v251, v250
AW a69,v251
AR v254,a70
v_sub_f32 v254, v254, v249
v_mul_f32 v254, v254, v250
AW a70,v254
AR v251,a71
v_sub_f32 v251, v251, v249
v_mul_f32 v251, v251, v250
AW a71,v251
AR v254,a96
v_sub_f32 v254, v254, v249
v_mul_f32 v254, v254, v250
AW a96,v254
AR v251,a97
v_sub_f32 v251, v251, v249
v_mul_f32 v251, v251, v250
AW a97,v251
AR v254,a98
v_sub_f32 v254, v254, v249
v_mul_f32 v254, v254, v250
AW a98,v254
AR v251,a99
v_sub_f32 v251, v251, v249
v_mul_f32 v251, v251, v250
AW a99,v251
AR v254,a100
v_sub_f32 v254, v254, v249
v_mul_f32 v254, v254, v250
AW a100,v254
AR v251,a101
v_sub_f32 v251, v251, v249
v_mul_f32 v251, v251, v250
AW a101,v251
AR v254,a102
v_sub_f32 v254, v254, v249
v_mul_f32 v254, v254, v250
AW a102,v254
AR v251,a103
v_sub_f32 v251, v251, v249
v_mul_f32 v251, v251, v250
AW a103,v251
v_max3_f32 v249, v72, v73, v74
v_max3_f32 v251, v104, v105, v106
v_max3_f32 v249, v249, v75, v76
v_max3_f32 v251, v251, v107, v108
v_max3_f32 v249, v249, v77, v78
v_max3_f32 v251, v251, v109, v110
v_max3_f32 v249, v249, v79, v111
v_max_f32 v249, v249, v251
v_mov_b32 v251, v249
s_nop 1
v_permlane16_swap_b32 v249, v251
v_max_f32 v249, v249, v251
v_mov_b32 v251, v249
s_nop 1
v_permlane32_swap_b32 v249, v251
v_max_f32 v249, v249, v251
v_log_f32 v250, v245
s_nop 0
v_max3_f32 v249, 0, v249, v250
v_sub_f32 v250, 0, v249
EX v250,v250
v_sub_f32 v132, v132, v249
v_sub_f32 v133, v133, v249
v_sub_f32 v134, v134, v249
v_sub_f32 v135, v135, v249
v_sub_f32 v8, v8, v249
v_sub_f32 v72, v72, v249
v_sub_f32 v9, v9, v249
v_sub_f32 v73, v73, v249
v_sub_f32 v10, v10, v249
v_sub_f32 v74, v74, v249
v_sub_f32 v11, v11, v249
v_sub_f32 v75, v75, v249
v_sub_f32 v12, v12, v249
v_sub_f32 v76, v76, v249
v_sub_f32 v13, v13, v249
v_sub_f32 v77, v77, v249
v_sub_f32 v14, v14, v249
v_sub_f32 v78, v78, v249
v_sub_f32 v15, v15, v249
v_sub_f32 v79, v79, v249
v_sub_f32 v40, v40, v249
v_sub_f32 v104, v104, v249
v_sub_f32 v41, v41, v249
v_sub_f32 v105, v105, v249
v_sub_f32 v42, v42, v249
v_sub_f32 v106, v106, v249
v_sub_f32 v43, v43, v249
v_sub_f32 v107, v107, v249
v_sub_f32 v44, v44, v249
v_sub_f32 v108, v108, v249
v_sub_f32 v45, v45, v249
v_sub_f32 v109, v109, v249
v_sub_f32 v46, v46, v249
v_sub_f32 v110, v110, v249
v_sub_f32 v47, v47, v249
v_sub_f32 v111, v111, v249
v_mul_f32 v148, v245, v250
v_mov_b32 v149, 0
v_bfe_u32 v249, v241, 8, 8
v_lshl_or_b32 v249, v249, 1, 1
v_lshlrev_b32 v249, 22, v249
v_and_b32 v241, 0xffff00ff, v241
AR v254,a8
v_sub_f32 v254, v254, v249
v_mul_f32 v254, v254, v250
AW a8,v254
AR v251,a9
v_sub_f32 v251, v251, v249
v_mul_f32 v251, v251, v250
AW a9,v251
AR v254,a10
v_sub_f32 v254, v254, v249
v_mul_f32 v254, v254, v250
AW a10,v254
AR v251,a11
v_sub_f32 v251, v251, v249
v_mul_f32 v251, v251, v250
AW a11,v251
AR v254,a12
v_sub_f32 v254, v254, v249
v_mul_f32 v254, v254, v250
AW a12,v254
AR v251,a13
v_sub_f32 v251, v251, v249
v_mul_f32 v251, v251, v250
AW a13,v251
AR v254,a14
v_sub_f32 v254, v254, v249
v_mul_f32 v254, v254, v250
AW a14,v254
AR v251,a15
v_sub_f32 v251, v251, v249
v_mul_f32 v251, v251, v250
AW a15,v251
AR v254,a40
v_sub_f32 v254, v254, v249
v_mul_f32 v254, v254, v250
AW a40,v254
AR v251,a41
v_sub_f32 v251, v251, v249
v_mul_f32 v251, v251, v250
AW a41,v251
AR v254,a42
v_sub_f32 v254, v254, v249
v_mul_f32 v254, v254, v250
AW a42,v254
AR v251,a43
v_sub_f32 v251, v251, v249
v_mul_f32 v251, v251, v250
AW a43,v251
AR v254,a44
v_sub_f32 v254, v254, v249
v_mul_f32 v254, v254, v250
AW a44,v254
AR v251,a45
v_sub_f32 v251, v251, v249
v_mul_f32 v251, v251, v250
AW a45,v251
AR v254,a46
v_sub_f32 v254, v254, v249
v_mul_f32 v254, v254, v250
AW a46,v254
AR v251,a47
v_sub_f32 v251, v251, v249
v_mul_f32 v251, v251, v250
AW a47,v251
AR v254,a72
v_sub_f32 v254, v254, v249
v_mul_f32 v254, v254, v250
AW a72,v254
AR v251,a73
v_sub_f32 v251, v251, v249
v_mul_f32 v251, v251, v250
AW a73,v251
AR v254,a74
v_sub_f32 v254, v254, v249
v_mul_f32 v254, v254, v250
AW a74,v254
AR v251,a75
v_sub_f32 v251, v251, v249
v_mul_f32 v251, v251, v250
AW a75,v251
AR v254,a76
v_sub_f32 v254, v254, v249
v_mul_f32 v254, v254, v250
AW a76,v254
AR v251,a77
v_sub_f32 v251, v251, v249
v_mul_f32 v251, v251, v250
AW a77,v251
AR v254,a78
v_sub_f32 v254, v254, v249
v_mul_f32 v254, v254, v250
AW a78,v254
AR v251,a79
v_sub_f32 v251, v251, v249
v_mul_f32 v251, v251, v250
AW a79,v251
AR v254,a104
v_sub_f32 v254, v254, v249
v_mul_f32 v254, v254, v250
AW a104,v254
AR v251,a105
v_sub_f32 v251, v251, v249
v_mul_f32 v251, v251, v250
AW a105,v251
AR v254,a106
v_sub_f32 v254, v254, v249
v_mul_f32 v254, v254, v250
AW a106,v254
AR v251,a107
v_sub_f32 v251, v251, v249
v_mul_f32 v251, v251, v250
AW a107,v251
AR v254,a108
v_sub_f32 v254, v254, v249
v_mul_f32 v254, v254, v250
AW a108,v254
AR v251,a109
v_sub_f32 v251, v251, v249
v_mul_f32 v251, v251, v250
AW a109,v251
)ASM"
R"ASM(
AR v254,a110
v_sub_f32 v254, v254, v249
v_mul_f32 v254, v254, v250
AW a110,v254
AR v251,a111
v_sub_f32 v251, v251, v249
v_mul_f32 v251, v251, v250
AW a111,v251
EX v64,v64
EX v65,v65
EX v66,v66
EX v67,v67
EX v68,v68
EX v69,v69
EX v70,v70
EX v71,v71
EX v72,v72
EX v73,v73
EX v74,v74
EX v75,v75
EX v76,v76
EX v77,v77
EX v78,v78
EX v79,v79
EX v96,v96
EX v97,v97
EX v98,v98
EX v99,v99
EX v100,v100
EX v101,v101
EX v102,v102
EX v103,v103
EX v104,v104
EX v105,v105
EX v106,v106
EX v107,v107
EX v108,v108
EX v109,v109
EX v110,v110
EX v111,v111
CV v64,v64,v65
CV v65,v66,v67
CV v66,v68,v69
CV v67,v70,v71
CV v72,v72,v73
CV v73,v74,v75
CV v74,v76,v77
CV v75,v78,v79
CV v68,v96,v97
CV v69,v98,v99
CV v70,v100,v101
CV v71,v102,v103
CV v76,v104,v105
CV v77,v106,v107
CV v78,v108,v109
CV v79,v110,v111
v_smfmac_f32_16x16x64_bf16 v[144:147], v[236:239], v[64:71], v234
s_nop 0
v_smfmac_f32_16x16x64_bf16 v[148:151], v[236:239], v[72:79], v234
s_nop 9
s_branch LRET_%=
LRARE104_%=:
s_nop 15
s_waitcnt vmcnt(0) lgkmcnt(0)
v_add_u32 v249, s74, v224
RD a[192:195],v249 offset:0
RD a[208:211],v249 offset:4096
RD a[224:227],v249 offset:8192
RD a[240:243],v249 offset:12288
v_add_u32 v250, s74, v225
RD a[196:199],v250 offset:0
RD a[212:215],v250 offset:4096
RD a[228:231],v250 offset:8192
RD a[244:247],v250 offset:12288
v_add_u32 v251, s74, v226
RD a[200:203],v251 offset:0
RD a[216:219],v251 offset:4096
RD a[232:235],v251 offset:8192
RD a[248:251],v251 offset:12288
v_add_u32 v254, s74, v227
RD a[204:207],v254 offset:0
RD a[220:223],v254 offset:4096
RD a[236:239],v254 offset:8192
RD a[252:255],v254 offset:12288
s_waitcnt lgkmcnt(0)
MM v[64:67],a[192:195],v[160:163],v[128:131]
MM v[64:67],a[196:199],v[176:179],v[64:67]
MM v[64:67],a[200:203],v[192:195],v[64:67]
MM v[64:67],a[204:207],v[208:211],v[64:67]
MM v[68:71],a[220:223],v[208:211],v[128:131]
MM v[68:71],a[216:219],v[192:195],v[68:71]
MM v[68:71],a[212:215],v[176:179],v[68:71]
MM v[68:71],a[208:211],v[160:163],v[68:71]
MM v[76:79],a[208:211],v[164:167],v[132:135]
MM v[76:79],a[212:215],v[180:183],v[76:79]
MM v[76:79],a[216:219],v[196:199],v[76:79]
MM v[76:79],a[220:223],v[212:215],v[76:79]
MM v[72:75],a[204:207],v[212:215],v[132:135]
MM v[72:75],a[200:203],v[196:199],v[72:75]
MM v[72:75],a[196:199],v[180:183],v[72:75]
MM v[72:75],a[192:195],v[164:167],v[72:75]
MM v[80:83],a[192:195],v[168:171],v[136:139]
MM v[80:83],a[196:199],v[184:187],v[80:83]
MM v[80:83],a[200:203],v[200:203],v[80:83]
MM v[80:83],a[204:207],v[216:219],v[80:83]
MM v[84:87],a[220:223],v[216:219],v[136:139]
MM v[84:87],a[216:219],v[200:203],v[84:87]
MM v[84:87],a[212:215],v[184:187],v[84:87]
MM v[84:87],a[208:211],v[168:171],v[84:87]
MM v[92:95],a[208:211],v[172:175],v[140:143]
MM v[92:95],a[212:215],v[188:191],v[92:95]
MM v[92:95],a[216:219],v[204:207],v[92:95]
MM v[92:95],a[220:223],v[220:223],v[92:95]
MM v[88:91],a[204:207],v[220:223],v[140:143]
MM v[88:91],a[200:203],v[204:207],v[88:91]
MM v[88:91],a[196:199],v[188:191],v[88:91]
MM v[88:91],a[192:195],v[172:175],v[88:91]
MM v[96:99],a[224:227],v[160:163],v[128:131]
MM v[96:99],a[228:231],v[176:179],v[96:99]
MM v[96:99],a[232:235],v[192:195],v[96:99]
MM v[96:99],a[236:239],v[208:211],v[96:99]
MM v[100:103],a[252:255],v[208:211],v[128:131]
MM v[100:103],a[248:251],v[192:195],v[100:103]
MM v[100:103],a[244:247],v[176:179],v[100:103]
MM v[100:103],a[240:243],v[160:163],v[100:103]
MM v[108:111],a[240:243],v[164:167],v[132:135]
MM v[108:111],a[244:247],v[180:183],v[108:111]
MM v[108:111],a[248:251],v[196:199],v[108:111]
MM v[108:111],a[252:255],v[212:215],v[108:111]
MM v[104:107],a[236:239],v[212:215],v[132:135]
MM v[104:107],a[232:235],v[196:199],v[104:107]
MM v[104:107],a[228:231],v[180:183],v[104:107]
MM v[104:107],a[224:227],v[164:167],v[104:107]
MM v[112:115],a[224:227],v[168:171],v[136:139]
MM v[112:115],a[228:231],v[184:187],v[112:115]
MM v[112:115],a[232:235],v[200:203],v[112:115]
MM v[112:115],a[236:239],v[216:219],v[112:115]
MM v[116:119],a[252:255],v[216:219],v[136:139]
MM v[116:119],a[248:251],v[200:203],v[116:119]
MM v[116:119],a[244:247],v[184:187],v[116:119]
MM v[116:119],a[240:243],v[168:171],v[116:119]
MM v[124:127],a[240:243],v[172:175],v[140:143]
MM v[124:127],a[244:247],v[188:191],v[124:127]
MM v[124:127],a[248:251],v[204:207],v[124:127]
MM v[124:127],a[252:255],v[220:223],v[124:127]
MM v[120:123],a[236:239],v[220:223],v[140:143]
MM v[120:123],a[232:235],v[204:207],v[120:123]
MM v[120:123],a[228:231],v[188:191],v[120:123]
MM v[120:123],a[224:227],v[172:175],v[120:123]
s_nop 15
v_add_u32 v249, s75, v224
RD a[192:195],v249 offset:0
RD a[208:211],v249 offset:4096
RD a[224:227],v249 offset:8192
RD a[240:243],v249 offset:12288
v_add_u32 v250, s75, v225
RD a[196:199],v250 offset:0
RD a[212:215],v250 offset:4096
RD a[228:231],v250 offset:8192
RD a[244:247],v250 offset:12288
v_add_u32 v251, s75, v226
RD a[200:203],v251 offset:0
RD a[216:219],v251 offset:4096
RD a[232:235],v251 offset:8192
RD a[248:251],v251 offset:12288
v_add_u32 v254, s75, v227
RD a[204:207],v254 offset:0
RD a[220:223],v254 offset:4096
RD a[236:239],v254 offset:8192
RD a[252:255],v254 offset:12288
s_waitcnt lgkmcnt(0)
v_max3_f32 v249, v64, v65, v66
v_max3_f32 v251, v96, v97, v98
v_max3_f32 v249, v249, v67, v68
v_max3_f32 v251, v251, v99, v100
v_max3_f32 v249, v249, v69, v70
v_max3_f32 v251, v251, v101, v102
v_max3_f32 v249, v249, v71, v103
v_max_f32 v249, v249, v251
v_mov_b32 v251, v249
s_nop 1
v_permlane16_swap_b32 v249, v251
v_max_f32 v249, v249, v251
v_mov_b32 v251, v249
s_nop 1
v_permlane32_swap_b32 v249, v251
v_max_f32 v249, v249, v251
v_log_f32 v250, v244
s_nop 0
v_max3_f32 v249, 0, v249, v250
v_sub_f32 v250, 0, v249
EX v250,v250
v_sub_f32 v128, v128, v249
v_sub_f32 v129, v129, v249
v_sub_f32 v130, v130, v249
v_sub_f32 v131, v131, v249
v_sub_f32 v0, v0, v249
v_sub_f32 v64, v64, v249
v_sub_f32 v1, v1, v249
v_sub_f32 v65, v65, v249
v_sub_f32 v2, v2, v249
v_sub_f32 v66, v66, v249
v_sub_f32 v3, v3, v249
v_sub_f32 v67, v67, v249
v_sub_f32 v4, v4, v249
v_sub_f32 v68, v68, v249
v_sub_f32 v5, v5, v249
v_sub_f32 v69, v69, v249
v_sub_f32 v6, v6, v249
v_sub_f32 v70, v70, v249
v_sub_f32 v7, v7, v249
v_sub_f32 v71, v71, v249
v_sub_f32 v32, v32, v249
v_sub_f32 v96, v96, v249
v_sub_f32 v33, v33, v249
v_sub_f32 v97, v97, v249
v_sub_f32 v34, v34, v249
v_sub_f32 v98, v98, v249
v_sub_f32 v35, v35, v249
v_sub_f32 v99, v99, v249
v_sub_f32 v36, v36, v249
v_sub_f32 v100, v100, v249
v_sub_f32 v37, v37, v249
v_sub_f32 v101, v101, v249
v_sub_f32 v38, v38, v249
v_sub_f32 v102, v102, v249
v_sub_f32 v39, v39, v249
v_sub_f32 v103, v103, v249
v_mul_f32 v144, v244, v250
v_mov_b32 v145, 0
v_bfe_u32 v249, v241, 0, 8
v_lshl_or_b32 v249, v249, 1, 1
v_lshlrev_b32 v249, 22, v249
v_and_b32 v241, 0xffffff00, v241
AR v254,a0
v_sub_f32 v254, v254, v249
v_mul_f32 v254, v254, v250
AW a0,v254
AR v251,a1
v_sub_f32 v251, v251, v249
v_mul_f32 v251, v251, v250
AW a1,v251
AR v254,a2
v_sub_f32 v254, v254, v249
v_mul_f32 v254, v254, v250
AW a2,v254
AR v251,a3
v_sub_f32 v251, v251, v249
v_mul_f32 v251, v251, v250
AW a3,v251
AR v254,a4
v_sub_f32 v254, v254, v249
v_mul_f32 v254, v254, v250
AW a4,v254
AR v251,a5
v_sub_f32 v251, v251, v249
v_mul_f32 v251, v251, v250
AW a5,v251
AR v254,a6
v_sub_f32 v254, v254, v249
v_mul_f32 v254, v254, v250
AW a6,v254
AR v251,a7
v_sub_f32 v251, v251, v249
v_mul_f32 v251, v251, v250
AW a7,v251
AR v254,a32
v_sub_f32 v254, v254, v249
v_mul_f32 v254, v254, v250
AW a32,v254
AR v251,a33
v_sub_f32 v251, v251, v249
v_mul_f32 v251, v251, v250
AW a33,v251
AR v254,a34
v_sub_f32 v254, v254, v249
v_mul_f32 v254, v254, v250
AW a34,v254
AR v251,a35
v_sub_f32 v251, v251, v249
v_mul_f32 v251, v251, v250
AW a35,v251
AR v254,a36
v_sub_f32 v254, v254, v249
v_mul_f32 v254, v254, v250
AW a36,v254
AR v251,a37
v_sub_f32 v251, v251, v249
v_mul_f32 v251, v251, v250
AW a37,v251
AR v254,a38
v_sub_f32 v254, v254, v249
v_mul_f32 v254, v254, v250
AW a38,v254
AR v251,a39
v_sub_f32 v251, v251, v249
v_mul_f32 v251, v251, v250
AW a39,v251
AR v254,a64
v_sub_f32 v254, v254, v249
v_mul_f32 v254, v254, v250
AW a64,v254
AR v251,a65
v_sub_f32 v251, v251, v249
v_mul_f32 v251, v251, v250
AW a65,v251
AR v254,a66
v_sub_f32 v254, v254, v249
v_mul_f32 v254, v254, v250
AW a66,v254
AR v251,a67
v_sub_f32 v251, v251, v249
v_mul_f32 v251, v251, v250
AW a67,v251
AR v254,a68
v_sub_f32 v254, v254, v249
v_mul_f32 v254, v254, v250
AW a68,v254
AR v251,a69
v_sub_f32 v251, v251, v249
v_mul_f32 v251, v251, v250
AW a69,v251
AR v254,a70
v_sub_f32 v254, v254, v249
v_mul_f32 v254, v254, v250
AW a70,v254
AR v251,a71
v_sub_f32 v251, v251, v249
v_mul_f32 v251, v251, v250
AW a71,v251
AR v254,a96
v_sub_f32 v254, v254, v249
v_mul_f32 v254, v254, v250
AW a96,v254
AR v251,a97
v_sub_f32 v251, v251, v249
v_mul_f32 v251, v251, v250
AW a97,v251
AR v254,a98
v_sub_f32 v254, v254, v249
v_mul_f32 v254, v254, v250
AW a98,v254
AR v251,a99
v_sub_f32 v251, v251, v249
v_mul_f32 v251, v251, v250
AW a99,v251
AR v254,a100
v_sub_f32 v254, v254, v249
v_mul_f32 v254, v254, v250
AW a100,v254
AR v251,a101
v_sub_f32 v251, v251, v249
v_mul_f32 v251, v251, v250
AW a101,v251
AR v254,a102
v_sub_f32 v254, v254, v249
v_mul_f32 v254, v254, v250
AW a102,v254
AR v251,a103
v_sub_f32 v251, v251, v249
v_mul_f32 v251, v251, v250
AW a103,v251
v_max3_f32 v249, v72, v73, v74
v_max3_f32 v251, v104, v105, v106
v_max3_f32 v249, v249, v75, v76
v_max3_f32 v251, v251, v107, v108
v_max3_f32 v249, v249, v77, v78
v_max3_f32 v251, v251, v109, v110
v_max3_f32 v249, v249, v79, v111
v_max_f32 v249, v249, v251
v_mov_b32 v251, v249
s_nop 1
v_permlane16_swap_b32 v249, v251
v_max_f32 v249, v249, v251
v_mov_b32 v251, v249
s_nop 1
v_permlane32_swap_b32 v249, v251
v_max_f32 v249, v249, v251
v_log_f32 v250, v245
s_nop 0
v_max3_f32 v249, 0, v249, v250
v_sub_f32 v250, 0, v249
EX v250,v250
v_sub_f32 v132, v132, v249
v_sub_f32 v133, v133, v249
v_sub_f32 v134, v134, v249
v_sub_f32 v135, v135, v249
v_sub_f32 v8, v8, v249
v_sub_f32 v72, v72, v249
v_sub_f32 v9, v9, v249
v_sub_f32 v73, v73, v249
v_sub_f32 v10, v10, v249
v_sub_f32 v74, v74, v249
v_sub_f32 v11, v11, v249
v_sub_f32 v75, v75, v249
v_sub_f32 v12, v12, v249
v_sub_f32 v76, v76, v249
v_sub_f32 v13, v13, v249
v_sub_f32 v77, v77, v249
v_sub_f32 v14, v14, v249
v_sub_f32 v78, v78, v249
v_sub_f32 v15, v15, v249
v_sub_f32 v79, v79, v249
v_sub_f32 v40, v40, v249
v_sub_f32 v104, v104, v249
v_sub_f32 v41, v41, v249
v_sub_f32 v105, v105, v249
v_sub_f32 v42, v42, v249
v_sub_f32 v106, v106, v249
v_sub_f32 v43, v43, v249
v_sub_f32 v107, v107, v249
v_sub_f32 v44, v44, v249
v_sub_f32 v108, v108, v249
v_sub_f32 v45, v45, v249
v_sub_f32 v109, v109, v249
v_sub_f32 v46, v46, v249
v_sub_f32 v110, v110, v249
v_sub_f32 v47, v47, v249
v_sub_f32 v111, v111, v249
v_mul_f32 v148, v245, v250
v_mov_b32 v149, 0
v_bfe_u32 v249, v241, 8, 8
v_lshl_or_b32 v249, v249, 1, 1
v_lshlrev_b32 v249, 22, v249
v_and_b32 v241, 0xffff00ff, v241
AR v254,a8
v_sub_f32 v254, v254, v249
v_mul_f32 v254, v254, v250
AW a8,v254
AR v251,a9
v_sub_f32 v251, v251, v249
v_mul_f32 v251, v251, v250
AW a9,v251
AR v254,a10
v_sub_f32 v254, v254, v249
v_mul_f32 v254, v254, v250
AW a10,v254
AR v251,a11
v_sub_f32 v251, v251, v249
v_mul_f32 v251, v251, v250
AW a11,v251
AR v254,a12
v_sub_f32 v254, v254, v249
v_mul_f32 v254, v254, v250
AW a12,v254
AR v251,a13
v_sub_f32 v251, v251, v249
v_mul_f32 v251, v251, v250
AW a13,v251
AR v254,a14
v_sub_f32 v254, v254, v249
v_mul_f32 v254, v254, v250
AW a14,v254
AR v251,a15
v_sub_f32 v251, v251, v249
v_mul_f32 v251, v251, v250
AW a15,v251
AR v254,a40
v_sub_f32 v254, v254, v249
v_mul_f32 v254, v254, v250
AW a40,v254
AR v251,a41
v_sub_f32 v251, v251, v249
v_mul_f32 v251, v251, v250
AW a41,v251
AR v254,a42
v_sub_f32 v254, v254, v249
v_mul_f32 v254, v254, v250
AW a42,v254
AR v251,a43
v_sub_f32 v251, v251, v249
v_mul_f32 v251, v251, v250
AW a43,v251
AR v254,a44
v_sub_f32 v254, v254, v249
v_mul_f32 v254, v254, v250
AW a44,v254
AR v251,a45
v_sub_f32 v251, v251, v249
v_mul_f32 v251, v251, v250
AW a45,v251
AR v254,a46
v_sub_f32 v254, v254, v249
v_mul_f32 v254, v254, v250
AW a46,v254
AR v251,a47
v_sub_f32 v251, v251, v249
)ASM"
R"ASM(
v_mul_f32 v251, v251, v250
AW a47,v251
AR v254,a72
v_sub_f32 v254, v254, v249
v_mul_f32 v254, v254, v250
AW a72,v254
AR v251,a73
v_sub_f32 v251, v251, v249
v_mul_f32 v251, v251, v250
AW a73,v251
AR v254,a74
v_sub_f32 v254, v254, v249
v_mul_f32 v254, v254, v250
AW a74,v254
AR v251,a75
v_sub_f32 v251, v251, v249
v_mul_f32 v251, v251, v250
AW a75,v251
AR v254,a76
v_sub_f32 v254, v254, v249
v_mul_f32 v254, v254, v250
AW a76,v254
AR v251,a77
v_sub_f32 v251, v251, v249
v_mul_f32 v251, v251, v250
AW a77,v251
AR v254,a78
v_sub_f32 v254, v254, v249
v_mul_f32 v254, v254, v250
AW a78,v254
AR v251,a79
v_sub_f32 v251, v251, v249
v_mul_f32 v251, v251, v250
AW a79,v251
AR v254,a104
v_sub_f32 v254, v254, v249
v_mul_f32 v254, v254, v250
AW a104,v254
AR v251,a105
v_sub_f32 v251, v251, v249
v_mul_f32 v251, v251, v250
AW a105,v251
AR v254,a106
v_sub_f32 v254, v254, v249
v_mul_f32 v254, v254, v250
AW a106,v254
AR v251,a107
v_sub_f32 v251, v251, v249
v_mul_f32 v251, v251, v250
AW a107,v251
AR v254,a108
v_sub_f32 v254, v254, v249
v_mul_f32 v254, v254, v250
AW a108,v254
AR v251,a109
v_sub_f32 v251, v251, v249
v_mul_f32 v251, v251, v250
AW a109,v251
AR v254,a110
v_sub_f32 v254, v254, v249
v_mul_f32 v254, v254, v250
AW a110,v254
AR v251,a111
v_sub_f32 v251, v251, v249
v_mul_f32 v251, v251, v250
AW a111,v251
v_max3_f32 v249, v80, v81, v82
v_max3_f32 v251, v112, v113, v114
v_max3_f32 v249, v249, v83, v84
v_max3_f32 v251, v251, v115, v116
v_max3_f32 v249, v249, v85, v86
v_max3_f32 v251, v251, v117, v118
v_max3_f32 v249, v249, v87, v119
v_max_f32 v249, v249, v251
v_mov_b32 v251, v249
s_nop 1
v_permlane16_swap_b32 v249, v251
v_max_f32 v249, v249, v251
v_mov_b32 v251, v249
s_nop 1
v_permlane32_swap_b32 v249, v251
v_max_f32 v249, v249, v251
v_log_f32 v250, v246
s_nop 0
v_max3_f32 v249, 0, v249, v250
v_sub_f32 v250, 0, v249
EX v250,v250
v_sub_f32 v136, v136, v249
v_sub_f32 v137, v137, v249
v_sub_f32 v138, v138, v249
v_sub_f32 v139, v139, v249
v_sub_f32 v16, v16, v249
v_sub_f32 v80, v80, v249
v_sub_f32 v17, v17, v249
v_sub_f32 v81, v81, v249
v_sub_f32 v18, v18, v249
v_sub_f32 v82, v82, v249
v_sub_f32 v19, v19, v249
v_sub_f32 v83, v83, v249
v_sub_f32 v20, v20, v249
v_sub_f32 v84, v84, v249
v_sub_f32 v21, v21, v249
v_sub_f32 v85, v85, v249
v_sub_f32 v22, v22, v249
v_sub_f32 v86, v86, v249
v_sub_f32 v23, v23, v249
v_sub_f32 v87, v87, v249
v_sub_f32 v48, v48, v249
v_sub_f32 v112, v112, v249
v_sub_f32 v49, v49, v249
v_sub_f32 v113, v113, v249
v_sub_f32 v50, v50, v249
v_sub_f32 v114, v114, v249
v_sub_f32 v51, v51, v249
v_sub_f32 v115, v115, v249
v_sub_f32 v52, v52, v249
v_sub_f32 v116, v116, v249
v_sub_f32 v53, v53, v249
v_sub_f32 v117, v117, v249
v_sub_f32 v54, v54, v249
v_sub_f32 v118, v118, v249
v_sub_f32 v55, v55, v249
v_sub_f32 v119, v119, v249
v_mul_f32 v152, v246, v250
v_mov_b32 v153, 0
v_bfe_u32 v249, v241, 16, 8
v_lshl_or_b32 v249, v249, 1, 1
v_lshlrev_b32 v249, 22, v249
v_and_b32 v241, 0xff00ffff, v241
AR v254,a16
v_sub_f32 v254, v254, v249
v_mul_f32 v254, v254, v250
AW a16,v254
AR v251,a17
v_sub_f32 v251, v251, v249
v_mul_f32 v251, v251, v250
AW a17,v251
AR v254,a18
v_sub_f32 v254, v254, v249
v_mul_f32 v254, v254, v250
AW a18,v254
AR v251,a19
v_sub_f32 v251, v251, v249
v_mul_f32 v251, v251, v250
AW a19,v251
AR v254,a20
v_sub_f32 v254, v254, v249
v_mul_f32 v254, v254, v250
AW a20,v254
AR v251,a21
v_sub_f32 v251, v251, v249
v_mul_f32 v251, v251, v250
AW a21,v251
AR v254,a22
v_sub_f32 v254, v254, v249
v_mul_f32 v254, v254, v250
AW a22,v254
AR v251,a23
v_sub_f32 v251, v251, v249
v_mul_f32 v251, v251, v250
AW a23,v251
AR v254,a48
v_sub_f32 v254, v254, v249
v_mul_f32 v254, v254, v250
AW a48,v254
AR v251,a49
v_sub_f32 v251, v251, v249
v_mul_f32 v251, v251, v250
AW a49,v251
AR v254,a50
v_sub_f32 v254, v254, v249
v_mul_f32 v254, v254, v250
AW a50,v254
AR v251,a51
v_sub_f32 v251, v251, v249
v_mul_f32 v251, v251, v250
AW a51,v251
AR v254,a52
v_sub_f32 v254, v254, v249
v_mul_f32 v254, v254, v250
AW a52,v254
AR v251,a53
v_sub_f32 v251, v251, v249
v_mul_f32 v251, v251, v250
AW a53,v251
AR v254,a54
v_sub_f32 v254, v254, v249
v_mul_f32 v254, v254, v250
AW a54,v254
AR v251,a55
v_sub_f32 v251, v251, v249
v_mul_f32 v251, v251, v250
AW a55,v251
AR v254,a80
v_sub_f32 v254, v254, v249
v_mul_f32 v254, v254, v250
AW a80,v254
AR v251,a81
v_sub_f32 v251, v251, v249
v_mul_f32 v251, v251, v250
AW a81,v251
AR v254,a82
v_sub_f32 v254, v254, v249
v_mul_f32 v254, v254, v250
AW a82,v254
AR v251,a83
v_sub_f32 v251, v251, v249
v_mul_f32 v251, v251, v250
AW a83,v251
AR v254,a84
v_sub_f32 v254, v254, v249
v_mul_f32 v254, v254, v250
AW a84,v254
AR v251,a85
v_sub_f32 v251, v251, v249
v_mul_f32 v251, v251, v250
AW a85,v251
AR v254,a86
v_sub_f32 v254, v254, v249
v_mul_f32 v254, v254, v250
AW a86,v254
AR v251,a87
v_sub_f32 v251, v251, v249
v_mul_f32 v251, v251, v250
AW a87,v251
AR v254,a112
v_sub_f32 v254, v254, v249
v_mul_f32 v254, v254, v250
AW a112,v254
AR v251,a113
v_sub_f32 v251, v251, v249
v_mul_f32 v251, v251, v250
AW a113,v251
AR v254,a114
v_sub_f32 v254, v254, v249
v_mul_f32 v254, v254, v250
AW a114,v254
AR v251,a115
v_sub_f32 v251, v251, v249
v_mul_f32 v251, v251, v250
AW a115,v251
AR v254,a116
v_sub_f32 v254, v254, v249
v_mul_f32 v254, v254, v250
AW a116,v254
AR v251,a117
v_sub_f32 v251, v251, v249
v_mul_f32 v251, v251, v250
AW a117,v251
AR v254,a118
v_sub_f32 v254, v254, v249
v_mul_f32 v254, v254, v250
AW a118,v254
AR v251,a119
v_sub_f32 v251, v251, v249
v_mul_f32 v251, v251, v250
AW a119,v251
v_max3_f32 v249, v88, v89, v90
v_max3_f32 v251, v120, v121, v122
v_max3_f32 v249, v249, v91, v92
v_max3_f32 v251, v251, v123, v124
v_max3_f32 v249, v249, v93, v94
v_max3_f32 v251, v251, v125, v126
v_max3_f32 v249, v249, v95, v127
v_max_f32 v249, v249, v251
v_mov_b32 v251, v249
s_nop 1
v_permlane16_swap_b32 v249, v251
v_max_f32 v249, v249, v251
v_mov_b32 v251, v249
s_nop 1
v_permlane32_swap_b32 v249, v251
v_max_f32 v249, v249, v251
v_log_f32 v250, v247
s_nop 0
v_max3_f32 v249, 0, v249, v250
v_sub_f32 v250, 0, v249
EX v250,v250
v_sub_f32 v140, v140, v249
v_sub_f32 v141, v141, v249
v_sub_f32 v142, v142, v249
v_sub_f32 v143, v143, v249
v_sub_f32 v24, v24, v249
v_sub_f32 v88, v88, v249
v_sub_f32 v25, v25, v249
v_sub_f32 v89, v89, v249
v_sub_f32 v26, v26, v249
v_sub_f32 v90, v90, v249
v_sub_f32 v27, v27, v249
v_sub_f32 v91, v91, v249
v_sub_f32 v28, v28, v249
v_sub_f32 v92, v92, v249
v_sub_f32 v29, v29, v249
v_sub_f32 v93, v93, v249
v_sub_f32 v30, v30, v249
v_sub_f32 v94, v94, v249
v_sub_f32 v31, v31, v249
v_sub_f32 v95, v95, v249
v_sub_f32 v56, v56, v249
v_sub_f32 v120, v120, v249
v_sub_f32 v57, v57, v249
v_sub_f32 v121, v121, v249
v_sub_f32 v58, v58, v249
v_sub_f32 v122, v122, v249
v_sub_f32 v59, v59, v249
v_sub_f32 v123, v123, v249
v_sub_f32 v60, v60, v249
v_sub_f32 v124, v124, v249
v_sub_f32 v61, v61, v249
v_sub_f32 v125, v125, v249
v_sub_f32 v62, v62, v249
v_sub_f32 v126, v126, v249
v_sub_f32 v63, v63, v249
v_sub_f32 v127, v127, v249
v_mul_f32 v156, v247, v250
v_mov_b32 v157, 0
v_bfe_u32 v249, v241, 24, 8
v_lshl_or_b32 v249, v249, 1, 1
v_lshlrev_b32 v249, 22, v249
v_and_b32 v241, 0xffffff, v241
AR v254,a24
v_sub_f32 v254, v254, v249
v_mul_f32 v254, v254, v250
AW a24,v254
AR v251,a25
v_sub_f32 v251, v251, v249
v_mul_f32 v251, v251, v250
AW a25,v251
AR v254,a26
v_sub_f32 v254, v254, v249
v_mul_f32 v254, v254, v250
AW a26,v254
AR v251,a27
v_sub_f32 v251, v251, v249
v_mul_f32 v251, v251, v250
AW a27,v251
AR v254,a28
v_sub_f32 v254, v254, v249
v_mul_f32 v254, v254, v250
AW a28,v254
AR v251,a29
v_sub_f32 v251, v251, v249
v_mul_f32 v251, v251, v250
AW a29,v251
AR v254,a30
v_sub_f32 v254, v254, v249
v_mul_f32 v254, v254, v250
AW a30,v254
AR v251,a31
v_sub_f32 v251, v251, v249
v_mul_f32 v251, v251, v250
AW a31,v251
AR v254,a56
v_sub_f32 v254, v254, v249
v_mul_f32 v254, v254, v250
AW a56,v254
AR v251,a57
v_sub_f32 v251, v251, v249
v_mul_f32 v251, v251, v250
AW a57,v251
AR v254,a58
v_sub_f32 v254, v254, v249
v_mul_f32 v254, v254, v250
AW a58,v254
AR v251,a59
v_sub_f32 v251, v251, v249
v_mul_f32 v251, v251, v250
AW a59,v251
AR v254,a60
v_sub_f32 v254, v254, v249
v_mul_f32 v254, v254, v250
AW a60,v254
AR v251,a61
v_sub_f32 v251, v251, v249
v_mul_f32 v251, v251, v250
AW a61,v251
AR v254,a62
v_sub_f32 v254, v254, v249
v_mul_f32 v254, v254, v250
AW a62,v254
AR v251,a63
v_sub_f32 v251, v251, v249
v_mul_f32 v251, v251, v250
AW a63,v251
AR v254,a88
v_sub_f32 v254, v254, v249
v_mul_f32 v254, v254, v250
AW a88,v254
AR v251,a89
v_sub_f32 v251, v251, v249
v_mul_f32 v251, v251, v250
AW a89,v251
AR v254,a90
v_sub_f32 v254, v254, v249
v_mul_f32 v254, v254, v250
AW a90,v254
AR v251,a91
v_sub_f32 v251, v251, v249
v_mul_f32 v251, v251, v250
AW a91,v251
AR v254,a92
v_sub_f32 v254, v254, v249
v_mul_f32 v254, v254, v250
AW a92,v254
AR v251,a93
v_sub_f32 v251, v251, v249
v_mul_f32 v251, v251, v250
AW a93,v251
AR v254,a94
v_sub_f32 v254, v254, v249
v_mul_f32 v254, v254, v250
AW a94,v254
AR v251,a95
v_sub_f32 v251, v251, v249
v_mul_f32 v251, v251, v250
AW a95,v251
AR v254,a120
v_sub_f32 v254, v254, v249
v_mul_f32 v254, v254, v250
AW a120,v254
AR v251,a121
v_sub_f32 v251, v251, v249
v_mul_f32 v251, v251, v250
AW a121,v251
AR v254,a122
v_sub_f32 v254, v254, v249
v_mul_f32 v254, v254, v250
AW a122,v254
AR v251,a123
v_sub_f32 v251, v251, v249
v_mul_f32 v251, v251, v250
AW a123,v251
AR v254,a124
v_sub_f32 v254, v254, v249
v_mul_f32 v254, v254, v250
AW a124,v254
AR v251,a125
v_sub_f32 v251, v251, v249
v_mul_f32 v251, v251, v250
AW a125,v251
AR v254,a126
v_sub_f32 v254, v254, v249
v_mul_f32 v254, v254, v250
AW a126,v254
AR v251,a127
v_sub_f32 v251, v251, v249
v_mul_f32 v251, v251, v250
AW a127,v251
EX v64,v64
EX v65,v65
EX v66,v66
EX v67,v67
EX v68,v68
EX v69,v69
EX v70,v70
EX v71,v71
EX v72,v72
EX v73,v73
EX v74,v74
EX v75,v75
EX v76,v76
EX v77,v77
EX v78,v78
EX v79,v79
EX v80,v80
EX v81,v81
EX v82,v82
EX v83,v83
EX v84,v84
EX v85,v85
EX v86,v86
EX v87,v87
EX v88,v88
EX v89,v89
EX v90,v90
EX v91,v91
EX v92,v92
EX v93,v93
EX v94,v94
EX v95,v95
EX v96,v96
EX v97,v97
EX v98,v98
EX v99,v99
EX v100,v100
EX v101,v101
EX v102,v102
EX v103,v103
EX v104,v104
EX v105,v105
EX v106,v106
EX v107,v107
EX v108,v108
EX v109,v109
EX v110,v110
EX v111,v111
EX v112,v112
EX v113,v113
EX v114,v114
EX v115,v115
EX v116,v116
EX v117,v117
EX v118,v118
EX v119,v119
EX v120,v120
EX v121,v121
EX v122,v122
EX v123,v123
EX v124,v124
EX v125,v125
EX v126,v126
EX v127,v127
CV v64,v64,v65
CV v65,v66,v67
CV v66,v68,v69
CV v67,v70,v71
CV v72,v72,v73
CV v73,v74,v75
CV v74,v76,v77
CV v75,v78,v79
CV v80,v80,v81
CV v81,v82,v83
CV v82,v84,v85
CV v83,v86,v87
CV v88,v88,v89
CV v89,v90,v91
CV v90,v92,v93
CV v91,v94,v95
CV v68,v96,v97
CV v69,v98,v99
CV v70,v100,v101
CV v71,v102,v103
CV v76,v104,v105
CV v77,v106,v107
CV v78,v108,v109
CV v79,v110,v111
CV v84,v112,v113
CV v85,v114,v115
CV v86,v116,v117
CV v87,v118,v119
CV v92,v120,v121
CV v93,v122,v123
CV v94,v124,v125
CV v95,v126,v127
v_smfmac_f32_16x16x64_bf16 v[144:147], v[236:239], v[64:71], v234
v_smfmac_f32_16x16x64_bf16 v[148:151], v[236:239], v[72:79], v234
v_smfmac_f32_16x16x64_bf16 v[152:155], v[236:239], v[80:87], v234
v_smfmac_f32_16x16x64_bf16 v[156:159], v[236:239], v[88:95], v234
s_nop 9
s_branch LRET_%=
LRARE004_%=:
s_nop 15
s_waitcnt vmcnt(0) lgkmcnt(0)
v_add_u32 v249, s74, v224
RD a[192:195],v249 offset:0
RD a[208:211],v249 offset:4096
RD a[224:227],v249 offset:8192
RD a[240:243],v249 offset:12288
v_add_u32 v250, s74, v225
RD a[196:199],v250 offset:0
RD a[212:215],v250 offset:4096
RD a[228:231],v250 offset:8192
RD a[244:247],v250 offset:12288
v_add_u32 v251, s74, v226
RD a[200:203],v251 offset:0
RD a[216:219],v251 offset:4096
RD a[232:235],v251 offset:8192
RD a[248:251],v251 offset:12288
v_add_u32 v254, s74, v227
RD a[204:207],v254 offset:0
RD a[220:223],v254 offset:4096
RD a[236:239],v254 offset:8192
RD a[252:255],v254 offset:12288
s_waitcnt lgkmcnt(0)
MM v[0:3],a[192:195],v[160:163],v[128:131]
MM v[0:3],a[196:199],v[176:179],v[0:3]
MM v[0:3],a[200:203],v[192:195],v[0:3]
MM v[0:3],a[204:207],v[208:211],v[0:3]
MM v[4:7],a[220:223],v[208:211],v[128:131]
MM v[4:7],a[216:219],v[192:195],v[4:7]
)ASM"
R"ASM(
MM v[4:7],a[212:215],v[176:179],v[4:7]
MM v[4:7],a[208:211],v[160:163],v[4:7]
MM v[12:15],a[208:211],v[164:167],v[132:135]
MM v[12:15],a[212:215],v[180:183],v[12:15]
MM v[12:15],a[216:219],v[196:199],v[12:15]
MM v[12:15],a[220:223],v[212:215],v[12:15]
MM v[8:11],a[204:207],v[212:215],v[132:135]
MM v[8:11],a[200:203],v[196:199],v[8:11]
MM v[8:11],a[196:199],v[180:183],v[8:11]
MM v[8:11],a[192:195],v[164:167],v[8:11]
MM v[16:19],a[192:195],v[168:171],v[136:139]
MM v[16:19],a[196:199],v[184:187],v[16:19]
MM v[16:19],a[200:203],v[200:203],v[16:19]
MM v[16:19],a[204:207],v[216:219],v[16:19]
MM v[20:23],a[220:223],v[216:219],v[136:139]
MM v[20:23],a[216:219],v[200:203],v[20:23]
MM v[20:23],a[212:215],v[184:187],v[20:23]
MM v[20:23],a[208:211],v[168:171],v[20:23]
MM v[28:31],a[208:211],v[172:175],v[140:143]
MM v[28:31],a[212:215],v[188:191],v[28:31]
MM v[28:31],a[216:219],v[204:207],v[28:31]
MM v[28:31],a[220:223],v[220:223],v[28:31]
MM v[24:27],a[204:207],v[220:223],v[140:143]
MM v[24:27],a[200:203],v[204:207],v[24:27]
MM v[24:27],a[196:199],v[188:191],v[24:27]
MM v[24:27],a[192:195],v[172:175],v[24:27]
MM v[32:35],a[224:227],v[160:163],v[128:131]
MM v[32:35],a[228:231],v[176:179],v[32:35]
MM v[32:35],a[232:235],v[192:195],v[32:35]
MM v[32:35],a[236:239],v[208:211],v[32:35]
MM v[36:39],a[252:255],v[208:211],v[128:131]
MM v[36:39],a[248:251],v[192:195],v[36:39]
MM v[36:39],a[244:247],v[176:179],v[36:39]
MM v[36:39],a[240:243],v[160:163],v[36:39]
MM v[44:47],a[240:243],v[164:167],v[132:135]
MM v[44:47],a[244:247],v[180:183],v[44:47]
MM v[44:47],a[248:251],v[196:199],v[44:47]
MM v[44:47],a[252:255],v[212:215],v[44:47]
MM v[40:43],a[236:239],v[212:215],v[132:135]
MM v[40:43],a[232:235],v[196:199],v[40:43]
MM v[40:43],a[228:231],v[180:183],v[40:43]
MM v[40:43],a[224:227],v[164:167],v[40:43]
MM v[48:51],a[224:227],v[168:171],v[136:139]
MM v[48:51],a[228:231],v[184:187],v[48:51]
MM v[48:51],a[232:235],v[200:203],v[48:51]
MM v[48:51],a[236:239],v[216:219],v[48:51]
MM v[52:55],a[252:255],v[216:219],v[136:139]
MM v[52:55],a[248:251],v[200:203],v[52:55]
MM v[52:55],a[244:247],v[184:187],v[52:55]
MM v[52:55],a[240:243],v[168:171],v[52:55]
MM v[60:63],a[240:243],v[172:175],v[140:143]
MM v[60:63],a[244:247],v[188:191],v[60:63]
MM v[60:63],a[248:251],v[204:207],v[60:63]
MM v[60:63],a[252:255],v[220:223],v[60:63]
MM v[56:59],a[236:239],v[220:223],v[140:143]
MM v[56:59],a[232:235],v[204:207],v[56:59]
MM v[56:59],a[228:231],v[188:191],v[56:59]
MM v[56:59],a[224:227],v[172:175],v[56:59]
s_nop 15
v_add_u32 v249, s75, v224
RD a[192:195],v249 offset:0
RD a[208:211],v249 offset:4096
RD a[224:227],v249 offset:8192
RD a[240:243],v249 offset:12288
v_add_u32 v250, s75, v225
RD a[196:199],v250 offset:0
RD a[212:215],v250 offset:4096
RD a[228:231],v250 offset:8192
RD a[244:247],v250 offset:12288
v_add_u32 v251, s75, v226
RD a[200:203],v251 offset:0
RD a[216:219],v251 offset:4096
RD a[232:235],v251 offset:8192
RD a[248:251],v251 offset:12288
v_add_u32 v254, s75, v227
RD a[204:207],v254 offset:0
RD a[220:223],v254 offset:4096
RD a[236:239],v254 offset:8192
RD a[252:255],v254 offset:12288
s_waitcnt lgkmcnt(0)
v_max3_f32 v249, v0, v1, v2
v_max3_f32 v251, v32, v33, v34
v_max3_f32 v249, v249, v3, v4
v_max3_f32 v251, v251, v35, v36
v_max3_f32 v249, v249, v5, v6
v_max3_f32 v251, v251, v37, v38
v_max3_f32 v249, v249, v7, v39
v_max_f32 v249, v249, v251
v_mov_b32 v251, v249
s_nop 1
v_permlane16_swap_b32 v249, v251
v_max_f32 v249, v249, v251
v_mov_b32 v251, v249
s_nop 1
v_permlane32_swap_b32 v249, v251
v_max_f32 v249, v249, v251
v_log_f32 v250, v244
s_nop 0
v_max3_f32 v249, 0, v249, v250
v_sub_f32 v250, 0, v249
EX v250,v250
v_sub_f32 v128, v128, v249
v_sub_f32 v129, v129, v249
v_sub_f32 v130, v130, v249
v_sub_f32 v131, v131, v249
v_sub_f32 v64, v64, v249
v_sub_f32 v0, v0, v249
v_sub_f32 v65, v65, v249
v_sub_f32 v1, v1, v249
v_sub_f32 v66, v66, v249
v_sub_f32 v2, v2, v249
v_sub_f32 v67, v67, v249
v_sub_f32 v3, v3, v249
v_sub_f32 v68, v68, v249
v_sub_f32 v4, v4, v249
v_sub_f32 v69, v69, v249
v_sub_f32 v5, v5, v249
v_sub_f32 v70, v70, v249
v_sub_f32 v6, v6, v249
v_sub_f32 v71, v71, v249
v_sub_f32 v7, v7, v249
v_sub_f32 v96, v96, v249
v_sub_f32 v32, v32, v249
v_sub_f32 v97, v97, v249
v_sub_f32 v33, v33, v249
v_sub_f32 v98, v98, v249
v_sub_f32 v34, v34, v249
v_sub_f32 v99, v99, v249
v_sub_f32 v35, v35, v249
v_sub_f32 v100, v100, v249
v_sub_f32 v36, v36, v249
v_sub_f32 v101, v101, v249
v_sub_f32 v37, v37, v249
v_sub_f32 v102, v102, v249
v_sub_f32 v38, v38, v249
v_sub_f32 v103, v103, v249
v_sub_f32 v39, v39, v249
v_mul_f32 v144, v244, v250
v_mov_b32 v145, 0
v_bfe_u32 v249, v241, 0, 8
v_lshl_or_b32 v249, v249, 1, 1
v_lshlrev_b32 v249, 22, v249
v_and_b32 v241, 0xffffff00, v241
AR v254,a0
v_sub_f32 v254, v254, v249
v_mul_f32 v254, v254, v250
AW a0,v254
AR v251,a1
v_sub_f32 v251, v251, v249
v_mul_f32 v251, v251, v250
AW a1,v251
AR v254,a2
v_sub_f32 v254, v254, v249
v_mul_f32 v254, v254, v250
AW a2,v254
AR v251,a3
v_sub_f32 v251, v251, v249
v_mul_f32 v251, v251, v250
AW a3,v251
AR v254,a4
v_sub_f32 v254, v254, v249
v_mul_f32 v254, v254, v250
AW a4,v254
AR v251,a5
v_sub_f32 v251, v251, v249
v_mul_f32 v251, v251, v250
AW a5,v251
AR v254,a6
v_sub_f32 v254, v254, v249
v_mul_f32 v254, v254, v250
AW a6,v254
AR v251,a7
v_sub_f32 v251, v251, v249
v_mul_f32 v251, v251, v250
AW a7,v251
AR v254,a32
v_sub_f32 v254, v254, v249
v_mul_f32 v254, v254, v250
AW a32,v254
AR v251,a33
v_sub_f32 v251, v251, v249
v_mul_f32 v251, v251, v250
AW a33,v251
AR v254,a34
v_sub_f32 v254, v254, v249
v_mul_f32 v254, v254, v250
AW a34,v254
AR v251,a35
v_sub_f32 v251, v251, v249
v_mul_f32 v251, v251, v250
AW a35,v251
AR v254,a36
v_sub_f32 v254, v254, v249
v_mul_f32 v254, v254, v250
AW a36,v254
AR v251,a37
v_sub_f32 v251, v251, v249
v_mul_f32 v251, v251, v250
AW a37,v251
AR v254,a38
v_sub_f32 v254, v254, v249
v_mul_f32 v254, v254, v250
AW a38,v254
AR v251,a39
v_sub_f32 v251, v251, v249
v_mul_f32 v251, v251, v250
AW a39,v251
AR v254,a64
v_sub_f32 v254, v254, v249
v_mul_f32 v254, v254, v250
AW a64,v254
AR v251,a65
v_sub_f32 v251, v251, v249
v_mul_f32 v251, v251, v250
AW a65,v251
AR v254,a66
v_sub_f32 v254, v254, v249
v_mul_f32 v254, v254, v250
AW a66,v254
AR v251,a67
v_sub_f32 v251, v251, v249
v_mul_f32 v251, v251, v250
AW a67,v251
AR v254,a68
v_sub_f32 v254, v254, v249
v_mul_f32 v254, v254, v250
AW a68,v254
AR v251,a69
v_sub_f32 v251, v251, v249
v_mul_f32 v251, v251, v250
AW a69,v251
AR v254,a70
v_sub_f32 v254, v254, v249
v_mul_f32 v254, v254, v250
AW a70,v254
AR v251,a71
v_sub_f32 v251, v251, v249
v_mul_f32 v251, v251, v250
AW a71,v251
AR v254,a96
v_sub_f32 v254, v254, v249
v_mul_f32 v254, v254, v250
AW a96,v254
AR v251,a97
v_sub_f32 v251, v251, v249
v_mul_f32 v251, v251, v250
AW a97,v251
AR v254,a98
v_sub_f32 v254, v254, v249
v_mul_f32 v254, v254, v250
AW a98,v254
AR v251,a99
v_sub_f32 v251, v251, v249
v_mul_f32 v251, v251, v250
AW a99,v251
AR v254,a100
v_sub_f32 v254, v254, v249
v_mul_f32 v254, v254, v250
AW a100,v254
AR v251,a101
v_sub_f32 v251, v251, v249
v_mul_f32 v251, v251, v250
AW a101,v251
AR v254,a102
v_sub_f32 v254, v254, v249
v_mul_f32 v254, v254, v250
AW a102,v254
AR v251,a103
v_sub_f32 v251, v251, v249
v_mul_f32 v251, v251, v250
AW a103,v251
v_max3_f32 v249, v8, v9, v10
v_max3_f32 v251, v40, v41, v42
v_max3_f32 v249, v249, v11, v12
v_max3_f32 v251, v251, v43, v44
v_max3_f32 v249, v249, v13, v14
v_max3_f32 v251, v251, v45, v46
v_max3_f32 v249, v249, v15, v47
v_max_f32 v249, v249, v251
v_mov_b32 v251, v249
s_nop 1
v_permlane16_swap_b32 v249, v251
v_max_f32 v249, v249, v251
v_mov_b32 v251, v249
s_nop 1
v_permlane32_swap_b32 v249, v251
v_max_f32 v249, v249, v251
v_log_f32 v250, v245
s_nop 0
v_max3_f32 v249, 0, v249, v250
v_sub_f32 v250, 0, v249
EX v250,v250
v_sub_f32 v132, v132, v249
v_sub_f32 v133, v133, v249
v_sub_f32 v134, v134, v249
v_sub_f32 v135, v135, v249
v_sub_f32 v72, v72, v249
v_sub_f32 v8, v8, v249
v_sub_f32 v73, v73, v249
v_sub_f32 v9, v9, v249
v_sub_f32 v74, v74, v249
v_sub_f32 v10, v10, v249
v_sub_f32 v75, v75, v249
v_sub_f32 v11, v11, v249
v_sub_f32 v76, v76, v249
v_sub_f32 v12, v12, v249
v_sub_f32 v77, v77, v249
v_sub_f32 v13, v13, v249
v_sub_f32 v78, v78, v249
v_sub_f32 v14, v14, v249
v_sub_f32 v79, v79, v249
v_sub_f32 v15, v15, v249
v_sub_f32 v104, v104, v249
v_sub_f32 v40, v40, v249
v_sub_f32 v105, v105, v249
v_sub_f32 v41, v41, v249
v_sub_f32 v106, v106, v249
v_sub_f32 v42, v42, v249
v_sub_f32 v107, v107, v249
v_sub_f32 v43, v43, v249
v_sub_f32 v108, v108, v249
v_sub_f32 v44, v44, v249
v_sub_f32 v109, v109, v249
v_sub_f32 v45, v45, v249
v_sub_f32 v110, v110, v249
v_sub_f32 v46, v46, v249
v_sub_f32 v111, v111, v249
v_sub_f32 v47, v47, v249
v_mul_f32 v148, v245, v250
v_mov_b32 v149, 0
v_bfe_u32 v249, v241, 8, 8
v_lshl_or_b32 v249, v249, 1, 1
v_lshlrev_b32 v249, 22, v249
v_and_b32 v241, 0xffff00ff, v241
AR v254,a8
v_sub_f32 v254, v254, v249
v_mul_f32 v254, v254, v250
AW a8,v254
AR v251,a9
v_sub_f32 v251, v251, v249
v_mul_f32 v251, v251, v250
AW a9,v251
AR v254,a10
v_sub_f32 v254, v254, v249
v_mul_f32 v254, v254, v250
AW a10,v254
AR v251,a11
v_sub_f32 v251, v251, v249
v_mul_f32 v251, v251, v250
AW a11,v251
AR v254,a12
v_sub_f32 v254, v254, v249
v_mul_f32 v254, v254, v250
AW a12,v254
AR v251,a13
v_sub_f32 v251, v251, v249
v_mul_f32 v251, v251, v250
AW a13,v251
AR v254,a14
v_sub_f32 v254, v254, v249
v_mul_f32 v254, v254, v250
AW a14,v254
AR v251,a15
v_sub_f32 v251, v251, v249
v_mul_f32 v251, v251, v250
AW a15,v251
AR v254,a40
v_sub_f32 v254, v254, v249
v_mul_f32 v254, v254, v250
AW a40,v254
AR v251,a41
v_sub_f32 v251, v251, v249
v_mul_f32 v251, v251, v250
AW a41,v251
AR v254,a42
v_sub_f32 v254, v254, v249
v_mul_f32 v254, v254, v250
AW a42,v254
AR v251,a43
v_sub_f32 v251, v251, v249
v_mul_f32 v251, v251, v250
AW a43,v251
AR v254,a44
v_sub_f32 v254, v254, v249
v_mul_f32 v254, v254, v250
AW a44,v254
AR v251,a45
v_sub_f32 v251, v251, v249
v_mul_f32 v251, v251, v250
AW a45,v251
AR v254,a46
v_sub_f32 v254, v254, v249
v_mul_f32 v254, v254, v250
AW a46,v254
AR v251,a47
v_sub_f32 v251, v251, v249
v_mul_f32 v251, v251, v250
AW a47,v251
AR v254,a72
v_sub_f32 v254, v254, v249
v_mul_f32 v254, v254, v250
AW a72,v254
AR v251,a73
v_sub_f32 v251, v251, v249
v_mul_f32 v251, v251, v250
AW a73,v251
AR v254,a74
v_sub_f32 v254, v254, v249
v_mul_f32 v254, v254, v250
AW a74,v254
AR v251,a75
v_sub_f32 v251, v251, v249
v_mul_f32 v251, v251, v250
AW a75,v251
AR v254,a76
v_sub_f32 v254, v254, v249
v_mul_f32 v254, v254, v250
AW a76,v254
AR v251,a77
v_sub_f32 v251, v251, v249
v_mul_f32 v251, v251, v250
AW a77,v251
AR v254,a78
v_sub_f32 v254, v254, v249
v_mul_f32 v254, v254, v250
AW a78,v254
AR v251,a79
v_sub_f32 v251, v251, v249
v_mul_f32 v251, v251, v250
AW a79,v251
AR v254,a104
v_sub_f32 v254, v254, v249
v_mul_f32 v254, v254, v250
AW a104,v254
AR v251,a105
v_sub_f32 v251, v251, v249
v_mul_f32 v251, v251, v250
AW a105,v251
AR v254,a106
v_sub_f32 v254, v254, v249
v_mul_f32 v254, v254, v250
AW a106,v254
AR v251,a107
v_sub_f32 v251, v251, v249
v_mul_f32 v251, v251, v250
AW a107,v251
AR v254,a108
v_sub_f32 v254, v254, v249
v_mul_f32 v254, v254, v250
AW a108,v254
AR v251,a109
v_sub_f32 v251, v251, v249
v_mul_f32 v251, v251, v250
AW a109,v251
AR v254,a110
v_sub_f32 v254, v254, v249
v_mul_f32 v254, v254, v250
AW a110,v254
AR v251,a111
v_sub_f32 v251, v251, v249
v_mul_f32 v251, v251, v250
AW a111,v251
v_max3_f32 v249, v16, v17, v18
v_max3_f32 v251, v48, v49, v50
v_max3_f32 v249, v249, v19, v20
v_max3_f32 v251, v251, v51, v52
v_max3_f32 v249, v249, v21, v22
v_max3_f32 v251, v251, v53, v54
v_max3_f32 v249, v249, v23, v55
v_max_f32 v249, v249, v251
v_mov_b32 v251, v249
s_nop 1
v_permlane16_swap_b32 v249, v251
v_max_f32 v249, v249, v251
v_mov_b32 v251, v249
s_nop 1
v_permlane32_swap_b32 v249, v251
v_max_f32 v249, v249, v251
v_log_f32 v250, v246
s_nop 0
v_max3_f32 v249, 0, v249, v250
v_sub_f32 v250, 0, v249
EX v250,v250
v_sub_f32 v136, v136, v249
v_sub_f32 v137, v137, v249
v_sub_f32 v138, v138, v249
v_sub_f32 v139, v139, v249
v_sub_f32 v80, v80, v249
v_sub_f32 v16, v16, v249
v_sub_f32 v81, v81, v249
)ASM"
R"ASM(
v_sub_f32 v17, v17, v249
v_sub_f32 v82, v82, v249
v_sub_f32 v18, v18, v249
v_sub_f32 v83, v83, v249
v_sub_f32 v19, v19, v249
v_sub_f32 v84, v84, v249
v_sub_f32 v20, v20, v249
v_sub_f32 v85, v85, v249
v_sub_f32 v21, v21, v249
v_sub_f32 v86, v86, v249
v_sub_f32 v22, v22, v249
v_sub_f32 v87, v87, v249
v_sub_f32 v23, v23, v249
v_sub_f32 v112, v112, v249
v_sub_f32 v48, v48, v249
v_sub_f32 v113, v113, v249
v_sub_f32 v49, v49, v249
v_sub_f32 v114, v114, v249
v_sub_f32 v50, v50, v249
v_sub_f32 v115, v115, v249
v_sub_f32 v51, v51, v249
v_sub_f32 v116, v116, v249
v_sub_f32 v52, v52, v249
v_sub_f32 v117, v117, v249
v_sub_f32 v53, v53, v249
v_sub_f32 v118, v118, v249
v_sub_f32 v54, v54, v249
v_sub_f32 v119, v119, v249
v_sub_f32 v55, v55, v249
v_mul_f32 v152, v246, v250
v_mov_b32 v153, 0
v_bfe_u32 v249, v241, 16, 8
v_lshl_or_b32 v249, v249, 1, 1
v_lshlrev_b32 v249, 22, v249
v_and_b32 v241, 0xff00ffff, v241
AR v254,a16
v_sub_f32 v254, v254, v249
v_mul_f32 v254, v254, v250
AW a16,v254
AR v251,a17
v_sub_f32 v251, v251, v249
v_mul_f32 v251, v251, v250
AW a17,v251
AR v254,a18
v_sub_f32 v254, v254, v249
v_mul_f32 v254, v254, v250
AW a18,v254
AR v251,a19
v_sub_f32 v251, v251, v249
v_mul_f32 v251, v251, v250
AW a19,v251
AR v254,a20
v_sub_f32 v254, v254, v249
v_mul_f32 v254, v254, v250
AW a20,v254
AR v251,a21
v_sub_f32 v251, v251, v249
v_mul_f32 v251, v251, v250
AW a21,v251
AR v254,a22
v_sub_f32 v254, v254, v249
v_mul_f32 v254, v254, v250
AW a22,v254
AR v251,a23
v_sub_f32 v251, v251, v249
v_mul_f32 v251, v251, v250
AW a23,v251
AR v254,a48
v_sub_f32 v254, v254, v249
v_mul_f32 v254, v254, v250
AW a48,v254
AR v251,a49
v_sub_f32 v251, v251, v249
v_mul_f32 v251, v251, v250
AW a49,v251
AR v254,a50
v_sub_f32 v254, v254, v249
v_mul_f32 v254, v254, v250
AW a50,v254
AR v251,a51
v_sub_f32 v251, v251, v249
v_mul_f32 v251, v251, v250
AW a51,v251
AR v254,a52
v_sub_f32 v254, v254, v249
v_mul_f32 v254, v254, v250
AW a52,v254
AR v251,a53
v_sub_f32 v251, v251, v249
v_mul_f32 v251, v251, v250
AW a53,v251
AR v254,a54
v_sub_f32 v254, v254, v249
v_mul_f32 v254, v254, v250
AW a54,v254
AR v251,a55
v_sub_f32 v251, v251, v249
v_mul_f32 v251, v251, v250
AW a55,v251
AR v254,a80
v_sub_f32 v254, v254, v249
v_mul_f32 v254, v254, v250
AW a80,v254
AR v251,a81
v_sub_f32 v251, v251, v249
v_mul_f32 v251, v251, v250
AW a81,v251
AR v254,a82
v_sub_f32 v254, v254, v249
v_mul_f32 v254, v254, v250
AW a82,v254
AR v251,a83
v_sub_f32 v251, v251, v249
v_mul_f32 v251, v251, v250
AW a83,v251
AR v254,a84
v_sub_f32 v254, v254, v249
v_mul_f32 v254, v254, v250
AW a84,v254
AR v251,a85
v_sub_f32 v251, v251, v249
v_mul_f32 v251, v251, v250
AW a85,v251
AR v254,a86
v_sub_f32 v254, v254, v249
v_mul_f32 v254, v254, v250
AW a86,v254
AR v251,a87
v_sub_f32 v251, v251, v249
v_mul_f32 v251, v251, v250
AW a87,v251
AR v254,a112
v_sub_f32 v254, v254, v249
v_mul_f32 v254, v254, v250
AW a112,v254
AR v251,a113
v_sub_f32 v251, v251, v249
v_mul_f32 v251, v251, v250
AW a113,v251
AR v254,a114
v_sub_f32 v254, v254, v249
v_mul_f32 v254, v254, v250
AW a114,v254
AR v251,a115
v_sub_f32 v251, v251, v249
v_mul_f32 v251, v251, v250
AW a115,v251
AR v254,a116
v_sub_f32 v254, v254, v249
v_mul_f32 v254, v254, v250
AW a116,v254
AR v251,a117
v_sub_f32 v251, v251, v249
v_mul_f32 v251, v251, v250
AW a117,v251
AR v254,a118
v_sub_f32 v254, v254, v249
v_mul_f32 v254, v254, v250
AW a118,v254
AR v251,a119
v_sub_f32 v251, v251, v249
v_mul_f32 v251, v251, v250
AW a119,v251
v_max3_f32 v249, v24, v25, v26
v_max3_f32 v251, v56, v57, v58
v_max3_f32 v249, v249, v27, v28
v_max3_f32 v251, v251, v59, v60
v_max3_f32 v249, v249, v29, v30
v_max3_f32 v251, v251, v61, v62
v_max3_f32 v249, v249, v31, v63
v_max_f32 v249, v249, v251
v_mov_b32 v251, v249
s_nop 1
v_permlane16_swap_b32 v249, v251
v_max_f32 v249, v249, v251
v_mov_b32 v251, v249
s_nop 1
v_permlane32_swap_b32 v249, v251
v_max_f32 v249, v249, v251
v_log_f32 v250, v247
s_nop 0
v_max3_f32 v249, 0, v249, v250
v_sub_f32 v250, 0, v249
EX v250,v250
v_sub_f32 v140, v140, v249
v_sub_f32 v141, v141, v249
v_sub_f32 v142, v142, v249
v_sub_f32 v143, v143, v249
v_sub_f32 v88, v88, v249
v_sub_f32 v24, v24, v249
v_sub_f32 v89, v89, v249
v_sub_f32 v25, v25, v249
v_sub_f32 v90, v90, v249
v_sub_f32 v26, v26, v249
v_sub_f32 v91, v91, v249
v_sub_f32 v27, v27, v249
v_sub_f32 v92, v92, v249
v_sub_f32 v28, v28, v249
v_sub_f32 v93, v93, v249
v_sub_f32 v29, v29, v249
v_sub_f32 v94, v94, v249
v_sub_f32 v30, v30, v249
v_sub_f32 v95, v95, v249
v_sub_f32 v31, v31, v249
v_sub_f32 v120, v120, v249
v_sub_f32 v56, v56, v249
v_sub_f32 v121, v121, v249
v_sub_f32 v57, v57, v249
v_sub_f32 v122, v122, v249
v_sub_f32 v58, v58, v249
v_sub_f32 v123, v123, v249
v_sub_f32 v59, v59, v249
v_sub_f32 v124, v124, v249
v_sub_f32 v60, v60, v249
v_sub_f32 v125, v125, v249
v_sub_f32 v61, v61, v249
v_sub_f32 v126, v126, v249
v_sub_f32 v62, v62, v249
v_sub_f32 v127, v127, v249
v_sub_f32 v63, v63, v249
v_mul_f32 v156, v247, v250
v_mov_b32 v157, 0
v_bfe_u32 v249, v241, 24, 8
v_lshl_or_b32 v249, v249, 1, 1
v_lshlrev_b32 v249, 22, v249
v_and_b32 v241, 0xffffff, v241
AR v254,a24
v_sub_f32 v254, v254, v249
v_mul_f32 v254, v254, v250
AW a24,v254
AR v251,a25
v_sub_f32 v251, v251, v249
v_mul_f32 v251, v251, v250
AW a25,v251
AR v254,a26
v_sub_f32 v254, v254, v249
v_mul_f32 v254, v254, v250
AW a26,v254
AR v251,a27
v_sub_f32 v251, v251, v249
v_mul_f32 v251, v251, v250
AW a27,v251
AR v254,a28
v_sub_f32 v254, v254, v249
v_mul_f32 v254, v254, v250
AW a28,v254
AR v251,a29
v_sub_f32 v251, v251, v249
v_mul_f32 v251, v251, v250
AW a29,v251
AR v254,a30
v_sub_f32 v254, v254, v249
v_mul_f32 v254, v254, v250
AW a30,v254
AR v251,a31
v_sub_f32 v251, v251, v249
v_mul_f32 v251, v251, v250
AW a31,v251
AR v254,a56
v_sub_f32 v254, v254, v249
v_mul_f32 v254, v254, v250
AW a56,v254
AR v251,a57
v_sub_f32 v251, v251, v249
v_mul_f32 v251, v251, v250
AW a57,v251
AR v254,a58
v_sub_f32 v254, v254, v249
v_mul_f32 v254, v254, v250
AW a58,v254
AR v251,a59
v_sub_f32 v251, v251, v249
v_mul_f32 v251, v251, v250
AW a59,v251
AR v254,a60
v_sub_f32 v254, v254, v249
v_mul_f32 v254, v254, v250
AW a60,v254
AR v251,a61
v_sub_f32 v251, v251, v249
v_mul_f32 v251, v251, v250
AW a61,v251
AR v254,a62
v_sub_f32 v254, v254, v249
v_mul_f32 v254, v254, v250
AW a62,v254
AR v251,a63
v_sub_f32 v251, v251, v249
v_mul_f32 v251, v251, v250
AW a63,v251
AR v254,a88
v_sub_f32 v254, v254, v249
v_mul_f32 v254, v254, v250
AW a88,v254
AR v251,a89
v_sub_f32 v251, v251, v249
v_mul_f32 v251, v251, v250
AW a89,v251
AR v254,a90
v_sub_f32 v254, v254, v249
v_mul_f32 v254, v254, v250
AW a90,v254
AR v251,a91
v_sub_f32 v251, v251, v249
v_mul_f32 v251, v251, v250
AW a91,v251
AR v254,a92
v_sub_f32 v254, v254, v249
v_mul_f32 v254, v254, v250
AW a92,v254
AR v251,a93
v_sub_f32 v251, v251, v249
v_mul_f32 v251, v251, v250
AW a93,v251
AR v254,a94
v_sub_f32 v254, v254, v249
v_mul_f32 v254, v254, v250
AW a94,v254
AR v251,a95
v_sub_f32 v251, v251, v249
v_mul_f32 v251, v251, v250
AW a95,v251
AR v254,a120
v_sub_f32 v254, v254, v249
v_mul_f32 v254, v254, v250
AW a120,v254
AR v251,a121
v_sub_f32 v251, v251, v249
v_mul_f32 v251, v251, v250
AW a121,v251
AR v254,a122
v_sub_f32 v254, v254, v249
v_mul_f32 v254, v254, v250
AW a122,v254
AR v251,a123
v_sub_f32 v251, v251, v249
v_mul_f32 v251, v251, v250
AW a123,v251
AR v254,a124
v_sub_f32 v254, v254, v249
v_mul_f32 v254, v254, v250
AW a124,v254
AR v251,a125
v_sub_f32 v251, v251, v249
v_mul_f32 v251, v251, v250
AW a125,v251
AR v254,a126
v_sub_f32 v254, v254, v249
v_mul_f32 v254, v254, v250
AW a126,v254
AR v251,a127
v_sub_f32 v251, v251, v249
v_mul_f32 v251, v251, v250
AW a127,v251
EX v0,v0
EX v1,v1
EX v2,v2
EX v3,v3
EX v4,v4
EX v5,v5
EX v6,v6
EX v7,v7
EX v8,v8
EX v9,v9
EX v10,v10
EX v11,v11
EX v12,v12
EX v13,v13
EX v14,v14
EX v15,v15
EX v16,v16
EX v17,v17
EX v18,v18
EX v19,v19
EX v20,v20
EX v21,v21
EX v22,v22
EX v23,v23
EX v24,v24
EX v25,v25
EX v26,v26
EX v27,v27
EX v28,v28
EX v29,v29
EX v30,v30
EX v31,v31
EX v32,v32
EX v33,v33
EX v34,v34
EX v35,v35
EX v36,v36
EX v37,v37
EX v38,v38
EX v39,v39
EX v40,v40
EX v41,v41
EX v42,v42
EX v43,v43
EX v44,v44
EX v45,v45
EX v46,v46
EX v47,v47
EX v48,v48
EX v49,v49
EX v50,v50
EX v51,v51
EX v52,v52
EX v53,v53
EX v54,v54
EX v55,v55
EX v56,v56
EX v57,v57
EX v58,v58
EX v59,v59
EX v60,v60
EX v61,v61
EX v62,v62
EX v63,v63
CV v0,v0,v1
CV v1,v2,v3
CV v2,v4,v5
CV v3,v6,v7
CV v8,v8,v9
CV v9,v10,v11
CV v10,v12,v13
CV v11,v14,v15
CV v16,v16,v17
CV v17,v18,v19
CV v18,v20,v21
CV v19,v22,v23
CV v24,v24,v25
CV v25,v26,v27
CV v26,v28,v29
CV v27,v30,v31
CV v4,v32,v33
CV v5,v34,v35
CV v6,v36,v37
CV v7,v38,v39
CV v12,v40,v41
CV v13,v42,v43
CV v14,v44,v45
CV v15,v46,v47
CV v20,v48,v49
CV v21,v50,v51
CV v22,v52,v53
CV v23,v54,v55
CV v28,v56,v57
CV v29,v58,v59
CV v30,v60,v61
CV v31,v62,v63
v_smfmac_f32_16x16x64_bf16 v[144:147], v[236:239], v[0:7], v234
v_smfmac_f32_16x16x64_bf16 v[148:151], v[236:239], v[8:15], v234
v_smfmac_f32_16x16x64_bf16 v[152:155], v[236:239], v[16:23], v234
v_smfmac_f32_16x16x64_bf16 v[156:159], v[236:239], v[24:31], v234
s_nop 9
s_branch LRET_%=
LRARE022_%=:
s_nop 15
s_waitcnt vmcnt(0) lgkmcnt(0)
v_add_u32 v249, s74, v224
RD a[192:195],v249 offset:0
RD a[208:211],v249 offset:4096
RD a[224:227],v249 offset:8192
RD a[240:243],v249 offset:12288
v_add_u32 v250, s74, v225
RD a[196:199],v250 offset:0
RD a[212:215],v250 offset:4096
RD a[228:231],v250 offset:8192
RD a[244:247],v250 offset:12288
v_add_u32 v251, s74, v226
RD a[200:203],v251 offset:0
RD a[216:219],v251 offset:4096
RD a[232:235],v251 offset:8192
RD a[248:251],v251 offset:12288
v_add_u32 v254, s74, v227
RD a[204:207],v254 offset:0
RD a[220:223],v254 offset:4096
RD a[236:239],v254 offset:8192
RD a[252:255],v254 offset:12288
s_waitcnt lgkmcnt(0)
MM v[16:19],a[192:195],v[168:171],v[136:139]
MM v[16:19],a[196:199],v[184:187],v[16:19]
MM v[16:19],a[200:203],v[200:203],v[16:19]
MM v[16:19],a[204:207],v[216:219],v[16:19]
MM v[20:23],a[220:223],v[216:219],v[136:139]
MM v[20:23],a[216:219],v[200:203],v[20:23]
MM v[20:23],a[212:215],v[184:187],v[20:23]
MM v[20:23],a[208:211],v[168:171],v[20:23]
MM v[28:31],a[208:211],v[172:175],v[140:143]
MM v[28:31],a[212:215],v[188:191],v[28:31]
MM v[28:31],a[216:219],v[204:207],v[28:31]
MM v[28:31],a[220:223],v[220:223],v[28:31]
MM v[24:27],a[204:207],v[220:223],v[140:143]
MM v[24:27],a[200:203],v[204:207],v[24:27]
MM v[24:27],a[196:199],v[188:191],v[24:27]
MM v[24:27],a[192:195],v[172:175],v[24:27]
MM v[48:51],a[224:227],v[168:171],v[136:139]
MM v[48:51],a[228:231],v[184:187],v[48:51]
MM v[48:51],a[232:235],v[200:203],v[48:51]
MM v[48:51],a[236:239],v[216:219],v[48:51]
MM v[52:55],a[252:255],v[216:219],v[136:139]
MM v[52:55],a[248:251],v[200:203],v[52:55]
MM v[52:55],a[244:247],v[184:187],v[52:55]
MM v[52:55],a[240:243],v[168:171],v[52:55]
MM v[60:63],a[240:243],v[172:175],v[140:143]
MM v[60:63],a[244:247],v[188:191],v[60:63]
MM v[60:63],a[248:251],v[204:207],v[60:63]
MM v[60:63],a[252:255],v[220:223],v[60:63]
MM v[56:59],a[236:239],v[220:223],v[140:143]
MM v[56:59],a[232:235],v[204:207],v[56:59]
MM v[56:59],a[228:231],v[188:191],v[56:59]
MM v[56:59],a[224:227],v[172:175],v[56:59]
s_nop 15
v_add_u32 v249, s75, v224
RD a[192:195],v249 offset:0
RD a[208:211],v249 offset:4096
RD a[224:227],v249 offset:8192
RD a[240:243],v249 offset:12288
v_add_u32 v250, s75, v225
RD a[196:199],v250 offset:0
RD a[212:215],v250 offset:4096
RD a[228:231],v250 offset:8192
RD a[244:247],v250 offset:12288
v_add_u32 v251, s75, v226
RD a[200:203],v251 offset:0
RD a[216:219],v251 offset:4096
RD a[232:235],v251 offset:8192
RD a[248:251],v251 offset:12288
v_add_u32 v254, s75, v227
RD a[204:207],v254 offset:0
RD a[220:223],v254 offset:4096
RD a[236:239],v254 offset:8192
RD a[252:255],v254 offset:12288
s_waitcnt lgkmcnt(0)
v_max3_f32 v249, v16, v17, v18
v_max3_f32 v251, v48, v49, v50
v_max3_f32 v249, v249, v19, v20
v_max3_f32 v251, v251, v51, v52
v_max3_f32 v249, v249, v21, v22
v_max3_f32 v251, v251, v53, v54
v_max3_f32 v249, v249, v23, v55
v_max_f32 v249, v249, v251
v_mov_b32 v251, v249
s_nop 1
v_permlane16_swap_b32 v249, v251
v_max_f32 v249, v249, v251
v_mov_b32 v251, v249
)ASM"
R"ASM(
s_nop 1
v_permlane32_swap_b32 v249, v251
v_max_f32 v249, v249, v251
v_log_f32 v250, v246
s_nop 0
v_max3_f32 v249, 0, v249, v250
v_sub_f32 v250, 0, v249
EX v250,v250
v_sub_f32 v136, v136, v249
v_sub_f32 v137, v137, v249
v_sub_f32 v138, v138, v249
v_sub_f32 v139, v139, v249
v_sub_f32 v80, v80, v249
v_sub_f32 v16, v16, v249
v_sub_f32 v81, v81, v249
v_sub_f32 v17, v17, v249
v_sub_f32 v82, v82, v249
v_sub_f32 v18, v18, v249
v_sub_f32 v83, v83, v249
v_sub_f32 v19, v19, v249
v_sub_f32 v84, v84, v249
v_sub_f32 v20, v20, v249
v_sub_f32 v85, v85, v249
v_sub_f32 v21, v21, v249
v_sub_f32 v86, v86, v249
v_sub_f32 v22, v22, v249
v_sub_f32 v87, v87, v249
v_sub_f32 v23, v23, v249
v_sub_f32 v112, v112, v249
v_sub_f32 v48, v48, v249
v_sub_f32 v113, v113, v249
v_sub_f32 v49, v49, v249
v_sub_f32 v114, v114, v249
v_sub_f32 v50, v50, v249
v_sub_f32 v115, v115, v249
v_sub_f32 v51, v51, v249
v_sub_f32 v116, v116, v249
v_sub_f32 v52, v52, v249
v_sub_f32 v117, v117, v249
v_sub_f32 v53, v53, v249
v_sub_f32 v118, v118, v249
v_sub_f32 v54, v54, v249
v_sub_f32 v119, v119, v249
v_sub_f32 v55, v55, v249
v_mul_f32 v152, v246, v250
v_mov_b32 v153, 0
v_bfe_u32 v249, v241, 16, 8
v_lshl_or_b32 v249, v249, 1, 1
v_lshlrev_b32 v249, 22, v249
v_and_b32 v241, 0xff00ffff, v241
AR v254,a16
v_sub_f32 v254, v254, v249
v_mul_f32 v254, v254, v250
AW a16,v254
AR v251,a17
v_sub_f32 v251, v251, v249
v_mul_f32 v251, v251, v250
AW a17,v251
AR v254,a18
v_sub_f32 v254, v254, v249
v_mul_f32 v254, v254, v250
AW a18,v254
AR v251,a19
v_sub_f32 v251, v251, v249
v_mul_f32 v251, v251, v250
AW a19,v251
AR v254,a20
v_sub_f32 v254, v254, v249
v_mul_f32 v254, v254, v250
AW a20,v254
AR v251,a21
v_sub_f32 v251, v251, v249
v_mul_f32 v251, v251, v250
AW a21,v251
AR v254,a22
v_sub_f32 v254, v254, v249
v_mul_f32 v254, v254, v250
AW a22,v254
AR v251,a23
v_sub_f32 v251, v251, v249
v_mul_f32 v251, v251, v250
AW a23,v251
AR v254,a48
v_sub_f32 v254, v254, v249
v_mul_f32 v254, v254, v250
AW a48,v254
AR v251,a49
v_sub_f32 v251, v251, v249
v_mul_f32 v251, v251, v250
AW a49,v251
AR v254,a50
v_sub_f32 v254, v254, v249
v_mul_f32 v254, v254, v250
AW a50,v254
AR v251,a51
v_sub_f32 v251, v251, v249
v_mul_f32 v251, v251, v250
AW a51,v251
AR v254,a52
v_sub_f32 v254, v254, v249
v_mul_f32 v254, v254, v250
AW a52,v254
AR v251,a53
v_sub_f32 v251, v251, v249
v_mul_f32 v251, v251, v250
AW a53,v251
AR v254,a54
v_sub_f32 v254, v254, v249
v_mul_f32 v254, v254, v250
AW a54,v254
AR v251,a55
v_sub_f32 v251, v251, v249
v_mul_f32 v251, v251, v250
AW a55,v251
AR v254,a80
v_sub_f32 v254, v254, v249
v_mul_f32 v254, v254, v250
AW a80,v254
AR v251,a81
v_sub_f32 v251, v251, v249
v_mul_f32 v251, v251, v250
AW a81,v251
AR v254,a82
v_sub_f32 v254, v254, v249
v_mul_f32 v254, v254, v250
AW a82,v254
AR v251,a83
v_sub_f32 v251, v251, v249
v_mul_f32 v251, v251, v250
AW a83,v251
AR v254,a84
v_sub_f32 v254, v254, v249
v_mul_f32 v254, v254, v250
AW a84,v254
AR v251,a85
v_sub_f32 v251, v251, v249
v_mul_f32 v251, v251, v250
AW a85,v251
AR v254,a86
v_sub_f32 v254, v254, v249
v_mul_f32 v254, v254, v250
AW a86,v254
AR v251,a87
v_sub_f32 v251, v251, v249
v_mul_f32 v251, v251, v250
AW a87,v251
AR v254,a112
v_sub_f32 v254, v254, v249
v_mul_f32 v254, v254, v250
AW a112,v254
AR v251,a113
v_sub_f32 v251, v251, v249
v_mul_f32 v251, v251, v250
AW a113,v251
AR v254,a114
v_sub_f32 v254, v254, v249
v_mul_f32 v254, v254, v250
AW a114,v254
AR v251,a115
v_sub_f32 v251, v251, v249
v_mul_f32 v251, v251, v250
AW a115,v251
AR v254,a116
v_sub_f32 v254, v254, v249
v_mul_f32 v254, v254, v250
AW a116,v254
AR v251,a117
v_sub_f32 v251, v251, v249
v_mul_f32 v251, v251, v250
AW a117,v251
AR v254,a118
v_sub_f32 v254, v254, v249
v_mul_f32 v254, v254, v250
AW a118,v254
AR v251,a119
v_sub_f32 v251, v251, v249
v_mul_f32 v251, v251, v250
AW a119,v251
v_max3_f32 v249, v24, v25, v26
v_max3_f32 v251, v56, v57, v58
v_max3_f32 v249, v249, v27, v28
v_max3_f32 v251, v251, v59, v60
v_max3_f32 v249, v249, v29, v30
v_max3_f32 v251, v251, v61, v62
v_max3_f32 v249, v249, v31, v63
v_max_f32 v249, v249, v251
v_mov_b32 v251, v249
s_nop 1
v_permlane16_swap_b32 v249, v251
v_max_f32 v249, v249, v251
v_mov_b32 v251, v249
s_nop 1
v_permlane32_swap_b32 v249, v251
v_max_f32 v249, v249, v251
v_log_f32 v250, v247
s_nop 0
v_max3_f32 v249, 0, v249, v250
v_sub_f32 v250, 0, v249
EX v250,v250
v_sub_f32 v140, v140, v249
v_sub_f32 v141, v141, v249
v_sub_f32 v142, v142, v249
v_sub_f32 v143, v143, v249
v_sub_f32 v88, v88, v249
v_sub_f32 v24, v24, v249
v_sub_f32 v89, v89, v249
v_sub_f32 v25, v25, v249
v_sub_f32 v90, v90, v249
v_sub_f32 v26, v26, v249
v_sub_f32 v91, v91, v249
v_sub_f32 v27, v27, v249
v_sub_f32 v92, v92, v249
v_sub_f32 v28, v28, v249
v_sub_f32 v93, v93, v249
v_sub_f32 v29, v29, v249
v_sub_f32 v94, v94, v249
v_sub_f32 v30, v30, v249
v_sub_f32 v95, v95, v249
v_sub_f32 v31, v31, v249
v_sub_f32 v120, v120, v249
v_sub_f32 v56, v56, v249
v_sub_f32 v121, v121, v249
v_sub_f32 v57, v57, v249
v_sub_f32 v122, v122, v249
v_sub_f32 v58, v58, v249
v_sub_f32 v123, v123, v249
v_sub_f32 v59, v59, v249
v_sub_f32 v124, v124, v249
v_sub_f32 v60, v60, v249
v_sub_f32 v125, v125, v249
v_sub_f32 v61, v61, v249
v_sub_f32 v126, v126, v249
v_sub_f32 v62, v62, v249
v_sub_f32 v127, v127, v249
v_sub_f32 v63, v63, v249
v_mul_f32 v156, v247, v250
v_mov_b32 v157, 0
v_bfe_u32 v249, v241, 24, 8
v_lshl_or_b32 v249, v249, 1, 1
v_lshlrev_b32 v249, 22, v249
v_and_b32 v241, 0xffffff, v241
AR v254,a24
v_sub_f32 v254, v254, v249
v_mul_f32 v254, v254, v250
AW a24,v254
AR v251,a25
v_sub_f32 v251, v251, v249
v_mul_f32 v251, v251, v250
AW a25,v251
AR v254,a26
v_sub_f32 v254, v254, v249
v_mul_f32 v254, v254, v250
AW a26,v254
AR v251,a27
v_sub_f32 v251, v251, v249
v_mul_f32 v251, v251, v250
AW a27,v251
AR v254,a28
v_sub_f32 v254, v254, v249
v_mul_f32 v254, v254, v250
AW a28,v254
AR v251,a29
v_sub_f32 v251, v251, v249
v_mul_f32 v251, v251, v250
AW a29,v251
AR v254,a30
v_sub_f32 v254, v254, v249
v_mul_f32 v254, v254, v250
AW a30,v254
AR v251,a31
v_sub_f32 v251, v251, v249
v_mul_f32 v251, v251, v250
AW a31,v251
AR v254,a56
v_sub_f32 v254, v254, v249
v_mul_f32 v254, v254, v250
AW a56,v254
AR v251,a57
v_sub_f32 v251, v251, v249
v_mul_f32 v251, v251, v250
AW a57,v251
AR v254,a58
v_sub_f32 v254, v254, v249
v_mul_f32 v254, v254, v250
AW a58,v254
AR v251,a59
v_sub_f32 v251, v251, v249
v_mul_f32 v251, v251, v250
AW a59,v251
AR v254,a60
v_sub_f32 v254, v254, v249
v_mul_f32 v254, v254, v250
AW a60,v254
AR v251,a61
v_sub_f32 v251, v251, v249
v_mul_f32 v251, v251, v250
AW a61,v251
AR v254,a62
v_sub_f32 v254, v254, v249
v_mul_f32 v254, v254, v250
AW a62,v254
AR v251,a63
v_sub_f32 v251, v251, v249
v_mul_f32 v251, v251, v250
AW a63,v251
AR v254,a88
v_sub_f32 v254, v254, v249
v_mul_f32 v254, v254, v250
AW a88,v254
AR v251,a89
v_sub_f32 v251, v251, v249
v_mul_f32 v251, v251, v250
AW a89,v251
AR v254,a90
v_sub_f32 v254, v254, v249
v_mul_f32 v254, v254, v250
AW a90,v254
AR v251,a91
v_sub_f32 v251, v251, v249
v_mul_f32 v251, v251, v250
AW a91,v251
AR v254,a92
v_sub_f32 v254, v254, v249
v_mul_f32 v254, v254, v250
AW a92,v254
AR v251,a93
v_sub_f32 v251, v251, v249
v_mul_f32 v251, v251, v250
AW a93,v251
AR v254,a94
v_sub_f32 v254, v254, v249
v_mul_f32 v254, v254, v250
AW a94,v254
AR v251,a95
v_sub_f32 v251, v251, v249
v_mul_f32 v251, v251, v250
AW a95,v251
AR v254,a120
v_sub_f32 v254, v254, v249
v_mul_f32 v254, v254, v250
AW a120,v254
AR v251,a121
v_sub_f32 v251, v251, v249
v_mul_f32 v251, v251, v250
AW a121,v251
AR v254,a122
v_sub_f32 v254, v254, v249
v_mul_f32 v254, v254, v250
AW a122,v254
AR v251,a123
v_sub_f32 v251, v251, v249
v_mul_f32 v251, v251, v250
AW a123,v251
AR v254,a124
v_sub_f32 v254, v254, v249
v_mul_f32 v254, v254, v250
AW a124,v254
AR v251,a125
v_sub_f32 v251, v251, v249
v_mul_f32 v251, v251, v250
AW a125,v251
AR v254,a126
v_sub_f32 v254, v254, v249
v_mul_f32 v254, v254, v250
AW a126,v254
AR v251,a127
v_sub_f32 v251, v251, v249
v_mul_f32 v251, v251, v250
AW a127,v251
EX v16,v16
EX v17,v17
EX v18,v18
EX v19,v19
EX v20,v20
EX v21,v21
EX v22,v22
EX v23,v23
EX v24,v24
EX v25,v25
EX v26,v26
EX v27,v27
EX v28,v28
EX v29,v29
EX v30,v30
EX v31,v31
EX v48,v48
EX v49,v49
EX v50,v50
EX v51,v51
EX v52,v52
EX v53,v53
EX v54,v54
EX v55,v55
EX v56,v56
EX v57,v57
EX v58,v58
EX v59,v59
EX v60,v60
EX v61,v61
EX v62,v62
EX v63,v63
CV v16,v16,v17
CV v17,v18,v19
CV v18,v20,v21
CV v19,v22,v23
CV v24,v24,v25
CV v25,v26,v27
CV v26,v28,v29
CV v27,v30,v31
CV v20,v48,v49
CV v21,v50,v51
CV v22,v52,v53
CV v23,v54,v55
CV v28,v56,v57
CV v29,v58,v59
CV v30,v60,v61
CV v31,v62,v63
v_smfmac_f32_16x16x64_bf16 v[152:155], v[236:239], v[16:23], v234
s_nop 0
v_smfmac_f32_16x16x64_bf16 v[156:159], v[236:239], v[24:31], v234
s_nop 9
s_branch LRET_%=
LRARE122_%=:
s_nop 15
s_waitcnt vmcnt(0) lgkmcnt(0)
v_add_u32 v249, s74, v224
RD a[192:195],v249 offset:0
RD a[208:211],v249 offset:4096
RD a[224:227],v249 offset:8192
RD a[240:243],v249 offset:12288
v_add_u32 v250, s74, v225
RD a[196:199],v250 offset:0
RD a[212:215],v250 offset:4096
RD a[228:231],v250 offset:8192
RD a[244:247],v250 offset:12288
v_add_u32 v251, s74, v226
RD a[200:203],v251 offset:0
RD a[216:219],v251 offset:4096
RD a[232:235],v251 offset:8192
RD a[248:251],v251 offset:12288
v_add_u32 v254, s74, v227
RD a[204:207],v254 offset:0
RD a[220:223],v254 offset:4096
RD a[236:239],v254 offset:8192
RD a[252:255],v254 offset:12288
s_waitcnt lgkmcnt(0)
MM v[80:83],a[192:195],v[168:171],v[136:139]
MM v[80:83],a[196:199],v[184:187],v[80:83]
MM v[80:83],a[200:203],v[200:203],v[80:83]
MM v[80:83],a[204:207],v[216:219],v[80:83]
MM v[84:87],a[220:223],v[216:219],v[136:139]
MM v[84:87],a[216:219],v[200:203],v[84:87]
MM v[84:87],a[212:215],v[184:187],v[84:87]
MM v[84:87],a[208:211],v[168:171],v[84:87]
MM v[92:95],a[208:211],v[172:175],v[140:143]
MM v[92:95],a[212:215],v[188:191],v[92:95]
MM v[92:95],a[216:219],v[204:207],v[92:95]
MM v[92:95],a[220:223],v[220:223],v[92:95]
MM v[88:91],a[204:207],v[220:223],v[140:143]
MM v[88:91],a[200:203],v[204:207],v[88:91]
MM v[88:91],a[196:199],v[188:191],v[88:91]
MM v[88:91],a[192:195],v[172:175],v[88:91]
MM v[112:115],a[224:227],v[168:171],v[136:139]
MM v[112:115],a[228:231],v[184:187],v[112:115]
MM v[112:115],a[232:235],v[200:203],v[112:115]
MM v[112:115],a[236:239],v[216:219],v[112:115]
MM v[116:119],a[252:255],v[216:219],v[136:139]
MM v[116:119],a[248:251],v[200:203],v[116:119]
MM v[116:119],a[244:247],v[184:187],v[116:119]
MM v[116:119],a[240:243],v[168:171],v[116:119]
MM v[124:127],a[240:243],v[172:175],v[140:143]
MM v[124:127],a[244:247],v[188:191],v[124:127]
MM v[124:127],a[248:251],v[204:207],v[124:127]
MM v[124:127],a[252:255],v[220:223],v[124:127]
MM v[120:123],a[236:239],v[220:223],v[140:143]
MM v[120:123],a[232:235],v[204:207],v[120:123]
MM v[120:123],a[228:231],v[188:191],v[120:123]
MM v[120:123],a[224:227],v[172:175],v[120:123]
s_nop 15
v_add_u32 v249, s75, v224
RD a[192:195],v249 offset:0
RD a[208:211],v249 offset:4096
RD a[224:227],v249 offset:8192
RD a[240:243],v249 offset:12288
v_add_u32 v250, s75, v225
RD a[196:199],v250 offset:0
RD a[212:215],v250 offset:4096
RD a[228:231],v250 offset:8192
RD a[244:247],v250 offset:12288
v_add_u32 v251, s75, v226
RD a[200:203],v251 offset:0
RD a[216:219],v251 offset:4096
RD a[232:235],v251 offset:8192
RD a[248:251],v251 offset:12288
v_add_u32 v254, s75, v227
RD a[204:207],v254 offset:0
RD a[220:223],v254 offset:4096
RD a[236:239],v254 offset:8192
RD a[252:255],v254 offset:12288
s_waitcnt lgkmcnt(0)
v_max3_f32 v249, v80, v81, v82
v_max3_f32 v251, v112, v113, v114
v_max3_f32 v249, v249, v83, v84
v_max3_f32 v251, v251, v115, v116
v_max3_f32 v249, v249, v85, v86
v_max3_f32 v251, v251, v117, v118
v_max3_f32 v249, v249, v87, v119
v_max_f32 v249, v249, v251
v_mov_b32 v251, v249
s_nop 1
v_permlane16_swap_b32 v249, v251
v_max_f32 v249, v249, v251
v_mov_b32 v251, v249
s_nop 1
v_permlane32_swap_b32 v249, v251
v_max_f32 v249, v249, v251
v_log_f32 v250, v246
s_nop 0
v_max3_f32 v249, 0, v249, v250
v_sub_f32 v250, 0, v249
EX v250,v250
v_sub_f32 v136, v136, v249
v_sub_f32 v137, v137, v249
v_sub_f32 v138, v138, v249
v_sub_f32 v139, v139, v249
)ASM"
R"ASM(
v_sub_f32 v16, v16, v249
v_sub_f32 v80, v80, v249
v_sub_f32 v17, v17, v249
v_sub_f32 v81, v81, v249
v_sub_f32 v18, v18, v249
v_sub_f32 v82, v82, v249
v_sub_f32 v19, v19, v249
v_sub_f32 v83, v83, v249
v_sub_f32 v20, v20, v249
v_sub_f32 v84, v84, v249
v_sub_f32 v21, v21, v249
v_sub_f32 v85, v85, v249
v_sub_f32 v22, v22, v249
v_sub_f32 v86, v86, v249
v_sub_f32 v23, v23, v249
v_sub_f32 v87, v87, v249
v_sub_f32 v48, v48, v249
v_sub_f32 v112, v112, v249
v_sub_f32 v49, v49, v249
v_sub_f32 v113, v113, v249
v_sub_f32 v50, v50, v249
v_sub_f32 v114, v114, v249
v_sub_f32 v51, v51, v249
v_sub_f32 v115, v115, v249
v_sub_f32 v52, v52, v249
v_sub_f32 v116, v116, v249
v_sub_f32 v53, v53, v249
v_sub_f32 v117, v117, v249
v_sub_f32 v54, v54, v249
v_sub_f32 v118, v118, v249
v_sub_f32 v55, v55, v249
v_sub_f32 v119, v119, v249
v_mul_f32 v152, v246, v250
v_mov_b32 v153, 0
v_bfe_u32 v249, v241, 16, 8
v_lshl_or_b32 v249, v249, 1, 1
v_lshlrev_b32 v249, 22, v249
v_and_b32 v241, 0xff00ffff, v241
AR v254,a16
v_sub_f32 v254, v254, v249
v_mul_f32 v254, v254, v250
AW a16,v254
AR v251,a17
v_sub_f32 v251, v251, v249
v_mul_f32 v251, v251, v250
AW a17,v251
AR v254,a18
v_sub_f32 v254, v254, v249
v_mul_f32 v254, v254, v250
AW a18,v254
AR v251,a19
v_sub_f32 v251, v251, v249
v_mul_f32 v251, v251, v250
AW a19,v251
AR v254,a20
v_sub_f32 v254, v254, v249
v_mul_f32 v254, v254, v250
AW a20,v254
AR v251,a21
v_sub_f32 v251, v251, v249
v_mul_f32 v251, v251, v250
AW a21,v251
AR v254,a22
v_sub_f32 v254, v254, v249
v_mul_f32 v254, v254, v250
AW a22,v254
AR v251,a23
v_sub_f32 v251, v251, v249
v_mul_f32 v251, v251, v250
AW a23,v251
AR v254,a48
v_sub_f32 v254, v254, v249
v_mul_f32 v254, v254, v250
AW a48,v254
AR v251,a49
v_sub_f32 v251, v251, v249
v_mul_f32 v251, v251, v250
AW a49,v251
AR v254,a50
v_sub_f32 v254, v254, v249
v_mul_f32 v254, v254, v250
AW a50,v254
AR v251,a51
v_sub_f32 v251, v251, v249
v_mul_f32 v251, v251, v250
AW a51,v251
AR v254,a52
v_sub_f32 v254, v254, v249
v_mul_f32 v254, v254, v250
AW a52,v254
AR v251,a53
v_sub_f32 v251, v251, v249
v_mul_f32 v251, v251, v250
AW a53,v251
AR v254,a54
v_sub_f32 v254, v254, v249
v_mul_f32 v254, v254, v250
AW a54,v254
AR v251,a55
v_sub_f32 v251, v251, v249
v_mul_f32 v251, v251, v250
AW a55,v251
AR v254,a80
v_sub_f32 v254, v254, v249
v_mul_f32 v254, v254, v250
AW a80,v254
AR v251,a81
v_sub_f32 v251, v251, v249
v_mul_f32 v251, v251, v250
AW a81,v251
AR v254,a82
v_sub_f32 v254, v254, v249
v_mul_f32 v254, v254, v250
AW a82,v254
AR v251,a83
v_sub_f32 v251, v251, v249
v_mul_f32 v251, v251, v250
AW a83,v251
AR v254,a84
v_sub_f32 v254, v254, v249
v_mul_f32 v254, v254, v250
AW a84,v254
AR v251,a85
v_sub_f32 v251, v251, v249
v_mul_f32 v251, v251, v250
AW a85,v251
AR v254,a86
v_sub_f32 v254, v254, v249
v_mul_f32 v254, v254, v250
AW a86,v254
AR v251,a87
v_sub_f32 v251, v251, v249
v_mul_f32 v251, v251, v250
AW a87,v251
AR v254,a112
v_sub_f32 v254, v254, v249
v_mul_f32 v254, v254, v250
AW a112,v254
AR v251,a113
v_sub_f32 v251, v251, v249
v_mul_f32 v251, v251, v250
AW a113,v251
AR v254,a114
v_sub_f32 v254, v254, v249
v_mul_f32 v254, v254, v250
AW a114,v254
AR v251,a115
v_sub_f32 v251, v251, v249
v_mul_f32 v251, v251, v250
AW a115,v251
AR v254,a116
v_sub_f32 v254, v254, v249
v_mul_f32 v254, v254, v250
AW a116,v254
AR v251,a117
v_sub_f32 v251, v251, v249
v_mul_f32 v251, v251, v250
AW a117,v251
AR v254,a118
v_sub_f32 v254, v254, v249
v_mul_f32 v254, v254, v250
AW a118,v254
AR v251,a119
v_sub_f32 v251, v251, v249
v_mul_f32 v251, v251, v250
AW a119,v251
v_max3_f32 v249, v88, v89, v90
v_max3_f32 v251, v120, v121, v122
v_max3_f32 v249, v249, v91, v92
v_max3_f32 v251, v251, v123, v124
v_max3_f32 v249, v249, v93, v94
v_max3_f32 v251, v251, v125, v126
v_max3_f32 v249, v249, v95, v127
v_max_f32 v249, v249, v251
v_mov_b32 v251, v249
s_nop 1
v_permlane16_swap_b32 v249, v251
v_max_f32 v249, v249, v251
v_mov_b32 v251, v249
s_nop 1
v_permlane32_swap_b32 v249, v251
v_max_f32 v249, v249, v251
v_log_f32 v250, v247
s_nop 0
v_max3_f32 v249, 0, v249, v250
v_sub_f32 v250, 0, v249
EX v250,v250
v_sub_f32 v140, v140, v249
v_sub_f32 v141, v141, v249
v_sub_f32 v142, v142, v249
v_sub_f32 v143, v143, v249
v_sub_f32 v24, v24, v249
v_sub_f32 v88, v88, v249
v_sub_f32 v25, v25, v249
v_sub_f32 v89, v89, v249
v_sub_f32 v26, v26, v249
v_sub_f32 v90, v90, v249
v_sub_f32 v27, v27, v249
v_sub_f32 v91, v91, v249
v_sub_f32 v28, v28, v249
v_sub_f32 v92, v92, v249
v_sub_f32 v29, v29, v249
v_sub_f32 v93, v93, v249
v_sub_f32 v30, v30, v249
v_sub_f32 v94, v94, v249
v_sub_f32 v31, v31, v249
v_sub_f32 v95, v95, v249
v_sub_f32 v56, v56, v249
v_sub_f32 v120, v120, v249
v_sub_f32 v57, v57, v249
v_sub_f32 v121, v121, v249
v_sub_f32 v58, v58, v249
v_sub_f32 v122, v122, v249
v_sub_f32 v59, v59, v249
v_sub_f32 v123, v123, v249
v_sub_f32 v60, v60, v249
v_sub_f32 v124, v124, v249
v_sub_f32 v61, v61, v249
v_sub_f32 v125, v125, v249
v_sub_f32 v62, v62, v249
v_sub_f32 v126, v126, v249
v_sub_f32 v63, v63, v249
v_sub_f32 v127, v127, v249
v_mul_f32 v156, v247, v250
v_mov_b32 v157, 0
v_bfe_u32 v249, v241, 24, 8
v_lshl_or_b32 v249, v249, 1, 1
v_lshlrev_b32 v249, 22, v249
v_and_b32 v241, 0xffffff, v241
AR v254,a24
v_sub_f32 v254, v254, v249
v_mul_f32 v254, v254, v250
AW a24,v254
AR v251,a25
v_sub_f32 v251, v251, v249
v_mul_f32 v251, v251, v250
AW a25,v251
AR v254,a26
v_sub_f32 v254, v254, v249
v_mul_f32 v254, v254, v250
AW a26,v254
AR v251,a27
v_sub_f32 v251, v251, v249
v_mul_f32 v251, v251, v250
AW a27,v251
AR v254,a28
v_sub_f32 v254, v254, v249
v_mul_f32 v254, v254, v250
AW a28,v254
AR v251,a29
v_sub_f32 v251, v251, v249
v_mul_f32 v251, v251, v250
AW a29,v251
AR v254,a30
v_sub_f32 v254, v254, v249
v_mul_f32 v254, v254, v250
AW a30,v254
AR v251,a31
v_sub_f32 v251, v251, v249
v_mul_f32 v251, v251, v250
AW a31,v251
AR v254,a56
v_sub_f32 v254, v254, v249
v_mul_f32 v254, v254, v250
AW a56,v254
AR v251,a57
v_sub_f32 v251, v251, v249
v_mul_f32 v251, v251, v250
AW a57,v251
AR v254,a58
v_sub_f32 v254, v254, v249
v_mul_f32 v254, v254, v250
AW a58,v254
AR v251,a59
v_sub_f32 v251, v251, v249
v_mul_f32 v251, v251, v250
AW a59,v251
AR v254,a60
v_sub_f32 v254, v254, v249
v_mul_f32 v254, v254, v250
AW a60,v254
AR v251,a61
v_sub_f32 v251, v251, v249
v_mul_f32 v251, v251, v250
AW a61,v251
AR v254,a62
v_sub_f32 v254, v254, v249
v_mul_f32 v254, v254, v250
AW a62,v254
AR v251,a63
v_sub_f32 v251, v251, v249
v_mul_f32 v251, v251, v250
AW a63,v251
AR v254,a88
v_sub_f32 v254, v254, v249
v_mul_f32 v254, v254, v250
AW a88,v254
AR v251,a89
v_sub_f32 v251, v251, v249
v_mul_f32 v251, v251, v250
AW a89,v251
AR v254,a90
v_sub_f32 v254, v254, v249
v_mul_f32 v254, v254, v250
AW a90,v254
AR v251,a91
v_sub_f32 v251, v251, v249
v_mul_f32 v251, v251, v250
AW a91,v251
AR v254,a92
v_sub_f32 v254, v254, v249
v_mul_f32 v254, v254, v250
AW a92,v254
AR v251,a93
v_sub_f32 v251, v251, v249
v_mul_f32 v251, v251, v250
AW a93,v251
AR v254,a94
v_sub_f32 v254, v254, v249
v_mul_f32 v254, v254, v250
AW a94,v254
AR v251,a95
v_sub_f32 v251, v251, v249
v_mul_f32 v251, v251, v250
AW a95,v251
AR v254,a120
v_sub_f32 v254, v254, v249
v_mul_f32 v254, v254, v250
AW a120,v254
AR v251,a121
v_sub_f32 v251, v251, v249
v_mul_f32 v251, v251, v250
AW a121,v251
AR v254,a122
v_sub_f32 v254, v254, v249
v_mul_f32 v254, v254, v250
AW a122,v254
AR v251,a123
v_sub_f32 v251, v251, v249
v_mul_f32 v251, v251, v250
AW a123,v251
AR v254,a124
v_sub_f32 v254, v254, v249
v_mul_f32 v254, v254, v250
AW a124,v254
AR v251,a125
v_sub_f32 v251, v251, v249
v_mul_f32 v251, v251, v250
AW a125,v251
AR v254,a126
v_sub_f32 v254, v254, v249
v_mul_f32 v254, v254, v250
AW a126,v254
AR v251,a127
v_sub_f32 v251, v251, v249
v_mul_f32 v251, v251, v250
AW a127,v251
EX v80,v80
EX v81,v81
EX v82,v82
EX v83,v83
EX v84,v84
EX v85,v85
EX v86,v86
EX v87,v87
EX v88,v88
EX v89,v89
EX v90,v90
EX v91,v91
EX v92,v92
EX v93,v93
EX v94,v94
EX v95,v95
EX v112,v112
EX v113,v113
EX v114,v114
EX v115,v115
EX v116,v116
EX v117,v117
EX v118,v118
EX v119,v119
EX v120,v120
EX v121,v121
EX v122,v122
EX v123,v123
EX v124,v124
EX v125,v125
EX v126,v126
EX v127,v127
CV v80,v80,v81
CV v81,v82,v83
CV v82,v84,v85
CV v83,v86,v87
CV v88,v88,v89
CV v89,v90,v91
CV v90,v92,v93
CV v91,v94,v95
CV v84,v112,v113
CV v85,v114,v115
CV v86,v116,v117
CV v87,v118,v119
CV v92,v120,v121
CV v93,v122,v123
CV v94,v124,v125
CV v95,v126,v127
v_smfmac_f32_16x16x64_bf16 v[152:155], v[236:239], v[80:87], v234
s_nop 0
v_smfmac_f32_16x16x64_bf16 v[156:159], v[236:239], v[88:95], v234
s_nop 9
s_branch LRET_%=
LEND_%=:
)ASM"

    :: [ka] "s"(__builtin_amdgcn_kernarg_segment_ptr()), [slds] "s"(slds), [swave] "s"(w), [sb] "s"(blockIdx.x)
    : "memory", "vcc", "scc", CLOB_V, CLOB_A, CLOB_S);
}
constexpr int DYN_LDS = 163840;

extern "C" void kernel_launch(void* const* d_in, const int* in_sizes, int n_in, void* d_out, int out_size, void* d_ws, size_t ws_size, hipStream_t stream) {
#if !defined(__HIP_DEVICE_COMPILE__)
  if (DYN_LDS) hipFuncSetAttribute((const void*)ka_fused, hipFuncAttributeMaxDynamicSharedMemorySize, DYN_LDS);
  ka_fused<<<256, 256, DYN_LDS, stream>>>(d_in[3], d_in[0], d_ws, d_in[1], d_in[6], d_in[2], d_in[4], d_in[7], d_in[8], d_in[9], d_out);
#endif
}
```

```cpp
#include <hip/hip_runtime.h>
#include <stdint.h>

using f32x4  = __attribute__((ext_vector_type(4))) float;
using u32x4  = __attribute__((ext_vector_type(4))) unsigned;
using u32x2  = __attribute__((ext_vector_type(2))) unsigned;

constexpr int SEQ = 2048, DM = 640, NH = 32, DH = 128, HD = NH * DH, FF = 1728, TOPC = 8;

constexpr unsigned WS_FO  = 0;
constexpr unsigned WS_EP  = 32768;
constexpr unsigned WS_G1  = 36864;
constexpr unsigned WS_G2  = 53248;
constexpr unsigned WS_FH  = 69632;
constexpr unsigned WS_FY  = 73728;
constexpr unsigned WS_FG  = 77824;
constexpr unsigned WS_FR  = 81920;
constexpr unsigned WS_RK  = 86016;
constexpr unsigned WS_XN  = 131072;
constexpr unsigned WS_OF  = WS_XN + SEQ * DM * 2;
constexpr unsigned WS_H   = WS_OF + (18u << 20);
constexpr unsigned WS_Y   = WS_H + 32768;
constexpr unsigned WS_P   = WS_Y + 1048576;
constexpr unsigned WS_SPAN = 64u << 20;

#define CLOB_V "v0","v1","v2","v3","v4","v5","v6","v7","v8","v9","v10","v11","v12","v13","v14","v15","v16","v17","v18","v19","v20","v21","v22","v23","v24","v25","v26","v27","v28","v29","v30","v31","v32","v33","v34","v35","v36","v37","v38","v39","v40","v41","v42","v43","v44","v45","v46","v47","v48","v49","v50","v51","v52","v53","v54","v55","v56","v57","v58","v59","v60","v61","v62","v63","v64","v65","v66","v67","v68","v69","v70","v71","v72","v73","v74","v75","v76","v77","v78","v79","v80","v81","v82","v83","v84","v85","v86","v87","v88","v89","v90","v91","v92","v93","v94","v95","v96","v97","v98","v99","v100","v101","v102","v103","v104","v105","v106","v107","v108","v109","v110","v111","v112","v113","v114","v115","v116","v117","v118","v119","v120","v121","v122","v123","v124","v125","v126","v127","v128","v129","v130","v131","v132","v133","v134","v135","v136","v137","v138","v139","v140","v141","v142","v143","v144","v145","v146","v147","v148","v149","v150","v151","v152","v153","v154","v155","v156","v157","v158","v159","v160","v161","v162","v163","v164","v165","v166","v167","v168","v169","v170","v171","v172","v173","v174","v175","v176","v177","v178","v179","v180","v181","v182","v183","v184","v185","v186","v187","v188","v189","v190","v191","v192","v193","v194","v195","v196","v197","v198","v199","v200","v201","v202","v203","v204","v205","v206","v207","v208","v209","v210","v211","v212","v213","v214","v215","v216","v217","v218","v219","v220","v221","v222","v223","v224","v225","v226","v227","v228","v229","v230","v231","v232","v233","v234","v235","v236","v237","v238","v239","v240","v241","v242","v243","v244","v245","v246","v247","v248","v249","v250","v251","v252","v253","v254","v255"
#define CLOB_A "a0","a1","a2","a3","a4","a5","a6","a7","a8","a9","a10","a11","a12","a13","a14","a15","a16","a17","a18","a19","a20","a21","a22","a23","a24","a25","a26","a27","a28","a29","a30","a31","a32","a33","a34","a35","a36","a37","a38","a39","a40","a41","a42","a43","a44","a45","a46","a47","a48","a49","a50","a51","a52","a53","a54","a55","a56","a57","a58","a59","a60","a61","a62","a63","a64","a65","a66","a67","a68","a69","a70","a71","a72","a73","a74","a75","a76","a77","a78","a79","a80","a81","a82","a83","a84","a85","a86","a87","a88","a89","a90","a91","a92","a93","a94","a95","a96","a97","a98","a99","a100","a101","a102","a103","a104","a105","a106","a107","a108","a109","a110","a111","a112","a113","a114","a115","a116","a117","a118","a119","a120","a121","a122","a123","a124","a125","a126","a127","a128","a129","a130","a131","a132","a133","a134","a135","a136","a137","a138","a139","a140","a141","a142","a143","a144","a145","a146","a147","a148","a149","a150","a151","a152","a153","a154","a155","a156","a157","a158","a159","a160","a161","a162","a163","a164","a165","a166","a167","a168","a169","a170","a171","a172","a173","a174","a175","a176","a177","a178","a179","a180","a181","a182","a183","a184","a185","a186","a187","a188","a189","a190","a191","a192","a193","a194","a195","a196","a197","a198","a199","a200","a201","a202","a203","a204","a205","a206","a207","a208","a209","a210","a211","a212","a213","a214","a215","a216","a217","a218","a219","a220","a221","a222","a223","a224","a225","a226","a227","a228","a229","a230","a231","a232","a233","a234","a235","a236","a237","a238","a239","a240","a241","a242","a243","a244","a245","a246","a247","a248","a249","a250","a251","a252","a253","a254","a255"
#define CLOB_S "s8","s9","s10","s11","s12","s13","s14","s15","s16","s17","s18","s19","s20","s21","s22","s23","s24","s25","s26","s27","s28","s29","s30","s31","s32","s33","s34","s35","s36","s37","s38","s39","s40","s41","s42","s43","s44","s45","s46","s47","s48","s49","s50","s51","s52","s53","s54","s55","s56","s57","s58","s59","s60","s61","s62","s63","s64","s65","s66","s67","s68","s69","s70","s71","s72","s73","s74","s75","s76","s77","s78","s79","s80","s81","s82","s83","s84","s85","s86","s87","s88","s89","s90","s91","s92","s93","s94","s95","s96","s97","s98","s99"
__global__ __launch_bounds__(256, 1) __attribute__((aligned(4096))) void ka_fused(const void* wq, const void* x, void* ws, const void* Kc, const void* wr, const void* Vc, const void* wo, const void* wg, const void* wu, const void* wd, void* out) {
  extern __shared__ __attribute__((aligned(16))) char lds[];
  const unsigned w = __builtin_amdgcn_readfirstlane(threadIdx.x >> 6);
  const unsigned slds = __builtin_amdgcn_readfirstlane((unsigned)(size_t)lds);
  asm volatile(
R"ASM(
.macro MM a, b, c, d:vararg
v_mfma_f32_16x16x32_bf16 \a, \b, \c, \d
.endm
.macro TR a, b:vararg
ds_read_b64_tr_b16 \a, \b
.endm
.macro CV a, b, c:vararg
v_cvt_pk_bf16_f32 \a, \b, \c
.endm
.macro AR a, b:vararg
v_accvgpr_read_b32 \a, \b
.endm
.macro AW a, b:vararg
v_accvgpr_write_b32 \a, \b
.endm
.macro EX a, b:vararg
v_exp_f32 \a, \b
.endm
.macro RD a, b:vararg
ds_read_b128 \a, \b
.endm
s_load_dwordx2 s[16:17], %[ka], 0
s_load_dwordx2 s[32:33], %[ka], 8
s_lshr_b32 s77, %[sb], 3
v_lshlrev_b32 v118, 4, v0
s_getpc_b64 s[98:99]
s_andn2_b32 s98, s98, 63
v_lshl_add_u32 v118, s77, 12, v118
global_load_dwordx4 a[252:255], v118, s[98:99]
.p2align 6
LPG0_%=:
s_load_dwordx2 s[20:21], %[ka], 16
v_mbcnt_lo_u32_b32 v127, -1, 0
v_mbcnt_hi_u32_b32 v127, -1, v127
v_lshlrev_b32 v248, 4, v127
s_and_b32 s77, %[sb], 7
s_lshl_b32 s77, s77, 2
s_bfe_u32 s84, %[sb], 131075
s_add_u32 s49, s77, s84
s_lshr_b32 s50, %[sb], 5
s_lshl_b32 s76, %[sb], 2
s_add_u32 s76, s76, %[swave]
v_lshrrev_b32 v180, 5, v127
s_mul_i32 s82, s76, 2560
v_and_b32 v182, 496, v248
v_mul_u32_u24 v181, 1280, v180
v_add_u32 v168, v181, v182
v_lshrrev_b32 v210, 1, v182
v_add_u32 v181, v181, v210
s_lshl_b32 s77, %[swave], 12
s_add_u32 s65, s77, %[slds]
s_lshl_b32 s89, %[swave], 17
v_bfe_u32 v210, v127, 2, 3
v_and_b32 v212, 3, v127
v_lshlrev_b32 v213, 4, v212
v_lshl_add_u32 v213, v180, 6, v213
v_lshrrev_b32 v253, 3, v127
v_lshlrev_b32 v253, 13, v253
v_and_b32 v254, 112, v248
v_or_b32 v253, v253, v254
v_and_b32 v254, 16, v127
v_lshlrev_b32 v254, 2, v254
v_xor_b32 v253, v253, v254
v_add_u32 v254, 128, v253
s_mul_i32 s96, %[swave], 4112
s_add_u32 s96, s96, %[slds]
s_lshl_b32 s77, s50, 2
s_add_u32 s77, s77, %[swave]
s_mul_i32 s68, s77, 81920
s_and_b32 s62, %[sb], 1
s_mul_i32 s63, s62, 40960
s_add_u32 s68, s68, s63
s_lshl_b32 s63, s63, 1
s_mul_i32 s83, s77, 560128
s_add_u32 s83, s83, 2752512
s_lshl_b32 s93, s77, 9
s_lshl_b32 s94, %[swave], 13
s_add_u32 s94, s94, %[slds]
v_lshrrev_b32 v211, 3, v127
v_mul_u32_u24 v220, 1280, v211
v_lshrrev_b32 v212, 4, v127
v_and_b32 v211, 7, v127
v_xor_b32 v211, v211, v212
v_lshl_add_u32 v220, v211, 4, v220
v_xor_b32 v221, 64, v220
s_bitcmp1_b32 s84, 0
s_mov_b32 s77, 0x77552200
s_cselect_b32 s77, 0x77552200, s77
s_lshl_b32 s91, s50, 2
s_lshr_b32 s77, s77, s91
s_and_b32 s77, s77, 15
s_lshl_b32 s86, s77, 7
s_lshl_b32 s85, s77, 19
s_lshl_b32 s90, s49, 8
s_sub_u32 s18, 5242880, s90
s_mov_b32 s19, 131072
s_mov_b32 s34, 2621440
s_mov_b32 s35, 131072
s_mov_b32 s22, 67108864
s_mov_b32 s23, 131072
s_add_u32 s87, s89, s85
s_add_u32 s85, s85, 524288
s_cmp_ge_u32 s85, 5242880
s_cselect_b32 s85, 0, s85
s_add_u32 m0, s96, 65536
s_waitcnt lgkmcnt(0)
s_add_u32 s16, s16, s90
s_addc_u32 s17, s17, 0
buffer_load_dwordx4 v253, s[16:19], s87 offen offset:0 lds
s_add_u32 m0, s96, 66560
s_nop 0
buffer_load_dwordx4 v254, s[16:19], s87 offen offset:0 lds
s_add_u32 s87, s87, 65536
s_add_u32 m0, s96, 67592
s_nop 0
buffer_load_dwordx4 v253, s[16:19], s87 offen offset:0 lds
s_add_u32 m0, s96, 68616
s_nop 0
buffer_load_dwordx4 v254, s[16:19], s87 offen offset:0 lds
s_add_u32 s88, s68, s86
s_add_u32 s86, s86, 128
s_cmp_ge_u32 s86, 1280
s_cselect_b32 s86, 0, s86
s_add_u32 m0, s94, 0
s_add_u32 s95, s88, 0
buffer_load_dwordx4 v220, s[32:35], s95 offen offset:0 lds
s_add_u32 s95, s88, 9216
buffer_load_dwordx4 v221, s[32:35], s95 offen offset:1024 lds
s_add_u32 s95, s88, 18432
buffer_load_dwordx4 v220, s[32:35], s95 offen offset:2048 lds
s_add_u32 s95, s88, 27648
buffer_load_dwordx4 v221, s[32:35], s95 offen offset:3072 lds
s_load_dwordx2 s[8:9], %[ka], 24
s_load_dwordx2 s[12:13], %[ka], 40
s_load_dwordx2 s[24:25], %[ka], 48
s_load_dwordx2 s[44:45], %[ka], 32
s_load_dwordx2 s[28:29], %[ka], 72
s_load_dwordx2 s[40:41], %[ka], 80
s_lshl_b32 s77, %[swave], 2
s_and_b32 s77, s77, 8
s_add_u32 s77, s77, 56
s_load_dwordx2 s[36:37], %[ka], s77
s_mov_b32 s66, 32768
buffer_load_dword v169, off, s[20:23], s66 sc1
buffer_load_dwordx4 v[128:131], v168, s[32:35], s82 offen offset:0
buffer_load_dwordx4 v[132:135], v168, s[32:35], s82 offen offset:512
buffer_load_dwordx2 v[136:137], v181, s[32:35], s82 offen offset:1024
s_add_u32 s82, s82, 131072
s_sub_u32 s88, s88, s63
s_add_u32 m0, s94, 4096
s_add_u32 s95, s88, 40960
buffer_load_dwordx4 v220, s[32:35], s95 offen offset:0 lds
s_add_u32 s95, s88, 50176
buffer_load_dwordx4 v221, s[32:35], s95 offen offset:1024 lds
s_add_u32 s95, s88, 59392
buffer_load_dwordx4 v220, s[32:35], s95 offen offset:2048 lds
s_add_u32 s95, s88, 68608
buffer_load_dwordx4 v221, s[32:35], s95 offen offset:3072 lds
v_and_b32 v211, 15, v127
v_bfe_u32 v222, v127, 1, 3
v_xor_b32 v222, v222, v212
v_lshlrev_b32 v222, 4, v222
v_lshl_add_u32 v222, v211, 7, v222
v_xor_b32 v223, 64, v222
v_add_u32 v222, s94, v222
v_add_u32 v223, s94, v223
v_and_b32 v214, 1, v212
v_lshlrev_b32 v215, 5, v214
v_xor_b32 v215, v215, v213
v_lshl_add_u32 v235, v210, 8, v215
v_add_u32 v235, 128, v235
v_and_b32 v234, 1, v127
v_mul_u32_u24 v234, 43690, v234
v_add_u32 v234, 17476, v234
v_bfe_u32 v215, v127, 2, 2
v_and_b32 v216, 3, v127
v_lshlrev_b32 v252, 7, v215
v_lshl_add_u32 v252, v216, 4, v252
v_and_b32 v251, 8, v127
v_lshl_add_u32 v252, v251, 3, v252
v_mul_u32_u24 v251, 2056, v212
s_xor_b32 s77, %[swave], 0
s_and_b32 s91, s77, 1
s_lshl_b32 s91, s91, 6
s_lshr_b32 s77, s77, 1
s_lshl_b32 s77, s77, 10
s_add_u32 s77, s77, %[slds]
s_add_u32 s77, s77, 65536
v_xor_b32 v192, s91, v252
v_add3_u32 v192, s77, v192, v251
v_add_u32 v196, 49344, v192
s_xor_b32 s77, %[swave], 1
s_and_b32 s91, s77, 1
s_lshl_b32 s91, s91, 6
s_lshr_b32 s77, s77, 1
s_lshl_b32 s77, s77, 10
s_add_u32 s77, s77, %[slds]
s_add_u32 s77, s77, 65536
v_xor_b32 v193, s91, v252
v_add3_u32 v193, s77, v193, v251
v_add_u32 v197, 49344, v193
s_xor_b32 s77, %[swave], 2
s_and_b32 s91, s77, 1
s_lshl_b32 s91, s91, 6
s_lshr_b32 s77, s77, 1
s_lshl_b32 s77, s77, 10
s_add_u32 s77, s77, %[slds]
s_add_u32 s77, s77, 65536
v_xor_b32 v194, s91, v252
v_add3_u32 v194, s77, v194, v251
v_add_u32 v198, 49344, v194
s_xor_b32 s77, %[swave], 3
s_and_b32 s91, s77, 1
s_lshl_b32 s91, s91, 6
s_lshr_b32 s77, s77, 1
s_lshl_b32 s77, s77, 10
s_add_u32 s77, s77, %[slds]
s_add_u32 s77, s77, 65536
v_xor_b32 v195, s91, v252
v_add3_u32 v195, s77, v195, v251
v_add_u32 v199, 49344, v195
v_mov_b32 v236, 0x3f803f80
v_mov_b32 v184, 0
v_mov_b32 v237, 0x3f803f80
v_mov_b32 v185, 0
v_mov_b32 v238, 0x3f803f80
v_mov_b32 v188, 0
v_mov_b32 v239, 0x3f803f80
v_mov_b32 v189, 0
s_add_u32 s87, s89, s85
s_add_u32 s85, s85, 524288
s_cmp_ge_u32 s85, 5242880
s_cselect_b32 s85, 0, s85
s_add_u32 m0, s96, 81984
s_nop 0
buffer_load_dwordx4 v253, s[16:19], s87 offen offset:0 lds
s_add_u32 m0, s96, 83008
s_nop 0
buffer_load_dwordx4 v254, s[16:19], s87 offen offset:0 lds
s_add_u32 s87, s87, 65536
s_add_u32 m0, s96, 84040
s_nop 0
buffer_load_dwordx4 v253, s[16:19], s87 offen offset:0 lds
s_add_u32 m0, s96, 85064
s_nop 0
buffer_load_dwordx4 v254, s[16:19], s87 offen offset:0 lds
s_getpc_b64 s[98:99]
LTA0_%=:
s_min_u32 s73, %[swave], 3
s_lshl_b32 s73, s73, 12
s_add_u32 s73, s73, LPG1_%=-LTA0_%=
s_add_u32 s98, s98, s73
s_addc_u32 s99, s99, 0
s_mov_b64 exec, 0
s_setpc_b64 s[98:99]
LTR0_%=:
s_mov_b64 exec, -1
s_waitcnt lgkmcnt(0)
s_add_u32 s88, s68, s86
s_add_u32 s86, s86, 128
s_cmp_ge_u32 s86, 1280
s_cselect_b32 s86, 0, s86
s_add_u32 m0, s94, 32768
s_add_u32 s95, s88, 0
buffer_load_dwordx4 v220, s[32:35], s95 offen offset:0 lds
s_add_u32 s95, s88, 9216
buffer_load_dwordx4 v221, s[32:35], s95 offen offset:1024 lds
s_add_u32 s95, s88, 18432
buffer_load_dwordx4 v220, s[32:35], s95 offen offset:2048 lds
s_add_u32 s95, s88, 27648
buffer_load_dwordx4 v221, s[32:35], s95 offen offset:3072 lds
s_sub_u32 s88, s88, s63
s_add_u32 m0, s94, 36864
s_add_u32 s95, s88, 40960
buffer_load_dwordx4 v220, s[32:35], s95 offen offset:0 lds
s_add_u32 s95, s88, 50176
buffer_load_dwordx4 v221, s[32:35], s95 offen offset:1024 lds
s_add_u32 s95, s88, 59392
buffer_load_dwordx4 v220, s[32:35], s95 offen offset:2048 lds
s_add_u32 s95, s88, 68608
buffer_load_dwordx4 v221, s[32:35], s95 offen offset:3072 lds
s_waitcnt vmcnt(20)
RD a[128:131],v222 offset:0
RD a[132:135],v223 offset:0
RD a[136:139],v222 offset:2048
RD a[140:143],v223 offset:2048
s_barrier
TR a[0:1],v192 offset:0
TR a[2:3],v192 offset:512
TR a[4:5],v192 offset:8224
TR a[6:7],v192 offset:8736
TR a[8:9],v192 offset:8
TR a[10:11],v192 offset:520
TR a[12:13],v192 offset:8232
TR a[14:15],v192 offset:8744
TR a[16:17],v193 offset:0
TR a[18:19],v193 offset:512
TR a[20:21],v193 offset:8224
TR a[22:23],v193 offset:8736
TR a[24:25],v193 offset:8
TR a[26:27],v193 offset:520
TR a[28:29],v193 offset:8232
TR a[30:31],v193 offset:8744
s_waitcnt lgkmcnt(15)
s_waitcnt lgkmcnt(12)
MM v[0:3],a[0:3],a[128:131],0
s_add_u32 s87, s89, s85
TR a[32:33],v194 offset:0
TR a[34:35],v194 offset:512
MM v[0:3],a[4:7],a[132:135],v[0:3]
s_add_u32 s85, s85, 524288
TR a[36:37],v194 offset:8224
TR a[38:39],v194 offset:8736
MM v[8:11],a[4:7],a[140:143],0
s_waitcnt vmcnt(16)
TR a[40:41],v194 offset:8
TR a[42:43],v194 offset:520
MM v[8:11],a[0:3],a[136:139],v[8:11]
s_waitcnt lgkmcnt(14)
TR a[44:45],v194 offset:8232
TR a[46:47],v194 offset:8744
s_cmp_ge_u32 s85, 5242880
s_cselect_b32 s85, 0, s85
MM v[4:7],a[8:11],a[128:131],0
s_add_u32 m0, s96, 98432
v_max3_f32 v200, |v0|, |v1|, |v2|
v_max_f32 v200, v200, |v3|
v_readfirstlane_b32 s69, v169
MM v[4:7],a[12:15],a[132:135],v[4:7]
s_lshl_b32 s90, s76, 1
TR a[48:49],v195 offset:0
TR a[50:51],v195 offset:512
MM v[12:15],a[12:15],a[140:143],0
s_and_b32 s77, s90, 15
buffer_load_dwordx4 v253, s[16:19], s87 offen offset:0 lds
v_max3_f32 v201, |v8|, |v9|, |v10|
v_max_f32 v201, v201, |v11|
MM v[12:15],a[8:11],a[136:139],v[12:15]
s_add_u32 m0, s96, 99456
TR a[52:53],v195 offset:8224
v_min_f32 v200, v200, v201
v_and_b32 v200, 0x7f800000, v200
v_lshlrev_b32 v148, 16, v128
v_smfmac_f32_16x16x64_bf16 v[184:187], v[236:239], a[0:7], v234
s_lshl_b32 s77, s77, 1
v_add_u32 v200, 0x2c00000, v200
v_smfmac_f32_16x16x64_bf16 v[188:191], v[236:239], a[8:15], v234
s_waitcnt lgkmcnt(15)
v_min_u32 v200, 0x78400000, v200
v_mov_b32 v201, v200
v_mov_b32 v202, v200
v_mov_b32 v203, v200
s_nop 1
MM v[32:35],a[16:19],a[128:131],v[200:203]
s_bfe_u32 s91, s90, 65540
TR a[54:55],v195 offset:8736
buffer_load_dwordx4 v254, s[16:19], s87 offen offset:0 lds
v_mov_b32 v250, v200
v_and_b32 v149, 0xffff0000, v128
MM v[32:35],a[20:23],a[132:135],v[32:35]
s_add_u32 s87, s87, 65536
TR a[56:57],v195 offset:8
TR a[58:59],v195 offset:520
MM v[40:43],a[20:23],a[140:143],v[200:203]
s_add_u32 m0, s96, 100488
TR a[60:61],v195 offset:8232
TR a[62:63],v195 offset:8744
MM v[40:43],a[16:19],a[136:139],v[40:43]
s_waitcnt lgkmcnt(15)
buffer_load_dwordx4 v253, s[16:19], s87 offen offset:0 lds
v_lshlrev_b32 v150, 16, v129
MM v[36:39],a[24:27],a[128:131],v[200:203]
s_add_u32 m0, s96, 101512
v_and_b32 v151, 0xffff0000, v129
v_lshlrev_b32 v152, 16, v130
MM v[36:39],a[28:31],a[132:135],v[36:39]
s_add_u32 s77, s77, s91
v_and_b32 v153, 0xffff0000, v130
v_lshlrev_b32 v154, 16, v131
MM v[44:47],a[28:31],a[140:143],v[200:203]
s_and_b32 s91, s90, 0xffffffe0
buffer_load_dwordx4 v254, s[16:19], s87 offen offset:0 lds
MM v[44:47],a[24:27],a[136:139],v[44:47]
s_waitcnt lgkmcnt(12)
s_waitcnt vmcnt(16)
RD a[144:147],v222 offset:4096
v_and_b32 v155, 0xffff0000, v131
MM v[64:67],a[32:35],a[128:131],v[200:203]
s_add_u32 s77, s77, s91
RD a[148:151],v223 offset:4096
RD a[152:155],v222 offset:6144
MM v[64:67],a[36:39],a[132:135],v[64:67]
s_lshl_b32 s77, s77, 3
RD a[156:159],v223 offset:6144
v_lshlrev_b32 v156, 16, v132
MM v[72:75],a[36:39],a[140:143],v[200:203]
s_add_u32 s97, s77, 36864
s_waitcnt vmcnt(12)
s_barrier
v_and_b32 v157, 0xffff0000, v132
MM v[72:75],a[32:35],a[136:139],v[72:75]
s_waitcnt lgkmcnt(12)
global_load_dwordx4 v[138:141], v182, s[44:45] offset:0
MM v[68:71],a[40:43],a[128:131],v[200:203]
s_sub_u32 s79, 65535, s90
v_lshlrev_b32 v158, 16, v133
v_and_b32 v159, 0xffff0000, v133
MM v[68:71],a[44:47],a[132:135],v[68:71]
s_lshl_b32 s78, s90, 2
v_lshlrev_b32 v160, 16, v134
v_and_b32 v161, 0xffff0000, v134
MM v[76:79],a[44:47],a[140:143],v[200:203]
s_add_u32 s78, s78, 53248
global_load_dwordx4 v[142:145], v182, s[44:45] offset:512
MM v[76:79],a[40:43],a[136:139],v[76:79]
s_waitcnt lgkmcnt(8)
v_lshrrev_b32 v182, 1, v182
v_lshlrev_b32 v162, 16, v135
MM v[96:99],a[48:51],a[128:131],v[200:203]
s_lshl_b32 s80, s76, 2
global_load_dwordx2 v[146:147], v182, s[44:45] offset:1024
MM v[96:99],a[52:55],a[132:135],v[96:99]
)ASM"
R"ASM(
s_waitcnt lgkmcnt(0)
s_add_u32 s88, s68, s86
s_add_u32 s86, s86, 128
v_and_b32 v163, 0xffff0000, v135
MM v[104:107],a[52:55],a[140:143],v[200:203]
s_add_u32 s80, s80, 81920
s_cmp_ge_u32 s86, 1280
s_cselect_b32 s86, 0, s86
MM v[104:107],a[48:51],a[136:139],v[104:107]
s_add_u32 m0, s94, 0
s_add_u32 s95, s88, 0
v_lshlrev_b32 v164, 16, v136
MM v[100:103],a[56:59],a[128:131],v[200:203]
s_mov_b32 s42, 2621440
buffer_load_dwordx4 v220, s[32:35], s95 offen offset:0 lds
MM v[100:103],a[60:63],a[132:135],v[100:103]
s_add_u32 s95, s88, 9216
v_and_b32 v165, 0xffff0000, v136
v_lshlrev_b32 v166, 16, v137
MM v[108:111],a[60:63],a[140:143],v[200:203]
s_mov_b32 s43, 131072
buffer_load_dwordx4 v221, s[32:35], s95 offen offset:1024 lds
MM v[108:111],a[56:59],a[136:139],v[108:111]
s_add_u32 s95, s88, 18432
v_and_b32 v167, 0xffff0000, v137
s_add_u32 s69, s69, 1
MM v[16:19],a[0:3],a[144:147],0
v_add_f32 v170, v148, v149
v_add_f32 v171, v150, v151
MM v[16:19],a[4:7],a[148:151],v[16:19]
v_add_f32 v170, v170, v152
v_add_f32 v171, v171, v153
MM v[24:27],a[4:7],a[156:159],0
buffer_load_dwordx4 v220, s[32:35], s95 offen offset:2048 lds
MM v[24:27],a[0:3],a[152:155],v[24:27]
s_add_u32 s95, s88, 27648
v_add_f32 v170, v170, v154
v_add_f32 v171, v171, v155
MM v[20:23],a[8:11],a[144:147],0
v_max3_f32 v204, |v16|, |v17|, |v18|
v_max_f32 v204, v204, |v19|
MM v[20:23],a[12:15],a[148:151],v[20:23]
TR a[0:1],v192 offset:16448
TR a[2:3],v192 offset:16960
v_add_f32 v170, v170, v156
MM v[28:31],a[12:15],a[156:159],0
v_max3_f32 v205, |v24|, |v25|, |v26|
v_max_f32 v205, v205, |v27|
MM v[28:31],a[8:11],a[152:155],v[28:31]
TR a[4:5],v192 offset:24672
v_min_f32 v204, v204, v205
v_and_b32 v204, 0x7f800000, v204
v_add_u32 v204, 0x2c00000, v204
v_min_u32 v204, 0x78400000, v204
v_mov_b32 v205, v204
v_mov_b32 v206, v204
v_mov_b32 v207, v204
s_nop 1
MM v[48:51],a[16:19],a[144:147],v[204:207]
TR a[6:7],v192 offset:25184
buffer_load_dwordx4 v221, s[32:35], s95 offen offset:3072 lds
v_mov_b32 v252, v204
v_add_f32 v171, v171, v157
MM v[48:51],a[20:23],a[148:151],v[48:51]
s_sub_u32 s88, s88, s63
TR a[8:9],v192 offset:16456
TR a[10:11],v192 offset:16968
MM v[56:59],a[20:23],a[156:159],v[204:207]
s_add_u32 m0, s94, 4096
TR a[12:13],v192 offset:24680
TR a[14:15],v192 offset:25192
MM v[56:59],a[16:19],a[152:155],v[56:59]
s_add_u32 s95, s88, 40960
buffer_load_dwordx4 v220, s[32:35], s95 offen offset:0 lds
v_add_f32 v170, v170, v158
MM v[52:55],a[24:27],a[144:147],v[204:207]
s_add_u32 s95, s88, 50176
TR a[16:17],v193 offset:16448
TR a[18:19],v193 offset:16960
MM v[52:55],a[28:31],a[148:151],v[52:55]
TR a[20:21],v193 offset:24672
TR a[22:23],v193 offset:25184
MM v[60:63],a[28:31],a[156:159],v[204:207]
buffer_load_dwordx4 v221, s[32:35], s95 offen offset:1024 lds
v_add_f32 v171, v171, v159
MM v[60:63],a[24:27],a[152:155],v[60:63]
s_add_u32 s95, s88, 59392
v_add_f32 v170, v170, v160
v_add_f32 v171, v171, v161
MM v[80:83],a[32:35],a[144:147],v[204:207]
TR a[24:25],v193 offset:16456
TR a[26:27],v193 offset:16968
MM v[80:83],a[36:39],a[148:151],v[80:83]
TR a[28:29],v193 offset:24680
TR a[30:31],v193 offset:25192
v_add_f32 v170, v170, v162
MM v[88:91],a[36:39],a[156:159],v[204:207]
buffer_load_dwordx4 v220, s[32:35], s95 offen offset:2048 lds
MM v[88:91],a[32:35],a[152:155],v[88:91]
s_add_u32 s95, s88, 68608
v_add_f32 v171, v171, v163
v_add_f32 v170, v170, v164
MM v[84:87],a[40:43],a[144:147],v[204:207]
TR a[32:33],v194 offset:16448
TR a[34:35],v194 offset:16960
MM v[84:87],a[44:47],a[148:151],v[84:87]
TR a[36:37],v194 offset:24672
TR a[38:39],v194 offset:25184
v_add_f32 v171, v171, v165
MM v[92:95],a[44:47],a[156:159],v[204:207]
buffer_load_dwordx4 v221, s[32:35], s95 offen offset:3072 lds
MM v[92:95],a[40:43],a[152:155],v[92:95]
s_add_u32 s87, s89, s85
s_add_u32 s85, s85, 524288
v_add_f32 v170, v170, v166
MM v[112:115],a[48:51],a[144:147],v[204:207]
TR a[40:41],v194 offset:16456
TR a[42:43],v194 offset:16968
MM v[112:115],a[52:55],a[148:151],v[112:115]
TR a[44:45],v194 offset:24680
TR a[46:47],v194 offset:25192
s_cmp_ge_u32 s85, 5242880
s_cselect_b32 s85, 0, s85
v_add_f32 v171, v171, v167
MM v[120:123],a[52:55],a[156:159],v[204:207]
s_add_u32 m0, s96, 114880
s_nop 0
buffer_load_dwordx4 v253, s[16:19], s87 offen offset:0 lds
MM v[120:123],a[48:51],a[152:155],v[120:123]
s_add_u32 m0, s96, 115904
v_add_f32 v172, v170, v171
v_lshlrev_b32 v232, 11, v180
MM v[116:119],a[56:59],a[144:147],v[204:207]
TR a[48:49],v195 offset:16448
TR a[50:51],v195 offset:16960
MM v[116:119],a[60:63],a[148:151],v[116:119]
TR a[52:53],v195 offset:24672
TR a[54:55],v195 offset:25184
MM v[124:127],a[60:63],a[156:159],v[204:207]
buffer_load_dwordx4 v254, s[16:19], s87 offen offset:0 lds
v_add_f32_dpp v172, v172, v172 row_ror:8 row_mask:0xf bank_mask:0xf
MM v[124:127],a[56:59],a[152:155],v[124:127]
s_add_u32 s87, s87, 65536
s_add_u32 m0, s96, 116936
v_add_f32_dpp v172, v172, v172 row_ror:4 row_mask:0xf bank_mask:0xf
buffer_load_dwordx4 v253, s[16:19], s87 offen offset:0 lds
s_add_u32 m0, s96, 117960
s_nop 0
buffer_load_dwordx4 v254, s[16:19], s87 offen offset:0 lds
s_waitcnt vmcnt(19)
RD a[160:163],v222 offset:32768
RD a[164:167],v223 offset:32768
RD a[168:171],v222 offset:34816
RD a[172:175],v223 offset:34816
RD a[176:179],v222 offset:36864
RD a[180:183],v223 offset:36864
RD a[184:187],v222 offset:38912
RD a[188:191],v223 offset:38912
s_waitcnt vmcnt(15) lgkmcnt(0)
s_barrier
MM v[0:3],a[0:3],a[160:163],v[0:3]
TR a[56:57],v195 offset:16456
TR a[58:59],v195 offset:16968
MM v[0:3],a[4:7],a[164:167],v[0:3]
TR a[60:61],v195 offset:24680
TR a[62:63],v195 offset:25192
MM v[8:11],a[4:7],a[172:175],v[8:11]
v_add_f32_dpp v172, v172, v172 row_ror:2 row_mask:0xf bank_mask:0xf
v_lshl_add_u32 v232, v214, 8, v232
MM v[8:11],a[0:3],a[168:171],v[8:11]
s_add_u32 s88, s68, s86
s_add_u32 s86, s86, 128
MM v[16:19],a[0:3],a[176:179],v[16:19]
s_cmp_ge_u32 s86, 1280
s_cselect_b32 s86, 0, s86
s_add_u32 m0, s94, 32768
MM v[16:19],a[4:7],a[180:183],v[16:19]
s_add_u32 s95, s88, 0
buffer_load_dwordx4 v220, s[32:35], s95 offen offset:0 lds
v_add_f32_dpp v172, v172, v172 row_ror:1 row_mask:0xf bank_mask:0xf
MM v[24:27],a[4:7],a[188:191],v[24:27]
s_add_u32 s95, s88, 9216
v_mov_b32 v173, v172
v_lshl_add_u32 v232, v215, 6, v232
MM v[24:27],a[0:3],a[184:187],v[24:27]
buffer_load_dwordx4 v221, s[32:35], s95 offen offset:1024 lds
MM v[4:7],a[8:11],a[160:163],v[4:7]
s_add_u32 s95, s88, 18432
v_permlane16_swap_b32 v172, v173
v_add_f32 v172, v172, v173
MM v[4:7],a[12:15],a[164:167],v[4:7]
v_mul_f32 v172, 0x3acccccd, v172
v_sub_f32 v176, v148, v172
MM v[12:15],a[12:15],a[172:175],v[12:15]
v_sub_f32 v177, v149, v172
v_mul_f32 v170, v176, v176
MM v[12:15],a[8:11],a[168:171],v[12:15]
buffer_load_dwordx4 v220, s[32:35], s95 offen offset:2048 lds
MM v[20:23],a[8:11],a[176:179],v[20:23]
s_add_u32 s95, s88, 27648
v_mul_f32 v171, v177, v177
v_sub_f32 v176, v150, v172
MM v[20:23],a[12:15],a[180:183],v[20:23]
v_sub_f32 v177, v151, v172
v_fma_f32 v170, v176, v176, v170
MM v[28:31],a[12:15],a[188:191],v[28:31]
v_fma_f32 v171, v177, v177, v171
v_sub_f32 v176, v152, v172
MM v[28:31],a[8:11],a[184:187],v[28:31]
buffer_load_dwordx4 v221, s[32:35], s95 offen offset:3072 lds
v_smfmac_f32_16x16x64_bf16 v[184:187], v[236:239], a[0:7], v234
s_sub_u32 s88, s88, s63
s_add_u32 m0, s94, 36864
v_sub_f32 v177, v153, v172
v_smfmac_f32_16x16x64_bf16 v[188:191], v[236:239], a[8:15], v234
s_add_u32 s95, s88, 40960
v_fma_f32 v170, v176, v176, v170
MM v[32:35],a[16:19],a[160:163],v[32:35]
TR a[0:1],v192 offset:32896
TR a[2:3],v192 offset:33408
MM v[32:35],a[20:23],a[164:167],v[32:35]
TR a[4:5],v192 offset:41120
TR a[6:7],v192 offset:41632
v_fma_f32 v171, v177, v177, v171
MM v[40:43],a[20:23],a[172:175],v[40:43]
LPG1_%=:
s_cbranch_execz LPG5_%=
TR a[8:9],v192 offset:32904
TR a[10:11],v192 offset:33416
MM v[40:43],a[16:19],a[168:171],v[40:43]
TR a[12:13],v192 offset:41128
TR a[14:15],v192 offset:41640
buffer_load_dwordx4 v220, s[32:35], s95 offen offset:0 lds
MM v[48:51],a[16:19],a[176:179],v[48:51]
s_add_u32 s95, s88, 50176
v_sub_f32 v176, v154, v172
v_sub_f32 v177, v155, v172
MM v[48:51],a[20:23],a[180:183],v[48:51]
buffer_load_dwordx4 v221, s[32:35], s95 offen offset:1024 lds
MM v[56:59],a[20:23],a[188:191],v[56:59]
s_add_u32 s95, s88, 59392
v_fma_f32 v170, v176, v176, v170
v_fma_f32 v171, v177, v177, v171
MM v[56:59],a[16:19],a[184:187],v[56:59]
v_sub_f32 v176, v156, v172
v_sub_f32 v177, v157, v172
MM v[36:39],a[24:27],a[160:163],v[36:39]
TR a[16:17],v193 offset:32896
TR a[18:19],v193 offset:33408
MM v[36:39],a[28:31],a[164:167],v[36:39]
TR a[20:21],v193 offset:41120
TR a[22:23],v193 offset:41632
v_fma_f32 v170, v176, v176, v170
MM v[44:47],a[28:31],a[172:175],v[44:47]
buffer_load_dwordx4 v220, s[32:35], s95 offen offset:2048 lds
MM v[44:47],a[24:27],a[168:171],v[44:47]
s_add_u32 s95, s88, 68608
v_fma_f32 v171, v177, v177, v171
v_sub_f32 v176, v158, v172
MM v[52:55],a[24:27],a[176:179],v[52:55]
v_sub_f32 v177, v159, v172
v_fma_f32 v170, v176, v176, v170
MM v[52:55],a[28:31],a[180:183],v[52:55]
buffer_load_dwordx4 v221, s[32:35], s95 offen offset:3072 lds
MM v[60:63],a[28:31],a[188:191],v[60:63]
s_add_u32 s87, s89, s85
s_add_u32 s85, s85, 524288
v_fma_f32 v171, v177, v177, v171
MM v[60:63],a[24:27],a[184:187],v[60:63]
s_cmp_ge_u32 s85, 5242880
s_cselect_b32 s85, 0, s85
s_add_u32 m0, s96, 131328
MM v[64:67],a[32:35],a[160:163],v[64:67]
TR a[24:25],v193 offset:32904
TR a[26:27],v193 offset:33416
v_sub_f32 v176, v160, v172
MM v[64:67],a[36:39],a[164:167],v[64:67]
TR a[28:29],v193 offset:41128
TR a[30:31],v193 offset:41640
MM v[72:75],a[36:39],a[172:175],v[72:75]
buffer_load_dwordx4 v253, s[16:19], s87 offen offset:0 lds
MM v[72:75],a[32:35],a[168:171],v[72:75]
s_add_u32 m0, s96, 132352
v_sub_f32 v177, v161, v172
v_fma_f32 v170, v176, v176, v170
MM v[80:83],a[32:35],a[176:179],v[80:83]
v_fma_f32 v171, v177, v177, v171
v_sub_f32 v176, v162, v172
MM v[80:83],a[36:39],a[180:183],v[80:83]
buffer_load_dwordx4 v254, s[16:19], s87 offen offset:0 lds
MM v[88:91],a[36:39],a[188:191],v[88:91]
s_add_u32 s87, s87, 65536
s_add_u32 m0, s96, 133384
v_sub_f32 v177, v163, v172
MM v[88:91],a[32:35],a[184:187],v[88:91]
v_fma_f32 v170, v176, v176, v170
v_fma_f32 v171, v177, v177, v171
MM v[68:71],a[40:43],a[160:163],v[68:71]
TR a[32:33],v194 offset:32896
TR a[34:35],v194 offset:33408
MM v[68:71],a[44:47],a[164:167],v[68:71]
TR a[36:37],v194 offset:41120
TR a[38:39],v194 offset:41632
v_sub_f32 v176, v164, v172
MM v[76:79],a[44:47],a[172:175],v[76:79]
buffer_load_dwordx4 v253, s[16:19], s87 offen offset:0 lds
MM v[76:79],a[40:43],a[168:171],v[76:79]
s_add_u32 m0, s96, 134408
v_sub_f32 v177, v165, v172
v_fma_f32 v170, v176, v176, v170
MM v[84:87],a[40:43],a[176:179],v[84:87]
v_fma_f32 v171, v177, v177, v171
v_sub_f32 v176, v166, v172
MM v[84:87],a[44:47],a[180:183],v[84:87]
buffer_load_dwordx4 v254, s[16:19], s87 offen offset:0 lds
MM v[92:95],a[44:47],a[188:191],v[92:95]
s_waitcnt vmcnt(16)
RD a[192:195],v222 offset:0
v_sub_f32 v177, v167, v172
MM v[92:95],a[40:43],a[184:187],v[92:95]
RD a[196:199],v223 offset:0
v_fma_f32 v170, v176, v176, v170
MM v[96:99],a[48:51],a[160:163],v[96:99]
TR a[40:41],v194 offset:32904
TR a[42:43],v194 offset:33416
MM v[96:99],a[52:55],a[164:167],v[96:99]
TR a[44:45],v194 offset:41128
TR a[46:47],v194 offset:41640
v_fma_f32 v171, v177, v177, v171
MM v[104:107],a[52:55],a[172:175],v[104:107]
RD a[200:203],v222 offset:2048
v_add_f32 v170, v170, v171
MM v[104:107],a[48:51],a[168:171],v[104:107]
RD a[204:207],v223 offset:2048
v_add_f32_dpp v170, v170, v170 row_ror:8 row_mask:0xf bank_mask:0xf
MM v[112:115],a[48:51],a[176:179],v[112:115]
RD a[208:211],v222 offset:4096
v_add_f32_dpp v170, v170, v170 row_ror:4 row_mask:0xf bank_mask:0xf
MM v[112:115],a[52:55],a[180:183],v[112:115]
RD a[212:215],v223 offset:4096
v_add_f32_dpp v170, v170, v170 row_ror:2 row_mask:0xf bank_mask:0xf
MM v[120:123],a[52:55],a[188:191],v[120:123]
RD a[216:219],v222 offset:6144
v_add_f32_dpp v170, v170, v170 row_ror:1 row_mask:0xf bank_mask:0xf
MM v[120:123],a[48:51],a[184:187],v[120:123]
)ASM"
R"ASM(
s_waitcnt lgkmcnt(15)
RD a[220:223],v223 offset:6144
v_mov_b32 v173, v170
MM v[100:103],a[56:59],a[160:163],v[100:103]
TR a[48:49],v195 offset:32896
TR a[50:51],v195 offset:33408
MM v[100:103],a[60:63],a[164:167],v[100:103]
TR a[52:53],v195 offset:41120
TR a[54:55],v195 offset:41632
MM v[108:111],a[60:63],a[172:175],v[108:111]
v_permlane16_swap_b32 v170, v173
v_add_f32 v170, v170, v173
MM v[108:111],a[56:59],a[168:171],v[108:111]
s_waitcnt lgkmcnt(4)
s_add_u32 s88, s68, s86
s_add_u32 s86, s86, 128
MM v[116:119],a[56:59],a[176:179],v[116:119]
s_cmp_ge_u32 s86, 1280
s_cselect_b32 s86, 0, s86
s_add_u32 m0, s94, 0
v_mul_f32 v170, 0x3acccccd, v170
MM v[116:119],a[60:63],a[180:183],v[116:119]
s_add_u32 s95, s88, 0
buffer_load_dwordx4 v220, s[32:35], s95 offen offset:0 lds
MM v[124:127],a[60:63],a[188:191],v[124:127]
s_add_u32 s95, s88, 9216
CV v178,v172,v170
v_lshlrev_b32 v174, 16, v178
MM v[124:127],a[56:59],a[184:187],v[124:127]
buffer_load_dwordx4 v221, s[32:35], s95 offen offset:1024 lds
s_waitcnt vmcnt(14)
s_barrier
MM v[0:3],a[0:3],a[192:195],v[0:3]
s_add_u32 s95, s88, 18432
TR a[56:57],v195 offset:32904
TR a[58:59],v195 offset:33416
v_and_b32 v170, 0xffff0000, v178
MM v[0:3],a[4:7],a[196:199],v[0:3]
TR a[60:61],v195 offset:41128
TR a[62:63],v195 offset:41640
MM v[8:11],a[4:7],a[204:207],v[8:11]
v_add_f32 v170, 0x3727c5ac, v170
CV v170,v170,v170
MM v[8:11],a[0:3],a[200:203],v[8:11]
buffer_load_dwordx4 v220, s[32:35], s95 offen offset:2048 lds
MM v[16:19],a[0:3],a[208:211],v[16:19]
s_add_u32 s95, s88, 27648
v_and_b32 v170, 0xffff0000, v170
v_lshl_add_u32 v232, v216, 3, v232
MM v[16:19],a[4:7],a[212:215],v[16:19]
v_sqrt_f32 v170, v170
MM v[24:27],a[4:7],a[220:223],v[24:27]
v_mov_b32 v179, s69
CV v178,v174,v170
MM v[24:27],a[0:3],a[216:219],v[24:27]
buffer_load_dwordx4 v221, s[32:35], s95 offen offset:3072 lds
MM v[4:7],a[8:11],a[192:195],v[4:7]
s_sub_u32 s88, s88, s63
s_add_u32 m0, s94, 4096
s_add_u32 s95, s88, 40960
v_and_b32 v170, 0xffff0000, v178
MM v[4:7],a[12:15],a[196:199],v[4:7]
v_rcp_f32 v175, v170
MM v[12:15],a[12:15],a[204:207],v[12:15]
v_lshlrev_b32 v173, 4, v180
v_lshl_add_u32 v232, v214, 5, v232
MM v[12:15],a[8:11],a[200:203],v[12:15]
buffer_load_dwordx4 v220, s[32:35], s95 offen offset:0 lds
MM v[20:23],a[8:11],a[208:211],v[20:23]
s_add_u32 s95, s88, 50176
s_mov_b32 exec_lo, 1
s_mov_b32 exec_hi, 1
buffer_store_dwordx2 v[178:179], v173, s[20:23], s97 offen offset:0 sc1
s_mov_b64 exec, -1
MM v[20:23],a[12:15],a[212:215],v[20:23]
v_sub_f32 v148, v148, v174
v_sub_f32 v149, v149, v174
MM v[28:31],a[12:15],a[220:223],v[28:31]
v_sub_f32 v150, v150, v174
v_sub_f32 v151, v151, v174
MM v[28:31],a[8:11],a[216:219],v[28:31]
buffer_load_dwordx4 v221, s[32:35], s95 offen offset:1024 lds
v_smfmac_f32_16x16x64_bf16 v[184:187], v[236:239], a[0:7], v234
s_add_u32 s95, s88, 59392
v_sub_f32 v152, v152, v174
v_smfmac_f32_16x16x64_bf16 v[188:191], v[236:239], a[8:15], v234
v_sub_f32 v153, v153, v174
MM v[32:35],a[16:19],a[192:195],v[32:35]
TR a[0:1],v196 offset:0
TR a[2:3],v196 offset:512
MM v[32:35],a[20:23],a[196:199],v[32:35]
TR a[4:5],v196 offset:8224
TR a[6:7],v196 offset:8736
v_sub_f32 v154, v154, v174
MM v[40:43],a[20:23],a[204:207],v[40:43]
TR a[8:9],v196 offset:8
TR a[10:11],v196 offset:520
MM v[40:43],a[16:19],a[200:203],v[40:43]
TR a[12:13],v196 offset:8232
TR a[14:15],v196 offset:8744
buffer_load_dwordx4 v220, s[32:35], s95 offen offset:2048 lds
MM v[48:51],a[16:19],a[208:211],v[48:51]
s_add_u32 s95, s88, 68608
v_sub_f32 v155, v155, v174
v_sub_f32 v156, v156, v174
MM v[48:51],a[20:23],a[212:215],v[48:51]
buffer_load_dwordx4 v221, s[32:35], s95 offen offset:3072 lds
MM v[56:59],a[20:23],a[220:223],v[56:59]
s_add_u32 s87, s89, s85
s_add_u32 s85, s85, 524288
v_sub_f32 v157, v157, v174
MM v[56:59],a[16:19],a[216:219],v[56:59]
s_cmp_ge_u32 s85, 5242880
s_cselect_b32 s85, 0, s85
s_add_u32 m0, s96, 65536
MM v[36:39],a[24:27],a[192:195],v[36:39]
TR a[16:17],v197 offset:0
TR a[18:19],v197 offset:512
v_sub_f32 v158, v158, v174
MM v[36:39],a[28:31],a[196:199],v[36:39]
TR a[20:21],v197 offset:8224
TR a[22:23],v197 offset:8736
MM v[44:47],a[28:31],a[204:207],v[44:47]
buffer_load_dwordx4 v253, s[16:19], s87 offen offset:0 lds
MM v[44:47],a[24:27],a[200:203],v[44:47]
s_add_u32 m0, s96, 66560
v_sub_f32 v159, v159, v174
v_sub_f32 v160, v160, v174
MM v[52:55],a[24:27],a[208:211],v[52:55]
v_sub_f32 v161, v161, v174
v_sub_f32 v162, v162, v174
MM v[52:55],a[28:31],a[212:215],v[52:55]
buffer_load_dwordx4 v254, s[16:19], s87 offen offset:0 lds
MM v[60:63],a[28:31],a[220:223],v[60:63]
s_add_u32 s87, s87, 65536
s_add_u32 m0, s96, 67592
v_sub_f32 v163, v163, v174
MM v[60:63],a[24:27],a[216:219],v[60:63]
v_sub_f32 v164, v164, v174
v_sub_f32 v165, v165, v174
MM v[64:67],a[32:35],a[192:195],v[64:67]
TR a[24:25],v197 offset:8
TR a[26:27],v197 offset:520
MM v[64:67],a[36:39],a[196:199],v[64:67]
TR a[28:29],v197 offset:8232
TR a[30:31],v197 offset:8744
v_sub_f32 v166, v166, v174
MM v[72:75],a[36:39],a[204:207],v[72:75]
buffer_load_dwordx4 v253, s[16:19], s87 offen offset:0 lds
MM v[72:75],a[32:35],a[200:203],v[72:75]
s_add_u32 m0, s96, 68616
v_sub_f32 v167, v167, v174
CV v128,v148,v149
MM v[80:83],a[32:35],a[208:211],v[80:83]
CV v129,v150,v151
CV v130,v152,v153
MM v[80:83],a[36:39],a[212:215],v[80:83]
buffer_load_dwordx4 v254, s[16:19], s87 offen offset:0 lds
MM v[88:91],a[36:39],a[220:223],v[88:91]
s_waitcnt vmcnt(17)
RD a[224:227],v222 offset:32768
CV v131,v154,v155
MM v[88:91],a[32:35],a[216:219],v[88:91]
RD a[228:231],v223 offset:32768
CV v132,v156,v157
MM v[68:71],a[40:43],a[192:195],v[68:71]
TR a[32:33],v198 offset:0
TR a[34:35],v198 offset:512
MM v[68:71],a[44:47],a[196:199],v[68:71]
TR a[36:37],v198 offset:8224
TR a[38:39],v198 offset:8736
CV v133,v158,v159
MM v[76:79],a[44:47],a[204:207],v[76:79]
RD a[232:235],v222 offset:34816
CV v134,v160,v161
MM v[76:79],a[40:43],a[200:203],v[76:79]
RD a[236:239],v223 offset:34816
CV v135,v162,v163
MM v[84:87],a[40:43],a[208:211],v[84:87]
RD a[240:243],v222 offset:36864
CV v136,v164,v165
MM v[84:87],a[44:47],a[212:215],v[84:87]
RD a[244:247],v223 offset:36864
CV v137,v166,v167
MM v[92:95],a[44:47],a[220:223],v[92:95]
RD a[248:251],v222 offset:38912
v_lshlrev_b32 v148, 16, v128
MM v[92:95],a[40:43],a[216:219],v[92:95]
s_waitcnt lgkmcnt(15)
RD a[252:255],v223 offset:38912
v_and_b32 v149, 0xffff0000, v128
MM v[96:99],a[48:51],a[192:195],v[96:99]
TR a[40:41],v198 offset:8
TR a[42:43],v198 offset:520
MM v[96:99],a[52:55],a[196:199],v[96:99]
TR a[44:45],v198 offset:8232
TR a[46:47],v198 offset:8744
v_lshlrev_b32 v150, 16, v129
MM v[104:107],a[52:55],a[204:207],v[104:107]
v_and_b32 v151, 0xffff0000, v129
v_lshlrev_b32 v152, 16, v130
MM v[104:107],a[48:51],a[200:203],v[104:107]
s_waitcnt lgkmcnt(4)
s_add_u32 s88, s68, s86
s_add_u32 s86, s86, 128
MM v[112:115],a[48:51],a[208:211],v[112:115]
s_cmp_ge_u32 s86, 1280
s_cselect_b32 s86, 0, s86
s_add_u32 m0, s94, 32768
v_and_b32 v153, 0xffff0000, v130
MM v[112:115],a[52:55],a[212:215],v[112:115]
s_add_u32 s95, s88, 0
buffer_load_dwordx4 v220, s[32:35], s95 offen offset:0 lds
MM v[120:123],a[52:55],a[220:223],v[120:123]
s_add_u32 s95, s88, 9216
v_lshlrev_b32 v154, 16, v131
v_and_b32 v155, 0xffff0000, v131
MM v[120:123],a[48:51],a[216:219],v[120:123]
buffer_load_dwordx4 v221, s[32:35], s95 offen offset:1024 lds
MM v[100:103],a[56:59],a[192:195],v[100:103]
s_add_u32 s95, s88, 18432
TR a[48:49],v199 offset:0
TR a[50:51],v199 offset:512
v_lshlrev_b32 v156, 16, v132
MM v[100:103],a[60:63],a[196:199],v[100:103]
TR a[52:53],v199 offset:8224
TR a[54:55],v199 offset:8736
MM v[108:111],a[60:63],a[204:207],v[108:111]
v_and_b32 v157, 0xffff0000, v132
v_lshlrev_b32 v158, 16, v133
MM v[108:111],a[56:59],a[200:203],v[108:111]
buffer_load_dwordx4 v220, s[32:35], s95 offen offset:2048 lds
MM v[116:119],a[56:59],a[208:211],v[116:119]
s_add_u32 s95, s88, 27648
v_and_b32 v159, 0xffff0000, v133
v_lshlrev_b32 v160, 16, v134
MM v[116:119],a[60:63],a[212:215],v[116:119]
v_and_b32 v161, 0xffff0000, v134
v_lshlrev_b32 v162, 16, v135
MM v[124:127],a[60:63],a[220:223],v[124:127]
v_and_b32 v163, 0xffff0000, v135
v_lshlrev_b32 v164, 16, v136
MM v[124:127],a[56:59],a[216:219],v[124:127]
buffer_load_dwordx4 v221, s[32:35], s95 offen offset:3072 lds
s_waitcnt vmcnt(17)
s_barrier
MM v[0:3],a[0:3],a[224:227],v[0:3]
s_sub_u32 s88, s88, s63
TR a[56:57],v199 offset:8
TR a[58:59],v199 offset:520
v_and_b32 v165, 0xffff0000, v136
MM v[0:3],a[4:7],a[228:231],v[0:3]
s_add_u32 m0, s94, 36864
TR a[60:61],v199 offset:8232
TR a[62:63],v199 offset:8744
MM v[8:11],a[4:7],a[236:239],v[8:11]
s_add_u32 s95, s88, 40960
v_lshlrev_b32 v166, 16, v137
v_and_b32 v167, 0xffff0000, v137
MM v[8:11],a[0:3],a[232:235],v[8:11]
buffer_load_dwordx4 v220, s[32:35], s95 offen offset:0 lds
MM v[16:19],a[0:3],a[240:243],v[16:19]
s_add_u32 s95, s88, 50176
v_mul_f32 v148, v148, v175
v_mul_f32 v149, v149, v175
MM v[16:19],a[4:7],a[244:247],v[16:19]
v_mul_f32 v150, v150, v175
v_mul_f32 v151, v151, v175
MM v[24:27],a[4:7],a[252:255],v[24:27]
v_mul_f32 v152, v152, v175
v_mul_f32 v153, v153, v175
MM v[24:27],a[0:3],a[248:251],v[24:27]
buffer_load_dwordx4 v221, s[32:35], s95 offen offset:1024 lds
MM v[4:7],a[8:11],a[224:227],v[4:7]
s_add_u32 s95, s88, 59392
v_mul_f32 v154, v154, v175
v_mul_f32 v155, v155, v175
MM v[4:7],a[12:15],a[228:231],v[4:7]
v_mul_f32 v156, v156, v175
v_mul_f32 v157, v157, v175
MM v[12:15],a[12:15],a[236:239],v[12:15]
v_mul_f32 v158, v158, v175
v_mul_f32 v159, v159, v175
MM v[12:15],a[8:11],a[232:235],v[12:15]
buffer_load_dwordx4 v220, s[32:35], s95 offen offset:2048 lds
MM v[20:23],a[8:11],a[240:243],v[20:23]
s_add_u32 s95, s88, 68608
v_mul_f32 v160, v160, v175
v_mul_f32 v161, v161, v175
MM v[20:23],a[12:15],a[244:247],v[20:23]
v_mul_f32 v162, v162, v175
v_mul_f32 v163, v163, v175
MM v[28:31],a[12:15],a[252:255],v[28:31]
v_mul_f32 v164, v164, v175
v_mul_f32 v165, v165, v175
MM v[28:31],a[8:11],a[248:251],v[28:31]
buffer_load_dwordx4 v221, s[32:35], s95 offen offset:3072 lds
v_smfmac_f32_16x16x64_bf16 v[184:187], v[236:239], a[0:7], v234
s_add_u32 s87, s89, s85
s_add_u32 s85, s85, 524288
v_mul_f32 v166, v166, v175
v_smfmac_f32_16x16x64_bf16 v[188:191], v[236:239], a[8:15], v234
s_cmp_ge_u32 s85, 5242880
s_cselect_b32 s85, 0, s85
MM v[32:35],a[16:19],a[224:227],v[32:35]
s_add_u32 m0, s96, 81984
TR a[0:1],v196 offset:16448
TR a[2:3],v196 offset:16960
MM v[32:35],a[20:23],a[228:231],v[32:35]
TR a[4:5],v196 offset:24672
TR a[6:7],v196 offset:25184
v_mul_f32 v167, v167, v175
MM v[40:43],a[20:23],a[236:239],v[40:43]
TR a[8:9],v196 offset:16456
TR a[10:11],v196 offset:16968
MM v[40:43],a[16:19],a[232:235],v[40:43]
TR a[12:13],v196 offset:24680
TR a[14:15],v196 offset:25192
buffer_load_dwordx4 v253, s[16:19], s87 offen offset:0 lds
MM v[48:51],a[16:19],a[240:243],v[48:51]
s_add_u32 m0, s96, 83008
CV v128,v148,v149
CV v129,v150,v151
MM v[48:51],a[20:23],a[244:247],v[48:51]
buffer_load_dwordx4 v254, s[16:19], s87 offen offset:0 lds
MM v[56:59],a[20:23],a[252:255],v[56:59]
s_add_u32 s87, s87, 65536
s_add_u32 m0, s96, 84040
CV v130,v152,v153
MM v[56:59],a[16:19],a[248:251],v[56:59]
CV v131,v154,v155
CV v132,v156,v157
MM v[36:39],a[24:27],a[224:227],v[36:39]
TR a[16:17],v197 offset:16448
TR a[18:19],v197 offset:16960
MM v[36:39],a[28:31],a[228:231],v[36:39]
TR a[20:21],v197 offset:24672
TR a[22:23],v197 offset:25184
CV v133,v158,v159
MM v[44:47],a[28:31],a[236:239],v[44:47]
buffer_load_dwordx4 v253, s[16:19], s87 offen offset:0 lds
MM v[44:47],a[24:27],a[232:235],v[44:47]
s_add_u32 m0, s96, 85064
CV v134,v160,v161
CV v135,v162,v163
MM v[52:55],a[24:27],a[240:243],v[52:55]
CV v136,v164,v165
CV v137,v166,v167
MM v[52:55],a[28:31],a[244:247],v[52:55]
buffer_load_dwordx4 v254, s[16:19], s87 offen offset:0 lds
MM v[60:63],a[28:31],a[252:255],v[60:63]
s_waitcnt vmcnt(16)
RD a[128:131],v222 offset:0
v_xor_b32 v233, 32, v232
MM v[60:63],a[24:27],a[248:251],v[60:63]
s_add_u32 s77, %[slds], 65536
RD a[132:135],v223 offset:0
v_add_u32 v232, s77, v232
MM v[64:67],a[32:35],a[224:227],v[64:67]
TR a[24:25],v197 offset:16456
)ASM"
R"ASM(
TR a[26:27],v197 offset:16968
MM v[64:67],a[36:39],a[228:231],v[64:67]
TR a[28:29],v197 offset:24680
TR a[30:31],v197 offset:25192
MM v[72:75],a[36:39],a[236:239],v[72:75]
RD a[136:139],v222 offset:2048
v_add_u32 v233, s77, v233
MM v[72:75],a[32:35],a[232:235],v[72:75]
RD a[140:143],v223 offset:2048
buffer_store_dwordx4 v[128:131], v168, s[20:23], s82 offen offset:0 sc1
v_lshlrev_b32 v214, 8, v211
MM v[80:83],a[32:35],a[240:243],v[80:83]
RD a[144:147],v222 offset:4096
v_add_u32 v214, %[slds], v214
MM v[80:83],a[36:39],a[244:247],v[80:83]
RD a[148:151],v223 offset:4096
v_xor_b32 v215, v211, v212
MM v[88:91],a[36:39],a[252:255],v[88:91]
s_lshl_b32 s77, %[swave], 2
RD a[152:155],v222 offset:6144
v_xor_b32 v213, s77, v215
MM v[88:91],a[32:35],a[248:251],v[88:91]
s_waitcnt lgkmcnt(15)
RD a[156:159],v223 offset:6144
buffer_store_dwordx4 v[132:135], v168, s[20:23], s82 offen offset:512 sc1
v_xor_b32 v216, 0, v213
MM v[68:71],a[40:43],a[224:227],v[68:71]
TR a[32:33],v198 offset:16448
TR a[34:35],v198 offset:16960
MM v[68:71],a[44:47],a[228:231],v[68:71]
TR a[36:37],v198 offset:24672
TR a[38:39],v198 offset:25184
MM v[76:79],a[44:47],a[236:239],v[76:79]
buffer_store_dwordx2 v[136:137], v181, s[20:23], s82 offen offset:1024 sc1
MM v[76:79],a[40:43],a[232:235],v[76:79]
s_waitcnt lgkmcnt(4)
s_add_u32 s88, s68, s86
s_add_u32 s86, s86, 128
MM v[84:87],a[40:43],a[240:243],v[84:87]
s_cmp_ge_u32 s86, 1280
s_cselect_b32 s86, 0, s86
MM v[84:87],a[44:47],a[244:247],v[84:87]
s_add_u32 m0, s94, 0
s_add_u32 s95, s88, 0
v_lshlrev_b32 v148, 16, v128
MM v[92:95],a[44:47],a[252:255],v[92:95]
buffer_load_dwordx4 v220, s[32:35], s95 offen offset:0 lds
MM v[92:95],a[40:43],a[248:251],v[92:95]
s_add_u32 s95, s88, 9216
v_and_b32 v149, 0xffff0000, v128
v_lshlrev_b32 v176, 16, v138
MM v[96:99],a[48:51],a[224:227],v[96:99]
TR a[40:41],v198 offset:16456
TR a[42:43],v198 offset:16968
MM v[96:99],a[52:55],a[228:231],v[96:99]
TR a[44:45],v198 offset:24680
TR a[46:47],v198 offset:25192
v_and_b32 v177, 0xffff0000, v138
MM v[104:107],a[52:55],a[236:239],v[104:107]
buffer_load_dwordx4 v221, s[32:35], s95 offen offset:1024 lds
MM v[104:107],a[48:51],a[232:235],v[104:107]
s_add_u32 s95, s88, 18432
v_mul_f32 v170, v148, v176
v_mul_f32 v171, v149, v177
MM v[112:115],a[48:51],a[240:243],v[112:115]
buffer_load_dwordx4 v220, s[32:35], s95 offen offset:2048 lds
MM v[112:115],a[52:55],a[244:247],v[112:115]
s_add_u32 s95, s88, 27648
v_lshlrev_b32 v148, 16, v129
v_and_b32 v149, 0xffff0000, v129
MM v[120:123],a[52:55],a[252:255],v[120:123]
v_lshlrev_b32 v176, 16, v139
v_and_b32 v177, 0xffff0000, v139
MM v[120:123],a[48:51],a[248:251],v[120:123]
v_fma_f32 v170, v148, v176, v170
v_fma_f32 v171, v149, v177, v171
MM v[100:103],a[56:59],a[224:227],v[100:103]
TR a[48:49],v199 offset:16448
TR a[50:51],v199 offset:16960
MM v[100:103],a[60:63],a[228:231],v[100:103]
TR a[52:53],v199 offset:24672
TR a[54:55],v199 offset:25184
v_lshlrev_b32 v148, 16, v130
MM v[108:111],a[60:63],a[236:239],v[108:111]
buffer_load_dwordx4 v221, s[32:35], s95 offen offset:3072 lds
MM v[108:111],a[56:59],a[232:235],v[108:111]
s_sub_u32 s88, s88, s63
s_add_u32 m0, s94, 4096
s_add_u32 s95, s88, 40960
MM v[116:119],a[56:59],a[240:243],v[116:119]
buffer_load_dwordx4 v220, s[32:35], s95 offen offset:0 lds
v_and_b32 v149, 0xffff0000, v130
MM v[116:119],a[60:63],a[244:247],v[116:119]
s_add_u32 s95, s88, 50176
v_lshlrev_b32 v176, 16, v140
v_and_b32 v177, 0xffff0000, v140
s_nop 0
LPG2_%=:
s_cbranch_execz LPG6_%=
MM v[124:127],a[60:63],a[252:255],v[124:127]
v_fma_f32 v170, v148, v176, v170
v_fma_f32 v171, v149, v177, v171
MM v[124:127],a[56:59],a[248:251],v[124:127]
v_lshlrev_b32 v148, 16, v131
v_and_b32 v149, 0xffff0000, v131
s_waitcnt vmcnt(20)
s_barrier
MM v[0:3],a[0:3],a[128:131],v[0:3]
TR a[56:57],v199 offset:16456
TR a[58:59],v199 offset:16968
MM v[0:3],a[4:7],a[132:135],v[0:3]
TR a[60:61],v199 offset:24680
TR a[62:63],v199 offset:25192
v_lshlrev_b32 v176, 16, v141
MM v[8:11],a[4:7],a[140:143],v[8:11]
buffer_load_dwordx4 v221, s[32:35], s95 offen offset:1024 lds
MM v[8:11],a[0:3],a[136:139],v[8:11]
s_add_u32 s95, s88, 59392
v_and_b32 v177, 0xffff0000, v141
v_fma_f32 v170, v148, v176, v170
MM v[16:19],a[0:3],a[144:147],v[16:19]
buffer_load_dwordx4 v220, s[32:35], s95 offen offset:2048 lds
MM v[16:19],a[4:7],a[148:151],v[16:19]
s_add_u32 s95, s88, 68608
v_fma_f32 v171, v149, v177, v171
v_lshlrev_b32 v148, 16, v132
MM v[24:27],a[4:7],a[156:159],v[24:27]
v_and_b32 v149, 0xffff0000, v132
v_lshlrev_b32 v176, 16, v142
MM v[24:27],a[0:3],a[152:155],v[24:27]
v_and_b32 v177, 0xffff0000, v142
v_fma_f32 v170, v148, v176, v170
MM v[4:7],a[8:11],a[128:131],v[4:7]
buffer_load_dwordx4 v221, s[32:35], s95 offen offset:3072 lds
MM v[4:7],a[12:15],a[132:135],v[4:7]
s_add_u32 s87, s89, s85
s_add_u32 s85, s85, 524288
v_fma_f32 v171, v149, v177, v171
MM v[12:15],a[12:15],a[140:143],v[12:15]
s_cmp_ge_u32 s85, 5242880
s_cselect_b32 s85, 0, s85
s_add_u32 m0, s96, 98432
MM v[12:15],a[8:11],a[136:139],v[12:15]
v_lshlrev_b32 v148, 16, v133
v_and_b32 v149, 0xffff0000, v133
MM v[20:23],a[8:11],a[144:147],v[20:23]
buffer_load_dwordx4 v253, s[16:19], s87 offen offset:0 lds
MM v[20:23],a[12:15],a[148:151],v[20:23]
s_add_u32 m0, s96, 99456
v_lshlrev_b32 v176, 16, v143
v_and_b32 v177, 0xffff0000, v143
MM v[28:31],a[12:15],a[156:159],v[28:31]
v_fma_f32 v170, v148, v176, v170
v_fma_f32 v171, v149, v177, v171
MM v[28:31],a[8:11],a[152:155],v[28:31]
v_lshlrev_b32 v148, 16, v134
v_and_b32 v149, 0xffff0000, v134
v_smfmac_f32_16x16x64_bf16 v[184:187], v[236:239], a[0:7], v234
v_lshlrev_b32 v176, 16, v144
v_smfmac_f32_16x16x64_bf16 v[188:191], v[236:239], a[8:15], v234
v_and_b32 v177, 0xffff0000, v144
MM v[32:35],a[16:19],a[128:131],v[32:35]
TR a[0:1],v192 offset:0
TR a[2:3],v192 offset:512
buffer_load_dwordx4 v254, s[16:19], s87 offen offset:0 lds
MM v[32:35],a[20:23],a[132:135],v[32:35]
s_add_u32 s87, s87, 65536
TR a[4:5],v192 offset:8224
TR a[6:7],v192 offset:8736
v_fma_f32 v170, v148, v176, v170
MM v[40:43],a[20:23],a[140:143],v[40:43]
s_add_u32 m0, s96, 100488
TR a[8:9],v192 offset:8
TR a[10:11],v192 offset:520
MM v[40:43],a[16:19],a[136:139],v[40:43]
TR a[12:13],v192 offset:8232
TR a[14:15],v192 offset:8744
MM v[48:51],a[16:19],a[144:147],v[48:51]
buffer_load_dwordx4 v253, s[16:19], s87 offen offset:0 lds
v_fma_f32 v171, v149, v177, v171
MM v[48:51],a[20:23],a[148:151],v[48:51]
s_add_u32 m0, s96, 101512
v_lshlrev_b32 v148, 16, v135
v_and_b32 v149, 0xffff0000, v135
MM v[56:59],a[20:23],a[156:159],v[56:59]
buffer_load_dwordx4 v254, s[16:19], s87 offen offset:0 lds
MM v[56:59],a[16:19],a[152:155],v[56:59]
s_waitcnt vmcnt(19)
RD a[160:163],v222 offset:32768
v_lshlrev_b32 v176, 16, v145
MM v[36:39],a[24:27],a[128:131],v[36:39]
TR a[16:17],v193 offset:0
TR a[18:19],v193 offset:512
MM v[36:39],a[28:31],a[132:135],v[36:39]
TR a[20:21],v193 offset:8224
TR a[22:23],v193 offset:8736
v_and_b32 v177, 0xffff0000, v145
MM v[44:47],a[28:31],a[140:143],v[44:47]
RD a[164:167],v223 offset:32768
v_fma_f32 v170, v148, v176, v170
MM v[44:47],a[24:27],a[136:139],v[44:47]
RD a[168:171],v222 offset:34816
v_fma_f32 v171, v149, v177, v171
MM v[52:55],a[24:27],a[144:147],v[52:55]
RD a[172:175],v223 offset:34816
v_lshlrev_b32 v148, 16, v136
MM v[52:55],a[28:31],a[148:151],v[52:55]
RD a[176:179],v222 offset:36864
v_and_b32 v149, 0xffff0000, v136
MM v[60:63],a[28:31],a[156:159],v[60:63]
RD a[180:183],v223 offset:36864
v_lshlrev_b32 v176, 16, v146
MM v[60:63],a[24:27],a[152:155],v[60:63]
s_waitcnt lgkmcnt(15)
RD a[184:187],v222 offset:38912
v_and_b32 v177, 0xffff0000, v146
MM v[64:67],a[32:35],a[128:131],v[64:67]
TR a[24:25],v193 offset:8
TR a[26:27],v193 offset:520
MM v[64:67],a[36:39],a[132:135],v[64:67]
TR a[28:29],v193 offset:8232
TR a[30:31],v193 offset:8744
v_fma_f32 v170, v148, v176, v170
MM v[72:75],a[36:39],a[140:143],v[72:75]
RD a[188:191],v223 offset:38912
v_fma_f32 v171, v149, v177, v171
MM v[72:75],a[32:35],a[136:139],v[72:75]
v_lshlrev_b32 v148, 16, v137
v_and_b32 v149, 0xffff0000, v137
MM v[80:83],a[32:35],a[144:147],v[80:83]
v_lshlrev_b32 v176, 16, v147
v_and_b32 v177, 0xffff0000, v147
MM v[80:83],a[36:39],a[148:151],v[80:83]
v_fma_f32 v170, v148, v176, v170
v_fma_f32 v171, v149, v177, v171
MM v[88:91],a[36:39],a[156:159],v[88:91]
s_waitcnt lgkmcnt(0)
s_add_u32 s88, s68, s86
s_add_u32 s86, s86, 128
MM v[88:91],a[32:35],a[152:155],v[88:91]
s_cmp_ge_u32 s86, 1280
s_cselect_b32 s86, 0, s86
s_add_u32 m0, s94, 32768
v_add_f32 v170, v170, v171
MM v[68:71],a[40:43],a[128:131],v[68:71]
s_add_u32 s95, s88, 0
TR a[32:33],v194 offset:0
TR a[34:35],v194 offset:512
MM v[68:71],a[44:47],a[132:135],v[68:71]
TR a[36:37],v194 offset:8224
TR a[38:39],v194 offset:8736
MM v[76:79],a[44:47],a[140:143],v[76:79]
buffer_load_dwordx4 v220, s[32:35], s95 offen offset:0 lds
v_add_f32_dpp v170, v170, v170 row_ror:8 row_mask:0xf bank_mask:0xf
MM v[76:79],a[40:43],a[136:139],v[76:79]
s_add_u32 s95, s88, 9216
v_add_f32_dpp v170, v170, v170 row_ror:4 row_mask:0xf bank_mask:0xf
v_lshl_add_u32 v224, v216, 4, v214
MM v[84:87],a[40:43],a[144:147],v[84:87]
buffer_load_dwordx4 v221, s[32:35], s95 offen offset:1024 lds
MM v[84:87],a[44:47],a[148:151],v[84:87]
s_add_u32 s95, s88, 18432
v_add_f32_dpp v170, v170, v170 row_ror:2 row_mask:0xf bank_mask:0xf
v_xor_b32 v216, 4, v213
MM v[92:95],a[44:47],a[156:159],v[92:95]
v_add_f32_dpp v170, v170, v170 row_ror:1 row_mask:0xf bank_mask:0xf
v_mov_b32 v173, v170
MM v[92:95],a[40:43],a[152:155],v[92:95]
s_nop 0
v_permlane16_swap_b32 v170, v173
v_add_f32 v170, v170, v173
MM v[96:99],a[48:51],a[128:131],v[96:99]
TR a[40:41],v194 offset:8
TR a[42:43],v194 offset:520
MM v[96:99],a[52:55],a[132:135],v[96:99]
TR a[44:45],v194 offset:8232
TR a[46:47],v194 offset:8744
CV v170,v170,v170
MM v[104:107],a[52:55],a[140:143],v[104:107]
buffer_load_dwordx4 v220, s[32:35], s95 offen offset:2048 lds
MM v[104:107],a[48:51],a[136:139],v[104:107]
s_add_u32 s95, s88, 27648
v_ashrrev_i32 v171, 31, v170
v_or_b32 v171, 0x80000000, v171
MM v[112:115],a[48:51],a[144:147],v[112:115]
buffer_load_dwordx4 v221, s[32:35], s95 offen offset:3072 lds
MM v[112:115],a[52:55],a[148:151],v[112:115]
s_sub_u32 s88, s88, s63
s_add_u32 m0, s94, 36864
s_add_u32 s95, s88, 40960
v_xor_b32 v170, v170, v171
MM v[120:123],a[52:55],a[156:159],v[120:123]
v_and_b32 v170, 0xffff0000, v170
v_sub_u32 v171, s79, v180
MM v[120:123],a[48:51],a[152:155],v[120:123]
v_or_b32 v170, v170, v171
v_lshlrev_b32 v173, 2, v180
MM v[100:103],a[56:59],a[128:131],v[100:103]
TR a[48:49],v195 offset:0
TR a[50:51],v195 offset:512
MM v[100:103],a[60:63],a[132:135],v[100:103]
TR a[52:53],v195 offset:8224
TR a[54:55],v195 offset:8736
MM v[108:111],a[60:63],a[140:143],v[108:111]
buffer_load_dwordx4 v220, s[32:35], s95 offen offset:0 lds
s_mov_b32 exec_lo, 1
s_mov_b32 exec_hi, 1
buffer_store_dword v170, v173, s[20:23], s78 offen offset:0 sc1
s_mov_b64 exec, -1
MM v[108:111],a[56:59],a[136:139],v[108:111]
s_add_u32 s95, s88, 50176
v_mov_b32 v171, s69
v_lshl_add_u32 v225, v216, 4, v214
MM v[116:119],a[56:59],a[144:147],v[116:119]
buffer_load_dwordx4 v221, s[32:35], s95 offen offset:1024 lds
MM v[116:119],a[60:63],a[148:151],v[116:119]
s_add_u32 s95, s88, 59392
v_xor_b32 v216, 8, v213
v_lshl_add_u32 v226, v216, 4, v214
MM v[124:127],a[60:63],a[156:159],v[124:127]
v_xor_b32 v216, 12, v213
v_lshl_add_u32 v227, v216, 4, v214
MM v[124:127],a[56:59],a[152:155],v[124:127]
v_lshlrev_b32 v214, 8, v212
v_xor_b32 v216, 0, v215
s_waitcnt vmcnt(22)
s_barrier
MM v[0:3],a[0:3],a[160:163],v[0:3]
TR a[56:57],v195 offset:8
TR a[58:59],v195 offset:520
MM v[0:3],a[4:7],a[164:167],v[0:3]
TR a[60:61],v195 offset:8232
TR a[62:63],v195 offset:8744
MM v[8:11],a[4:7],a[172:175],v[8:11]
buffer_load_dwordx4 v220, s[32:35], s95 offen offset:2048 lds
MM v[8:11],a[0:3],a[168:171],v[8:11]
s_add_u32 s95, s88, 68608
v_lshl_add_u32 v228, v216, 4, v214
v_xor_b32 v216, 4, v215
MM v[16:19],a[0:3],a[176:179],v[16:19]
buffer_load_dwordx4 v221, s[32:35], s95 offen offset:3072 lds
MM v[16:19],a[4:7],a[180:183],v[16:19]
s_add_u32 s87, s89, s85
)ASM"
R"ASM(
s_add_u32 s85, s85, 524288
v_lshl_add_u32 v229, v216, 4, v214
MM v[24:27],a[4:7],a[188:191],v[24:27]
s_cmp_ge_u32 s85, 5242880
s_cselect_b32 s85, 0, s85
s_add_u32 m0, s96, 114880
MM v[24:27],a[0:3],a[184:187],v[24:27]
v_xor_b32 v216, 8, v215
v_lshl_add_u32 v230, v216, 4, v214
MM v[4:7],a[8:11],a[160:163],v[4:7]
buffer_load_dwordx4 v253, s[16:19], s87 offen offset:0 lds
MM v[4:7],a[12:15],a[164:167],v[4:7]
s_add_u32 m0, s96, 115904
v_xor_b32 v216, 12, v215
v_lshl_add_u32 v231, v216, 4, v214
MM v[12:15],a[12:15],a[172:175],v[12:15]
s_lshl_b32 s90, s49, 19
s_add_u32 s8, s8, s90
s_addc_u32 s9, s9, 0
MM v[12:15],a[8:11],a[168:171],v[12:15]
s_mov_b32 s10, 524288
s_mov_b32 s11, 131072
MM v[20:23],a[8:11],a[176:179],v[20:23]
buffer_load_dwordx4 v254, s[16:19], s87 offen offset:0 lds
MM v[20:23],a[12:15],a[180:183],v[20:23]
s_add_u32 s87, s87, 65536
s_add_u32 s12, s12, s90
s_addc_u32 s13, s13, 0
MM v[28:31],a[12:15],a[188:191],v[28:31]
s_add_u32 m0, s96, 116936
s_mov_b32 s14, 525312
s_mov_b32 s15, 131072
MM v[28:31],a[8:11],a[184:187],v[28:31]
s_sub_u32 s12, s12, 1024
s_subb_u32 s13, s13, 0
s_lshr_b32 s90, s49, 3
v_smfmac_f32_16x16x64_bf16 v[184:187], v[236:239], a[0:7], v234
s_mul_i32 s90, s90, 1310720
s_mul_i32 s77, s84, 320
v_smfmac_f32_16x16x64_bf16 v[188:191], v[236:239], a[8:15], v234
s_add_u32 s90, s90, s77
MM v[32:35],a[16:19],a[160:163],v[32:35]
TR a[0:1],v192 offset:16448
TR a[2:3],v192 offset:16960
buffer_load_dwordx4 v253, s[16:19], s87 offen offset:0 lds
MM v[32:35],a[20:23],a[164:167],v[32:35]
s_add_u32 m0, s96, 117960
TR a[4:5],v192 offset:24672
TR a[6:7],v192 offset:25184
MM v[40:43],a[20:23],a[172:175],v[40:43]
TR a[8:9],v192 offset:16456
TR a[10:11],v192 offset:16968
MM v[40:43],a[16:19],a[168:171],v[40:43]
TR a[12:13],v192 offset:24680
TR a[14:15],v192 offset:25192
MM v[48:51],a[16:19],a[176:179],v[48:51]
buffer_load_dwordx4 v254, s[16:19], s87 offen offset:0 lds
MM v[48:51],a[20:23],a[180:183],v[48:51]
s_waitcnt vmcnt(17)
s_add_u32 s24, s24, s90
s_addc_u32 s25, s25, 0
MM v[56:59],a[20:23],a[188:191],v[56:59]
s_sub_u32 s26, 5242880, s90
RD a[192:195],v222 offset:0
s_mov_b32 s27, 131072
MM v[56:59],a[16:19],a[184:187],v[56:59]
s_mov_b32 s38, 2211840
RD a[196:199],v223 offset:0
s_mov_b32 s39, 131072
MM v[36:39],a[24:27],a[160:163],v[36:39]
s_mov_b32 s71, 0x4e800000
TR a[16:17],v193 offset:16448
TR a[18:19],v193 offset:16960
MM v[36:39],a[28:31],a[164:167],v[36:39]
s_mov_b32 s72, 0x3e0293ee
TR a[20:21],v193 offset:24672
TR a[22:23],v193 offset:25184
MM v[44:47],a[28:31],a[172:175],v[44:47]
s_lshl_b32 s64, %[swave], 12
RD a[200:203],v222 offset:2048
MM v[44:47],a[24:27],a[168:171],v[44:47]
RD a[204:207],v223 offset:2048
MM v[52:55],a[24:27],a[176:179],v[52:55]
RD a[208:211],v222 offset:4096
MM v[52:55],a[28:31],a[180:183],v[52:55]
RD a[212:215],v223 offset:4096
MM v[60:63],a[28:31],a[188:191],v[60:63]
RD a[216:219],v222 offset:6144
MM v[60:63],a[24:27],a[184:187],v[60:63]
s_waitcnt lgkmcnt(15)
RD a[220:223],v223 offset:6144
MM v[64:67],a[32:35],a[160:163],v[64:67]
TR a[24:25],v193 offset:16456
TR a[26:27],v193 offset:16968
MM v[64:67],a[36:39],a[164:167],v[64:67]
TR a[28:29],v193 offset:24680
TR a[30:31],v193 offset:25192
MM v[72:75],a[36:39],a[172:175],v[72:75]
MM v[72:75],a[32:35],a[168:171],v[72:75]
s_waitcnt lgkmcnt(4)
s_add_u32 s88, s68, s86
s_add_u32 s86, s86, 128
MM v[80:83],a[32:35],a[176:179],v[80:83]
s_cmp_ge_u32 s86, 1280
s_cselect_b32 s86, 0, s86
s_add_u32 m0, s94, 0
MM v[80:83],a[36:39],a[180:183],v[80:83]
s_add_u32 s95, s88, 0
buffer_load_dwordx4 v220, s[32:35], s95 offen offset:0 lds
MM v[88:91],a[36:39],a[188:191],v[88:91]
s_add_u32 s95, s88, 9216
MM v[88:91],a[32:35],a[184:187],v[88:91]
buffer_load_dwordx4 v221, s[32:35], s95 offen offset:1024 lds
MM v[68:71],a[40:43],a[160:163],v[68:71]
s_add_u32 s95, s88, 18432
TR a[32:33],v194 offset:16448
TR a[34:35],v194 offset:16960
MM v[68:71],a[44:47],a[164:167],v[68:71]
TR a[36:37],v194 offset:24672
TR a[38:39],v194 offset:25184
MM v[76:79],a[44:47],a[172:175],v[76:79]
MM v[76:79],a[40:43],a[168:171],v[76:79]
buffer_load_dwordx4 v220, s[32:35], s95 offen offset:2048 lds
MM v[84:87],a[40:43],a[176:179],v[84:87]
s_add_u32 s95, s88, 27648
MM v[84:87],a[44:47],a[180:183],v[84:87]
MM v[92:95],a[44:47],a[188:191],v[92:95]
MM v[92:95],a[40:43],a[184:187],v[92:95]
buffer_load_dwordx4 v221, s[32:35], s95 offen offset:3072 lds
MM v[96:99],a[48:51],a[160:163],v[96:99]
s_sub_u32 s88, s88, s63
TR a[40:41],v194 offset:16456
TR a[42:43],v194 offset:16968
MM v[96:99],a[52:55],a[164:167],v[96:99]
s_add_u32 m0, s94, 4096
TR a[44:45],v194 offset:24680
TR a[46:47],v194 offset:25192
MM v[104:107],a[52:55],a[172:175],v[104:107]
s_add_u32 s95, s88, 40960
MM v[104:107],a[48:51],a[168:171],v[104:107]
buffer_load_dwordx4 v220, s[32:35], s95 offen offset:0 lds
MM v[112:115],a[48:51],a[176:179],v[112:115]
s_add_u32 s95, s88, 50176
MM v[112:115],a[52:55],a[180:183],v[112:115]
MM v[120:123],a[52:55],a[188:191],v[120:123]
MM v[120:123],a[48:51],a[184:187],v[120:123]
buffer_load_dwordx4 v221, s[32:35], s95 offen offset:1024 lds
MM v[100:103],a[56:59],a[160:163],v[100:103]
s_add_u32 s95, s88, 59392
TR a[48:49],v195 offset:16448
TR a[50:51],v195 offset:16960
MM v[100:103],a[60:63],a[164:167],v[100:103]
TR a[52:53],v195 offset:24672
TR a[54:55],v195 offset:25184
MM v[108:111],a[60:63],a[172:175],v[108:111]
MM v[108:111],a[56:59],a[168:171],v[108:111]
buffer_load_dwordx4 v220, s[32:35], s95 offen offset:2048 lds
MM v[116:119],a[56:59],a[176:179],v[116:119]
s_add_u32 s95, s88, 68608
MM v[116:119],a[60:63],a[180:183],v[116:119]
MM v[124:127],a[60:63],a[188:191],v[124:127]
MM v[124:127],a[56:59],a[184:187],v[124:127]
buffer_load_dwordx4 v221, s[32:35], s95 offen offset:3072 lds
s_waitcnt vmcnt(21)
s_barrier
MM v[0:3],a[0:3],a[192:195],v[0:3]
s_add_u32 s87, s89, s85
TR a[56:57],v195 offset:16456
TR a[58:59],v195 offset:16968
MM v[0:3],a[4:7],a[196:199],v[0:3]
s_add_u32 s85, s85, 524288
TR a[60:61],v195 offset:24680
TR a[62:63],v195 offset:25192
MM v[8:11],a[4:7],a[204:207],v[8:11]
s_cmp_ge_u32 s85, 5242880
s_cselect_b32 s85, 0, s85
s_add_u32 m0, s96, 131328
v_and_b32 v208, 240, v248
MM v[8:11],a[0:3],a[200:203],v[8:11]
s_lshl_b32 s62, s62, 8
buffer_load_dwordx4 v253, s[16:19], s87 offen offset:0 lds
MM v[16:19],a[0:3],a[208:211],v[16:19]
s_add_u32 m0, s96, 132352
s_add_u32 s93, s93, 36864
s_add_u32 s62, s62, s93
MM v[16:19],a[4:7],a[212:215],v[16:19]
s_xor_b32 s93, s62, 256
MM v[24:27],a[4:7],a[220:223],v[24:27]
MM v[24:27],a[0:3],a[216:219],v[24:27]
buffer_load_dwordx4 v254, s[16:19], s87 offen offset:0 lds
MM v[4:7],a[8:11],a[192:195],v[4:7]
s_add_u32 s87, s87, 65536
s_add_u32 m0, s96, 133384
MM v[4:7],a[12:15],a[196:199],v[4:7]
MM v[12:15],a[12:15],a[204:207],v[12:15]
MM v[12:15],a[8:11],a[200:203],v[12:15]
buffer_load_dwordx4 v253, s[16:19], s87 offen offset:0 lds
MM v[20:23],a[8:11],a[208:211],v[20:23]
s_add_u32 m0, s96, 134408
MM v[20:23],a[12:15],a[212:215],v[20:23]
MM v[28:31],a[12:15],a[220:223],v[28:31]
MM v[28:31],a[8:11],a[216:219],v[28:31]
buffer_load_dwordx4 v254, s[16:19], s87 offen offset:0 lds
v_smfmac_f32_16x16x64_bf16 v[184:187], v[236:239], a[0:7], v234
s_waitcnt vmcnt(16)
RD a[224:227],v222 offset:32768
v_smfmac_f32_16x16x64_bf16 v[188:191], v[236:239], a[8:15], v234
RD a[228:231],v223 offset:32768
MM v[32:35],a[16:19],a[192:195],v[32:35]
TR a[0:1],v192 offset:32896
TR a[2:3],v192 offset:33408
MM v[32:35],a[20:23],a[196:199],v[32:35]
TR a[4:5],v192 offset:41120
TR a[6:7],v192 offset:41632
MM v[40:43],a[20:23],a[204:207],v[40:43]
TR a[8:9],v192 offset:32904
TR a[10:11],v192 offset:33416
RD a[232:235],v222 offset:34816
MM v[40:43],a[16:19],a[200:203],v[40:43]
TR a[12:13],v192 offset:41128
TR a[14:15],v192 offset:41640
buffer_load_dwordx4 v[200:203], v208, s[20:23], s62 offen offset:0 sc1
MM v[48:51],a[16:19],a[208:211],v[48:51]
RD a[236:239],v223 offset:34816
MM v[48:51],a[20:23],a[212:215],v[48:51]
buffer_load_dwordx4 v[204:207], v208, s[20:23], s93 offen offset:0 sc1
MM v[56:59],a[20:23],a[220:223],v[56:59]
RD a[240:243],v222 offset:36864
MM v[56:59],a[16:19],a[216:219],v[56:59]
s_waitcnt lgkmcnt(15)
RD a[244:247],v223 offset:36864
MM v[36:39],a[24:27],a[192:195],v[36:39]
TR a[16:17],v193 offset:32896
TR a[18:19],v193 offset:33408
MM v[36:39],a[28:31],a[196:199],v[36:39]
TR a[20:21],v193 offset:41120
TR a[22:23],v193 offset:41632
MM v[44:47],a[28:31],a[204:207],v[44:47]
RD a[248:251],v222 offset:38912
MM v[44:47],a[24:27],a[200:203],v[44:47]
RD a[252:255],v223 offset:38912
MM v[52:55],a[24:27],a[208:211],v[52:55]
MM v[52:55],a[28:31],a[212:215],v[52:55]
MM v[60:63],a[28:31],a[220:223],v[60:63]
MM v[60:63],a[24:27],a[216:219],v[60:63]
s_waitcnt lgkmcnt(0)
s_add_u32 s88, s68, s86
s_add_u32 s86, s86, 128
MM v[64:67],a[32:35],a[192:195],v[64:67]
TR a[24:25],v193 offset:32904
TR a[26:27],v193 offset:33416
MM v[64:67],a[36:39],a[196:199],v[64:67]
TR a[28:29],v193 offset:41128
TR a[30:31],v193 offset:41640
s_cmp_ge_u32 s86, 1280
s_cselect_b32 s86, 0, s86
MM v[72:75],a[36:39],a[204:207],v[72:75]
s_add_u32 m0, s94, 32768
s_add_u32 s95, s88, 0
MM v[72:75],a[32:35],a[200:203],v[72:75]
buffer_load_dwordx4 v220, s[32:35], s95 offen offset:0 lds
MM v[80:83],a[32:35],a[208:211],v[80:83]
s_add_u32 s95, s88, 9216
MM v[80:83],a[36:39],a[212:215],v[80:83]
buffer_load_dwordx4 v221, s[32:35], s95 offen offset:1024 lds
MM v[88:91],a[36:39],a[220:223],v[88:91]
s_add_u32 s95, s88, 18432
MM v[88:91],a[32:35],a[216:219],v[88:91]
MM v[68:71],a[40:43],a[192:195],v[68:71]
TR a[32:33],v194 offset:32896
TR a[34:35],v194 offset:33408
MM v[68:71],a[44:47],a[196:199],v[68:71]
TR a[36:37],v194 offset:41120
TR a[38:39],v194 offset:41632
MM v[76:79],a[44:47],a[204:207],v[76:79]
buffer_load_dwordx4 v220, s[32:35], s95 offen offset:2048 lds
MM v[76:79],a[40:43],a[200:203],v[76:79]
s_add_u32 s95, s88, 27648
MM v[84:87],a[40:43],a[208:211],v[84:87]
MM v[84:87],a[44:47],a[212:215],v[84:87]
buffer_load_dwordx4 v221, s[32:35], s95 offen offset:3072 lds
MM v[92:95],a[44:47],a[220:223],v[92:95]
s_waitcnt vmcnt(10)
RD a[128:131],v222 offset:0
MM v[92:95],a[40:43],a[216:219],v[92:95]
RD a[132:135],v223 offset:0
MM v[96:99],a[48:51],a[192:195],v[96:99]
TR a[40:41],v194 offset:32904
TR a[42:43],v194 offset:33416
MM v[96:99],a[52:55],a[196:199],v[96:99]
TR a[44:45],v194 offset:41128
TR a[46:47],v194 offset:41640
MM v[104:107],a[52:55],a[204:207],v[104:107]
RD a[136:139],v222 offset:2048
MM v[104:107],a[48:51],a[200:203],v[104:107]
RD a[140:143],v223 offset:2048
MM v[112:115],a[48:51],a[208:211],v[112:115]
RD a[144:147],v222 offset:4096
MM v[112:115],a[52:55],a[212:215],v[112:115]
RD a[148:151],v223 offset:4096
MM v[120:123],a[52:55],a[220:223],v[120:123]
RD a[152:155],v222 offset:6144
MM v[120:123],a[48:51],a[216:219],v[120:123]
RD a[156:159],v223 offset:6144
MM v[100:103],a[56:59],a[192:195],v[100:103]
s_sub_u32 s88, s88, s63
TR a[48:49],v195 offset:32896
TR a[50:51],v195 offset:33408
MM v[100:103],a[60:63],a[196:199],v[100:103]
s_nop 0
LPG3_%=:
s_cbranch_execz LPG7_%=
s_add_u32 m0, s94, 36864
TR a[52:53],v195 offset:41120
TR a[54:55],v195 offset:41632
MM v[108:111],a[60:63],a[204:207],v[108:111]
s_add_u32 s95, s88, 40960
buffer_load_dwordx4 v220, s[32:35], s95 offen offset:0 lds
MM v[108:111],a[56:59],a[200:203],v[108:111]
s_add_u32 s95, s88, 50176
MM v[116:119],a[56:59],a[208:211],v[116:119]
buffer_load_dwordx4 v221, s[32:35], s95 offen offset:1024 lds
MM v[116:119],a[60:63],a[212:215],v[116:119]
s_add_u32 s95, s88, 59392
MM v[124:127],a[60:63],a[220:223],v[124:127]
MM v[124:127],a[56:59],a[216:219],v[124:127]
buffer_load_dwordx4 v220, s[32:35], s95 offen offset:2048 lds
s_add_u32 s95, s88, 68608
buffer_load_dwordx4 v221, s[32:35], s95 offen offset:3072 lds
s_waitcnt lgkmcnt(4)
s_waitcnt vmcnt(10)
s_barrier
MM v[0:3],a[0:3],a[224:227],v[0:3]
s_add_u32 s77, s64, 0
TR a[56:57],v195 offset:32904
TR a[58:59],v195 offset:33416
MM v[0:3],a[4:7],a[228:231],v[0:3]
s_add_u32 m0, s65, 0
TR a[60:61],v195 offset:41128
TR a[62:63],v195 offset:41640
MM v[8:11],a[4:7],a[236:239],v[8:11]
)ASM"
R"ASM(
s_waitcnt vmcnt(8)
TR a[64:65],v196 offset:0
TR a[66:67],v196 offset:512
MM v[8:11],a[0:3],a[232:235],v[8:11]
s_lshl_b32 s76, %[swave], 7
buffer_load_dwordx4 v228, s[8:11], s77 offen offset:0 lds
v_lshrrev_b32 v253, 8, v248
v_lshlrev_b32 v253, 5, v253
MM v[16:19],a[0:3],a[240:243],v[16:19]
s_add_u32 s76, s76, %[slds]
TR a[68:69],v196 offset:8224
TR a[70:71],v196 offset:8736
v_xor_b32 v201, s69, v201
MM v[16:19],a[4:7],a[244:247],v[16:19]
s_mov_b64 exec, 1
buffer_store_dword v171, off, s[20:23], s80 sc1
s_mov_b64 exec, -1
v_add_u32 v253, s76, v253
v_add_u32 v253, 151552, v253
MM v[24:27],a[4:7],a[252:255],v[24:27]
TR a[72:73],v196 offset:16448
TR a[74:75],v196 offset:16960
MM v[24:27],a[0:3],a[248:251],v[24:27]
TR a[76:77],v196 offset:24672
TR a[78:79],v196 offset:25184
buffer_load_dwordx4 v229, s[8:11], s77 offen offset:1024 lds
v_xor_b32 v203, s69, v203
MM v[4:7],a[8:11],a[224:227],v[4:7]
TR a[80:81],v196 offset:8
TR a[82:83],v196 offset:520
MM v[4:7],a[12:15],a[228:231],v[4:7]
TR a[84:85],v196 offset:8232
TR a[86:87],v196 offset:8744
MM v[12:15],a[12:15],a[236:239],v[12:15]
TR a[88:89],v196 offset:16456
TR a[90:91],v196 offset:16968
v_xor_b32 v205, s69, v205
MM v[12:15],a[8:11],a[232:235],v[12:15]
TR a[92:93],v196 offset:24680
TR a[94:95],v196 offset:25192
buffer_load_dwordx4 v230, s[8:11], s77 offen offset:2048 lds
MM v[20:23],a[8:11],a[240:243],v[20:23]
TR a[96:97],v197 offset:0
TR a[98:99],v197 offset:512
MM v[20:23],a[12:15],a[244:247],v[20:23]
TR a[100:101],v197 offset:8224
TR a[102:103],v197 offset:8736
v_xor_b32 v207, s69, v207
MM v[28:31],a[12:15],a[252:255],v[28:31]
TR a[104:105],v197 offset:16448
TR a[106:107],v197 offset:16960
MM v[28:31],a[8:11],a[248:251],v[28:31]
TR a[108:109],v197 offset:24672
TR a[110:111],v197 offset:25184
buffer_load_dwordx4 v231, s[8:11], s77 offen offset:3072 lds
v_smfmac_f32_16x16x64_bf16 v[184:187], v[236:239], a[0:7], v234
s_add_u32 s77, s64, 16384
TR a[112:113],v197 offset:8
v_or3_b32 v209, v201, v203, v205
v_smfmac_f32_16x16x64_bf16 v[188:191], v[236:239], a[8:15], v234
s_add_u32 m0, s65, 16384
TR a[114:115],v197 offset:520
MM v[32:35],a[16:19],a[224:227],v[32:35]
TR a[116:117],v197 offset:8232
TR a[118:119],v197 offset:8744
MM v[32:35],a[20:23],a[228:231],v[32:35]
TR a[120:121],v197 offset:16456
TR a[122:123],v197 offset:16968
v_or_b32 v209, v209, v207
MM v[40:43],a[20:23],a[236:239],v[40:43]
TR a[124:125],v197 offset:24680
TR a[126:127],v197 offset:25192
MM v[40:43],a[16:19],a[232:235],v[40:43]
TR a[0:1],v198 offset:0
TR a[2:3],v198 offset:512
buffer_load_dwordx4 v228, s[8:11], s77 offen offset:0 lds
MM v[48:51],a[16:19],a[240:243],v[48:51]
TR a[4:5],v198 offset:8224
TR a[6:7],v198 offset:8736
v_cmp_ne_u32 vcc, 0, v209
s_cbranch_vccnz LRG1o_%=
LRG1ob_%=:
MM v[48:51],a[20:23],a[244:247],v[48:51]
TR a[8:9],v198 offset:16448
TR a[10:11],v198 offset:16960
MM v[56:59],a[20:23],a[252:255],v[56:59]
TR a[12:13],v198 offset:24672
TR a[14:15],v198 offset:25184
MM v[56:59],a[16:19],a[248:251],v[56:59]
v_lshlrev_b32 v240, 16, v200
v_and_b32 v200, 0xffff0000, v200
MM v[36:39],a[24:27],a[224:227],v[36:39]
v_rcp_f32 v244, v200
MM v[36:39],a[28:31],a[228:231],v[36:39]
v_xor_b32 v240, 0x80000000, v240
v_mul_f32 v244, s72, v244
MM v[44:47],a[28:31],a[236:239],v[44:47]
v_lshlrev_b32 v241, 16, v202
v_and_b32 v202, 0xffff0000, v202
MM v[44:47],a[24:27],a[232:235],v[44:47]
buffer_load_dwordx4 v229, s[8:11], s77 offen offset:1024 lds
MM v[52:55],a[24:27],a[240:243],v[52:55]
v_rcp_f32 v245, v202
MM v[52:55],a[28:31],a[244:247],v[52:55]
v_xor_b32 v241, 0x80000000, v241
v_mul_f32 v245, s72, v245
MM v[60:63],a[28:31],a[252:255],v[60:63]
v_lshlrev_b32 v242, 16, v204
v_and_b32 v204, 0xffff0000, v204
MM v[60:63],a[24:27],a[248:251],v[60:63]
v_rcp_f32 v246, v204
MM v[64:67],a[32:35],a[224:227],v[64:67]
TR a[16:17],v198 offset:8
TR a[18:19],v198 offset:520
MM v[64:67],a[36:39],a[228:231],v[64:67]
TR a[20:21],v198 offset:8232
TR a[22:23],v198 offset:8744
v_xor_b32 v242, 0x80000000, v242
MM v[72:75],a[36:39],a[236:239],v[72:75]
TR a[24:25],v198 offset:16456
TR a[26:27],v198 offset:16968
MM v[72:75],a[32:35],a[232:235],v[72:75]
TR a[28:29],v198 offset:24680
TR a[30:31],v198 offset:25192
MM v[80:83],a[32:35],a[240:243],v[80:83]
v_mul_f32 v246, s72, v246
v_lshlrev_b32 v243, 16, v206
MM v[80:83],a[36:39],a[244:247],v[80:83]
buffer_load_dwordx4 v230, s[8:11], s77 offen offset:2048 lds
MM v[88:91],a[36:39],a[252:255],v[88:91]
v_and_b32 v206, 0xffff0000, v206
MM v[88:91],a[32:35],a[248:251],v[88:91]
s_waitcnt lgkmcnt(15)
v_rcp_f32 v247, v206
v_smfmac_f32_16x16x64_bf16 v[184:187], v[236:239], a[64:71], v234
v_xor_b32 v243, 0x80000000, v243
v_smfmac_f32_16x16x64_bf16 v[184:187], v[236:239], a[72:79], v234
v_mul_f32 v247, s72, v247
v_smfmac_f32_16x16x64_bf16 v[188:191], v[236:239], a[80:87], v234
v_smfmac_f32_16x16x64_bf16 v[188:191], v[236:239], a[88:95], v234
MM v[68:71],a[40:43],a[224:227],v[68:71]
v_bfe_u32 v183, v248, 6, 2
v_lshlrev_b32 v183, 5, v183
MM v[68:71],a[44:47],a[228:231],v[68:71]
v_bfe_u32 v182, v248, 4, 2
v_lshl_add_u32 v183, v182, 2, v183
MM v[76:79],a[44:47],a[236:239],v[76:79]
v_add_u32 v183, s76, v183
v_add_u32 v183, 151552, v183
MM v[76:79],a[40:43],a[232:235],v[76:79]
buffer_load_dwordx4 v231, s[8:11], s77 offen offset:3072 lds
MM v[84:87],a[40:43],a[240:243],v[84:87]
s_waitcnt vmcnt(9)
RD a[160:163],v222 offset:32768
MM v[84:87],a[44:47],a[244:247],v[84:87]
RD a[164:167],v223 offset:32768
MM v[92:95],a[44:47],a[252:255],v[92:95]
RD a[168:171],v222 offset:34816
MM v[92:95],a[40:43],a[248:251],v[92:95]
v_add_f32 v184, v184, v185
v_add_f32 v188, v188, v189
MM v[96:99],a[48:51],a[224:227],v[96:99]
TR a[32:33],v199 offset:0
TR a[34:35],v199 offset:512
MM v[96:99],a[52:55],a[228:231],v[96:99]
RD a[172:175],v223 offset:34816
ds_write_b32 v183, v184 offset:0
ds_write_b32 v183, v188 offset:16
MM v[104:107],a[52:55],a[236:239],v[104:107]
TR a[36:37],v199 offset:8224
TR a[38:39],v199 offset:8736
MM v[104:107],a[48:51],a[232:235],v[104:107]
TR a[40:41],v199 offset:16448
TR a[42:43],v199 offset:16960
MM v[112:115],a[48:51],a[240:243],v[112:115]
TR a[44:45],v199 offset:24672
TR a[46:47],v199 offset:25184
RD a[176:179],v222 offset:36864
MM v[112:115],a[52:55],a[244:247],v[112:115]
RD a[180:183],v223 offset:36864
MM v[120:123],a[52:55],a[252:255],v[120:123]
RD a[184:187],v222 offset:38912
MM v[120:123],a[48:51],a[248:251],v[120:123]
RD a[188:191],v223 offset:38912
MM v[100:103],a[56:59],a[224:227],v[100:103]
s_add_u32 s77, s64, 0
s_add_u32 s81, s77, 896
s_add_u32 m0, s65, 65536
MM v[100:103],a[60:63],a[228:231],v[100:103]
buffer_load_dwordx4 v235, s[12:15], s81 offen offset:0 lds
MM v[108:111],a[60:63],a[236:239],v[108:111]
MM v[108:111],a[56:59],a[232:235],v[108:111]
MM v[116:119],a[56:59],a[240:243],v[116:119]
MM v[116:119],a[60:63],a[244:247],v[116:119]
MM v[124:127],a[60:63],a[252:255],v[124:127]
MM v[124:127],a[56:59],a[248:251],v[124:127]
buffer_load_dwordx4 v235, s[12:15], s77 offen offset:1024 lds
s_waitcnt lgkmcnt(0)
s_barrier
MM v[0:3],a[64:67],a[128:131],v[0:3]
s_memrealtime s[52:53]
TR a[48:49],v199 offset:8
TR a[50:51],v199 offset:520
MM v[0:3],a[68:71],a[132:135],v[0:3]
s_memtime s[54:55]
TR a[52:53],v199 offset:8232
TR a[54:55],v199 offset:8744
MM v[0:3],a[72:75],a[160:163],v[0:3]
TR a[56:57],v199 offset:16456
TR a[58:59],v199 offset:16968
MM v[0:3],a[76:79],a[164:167],v[0:3]
TR a[60:61],v199 offset:24680
TR a[62:63],v199 offset:25192
MM v[8:11],a[76:79],a[172:175],v[8:11]
v_xor_b32 v151, 0, v253
v_xor_b32 v143, 0, v253
MM v[8:11],a[72:75],a[168:171],v[8:11]
RD v[144:147],v151 offset:0
RD v[148:151],v151 offset:16
MM v[8:11],a[68:71],a[140:143],v[8:11]
RD v[136:139],v143 offset:0
RD v[140:143],v143 offset:16
MM v[8:11],a[64:67],a[136:139],v[8:11]
MM v[4:7],a[80:83],a[128:131],v[4:7]
MM v[4:7],a[84:87],a[132:135],v[4:7]
s_waitcnt lgkmcnt(2)
buffer_load_dwordx4 v235, s[12:15], s81 offen offset:2048 lds
MM v[4:7],a[88:91],a[160:163],v[4:7]
s_waitcnt lgkmcnt(0)
MM v[4:7],a[92:95],a[164:167],v[4:7]
MM v[12:15],a[92:95],a[172:175],v[12:15]
MM v[12:15],a[88:91],a[168:171],v[12:15]
MM v[12:15],a[84:87],a[140:143],v[12:15]
MM v[12:15],a[80:83],a[136:139],v[12:15]
MM v[32:35],a[96:99],a[128:131],v[32:35]
MM v[32:35],a[100:103],a[132:135],v[32:35]
MM v[32:35],a[104:107],a[160:163],v[32:35]
MM v[32:35],a[108:111],a[164:167],v[32:35]
buffer_load_dwordx4 v235, s[12:15], s77 offen offset:3072 lds
v_fmac_f32 v0, v240, v144
v_fmac_f32 v1, v240, v145
v_fmac_f32 v2, v240, v146
MM v[40:43],a[108:111],a[172:175],v[40:43]
s_add_u32 s77, s64, 32768
s_add_u32 m0, s65, 32768
v_fmac_f32 v3, v240, v147
v_fmac_f32 v4, v240, v148
MM v[40:43],a[104:107],a[168:171],v[40:43]
v_fmac_f32 v5, v240, v149
v_fmac_f32 v6, v240, v150
MM v[40:43],a[100:103],a[140:143],v[40:43]
v_fmac_f32 v7, v240, v151
v_mul_f32 v0, v244, v0
v_mul_f32 v1, v244, v1
MM v[40:43],a[96:99],a[136:139],v[40:43]
v_mul_f32 v2, v244, v2
v_mul_f32 v3, v244, v3
MM v[36:39],a[112:115],a[128:131],v[36:39]
v_mul_f32 v4, v244, v4
v_mul_f32 v5, v244, v5
MM v[36:39],a[116:119],a[132:135],v[36:39]
v_mul_f32 v6, v244, v6
v_mul_f32 v7, v244, v7
CV v160,v0,v1
MM v[36:39],a[120:123],a[160:163],v[36:39]
CV v161,v2,v3
CV v162,v4,v5
MM v[36:39],a[124:127],a[164:167],v[36:39]
CV v163,v6,v7
v_fmac_f32 v8, v241, v144
MM v[44:47],a[124:127],a[172:175],v[44:47]
v_fmac_f32 v9, v241, v145
v_fmac_f32 v10, v241, v146
v_fmac_f32 v11, v241, v147
MM v[44:47],a[120:123],a[168:171],v[44:47]
buffer_load_dwordx4 v228, s[8:11], s77 offen offset:0 lds
v_fmac_f32 v12, v241, v148
v_fmac_f32 v13, v241, v149
MM v[44:47],a[116:119],a[140:143],v[44:47]
v_fmac_f32 v14, v241, v150
v_fmac_f32 v15, v241, v151
v_mul_f32 v8, v245, v8
MM v[44:47],a[112:115],a[136:139],v[44:47]
v_mul_f32 v9, v245, v9
v_mul_f32 v10, v245, v10
MM v[64:67],a[0:3],a[128:131],v[64:67]
v_mul_f32 v11, v245, v11
v_mul_f32 v12, v245, v12
MM v[64:67],a[4:7],a[132:135],v[64:67]
v_mul_f32 v13, v245, v13
v_mul_f32 v14, v245, v14
v_mul_f32 v15, v245, v15
MM v[64:67],a[8:11],a[160:163],v[64:67]
CV v164,v8,v9
CV v165,v10,v11
MM v[64:67],a[12:15],a[164:167],v[64:67]
CV v166,v12,v13
CV v167,v14,v15
MM v[72:75],a[12:15],a[172:175],v[72:75]
v_xor_b32 v159, 128, v253
MM v[72:75],a[8:11],a[168:171],v[72:75]
RD v[152:155],v159 offset:0
RD v[156:159],v159 offset:16
MM v[72:75],a[4:7],a[140:143],v[72:75]
MM v[72:75],a[0:3],a[136:139],v[72:75]
buffer_load_dwordx4 v229, s[8:11], s77 offen offset:1024 lds
MM v[68:71],a[16:19],a[128:131],v[68:71]
MM v[68:71],a[20:23],a[132:135],v[68:71]
s_waitcnt lgkmcnt(0)
v_fmac_f32 v32, v240, v152
v_fmac_f32 v33, v240, v153
v_fmac_f32 v34, v240, v154
v_fmac_f32 v35, v240, v155
v_fmac_f32 v36, v240, v156
v_fmac_f32 v37, v240, v157
v_fmac_f32 v38, v240, v158
v_fmac_f32 v39, v240, v159
v_mul_f32 v179, -v250, v244
v_fma_f32 v32, v244, v32, v179
v_fma_f32 v33, v244, v33, v179
v_fma_f32 v34, v244, v34, v179
MM v[68:71],a[24:27],a[160:163],v[68:71]
v_fma_f32 v35, v244, v35, v179
v_fma_f32 v36, v244, v36, v179
MM v[68:71],a[28:31],a[164:167],v[68:71]
v_fma_f32 v37, v244, v37, v179
v_fma_f32 v38, v244, v38, v179
v_fma_f32 v39, v244, v39, v179
MM v[76:79],a[28:31],a[172:175],v[76:79]
CV v176,v32,v33
CV v177,v34,v35
MM v[76:79],a[24:27],a[168:171],v[76:79]
CV v178,v36,v37
CV v179,v38,v39
v_fmac_f32 v40, v241, v152
MM v[76:79],a[20:23],a[140:143],v[76:79]
v_fmac_f32 v41, v241, v153
v_fmac_f32 v42, v241, v154
v_fmac_f32 v43, v241, v155
MM v[76:79],a[16:19],a[136:139],v[76:79]
v_fmac_f32 v44, v241, v156
v_fmac_f32 v45, v241, v157
MM v[96:99],a[32:35],a[128:131],v[96:99]
v_fmac_f32 v46, v241, v158
v_fmac_f32 v47, v241, v159
MM v[96:99],a[36:39],a[132:135],v[96:99]
buffer_load_dwordx4 v230, s[8:11], s77 offen offset:2048 lds
v_mul_f32 v183, -v250, v245
v_fma_f32 v40, v245, v40, v183
v_fma_f32 v41, v245, v41, v183
MM v[96:99],a[40:43],a[160:163],v[96:99]
v_fma_f32 v42, v245, v42, v183
v_fma_f32 v43, v245, v43, v183
MM v[96:99],a[44:47],a[164:167],v[96:99]
v_fma_f32 v44, v245, v44, v183
v_fma_f32 v45, v245, v45, v183
MM v[104:107],a[44:47],a[172:175],v[104:107]
v_fma_f32 v46, v245, v46, v183
)ASM"
R"ASM(
v_fma_f32 v47, v245, v47, v183
CV v180,v40,v41
MM v[104:107],a[40:43],a[168:171],v[104:107]
CV v181,v42,v43
CV v182,v44,v45
MM v[104:107],a[36:39],a[140:143],v[104:107]
CV v183,v46,v47
v_xor_b32 v151, 256, v253
MM v[104:107],a[32:35],a[136:139],v[104:107]
RD v[144:147],v151 offset:0
RD v[148:151],v151 offset:16
MM v[100:103],a[48:51],a[128:131],v[100:103]
MM v[100:103],a[52:55],a[132:135],v[100:103]
MM v[100:103],a[56:59],a[160:163],v[100:103]
MM v[100:103],a[60:63],a[164:167],v[100:103]
s_waitcnt lgkmcnt(0)
buffer_load_dwordx4 v231, s[8:11], s77 offen offset:3072 lds
v_fmac_f32 v64, v240, v144
v_fmac_f32 v65, v240, v145
v_fmac_f32 v66, v240, v146
v_fmac_f32 v67, v240, v147
v_fmac_f32 v68, v240, v148
v_fmac_f32 v69, v240, v149
v_fmac_f32 v70, v240, v150
v_fmac_f32 v71, v240, v151
v_mul_f32 v195, -v250, v244
v_fma_f32 v64, v244, v64, v195
v_fma_f32 v65, v244, v65, v195
MM v[108:111],a[60:63],a[172:175],v[108:111]
s_add_u32 s77, s64, 16384
s_add_u32 s81, s77, 896
s_add_u32 m0, s65, 81920
v_fma_f32 v66, v244, v66, v195
v_fma_f32 v67, v244, v67, v195
MM v[108:111],a[56:59],a[168:171],v[108:111]
v_fma_f32 v68, v244, v68, v195
v_fma_f32 v69, v244, v69, v195
MM v[108:111],a[52:55],a[140:143],v[108:111]
v_fma_f32 v70, v244, v70, v195
v_fma_f32 v71, v244, v71, v195
CV v192,v64,v65
MM v[108:111],a[48:51],a[136:139],v[108:111]
CV v193,v66,v67
CV v194,v68,v69
MM v[16:19],a[64:67],a[144:147],v[16:19]
CV v195,v70,v71
v_fmac_f32 v72, v241, v144
v_fmac_f32 v73, v241, v145
MM v[16:19],a[68:71],a[148:151],v[16:19]
v_fmac_f32 v74, v241, v146
v_fmac_f32 v75, v241, v147
v_fmac_f32 v76, v241, v148
MM v[16:19],a[72:75],a[176:179],v[16:19]
v_fmac_f32 v77, v241, v149
v_fmac_f32 v78, v241, v150
MM v[16:19],a[76:79],a[180:183],v[16:19]
v_fmac_f32 v79, v241, v151
v_mul_f32 v199, -v250, v245
v_fma_f32 v72, v245, v72, v199
MM v[24:27],a[76:79],a[188:191],v[24:27]
v_fma_f32 v73, v245, v73, v199
v_fma_f32 v74, v245, v74, v199
MM v[24:27],a[72:75],a[184:187],v[24:27]
buffer_load_dwordx4 v235, s[12:15], s81 offen offset:0 lds
v_fma_f32 v75, v245, v75, v199
v_fma_f32 v76, v245, v76, v199
MM v[24:27],a[68:71],a[156:159],v[24:27]
s_waitcnt vmcnt(13)
s_barrier
RD a[192:195],v224 offset:0
v_fma_f32 v77, v245, v77, v199
v_fma_f32 v78, v245, v78, v199
v_fma_f32 v79, v245, v79, v199
MM v[24:27],a[64:67],a[152:155],v[24:27]
RD a[196:199],v225 offset:0
RD a[200:203],v226 offset:0
CV v196,v72,v73
CV v197,v74,v75
MM v[20:23],a[80:83],a[144:147],v[20:23]
RD a[204:207],v227 offset:0
RD a[208:211],v224 offset:4096
CV v198,v76,v77
CV v199,v78,v79
MM v[20:23],a[84:87],a[148:151],v[20:23]
RD a[212:215],v225 offset:4096
RD a[216:219],v226 offset:4096
v_xor_b32 v159, 384, v253
MM v[20:23],a[88:91],a[176:179],v[20:23]
RD a[220:223],v227 offset:4096
RD a[224:227],v224 offset:8192
RD v[152:155],v159 offset:0
RD v[156:159],v159 offset:16
MM v[20:23],a[92:95],a[180:183],v[20:23]
RD a[228:231],v225 offset:8192
RD a[232:235],v226 offset:8192
MM v[28:31],a[92:95],a[188:191],v[28:31]
RD a[236:239],v227 offset:8192
RD a[240:243],v224 offset:12288
MM v[28:31],a[88:91],a[184:187],v[28:31]
RD a[244:247],v225 offset:12288
RD a[248:251],v226 offset:12288
MM v[28:31],a[84:87],a[156:159],v[28:31]
s_waitcnt lgkmcnt(6)
RD a[252:255],v227 offset:12288
v_fmac_f32 v96, v240, v152
v_fmac_f32 v97, v240, v153
v_fmac_f32 v98, v240, v154
v_fmac_f32 v99, v240, v155
v_fmac_f32 v100, v240, v156
v_fmac_f32 v101, v240, v157
v_fmac_f32 v102, v240, v158
v_fmac_f32 v103, v240, v159
v_mul_f32 v211, -v250, v244
v_fma_f32 v96, v244, v96, v211
v_fma_f32 v97, v244, v97, v211
v_fma_f32 v98, v244, v98, v211
MM v[28:31],a[80:83],a[152:155],v[28:31]
buffer_load_dwordx4 v235, s[12:15], s77 offen offset:1024 lds
v_fma_f32 v99, v244, v99, v211
v_fma_f32 v100, v244, v100, v211
MM v[48:51],a[96:99],a[144:147],v[48:51]
v_fma_f32 v101, v244, v101, v211
v_fma_f32 v102, v244, v102, v211
v_fma_f32 v103, v244, v103, v211
MM v[48:51],a[100:103],a[148:151],v[48:51]
CV v208,v96,v97
CV v209,v98,v99
MM v[48:51],a[104:107],a[176:179],v[48:51]
CV v210,v100,v101
CV v211,v102,v103
v_fmac_f32 v104, v241, v152
MM v[48:51],a[108:111],a[180:183],v[48:51]
v_fmac_f32 v105, v241, v153
v_fmac_f32 v106, v241, v154
v_fmac_f32 v107, v241, v155
MM v[56:59],a[108:111],a[188:191],v[56:59]
v_fmac_f32 v108, v241, v156
v_fmac_f32 v109, v241, v157
MM v[56:59],a[104:107],a[184:187],v[56:59]
v_fmac_f32 v110, v241, v158
v_fmac_f32 v111, v241, v159
MM v[56:59],a[100:103],a[156:159],v[56:59]
v_mul_f32 v215, -v250, v245
v_fma_f32 v104, v245, v104, v215
v_fma_f32 v105, v245, v105, v215
MM v[56:59],a[96:99],a[152:155],v[56:59]
v_fma_f32 v106, v245, v106, v215
v_fma_f32 v107, v245, v107, v215
MM v[52:55],a[112:115],a[144:147],v[52:55]
v_fma_f32 v108, v245, v108, v215
v_fma_f32 v109, v245, v109, v215
MM v[52:55],a[116:119],a[148:151],v[52:55]
buffer_load_dwordx4 v235, s[12:15], s81 offen offset:2048 lds
v_fma_f32 v110, v245, v110, v215
v_fma_f32 v111, v245, v111, v215
CV v212,v104,v105
MM v[52:55],a[120:123],a[176:179],v[52:55]
CV v213,v106,v107
CV v214,v108,v109
CV v215,v110,v111
MM v[52:55],a[124:127],a[180:183],v[52:55]
v_mov_b32 v241, 0
v_mov_b32 v244, 0
s_waitcnt lgkmcnt(0)
MM v[0:3],a[192:195],v[160:163],0
v_mov_b32 v144, 0
v_mov_b32 v145, 0
MM v[0:3],a[196:199],v[176:179],v[0:3]
v_mov_b32 v245, 0
v_mov_b32 v148, 0
MM v[0:3],a[200:203],v[192:195],v[0:3]
v_mov_b32 v149, 0
v_fmac_f32 v16, v242, v136
MM v[0:3],a[204:207],v[208:211],v[0:3]
v_fmac_f32 v17, v242, v137
v_fmac_f32 v18, v242, v138
MM v[8:11],a[192:195],v[164:167],0
v_fmac_f32 v19, v242, v139
v_fmac_f32 v20, v242, v140
MM v[8:11],a[196:199],v[180:183],v[8:11]
v_fmac_f32 v21, v242, v141
v_fmac_f32 v22, v242, v142
MM v[8:11],a[200:203],v[196:199],v[8:11]
v_fmac_f32 v23, v242, v143
v_mul_f32 v16, v246, v16
MM v[8:11],a[204:207],v[212:215],v[8:11]
buffer_load_dwordx4 v235, s[12:15], s77 offen offset:3072 lds
MM v[4:7],a[208:211],v[160:163],0
s_add_u32 s77, s64, 32768
s_add_u32 s81, s77, 896
s_add_u32 m0, s65, 98304
MM v[4:7],a[212:215],v[176:179],v[4:7]
v_max3_f32 v146, v0, v1, v2
v_max_f32 v146, v146, v3
MM v[4:7],a[216:219],v[192:195],v[4:7]
v_mov_b32 v147, v146
v_mul_f32 v17, v246, v17
MM v[4:7],a[220:223],v[208:211],v[4:7]
v_permlane16_swap_b32 v146, v147
v_max_f32 v146, v146, v147
MM v[12:15],a[208:211],v[164:167],0
s_waitcnt vmcnt(8)
LPG4_%=:
s_cbranch_execz LPG8_%=
s_barrier
RD a[192:195],v224 offset:16384
MM v[12:15],a[212:215],v[180:183],v[12:15]
RD a[196:199],v225 offset:16384
RD a[200:203],v226 offset:16384
MM v[12:15],a[216:219],v[196:199],v[12:15]
RD a[204:207],v227 offset:16384
v_mov_b32 v147, v146
MM v[12:15],a[220:223],v[212:215],v[12:15]
s_nop 0
v_permlane32_swap_b32 v146, v147
v_max_f32 v146, v146, v147
v_sub_f32 v128, 0xc1600000, v146
v_sub_f32 v129, 0xc1600000, v146
MM v[32:35],a[224:227],v[160:163],0
RD a[208:211],v224 offset:20480
RD a[212:215],v225 offset:20480
v_sub_f32 v130, 0xc1600000, v146
v_sub_f32 v131, 0xc1600000, v146
v_max3_f32 v150, v8, v9, v10
MM v[32:35],a[228:231],v[176:179],v[32:35]
buffer_load_dwordx4 v235, s[12:15], s81 offen offset:0 lds
v_max_f32 v150, v150, v11
v_mov_b32 v151, v150
MM v[32:35],a[232:235],v[192:195],v[32:35]
RD a[216:219],v226 offset:20480
RD a[220:223],v227 offset:20480
v_permlane16_swap_b32 v150, v151
v_max_f32 v150, v150, v151
v_mov_b32 v151, v150
MM v[32:35],a[236:239],v[208:211],v[32:35]
s_nop 0
v_permlane32_swap_b32 v150, v151
v_max_f32 v150, v150, v151
v_add_f32 v0, v0, v128
v_sub_f32 v132, 0xc1600000, v150
MM v[40:43],a[224:227],v[164:167],0
v_sub_f32 v133, 0xc1600000, v150
v_sub_f32 v134, 0xc1600000, v150
v_sub_f32 v135, 0xc1600000, v150
v_add_f32 v1, v1, v128
EX v0,v0
MM v[40:43],a[228:231],v[180:183],v[40:43]
EX v1,v1
MM v[40:43],a[232:235],v[196:199],v[40:43]
v_add_f32 v2, v2, v128
v_add_f32 v3, v3, v128
MM v[40:43],a[236:239],v[212:215],v[40:43]
EX v2,v2
MM v[36:39],a[240:243],v[160:163],v[128:131]
RD a[224:227],v224 offset:24576
RD a[228:231],v225 offset:24576
EX v3,v3
MM v[36:39],a[244:247],v[176:179],v[36:39]
RD a[232:235],v226 offset:24576
RD a[236:239],v227 offset:24576
CV v0,v0,v1
v_add_f32 v4, v4, v128
MM v[36:39],a[248:251],v[192:195],v[36:39]
v_add_f32 v5, v5, v128
v_mul_f32 v18, v246, v18
EX v4,v4
MM v[36:39],a[252:255],v[208:211],v[36:39]
buffer_load_dwordx4 v235, s[12:15], s77 offen offset:1024 lds
EX v5,v5
MM v[44:47],a[240:243],v[164:167],v[132:135]
CV v1,v2,v3
v_add_f32 v6, v6, v128
MM v[44:47],a[244:247],v[180:183],v[44:47]
v_add_f32 v7, v7, v128
v_mul_f32 v19, v246, v19
MM v[44:47],a[248:251],v[196:199],v[44:47]
EX v6,v6
EX v7,v7
MM v[44:47],a[252:255],v[212:215],v[44:47]
CV v2,v4,v5
v_add_f32 v8, v8, v132
MM v[60:63],a[124:127],a[188:191],v[60:63]
RD a[240:243],v224 offset:28672
RD a[244:247],v225 offset:28672
v_add_f32 v9, v9, v132
MM v[60:63],a[120:123],a[184:187],v[60:63]
RD a[248:251],v226 offset:28672
RD a[252:255],v227 offset:28672
EX v8,v8
MM v[60:63],a[116:119],a[156:159],v[60:63]
EX v9,v9
MM v[60:63],a[112:115],a[152:155],v[60:63]
CV v3,v6,v7
v_add_f32 v10, v10, v132
MM v[80:83],a[0:3],a[144:147],v[80:83]
v_add_f32 v11, v11, v132
v_mul_f32 v20, v246, v20
EX v10,v10
MM v[80:83],a[4:7],a[148:151],v[80:83]
buffer_load_dwordx4 v235, s[12:15], s81 offen offset:2048 lds
EX v11,v11
MM v[80:83],a[8:11],a[176:179],v[80:83]
CV v8,v8,v9
v_add_f32 v12, v12, v132
MM v[80:83],a[12:15],a[180:183],v[80:83]
v_add_f32 v13, v13, v132
v_mul_f32 v21, v246, v21
EX v12,v12
MM v[88:91],a[12:15],a[188:191],v[88:91]
EX v13,v13
MM v[88:91],a[8:11],a[184:187],v[88:91]
CV v9,v10,v11
v_add_f32 v14, v14, v132
MM v[88:91],a[4:7],a[156:159],v[88:91]
v_add_f32 v15, v15, v132
v_mul_f32 v22, v246, v22
EX v14,v14
MM v[88:91],a[0:3],a[152:155],v[88:91]
EX v15,v15
MM v[84:87],a[16:19],a[144:147],v[84:87]
CV v10,v12,v13
v_add_f32 v32, v32, v128
MM v[84:87],a[20:23],a[148:151],v[84:87]
v_add_f32 v33, v33, v128
v_mul_f32 v23, v246, v23
MM v[84:87],a[24:27],a[176:179],v[84:87]
EX v32,v32
MM v[84:87],a[28:31],a[180:183],v[84:87]
buffer_load_dwordx4 v235, s[12:15], s77 offen offset:3072 lds
EX v33,v33
MM v[92:95],a[28:31],a[188:191],v[92:95]
s_add_u32 s77, s64, 49152
s_add_u32 m0, s65, 49152
CV v11,v14,v15
v_add_f32 v34, v34, v128
v_add_f32 v35, v35, v128
MM v[92:95],a[24:27],a[184:187],v[92:95]
buffer_load_dwordx4 v228, s[8:11], s77 offen offset:0 lds
EX v34,v34
MM v[92:95],a[20:23],a[156:159],v[92:95]
EX v35,v35
MM v[92:95],a[16:19],a[152:155],v[92:95]
CV v4,v32,v33
CV v168,v16,v17
EX v36,v36
MM v[112:115],a[32:35],a[144:147],v[112:115]
EX v37,v37
MM v[112:115],a[36:39],a[148:151],v[112:115]
buffer_load_dwordx4 v229, s[8:11], s77 offen offset:1024 lds
CV v5,v34,v35
MM v[112:115],a[40:43],a[176:179],v[112:115]
EX v38,v38
EX v39,v39
MM v[112:115],a[44:47],a[180:183],v[112:115]
CV v6,v36,v37
v_add_f32 v40, v40, v132
MM v[120:123],a[44:47],a[188:191],v[120:123]
v_add_f32 v41, v41, v132
CV v169,v18,v19
MM v[120:123],a[40:43],a[184:187],v[120:123]
buffer_load_dwordx4 v230, s[8:11], s77 offen offset:2048 lds
EX v40,v40
MM v[120:123],a[36:39],a[156:159],v[120:123]
EX v41,v41
MM v[120:123],a[32:35],a[152:155],v[120:123]
CV v7,v38,v39
v_add_f32 v42, v42, v132
MM v[116:119],a[48:51],a[144:147],v[116:119]
v_add_f32 v43, v43, v132
CV v170,v20,v21
EX v42,v42
MM v[116:119],a[52:55],a[148:151],v[116:119]
buffer_load_dwordx4 v231, s[8:11], s77 offen offset:3072 lds
EX v43,v43
MM v[116:119],a[56:59],a[176:179],v[116:119]
s_add_u32 s77, s64, 49152
s_add_u32 s81, s77, 896
s_add_u32 m0, s65, 114688
CV v12,v40,v41
EX v44,v44
MM v[116:119],a[60:63],a[180:183],v[116:119]
EX v45,v45
MM v[124:127],a[60:63],a[188:191],v[124:127]
CV v13,v42,v43
CV v171,v22,v23
MM v[124:127],a[56:59],a[184:187],v[124:127]
buffer_load_dwordx4 v235, s[12:15], s81 offen offset:0 lds
EX v46,v46
MM v[124:127],a[52:55],a[156:159],v[124:127]
EX v47,v47
CV v14,v44,v45
CV v15,v46,v47
MM v[124:127],a[48:51],a[152:155],v[124:127]
v_fmac_f32 v24, v243, v136
v_fmac_f32 v25, v243, v137
v_smfmac_f32_16x16x64_bf16 v[144:147], v[236:239], v[0:7], v234
v_fmac_f32 v26, v243, v138
v_smfmac_f32_16x16x64_bf16 v[148:151], v[236:239], v[8:15], v234
s_waitcnt lgkmcnt(0)
)ASM"
R"ASM(
v_fmac_f32 v27, v243, v139
buffer_load_dwordx4 v235, s[12:15], s77 offen offset:1024 lds
buffer_load_dwordx4 v235, s[12:15], s81 offen offset:2048 lds
buffer_load_dwordx4 v235, s[12:15], s77 offen offset:3072 lds
s_add_u32 s64, s64, 49152
MM v[64:67],a[192:195],v[160:163],v[128:131]
s_add_u32 s64, s64, 16384
v_fmac_f32 v28, v243, v140
v_fmac_f32 v29, v243, v141
MM v[64:67],a[196:199],v[176:179],v[64:67]
s_add_u32 s81, s64, 896
TR a[128:129],v232 offset:0
v_fmac_f32 v30, v243, v142
MM v[64:67],a[200:203],v[192:195],v[64:67]
v_fmac_f32 v31, v243, v143
v_mul_f32 v24, v247, v24
MM v[64:67],a[204:207],v[208:211],v[64:67]
v_max3_f32 v249, v144, v145, v148
v_max_f32 v249, v249, v149
v_mul_f32 v25, v247, v25
MM v[68:71],a[220:223],v[208:211],v[128:131]
TR a[130:131],v232 offset:4096
TR a[132:133],v232 offset:8192
v_cmp_lt_f32 vcc, s71, v249
v_mul_f32 v26, v247, v26
v_mul_f32 v27, v247, v27
MM v[68:71],a[216:219],v[192:195],v[68:71]
s_cbranch_vccnz LSITE0_%=
LBACK0_%=:
TR a[134:135],v232 offset:12288
v_add_f32 v244, v144, v145
v_mul_f32 v28, v247, v28
v_mul_f32 v29, v247, v29
v_mul_f32 v30, v247, v30
MM v[68:71],a[212:215],v[176:179],v[68:71]
TR a[136:137],v233 offset:0
v_add_f32 v245, v148, v149
v_mul_f32 v31, v247, v31
CV v172,v24,v25
MM v[68:71],a[208:211],v[160:163],v[68:71]
TR a[138:139],v233 offset:4096
CV v173,v26,v27
CV v174,v28,v29
MM v[76:79],a[208:211],v[164:167],v[132:135]
TR a[140:141],v233 offset:8192
TR a[142:143],v233 offset:12288
CV v175,v30,v31
v_xor_b32 v143, 128, v253
MM v[76:79],a[212:215],v[180:183],v[76:79]
RD v[136:139],v143 offset:0
RD v[140:143],v143 offset:16
TR a[144:145],v232 offset:512
MM v[76:79],a[216:219],v[196:199],v[76:79]
s_waitcnt lgkmcnt(9)
TR a[146:147],v232 offset:4608
TR a[148:149],v232 offset:8704
MM v[76:79],a[220:223],v[212:215],v[76:79]
EX v64,v64
MM v[32:35],a[128:131],v[0:3],0
MM v[36:39],a[128:131],v[8:11],0
MM v[72:75],a[204:207],v[212:215],v[132:135]
TR a[150:151],v232 offset:12800
TR a[152:153],v233 offset:512
MM v[72:75],a[200:203],v[196:199],v[72:75]
s_waitcnt lgkmcnt(5)
TR a[154:155],v233 offset:4608
v_fmac_f32 v48, v242, v136
v_fmac_f32 v49, v242, v137
v_fmac_f32 v50, v242, v138
v_fmac_f32 v51, v242, v139
v_fmac_f32 v52, v242, v140
v_fmac_f32 v53, v242, v141
v_fmac_f32 v54, v242, v142
v_fmac_f32 v55, v242, v143
v_mul_f32 v187, -v252, v246
v_fma_f32 v48, v246, v48, v187
v_fma_f32 v49, v246, v49, v187
EX v65,v65
MM v[72:75],a[196:199],v[180:183],v[72:75]
TR a[156:157],v233 offset:8704
v_fma_f32 v50, v246, v50, v187
v_fma_f32 v51, v246, v51, v187
v_fma_f32 v52, v246, v52, v187
MM v[72:75],a[192:195],v[164:167],v[72:75]
TR a[158:159],v233 offset:12800
v_fma_f32 v53, v246, v53, v187
EX v66,v66
v_fma_f32 v54, v246, v54, v187
MM v[96:99],a[224:227],v[160:163],v[128:131]
TR a[160:161],v232 offset:1024
v_max3_f32 v32, |v32|, |v33|, |v34|
v_max_f32 v32, v32, |v35|
v_fma_f32 v55, v246, v55, v187
CV v184,v48,v49
EX v67,v67
MM v[96:99],a[228:231],v[176:179],v[96:99]
TR a[162:163],v232 offset:5120
TR a[164:165],v232 offset:9216
v_bfe_u32 v32, v32, 23, 8
v_add_u32 v32, 7, v32
CV v185,v50,v51
CV v186,v52,v53
CV v187,v54,v55
v_fmac_f32 v56, v243, v136
MM v[96:99],a[232:235],v[192:195],v[96:99]
TR a[166:167],v232 offset:13312
v_min_u32 v32, 240, v32
v_lshl_or_b32 v241, v32, 0, v241
v_fmac_f32 v57, v243, v137
v_fmac_f32 v58, v243, v138
EX v68,v68
MM v[96:99],a[236:239],v[208:211],v[96:99]
TR a[168:169],v233 offset:1024
v_lshl_or_b32 v32, v32, 1, 1
v_lshlrev_b32 v32, 22, v32
v_fmac_f32 v59, v243, v139
v_fmac_f32 v60, v243, v140
MM v[100:103],a[252:255],v[208:211],v[128:131]
TR a[170:171],v233 offset:5120
AW a100,v32
AW a101,v32
v_fmac_f32 v61, v243, v141
v_fmac_f32 v62, v243, v142
EX v69,v69
v_fmac_f32 v63, v243, v143
MM v[100:103],a[248:251],v[192:195],v[100:103]
TR a[172:173],v233 offset:9216
TR a[174:175],v233 offset:13312
AW a102,v32
AW a103,v32
v_mul_f32 v191, -v252, v247
v_fma_f32 v56, v247, v56, v191
MM v[100:103],a[244:247],v[176:179],v[100:103]
TR a[176:177],v232 offset:1536
v_max3_f32 v36, |v36|, |v37|, |v38|
v_max_f32 v36, v36, |v39|
v_fma_f32 v57, v247, v57, v191
EX v70,v70
v_fma_f32 v58, v247, v58, v191
MM v[100:103],a[240:243],v[160:163],v[100:103]
TR a[178:179],v232 offset:5632
v_bfe_u32 v36, v36, 23, 8
v_add_u32 v36, 7, v36
v_fma_f32 v59, v247, v59, v191
v_fma_f32 v60, v247, v60, v191
EX v71,v71
v_fma_f32 v61, v247, v61, v191
MM v[108:111],a[240:243],v[164:167],v[132:135]
TR a[180:181],v232 offset:9728
v_min_u32 v36, 240, v36
v_lshl_or_b32 v241, v36, 8, v241
v_fma_f32 v62, v247, v62, v191
v_fma_f32 v63, v247, v63, v191
CV v64,v64,v65
MM v[108:111],a[244:247],v[180:183],v[108:111]
TR a[182:183],v232 offset:13824
v_lshl_or_b32 v36, v36, 1, 1
v_lshlrev_b32 v36, 22, v36
CV v188,v56,v57
CV v189,v58,v59
EX v72,v72
CV v190,v60,v61
MM v[108:111],a[248:251],v[196:199],v[108:111]
TR a[184:185],v233 offset:1536
TR a[186:187],v233 offset:5632
TR a[188:189],v233 offset:9728
TR a[190:191],v233 offset:13824
AW a108,v36
AW a109,v36
CV v191,v62,v63
v_xor_b32 v143, 256, v253
EX v73,v73
MM v[108:111],a[252:255],v[212:215],v[108:111]
RD v[136:139],v143 offset:0
RD v[140:143],v143 offset:16
AW a110,v36
AW a111,v36
CV v65,v66,v67
MM v[104:107],a[236:239],v[212:215],v[132:135]
EX v74,v74
MM v[104:107],a[232:235],v[196:199],v[104:107]
EX v75,v75
MM v[104:107],a[228:231],v[180:183],v[104:107]
EX v76,v76
CV v66,v68,v69
MM v[104:107],a[224:227],v[164:167],v[104:107]
s_waitcnt vmcnt(12) lgkmcnt(0)
s_barrier
v_fmac_f32 v80, v242, v136
v_fmac_f32 v81, v242, v137
v_fmac_f32 v82, v242, v138
v_fmac_f32 v83, v242, v139
v_fmac_f32 v84, v242, v140
v_fmac_f32 v85, v242, v141
v_fmac_f32 v86, v242, v142
v_fmac_f32 v87, v242, v143
v_mul_f32 v203, -v252, v246
v_fma_f32 v80, v246, v80, v203
MM a[0:3],a[128:131],v[0:3],a[100:103]
s_add_u32 m0, s65, 0
v_fma_f32 v81, v246, v81, v203
v_fma_f32 v82, v246, v82, v203
v_fma_f32 v83, v246, v83, v203
MM a[0:3],a[132:135],v[4:7],a[0:3]
v_fma_f32 v84, v246, v84, v203
v_fma_f32 v85, v246, v85, v203
EX v77,v77
CV v67,v70,v71
MM a[8:11],a[132:135],v[12:15],a[108:111]
v_fma_f32 v86, v246, v86, v203
v_fma_f32 v87, v246, v87, v203
MM a[8:11],a[128:131],v[8:11],a[8:11]
CV v200,v80,v81
CV v201,v82,v83
EX v78,v78
CV v202,v84,v85
MM a[12:15],a[136:139],v[8:11],a[108:111]
CV v203,v86,v87
v_fmac_f32 v88, v243, v136
v_fmac_f32 v89, v243, v137
EX v79,v79
MM a[12:15],a[140:143],v[12:15],a[12:15]
v_fmac_f32 v90, v243, v138
v_fmac_f32 v91, v243, v139
CV v72,v72,v73
MM a[4:7],a[140:143],v[4:7],a[100:103]
v_fmac_f32 v92, v243, v140
v_fmac_f32 v93, v243, v141
EX v96,v96
v_fmac_f32 v94, v243, v142
MM a[4:7],a[136:139],v[0:3],a[4:7]
v_fmac_f32 v95, v243, v143
v_mul_f32 v207, -v252, v247
MM a[32:35],a[144:147],v[0:3],a[100:103]
RD a[192:195],v224 offset:32768
v_fma_f32 v88, v247, v88, v207
v_fma_f32 v89, v247, v89, v207
EX v97,v97
CV v73,v74,v75
v_fma_f32 v90, v247, v90, v207
MM a[32:35],a[148:151],v[4:7],a[32:35]
buffer_load_dwordx4 v228, s[8:11], s64 offen offset:0 lds
v_fma_f32 v91, v247, v91, v207
v_fma_f32 v92, v247, v92, v207
EX v98,v98
MM a[40:43],a[148:151],v[12:15],a[108:111]
RD a[196:199],v225 offset:32768
v_fma_f32 v93, v247, v93, v207
v_fma_f32 v94, v247, v94, v207
MM a[40:43],a[144:147],v[8:11],a[40:43]
RD a[200:203],v226 offset:32768
v_fma_f32 v95, v247, v95, v207
CV v204,v88,v89
EX v99,v99
CV v74,v76,v77
CV v205,v90,v91
MM a[44:47],a[152:155],v[8:11],a[108:111]
CV v206,v92,v93
CV v207,v94,v95
MM a[44:47],a[156:159],v[12:15],a[44:47]
RD a[204:207],v227 offset:32768
buffer_load_dwordx4 v229, s[8:11], s64 offen offset:1024 lds
v_xor_b32 v143, 384, v253
EX v100,v100
MM a[36:39],a[156:159],v[4:7],a[100:103]
RD v[136:139],v143 offset:0
RD v[140:143],v143 offset:16
EX v101,v101
CV v75,v78,v79
MM a[36:39],a[152:155],v[0:3],a[36:39]
RD a[208:211],v224 offset:36864
RD a[212:215],v225 offset:36864
MM a[64:67],a[160:163],v[0:3],a[100:103]
EX v102,v102
MM a[64:67],a[164:167],v[4:7],a[64:67]
buffer_load_dwordx4 v230, s[8:11], s64 offen offset:2048 lds
MM a[72:75],a[164:167],v[12:15],a[108:111]
s_waitcnt lgkmcnt(2)
v_fmac_f32 v112, v242, v136
v_fmac_f32 v113, v242, v137
v_fmac_f32 v114, v242, v138
v_fmac_f32 v115, v242, v139
v_fmac_f32 v116, v242, v140
v_fmac_f32 v117, v242, v141
v_fmac_f32 v118, v242, v142
v_fmac_f32 v119, v242, v143
v_mul_f32 v219, -v252, v246
v_fma_f32 v112, v246, v112, v219
v_fma_f32 v113, v246, v113, v219
EX v103,v103
CV v68,v96,v97
v_fma_f32 v114, v246, v114, v219
MM a[72:75],a[160:163],v[8:11],a[72:75]
RD a[216:219],v226 offset:36864
v_fma_f32 v115, v246, v115, v219
v_fma_f32 v116, v246, v116, v219
EX v104,v104
MM a[76:79],a[168:171],v[8:11],a[108:111]
buffer_load_dwordx4 v231, s[8:11], s64 offen offset:3072 lds
RD a[220:223],v227 offset:36864
v_fma_f32 v117, v246, v117, v219
v_fma_f32 v118, v246, v118, v219
MM a[76:79],a[172:175],v[12:15],a[76:79]
s_add_u32 m0, s65, 65536
v_fma_f32 v119, v246, v119, v219
CV v216,v112,v113
EX v105,v105
CV v217,v114,v115
CV v69,v98,v99
MM a[68:71],a[172:175],v[4:7],a[100:103]
RD a[224:227],v224 offset:40960
CV v218,v116,v117
CV v219,v118,v119
v_fmac_f32 v120, v243, v136
MM a[68:71],a[168:171],v[0:3],a[68:71]
v_fmac_f32 v121, v243, v137
v_fmac_f32 v122, v243, v138
EX v106,v106
v_fmac_f32 v123, v243, v139
MM a[96:99],a[176:179],v[0:3],a[100:103]
RD a[228:231],v225 offset:40960
buffer_load_dwordx4 v235, s[12:15], s81 offen offset:0 lds
v_fmac_f32 v124, v243, v140
v_fmac_f32 v125, v243, v141
EX v107,v107
CV v70,v100,v101
MM a[96:99],a[180:183],v[4:7],a[96:99]
v_fmac_f32 v126, v243, v142
v_fmac_f32 v127, v243, v143
MM a[104:107],a[180:183],v[12:15],a[108:111]
RD a[232:235],v226 offset:40960
v_mul_f32 v223, -v252, v247
v_fma_f32 v120, v247, v120, v223
EX v108,v108
v_fma_f32 v121, v247, v121, v223
MM a[104:107],a[176:179],v[8:11],a[104:107]
v_fma_f32 v122, v247, v122, v223
v_fma_f32 v123, v247, v123, v223
CV v71,v102,v103
MM a[108:111],a[184:187],v[8:11],a[108:111]
RD a[236:239],v227 offset:40960
buffer_load_dwordx4 v235, s[12:15], s64 offen offset:1024 lds
v_fma_f32 v124, v247, v124, v223
v_fma_f32 v125, v247, v125, v223
EX v109,v109
MM a[108:111],a[188:191],v[12:15],a[108:111]
v_fma_f32 v126, v247, v126, v223
v_fma_f32 v127, v247, v127, v223
EX v110,v110
CV v220,v120,v121
MM a[100:103],a[188:191],v[4:7],a[100:103]
RD a[240:243],v224 offset:45056
RD a[244:247],v225 offset:45056
RD a[248:251],v226 offset:45056
RD a[252:255],v227 offset:45056
CV v221,v122,v123
EX v111,v111
CV v222,v124,v125
CV v223,v126,v127
MM a[100:103],a[184:187],v[0:3],a[100:103]
buffer_load_dwordx4 v235, s[12:15], s81 offen offset:2048 lds
CV v76,v104,v105
v_smfmac_f32_16x16x64_bf16 v[144:147], v[236:239], v[64:71], v234
CV v77,v106,v107
CV v78,v108,v109
CV v79,v110,v111
s_nop 1
v_smfmac_f32_16x16x64_bf16 v[148:151], v[236:239], v[72:79], v234
s_waitcnt lgkmcnt(0)
buffer_load_dwordx4 v235, s[12:15], s64 offen offset:3072 lds
MM v[0:3],a[192:195],v[160:163],v[128:131]
s_add_u32 s64, s64, 16384
TR a[128:129],v232 offset:16384
TR a[130:131],v232 offset:20480
MM v[0:3],a[196:199],v[176:179],v[0:3]
s_add_u32 s81, s64, 896
TR a[132:133],v232 offset:24576
TR a[134:135],v232 offset:28672
MM v[0:3],a[200:203],v[192:195],v[0:3]
TR a[136:137],v233 offset:16384
TR a[138:139],v233 offset:20480
MM v[0:3],a[204:207],v[208:211],v[0:3]
v_max3_f32 v249, v144, v145, v148
v_max_f32 v249, v249, v149
MM v[4:7],a[220:223],v[208:211],v[128:131]
TR a[140:141],v233 offset:24576
v_cmp_lt_f32 vcc, s71, v249
MM v[4:7],a[216:219],v[192:195],v[4:7]
s_cbranch_vccnz LSITE1_%=
LBACK1_%=:
TR a[142:143],v233 offset:28672
v_add_f32 v244, v144, v145
MM v[4:7],a[212:215],v[176:179],v[4:7]
TR a[144:145],v232 offset:16896
v_add_f32 v245, v148, v149
MM v[4:7],a[208:211],v[160:163],v[4:7]
TR a[146:147],v232 offset:20992
TR a[148:149],v232 offset:25088
MM v[12:15],a[208:211],v[164:167],v[132:135]
EX v0,v0
MM v[12:15],a[212:215],v[180:183],v[12:15]
EX v1,v1
MM v[12:15],a[216:219],v[196:199],v[12:15]
TR a[150:151],v232 offset:29184
CV v0,v0,v1
MM v[12:15],a[220:223],v[212:215],v[12:15]
TR a[152:153],v233 offset:16896
TR a[154:155],v233 offset:20992
MM v[8:11],a[204:207],v[212:215],v[132:135]
)ASM"
R"ASM(
EX v2,v2
MM v[8:11],a[200:203],v[196:199],v[8:11]
EX v3,v3
MM v[8:11],a[196:199],v[180:183],v[8:11]
TR a[156:157],v233 offset:25088
CV v1,v2,v3
MM v[8:11],a[192:195],v[164:167],v[8:11]
TR a[158:159],v233 offset:29184
s_nop 0
LPG5_%=:
s_cbranch_execz LPG9_%=
TR a[160:161],v232 offset:17408
MM v[32:35],a[224:227],v[160:163],v[128:131]
EX v4,v4
MM v[32:35],a[228:231],v[176:179],v[32:35]
TR a[162:163],v232 offset:21504
TR a[164:165],v232 offset:25600
MM v[32:35],a[232:235],v[192:195],v[32:35]
EX v5,v5
MM v[32:35],a[236:239],v[208:211],v[32:35]
TR a[166:167],v232 offset:29696
CV v2,v4,v5
MM v[36:39],a[252:255],v[208:211],v[128:131]
EX v6,v6
MM v[36:39],a[248:251],v[192:195],v[36:39]
TR a[168:169],v233 offset:17408
TR a[170:171],v233 offset:21504
MM v[36:39],a[244:247],v[176:179],v[36:39]
EX v7,v7
MM v[36:39],a[240:243],v[160:163],v[36:39]
TR a[172:173],v233 offset:25600
CV v3,v6,v7
MM v[44:47],a[240:243],v[164:167],v[132:135]
EX v8,v8
MM v[44:47],a[244:247],v[180:183],v[44:47]
TR a[174:175],v233 offset:29696
TR a[176:177],v232 offset:17920
MM v[44:47],a[248:251],v[196:199],v[44:47]
EX v9,v9
MM v[44:47],a[252:255],v[212:215],v[44:47]
TR a[178:179],v232 offset:22016
TR a[180:181],v232 offset:26112
MM v[40:43],a[236:239],v[212:215],v[132:135]
EX v10,v10
MM v[40:43],a[232:235],v[196:199],v[40:43]
TR a[182:183],v232 offset:30208
CV v8,v8,v9
MM v[40:43],a[228:231],v[180:183],v[40:43]
EX v11,v11
MM v[40:43],a[224:227],v[164:167],v[40:43]
TR a[184:185],v233 offset:17920
TR a[186:187],v233 offset:22016
MM v[16:19],a[192:195],v[168:171],0
EX v12,v12
MM v[16:19],a[196:199],v[184:187],v[16:19]
TR a[188:189],v233 offset:26112
TR a[190:191],v233 offset:30208
MM v[16:19],a[200:203],v[200:203],v[16:19]
EX v13,v13
MM v[16:19],a[204:207],v[216:219],v[16:19]
EX v14,v14
MM v[24:27],a[192:195],v[172:175],0
EX v15,v15
MM v[24:27],a[196:199],v[188:191],v[24:27]
EX v32,v32
MM v[24:27],a[200:203],v[204:207],v[24:27]
EX v33,v33
MM v[24:27],a[204:207],v[220:223],v[24:27]
s_waitcnt vmcnt(12) lgkmcnt(0)
s_barrier
CV v9,v10,v11
MM a[0:3],a[128:131],v[64:67],a[0:3]
s_add_u32 m0, s65, 16384
CV v10,v12,v13
CV v11,v14,v15
MM a[0:3],a[132:135],v[68:71],a[0:3]
RD a[192:195],v224 offset:49152
CV v4,v32,v33
MM a[8:11],a[132:135],v[76:79],a[8:11]
v_max3_f32 v154, v16, v17, v18
v_max_f32 v154, v154, v19
MM a[8:11],a[128:131],v[72:75],a[8:11]
buffer_load_dwordx4 v228, s[8:11], s64 offen offset:0 lds
MM a[12:15],a[136:139],v[72:75],a[12:15]
EX v34,v34
MM a[12:15],a[140:143],v[76:79],a[12:15]
RD a[196:199],v225 offset:49152
v_mov_b32 v155, v154
MM a[4:7],a[140:143],v[68:71],a[4:7]
s_nop 0
v_permlane16_swap_b32 v154, v155
v_max_f32 v154, v154, v155
MM a[4:7],a[136:139],v[64:67],a[4:7]
EX v35,v35
MM a[32:35],a[144:147],v[64:67],a[32:35]
RD a[200:203],v226 offset:49152
v_mov_b32 v155, v154
MM a[32:35],a[148:151],v[68:71],a[32:35]
s_nop 0
v_permlane32_swap_b32 v154, v155
v_max_f32 v154, v154, v155
MM a[40:43],a[148:151],v[76:79],a[40:43]
EX v36,v36
MM a[40:43],a[144:147],v[72:75],a[40:43]
RD a[204:207],v227 offset:49152
v_sub_f32 v136, 0xc1600000, v154
MM a[44:47],a[152:155],v[72:75],a[44:47]
EX v37,v37
MM a[44:47],a[156:159],v[76:79],a[44:47]
buffer_load_dwordx4 v229, s[8:11], s64 offen offset:1024 lds
v_sub_f32 v137, 0xc1600000, v154
v_sub_f32 v138, 0xc1600000, v154
MM a[36:39],a[156:159],v[68:71],a[36:39]
EX v38,v38
MM a[36:39],a[152:155],v[64:67],a[36:39]
RD a[208:211],v224 offset:53248
v_sub_f32 v139, 0xc1600000, v154
v_mov_b32 v246, 0
MM a[64:67],a[160:163],v[64:67],a[64:67]
EX v39,v39
v_mov_b32 v152, 0
MM a[64:67],a[164:167],v[68:71],a[64:67]
buffer_load_dwordx4 v230, s[8:11], s64 offen offset:2048 lds
RD a[212:215],v225 offset:53248
v_mov_b32 v153, 0
MM a[72:75],a[164:167],v[76:79],a[72:75]
EX v40,v40
v_max3_f32 v158, v24, v25, v26
MM a[72:75],a[160:163],v[72:75],a[72:75]
RD a[216:219],v226 offset:53248
v_max_f32 v158, v158, v27
CV v5,v34,v35
MM a[76:79],a[168:171],v[72:75],a[76:79]
buffer_load_dwordx4 v231, s[8:11], s64 offen offset:3072 lds
RD a[220:223],v227 offset:53248
EX v41,v41
v_mov_b32 v159, v158
MM a[76:79],a[172:175],v[76:79],a[76:79]
s_add_u32 m0, s65, 81920
v_permlane16_swap_b32 v158, v159
v_max_f32 v158, v158, v159
MM a[68:71],a[172:175],v[68:71],a[68:71]
RD a[224:227],v224 offset:57344
EX v42,v42
v_mov_b32 v159, v158
MM a[68:71],a[168:171],v[64:67],a[68:71]
s_nop 0
v_permlane32_swap_b32 v158, v159
CV v6,v36,v37
EX v43,v43
MM a[96:99],a[176:179],v[64:67],a[96:99]
RD a[228:231],v225 offset:57344
buffer_load_dwordx4 v235, s[12:15], s81 offen offset:0 lds
v_max_f32 v158, v158, v159
MM a[96:99],a[180:183],v[68:71],a[96:99]
v_sub_f32 v140, 0xc1600000, v158
CV v7,v38,v39
EX v44,v44
MM a[104:107],a[180:183],v[76:79],a[104:107]
RD a[232:235],v226 offset:57344
v_sub_f32 v141, 0xc1600000, v158
MM a[104:107],a[176:179],v[72:75],a[104:107]
v_sub_f32 v142, 0xc1600000, v158
v_sub_f32 v143, 0xc1600000, v158
EX v45,v45
MM a[108:111],a[184:187],v[72:75],a[108:111]
RD a[236:239],v227 offset:57344
buffer_load_dwordx4 v235, s[12:15], s64 offen offset:1024 lds
v_mov_b32 v247, 0
v_mov_b32 v156, 0
v_mov_b32 v157, 0
MM a[108:111],a[188:191],v[76:79],a[108:111]
EX v46,v46
MM a[100:103],a[188:191],v[68:71],a[100:103]
RD a[240:243],v224 offset:61440
RD a[244:247],v225 offset:61440
RD a[248:251],v226 offset:61440
RD a[252:255],v227 offset:61440
EX v47,v47
MM a[100:103],a[184:187],v[64:67],a[100:103]
buffer_load_dwordx4 v235, s[12:15], s81 offen offset:2048 lds
CV v12,v40,v41
v_smfmac_f32_16x16x64_bf16 v[144:147], v[236:239], v[0:7], v234
CV v13,v42,v43
CV v14,v44,v45
CV v15,v46,v47
s_nop 1
v_smfmac_f32_16x16x64_bf16 v[148:151], v[236:239], v[8:15], v234
s_waitcnt lgkmcnt(0)
buffer_load_dwordx4 v235, s[12:15], s64 offen offset:3072 lds
MM v[64:67],a[192:195],v[160:163],v[128:131]
s_add_u32 s64, s64, 16384
TR a[128:129],v232 offset:32768
s_add_u32 s81, s64, 896
MM v[64:67],a[196:199],v[176:179],v[64:67]
TR a[130:131],v232 offset:36864
TR a[132:133],v232 offset:40960
MM v[64:67],a[200:203],v[192:195],v[64:67]
TR a[134:135],v232 offset:45056
TR a[136:137],v233 offset:32768
MM v[64:67],a[204:207],v[208:211],v[64:67]
v_max3_f32 v249, v144, v145, v148
v_max_f32 v249, v249, v149
MM v[68:71],a[220:223],v[208:211],v[128:131]
TR a[138:139],v233 offset:36864
v_cmp_lt_f32 vcc, s71, v249
MM v[68:71],a[216:219],v[192:195],v[68:71]
s_cbranch_vccnz LSITE2_%=
LBACK2_%=:
TR a[140:141],v233 offset:40960
v_add_f32 v244, v144, v145
MM v[68:71],a[212:215],v[176:179],v[68:71]
TR a[142:143],v233 offset:45056
v_add_f32 v245, v148, v149
MM v[68:71],a[208:211],v[160:163],v[68:71]
TR a[144:145],v232 offset:33280
TR a[146:147],v232 offset:37376
MM v[76:79],a[208:211],v[164:167],v[132:135]
s_lshl_b32 s90, %[swave], 10
s_add_u32 s90, s90, 81920
TR a[148:149],v232 offset:41472
MM v[76:79],a[212:215],v[180:183],v[76:79]
buffer_load_dwordx4 v[56:59], v248, s[20:23], s90 offen offset:0 sc1
MM v[76:79],a[216:219],v[196:199],v[76:79]
EX v64,v64
MM v[76:79],a[220:223],v[212:215],v[76:79]
EX v65,v65
MM v[72:75],a[204:207],v[212:215],v[132:135]
EX v66,v66
MM v[72:75],a[200:203],v[196:199],v[72:75]
EX v67,v67
MM v[72:75],a[196:199],v[180:183],v[72:75]
CV v64,v64,v65
CV v65,v66,v67
MM v[72:75],a[192:195],v[164:167],v[72:75]
EX v68,v68
MM v[80:83],a[192:195],v[168:171],v[136:139]
EX v69,v69
MM v[80:83],a[196:199],v[184:187],v[80:83]
EX v70,v70
MM v[80:83],a[200:203],v[200:203],v[80:83]
EX v71,v71
MM v[80:83],a[204:207],v[216:219],v[80:83]
CV v66,v68,v69
CV v67,v70,v71
MM v[84:87],a[220:223],v[216:219],v[136:139]
EX v72,v72
MM v[84:87],a[216:219],v[200:203],v[84:87]
EX v73,v73
MM v[84:87],a[212:215],v[184:187],v[84:87]
TR a[150:151],v232 offset:45568
TR a[152:153],v233 offset:33280
MM v[84:87],a[208:211],v[168:171],v[84:87]
EX v74,v74
MM v[92:95],a[208:211],v[172:175],v[140:143]
EX v75,v75
MM v[92:95],a[212:215],v[188:191],v[92:95]
TR a[154:155],v233 offset:37376
CV v72,v72,v73
MM v[92:95],a[216:219],v[204:207],v[92:95]
EX v76,v76
MM v[92:95],a[220:223],v[220:223],v[92:95]
EX v77,v77
MM v[88:91],a[204:207],v[220:223],v[140:143]
TR a[156:157],v233 offset:41472
CV v73,v74,v75
MM v[88:91],a[200:203],v[204:207],v[88:91]
EX v78,v78
MM v[88:91],a[196:199],v[188:191],v[88:91]
EX v79,v79
MM v[88:91],a[192:195],v[172:175],v[88:91]
TR a[158:159],v233 offset:45568
CV v74,v76,v77
MM v[96:99],a[224:227],v[160:163],v[128:131]
EX v80,v80
MM v[96:99],a[228:231],v[176:179],v[96:99]
EX v81,v81
MM v[96:99],a[232:235],v[192:195],v[96:99]
TR a[160:161],v232 offset:33792
TR a[162:163],v232 offset:37888
MM v[96:99],a[236:239],v[208:211],v[96:99]
EX v82,v82
MM v[100:103],a[252:255],v[208:211],v[128:131]
EX v83,v83
MM v[100:103],a[248:251],v[192:195],v[100:103]
TR a[164:165],v232 offset:41984
CV v75,v78,v79
MM v[100:103],a[244:247],v[176:179],v[100:103]
EX v84,v84
MM v[100:103],a[240:243],v[160:163],v[100:103]
EX v85,v85
MM v[108:111],a[240:243],v[164:167],v[132:135]
TR a[166:167],v232 offset:46080
CV v80,v80,v81
MM v[108:111],a[244:247],v[180:183],v[108:111]
EX v86,v86
MM v[108:111],a[248:251],v[196:199],v[108:111]
EX v87,v87
MM v[108:111],a[252:255],v[212:215],v[108:111]
TR a[168:169],v233 offset:33792
TR a[170:171],v233 offset:37888
MM v[104:107],a[236:239],v[212:215],v[132:135]
EX v88,v88
MM v[104:107],a[232:235],v[196:199],v[104:107]
EX v89,v89
MM v[104:107],a[228:231],v[180:183],v[104:107]
TR a[172:173],v233 offset:41984
CV v81,v82,v83
EX v90,v90
MM v[104:107],a[224:227],v[164:167],v[104:107]
TR a[174:175],v233 offset:46080
CV v82,v84,v85
MM v[112:115],a[224:227],v[168:171],v[136:139]
EX v91,v91
MM v[112:115],a[228:231],v[184:187],v[112:115]
EX v92,v92
MM v[112:115],a[232:235],v[200:203],v[112:115]
TR a[176:177],v232 offset:34304
EX v93,v93
MM v[112:115],a[236:239],v[216:219],v[112:115]
TR a[178:179],v232 offset:38400
CV v83,v86,v87
MM v[116:119],a[252:255],v[216:219],v[136:139]
EX v94,v94
MM v[116:119],a[248:251],v[200:203],v[116:119]
EX v95,v95
MM v[116:119],a[244:247],v[184:187],v[116:119]
TR a[180:181],v232 offset:42496
CV v88,v88,v89
MM v[116:119],a[240:243],v[168:171],v[116:119]
s_memrealtime s[56:57]
EX v96,v96
MM v[124:127],a[240:243],v[172:175],v[140:143]
s_memtime s[58:59]
TR a[182:183],v232 offset:46592
EX v97,v97
CV v89,v90,v91
MM v[124:127],a[244:247],v[188:191],v[124:127]
EX v98,v98
MM v[124:127],a[248:251],v[204:207],v[124:127]
TR a[184:185],v233 offset:34304
TR a[186:187],v233 offset:38400
TR a[188:189],v233 offset:42496
TR a[190:191],v233 offset:46592
MM v[124:127],a[252:255],v[220:223],v[124:127]
EX v99,v99
CV v90,v92,v93
MM v[120:123],a[236:239],v[220:223],v[140:143]
EX v100,v100
MM v[120:123],a[232:235],v[204:207],v[120:123]
EX v101,v101
CV v91,v94,v95
MM v[120:123],a[228:231],v[188:191],v[120:123]
EX v102,v102
MM v[120:123],a[224:227],v[172:175],v[120:123]
s_waitcnt vmcnt(13) lgkmcnt(0)
s_barrier
CV v68,v96,v97
MM a[0:3],a[128:131],v[0:3],a[0:3]
s_sub_u32 s52, s56, s52
s_sub_u32 s54, s58, s54
v_cvt_f32_u32 v250, s52
EX v103,v103
MM a[0:3],a[132:135],v[4:7],a[0:3]
s_add_u32 m0, s65, 32768
v_cvt_f32_u32 v251, s54
CV v69,v98,v99
EX v104,v104
MM a[8:11],a[132:135],v[12:15],a[8:11]
v_rcp_f32 v251, v251
MM a[8:11],a[128:131],v[8:11],a[8:11]
v_mul_f32 v250, 0x47abe000, v250
s_lshl_b32 s52, %[swave], 11
EX v105,v105
MM a[12:15],a[136:139],v[8:11],a[12:15]
v_mul_f32 v250, v250, v251
s_lshl_b32 s54, %[sb], 3
EX v106,v106
MM a[12:15],a[140:143],v[12:15],a[12:15]
v_cvt_u32_f32 v250, v250
s_add_u32 s52, s52, s54
EX v107,v107
MM a[4:7],a[140:143],v[4:7],a[4:7]
s_waitcnt vmcnt(0)
v_add_u32 v250, s56, v250
v_mov_b32 v251, s69
CV v70,v100,v101
MM a[4:7],a[136:139],v[0:3],a[4:7]
s_add_u32 s52, s52, 86016
s_mov_b64 exec, 1
buffer_store_dwordx2 v[250:251], off, s[20:23], s52 sc1
s_mov_b64 exec, -1
s_nop 0
EX v108,v108
MM a[32:35],a[144:147],v[0:3],a[32:35]
RD a[192:195],v224 offset:0
v_xor_b32 v56, s69, v56
v_xor_b32 v57, s69, v57
EX v109,v109
CV v71,v102,v103
MM a[32:35],a[148:151],v[4:7],a[32:35]
buffer_load_dwordx4 v228, s[8:11], s64 offen offset:0 lds
v_xor_b32 v58, s69, v58
v_xor_b32 v59, s69, v59
MM a[40:43],a[148:151],v[12:15],a[40:43]
RD a[196:199],v225 offset:0
v_or3_b32 v252, v56, v57, v58
)ASM"
R"ASM(
v_or_b32 v252, v252, v59
EX v110,v110
MM a[40:43],a[144:147],v[8:11],a[40:43]
v_cmp_ne_u32 vcc, 0, v252
s_cbranch_vccnz LRFR_%=
LRFRb_%=:
EX v111,v111
CV v76,v104,v105
MM a[44:47],a[152:155],v[8:11],a[44:47]
s_lshl_b32 s90, %[swave], 11
s_add_u32 s78, s90, %[slds]
s_add_u32 s78, s78, 155648
RD a[200:203],v226 offset:0
EX v112,v112
MM a[44:47],a[156:159],v[12:15],a[44:47]
s_add_u32 s90, s90, 53248
buffer_load_dwordx4 v229, s[8:11], s64 offen offset:1024 lds
MM a[36:39],a[156:159],v[4:7],a[36:39]
RD a[204:207],v227 offset:0
EX v113,v113
CV v77,v106,v107
MM a[36:39],a[152:155],v[0:3],a[36:39]
EX v114,v114
MM a[64:67],a[160:163],v[0:3],a[64:67]
RD a[208:211],v224 offset:4096
EX v115,v115
CV v78,v108,v109
MM a[64:67],a[164:167],v[4:7],a[64:67]
buffer_load_dwordx4 v230, s[8:11], s64 offen offset:2048 lds
MM a[72:75],a[164:167],v[12:15],a[72:75]
RD a[212:215],v225 offset:4096
EX v116,v116
MM a[72:75],a[160:163],v[8:11],a[72:75]
EX v117,v117
CV v79,v110,v111
MM a[76:79],a[168:171],v[8:11],a[76:79]
RD a[216:219],v226 offset:4096
CV v84,v112,v113
EX v118,v118
MM a[76:79],a[172:175],v[12:15],a[76:79]
buffer_load_dwordx4 v231, s[8:11], s64 offen offset:3072 lds
MM a[68:71],a[172:175],v[4:7],a[68:71]
s_add_u32 m0, s65, 98304
RD a[220:223],v227 offset:4096
EX v119,v119
MM a[68:71],a[168:171],v[0:3],a[68:71]
EX v120,v120
MM a[96:99],a[176:179],v[0:3],a[96:99]
RD a[224:227],v224 offset:8192
CV v85,v114,v115
EX v121,v121
MM a[96:99],a[180:183],v[4:7],a[96:99]
RD a[228:231],v225 offset:8192
CV v86,v116,v117
MM a[104:107],a[180:183],v[12:15],a[104:107]
buffer_load_dwordx4 v235, s[12:15], s81 offen offset:0 lds
EX v122,v122
MM a[104:107],a[176:179],v[8:11],a[104:107]
EX v123,v123
MM a[108:111],a[184:187],v[8:11],a[108:111]
RD a[232:235],v226 offset:8192
CV v87,v118,v119
MM a[108:111],a[188:191],v[12:15],a[108:111]
EX v124,v124
MM a[100:103],a[188:191],v[4:7],a[100:103]
RD a[236:239],v227 offset:8192
buffer_load_dwordx4 v235, s[12:15], s64 offen offset:1024 lds
EX v125,v125
MM a[100:103],a[184:187],v[0:3],a[100:103]
EX v126,v126
v_smfmac_f32_16x16x64_bf16 v[144:147], v[236:239], v[64:71], v234
RD a[240:243],v224 offset:12288
RD a[244:247],v225 offset:12288
RD a[248:251],v226 offset:12288
RD a[252:255],v227 offset:12288
EX v127,v127
v_smfmac_f32_16x16x64_bf16 v[148:151], v[236:239], v[72:79], v234
CV v92,v120,v121
v_smfmac_f32_16x16x64_bf16 v[152:155], v[236:239], v[80:87], v234
buffer_load_dwordx4 v235, s[12:15], s81 offen offset:2048 lds
CV v93,v122,v123
CV v94,v124,v125
CV v95,v126,v127
s_nop 1
v_smfmac_f32_16x16x64_bf16 v[156:159], v[236:239], v[88:95], v234
s_waitcnt lgkmcnt(0)
buffer_load_dwordx4 v235, s[12:15], s64 offen offset:3072 lds
MM v[0:3],a[192:195],v[160:163],v[128:131]
s_add_u32 s64, s64, 16384
TR a[128:129],v232 offset:49152
s_add_u32 s81, s64, 896
MM v[0:3],a[196:199],v[176:179],v[0:3]
TR a[130:131],v232 offset:53248
v_max3_f32 v249, v144, v145, v148
MM v[0:3],a[200:203],v[192:195],v[0:3]
TR a[132:133],v232 offset:57344
v_max3_f32 v249, v249, v149, v152
MM v[0:3],a[204:207],v[208:211],v[0:3]
TR a[134:135],v232 offset:61440
v_max3_f32 v249, v249, v153, v156
MM v[4:7],a[220:223],v[208:211],v[128:131]
TR a[136:137],v233 offset:49152
v_max_f32 v249, v249, v157
MM v[4:7],a[216:219],v[192:195],v[4:7]
TR a[138:139],v233 offset:53248
v_cmp_lt_f32 vcc, s71, v249
MM v[4:7],a[212:215],v[176:179],v[4:7]
TR a[140:141],v233 offset:57344
TR a[142:143],v233 offset:61440
MM v[4:7],a[208:211],v[160:163],v[4:7]
s_cbranch_vccnz LSITE3_%=
LBACK3_%=:
MM v[12:15],a[208:211],v[164:167],v[132:135]
s_mov_b32 m0, s78
EX v0,v0
MM v[12:15],a[212:215],v[180:183],v[12:15]
s_nop 0
buffer_load_dwordx4 v248, s[20:23], s90 offen offset:0 sc1 lds
MM v[12:15],a[216:219],v[196:199],v[12:15]
s_waitcnt lgkmcnt(6)
buffer_load_dwordx4 v248, s[20:23], s90 offen offset:1024 sc1 lds
MM v[12:15],a[220:223],v[212:215],v[12:15]
EX v1,v1
MM v[112:115],a[128:131],v[80:83],0
MM v[116:119],a[128:131],v[88:91],0
MM v[8:11],a[204:207],v[212:215],v[132:135]
EX v2,v2
MM v[8:11],a[200:203],v[196:199],v[8:11]
TR a[144:145],v232 offset:49664
CV v0,v0,v1
MM v[8:11],a[196:199],v[180:183],v[8:11]
EX v3,v3
MM v[8:11],a[192:195],v[164:167],v[8:11]
TR a[146:147],v232 offset:53760
CV v1,v2,v3
MM v[16:19],a[192:195],v[168:171],v[136:139]
v_max3_f32 v112, |v112|, |v113|, |v114|
v_max_f32 v112, v112, |v115|
MM v[16:19],a[196:199],v[184:187],v[16:19]
v_bfe_u32 v112, v112, 23, 8
v_add_u32 v112, 7, v112
MM v[16:19],a[200:203],v[200:203],v[16:19]
v_min_u32 v112, 240, v112
v_lshl_or_b32 v241, v112, 16, v241
MM v[16:19],a[204:207],v[216:219],v[16:19]
v_lshl_or_b32 v112, v112, 1, 1
v_lshlrev_b32 v112, 22, v112
EX v4,v4
MM v[20:23],a[220:223],v[216:219],v[136:139]
AW a116,v112
AW a117,v112
MM v[20:23],a[216:219],v[200:203],v[20:23]
AW a118,v112
AW a119,v112
EX v5,v5
MM v[20:23],a[212:215],v[184:187],v[20:23]
v_max3_f32 v116, |v116|, |v117|, |v118|
v_max_f32 v116, v116, |v119|
MM v[20:23],a[208:211],v[168:171],v[20:23]
v_bfe_u32 v116, v116, 23, 8
v_add_u32 v116, 7, v116
EX v6,v6
MM v[28:31],a[208:211],v[172:175],v[140:143]
v_min_u32 v116, 240, v116
v_lshl_or_b32 v241, v116, 24, v241
MM v[28:31],a[212:215],v[188:191],v[28:31]
TR a[148:149],v232 offset:57856
v_lshl_or_b32 v116, v116, 1, 1
v_lshlrev_b32 v116, 22, v116
EX v7,v7
MM v[28:31],a[216:219],v[204:207],v[28:31]
AW a124,v116
AW a125,v116
MM v[28:31],a[220:223],v[220:223],v[28:31]
TR a[150:151],v232 offset:61952
AW a126,v116
AW a127,v116
EX v8,v8
MM v[24:27],a[204:207],v[220:223],v[140:143]
TR a[152:153],v233 offset:49664
TR a[154:155],v233 offset:53760
MM v[24:27],a[200:203],v[204:207],v[24:27]
EX v9,v9
MM v[24:27],a[196:199],v[188:191],v[24:27]
EX v10,v10
MM v[24:27],a[192:195],v[172:175],v[24:27]
TR a[156:157],v233 offset:57856
CV v2,v4,v5
MM v[32:35],a[224:227],v[160:163],v[128:131]
EX v11,v11
MM v[32:35],a[228:231],v[176:179],v[32:35]
TR a[158:159],v233 offset:61952
CV v3,v6,v7
MM v[32:35],a[232:235],v[192:195],v[32:35]
EX v12,v12
MM v[32:35],a[236:239],v[208:211],v[32:35]
TR a[160:161],v232 offset:50176
TR a[162:163],v232 offset:54272
MM v[36:39],a[252:255],v[208:211],v[128:131]
EX v13,v13
MM v[36:39],a[248:251],v[192:195],v[36:39]
EX v14,v14
MM v[36:39],a[244:247],v[176:179],v[36:39]
TR a[164:165],v232 offset:58368
CV v8,v8,v9
MM v[36:39],a[240:243],v[160:163],v[36:39]
EX v15,v15
MM v[44:47],a[240:243],v[164:167],v[132:135]
s_lshl_b32 s76, s50, 1
LPG6_%=:
s_cbranch_execz LPG10_%=
TR a[166:167],v232 offset:62464
CV v9,v10,v11
MM v[44:47],a[244:247],v[180:183],v[44:47]
s_bfe_u32 s77, s49, 65538
EX v16,v16
MM v[44:47],a[248:251],v[196:199],v[44:47]
s_add_u32 s76, s76, s77
TR a[168:169],v233 offset:50176
TR a[170:171],v233 offset:54272
MM v[44:47],a[252:255],v[212:215],v[44:47]
s_lshl_b32 s76, s76, 2
EX v17,v17
MM v[40:43],a[236:239],v[212:215],v[132:135]
s_add_u32 s76, s76, s84
TR a[172:173],v233 offset:58368
CV v10,v12,v13
MM v[40:43],a[232:235],v[196:199],v[40:43]
s_lshl_b32 s76, s76, 2
EX v18,v18
MM v[40:43],a[228:231],v[180:183],v[40:43]
s_lshr_b32 s77, s49, 3
EX v19,v19
MM v[40:43],a[224:227],v[164:167],v[40:43]
s_add_u32 s76, s76, s77
TR a[174:175],v233 offset:62464
CV v11,v14,v15
MM v[48:51],a[224:227],v[168:171],v[136:139]
s_mul_i32 s95, s76, 33792
EX v20,v20
MM v[48:51],a[228:231],v[184:187],v[48:51]
s_lshl_b32 s76, %[swave], 13
TR a[176:177],v232 offset:50688
TR a[178:179],v232 offset:54784
MM v[48:51],a[232:235],v[200:203],v[48:51]
s_add_u32 s95, s95, s76
EX v21,v21
MM v[48:51],a[236:239],v[216:219],v[48:51]
s_add_u32 s95, s95, 22708224
v_mov_b32 v243, -1
CV v16,v16,v17
MM v[52:55],a[252:255],v[216:219],v[136:139]
s_mov_b32 s67, s95
EX v22,v22
MM v[52:55],a[248:251],v[200:203],v[52:55]
s_add_u32 s87, s95, 6144
buffer_store_dword v243, v248, s[20:23], s87 offen offset:12 sc1
MM v[52:55],a[244:247],v[184:187],v[52:55]
TR a[180:181],v232 offset:58880
TR a[182:183],v232 offset:62976
MM v[52:55],a[240:243],v[168:171],v[52:55]
EX v23,v23
MM v[60:63],a[240:243],v[172:175],v[140:143]
EX v24,v24
MM v[60:63],a[244:247],v[188:191],v[60:63]
TR a[184:185],v233 offset:50688
CV v17,v18,v19
MM v[60:63],a[248:251],v[204:207],v[60:63]
TR a[186:187],v233 offset:54784
TR a[188:189],v233 offset:58880
TR a[190:191],v233 offset:62976
EX v25,v25
MM v[60:63],a[252:255],v[220:223],v[60:63]
s_add_u32 s87, s95, 7168
buffer_store_dword v243, v248, s[20:23], s87 offen offset:12 sc1
MM v[56:59],a[236:239],v[220:223],v[140:143]
EX v26,v26
MM v[56:59],a[232:235],v[204:207],v[56:59]
EX v27,v27
MM v[56:59],a[228:231],v[188:191],v[56:59]
EX v28,v28
CV v18,v20,v21
MM v[56:59],a[224:227],v[172:175],v[56:59]
s_waitcnt lgkmcnt(0)
s_barrier
MM a[0:3],a[128:131],v[64:67],a[0:3]
s_add_u32 m0, s65, 49152
CV v19,v22,v23
CV v24,v24,v25
MM a[0:3],a[132:135],v[68:71],a[0:3]
EX v29,v29
MM a[8:11],a[132:135],v[76:79],a[8:11]
EX v30,v30
MM a[8:11],a[128:131],v[72:75],a[8:11]
CV v25,v26,v27
CV v26,v28,v29
MM a[16:19],a[128:131],v[80:83],a[116:119]
EX v31,v31
MM a[16:19],a[132:135],v[84:87],a[16:19]
EX v32,v32
MM a[24:27],a[132:135],v[92:95],a[124:127]
RD a[192:195],v224 offset:16384
CV v27,v30,v31
MM a[24:27],a[128:131],v[88:91],a[24:27]
EX v33,v33
MM a[28:31],a[136:139],v[88:91],a[124:127]
buffer_load_dwordx4 v228, s[8:11], s64 offen offset:0 lds
MM a[28:31],a[140:143],v[92:95],a[28:31]
EX v34,v34
MM a[20:23],a[140:143],v[84:87],a[116:119]
RD a[196:199],v225 offset:16384
CV v4,v32,v33
MM a[20:23],a[136:139],v[80:83],a[20:23]
EX v35,v35
MM a[12:15],a[136:139],v[72:75],a[12:15]
EX v36,v36
MM a[12:15],a[140:143],v[76:79],a[12:15]
RD a[200:203],v226 offset:16384
CV v5,v34,v35
MM a[4:7],a[140:143],v[68:71],a[4:7]
EX v37,v37
MM a[4:7],a[136:139],v[64:67],a[4:7]
buffer_load_dwordx4 v229, s[8:11], s64 offen offset:1024 lds
MM a[32:35],a[144:147],v[64:67],a[32:35]
EX v38,v38
MM a[32:35],a[148:151],v[68:71],a[32:35]
RD a[204:207],v227 offset:16384
CV v6,v36,v37
MM a[40:43],a[148:151],v[76:79],a[40:43]
EX v39,v39
MM a[40:43],a[144:147],v[72:75],a[40:43]
RD a[208:211],v224 offset:20480
CV v7,v38,v39
MM a[48:51],a[144:147],v[80:83],a[116:119]
EX v40,v40
MM a[48:51],a[148:151],v[84:87],a[48:51]
EX v41,v41
MM a[56:59],a[148:151],v[92:95],a[124:127]
buffer_load_dwordx4 v230, s[8:11], s64 offen offset:2048 lds
MM a[56:59],a[144:147],v[88:91],a[56:59]
EX v42,v42
MM a[60:63],a[152:155],v[88:91],a[124:127]
RD a[212:215],v225 offset:20480
CV v12,v40,v41
MM a[60:63],a[156:159],v[92:95],a[60:63]
EX v43,v43
MM a[52:55],a[156:159],v[84:87],a[116:119]
RD a[216:219],v226 offset:20480
CV v13,v42,v43
MM a[52:55],a[152:155],v[80:83],a[52:55]
EX v44,v44
MM a[44:47],a[152:155],v[72:75],a[44:47]
EX v45,v45
MM a[44:47],a[156:159],v[76:79],a[44:47]
buffer_load_dwordx4 v231, s[8:11], s64 offen offset:3072 lds
MM a[36:39],a[156:159],v[68:71],a[36:39]
s_add_u32 m0, s65, 114688
RD a[220:223],v227 offset:20480
CV v14,v44,v45
MM a[36:39],a[152:155],v[64:67],a[36:39]
EX v46,v46
MM a[64:67],a[160:163],v[64:67],a[64:67]
EX v47,v47
MM a[64:67],a[164:167],v[68:71],a[64:67]
RD a[224:227],v224 offset:24576
CV v15,v46,v47
MM a[72:75],a[164:167],v[76:79],a[72:75]
EX v48,v48
MM a[72:75],a[160:163],v[72:75],a[72:75]
buffer_load_dwordx4 v235, s[12:15], s81 offen offset:0 lds
MM a[80:83],a[160:163],v[80:83],a[116:119]
EX v49,v49
MM a[80:83],a[164:167],v[84:87],a[80:83]
RD a[228:231],v225 offset:24576
CV v20,v48,v49
MM a[88:91],a[164:167],v[92:95],a[124:127]
EX v50,v50
MM a[88:91],a[160:163],v[88:91],a[88:91]
EX v51,v51
MM a[92:95],a[168:171],v[88:91],a[124:127]
RD a[232:235],v226 offset:24576
CV v21,v50,v51
MM a[92:95],a[172:175],v[92:95],a[92:95]
EX v52,v52
MM a[84:87],a[172:175],v[84:87],a[116:119]
buffer_load_dwordx4 v235, s[12:15], s64 offen offset:1024 lds
MM a[84:87],a[168:171],v[80:83],a[84:87]
EX v53,v53
MM a[76:79],a[168:171],v[72:75],a[76:79]
RD a[236:239],v227 offset:24576
CV v22,v52,v53
MM a[76:79],a[172:175],v[76:79],a[76:79]
EX v54,v54
MM a[68:71],a[172:175],v[68:71],a[68:71]
EX v55,v55
MM a[68:71],a[168:171],v[64:67],a[68:71]
RD a[240:243],v224 offset:28672
CV v23,v54,v55
MM a[96:99],a[176:179],v[64:67],a[96:99]
EX v56,v56
MM a[96:99],a[180:183],v[68:71],a[96:99]
buffer_load_dwordx4 v235, s[12:15], s81 offen offset:2048 lds
)ASM"
R"ASM(
MM a[104:107],a[180:183],v[76:79],a[104:107]
EX v57,v57
MM a[104:107],a[176:179],v[72:75],a[104:107]
RD a[244:247],v225 offset:28672
CV v28,v56,v57
MM a[112:115],a[176:179],v[80:83],a[116:119]
EX v58,v58
MM a[112:115],a[180:183],v[84:87],a[112:115]
RD a[248:251],v226 offset:28672
RD a[252:255],v227 offset:28672
MM a[120:123],a[180:183],v[92:95],a[124:127]
EX v59,v59
MM a[120:123],a[176:179],v[88:91],a[120:123]
EX v60,v60
MM a[124:127],a[184:187],v[88:91],a[124:127]
v_add_f32 v244, v144, v145
v_add_f32 v245, v148, v149
MM a[124:127],a[188:191],v[92:95],a[124:127]
v_add_f32 v246, v152, v153
v_add_f32 v247, v156, v157
MM a[116:119],a[188:191],v[84:87],a[116:119]
EX v61,v61
MM a[116:119],a[184:187],v[80:83],a[116:119]
buffer_load_dwordx4 v235, s[12:15], s64 offen offset:3072 lds
MM a[108:111],a[184:187],v[72:75],a[108:111]
EX v62,v62
MM a[108:111],a[188:191],v[76:79],a[108:111]
EX v63,v63
MM a[100:103],a[188:191],v[68:71],a[100:103]
CV v29,v58,v59
CV v30,v60,v61
MM a[100:103],a[184:187],v[64:67],a[100:103]
CV v31,v62,v63
v_smfmac_f32_16x16x64_bf16 v[144:147], v[236:239], v[0:7], v234
v_smfmac_f32_16x16x64_bf16 v[148:151], v[236:239], v[8:15], v234
v_smfmac_f32_16x16x64_bf16 v[152:155], v[236:239], v[16:23], v234
v_smfmac_f32_16x16x64_bf16 v[156:159], v[236:239], v[24:31], v234
s_waitcnt lgkmcnt(0)
MM v[64:67],a[192:195],v[160:163],v[128:131]
s_add_u32 s64, s64, 16384
TR a[128:129],v232 offset:0
s_add_u32 s81, s64, 896
MM v[64:67],a[196:199],v[176:179],v[64:67]
TR a[130:131],v232 offset:4096
v_max3_f32 v249, v144, v145, v148
MM v[64:67],a[200:203],v[192:195],v[64:67]
TR a[132:133],v232 offset:8192
v_max3_f32 v249, v249, v149, v152
MM v[64:67],a[204:207],v[208:211],v[64:67]
TR a[134:135],v232 offset:12288
v_max3_f32 v249, v249, v153, v156
MM v[68:71],a[220:223],v[208:211],v[128:131]
TR a[136:137],v233 offset:0
v_max_f32 v249, v249, v157
MM v[68:71],a[216:219],v[192:195],v[68:71]
TR a[138:139],v233 offset:4096
v_cmp_lt_f32 vcc, s71, v249
MM v[68:71],a[212:215],v[176:179],v[68:71]
TR a[140:141],v233 offset:8192
TR a[142:143],v233 offset:12288
MM v[68:71],a[208:211],v[160:163],v[68:71]
s_cbranch_vccnz LSITE4_%=
LBACK4_%=:
MM v[76:79],a[208:211],v[164:167],v[132:135]
s_cmp_lg_u32 %[sb], 0
s_cbranch_scc1 LEP_%=
v_mov_b32 v250, s69
s_mov_b32 s76, 32768
s_mov_b64 exec, 1
buffer_store_dword v250, off, s[20:23], s76 sc1
s_mov_b64 exec, -1
s_nop 1
LEP_%=:
MM v[76:79],a[212:215],v[180:183],v[76:79]
EX v64,v64
MM v[76:79],a[216:219],v[196:199],v[76:79]
EX v65,v65
MM v[76:79],a[220:223],v[212:215],v[76:79]
TR a[144:145],v232 offset:512
CV v64,v64,v65
MM v[72:75],a[204:207],v[212:215],v[132:135]
TR a[146:147],v232 offset:4608
v_lshrrev_b32 v34, 1, v248
MM v[72:75],a[200:203],v[196:199],v[72:75]
s_mov_b32 s90, 86016
buffer_load_dwordx2 v[32:33], v34, s[20:23], s90 offen offset:0 sc1
MM v[72:75],a[196:199],v[180:183],v[72:75]
EX v66,v66
MM v[72:75],a[192:195],v[164:167],v[72:75]
EX v67,v67
MM v[80:83],a[192:195],v[168:171],v[136:139]
EX v68,v68
MM v[80:83],a[196:199],v[184:187],v[80:83]
TR a[148:149],v232 offset:8704
CV v65,v66,v67
MM v[80:83],a[200:203],v[200:203],v[80:83]
EX v69,v69
MM v[80:83],a[204:207],v[216:219],v[80:83]
TR a[150:151],v232 offset:12800
CV v66,v68,v69
MM v[84:87],a[220:223],v[216:219],v[136:139]
EX v70,v70
MM v[84:87],a[216:219],v[200:203],v[84:87]
TR a[152:153],v233 offset:512
TR a[154:155],v233 offset:4608
MM v[84:87],a[212:215],v[184:187],v[84:87]
EX v71,v71
MM v[84:87],a[208:211],v[168:171],v[84:87]
TR a[156:157],v233 offset:8704
CV v67,v70,v71
MM v[92:95],a[208:211],v[172:175],v[140:143]
EX v72,v72
MM v[92:95],a[212:215],v[188:191],v[92:95]
EX v73,v73
MM v[92:95],a[216:219],v[204:207],v[92:95]
TR a[158:159],v233 offset:12800
CV v72,v72,v73
MM v[92:95],a[220:223],v[220:223],v[92:95]
EX v74,v74
MM v[88:91],a[204:207],v[220:223],v[140:143]
TR a[160:161],v232 offset:1024
TR a[162:163],v232 offset:5120
MM v[88:91],a[200:203],v[204:207],v[88:91]
EX v75,v75
MM v[88:91],a[196:199],v[188:191],v[88:91]
TR a[164:165],v232 offset:9216
CV v73,v74,v75
MM v[88:91],a[192:195],v[172:175],v[88:91]
EX v76,v76
MM v[96:99],a[224:227],v[160:163],v[128:131]
EX v77,v77
MM v[96:99],a[228:231],v[176:179],v[96:99]
TR a[166:167],v232 offset:13312
CV v74,v76,v77
MM v[96:99],a[232:235],v[192:195],v[96:99]
EX v78,v78
MM v[96:99],a[236:239],v[208:211],v[96:99]
TR a[168:169],v233 offset:1024
TR a[170:171],v233 offset:5120
MM v[100:103],a[252:255],v[208:211],v[128:131]
EX v79,v79
MM v[100:103],a[248:251],v[192:195],v[100:103]
TR a[172:173],v233 offset:9216
CV v75,v78,v79
MM v[100:103],a[244:247],v[176:179],v[100:103]
EX v80,v80
MM v[100:103],a[240:243],v[160:163],v[100:103]
EX v81,v81
MM v[108:111],a[240:243],v[164:167],v[132:135]
TR a[174:175],v233 offset:13312
CV v80,v80,v81
MM v[108:111],a[244:247],v[180:183],v[108:111]
EX v82,v82
MM v[108:111],a[248:251],v[196:199],v[108:111]
TR a[176:177],v232 offset:1536
TR a[178:179],v232 offset:5632
MM v[108:111],a[252:255],v[212:215],v[108:111]
EX v83,v83
MM v[104:107],a[236:239],v[212:215],v[132:135]
TR a[180:181],v232 offset:9728
CV v81,v82,v83
MM v[104:107],a[232:235],v[196:199],v[104:107]
EX v84,v84
MM v[104:107],a[228:231],v[180:183],v[104:107]
TR a[182:183],v232 offset:13824
TR a[184:185],v233 offset:1536
MM v[104:107],a[224:227],v[164:167],v[104:107]
EX v85,v85
MM v[112:115],a[224:227],v[168:171],v[136:139]
TR a[186:187],v233 offset:5632
CV v82,v84,v85
MM v[112:115],a[228:231],v[184:187],v[112:115]
EX v86,v86
MM v[112:115],a[232:235],v[200:203],v[112:115]
EX v87,v87
MM v[112:115],a[236:239],v[216:219],v[112:115]
TR a[188:189],v233 offset:9728
CV v83,v86,v87
MM v[116:119],a[252:255],v[216:219],v[136:139]
EX v88,v88
MM v[116:119],a[248:251],v[200:203],v[116:119]
EX v89,v89
MM v[116:119],a[244:247],v[184:187],v[116:119]
TR a[190:191],v233 offset:13824
CV v88,v88,v89
MM v[116:119],a[240:243],v[168:171],v[116:119]
EX v90,v90
MM v[124:127],a[240:243],v[172:175],v[140:143]
EX v91,v91
MM v[124:127],a[244:247],v[188:191],v[124:127]
EX v92,v92
MM v[124:127],a[248:251],v[204:207],v[124:127]
EX v93,v93
MM v[124:127],a[252:255],v[220:223],v[124:127]
EX v94,v94
MM v[120:123],a[236:239],v[220:223],v[140:143]
EX v95,v95
MM v[120:123],a[232:235],v[204:207],v[120:123]
EX v96,v96
MM v[120:123],a[228:231],v[188:191],v[120:123]
EX v97,v97
MM v[120:123],a[224:227],v[172:175],v[120:123]
s_waitcnt vmcnt(11) lgkmcnt(0)
s_barrier
MM a[0:3],a[128:131],v[0:3],a[0:3]
s_add_u32 m0, s65, 0
CV v89,v90,v91
CV v90,v92,v93
MM a[0:3],a[132:135],v[4:7],a[0:3]
RD a[192:195],v224 offset:32768
CV v91,v94,v95
MM a[8:11],a[132:135],v[12:15],a[8:11]
buffer_load_dwordx4 v228, s[8:11], s64 offen offset:0 lds
MM a[8:11],a[128:131],v[8:11],a[8:11]
RD a[196:199],v225 offset:32768
CV v68,v96,v97
MM a[16:19],a[128:131],v[16:19],a[16:19]
RD a[200:203],v226 offset:32768
RD a[204:207],v227 offset:32768
MM a[16:19],a[132:135],v[20:23],a[16:19]
buffer_load_dwordx4 v229, s[8:11], s64 offen offset:1024 lds
MM a[24:27],a[132:135],v[28:31],a[24:27]
EX v98,v98
MM a[24:27],a[128:131],v[24:27],a[24:27]
RD a[208:211],v224 offset:36864
RD a[212:215],v225 offset:36864
MM a[28:31],a[136:139],v[24:27],a[28:31]
EX v99,v99
MM a[28:31],a[140:143],v[28:31],a[28:31]
RD a[216:219],v226 offset:36864
CV v69,v98,v99
MM a[20:23],a[140:143],v[20:23],a[20:23]
EX v100,v100
MM a[20:23],a[136:139],v[16:19],a[20:23]
EX v101,v101
MM a[12:15],a[136:139],v[8:11],a[12:15]
s_waitcnt vmcnt(2)
v_cmp_ne_u32 vcc, s69, v33
s_cbranch_vccnz LRRK_%=
LRRKb_%=:
MM a[12:15],a[140:143],v[12:15],a[12:15]
v_add_u32_dpp v32, v32, v32 row_ror:8 row_mask:0xf bank_mask:0xf
CV v70,v100,v101
MM a[4:7],a[140:143],v[4:7],a[4:7]
s_and_b32 s76, %[sb], 7
RD a[220:223],v227 offset:36864
v_mov_b32 v35, v32
MM a[4:7],a[136:139],v[0:3],a[4:7]
s_lshl_b32 s54, s50, 2
v_permlane16_swap_b32 v32, v35
v_add_u32 v32, v32, v35
MM a[32:35],a[144:147],v[0:3],a[32:35]
s_add_u32 s54, s54, %[swave]
RD a[224:227],v224 offset:40960
v_mov_b32 v35, v32
MM a[32:35],a[148:151],v[4:7],a[32:35]
s_lshl_b32 s54, s54, 1
v_permlane32_swap_b32 v32, v35
v_add_u32 v32, v32, v35
MM a[40:43],a[148:151],v[12:15],a[40:43]
s_bfe_u32 s55, s49, 65538
RD a[228:231],v225 offset:40960
v_readlane_b32 s77, v32, s76
MM a[40:43],a[144:147],v[8:11],a[40:43]
s_bfm_b32 s76, s76, 0
EX v102,v102
MM a[48:51],a[144:147],v[16:19],a[48:51]
s_xor_b32 s55, s55, 1
v_cmp_gt_u32 s[60:61], s77, v32
v_cmp_eq_u32 s[62:63], s77, v32
MM a[48:51],a[148:151],v[20:23],a[48:51]
s_and_b32 s62, s62, s76
EX v103,v103
MM a[56:59],a[148:151],v[28:31],a[56:59]
s_or_b32 s60, s60, s62
EX v104,v104
MM a[56:59],a[144:147],v[24:27],a[56:59]
s_and_b32 s60, s60, 255
EX v105,v105
MM a[60:63],a[152:155],v[24:27],a[60:63]
s_bcnt1_i32_b32 s51, s60
s_cmp_lt_u32 s51, 4
s_cselect_b32 s82, 3, 99
MM a[60:63],a[156:159],v[28:31],a[60:63]
s_lshr_b32 s16, %[sb], 3
EX v106,v106
MM a[52:55],a[156:159],v[20:23],a[52:55]
s_lshl_b32 s76, s51, 5
CV v71,v102,v103
CV v76,v104,v105
MM a[52:55],a[152:155],v[16:19],a[52:55]
s_add_u32 s16, s16, s76
EX v107,v107
MM a[44:47],a[152:155],v[8:11],a[44:47]
s_add_u32 s54, s54, s55
buffer_load_dwordx4 v230, s[8:11], s64 offen offset:2048 lds
MM a[44:47],a[156:159],v[12:15],a[44:47]
s_mul_i32 s52, s54, 40960
EX v108,v108
MM a[36:39],a[156:159],v[4:7],a[36:39]
s_lshr_b32 s54, s84, 1
EX v109,v109
MM a[36:39],a[152:155],v[0:3],a[36:39]
s_mul_i32 s54, s54, 640
CV v77,v106,v107
MM a[64:67],a[160:163],v[0:3],a[64:67]
s_add_u32 s52, s52, s54
EX v110,v110
MM a[64:67],a[164:167],v[4:7],a[64:67]
s_lshr_b32 s56, s49, 3
EX v111,v111
MM a[72:75],a[164:167],v[12:15],a[72:75]
s_and_b32 s54, s84, 1
buffer_load_dwordx4 v231, s[8:11], s64 offen offset:3072 lds
MM a[72:75],a[160:163],v[8:11],a[72:75]
s_lshl_b32 s55, s56, 1
CV v78,v108,v109
CV v79,v110,v111
MM a[80:83],a[160:163],v[16:19],a[80:83]
s_add_u32 s55, s55, s54
EX v112,v112
MM a[80:83],a[164:167],v[20:23],a[80:83]
s_mul_i32 s53, s55, 5120
EX v113,v113
MM a[88:91],a[164:167],v[28:31],a[88:91]
s_add_u32 s53, s53, s52
EX v114,v114
MM a[88:91],a[160:163],v[24:27],a[88:91]
s_mul_i32 s54, s54, 384
s_add_u32 m0, s65, 65536
CV v84,v112,v113
MM a[92:95],a[168:171],v[24:27],a[92:95]
s_add_u32 s52, s52, s54
EX v115,v115
MM a[92:95],a[172:175],v[28:31],a[92:95]
s_and_b32 s55, s56, 2
buffer_load_dwordx4 v235, s[12:15], s81 offen offset:0 lds
MM a[84:87],a[172:175],v[20:23],a[84:87]
s_lshl_b32 s55, s55, 6
EX v116,v116
MM a[84:87],a[168:171],v[16:19],a[84:87]
s_add_u32 s52, s52, s55
RD a[232:235],v226 offset:40960
CV v85,v114,v115
MM a[76:79],a[168:171],v[8:11],a[76:79]
s_and_b32 s55, s56, 1
EX v117,v117
MM a[76:79],a[172:175],v[12:15],a[76:79]
s_mul_i32 s55, s55, 20480
CV v86,v116,v117
MM a[68:71],a[172:175],v[4:7],a[68:71]
s_add_u32 s52, s52, s55
v_lshrrev_b32 v40, 7, v248
v_mul_u32_u24 v40, 1280, v40
MM a[68:71],a[168:171],v[0:3],a[68:71]
v_and_b32 v41, 112, v248
v_add_u32 v40, v40, v41
MM a[96:99],a[176:179],v[0:3],a[96:99]
buffer_store_dwordx4 v[236:239], v40, s[40:43], s52 offen offset:0 sc1
EX v118,v118
MM a[96:99],a[180:183],v[4:7],a[96:99]
s_add_u32 s52, s52, 10240
EX v119,v119
MM a[104:107],a[180:183],v[12:15],a[104:107]
EX v120,v120
MM a[104:107],a[176:179],v[8:11],a[104:107]
buffer_load_dwordx4 v235, s[12:15], s64 offen offset:1024 lds
MM a[112:115],a[176:179],v[16:19],a[112:115]
EX v121,v121
MM a[112:115],a[180:183],v[20:23],a[112:115]
RD a[236:239],v227 offset:40960
CV v87,v118,v119
MM a[120:123],a[180:183],v[28:31],a[120:123]
EX v122,v122
MM a[120:123],a[176:179],v[24:27],a[120:123]
buffer_store_dwordx4 v[236:239], v40, s[40:43], s52 offen offset:0 sc1
MM a[124:127],a[184:187],v[24:27],a[124:127]
v_add_f32 v244, v144, v145
v_add_f32 v245, v148, v149
MM a[124:127],a[188:191],v[28:31],a[124:127]
v_add_f32 v246, v152, v153
v_add_f32 v247, v156, v157
MM a[116:119],a[188:191],v[20:23],a[116:119]
EX v123,v123
MM a[116:119],a[184:187],v[16:19],a[116:119]
EX v124,v124
MM a[108:111],a[184:187],v[8:11],a[108:111]
RD a[240:243],v224 offset:45056
CV v92,v120,v121
MM a[108:111],a[188:191],v[12:15],a[108:111]
s_mov_b32 exec_hi, 0
buffer_store_dwordx4 v[236:239], v40, s[40:43], s53 offen offset:256 sc1
)ASM"
R"ASM(
s_mov_b64 exec, -1
EX v125,v125
MM a[100:103],a[188:191],v[4:7],a[100:103]
s_nop 0
buffer_load_dwordx4 v235, s[12:15], s81 offen offset:2048 lds
MM a[100:103],a[184:187],v[0:3],a[100:103]
EX v126,v126
v_smfmac_f32_16x16x64_bf16 v[144:147], v[236:239], v[64:71], v234
RD a[244:247],v225 offset:45056
RD a[248:251],v226 offset:45056
RD a[252:255],v227 offset:45056
EX v127,v127
v_smfmac_f32_16x16x64_bf16 v[148:151], v[236:239], v[72:79], v234
CV v93,v122,v123
v_smfmac_f32_16x16x64_bf16 v[152:155], v[236:239], v[80:87], v234
CV v94,v124,v125
CV v95,v126,v127
s_nop 1
v_smfmac_f32_16x16x64_bf16 v[156:159], v[236:239], v[88:95], v234
s_waitcnt lgkmcnt(0)
buffer_load_dwordx4 v235, s[12:15], s64 offen offset:3072 lds
s_cmp_lt_u32 s51, 4
s_cbranch_scc1 LLAR_%=
s_mov_b32 s70, 6
s_branch LLAP_%=
s_nop 0
s_nop 0
s_nop 0
s_nop 0
s_nop 0
s_nop 0
LPG7_%=:
s_cbranch_execz LPG11_%=
s_nop 0
s_nop 0
s_nop 0
s_nop 0
s_nop 0
s_nop 0
s_nop 0
s_nop 0
s_nop 0
s_nop 0
s_nop 0
s_nop 0
s_nop 0
s_nop 0
s_nop 0
s_nop 0
s_nop 0
s_nop 0
s_nop 0
s_nop 0
s_nop 0
s_nop 0
s_nop 0
s_nop 0
s_nop 0
s_nop 0
s_nop 0
s_nop 0
s_nop 0
s_nop 0
s_nop 0
s_nop 0
s_nop 0
s_nop 0
s_nop 0
s_nop 0
s_nop 0
s_nop 0
s_nop 0
s_nop 0
s_nop 0
s_nop 0
s_nop 0
s_nop 0
s_nop 0
s_nop 0
s_nop 0
s_nop 0
s_nop 0
s_nop 0
s_nop 0
s_nop 0
s_nop 0
s_nop 0
s_nop 0
s_nop 0
s_nop 0
s_nop 0
s_nop 0
s_nop 0
s_nop 0
s_nop 0
s_nop 0
LLAP_%=:
MM v[0:3],a[192:195],v[160:163],v[128:131]
s_add_u32 s64, s64, 16384
TR a[128:129],v232 offset:16384
s_add_u32 s81, s64, 896
MM v[0:3],a[196:199],v[176:179],v[0:3]
TR a[130:131],v232 offset:20480
v_max3_f32 v249, v144, v145, v148
MM v[0:3],a[200:203],v[192:195],v[0:3]
TR a[132:133],v232 offset:24576
v_max3_f32 v249, v249, v149, v152
MM v[0:3],a[204:207],v[208:211],v[0:3]
TR a[134:135],v232 offset:28672
v_max3_f32 v249, v249, v153, v156
MM v[4:7],a[220:223],v[208:211],v[128:131]
TR a[136:137],v233 offset:16384
v_max_f32 v249, v249, v157
MM v[4:7],a[216:219],v[192:195],v[4:7]
TR a[138:139],v233 offset:20480
v_cmp_lt_f32 vcc, s71, v249
MM v[4:7],a[212:215],v[176:179],v[4:7]
TR a[140:141],v233 offset:24576
TR a[142:143],v233 offset:28672
MM v[4:7],a[208:211],v[160:163],v[4:7]
s_cbranch_vccnz LSITE5_%=
LBACK5_%=:
MM v[12:15],a[208:211],v[164:167],v[132:135]
EX v0,v0
MM v[12:15],a[212:215],v[180:183],v[12:15]
EX v1,v1
MM v[12:15],a[216:219],v[196:199],v[12:15]
TR a[144:145],v232 offset:16896
CV v0,v0,v1
MM v[12:15],a[220:223],v[212:215],v[12:15]
EX v2,v2
MM v[8:11],a[204:207],v[212:215],v[132:135]
EX v3,v3
MM v[8:11],a[200:203],v[196:199],v[8:11]
TR a[146:147],v232 offset:20992
CV v1,v2,v3
MM v[8:11],a[196:199],v[180:183],v[8:11]
EX v4,v4
MM v[8:11],a[192:195],v[164:167],v[8:11]
TR a[148:149],v232 offset:25088
TR a[150:151],v232 offset:29184
MM v[16:19],a[192:195],v[168:171],v[136:139]
EX v5,v5
MM v[16:19],a[196:199],v[184:187],v[16:19]
TR a[152:153],v233 offset:16896
CV v2,v4,v5
MM v[16:19],a[200:203],v[200:203],v[16:19]
EX v6,v6
MM v[16:19],a[204:207],v[216:219],v[16:19]
TR a[154:155],v233 offset:20992
TR a[156:157],v233 offset:25088
MM v[20:23],a[220:223],v[216:219],v[136:139]
EX v7,v7
MM v[20:23],a[216:219],v[200:203],v[20:23]
TR a[158:159],v233 offset:29184
CV v3,v6,v7
MM v[20:23],a[212:215],v[184:187],v[20:23]
EX v8,v8
MM v[20:23],a[208:211],v[168:171],v[20:23]
EX v9,v9
MM v[28:31],a[208:211],v[172:175],v[140:143]
TR a[160:161],v232 offset:17408
CV v8,v8,v9
MM v[28:31],a[212:215],v[188:191],v[28:31]
EX v10,v10
MM v[28:31],a[216:219],v[204:207],v[28:31]
TR a[162:163],v232 offset:21504
TR a[164:165],v232 offset:25600
MM v[28:31],a[220:223],v[220:223],v[28:31]
EX v11,v11
MM v[24:27],a[204:207],v[220:223],v[140:143]
TR a[166:167],v232 offset:29696
CV v9,v10,v11
MM v[24:27],a[200:203],v[204:207],v[24:27]
EX v12,v12
MM v[24:27],a[196:199],v[188:191],v[24:27]
EX v13,v13
MM v[24:27],a[192:195],v[172:175],v[24:27]
TR a[168:169],v233 offset:17408
CV v10,v12,v13
MM v[32:35],a[224:227],v[160:163],v[128:131]
EX v14,v14
MM v[32:35],a[228:231],v[176:179],v[32:35]
TR a[170:171],v233 offset:21504
TR a[172:173],v233 offset:25600
MM v[32:35],a[232:235],v[192:195],v[32:35]
EX v15,v15
MM v[32:35],a[236:239],v[208:211],v[32:35]
TR a[174:175],v233 offset:29696
CV v11,v14,v15
MM v[36:39],a[252:255],v[208:211],v[128:131]
EX v16,v16
MM v[36:39],a[248:251],v[192:195],v[36:39]
EX v17,v17
MM v[36:39],a[244:247],v[176:179],v[36:39]
TR a[176:177],v232 offset:17920
CV v16,v16,v17
MM v[36:39],a[240:243],v[160:163],v[36:39]
EX v18,v18
MM v[44:47],a[240:243],v[164:167],v[132:135]
TR a[178:179],v232 offset:22016
TR a[180:181],v232 offset:26112
MM v[44:47],a[244:247],v[180:183],v[44:47]
EX v19,v19
MM v[44:47],a[248:251],v[196:199],v[44:47]
TR a[182:183],v232 offset:30208
CV v17,v18,v19
MM v[44:47],a[252:255],v[212:215],v[44:47]
EX v20,v20
MM v[40:43],a[236:239],v[212:215],v[132:135]
TR a[184:185],v233 offset:17920
TR a[186:187],v233 offset:22016
MM v[40:43],a[232:235],v[196:199],v[40:43]
EX v21,v21
MM v[40:43],a[228:231],v[180:183],v[40:43]
TR a[188:189],v233 offset:26112
CV v18,v20,v21
MM v[40:43],a[224:227],v[164:167],v[40:43]
EX v22,v22
MM v[48:51],a[224:227],v[168:171],v[136:139]
EX v23,v23
MM v[48:51],a[228:231],v[184:187],v[48:51]
TR a[190:191],v233 offset:30208
CV v19,v22,v23
MM v[48:51],a[232:235],v[200:203],v[48:51]
EX v24,v24
MM v[48:51],a[236:239],v[216:219],v[48:51]
EX v25,v25
MM v[52:55],a[252:255],v[216:219],v[136:139]
EX v26,v26
MM v[52:55],a[248:251],v[200:203],v[52:55]
EX v27,v27
MM v[52:55],a[244:247],v[184:187],v[52:55]
EX v28,v28
MM v[52:55],a[240:243],v[168:171],v[52:55]
EX v29,v29
MM v[60:63],a[240:243],v[172:175],v[140:143]
EX v30,v30
MM v[60:63],a[244:247],v[188:191],v[60:63]
EX v31,v31
MM v[60:63],a[248:251],v[204:207],v[60:63]
EX v32,v32
MM v[60:63],a[252:255],v[220:223],v[60:63]
EX v33,v33
MM v[56:59],a[236:239],v[220:223],v[140:143]
EX v34,v34
MM v[56:59],a[232:235],v[204:207],v[56:59]
EX v35,v35
MM v[56:59],a[228:231],v[188:191],v[56:59]
EX v36,v36
MM v[56:59],a[224:227],v[172:175],v[56:59]
s_waitcnt vmcnt(12) lgkmcnt(0)
s_barrier
MM a[0:3],a[128:131],v[64:67],a[0:3]
s_add_u32 m0, s65, 16384
CV v24,v24,v25
CV v25,v26,v27
MM a[0:3],a[132:135],v[68:71],a[0:3]
RD a[192:195],v224 offset:49152
CV v26,v28,v29
MM a[8:11],a[132:135],v[76:79],a[8:11]
RD a[196:199],v225 offset:49152
CV v27,v30,v31
MM a[8:11],a[128:131],v[72:75],a[8:11]
buffer_load_dwordx4 v228, s[8:11], s64 offen offset:0 lds
MM a[16:19],a[128:131],v[80:83],a[16:19]
RD a[200:203],v226 offset:49152
CV v4,v32,v33
MM a[16:19],a[132:135],v[84:87],a[16:19]
RD a[204:207],v227 offset:49152
CV v5,v34,v35
MM a[24:27],a[132:135],v[92:95],a[24:27]
buffer_load_dwordx4 v229, s[8:11], s64 offen offset:1024 lds
MM a[24:27],a[128:131],v[88:91],a[24:27]
RD a[208:211],v224 offset:53248
RD a[212:215],v225 offset:53248
MM a[28:31],a[136:139],v[88:91],a[28:31]
buffer_load_dwordx4 v230, s[8:11], s64 offen offset:2048 lds
MM a[28:31],a[140:143],v[92:95],a[28:31]
RD a[216:219],v226 offset:53248
RD a[220:223],v227 offset:53248
MM a[20:23],a[140:143],v[84:87],a[20:23]
EX v37,v37
MM a[20:23],a[136:139],v[80:83],a[20:23]
RD a[224:227],v224 offset:57344
CV v6,v36,v37
MM a[12:15],a[136:139],v[72:75],a[12:15]
EX v38,v38
MM a[12:15],a[140:143],v[76:79],a[12:15]
buffer_load_dwordx4 v231, s[8:11], s64 offen offset:3072 lds
MM a[4:7],a[140:143],v[68:71],a[4:7]
s_add_u32 m0, s65, 81920
EX v39,v39
MM a[4:7],a[136:139],v[64:67],a[4:7]
RD a[228:231],v225 offset:57344
CV v7,v38,v39
MM a[32:35],a[144:147],v[64:67],a[32:35]
EX v40,v40
MM a[32:35],a[148:151],v[68:71],a[32:35]
EX v41,v41
MM a[40:43],a[148:151],v[76:79],a[40:43]
RD a[232:235],v226 offset:57344
CV v12,v40,v41
MM a[40:43],a[144:147],v[72:75],a[40:43]
EX v42,v42
MM a[48:51],a[144:147],v[80:83],a[48:51]
buffer_load_dwordx4 v235, s[12:15], s81 offen offset:0 lds
MM a[48:51],a[148:151],v[84:87],a[48:51]
EX v43,v43
MM a[56:59],a[148:151],v[92:95],a[56:59]
RD a[236:239],v227 offset:57344
CV v13,v42,v43
MM a[56:59],a[144:147],v[88:91],a[56:59]
EX v44,v44
MM a[60:63],a[152:155],v[88:91],a[60:63]
EX v45,v45
MM a[60:63],a[156:159],v[92:95],a[60:63]
RD a[240:243],v224 offset:61440
CV v14,v44,v45
MM a[52:55],a[156:159],v[84:87],a[52:55]
EX v46,v46
MM a[52:55],a[152:155],v[80:83],a[52:55]
buffer_load_dwordx4 v235, s[12:15], s64 offen offset:1024 lds
MM a[44:47],a[152:155],v[72:75],a[44:47]
EX v47,v47
MM a[44:47],a[156:159],v[76:79],a[44:47]
RD a[244:247],v225 offset:61440
CV v15,v46,v47
MM a[36:39],a[156:159],v[68:71],a[36:39]
EX v48,v48
MM a[36:39],a[152:155],v[64:67],a[36:39]
EX v49,v49
MM a[64:67],a[160:163],v[64:67],a[64:67]
RD a[248:251],v226 offset:61440
CV v20,v48,v49
MM a[64:67],a[164:167],v[68:71],a[64:67]
EX v50,v50
MM a[72:75],a[164:167],v[76:79],a[72:75]
buffer_load_dwordx4 v235, s[12:15], s81 offen offset:2048 lds
MM a[72:75],a[160:163],v[72:75],a[72:75]
EX v51,v51
MM a[80:83],a[160:163],v[80:83],a[80:83]
RD a[252:255],v227 offset:61440
CV v21,v50,v51
MM a[80:83],a[164:167],v[84:87],a[80:83]
EX v52,v52
MM a[88:91],a[164:167],v[92:95],a[88:91]
EX v53,v53
MM a[88:91],a[160:163],v[88:91],a[88:91]
buffer_load_dwordx4 v235, s[12:15], s64 offen offset:3072 lds
MM a[92:95],a[168:171],v[88:91],a[92:95]
EX v54,v54
MM a[92:95],a[172:175],v[92:95],a[92:95]
EX v55,v55
MM a[84:87],a[172:175],v[84:87],a[84:87]
EX v56,v56
MM a[84:87],a[168:171],v[80:83],a[84:87]
EX v57,v57
MM a[76:79],a[168:171],v[72:75],a[76:79]
EX v58,v58
MM a[76:79],a[172:175],v[76:79],a[76:79]
EX v59,v59
MM a[68:71],a[172:175],v[68:71],a[68:71]
EX v60,v60
MM a[68:71],a[168:171],v[64:67],a[68:71]
EX v61,v61
MM a[96:99],a[176:179],v[64:67],a[96:99]
EX v62,v62
MM a[96:99],a[180:183],v[68:71],a[96:99]
EX v63,v63
MM a[104:107],a[180:183],v[76:79],a[104:107]
CV v22,v52,v53
CV v23,v54,v55
MM a[104:107],a[176:179],v[72:75],a[104:107]
CV v28,v56,v57
CV v29,v58,v59
MM a[112:115],a[176:179],v[80:83],a[112:115]
CV v30,v60,v61
CV v31,v62,v63
MM a[112:115],a[180:183],v[84:87],a[112:115]
MM a[120:123],a[180:183],v[92:95],a[120:123]
MM a[120:123],a[176:179],v[88:91],a[120:123]
MM a[124:127],a[184:187],v[88:91],a[124:127]
v_add_f32 v244, v144, v145
v_add_f32 v245, v148, v149
MM a[124:127],a[188:191],v[92:95],a[124:127]
v_add_f32 v246, v152, v153
v_add_f32 v247, v156, v157
MM a[116:119],a[188:191],v[84:87],a[116:119]
s_cmp_eq_u32 s70, s82
s_cbranch_scc1 LWW_%=
LLALWW_%=:
MM a[116:119],a[184:187],v[80:83],a[116:119]
MM a[108:111],a[184:187],v[72:75],a[108:111]
MM a[108:111],a[188:191],v[76:79],a[108:111]
MM a[100:103],a[188:191],v[68:71],a[100:103]
MM a[100:103],a[184:187],v[64:67],a[100:103]
v_smfmac_f32_16x16x64_bf16 v[144:147], v[236:239], v[0:7], v234
v_smfmac_f32_16x16x64_bf16 v[148:151], v[236:239], v[8:15], v234
v_smfmac_f32_16x16x64_bf16 v[152:155], v[236:239], v[16:23], v234
v_smfmac_f32_16x16x64_bf16 v[156:159], v[236:239], v[24:31], v234
s_waitcnt lgkmcnt(0)
MM v[64:67],a[192:195],v[160:163],v[128:131]
s_add_u32 s64, s64, 16384
TR a[128:129],v232 offset:32768
s_add_u32 s81, s64, 896
MM v[64:67],a[196:199],v[176:179],v[64:67]
TR a[130:131],v232 offset:36864
v_max3_f32 v249, v144, v145, v148
MM v[64:67],a[200:203],v[192:195],v[64:67]
TR a[132:133],v232 offset:40960
v_max3_f32 v249, v249, v149, v152
MM v[64:67],a[204:207],v[208:211],v[64:67]
TR a[134:135],v232 offset:45056
v_max3_f32 v249, v249, v153, v156
MM v[68:71],a[220:223],v[208:211],v[128:131]
TR a[136:137],v233 offset:32768
v_max_f32 v249, v249, v157
MM v[68:71],a[216:219],v[192:195],v[68:71]
TR a[138:139],v233 offset:36864
v_cmp_lt_f32 vcc, s71, v249
MM v[68:71],a[212:215],v[176:179],v[68:71]
TR a[140:141],v233 offset:40960
TR a[142:143],v233 offset:45056
MM v[68:71],a[208:211],v[160:163],v[68:71]
s_cbranch_vccnz LSITE6_%=
LBACK6_%=:
MM v[76:79],a[208:211],v[164:167],v[132:135]
EX v64,v64
MM v[76:79],a[212:215],v[180:183],v[76:79]
EX v65,v65
MM v[76:79],a[216:219],v[196:199],v[76:79]
TR a[144:145],v232 offset:33280
CV v64,v64,v65
MM v[76:79],a[220:223],v[212:215],v[76:79]
EX v66,v66
MM v[72:75],a[204:207],v[212:215],v[132:135]
EX v67,v67
MM v[72:75],a[200:203],v[196:199],v[72:75]
TR a[146:147],v232 offset:37376
CV v65,v66,v67
MM v[72:75],a[196:199],v[180:183],v[72:75]
)ASM"
R"ASM(
EX v68,v68
MM v[72:75],a[192:195],v[164:167],v[72:75]
TR a[148:149],v232 offset:41472
TR a[150:151],v232 offset:45568
MM v[80:83],a[192:195],v[168:171],v[136:139]
EX v69,v69
MM v[80:83],a[196:199],v[184:187],v[80:83]
TR a[152:153],v233 offset:33280
CV v66,v68,v69
MM v[80:83],a[200:203],v[200:203],v[80:83]
EX v70,v70
MM v[80:83],a[204:207],v[216:219],v[80:83]
TR a[154:155],v233 offset:37376
TR a[156:157],v233 offset:41472
MM v[84:87],a[220:223],v[216:219],v[136:139]
EX v71,v71
MM v[84:87],a[216:219],v[200:203],v[84:87]
TR a[158:159],v233 offset:45568
CV v67,v70,v71
MM v[84:87],a[212:215],v[184:187],v[84:87]
EX v72,v72
MM v[84:87],a[208:211],v[168:171],v[84:87]
EX v73,v73
MM v[92:95],a[208:211],v[172:175],v[140:143]
TR a[160:161],v232 offset:33792
CV v72,v72,v73
MM v[92:95],a[212:215],v[188:191],v[92:95]
EX v74,v74
MM v[92:95],a[216:219],v[204:207],v[92:95]
TR a[162:163],v232 offset:37888
TR a[164:165],v232 offset:41984
MM v[92:95],a[220:223],v[220:223],v[92:95]
EX v75,v75
MM v[88:91],a[204:207],v[220:223],v[140:143]
TR a[166:167],v232 offset:46080
CV v73,v74,v75
MM v[88:91],a[200:203],v[204:207],v[88:91]
EX v76,v76
MM v[88:91],a[196:199],v[188:191],v[88:91]
EX v77,v77
MM v[88:91],a[192:195],v[172:175],v[88:91]
TR a[168:169],v233 offset:33792
CV v74,v76,v77
MM v[96:99],a[224:227],v[160:163],v[128:131]
EX v78,v78
MM v[96:99],a[228:231],v[176:179],v[96:99]
TR a[170:171],v233 offset:37888
TR a[172:173],v233 offset:41984
MM v[96:99],a[232:235],v[192:195],v[96:99]
EX v79,v79
MM v[96:99],a[236:239],v[208:211],v[96:99]
TR a[174:175],v233 offset:46080
CV v75,v78,v79
MM v[100:103],a[252:255],v[208:211],v[128:131]
EX v80,v80
MM v[100:103],a[248:251],v[192:195],v[100:103]
EX v81,v81
MM v[100:103],a[244:247],v[176:179],v[100:103]
TR a[176:177],v232 offset:34304
CV v80,v80,v81
MM v[100:103],a[240:243],v[160:163],v[100:103]
EX v82,v82
MM v[108:111],a[240:243],v[164:167],v[132:135]
buffer_store_dword v243, v248, s[20:23], s67 offen offset:12 sc1
MM v[108:111],a[244:247],v[180:183],v[108:111]
s_add_u32 s67, s67, 1024
TR a[178:179],v232 offset:38400
TR a[180:181],v232 offset:42496
MM v[108:111],a[248:251],v[196:199],v[108:111]
EX v83,v83
MM v[108:111],a[252:255],v[212:215],v[108:111]
TR a[182:183],v232 offset:46592
CV v81,v82,v83
MM v[104:107],a[236:239],v[212:215],v[132:135]
EX v84,v84
MM v[104:107],a[232:235],v[196:199],v[104:107]
EX v85,v85
MM v[104:107],a[228:231],v[180:183],v[104:107]
TR a[184:185],v233 offset:34304
CV v82,v84,v85
MM v[104:107],a[224:227],v[164:167],v[104:107]
EX v86,v86
MM v[112:115],a[224:227],v[168:171],v[136:139]
TR a[186:187],v233 offset:38400
TR a[188:189],v233 offset:42496
MM v[112:115],a[228:231],v[184:187],v[112:115]
EX v87,v87
MM v[112:115],a[232:235],v[200:203],v[112:115]
TR a[190:191],v233 offset:46592
CV v83,v86,v87
MM v[112:115],a[236:239],v[216:219],v[112:115]
EX v88,v88
MM v[116:119],a[252:255],v[216:219],v[136:139]
EX v89,v89
MM v[116:119],a[248:251],v[200:203],v[116:119]
EX v90,v90
MM v[116:119],a[244:247],v[184:187],v[116:119]
EX v91,v91
MM v[116:119],a[240:243],v[168:171],v[116:119]
EX v92,v92
MM v[124:127],a[240:243],v[172:175],v[140:143]
EX v93,v93
MM v[124:127],a[244:247],v[188:191],v[124:127]
EX v94,v94
MM v[124:127],a[248:251],v[204:207],v[124:127]
EX v95,v95
MM v[124:127],a[252:255],v[220:223],v[124:127]
EX v96,v96
MM v[120:123],a[236:239],v[220:223],v[140:143]
EX v97,v97
MM v[120:123],a[232:235],v[204:207],v[120:123]
EX v98,v98
MM v[120:123],a[228:231],v[188:191],v[120:123]
EX v99,v99
MM v[120:123],a[224:227],v[172:175],v[120:123]
s_waitcnt vmcnt(12) lgkmcnt(0)
s_barrier
MM a[0:3],a[128:131],v[0:3],a[0:3]
s_add_u32 m0, s65, 32768
CV v88,v88,v89
CV v89,v90,v91
MM a[0:3],a[132:135],v[4:7],a[0:3]
RD a[192:195],v224 offset:0
CV v90,v92,v93
MM a[8:11],a[132:135],v[12:15],a[8:11]
RD a[196:199],v225 offset:0
CV v91,v94,v95
MM a[8:11],a[128:131],v[8:11],a[8:11]
buffer_load_dwordx4 v228, s[8:11], s64 offen offset:0 lds
MM a[16:19],a[128:131],v[16:19],a[16:19]
RD a[200:203],v226 offset:0
CV v68,v96,v97
MM a[16:19],a[132:135],v[20:23],a[16:19]
RD a[204:207],v227 offset:0
CV v69,v98,v99
MM a[24:27],a[132:135],v[28:31],a[24:27]
buffer_load_dwordx4 v229, s[8:11], s64 offen offset:1024 lds
MM a[24:27],a[128:131],v[24:27],a[24:27]
RD a[208:211],v224 offset:4096
RD a[212:215],v225 offset:4096
MM a[28:31],a[136:139],v[24:27],a[28:31]
buffer_load_dwordx4 v230, s[8:11], s64 offen offset:2048 lds
MM a[28:31],a[140:143],v[28:31],a[28:31]
EX v100,v100
MM a[20:23],a[140:143],v[20:23],a[20:23]
EX v101,v101
MM a[20:23],a[136:139],v[16:19],a[20:23]
RD a[216:219],v226 offset:4096
CV v70,v100,v101
MM a[12:15],a[136:139],v[8:11],a[12:15]
EX v102,v102
MM a[12:15],a[140:143],v[12:15],a[12:15]
RD a[220:223],v227 offset:4096
RD a[224:227],v224 offset:8192
MM a[4:7],a[140:143],v[4:7],a[4:7]
EX v103,v103
MM a[4:7],a[136:139],v[0:3],a[4:7]
RD a[228:231],v225 offset:8192
CV v71,v102,v103
MM a[32:35],a[144:147],v[0:3],a[32:35]
EX v104,v104
MM a[32:35],a[148:151],v[4:7],a[32:35]
buffer_load_dwordx4 v231, s[8:11], s64 offen offset:3072 lds
MM a[40:43],a[148:151],v[12:15],a[40:43]
s_add_u32 m0, s65, 98304
EX v105,v105
MM a[40:43],a[144:147],v[8:11],a[40:43]
RD a[232:235],v226 offset:8192
CV v76,v104,v105
MM a[48:51],a[144:147],v[16:19],a[48:51]
EX v106,v106
MM a[48:51],a[148:151],v[20:23],a[48:51]
EX v107,v107
MM a[56:59],a[148:151],v[28:31],a[56:59]
RD a[236:239],v227 offset:8192
CV v77,v106,v107
MM a[56:59],a[144:147],v[24:27],a[56:59]
EX v108,v108
MM a[60:63],a[152:155],v[24:27],a[60:63]
buffer_load_dwordx4 v235, s[12:15], s81 offen offset:0 lds
MM a[60:63],a[156:159],v[28:31],a[60:63]
EX v109,v109
MM a[52:55],a[156:159],v[20:23],a[52:55]
RD a[240:243],v224 offset:12288
CV v78,v108,v109
MM a[52:55],a[152:155],v[16:19],a[52:55]
EX v110,v110
MM a[44:47],a[152:155],v[8:11],a[44:47]
EX v111,v111
MM a[44:47],a[156:159],v[12:15],a[44:47]
RD a[244:247],v225 offset:12288
CV v79,v110,v111
MM a[36:39],a[156:159],v[4:7],a[36:39]
buffer_load_dwordx4 v235, s[12:15], s64 offen offset:1024 lds
MM a[36:39],a[152:155],v[0:3],a[36:39]
EX v112,v112
MM a[64:67],a[160:163],v[0:3],a[64:67]
EX v113,v113
MM a[64:67],a[164:167],v[4:7],a[64:67]
RD a[248:251],v226 offset:12288
CV v84,v112,v113
MM a[72:75],a[164:167],v[12:15],a[72:75]
EX v114,v114
MM a[72:75],a[160:163],v[8:11],a[72:75]
LPG8_%=:
s_cbranch_execz LPG12_%=
EX v115,v115
MM a[80:83],a[160:163],v[16:19],a[80:83]
RD a[252:255],v227 offset:12288
CV v85,v114,v115
MM a[80:83],a[164:167],v[20:23],a[80:83]
buffer_load_dwordx4 v235, s[12:15], s81 offen offset:2048 lds
MM a[88:91],a[164:167],v[28:31],a[88:91]
EX v116,v116
MM a[88:91],a[160:163],v[24:27],a[88:91]
EX v117,v117
MM a[92:95],a[168:171],v[24:27],a[92:95]
EX v118,v118
MM a[92:95],a[172:175],v[28:31],a[92:95]
buffer_load_dwordx4 v235, s[12:15], s64 offen offset:3072 lds
MM a[84:87],a[172:175],v[20:23],a[84:87]
EX v119,v119
MM a[84:87],a[168:171],v[16:19],a[84:87]
EX v120,v120
MM a[76:79],a[168:171],v[8:11],a[76:79]
EX v121,v121
MM a[76:79],a[172:175],v[12:15],a[76:79]
EX v122,v122
MM a[68:71],a[172:175],v[4:7],a[68:71]
EX v123,v123
MM a[68:71],a[168:171],v[0:3],a[68:71]
EX v124,v124
MM a[96:99],a[176:179],v[0:3],a[96:99]
EX v125,v125
MM a[96:99],a[180:183],v[4:7],a[96:99]
EX v126,v126
MM a[104:107],a[180:183],v[12:15],a[104:107]
EX v127,v127
MM a[104:107],a[176:179],v[8:11],a[104:107]
CV v86,v116,v117
CV v87,v118,v119
MM a[112:115],a[176:179],v[16:19],a[112:115]
CV v92,v120,v121
CV v93,v122,v123
MM a[112:115],a[180:183],v[20:23],a[112:115]
CV v94,v124,v125
CV v95,v126,v127
MM a[120:123],a[180:183],v[28:31],a[120:123]
MM a[120:123],a[176:179],v[24:27],a[120:123]
MM a[124:127],a[184:187],v[24:27],a[124:127]
v_add_f32 v244, v144, v145
v_add_f32 v245, v148, v149
MM a[124:127],a[188:191],v[28:31],a[124:127]
v_add_f32 v246, v152, v153
v_add_f32 v247, v156, v157
MM a[116:119],a[188:191],v[20:23],a[116:119]
MM a[116:119],a[184:187],v[16:19],a[116:119]
MM a[108:111],a[184:187],v[8:11],a[108:111]
MM a[108:111],a[188:191],v[12:15],a[108:111]
MM a[100:103],a[188:191],v[4:7],a[100:103]
MM a[100:103],a[184:187],v[0:3],a[100:103]
v_smfmac_f32_16x16x64_bf16 v[144:147], v[236:239], v[64:71], v234
v_smfmac_f32_16x16x64_bf16 v[148:151], v[236:239], v[72:79], v234
v_smfmac_f32_16x16x64_bf16 v[152:155], v[236:239], v[80:87], v234
v_smfmac_f32_16x16x64_bf16 v[156:159], v[236:239], v[88:95], v234
s_waitcnt lgkmcnt(0)
MM v[0:3],a[192:195],v[160:163],v[128:131]
s_add_u32 s64, s64, 16384
TR a[128:129],v232 offset:49152
s_add_u32 s81, s64, 896
MM v[0:3],a[196:199],v[176:179],v[0:3]
TR a[130:131],v232 offset:53248
v_max3_f32 v249, v144, v145, v148
MM v[0:3],a[200:203],v[192:195],v[0:3]
TR a[132:133],v232 offset:57344
v_max3_f32 v249, v249, v149, v152
MM v[0:3],a[204:207],v[208:211],v[0:3]
TR a[134:135],v232 offset:61440
v_max3_f32 v249, v249, v153, v156
MM v[4:7],a[220:223],v[208:211],v[128:131]
TR a[136:137],v233 offset:49152
v_max_f32 v249, v249, v157
MM v[4:7],a[216:219],v[192:195],v[4:7]
TR a[138:139],v233 offset:53248
v_cmp_lt_f32 vcc, s71, v249
MM v[4:7],a[212:215],v[176:179],v[4:7]
TR a[140:141],v233 offset:57344
TR a[142:143],v233 offset:61440
MM v[4:7],a[208:211],v[160:163],v[4:7]
s_cbranch_vccnz LSITE7_%=
LBACK7_%=:
MM v[12:15],a[208:211],v[164:167],v[132:135]
EX v0,v0
MM v[12:15],a[212:215],v[180:183],v[12:15]
EX v1,v1
MM v[12:15],a[216:219],v[196:199],v[12:15]
TR a[144:145],v232 offset:49664
CV v0,v0,v1
MM v[12:15],a[220:223],v[212:215],v[12:15]
EX v2,v2
MM v[8:11],a[204:207],v[212:215],v[132:135]
EX v3,v3
MM v[8:11],a[200:203],v[196:199],v[8:11]
TR a[146:147],v232 offset:53760
CV v1,v2,v3
MM v[8:11],a[196:199],v[180:183],v[8:11]
EX v4,v4
MM v[8:11],a[192:195],v[164:167],v[8:11]
TR a[148:149],v232 offset:57856
TR a[150:151],v232 offset:61952
MM v[16:19],a[192:195],v[168:171],v[136:139]
EX v5,v5
MM v[16:19],a[196:199],v[184:187],v[16:19]
TR a[152:153],v233 offset:49664
CV v2,v4,v5
MM v[16:19],a[200:203],v[200:203],v[16:19]
EX v6,v6
MM v[16:19],a[204:207],v[216:219],v[16:19]
TR a[154:155],v233 offset:53760
TR a[156:157],v233 offset:57856
MM v[20:23],a[220:223],v[216:219],v[136:139]
EX v7,v7
MM v[20:23],a[216:219],v[200:203],v[20:23]
TR a[158:159],v233 offset:61952
CV v3,v6,v7
MM v[20:23],a[212:215],v[184:187],v[20:23]
EX v8,v8
MM v[20:23],a[208:211],v[168:171],v[20:23]
EX v9,v9
MM v[28:31],a[208:211],v[172:175],v[140:143]
TR a[160:161],v232 offset:50176
CV v8,v8,v9
MM v[28:31],a[212:215],v[188:191],v[28:31]
EX v10,v10
MM v[28:31],a[216:219],v[204:207],v[28:31]
TR a[162:163],v232 offset:54272
TR a[164:165],v232 offset:58368
MM v[28:31],a[220:223],v[220:223],v[28:31]
EX v11,v11
MM v[24:27],a[204:207],v[220:223],v[140:143]
TR a[166:167],v232 offset:62464
CV v9,v10,v11
MM v[24:27],a[200:203],v[204:207],v[24:27]
EX v12,v12
MM v[24:27],a[196:199],v[188:191],v[24:27]
EX v13,v13
MM v[24:27],a[192:195],v[172:175],v[24:27]
TR a[168:169],v233 offset:50176
CV v10,v12,v13
MM v[32:35],a[224:227],v[160:163],v[128:131]
EX v14,v14
MM v[32:35],a[228:231],v[176:179],v[32:35]
TR a[170:171],v233 offset:54272
TR a[172:173],v233 offset:58368
MM v[32:35],a[232:235],v[192:195],v[32:35]
EX v15,v15
MM v[32:35],a[236:239],v[208:211],v[32:35]
TR a[174:175],v233 offset:62464
CV v11,v14,v15
MM v[36:39],a[252:255],v[208:211],v[128:131]
EX v16,v16
MM v[36:39],a[248:251],v[192:195],v[36:39]
EX v17,v17
MM v[36:39],a[244:247],v[176:179],v[36:39]
TR a[176:177],v232 offset:50688
CV v16,v16,v17
MM v[36:39],a[240:243],v[160:163],v[36:39]
EX v18,v18
MM v[44:47],a[240:243],v[164:167],v[132:135]
TR a[178:179],v232 offset:54784
TR a[180:181],v232 offset:58880
MM v[44:47],a[244:247],v[180:183],v[44:47]
EX v19,v19
MM v[44:47],a[248:251],v[196:199],v[44:47]
TR a[182:183],v232 offset:62976
CV v17,v18,v19
MM v[44:47],a[252:255],v[212:215],v[44:47]
EX v20,v20
MM v[40:43],a[236:239],v[212:215],v[132:135]
TR a[184:185],v233 offset:50688
TR a[186:187],v233 offset:54784
MM v[40:43],a[232:235],v[196:199],v[40:43]
EX v21,v21
MM v[40:43],a[228:231],v[180:183],v[40:43]
TR a[188:189],v233 offset:58880
CV v18,v20,v21
MM v[40:43],a[224:227],v[164:167],v[40:43]
EX v22,v22
MM v[48:51],a[224:227],v[168:171],v[136:139]
)ASM"
R"ASM(
EX v23,v23
MM v[48:51],a[228:231],v[184:187],v[48:51]
TR a[190:191],v233 offset:62976
CV v19,v22,v23
MM v[48:51],a[232:235],v[200:203],v[48:51]
EX v24,v24
MM v[48:51],a[236:239],v[216:219],v[48:51]
EX v25,v25
MM v[52:55],a[252:255],v[216:219],v[136:139]
EX v26,v26
MM v[52:55],a[248:251],v[200:203],v[52:55]
EX v27,v27
MM v[52:55],a[244:247],v[184:187],v[52:55]
EX v28,v28
MM v[52:55],a[240:243],v[168:171],v[52:55]
EX v29,v29
MM v[60:63],a[240:243],v[172:175],v[140:143]
EX v30,v30
MM v[60:63],a[244:247],v[188:191],v[60:63]
EX v31,v31
MM v[60:63],a[248:251],v[204:207],v[60:63]
EX v32,v32
MM v[60:63],a[252:255],v[220:223],v[60:63]
EX v33,v33
MM v[56:59],a[236:239],v[220:223],v[140:143]
EX v34,v34
MM v[56:59],a[232:235],v[204:207],v[56:59]
EX v35,v35
MM v[56:59],a[228:231],v[188:191],v[56:59]
EX v36,v36
MM v[56:59],a[224:227],v[172:175],v[56:59]
s_waitcnt vmcnt(12) lgkmcnt(0)
s_barrier
MM a[0:3],a[128:131],v[64:67],a[0:3]
s_add_u32 m0, s65, 49152
CV v24,v24,v25
CV v25,v26,v27
MM a[0:3],a[132:135],v[68:71],a[0:3]
RD a[192:195],v224 offset:16384
CV v26,v28,v29
MM a[8:11],a[132:135],v[76:79],a[8:11]
RD a[196:199],v225 offset:16384
CV v27,v30,v31
MM a[8:11],a[128:131],v[72:75],a[8:11]
buffer_load_dwordx4 v228, s[8:11], s64 offen offset:0 lds
MM a[16:19],a[128:131],v[80:83],a[16:19]
RD a[200:203],v226 offset:16384
CV v4,v32,v33
MM a[16:19],a[132:135],v[84:87],a[16:19]
RD a[204:207],v227 offset:16384
CV v5,v34,v35
MM a[24:27],a[132:135],v[92:95],a[24:27]
buffer_load_dwordx4 v229, s[8:11], s64 offen offset:1024 lds
MM a[24:27],a[128:131],v[88:91],a[24:27]
RD a[208:211],v224 offset:20480
RD a[212:215],v225 offset:20480
MM a[28:31],a[136:139],v[88:91],a[28:31]
buffer_load_dwordx4 v230, s[8:11], s64 offen offset:2048 lds
MM a[28:31],a[140:143],v[92:95],a[28:31]
RD a[216:219],v226 offset:20480
RD a[220:223],v227 offset:20480
MM a[20:23],a[140:143],v[84:87],a[20:23]
EX v37,v37
MM a[20:23],a[136:139],v[80:83],a[20:23]
RD a[224:227],v224 offset:24576
CV v6,v36,v37
MM a[12:15],a[136:139],v[72:75],a[12:15]
EX v38,v38
MM a[12:15],a[140:143],v[76:79],a[12:15]
buffer_load_dwordx4 v231, s[8:11], s64 offen offset:3072 lds
MM a[4:7],a[140:143],v[68:71],a[4:7]
s_add_u32 m0, s65, 114688
EX v39,v39
MM a[4:7],a[136:139],v[64:67],a[4:7]
RD a[228:231],v225 offset:24576
CV v7,v38,v39
MM a[32:35],a[144:147],v[64:67],a[32:35]
EX v40,v40
MM a[32:35],a[148:151],v[68:71],a[32:35]
EX v41,v41
MM a[40:43],a[148:151],v[76:79],a[40:43]
RD a[232:235],v226 offset:24576
CV v12,v40,v41
MM a[40:43],a[144:147],v[72:75],a[40:43]
EX v42,v42
MM a[48:51],a[144:147],v[80:83],a[48:51]
buffer_load_dwordx4 v235, s[12:15], s81 offen offset:0 lds
MM a[48:51],a[148:151],v[84:87],a[48:51]
EX v43,v43
MM a[56:59],a[148:151],v[92:95],a[56:59]
RD a[236:239],v227 offset:24576
CV v13,v42,v43
MM a[56:59],a[144:147],v[88:91],a[56:59]
EX v44,v44
MM a[60:63],a[152:155],v[88:91],a[60:63]
EX v45,v45
MM a[60:63],a[156:159],v[92:95],a[60:63]
RD a[240:243],v224 offset:28672
CV v14,v44,v45
MM a[52:55],a[156:159],v[84:87],a[52:55]
EX v46,v46
MM a[52:55],a[152:155],v[80:83],a[52:55]
buffer_load_dwordx4 v235, s[12:15], s64 offen offset:1024 lds
MM a[44:47],a[152:155],v[72:75],a[44:47]
EX v47,v47
MM a[44:47],a[156:159],v[76:79],a[44:47]
RD a[244:247],v225 offset:28672
CV v15,v46,v47
MM a[36:39],a[156:159],v[68:71],a[36:39]
EX v48,v48
MM a[36:39],a[152:155],v[64:67],a[36:39]
EX v49,v49
MM a[64:67],a[160:163],v[64:67],a[64:67]
RD a[248:251],v226 offset:28672
CV v20,v48,v49
MM a[64:67],a[164:167],v[68:71],a[64:67]
EX v50,v50
MM a[72:75],a[164:167],v[76:79],a[72:75]
buffer_load_dwordx4 v235, s[12:15], s81 offen offset:2048 lds
MM a[72:75],a[160:163],v[72:75],a[72:75]
EX v51,v51
MM a[80:83],a[160:163],v[80:83],a[80:83]
RD a[252:255],v227 offset:28672
CV v21,v50,v51
MM a[80:83],a[164:167],v[84:87],a[80:83]
EX v52,v52
MM a[88:91],a[164:167],v[92:95],a[88:91]
EX v53,v53
MM a[88:91],a[160:163],v[88:91],a[88:91]
buffer_load_dwordx4 v235, s[12:15], s64 offen offset:3072 lds
MM a[92:95],a[168:171],v[88:91],a[92:95]
EX v54,v54
MM a[92:95],a[172:175],v[92:95],a[92:95]
EX v55,v55
MM a[84:87],a[172:175],v[84:87],a[84:87]
EX v56,v56
MM a[84:87],a[168:171],v[80:83],a[84:87]
EX v57,v57
MM a[76:79],a[168:171],v[72:75],a[76:79]
EX v58,v58
MM a[76:79],a[172:175],v[76:79],a[76:79]
EX v59,v59
MM a[68:71],a[172:175],v[68:71],a[68:71]
EX v60,v60
MM a[68:71],a[168:171],v[64:67],a[68:71]
EX v61,v61
MM a[96:99],a[176:179],v[64:67],a[96:99]
EX v62,v62
MM a[96:99],a[180:183],v[68:71],a[96:99]
EX v63,v63
MM a[104:107],a[180:183],v[76:79],a[104:107]
CV v22,v52,v53
CV v23,v54,v55
MM a[104:107],a[176:179],v[72:75],a[104:107]
CV v28,v56,v57
CV v29,v58,v59
MM a[112:115],a[176:179],v[80:83],a[112:115]
CV v30,v60,v61
CV v31,v62,v63
MM a[112:115],a[180:183],v[84:87],a[112:115]
MM a[120:123],a[180:183],v[92:95],a[120:123]
MM a[120:123],a[176:179],v[88:91],a[120:123]
MM a[124:127],a[184:187],v[88:91],a[124:127]
v_add_f32 v244, v144, v145
v_add_f32 v245, v148, v149
MM a[124:127],a[188:191],v[92:95],a[124:127]
v_add_f32 v246, v152, v153
v_add_f32 v247, v156, v157
MM a[116:119],a[188:191],v[84:87],a[116:119]
s_cmp_eq_u32 s70, 2
s_cbranch_scc1 LIP_%=
LLALIP_%=:
MM a[116:119],a[184:187],v[80:83],a[116:119]
MM a[108:111],a[184:187],v[72:75],a[108:111]
MM a[108:111],a[188:191],v[76:79],a[108:111]
MM a[100:103],a[188:191],v[68:71],a[100:103]
MM a[100:103],a[184:187],v[64:67],a[100:103]
v_smfmac_f32_16x16x64_bf16 v[144:147], v[236:239], v[0:7], v234
v_smfmac_f32_16x16x64_bf16 v[148:151], v[236:239], v[8:15], v234
v_smfmac_f32_16x16x64_bf16 v[152:155], v[236:239], v[16:23], v234
v_smfmac_f32_16x16x64_bf16 v[156:159], v[236:239], v[24:31], v234
s_waitcnt lgkmcnt(0)
MM v[64:67],a[192:195],v[160:163],v[128:131]
s_add_u32 s64, s64, 16384
TR a[128:129],v232 offset:0
s_and_b32 s64, s64, 524287
MM v[64:67],a[196:199],v[176:179],v[64:67]
s_add_u32 s81, s64, 896
TR a[130:131],v232 offset:4096
v_max3_f32 v249, v144, v145, v148
MM v[64:67],a[200:203],v[192:195],v[64:67]
TR a[132:133],v232 offset:8192
v_max3_f32 v249, v249, v149, v152
MM v[64:67],a[204:207],v[208:211],v[64:67]
TR a[134:135],v232 offset:12288
v_max3_f32 v249, v249, v153, v156
MM v[68:71],a[220:223],v[208:211],v[128:131]
TR a[136:137],v233 offset:0
v_max_f32 v249, v249, v157
MM v[68:71],a[216:219],v[192:195],v[68:71]
TR a[138:139],v233 offset:4096
v_cmp_lt_f32 vcc, s71, v249
MM v[68:71],a[212:215],v[176:179],v[68:71]
TR a[140:141],v233 offset:8192
TR a[142:143],v233 offset:12288
MM v[68:71],a[208:211],v[160:163],v[68:71]
s_cbranch_vccnz LSITE8_%=
LBACK8_%=:
MM v[76:79],a[208:211],v[164:167],v[132:135]
EX v64,v64
MM v[76:79],a[212:215],v[180:183],v[76:79]
EX v65,v65
MM v[76:79],a[216:219],v[196:199],v[76:79]
TR a[144:145],v232 offset:512
CV v64,v64,v65
MM v[76:79],a[220:223],v[212:215],v[76:79]
EX v66,v66
MM v[72:75],a[204:207],v[212:215],v[132:135]
EX v67,v67
MM v[72:75],a[200:203],v[196:199],v[72:75]
TR a[146:147],v232 offset:4608
CV v65,v66,v67
MM v[72:75],a[196:199],v[180:183],v[72:75]
EX v68,v68
MM v[72:75],a[192:195],v[164:167],v[72:75]
TR a[148:149],v232 offset:8704
TR a[150:151],v232 offset:12800
MM v[80:83],a[192:195],v[168:171],v[136:139]
EX v69,v69
MM v[80:83],a[196:199],v[184:187],v[80:83]
TR a[152:153],v233 offset:512
CV v66,v68,v69
MM v[80:83],a[200:203],v[200:203],v[80:83]
EX v70,v70
MM v[80:83],a[204:207],v[216:219],v[80:83]
TR a[154:155],v233 offset:4608
TR a[156:157],v233 offset:8704
MM v[84:87],a[220:223],v[216:219],v[136:139]
EX v71,v71
MM v[84:87],a[216:219],v[200:203],v[84:87]
TR a[158:159],v233 offset:12800
CV v67,v70,v71
MM v[84:87],a[212:215],v[184:187],v[84:87]
EX v72,v72
MM v[84:87],a[208:211],v[168:171],v[84:87]
EX v73,v73
MM v[92:95],a[208:211],v[172:175],v[140:143]
TR a[160:161],v232 offset:1024
CV v72,v72,v73
MM v[92:95],a[212:215],v[188:191],v[92:95]
EX v74,v74
MM v[92:95],a[216:219],v[204:207],v[92:95]
TR a[162:163],v232 offset:5120
TR a[164:165],v232 offset:9216
MM v[92:95],a[220:223],v[220:223],v[92:95]
EX v75,v75
MM v[88:91],a[204:207],v[220:223],v[140:143]
TR a[166:167],v232 offset:13312
CV v73,v74,v75
MM v[88:91],a[200:203],v[204:207],v[88:91]
EX v76,v76
MM v[88:91],a[196:199],v[188:191],v[88:91]
EX v77,v77
MM v[88:91],a[192:195],v[172:175],v[88:91]
TR a[168:169],v233 offset:1024
CV v74,v76,v77
MM v[96:99],a[224:227],v[160:163],v[128:131]
EX v78,v78
MM v[96:99],a[228:231],v[176:179],v[96:99]
TR a[170:171],v233 offset:5120
TR a[172:173],v233 offset:9216
MM v[96:99],a[232:235],v[192:195],v[96:99]
EX v79,v79
MM v[96:99],a[236:239],v[208:211],v[96:99]
TR a[174:175],v233 offset:13312
CV v75,v78,v79
MM v[100:103],a[252:255],v[208:211],v[128:131]
EX v80,v80
MM v[100:103],a[248:251],v[192:195],v[100:103]
EX v81,v81
MM v[100:103],a[244:247],v[176:179],v[100:103]
TR a[176:177],v232 offset:1536
CV v80,v80,v81
MM v[100:103],a[240:243],v[160:163],v[100:103]
EX v82,v82
MM v[108:111],a[240:243],v[164:167],v[132:135]
TR a[178:179],v232 offset:5632
TR a[180:181],v232 offset:9728
MM v[108:111],a[244:247],v[180:183],v[108:111]
EX v83,v83
MM v[108:111],a[248:251],v[196:199],v[108:111]
TR a[182:183],v232 offset:13824
CV v81,v82,v83
MM v[108:111],a[252:255],v[212:215],v[108:111]
EX v84,v84
MM v[104:107],a[236:239],v[212:215],v[132:135]
TR a[184:185],v233 offset:1536
TR a[186:187],v233 offset:5632
MM v[104:107],a[232:235],v[196:199],v[104:107]
EX v85,v85
MM v[104:107],a[228:231],v[180:183],v[104:107]
TR a[188:189],v233 offset:9728
CV v82,v84,v85
MM v[104:107],a[224:227],v[164:167],v[104:107]
EX v86,v86
MM v[112:115],a[224:227],v[168:171],v[136:139]
EX v87,v87
MM v[112:115],a[228:231],v[184:187],v[112:115]
TR a[190:191],v233 offset:13824
CV v83,v86,v87
MM v[112:115],a[232:235],v[200:203],v[112:115]
EX v88,v88
MM v[112:115],a[236:239],v[216:219],v[112:115]
EX v89,v89
MM v[116:119],a[252:255],v[216:219],v[136:139]
EX v90,v90
MM v[116:119],a[248:251],v[200:203],v[116:119]
EX v91,v91
MM v[116:119],a[244:247],v[184:187],v[116:119]
EX v92,v92
MM v[116:119],a[240:243],v[168:171],v[116:119]
EX v93,v93
MM v[124:127],a[240:243],v[172:175],v[140:143]
EX v94,v94
MM v[124:127],a[244:247],v[188:191],v[124:127]
EX v95,v95
MM v[124:127],a[248:251],v[204:207],v[124:127]
EX v96,v96
MM v[124:127],a[252:255],v[220:223],v[124:127]
EX v97,v97
MM v[120:123],a[236:239],v[220:223],v[140:143]
EX v98,v98
MM v[120:123],a[232:235],v[204:207],v[120:123]
EX v99,v99
MM v[120:123],a[228:231],v[188:191],v[120:123]
s_cmp_eq_u32 s70, 1
s_cbranch_scc1 LTP_%=
LLALTP_%=:
MM v[120:123],a[224:227],v[172:175],v[120:123]
s_waitcnt vmcnt(12) lgkmcnt(0)
s_barrier
MM a[0:3],a[128:131],v[0:3],a[0:3]
s_add_u32 m0, s65, 0
CV v88,v88,v89
CV v89,v90,v91
MM a[0:3],a[132:135],v[4:7],a[0:3]
RD a[192:195],v224 offset:32768
CV v90,v92,v93
MM a[8:11],a[132:135],v[12:15],a[8:11]
RD a[196:199],v225 offset:32768
CV v91,v94,v95
MM a[8:11],a[128:131],v[8:11],a[8:11]
buffer_load_dwordx4 v228, s[8:11], s64 offen offset:0 lds
MM a[16:19],a[128:131],v[16:19],a[16:19]
RD a[200:203],v226 offset:32768
CV v68,v96,v97
MM a[16:19],a[132:135],v[20:23],a[16:19]
RD a[204:207],v227 offset:32768
CV v69,v98,v99
MM a[24:27],a[132:135],v[28:31],a[24:27]
buffer_load_dwordx4 v229, s[8:11], s64 offen offset:1024 lds
MM a[24:27],a[128:131],v[24:27],a[24:27]
RD a[208:211],v224 offset:36864
RD a[212:215],v225 offset:36864
MM a[28:31],a[136:139],v[24:27],a[28:31]
buffer_load_dwordx4 v230, s[8:11], s64 offen offset:2048 lds
MM a[28:31],a[140:143],v[28:31],a[28:31]
EX v100,v100
MM a[20:23],a[140:143],v[20:23],a[20:23]
EX v101,v101
MM a[20:23],a[136:139],v[16:19],a[20:23]
RD a[216:219],v226 offset:36864
CV v70,v100,v101
MM a[12:15],a[136:139],v[8:11],a[12:15]
EX v102,v102
MM a[12:15],a[140:143],v[12:15],a[12:15]
RD a[220:223],v227 offset:36864
RD a[224:227],v224 offset:40960
MM a[4:7],a[140:143],v[4:7],a[4:7]
EX v103,v103
MM a[4:7],a[136:139],v[0:3],a[4:7]
RD a[228:231],v225 offset:40960
CV v71,v102,v103
MM a[32:35],a[144:147],v[0:3],a[32:35]
EX v104,v104
MM a[32:35],a[148:151],v[4:7],a[32:35]
buffer_load_dwordx4 v231, s[8:11], s64 offen offset:3072 lds
MM a[40:43],a[148:151],v[12:15],a[40:43]
s_add_u32 m0, s65, 65536
EX v105,v105
)ASM"
R"ASM(
MM a[40:43],a[144:147],v[8:11],a[40:43]
RD a[232:235],v226 offset:40960
CV v76,v104,v105
MM a[48:51],a[144:147],v[16:19],a[48:51]
EX v106,v106
MM a[48:51],a[148:151],v[20:23],a[48:51]
EX v107,v107
MM a[56:59],a[148:151],v[28:31],a[56:59]
RD a[236:239],v227 offset:40960
CV v77,v106,v107
MM a[56:59],a[144:147],v[24:27],a[56:59]
EX v108,v108
MM a[60:63],a[152:155],v[24:27],a[60:63]
buffer_load_dwordx4 v235, s[12:15], s81 offen offset:0 lds
MM a[60:63],a[156:159],v[28:31],a[60:63]
EX v109,v109
s_nop 0
LPG9_%=:
s_cbranch_execz LTR0_%=
MM a[52:55],a[156:159],v[20:23],a[52:55]
RD a[240:243],v224 offset:45056
CV v78,v108,v109
MM a[52:55],a[152:155],v[16:19],a[52:55]
EX v110,v110
MM a[44:47],a[152:155],v[8:11],a[44:47]
EX v111,v111
MM a[44:47],a[156:159],v[12:15],a[44:47]
RD a[244:247],v225 offset:45056
CV v79,v110,v111
MM a[36:39],a[156:159],v[4:7],a[36:39]
EX v112,v112
MM a[36:39],a[152:155],v[0:3],a[36:39]
buffer_load_dwordx4 v235, s[12:15], s64 offen offset:1024 lds
MM a[64:67],a[160:163],v[0:3],a[64:67]
EX v113,v113
MM a[64:67],a[164:167],v[4:7],a[64:67]
RD a[248:251],v226 offset:45056
CV v84,v112,v113
MM a[72:75],a[164:167],v[12:15],a[72:75]
EX v114,v114
MM a[72:75],a[160:163],v[8:11],a[72:75]
EX v115,v115
MM a[80:83],a[160:163],v[16:19],a[80:83]
RD a[252:255],v227 offset:45056
CV v85,v114,v115
MM a[80:83],a[164:167],v[20:23],a[80:83]
buffer_load_dwordx4 v235, s[12:15], s81 offen offset:2048 lds
MM a[88:91],a[164:167],v[28:31],a[88:91]
EX v116,v116
MM a[88:91],a[160:163],v[24:27],a[88:91]
EX v117,v117
MM a[92:95],a[168:171],v[24:27],a[92:95]
EX v118,v118
MM a[92:95],a[172:175],v[28:31],a[92:95]
buffer_load_dwordx4 v235, s[12:15], s64 offen offset:3072 lds
MM a[84:87],a[172:175],v[20:23],a[84:87]
EX v119,v119
MM a[84:87],a[168:171],v[16:19],a[84:87]
EX v120,v120
MM a[76:79],a[168:171],v[8:11],a[76:79]
EX v121,v121
MM a[76:79],a[172:175],v[12:15],a[76:79]
EX v122,v122
MM a[68:71],a[172:175],v[4:7],a[68:71]
EX v123,v123
MM a[68:71],a[168:171],v[0:3],a[68:71]
EX v124,v124
MM a[96:99],a[176:179],v[0:3],a[96:99]
EX v125,v125
MM a[96:99],a[180:183],v[4:7],a[96:99]
EX v126,v126
MM a[104:107],a[180:183],v[12:15],a[104:107]
EX v127,v127
MM a[104:107],a[176:179],v[8:11],a[104:107]
CV v86,v116,v117
CV v87,v118,v119
MM a[112:115],a[176:179],v[16:19],a[112:115]
CV v92,v120,v121
CV v93,v122,v123
MM a[112:115],a[180:183],v[20:23],a[112:115]
CV v94,v124,v125
CV v95,v126,v127
MM a[120:123],a[180:183],v[28:31],a[120:123]
MM a[120:123],a[176:179],v[24:27],a[120:123]
MM a[124:127],a[184:187],v[24:27],a[124:127]
v_add_f32 v244, v144, v145
v_add_f32 v245, v148, v149
MM a[124:127],a[188:191],v[28:31],a[124:127]
v_add_f32 v246, v152, v153
v_add_f32 v247, v156, v157
MM a[116:119],a[188:191],v[20:23],a[116:119]
s_cmp_eq_u32 s70, s82
s_cbranch_scc1 LWV_%=
LLALWV_%=:
MM a[116:119],a[184:187],v[16:19],a[116:119]
MM a[108:111],a[184:187],v[8:11],a[108:111]
MM a[108:111],a[188:191],v[12:15],a[108:111]
MM a[100:103],a[188:191],v[4:7],a[100:103]
MM a[100:103],a[184:187],v[0:3],a[100:103]
v_smfmac_f32_16x16x64_bf16 v[144:147], v[236:239], v[64:71], v234
v_smfmac_f32_16x16x64_bf16 v[148:151], v[236:239], v[72:79], v234
v_smfmac_f32_16x16x64_bf16 v[152:155], v[236:239], v[80:87], v234
v_smfmac_f32_16x16x64_bf16 v[156:159], v[236:239], v[88:95], v234
s_waitcnt lgkmcnt(0)
s_sub_u32 s70, s70, 1
s_cmp_lg_u32 s70, 0
s_cbranch_scc1 LLAP_%=
s_branch LLAJ_%=
LWW_%=:
v_lshlrev_b32 v250, 3, v248
v_min_u32 v250, 4992, v250
s_sub_u32 s91, 5242880, s26
s_sub_u32 s60, s24, s91
s_subb_u32 s61, s25, 0
s_mov_b32 s62, 5242880
s_mov_b32 s63, 131072
s_lshl_b32 s90, s16, 2
s_add_u32 s90, s90, %[swave]
s_mul_i32 s90, s90, 10240
s_add_u32 s90, s90, 0
s_add_u32 s91, %[slds], 135168
s_mov_b32 m0, s91
s_nop 0
buffer_load_dword v250, s[60:63], s90 offen offset:0 lds
s_branch LLALWW_%=
LIP_%=:
s_lshr_b32 s77, %[sb], 3
s_lshl_b32 s77, s77, 2
s_add_u32 s77, s77, %[swave]
s_lshl_b32 s77, s77, 9
s_add_u32 s77, s77, LLAJ_%=-LPDP_%=
v_lshrrev_b32 v253, 1, v248
v_add_u32 v253, s77, v253
s_mov_b32 s77, LKEND_ka-LPDP_%=-16
v_min_u32 v253, s77, v253
s_getpc_b64 s[98:99]
LPDP_%=:
global_load_dword v253, v253, s[98:99]
s_branch LLALIP_%=
LTP_%=:
s_getpc_b64 s[98:99]
LTA1_%=:
s_min_u32 s73, %[swave], 3
s_lshl_b32 s73, s73, 12
s_add_u32 s73, s73, LPG23_%=-LTA1_%=
s_add_u32 s98, s98, s73
s_addc_u32 s99, s99, 0
s_mov_b64 exec, 0
s_setpc_b64 s[98:99]
LTR1_%=:
s_mov_b64 exec, -1
s_branch LLALTP_%=
LWV_%=:
v_lshlrev_b32 v250, 3, v248
v_min_u32 v250, 4992, v250
s_sub_u32 s91, 5242880, s26
s_sub_u32 s60, s24, s91
s_subb_u32 s61, s25, 0
s_mov_b32 s62, 5242880
s_mov_b32 s63, 131072
s_lshl_b32 s90, s16, 2
s_add_u32 s90, s90, %[swave]
s_mul_i32 s90, s90, 10240
s_add_u32 s90, s90, 5120
s_add_u32 s91, %[slds], 135168
s_mov_b32 m0, s91
s_nop 0
buffer_load_dword v250, s[60:63], s90 offen offset:0 lds
s_branch LLALWV_%=
s_nop 0
s_nop 0
s_nop 0
s_nop 0
s_nop 0
s_nop 0
s_nop 0
s_nop 0
s_nop 0
s_nop 0
s_nop 0
s_nop 0
s_nop 0
s_nop 0
s_nop 0
s_nop 0
s_nop 0
s_nop 0
s_nop 0
s_nop 0
s_nop 0
s_nop 0
s_nop 0
s_nop 0
s_nop 0
s_nop 0
s_nop 0
s_nop 0
s_nop 0
s_nop 0
s_nop 0
s_nop 0
s_nop 0
s_nop 0
s_nop 0
s_nop 0
s_nop 0
s_nop 0
s_nop 0
s_nop 0
s_nop 0
s_nop 0
s_nop 0
s_nop 0
s_nop 0
s_nop 0
s_nop 0
s_nop 0
s_nop 0
s_nop 0
s_nop 0
s_nop 0
s_nop 0
s_nop 0
s_nop 0
s_nop 0
s_nop 0
s_nop 0
s_nop 0
s_nop 0
s_nop 0
s_nop 0
s_nop 0
s_nop 0
s_nop 0
s_nop 0
s_nop 0
s_nop 0
s_nop 0
s_nop 0
s_nop 0
s_nop 0
s_nop 0
s_nop 0
s_nop 0
s_nop 0
s_nop 0
s_nop 0
s_nop 0
s_nop 0
s_nop 0
s_nop 0
s_nop 0
s_nop 0
s_nop 0
s_nop 0
s_nop 0
s_nop 0
s_nop 0
s_nop 0
s_nop 0
s_nop 0
s_nop 0
s_nop 0
s_nop 0
s_nop 0
s_nop 0
s_nop 0
s_nop 0
s_nop 0
s_nop 0
s_nop 0
s_nop 0
s_nop 0
s_nop 0
s_nop 0
s_nop 0
s_nop 0
s_nop 0
s_nop 0
s_nop 0
s_nop 0
s_nop 0
s_nop 0
s_nop 0
s_nop 0
s_nop 0
s_nop 0
s_nop 0
s_nop 0
s_nop 0
s_nop 0
s_nop 0
s_nop 0
s_nop 0
s_nop 0
s_nop 0
s_nop 0
s_nop 0
s_nop 0
s_nop 0
s_nop 0
s_nop 0
s_nop 0
s_nop 0
s_nop 0
s_nop 0
s_nop 0
s_nop 0
s_nop 0
s_nop 0
s_nop 0
s_nop 0
s_nop 0
s_nop 0
s_nop 0
s_nop 0
s_nop 0
s_nop 0
s_nop 0
s_nop 0
s_nop 0
s_nop 0
s_nop 0
s_nop 0
s_nop 0
s_nop 0
s_nop 0
s_nop 0
s_nop 0
s_nop 0
s_nop 0
s_nop 0
s_nop 0
s_nop 0
s_nop 0
s_nop 0
s_nop 0
s_nop 0
s_nop 0
s_nop 0
s_nop 0
s_nop 0
s_nop 0
s_nop 0
s_nop 0
s_nop 0
s_nop 0
s_nop 0
s_nop 0
s_nop 0
s_nop 0
s_nop 0
s_nop 0
s_nop 0
s_nop 0
s_nop 0
s_nop 0
s_nop 0
s_nop 0
s_nop 0
s_nop 0
s_nop 0
s_nop 0
s_nop 0
s_nop 0
s_nop 0
s_nop 0
s_nop 0
s_nop 0
s_nop 0
s_nop 0
s_nop 0
s_nop 0
s_nop 0
s_nop 0
s_nop 0
s_nop 0
s_nop 0
s_nop 0
s_nop 0
s_nop 0
s_nop 0
s_nop 0
s_nop 0
s_nop 0
s_nop 0
s_nop 0
s_nop 0
s_nop 0
s_nop 0
s_nop 0
s_nop 0
s_nop 0
s_nop 0
s_nop 0
s_nop 0
s_nop 0
s_nop 0
s_nop 0
s_nop 0
s_nop 0
s_nop 0
s_nop 0
s_nop 0
s_nop 0
s_nop 0
s_nop 0
s_nop 0
s_nop 0
s_nop 0
s_nop 0
s_nop 0
s_nop 0
s_nop 0
s_nop 0
s_nop 0
s_nop 0
s_nop 0
s_nop 0
s_nop 0
s_nop 0
s_nop 0
s_nop 0
s_nop 0
s_nop 0
s_nop 0
s_nop 0
s_nop 0
s_nop 0
s_nop 0
s_nop 0
s_nop 0
s_nop 0
s_nop 0
s_nop 0
s_nop 0
s_nop 0
s_nop 0
s_nop 0
s_nop 0
s_nop 0
s_nop 0
s_nop 0
s_nop 0
s_nop 0
s_nop 0
s_nop 0
s_nop 0
s_nop 0
s_nop 0
s_nop 0
s_nop 0
s_nop 0
s_nop 0
s_nop 0
s_nop 0
s_nop 0
s_nop 0
s_nop 0
s_nop 0
s_nop 0
s_nop 0
s_nop 0
s_nop 0
s_nop 0
s_nop 0
s_nop 0
s_nop 0
s_nop 0
s_nop 0
s_nop 0
s_nop 0
s_nop 0
s_nop 0
s_nop 0
s_nop 0
s_nop 0
s_nop 0
s_nop 0
s_nop 0
s_nop 0
s_nop 0
s_nop 0
s_nop 0
s_nop 0
s_nop 0
s_nop 0
s_nop 0
s_nop 0
s_nop 0
s_nop 0
s_nop 0
s_nop 0
s_nop 0
s_nop 0
s_nop 0
s_nop 0
s_nop 0
s_nop 0
s_nop 0
s_nop 0
s_nop 0
s_nop 0
s_nop 0
s_nop 0
s_nop 0
s_nop 0
s_nop 0
s_nop 0
s_nop 0
s_nop 0
s_nop 0
s_nop 0
s_nop 0
s_nop 0
s_nop 0
s_nop 0
s_nop 0
s_nop 0
s_nop 0
s_nop 0
s_nop 0
s_nop 0
s_nop 0
s_nop 0
s_nop 0
s_nop 0
s_nop 0
s_nop 0
s_nop 0
s_nop 0
s_nop 0
s_nop 0
s_nop 0
s_nop 0
s_nop 0
s_nop 0
s_nop 0
s_nop 0
s_nop 0
s_nop 0
s_nop 0
s_nop 0
s_nop 0
s_nop 0
LLAR_%=:
MM v[0:3],a[192:195],v[160:163],v[128:131]
s_add_u32 s64, s64, 16384
TR a[128:129],v232 offset:16384
s_add_u32 s81, s64, 896
MM v[0:3],a[196:199],v[176:179],v[0:3]
TR a[130:131],v232 offset:20480
v_max3_f32 v249, v144, v145, v148
MM v[0:3],a[200:203],v[192:195],v[0:3]
TR a[132:133],v232 offset:24576
v_max3_f32 v249, v249, v149, v152
MM v[0:3],a[204:207],v[208:211],v[0:3]
TR a[134:135],v232 offset:28672
v_max3_f32 v249, v249, v153, v156
MM v[4:7],a[220:223],v[208:211],v[128:131]
TR a[136:137],v233 offset:16384
v_max_f32 v249, v249, v157
MM v[4:7],a[216:219],v[192:195],v[4:7]
TR a[138:139],v233 offset:20480
v_cmp_lt_f32 vcc, s71, v249
MM v[4:7],a[212:215],v[176:179],v[4:7]
TR a[140:141],v233 offset:24576
TR a[142:143],v233 offset:28672
MM v[4:7],a[208:211],v[160:163],v[4:7]
s_cbranch_vccnz LSITE9_%=
LBACK9_%=:
MM v[12:15],a[208:211],v[164:167],v[132:135]
s_lshl_b32 s17, s16, 2
EX v0,v0
MM v[12:15],a[212:215],v[180:183],v[12:15]
s_add_u32 s17, s17, %[swave]
EX v1,v1
MM v[12:15],a[216:219],v[196:199],v[12:15]
s_sub_u32 s90, s16, 108
TR a[144:145],v232 offset:16896
CV v0,v0,v1
MM v[12:15],a[220:223],v[212:215],v[12:15]
s_min_u32 s90, s90, s16
EX v2,v2
MM v[8:11],a[204:207],v[212:215],v[132:135]
s_lshl_b32 s48, s90, 5
EX v3,v3
MM v[8:11],a[200:203],v[196:199],v[8:11]
s_add_u32 s76, %[slds], 155648
v_add_u32 v250, s76, v248
CV v1,v2,v3
MM v[8:11],a[196:199],v[180:183],v[8:11]
s_lshl_b32 s77, s17, 4
TR a[146:147],v232 offset:20992
TR a[148:149],v232 offset:25088
MM v[8:11],a[192:195],v[164:167],v[8:11]
s_add_u32 s76, s76, s77
TR a[150:151],v232 offset:29184
v_mov_b32 v251, s76
MM v[16:19],a[192:195],v[168:171],v[136:139]
RD v[124:127],v251 offset:0
RD v[96:99],v250 offset:0
MM v[16:19],a[196:199],v[184:187],v[16:19]
RD v[100:103],v250 offset:1024
RD v[104:107],v250 offset:2048
MM v[16:19],a[200:203],v[200:203],v[16:19]
RD v[108:111],v250 offset:3072
RD v[112:115],v250 offset:4096
MM v[16:19],a[204:207],v[216:219],v[16:19]
RD v[116:119],v250 offset:5120
RD v[120:123],v250 offset:6144
MM v[20:23],a[220:223],v[216:219],v[136:139]
EX v4,v4
MM v[20:23],a[216:219],v[200:203],v[20:23]
EX v5,v5
MM v[20:23],a[212:215],v[184:187],v[20:23]
s_waitcnt lgkmcnt(7)
v_readfirstlane_b32 s52, v124
v_readfirstlane_b32 s53, v125
MM v[20:23],a[208:211],v[168:171],v[20:23]
v_readfirstlane_b32 s54, v126
v_readfirstlane_b32 s55, v127
EX v6,v6
MM v[28:31],a[208:211],v[172:175],v[140:143]
RD v[124:127],v250 offset:7168
TR a[152:153],v233 offset:16896
MM v[28:31],a[212:215],v[188:191],v[28:31]
s_waitcnt lgkmcnt(2)
v_max3_u32 v253, v96, v97, v98
v_max3_u32 v251, v99, v100, v101
EX v7,v7
MM v[28:31],a[216:219],v[204:207],v[28:31]
v_max3_u32 v253, v253, v102, v103
v_max3_u32 v251, v251, v104, v105
MM v[28:31],a[220:223],v[220:223],v[28:31]
v_max3_u32 v253, v253, v106, v107
v_max3_u32 v251, v251, v108, v109
EX v8,v8
MM v[24:27],a[204:207],v[220:223],v[140:143]
v_max3_u32 v253, v253, v110, v111
v_max3_u32 v251, v251, v112, v113
MM v[24:27],a[200:203],v[204:207],v[24:27]
v_max3_u32 v253, v253, v114, v115
v_max3_u32 v251, v251, v116, v117
EX v9,v9
MM v[24:27],a[196:199],v[188:191],v[24:27]
TR a[154:155],v233 offset:20992
v_max3_u32 v253, v253, v118, v119
v_max3_u32 v251, v251, v120, v121
MM v[24:27],a[192:195],v[172:175],v[24:27]
v_max3_u32 v253, v253, v122, v123
CV v2,v4,v5
EX v10,v10
MM v[32:35],a[224:227],v[160:163],v[128:131]
s_waitcnt lgkmcnt(2)
TR a[156:157],v233 offset:25088
v_max3_u32 v251, v251, v124, v125
v_max3_u32 v253, v253, v126, v127
EX v11,v11
MM v[32:35],a[228:231],v[176:179],v[32:35]
v_max_u32 v253, v253, v251
v_cmp_lt_u32 s[60:61], s52, v253
MM v[32:35],a[232:235],v[192:195],v[32:35]
TR a[158:159],v233 offset:29184
v_cmp_lt_u32 s[62:63], s53, v253
v_cmp_lt_u32 s[56:57], s54, v253
EX v12,v12
MM v[32:35],a[236:239],v[208:211],v[32:35]
v_cmp_lt_u32 s[58:59], s55, v253
CV v3,v6,v7
MM v[36:39],a[252:255],v[208:211],v[128:131]
TR a[160:161],v232 offset:17408
EX v13,v13
MM v[36:39],a[248:251],v[192:195],v[36:39]
s_bcnt1_i32_b64 s60, s[60:61]
TR a[162:163],v232 offset:21504
CV v8,v8,v9
MM v[36:39],a[244:247],v[176:179],v[36:39]
s_bcnt1_i32_b64 s62, s[62:63]
EX v14,v14
MM v[36:39],a[240:243],v[160:163],v[36:39]
s_bcnt1_i32_b64 s56, s[56:57]
)ASM"
R"ASM(
TR a[164:165],v232 offset:25600
TR a[166:167],v232 offset:29696
MM v[44:47],a[240:243],v[164:167],v[132:135]
s_bcnt1_i32_b64 s58, s[58:59]
EX v15,v15
MM v[44:47],a[244:247],v[180:183],v[44:47]
s_min_u32 s76, s60, s62
EX v16,v16
MM v[44:47],a[248:251],v[196:199],v[44:47]
s_min_u32 s77, s56, s58
TR a[168:169],v233 offset:17408
CV v9,v10,v11
MM v[44:47],a[252:255],v[212:215],v[44:47]
s_min_u32 s76, s76, s77
s_cmp_lt_u32 s76, 8
s_cbranch_scc1 LRK_%=
LRKb_%=:
EX v17,v17
MM v[40:43],a[236:239],v[212:215],v[132:135]
v_lshrrev_b32 v252, 6, v248
v_mul_u32_u24 v252, 3456, v252
MM v[40:43],a[232:235],v[196:199],v[40:43]
TR a[170:171],v233 offset:21504
v_and_b32 v251, 48, v248
v_add_u32 v252, v252, v251
EX v18,v18
MM v[40:43],a[228:231],v[180:183],v[40:43]
s_and_b32 s90, %[swave], 1
TR a[172:173],v233 offset:25600
CV v10,v12,v13
MM v[40:43],a[224:227],v[164:167],v[40:43]
s_mul_i32 s90, s90, 276480
EX v19,v19
MM v[48:51],a[224:227],v[168:171],v[136:139]
s_bfe_u32 s91, s48, 131077
TR a[174:175],v233 offset:29696
CV v11,v14,v15
MM v[48:51],a[228:231],v[184:187],v[48:51]
s_mul_i32 s91, s91, 552960
EX v20,v20
MM v[48:51],a[232:235],v[200:203],v[48:51]
s_add_u32 s90, s90, s91
TR a[176:177],v232 offset:17920
TR a[178:179],v232 offset:22016
MM v[48:51],a[236:239],v[216:219],v[48:51]
s_and_b32 s91, s48, 0xffffff80
EX v21,v21
MM v[52:55],a[252:255],v[216:219],v[136:139]
s_add_u32 s90, s90, s91
EX v22,v22
MM v[52:55],a[248:251],v[200:203],v[52:55]
s_and_b32 s91, s48, 16
TR a[180:181],v232 offset:26112
CV v16,v16,v17
MM v[52:55],a[244:247],v[184:187],v[52:55]
s_lshl_b32 s91, s91, 2
EX v23,v23
MM v[52:55],a[240:243],v[168:171],v[52:55]
s_add_u32 s66, s90, s91
TR a[182:183],v232 offset:30208
CV v17,v18,v19
MM v[60:63],a[240:243],v[172:175],v[140:143]
s_mul_i32 s91, %[swave], 5120
EX v24,v24
MM v[60:63],a[244:247],v[188:191],v[60:63]
s_add_u32 s91, s91, %[slds]
TR a[184:185],v233 offset:17920
TR a[186:187],v233 offset:22016
MM v[60:63],a[248:251],v[204:207],v[60:63]
s_add_u32 s67, s91, 131072
TR a[188:189],v233 offset:26112
TR a[190:191],v233 offset:30208
EX v25,v25
MM v[60:63],a[252:255],v[220:223],v[60:63]
EX v26,v26
MM v[56:59],a[236:239],v[220:223],v[140:143]
EX v27,v27
MM v[56:59],a[232:235],v[204:207],v[56:59]
EX v28,v28
MM v[56:59],a[228:231],v[188:191],v[56:59]
EX v29,v29
CV v18,v20,v21
MM v[56:59],a[224:227],v[172:175],v[56:59]
s_waitcnt lgkmcnt(0)
s_barrier
MM a[0:3],a[128:131],v[64:67],a[0:3]
s_add_u32 m0, s65, 16384
CV v19,v22,v23
CV v24,v24,v25
MM a[0:3],a[132:135],v[68:71],a[0:3]
CV v25,v26,v27
CV v26,v28,v29
MM a[8:11],a[132:135],v[76:79],a[8:11]
EX v30,v30
MM a[8:11],a[128:131],v[72:75],a[8:11]
EX v31,v31
MM a[16:19],a[128:131],v[80:83],a[16:19]
RD a[192:195],v224 offset:49152
CV v27,v30,v31
MM a[16:19],a[132:135],v[84:87],a[16:19]
EX v32,v32
MM a[24:27],a[132:135],v[92:95],a[24:27]
buffer_load_dwordx4 v228, s[8:11], s64 offen offset:0 lds
MM a[24:27],a[128:131],v[88:91],a[24:27]
EX v33,v33
MM a[28:31],a[136:139],v[88:91],a[28:31]
RD a[196:199],v225 offset:49152
CV v4,v32,v33
MM a[28:31],a[140:143],v[92:95],a[28:31]
EX v34,v34
MM a[20:23],a[140:143],v[84:87],a[20:23]
EX v35,v35
MM a[20:23],a[136:139],v[80:83],a[20:23]
RD a[200:203],v226 offset:49152
CV v5,v34,v35
MM a[12:15],a[136:139],v[72:75],a[12:15]
EX v36,v36
MM a[12:15],a[140:143],v[76:79],a[12:15]
LPG10_%=:
s_cbranch_execz LTR0_%=
buffer_load_dwordx4 v229, s[8:11], s64 offen offset:1024 lds
MM a[4:7],a[140:143],v[68:71],a[4:7]
EX v37,v37
MM a[4:7],a[136:139],v[64:67],a[4:7]
RD a[204:207],v227 offset:49152
CV v6,v36,v37
MM a[32:35],a[144:147],v[64:67],a[32:35]
EX v38,v38
MM a[32:35],a[148:151],v[68:71],a[32:35]
EX v39,v39
MM a[40:43],a[148:151],v[76:79],a[40:43]
RD a[208:211],v224 offset:53248
CV v7,v38,v39
MM a[40:43],a[144:147],v[72:75],a[40:43]
EX v40,v40
MM a[48:51],a[144:147],v[80:83],a[48:51]
buffer_load_dwordx4 v230, s[8:11], s64 offen offset:2048 lds
MM a[48:51],a[148:151],v[84:87],a[48:51]
EX v41,v41
MM a[56:59],a[148:151],v[92:95],a[56:59]
RD a[212:215],v225 offset:53248
CV v12,v40,v41
MM a[56:59],a[144:147],v[88:91],a[56:59]
EX v42,v42
MM a[60:63],a[152:155],v[88:91],a[60:63]
EX v43,v43
MM a[60:63],a[156:159],v[92:95],a[60:63]
RD a[216:219],v226 offset:53248
CV v13,v42,v43
MM a[52:55],a[156:159],v[84:87],a[52:55]
EX v44,v44
MM a[52:55],a[152:155],v[80:83],a[52:55]
buffer_load_dwordx4 v231, s[8:11], s64 offen offset:3072 lds
MM a[44:47],a[152:155],v[72:75],a[44:47]
s_add_u32 m0, s65, 81920
EX v45,v45
MM a[44:47],a[156:159],v[76:79],a[44:47]
RD a[220:223],v227 offset:53248
CV v14,v44,v45
MM a[36:39],a[156:159],v[68:71],a[36:39]
EX v46,v46
MM a[36:39],a[152:155],v[64:67],a[36:39]
RD a[224:227],v224 offset:57344
RD a[228:231],v225 offset:57344
MM a[64:67],a[160:163],v[64:67],a[64:67]
EX v47,v47
MM a[64:67],a[164:167],v[68:71],a[64:67]
RD a[232:235],v226 offset:57344
CV v15,v46,v47
MM a[72:75],a[164:167],v[76:79],a[72:75]
EX v48,v48
MM a[72:75],a[160:163],v[72:75],a[72:75]
buffer_load_dwordx4 v235, s[12:15], s81 offen offset:0 lds
MM a[80:83],a[160:163],v[80:83],a[80:83]
s_add_u32 m0, s67, 0
s_add_u32 s85, s66, 0
buffer_load_dwordx4 v252, s[36:39], s85 offen offset:0 lds
MM a[80:83],a[164:167],v[84:87],a[80:83]
EX v49,v49
MM a[88:91],a[164:167],v[92:95],a[88:91]
EX v50,v50
MM a[88:91],a[160:163],v[88:91],a[88:91]
RD a[236:239],v227 offset:57344
CV v20,v48,v49
MM a[92:95],a[168:171],v[88:91],a[92:95]
EX v51,v51
MM a[92:95],a[172:175],v[92:95],a[92:95]
RD a[240:243],v224 offset:61440
CV v21,v50,v51
MM a[84:87],a[172:175],v[84:87],a[84:87]
EX v52,v52
MM a[84:87],a[168:171],v[80:83],a[84:87]
EX v53,v53
MM a[76:79],a[168:171],v[72:75],a[76:79]
RD a[244:247],v225 offset:61440
CV v22,v52,v53
MM a[76:79],a[172:175],v[76:79],a[76:79]
EX v54,v54
MM a[68:71],a[172:175],v[68:71],a[68:71]
s_add_u32 m0, s65, 81920
s_nop 0
buffer_load_dwordx4 v235, s[12:15], s64 offen offset:1024 lds
MM a[68:71],a[168:171],v[64:67],a[68:71]
EX v55,v55
MM a[96:99],a[176:179],v[64:67],a[96:99]
RD a[248:251],v226 offset:61440
CV v23,v54,v55
MM a[96:99],a[180:183],v[68:71],a[96:99]
EX v56,v56
MM a[104:107],a[180:183],v[76:79],a[104:107]
EX v57,v57
MM a[104:107],a[176:179],v[72:75],a[104:107]
RD a[252:255],v227 offset:61440
CV v28,v56,v57
MM a[112:115],a[176:179],v[80:83],a[112:115]
buffer_load_dwordx4 v235, s[12:15], s81 offen offset:2048 lds
MM a[112:115],a[180:183],v[84:87],a[112:115]
EX v58,v58
MM a[120:123],a[180:183],v[92:95],a[120:123]
EX v59,v59
MM a[120:123],a[176:179],v[88:91],a[120:123]
EX v60,v60
MM a[124:127],a[184:187],v[88:91],a[124:127]
v_add_f32 v244, v144, v145
v_add_f32 v245, v148, v149
MM a[124:127],a[188:191],v[92:95],a[124:127]
v_add_f32 v246, v152, v153
v_add_f32 v247, v156, v157
MM a[116:119],a[188:191],v[84:87],a[116:119]
EX v61,v61
MM a[116:119],a[184:187],v[80:83],a[116:119]
buffer_load_dwordx4 v235, s[12:15], s64 offen offset:3072 lds
MM a[108:111],a[184:187],v[72:75],a[108:111]
EX v62,v62
MM a[108:111],a[188:191],v[76:79],a[108:111]
EX v63,v63
MM a[100:103],a[188:191],v[68:71],a[100:103]
CV v29,v58,v59
CV v30,v60,v61
MM a[100:103],a[184:187],v[64:67],a[100:103]
CV v31,v62,v63
v_smfmac_f32_16x16x64_bf16 v[144:147], v[236:239], v[0:7], v234
v_smfmac_f32_16x16x64_bf16 v[148:151], v[236:239], v[8:15], v234
v_smfmac_f32_16x16x64_bf16 v[152:155], v[236:239], v[16:23], v234
v_smfmac_f32_16x16x64_bf16 v[156:159], v[236:239], v[24:31], v234
s_waitcnt lgkmcnt(0)
MM v[64:67],a[192:195],v[160:163],v[128:131]
s_add_u32 s64, s64, 16384
TR a[128:129],v232 offset:32768
s_add_u32 s81, s64, 896
MM v[64:67],a[196:199],v[176:179],v[64:67]
TR a[130:131],v232 offset:36864
v_max3_f32 v249, v144, v145, v148
MM v[64:67],a[200:203],v[192:195],v[64:67]
TR a[132:133],v232 offset:40960
v_max3_f32 v249, v249, v149, v152
MM v[64:67],a[204:207],v[208:211],v[64:67]
TR a[134:135],v232 offset:45056
v_max3_f32 v249, v249, v153, v156
MM v[68:71],a[220:223],v[208:211],v[128:131]
TR a[136:137],v233 offset:32768
v_max_f32 v249, v249, v157
MM v[68:71],a[216:219],v[192:195],v[68:71]
TR a[138:139],v233 offset:36864
v_cmp_lt_f32 vcc, s71, v249
MM v[68:71],a[212:215],v[176:179],v[68:71]
TR a[140:141],v233 offset:40960
TR a[142:143],v233 offset:45056
MM v[68:71],a[208:211],v[160:163],v[68:71]
s_cbranch_vccnz LSITE10_%=
LBACK10_%=:
MM v[76:79],a[208:211],v[164:167],v[132:135]
s_add_u32 m0, s67, 1024
s_add_u32 s85, s66, 55296
buffer_load_dwordx4 v252, s[36:39], s85 offen offset:0 lds
MM v[76:79],a[212:215],v[180:183],v[76:79]
EX v64,v64
MM v[76:79],a[216:219],v[196:199],v[76:79]
EX v65,v65
MM v[76:79],a[220:223],v[212:215],v[76:79]
TR a[144:145],v232 offset:33280
CV v64,v64,v65
MM v[72:75],a[204:207],v[212:215],v[132:135]
EX v66,v66
MM v[72:75],a[200:203],v[196:199],v[72:75]
EX v67,v67
MM v[72:75],a[196:199],v[180:183],v[72:75]
TR a[146:147],v232 offset:37376
CV v65,v66,v67
MM v[72:75],a[192:195],v[164:167],v[72:75]
EX v68,v68
MM v[80:83],a[192:195],v[168:171],v[136:139]
TR a[148:149],v232 offset:41472
TR a[150:151],v232 offset:45568
MM v[80:83],a[196:199],v[184:187],v[80:83]
EX v69,v69
MM v[80:83],a[200:203],v[200:203],v[80:83]
TR a[152:153],v233 offset:33280
CV v66,v68,v69
MM v[80:83],a[204:207],v[216:219],v[80:83]
EX v70,v70
MM v[84:87],a[220:223],v[216:219],v[136:139]
EX v71,v71
MM v[84:87],a[216:219],v[200:203],v[84:87]
TR a[154:155],v233 offset:37376
CV v67,v70,v71
MM v[84:87],a[212:215],v[184:187],v[84:87]
EX v72,v72
MM v[84:87],a[208:211],v[168:171],v[84:87]
TR a[156:157],v233 offset:41472
TR a[158:159],v233 offset:45568
MM v[92:95],a[208:211],v[172:175],v[140:143]
EX v73,v73
MM v[92:95],a[212:215],v[188:191],v[92:95]
TR a[160:161],v232 offset:33792
CV v72,v72,v73
MM v[92:95],a[216:219],v[204:207],v[92:95]
EX v74,v74
MM v[92:95],a[220:223],v[220:223],v[92:95]
EX v75,v75
MM v[88:91],a[204:207],v[220:223],v[140:143]
TR a[162:163],v232 offset:37888
CV v73,v74,v75
MM v[88:91],a[200:203],v[204:207],v[88:91]
TR a[164:165],v232 offset:41984
TR a[166:167],v232 offset:46080
MM v[88:91],a[196:199],v[188:191],v[88:91]
EX v76,v76
MM v[88:91],a[192:195],v[172:175],v[88:91]
EX v77,v77
MM v[96:99],a[224:227],v[160:163],v[128:131]
TR a[168:169],v233 offset:33792
CV v74,v76,v77
MM v[96:99],a[228:231],v[176:179],v[96:99]
EX v78,v78
MM v[96:99],a[232:235],v[192:195],v[96:99]
TR a[170:171],v233 offset:37888
TR a[172:173],v233 offset:41984
MM v[96:99],a[236:239],v[208:211],v[96:99]
EX v79,v79
MM v[100:103],a[252:255],v[208:211],v[128:131]
TR a[174:175],v233 offset:46080
CV v75,v78,v79
MM v[100:103],a[248:251],v[192:195],v[100:103]
EX v80,v80
MM v[100:103],a[244:247],v[176:179],v[100:103]
EX v81,v81
MM v[100:103],a[240:243],v[160:163],v[100:103]
TR a[176:177],v232 offset:34304
CV v80,v80,v81
MM v[108:111],a[240:243],v[164:167],v[132:135]
s_add_u32 m0, s67, 2048
s_add_u32 s85, s66, 110592
buffer_load_dwordx4 v252, s[36:39], s85 offen offset:0 lds
MM v[108:111],a[244:247],v[180:183],v[108:111]
EX v82,v82
MM v[108:111],a[248:251],v[196:199],v[108:111]
TR a[178:179],v232 offset:38400
TR a[180:181],v232 offset:42496
MM v[108:111],a[252:255],v[212:215],v[108:111]
EX v83,v83
MM v[104:107],a[236:239],v[212:215],v[132:135]
TR a[182:183],v232 offset:46592
CV v81,v82,v83
MM v[104:107],a[232:235],v[196:199],v[104:107]
EX v84,v84
MM v[104:107],a[228:231],v[180:183],v[104:107]
EX v85,v85
MM v[104:107],a[224:227],v[164:167],v[104:107]
TR a[184:185],v233 offset:34304
CV v82,v84,v85
MM v[112:115],a[224:227],v[168:171],v[136:139]
EX v86,v86
MM v[112:115],a[228:231],v[184:187],v[112:115]
TR a[186:187],v233 offset:38400
TR a[188:189],v233 offset:42496
MM v[112:115],a[232:235],v[200:203],v[112:115]
EX v87,v87
MM v[112:115],a[236:239],v[216:219],v[112:115]
TR a[190:191],v233 offset:46592
CV v83,v86,v87
MM v[116:119],a[252:255],v[216:219],v[136:139]
EX v88,v88
MM v[116:119],a[248:251],v[200:203],v[116:119]
EX v89,v89
MM v[116:119],a[244:247],v[184:187],v[116:119]
EX v90,v90
MM v[116:119],a[240:243],v[168:171],v[116:119]
EX v91,v91
MM v[124:127],a[240:243],v[172:175],v[140:143]
EX v92,v92
MM v[124:127],a[244:247],v[188:191],v[124:127]
EX v93,v93
MM v[124:127],a[248:251],v[204:207],v[124:127]
EX v94,v94
MM v[124:127],a[252:255],v[220:223],v[124:127]
)ASM"
R"ASM(
EX v95,v95
MM v[120:123],a[236:239],v[220:223],v[140:143]
EX v96,v96
MM v[120:123],a[232:235],v[204:207],v[120:123]
EX v97,v97
MM v[120:123],a[228:231],v[188:191],v[120:123]
EX v98,v98
MM v[120:123],a[224:227],v[172:175],v[120:123]
s_waitcnt vmcnt(15) lgkmcnt(0)
s_barrier
MM a[0:3],a[128:131],v[0:3],a[0:3]
s_add_u32 m0, s65, 32768
CV v88,v88,v89
CV v89,v90,v91
MM a[0:3],a[132:135],v[4:7],a[0:3]
RD a[192:195],v224 offset:0
CV v90,v92,v93
MM a[8:11],a[132:135],v[12:15],a[8:11]
RD a[196:199],v225 offset:0
CV v91,v94,v95
MM a[8:11],a[128:131],v[8:11],a[8:11]
buffer_load_dwordx4 v228, s[8:11], s64 offen offset:0 lds
MM a[16:19],a[128:131],v[16:19],a[16:19]
RD a[200:203],v226 offset:0
CV v68,v96,v97
MM a[16:19],a[132:135],v[20:23],a[16:19]
buffer_load_dwordx4 v229, s[8:11], s64 offen offset:1024 lds
MM a[24:27],a[132:135],v[28:31],a[24:27]
RD a[204:207],v227 offset:0
RD a[208:211],v224 offset:4096
MM a[24:27],a[128:131],v[24:27],a[24:27]
EX v99,v99
MM a[28:31],a[136:139],v[24:27],a[28:31]
RD a[212:215],v225 offset:4096
CV v69,v98,v99
MM a[28:31],a[140:143],v[28:31],a[28:31]
EX v100,v100
MM a[20:23],a[140:143],v[20:23],a[20:23]
buffer_load_dwordx4 v230, s[8:11], s64 offen offset:2048 lds
MM a[20:23],a[136:139],v[16:19],a[20:23]
EX v101,v101
MM a[12:15],a[136:139],v[8:11],a[12:15]
RD a[216:219],v226 offset:4096
CV v70,v100,v101
MM a[12:15],a[140:143],v[12:15],a[12:15]
EX v102,v102
MM a[4:7],a[140:143],v[4:7],a[4:7]
EX v103,v103
MM a[4:7],a[136:139],v[0:3],a[4:7]
RD a[220:223],v227 offset:4096
CV v71,v102,v103
MM a[32:35],a[144:147],v[0:3],a[32:35]
EX v104,v104
MM a[32:35],a[148:151],v[4:7],a[32:35]
buffer_load_dwordx4 v231, s[8:11], s64 offen offset:3072 lds
MM a[40:43],a[148:151],v[12:15],a[40:43]
s_add_u32 m0, s65, 98304
EX v105,v105
MM a[40:43],a[144:147],v[8:11],a[40:43]
RD a[224:227],v224 offset:8192
CV v76,v104,v105
MM a[48:51],a[144:147],v[16:19],a[48:51]
EX v106,v106
MM a[48:51],a[148:151],v[20:23],a[48:51]
EX v107,v107
MM a[56:59],a[148:151],v[28:31],a[56:59]
RD a[228:231],v225 offset:8192
CV v77,v106,v107
MM a[56:59],a[144:147],v[24:27],a[56:59]
EX v108,v108
MM a[60:63],a[152:155],v[24:27],a[60:63]
buffer_load_dwordx4 v235, s[12:15], s81 offen offset:0 lds
MM a[60:63],a[156:159],v[28:31],a[60:63]
EX v109,v109
MM a[52:55],a[156:159],v[20:23],a[52:55]
RD a[232:235],v226 offset:8192
CV v78,v108,v109
MM a[52:55],a[152:155],v[16:19],a[52:55]
EX v110,v110
MM a[44:47],a[152:155],v[8:11],a[44:47]
EX v111,v111
MM a[44:47],a[156:159],v[12:15],a[44:47]
RD a[236:239],v227 offset:8192
CV v79,v110,v111
MM a[36:39],a[156:159],v[4:7],a[36:39]
buffer_load_dwordx4 v235, s[12:15], s64 offen offset:1024 lds
MM a[36:39],a[152:155],v[0:3],a[36:39]
EX v112,v112
MM a[64:67],a[160:163],v[0:3],a[64:67]
EX v113,v113
MM a[64:67],a[164:167],v[4:7],a[64:67]
RD a[240:243],v224 offset:12288
CV v84,v112,v113
MM a[72:75],a[164:167],v[12:15],a[72:75]
EX v114,v114
MM a[72:75],a[160:163],v[8:11],a[72:75]
RD a[244:247],v225 offset:12288
RD a[248:251],v226 offset:12288
MM a[80:83],a[160:163],v[16:19],a[80:83]
s_add_u32 m0, s67, 3072
s_add_u32 s85, s66, 165888
buffer_load_dwordx4 v252, s[36:39], s85 offen offset:0 lds
MM a[80:83],a[164:167],v[20:23],a[80:83]
EX v115,v115
MM a[88:91],a[164:167],v[28:31],a[88:91]
RD a[252:255],v227 offset:12288
CV v85,v114,v115
MM a[88:91],a[160:163],v[24:27],a[88:91]
EX v116,v116
MM a[92:95],a[168:171],v[24:27],a[92:95]
EX v117,v117
MM a[92:95],a[172:175],v[28:31],a[92:95]
s_add_u32 m0, s65, 98304
s_nop 0
buffer_load_dwordx4 v235, s[12:15], s81 offen offset:2048 lds
MM a[84:87],a[172:175],v[20:23],a[84:87]
EX v118,v118
MM a[84:87],a[168:171],v[16:19],a[84:87]
EX v119,v119
MM a[76:79],a[168:171],v[8:11],a[76:79]
buffer_load_dwordx4 v235, s[12:15], s64 offen offset:3072 lds
MM a[76:79],a[172:175],v[12:15],a[76:79]
EX v120,v120
MM a[68:71],a[172:175],v[4:7],a[68:71]
EX v121,v121
MM a[68:71],a[168:171],v[0:3],a[68:71]
EX v122,v122
MM a[96:99],a[176:179],v[0:3],a[96:99]
EX v123,v123
MM a[96:99],a[180:183],v[4:7],a[96:99]
EX v124,v124
MM a[104:107],a[180:183],v[12:15],a[104:107]
EX v125,v125
MM a[104:107],a[176:179],v[8:11],a[104:107]
EX v126,v126
MM a[112:115],a[176:179],v[16:19],a[112:115]
EX v127,v127
MM a[112:115],a[180:183],v[20:23],a[112:115]
CV v86,v116,v117
CV v87,v118,v119
MM a[120:123],a[180:183],v[28:31],a[120:123]
CV v92,v120,v121
CV v93,v122,v123
MM a[120:123],a[176:179],v[24:27],a[120:123]
CV v94,v124,v125
CV v95,v126,v127
MM a[124:127],a[184:187],v[24:27],a[124:127]
v_add_f32 v244, v144, v145
v_add_f32 v245, v148, v149
MM a[124:127],a[188:191],v[28:31],a[124:127]
v_add_f32 v246, v152, v153
v_add_f32 v247, v156, v157
MM a[116:119],a[188:191],v[20:23],a[116:119]
MM a[116:119],a[184:187],v[16:19],a[116:119]
MM a[108:111],a[184:187],v[8:11],a[108:111]
MM a[108:111],a[188:191],v[12:15],a[108:111]
MM a[100:103],a[188:191],v[4:7],a[100:103]
MM a[100:103],a[184:187],v[0:3],a[100:103]
v_smfmac_f32_16x16x64_bf16 v[144:147], v[236:239], v[64:71], v234
v_smfmac_f32_16x16x64_bf16 v[148:151], v[236:239], v[72:79], v234
v_smfmac_f32_16x16x64_bf16 v[152:155], v[236:239], v[80:87], v234
v_smfmac_f32_16x16x64_bf16 v[156:159], v[236:239], v[88:95], v234
s_waitcnt lgkmcnt(0)
MM v[0:3],a[192:195],v[160:163],v[128:131]
s_add_u32 s64, s64, 16384
TR a[128:129],v232 offset:49152
s_add_u32 s81, s64, 896
MM v[0:3],a[196:199],v[176:179],v[0:3]
TR a[130:131],v232 offset:53248
v_max3_f32 v249, v144, v145, v148
MM v[0:3],a[200:203],v[192:195],v[0:3]
TR a[132:133],v232 offset:57344
v_max3_f32 v249, v249, v149, v152
MM v[0:3],a[204:207],v[208:211],v[0:3]
TR a[134:135],v232 offset:61440
v_max3_f32 v249, v249, v153, v156
MM v[4:7],a[220:223],v[208:211],v[128:131]
TR a[136:137],v233 offset:49152
v_max_f32 v249, v249, v157
MM v[4:7],a[216:219],v[192:195],v[4:7]
TR a[138:139],v233 offset:53248
v_cmp_lt_f32 vcc, s71, v249
MM v[4:7],a[212:215],v[176:179],v[4:7]
TR a[140:141],v233 offset:57344
TR a[142:143],v233 offset:61440
MM v[4:7],a[208:211],v[160:163],v[4:7]
s_cbranch_vccnz LSITE11_%=
LBACK11_%=:
MM v[12:15],a[208:211],v[164:167],v[132:135]
s_add_u32 m0, s67, 4096
s_add_u32 s85, s66, 221184
buffer_load_dwordx4 v252, s[36:39], s85 offen offset:0 lds
MM v[12:15],a[212:215],v[180:183],v[12:15]
EX v0,v0
MM v[12:15],a[216:219],v[196:199],v[12:15]
EX v1,v1
MM v[12:15],a[220:223],v[212:215],v[12:15]
TR a[144:145],v232 offset:49664
CV v0,v0,v1
MM v[8:11],a[204:207],v[212:215],v[132:135]
EX v2,v2
MM v[8:11],a[200:203],v[196:199],v[8:11]
EX v3,v3
MM v[8:11],a[196:199],v[180:183],v[8:11]
TR a[146:147],v232 offset:53760
CV v1,v2,v3
MM v[8:11],a[192:195],v[164:167],v[8:11]
EX v4,v4
MM v[16:19],a[192:195],v[168:171],v[136:139]
TR a[148:149],v232 offset:57856
TR a[150:151],v232 offset:61952
MM v[16:19],a[196:199],v[184:187],v[16:19]
EX v5,v5
MM v[16:19],a[200:203],v[200:203],v[16:19]
TR a[152:153],v233 offset:49664
CV v2,v4,v5
MM v[16:19],a[204:207],v[216:219],v[16:19]
EX v6,v6
MM v[20:23],a[220:223],v[216:219],v[136:139]
EX v7,v7
MM v[20:23],a[216:219],v[200:203],v[20:23]
TR a[154:155],v233 offset:53760
CV v3,v6,v7
MM v[20:23],a[212:215],v[184:187],v[20:23]
EX v8,v8
MM v[20:23],a[208:211],v[168:171],v[20:23]
TR a[156:157],v233 offset:57856
TR a[158:159],v233 offset:61952
MM v[28:31],a[208:211],v[172:175],v[140:143]
EX v9,v9
MM v[28:31],a[212:215],v[188:191],v[28:31]
TR a[160:161],v232 offset:50176
CV v8,v8,v9
MM v[28:31],a[216:219],v[204:207],v[28:31]
EX v10,v10
MM v[28:31],a[220:223],v[220:223],v[28:31]
EX v11,v11
MM v[24:27],a[204:207],v[220:223],v[140:143]
TR a[162:163],v232 offset:54272
CV v9,v10,v11
MM v[24:27],a[200:203],v[204:207],v[24:27]
TR a[164:165],v232 offset:58368
TR a[166:167],v232 offset:62464
MM v[24:27],a[196:199],v[188:191],v[24:27]
EX v12,v12
MM v[24:27],a[192:195],v[172:175],v[24:27]
EX v13,v13
MM v[32:35],a[224:227],v[160:163],v[128:131]
TR a[168:169],v233 offset:50176
CV v10,v12,v13
MM v[32:35],a[228:231],v[176:179],v[32:35]
EX v14,v14
MM v[32:35],a[232:235],v[192:195],v[32:35]
TR a[170:171],v233 offset:54272
TR a[172:173],v233 offset:58368
MM v[32:35],a[236:239],v[208:211],v[32:35]
EX v15,v15
MM v[36:39],a[252:255],v[208:211],v[128:131]
TR a[174:175],v233 offset:62464
CV v11,v14,v15
MM v[36:39],a[248:251],v[192:195],v[36:39]
EX v16,v16
MM v[36:39],a[244:247],v[176:179],v[36:39]
EX v17,v17
MM v[36:39],a[240:243],v[160:163],v[36:39]
TR a[176:177],v232 offset:50688
CV v16,v16,v17
MM v[44:47],a[240:243],v[164:167],v[132:135]
EX v18,v18
MM v[44:47],a[244:247],v[180:183],v[44:47]
TR a[178:179],v232 offset:54784
TR a[180:181],v232 offset:58880
MM v[44:47],a[248:251],v[196:199],v[44:47]
EX v19,v19
MM v[44:47],a[252:255],v[212:215],v[44:47]
TR a[182:183],v232 offset:62976
CV v17,v18,v19
MM v[40:43],a[236:239],v[212:215],v[132:135]
EX v20,v20
MM v[40:43],a[232:235],v[196:199],v[40:43]
EX v21,v21
MM v[40:43],a[228:231],v[180:183],v[40:43]
TR a[184:185],v233 offset:50688
CV v18,v20,v21
MM v[40:43],a[224:227],v[164:167],v[40:43]
EX v22,v22
MM v[48:51],a[224:227],v[168:171],v[136:139]
TR a[186:187],v233 offset:54784
TR a[188:189],v233 offset:58880
MM v[48:51],a[228:231],v[184:187],v[48:51]
EX v23,v23
MM v[48:51],a[232:235],v[200:203],v[48:51]
TR a[190:191],v233 offset:62976
CV v19,v22,v23
MM v[48:51],a[236:239],v[216:219],v[48:51]
EX v24,v24
MM v[52:55],a[252:255],v[216:219],v[136:139]
EX v25,v25
MM v[52:55],a[248:251],v[200:203],v[52:55]
EX v26,v26
MM v[52:55],a[244:247],v[184:187],v[52:55]
EX v27,v27
MM v[52:55],a[240:243],v[168:171],v[52:55]
EX v28,v28
MM v[60:63],a[240:243],v[172:175],v[140:143]
EX v29,v29
MM v[60:63],a[244:247],v[188:191],v[60:63]
EX v30,v30
MM v[60:63],a[248:251],v[204:207],v[60:63]
EX v31,v31
LPG11_%=:
s_cbranch_execz LTR0_%=
MM v[60:63],a[252:255],v[220:223],v[60:63]
EX v32,v32
MM v[56:59],a[236:239],v[220:223],v[140:143]
EX v33,v33
MM v[56:59],a[232:235],v[204:207],v[56:59]
EX v34,v34
MM v[56:59],a[228:231],v[188:191],v[56:59]
EX v35,v35
MM v[56:59],a[224:227],v[172:175],v[56:59]
s_waitcnt vmcnt(17) lgkmcnt(0)
s_barrier
MM a[0:3],a[128:131],v[64:67],a[0:3]
s_mov_b32 s86, 94208
v_bfe_u32 v251, v248, 4, 3
v_lshlrev_b32 v251, 3, v251
buffer_load_dwordx2 v[252:253], v251, s[20:23], s86 offen offset:0 sc1
MM a[0:3],a[132:135],v[68:71],a[0:3]
s_add_u32 m0, s65, 49152
CV v24,v24,v25
CV v25,v26,v27
MM a[8:11],a[132:135],v[76:79],a[8:11]
RD a[192:195],v224 offset:16384
CV v26,v28,v29
MM a[8:11],a[128:131],v[72:75],a[8:11]
RD a[196:199],v225 offset:16384
CV v27,v30,v31
MM a[16:19],a[128:131],v[80:83],a[16:19]
buffer_load_dwordx4 v228, s[8:11], s64 offen offset:0 lds
MM a[16:19],a[132:135],v[84:87],a[16:19]
RD a[200:203],v226 offset:16384
CV v4,v32,v33
MM a[24:27],a[132:135],v[92:95],a[24:27]
RD a[204:207],v227 offset:16384
CV v5,v34,v35
MM a[24:27],a[128:131],v[88:91],a[24:27]
buffer_load_dwordx4 v229, s[8:11], s64 offen offset:1024 lds
MM a[28:31],a[136:139],v[88:91],a[28:31]
RD a[208:211],v224 offset:20480
RD a[212:215],v225 offset:20480
MM a[28:31],a[140:143],v[92:95],a[28:31]
EX v36,v36
MM a[20:23],a[140:143],v[84:87],a[20:23]
buffer_load_dwordx4 v230, s[8:11], s64 offen offset:2048 lds
MM a[20:23],a[136:139],v[80:83],a[20:23]
EX v37,v37
MM a[12:15],a[136:139],v[72:75],a[12:15]
RD a[216:219],v226 offset:20480
CV v6,v36,v37
MM a[12:15],a[140:143],v[76:79],a[12:15]
EX v38,v38
MM a[4:7],a[140:143],v[68:71],a[4:7]
EX v39,v39
MM a[4:7],a[136:139],v[64:67],a[4:7]
RD a[220:223],v227 offset:20480
CV v7,v38,v39
MM a[32:35],a[144:147],v[64:67],a[32:35]
EX v40,v40
MM a[32:35],a[148:151],v[68:71],a[32:35]
buffer_load_dwordx4 v231, s[8:11], s64 offen offset:3072 lds
MM a[40:43],a[148:151],v[76:79],a[40:43]
s_add_u32 m0, s65, 114688
EX v41,v41
MM a[40:43],a[144:147],v[72:75],a[40:43]
RD a[224:227],v224 offset:24576
CV v12,v40,v41
MM a[48:51],a[144:147],v[80:83],a[48:51]
EX v42,v42
MM a[48:51],a[148:151],v[84:87],a[48:51]
EX v43,v43
MM a[56:59],a[148:151],v[92:95],a[56:59]
RD a[228:231],v225 offset:24576
CV v13,v42,v43
MM a[56:59],a[144:147],v[88:91],a[56:59]
EX v44,v44
MM a[60:63],a[152:155],v[88:91],a[60:63]
buffer_load_dwordx4 v235, s[12:15], s81 offen offset:0 lds
MM a[60:63],a[156:159],v[92:95],a[60:63]
)ASM"
R"ASM(
EX v45,v45
MM a[52:55],a[156:159],v[84:87],a[52:55]
RD a[232:235],v226 offset:24576
CV v14,v44,v45
MM a[52:55],a[152:155],v[80:83],a[52:55]
EX v46,v46
MM a[44:47],a[152:155],v[72:75],a[44:47]
EX v47,v47
MM a[44:47],a[156:159],v[76:79],a[44:47]
RD a[236:239],v227 offset:24576
CV v15,v46,v47
MM a[36:39],a[156:159],v[68:71],a[36:39]
buffer_load_dwordx4 v235, s[12:15], s64 offen offset:1024 lds
MM a[36:39],a[152:155],v[64:67],a[36:39]
EX v48,v48
MM a[64:67],a[160:163],v[64:67],a[64:67]
EX v49,v49
MM a[64:67],a[164:167],v[68:71],a[64:67]
RD a[240:243],v224 offset:28672
CV v20,v48,v49
MM a[72:75],a[164:167],v[76:79],a[72:75]
EX v50,v50
MM a[72:75],a[160:163],v[72:75],a[72:75]
RD a[244:247],v225 offset:28672
RD a[248:251],v226 offset:28672
MM a[80:83],a[160:163],v[80:83],a[80:83]
EX v51,v51
MM a[80:83],a[164:167],v[84:87],a[80:83]
RD a[252:255],v227 offset:28672
CV v21,v50,v51
MM a[88:91],a[164:167],v[92:95],a[88:91]
EX v52,v52
MM a[88:91],a[160:163],v[88:91],a[88:91]
buffer_load_dwordx4 v235, s[12:15], s81 offen offset:2048 lds
MM a[92:95],a[168:171],v[88:91],a[92:95]
EX v53,v53
MM a[92:95],a[172:175],v[92:95],a[92:95]
EX v54,v54
MM a[84:87],a[172:175],v[84:87],a[84:87]
buffer_load_dwordx4 v235, s[12:15], s64 offen offset:3072 lds
MM a[84:87],a[168:171],v[80:83],a[84:87]
EX v55,v55
MM a[76:79],a[168:171],v[72:75],a[76:79]
EX v56,v56
MM a[76:79],a[172:175],v[76:79],a[76:79]
EX v57,v57
MM a[68:71],a[172:175],v[68:71],a[68:71]
EX v58,v58
MM a[68:71],a[168:171],v[64:67],a[68:71]
EX v59,v59
MM a[96:99],a[176:179],v[64:67],a[96:99]
EX v60,v60
MM a[96:99],a[180:183],v[68:71],a[96:99]
EX v61,v61
MM a[104:107],a[180:183],v[76:79],a[104:107]
EX v62,v62
MM a[104:107],a[176:179],v[72:75],a[104:107]
EX v63,v63
MM a[112:115],a[176:179],v[80:83],a[112:115]
CV v22,v52,v53
CV v23,v54,v55
MM a[112:115],a[180:183],v[84:87],a[112:115]
CV v28,v56,v57
CV v29,v58,v59
MM a[120:123],a[180:183],v[92:95],a[120:123]
CV v30,v60,v61
CV v31,v62,v63
MM a[120:123],a[176:179],v[88:91],a[120:123]
MM a[124:127],a[184:187],v[88:91],a[124:127]
v_add_f32 v244, v144, v145
v_add_f32 v245, v148, v149
MM a[124:127],a[188:191],v[92:95],a[124:127]
v_add_f32 v246, v152, v153
v_add_f32 v247, v156, v157
MM a[116:119],a[188:191],v[84:87],a[116:119]
MM a[116:119],a[184:187],v[80:83],a[116:119]
MM a[108:111],a[184:187],v[72:75],a[108:111]
MM a[108:111],a[188:191],v[76:79],a[108:111]
MM a[100:103],a[188:191],v[68:71],a[100:103]
MM a[100:103],a[184:187],v[64:67],a[100:103]
v_smfmac_f32_16x16x64_bf16 v[144:147], v[236:239], v[0:7], v234
v_smfmac_f32_16x16x64_bf16 v[148:151], v[236:239], v[8:15], v234
v_smfmac_f32_16x16x64_bf16 v[152:155], v[236:239], v[16:23], v234
v_smfmac_f32_16x16x64_bf16 v[156:159], v[236:239], v[24:31], v234
s_waitcnt lgkmcnt(0)
MM v[64:67],a[192:195],v[160:163],v[128:131]
s_add_u32 s64, s64, 16384
TR a[128:129],v232 offset:0
s_add_u32 s81, s64, 896
MM v[64:67],a[196:199],v[176:179],v[64:67]
TR a[130:131],v232 offset:4096
v_max3_f32 v249, v144, v145, v148
MM v[64:67],a[200:203],v[192:195],v[64:67]
TR a[132:133],v232 offset:8192
v_max3_f32 v249, v249, v149, v152
MM v[64:67],a[204:207],v[208:211],v[64:67]
TR a[134:135],v232 offset:12288
v_max3_f32 v249, v249, v153, v156
MM v[68:71],a[220:223],v[208:211],v[128:131]
TR a[136:137],v233 offset:0
v_max_f32 v249, v249, v157
MM v[68:71],a[216:219],v[192:195],v[68:71]
TR a[138:139],v233 offset:4096
v_cmp_lt_f32 vcc, s71, v249
MM v[68:71],a[212:215],v[176:179],v[68:71]
TR a[140:141],v233 offset:8192
TR a[142:143],v233 offset:12288
MM v[68:71],a[208:211],v[160:163],v[68:71]
s_cbranch_vccnz LSITE12_%=
LBACK12_%=:
MM v[76:79],a[208:211],v[164:167],v[132:135]
s_waitcnt vmcnt(8)
v_cmp_ne_u32 vcc, s69, v253
s_cbranch_vccnz LRIX_%=
LRIXb_%=:
MM v[76:79],a[212:215],v[180:183],v[76:79]
s_and_b32 s90, s17, 7
v_and_b32 v253, 65535, v252
v_readlane_b32 s19, v252, s90
MM v[76:79],a[216:219],v[196:199],v[76:79]
v_sub_u32 v253, 65535, v253
v_mul_u32_u24 v253, 1280, v253
MM v[76:79],a[220:223],v[212:215],v[76:79]
v_lshrrev_b32 v252, 7, v248
v_lshl_add_u32 v253, v252, 4, v253
MM v[72:75],a[204:207],v[212:215],v[132:135]
s_mul_i32 s91, %[swave], 3072
EX v64,v64
MM v[72:75],a[200:203],v[196:199],v[72:75]
s_add_u32 s91, s91, %[slds]
EX v65,v65
MM v[72:75],a[196:199],v[180:183],v[72:75]
s_bfe_u32 s90, s48, 131077
EX v66,v66
MM v[72:75],a[192:195],v[164:167],v[72:75]
s_mul_i32 s90, s90, 320
EX v67,v67
MM v[80:83],a[192:195],v[168:171],v[136:139]
s_add_u32 s90, s90, 131072
s_add_u32 m0, s91, 151552
s_nop 0
buffer_load_dwordx4 v253, s[20:23], s90 offen offset:0 sc1 lds
MM v[80:83],a[196:199],v[184:187],v[80:83]
TR a[144:145],v232 offset:512
CV v64,v64,v65
MM v[80:83],a[200:203],v[200:203],v[80:83]
s_add_u32 m0, s91, 152448
s_nop 0
buffer_load_dwordx4 v253, s[20:23], s90 offen offset:128 sc1 lds
MM v[80:83],a[204:207],v[216:219],v[80:83]
EX v68,v68
MM v[84:87],a[220:223],v[216:219],v[136:139]
s_add_u32 m0, s91, 153344
s_nop 0
buffer_load_dwordx4 v253, s[20:23], s90 offen offset:256 sc1 lds
MM v[84:87],a[216:219],v[200:203],v[84:87]
EX v69,v69
MM v[84:87],a[212:215],v[184:187],v[84:87]
s_lshr_b32 s77, %[sb], 3
TR a[146:147],v232 offset:4608
TR a[148:149],v232 offset:8704
MM v[84:87],a[208:211],v[168:171],v[84:87]
s_lshl_b32 s77, s77, 2
EX v70,v70
MM v[92:95],a[208:211],v[172:175],v[140:143]
s_add_u32 s77, s77, %[swave]
EX v71,v71
MM v[92:95],a[212:215],v[188:191],v[92:95]
s_lshl_b32 s77, s77, 9
TR a[150:151],v232 offset:12800
CV v65,v66,v67
MM v[92:95],a[216:219],v[204:207],v[92:95]
s_add_u32 s77, s77, 0
v_lshrrev_b32 v253, 1, v248
v_add_u32 v253, s77, v253
MM v[92:95],a[220:223],v[220:223],v[92:95]
s_mov_b32 s77, LKEND_ka-LPD0_%=-16
TR a[152:153],v233 offset:512
v_min_u32 v253, s77, v253
EX v72,v72
MM v[88:91],a[204:207],v[220:223],v[140:143]
s_getpc_b64 s[98:99]
LPD0_%=:
global_load_dword v253, v253, s[98:99]
MM v[88:91],a[200:203],v[204:207],v[88:91]
s_add_u32 s67, s95, 1024
EX v73,v73
MM v[88:91],a[196:199],v[188:191],v[88:91]
s_mov_b32 s70, 5
TR a[154:155],v233 offset:4608
CV v66,v68,v69
MM v[88:91],a[192:195],v[172:175],v[88:91]
EX v74,v74
MM v[96:99],a[224:227],v[160:163],v[128:131]
TR a[156:157],v233 offset:8704
TR a[158:159],v233 offset:12800
EX v75,v75
MM v[96:99],a[228:231],v[176:179],v[96:99]
TR a[160:161],v232 offset:1024
CV v67,v70,v71
MM v[96:99],a[232:235],v[192:195],v[96:99]
EX v76,v76
MM v[96:99],a[236:239],v[208:211],v[96:99]
EX v77,v77
MM v[100:103],a[252:255],v[208:211],v[128:131]
TR a[162:163],v232 offset:5120
CV v72,v72,v73
MM v[100:103],a[248:251],v[192:195],v[100:103]
EX v78,v78
MM v[100:103],a[244:247],v[176:179],v[100:103]
TR a[164:165],v232 offset:9216
CV v73,v74,v75
MM v[100:103],a[240:243],v[160:163],v[100:103]
EX v79,v79
MM v[108:111],a[240:243],v[164:167],v[132:135]
TR a[166:167],v232 offset:13312
TR a[168:169],v233 offset:1024
MM v[108:111],a[244:247],v[180:183],v[108:111]
EX v80,v80
MM v[108:111],a[248:251],v[196:199],v[108:111]
EX v81,v81
MM v[108:111],a[252:255],v[212:215],v[108:111]
TR a[170:171],v233 offset:5120
CV v74,v76,v77
MM v[104:107],a[236:239],v[212:215],v[132:135]
EX v82,v82
MM v[104:107],a[232:235],v[196:199],v[104:107]
TR a[172:173],v233 offset:9216
CV v75,v78,v79
MM v[104:107],a[228:231],v[180:183],v[104:107]
EX v83,v83
MM v[104:107],a[224:227],v[164:167],v[104:107]
TR a[174:175],v233 offset:13312
TR a[176:177],v232 offset:1536
MM v[112:115],a[224:227],v[168:171],v[136:139]
EX v84,v84
MM v[112:115],a[228:231],v[184:187],v[112:115]
TR a[178:179],v232 offset:5632
CV v80,v80,v81
MM v[112:115],a[232:235],v[200:203],v[112:115]
EX v85,v85
MM v[112:115],a[236:239],v[216:219],v[112:115]
TR a[180:181],v232 offset:9728
CV v81,v82,v83
MM v[116:119],a[252:255],v[216:219],v[136:139]
EX v86,v86
MM v[116:119],a[248:251],v[200:203],v[116:119]
TR a[182:183],v232 offset:13824
TR a[184:185],v233 offset:1536
MM v[116:119],a[244:247],v[184:187],v[116:119]
EX v87,v87
MM v[116:119],a[240:243],v[168:171],v[116:119]
EX v88,v88
MM v[124:127],a[240:243],v[172:175],v[140:143]
TR a[186:187],v233 offset:5632
CV v82,v84,v85
MM v[124:127],a[244:247],v[188:191],v[124:127]
EX v89,v89
MM v[124:127],a[248:251],v[204:207],v[124:127]
TR a[188:189],v233 offset:9728
TR a[190:191],v233 offset:13824
MM v[124:127],a[252:255],v[220:223],v[124:127]
EX v90,v90
MM v[120:123],a[236:239],v[220:223],v[140:143]
CV v83,v86,v87
CV v88,v88,v89
MM v[120:123],a[232:235],v[204:207],v[120:123]
EX v91,v91
MM v[120:123],a[228:231],v[188:191],v[120:123]
EX v92,v92
MM v[120:123],a[224:227],v[172:175],v[120:123]
s_waitcnt lgkmcnt(0)
s_barrier
MM a[0:3],a[128:131],v[0:3],a[0:3]
s_add_u32 m0, s65, 0
EX v93,v93
MM a[0:3],a[132:135],v[4:7],a[0:3]
CV v89,v90,v91
CV v90,v92,v93
MM a[8:11],a[132:135],v[12:15],a[8:11]
EX v94,v94
MM a[8:11],a[128:131],v[8:11],a[8:11]
EX v95,v95
MM a[16:19],a[128:131],v[16:19],a[16:19]
RD a[192:195],v224 offset:32768
CV v91,v94,v95
MM a[16:19],a[132:135],v[20:23],a[16:19]
EX v96,v96
MM a[24:27],a[132:135],v[28:31],a[24:27]
buffer_load_dwordx4 v228, s[8:11], s64 offen offset:0 lds
MM a[24:27],a[128:131],v[24:27],a[24:27]
EX v97,v97
MM a[28:31],a[136:139],v[24:27],a[28:31]
RD a[196:199],v225 offset:32768
CV v68,v96,v97
MM a[28:31],a[140:143],v[28:31],a[28:31]
EX v98,v98
MM a[20:23],a[140:143],v[20:23],a[20:23]
EX v99,v99
MM a[20:23],a[136:139],v[16:19],a[20:23]
RD a[200:203],v226 offset:32768
CV v69,v98,v99
MM a[12:15],a[136:139],v[8:11],a[12:15]
EX v100,v100
MM a[12:15],a[140:143],v[12:15],a[12:15]
buffer_load_dwordx4 v229, s[8:11], s64 offen offset:1024 lds
MM a[4:7],a[140:143],v[4:7],a[4:7]
EX v101,v101
MM a[4:7],a[136:139],v[0:3],a[4:7]
RD a[204:207],v227 offset:32768
CV v70,v100,v101
MM a[32:35],a[144:147],v[0:3],a[32:35]
EX v102,v102
MM a[32:35],a[148:151],v[4:7],a[32:35]
EX v103,v103
MM a[40:43],a[148:151],v[12:15],a[40:43]
RD a[208:211],v224 offset:36864
CV v71,v102,v103
MM a[40:43],a[144:147],v[8:11],a[40:43]
EX v104,v104
MM a[48:51],a[144:147],v[16:19],a[48:51]
buffer_load_dwordx4 v230, s[8:11], s64 offen offset:2048 lds
MM a[48:51],a[148:151],v[20:23],a[48:51]
EX v105,v105
MM a[56:59],a[148:151],v[28:31],a[56:59]
RD a[212:215],v225 offset:36864
CV v76,v104,v105
MM a[56:59],a[144:147],v[24:27],a[56:59]
EX v106,v106
MM a[60:63],a[152:155],v[24:27],a[60:63]
EX v107,v107
MM a[60:63],a[156:159],v[28:31],a[60:63]
RD a[216:219],v226 offset:36864
CV v77,v106,v107
MM a[52:55],a[156:159],v[20:23],a[52:55]
EX v108,v108
MM a[52:55],a[152:155],v[16:19],a[52:55]
buffer_load_dwordx4 v231, s[8:11], s64 offen offset:3072 lds
MM a[44:47],a[152:155],v[8:11],a[44:47]
s_add_u32 m0, s65, 65536
EX v109,v109
MM a[44:47],a[156:159],v[12:15],a[44:47]
RD a[220:223],v227 offset:36864
CV v78,v108,v109
MM a[36:39],a[156:159],v[4:7],a[36:39]
EX v110,v110
MM a[36:39],a[152:155],v[0:3],a[36:39]
RD a[224:227],v224 offset:40960
RD a[228:231],v225 offset:40960
MM a[64:67],a[160:163],v[0:3],a[64:67]
EX v111,v111
MM a[64:67],a[164:167],v[4:7],a[64:67]
RD a[232:235],v226 offset:40960
CV v79,v110,v111
MM a[72:75],a[164:167],v[12:15],a[72:75]
EX v112,v112
MM a[72:75],a[160:163],v[8:11],a[72:75]
buffer_load_dwordx4 v235, s[12:15], s81 offen offset:0 lds
MM a[80:83],a[160:163],v[16:19],a[80:83]
EX v113,v113
MM a[80:83],a[164:167],v[20:23],a[80:83]
RD a[236:239],v227 offset:40960
CV v84,v112,v113
MM a[88:91],a[164:167],v[28:31],a[88:91]
EX v114,v114
MM a[88:91],a[160:163],v[24:27],a[88:91]
EX v115,v115
MM a[92:95],a[168:171],v[24:27],a[92:95]
RD a[240:243],v224 offset:45056
CV v85,v114,v115
MM a[92:95],a[172:175],v[28:31],a[92:95]
EX v116,v116
MM a[84:87],a[172:175],v[20:23],a[84:87]
buffer_load_dwordx4 v235, s[12:15], s64 offen offset:1024 lds
MM a[84:87],a[168:171],v[16:19],a[84:87]
EX v117,v117
MM a[76:79],a[168:171],v[8:11],a[76:79]
RD a[244:247],v225 offset:45056
CV v86,v116,v117
MM a[76:79],a[172:175],v[12:15],a[76:79]
EX v118,v118
MM a[68:71],a[172:175],v[4:7],a[68:71]
EX v119,v119
MM a[68:71],a[168:171],v[0:3],a[68:71]
RD a[248:251],v226 offset:45056
CV v87,v118,v119
MM a[96:99],a[176:179],v[0:3],a[96:99]
EX v120,v120
MM a[96:99],a[180:183],v[4:7],a[96:99]
buffer_load_dwordx4 v235, s[12:15], s81 offen offset:2048 lds
)ASM"
R"ASM(
MM a[104:107],a[180:183],v[12:15],a[104:107]
EX v121,v121
MM a[104:107],a[176:179],v[8:11],a[104:107]
RD a[252:255],v227 offset:45056
CV v92,v120,v121
MM a[112:115],a[176:179],v[16:19],a[112:115]
EX v122,v122
MM a[112:115],a[180:183],v[20:23],a[112:115]
EX v123,v123
MM a[120:123],a[180:183],v[28:31],a[120:123]
s_add_u32 s87, s95, 0
buffer_store_dword v243, v248, s[20:23], s87 offen offset:12 sc1
MM a[120:123],a[176:179],v[24:27],a[120:123]
EX v124,v124
MM a[124:127],a[184:187],v[24:27],a[124:127]
v_add_f32 v244, v144, v145
v_add_f32 v245, v148, v149
MM a[124:127],a[188:191],v[28:31],a[124:127]
v_add_f32 v246, v152, v153
v_add_f32 v247, v156, v157
MM a[116:119],a[188:191],v[20:23],a[116:119]
EX v125,v125
MM a[116:119],a[184:187],v[16:19],a[116:119]
buffer_load_dwordx4 v235, s[12:15], s64 offen offset:3072 lds
MM a[108:111],a[184:187],v[8:11],a[108:111]
EX v126,v126
MM a[108:111],a[188:191],v[12:15],a[108:111]
EX v127,v127
MM a[100:103],a[188:191],v[4:7],a[100:103]
CV v93,v122,v123
CV v94,v124,v125
MM a[100:103],a[184:187],v[0:3],a[100:103]
CV v95,v126,v127
v_smfmac_f32_16x16x64_bf16 v[144:147], v[236:239], v[64:71], v234
v_smfmac_f32_16x16x64_bf16 v[148:151], v[236:239], v[72:79], v234
v_smfmac_f32_16x16x64_bf16 v[152:155], v[236:239], v[80:87], v234
v_smfmac_f32_16x16x64_bf16 v[156:159], v[236:239], v[88:95], v234
s_waitcnt lgkmcnt(0)
s_cmp_ge_u32 s16, 108
s_cbranch_scc1 LLAP_%=
MM v[0:3],a[192:195],v[160:163],v[128:131]
s_add_u32 s64, s64, 16384
TR a[128:129],v232 offset:16384
s_add_u32 s81, s64, 896
MM v[0:3],a[196:199],v[176:179],v[0:3]
TR a[130:131],v232 offset:20480
v_max3_f32 v249, v144, v145, v148
MM v[0:3],a[200:203],v[192:195],v[0:3]
TR a[132:133],v232 offset:24576
v_max3_f32 v249, v249, v149, v152
MM v[0:3],a[204:207],v[208:211],v[0:3]
TR a[134:135],v232 offset:28672
v_max3_f32 v249, v249, v153, v156
MM v[4:7],a[220:223],v[208:211],v[128:131]
TR a[136:137],v233 offset:16384
v_max_f32 v249, v249, v157
MM v[4:7],a[216:219],v[192:195],v[4:7]
TR a[138:139],v233 offset:20480
v_cmp_lt_f32 vcc, s71, v249
MM v[4:7],a[212:215],v[176:179],v[4:7]
TR a[140:141],v233 offset:24576
TR a[142:143],v233 offset:28672
MM v[4:7],a[208:211],v[160:163],v[4:7]
s_cbranch_vccnz LSITE13_%=
LBACK13_%=:
MM v[12:15],a[208:211],v[164:167],v[132:135]
v_lshrrev_b32 v127, 8, v248
v_lshlrev_b32 v124, 9, v127
MM v[12:15],a[212:215],v[180:183],v[12:15]
v_and_b32 v126, 192, v248
v_add_u32 v124, v124, v126
MM v[12:15],a[216:219],v[196:199],v[12:15]
v_bfe_u32 v126, v248, 4, 2
v_lshl_add_u32 v124, v126, 3, v124
MM v[12:15],a[220:223],v[212:215],v[12:15]
s_lshr_b32 s76, %[swave], 1
EX v0,v0
MM v[8:11],a[204:207],v[212:215],v[132:135]
s_mul_i32 s76, s76, 10240
EX v1,v1
MM v[8:11],a[200:203],v[196:199],v[8:11]
s_and_b32 s77, %[swave], 1
EX v2,v2
MM v[8:11],a[196:199],v[180:183],v[8:11]
s_lshl_b32 s77, s77, 5
TR a[144:145],v232 offset:16896
CV v0,v0,v1
MM v[8:11],a[192:195],v[164:167],v[8:11]
s_add_u32 s76, s76, s77
EX v3,v3
MM v[16:19],a[192:195],v[168:171],v[136:139]
s_add_u32 s76, s76, %[slds]
EX v4,v4
MM v[16:19],a[196:199],v[184:187],v[16:19]
s_add_u32 s76, s76, 131072
v_add_u32 v124, s76, v124
v_bfe_u32 v126, v248, 4, 3
MM v[16:19],a[200:203],v[200:203],v[16:19]
v_lshl_add_u32 v126, v127, 3, v126
v_lshlrev_b32 v126, 4, v126
MM v[16:19],a[204:207],v[216:219],v[16:19]
s_mul_i32 s76, %[swave], 3072
EX v5,v5
MM v[20:23],a[220:223],v[216:219],v[136:139]
s_add_u32 s76, s76, %[slds]
TR a[146:147],v232 offset:20992
CV v1,v2,v3
MM v[20:23],a[216:219],v[200:203],v[20:23]
s_add_u32 s76, s76, 151552
TR a[148:149],v232 offset:25088
v_add_u32 v125, s76, v126
MM v[20:23],a[212:215],v[184:187],v[20:23]
s_lshr_b32 s76, s48, 7
EX v6,v6
MM v[20:23],a[208:211],v[168:171],v[20:23]
s_lshl_b32 s76, s76, 5
EX v7,v7
MM v[28:31],a[208:211],v[172:175],v[140:143]
s_and_b32 s77, s48, 16
TR a[150:151],v232 offset:29184
TR a[152:153],v233 offset:16896
MM v[28:31],a[212:215],v[188:191],v[28:31]
s_add_u32 s76, s76, s77
EX v8,v8
MM v[28:31],a[216:219],v[204:207],v[28:31]
s_bfe_u32 s77, s48, 131077
TR a[154:155],v233 offset:20992
CV v2,v4,v5
MM v[28:31],a[220:223],v[220:223],v[28:31]
s_lshl_b32 s77, s77, 2
EX v9,v9
s_nop 0
LPG12_%=:
s_cbranch_execz LTR0_%=
MM v[24:27],a[204:207],v[220:223],v[140:143]
s_add_u32 s76, s76, s77
TR a[156:157],v233 offset:25088
CV v3,v6,v7
MM v[24:27],a[200:203],v[204:207],v[24:27]
s_add_u32 s76, s76, %[swave]
EX v10,v10
MM v[24:27],a[196:199],v[188:191],v[24:27]
s_lshl_b32 s97, s76, 2
TR a[158:159],v233 offset:29184
CV v8,v8,v9
MM v[24:27],a[192:195],v[172:175],v[24:27]
s_add_u32 s97, s97, 77824
EX v11,v11
MM v[32:35],a[224:227],v[160:163],v[128:131]
s_lshl_b32 s76, s76, 9
TR a[160:161],v232 offset:17408
TR a[162:163],v232 offset:21504
MM v[32:35],a[228:231],v[176:179],v[32:35]
s_add_u32 s76, s76, 22183936
EX v12,v12
MM v[32:35],a[232:235],v[192:195],v[32:35]
EX v13,v13
MM v[32:35],a[236:239],v[208:211],v[32:35]
TR a[164:165],v232 offset:25600
CV v9,v10,v11
MM v[36:39],a[252:255],v[208:211],v[128:131]
EX v14,v14
MM v[36:39],a[248:251],v[192:195],v[36:39]
TR a[166:167],v232 offset:29696
CV v10,v12,v13
MM v[36:39],a[244:247],v[176:179],v[36:39]
EX v15,v15
MM v[36:39],a[240:243],v[160:163],v[36:39]
TR a[168:169],v233 offset:17408
TR a[170:171],v233 offset:21504
MM v[44:47],a[240:243],v[164:167],v[132:135]
EX v16,v16
MM v[44:47],a[244:247],v[180:183],v[44:47]
EX v17,v17
MM v[44:47],a[248:251],v[196:199],v[44:47]
TR a[172:173],v233 offset:25600
CV v11,v14,v15
MM v[44:47],a[252:255],v[212:215],v[44:47]
EX v18,v18
MM v[40:43],a[236:239],v[212:215],v[132:135]
TR a[174:175],v233 offset:29696
CV v16,v16,v17
MM v[40:43],a[232:235],v[196:199],v[40:43]
EX v19,v19
MM v[40:43],a[228:231],v[180:183],v[40:43]
TR a[176:177],v232 offset:17920
TR a[178:179],v232 offset:22016
MM v[40:43],a[224:227],v[164:167],v[40:43]
EX v20,v20
MM v[48:51],a[224:227],v[168:171],v[136:139]
EX v21,v21
MM v[48:51],a[228:231],v[184:187],v[48:51]
TR a[180:181],v232 offset:26112
CV v17,v18,v19
MM v[48:51],a[232:235],v[200:203],v[48:51]
EX v22,v22
MM v[48:51],a[236:239],v[216:219],v[48:51]
TR a[182:183],v232 offset:30208
CV v18,v20,v21
MM v[52:55],a[252:255],v[216:219],v[136:139]
EX v23,v23
MM v[52:55],a[248:251],v[200:203],v[52:55]
TR a[184:185],v233 offset:17920
TR a[186:187],v233 offset:22016
MM v[52:55],a[244:247],v[184:187],v[52:55]
EX v24,v24
MM v[52:55],a[240:243],v[168:171],v[52:55]
TR a[188:189],v233 offset:26112
CV v19,v22,v23
MM v[60:63],a[240:243],v[172:175],v[140:143]
EX v25,v25
MM v[60:63],a[244:247],v[188:191],v[60:63]
TR a[190:191],v233 offset:30208
CV v24,v24,v25
MM v[60:63],a[248:251],v[204:207],v[60:63]
EX v26,v26
MM v[60:63],a[252:255],v[220:223],v[60:63]
EX v27,v27
MM v[56:59],a[236:239],v[220:223],v[140:143]
EX v28,v28
MM v[56:59],a[232:235],v[204:207],v[56:59]
EX v29,v29
MM v[56:59],a[228:231],v[188:191],v[56:59]
s_getpc_b64 s[98:99]
LTA2_%=:
s_min_u32 s73, %[swave], 3
s_lshl_b32 s73, s73, 12
s_add_u32 s73, s73, LPG13_%=-LTA2_%=
s_add_u32 s98, s98, s73
s_addc_u32 s99, s99, 0
s_mov_b64 exec, 0
s_setpc_b64 s[98:99]
LTR2_%=:
s_mov_b64 exec, -1
CV v25,v26,v27
MM v[56:59],a[224:227],v[172:175],v[56:59]
s_waitcnt vmcnt(17) lgkmcnt(0)
s_barrier
MM a[0:3],a[128:131],v[64:67],a[0:3]
s_add_u32 m0, s65, 16384
RD a[192:195],v224 offset:49152
CV v26,v28,v29
MM a[0:3],a[132:135],v[68:71],a[0:3]
EX v30,v30
MM a[8:11],a[132:135],v[76:79],a[8:11]
EX v31,v31
MM a[8:11],a[128:131],v[72:75],a[8:11]
RD a[196:199],v225 offset:49152
CV v27,v30,v31
MM a[16:19],a[128:131],v[80:83],a[16:19]
s_waitcnt vmcnt(10)
TR v[96:97],v124 offset:0
TR v[98:99],v124 offset:256
MM a[16:19],a[132:135],v[84:87],a[16:19]
RD v[108:111],v125 offset:0
TR v[100:101],v124 offset:2048
MM a[24:27],a[132:135],v[92:95],a[24:27]
TR v[102:103],v124 offset:2304
RD v[112:115],v125 offset:512
MM a[24:27],a[128:131],v[88:91],a[24:27]
TR v[104:105],v124 offset:4096
TR v[106:107],v124 offset:4352
MM a[28:31],a[136:139],v[88:91],a[28:31]
RD v[116:119],v125 offset:1024
RD a[200:203],v226 offset:49152
EX v32,v32
MM a[28:31],a[140:143],v[92:95],a[28:31]
EX v33,v33
MM a[20:23],a[140:143],v[84:87],a[20:23]
EX v34,v34
MM a[20:23],a[136:139],v[80:83],a[20:23]
buffer_load_dwordx4 v228, s[8:11], s64 offen offset:0 lds
MM a[12:15],a[136:139],v[72:75],a[12:15]
EX v35,v35
MM a[12:15],a[140:143],v[76:79],a[12:15]
EX v36,v36
MM a[4:7],a[140:143],v[68:71],a[4:7]
CV v4,v32,v33
CV v5,v34,v35
MM a[4:7],a[136:139],v[64:67],a[4:7]
EX v37,v37
MM a[32:35],a[144:147],v[64:67],a[32:35]
EX v38,v38
MM a[32:35],a[148:151],v[68:71],a[32:35]
s_waitcnt lgkmcnt(1)
buffer_load_dwordx4 v229, s[8:11], s64 offen offset:1024 lds
MM a[40:43],a[148:151],v[76:79],a[40:43]
EX v39,v39
MM v[120:123],v[96:99],v[108:111],0
MM v[120:123],v[100:103],v[112:115],v[120:123]
MM v[120:123],v[104:107],v[116:119],v[120:123]
MM a[40:43],a[144:147],v[72:75],a[40:43]
TR v[96:97],v124 offset:6144
TR v[98:99],v124 offset:6400
MM a[48:51],a[144:147],v[80:83],a[48:51]
RD v[108:111],v125 offset:1536
TR v[100:101],v124 offset:8192
MM a[48:51],a[148:151],v[84:87],a[48:51]
TR v[102:103],v124 offset:8448
RD v[112:115],v125 offset:2048
MM a[56:59],a[148:151],v[92:95],a[56:59]
EX v40,v40
MM a[56:59],a[144:147],v[88:91],a[56:59]
EX v41,v41
MM a[60:63],a[152:155],v[88:91],a[60:63]
RD a[204:207],v227 offset:49152
CV v6,v36,v37
MM a[60:63],a[156:159],v[92:95],a[60:63]
EX v42,v42
MM a[52:55],a[156:159],v[84:87],a[52:55]
RD a[208:211],v224 offset:53248
CV v7,v38,v39
MM a[52:55],a[152:155],v[80:83],a[52:55]
EX v43,v43
MM a[44:47],a[152:155],v[72:75],a[44:47]
buffer_load_dwordx4 v230, s[8:11], s64 offen offset:2048 lds
MM a[44:47],a[156:159],v[76:79],a[44:47]
EX v44,v44
MM a[36:39],a[156:159],v[68:71],a[36:39]
s_waitcnt lgkmcnt(2)
EX v45,v45
MM a[36:39],a[152:155],v[64:67],a[36:39]
RD a[212:215],v225 offset:53248
CV v12,v40,v41
MM v[120:123],v[96:99],v[108:111],v[120:123]
MM v[120:123],v[100:103],v[112:115],v[120:123]
MM a[64:67],a[160:163],v[64:67],a[64:67]
EX v46,v46
MM a[64:67],a[164:167],v[68:71],a[64:67]
RD a[216:219],v226 offset:53248
CV v13,v42,v43
MM a[72:75],a[164:167],v[76:79],a[72:75]
EX v47,v47
MM a[72:75],a[160:163],v[72:75],a[72:75]
buffer_load_dwordx4 v231, s[8:11], s64 offen offset:3072 lds
MM a[80:83],a[160:163],v[80:83],a[80:83]
s_add_u32 m0, s65, 81920
EX v48,v48
MM a[80:83],a[164:167],v[84:87],a[80:83]
RD a[220:223],v227 offset:53248
CV v14,v44,v45
MM a[88:91],a[164:167],v[92:95],a[88:91]
s_mov_b32 exec_lo, 0x00ff00ff
s_mov_b32 exec_hi, 0x00ff00ff
buffer_store_dwordx4 v[120:123], v126, s[20:23], s76 offen offset:0 sc1
s_mov_b64 exec, -1
s_nop 0
MM a[88:91],a[160:163],v[88:91],a[88:91]
EX v49,v49
MM a[92:95],a[168:171],v[88:91],a[92:95]
EX v50,v50
MM a[92:95],a[172:175],v[92:95],a[92:95]
RD a[224:227],v224 offset:57344
CV v15,v46,v47
MM a[84:87],a[172:175],v[84:87],a[84:87]
EX v51,v51
MM a[84:87],a[168:171],v[80:83],a[84:87]
EX v52,v52
MM a[76:79],a[168:171],v[72:75],a[76:79]
buffer_load_dwordx4 v235, s[12:15], s81 offen offset:0 lds
MM a[76:79],a[172:175],v[76:79],a[76:79]
EX v53,v53
MM a[68:71],a[172:175],v[68:71],a[68:71]
RD a[228:231],v225 offset:57344
CV v20,v48,v49
MM a[68:71],a[168:171],v[64:67],a[68:71]
EX v54,v54
MM a[96:99],a[176:179],v[64:67],a[96:99]
RD a[232:235],v226 offset:57344
CV v21,v50,v51
MM a[96:99],a[180:183],v[68:71],a[96:99]
EX v55,v55
MM a[104:107],a[180:183],v[76:79],a[104:107]
RD a[236:239],v227 offset:57344
CV v22,v52,v53
MM a[104:107],a[176:179],v[72:75],a[104:107]
EX v56,v56
MM a[112:115],a[176:179],v[80:83],a[112:115]
buffer_load_dwordx4 v235, s[12:15], s64 offen offset:1024 lds
MM a[112:115],a[180:183],v[84:87],a[112:115]
EX v57,v57
MM a[120:123],a[180:183],v[92:95],a[120:123]
EX v58,v58
MM a[120:123],a[176:179],v[88:91],a[120:123]
RD a[240:243],v224 offset:61440
CV v23,v54,v55
MM a[124:127],a[184:187],v[88:91],a[124:127]
v_add_f32 v244, v144, v145
v_add_f32 v245, v148, v149
MM a[124:127],a[188:191],v[92:95],a[124:127]
v_add_f32 v246, v152, v153
v_add_f32 v247, v156, v157
MM a[116:119],a[188:191],v[84:87],a[116:119]
EX v59,v59
MM a[116:119],a[184:187],v[80:83],a[116:119]
EX v60,v60
MM a[108:111],a[184:187],v[72:75],a[108:111]
)ASM"
R"ASM(
EX v61,v61
MM a[108:111],a[188:191],v[76:79],a[108:111]
RD a[244:247],v225 offset:61440
CV v28,v56,v57
MM a[100:103],a[188:191],v[68:71],a[100:103]
buffer_load_dwordx4 v235, s[12:15], s81 offen offset:2048 lds
MM a[100:103],a[184:187],v[64:67],a[100:103]
EX v62,v62
v_smfmac_f32_16x16x64_bf16 v[144:147], v[236:239], v[0:7], v234
RD a[248:251],v226 offset:61440
RD a[252:255],v227 offset:61440
EX v63,v63
v_smfmac_f32_16x16x64_bf16 v[148:151], v[236:239], v[8:15], v234
CV v29,v58,v59
v_smfmac_f32_16x16x64_bf16 v[152:155], v[236:239], v[16:23], v234
CV v30,v60,v61
CV v31,v62,v63
s_nop 1
v_smfmac_f32_16x16x64_bf16 v[156:159], v[236:239], v[24:31], v234
s_waitcnt lgkmcnt(0)
buffer_load_dwordx4 v235, s[12:15], s64 offen offset:3072 lds
MM v[64:67],a[192:195],v[160:163],v[128:131]
s_add_u32 s64, s64, 16384
TR a[128:129],v232 offset:32768
s_add_u32 s81, s64, 896
MM v[64:67],a[196:199],v[176:179],v[64:67]
TR a[130:131],v232 offset:36864
v_max3_f32 v249, v144, v145, v148
MM v[64:67],a[200:203],v[192:195],v[64:67]
TR a[132:133],v232 offset:40960
v_max3_f32 v249, v249, v149, v152
MM v[64:67],a[204:207],v[208:211],v[64:67]
TR a[134:135],v232 offset:45056
v_max3_f32 v249, v249, v153, v156
MM v[68:71],a[220:223],v[208:211],v[128:131]
TR a[136:137],v233 offset:32768
v_max_f32 v249, v249, v157
MM v[68:71],a[216:219],v[192:195],v[68:71]
TR a[138:139],v233 offset:36864
v_cmp_lt_f32 vcc, s71, v249
MM v[68:71],a[212:215],v[176:179],v[68:71]
TR a[140:141],v233 offset:40960
TR a[142:143],v233 offset:45056
MM v[68:71],a[208:211],v[160:163],v[68:71]
s_cbranch_vccnz LSITE14_%=
LBACK14_%=:
MM v[76:79],a[208:211],v[164:167],v[132:135]
s_add_u32 s48, s48, 16
v_lshrrev_b32 v252, 6, v248
v_mul_u32_u24 v252, 3456, v252
MM v[76:79],a[212:215],v[180:183],v[76:79]
v_and_b32 v251, 48, v248
v_add_u32 v252, v252, v251
MM v[76:79],a[216:219],v[196:199],v[76:79]
s_and_b32 s90, %[swave], 1
EX v64,v64
MM v[76:79],a[220:223],v[212:215],v[76:79]
s_mul_i32 s90, s90, 276480
EX v65,v65
MM v[72:75],a[204:207],v[212:215],v[132:135]
s_bfe_u32 s91, s48, 131077
EX v66,v66
MM v[72:75],a[200:203],v[196:199],v[72:75]
s_mul_i32 s91, s91, 552960
TR a[144:145],v232 offset:33280
CV v64,v64,v65
MM v[72:75],a[196:199],v[180:183],v[72:75]
s_add_u32 s90, s90, s91
EX v67,v67
MM v[72:75],a[192:195],v[164:167],v[72:75]
s_and_b32 s91, s48, 0xffffff80
TR a[146:147],v232 offset:37376
CV v65,v66,v67
MM v[80:83],a[192:195],v[168:171],v[136:139]
s_add_u32 s90, s90, s91
EX v68,v68
MM v[80:83],a[196:199],v[184:187],v[80:83]
s_and_b32 s91, s48, 16
TR a[148:149],v232 offset:41472
TR a[150:151],v232 offset:45568
MM v[80:83],a[200:203],v[200:203],v[80:83]
s_lshl_b32 s91, s91, 2
EX v69,v69
MM v[80:83],a[204:207],v[216:219],v[80:83]
s_add_u32 s66, s90, s91
TR a[152:153],v233 offset:33280
CV v66,v68,v69
MM v[84:87],a[220:223],v[216:219],v[136:139]
s_mul_i32 s91, %[swave], 5120
EX v70,v70
MM v[84:87],a[216:219],v[200:203],v[84:87]
s_add_u32 s91, s91, %[slds]
EX v71,v71
MM v[84:87],a[212:215],v[184:187],v[84:87]
s_add_u32 s67, s91, 131072
TR a[154:155],v233 offset:37376
CV v67,v70,v71
MM v[84:87],a[208:211],v[168:171],v[84:87]
s_and_b32 s18, s17, 255
EX v72,v72
MM v[92:95],a[208:211],v[172:175],v[140:143]
s_sub_u32 s90, s18, 40
s_cmp_ge_u32 s18, 216
s_cselect_b32 s18, s90, s18
MM v[92:95],a[212:215],v[188:191],v[92:95]
s_lshl_b32 s18, s18, 4
EX v73,v73
MM v[92:95],a[216:219],v[204:207],v[92:95]
s_sub_u32 s90, s16, 90
TR a[156:157],v233 offset:41472
CV v72,v72,v73
MM v[92:95],a[220:223],v[220:223],v[92:95]
s_min_u32 s47, s90, s16
EX v74,v74
MM v[88:91],a[204:207],v[220:223],v[140:143]
s_mul_i32 s90, s47, 205
TR a[158:159],v233 offset:45568
TR a[160:161],v232 offset:33792
MM v[88:91],a[200:203],v[204:207],v[88:91]
s_lshr_b32 s46, s90, 11
EX v75,v75
MM v[88:91],a[196:199],v[188:191],v[88:91]
s_mul_i32 s90, s46, 244480
TR a[162:163],v232 offset:37888
CV v73,v74,v75
MM v[88:91],a[192:195],v[172:175],v[88:91]
s_lshl_b32 s91, s47, 7
EX v76,v76
MM v[96:99],a[224:227],v[160:163],v[128:131]
s_add_u32 s90, s90, s91
s_add_u32 s28, s28, s90
s_addc_u32 s29, s29, 0
MM v[96:99],a[228:231],v[176:179],v[96:99]
s_sub_u32 s30, 2211840, s90
EX v77,v77
MM v[96:99],a[232:235],v[192:195],v[96:99]
s_mov_b32 s31, 131072
TR a[164:165],v232 offset:41984
CV v74,v76,v77
MM v[96:99],a[236:239],v[208:211],v[96:99]
EX v78,v78
MM v[100:103],a[252:255],v[208:211],v[128:131]
TR a[166:167],v232 offset:46080
TR a[168:169],v233 offset:33792
MM v[100:103],a[248:251],v[192:195],v[100:103]
EX v79,v79
MM v[100:103],a[244:247],v[176:179],v[100:103]
TR a[170:171],v233 offset:37888
CV v75,v78,v79
MM v[100:103],a[240:243],v[160:163],v[100:103]
EX v80,v80
MM v[108:111],a[240:243],v[164:167],v[132:135]
TR a[172:173],v233 offset:41984
TR a[174:175],v233 offset:46080
MM v[108:111],a[244:247],v[180:183],v[108:111]
EX v81,v81
MM v[108:111],a[248:251],v[196:199],v[108:111]
EX v82,v82
MM v[108:111],a[252:255],v[212:215],v[108:111]
TR a[176:177],v232 offset:34304
CV v80,v80,v81
MM v[104:107],a[236:239],v[212:215],v[132:135]
EX v83,v83
MM v[104:107],a[232:235],v[196:199],v[104:107]
TR a[178:179],v232 offset:38400
CV v81,v82,v83
MM v[104:107],a[228:231],v[180:183],v[104:107]
EX v84,v84
MM v[104:107],a[224:227],v[164:167],v[104:107]
TR a[180:181],v232 offset:42496
TR a[182:183],v232 offset:46592
MM v[112:115],a[224:227],v[168:171],v[136:139]
EX v85,v85
MM v[112:115],a[228:231],v[184:187],v[112:115]
TR a[184:185],v233 offset:34304
CV v82,v84,v85
MM v[112:115],a[232:235],v[200:203],v[112:115]
EX v86,v86
MM v[112:115],a[236:239],v[216:219],v[112:115]
EX v87,v87
MM v[116:119],a[252:255],v[216:219],v[136:139]
TR a[186:187],v233 offset:38400
CV v83,v86,v87
MM v[116:119],a[248:251],v[200:203],v[116:119]
EX v88,v88
MM v[116:119],a[244:247],v[184:187],v[116:119]
TR a[188:189],v233 offset:42496
TR a[190:191],v233 offset:46592
MM v[116:119],a[240:243],v[168:171],v[116:119]
EX v89,v89
MM v[124:127],a[240:243],v[172:175],v[140:143]
EX v90,v90
MM v[124:127],a[244:247],v[188:191],v[124:127]
EX v91,v91
MM v[124:127],a[248:251],v[204:207],v[124:127]
EX v92,v92
MM v[124:127],a[252:255],v[220:223],v[124:127]
EX v93,v93
MM v[120:123],a[236:239],v[220:223],v[140:143]
EX v94,v94
MM v[120:123],a[232:235],v[204:207],v[120:123]
EX v95,v95
MM v[120:123],a[228:231],v[188:191],v[120:123]
EX v96,v96
MM v[120:123],a[224:227],v[172:175],v[120:123]
s_waitcnt vmcnt(14) lgkmcnt(0)
s_barrier
MM a[0:3],a[128:131],v[0:3],a[0:3]
s_add_u32 m0, s65, 32768
CV v88,v88,v89
CV v89,v90,v91
MM a[0:3],a[132:135],v[4:7],a[0:3]
RD a[192:195],v224 offset:0
CV v90,v92,v93
MM a[8:11],a[132:135],v[12:15],a[8:11]
RD a[196:199],v225 offset:0
CV v91,v94,v95
MM a[8:11],a[128:131],v[8:11],a[8:11]
buffer_load_dwordx4 v228, s[8:11], s64 offen offset:0 lds
MM a[16:19],a[128:131],v[16:19],a[16:19]
RD a[200:203],v226 offset:0
RD a[204:207],v227 offset:0
MM a[16:19],a[132:135],v[20:23],a[16:19]
EX v97,v97
MM a[24:27],a[132:135],v[28:31],a[24:27]
RD a[208:211],v224 offset:4096
CV v68,v96,v97
MM a[24:27],a[128:131],v[24:27],a[24:27]
EX v98,v98
MM a[28:31],a[136:139],v[24:27],a[28:31]
buffer_load_dwordx4 v229, s[8:11], s64 offen offset:1024 lds
MM a[28:31],a[140:143],v[28:31],a[28:31]
EX v99,v99
MM a[20:23],a[140:143],v[20:23],a[20:23]
RD a[212:215],v225 offset:4096
CV v69,v98,v99
MM a[20:23],a[136:139],v[16:19],a[20:23]
EX v100,v100
MM a[12:15],a[136:139],v[8:11],a[12:15]
EX v101,v101
MM a[12:15],a[140:143],v[12:15],a[12:15]
RD a[216:219],v226 offset:4096
CV v70,v100,v101
MM a[4:7],a[140:143],v[4:7],a[4:7]
EX v102,v102
MM a[4:7],a[136:139],v[0:3],a[4:7]
buffer_load_dwordx4 v230, s[8:11], s64 offen offset:2048 lds
MM a[32:35],a[144:147],v[0:3],a[32:35]
EX v103,v103
MM a[32:35],a[148:151],v[4:7],a[32:35]
RD a[220:223],v227 offset:4096
CV v71,v102,v103
MM a[40:43],a[148:151],v[12:15],a[40:43]
EX v104,v104
MM a[40:43],a[144:147],v[8:11],a[40:43]
EX v105,v105
MM a[48:51],a[144:147],v[16:19],a[48:51]
RD a[224:227],v224 offset:8192
CV v76,v104,v105
MM a[48:51],a[148:151],v[20:23],a[48:51]
EX v106,v106
MM a[56:59],a[148:151],v[28:31],a[56:59]
buffer_load_dwordx4 v231, s[8:11], s64 offen offset:3072 lds
MM a[56:59],a[144:147],v[24:27],a[56:59]
s_add_u32 m0, s65, 98304
EX v107,v107
MM a[60:63],a[152:155],v[24:27],a[60:63]
RD a[228:231],v225 offset:8192
CV v77,v106,v107
MM a[60:63],a[156:159],v[28:31],a[60:63]
EX v108,v108
MM a[52:55],a[156:159],v[20:23],a[52:55]
EX v109,v109
MM a[52:55],a[152:155],v[16:19],a[52:55]
RD a[232:235],v226 offset:8192
CV v78,v108,v109
MM a[44:47],a[152:155],v[8:11],a[44:47]
EX v110,v110
MM a[44:47],a[156:159],v[12:15],a[44:47]
buffer_load_dwordx4 v235, s[12:15], s81 offen offset:0 lds
MM a[36:39],a[156:159],v[4:7],a[36:39]
EX v111,v111
MM a[36:39],a[152:155],v[0:3],a[36:39]
RD a[236:239],v227 offset:8192
CV v79,v110,v111
MM a[64:67],a[160:163],v[0:3],a[64:67]
EX v112,v112
MM a[64:67],a[164:167],v[4:7],a[64:67]
EX v113,v113
MM a[72:75],a[164:167],v[12:15],a[72:75]
RD a[240:243],v224 offset:12288
CV v84,v112,v113
MM a[72:75],a[160:163],v[8:11],a[72:75]
buffer_load_dwordx4 v235, s[12:15], s64 offen offset:1024 lds
MM a[80:83],a[160:163],v[16:19],a[80:83]
s_waitcnt vmcnt(10)
v_mov_b32 v250, s69
s_mov_b64 exec, 1
buffer_store_dword v250, off, s[20:23], s97 offset:0 sc1
s_mov_b64 exec, -1
MM a[80:83],a[164:167],v[20:23],a[80:83]
s_add_u32 m0, s67, 0
s_add_u32 s85, s66, 0
buffer_load_dwordx4 v252, s[36:39], s85 offen offset:0 lds
MM a[88:91],a[164:167],v[28:31],a[88:91]
EX v114,v114
MM a[88:91],a[160:163],v[24:27],a[88:91]
EX v115,v115
MM a[92:95],a[168:171],v[24:27],a[92:95]
RD a[244:247],v225 offset:12288
CV v85,v114,v115
MM a[92:95],a[172:175],v[28:31],a[92:95]
EX v116,v116
MM a[84:87],a[172:175],v[20:23],a[84:87]
EX v117,v117
MM a[84:87],a[168:171],v[16:19],a[84:87]
RD a[248:251],v226 offset:12288
CV v86,v116,v117
MM a[76:79],a[168:171],v[8:11],a[76:79]
EX v118,v118
MM a[76:79],a[172:175],v[12:15],a[76:79]
EX v119,v119
MM a[68:71],a[172:175],v[4:7],a[68:71]
RD a[252:255],v227 offset:12288
CV v87,v118,v119
MM a[68:71],a[168:171],v[0:3],a[68:71]
s_add_u32 m0, s65, 98304
s_nop 0
buffer_load_dwordx4 v235, s[12:15], s81 offen offset:2048 lds
MM a[96:99],a[176:179],v[0:3],a[96:99]
EX v120,v120
MM a[96:99],a[180:183],v[4:7],a[96:99]
EX v121,v121
MM a[104:107],a[180:183],v[12:15],a[104:107]
EX v122,v122
MM a[104:107],a[176:179],v[8:11],a[104:107]
s_nop 0
LPG13_%=:
s_cbranch_execz LTR2_%=
buffer_load_dwordx4 v235, s[12:15], s64 offen offset:3072 lds
MM a[112:115],a[176:179],v[16:19],a[112:115]
EX v123,v123
MM a[112:115],a[180:183],v[20:23],a[112:115]
EX v124,v124
MM a[120:123],a[180:183],v[28:31],a[120:123]
EX v125,v125
MM a[120:123],a[176:179],v[24:27],a[120:123]
EX v126,v126
MM a[124:127],a[184:187],v[24:27],a[124:127]
v_add_f32 v244, v144, v145
v_add_f32 v245, v148, v149
MM a[124:127],a[188:191],v[28:31],a[124:127]
v_add_f32 v246, v152, v153
v_add_f32 v247, v156, v157
MM a[116:119],a[188:191],v[20:23],a[116:119]
EX v127,v127
MM a[116:119],a[184:187],v[16:19],a[116:119]
CV v92,v120,v121
CV v93,v122,v123
MM a[108:111],a[184:187],v[8:11],a[108:111]
CV v94,v124,v125
CV v95,v126,v127
MM a[108:111],a[188:191],v[12:15],a[108:111]
MM a[100:103],a[188:191],v[4:7],a[100:103]
MM a[100:103],a[184:187],v[0:3],a[100:103]
v_smfmac_f32_16x16x64_bf16 v[144:147], v[236:239], v[64:71], v234
v_smfmac_f32_16x16x64_bf16 v[148:151], v[236:239], v[72:79], v234
v_smfmac_f32_16x16x64_bf16 v[152:155], v[236:239], v[80:87], v234
v_smfmac_f32_16x16x64_bf16 v[156:159], v[236:239], v[88:95], v234
s_waitcnt lgkmcnt(0)
MM v[0:3],a[192:195],v[160:163],v[128:131]
s_add_u32 s64, s64, 16384
TR a[128:129],v232 offset:49152
s_add_u32 s81, s64, 896
MM v[0:3],a[196:199],v[176:179],v[0:3]
TR a[130:131],v232 offset:53248
v_max3_f32 v249, v144, v145, v148
MM v[0:3],a[200:203],v[192:195],v[0:3]
TR a[132:133],v232 offset:57344
v_max3_f32 v249, v249, v149, v152
MM v[0:3],a[204:207],v[208:211],v[0:3]
TR a[134:135],v232 offset:61440
v_max3_f32 v249, v249, v153, v156
MM v[4:7],a[220:223],v[208:211],v[128:131]
TR a[136:137],v233 offset:49152
v_max_f32 v249, v249, v157
MM v[4:7],a[216:219],v[192:195],v[4:7]
TR a[138:139],v233 offset:53248
)ASM"
R"ASM(
v_cmp_lt_f32 vcc, s71, v249
MM v[4:7],a[212:215],v[176:179],v[4:7]
TR a[140:141],v233 offset:57344
TR a[142:143],v233 offset:61440
MM v[4:7],a[208:211],v[160:163],v[4:7]
s_cbranch_vccnz LSITE15_%=
LBACK15_%=:
MM v[12:15],a[208:211],v[164:167],v[132:135]
s_add_u32 m0, s67, 1024
s_add_u32 s85, s66, 55296
buffer_load_dwordx4 v252, s[36:39], s85 offen offset:0 lds
MM v[12:15],a[212:215],v[180:183],v[12:15]
EX v0,v0
MM v[12:15],a[216:219],v[196:199],v[12:15]
EX v1,v1
MM v[12:15],a[220:223],v[212:215],v[12:15]
TR a[144:145],v232 offset:49664
CV v0,v0,v1
MM v[8:11],a[204:207],v[212:215],v[132:135]
EX v2,v2
MM v[8:11],a[200:203],v[196:199],v[8:11]
EX v3,v3
MM v[8:11],a[196:199],v[180:183],v[8:11]
TR a[146:147],v232 offset:53760
CV v1,v2,v3
MM v[8:11],a[192:195],v[164:167],v[8:11]
EX v4,v4
MM v[16:19],a[192:195],v[168:171],v[136:139]
TR a[148:149],v232 offset:57856
TR a[150:151],v232 offset:61952
MM v[16:19],a[196:199],v[184:187],v[16:19]
EX v5,v5
MM v[16:19],a[200:203],v[200:203],v[16:19]
TR a[152:153],v233 offset:49664
CV v2,v4,v5
MM v[16:19],a[204:207],v[216:219],v[16:19]
EX v6,v6
MM v[20:23],a[220:223],v[216:219],v[136:139]
EX v7,v7
MM v[20:23],a[216:219],v[200:203],v[20:23]
TR a[154:155],v233 offset:53760
CV v3,v6,v7
MM v[20:23],a[212:215],v[184:187],v[20:23]
EX v8,v8
MM v[20:23],a[208:211],v[168:171],v[20:23]
TR a[156:157],v233 offset:57856
TR a[158:159],v233 offset:61952
MM v[28:31],a[208:211],v[172:175],v[140:143]
EX v9,v9
MM v[28:31],a[212:215],v[188:191],v[28:31]
TR a[160:161],v232 offset:50176
CV v8,v8,v9
MM v[28:31],a[216:219],v[204:207],v[28:31]
EX v10,v10
MM v[28:31],a[220:223],v[220:223],v[28:31]
EX v11,v11
MM v[24:27],a[204:207],v[220:223],v[140:143]
TR a[162:163],v232 offset:54272
CV v9,v10,v11
MM v[24:27],a[200:203],v[204:207],v[24:27]
TR a[164:165],v232 offset:58368
TR a[166:167],v232 offset:62464
MM v[24:27],a[196:199],v[188:191],v[24:27]
EX v12,v12
MM v[24:27],a[192:195],v[172:175],v[24:27]
EX v13,v13
MM v[32:35],a[224:227],v[160:163],v[128:131]
TR a[168:169],v233 offset:50176
CV v10,v12,v13
MM v[32:35],a[228:231],v[176:179],v[32:35]
EX v14,v14
MM v[32:35],a[232:235],v[192:195],v[32:35]
TR a[170:171],v233 offset:54272
TR a[172:173],v233 offset:58368
MM v[32:35],a[236:239],v[208:211],v[32:35]
EX v15,v15
MM v[36:39],a[252:255],v[208:211],v[128:131]
TR a[174:175],v233 offset:62464
CV v11,v14,v15
MM v[36:39],a[248:251],v[192:195],v[36:39]
EX v16,v16
MM v[36:39],a[244:247],v[176:179],v[36:39]
EX v17,v17
MM v[36:39],a[240:243],v[160:163],v[36:39]
TR a[176:177],v232 offset:50688
CV v16,v16,v17
MM v[44:47],a[240:243],v[164:167],v[132:135]
s_add_u32 m0, s67, 2048
s_add_u32 s85, s66, 110592
buffer_load_dwordx4 v252, s[36:39], s85 offen offset:0 lds
MM v[44:47],a[244:247],v[180:183],v[44:47]
EX v18,v18
MM v[44:47],a[248:251],v[196:199],v[44:47]
TR a[178:179],v232 offset:54784
TR a[180:181],v232 offset:58880
MM v[44:47],a[252:255],v[212:215],v[44:47]
EX v19,v19
MM v[40:43],a[236:239],v[212:215],v[132:135]
TR a[182:183],v232 offset:62976
CV v17,v18,v19
MM v[40:43],a[232:235],v[196:199],v[40:43]
EX v20,v20
MM v[40:43],a[228:231],v[180:183],v[40:43]
EX v21,v21
MM v[40:43],a[224:227],v[164:167],v[40:43]
TR a[184:185],v233 offset:50688
CV v18,v20,v21
MM v[48:51],a[224:227],v[168:171],v[136:139]
EX v22,v22
MM v[48:51],a[228:231],v[184:187],v[48:51]
TR a[186:187],v233 offset:54784
TR a[188:189],v233 offset:58880
MM v[48:51],a[232:235],v[200:203],v[48:51]
EX v23,v23
MM v[48:51],a[236:239],v[216:219],v[48:51]
TR a[190:191],v233 offset:62976
CV v19,v22,v23
MM v[52:55],a[252:255],v[216:219],v[136:139]
EX v24,v24
MM v[52:55],a[248:251],v[200:203],v[52:55]
EX v25,v25
MM v[52:55],a[244:247],v[184:187],v[52:55]
EX v26,v26
MM v[52:55],a[240:243],v[168:171],v[52:55]
EX v27,v27
MM v[60:63],a[240:243],v[172:175],v[140:143]
EX v28,v28
MM v[60:63],a[244:247],v[188:191],v[60:63]
EX v29,v29
MM v[60:63],a[248:251],v[204:207],v[60:63]
EX v30,v30
MM v[60:63],a[252:255],v[220:223],v[60:63]
EX v31,v31
MM v[56:59],a[236:239],v[220:223],v[140:143]
EX v32,v32
MM v[56:59],a[232:235],v[204:207],v[56:59]
EX v33,v33
MM v[56:59],a[228:231],v[188:191],v[56:59]
EX v34,v34
MM v[56:59],a[224:227],v[172:175],v[56:59]
s_waitcnt lgkmcnt(0)
s_barrier
MM a[0:3],a[128:131],v[64:67],a[0:3]
s_add_u32 m0, s65, 49152
CV v24,v24,v25
CV v25,v26,v27
MM a[0:3],a[132:135],v[68:71],a[0:3]
RD a[192:195],v224 offset:16384
CV v26,v28,v29
MM a[8:11],a[132:135],v[76:79],a[8:11]
RD a[196:199],v225 offset:16384
CV v27,v30,v31
MM a[8:11],a[128:131],v[72:75],a[8:11]
buffer_load_dwordx4 v228, s[8:11], s64 offen offset:0 lds
MM a[16:19],a[128:131],v[80:83],a[16:19]
s_add_u32 m0, s67, 3072
s_add_u32 s85, s66, 165888
buffer_load_dwordx4 v252, s[36:39], s85 offen offset:0 lds
MM a[16:19],a[132:135],v[84:87],a[16:19]
RD a[200:203],v226 offset:16384
CV v4,v32,v33
MM a[24:27],a[132:135],v[92:95],a[24:27]
RD a[204:207],v227 offset:16384
RD a[208:211],v224 offset:20480
MM a[24:27],a[128:131],v[88:91],a[24:27]
s_add_u32 m0, s65, 49152
s_nop 0
buffer_load_dwordx4 v229, s[8:11], s64 offen offset:1024 lds
MM a[28:31],a[136:139],v[88:91],a[28:31]
EX v35,v35
MM a[28:31],a[140:143],v[92:95],a[28:31]
RD a[212:215],v225 offset:20480
CV v5,v34,v35
MM a[20:23],a[140:143],v[84:87],a[20:23]
EX v36,v36
MM a[20:23],a[136:139],v[80:83],a[20:23]
EX v37,v37
MM a[12:15],a[136:139],v[72:75],a[12:15]
RD a[216:219],v226 offset:20480
CV v6,v36,v37
MM a[12:15],a[140:143],v[76:79],a[12:15]
buffer_load_dwordx4 v230, s[8:11], s64 offen offset:2048 lds
MM a[4:7],a[140:143],v[68:71],a[4:7]
EX v38,v38
MM a[4:7],a[136:139],v[64:67],a[4:7]
EX v39,v39
MM a[32:35],a[144:147],v[64:67],a[32:35]
RD a[220:223],v227 offset:20480
CV v7,v38,v39
MM a[32:35],a[148:151],v[68:71],a[32:35]
EX v40,v40
MM a[40:43],a[148:151],v[76:79],a[40:43]
EX v41,v41
MM a[40:43],a[144:147],v[72:75],a[40:43]
RD a[224:227],v224 offset:24576
CV v12,v40,v41
MM a[48:51],a[144:147],v[80:83],a[48:51]
buffer_load_dwordx4 v231, s[8:11], s64 offen offset:3072 lds
MM a[48:51],a[148:151],v[84:87],a[48:51]
s_add_u32 m0, s65, 114688
EX v42,v42
MM a[56:59],a[148:151],v[92:95],a[56:59]
EX v43,v43
MM a[56:59],a[144:147],v[88:91],a[56:59]
RD a[228:231],v225 offset:24576
CV v13,v42,v43
MM a[60:63],a[152:155],v[88:91],a[60:63]
EX v44,v44
MM a[60:63],a[156:159],v[92:95],a[60:63]
buffer_load_dwordx4 v235, s[12:15], s81 offen offset:0 lds
MM a[52:55],a[156:159],v[84:87],a[52:55]
EX v45,v45
MM a[52:55],a[152:155],v[80:83],a[52:55]
RD a[232:235],v226 offset:24576
CV v14,v44,v45
MM a[44:47],a[152:155],v[72:75],a[44:47]
EX v46,v46
MM a[44:47],a[156:159],v[76:79],a[44:47]
EX v47,v47
MM a[36:39],a[156:159],v[68:71],a[36:39]
RD a[236:239],v227 offset:24576
CV v15,v46,v47
MM a[36:39],a[152:155],v[64:67],a[36:39]
EX v48,v48
MM a[64:67],a[160:163],v[64:67],a[64:67]
buffer_load_dwordx4 v235, s[12:15], s64 offen offset:1024 lds
MM a[64:67],a[164:167],v[68:71],a[64:67]
EX v49,v49
MM a[72:75],a[164:167],v[76:79],a[72:75]
RD a[240:243],v224 offset:28672
CV v20,v48,v49
MM a[72:75],a[160:163],v[72:75],a[72:75]
EX v50,v50
MM a[80:83],a[160:163],v[80:83],a[80:83]
EX v51,v51
MM a[80:83],a[164:167],v[84:87],a[80:83]
RD a[244:247],v225 offset:28672
CV v21,v50,v51
MM a[88:91],a[164:167],v[92:95],a[88:91]
EX v52,v52
MM a[88:91],a[160:163],v[88:91],a[88:91]
buffer_load_dwordx4 v235, s[12:15], s81 offen offset:2048 lds
MM a[92:95],a[168:171],v[88:91],a[92:95]
EX v53,v53
MM a[92:95],a[172:175],v[92:95],a[92:95]
RD a[248:251],v226 offset:28672
CV v22,v52,v53
MM a[84:87],a[172:175],v[84:87],a[84:87]
EX v54,v54
MM a[84:87],a[168:171],v[80:83],a[84:87]
EX v55,v55
MM a[76:79],a[168:171],v[72:75],a[76:79]
RD a[252:255],v227 offset:28672
CV v23,v54,v55
MM a[76:79],a[172:175],v[76:79],a[76:79]
EX v56,v56
MM a[68:71],a[172:175],v[68:71],a[68:71]
buffer_load_dwordx4 v235, s[12:15], s64 offen offset:3072 lds
MM a[68:71],a[168:171],v[64:67],a[68:71]
EX v57,v57
MM a[96:99],a[176:179],v[64:67],a[96:99]
EX v58,v58
MM a[96:99],a[180:183],v[68:71],a[96:99]
EX v59,v59
MM a[104:107],a[180:183],v[76:79],a[104:107]
EX v60,v60
MM a[104:107],a[176:179],v[72:75],a[104:107]
EX v61,v61
MM a[112:115],a[176:179],v[80:83],a[112:115]
EX v62,v62
MM a[112:115],a[180:183],v[84:87],a[112:115]
EX v63,v63
MM a[120:123],a[180:183],v[92:95],a[120:123]
s_add_u32 m0, s67, 4096
s_add_u32 s85, s66, 221184
buffer_load_dwordx4 v252, s[36:39], s85 offen offset:0 lds
MM a[120:123],a[176:179],v[88:91],a[120:123]
CV v28,v56,v57
CV v29,v58,v59
MM a[124:127],a[184:187],v[88:91],a[124:127]
v_add_f32 v244, v144, v145
v_add_f32 v245, v148, v149
MM a[124:127],a[188:191],v[92:95],a[124:127]
v_add_f32 v246, v152, v153
v_add_f32 v247, v156, v157
MM a[116:119],a[188:191],v[84:87],a[116:119]
CV v30,v60,v61
CV v31,v62,v63
MM a[116:119],a[184:187],v[80:83],a[116:119]
MM a[108:111],a[184:187],v[72:75],a[108:111]
MM a[108:111],a[188:191],v[76:79],a[108:111]
MM a[100:103],a[188:191],v[68:71],a[100:103]
MM a[100:103],a[184:187],v[64:67],a[100:103]
v_smfmac_f32_16x16x64_bf16 v[144:147], v[236:239], v[0:7], v234
v_smfmac_f32_16x16x64_bf16 v[148:151], v[236:239], v[8:15], v234
v_smfmac_f32_16x16x64_bf16 v[152:155], v[236:239], v[16:23], v234
v_smfmac_f32_16x16x64_bf16 v[156:159], v[236:239], v[24:31], v234
s_waitcnt lgkmcnt(0)
MM v[64:67],a[192:195],v[160:163],v[128:131]
s_add_u32 s64, s64, 16384
TR a[128:129],v232 offset:0
s_add_u32 s81, s64, 896
MM v[64:67],a[196:199],v[176:179],v[64:67]
TR a[130:131],v232 offset:4096
v_max3_f32 v249, v144, v145, v148
MM v[64:67],a[200:203],v[192:195],v[64:67]
TR a[132:133],v232 offset:8192
v_max3_f32 v249, v249, v149, v152
MM v[64:67],a[204:207],v[208:211],v[64:67]
TR a[134:135],v232 offset:12288
v_max3_f32 v249, v249, v153, v156
MM v[68:71],a[220:223],v[208:211],v[128:131]
TR a[136:137],v233 offset:0
v_max_f32 v249, v249, v157
MM v[68:71],a[216:219],v[192:195],v[68:71]
TR a[138:139],v233 offset:4096
v_cmp_lt_f32 vcc, s71, v249
MM v[68:71],a[212:215],v[176:179],v[68:71]
TR a[140:141],v233 offset:8192
TR a[142:143],v233 offset:12288
MM v[68:71],a[208:211],v[160:163],v[68:71]
s_cbranch_vccnz LSITE16_%=
LBACK16_%=:
MM v[76:79],a[208:211],v[164:167],v[132:135]
EX v64,v64
MM v[76:79],a[212:215],v[180:183],v[76:79]
EX v65,v65
MM v[76:79],a[216:219],v[196:199],v[76:79]
TR a[144:145],v232 offset:512
CV v64,v64,v65
MM v[76:79],a[220:223],v[212:215],v[76:79]
EX v66,v66
MM v[72:75],a[204:207],v[212:215],v[132:135]
EX v67,v67
MM v[72:75],a[200:203],v[196:199],v[72:75]
TR a[146:147],v232 offset:4608
CV v65,v66,v67
MM v[72:75],a[196:199],v[180:183],v[72:75]
EX v68,v68
MM v[72:75],a[192:195],v[164:167],v[72:75]
TR a[148:149],v232 offset:8704
TR a[150:151],v232 offset:12800
MM v[80:83],a[192:195],v[168:171],v[136:139]
EX v69,v69
MM v[80:83],a[196:199],v[184:187],v[80:83]
TR a[152:153],v233 offset:512
CV v66,v68,v69
MM v[80:83],a[200:203],v[200:203],v[80:83]
EX v70,v70
MM v[80:83],a[204:207],v[216:219],v[80:83]
TR a[154:155],v233 offset:4608
TR a[156:157],v233 offset:8704
MM v[84:87],a[220:223],v[216:219],v[136:139]
s_add_u32 s87, s95, 1024
buffer_store_dword v243, v248, s[20:23], s87 offen offset:12 sc1
MM v[84:87],a[216:219],v[200:203],v[84:87]
EX v71,v71
MM v[84:87],a[212:215],v[184:187],v[84:87]
TR a[158:159],v233 offset:12800
CV v67,v70,v71
MM v[84:87],a[208:211],v[168:171],v[84:87]
EX v72,v72
MM v[92:95],a[208:211],v[172:175],v[140:143]
EX v73,v73
MM v[92:95],a[212:215],v[188:191],v[92:95]
TR a[160:161],v232 offset:1024
CV v72,v72,v73
MM v[92:95],a[216:219],v[204:207],v[92:95]
EX v74,v74
MM v[92:95],a[220:223],v[220:223],v[92:95]
EX v75,v75
MM v[88:91],a[204:207],v[220:223],v[140:143]
TR a[162:163],v232 offset:5120
CV v73,v74,v75
MM v[88:91],a[200:203],v[204:207],v[88:91]
TR a[164:165],v232 offset:9216
TR a[166:167],v232 offset:13312
MM v[88:91],a[196:199],v[188:191],v[88:91]
EX v76,v76
MM v[88:91],a[192:195],v[172:175],v[88:91]
EX v77,v77
MM v[96:99],a[224:227],v[160:163],v[128:131]
TR a[168:169],v233 offset:1024
CV v74,v76,v77
MM v[96:99],a[228:231],v[176:179],v[96:99]
EX v78,v78
MM v[96:99],a[232:235],v[192:195],v[96:99]
TR a[170:171],v233 offset:5120
TR a[172:173],v233 offset:9216
MM v[96:99],a[236:239],v[208:211],v[96:99]
)ASM"
R"ASM(
EX v79,v79
MM v[100:103],a[252:255],v[208:211],v[128:131]
TR a[174:175],v233 offset:13312
CV v75,v78,v79
MM v[100:103],a[248:251],v[192:195],v[100:103]
EX v80,v80
MM v[100:103],a[244:247],v[176:179],v[100:103]
EX v81,v81
MM v[100:103],a[240:243],v[160:163],v[100:103]
TR a[176:177],v232 offset:1536
CV v80,v80,v81
MM v[108:111],a[240:243],v[164:167],v[132:135]
EX v82,v82
MM v[108:111],a[244:247],v[180:183],v[108:111]
TR a[178:179],v232 offset:5632
TR a[180:181],v232 offset:9728
MM v[108:111],a[248:251],v[196:199],v[108:111]
EX v83,v83
MM v[108:111],a[252:255],v[212:215],v[108:111]
TR a[182:183],v232 offset:13824
CV v81,v82,v83
MM v[104:107],a[236:239],v[212:215],v[132:135]
EX v84,v84
MM v[104:107],a[232:235],v[196:199],v[104:107]
EX v85,v85
MM v[104:107],a[228:231],v[180:183],v[104:107]
TR a[184:185],v233 offset:1536
CV v82,v84,v85
MM v[104:107],a[224:227],v[164:167],v[104:107]
EX v86,v86
MM v[112:115],a[224:227],v[168:171],v[136:139]
TR a[186:187],v233 offset:5632
TR a[188:189],v233 offset:9728
MM v[112:115],a[228:231],v[184:187],v[112:115]
EX v87,v87
MM v[112:115],a[232:235],v[200:203],v[112:115]
TR a[190:191],v233 offset:13824
CV v83,v86,v87
MM v[112:115],a[236:239],v[216:219],v[112:115]
EX v88,v88
MM v[116:119],a[252:255],v[216:219],v[136:139]
EX v89,v89
MM v[116:119],a[248:251],v[200:203],v[116:119]
EX v90,v90
MM v[116:119],a[244:247],v[184:187],v[116:119]
EX v91,v91
MM v[116:119],a[240:243],v[168:171],v[116:119]
EX v92,v92
MM v[124:127],a[240:243],v[172:175],v[140:143]
EX v93,v93
MM v[124:127],a[244:247],v[188:191],v[124:127]
EX v94,v94
MM v[124:127],a[248:251],v[204:207],v[124:127]
EX v95,v95
MM v[124:127],a[252:255],v[220:223],v[124:127]
EX v96,v96
MM v[120:123],a[236:239],v[220:223],v[140:143]
EX v97,v97
MM v[120:123],a[232:235],v[204:207],v[120:123]
EX v98,v98
MM v[120:123],a[228:231],v[188:191],v[120:123]
EX v99,v99
MM v[120:123],a[224:227],v[172:175],v[120:123]
s_waitcnt vmcnt(19) lgkmcnt(0)
s_barrier
MM a[0:3],a[128:131],v[0:3],a[0:3]
s_add_u32 m0, s65, 0
CV v88,v88,v89
CV v89,v90,v91
MM a[0:3],a[132:135],v[4:7],a[0:3]
RD a[192:195],v224 offset:32768
CV v90,v92,v93
MM a[8:11],a[132:135],v[12:15],a[8:11]
RD a[196:199],v225 offset:32768
CV v91,v94,v95
MM a[8:11],a[128:131],v[8:11],a[8:11]
buffer_load_dwordx4 v228, s[8:11], s64 offen offset:0 lds
MM a[16:19],a[128:131],v[16:19],a[16:19]
RD a[200:203],v226 offset:32768
CV v68,v96,v97
MM a[16:19],a[132:135],v[20:23],a[16:19]
RD a[204:207],v227 offset:32768
CV v69,v98,v99
MM a[24:27],a[132:135],v[28:31],a[24:27]
buffer_load_dwordx4 v229, s[8:11], s64 offen offset:1024 lds
MM a[24:27],a[128:131],v[24:27],a[24:27]
RD a[208:211],v224 offset:36864
RD a[212:215],v225 offset:36864
MM a[28:31],a[136:139],v[24:27],a[28:31]
buffer_load_dwordx4 v230, s[8:11], s64 offen offset:2048 lds
MM a[28:31],a[140:143],v[28:31],a[28:31]
EX v100,v100
MM a[20:23],a[140:143],v[20:23],a[20:23]
EX v101,v101
MM a[20:23],a[136:139],v[16:19],a[20:23]
RD a[216:219],v226 offset:36864
CV v70,v100,v101
MM a[12:15],a[136:139],v[8:11],a[12:15]
EX v102,v102
MM a[12:15],a[140:143],v[12:15],a[12:15]
RD a[220:223],v227 offset:36864
RD a[224:227],v224 offset:40960
MM a[4:7],a[140:143],v[4:7],a[4:7]
EX v103,v103
MM a[4:7],a[136:139],v[0:3],a[4:7]
RD a[228:231],v225 offset:40960
CV v71,v102,v103
MM a[32:35],a[144:147],v[0:3],a[32:35]
EX v104,v104
MM a[32:35],a[148:151],v[4:7],a[32:35]
buffer_load_dwordx4 v231, s[8:11], s64 offen offset:3072 lds
MM a[40:43],a[148:151],v[12:15],a[40:43]
s_add_u32 m0, s65, 65536
EX v105,v105
MM a[40:43],a[144:147],v[8:11],a[40:43]
RD a[232:235],v226 offset:40960
CV v76,v104,v105
MM a[48:51],a[144:147],v[16:19],a[48:51]
EX v106,v106
MM a[48:51],a[148:151],v[20:23],a[48:51]
EX v107,v107
MM a[56:59],a[148:151],v[28:31],a[56:59]
RD a[236:239],v227 offset:40960
CV v77,v106,v107
MM a[56:59],a[144:147],v[24:27],a[56:59]
EX v108,v108
MM a[60:63],a[152:155],v[24:27],a[60:63]
buffer_load_dwordx4 v235, s[12:15], s81 offen offset:0 lds
MM a[60:63],a[156:159],v[28:31],a[60:63]
EX v109,v109
MM a[52:55],a[156:159],v[20:23],a[52:55]
s_add_u32 s87, s95, 2048
buffer_store_dword v243, v248, s[20:23], s87 offen offset:12 sc1
MM a[52:55],a[152:155],v[16:19],a[52:55]
RD a[240:243],v224 offset:45056
CV v78,v108,v109
MM a[44:47],a[152:155],v[8:11],a[44:47]
EX v110,v110
MM a[44:47],a[156:159],v[12:15],a[44:47]
EX v111,v111
MM a[36:39],a[156:159],v[4:7],a[36:39]
RD a[244:247],v225 offset:45056
CV v79,v110,v111
MM a[36:39],a[152:155],v[0:3],a[36:39]
LPG14_%=:
s_cbranch_execz LTR2_%=
EX v112,v112
MM a[64:67],a[160:163],v[0:3],a[64:67]
buffer_load_dwordx4 v235, s[12:15], s64 offen offset:1024 lds
MM a[64:67],a[164:167],v[4:7],a[64:67]
EX v113,v113
MM a[72:75],a[164:167],v[12:15],a[72:75]
RD a[248:251],v226 offset:45056
CV v84,v112,v113
MM a[72:75],a[160:163],v[8:11],a[72:75]
EX v114,v114
MM a[80:83],a[160:163],v[16:19],a[80:83]
EX v115,v115
MM a[80:83],a[164:167],v[20:23],a[80:83]
RD a[252:255],v227 offset:45056
CV v85,v114,v115
MM a[88:91],a[164:167],v[28:31],a[88:91]
EX v116,v116
MM a[88:91],a[160:163],v[24:27],a[88:91]
buffer_load_dwordx4 v235, s[12:15], s81 offen offset:2048 lds
MM a[92:95],a[168:171],v[24:27],a[92:95]
EX v117,v117
MM a[92:95],a[172:175],v[28:31],a[92:95]
EX v118,v118
MM a[84:87],a[172:175],v[20:23],a[84:87]
buffer_load_dwordx4 v235, s[12:15], s64 offen offset:3072 lds
MM a[84:87],a[168:171],v[16:19],a[84:87]
EX v119,v119
MM a[76:79],a[168:171],v[8:11],a[76:79]
EX v120,v120
MM a[76:79],a[172:175],v[12:15],a[76:79]
EX v121,v121
MM a[68:71],a[172:175],v[4:7],a[68:71]
EX v122,v122
MM a[68:71],a[168:171],v[0:3],a[68:71]
EX v123,v123
MM a[96:99],a[176:179],v[0:3],a[96:99]
EX v124,v124
MM a[96:99],a[180:183],v[4:7],a[96:99]
EX v125,v125
MM a[104:107],a[180:183],v[12:15],a[104:107]
EX v126,v126
MM a[104:107],a[176:179],v[8:11],a[104:107]
EX v127,v127
MM a[112:115],a[176:179],v[16:19],a[112:115]
CV v86,v116,v117
CV v87,v118,v119
MM a[112:115],a[180:183],v[20:23],a[112:115]
CV v92,v120,v121
CV v93,v122,v123
MM a[120:123],a[180:183],v[28:31],a[120:123]
CV v94,v124,v125
CV v95,v126,v127
MM a[120:123],a[176:179],v[24:27],a[120:123]
MM a[124:127],a[184:187],v[24:27],a[124:127]
v_add_f32 v244, v144, v145
v_add_f32 v245, v148, v149
MM a[124:127],a[188:191],v[28:31],a[124:127]
v_add_f32 v246, v152, v153
v_add_f32 v247, v156, v157
MM a[116:119],a[188:191],v[20:23],a[116:119]
MM a[116:119],a[184:187],v[16:19],a[116:119]
MM a[108:111],a[184:187],v[8:11],a[108:111]
MM a[108:111],a[188:191],v[12:15],a[108:111]
MM a[100:103],a[188:191],v[4:7],a[100:103]
MM a[100:103],a[184:187],v[0:3],a[100:103]
v_smfmac_f32_16x16x64_bf16 v[144:147], v[236:239], v[64:71], v234
v_smfmac_f32_16x16x64_bf16 v[148:151], v[236:239], v[72:79], v234
v_smfmac_f32_16x16x64_bf16 v[152:155], v[236:239], v[80:87], v234
v_smfmac_f32_16x16x64_bf16 v[156:159], v[236:239], v[88:95], v234
s_waitcnt lgkmcnt(0)
MM v[0:3],a[192:195],v[160:163],v[128:131]
s_add_u32 s64, s64, 16384
TR a[128:129],v232 offset:16384
s_add_u32 s81, s64, 896
MM v[0:3],a[196:199],v[176:179],v[0:3]
TR a[130:131],v232 offset:20480
v_max3_f32 v249, v144, v145, v148
MM v[0:3],a[200:203],v[192:195],v[0:3]
TR a[132:133],v232 offset:24576
v_max3_f32 v249, v249, v149, v152
MM v[0:3],a[204:207],v[208:211],v[0:3]
TR a[134:135],v232 offset:28672
v_max3_f32 v249, v249, v153, v156
MM v[4:7],a[220:223],v[208:211],v[128:131]
TR a[136:137],v233 offset:16384
v_max_f32 v249, v249, v157
MM v[4:7],a[216:219],v[192:195],v[4:7]
TR a[138:139],v233 offset:20480
v_cmp_lt_f32 vcc, s71, v249
MM v[4:7],a[212:215],v[176:179],v[4:7]
TR a[140:141],v233 offset:24576
TR a[142:143],v233 offset:28672
MM v[4:7],a[208:211],v[160:163],v[4:7]
s_cbranch_vccnz LSITE17_%=
LBACK17_%=:
MM v[12:15],a[208:211],v[164:167],v[132:135]
v_lshrrev_b32 v127, 8, v248
v_lshlrev_b32 v124, 9, v127
MM v[12:15],a[212:215],v[180:183],v[12:15]
v_and_b32 v126, 192, v248
v_add_u32 v124, v124, v126
MM v[12:15],a[216:219],v[196:199],v[12:15]
v_bfe_u32 v126, v248, 4, 2
v_lshl_add_u32 v124, v126, 3, v124
MM v[12:15],a[220:223],v[212:215],v[12:15]
s_lshr_b32 s76, %[swave], 1
EX v0,v0
MM v[8:11],a[204:207],v[212:215],v[132:135]
s_mul_i32 s76, s76, 10240
EX v1,v1
MM v[8:11],a[200:203],v[196:199],v[8:11]
s_and_b32 s77, %[swave], 1
EX v2,v2
MM v[8:11],a[196:199],v[180:183],v[8:11]
s_lshl_b32 s77, s77, 5
TR a[144:145],v232 offset:16896
CV v0,v0,v1
MM v[8:11],a[192:195],v[164:167],v[8:11]
s_add_u32 s76, s76, s77
EX v3,v3
MM v[16:19],a[192:195],v[168:171],v[136:139]
s_add_u32 s76, s76, %[slds]
EX v4,v4
MM v[16:19],a[196:199],v[184:187],v[16:19]
s_add_u32 s76, s76, 131072
v_add_u32 v124, s76, v124
v_bfe_u32 v126, v248, 4, 3
MM v[16:19],a[200:203],v[200:203],v[16:19]
v_lshl_add_u32 v126, v127, 3, v126
v_lshlrev_b32 v126, 4, v126
MM v[16:19],a[204:207],v[216:219],v[16:19]
s_mul_i32 s76, %[swave], 3072
EX v5,v5
MM v[20:23],a[220:223],v[216:219],v[136:139]
s_add_u32 s76, s76, %[slds]
TR a[146:147],v232 offset:20992
CV v1,v2,v3
MM v[20:23],a[216:219],v[200:203],v[20:23]
s_add_u32 s76, s76, 151552
TR a[148:149],v232 offset:25088
v_add_u32 v125, s76, v126
MM v[20:23],a[212:215],v[184:187],v[20:23]
s_lshr_b32 s76, s48, 7
EX v6,v6
MM v[20:23],a[208:211],v[168:171],v[20:23]
s_lshl_b32 s76, s76, 5
EX v7,v7
MM v[28:31],a[208:211],v[172:175],v[140:143]
s_and_b32 s77, s48, 16
TR a[150:151],v232 offset:29184
TR a[152:153],v233 offset:16896
MM v[28:31],a[212:215],v[188:191],v[28:31]
s_add_u32 s76, s76, s77
EX v8,v8
MM v[28:31],a[216:219],v[204:207],v[28:31]
s_bfe_u32 s77, s48, 131077
TR a[154:155],v233 offset:20992
CV v2,v4,v5
MM v[28:31],a[220:223],v[220:223],v[28:31]
s_lshl_b32 s77, s77, 2
EX v9,v9
MM v[24:27],a[204:207],v[220:223],v[140:143]
s_add_u32 s76, s76, s77
TR a[156:157],v233 offset:25088
CV v3,v6,v7
MM v[24:27],a[200:203],v[204:207],v[24:27]
s_add_u32 s76, s76, %[swave]
EX v10,v10
MM v[24:27],a[196:199],v[188:191],v[24:27]
s_lshl_b32 s97, s76, 2
TR a[158:159],v233 offset:29184
CV v8,v8,v9
MM v[24:27],a[192:195],v[172:175],v[24:27]
s_add_u32 s97, s97, 77824
EX v11,v11
MM v[32:35],a[224:227],v[160:163],v[128:131]
s_lshl_b32 s76, s76, 9
TR a[160:161],v232 offset:17408
TR a[162:163],v232 offset:21504
MM v[32:35],a[228:231],v[176:179],v[32:35]
s_add_u32 s76, s76, 22183936
EX v12,v12
MM v[32:35],a[232:235],v[192:195],v[32:35]
EX v13,v13
MM v[32:35],a[236:239],v[208:211],v[32:35]
TR a[164:165],v232 offset:25600
CV v9,v10,v11
MM v[36:39],a[252:255],v[208:211],v[128:131]
EX v14,v14
MM v[36:39],a[248:251],v[192:195],v[36:39]
TR a[166:167],v232 offset:29696
CV v10,v12,v13
MM v[36:39],a[244:247],v[176:179],v[36:39]
EX v15,v15
MM v[36:39],a[240:243],v[160:163],v[36:39]
TR a[168:169],v233 offset:17408
TR a[170:171],v233 offset:21504
MM v[44:47],a[240:243],v[164:167],v[132:135]
EX v16,v16
MM v[44:47],a[244:247],v[180:183],v[44:47]
EX v17,v17
MM v[44:47],a[248:251],v[196:199],v[44:47]
TR a[172:173],v233 offset:25600
CV v11,v14,v15
MM v[44:47],a[252:255],v[212:215],v[44:47]
EX v18,v18
MM v[40:43],a[236:239],v[212:215],v[132:135]
TR a[174:175],v233 offset:29696
CV v16,v16,v17
MM v[40:43],a[232:235],v[196:199],v[40:43]
EX v19,v19
MM v[40:43],a[228:231],v[180:183],v[40:43]
TR a[176:177],v232 offset:17920
TR a[178:179],v232 offset:22016
MM v[40:43],a[224:227],v[164:167],v[40:43]
EX v20,v20
MM v[48:51],a[224:227],v[168:171],v[136:139]
EX v21,v21
MM v[48:51],a[228:231],v[184:187],v[48:51]
TR a[180:181],v232 offset:26112
CV v17,v18,v19
MM v[48:51],a[232:235],v[200:203],v[48:51]
EX v22,v22
MM v[48:51],a[236:239],v[216:219],v[48:51]
TR a[182:183],v232 offset:30208
CV v18,v20,v21
MM v[52:55],a[252:255],v[216:219],v[136:139]
EX v23,v23
MM v[52:55],a[248:251],v[200:203],v[52:55]
TR a[184:185],v233 offset:17920
TR a[186:187],v233 offset:22016
MM v[52:55],a[244:247],v[184:187],v[52:55]
EX v24,v24
MM v[52:55],a[240:243],v[168:171],v[52:55]
TR a[188:189],v233 offset:26112
CV v19,v22,v23
MM v[60:63],a[240:243],v[172:175],v[140:143]
EX v25,v25
MM v[60:63],a[244:247],v[188:191],v[60:63]
TR a[190:191],v233 offset:30208
CV v24,v24,v25
MM v[60:63],a[248:251],v[204:207],v[60:63]
EX v26,v26
MM v[60:63],a[252:255],v[220:223],v[60:63]
EX v27,v27
)ASM"
R"ASM(
MM v[56:59],a[236:239],v[220:223],v[140:143]
EX v28,v28
MM v[56:59],a[232:235],v[204:207],v[56:59]
EX v29,v29
MM v[56:59],a[228:231],v[188:191],v[56:59]
EX v30,v30
MM v[56:59],a[224:227],v[172:175],v[56:59]
s_waitcnt vmcnt(10) lgkmcnt(0)
s_barrier
MM a[0:3],a[128:131],v[64:67],a[0:3]
s_add_u32 m0, s65, 16384
CV v25,v26,v27
CV v26,v28,v29
MM a[0:3],a[132:135],v[68:71],a[0:3]
RD a[192:195],v224 offset:49152
RD a[196:199],v225 offset:49152
MM a[8:11],a[132:135],v[76:79],a[8:11]
EX v31,v31
MM a[8:11],a[128:131],v[72:75],a[8:11]
RD a[200:203],v226 offset:49152
CV v27,v30,v31
MM a[16:19],a[128:131],v[80:83],a[16:19]
TR v[96:97],v124 offset:0
TR v[98:99],v124 offset:256
MM a[16:19],a[132:135],v[84:87],a[16:19]
RD v[108:111],v125 offset:0
TR v[100:101],v124 offset:2048
MM a[24:27],a[132:135],v[92:95],a[24:27]
TR v[102:103],v124 offset:2304
RD v[112:115],v125 offset:512
MM a[24:27],a[128:131],v[88:91],a[24:27]
TR v[104:105],v124 offset:4096
TR v[106:107],v124 offset:4352
MM a[28:31],a[136:139],v[88:91],a[28:31]
RD v[116:119],v125 offset:1024
RD a[204:207],v227 offset:49152
EX v32,v32
MM a[28:31],a[140:143],v[92:95],a[28:31]
EX v33,v33
MM a[20:23],a[140:143],v[84:87],a[20:23]
EX v34,v34
MM a[20:23],a[136:139],v[80:83],a[20:23]
buffer_load_dwordx4 v228, s[8:11], s64 offen offset:0 lds
MM a[12:15],a[136:139],v[72:75],a[12:15]
EX v35,v35
MM a[12:15],a[140:143],v[76:79],a[12:15]
EX v36,v36
MM a[4:7],a[140:143],v[68:71],a[4:7]
CV v4,v32,v33
CV v5,v34,v35
MM a[4:7],a[136:139],v[64:67],a[4:7]
EX v37,v37
MM a[32:35],a[144:147],v[64:67],a[32:35]
EX v38,v38
MM a[32:35],a[148:151],v[68:71],a[32:35]
s_waitcnt lgkmcnt(1)
buffer_load_dwordx4 v229, s[8:11], s64 offen offset:1024 lds
MM a[40:43],a[148:151],v[76:79],a[40:43]
EX v39,v39
MM v[120:123],v[96:99],v[108:111],0
MM v[120:123],v[100:103],v[112:115],v[120:123]
MM v[120:123],v[104:107],v[116:119],v[120:123]
MM a[40:43],a[144:147],v[72:75],a[40:43]
TR v[96:97],v124 offset:6144
TR v[98:99],v124 offset:6400
MM a[48:51],a[144:147],v[80:83],a[48:51]
RD v[108:111],v125 offset:1536
TR v[100:101],v124 offset:8192
MM a[48:51],a[148:151],v[84:87],a[48:51]
TR v[102:103],v124 offset:8448
RD v[112:115],v125 offset:2048
MM a[56:59],a[148:151],v[92:95],a[56:59]
EX v40,v40
MM a[56:59],a[144:147],v[88:91],a[56:59]
EX v41,v41
MM a[60:63],a[152:155],v[88:91],a[60:63]
CV v6,v36,v37
CV v7,v38,v39
MM a[60:63],a[156:159],v[92:95],a[60:63]
EX v42,v42
MM a[52:55],a[156:159],v[84:87],a[52:55]
RD a[208:211],v224 offset:53248
RD a[212:215],v225 offset:53248
MM a[52:55],a[152:155],v[80:83],a[52:55]
EX v43,v43
MM a[44:47],a[152:155],v[72:75],a[44:47]
buffer_load_dwordx4 v230, s[8:11], s64 offen offset:2048 lds
MM a[44:47],a[156:159],v[76:79],a[44:47]
EX v44,v44
MM a[36:39],a[156:159],v[68:71],a[36:39]
s_waitcnt lgkmcnt(2)
EX v45,v45
MM a[36:39],a[152:155],v[64:67],a[36:39]
CV v12,v40,v41
CV v13,v42,v43
MM v[120:123],v[96:99],v[108:111],v[120:123]
MM v[120:123],v[100:103],v[112:115],v[120:123]
MM a[64:67],a[160:163],v[64:67],a[64:67]
EX v46,v46
MM a[64:67],a[164:167],v[68:71],a[64:67]
RD a[216:219],v226 offset:53248
CV v14,v44,v45
MM a[72:75],a[164:167],v[76:79],a[72:75]
EX v47,v47
MM a[72:75],a[160:163],v[72:75],a[72:75]
buffer_load_dwordx4 v231, s[8:11], s64 offen offset:3072 lds
MM a[80:83],a[160:163],v[80:83],a[80:83]
s_add_u32 m0, s65, 81920
EX v48,v48
MM a[80:83],a[164:167],v[84:87],a[80:83]
RD a[220:223],v227 offset:53248
CV v15,v46,v47
MM a[88:91],a[164:167],v[92:95],a[88:91]
s_mov_b32 exec_lo, 0x00ff00ff
s_mov_b32 exec_hi, 0x00ff00ff
buffer_store_dwordx4 v[120:123], v126, s[20:23], s76 offen offset:0 sc1
s_mov_b64 exec, -1
s_nop 0
MM a[88:91],a[160:163],v[88:91],a[88:91]
EX v49,v49
MM a[92:95],a[168:171],v[88:91],a[92:95]
EX v50,v50
MM a[92:95],a[172:175],v[92:95],a[92:95]
EX v51,v51
MM a[84:87],a[172:175],v[84:87],a[84:87]
RD a[224:227],v224 offset:57344
CV v20,v48,v49
MM a[84:87],a[168:171],v[80:83],a[84:87]
EX v52,v52
MM a[76:79],a[168:171],v[72:75],a[76:79]
buffer_load_dwordx4 v235, s[12:15], s81 offen offset:0 lds
MM a[76:79],a[172:175],v[76:79],a[76:79]
EX v53,v53
MM a[68:71],a[172:175],v[68:71],a[68:71]
RD a[228:231],v225 offset:57344
CV v21,v50,v51
MM a[68:71],a[168:171],v[64:67],a[68:71]
EX v54,v54
MM a[96:99],a[176:179],v[64:67],a[96:99]
RD a[232:235],v226 offset:57344
CV v22,v52,v53
MM a[96:99],a[180:183],v[68:71],a[96:99]
EX v55,v55
MM a[104:107],a[180:183],v[76:79],a[104:107]
EX v56,v56
MM a[104:107],a[176:179],v[72:75],a[104:107]
buffer_load_dwordx4 v235, s[12:15], s64 offen offset:1024 lds
MM a[112:115],a[176:179],v[80:83],a[112:115]
EX v57,v57
MM a[112:115],a[180:183],v[84:87],a[112:115]
RD a[236:239],v227 offset:57344
CV v23,v54,v55
MM a[120:123],a[180:183],v[92:95],a[120:123]
EX v58,v58
MM a[120:123],a[176:179],v[88:91],a[120:123]
RD a[240:243],v224 offset:61440
CV v28,v56,v57
MM a[124:127],a[184:187],v[88:91],a[124:127]
v_add_f32 v244, v144, v145
v_add_f32 v245, v148, v149
MM a[124:127],a[188:191],v[92:95],a[124:127]
v_add_f32 v246, v152, v153
v_add_f32 v247, v156, v157
MM a[116:119],a[188:191],v[84:87],a[116:119]
EX v59,v59
MM a[116:119],a[184:187],v[80:83],a[116:119]
EX v60,v60
MM a[108:111],a[184:187],v[72:75],a[108:111]
EX v61,v61
MM a[108:111],a[188:191],v[76:79],a[108:111]
buffer_load_dwordx4 v235, s[12:15], s81 offen offset:2048 lds
MM a[100:103],a[188:191],v[68:71],a[100:103]
EX v62,v62
MM a[100:103],a[184:187],v[64:67],a[100:103]
RD a[244:247],v225 offset:61440
CV v29,v58,v59
v_smfmac_f32_16x16x64_bf16 v[144:147], v[236:239], v[0:7], v234
RD a[248:251],v226 offset:61440
RD a[252:255],v227 offset:61440
EX v63,v63
v_smfmac_f32_16x16x64_bf16 v[148:151], v[236:239], v[8:15], v234
CV v30,v60,v61
v_smfmac_f32_16x16x64_bf16 v[152:155], v[236:239], v[16:23], v234
CV v31,v62,v63
s_nop 1
v_smfmac_f32_16x16x64_bf16 v[156:159], v[236:239], v[24:31], v234
s_waitcnt lgkmcnt(0)
buffer_load_dwordx4 v235, s[12:15], s64 offen offset:3072 lds
MM v[64:67],a[192:195],v[160:163],v[128:131]
s_add_u32 s64, s64, 16384
TR a[128:129],v232 offset:32768
s_add_u32 s81, s64, 896
MM v[64:67],a[196:199],v[176:179],v[64:67]
TR a[130:131],v232 offset:36864
v_max3_f32 v249, v144, v145, v148
MM v[64:67],a[200:203],v[192:195],v[64:67]
TR a[132:133],v232 offset:40960
v_max3_f32 v249, v249, v149, v152
MM v[64:67],a[204:207],v[208:211],v[64:67]
TR a[134:135],v232 offset:45056
v_max3_f32 v249, v249, v153, v156
MM v[68:71],a[220:223],v[208:211],v[128:131]
TR a[136:137],v233 offset:32768
v_max_f32 v249, v249, v157
MM v[68:71],a[216:219],v[192:195],v[68:71]
TR a[138:139],v233 offset:36864
v_cmp_lt_f32 vcc, s71, v249
MM v[68:71],a[212:215],v[176:179],v[68:71]
TR a[140:141],v233 offset:40960
TR a[142:143],v233 offset:45056
MM v[68:71],a[208:211],v[160:163],v[68:71]
s_cbranch_vccnz LSITE18_%=
LBACK18_%=:
MM v[76:79],a[208:211],v[164:167],v[132:135]
EX v64,v64
MM v[76:79],a[212:215],v[180:183],v[76:79]
EX v65,v65
MM v[76:79],a[216:219],v[196:199],v[76:79]
TR a[144:145],v232 offset:33280
CV v64,v64,v65
MM v[76:79],a[220:223],v[212:215],v[76:79]
EX v66,v66
MM v[72:75],a[204:207],v[212:215],v[132:135]
EX v67,v67
MM v[72:75],a[200:203],v[196:199],v[72:75]
TR a[146:147],v232 offset:37376
CV v65,v66,v67
MM v[72:75],a[196:199],v[180:183],v[72:75]
EX v68,v68
MM v[72:75],a[192:195],v[164:167],v[72:75]
TR a[148:149],v232 offset:41472
TR a[150:151],v232 offset:45568
MM v[80:83],a[192:195],v[168:171],v[136:139]
EX v69,v69
MM v[80:83],a[196:199],v[184:187],v[80:83]
TR a[152:153],v233 offset:33280
CV v66,v68,v69
MM v[80:83],a[200:203],v[200:203],v[80:83]
EX v70,v70
MM v[80:83],a[204:207],v[216:219],v[80:83]
TR a[154:155],v233 offset:37376
TR a[156:157],v233 offset:41472
MM v[84:87],a[220:223],v[216:219],v[136:139]
EX v71,v71
MM v[84:87],a[216:219],v[200:203],v[84:87]
TR a[158:159],v233 offset:45568
CV v67,v70,v71
MM v[84:87],a[212:215],v[184:187],v[84:87]
EX v72,v72
MM v[84:87],a[208:211],v[168:171],v[84:87]
EX v73,v73
MM v[92:95],a[208:211],v[172:175],v[140:143]
TR a[160:161],v232 offset:33792
CV v72,v72,v73
MM v[92:95],a[212:215],v[188:191],v[92:95]
EX v74,v74
MM v[92:95],a[216:219],v[204:207],v[92:95]
TR a[162:163],v232 offset:37888
TR a[164:165],v232 offset:41984
MM v[92:95],a[220:223],v[220:223],v[92:95]
EX v75,v75
MM v[88:91],a[204:207],v[220:223],v[140:143]
TR a[166:167],v232 offset:46080
CV v73,v74,v75
MM v[88:91],a[200:203],v[204:207],v[88:91]
EX v76,v76
MM v[88:91],a[196:199],v[188:191],v[88:91]
EX v77,v77
MM v[88:91],a[192:195],v[172:175],v[88:91]
TR a[168:169],v233 offset:33792
CV v74,v76,v77
MM v[96:99],a[224:227],v[160:163],v[128:131]
EX v78,v78
MM v[96:99],a[228:231],v[176:179],v[96:99]
TR a[170:171],v233 offset:37888
TR a[172:173],v233 offset:41984
MM v[96:99],a[232:235],v[192:195],v[96:99]
EX v79,v79
MM v[96:99],a[236:239],v[208:211],v[96:99]
TR a[174:175],v233 offset:46080
CV v75,v78,v79
MM v[100:103],a[252:255],v[208:211],v[128:131]
EX v80,v80
MM v[100:103],a[248:251],v[192:195],v[100:103]
EX v81,v81
MM v[100:103],a[244:247],v[176:179],v[100:103]
TR a[176:177],v232 offset:34304
CV v80,v80,v81
MM v[100:103],a[240:243],v[160:163],v[100:103]
EX v82,v82
MM v[108:111],a[240:243],v[164:167],v[132:135]
v_lshrrev_b32 v253, 7, v248
v_mul_u32_u24 v253, 1280, v253
MM v[108:111],a[244:247],v[180:183],v[108:111]
v_and_b32 v252, 112, v248
v_add_u32 v253, v253, v252
MM v[108:111],a[248:251],v[196:199],v[108:111]
s_mul_i32 s90, %[swave], 61440
TR a[178:179],v232 offset:38400
TR a[180:181],v232 offset:42496
MM v[108:111],a[252:255],v[212:215],v[108:111]
s_mul_i32 s91, %[swave], 6144
EX v83,v83
MM v[104:107],a[236:239],v[212:215],v[132:135]
s_add_u32 s91, s91, %[slds]
TR a[182:183],v232 offset:46592
CV v81,v82,v83
MM v[104:107],a[232:235],v[196:199],v[104:107]
EX v84,v84
MM v[104:107],a[228:231],v[180:183],v[104:107]
EX v85,v85
MM v[104:107],a[224:227],v[164:167],v[104:107]
TR a[184:185],v233 offset:34304
CV v82,v84,v85
MM v[112:115],a[224:227],v[168:171],v[136:139]
EX v86,v86
MM v[112:115],a[228:231],v[184:187],v[112:115]
TR a[186:187],v233 offset:38400
TR a[188:189],v233 offset:42496
MM v[112:115],a[232:235],v[200:203],v[112:115]
EX v87,v87
MM v[112:115],a[236:239],v[216:219],v[112:115]
TR a[190:191],v233 offset:46592
CV v83,v86,v87
MM v[116:119],a[252:255],v[216:219],v[136:139]
EX v88,v88
MM v[116:119],a[248:251],v[200:203],v[116:119]
EX v89,v89
MM v[116:119],a[244:247],v[184:187],v[116:119]
EX v90,v90
MM v[116:119],a[240:243],v[168:171],v[116:119]
EX v91,v91
MM v[124:127],a[240:243],v[172:175],v[140:143]
EX v92,v92
MM v[124:127],a[244:247],v[188:191],v[124:127]
EX v93,v93
MM v[124:127],a[248:251],v[204:207],v[124:127]
EX v94,v94
MM v[124:127],a[252:255],v[220:223],v[124:127]
EX v95,v95
MM v[120:123],a[236:239],v[220:223],v[140:143]
EX v96,v96
MM v[120:123],a[232:235],v[204:207],v[120:123]
EX v97,v97
MM v[120:123],a[228:231],v[188:191],v[120:123]
EX v98,v98
MM v[120:123],a[224:227],v[172:175],v[120:123]
s_waitcnt vmcnt(14) lgkmcnt(0)
s_barrier
MM a[0:3],a[128:131],v[0:3],a[0:3]
s_nop 0
LPG15_%=:
s_cbranch_execz LTR2_%=
s_add_u32 m0, s65, 32768
CV v88,v88,v89
CV v89,v90,v91
MM a[0:3],a[132:135],v[4:7],a[0:3]
RD a[192:195],v224 offset:0
CV v90,v92,v93
MM a[8:11],a[132:135],v[12:15],a[8:11]
RD a[196:199],v225 offset:0
CV v91,v94,v95
MM a[8:11],a[128:131],v[8:11],a[8:11]
buffer_load_dwordx4 v228, s[8:11], s64 offen offset:0 lds
MM a[16:19],a[128:131],v[16:19],a[16:19]
RD a[200:203],v226 offset:0
CV v68,v96,v97
MM a[16:19],a[132:135],v[20:23],a[16:19]
buffer_load_dwordx4 v229, s[8:11], s64 offen offset:1024 lds
MM a[24:27],a[132:135],v[28:31],a[24:27]
RD a[204:207],v227 offset:0
RD a[208:211],v224 offset:4096
MM a[24:27],a[128:131],v[24:27],a[24:27]
EX v99,v99
MM a[28:31],a[136:139],v[24:27],a[28:31]
RD a[212:215],v225 offset:4096
CV v69,v98,v99
MM a[28:31],a[140:143],v[28:31],a[28:31]
EX v100,v100
MM a[20:23],a[140:143],v[20:23],a[20:23]
buffer_load_dwordx4 v230, s[8:11], s64 offen offset:2048 lds
MM a[20:23],a[136:139],v[16:19],a[20:23]
EX v101,v101
MM a[12:15],a[136:139],v[8:11],a[12:15]
RD a[216:219],v226 offset:4096
CV v70,v100,v101
MM a[12:15],a[140:143],v[12:15],a[12:15]
EX v102,v102
MM a[4:7],a[140:143],v[4:7],a[4:7]
)ASM"
R"ASM(
EX v103,v103
MM a[4:7],a[136:139],v[0:3],a[4:7]
RD a[220:223],v227 offset:4096
CV v71,v102,v103
MM a[32:35],a[144:147],v[0:3],a[32:35]
EX v104,v104
MM a[32:35],a[148:151],v[4:7],a[32:35]
buffer_load_dwordx4 v231, s[8:11], s64 offen offset:3072 lds
MM a[40:43],a[148:151],v[12:15],a[40:43]
s_add_u32 m0, s65, 98304
EX v105,v105
MM a[40:43],a[144:147],v[8:11],a[40:43]
RD a[224:227],v224 offset:8192
CV v76,v104,v105
MM a[48:51],a[144:147],v[16:19],a[48:51]
EX v106,v106
MM a[48:51],a[148:151],v[20:23],a[48:51]
EX v107,v107
MM a[56:59],a[148:151],v[28:31],a[56:59]
RD a[228:231],v225 offset:8192
CV v77,v106,v107
MM a[56:59],a[144:147],v[24:27],a[56:59]
EX v108,v108
MM a[60:63],a[152:155],v[24:27],a[60:63]
buffer_load_dwordx4 v235, s[12:15], s81 offen offset:0 lds
MM a[60:63],a[156:159],v[28:31],a[60:63]
EX v109,v109
MM a[52:55],a[156:159],v[20:23],a[52:55]
RD a[232:235],v226 offset:8192
CV v78,v108,v109
MM a[52:55],a[152:155],v[16:19],a[52:55]
EX v110,v110
MM a[44:47],a[152:155],v[8:11],a[44:47]
EX v111,v111
MM a[44:47],a[156:159],v[12:15],a[44:47]
RD a[236:239],v227 offset:8192
CV v79,v110,v111
MM a[36:39],a[156:159],v[4:7],a[36:39]
buffer_load_dwordx4 v235, s[12:15], s64 offen offset:1024 lds
MM a[36:39],a[152:155],v[0:3],a[36:39]
EX v112,v112
MM a[64:67],a[160:163],v[0:3],a[64:67]
EX v113,v113
MM a[64:67],a[164:167],v[4:7],a[64:67]
RD a[240:243],v224 offset:12288
CV v84,v112,v113
MM a[72:75],a[164:167],v[12:15],a[72:75]
EX v114,v114
MM a[72:75],a[160:163],v[8:11],a[72:75]
RD a[244:247],v225 offset:12288
RD a[248:251],v226 offset:12288
MM a[80:83],a[160:163],v[16:19],a[80:83]
s_waitcnt vmcnt(10)
v_mov_b32 v250, s69
s_mov_b64 exec, 1
buffer_store_dword v250, off, s[20:23], s97 offset:0 sc1
s_mov_b64 exec, -1
MM a[80:83],a[164:167],v[20:23],a[80:83]
s_add_u32 m0, s91, 139264
s_add_u32 s77, s90, 0
buffer_load_dwordx4 v253, s[28:31], s77 offen offset:0 lds
MM a[88:91],a[164:167],v[28:31],a[88:91]
EX v115,v115
MM a[88:91],a[160:163],v[24:27],a[88:91]
RD a[252:255],v227 offset:12288
CV v85,v114,v115
MM a[92:95],a[168:171],v[24:27],a[92:95]
EX v116,v116
MM a[92:95],a[172:175],v[28:31],a[92:95]
EX v117,v117
MM a[84:87],a[172:175],v[20:23],a[84:87]
s_add_u32 m0, s65, 98304
s_nop 0
buffer_load_dwordx4 v235, s[12:15], s81 offen offset:2048 lds
MM a[84:87],a[168:171],v[16:19],a[84:87]
EX v118,v118
MM a[76:79],a[168:171],v[8:11],a[76:79]
EX v119,v119
MM a[76:79],a[172:175],v[12:15],a[76:79]
EX v120,v120
MM a[68:71],a[172:175],v[4:7],a[68:71]
buffer_load_dwordx4 v235, s[12:15], s64 offen offset:3072 lds
MM a[68:71],a[168:171],v[0:3],a[68:71]
EX v121,v121
MM a[96:99],a[176:179],v[0:3],a[96:99]
EX v122,v122
MM a[96:99],a[180:183],v[4:7],a[96:99]
EX v123,v123
MM a[104:107],a[180:183],v[12:15],a[104:107]
EX v124,v124
MM a[104:107],a[176:179],v[8:11],a[104:107]
EX v125,v125
MM a[112:115],a[176:179],v[16:19],a[112:115]
EX v126,v126
MM a[112:115],a[180:183],v[20:23],a[112:115]
EX v127,v127
MM a[120:123],a[180:183],v[28:31],a[120:123]
CV v86,v116,v117
CV v87,v118,v119
MM a[120:123],a[176:179],v[24:27],a[120:123]
CV v92,v120,v121
CV v93,v122,v123
MM a[124:127],a[184:187],v[24:27],a[124:127]
v_add_f32 v244, v144, v145
v_add_f32 v245, v148, v149
MM a[124:127],a[188:191],v[28:31],a[124:127]
v_add_f32 v246, v152, v153
v_add_f32 v247, v156, v157
MM a[116:119],a[188:191],v[20:23],a[116:119]
CV v94,v124,v125
CV v95,v126,v127
MM a[116:119],a[184:187],v[16:19],a[116:119]
MM a[108:111],a[184:187],v[8:11],a[108:111]
MM a[108:111],a[188:191],v[12:15],a[108:111]
MM a[100:103],a[188:191],v[4:7],a[100:103]
MM a[100:103],a[184:187],v[0:3],a[100:103]
v_smfmac_f32_16x16x64_bf16 v[144:147], v[236:239], v[64:71], v234
v_smfmac_f32_16x16x64_bf16 v[148:151], v[236:239], v[72:79], v234
v_smfmac_f32_16x16x64_bf16 v[152:155], v[236:239], v[80:87], v234
v_smfmac_f32_16x16x64_bf16 v[156:159], v[236:239], v[88:95], v234
s_waitcnt lgkmcnt(0)
MM v[0:3],a[192:195],v[160:163],v[128:131]
s_add_u32 s64, s64, 16384
TR a[128:129],v232 offset:49152
s_add_u32 s81, s64, 896
MM v[0:3],a[196:199],v[176:179],v[0:3]
TR a[130:131],v232 offset:53248
v_max3_f32 v249, v144, v145, v148
MM v[0:3],a[200:203],v[192:195],v[0:3]
TR a[132:133],v232 offset:57344
v_max3_f32 v249, v249, v149, v152
MM v[0:3],a[204:207],v[208:211],v[0:3]
TR a[134:135],v232 offset:61440
v_max3_f32 v249, v249, v153, v156
MM v[4:7],a[220:223],v[208:211],v[128:131]
TR a[136:137],v233 offset:49152
v_max_f32 v249, v249, v157
MM v[4:7],a[216:219],v[192:195],v[4:7]
TR a[138:139],v233 offset:53248
v_cmp_lt_f32 vcc, s71, v249
MM v[4:7],a[212:215],v[176:179],v[4:7]
TR a[140:141],v233 offset:57344
TR a[142:143],v233 offset:61440
MM v[4:7],a[208:211],v[160:163],v[4:7]
s_cbranch_vccnz LSITE19_%=
LBACK19_%=:
MM v[12:15],a[208:211],v[164:167],v[132:135]
s_add_u32 m0, s91, 140288
s_add_u32 s77, s90, 10240
buffer_load_dwordx4 v253, s[28:31], s77 offen offset:0 lds
MM v[12:15],a[212:215],v[180:183],v[12:15]
EX v0,v0
MM v[12:15],a[216:219],v[196:199],v[12:15]
EX v1,v1
MM v[12:15],a[220:223],v[212:215],v[12:15]
TR a[144:145],v232 offset:49664
CV v0,v0,v1
MM v[8:11],a[204:207],v[212:215],v[132:135]
EX v2,v2
MM v[8:11],a[200:203],v[196:199],v[8:11]
EX v3,v3
MM v[8:11],a[196:199],v[180:183],v[8:11]
TR a[146:147],v232 offset:53760
CV v1,v2,v3
MM v[8:11],a[192:195],v[164:167],v[8:11]
EX v4,v4
MM v[16:19],a[192:195],v[168:171],v[136:139]
TR a[148:149],v232 offset:57856
TR a[150:151],v232 offset:61952
MM v[16:19],a[196:199],v[184:187],v[16:19]
EX v5,v5
MM v[16:19],a[200:203],v[200:203],v[16:19]
TR a[152:153],v233 offset:49664
CV v2,v4,v5
MM v[16:19],a[204:207],v[216:219],v[16:19]
EX v6,v6
MM v[20:23],a[220:223],v[216:219],v[136:139]
EX v7,v7
MM v[20:23],a[216:219],v[200:203],v[20:23]
TR a[154:155],v233 offset:53760
CV v3,v6,v7
MM v[20:23],a[212:215],v[184:187],v[20:23]
EX v8,v8
MM v[20:23],a[208:211],v[168:171],v[20:23]
TR a[156:157],v233 offset:57856
TR a[158:159],v233 offset:61952
MM v[28:31],a[208:211],v[172:175],v[140:143]
EX v9,v9
MM v[28:31],a[212:215],v[188:191],v[28:31]
TR a[160:161],v232 offset:50176
CV v8,v8,v9
MM v[28:31],a[216:219],v[204:207],v[28:31]
EX v10,v10
MM v[28:31],a[220:223],v[220:223],v[28:31]
EX v11,v11
MM v[24:27],a[204:207],v[220:223],v[140:143]
TR a[162:163],v232 offset:54272
CV v9,v10,v11
MM v[24:27],a[200:203],v[204:207],v[24:27]
TR a[164:165],v232 offset:58368
TR a[166:167],v232 offset:62464
MM v[24:27],a[196:199],v[188:191],v[24:27]
EX v12,v12
MM v[24:27],a[192:195],v[172:175],v[24:27]
EX v13,v13
MM v[32:35],a[224:227],v[160:163],v[128:131]
TR a[168:169],v233 offset:50176
CV v10,v12,v13
MM v[32:35],a[228:231],v[176:179],v[32:35]
EX v14,v14
MM v[32:35],a[232:235],v[192:195],v[32:35]
TR a[170:171],v233 offset:54272
TR a[172:173],v233 offset:58368
MM v[32:35],a[236:239],v[208:211],v[32:35]
EX v15,v15
MM v[36:39],a[252:255],v[208:211],v[128:131]
TR a[174:175],v233 offset:62464
CV v11,v14,v15
MM v[36:39],a[248:251],v[192:195],v[36:39]
EX v16,v16
MM v[36:39],a[244:247],v[176:179],v[36:39]
EX v17,v17
MM v[36:39],a[240:243],v[160:163],v[36:39]
TR a[176:177],v232 offset:50688
CV v16,v16,v17
MM v[44:47],a[240:243],v[164:167],v[132:135]
s_add_u32 m0, s91, 141312
s_add_u32 s77, s90, 20480
buffer_load_dwordx4 v253, s[28:31], s77 offen offset:0 lds
MM v[44:47],a[244:247],v[180:183],v[44:47]
EX v18,v18
MM v[44:47],a[248:251],v[196:199],v[44:47]
TR a[178:179],v232 offset:54784
TR a[180:181],v232 offset:58880
MM v[44:47],a[252:255],v[212:215],v[44:47]
EX v19,v19
MM v[40:43],a[236:239],v[212:215],v[132:135]
TR a[182:183],v232 offset:62976
CV v17,v18,v19
MM v[40:43],a[232:235],v[196:199],v[40:43]
EX v20,v20
MM v[40:43],a[228:231],v[180:183],v[40:43]
EX v21,v21
MM v[40:43],a[224:227],v[164:167],v[40:43]
TR a[184:185],v233 offset:50688
CV v18,v20,v21
MM v[48:51],a[224:227],v[168:171],v[136:139]
EX v22,v22
MM v[48:51],a[228:231],v[184:187],v[48:51]
TR a[186:187],v233 offset:54784
TR a[188:189],v233 offset:58880
MM v[48:51],a[232:235],v[200:203],v[48:51]
EX v23,v23
MM v[48:51],a[236:239],v[216:219],v[48:51]
TR a[190:191],v233 offset:62976
CV v19,v22,v23
MM v[52:55],a[252:255],v[216:219],v[136:139]
EX v24,v24
MM v[52:55],a[248:251],v[200:203],v[52:55]
EX v25,v25
MM v[52:55],a[244:247],v[184:187],v[52:55]
EX v26,v26
MM v[52:55],a[240:243],v[168:171],v[52:55]
EX v27,v27
MM v[60:63],a[240:243],v[172:175],v[140:143]
EX v28,v28
MM v[60:63],a[244:247],v[188:191],v[60:63]
EX v29,v29
MM v[60:63],a[248:251],v[204:207],v[60:63]
EX v30,v30
MM v[60:63],a[252:255],v[220:223],v[60:63]
EX v31,v31
MM v[56:59],a[236:239],v[220:223],v[140:143]
EX v32,v32
MM v[56:59],a[232:235],v[204:207],v[56:59]
EX v33,v33
MM v[56:59],a[228:231],v[188:191],v[56:59]
EX v34,v34
MM v[56:59],a[224:227],v[172:175],v[56:59]
s_waitcnt lgkmcnt(0)
s_barrier
MM a[0:3],a[128:131],v[64:67],a[0:3]
s_add_u32 m0, s65, 49152
CV v24,v24,v25
CV v25,v26,v27
MM a[0:3],a[132:135],v[68:71],a[0:3]
RD a[192:195],v224 offset:16384
CV v26,v28,v29
MM a[8:11],a[132:135],v[76:79],a[8:11]
RD a[196:199],v225 offset:16384
CV v27,v30,v31
MM a[8:11],a[128:131],v[72:75],a[8:11]
buffer_load_dwordx4 v228, s[8:11], s64 offen offset:0 lds
MM a[16:19],a[128:131],v[80:83],a[16:19]
s_add_u32 m0, s91, 142336
s_add_u32 s77, s90, 30720
buffer_load_dwordx4 v253, s[28:31], s77 offen offset:0 lds
MM a[16:19],a[132:135],v[84:87],a[16:19]
RD a[200:203],v226 offset:16384
CV v4,v32,v33
MM a[24:27],a[132:135],v[92:95],a[24:27]
RD a[204:207],v227 offset:16384
RD a[208:211],v224 offset:20480
MM a[24:27],a[128:131],v[88:91],a[24:27]
s_add_u32 m0, s65, 49152
s_nop 0
buffer_load_dwordx4 v229, s[8:11], s64 offen offset:1024 lds
MM a[28:31],a[136:139],v[88:91],a[28:31]
EX v35,v35
MM a[28:31],a[140:143],v[92:95],a[28:31]
RD a[212:215],v225 offset:20480
CV v5,v34,v35
MM a[20:23],a[140:143],v[84:87],a[20:23]
EX v36,v36
MM a[20:23],a[136:139],v[80:83],a[20:23]
EX v37,v37
MM a[12:15],a[136:139],v[72:75],a[12:15]
RD a[216:219],v226 offset:20480
CV v6,v36,v37
MM a[12:15],a[140:143],v[76:79],a[12:15]
buffer_load_dwordx4 v230, s[8:11], s64 offen offset:2048 lds
MM a[4:7],a[140:143],v[68:71],a[4:7]
EX v38,v38
MM a[4:7],a[136:139],v[64:67],a[4:7]
EX v39,v39
MM a[32:35],a[144:147],v[64:67],a[32:35]
RD a[220:223],v227 offset:20480
CV v7,v38,v39
MM a[32:35],a[148:151],v[68:71],a[32:35]
EX v40,v40
MM a[40:43],a[148:151],v[76:79],a[40:43]
EX v41,v41
MM a[40:43],a[144:147],v[72:75],a[40:43]
RD a[224:227],v224 offset:24576
CV v12,v40,v41
MM a[48:51],a[144:147],v[80:83],a[48:51]
buffer_load_dwordx4 v231, s[8:11], s64 offen offset:3072 lds
MM a[48:51],a[148:151],v[84:87],a[48:51]
s_add_u32 m0, s65, 114688
EX v42,v42
MM a[56:59],a[148:151],v[92:95],a[56:59]
EX v43,v43
MM a[56:59],a[144:147],v[88:91],a[56:59]
RD a[228:231],v225 offset:24576
CV v13,v42,v43
MM a[60:63],a[152:155],v[88:91],a[60:63]
EX v44,v44
MM a[60:63],a[156:159],v[92:95],a[60:63]
buffer_load_dwordx4 v235, s[12:15], s81 offen offset:0 lds
MM a[52:55],a[156:159],v[84:87],a[52:55]
EX v45,v45
MM a[52:55],a[152:155],v[80:83],a[52:55]
RD a[232:235],v226 offset:24576
CV v14,v44,v45
MM a[44:47],a[152:155],v[72:75],a[44:47]
EX v46,v46
MM a[44:47],a[156:159],v[76:79],a[44:47]
EX v47,v47
MM a[36:39],a[156:159],v[68:71],a[36:39]
RD a[236:239],v227 offset:24576
CV v15,v46,v47
MM a[36:39],a[152:155],v[64:67],a[36:39]
EX v48,v48
MM a[64:67],a[160:163],v[64:67],a[64:67]
buffer_load_dwordx4 v235, s[12:15], s64 offen offset:1024 lds
MM a[64:67],a[164:167],v[68:71],a[64:67]
EX v49,v49
MM a[72:75],a[164:167],v[76:79],a[72:75]
RD a[240:243],v224 offset:28672
CV v20,v48,v49
MM a[72:75],a[160:163],v[72:75],a[72:75]
EX v50,v50
MM a[80:83],a[160:163],v[80:83],a[80:83]
s_add_u32 m0, s91, 143360
s_add_u32 s77, s90, 40960
buffer_load_dwordx4 v253, s[28:31], s77 offen offset:0 lds
MM a[80:83],a[164:167],v[84:87],a[80:83]
EX v51,v51
MM a[88:91],a[164:167],v[92:95],a[88:91]
RD a[244:247],v225 offset:28672
CV v21,v50,v51
MM a[88:91],a[160:163],v[88:91],a[88:91]
EX v52,v52
MM a[92:95],a[168:171],v[88:91],a[92:95]
EX v53,v53
MM a[92:95],a[172:175],v[92:95],a[92:95]
RD a[248:251],v226 offset:28672
CV v22,v52,v53
MM a[84:87],a[172:175],v[84:87],a[84:87]
s_add_u32 m0, s65, 114688
)ASM"
R"ASM(
s_nop 0
buffer_load_dwordx4 v235, s[12:15], s81 offen offset:2048 lds
MM a[84:87],a[168:171],v[80:83],a[84:87]
EX v54,v54
MM a[76:79],a[168:171],v[72:75],a[76:79]
EX v55,v55
MM a[76:79],a[172:175],v[76:79],a[76:79]
RD a[252:255],v227 offset:28672
CV v23,v54,v55
MM a[68:71],a[172:175],v[68:71],a[68:71]
EX v56,v56
MM a[68:71],a[168:171],v[64:67],a[68:71]
EX v57,v57
MM a[96:99],a[176:179],v[64:67],a[96:99]
buffer_load_dwordx4 v235, s[12:15], s64 offen offset:3072 lds
MM a[96:99],a[180:183],v[68:71],a[96:99]
EX v58,v58
MM a[104:107],a[180:183],v[76:79],a[104:107]
EX v59,v59
MM a[104:107],a[176:179],v[72:75],a[104:107]
EX v60,v60
MM a[112:115],a[176:179],v[80:83],a[112:115]
EX v61,v61
MM a[112:115],a[180:183],v[84:87],a[112:115]
EX v62,v62
MM a[120:123],a[180:183],v[92:95],a[120:123]
s_add_u32 m0, s91, 144384
s_add_u32 s77, s90, 51200
buffer_load_dwordx4 v253, s[28:31], s77 offen offset:0 lds
MM a[120:123],a[176:179],v[88:91],a[120:123]
EX v63,v63
MM a[124:127],a[184:187],v[88:91],a[124:127]
v_add_f32 v244, v144, v145
v_add_f32 v245, v148, v149
MM a[124:127],a[188:191],v[92:95],a[124:127]
v_add_f32 v246, v152, v153
v_add_f32 v247, v156, v157
MM a[116:119],a[188:191],v[84:87],a[116:119]
CV v28,v56,v57
CV v29,v58,v59
MM a[116:119],a[184:187],v[80:83],a[116:119]
CV v30,v60,v61
CV v31,v62,v63
MM a[108:111],a[184:187],v[72:75],a[108:111]
MM a[108:111],a[188:191],v[76:79],a[108:111]
MM a[100:103],a[188:191],v[68:71],a[100:103]
MM a[100:103],a[184:187],v[64:67],a[100:103]
v_smfmac_f32_16x16x64_bf16 v[144:147], v[236:239], v[0:7], v234
v_smfmac_f32_16x16x64_bf16 v[148:151], v[236:239], v[8:15], v234
v_smfmac_f32_16x16x64_bf16 v[152:155], v[236:239], v[16:23], v234
v_smfmac_f32_16x16x64_bf16 v[156:159], v[236:239], v[24:31], v234
s_waitcnt lgkmcnt(0)
MM v[64:67],a[192:195],v[160:163],v[128:131]
s_add_u32 s64, s64, 16384
TR a[128:129],v232 offset:0
s_add_u32 s81, s64, 896
MM v[64:67],a[196:199],v[176:179],v[64:67]
TR a[130:131],v232 offset:4096
v_max3_f32 v249, v144, v145, v148
MM v[64:67],a[200:203],v[192:195],v[64:67]
TR a[132:133],v232 offset:8192
v_max3_f32 v249, v249, v149, v152
MM v[64:67],a[204:207],v[208:211],v[64:67]
TR a[134:135],v232 offset:12288
v_max3_f32 v249, v249, v153, v156
MM v[68:71],a[220:223],v[208:211],v[128:131]
TR a[136:137],v233 offset:0
v_max_f32 v249, v249, v157
MM v[68:71],a[216:219],v[192:195],v[68:71]
TR a[138:139],v233 offset:4096
v_cmp_lt_f32 vcc, s71, v249
MM v[68:71],a[212:215],v[176:179],v[68:71]
TR a[140:141],v233 offset:8192
TR a[142:143],v233 offset:12288
MM v[68:71],a[208:211],v[160:163],v[68:71]
s_cbranch_vccnz LSITE20_%=
LBACK20_%=:
MM v[76:79],a[208:211],v[164:167],v[132:135]
v_bfe_u32 v253, v248, 4, 4
v_lshlrev_b32 v253, 2, v253
MM v[76:79],a[212:215],v[180:183],v[76:79]
s_and_b32 s76, s18, 0xffffffc0
EX v64,v64
MM v[76:79],a[216:219],v[196:199],v[76:79]
s_add_u32 s76, s76, 77824
buffer_load_dword v252, v253, s[20:23], s76 offen offset:0 sc1
MM v[76:79],a[220:223],v[212:215],v[76:79]
EX v65,v65
MM v[72:75],a[204:207],v[212:215],v[132:135]
EX v66,v66
MM v[72:75],a[200:203],v[196:199],v[72:75]
TR a[144:145],v232 offset:512
CV v64,v64,v65
MM v[72:75],a[196:199],v[180:183],v[72:75]
EX v67,v67
MM v[72:75],a[192:195],v[164:167],v[72:75]
TR a[146:147],v232 offset:4608
CV v65,v66,v67
MM v[80:83],a[192:195],v[168:171],v[136:139]
EX v68,v68
MM v[80:83],a[196:199],v[184:187],v[80:83]
TR a[148:149],v232 offset:8704
TR a[150:151],v232 offset:12800
MM v[80:83],a[200:203],v[200:203],v[80:83]
EX v69,v69
MM v[80:83],a[204:207],v[216:219],v[80:83]
TR a[152:153],v233 offset:512
CV v66,v68,v69
MM v[84:87],a[220:223],v[216:219],v[136:139]
EX v70,v70
MM v[84:87],a[216:219],v[200:203],v[84:87]
EX v71,v71
MM v[84:87],a[212:215],v[184:187],v[84:87]
TR a[154:155],v233 offset:4608
CV v67,v70,v71
MM v[84:87],a[208:211],v[168:171],v[84:87]
EX v72,v72
MM v[92:95],a[208:211],v[172:175],v[140:143]
TR a[156:157],v233 offset:8704
TR a[158:159],v233 offset:12800
MM v[92:95],a[212:215],v[188:191],v[92:95]
EX v73,v73
MM v[92:95],a[216:219],v[204:207],v[92:95]
TR a[160:161],v232 offset:1024
CV v72,v72,v73
MM v[92:95],a[220:223],v[220:223],v[92:95]
EX v74,v74
MM v[88:91],a[204:207],v[220:223],v[140:143]
EX v75,v75
MM v[88:91],a[200:203],v[204:207],v[88:91]
TR a[162:163],v232 offset:5120
CV v73,v74,v75
MM v[88:91],a[196:199],v[188:191],v[88:91]
EX v76,v76
MM v[88:91],a[192:195],v[172:175],v[88:91]
TR a[164:165],v232 offset:9216
TR a[166:167],v232 offset:13312
MM v[96:99],a[224:227],v[160:163],v[128:131]
EX v77,v77
MM v[96:99],a[228:231],v[176:179],v[96:99]
TR a[168:169],v233 offset:1024
CV v74,v76,v77
MM v[96:99],a[232:235],v[192:195],v[96:99]
EX v78,v78
MM v[96:99],a[236:239],v[208:211],v[96:99]
EX v79,v79
MM v[100:103],a[252:255],v[208:211],v[128:131]
TR a[170:171],v233 offset:5120
CV v75,v78,v79
MM v[100:103],a[248:251],v[192:195],v[100:103]
EX v80,v80
MM v[100:103],a[244:247],v[176:179],v[100:103]
TR a[172:173],v233 offset:9216
TR a[174:175],v233 offset:13312
MM v[100:103],a[240:243],v[160:163],v[100:103]
EX v81,v81
s_nop 0
LPG16_%=:
s_cbranch_execz LTR2_%=
MM v[108:111],a[240:243],v[164:167],v[132:135]
TR a[176:177],v232 offset:1536
CV v80,v80,v81
MM v[108:111],a[244:247],v[180:183],v[108:111]
EX v82,v82
MM v[108:111],a[248:251],v[196:199],v[108:111]
TR a[178:179],v232 offset:5632
TR a[180:181],v232 offset:9728
MM v[108:111],a[252:255],v[212:215],v[108:111]
EX v83,v83
MM v[104:107],a[236:239],v[212:215],v[132:135]
TR a[182:183],v232 offset:13824
CV v81,v82,v83
MM v[104:107],a[232:235],v[196:199],v[104:107]
EX v84,v84
MM v[104:107],a[228:231],v[180:183],v[104:107]
EX v85,v85
MM v[104:107],a[224:227],v[164:167],v[104:107]
TR a[184:185],v233 offset:1536
CV v82,v84,v85
MM v[112:115],a[224:227],v[168:171],v[136:139]
EX v86,v86
MM v[112:115],a[228:231],v[184:187],v[112:115]
TR a[186:187],v233 offset:5632
TR a[188:189],v233 offset:9728
MM v[112:115],a[232:235],v[200:203],v[112:115]
EX v87,v87
MM v[112:115],a[236:239],v[216:219],v[112:115]
TR a[190:191],v233 offset:13824
CV v83,v86,v87
MM v[116:119],a[252:255],v[216:219],v[136:139]
EX v88,v88
MM v[116:119],a[248:251],v[200:203],v[116:119]
EX v89,v89
MM v[116:119],a[244:247],v[184:187],v[116:119]
EX v90,v90
MM v[116:119],a[240:243],v[168:171],v[116:119]
EX v91,v91
MM v[124:127],a[240:243],v[172:175],v[140:143]
EX v92,v92
MM v[124:127],a[244:247],v[188:191],v[124:127]
EX v93,v93
MM v[124:127],a[248:251],v[204:207],v[124:127]
EX v94,v94
MM v[124:127],a[252:255],v[220:223],v[124:127]
EX v95,v95
MM v[120:123],a[236:239],v[220:223],v[140:143]
EX v96,v96
MM v[120:123],a[232:235],v[204:207],v[120:123]
EX v97,v97
MM v[120:123],a[228:231],v[188:191],v[120:123]
s_getpc_b64 s[98:99]
LTA3_%=:
s_min_u32 s73, %[swave], 3
s_lshl_b32 s73, s73, 12
s_add_u32 s73, s73, LPG17_%=-LTA3_%=
s_add_u32 s98, s98, s73
s_addc_u32 s99, s99, 0
s_mov_b64 exec, 0
s_setpc_b64 s[98:99]
LTR3_%=:
s_mov_b64 exec, -1
CV v88,v88,v89
MM v[120:123],a[224:227],v[172:175],v[120:123]
s_waitcnt vmcnt(20) lgkmcnt(0)
s_barrier
MM a[0:3],a[128:131],v[0:3],a[0:3]
s_add_u32 m0, s65, 0
CV v89,v90,v91
CV v90,v92,v93
MM a[0:3],a[132:135],v[4:7],a[0:3]
RD a[192:195],v224 offset:32768
CV v91,v94,v95
MM a[8:11],a[132:135],v[12:15],a[8:11]
buffer_load_dwordx4 v228, s[8:11], s64 offen offset:0 lds
MM a[8:11],a[128:131],v[8:11],a[8:11]
RD a[196:199],v225 offset:32768
CV v68,v96,v97
MM a[16:19],a[128:131],v[16:19],a[16:19]
RD a[200:203],v226 offset:32768
RD a[204:207],v227 offset:32768
MM a[16:19],a[132:135],v[20:23],a[16:19]
buffer_load_dwordx4 v229, s[8:11], s64 offen offset:1024 lds
MM a[24:27],a[132:135],v[28:31],a[24:27]
EX v98,v98
MM a[24:27],a[128:131],v[24:27],a[24:27]
RD a[208:211],v224 offset:36864
RD a[212:215],v225 offset:36864
MM a[28:31],a[136:139],v[24:27],a[28:31]
EX v99,v99
MM a[28:31],a[140:143],v[28:31],a[28:31]
RD a[216:219],v226 offset:36864
CV v69,v98,v99
MM a[20:23],a[140:143],v[20:23],a[20:23]
EX v100,v100
MM a[20:23],a[136:139],v[16:19],a[20:23]
EX v101,v101
MM a[12:15],a[136:139],v[8:11],a[12:15]
RD a[220:223],v227 offset:36864
CV v70,v100,v101
MM a[12:15],a[140:143],v[12:15],a[12:15]
buffer_load_dwordx4 v230, s[8:11], s64 offen offset:2048 lds
MM a[4:7],a[140:143],v[4:7],a[4:7]
EX v102,v102
MM a[4:7],a[136:139],v[0:3],a[4:7]
EX v103,v103
MM a[32:35],a[144:147],v[0:3],a[32:35]
RD a[224:227],v224 offset:40960
CV v71,v102,v103
MM a[32:35],a[148:151],v[4:7],a[32:35]
EX v104,v104
MM a[40:43],a[148:151],v[12:15],a[40:43]
EX v105,v105
MM a[40:43],a[144:147],v[8:11],a[40:43]
RD a[228:231],v225 offset:40960
CV v76,v104,v105
MM a[48:51],a[144:147],v[16:19],a[48:51]
buffer_load_dwordx4 v231, s[8:11], s64 offen offset:3072 lds
MM a[48:51],a[148:151],v[20:23],a[48:51]
s_add_u32 m0, s65, 65536
EX v106,v106
MM a[56:59],a[148:151],v[28:31],a[56:59]
EX v107,v107
MM a[56:59],a[144:147],v[24:27],a[56:59]
RD a[232:235],v226 offset:40960
CV v77,v106,v107
MM a[60:63],a[152:155],v[24:27],a[60:63]
EX v108,v108
MM a[60:63],a[156:159],v[28:31],a[60:63]
buffer_load_dwordx4 v235, s[12:15], s81 offen offset:0 lds
MM a[52:55],a[156:159],v[20:23],a[52:55]
EX v109,v109
MM a[52:55],a[152:155],v[16:19],a[52:55]
RD a[236:239],v227 offset:40960
CV v78,v108,v109
MM a[44:47],a[152:155],v[8:11],a[44:47]
EX v110,v110
MM a[44:47],a[156:159],v[12:15],a[44:47]
EX v111,v111
MM a[36:39],a[156:159],v[4:7],a[36:39]
RD a[240:243],v224 offset:45056
CV v79,v110,v111
MM a[36:39],a[152:155],v[0:3],a[36:39]
EX v112,v112
MM a[64:67],a[160:163],v[0:3],a[64:67]
buffer_load_dwordx4 v235, s[12:15], s64 offen offset:1024 lds
MM a[64:67],a[164:167],v[4:7],a[64:67]
EX v113,v113
MM a[72:75],a[164:167],v[12:15],a[72:75]
RD a[244:247],v225 offset:45056
CV v84,v112,v113
MM a[72:75],a[160:163],v[8:11],a[72:75]
EX v114,v114
MM a[80:83],a[160:163],v[16:19],a[80:83]
EX v115,v115
MM a[80:83],a[164:167],v[20:23],a[80:83]
RD a[248:251],v226 offset:45056
CV v85,v114,v115
MM a[88:91],a[164:167],v[28:31],a[88:91]
EX v116,v116
MM a[88:91],a[160:163],v[24:27],a[88:91]
buffer_load_dwordx4 v235, s[12:15], s81 offen offset:2048 lds
MM a[92:95],a[168:171],v[24:27],a[92:95]
EX v117,v117
MM a[92:95],a[172:175],v[28:31],a[92:95]
RD a[252:255],v227 offset:45056
CV v86,v116,v117
MM a[84:87],a[172:175],v[20:23],a[84:87]
EX v118,v118
MM a[84:87],a[168:171],v[16:19],a[84:87]
EX v119,v119
MM a[76:79],a[168:171],v[8:11],a[76:79]
CV v87,v118,v119
MM a[76:79],a[172:175],v[12:15],a[76:79]
EX v120,v120
MM a[68:71],a[172:175],v[4:7],a[68:71]
buffer_load_dwordx4 v235, s[12:15], s64 offen offset:3072 lds
MM a[68:71],a[168:171],v[0:3],a[68:71]
EX v121,v121
MM a[96:99],a[176:179],v[0:3],a[96:99]
EX v122,v122
MM a[96:99],a[180:183],v[4:7],a[96:99]
EX v123,v123
MM a[104:107],a[180:183],v[12:15],a[104:107]
EX v124,v124
MM a[104:107],a[176:179],v[8:11],a[104:107]
EX v125,v125
MM a[112:115],a[176:179],v[16:19],a[112:115]
EX v126,v126
MM a[112:115],a[180:183],v[20:23],a[112:115]
EX v127,v127
MM a[120:123],a[180:183],v[28:31],a[120:123]
s_waitcnt vmcnt(8)
v_cmp_ne_u32 vcc, s69, v252
s_cbranch_vccnz LRFG_%=
LRFGb_%=:
MM a[120:123],a[176:179],v[24:27],a[120:123]
v_lshrrev_b32 v253, 8, v248
v_lshlrev_b32 v253, 10, v253
MM a[124:127],a[184:187],v[24:27],a[124:127]
v_add_f32 v244, v144, v145
v_add_f32 v245, v148, v149
MM a[124:127],a[188:191],v[28:31],a[124:127]
v_add_f32 v246, v152, v153
v_add_f32 v247, v156, v157
v_and_b32 v252, 240, v248
MM a[116:119],a[188:191],v[20:23],a[116:119]
v_add_u32 v253, v253, v252
CV v92,v120,v121
MM a[116:119],a[184:187],v[16:19],a[116:119]
s_lshr_b32 s76, s18, 6
CV v93,v122,v123
CV v94,v124,v125
MM a[108:111],a[184:187],v[8:11],a[108:111]
s_lshl_b32 s76, s76, 13
s_bfe_u32 s90, s18, 131076
CV v95,v126,v127
MM a[108:111],a[188:191],v[12:15],a[108:111]
s_lshl_b32 s90, s90, 8
s_add_u32 s76, s76, s90
s_add_u32 s76, s76, 22183936
MM a[100:103],a[188:191],v[4:7],a[100:103]
s_lshl_b32 s90, %[swave], 11
s_add_u32 s90, s90, %[slds]
MM a[100:103],a[184:187],v[0:3],a[100:103]
s_add_u32 m0, s90, 131072
s_nop 0
buffer_load_dwordx4 v253, s[20:23], s76 offen offset:0 sc1 lds
v_smfmac_f32_16x16x64_bf16 v[144:147], v[236:239], v[64:71], v234
v_smfmac_f32_16x16x64_bf16 v[148:151], v[236:239], v[72:79], v234
)ASM"
R"ASM(
v_smfmac_f32_16x16x64_bf16 v[152:155], v[236:239], v[80:87], v234
s_add_u32 m0, s90, 132096
s_add_u32 s76, s76, 4096
buffer_load_dwordx4 v253, s[20:23], s76 offen offset:0 sc1 lds
v_smfmac_f32_16x16x64_bf16 v[156:159], v[236:239], v[88:95], v234
s_waitcnt lgkmcnt(0)
s_add_u32 s67, s95, 3072
s_mov_b32 s70, 3
s_cmp_ge_u32 s16, 90
s_cbranch_scc1 LLAP_%=
MM v[0:3],a[192:195],v[160:163],v[128:131]
s_add_u32 s64, s64, 16384
TR a[128:129],v232 offset:16384
s_add_u32 s81, s64, 896
MM v[0:3],a[196:199],v[176:179],v[0:3]
TR a[130:131],v232 offset:20480
v_max3_f32 v249, v144, v145, v148
MM v[0:3],a[200:203],v[192:195],v[0:3]
TR a[132:133],v232 offset:24576
v_max3_f32 v249, v249, v149, v152
MM v[0:3],a[204:207],v[208:211],v[0:3]
TR a[134:135],v232 offset:28672
v_max3_f32 v249, v249, v153, v156
MM v[4:7],a[220:223],v[208:211],v[128:131]
TR a[136:137],v233 offset:16384
v_max_f32 v249, v249, v157
MM v[4:7],a[216:219],v[192:195],v[4:7]
TR a[138:139],v233 offset:20480
v_cmp_lt_f32 vcc, s71, v249
MM v[4:7],a[212:215],v[176:179],v[4:7]
TR a[140:141],v233 offset:24576
TR a[142:143],v233 offset:28672
MM v[4:7],a[208:211],v[160:163],v[4:7]
s_cbranch_vccnz LSITE21_%=
LBACK21_%=:
MM v[12:15],a[208:211],v[164:167],v[132:135]
EX v0,v0
MM v[12:15],a[212:215],v[180:183],v[12:15]
EX v1,v1
MM v[12:15],a[216:219],v[196:199],v[12:15]
TR a[144:145],v232 offset:16896
CV v0,v0,v1
MM v[12:15],a[220:223],v[212:215],v[12:15]
EX v2,v2
MM v[8:11],a[204:207],v[212:215],v[132:135]
EX v3,v3
MM v[8:11],a[200:203],v[196:199],v[8:11]
TR a[146:147],v232 offset:20992
CV v1,v2,v3
MM v[8:11],a[196:199],v[180:183],v[8:11]
EX v4,v4
MM v[8:11],a[192:195],v[164:167],v[8:11]
TR a[148:149],v232 offset:25088
TR a[150:151],v232 offset:29184
MM v[16:19],a[192:195],v[168:171],v[136:139]
EX v5,v5
MM v[16:19],a[196:199],v[184:187],v[16:19]
TR a[152:153],v233 offset:16896
CV v2,v4,v5
MM v[16:19],a[200:203],v[200:203],v[16:19]
EX v6,v6
MM v[16:19],a[204:207],v[216:219],v[16:19]
TR a[154:155],v233 offset:20992
TR a[156:157],v233 offset:25088
MM v[20:23],a[220:223],v[216:219],v[136:139]
s_add_u32 s87, s95, 3072
buffer_store_dword v243, v248, s[20:23], s87 offen offset:12 sc1
MM v[20:23],a[216:219],v[200:203],v[20:23]
EX v7,v7
MM v[20:23],a[212:215],v[184:187],v[20:23]
TR a[158:159],v233 offset:29184
CV v3,v6,v7
MM v[20:23],a[208:211],v[168:171],v[20:23]
EX v8,v8
MM v[28:31],a[208:211],v[172:175],v[140:143]
EX v9,v9
MM v[28:31],a[212:215],v[188:191],v[28:31]
TR a[160:161],v232 offset:17408
CV v8,v8,v9
MM v[28:31],a[216:219],v[204:207],v[28:31]
EX v10,v10
MM v[28:31],a[220:223],v[220:223],v[28:31]
EX v11,v11
MM v[24:27],a[204:207],v[220:223],v[140:143]
TR a[162:163],v232 offset:21504
CV v9,v10,v11
MM v[24:27],a[200:203],v[204:207],v[24:27]
TR a[164:165],v232 offset:25600
TR a[166:167],v232 offset:29696
MM v[24:27],a[196:199],v[188:191],v[24:27]
EX v12,v12
MM v[24:27],a[192:195],v[172:175],v[24:27]
EX v13,v13
MM v[32:35],a[224:227],v[160:163],v[128:131]
TR a[168:169],v233 offset:17408
CV v10,v12,v13
MM v[32:35],a[228:231],v[176:179],v[32:35]
EX v14,v14
MM v[32:35],a[232:235],v[192:195],v[32:35]
TR a[170:171],v233 offset:21504
TR a[172:173],v233 offset:25600
MM v[32:35],a[236:239],v[208:211],v[32:35]
EX v15,v15
MM v[36:39],a[252:255],v[208:211],v[128:131]
TR a[174:175],v233 offset:29696
CV v11,v14,v15
MM v[36:39],a[248:251],v[192:195],v[36:39]
EX v16,v16
MM v[36:39],a[244:247],v[176:179],v[36:39]
EX v17,v17
MM v[36:39],a[240:243],v[160:163],v[36:39]
TR a[176:177],v232 offset:17920
CV v16,v16,v17
MM v[44:47],a[240:243],v[164:167],v[132:135]
EX v18,v18
MM v[44:47],a[244:247],v[180:183],v[44:47]
TR a[178:179],v232 offset:22016
TR a[180:181],v232 offset:26112
MM v[44:47],a[248:251],v[196:199],v[44:47]
EX v19,v19
MM v[44:47],a[252:255],v[212:215],v[44:47]
TR a[182:183],v232 offset:30208
CV v17,v18,v19
MM v[40:43],a[236:239],v[212:215],v[132:135]
EX v20,v20
MM v[40:43],a[232:235],v[196:199],v[40:43]
EX v21,v21
MM v[40:43],a[228:231],v[180:183],v[40:43]
TR a[184:185],v233 offset:17920
CV v18,v20,v21
MM v[40:43],a[224:227],v[164:167],v[40:43]
EX v22,v22
MM v[48:51],a[224:227],v[168:171],v[136:139]
TR a[186:187],v233 offset:22016
TR a[188:189],v233 offset:26112
MM v[48:51],a[228:231],v[184:187],v[48:51]
EX v23,v23
MM v[48:51],a[232:235],v[200:203],v[48:51]
TR a[190:191],v233 offset:30208
CV v19,v22,v23
MM v[48:51],a[236:239],v[216:219],v[48:51]
EX v24,v24
MM v[52:55],a[252:255],v[216:219],v[136:139]
EX v25,v25
MM v[52:55],a[248:251],v[200:203],v[52:55]
EX v26,v26
MM v[52:55],a[244:247],v[184:187],v[52:55]
EX v27,v27
MM v[52:55],a[240:243],v[168:171],v[52:55]
EX v28,v28
MM v[60:63],a[240:243],v[172:175],v[140:143]
EX v29,v29
MM v[60:63],a[244:247],v[188:191],v[60:63]
EX v30,v30
MM v[60:63],a[248:251],v[204:207],v[60:63]
EX v31,v31
MM v[60:63],a[252:255],v[220:223],v[60:63]
EX v32,v32
MM v[56:59],a[236:239],v[220:223],v[140:143]
EX v33,v33
MM v[56:59],a[232:235],v[204:207],v[56:59]
EX v34,v34
MM v[56:59],a[228:231],v[188:191],v[56:59]
EX v35,v35
MM v[56:59],a[224:227],v[172:175],v[56:59]
s_waitcnt lgkmcnt(0)
s_barrier
MM a[0:3],a[128:131],v[64:67],a[0:3]
s_add_u32 m0, s65, 16384
CV v24,v24,v25
CV v25,v26,v27
MM a[0:3],a[132:135],v[68:71],a[0:3]
RD a[192:195],v224 offset:49152
CV v26,v28,v29
MM a[8:11],a[132:135],v[76:79],a[8:11]
RD a[196:199],v225 offset:49152
CV v27,v30,v31
MM a[8:11],a[128:131],v[72:75],a[8:11]
buffer_load_dwordx4 v228, s[8:11], s64 offen offset:0 lds
MM a[16:19],a[128:131],v[80:83],a[16:19]
RD a[200:203],v226 offset:49152
CV v4,v32,v33
MM a[16:19],a[132:135],v[84:87],a[16:19]
RD a[204:207],v227 offset:49152
CV v5,v34,v35
MM a[24:27],a[132:135],v[92:95],a[24:27]
buffer_load_dwordx4 v229, s[8:11], s64 offen offset:1024 lds
MM a[24:27],a[128:131],v[88:91],a[24:27]
RD a[208:211],v224 offset:53248
RD a[212:215],v225 offset:53248
MM a[28:31],a[136:139],v[88:91],a[28:31]
buffer_load_dwordx4 v230, s[8:11], s64 offen offset:2048 lds
MM a[28:31],a[140:143],v[92:95],a[28:31]
EX v36,v36
MM a[20:23],a[140:143],v[84:87],a[20:23]
EX v37,v37
MM a[20:23],a[136:139],v[80:83],a[20:23]
RD a[216:219],v226 offset:53248
CV v6,v36,v37
MM a[12:15],a[136:139],v[72:75],a[12:15]
EX v38,v38
MM a[12:15],a[140:143],v[76:79],a[12:15]
RD a[220:223],v227 offset:53248
RD a[224:227],v224 offset:57344
MM a[4:7],a[140:143],v[68:71],a[4:7]
EX v39,v39
MM a[4:7],a[136:139],v[64:67],a[4:7]
RD a[228:231],v225 offset:57344
CV v7,v38,v39
MM a[32:35],a[144:147],v[64:67],a[32:35]
EX v40,v40
MM a[32:35],a[148:151],v[68:71],a[32:35]
buffer_load_dwordx4 v231, s[8:11], s64 offen offset:3072 lds
MM a[40:43],a[148:151],v[76:79],a[40:43]
s_add_u32 m0, s65, 81920
EX v41,v41
MM a[40:43],a[144:147],v[72:75],a[40:43]
RD a[232:235],v226 offset:57344
CV v12,v40,v41
MM a[48:51],a[144:147],v[80:83],a[48:51]
EX v42,v42
MM a[48:51],a[148:151],v[84:87],a[48:51]
EX v43,v43
MM a[56:59],a[148:151],v[92:95],a[56:59]
RD a[236:239],v227 offset:57344
CV v13,v42,v43
MM a[56:59],a[144:147],v[88:91],a[56:59]
EX v44,v44
MM a[60:63],a[152:155],v[88:91],a[60:63]
buffer_load_dwordx4 v235, s[12:15], s81 offen offset:0 lds
MM a[60:63],a[156:159],v[92:95],a[60:63]
EX v45,v45
MM a[52:55],a[156:159],v[84:87],a[52:55]
RD a[240:243],v224 offset:61440
CV v14,v44,v45
MM a[52:55],a[152:155],v[80:83],a[52:55]
EX v46,v46
MM a[44:47],a[152:155],v[72:75],a[44:47]
EX v47,v47
MM a[44:47],a[156:159],v[76:79],a[44:47]
RD a[244:247],v225 offset:61440
CV v15,v46,v47
MM a[36:39],a[156:159],v[68:71],a[36:39]
buffer_load_dwordx4 v235, s[12:15], s64 offen offset:1024 lds
MM a[36:39],a[152:155],v[64:67],a[36:39]
EX v48,v48
MM a[64:67],a[160:163],v[64:67],a[64:67]
EX v49,v49
MM a[64:67],a[164:167],v[68:71],a[64:67]
RD a[248:251],v226 offset:61440
CV v20,v48,v49
MM a[72:75],a[164:167],v[76:79],a[72:75]
EX v50,v50
MM a[72:75],a[160:163],v[72:75],a[72:75]
EX v51,v51
MM a[80:83],a[160:163],v[80:83],a[80:83]
RD a[252:255],v227 offset:61440
CV v21,v50,v51
MM a[80:83],a[164:167],v[84:87],a[80:83]
buffer_load_dwordx4 v235, s[12:15], s81 offen offset:2048 lds
MM a[88:91],a[164:167],v[92:95],a[88:91]
EX v52,v52
MM a[88:91],a[160:163],v[88:91],a[88:91]
EX v53,v53
MM a[92:95],a[168:171],v[88:91],a[92:95]
EX v54,v54
MM a[92:95],a[172:175],v[92:95],a[92:95]
buffer_load_dwordx4 v235, s[12:15], s64 offen offset:3072 lds
MM a[84:87],a[172:175],v[84:87],a[84:87]
EX v55,v55
MM a[84:87],a[168:171],v[80:83],a[84:87]
EX v56,v56
MM a[76:79],a[168:171],v[72:75],a[76:79]
EX v57,v57
MM a[76:79],a[172:175],v[76:79],a[76:79]
EX v58,v58
MM a[68:71],a[172:175],v[68:71],a[68:71]
EX v59,v59
MM a[68:71],a[168:171],v[64:67],a[68:71]
EX v60,v60
MM a[96:99],a[176:179],v[64:67],a[96:99]
EX v61,v61
MM a[96:99],a[180:183],v[68:71],a[96:99]
EX v62,v62
MM a[104:107],a[180:183],v[76:79],a[104:107]
EX v63,v63
MM a[104:107],a[176:179],v[72:75],a[104:107]
CV v22,v52,v53
CV v23,v54,v55
MM a[112:115],a[176:179],v[80:83],a[112:115]
CV v28,v56,v57
CV v29,v58,v59
MM a[112:115],a[180:183],v[84:87],a[112:115]
CV v30,v60,v61
CV v31,v62,v63
MM a[120:123],a[180:183],v[92:95],a[120:123]
MM a[120:123],a[176:179],v[88:91],a[120:123]
MM a[124:127],a[184:187],v[88:91],a[124:127]
v_add_f32 v244, v144, v145
v_add_f32 v245, v148, v149
MM a[124:127],a[188:191],v[92:95],a[124:127]
v_add_f32 v246, v152, v153
v_add_f32 v247, v156, v157
MM a[116:119],a[188:191],v[84:87],a[116:119]
MM a[116:119],a[184:187],v[80:83],a[116:119]
MM a[108:111],a[184:187],v[72:75],a[108:111]
MM a[108:111],a[188:191],v[76:79],a[108:111]
MM a[100:103],a[188:191],v[68:71],a[100:103]
MM a[100:103],a[184:187],v[64:67],a[100:103]
v_smfmac_f32_16x16x64_bf16 v[144:147], v[236:239], v[0:7], v234
v_smfmac_f32_16x16x64_bf16 v[148:151], v[236:239], v[8:15], v234
v_smfmac_f32_16x16x64_bf16 v[152:155], v[236:239], v[16:23], v234
v_smfmac_f32_16x16x64_bf16 v[156:159], v[236:239], v[24:31], v234
s_waitcnt lgkmcnt(0)
MM v[64:67],a[192:195],v[160:163],v[128:131]
s_add_u32 s64, s64, 16384
TR a[128:129],v232 offset:32768
s_add_u32 s81, s64, 896
MM v[64:67],a[196:199],v[176:179],v[64:67]
TR a[130:131],v232 offset:36864
v_max3_f32 v249, v144, v145, v148
MM v[64:67],a[200:203],v[192:195],v[64:67]
TR a[132:133],v232 offset:40960
v_max3_f32 v249, v249, v149, v152
MM v[64:67],a[204:207],v[208:211],v[64:67]
TR a[134:135],v232 offset:45056
v_max3_f32 v249, v249, v153, v156
MM v[68:71],a[220:223],v[208:211],v[128:131]
TR a[136:137],v233 offset:32768
v_max_f32 v249, v249, v157
MM v[68:71],a[216:219],v[192:195],v[68:71]
TR a[138:139],v233 offset:36864
v_cmp_lt_f32 vcc, s71, v249
MM v[68:71],a[212:215],v[176:179],v[68:71]
TR a[140:141],v233 offset:40960
TR a[142:143],v233 offset:45056
MM v[68:71],a[208:211],v[160:163],v[68:71]
s_cbranch_vccnz LSITE22_%=
LBACK22_%=:
MM v[76:79],a[208:211],v[164:167],v[132:135]
v_lshrrev_b32 v43, 7, v248
v_bfe_u32 v42, v248, 6, 1
MM v[76:79],a[212:215],v[180:183],v[76:79]
v_lshl_add_u32 v40, v42, 3, v43
v_lshlrev_b32 v40, 4, v40
MM v[76:79],a[216:219],v[196:199],v[76:79]
v_bfe_u32 v42, v248, 4, 2
v_lshl_add_u32 v40, v42, 2, v40
MM v[76:79],a[220:223],v[212:215],v[76:79]
s_lshl_b32 s76, %[swave], 11
EX v64,v64
MM v[72:75],a[204:207],v[212:215],v[132:135]
s_add_u32 s76, s76, %[slds]
EX v65,v65
MM v[72:75],a[200:203],v[196:199],v[72:75]
s_add_u32 s76, s76, 131072
v_add_u32 v40, s76, v40
CV v64,v64,v65
MM v[72:75],a[196:199],v[180:183],v[72:75]
s_waitcnt vmcnt(9)
ds_read_b32 v32, v40 offset:0
ds_read_b32 v36, v40 offset:256
MM v[72:75],a[192:195],v[164:167],v[72:75]
ds_read_b32 v33, v40 offset:512
ds_read_b32 v37, v40 offset:768
MM v[80:83],a[192:195],v[168:171],v[136:139]
ds_read_b32 v34, v40 offset:1024
s_nop 0
LPG17_%=:
s_cbranch_execz LTR3_%=
ds_read_b32 v38, v40 offset:1280
EX v66,v66
MM v[80:83],a[196:199],v[184:187],v[80:83]
ds_read_b32 v35, v40 offset:1536
ds_read_b32 v39, v40 offset:1792
EX v67,v67
MM v[80:83],a[200:203],v[200:203],v[80:83]
v_mul_u32_u24 v41, 3456, v43
v_bfe_u32 v42, v248, 4, 3
MM v[80:83],a[204:207],v[216:219],v[80:83]
TR a[144:145],v232 offset:33280
v_lshl_add_u32 v41, v42, 1, v41
EX v68,v68
MM v[84:87],a[220:223],v[216:219],v[136:139]
s_add_u32 s76, s18, 21626880
EX v69,v69
MM v[84:87],a[216:219],v[200:203],v[84:87]
)ASM"
R"ASM(
TR a[146:147],v232 offset:37376
TR a[148:149],v232 offset:41472
MM v[84:87],a[212:215],v[184:187],v[84:87]
EX v70,v70
MM v[84:87],a[208:211],v[168:171],v[84:87]
TR a[150:151],v232 offset:45568
CV v65,v66,v67
MM v[92:95],a[208:211],v[172:175],v[140:143]
EX v71,v71
MM v[92:95],a[212:215],v[188:191],v[92:95]
EX v72,v72
MM v[92:95],a[216:219],v[204:207],v[92:95]
TR a[152:153],v233 offset:33280
CV v66,v68,v69
MM v[92:95],a[220:223],v[220:223],v[92:95]
EX v73,v73
MM v[88:91],a[204:207],v[220:223],v[140:143]
s_waitcnt lgkmcnt(5)
v_add_f32 v32, v32, v33
v_add_f32 v36, v36, v37
MM v[88:91],a[200:203],v[204:207],v[88:91]
v_add_f32 v34, v34, v35
v_add_f32 v38, v38, v39
MM v[88:91],a[196:199],v[188:191],v[88:91]
TR a[154:155],v233 offset:37376
v_add_f32 v32, v32, v34
v_add_f32 v36, v36, v38
MM v[88:91],a[192:195],v[172:175],v[88:91]
CV v42,v32,v36
v_lshlrev_b32 v32, 16, v42
EX v74,v74
MM v[96:99],a[224:227],v[160:163],v[128:131]
TR a[156:157],v233 offset:41472
v_and_b32 v36, 0xffff0000, v42
v_mul_f32 v42, 0xbfb8aa3b, v32
EX v75,v75
MM v[96:99],a[228:231],v[176:179],v[96:99]
EX v42,v42
MM v[96:99],a[232:235],v[192:195],v[96:99]
TR a[158:159],v233 offset:45568
v_add_f32 v42, 1.0, v42
EX v76,v76
MM v[96:99],a[236:239],v[208:211],v[96:99]
v_rcp_f32 v42, v42
MM v[100:103],a[252:255],v[208:211],v[128:131]
TR a[160:161],v232 offset:33792
v_mul_f32 v42, v32, v42
CV v42,v42,v42
EX v77,v77
CV v67,v70,v71
MM v[100:103],a[248:251],v[192:195],v[100:103]
v_lshlrev_b32 v42, 16, v42
v_mul_f32 v32, v42, v36
MM v[100:103],a[244:247],v[176:179],v[100:103]
TR a[162:163],v232 offset:37888
CV v43,v32,v32
EX v78,v78
MM v[100:103],a[240:243],v[160:163],v[100:103]
buffer_store_short v43, v41, s[20:23], s76 offen offset:0 sc1
s_nop 1
TR a[164:165],v232 offset:41984
MM v[108:111],a[240:243],v[164:167],v[132:135]
EX v79,v79
CV v72,v72,v73
MM v[108:111],a[244:247],v[180:183],v[108:111]
TR a[166:167],v232 offset:46080
CV v73,v74,v75
MM v[108:111],a[248:251],v[196:199],v[108:111]
EX v80,v80
MM v[108:111],a[252:255],v[212:215],v[108:111]
TR a[168:169],v233 offset:33792
TR a[170:171],v233 offset:37888
EX v81,v81
MM v[104:107],a[236:239],v[212:215],v[132:135]
TR a[172:173],v233 offset:41984
CV v74,v76,v77
MM v[104:107],a[232:235],v[196:199],v[104:107]
EX v82,v82
MM v[104:107],a[228:231],v[180:183],v[104:107]
EX v83,v83
MM v[104:107],a[224:227],v[164:167],v[104:107]
TR a[174:175],v233 offset:46080
CV v75,v78,v79
MM v[112:115],a[224:227],v[168:171],v[136:139]
EX v84,v84
MM v[112:115],a[228:231],v[184:187],v[112:115]
TR a[176:177],v232 offset:34304
TR a[178:179],v232 offset:38400
MM v[112:115],a[232:235],v[200:203],v[112:115]
EX v85,v85
MM v[112:115],a[236:239],v[216:219],v[112:115]
TR a[180:181],v232 offset:42496
CV v80,v80,v81
MM v[116:119],a[252:255],v[216:219],v[136:139]
EX v86,v86
MM v[116:119],a[248:251],v[200:203],v[116:119]
TR a[182:183],v232 offset:46592
CV v81,v82,v83
MM v[116:119],a[244:247],v[184:187],v[116:119]
EX v87,v87
MM v[116:119],a[240:243],v[168:171],v[116:119]
TR a[184:185],v233 offset:34304
CV v82,v84,v85
MM v[124:127],a[240:243],v[172:175],v[140:143]
EX v88,v88
MM v[124:127],a[244:247],v[188:191],v[124:127]
TR a[186:187],v233 offset:38400
TR a[188:189],v233 offset:42496
MM v[124:127],a[248:251],v[204:207],v[124:127]
TR a[190:191],v233 offset:46592
CV v83,v86,v87
EX v89,v89
MM v[124:127],a[252:255],v[220:223],v[124:127]
EX v90,v90
MM v[120:123],a[236:239],v[220:223],v[140:143]
EX v91,v91
MM v[120:123],a[232:235],v[204:207],v[120:123]
EX v92,v92
MM v[120:123],a[228:231],v[188:191],v[120:123]
EX v93,v93
MM v[120:123],a[224:227],v[172:175],v[120:123]
s_waitcnt lgkmcnt(0)
s_barrier
MM a[0:3],a[128:131],v[0:3],a[0:3]
s_add_u32 m0, s65, 32768
CV v88,v88,v89
CV v89,v90,v91
MM a[0:3],a[132:135],v[4:7],a[0:3]
EX v94,v94
MM a[8:11],a[132:135],v[12:15],a[8:11]
RD a[192:195],v224 offset:0
CV v90,v92,v93
MM a[8:11],a[128:131],v[8:11],a[8:11]
EX v95,v95
MM a[16:19],a[128:131],v[16:19],a[16:19]
RD a[196:199],v225 offset:0
CV v91,v94,v95
MM a[16:19],a[132:135],v[20:23],a[16:19]
EX v96,v96
MM a[24:27],a[132:135],v[28:31],a[24:27]
buffer_load_dwordx4 v228, s[8:11], s64 offen offset:0 lds
MM a[24:27],a[128:131],v[24:27],a[24:27]
EX v97,v97
MM a[28:31],a[136:139],v[24:27],a[28:31]
EX v98,v98
MM a[28:31],a[140:143],v[28:31],a[28:31]
RD a[200:203],v226 offset:0
CV v68,v96,v97
MM a[20:23],a[140:143],v[20:23],a[20:23]
EX v99,v99
MM a[20:23],a[136:139],v[16:19],a[20:23]
RD a[204:207],v227 offset:0
CV v69,v98,v99
MM a[12:15],a[136:139],v[8:11],a[12:15]
EX v100,v100
MM a[12:15],a[140:143],v[12:15],a[12:15]
buffer_load_dwordx4 v229, s[8:11], s64 offen offset:1024 lds
MM a[4:7],a[140:143],v[4:7],a[4:7]
EX v101,v101
MM a[4:7],a[136:139],v[0:3],a[4:7]
RD a[208:211],v224 offset:4096
CV v70,v100,v101
MM a[32:35],a[144:147],v[0:3],a[32:35]
EX v102,v102
MM a[32:35],a[148:151],v[4:7],a[32:35]
EX v103,v103
MM a[40:43],a[148:151],v[12:15],a[40:43]
RD a[212:215],v225 offset:4096
CV v71,v102,v103
MM a[40:43],a[144:147],v[8:11],a[40:43]
EX v104,v104
MM a[48:51],a[144:147],v[16:19],a[48:51]
buffer_load_dwordx4 v230, s[8:11], s64 offen offset:2048 lds
MM a[48:51],a[148:151],v[20:23],a[48:51]
EX v105,v105
MM a[56:59],a[148:151],v[28:31],a[56:59]
RD a[216:219],v226 offset:4096
CV v76,v104,v105
MM a[56:59],a[144:147],v[24:27],a[56:59]
EX v106,v106
MM a[60:63],a[152:155],v[24:27],a[60:63]
EX v107,v107
MM a[60:63],a[156:159],v[28:31],a[60:63]
RD a[220:223],v227 offset:4096
CV v77,v106,v107
MM a[52:55],a[156:159],v[20:23],a[52:55]
EX v108,v108
MM a[52:55],a[152:155],v[16:19],a[52:55]
buffer_load_dwordx4 v231, s[8:11], s64 offen offset:3072 lds
MM a[44:47],a[152:155],v[8:11],a[44:47]
s_add_u32 m0, s65, 98304
EX v109,v109
MM a[44:47],a[156:159],v[12:15],a[44:47]
RD a[224:227],v224 offset:8192
CV v78,v108,v109
MM a[36:39],a[156:159],v[4:7],a[36:39]
EX v110,v110
MM a[36:39],a[152:155],v[0:3],a[36:39]
EX v111,v111
MM a[64:67],a[160:163],v[0:3],a[64:67]
RD a[228:231],v225 offset:8192
CV v79,v110,v111
MM a[64:67],a[164:167],v[4:7],a[64:67]
EX v112,v112
MM a[72:75],a[164:167],v[12:15],a[72:75]
buffer_load_dwordx4 v235, s[12:15], s81 offen offset:0 lds
MM a[72:75],a[160:163],v[8:11],a[72:75]
EX v113,v113
MM a[80:83],a[160:163],v[16:19],a[80:83]
RD a[232:235],v226 offset:8192
CV v84,v112,v113
MM a[80:83],a[164:167],v[20:23],a[80:83]
EX v114,v114
MM a[88:91],a[164:167],v[28:31],a[88:91]
EX v115,v115
MM a[88:91],a[160:163],v[24:27],a[88:91]
RD a[236:239],v227 offset:8192
CV v85,v114,v115
MM a[92:95],a[168:171],v[24:27],a[92:95]
EX v116,v116
MM a[92:95],a[172:175],v[28:31],a[92:95]
buffer_load_dwordx4 v235, s[12:15], s64 offen offset:1024 lds
MM a[84:87],a[172:175],v[20:23],a[84:87]
EX v117,v117
MM a[84:87],a[168:171],v[16:19],a[84:87]
RD a[240:243],v224 offset:12288
CV v86,v116,v117
MM a[76:79],a[168:171],v[8:11],a[76:79]
EX v118,v118
MM a[76:79],a[172:175],v[12:15],a[76:79]
EX v119,v119
MM a[68:71],a[172:175],v[4:7],a[68:71]
RD a[244:247],v225 offset:12288
CV v87,v118,v119
MM a[68:71],a[168:171],v[0:3],a[68:71]
buffer_load_dwordx4 v235, s[12:15], s81 offen offset:2048 lds
MM a[96:99],a[176:179],v[0:3],a[96:99]
EX v120,v120
MM a[96:99],a[180:183],v[4:7],a[96:99]
EX v121,v121
MM a[104:107],a[180:183],v[12:15],a[104:107]
RD a[248:251],v226 offset:12288
CV v92,v120,v121
MM a[104:107],a[176:179],v[8:11],a[104:107]
EX v122,v122
MM a[112:115],a[176:179],v[16:19],a[112:115]
EX v123,v123
MM a[112:115],a[180:183],v[20:23],a[112:115]
RD a[252:255],v227 offset:12288
CV v93,v122,v123
MM a[120:123],a[180:183],v[28:31],a[120:123]
buffer_load_dwordx4 v235, s[12:15], s64 offen offset:3072 lds
MM a[120:123],a[176:179],v[24:27],a[120:123]
EX v124,v124
MM a[124:127],a[184:187],v[24:27],a[124:127]
v_add_f32 v244, v144, v145
v_add_f32 v245, v148, v149
MM a[124:127],a[188:191],v[28:31],a[124:127]
v_add_f32 v246, v152, v153
v_add_f32 v247, v156, v157
MM a[116:119],a[188:191],v[20:23],a[116:119]
EX v125,v125
MM a[116:119],a[184:187],v[16:19],a[116:119]
EX v126,v126
MM a[108:111],a[184:187],v[8:11],a[108:111]
EX v127,v127
MM a[108:111],a[188:191],v[12:15],a[108:111]
CV v94,v124,v125
CV v95,v126,v127
MM a[100:103],a[188:191],v[4:7],a[100:103]
MM a[100:103],a[184:187],v[0:3],a[100:103]
v_smfmac_f32_16x16x64_bf16 v[144:147], v[236:239], v[64:71], v234
v_smfmac_f32_16x16x64_bf16 v[148:151], v[236:239], v[72:79], v234
v_smfmac_f32_16x16x64_bf16 v[152:155], v[236:239], v[80:87], v234
v_smfmac_f32_16x16x64_bf16 v[156:159], v[236:239], v[88:95], v234
s_waitcnt lgkmcnt(0)
MM v[0:3],a[192:195],v[160:163],v[128:131]
s_add_u32 s64, s64, 16384
TR a[128:129],v232 offset:49152
s_add_u32 s81, s64, 896
MM v[0:3],a[196:199],v[176:179],v[0:3]
TR a[130:131],v232 offset:53248
v_max3_f32 v249, v144, v145, v148
MM v[0:3],a[200:203],v[192:195],v[0:3]
TR a[132:133],v232 offset:57344
v_max3_f32 v249, v249, v149, v152
MM v[0:3],a[204:207],v[208:211],v[0:3]
TR a[134:135],v232 offset:61440
v_max3_f32 v249, v249, v153, v156
MM v[4:7],a[220:223],v[208:211],v[128:131]
TR a[136:137],v233 offset:49152
v_max_f32 v249, v249, v157
MM v[4:7],a[216:219],v[192:195],v[4:7]
TR a[138:139],v233 offset:53248
v_cmp_lt_f32 vcc, s71, v249
MM v[4:7],a[212:215],v[176:179],v[4:7]
TR a[140:141],v233 offset:57344
TR a[142:143],v233 offset:61440
MM v[4:7],a[208:211],v[160:163],v[4:7]
s_cbranch_vccnz LSITE23_%=
LBACK23_%=:
MM v[12:15],a[208:211],v[164:167],v[132:135]
EX v0,v0
MM v[12:15],a[212:215],v[180:183],v[12:15]
EX v1,v1
MM v[12:15],a[216:219],v[196:199],v[12:15]
TR a[144:145],v232 offset:49664
CV v0,v0,v1
MM v[12:15],a[220:223],v[212:215],v[12:15]
EX v2,v2
MM v[8:11],a[204:207],v[212:215],v[132:135]
EX v3,v3
MM v[8:11],a[200:203],v[196:199],v[8:11]
TR a[146:147],v232 offset:53760
CV v1,v2,v3
MM v[8:11],a[196:199],v[180:183],v[8:11]
EX v4,v4
MM v[8:11],a[192:195],v[164:167],v[8:11]
TR a[148:149],v232 offset:57856
TR a[150:151],v232 offset:61952
MM v[16:19],a[192:195],v[168:171],v[136:139]
EX v5,v5
MM v[16:19],a[196:199],v[184:187],v[16:19]
TR a[152:153],v233 offset:49664
CV v2,v4,v5
MM v[16:19],a[200:203],v[200:203],v[16:19]
EX v6,v6
MM v[16:19],a[204:207],v[216:219],v[16:19]
TR a[154:155],v233 offset:53760
TR a[156:157],v233 offset:57856
MM v[20:23],a[220:223],v[216:219],v[136:139]
EX v7,v7
MM v[20:23],a[216:219],v[200:203],v[20:23]
TR a[158:159],v233 offset:61952
CV v3,v6,v7
MM v[20:23],a[212:215],v[184:187],v[20:23]
EX v8,v8
MM v[20:23],a[208:211],v[168:171],v[20:23]
EX v9,v9
MM v[28:31],a[208:211],v[172:175],v[140:143]
TR a[160:161],v232 offset:50176
CV v8,v8,v9
MM v[28:31],a[212:215],v[188:191],v[28:31]
EX v10,v10
MM v[28:31],a[216:219],v[204:207],v[28:31]
TR a[162:163],v232 offset:54272
TR a[164:165],v232 offset:58368
MM v[28:31],a[220:223],v[220:223],v[28:31]
EX v11,v11
MM v[24:27],a[204:207],v[220:223],v[140:143]
TR a[166:167],v232 offset:62464
CV v9,v10,v11
MM v[24:27],a[200:203],v[204:207],v[24:27]
EX v12,v12
MM v[24:27],a[196:199],v[188:191],v[24:27]
EX v13,v13
MM v[24:27],a[192:195],v[172:175],v[24:27]
TR a[168:169],v233 offset:50176
CV v10,v12,v13
MM v[32:35],a[224:227],v[160:163],v[128:131]
EX v14,v14
MM v[32:35],a[228:231],v[176:179],v[32:35]
TR a[170:171],v233 offset:54272
TR a[172:173],v233 offset:58368
MM v[32:35],a[232:235],v[192:195],v[32:35]
EX v15,v15
MM v[32:35],a[236:239],v[208:211],v[32:35]
TR a[174:175],v233 offset:62464
CV v11,v14,v15
MM v[36:39],a[252:255],v[208:211],v[128:131]
EX v16,v16
MM v[36:39],a[248:251],v[192:195],v[36:39]
EX v17,v17
MM v[36:39],a[244:247],v[176:179],v[36:39]
TR a[176:177],v232 offset:50688
CV v16,v16,v17
MM v[36:39],a[240:243],v[160:163],v[36:39]
EX v18,v18
MM v[44:47],a[240:243],v[164:167],v[132:135]
TR a[178:179],v232 offset:54784
TR a[180:181],v232 offset:58880
MM v[44:47],a[244:247],v[180:183],v[44:47]
EX v19,v19
MM v[44:47],a[248:251],v[196:199],v[44:47]
TR a[182:183],v232 offset:62976
CV v17,v18,v19
MM v[44:47],a[252:255],v[212:215],v[44:47]
EX v20,v20
MM v[40:43],a[236:239],v[212:215],v[132:135]
TR a[184:185],v233 offset:50688
TR a[186:187],v233 offset:54784
MM v[40:43],a[232:235],v[196:199],v[40:43]
EX v21,v21
MM v[40:43],a[228:231],v[180:183],v[40:43]
TR a[188:189],v233 offset:58880
)ASM"
R"ASM(
CV v18,v20,v21
MM v[40:43],a[224:227],v[164:167],v[40:43]
EX v22,v22
MM v[48:51],a[224:227],v[168:171],v[136:139]
EX v23,v23
MM v[48:51],a[228:231],v[184:187],v[48:51]
TR a[190:191],v233 offset:62976
CV v19,v22,v23
MM v[48:51],a[232:235],v[200:203],v[48:51]
EX v24,v24
MM v[48:51],a[236:239],v[216:219],v[48:51]
EX v25,v25
MM v[52:55],a[252:255],v[216:219],v[136:139]
EX v26,v26
MM v[52:55],a[248:251],v[200:203],v[52:55]
EX v27,v27
MM v[52:55],a[244:247],v[184:187],v[52:55]
EX v28,v28
MM v[52:55],a[240:243],v[168:171],v[52:55]
EX v29,v29
MM v[60:63],a[240:243],v[172:175],v[140:143]
EX v30,v30
MM v[60:63],a[244:247],v[188:191],v[60:63]
EX v31,v31
MM v[60:63],a[248:251],v[204:207],v[60:63]
EX v32,v32
MM v[60:63],a[252:255],v[220:223],v[60:63]
EX v33,v33
MM v[56:59],a[236:239],v[220:223],v[140:143]
EX v34,v34
MM v[56:59],a[232:235],v[204:207],v[56:59]
EX v35,v35
MM v[56:59],a[228:231],v[188:191],v[56:59]
EX v36,v36
MM v[56:59],a[224:227],v[172:175],v[56:59]
s_waitcnt vmcnt(13) lgkmcnt(0)
s_barrier
MM a[0:3],a[128:131],v[64:67],a[0:3]
s_add_u32 m0, s65, 49152
CV v24,v24,v25
CV v25,v26,v27
MM a[0:3],a[132:135],v[68:71],a[0:3]
RD a[192:195],v224 offset:16384
CV v26,v28,v29
MM a[8:11],a[132:135],v[76:79],a[8:11]
RD a[196:199],v225 offset:16384
CV v27,v30,v31
MM a[8:11],a[128:131],v[72:75],a[8:11]
buffer_load_dwordx4 v228, s[8:11], s64 offen offset:0 lds
MM a[16:19],a[128:131],v[80:83],a[16:19]
RD a[200:203],v226 offset:16384
CV v4,v32,v33
MM a[16:19],a[132:135],v[84:87],a[16:19]
RD a[204:207],v227 offset:16384
CV v5,v34,v35
MM a[24:27],a[132:135],v[92:95],a[24:27]
buffer_load_dwordx4 v229, s[8:11], s64 offen offset:1024 lds
MM a[24:27],a[128:131],v[88:91],a[24:27]
RD a[208:211],v224 offset:20480
RD a[212:215],v225 offset:20480
MM a[28:31],a[136:139],v[88:91],a[28:31]
buffer_load_dwordx4 v230, s[8:11], s64 offen offset:2048 lds
MM a[28:31],a[140:143],v[92:95],a[28:31]
RD a[216:219],v226 offset:20480
RD a[220:223],v227 offset:20480
MM a[20:23],a[140:143],v[84:87],a[20:23]
EX v37,v37
MM a[20:23],a[136:139],v[80:83],a[20:23]
RD a[224:227],v224 offset:24576
CV v6,v36,v37
MM a[12:15],a[136:139],v[72:75],a[12:15]
EX v38,v38
MM a[12:15],a[140:143],v[76:79],a[12:15]
buffer_load_dwordx4 v231, s[8:11], s64 offen offset:3072 lds
MM a[4:7],a[140:143],v[68:71],a[4:7]
s_add_u32 m0, s65, 114688
EX v39,v39
MM a[4:7],a[136:139],v[64:67],a[4:7]
RD a[228:231],v225 offset:24576
CV v7,v38,v39
MM a[32:35],a[144:147],v[64:67],a[32:35]
EX v40,v40
MM a[32:35],a[148:151],v[68:71],a[32:35]
EX v41,v41
MM a[40:43],a[148:151],v[76:79],a[40:43]
RD a[232:235],v226 offset:24576
CV v12,v40,v41
MM a[40:43],a[144:147],v[72:75],a[40:43]
EX v42,v42
MM a[48:51],a[144:147],v[80:83],a[48:51]
buffer_load_dwordx4 v235, s[12:15], s81 offen offset:0 lds
MM a[48:51],a[148:151],v[84:87],a[48:51]
EX v43,v43
MM a[56:59],a[148:151],v[92:95],a[56:59]
RD a[236:239],v227 offset:24576
CV v13,v42,v43
MM a[56:59],a[144:147],v[88:91],a[56:59]
EX v44,v44
MM a[60:63],a[152:155],v[88:91],a[60:63]
EX v45,v45
MM a[60:63],a[156:159],v[92:95],a[60:63]
RD a[240:243],v224 offset:28672
CV v14,v44,v45
MM a[52:55],a[156:159],v[84:87],a[52:55]
EX v46,v46
MM a[52:55],a[152:155],v[80:83],a[52:55]
buffer_load_dwordx4 v235, s[12:15], s64 offen offset:1024 lds
MM a[44:47],a[152:155],v[72:75],a[44:47]
EX v47,v47
MM a[44:47],a[156:159],v[76:79],a[44:47]
RD a[244:247],v225 offset:28672
CV v15,v46,v47
MM a[36:39],a[156:159],v[68:71],a[36:39]
EX v48,v48
MM a[36:39],a[152:155],v[64:67],a[36:39]
EX v49,v49
MM a[64:67],a[160:163],v[64:67],a[64:67]
RD a[248:251],v226 offset:28672
CV v20,v48,v49
MM a[64:67],a[164:167],v[68:71],a[64:67]
EX v50,v50
MM a[72:75],a[164:167],v[76:79],a[72:75]
buffer_load_dwordx4 v235, s[12:15], s81 offen offset:2048 lds
MM a[72:75],a[160:163],v[72:75],a[72:75]
EX v51,v51
MM a[80:83],a[160:163],v[80:83],a[80:83]
RD a[252:255],v227 offset:28672
CV v21,v50,v51
MM a[80:83],a[164:167],v[84:87],a[80:83]
EX v52,v52
MM a[88:91],a[164:167],v[92:95],a[88:91]
EX v53,v53
MM a[88:91],a[160:163],v[88:91],a[88:91]
buffer_load_dwordx4 v235, s[12:15], s64 offen offset:3072 lds
MM a[92:95],a[168:171],v[88:91],a[92:95]
EX v54,v54
MM a[92:95],a[172:175],v[92:95],a[92:95]
EX v55,v55
MM a[84:87],a[172:175],v[84:87],a[84:87]
EX v56,v56
MM a[84:87],a[168:171],v[80:83],a[84:87]
EX v57,v57
MM a[76:79],a[168:171],v[72:75],a[76:79]
EX v58,v58
MM a[76:79],a[172:175],v[76:79],a[76:79]
EX v59,v59
MM a[68:71],a[172:175],v[68:71],a[68:71]
EX v60,v60
MM a[68:71],a[168:171],v[64:67],a[68:71]
EX v61,v61
MM a[96:99],a[176:179],v[64:67],a[96:99]
EX v62,v62
MM a[96:99],a[180:183],v[68:71],a[96:99]
EX v63,v63
MM a[104:107],a[180:183],v[76:79],a[104:107]
CV v22,v52,v53
CV v23,v54,v55
MM a[104:107],a[176:179],v[72:75],a[104:107]
CV v28,v56,v57
CV v29,v58,v59
MM a[112:115],a[176:179],v[80:83],a[112:115]
CV v30,v60,v61
CV v31,v62,v63
MM a[112:115],a[180:183],v[84:87],a[112:115]
MM a[120:123],a[180:183],v[92:95],a[120:123]
s_lshr_b32 s91, s18, 2
s_add_u32 s91, s91, 69632
s_waitcnt vmcnt(16)
MM a[120:123],a[176:179],v[88:91],a[120:123]
v_mov_b32 v250, s69
s_mov_b64 exec, 1
buffer_store_dword v250, off, s[20:23], s91 offset:0 sc1
s_mov_b64 exec, -1
MM a[124:127],a[184:187],v[88:91],a[124:127]
v_add_f32 v244, v144, v145
v_add_f32 v245, v148, v149
MM a[124:127],a[188:191],v[92:95],a[124:127]
v_add_f32 v246, v152, v153
v_add_f32 v247, v156, v157
v_lshlrev_b32 v96, 3, v248
MM a[116:119],a[188:191],v[84:87],a[116:119]
v_min_u32 v96, 4992, v96
LPG18_%=:
s_cbranch_execz LTR3_%=
s_sub_u32 s91, 5242880, s26
MM a[116:119],a[184:187],v[80:83],a[116:119]
s_sub_u32 s60, s24, s91
s_subb_u32 s61, s25, 0
s_mov_b32 s62, 5242880
MM a[108:111],a[184:187],v[72:75],a[108:111]
s_mov_b32 s63, 131072
s_lshl_b32 s90, s16, 2
s_add_u32 s90, s90, %[swave]
MM a[108:111],a[188:191],v[76:79],a[108:111]
s_mul_i32 s90, s90, 10240
s_add_u32 s90, s90, 0
s_add_u32 s91, %[slds], 135168
MM a[100:103],a[188:191],v[68:71],a[100:103]
s_mov_b32 m0, s91
s_nop 0
buffer_load_dword v96, s[60:63], s90 offen offset:0 lds
MM a[100:103],a[184:187],v[64:67],a[100:103]
s_lshr_b32 s77, %[sb], 3
s_lshl_b32 s77, s77, 2
s_add_u32 s77, s77, %[swave]
v_smfmac_f32_16x16x64_bf16 v[144:147], v[236:239], v[0:7], v234
s_lshl_b32 s77, s77, 9
s_add_u32 s77, s77, 0
v_smfmac_f32_16x16x64_bf16 v[148:151], v[236:239], v[8:15], v234
v_lshrrev_b32 v253, 1, v248
v_smfmac_f32_16x16x64_bf16 v[152:155], v[236:239], v[16:23], v234
v_add_u32 v253, s77, v253
v_smfmac_f32_16x16x64_bf16 v[156:159], v[236:239], v[24:31], v234
s_waitcnt lgkmcnt(0)
s_mov_b32 s77, LKEND_ka-LPD1_%=-16
v_min_u32 v253, s77, v253
s_getpc_b64 s[98:99]
LPD1_%=:
global_load_dword v253, v253, s[98:99]
MM v[64:67],a[192:195],v[160:163],v[128:131]
s_add_u32 s64, s64, 16384
TR a[128:129],v232 offset:0
s_add_u32 s81, s64, 896
MM v[64:67],a[196:199],v[176:179],v[64:67]
TR a[130:131],v232 offset:4096
v_max3_f32 v249, v144, v145, v148
MM v[64:67],a[200:203],v[192:195],v[64:67]
TR a[132:133],v232 offset:8192
v_max3_f32 v249, v249, v149, v152
MM v[64:67],a[204:207],v[208:211],v[64:67]
TR a[134:135],v232 offset:12288
v_max3_f32 v249, v249, v153, v156
MM v[68:71],a[220:223],v[208:211],v[128:131]
TR a[136:137],v233 offset:0
v_max_f32 v249, v249, v157
MM v[68:71],a[216:219],v[192:195],v[68:71]
TR a[138:139],v233 offset:4096
v_cmp_lt_f32 vcc, s71, v249
MM v[68:71],a[212:215],v[176:179],v[68:71]
TR a[140:141],v233 offset:8192
TR a[142:143],v233 offset:12288
MM v[68:71],a[208:211],v[160:163],v[68:71]
s_cbranch_vccnz LSITE24_%=
LBACK24_%=:
MM v[76:79],a[208:211],v[164:167],v[132:135]
EX v64,v64
MM v[76:79],a[212:215],v[180:183],v[76:79]
EX v65,v65
MM v[76:79],a[216:219],v[196:199],v[76:79]
TR a[144:145],v232 offset:512
CV v64,v64,v65
MM v[76:79],a[220:223],v[212:215],v[76:79]
EX v66,v66
MM v[72:75],a[204:207],v[212:215],v[132:135]
EX v67,v67
MM v[72:75],a[200:203],v[196:199],v[72:75]
TR a[146:147],v232 offset:4608
CV v65,v66,v67
MM v[72:75],a[196:199],v[180:183],v[72:75]
EX v68,v68
MM v[72:75],a[192:195],v[164:167],v[72:75]
TR a[148:149],v232 offset:8704
TR a[150:151],v232 offset:12800
MM v[80:83],a[192:195],v[168:171],v[136:139]
EX v69,v69
MM v[80:83],a[196:199],v[184:187],v[80:83]
TR a[152:153],v233 offset:512
CV v66,v68,v69
MM v[80:83],a[200:203],v[200:203],v[80:83]
EX v70,v70
MM v[80:83],a[204:207],v[216:219],v[80:83]
TR a[154:155],v233 offset:4608
TR a[156:157],v233 offset:8704
MM v[84:87],a[220:223],v[216:219],v[136:139]
s_add_u32 s87, s95, 4096
buffer_store_dword v243, v248, s[20:23], s87 offen offset:12 sc1
MM v[84:87],a[216:219],v[200:203],v[84:87]
EX v71,v71
MM v[84:87],a[212:215],v[184:187],v[84:87]
TR a[158:159],v233 offset:12800
CV v67,v70,v71
MM v[84:87],a[208:211],v[168:171],v[84:87]
EX v72,v72
MM v[92:95],a[208:211],v[172:175],v[140:143]
EX v73,v73
MM v[92:95],a[212:215],v[188:191],v[92:95]
TR a[160:161],v232 offset:1024
CV v72,v72,v73
MM v[92:95],a[216:219],v[204:207],v[92:95]
EX v74,v74
MM v[92:95],a[220:223],v[220:223],v[92:95]
EX v75,v75
MM v[88:91],a[204:207],v[220:223],v[140:143]
TR a[162:163],v232 offset:5120
CV v73,v74,v75
MM v[88:91],a[200:203],v[204:207],v[88:91]
TR a[164:165],v232 offset:9216
TR a[166:167],v232 offset:13312
MM v[88:91],a[196:199],v[188:191],v[88:91]
EX v76,v76
MM v[88:91],a[192:195],v[172:175],v[88:91]
EX v77,v77
MM v[96:99],a[224:227],v[160:163],v[128:131]
TR a[168:169],v233 offset:1024
CV v74,v76,v77
MM v[96:99],a[228:231],v[176:179],v[96:99]
EX v78,v78
MM v[96:99],a[232:235],v[192:195],v[96:99]
TR a[170:171],v233 offset:5120
TR a[172:173],v233 offset:9216
MM v[96:99],a[236:239],v[208:211],v[96:99]
EX v79,v79
MM v[100:103],a[252:255],v[208:211],v[128:131]
TR a[174:175],v233 offset:13312
CV v75,v78,v79
MM v[100:103],a[248:251],v[192:195],v[100:103]
EX v80,v80
MM v[100:103],a[244:247],v[176:179],v[100:103]
EX v81,v81
MM v[100:103],a[240:243],v[160:163],v[100:103]
TR a[176:177],v232 offset:1536
CV v80,v80,v81
MM v[108:111],a[240:243],v[164:167],v[132:135]
EX v82,v82
MM v[108:111],a[244:247],v[180:183],v[108:111]
TR a[178:179],v232 offset:5632
TR a[180:181],v232 offset:9728
MM v[108:111],a[248:251],v[196:199],v[108:111]
EX v83,v83
MM v[108:111],a[252:255],v[212:215],v[108:111]
TR a[182:183],v232 offset:13824
CV v81,v82,v83
MM v[104:107],a[236:239],v[212:215],v[132:135]
EX v84,v84
MM v[104:107],a[232:235],v[196:199],v[104:107]
EX v85,v85
MM v[104:107],a[228:231],v[180:183],v[104:107]
TR a[184:185],v233 offset:1536
CV v82,v84,v85
MM v[104:107],a[224:227],v[164:167],v[104:107]
EX v86,v86
MM v[112:115],a[224:227],v[168:171],v[136:139]
TR a[186:187],v233 offset:5632
TR a[188:189],v233 offset:9728
MM v[112:115],a[228:231],v[184:187],v[112:115]
EX v87,v87
MM v[112:115],a[232:235],v[200:203],v[112:115]
TR a[190:191],v233 offset:13824
CV v83,v86,v87
MM v[112:115],a[236:239],v[216:219],v[112:115]
EX v88,v88
MM v[116:119],a[252:255],v[216:219],v[136:139]
EX v89,v89
MM v[116:119],a[248:251],v[200:203],v[116:119]
EX v90,v90
MM v[116:119],a[244:247],v[184:187],v[116:119]
EX v91,v91
MM v[116:119],a[240:243],v[168:171],v[116:119]
EX v92,v92
MM v[124:127],a[240:243],v[172:175],v[140:143]
EX v93,v93
MM v[124:127],a[244:247],v[188:191],v[124:127]
EX v94,v94
MM v[124:127],a[248:251],v[204:207],v[124:127]
EX v95,v95
MM v[124:127],a[252:255],v[220:223],v[124:127]
EX v96,v96
MM v[120:123],a[236:239],v[220:223],v[140:143]
EX v97,v97
MM v[120:123],a[232:235],v[204:207],v[120:123]
EX v98,v98
MM v[120:123],a[228:231],v[188:191],v[120:123]
EX v99,v99
MM v[120:123],a[224:227],v[172:175],v[120:123]
s_waitcnt vmcnt(16) lgkmcnt(0)
s_barrier
MM a[0:3],a[128:131],v[0:3],a[0:3]
s_add_u32 m0, s65, 0
CV v88,v88,v89
CV v89,v90,v91
MM a[0:3],a[132:135],v[4:7],a[0:3]
RD a[192:195],v224 offset:32768
CV v90,v92,v93
MM a[8:11],a[132:135],v[12:15],a[8:11]
RD a[196:199],v225 offset:32768
CV v91,v94,v95
MM a[8:11],a[128:131],v[8:11],a[8:11]
buffer_load_dwordx4 v228, s[8:11], s64 offen offset:0 lds
MM a[16:19],a[128:131],v[16:19],a[16:19]
RD a[200:203],v226 offset:32768
CV v68,v96,v97
MM a[16:19],a[132:135],v[20:23],a[16:19]
RD a[204:207],v227 offset:32768
CV v69,v98,v99
MM a[24:27],a[132:135],v[28:31],a[24:27]
)ASM"
R"ASM(
buffer_load_dwordx4 v229, s[8:11], s64 offen offset:1024 lds
MM a[24:27],a[128:131],v[24:27],a[24:27]
RD a[208:211],v224 offset:36864
RD a[212:215],v225 offset:36864
MM a[28:31],a[136:139],v[24:27],a[28:31]
buffer_load_dwordx4 v230, s[8:11], s64 offen offset:2048 lds
MM a[28:31],a[140:143],v[28:31],a[28:31]
EX v100,v100
MM a[20:23],a[140:143],v[20:23],a[20:23]
EX v101,v101
MM a[20:23],a[136:139],v[16:19],a[20:23]
RD a[216:219],v226 offset:36864
CV v70,v100,v101
MM a[12:15],a[136:139],v[8:11],a[12:15]
EX v102,v102
MM a[12:15],a[140:143],v[12:15],a[12:15]
RD a[220:223],v227 offset:36864
RD a[224:227],v224 offset:40960
MM a[4:7],a[140:143],v[4:7],a[4:7]
EX v103,v103
MM a[4:7],a[136:139],v[0:3],a[4:7]
RD a[228:231],v225 offset:40960
CV v71,v102,v103
MM a[32:35],a[144:147],v[0:3],a[32:35]
EX v104,v104
MM a[32:35],a[148:151],v[4:7],a[32:35]
buffer_load_dwordx4 v231, s[8:11], s64 offen offset:3072 lds
MM a[40:43],a[148:151],v[12:15],a[40:43]
s_add_u32 m0, s65, 65536
EX v105,v105
MM a[40:43],a[144:147],v[8:11],a[40:43]
RD a[232:235],v226 offset:40960
CV v76,v104,v105
MM a[48:51],a[144:147],v[16:19],a[48:51]
EX v106,v106
MM a[48:51],a[148:151],v[20:23],a[48:51]
EX v107,v107
MM a[56:59],a[148:151],v[28:31],a[56:59]
RD a[236:239],v227 offset:40960
CV v77,v106,v107
MM a[56:59],a[144:147],v[24:27],a[56:59]
EX v108,v108
MM a[60:63],a[152:155],v[24:27],a[60:63]
buffer_load_dwordx4 v235, s[12:15], s81 offen offset:0 lds
MM a[60:63],a[156:159],v[28:31],a[60:63]
EX v109,v109
MM a[52:55],a[156:159],v[20:23],a[52:55]
RD a[240:243],v224 offset:45056
CV v78,v108,v109
MM a[52:55],a[152:155],v[16:19],a[52:55]
EX v110,v110
MM a[44:47],a[152:155],v[8:11],a[44:47]
EX v111,v111
MM a[44:47],a[156:159],v[12:15],a[44:47]
RD a[244:247],v225 offset:45056
CV v79,v110,v111
MM a[36:39],a[156:159],v[4:7],a[36:39]
buffer_load_dwordx4 v235, s[12:15], s64 offen offset:1024 lds
MM a[36:39],a[152:155],v[0:3],a[36:39]
EX v112,v112
MM a[64:67],a[160:163],v[0:3],a[64:67]
EX v113,v113
MM a[64:67],a[164:167],v[4:7],a[64:67]
RD a[248:251],v226 offset:45056
CV v84,v112,v113
MM a[72:75],a[164:167],v[12:15],a[72:75]
EX v114,v114
MM a[72:75],a[160:163],v[8:11],a[72:75]
EX v115,v115
MM a[80:83],a[160:163],v[16:19],a[80:83]
RD a[252:255],v227 offset:45056
CV v85,v114,v115
MM a[80:83],a[164:167],v[20:23],a[80:83]
buffer_load_dwordx4 v235, s[12:15], s81 offen offset:2048 lds
MM a[88:91],a[164:167],v[28:31],a[88:91]
EX v116,v116
MM a[88:91],a[160:163],v[24:27],a[88:91]
EX v117,v117
MM a[92:95],a[168:171],v[24:27],a[92:95]
EX v118,v118
MM a[92:95],a[172:175],v[28:31],a[92:95]
buffer_load_dwordx4 v235, s[12:15], s64 offen offset:3072 lds
MM a[84:87],a[172:175],v[20:23],a[84:87]
EX v119,v119
MM a[84:87],a[168:171],v[16:19],a[84:87]
EX v120,v120
MM a[76:79],a[168:171],v[8:11],a[76:79]
EX v121,v121
MM a[76:79],a[172:175],v[12:15],a[76:79]
EX v122,v122
MM a[68:71],a[172:175],v[4:7],a[68:71]
EX v123,v123
MM a[68:71],a[168:171],v[0:3],a[68:71]
EX v124,v124
MM a[96:99],a[176:179],v[0:3],a[96:99]
EX v125,v125
MM a[96:99],a[180:183],v[4:7],a[96:99]
EX v126,v126
MM a[104:107],a[180:183],v[12:15],a[104:107]
EX v127,v127
MM a[104:107],a[176:179],v[8:11],a[104:107]
CV v86,v116,v117
CV v87,v118,v119
MM a[112:115],a[176:179],v[16:19],a[112:115]
CV v92,v120,v121
CV v93,v122,v123
MM a[112:115],a[180:183],v[20:23],a[112:115]
CV v94,v124,v125
CV v95,v126,v127
MM a[120:123],a[180:183],v[28:31],a[120:123]
MM a[120:123],a[176:179],v[24:27],a[120:123]
MM a[124:127],a[184:187],v[24:27],a[124:127]
v_add_f32 v244, v144, v145
v_add_f32 v245, v148, v149
MM a[124:127],a[188:191],v[28:31],a[124:127]
v_add_f32 v246, v152, v153
v_add_f32 v247, v156, v157
MM a[116:119],a[188:191],v[20:23],a[116:119]
MM a[116:119],a[184:187],v[16:19],a[116:119]
MM a[108:111],a[184:187],v[8:11],a[108:111]
MM a[108:111],a[188:191],v[12:15],a[108:111]
MM a[100:103],a[188:191],v[4:7],a[100:103]
MM a[100:103],a[184:187],v[0:3],a[100:103]
v_smfmac_f32_16x16x64_bf16 v[144:147], v[236:239], v[64:71], v234
v_smfmac_f32_16x16x64_bf16 v[148:151], v[236:239], v[72:79], v234
v_smfmac_f32_16x16x64_bf16 v[152:155], v[236:239], v[80:87], v234
v_smfmac_f32_16x16x64_bf16 v[156:159], v[236:239], v[88:95], v234
s_waitcnt lgkmcnt(0)
MM v[0:3],a[192:195],v[160:163],v[128:131]
s_add_u32 s64, s64, 16384
TR a[128:129],v232 offset:16384
s_add_u32 s81, s64, 896
MM v[0:3],a[196:199],v[176:179],v[0:3]
TR a[130:131],v232 offset:20480
v_max3_f32 v249, v144, v145, v148
MM v[0:3],a[200:203],v[192:195],v[0:3]
TR a[132:133],v232 offset:24576
v_max3_f32 v249, v249, v149, v152
MM v[0:3],a[204:207],v[208:211],v[0:3]
TR a[134:135],v232 offset:28672
v_max3_f32 v249, v249, v153, v156
MM v[4:7],a[220:223],v[208:211],v[128:131]
TR a[136:137],v233 offset:16384
v_max_f32 v249, v249, v157
MM v[4:7],a[216:219],v[192:195],v[4:7]
TR a[138:139],v233 offset:20480
v_cmp_lt_f32 vcc, s71, v249
MM v[4:7],a[212:215],v[176:179],v[4:7]
TR a[140:141],v233 offset:24576
TR a[142:143],v233 offset:28672
MM v[4:7],a[208:211],v[160:163],v[4:7]
s_cbranch_vccnz LSITE25_%=
LBACK25_%=:
MM v[12:15],a[208:211],v[164:167],v[132:135]
s_waitcnt vmcnt(9)
EX v0,v0
MM v[12:15],a[212:215],v[180:183],v[12:15]
s_mul_i32 s76, s46, 96
EX v1,v1
MM v[12:15],a[216:219],v[196:199],v[12:15]
s_add_u32 s76, s76, 69632
v_lshrrev_b32 v253, 2, v248
v_min_u32 v253, 92, v253
MM v[12:15],a[220:223],v[212:215],v[12:15]
buffer_load_dword v252, v253, s[20:23], s76 offen offset:0 sc1
MM v[8:11],a[204:207],v[212:215],v[132:135]
EX v2,v2
MM v[8:11],a[200:203],v[196:199],v[8:11]
TR a[144:145],v232 offset:16896
CV v0,v0,v1
MM v[8:11],a[196:199],v[180:183],v[8:11]
EX v3,v3
MM v[8:11],a[192:195],v[164:167],v[8:11]
TR a[146:147],v232 offset:20992
CV v1,v2,v3
MM v[16:19],a[192:195],v[168:171],v[136:139]
EX v4,v4
MM v[16:19],a[196:199],v[184:187],v[16:19]
TR a[148:149],v232 offset:25088
TR a[150:151],v232 offset:29184
MM v[16:19],a[200:203],v[200:203],v[16:19]
EX v5,v5
MM v[16:19],a[204:207],v[216:219],v[16:19]
TR a[152:153],v233 offset:16896
CV v2,v4,v5
MM v[20:23],a[220:223],v[216:219],v[136:139]
EX v6,v6
MM v[20:23],a[216:219],v[200:203],v[20:23]
EX v7,v7
MM v[20:23],a[212:215],v[184:187],v[20:23]
TR a[154:155],v233 offset:20992
CV v3,v6,v7
MM v[20:23],a[208:211],v[168:171],v[20:23]
EX v8,v8
MM v[28:31],a[208:211],v[172:175],v[140:143]
TR a[156:157],v233 offset:25088
TR a[158:159],v233 offset:29184
MM v[28:31],a[212:215],v[188:191],v[28:31]
EX v9,v9
MM v[28:31],a[216:219],v[204:207],v[28:31]
TR a[160:161],v232 offset:17408
CV v8,v8,v9
MM v[28:31],a[220:223],v[220:223],v[28:31]
EX v10,v10
MM v[24:27],a[204:207],v[220:223],v[140:143]
EX v11,v11
MM v[24:27],a[200:203],v[204:207],v[24:27]
TR a[162:163],v232 offset:21504
CV v9,v10,v11
MM v[24:27],a[196:199],v[188:191],v[24:27]
EX v12,v12
MM v[24:27],a[192:195],v[172:175],v[24:27]
TR a[164:165],v232 offset:25600
TR a[166:167],v232 offset:29696
MM v[32:35],a[224:227],v[160:163],v[128:131]
EX v13,v13
MM v[32:35],a[228:231],v[176:179],v[32:35]
TR a[168:169],v233 offset:17408
CV v10,v12,v13
MM v[32:35],a[232:235],v[192:195],v[32:35]
EX v14,v14
MM v[32:35],a[236:239],v[208:211],v[32:35]
EX v15,v15
MM v[36:39],a[252:255],v[208:211],v[128:131]
TR a[170:171],v233 offset:21504
CV v11,v14,v15
MM v[36:39],a[248:251],v[192:195],v[36:39]
EX v16,v16
MM v[36:39],a[244:247],v[176:179],v[36:39]
TR a[172:173],v233 offset:25600
TR a[174:175],v233 offset:29696
MM v[36:39],a[240:243],v[160:163],v[36:39]
EX v17,v17
MM v[44:47],a[240:243],v[164:167],v[132:135]
TR a[176:177],v232 offset:17920
CV v16,v16,v17
MM v[44:47],a[244:247],v[180:183],v[44:47]
EX v18,v18
MM v[44:47],a[248:251],v[196:199],v[44:47]
TR a[178:179],v232 offset:22016
TR a[180:181],v232 offset:26112
MM v[44:47],a[252:255],v[212:215],v[44:47]
EX v19,v19
MM v[40:43],a[236:239],v[212:215],v[132:135]
TR a[182:183],v232 offset:30208
CV v17,v18,v19
MM v[40:43],a[232:235],v[196:199],v[40:43]
EX v20,v20
MM v[40:43],a[228:231],v[180:183],v[40:43]
EX v21,v21
MM v[40:43],a[224:227],v[164:167],v[40:43]
TR a[184:185],v233 offset:17920
CV v18,v20,v21
MM v[48:51],a[224:227],v[168:171],v[136:139]
EX v22,v22
MM v[48:51],a[228:231],v[184:187],v[48:51]
TR a[186:187],v233 offset:22016
TR a[188:189],v233 offset:26112
MM v[48:51],a[232:235],v[200:203],v[48:51]
EX v23,v23
MM v[48:51],a[236:239],v[216:219],v[48:51]
TR a[190:191],v233 offset:30208
CV v19,v22,v23
MM v[52:55],a[252:255],v[216:219],v[136:139]
EX v24,v24
MM v[52:55],a[248:251],v[200:203],v[52:55]
EX v25,v25
MM v[52:55],a[244:247],v[184:187],v[52:55]
EX v26,v26
MM v[52:55],a[240:243],v[168:171],v[52:55]
EX v27,v27
MM v[60:63],a[240:243],v[172:175],v[140:143]
EX v28,v28
MM v[60:63],a[244:247],v[188:191],v[60:63]
EX v29,v29
MM v[60:63],a[248:251],v[204:207],v[60:63]
EX v30,v30
MM v[60:63],a[252:255],v[220:223],v[60:63]
EX v31,v31
MM v[56:59],a[236:239],v[220:223],v[140:143]
EX v32,v32
MM v[56:59],a[232:235],v[204:207],v[56:59]
EX v33,v33
MM v[56:59],a[228:231],v[188:191],v[56:59]
EX v34,v34
MM v[56:59],a[224:227],v[172:175],v[56:59]
s_waitcnt lgkmcnt(0)
s_barrier
MM a[0:3],a[128:131],v[64:67],a[0:3]
s_add_u32 m0, s65, 16384
CV v24,v24,v25
CV v25,v26,v27
MM a[0:3],a[132:135],v[68:71],a[0:3]
RD a[192:195],v224 offset:49152
CV v26,v28,v29
MM a[8:11],a[132:135],v[76:79],a[8:11]
RD a[196:199],v225 offset:49152
CV v27,v30,v31
MM a[8:11],a[128:131],v[72:75],a[8:11]
buffer_load_dwordx4 v228, s[8:11], s64 offen offset:0 lds
MM a[16:19],a[128:131],v[80:83],a[16:19]
RD a[200:203],v226 offset:49152
CV v4,v32,v33
MM a[16:19],a[132:135],v[84:87],a[16:19]
buffer_load_dwordx4 v229, s[8:11], s64 offen offset:1024 lds
MM a[24:27],a[132:135],v[92:95],a[24:27]
RD a[204:207],v227 offset:49152
RD a[208:211],v224 offset:53248
MM a[24:27],a[128:131],v[88:91],a[24:27]
EX v35,v35
MM a[28:31],a[136:139],v[88:91],a[28:31]
RD a[212:215],v225 offset:53248
CV v5,v34,v35
MM a[28:31],a[140:143],v[92:95],a[28:31]
EX v36,v36
MM a[20:23],a[140:143],v[84:87],a[20:23]
buffer_load_dwordx4 v230, s[8:11], s64 offen offset:2048 lds
MM a[20:23],a[136:139],v[80:83],a[20:23]
EX v37,v37
MM a[12:15],a[136:139],v[72:75],a[12:15]
RD a[216:219],v226 offset:53248
CV v6,v36,v37
MM a[12:15],a[140:143],v[76:79],a[12:15]
EX v38,v38
MM a[4:7],a[140:143],v[68:71],a[4:7]
EX v39,v39
MM a[4:7],a[136:139],v[64:67],a[4:7]
RD a[220:223],v227 offset:53248
CV v7,v38,v39
MM a[32:35],a[144:147],v[64:67],a[32:35]
EX v40,v40
MM a[32:35],a[148:151],v[68:71],a[32:35]
buffer_load_dwordx4 v231, s[8:11], s64 offen offset:3072 lds
MM a[40:43],a[148:151],v[76:79],a[40:43]
s_add_u32 m0, s65, 81920
EX v41,v41
MM a[40:43],a[144:147],v[72:75],a[40:43]
RD a[224:227],v224 offset:57344
CV v12,v40,v41
MM a[48:51],a[144:147],v[80:83],a[48:51]
EX v42,v42
MM a[48:51],a[148:151],v[84:87],a[48:51]
EX v43,v43
MM a[56:59],a[148:151],v[92:95],a[56:59]
RD a[228:231],v225 offset:57344
CV v13,v42,v43
MM a[56:59],a[144:147],v[88:91],a[56:59]
EX v44,v44
MM a[60:63],a[152:155],v[88:91],a[60:63]
buffer_load_dwordx4 v235, s[12:15], s81 offen offset:0 lds
MM a[60:63],a[156:159],v[92:95],a[60:63]
EX v45,v45
MM a[52:55],a[156:159],v[84:87],a[52:55]
RD a[232:235],v226 offset:57344
CV v14,v44,v45
MM a[52:55],a[152:155],v[80:83],a[52:55]
EX v46,v46
MM a[44:47],a[152:155],v[72:75],a[44:47]
EX v47,v47
MM a[44:47],a[156:159],v[76:79],a[44:47]
RD a[236:239],v227 offset:57344
CV v15,v46,v47
MM a[36:39],a[156:159],v[68:71],a[36:39]
buffer_load_dwordx4 v235, s[12:15], s64 offen offset:1024 lds
MM a[36:39],a[152:155],v[64:67],a[36:39]
EX v48,v48
MM a[64:67],a[160:163],v[64:67],a[64:67]
EX v49,v49
MM a[64:67],a[164:167],v[68:71],a[64:67]
RD a[240:243],v224 offset:61440
CV v20,v48,v49
MM a[72:75],a[164:167],v[76:79],a[72:75]
EX v50,v50
MM a[72:75],a[160:163],v[72:75],a[72:75]
RD a[244:247],v225 offset:61440
RD a[248:251],v226 offset:61440
MM a[80:83],a[160:163],v[80:83],a[80:83]
EX v51,v51
MM a[80:83],a[164:167],v[84:87],a[80:83]
LPG19_%=:
s_cbranch_execz LTR3_%=
RD a[252:255],v227 offset:61440
CV v21,v50,v51
MM a[88:91],a[164:167],v[92:95],a[88:91]
EX v52,v52
MM a[88:91],a[160:163],v[88:91],a[88:91]
buffer_load_dwordx4 v235, s[12:15], s81 offen offset:2048 lds
MM a[92:95],a[168:171],v[88:91],a[92:95]
)ASM"
R"ASM(
EX v53,v53
MM a[92:95],a[172:175],v[92:95],a[92:95]
EX v54,v54
MM a[84:87],a[172:175],v[84:87],a[84:87]
buffer_load_dwordx4 v235, s[12:15], s64 offen offset:3072 lds
MM a[84:87],a[168:171],v[80:83],a[84:87]
EX v55,v55
MM a[76:79],a[168:171],v[72:75],a[76:79]
EX v56,v56
MM a[76:79],a[172:175],v[76:79],a[76:79]
EX v57,v57
MM a[68:71],a[172:175],v[68:71],a[68:71]
EX v58,v58
MM a[68:71],a[168:171],v[64:67],a[68:71]
EX v59,v59
MM a[96:99],a[176:179],v[64:67],a[96:99]
EX v60,v60
MM a[96:99],a[180:183],v[68:71],a[96:99]
EX v61,v61
MM a[104:107],a[180:183],v[76:79],a[104:107]
EX v62,v62
MM a[104:107],a[176:179],v[72:75],a[104:107]
EX v63,v63
MM a[112:115],a[176:179],v[80:83],a[112:115]
CV v22,v52,v53
CV v23,v54,v55
MM a[112:115],a[180:183],v[84:87],a[112:115]
CV v28,v56,v57
CV v29,v58,v59
MM a[120:123],a[180:183],v[92:95],a[120:123]
s_waitcnt vmcnt(8)
v_cmp_ne_u32 vcc, s69, v252
s_cbranch_vccnz LRFH_%=
LRFHb_%=:
MM a[120:123],a[176:179],v[88:91],a[120:123]
v_bfe_u32 v253, v248, 4, 3
v_mul_u32_u24 v253, 3456, v253
MM a[124:127],a[184:187],v[88:91],a[124:127]
v_add_f32 v244, v144, v145
v_add_f32 v245, v148, v149
MM a[124:127],a[188:191],v[92:95],a[124:127]
v_add_f32 v246, v152, v153
v_add_f32 v247, v156, v157
v_lshrrev_b32 v252, 7, v248
MM a[116:119],a[188:191],v[84:87],a[116:119]
v_lshl_add_u32 v253, v252, 4, v253
CV v30,v60,v61
MM a[116:119],a[184:187],v[80:83],a[116:119]
s_min_u32 s90, %[swave], 2
s_mul_i32 s76, s46, 384
CV v31,v62,v63
MM a[108:111],a[184:187],v[72:75],a[108:111]
s_lshl_b32 s91, s90, 7
s_add_u32 s76, s76, s91
s_add_u32 s76, s76, 21626880
MM a[108:111],a[188:191],v[76:79],a[108:111]
s_lshl_b32 s90, s90, 10
s_add_u32 s90, s90, %[slds]
MM a[100:103],a[188:191],v[68:71],a[100:103]
s_add_u32 m0, s90, 131072
s_nop 0
buffer_load_dwordx4 v253, s[20:23], s76 offen offset:0 sc1 lds
MM a[100:103],a[184:187],v[64:67],a[100:103]
v_smfmac_f32_16x16x64_bf16 v[144:147], v[236:239], v[0:7], v234
v_smfmac_f32_16x16x64_bf16 v[148:151], v[236:239], v[8:15], v234
v_smfmac_f32_16x16x64_bf16 v[152:155], v[236:239], v[16:23], v234
v_smfmac_f32_16x16x64_bf16 v[156:159], v[236:239], v[24:31], v234
s_waitcnt lgkmcnt(0)
MM v[64:67],a[192:195],v[160:163],v[128:131]
s_add_u32 s64, s64, 16384
TR a[128:129],v232 offset:32768
s_add_u32 s81, s64, 896
MM v[64:67],a[196:199],v[176:179],v[64:67]
TR a[130:131],v232 offset:36864
v_max3_f32 v249, v144, v145, v148
MM v[64:67],a[200:203],v[192:195],v[64:67]
TR a[132:133],v232 offset:40960
v_max3_f32 v249, v249, v149, v152
MM v[64:67],a[204:207],v[208:211],v[64:67]
TR a[134:135],v232 offset:45056
v_max3_f32 v249, v249, v153, v156
MM v[68:71],a[220:223],v[208:211],v[128:131]
TR a[136:137],v233 offset:32768
v_max_f32 v249, v249, v157
MM v[68:71],a[216:219],v[192:195],v[68:71]
TR a[138:139],v233 offset:36864
v_cmp_lt_f32 vcc, s71, v249
MM v[68:71],a[212:215],v[176:179],v[68:71]
TR a[140:141],v233 offset:40960
TR a[142:143],v233 offset:45056
MM v[68:71],a[208:211],v[160:163],v[68:71]
s_cbranch_vccnz LSITE26_%=
LBACK26_%=:
MM v[76:79],a[208:211],v[164:167],v[132:135]
EX v64,v64
MM v[76:79],a[212:215],v[180:183],v[76:79]
EX v65,v65
MM v[76:79],a[216:219],v[196:199],v[76:79]
TR a[144:145],v232 offset:33280
CV v64,v64,v65
MM v[76:79],a[220:223],v[212:215],v[76:79]
EX v66,v66
MM v[72:75],a[204:207],v[212:215],v[132:135]
EX v67,v67
MM v[72:75],a[200:203],v[196:199],v[72:75]
TR a[146:147],v232 offset:37376
CV v65,v66,v67
MM v[72:75],a[196:199],v[180:183],v[72:75]
EX v68,v68
MM v[72:75],a[192:195],v[164:167],v[72:75]
TR a[148:149],v232 offset:41472
TR a[150:151],v232 offset:45568
MM v[80:83],a[192:195],v[168:171],v[136:139]
EX v69,v69
MM v[80:83],a[196:199],v[184:187],v[80:83]
TR a[152:153],v233 offset:33280
CV v66,v68,v69
MM v[80:83],a[200:203],v[200:203],v[80:83]
EX v70,v70
MM v[80:83],a[204:207],v[216:219],v[80:83]
TR a[154:155],v233 offset:37376
TR a[156:157],v233 offset:41472
MM v[84:87],a[220:223],v[216:219],v[136:139]
EX v71,v71
MM v[84:87],a[216:219],v[200:203],v[84:87]
TR a[158:159],v233 offset:45568
CV v67,v70,v71
MM v[84:87],a[212:215],v[184:187],v[84:87]
EX v72,v72
MM v[84:87],a[208:211],v[168:171],v[84:87]
EX v73,v73
MM v[92:95],a[208:211],v[172:175],v[140:143]
TR a[160:161],v232 offset:33792
CV v72,v72,v73
MM v[92:95],a[212:215],v[188:191],v[92:95]
EX v74,v74
MM v[92:95],a[216:219],v[204:207],v[92:95]
TR a[162:163],v232 offset:37888
TR a[164:165],v232 offset:41984
MM v[92:95],a[220:223],v[220:223],v[92:95]
EX v75,v75
MM v[88:91],a[204:207],v[220:223],v[140:143]
TR a[166:167],v232 offset:46080
CV v73,v74,v75
MM v[88:91],a[200:203],v[204:207],v[88:91]
EX v76,v76
MM v[88:91],a[196:199],v[188:191],v[88:91]
EX v77,v77
MM v[88:91],a[192:195],v[172:175],v[88:91]
TR a[168:169],v233 offset:33792
CV v74,v76,v77
MM v[96:99],a[224:227],v[160:163],v[128:131]
EX v78,v78
MM v[96:99],a[228:231],v[176:179],v[96:99]
TR a[170:171],v233 offset:37888
TR a[172:173],v233 offset:41984
MM v[96:99],a[232:235],v[192:195],v[96:99]
EX v79,v79
MM v[96:99],a[236:239],v[208:211],v[96:99]
TR a[174:175],v233 offset:46080
CV v75,v78,v79
MM v[100:103],a[252:255],v[208:211],v[128:131]
EX v80,v80
MM v[100:103],a[248:251],v[192:195],v[100:103]
EX v81,v81
MM v[100:103],a[244:247],v[176:179],v[100:103]
TR a[176:177],v232 offset:34304
CV v80,v80,v81
MM v[100:103],a[240:243],v[160:163],v[100:103]
EX v82,v82
MM v[108:111],a[240:243],v[164:167],v[132:135]
TR a[178:179],v232 offset:38400
TR a[180:181],v232 offset:42496
MM v[108:111],a[244:247],v[180:183],v[108:111]
EX v83,v83
MM v[108:111],a[248:251],v[196:199],v[108:111]
TR a[182:183],v232 offset:46592
CV v81,v82,v83
MM v[108:111],a[252:255],v[212:215],v[108:111]
EX v84,v84
MM v[104:107],a[236:239],v[212:215],v[132:135]
TR a[184:185],v233 offset:34304
TR a[186:187],v233 offset:38400
MM v[104:107],a[232:235],v[196:199],v[104:107]
EX v85,v85
MM v[104:107],a[228:231],v[180:183],v[104:107]
TR a[188:189],v233 offset:42496
CV v82,v84,v85
MM v[104:107],a[224:227],v[164:167],v[104:107]
EX v86,v86
MM v[112:115],a[224:227],v[168:171],v[136:139]
EX v87,v87
MM v[112:115],a[228:231],v[184:187],v[112:115]
TR a[190:191],v233 offset:46592
CV v83,v86,v87
MM v[112:115],a[232:235],v[200:203],v[112:115]
EX v88,v88
MM v[112:115],a[236:239],v[216:219],v[112:115]
EX v89,v89
MM v[116:119],a[252:255],v[216:219],v[136:139]
EX v90,v90
MM v[116:119],a[248:251],v[200:203],v[116:119]
EX v91,v91
MM v[116:119],a[244:247],v[184:187],v[116:119]
EX v92,v92
MM v[116:119],a[240:243],v[168:171],v[116:119]
EX v93,v93
MM v[124:127],a[240:243],v[172:175],v[140:143]
EX v94,v94
MM v[124:127],a[244:247],v[188:191],v[124:127]
EX v95,v95
MM v[124:127],a[248:251],v[204:207],v[124:127]
EX v96,v96
MM v[124:127],a[252:255],v[220:223],v[124:127]
EX v97,v97
MM v[120:123],a[236:239],v[220:223],v[140:143]
EX v98,v98
MM v[120:123],a[232:235],v[204:207],v[120:123]
EX v99,v99
MM v[120:123],a[228:231],v[188:191],v[120:123]
s_getpc_b64 s[98:99]
LTA4_%=:
s_min_u32 s73, %[swave], 1
s_lshl_b32 s73, s73, 12
s_add_u32 s73, s73, LPG21_%=-LTA4_%=
s_add_u32 s98, s98, s73
s_addc_u32 s99, s99, 0
s_mov_b64 exec, 0
s_setpc_b64 s[98:99]
LTR4_%=:
s_mov_b64 exec, -1
CV v88,v88,v89
MM v[120:123],a[224:227],v[172:175],v[120:123]
s_waitcnt lgkmcnt(0)
s_barrier
MM a[0:3],a[128:131],v[0:3],a[0:3]
s_add_u32 m0, s65, 32768
CV v89,v90,v91
CV v90,v92,v93
MM a[0:3],a[132:135],v[4:7],a[0:3]
RD a[192:195],v224 offset:0
CV v91,v94,v95
MM a[8:11],a[132:135],v[12:15],a[8:11]
buffer_load_dwordx4 v228, s[8:11], s64 offen offset:0 lds
MM a[8:11],a[128:131],v[8:11],a[8:11]
RD a[196:199],v225 offset:0
CV v68,v96,v97
MM a[16:19],a[128:131],v[16:19],a[16:19]
RD a[200:203],v226 offset:0
CV v69,v98,v99
MM a[16:19],a[132:135],v[20:23],a[16:19]
buffer_load_dwordx4 v229, s[8:11], s64 offen offset:1024 lds
MM a[24:27],a[132:135],v[28:31],a[24:27]
RD a[204:207],v227 offset:0
RD a[208:211],v224 offset:4096
MM a[24:27],a[128:131],v[24:27],a[24:27]
RD a[212:215],v225 offset:4096
RD a[216:219],v226 offset:4096
MM a[28:31],a[136:139],v[24:27],a[28:31]
buffer_load_dwordx4 v230, s[8:11], s64 offen offset:2048 lds
MM a[28:31],a[140:143],v[28:31],a[28:31]
EX v100,v100
MM a[20:23],a[140:143],v[20:23],a[20:23]
EX v101,v101
MM a[20:23],a[136:139],v[16:19],a[20:23]
RD a[220:223],v227 offset:4096
CV v70,v100,v101
MM a[12:15],a[136:139],v[8:11],a[12:15]
EX v102,v102
MM a[12:15],a[140:143],v[12:15],a[12:15]
buffer_load_dwordx4 v231, s[8:11], s64 offen offset:3072 lds
MM a[4:7],a[140:143],v[4:7],a[4:7]
s_add_u32 m0, s65, 98304
EX v103,v103
MM a[4:7],a[136:139],v[0:3],a[4:7]
RD a[224:227],v224 offset:8192
CV v71,v102,v103
MM a[32:35],a[144:147],v[0:3],a[32:35]
EX v104,v104
MM a[32:35],a[148:151],v[4:7],a[32:35]
EX v105,v105
MM a[40:43],a[148:151],v[12:15],a[40:43]
RD a[228:231],v225 offset:8192
CV v76,v104,v105
MM a[40:43],a[144:147],v[8:11],a[40:43]
EX v106,v106
MM a[48:51],a[144:147],v[16:19],a[48:51]
buffer_load_dwordx4 v235, s[12:15], s81 offen offset:0 lds
MM a[48:51],a[148:151],v[20:23],a[48:51]
EX v107,v107
MM a[56:59],a[148:151],v[28:31],a[56:59]
RD a[232:235],v226 offset:8192
CV v77,v106,v107
MM a[56:59],a[144:147],v[24:27],a[56:59]
EX v108,v108
MM a[60:63],a[152:155],v[24:27],a[60:63]
EX v109,v109
MM a[60:63],a[156:159],v[28:31],a[60:63]
RD a[236:239],v227 offset:8192
CV v78,v108,v109
MM a[52:55],a[156:159],v[20:23],a[52:55]
EX v110,v110
MM a[52:55],a[152:155],v[16:19],a[52:55]
buffer_load_dwordx4 v235, s[12:15], s64 offen offset:1024 lds
MM a[44:47],a[152:155],v[8:11],a[44:47]
EX v111,v111
MM a[44:47],a[156:159],v[12:15],a[44:47]
RD a[240:243],v224 offset:12288
CV v79,v110,v111
MM a[36:39],a[156:159],v[4:7],a[36:39]
EX v112,v112
MM a[36:39],a[152:155],v[0:3],a[36:39]
EX v113,v113
MM a[64:67],a[160:163],v[0:3],a[64:67]
RD a[244:247],v225 offset:12288
CV v84,v112,v113
MM a[64:67],a[164:167],v[4:7],a[64:67]
EX v114,v114
MM a[72:75],a[164:167],v[12:15],a[72:75]
buffer_load_dwordx4 v235, s[12:15], s81 offen offset:2048 lds
MM a[72:75],a[160:163],v[8:11],a[72:75]
EX v115,v115
MM a[80:83],a[160:163],v[16:19],a[80:83]
RD a[248:251],v226 offset:12288
CV v85,v114,v115
MM a[80:83],a[164:167],v[20:23],a[80:83]
EX v116,v116
MM a[88:91],a[164:167],v[28:31],a[88:91]
EX v117,v117
MM a[88:91],a[160:163],v[24:27],a[88:91]
RD a[252:255],v227 offset:12288
CV v86,v116,v117
MM a[92:95],a[168:171],v[24:27],a[92:95]
EX v118,v118
MM a[92:95],a[172:175],v[28:31],a[92:95]
buffer_load_dwordx4 v235, s[12:15], s64 offen offset:3072 lds
MM a[84:87],a[172:175],v[20:23],a[84:87]
EX v119,v119
MM a[84:87],a[168:171],v[16:19],a[84:87]
CV v87,v118,v119
MM a[76:79],a[168:171],v[8:11],a[76:79]
EX v120,v120
MM a[76:79],a[172:175],v[12:15],a[76:79]
EX v121,v121
MM a[68:71],a[172:175],v[4:7],a[68:71]
EX v122,v122
MM a[68:71],a[168:171],v[0:3],a[68:71]
EX v123,v123
MM a[96:99],a[176:179],v[0:3],a[96:99]
EX v124,v124
MM a[96:99],a[180:183],v[4:7],a[96:99]
EX v125,v125
MM a[104:107],a[180:183],v[12:15],a[104:107]
EX v126,v126
MM a[104:107],a[176:179],v[8:11],a[104:107]
EX v127,v127
MM a[112:115],a[176:179],v[16:19],a[112:115]
CV v92,v120,v121
CV v93,v122,v123
MM a[112:115],a[180:183],v[20:23],a[112:115]
CV v94,v124,v125
CV v95,v126,v127
MM a[120:123],a[180:183],v[28:31],a[120:123]
MM a[120:123],a[176:179],v[24:27],a[120:123]
MM a[124:127],a[184:187],v[24:27],a[124:127]
v_add_f32 v244, v144, v145
v_add_f32 v245, v148, v149
MM a[124:127],a[188:191],v[28:31],a[124:127]
v_add_f32 v246, v152, v153
v_add_f32 v247, v156, v157
MM a[116:119],a[188:191],v[20:23],a[116:119]
MM a[116:119],a[184:187],v[16:19],a[116:119]
MM a[108:111],a[184:187],v[8:11],a[108:111]
MM a[108:111],a[188:191],v[12:15],a[108:111]
MM a[100:103],a[188:191],v[4:7],a[100:103]
MM a[100:103],a[184:187],v[0:3],a[100:103]
v_smfmac_f32_16x16x64_bf16 v[144:147], v[236:239], v[64:71], v234
v_smfmac_f32_16x16x64_bf16 v[148:151], v[236:239], v[72:79], v234
v_smfmac_f32_16x16x64_bf16 v[152:155], v[236:239], v[80:87], v234
v_smfmac_f32_16x16x64_bf16 v[156:159], v[236:239], v[88:95], v234
s_waitcnt lgkmcnt(0)
MM v[0:3],a[192:195],v[160:163],v[128:131]
s_add_u32 s64, s64, 16384
TR a[128:129],v232 offset:49152
s_add_u32 s81, s64, 896
MM v[0:3],a[196:199],v[176:179],v[0:3]
)ASM"
R"ASM(
TR a[130:131],v232 offset:53248
v_max3_f32 v249, v144, v145, v148
MM v[0:3],a[200:203],v[192:195],v[0:3]
TR a[132:133],v232 offset:57344
v_max3_f32 v249, v249, v149, v152
MM v[0:3],a[204:207],v[208:211],v[0:3]
TR a[134:135],v232 offset:61440
v_max3_f32 v249, v249, v153, v156
MM v[4:7],a[220:223],v[208:211],v[128:131]
TR a[136:137],v233 offset:49152
v_max_f32 v249, v249, v157
MM v[4:7],a[216:219],v[192:195],v[4:7]
TR a[138:139],v233 offset:53248
v_cmp_lt_f32 vcc, s71, v249
MM v[4:7],a[212:215],v[176:179],v[4:7]
TR a[140:141],v233 offset:57344
TR a[142:143],v233 offset:61440
MM v[4:7],a[208:211],v[160:163],v[4:7]
s_cbranch_vccnz LSITE27_%=
LBACK27_%=:
MM v[12:15],a[208:211],v[164:167],v[132:135]
v_lshrrev_b32 v125, 8, v248
v_lshlrev_b32 v124, 10, v125
MM v[12:15],a[212:215],v[180:183],v[12:15]
v_bfe_u32 v126, v248, 6, 2
v_lshl_add_u32 v124, v126, 7, v124
MM v[12:15],a[216:219],v[196:199],v[12:15]
v_bfe_u32 v126, v248, 4, 2
v_lshl_add_u32 v124, v126, 3, v124
MM v[12:15],a[220:223],v[212:215],v[12:15]
s_lshl_b32 s91, %[swave], 5
EX v0,v0
MM v[8:11],a[204:207],v[212:215],v[132:135]
s_add_u32 s91, s91, %[slds]
EX v1,v1
MM v[8:11],a[200:203],v[196:199],v[8:11]
s_add_u32 s91, s91, 139264
v_add_u32 v124, s91, v124
v_and_b32 v126, 112, v248
MM v[8:11],a[196:199],v[180:183],v[8:11]
s_add_u32 s91, %[slds], 131072
v_lshl_add_u32 v126, v125, 7, v126
v_add_u32 v126, s91, v126
MM v[8:11],a[192:195],v[164:167],v[8:11]
v_bfe_u32 v127, v248, 4, 3
v_mul_u32_u24 v127, 2560, v127
MM v[16:19],a[192:195],v[168:171],v[136:139]
v_lshl_add_u32 v127, v125, 4, v127
CV v0,v0,v1
EX v2,v2
MM v[16:19],a[196:199],v[184:187],v[16:19]
s_lshl_b32 s90, %[swave], 6
EX v3,v3
MM v[16:19],a[200:203],v[200:203],v[16:19]
s_lshl_b32 s91, s47, 8
EX v4,v4
MM v[16:19],a[204:207],v[216:219],v[16:19]
s_add_u32 s90, s90, s91
TR a[144:145],v232 offset:49664
TR a[146:147],v232 offset:53760
MM v[20:23],a[220:223],v[216:219],v[136:139]
s_mul_i32 s91, s46, 17920
EX v5,v5
MM v[20:23],a[216:219],v[200:203],v[20:23]
s_add_u32 s90, s90, s91
TR a[148:149],v232 offset:57856
CV v1,v2,v3
MM v[20:23],a[212:215],v[184:187],v[20:23]
s_add_u32 s90, s90, 21659648
EX v6,v6
MM v[20:23],a[208:211],v[168:171],v[20:23]
EX v7,v7
MM v[28:31],a[208:211],v[172:175],v[140:143]
TR a[150:151],v232 offset:61952
TR a[152:153],v233 offset:49664
MM v[28:31],a[212:215],v[188:191],v[28:31]
EX v8,v8
MM v[28:31],a[216:219],v[204:207],v[28:31]
TR a[154:155],v233 offset:53760
CV v2,v4,v5
MM v[28:31],a[220:223],v[220:223],v[28:31]
EX v9,v9
MM v[24:27],a[204:207],v[220:223],v[140:143]
TR a[156:157],v233 offset:57856
CV v3,v6,v7
MM v[24:27],a[200:203],v[204:207],v[24:27]
EX v10,v10
MM v[24:27],a[196:199],v[188:191],v[24:27]
TR a[158:159],v233 offset:61952
CV v8,v8,v9
MM v[24:27],a[192:195],v[172:175],v[24:27]
EX v11,v11
MM v[32:35],a[224:227],v[160:163],v[128:131]
TR a[160:161],v232 offset:50176
TR a[162:163],v232 offset:54272
MM v[32:35],a[228:231],v[176:179],v[32:35]
EX v12,v12
MM v[32:35],a[232:235],v[192:195],v[32:35]
EX v13,v13
MM v[32:35],a[236:239],v[208:211],v[32:35]
TR a[164:165],v232 offset:58368
CV v9,v10,v11
MM v[36:39],a[252:255],v[208:211],v[128:131]
EX v14,v14
MM v[36:39],a[248:251],v[192:195],v[36:39]
TR a[166:167],v232 offset:62464
CV v10,v12,v13
MM v[36:39],a[244:247],v[176:179],v[36:39]
EX v15,v15
MM v[36:39],a[240:243],v[160:163],v[36:39]
TR a[168:169],v233 offset:50176
TR a[170:171],v233 offset:54272
MM v[44:47],a[240:243],v[164:167],v[132:135]
EX v16,v16
MM v[44:47],a[244:247],v[180:183],v[44:47]
EX v17,v17
MM v[44:47],a[248:251],v[196:199],v[44:47]
TR a[172:173],v233 offset:58368
CV v11,v14,v15
MM v[44:47],a[252:255],v[212:215],v[44:47]
EX v18,v18
MM v[40:43],a[236:239],v[212:215],v[132:135]
TR a[174:175],v233 offset:62464
CV v16,v16,v17
MM v[40:43],a[232:235],v[196:199],v[40:43]
EX v19,v19
MM v[40:43],a[228:231],v[180:183],v[40:43]
TR a[176:177],v232 offset:50688
TR a[178:179],v232 offset:54784
MM v[40:43],a[224:227],v[164:167],v[40:43]
EX v20,v20
MM v[48:51],a[224:227],v[168:171],v[136:139]
EX v21,v21
MM v[48:51],a[228:231],v[184:187],v[48:51]
TR a[180:181],v232 offset:58880
CV v17,v18,v19
MM v[48:51],a[232:235],v[200:203],v[48:51]
EX v22,v22
MM v[48:51],a[236:239],v[216:219],v[48:51]
TR a[182:183],v232 offset:62976
CV v18,v20,v21
MM v[52:55],a[252:255],v[216:219],v[136:139]
EX v23,v23
MM v[52:55],a[248:251],v[200:203],v[52:55]
TR a[184:185],v233 offset:50688
TR a[186:187],v233 offset:54784
MM v[52:55],a[244:247],v[184:187],v[52:55]
EX v24,v24
MM v[52:55],a[240:243],v[168:171],v[52:55]
TR a[188:189],v233 offset:58880
CV v19,v22,v23
MM v[60:63],a[240:243],v[172:175],v[140:143]
EX v25,v25
MM v[60:63],a[244:247],v[188:191],v[60:63]
TR a[190:191],v233 offset:62976
CV v24,v24,v25
MM v[60:63],a[248:251],v[204:207],v[60:63]
EX v26,v26
MM v[60:63],a[252:255],v[220:223],v[60:63]
EX v27,v27
MM v[56:59],a[236:239],v[220:223],v[140:143]
EX v28,v28
MM v[56:59],a[232:235],v[204:207],v[56:59]
EX v29,v29
MM v[56:59],a[228:231],v[188:191],v[56:59]
EX v30,v30
MM v[56:59],a[224:227],v[172:175],v[56:59]
s_waitcnt vmcnt(8) lgkmcnt(0)
s_barrier
MM a[0:3],a[128:131],v[64:67],a[0:3]
s_add_u32 m0, s65, 49152
CV v25,v26,v27
CV v26,v28,v29
MM a[0:3],a[132:135],v[68:71],a[0:3]
RD a[192:195],v224 offset:16384
RD a[196:199],v225 offset:16384
MM a[8:11],a[132:135],v[76:79],a[8:11]
EX v31,v31
MM a[8:11],a[128:131],v[72:75],a[8:11]
RD a[200:203],v226 offset:16384
CV v27,v30,v31
MM a[16:19],a[128:131],v[80:83],a[16:19]
TR v[96:97],v124 offset:0
TR v[98:99],v124 offset:512
MM a[16:19],a[132:135],v[84:87],a[16:19]
RD v[108:111],v126 offset:0
TR v[100:101],v124 offset:4096
MM a[24:27],a[132:135],v[92:95],a[24:27]
TR v[102:103],v124 offset:4608
RD v[112:115],v126 offset:512
MM a[24:27],a[128:131],v[88:91],a[24:27]
TR v[104:105],v124 offset:8192
TR v[106:107],v124 offset:8704
MM a[28:31],a[136:139],v[88:91],a[28:31]
RD v[116:119],v126 offset:1024
RD a[204:207],v227 offset:16384
EX v32,v32
MM a[28:31],a[140:143],v[92:95],a[28:31]
EX v33,v33
MM a[20:23],a[140:143],v[84:87],a[20:23]
EX v34,v34
MM a[20:23],a[136:139],v[80:83],a[20:23]
buffer_load_dwordx4 v228, s[8:11], s64 offen offset:0 lds
LPG20_%=:
s_cbranch_execz LTR3_%=
MM a[12:15],a[136:139],v[72:75],a[12:15]
EX v35,v35
MM a[12:15],a[140:143],v[76:79],a[12:15]
EX v36,v36
MM a[4:7],a[140:143],v[68:71],a[4:7]
CV v4,v32,v33
CV v5,v34,v35
MM a[4:7],a[136:139],v[64:67],a[4:7]
EX v37,v37
MM a[32:35],a[144:147],v[64:67],a[32:35]
EX v38,v38
MM a[32:35],a[148:151],v[68:71],a[32:35]
buffer_load_dwordx4 v229, s[8:11], s64 offen offset:1024 lds
MM a[40:43],a[148:151],v[76:79],a[40:43]
EX v39,v39
MM a[40:43],a[144:147],v[72:75],a[40:43]
RD a[208:211],v224 offset:20480
CV v6,v36,v37
MM a[48:51],a[144:147],v[80:83],a[48:51]
s_waitcnt lgkmcnt(2)
EX v40,v40
MM a[48:51],a[148:151],v[84:87],a[48:51]
RD a[212:215],v225 offset:20480
CV v7,v38,v39
MM v[120:123],v[96:99],v[108:111],0
MM v[120:123],v[100:103],v[112:115],v[120:123]
MM v[120:123],v[104:107],v[116:119],v[120:123]
MM a[56:59],a[148:151],v[92:95],a[56:59]
TR v[96:97],v124 offset:12288
TR v[98:99],v124 offset:12800
MM a[56:59],a[144:147],v[88:91],a[56:59]
RD v[108:111],v126 offset:1536
TR v[100:101],v124 offset:16384
MM a[60:63],a[152:155],v[88:91],a[60:63]
TR v[102:103],v124 offset:16896
RD v[112:115],v126 offset:2048
EX v41,v41
MM a[60:63],a[156:159],v[92:95],a[60:63]
TR v[104:105],v124 offset:20480
TR v[106:107],v124 offset:20992
MM a[52:55],a[156:159],v[84:87],a[52:55]
RD v[116:119],v126 offset:2560
CV v12,v40,v41
EX v42,v42
MM a[52:55],a[152:155],v[80:83],a[52:55]
EX v43,v43
MM a[44:47],a[152:155],v[72:75],a[44:47]
buffer_load_dwordx4 v230, s[8:11], s64 offen offset:2048 lds
MM a[44:47],a[156:159],v[76:79],a[44:47]
EX v44,v44
MM a[36:39],a[156:159],v[68:71],a[36:39]
EX v45,v45
MM a[36:39],a[152:155],v[64:67],a[36:39]
RD a[216:219],v226 offset:20480
CV v13,v42,v43
MM a[64:67],a[160:163],v[64:67],a[64:67]
EX v46,v46
MM a[64:67],a[164:167],v[68:71],a[64:67]
EX v47,v47
MM a[72:75],a[164:167],v[76:79],a[72:75]
buffer_load_dwordx4 v231, s[8:11], s64 offen offset:3072 lds
MM a[72:75],a[160:163],v[72:75],a[72:75]
s_add_u32 m0, s65, 114688
RD a[220:223],v227 offset:20480
CV v14,v44,v45
MM a[80:83],a[160:163],v[80:83],a[80:83]
EX v48,v48
MM a[80:83],a[164:167],v[84:87],a[80:83]
EX v49,v49
MM a[88:91],a[164:167],v[92:95],a[88:91]
s_waitcnt lgkmcnt(2)
RD a[224:227],v224 offset:24576
CV v15,v46,v47
MM a[88:91],a[160:163],v[88:91],a[88:91]
EX v50,v50
MM v[120:123],v[96:99],v[108:111],v[120:123]
MM v[120:123],v[100:103],v[112:115],v[120:123]
MM v[120:123],v[104:107],v[116:119],v[120:123]
MM a[92:95],a[168:171],v[88:91],a[92:95]
EX v51,v51
MM a[92:95],a[172:175],v[92:95],a[92:95]
buffer_load_dwordx4 v235, s[12:15], s81 offen offset:0 lds
MM a[84:87],a[172:175],v[84:87],a[84:87]
RD a[228:231],v225 offset:24576
CV v20,v48,v49
MM a[84:87],a[168:171],v[80:83],a[84:87]
EX v52,v52
MM a[76:79],a[168:171],v[72:75],a[76:79]
EX v53,v53
MM a[76:79],a[172:175],v[76:79],a[76:79]
RD a[232:235],v226 offset:24576
CV v21,v50,v51
MM a[68:71],a[172:175],v[68:71],a[68:71]
s_mov_b32 exec_lo, 0x00ff00ff
s_mov_b32 exec_hi, 0x00ff00ff
buffer_store_dwordx4 v[120:123], v127, s[20:23], s90 offen offset:0 sc1
s_mov_b64 exec, -1
s_nop 0
MM a[68:71],a[168:171],v[64:67],a[68:71]
EX v54,v54
MM a[96:99],a[176:179],v[64:67],a[96:99]
EX v55,v55
MM a[96:99],a[180:183],v[68:71],a[96:99]
RD a[236:239],v227 offset:24576
CV v22,v52,v53
MM a[104:107],a[180:183],v[76:79],a[104:107]
EX v56,v56
MM a[104:107],a[176:179],v[72:75],a[104:107]
buffer_load_dwordx4 v235, s[12:15], s64 offen offset:1024 lds
MM a[112:115],a[176:179],v[80:83],a[112:115]
EX v57,v57
MM a[112:115],a[180:183],v[84:87],a[112:115]
RD a[240:243],v224 offset:28672
CV v23,v54,v55
MM a[120:123],a[180:183],v[92:95],a[120:123]
EX v58,v58
MM a[120:123],a[176:179],v[88:91],a[120:123]
EX v59,v59
MM a[124:127],a[184:187],v[88:91],a[124:127]
v_add_f32 v244, v144, v145
v_add_f32 v245, v148, v149
MM a[124:127],a[188:191],v[92:95],a[124:127]
v_add_f32 v246, v152, v153
v_add_f32 v247, v156, v157
MM a[116:119],a[188:191],v[84:87],a[116:119]
EX v60,v60
MM a[116:119],a[184:187],v[80:83],a[116:119]
RD a[244:247],v225 offset:28672
CV v28,v56,v57
MM a[108:111],a[184:187],v[72:75],a[108:111]
EX v61,v61
MM a[108:111],a[188:191],v[76:79],a[108:111]
buffer_load_dwordx4 v235, s[12:15], s81 offen offset:2048 lds
MM a[100:103],a[188:191],v[68:71],a[100:103]
EX v62,v62
MM a[100:103],a[184:187],v[64:67],a[100:103]
RD a[248:251],v226 offset:28672
CV v29,v58,v59
v_smfmac_f32_16x16x64_bf16 v[144:147], v[236:239], v[0:7], v234
RD a[252:255],v227 offset:28672
EX v63,v63
v_smfmac_f32_16x16x64_bf16 v[148:151], v[236:239], v[8:15], v234
CV v30,v60,v61
v_smfmac_f32_16x16x64_bf16 v[152:155], v[236:239], v[16:23], v234
CV v31,v62,v63
s_nop 1
v_smfmac_f32_16x16x64_bf16 v[156:159], v[236:239], v[24:31], v234
s_waitcnt lgkmcnt(0)
buffer_load_dwordx4 v235, s[12:15], s64 offen offset:3072 lds
MM v[64:67],a[192:195],v[160:163],v[128:131]
s_add_u32 s64, s64, 16384
TR a[128:129],v232 offset:0
s_add_u32 s81, s64, 896
MM v[64:67],a[196:199],v[176:179],v[64:67]
TR a[130:131],v232 offset:4096
v_max3_f32 v249, v144, v145, v148
MM v[64:67],a[200:203],v[192:195],v[64:67]
TR a[132:133],v232 offset:8192
v_max3_f32 v249, v249, v149, v152
MM v[64:67],a[204:207],v[208:211],v[64:67]
TR a[134:135],v232 offset:12288
v_max3_f32 v249, v249, v153, v156
MM v[68:71],a[220:223],v[208:211],v[128:131]
TR a[136:137],v233 offset:0
v_max_f32 v249, v249, v157
MM v[68:71],a[216:219],v[192:195],v[68:71]
TR a[138:139],v233 offset:4096
v_cmp_lt_f32 vcc, s71, v249
MM v[68:71],a[212:215],v[176:179],v[68:71]
TR a[140:141],v233 offset:8192
TR a[142:143],v233 offset:12288
MM v[68:71],a[208:211],v[160:163],v[68:71]
s_cbranch_vccnz LSITE28_%=
LBACK28_%=:
MM v[76:79],a[208:211],v[164:167],v[132:135]
EX v64,v64
MM v[76:79],a[212:215],v[180:183],v[76:79]
EX v65,v65
MM v[76:79],a[216:219],v[196:199],v[76:79]
TR a[144:145],v232 offset:512
CV v64,v64,v65
MM v[76:79],a[220:223],v[212:215],v[76:79]
EX v66,v66
MM v[72:75],a[204:207],v[212:215],v[132:135]
EX v67,v67
MM v[72:75],a[200:203],v[196:199],v[72:75]
)ASM"
R"ASM(
TR a[146:147],v232 offset:4608
CV v65,v66,v67
MM v[72:75],a[196:199],v[180:183],v[72:75]
EX v68,v68
MM v[72:75],a[192:195],v[164:167],v[72:75]
TR a[148:149],v232 offset:8704
TR a[150:151],v232 offset:12800
MM v[80:83],a[192:195],v[168:171],v[136:139]
EX v69,v69
MM v[80:83],a[196:199],v[184:187],v[80:83]
TR a[152:153],v233 offset:512
CV v66,v68,v69
MM v[80:83],a[200:203],v[200:203],v[80:83]
EX v70,v70
MM v[80:83],a[204:207],v[216:219],v[80:83]
TR a[154:155],v233 offset:4608
TR a[156:157],v233 offset:8704
MM v[84:87],a[220:223],v[216:219],v[136:139]
v_lshlrev_b32 v32, 3, v248
v_min_u32 v32, 4992, v32
MM v[84:87],a[216:219],v[200:203],v[84:87]
s_sub_u32 s91, 5242880, s26
s_sub_u32 s60, s24, s91
s_subb_u32 s61, s25, 0
MM v[84:87],a[212:215],v[184:187],v[84:87]
s_mov_b32 s62, 5242880
EX v71,v71
MM v[84:87],a[208:211],v[168:171],v[84:87]
s_mov_b32 s63, 131072
TR a[158:159],v233 offset:12800
CV v67,v70,v71
MM v[92:95],a[208:211],v[172:175],v[140:143]
s_lshl_b32 s90, s16, 2
EX v72,v72
MM v[92:95],a[212:215],v[188:191],v[92:95]
s_add_u32 s90, s90, %[swave]
EX v73,v73
MM v[92:95],a[216:219],v[204:207],v[92:95]
s_mul_i32 s90, s90, 10240
TR a[160:161],v232 offset:1024
CV v72,v72,v73
MM v[92:95],a[220:223],v[220:223],v[92:95]
s_add_u32 s90, s90, 5120
EX v74,v74
MM v[88:91],a[204:207],v[220:223],v[140:143]
s_add_u32 s91, %[slds], 135168
s_mov_b32 m0, s91
s_nop 0
buffer_load_dword v32, s[60:63], s90 offen offset:0 lds
MM v[88:91],a[200:203],v[204:207],v[88:91]
EX v75,v75
MM v[88:91],a[196:199],v[188:191],v[88:91]
TR a[162:163],v232 offset:5120
CV v73,v74,v75
MM v[88:91],a[192:195],v[172:175],v[88:91]
EX v76,v76
MM v[96:99],a[224:227],v[160:163],v[128:131]
TR a[164:165],v232 offset:9216
TR a[166:167],v232 offset:13312
MM v[96:99],a[228:231],v[176:179],v[96:99]
EX v77,v77
MM v[96:99],a[232:235],v[192:195],v[96:99]
TR a[168:169],v233 offset:1024
CV v74,v76,v77
MM v[96:99],a[236:239],v[208:211],v[96:99]
EX v78,v78
MM v[100:103],a[252:255],v[208:211],v[128:131]
EX v79,v79
MM v[100:103],a[248:251],v[192:195],v[100:103]
TR a[170:171],v233 offset:5120
CV v75,v78,v79
MM v[100:103],a[244:247],v[176:179],v[100:103]
EX v80,v80
MM v[100:103],a[240:243],v[160:163],v[100:103]
TR a[172:173],v233 offset:9216
TR a[174:175],v233 offset:13312
MM v[108:111],a[240:243],v[164:167],v[132:135]
s_add_u32 s67, s95, 5120
EX v81,v81
MM v[108:111],a[244:247],v[180:183],v[108:111]
s_mov_b32 s70, 1
TR a[176:177],v232 offset:1536
CV v80,v80,v81
MM v[108:111],a[248:251],v[196:199],v[108:111]
EX v82,v82
MM v[108:111],a[252:255],v[212:215],v[108:111]
EX v83,v83
MM v[104:107],a[236:239],v[212:215],v[132:135]
TR a[178:179],v232 offset:5632
CV v81,v82,v83
MM v[104:107],a[232:235],v[196:199],v[104:107]
EX v84,v84
MM v[104:107],a[228:231],v[180:183],v[104:107]
TR a[180:181],v232 offset:9728
TR a[182:183],v232 offset:13824
MM v[104:107],a[224:227],v[164:167],v[104:107]
EX v85,v85
MM v[112:115],a[224:227],v[168:171],v[136:139]
TR a[184:185],v233 offset:1536
CV v82,v84,v85
MM v[112:115],a[228:231],v[184:187],v[112:115]
EX v86,v86
MM v[112:115],a[232:235],v[200:203],v[112:115]
EX v87,v87
MM v[112:115],a[236:239],v[216:219],v[112:115]
TR a[186:187],v233 offset:5632
CV v83,v86,v87
MM v[116:119],a[252:255],v[216:219],v[136:139]
EX v88,v88
MM v[116:119],a[248:251],v[200:203],v[116:119]
TR a[188:189],v233 offset:9728
TR a[190:191],v233 offset:13824
MM v[116:119],a[244:247],v[184:187],v[116:119]
EX v89,v89
MM v[116:119],a[240:243],v[168:171],v[116:119]
EX v90,v90
MM v[124:127],a[240:243],v[172:175],v[140:143]
EX v91,v91
MM v[124:127],a[244:247],v[188:191],v[124:127]
EX v92,v92
MM v[124:127],a[248:251],v[204:207],v[124:127]
EX v93,v93
MM v[124:127],a[252:255],v[220:223],v[124:127]
EX v94,v94
MM v[120:123],a[236:239],v[220:223],v[140:143]
EX v95,v95
MM v[120:123],a[232:235],v[204:207],v[120:123]
EX v96,v96
MM v[120:123],a[228:231],v[188:191],v[120:123]
EX v97,v97
MM v[120:123],a[224:227],v[172:175],v[120:123]
s_waitcnt vmcnt(14) lgkmcnt(0)
s_barrier
MM a[0:3],a[128:131],v[0:3],a[0:3]
s_add_u32 m0, s65, 0
CV v88,v88,v89
CV v89,v90,v91
MM a[0:3],a[132:135],v[4:7],a[0:3]
RD a[192:195],v224 offset:32768
CV v90,v92,v93
MM a[8:11],a[132:135],v[12:15],a[8:11]
RD a[196:199],v225 offset:32768
CV v91,v94,v95
MM a[8:11],a[128:131],v[8:11],a[8:11]
buffer_load_dwordx4 v228, s[8:11], s64 offen offset:0 lds
MM a[16:19],a[128:131],v[16:19],a[16:19]
RD a[200:203],v226 offset:32768
CV v68,v96,v97
MM a[16:19],a[132:135],v[20:23],a[16:19]
buffer_load_dwordx4 v229, s[8:11], s64 offen offset:1024 lds
MM a[24:27],a[132:135],v[28:31],a[24:27]
EX v98,v98
MM a[24:27],a[128:131],v[24:27],a[24:27]
RD a[204:207],v227 offset:32768
RD a[208:211],v224 offset:36864
MM a[28:31],a[136:139],v[24:27],a[28:31]
EX v99,v99
MM a[28:31],a[140:143],v[28:31],a[28:31]
RD a[212:215],v225 offset:36864
CV v69,v98,v99
MM a[20:23],a[140:143],v[20:23],a[20:23]
EX v100,v100
MM a[20:23],a[136:139],v[16:19],a[20:23]
EX v101,v101
MM a[12:15],a[136:139],v[8:11],a[12:15]
RD a[216:219],v226 offset:36864
CV v70,v100,v101
MM a[12:15],a[140:143],v[12:15],a[12:15]
buffer_load_dwordx4 v230, s[8:11], s64 offen offset:2048 lds
MM a[4:7],a[140:143],v[4:7],a[4:7]
EX v102,v102
MM a[4:7],a[136:139],v[0:3],a[4:7]
EX v103,v103
MM a[32:35],a[144:147],v[0:3],a[32:35]
RD a[220:223],v227 offset:36864
CV v71,v102,v103
MM a[32:35],a[148:151],v[4:7],a[32:35]
EX v104,v104
MM a[40:43],a[148:151],v[12:15],a[40:43]
EX v105,v105
MM a[40:43],a[144:147],v[8:11],a[40:43]
RD a[224:227],v224 offset:40960
CV v76,v104,v105
MM a[48:51],a[144:147],v[16:19],a[48:51]
buffer_load_dwordx4 v231, s[8:11], s64 offen offset:3072 lds
MM a[48:51],a[148:151],v[20:23],a[48:51]
s_add_u32 m0, s65, 65536
EX v106,v106
MM a[56:59],a[148:151],v[28:31],a[56:59]
EX v107,v107
MM a[56:59],a[144:147],v[24:27],a[56:59]
RD a[228:231],v225 offset:40960
CV v77,v106,v107
MM a[60:63],a[152:155],v[24:27],a[60:63]
EX v108,v108
MM a[60:63],a[156:159],v[28:31],a[60:63]
buffer_load_dwordx4 v235, s[12:15], s81 offen offset:0 lds
MM a[52:55],a[156:159],v[20:23],a[52:55]
EX v109,v109
MM a[52:55],a[152:155],v[16:19],a[52:55]
RD a[232:235],v226 offset:40960
CV v78,v108,v109
MM a[44:47],a[152:155],v[8:11],a[44:47]
EX v110,v110
MM a[44:47],a[156:159],v[12:15],a[44:47]
EX v111,v111
MM a[36:39],a[156:159],v[4:7],a[36:39]
RD a[236:239],v227 offset:40960
CV v79,v110,v111
MM a[36:39],a[152:155],v[0:3],a[36:39]
EX v112,v112
MM a[64:67],a[160:163],v[0:3],a[64:67]
buffer_load_dwordx4 v235, s[12:15], s64 offen offset:1024 lds
MM a[64:67],a[164:167],v[4:7],a[64:67]
EX v113,v113
MM a[72:75],a[164:167],v[12:15],a[72:75]
RD a[240:243],v224 offset:45056
CV v84,v112,v113
MM a[72:75],a[160:163],v[8:11],a[72:75]
EX v114,v114
MM a[80:83],a[160:163],v[16:19],a[80:83]
EX v115,v115
MM a[80:83],a[164:167],v[20:23],a[80:83]
RD a[244:247],v225 offset:45056
CV v85,v114,v115
MM a[88:91],a[164:167],v[28:31],a[88:91]
EX v116,v116
MM a[88:91],a[160:163],v[24:27],a[88:91]
buffer_load_dwordx4 v235, s[12:15], s81 offen offset:2048 lds
MM a[92:95],a[168:171],v[24:27],a[92:95]
EX v117,v117
MM a[92:95],a[172:175],v[28:31],a[92:95]
RD a[248:251],v226 offset:45056
CV v86,v116,v117
MM a[84:87],a[172:175],v[20:23],a[84:87]
EX v118,v118
MM a[84:87],a[168:171],v[16:19],a[84:87]
EX v119,v119
MM a[76:79],a[168:171],v[8:11],a[76:79]
RD a[252:255],v227 offset:45056
CV v87,v118,v119
MM a[76:79],a[172:175],v[12:15],a[76:79]
EX v120,v120
MM a[68:71],a[172:175],v[4:7],a[68:71]
buffer_load_dwordx4 v235, s[12:15], s64 offen offset:3072 lds
MM a[68:71],a[168:171],v[0:3],a[68:71]
EX v121,v121
MM a[96:99],a[176:179],v[0:3],a[96:99]
EX v122,v122
MM a[96:99],a[180:183],v[4:7],a[96:99]
EX v123,v123
MM a[104:107],a[180:183],v[12:15],a[104:107]
EX v124,v124
MM a[104:107],a[176:179],v[8:11],a[104:107]
EX v125,v125
MM a[112:115],a[176:179],v[16:19],a[112:115]
EX v126,v126
MM a[112:115],a[180:183],v[20:23],a[112:115]
EX v127,v127
MM a[120:123],a[180:183],v[28:31],a[120:123]
s_lshl_b32 s91, s47, 2
CV v92,v120,v121
CV v93,v122,v123
MM a[120:123],a[176:179],v[24:27],a[120:123]
s_add_u32 s91, s91, %[swave]
CV v94,v124,v125
CV v95,v126,v127
MM a[124:127],a[184:187],v[24:27],a[124:127]
s_lshl_b32 s91, s91, 2
v_add_f32 v244, v144, v145
v_add_f32 v245, v148, v149
MM a[124:127],a[188:191],v[28:31],a[124:127]
s_add_u32 s91, s91, 73728
v_add_f32 v246, v152, v153
v_add_f32 v247, v156, v157
MM a[116:119],a[188:191],v[20:23],a[116:119]
s_waitcnt vmcnt(12)
v_mov_b32 v250, s69
s_mov_b64 exec, 1
buffer_store_dword v250, off, s[20:23], s91 offset:0 sc1
s_mov_b64 exec, -1
MM a[116:119],a[184:187],v[16:19],a[116:119]
MM a[108:111],a[184:187],v[8:11],a[108:111]
MM a[108:111],a[188:191],v[12:15],a[108:111]
MM a[100:103],a[188:191],v[4:7],a[100:103]
MM a[100:103],a[184:187],v[0:3],a[100:103]
v_smfmac_f32_16x16x64_bf16 v[144:147], v[236:239], v[64:71], v234
v_smfmac_f32_16x16x64_bf16 v[148:151], v[236:239], v[72:79], v234
v_smfmac_f32_16x16x64_bf16 v[152:155], v[236:239], v[80:87], v234
v_smfmac_f32_16x16x64_bf16 v[156:159], v[236:239], v[88:95], v234
s_waitcnt lgkmcnt(0)
s_cmp_ge_u32 s16, 20
s_cbranch_scc1 LLAP_%=
MM v[0:3],a[192:195],v[160:163],v[128:131]
s_add_u32 s64, s64, 16384
TR a[128:129],v232 offset:16384
s_add_u32 s81, s64, 896
MM v[0:3],a[196:199],v[176:179],v[0:3]
TR a[130:131],v232 offset:20480
v_max3_f32 v249, v144, v145, v148
MM v[0:3],a[200:203],v[192:195],v[0:3]
TR a[132:133],v232 offset:24576
v_max3_f32 v249, v249, v149, v152
MM v[0:3],a[204:207],v[208:211],v[0:3]
TR a[134:135],v232 offset:28672
v_max3_f32 v249, v249, v153, v156
MM v[4:7],a[220:223],v[208:211],v[128:131]
TR a[136:137],v233 offset:16384
v_max_f32 v249, v249, v157
MM v[4:7],a[216:219],v[192:195],v[4:7]
TR a[138:139],v233 offset:20480
v_cmp_lt_f32 vcc, s71, v249
MM v[4:7],a[212:215],v[176:179],v[4:7]
TR a[140:141],v233 offset:24576
TR a[142:143],v233 offset:28672
MM v[4:7],a[208:211],v[160:163],v[4:7]
s_cbranch_vccnz LSITE29_%=
LBACK29_%=:
MM v[12:15],a[208:211],v[164:167],v[132:135]
EX v0,v0
MM v[12:15],a[212:215],v[180:183],v[12:15]
EX v1,v1
MM v[12:15],a[216:219],v[196:199],v[12:15]
TR a[144:145],v232 offset:16896
CV v0,v0,v1
MM v[12:15],a[220:223],v[212:215],v[12:15]
EX v2,v2
MM v[8:11],a[204:207],v[212:215],v[132:135]
EX v3,v3
MM v[8:11],a[200:203],v[196:199],v[8:11]
TR a[146:147],v232 offset:20992
CV v1,v2,v3
MM v[8:11],a[196:199],v[180:183],v[8:11]
EX v4,v4
MM v[8:11],a[192:195],v[164:167],v[8:11]
TR a[148:149],v232 offset:25088
TR a[150:151],v232 offset:29184
MM v[16:19],a[192:195],v[168:171],v[136:139]
EX v5,v5
MM v[16:19],a[196:199],v[184:187],v[16:19]
TR a[152:153],v233 offset:16896
CV v2,v4,v5
MM v[16:19],a[200:203],v[200:203],v[16:19]
EX v6,v6
MM v[16:19],a[204:207],v[216:219],v[16:19]
TR a[154:155],v233 offset:20992
TR a[156:157],v233 offset:25088
MM v[20:23],a[220:223],v[216:219],v[136:139]
s_add_u32 s87, s95, 5120
buffer_store_dword v243, v248, s[20:23], s87 offen offset:12 sc1
MM v[20:23],a[216:219],v[200:203],v[20:23]
EX v7,v7
MM v[20:23],a[212:215],v[184:187],v[20:23]
TR a[158:159],v233 offset:29184
CV v3,v6,v7
MM v[20:23],a[208:211],v[168:171],v[20:23]
EX v8,v8
MM v[28:31],a[208:211],v[172:175],v[140:143]
EX v9,v9
MM v[28:31],a[212:215],v[188:191],v[28:31]
TR a[160:161],v232 offset:17408
CV v8,v8,v9
MM v[28:31],a[216:219],v[204:207],v[28:31]
EX v10,v10
MM v[28:31],a[220:223],v[220:223],v[28:31]
EX v11,v11
MM v[24:27],a[204:207],v[220:223],v[140:143]
TR a[162:163],v232 offset:21504
CV v9,v10,v11
MM v[24:27],a[200:203],v[204:207],v[24:27]
TR a[164:165],v232 offset:25600
TR a[166:167],v232 offset:29696
MM v[24:27],a[196:199],v[188:191],v[24:27]
EX v12,v12
MM v[24:27],a[192:195],v[172:175],v[24:27]
EX v13,v13
MM v[32:35],a[224:227],v[160:163],v[128:131]
TR a[168:169],v233 offset:17408
CV v10,v12,v13
MM v[32:35],a[228:231],v[176:179],v[32:35]
EX v14,v14
MM v[32:35],a[232:235],v[192:195],v[32:35]
TR a[170:171],v233 offset:21504
TR a[172:173],v233 offset:25600
MM v[32:35],a[236:239],v[208:211],v[32:35]
EX v15,v15
MM v[36:39],a[252:255],v[208:211],v[128:131]
TR a[174:175],v233 offset:29696
CV v11,v14,v15
MM v[36:39],a[248:251],v[192:195],v[36:39]
EX v16,v16
MM v[36:39],a[244:247],v[176:179],v[36:39]
)ASM"
R"ASM(
EX v17,v17
MM v[36:39],a[240:243],v[160:163],v[36:39]
TR a[176:177],v232 offset:17920
CV v16,v16,v17
MM v[44:47],a[240:243],v[164:167],v[132:135]
EX v18,v18
MM v[44:47],a[244:247],v[180:183],v[44:47]
TR a[178:179],v232 offset:22016
TR a[180:181],v232 offset:26112
MM v[44:47],a[248:251],v[196:199],v[44:47]
EX v19,v19
MM v[44:47],a[252:255],v[212:215],v[44:47]
s_nop 0
LPG21_%=:
s_cbranch_execz LTR4_%=
TR a[182:183],v232 offset:30208
CV v17,v18,v19
MM v[40:43],a[236:239],v[212:215],v[132:135]
EX v20,v20
MM v[40:43],a[232:235],v[196:199],v[40:43]
EX v21,v21
MM v[40:43],a[228:231],v[180:183],v[40:43]
TR a[184:185],v233 offset:17920
CV v18,v20,v21
MM v[40:43],a[224:227],v[164:167],v[40:43]
EX v22,v22
MM v[48:51],a[224:227],v[168:171],v[136:139]
TR a[186:187],v233 offset:22016
TR a[188:189],v233 offset:26112
MM v[48:51],a[228:231],v[184:187],v[48:51]
EX v23,v23
MM v[48:51],a[232:235],v[200:203],v[48:51]
TR a[190:191],v233 offset:30208
CV v19,v22,v23
MM v[48:51],a[236:239],v[216:219],v[48:51]
EX v24,v24
MM v[52:55],a[252:255],v[216:219],v[136:139]
EX v25,v25
MM v[52:55],a[248:251],v[200:203],v[52:55]
EX v26,v26
MM v[52:55],a[244:247],v[184:187],v[52:55]
EX v27,v27
MM v[52:55],a[240:243],v[168:171],v[52:55]
EX v28,v28
MM v[60:63],a[240:243],v[172:175],v[140:143]
EX v29,v29
MM v[60:63],a[244:247],v[188:191],v[60:63]
EX v30,v30
MM v[60:63],a[248:251],v[204:207],v[60:63]
EX v31,v31
MM v[60:63],a[252:255],v[220:223],v[60:63]
EX v32,v32
MM v[56:59],a[236:239],v[220:223],v[140:143]
EX v33,v33
MM v[56:59],a[232:235],v[204:207],v[56:59]
EX v34,v34
MM v[56:59],a[228:231],v[188:191],v[56:59]
EX v35,v35
MM v[56:59],a[224:227],v[172:175],v[56:59]
s_waitcnt lgkmcnt(0)
s_barrier
MM a[0:3],a[128:131],v[64:67],a[0:3]
s_add_u32 m0, s65, 16384
CV v24,v24,v25
CV v25,v26,v27
MM a[0:3],a[132:135],v[68:71],a[0:3]
RD a[192:195],v224 offset:49152
CV v26,v28,v29
MM a[8:11],a[132:135],v[76:79],a[8:11]
RD a[196:199],v225 offset:49152
CV v27,v30,v31
MM a[8:11],a[128:131],v[72:75],a[8:11]
buffer_load_dwordx4 v228, s[8:11], s64 offen offset:0 lds
MM a[16:19],a[128:131],v[80:83],a[16:19]
RD a[200:203],v226 offset:49152
CV v4,v32,v33
MM a[16:19],a[132:135],v[84:87],a[16:19]
RD a[204:207],v227 offset:49152
CV v5,v34,v35
MM a[24:27],a[132:135],v[92:95],a[24:27]
buffer_load_dwordx4 v229, s[8:11], s64 offen offset:1024 lds
MM a[24:27],a[128:131],v[88:91],a[24:27]
RD a[208:211],v224 offset:53248
RD a[212:215],v225 offset:53248
MM a[28:31],a[136:139],v[88:91],a[28:31]
buffer_load_dwordx4 v230, s[8:11], s64 offen offset:2048 lds
MM a[28:31],a[140:143],v[92:95],a[28:31]
EX v36,v36
MM a[20:23],a[140:143],v[84:87],a[20:23]
EX v37,v37
MM a[20:23],a[136:139],v[80:83],a[20:23]
RD a[216:219],v226 offset:53248
CV v6,v36,v37
MM a[12:15],a[136:139],v[72:75],a[12:15]
EX v38,v38
MM a[12:15],a[140:143],v[76:79],a[12:15]
RD a[220:223],v227 offset:53248
RD a[224:227],v224 offset:57344
MM a[4:7],a[140:143],v[68:71],a[4:7]
EX v39,v39
MM a[4:7],a[136:139],v[64:67],a[4:7]
RD a[228:231],v225 offset:57344
CV v7,v38,v39
MM a[32:35],a[144:147],v[64:67],a[32:35]
EX v40,v40
MM a[32:35],a[148:151],v[68:71],a[32:35]
buffer_load_dwordx4 v231, s[8:11], s64 offen offset:3072 lds
MM a[40:43],a[148:151],v[76:79],a[40:43]
s_add_u32 m0, s65, 81920
EX v41,v41
MM a[40:43],a[144:147],v[72:75],a[40:43]
RD a[232:235],v226 offset:57344
CV v12,v40,v41
MM a[48:51],a[144:147],v[80:83],a[48:51]
EX v42,v42
MM a[48:51],a[148:151],v[84:87],a[48:51]
EX v43,v43
MM a[56:59],a[148:151],v[92:95],a[56:59]
RD a[236:239],v227 offset:57344
CV v13,v42,v43
MM a[56:59],a[144:147],v[88:91],a[56:59]
EX v44,v44
MM a[60:63],a[152:155],v[88:91],a[60:63]
buffer_load_dwordx4 v235, s[12:15], s81 offen offset:0 lds
MM a[60:63],a[156:159],v[92:95],a[60:63]
EX v45,v45
MM a[52:55],a[156:159],v[84:87],a[52:55]
RD a[240:243],v224 offset:61440
CV v14,v44,v45
MM a[52:55],a[152:155],v[80:83],a[52:55]
EX v46,v46
MM a[44:47],a[152:155],v[72:75],a[44:47]
EX v47,v47
MM a[44:47],a[156:159],v[76:79],a[44:47]
RD a[244:247],v225 offset:61440
CV v15,v46,v47
MM a[36:39],a[156:159],v[68:71],a[36:39]
buffer_load_dwordx4 v235, s[12:15], s64 offen offset:1024 lds
MM a[36:39],a[152:155],v[64:67],a[36:39]
EX v48,v48
MM a[64:67],a[160:163],v[64:67],a[64:67]
EX v49,v49
MM a[64:67],a[164:167],v[68:71],a[64:67]
RD a[248:251],v226 offset:61440
CV v20,v48,v49
MM a[72:75],a[164:167],v[76:79],a[72:75]
EX v50,v50
MM a[72:75],a[160:163],v[72:75],a[72:75]
EX v51,v51
MM a[80:83],a[160:163],v[80:83],a[80:83]
RD a[252:255],v227 offset:61440
CV v21,v50,v51
MM a[80:83],a[164:167],v[84:87],a[80:83]
buffer_load_dwordx4 v235, s[12:15], s81 offen offset:2048 lds
MM a[88:91],a[164:167],v[92:95],a[88:91]
EX v52,v52
MM a[88:91],a[160:163],v[88:91],a[88:91]
EX v53,v53
MM a[92:95],a[168:171],v[88:91],a[92:95]
EX v54,v54
MM a[92:95],a[172:175],v[92:95],a[92:95]
buffer_load_dwordx4 v235, s[12:15], s64 offen offset:3072 lds
MM a[84:87],a[172:175],v[84:87],a[84:87]
EX v55,v55
MM a[84:87],a[168:171],v[80:83],a[84:87]
EX v56,v56
MM a[76:79],a[168:171],v[72:75],a[76:79]
EX v57,v57
MM a[76:79],a[172:175],v[76:79],a[76:79]
EX v58,v58
MM a[68:71],a[172:175],v[68:71],a[68:71]
EX v59,v59
MM a[68:71],a[168:171],v[64:67],a[68:71]
EX v60,v60
MM a[96:99],a[176:179],v[64:67],a[96:99]
EX v61,v61
MM a[96:99],a[180:183],v[68:71],a[96:99]
EX v62,v62
MM a[104:107],a[180:183],v[76:79],a[104:107]
EX v63,v63
MM a[104:107],a[176:179],v[72:75],a[104:107]
CV v22,v52,v53
CV v23,v54,v55
MM a[112:115],a[176:179],v[80:83],a[112:115]
CV v28,v56,v57
CV v29,v58,v59
MM a[112:115],a[180:183],v[84:87],a[112:115]
CV v30,v60,v61
CV v31,v62,v63
MM a[120:123],a[180:183],v[92:95],a[120:123]
MM a[120:123],a[176:179],v[88:91],a[120:123]
MM a[124:127],a[184:187],v[88:91],a[124:127]
v_add_f32 v244, v144, v145
v_add_f32 v245, v148, v149
MM a[124:127],a[188:191],v[92:95],a[124:127]
v_add_f32 v246, v152, v153
v_add_f32 v247, v156, v157
MM a[116:119],a[188:191],v[84:87],a[116:119]
MM a[116:119],a[184:187],v[80:83],a[116:119]
MM a[108:111],a[184:187],v[72:75],a[108:111]
MM a[108:111],a[188:191],v[76:79],a[108:111]
MM a[100:103],a[188:191],v[68:71],a[100:103]
MM a[100:103],a[184:187],v[64:67],a[100:103]
v_smfmac_f32_16x16x64_bf16 v[144:147], v[236:239], v[0:7], v234
v_smfmac_f32_16x16x64_bf16 v[148:151], v[236:239], v[8:15], v234
v_smfmac_f32_16x16x64_bf16 v[152:155], v[236:239], v[16:23], v234
v_smfmac_f32_16x16x64_bf16 v[156:159], v[236:239], v[24:31], v234
s_waitcnt lgkmcnt(0)
MM v[64:67],a[192:195],v[160:163],v[128:131]
s_add_u32 s64, s64, 16384
TR a[128:129],v232 offset:32768
s_add_u32 s81, s64, 896
MM v[64:67],a[196:199],v[176:179],v[64:67]
TR a[130:131],v232 offset:36864
v_max3_f32 v249, v144, v145, v148
MM v[64:67],a[200:203],v[192:195],v[64:67]
TR a[132:133],v232 offset:40960
v_max3_f32 v249, v249, v149, v152
MM v[64:67],a[204:207],v[208:211],v[64:67]
TR a[134:135],v232 offset:45056
v_max3_f32 v249, v249, v153, v156
MM v[68:71],a[220:223],v[208:211],v[128:131]
TR a[136:137],v233 offset:32768
v_max_f32 v249, v249, v157
MM v[68:71],a[216:219],v[192:195],v[68:71]
TR a[138:139],v233 offset:36864
v_cmp_lt_f32 vcc, s71, v249
MM v[68:71],a[212:215],v[176:179],v[68:71]
TR a[140:141],v233 offset:40960
TR a[142:143],v233 offset:45056
MM v[68:71],a[208:211],v[160:163],v[68:71]
s_cbranch_vccnz LSITE30_%=
LBACK30_%=:
MM v[76:79],a[208:211],v[164:167],v[132:135]
s_and_b32 s90, s17, 63
s_cmp_lt_u32 s17, 80
s_cselect_b32 s17, s17, s90
s_cselect_b64 s[44:45], -1, 0
MM v[76:79],a[212:215],v[180:183],v[76:79]
v_lshrrev_b32 v253, 4, v248
v_min_u32 v253, 35, v253
MM v[76:79],a[216:219],v[196:199],v[76:79]
v_lshrrev_b32 v252, 2, v253
v_mul_u32_u24 v252, 144, v252
MM v[76:79],a[220:223],v[212:215],v[76:79]
TR a[144:145],v232 offset:33280
v_lshl_add_u32 v253, v253, 2, v252
MM v[72:75],a[204:207],v[212:215],v[132:135]
s_lshr_b32 s90, s17, 3
EX v64,v64
MM v[72:75],a[200:203],v[196:199],v[72:75]
s_lshl_b32 s76, s90, 4
EX v65,v65
MM v[72:75],a[196:199],v[180:183],v[72:75]
s_add_u32 s76, s76, 73728
buffer_load_dword v252, v253, s[20:23], s76 offen offset:0 sc1
MM v[72:75],a[192:195],v[164:167],v[72:75]
EX v66,v66
MM v[80:83],a[192:195],v[168:171],v[136:139]
EX v67,v67
MM v[80:83],a[196:199],v[184:187],v[80:83]
EX v68,v68
MM v[80:83],a[200:203],v[200:203],v[80:83]
TR a[146:147],v232 offset:37376
CV v64,v64,v65
MM v[80:83],a[204:207],v[216:219],v[80:83]
EX v69,v69
MM v[84:87],a[220:223],v[216:219],v[136:139]
TR a[148:149],v232 offset:41472
CV v65,v66,v67
MM v[84:87],a[216:219],v[200:203],v[84:87]
EX v70,v70
MM v[84:87],a[212:215],v[184:187],v[84:87]
TR a[150:151],v232 offset:45568
CV v66,v68,v69
MM v[84:87],a[208:211],v[168:171],v[84:87]
EX v71,v71
MM v[92:95],a[208:211],v[172:175],v[140:143]
TR a[152:153],v233 offset:33280
TR a[154:155],v233 offset:37376
MM v[92:95],a[212:215],v[188:191],v[92:95]
EX v72,v72
MM v[92:95],a[216:219],v[204:207],v[92:95]
EX v73,v73
MM v[92:95],a[220:223],v[220:223],v[92:95]
TR a[156:157],v233 offset:41472
CV v67,v70,v71
MM v[88:91],a[204:207],v[220:223],v[140:143]
EX v74,v74
MM v[88:91],a[200:203],v[204:207],v[88:91]
TR a[158:159],v233 offset:45568
CV v72,v72,v73
MM v[88:91],a[196:199],v[188:191],v[88:91]
EX v75,v75
MM v[88:91],a[192:195],v[172:175],v[88:91]
TR a[160:161],v232 offset:33792
TR a[162:163],v232 offset:37888
MM v[96:99],a[224:227],v[160:163],v[128:131]
EX v76,v76
MM v[96:99],a[228:231],v[176:179],v[96:99]
TR a[164:165],v232 offset:41984
CV v73,v74,v75
MM v[96:99],a[232:235],v[192:195],v[96:99]
EX v77,v77
MM v[96:99],a[236:239],v[208:211],v[96:99]
EX v78,v78
MM v[100:103],a[252:255],v[208:211],v[128:131]
TR a[166:167],v232 offset:46080
CV v74,v76,v77
MM v[100:103],a[248:251],v[192:195],v[100:103]
EX v79,v79
MM v[100:103],a[244:247],v[176:179],v[100:103]
TR a[168:169],v233 offset:33792
CV v75,v78,v79
MM v[100:103],a[240:243],v[160:163],v[100:103]
EX v80,v80
MM v[108:111],a[240:243],v[164:167],v[132:135]
TR a[170:171],v233 offset:37888
TR a[172:173],v233 offset:41984
MM v[108:111],a[244:247],v[180:183],v[108:111]
EX v81,v81
MM v[108:111],a[248:251],v[196:199],v[108:111]
TR a[174:175],v233 offset:46080
CV v80,v80,v81
MM v[108:111],a[252:255],v[212:215],v[108:111]
EX v82,v82
MM v[104:107],a[236:239],v[212:215],v[132:135]
EX v83,v83
MM v[104:107],a[232:235],v[196:199],v[104:107]
TR a[176:177],v232 offset:34304
TR a[178:179],v232 offset:38400
MM v[104:107],a[228:231],v[180:183],v[104:107]
EX v84,v84
MM v[104:107],a[224:227],v[164:167],v[104:107]
TR a[180:181],v232 offset:42496
CV v81,v82,v83
MM v[112:115],a[224:227],v[168:171],v[136:139]
EX v85,v85
MM v[112:115],a[228:231],v[184:187],v[112:115]
TR a[182:183],v232 offset:46592
CV v82,v84,v85
MM v[112:115],a[232:235],v[200:203],v[112:115]
EX v86,v86
MM v[112:115],a[236:239],v[216:219],v[112:115]
TR a[184:185],v233 offset:34304
TR a[186:187],v233 offset:38400
MM v[116:119],a[252:255],v[216:219],v[136:139]
EX v87,v87
MM v[116:119],a[248:251],v[200:203],v[116:119]
TR a[188:189],v233 offset:42496
CV v83,v86,v87
MM v[116:119],a[244:247],v[184:187],v[116:119]
EX v88,v88
MM v[116:119],a[240:243],v[168:171],v[116:119]
EX v89,v89
MM v[124:127],a[240:243],v[172:175],v[140:143]
TR a[190:191],v233 offset:46592
CV v88,v88,v89
MM v[124:127],a[244:247],v[188:191],v[124:127]
EX v90,v90
MM v[124:127],a[248:251],v[204:207],v[124:127]
EX v91,v91
MM v[124:127],a[252:255],v[220:223],v[124:127]
EX v92,v92
MM v[120:123],a[236:239],v[220:223],v[140:143]
EX v93,v93
MM v[120:123],a[232:235],v[204:207],v[120:123]
EX v94,v94
MM v[120:123],a[228:231],v[188:191],v[120:123]
EX v95,v95
MM v[120:123],a[224:227],v[172:175],v[120:123]
s_waitcnt vmcnt(15) lgkmcnt(0)
s_barrier
MM a[0:3],a[128:131],v[0:3],a[0:3]
s_add_u32 m0, s65, 32768
CV v89,v90,v91
CV v90,v92,v93
MM a[0:3],a[132:135],v[4:7],a[0:3]
RD a[192:195],v224 offset:0
CV v91,v94,v95
MM a[8:11],a[132:135],v[12:15],a[8:11]
buffer_load_dwordx4 v228, s[8:11], s64 offen offset:0 lds
MM a[8:11],a[128:131],v[8:11],a[8:11]
RD a[196:199],v225 offset:0
RD a[200:203],v226 offset:0
MM a[16:19],a[128:131],v[16:19],a[16:19]
EX v96,v96
MM a[16:19],a[132:135],v[20:23],a[16:19]
EX v97,v97
MM a[24:27],a[132:135],v[28:31],a[24:27]
)ASM"
R"ASM(
RD a[204:207],v227 offset:0
CV v68,v96,v97
MM a[24:27],a[128:131],v[24:27],a[24:27]
EX v98,v98
MM a[28:31],a[136:139],v[24:27],a[28:31]
buffer_load_dwordx4 v229, s[8:11], s64 offen offset:1024 lds
MM a[28:31],a[140:143],v[28:31],a[28:31]
EX v99,v99
MM a[20:23],a[140:143],v[20:23],a[20:23]
RD a[208:211],v224 offset:4096
CV v69,v98,v99
MM a[20:23],a[136:139],v[16:19],a[20:23]
EX v100,v100
MM a[12:15],a[136:139],v[8:11],a[12:15]
EX v101,v101
MM a[12:15],a[140:143],v[12:15],a[12:15]
RD a[212:215],v225 offset:4096
CV v70,v100,v101
MM a[4:7],a[140:143],v[4:7],a[4:7]
EX v102,v102
MM a[4:7],a[136:139],v[0:3],a[4:7]
buffer_load_dwordx4 v230, s[8:11], s64 offen offset:2048 lds
MM a[32:35],a[144:147],v[0:3],a[32:35]
EX v103,v103
MM a[32:35],a[148:151],v[4:7],a[32:35]
RD a[216:219],v226 offset:4096
CV v71,v102,v103
MM a[40:43],a[148:151],v[12:15],a[40:43]
EX v104,v104
MM a[40:43],a[144:147],v[8:11],a[40:43]
EX v105,v105
MM a[48:51],a[144:147],v[16:19],a[48:51]
RD a[220:223],v227 offset:4096
CV v76,v104,v105
MM a[48:51],a[148:151],v[20:23],a[48:51]
EX v106,v106
MM a[56:59],a[148:151],v[28:31],a[56:59]
buffer_load_dwordx4 v231, s[8:11], s64 offen offset:3072 lds
MM a[56:59],a[144:147],v[24:27],a[56:59]
s_add_u32 m0, s65, 98304
EX v107,v107
MM a[60:63],a[152:155],v[24:27],a[60:63]
RD a[224:227],v224 offset:8192
CV v77,v106,v107
MM a[60:63],a[156:159],v[28:31],a[60:63]
EX v108,v108
MM a[52:55],a[156:159],v[20:23],a[52:55]
EX v109,v109
MM a[52:55],a[152:155],v[16:19],a[52:55]
RD a[228:231],v225 offset:8192
CV v78,v108,v109
MM a[44:47],a[152:155],v[8:11],a[44:47]
EX v110,v110
MM a[44:47],a[156:159],v[12:15],a[44:47]
buffer_load_dwordx4 v235, s[12:15], s81 offen offset:0 lds
MM a[36:39],a[156:159],v[4:7],a[36:39]
EX v111,v111
MM a[36:39],a[152:155],v[0:3],a[36:39]
RD a[232:235],v226 offset:8192
CV v79,v110,v111
MM a[64:67],a[160:163],v[0:3],a[64:67]
EX v112,v112
MM a[64:67],a[164:167],v[4:7],a[64:67]
EX v113,v113
MM a[72:75],a[164:167],v[12:15],a[72:75]
RD a[236:239],v227 offset:8192
CV v84,v112,v113
MM a[72:75],a[160:163],v[8:11],a[72:75]
buffer_load_dwordx4 v235, s[12:15], s64 offen offset:1024 lds
MM a[80:83],a[160:163],v[16:19],a[80:83]
EX v114,v114
MM a[80:83],a[164:167],v[20:23],a[80:83]
EX v115,v115
MM a[88:91],a[164:167],v[28:31],a[88:91]
RD a[240:243],v224 offset:12288
CV v85,v114,v115
MM a[88:91],a[160:163],v[24:27],a[88:91]
EX v116,v116
MM a[92:95],a[168:171],v[24:27],a[92:95]
EX v117,v117
MM a[92:95],a[172:175],v[28:31],a[92:95]
RD a[244:247],v225 offset:12288
CV v86,v116,v117
MM a[84:87],a[172:175],v[20:23],a[84:87]
buffer_load_dwordx4 v235, s[12:15], s81 offen offset:2048 lds
MM a[84:87],a[168:171],v[16:19],a[84:87]
EX v118,v118
MM a[76:79],a[168:171],v[8:11],a[76:79]
EX v119,v119
MM a[76:79],a[172:175],v[12:15],a[76:79]
RD a[248:251],v226 offset:12288
CV v87,v118,v119
MM a[68:71],a[172:175],v[4:7],a[68:71]
EX v120,v120
MM a[68:71],a[168:171],v[0:3],a[68:71]
EX v121,v121
MM a[96:99],a[176:179],v[0:3],a[96:99]
RD a[252:255],v227 offset:12288
CV v92,v120,v121
MM a[96:99],a[180:183],v[4:7],a[96:99]
buffer_load_dwordx4 v235, s[12:15], s64 offen offset:3072 lds
MM a[104:107],a[180:183],v[12:15],a[104:107]
EX v122,v122
MM a[104:107],a[176:179],v[8:11],a[104:107]
EX v123,v123
MM a[112:115],a[176:179],v[16:19],a[112:115]
EX v124,v124
MM a[112:115],a[180:183],v[20:23],a[112:115]
EX v125,v125
MM a[120:123],a[180:183],v[28:31],a[120:123]
EX v126,v126
MM a[120:123],a[176:179],v[24:27],a[120:123]
EX v127,v127
MM a[124:127],a[184:187],v[24:27],a[124:127]
v_add_f32 v244, v144, v145
v_add_f32 v245, v148, v149
MM a[124:127],a[188:191],v[28:31],a[124:127]
v_add_f32 v246, v152, v153
v_add_f32 v247, v156, v157
MM a[116:119],a[188:191],v[20:23],a[116:119]
CV v93,v122,v123
CV v94,v124,v125
MM a[116:119],a[184:187],v[16:19],a[116:119]
CV v95,v126,v127
MM a[108:111],a[184:187],v[8:11],a[108:111]
MM a[108:111],a[188:191],v[12:15],a[108:111]
MM a[100:103],a[188:191],v[4:7],a[100:103]
MM a[100:103],a[184:187],v[0:3],a[100:103]
v_smfmac_f32_16x16x64_bf16 v[144:147], v[236:239], v[64:71], v234
v_smfmac_f32_16x16x64_bf16 v[148:151], v[236:239], v[72:79], v234
v_smfmac_f32_16x16x64_bf16 v[152:155], v[236:239], v[80:87], v234
v_smfmac_f32_16x16x64_bf16 v[156:159], v[236:239], v[88:95], v234
s_waitcnt lgkmcnt(0)
MM v[0:3],a[192:195],v[160:163],v[128:131]
s_add_u32 s64, s64, 16384
TR a[128:129],v232 offset:49152
s_add_u32 s81, s64, 896
MM v[0:3],a[196:199],v[176:179],v[0:3]
TR a[130:131],v232 offset:53248
v_max3_f32 v249, v144, v145, v148
MM v[0:3],a[200:203],v[192:195],v[0:3]
TR a[132:133],v232 offset:57344
v_max3_f32 v249, v249, v149, v152
MM v[0:3],a[204:207],v[208:211],v[0:3]
TR a[134:135],v232 offset:61440
v_max3_f32 v249, v249, v153, v156
MM v[4:7],a[220:223],v[208:211],v[128:131]
TR a[136:137],v233 offset:49152
v_max_f32 v249, v249, v157
MM v[4:7],a[216:219],v[192:195],v[4:7]
TR a[138:139],v233 offset:53248
v_cmp_lt_f32 vcc, s71, v249
MM v[4:7],a[212:215],v[176:179],v[4:7]
TR a[140:141],v233 offset:57344
TR a[142:143],v233 offset:61440
MM v[4:7],a[208:211],v[160:163],v[4:7]
s_cbranch_vccnz LSITE31_%=
LBACK31_%=:
MM v[12:15],a[208:211],v[164:167],v[132:135]
s_waitcnt vmcnt(8)
v_cmp_ne_u32 vcc, s69, v252
s_cbranch_vccnz LRFY_%=
LRFYb_%=:
MM v[12:15],a[212:215],v[180:183],v[12:15]
v_lshrrev_b32 v109, 2, v248
v_lshrrev_b32 v110, 3, v248
MM v[12:15],a[216:219],v[196:199],v[12:15]
s_and_b32 s76, s17, 7
EX v0,v0
MM v[12:15],a[220:223],v[212:215],v[12:15]
s_mul_i32 s76, s76, 2560
EX v1,v1
MM v[8:11],a[204:207],v[212:215],v[132:135]
s_lshl_b32 s91, s90, 8
EX v2,v2
MM v[8:11],a[200:203],v[196:199],v[8:11]
s_add_u32 s76, s76, s91
TR a[144:145],v232 offset:49664
CV v0,v0,v1
MM v[8:11],a[196:199],v[180:183],v[8:11]
s_add_u32 s76, s76, 21659648
buffer_load_dword v96, v109, s[20:23], s76 offen offset:0 sc1
MM v[8:11],a[192:195],v[164:167],v[8:11]
s_add_u32 s76, s76, 20480
EX v3,v3
MM v[16:19],a[192:195],v[168:171],v[136:139]
buffer_load_dword v97, v109, s[20:23], s76 offen offset:0 sc1
MM v[16:19],a[196:199],v[184:187],v[16:19]
s_add_u32 s76, s76, 20480
EX v4,v4
MM v[16:19],a[200:203],v[200:203],v[16:19]
buffer_load_dword v98, v109, s[20:23], s76 offen offset:0 sc1
MM v[16:19],a[204:207],v[216:219],v[16:19]
s_add_u32 s76, s76, 20480
EX v5,v5
MM v[20:23],a[220:223],v[216:219],v[136:139]
buffer_load_dword v99, v109, s[20:23], s76 offen offset:0 sc1
MM v[20:23],a[216:219],v[200:203],v[20:23]
s_add_u32 s76, s76, 20480
TR a[146:147],v232 offset:53760
TR a[148:149],v232 offset:57856
MM v[20:23],a[212:215],v[184:187],v[20:23]
buffer_load_dword v100, v109, s[20:23], s76 offen offset:0 sc1
MM v[20:23],a[208:211],v[168:171],v[20:23]
s_add_u32 s76, s76, 20480
EX v6,v6
MM v[28:31],a[208:211],v[172:175],v[140:143]
buffer_load_dword v101, v109, s[20:23], s76 offen offset:0 sc1
MM v[28:31],a[212:215],v[188:191],v[28:31]
s_add_u32 s76, s76, 20480
EX v7,v7
MM v[28:31],a[216:219],v[204:207],v[28:31]
buffer_load_dword v102, v109, s[20:23], s76 offen offset:0 sc1
MM v[28:31],a[220:223],v[220:223],v[28:31]
s_add_u32 s76, s76, 20480
s_nop 0
LPG22_%=:
s_cbranch_execz LTR4_%=
TR a[150:151],v232 offset:61952
EX v8,v8
MM v[24:27],a[204:207],v[220:223],v[140:143]
buffer_load_dword v103, v109, s[20:23], s76 offen offset:0 sc1
MM v[24:27],a[200:203],v[204:207],v[24:27]
s_add_u32 s76, s76, 20480
TR a[152:153],v233 offset:49664
EX v9,v9
CV v1,v2,v3
MM v[24:27],a[196:199],v[188:191],v[24:27]
buffer_load_dword v104, v109, s[20:23], s76 offen offset:0 sc1
TR a[154:155],v233 offset:53760
MM v[24:27],a[192:195],v[172:175],v[24:27]
s_and_b32 s76, s19, 65535
EX v10,v10
MM v[32:35],a[224:227],v[160:163],v[128:131]
s_sub_u32 s76, 65535, s76
TR a[156:157],v233 offset:57856
CV v2,v4,v5
EX v11,v11
MM v[32:35],a[228:231],v[176:179],v[32:35]
s_mul_i32 s76, s76, 1280
TR a[158:159],v233 offset:61952
CV v3,v6,v7
MM v[32:35],a[232:235],v[192:195],v[32:35]
s_lshl_b32 s91, s90, 7
EX v12,v12
MM v[32:35],a[236:239],v[208:211],v[32:35]
s_add_u32 s76, s76, s91
TR a[160:161],v232 offset:50176
TR a[162:163],v232 offset:54272
MM v[36:39],a[252:255],v[208:211],v[128:131]
s_add_u32 s76, s76, 131072
buffer_load_ushort v108, v110, s[20:23], s76 offen sc1
EX v13,v13
MM v[36:39],a[248:251],v[192:195],v[36:39]
s_ashr_i32 s91, s19, 31
EX v14,v14
MM v[36:39],a[244:247],v[176:179],v[36:39]
s_not_b32 s91, s91
TR a[164:165],v232 offset:58368
CV v8,v8,v9
MM v[36:39],a[240:243],v[160:163],v[36:39]
s_or_b32 s91, s91, 0x80000000
EX v15,v15
MM v[44:47],a[240:243],v[164:167],v[132:135]
s_xor_b32 s19, s19, s91
TR a[166:167],v232 offset:62464
CV v9,v10,v11
MM v[44:47],a[244:247],v[180:183],v[44:47]
s_and_b32 s19, s19, 0xffff0000
v_mov_b32 v111, 0xbfb8aa3b
v_mul_f32 v111, s19, v111
MM v[44:47],a[248:251],v[196:199],v[44:47]
EX v111,v111
EX v16,v16
MM v[44:47],a[252:255],v[212:215],v[44:47]
TR a[168:169],v233 offset:50176
v_add_f32 v111, 1.0, v111
EX v17,v17
MM v[40:43],a[236:239],v[212:215],v[132:135]
v_rcp_f32 v111, v111
MM v[40:43],a[232:235],v[196:199],v[40:43]
TR a[170:171],v233 offset:54272
CV v111,v111,v111
v_and_b32 v111, 0xffff0000, v111
EX v18,v18
MM v[40:43],a[228:231],v[180:183],v[40:43]
TR a[172:173],v233 offset:58368
CV v10,v12,v13
MM v[40:43],a[224:227],v[164:167],v[40:43]
EX v19,v19
MM v[48:51],a[224:227],v[168:171],v[136:139]
TR a[174:175],v233 offset:62464
CV v11,v14,v15
MM v[48:51],a[228:231],v[184:187],v[48:51]
EX v20,v20
MM v[48:51],a[232:235],v[200:203],v[48:51]
TR a[176:177],v232 offset:50688
TR a[178:179],v232 offset:54784
MM v[48:51],a[236:239],v[216:219],v[48:51]
EX v21,v21
MM v[52:55],a[252:255],v[216:219],v[136:139]
EX v22,v22
MM v[52:55],a[248:251],v[200:203],v[52:55]
TR a[180:181],v232 offset:58880
CV v16,v16,v17
MM v[52:55],a[244:247],v[184:187],v[52:55]
EX v23,v23
MM v[52:55],a[240:243],v[168:171],v[52:55]
TR a[182:183],v232 offset:62976
CV v17,v18,v19
MM v[60:63],a[240:243],v[172:175],v[140:143]
EX v24,v24
MM v[60:63],a[244:247],v[188:191],v[60:63]
TR a[184:185],v233 offset:50688
TR a[186:187],v233 offset:54784
MM v[60:63],a[248:251],v[204:207],v[60:63]
TR a[188:189],v233 offset:58880
TR a[190:191],v233 offset:62976
EX v25,v25
MM v[60:63],a[252:255],v[220:223],v[60:63]
EX v26,v26
MM v[56:59],a[236:239],v[220:223],v[140:143]
EX v27,v27
MM v[56:59],a[232:235],v[204:207],v[56:59]
EX v28,v28
MM v[56:59],a[228:231],v[188:191],v[56:59]
EX v29,v29
CV v18,v20,v21
MM v[56:59],a[224:227],v[172:175],v[56:59]
s_waitcnt lgkmcnt(0)
s_barrier
MM a[0:3],a[128:131],v[64:67],a[0:3]
s_add_u32 m0, s65, 49152
CV v19,v22,v23
CV v24,v24,v25
MM a[0:3],a[132:135],v[68:71],a[0:3]
CV v25,v26,v27
CV v26,v28,v29
MM a[8:11],a[132:135],v[76:79],a[8:11]
EX v30,v30
MM a[8:11],a[128:131],v[72:75],a[8:11]
EX v31,v31
MM a[16:19],a[128:131],v[80:83],a[16:19]
RD a[192:195],v224 offset:16384
CV v27,v30,v31
MM a[16:19],a[132:135],v[84:87],a[16:19]
EX v32,v32
MM a[24:27],a[132:135],v[92:95],a[24:27]
buffer_load_dwordx4 v228, s[8:11], s64 offen offset:0 lds
MM a[24:27],a[128:131],v[88:91],a[24:27]
EX v33,v33
MM a[28:31],a[136:139],v[88:91],a[28:31]
RD a[196:199],v225 offset:16384
CV v4,v32,v33
MM a[28:31],a[140:143],v[92:95],a[28:31]
EX v34,v34
MM a[20:23],a[140:143],v[84:87],a[20:23]
EX v35,v35
MM a[20:23],a[136:139],v[80:83],a[20:23]
RD a[200:203],v226 offset:16384
CV v5,v34,v35
MM a[12:15],a[136:139],v[72:75],a[12:15]
EX v36,v36
MM a[12:15],a[140:143],v[76:79],a[12:15]
buffer_load_dwordx4 v229, s[8:11], s64 offen offset:1024 lds
MM a[4:7],a[140:143],v[68:71],a[4:7]
EX v37,v37
MM a[4:7],a[136:139],v[64:67],a[4:7]
RD a[204:207],v227 offset:16384
CV v6,v36,v37
MM a[32:35],a[144:147],v[64:67],a[32:35]
EX v38,v38
MM a[32:35],a[148:151],v[68:71],a[32:35]
EX v39,v39
MM a[40:43],a[148:151],v[76:79],a[40:43]
RD a[208:211],v224 offset:20480
CV v7,v38,v39
MM a[40:43],a[144:147],v[72:75],a[40:43]
EX v40,v40
MM a[48:51],a[144:147],v[80:83],a[48:51]
buffer_load_dwordx4 v230, s[8:11], s64 offen offset:2048 lds
MM a[48:51],a[148:151],v[84:87],a[48:51]
EX v41,v41
MM a[56:59],a[148:151],v[92:95],a[56:59]
RD a[212:215],v225 offset:20480
CV v12,v40,v41
MM a[56:59],a[144:147],v[88:91],a[56:59]
EX v42,v42
MM a[60:63],a[152:155],v[88:91],a[60:63]
)ASM"
R"ASM(
EX v43,v43
MM a[60:63],a[156:159],v[92:95],a[60:63]
RD a[216:219],v226 offset:20480
CV v13,v42,v43
MM a[52:55],a[156:159],v[84:87],a[52:55]
EX v44,v44
MM a[52:55],a[152:155],v[80:83],a[52:55]
buffer_load_dwordx4 v231, s[8:11], s64 offen offset:3072 lds
MM a[44:47],a[152:155],v[72:75],a[44:47]
s_add_u32 m0, s65, 114688
EX v45,v45
MM a[44:47],a[156:159],v[76:79],a[44:47]
RD a[220:223],v227 offset:20480
CV v14,v44,v45
MM a[36:39],a[156:159],v[68:71],a[36:39]
EX v46,v46
MM a[36:39],a[152:155],v[64:67],a[36:39]
RD a[224:227],v224 offset:24576
RD a[228:231],v225 offset:24576
MM a[64:67],a[160:163],v[64:67],a[64:67]
EX v47,v47
MM a[64:67],a[164:167],v[68:71],a[64:67]
RD a[232:235],v226 offset:24576
CV v15,v46,v47
MM a[72:75],a[164:167],v[76:79],a[72:75]
EX v48,v48
MM a[72:75],a[160:163],v[72:75],a[72:75]
buffer_load_dwordx4 v235, s[12:15], s81 offen offset:0 lds
MM a[80:83],a[160:163],v[80:83],a[80:83]
EX v49,v49
MM a[80:83],a[164:167],v[84:87],a[80:83]
RD a[236:239],v227 offset:24576
CV v20,v48,v49
MM a[88:91],a[164:167],v[92:95],a[88:91]
EX v50,v50
MM a[88:91],a[160:163],v[88:91],a[88:91]
EX v51,v51
MM a[92:95],a[168:171],v[88:91],a[92:95]
RD a[240:243],v224 offset:28672
CV v21,v50,v51
MM a[92:95],a[172:175],v[92:95],a[92:95]
EX v52,v52
MM a[84:87],a[172:175],v[84:87],a[84:87]
buffer_load_dwordx4 v235, s[12:15], s64 offen offset:1024 lds
MM a[84:87],a[168:171],v[80:83],a[84:87]
EX v53,v53
MM a[76:79],a[168:171],v[72:75],a[76:79]
RD a[244:247],v225 offset:28672
CV v22,v52,v53
MM a[76:79],a[172:175],v[76:79],a[76:79]
EX v54,v54
MM a[68:71],a[172:175],v[68:71],a[68:71]
s_waitcnt vmcnt(6)
v_add_f32 v96, v96, v100
v_add_f32 v97, v97, v101
MM a[68:71],a[168:171],v[64:67],a[68:71]
v_add_f32 v98, v98, v102
v_add_f32 v99, v99, v103
MM a[96:99],a[176:179],v[64:67],a[96:99]
v_add_f32 v96, v96, v98
v_add_f32 v97, v97, v99
MM a[96:99],a[180:183],v[68:71],a[96:99]
v_add_f32 v96, v96, v104
v_lshlrev_b32 v108, 16, v108
MM a[104:107],a[180:183],v[76:79],a[104:107]
v_add_f32 v96, v96, v97
CV v96,v96,v96
EX v55,v55
MM a[104:107],a[176:179],v[72:75],a[104:107]
v_and_b32 v96, 0xffff0000, v96
v_mul_f32 v96, v96, v111
MM a[112:115],a[176:179],v[80:83],a[112:115]
CV v96,v96,v96
v_and_b32 v96, 0xffff0000, v96
EX v56,v56
MM a[112:115],a[180:183],v[84:87],a[112:115]
v_add_f32 v96, v96, v108
CV v96,v96,v96
MM a[120:123],a[180:183],v[92:95],a[120:123]
s_mov_b64 exec, s[44:45]
buffer_store_short v96, v110, s[20:23], s76 offen offset:0 sc1
s_mov_b64 exec, -1
s_nop 0
EX v57,v57
MM a[120:123],a[176:179],v[88:91],a[120:123]
EX v58,v58
MM a[124:127],a[184:187],v[88:91],a[124:127]
v_add_f32 v244, v144, v145
v_add_f32 v245, v148, v149
MM a[124:127],a[188:191],v[92:95],a[124:127]
v_add_f32 v246, v152, v153
v_add_f32 v247, v156, v157
MM a[116:119],a[188:191],v[84:87],a[116:119]
EX v59,v59
MM a[116:119],a[184:187],v[80:83],a[116:119]
EX v60,v60
MM a[108:111],a[184:187],v[72:75],a[108:111]
CV v23,v54,v55
CV v28,v56,v57
MM a[108:111],a[188:191],v[76:79],a[108:111]
EX v61,v61
MM a[100:103],a[188:191],v[68:71],a[100:103]
buffer_load_dwordx4 v235, s[12:15], s81 offen offset:2048 lds
MM a[100:103],a[184:187],v[64:67],a[100:103]
EX v62,v62
v_smfmac_f32_16x16x64_bf16 v[144:147], v[236:239], v[0:7], v234
RD a[248:251],v226 offset:28672
RD a[252:255],v227 offset:28672
EX v63,v63
v_smfmac_f32_16x16x64_bf16 v[148:151], v[236:239], v[8:15], v234
CV v29,v58,v59
v_smfmac_f32_16x16x64_bf16 v[152:155], v[236:239], v[16:23], v234
CV v30,v60,v61
CV v31,v62,v63
s_nop 1
v_smfmac_f32_16x16x64_bf16 v[156:159], v[236:239], v[24:31], v234
s_waitcnt lgkmcnt(0)
buffer_load_dwordx4 v235, s[12:15], s64 offen offset:3072 lds
MM v[64:67],a[192:195],v[160:163],v[128:131]
s_sub_u32 s64, s64, 507904
TR a[128:129],v232 offset:0
s_add_u32 s81, s64, 896
MM v[64:67],a[196:199],v[176:179],v[64:67]
TR a[130:131],v232 offset:4096
v_max3_f32 v249, v144, v145, v148
MM v[64:67],a[200:203],v[192:195],v[64:67]
TR a[132:133],v232 offset:8192
v_max3_f32 v249, v249, v149, v152
MM v[64:67],a[204:207],v[208:211],v[64:67]
TR a[134:135],v232 offset:12288
v_max3_f32 v249, v249, v153, v156
MM v[68:71],a[220:223],v[208:211],v[128:131]
TR a[136:137],v233 offset:0
v_max_f32 v249, v249, v157
MM v[68:71],a[216:219],v[192:195],v[68:71]
TR a[138:139],v233 offset:4096
v_cmp_lt_f32 vcc, s71, v249
MM v[68:71],a[212:215],v[176:179],v[68:71]
TR a[140:141],v233 offset:8192
TR a[142:143],v233 offset:12288
MM v[68:71],a[208:211],v[160:163],v[68:71]
s_cbranch_vccnz LSITE32_%=
LBACK32_%=:
MM v[76:79],a[208:211],v[164:167],v[132:135]
EX v64,v64
MM v[76:79],a[212:215],v[180:183],v[76:79]
EX v65,v65
MM v[76:79],a[216:219],v[196:199],v[76:79]
TR a[144:145],v232 offset:512
CV v64,v64,v65
MM v[76:79],a[220:223],v[212:215],v[76:79]
EX v66,v66
MM v[72:75],a[204:207],v[212:215],v[132:135]
EX v67,v67
MM v[72:75],a[200:203],v[196:199],v[72:75]
TR a[146:147],v232 offset:4608
CV v65,v66,v67
MM v[72:75],a[196:199],v[180:183],v[72:75]
EX v68,v68
MM v[72:75],a[192:195],v[164:167],v[72:75]
TR a[148:149],v232 offset:8704
TR a[150:151],v232 offset:12800
MM v[80:83],a[192:195],v[168:171],v[136:139]
EX v69,v69
MM v[80:83],a[196:199],v[184:187],v[80:83]
TR a[152:153],v233 offset:512
CV v66,v68,v69
MM v[80:83],a[200:203],v[200:203],v[80:83]
EX v70,v70
MM v[80:83],a[204:207],v[216:219],v[80:83]
TR a[154:155],v233 offset:4608
TR a[156:157],v233 offset:8704
MM v[84:87],a[220:223],v[216:219],v[136:139]
EX v71,v71
MM v[84:87],a[216:219],v[200:203],v[84:87]
TR a[158:159],v233 offset:12800
CV v67,v70,v71
MM v[84:87],a[212:215],v[184:187],v[84:87]
EX v72,v72
MM v[84:87],a[208:211],v[168:171],v[84:87]
EX v73,v73
MM v[92:95],a[208:211],v[172:175],v[140:143]
TR a[160:161],v232 offset:1024
CV v72,v72,v73
MM v[92:95],a[212:215],v[188:191],v[92:95]
EX v74,v74
MM v[92:95],a[216:219],v[204:207],v[92:95]
TR a[162:163],v232 offset:5120
TR a[164:165],v232 offset:9216
MM v[92:95],a[220:223],v[220:223],v[92:95]
EX v75,v75
MM v[88:91],a[204:207],v[220:223],v[140:143]
TR a[166:167],v232 offset:13312
CV v73,v74,v75
MM v[88:91],a[200:203],v[204:207],v[88:91]
EX v76,v76
MM v[88:91],a[196:199],v[188:191],v[88:91]
EX v77,v77
MM v[88:91],a[192:195],v[172:175],v[88:91]
TR a[168:169],v233 offset:1024
CV v74,v76,v77
MM v[96:99],a[224:227],v[160:163],v[128:131]
EX v78,v78
MM v[96:99],a[228:231],v[176:179],v[96:99]
TR a[170:171],v233 offset:5120
TR a[172:173],v233 offset:9216
MM v[96:99],a[232:235],v[192:195],v[96:99]
EX v79,v79
MM v[96:99],a[236:239],v[208:211],v[96:99]
TR a[174:175],v233 offset:13312
CV v75,v78,v79
MM v[100:103],a[252:255],v[208:211],v[128:131]
EX v80,v80
MM v[100:103],a[248:251],v[192:195],v[100:103]
EX v81,v81
MM v[100:103],a[244:247],v[176:179],v[100:103]
TR a[176:177],v232 offset:1536
CV v80,v80,v81
MM v[100:103],a[240:243],v[160:163],v[100:103]
EX v82,v82
MM v[108:111],a[240:243],v[164:167],v[132:135]
TR a[178:179],v232 offset:5632
TR a[180:181],v232 offset:9728
MM v[108:111],a[244:247],v[180:183],v[108:111]
EX v83,v83
MM v[108:111],a[248:251],v[196:199],v[108:111]
TR a[182:183],v232 offset:13824
CV v81,v82,v83
MM v[108:111],a[252:255],v[212:215],v[108:111]
EX v84,v84
MM v[104:107],a[236:239],v[212:215],v[132:135]
TR a[184:185],v233 offset:1536
TR a[186:187],v233 offset:5632
MM v[104:107],a[232:235],v[196:199],v[104:107]
EX v85,v85
MM v[104:107],a[228:231],v[180:183],v[104:107]
TR a[188:189],v233 offset:9728
CV v82,v84,v85
MM v[104:107],a[224:227],v[164:167],v[104:107]
EX v86,v86
MM v[112:115],a[224:227],v[168:171],v[136:139]
EX v87,v87
MM v[112:115],a[228:231],v[184:187],v[112:115]
TR a[190:191],v233 offset:13824
CV v83,v86,v87
MM v[112:115],a[232:235],v[200:203],v[112:115]
EX v88,v88
MM v[112:115],a[236:239],v[216:219],v[112:115]
EX v89,v89
MM v[116:119],a[252:255],v[216:219],v[136:139]
EX v90,v90
MM v[116:119],a[248:251],v[200:203],v[116:119]
EX v91,v91
MM v[116:119],a[244:247],v[184:187],v[116:119]
EX v92,v92
MM v[116:119],a[240:243],v[168:171],v[116:119]
EX v93,v93
MM v[124:127],a[240:243],v[172:175],v[140:143]
EX v94,v94
MM v[124:127],a[244:247],v[188:191],v[124:127]
EX v95,v95
MM v[124:127],a[248:251],v[204:207],v[124:127]
EX v96,v96
MM v[124:127],a[252:255],v[220:223],v[124:127]
EX v97,v97
MM v[120:123],a[236:239],v[220:223],v[140:143]
EX v98,v98
MM v[120:123],a[232:235],v[204:207],v[120:123]
EX v99,v99
MM v[120:123],a[228:231],v[188:191],v[120:123]
EX v100,v100
MM v[120:123],a[224:227],v[172:175],v[120:123]
s_waitcnt lgkmcnt(0)
s_barrier
MM a[0:3],a[128:131],v[0:3],a[0:3]
s_add_u32 m0, s65, 0
CV v88,v88,v89
CV v89,v90,v91
MM a[0:3],a[132:135],v[4:7],a[0:3]
RD a[192:195],v224 offset:32768
CV v90,v92,v93
MM a[8:11],a[132:135],v[12:15],a[8:11]
RD a[196:199],v225 offset:32768
CV v91,v94,v95
MM a[8:11],a[128:131],v[8:11],a[8:11]
buffer_load_dwordx4 v228, s[8:11], s64 offen offset:0 lds
MM a[16:19],a[128:131],v[16:19],a[16:19]
RD a[200:203],v226 offset:32768
CV v68,v96,v97
MM a[16:19],a[132:135],v[20:23],a[16:19]
RD a[204:207],v227 offset:32768
CV v69,v98,v99
MM a[24:27],a[132:135],v[28:31],a[24:27]
buffer_load_dwordx4 v229, s[8:11], s64 offen offset:1024 lds
MM a[24:27],a[128:131],v[24:27],a[24:27]
RD a[208:211],v224 offset:36864
RD a[212:215],v225 offset:36864
MM a[28:31],a[136:139],v[24:27],a[28:31]
buffer_load_dwordx4 v230, s[8:11], s64 offen offset:2048 lds
MM a[28:31],a[140:143],v[28:31],a[28:31]
RD a[216:219],v226 offset:36864
RD a[220:223],v227 offset:36864
MM a[20:23],a[140:143],v[20:23],a[20:23]
EX v101,v101
MM a[20:23],a[136:139],v[16:19],a[20:23]
RD a[224:227],v224 offset:40960
CV v70,v100,v101
MM a[12:15],a[136:139],v[8:11],a[12:15]
EX v102,v102
MM a[12:15],a[140:143],v[12:15],a[12:15]
buffer_load_dwordx4 v231, s[8:11], s64 offen offset:3072 lds
MM a[4:7],a[140:143],v[4:7],a[4:7]
s_add_u32 m0, s65, 65536
EX v103,v103
MM a[4:7],a[136:139],v[0:3],a[4:7]
RD a[228:231],v225 offset:40960
CV v71,v102,v103
MM a[32:35],a[144:147],v[0:3],a[32:35]
EX v104,v104
MM a[32:35],a[148:151],v[4:7],a[32:35]
EX v105,v105
MM a[40:43],a[148:151],v[12:15],a[40:43]
RD a[232:235],v226 offset:40960
CV v76,v104,v105
MM a[40:43],a[144:147],v[8:11],a[40:43]
EX v106,v106
MM a[48:51],a[144:147],v[16:19],a[48:51]
buffer_load_dwordx4 v235, s[12:15], s81 offen offset:0 lds
MM a[48:51],a[148:151],v[20:23],a[48:51]
EX v107,v107
MM a[56:59],a[148:151],v[28:31],a[56:59]
RD a[236:239],v227 offset:40960
CV v77,v106,v107
MM a[56:59],a[144:147],v[24:27],a[56:59]
EX v108,v108
MM a[60:63],a[152:155],v[24:27],a[60:63]
EX v109,v109
MM a[60:63],a[156:159],v[28:31],a[60:63]
RD a[240:243],v224 offset:45056
CV v78,v108,v109
MM a[52:55],a[156:159],v[20:23],a[52:55]
EX v110,v110
MM a[52:55],a[152:155],v[16:19],a[52:55]
buffer_load_dwordx4 v235, s[12:15], s64 offen offset:1024 lds
MM a[44:47],a[152:155],v[8:11],a[44:47]
EX v111,v111
MM a[44:47],a[156:159],v[12:15],a[44:47]
RD a[244:247],v225 offset:45056
CV v79,v110,v111
MM a[36:39],a[156:159],v[4:7],a[36:39]
EX v112,v112
MM a[36:39],a[152:155],v[0:3],a[36:39]
EX v113,v113
MM a[64:67],a[160:163],v[0:3],a[64:67]
RD a[248:251],v226 offset:45056
CV v84,v112,v113
MM a[64:67],a[164:167],v[4:7],a[64:67]
EX v114,v114
MM a[72:75],a[164:167],v[12:15],a[72:75]
buffer_load_dwordx4 v235, s[12:15], s81 offen offset:2048 lds
MM a[72:75],a[160:163],v[8:11],a[72:75]
EX v115,v115
MM a[80:83],a[160:163],v[16:19],a[80:83]
RD a[252:255],v227 offset:45056
CV v85,v114,v115
MM a[80:83],a[164:167],v[20:23],a[80:83]
EX v116,v116
MM a[88:91],a[164:167],v[28:31],a[88:91]
EX v117,v117
MM a[88:91],a[160:163],v[24:27],a[88:91]
buffer_load_dwordx4 v235, s[12:15], s64 offen offset:3072 lds
MM a[92:95],a[168:171],v[24:27],a[92:95]
EX v118,v118
MM a[92:95],a[172:175],v[28:31],a[92:95]
EX v119,v119
MM a[84:87],a[172:175],v[20:23],a[84:87]
EX v120,v120
MM a[84:87],a[168:171],v[16:19],a[84:87]
EX v121,v121
MM a[76:79],a[168:171],v[8:11],a[76:79]
EX v122,v122
MM a[76:79],a[172:175],v[12:15],a[76:79]
EX v123,v123
MM a[68:71],a[172:175],v[4:7],a[68:71]
EX v124,v124
MM a[68:71],a[168:171],v[0:3],a[68:71]
EX v125,v125
MM a[96:99],a[176:179],v[0:3],a[96:99]
EX v126,v126
MM a[96:99],a[180:183],v[4:7],a[96:99]
EX v127,v127
MM a[104:107],a[180:183],v[12:15],a[104:107]
)ASM"
R"ASM(
CV v86,v116,v117
CV v87,v118,v119
MM a[104:107],a[176:179],v[8:11],a[104:107]
CV v92,v120,v121
CV v93,v122,v123
MM a[112:115],a[176:179],v[16:19],a[112:115]
CV v94,v124,v125
CV v95,v126,v127
MM a[112:115],a[180:183],v[20:23],a[112:115]
MM a[120:123],a[180:183],v[28:31],a[120:123]
s_lshl_b32 s91, s17, 2
s_add_u32 s91, s91, 94464
s_and_b64 s[44:45], s[44:45], 1
MM a[120:123],a[176:179],v[24:27],a[120:123]
s_waitcnt vmcnt(10)
v_mov_b32 v250, s69
s_mov_b64 exec, s[44:45]
buffer_store_dword v250, off, s[20:23], s91 sc1
s_mov_b64 exec, -1
MM a[124:127],a[184:187],v[24:27],a[124:127]
v_add_f32 v244, v144, v145
v_add_f32 v245, v148, v149
MM a[124:127],a[188:191],v[28:31],a[124:127]
v_add_f32 v246, v152, v153
v_add_f32 v247, v156, v157
MM a[116:119],a[188:191],v[20:23],a[116:119]
MM a[116:119],a[184:187],v[16:19],a[116:119]
MM a[108:111],a[184:187],v[8:11],a[108:111]
MM a[108:111],a[188:191],v[12:15],a[108:111]
MM a[100:103],a[188:191],v[4:7],a[100:103]
MM a[100:103],a[184:187],v[0:3],a[100:103]
v_smfmac_f32_16x16x64_bf16 v[144:147], v[236:239], v[64:71], v234
v_smfmac_f32_16x16x64_bf16 v[148:151], v[236:239], v[72:79], v234
s_nop 0
LPG23_%=:
s_cbranch_execz LPG27_%=
v_smfmac_f32_16x16x64_bf16 v[152:155], v[236:239], v[80:87], v234
v_smfmac_f32_16x16x64_bf16 v[156:159], v[236:239], v[88:95], v234
s_waitcnt lgkmcnt(0)
LLAJ_%=:
MM v[0:3],a[192:195],v[160:163],v[128:131]
s_add_u32 s64, s64, 16384
TR a[128:129],v232 offset:16384
s_add_u32 s81, s64, 896
MM v[0:3],a[196:199],v[176:179],v[0:3]
TR a[130:131],v232 offset:20480
v_max3_f32 v249, v144, v145, v148
MM v[0:3],a[200:203],v[192:195],v[0:3]
TR a[132:133],v232 offset:24576
v_max3_f32 v249, v249, v149, v152
MM v[0:3],a[204:207],v[208:211],v[0:3]
TR a[134:135],v232 offset:28672
v_max3_f32 v249, v249, v153, v156
MM v[4:7],a[220:223],v[208:211],v[128:131]
TR a[136:137],v233 offset:16384
v_max_f32 v249, v249, v157
MM v[4:7],a[216:219],v[192:195],v[4:7]
TR a[138:139],v233 offset:20480
v_cmp_lt_f32 vcc, s71, v249
MM v[4:7],a[212:215],v[176:179],v[4:7]
TR a[140:141],v233 offset:24576
TR a[142:143],v233 offset:28672
MM v[4:7],a[208:211],v[160:163],v[4:7]
s_cbranch_vccnz LSITE33_%=
LBACK33_%=:
MM v[12:15],a[208:211],v[164:167],v[132:135]
v_bfe_u32 v242, v248, 6, 4
v_mul_u32_u24 v242, 1280, v242
MM v[12:15],a[212:215],v[180:183],v[12:15]
v_and_b32 v252, 48, v248
v_bfe_u32 v243, v248, 8, 1
MM v[12:15],a[216:219],v[196:199],v[12:15]
v_lshlrev_b32 v243, 5, v243
v_xor_b32 v252, v252, v243
MM v[12:15],a[220:223],v[212:215],v[12:15]
v_add_u32 v252, v252, v242
v_lshrrev_b32 v242, 3, v248
MM v[8:11],a[204:207],v[212:215],v[132:135]
v_xor_b32 v243, v242, v248
v_and_b32 v243, 96, v243
MM v[8:11],a[200:203],v[196:199],v[8:11]
TR a[144:145],v232 offset:16896
v_and_or_b32 v243, v248, 16, v243
MM v[8:11],a[196:199],v[180:183],v[8:11]
s_and_b32 s66, s49, 8
EX v0,v0
MM v[8:11],a[192:195],v[164:167],v[8:11]
s_lshl_b32 s66, s66, 3
v_lshrrev_b32 v242, 7, v248
v_xor_b32 v243, s66, v243
EX v1,v1
MM v[16:19],a[192:195],v[168:171],v[136:139]
v_mul_u32_u24 v242, 1280, v242
v_add_u32 v240, v242, v243
EX v2,v2
MM v[16:19],a[196:199],v[184:187],v[16:19]
s_mul_i32 s88, %[swave], 40960
EX v3,v3
MM v[16:19],a[200:203],v[200:203],v[16:19]
s_and_b32 s66, s84, 1
EX v4,v4
MM v[16:19],a[204:207],v[216:219],v[16:19]
s_lshl_b32 s86, s66, 6
TR a[146:147],v232 offset:20992
CV v0,v0,v1
MM v[20:23],a[220:223],v[216:219],v[136:139]
s_add_u32 s86, s86, s88
EX v5,v5
MM v[20:23],a[216:219],v[200:203],v[20:23]
s_xor_b32 s66, s66, 1
TR a[148:149],v232 offset:25088
CV v1,v2,v3
MM v[20:23],a[212:215],v[184:187],v[20:23]
s_lshl_b32 s66, s66, 8
EX v6,v6
MM v[20:23],a[208:211],v[168:171],v[20:23]
s_add_u32 s88, s88, s66
EX v7,v7
MM v[28:31],a[208:211],v[172:175],v[140:143]
s_and_b32 s66, s49, 16
TR a[150:151],v232 offset:29184
TR a[152:153],v233 offset:16896
MM v[28:31],a[212:215],v[188:191],v[28:31]
s_lshl_b32 s66, s66, 3
EX v8,v8
MM v[28:31],a[216:219],v[204:207],v[28:31]
s_sub_u32 s87, s86, s66
TR a[154:155],v233 offset:20992
CV v2,v4,v5
MM v[28:31],a[220:223],v[220:223],v[28:31]
s_add_u32 s87, s87, 128
EX v9,v9
MM v[24:27],a[204:207],v[220:223],v[140:143]
s_add_u32 s86, s86, s66
TR a[156:157],v233 offset:25088
CV v3,v6,v7
MM v[24:27],a[200:203],v[204:207],v[24:27]
s_and_b32 s94, s49, 7
EX v10,v10
MM v[24:27],a[196:199],v[188:191],v[24:27]
s_lshl_b32 s89, %[swave], 11
TR a[158:159],v233 offset:29184
CV v8,v8,v9
MM v[24:27],a[192:195],v[172:175],v[24:27]
s_add_u32 s89, s89, %[slds]
EX v11,v11
MM v[32:35],a[224:227],v[160:163],v[128:131]
s_xor_b32 s66, s94, 4
TR a[160:161],v232 offset:17408
TR a[162:163],v232 offset:21504
MM v[32:35],a[228:231],v[176:179],v[32:35]
s_mul_i32 s66, s66, 163840
EX v12,v12
MM v[32:35],a[232:235],v[192:195],v[32:35]
s_add_u32 s66, s66, s88
s_add_u32 m0, s89, 131072
s_nop 0
buffer_load_dwordx4 v252, s[24:27], s66 offen offset:0 lds
MM v[32:35],a[236:239],v[208:211],v[32:35]
s_add_u32 s66, s66, 19456
EX v13,v13
MM v[36:39],a[252:255],v[208:211],v[128:131]
TR a[164:165],v232 offset:25600
CV v9,v10,v11
MM v[36:39],a[248:251],v[192:195],v[36:39]
EX v14,v14
MM v[36:39],a[244:247],v[176:179],v[36:39]
EX v15,v15
MM v[36:39],a[240:243],v[160:163],v[36:39]
TR a[166:167],v232 offset:29696
TR a[168:169],v233 offset:17408
MM v[44:47],a[240:243],v[164:167],v[132:135]
EX v16,v16
MM v[44:47],a[244:247],v[180:183],v[44:47]
TR a[170:171],v233 offset:21504
CV v10,v12,v13
MM v[44:47],a[248:251],v[196:199],v[44:47]
EX v17,v17
MM v[44:47],a[252:255],v[212:215],v[44:47]
s_add_u32 m0, s89, 131072
s_nop 0
buffer_load_dwordx4 v252, s[24:27], s66 offen offset:1024 lds
MM v[40:43],a[236:239],v[212:215],v[132:135]
EX v18,v18
MM v[40:43],a[232:235],v[196:199],v[40:43]
TR a[172:173],v233 offset:25600
CV v11,v14,v15
MM v[40:43],a[228:231],v[180:183],v[40:43]
EX v19,v19
MM v[40:43],a[224:227],v[164:167],v[40:43]
TR a[174:175],v233 offset:29696
TR a[176:177],v232 offset:17920
MM v[48:51],a[224:227],v[168:171],v[136:139]
EX v20,v20
MM v[48:51],a[228:231],v[184:187],v[48:51]
TR a[178:179],v232 offset:22016
CV v16,v16,v17
MM v[48:51],a[232:235],v[200:203],v[48:51]
EX v21,v21
MM v[48:51],a[236:239],v[216:219],v[48:51]
s_lshl_b32 s60, s50, 2
TR a[180:181],v232 offset:26112
CV v17,v18,v19
MM v[52:55],a[252:255],v[216:219],v[136:139]
s_add_u32 s60, s60, %[swave]
EX v22,v22
MM v[52:55],a[248:251],v[200:203],v[52:55]
s_lshl_b32 s60, s60, 1
TR a[182:183],v232 offset:30208
TR a[184:185],v233 offset:17920
MM v[52:55],a[244:247],v[184:187],v[52:55]
s_bfe_u32 s61, s49, 65538
EX v23,v23
MM v[52:55],a[240:243],v[168:171],v[52:55]
s_xor_b32 s61, s61, 1
EX v24,v24
MM v[60:63],a[240:243],v[172:175],v[140:143]
s_add_u32 s60, s60, s61
TR a[186:187],v233 offset:22016
CV v18,v20,v21
MM v[60:63],a[244:247],v[188:191],v[60:63]
s_mul_i32 s18, s60, 40960
EX v25,v25
MM v[60:63],a[248:251],v[204:207],v[60:63]
s_mul_i32 s60, s84, 320
TR a[188:189],v233 offset:26112
TR a[190:191],v233 offset:30208
MM v[60:63],a[252:255],v[220:223],v[60:63]
s_add_u32 s18, s18, s60
EX v26,v26
MM v[56:59],a[236:239],v[220:223],v[140:143]
s_lshr_b32 s62, s49, 3
CV v19,v22,v23
CV v24,v24,v25
MM v[56:59],a[232:235],v[204:207],v[56:59]
s_mul_i32 s63, s62, 10240
EX v27,v27
MM v[56:59],a[228:231],v[188:191],v[56:59]
s_add_u32 s63, s63, s18
EX v28,v28
MM v[56:59],a[224:227],v[172:175],v[56:59]
s_waitcnt vmcnt(14) lgkmcnt(0)
s_barrier
MM a[0:3],a[128:131],v[64:67],a[0:3]
s_and_b32 s60, s84, 1
EX v29,v29
MM a[0:3],a[132:135],v[68:71],a[0:3]
s_add_u32 s61, s60, s62
CV v25,v26,v27
CV v26,v28,v29
MM a[8:11],a[132:135],v[76:79],a[8:11]
s_lshl_b32 s61, s61, 6
EX v30,v30
MM a[8:11],a[128:131],v[72:75],a[8:11]
s_add_u32 s18, s18, s61
EX v31,v31
MM a[16:19],a[128:131],v[80:83],a[16:19]
s_xor_b32 s60, s60, 1
RD a[192:195],v224 offset:49152
CV v27,v30,v31
MM a[16:19],a[132:135],v[84:87],a[16:19]
s_lshl_b32 s60, s60, 8
EX v32,v32
MM a[24:27],a[132:135],v[92:95],a[24:27]
s_add_u32 s63, s63, s60
s_add_u32 m0, s65, 16384
RD a[196:199],v225 offset:49152
MM a[24:27],a[128:131],v[88:91],a[24:27]
s_mul_i32 s60, s62, 33792
EX v33,v33
MM a[28:31],a[136:139],v[88:91],a[28:31]
s_sub_u32 s60, s95, s60
buffer_load_dwordx4 v228, s[8:11], s64 offen offset:0 lds
MM a[28:31],a[140:143],v[92:95],a[28:31]
s_xor_b32 s61, s62, 1
EX v34,v34
MM a[20:23],a[140:143],v[84:87],a[20:23]
s_mul_i32 s61, s61, 33792
EX v35,v35
MM a[20:23],a[136:139],v[80:83],a[20:23]
s_add_u32 s61, s61, s60
RD a[200:203],v226 offset:49152
CV v4,v32,v33
MM a[12:15],a[136:139],v[72:75],a[12:15]
s_add_u32 s36, s61, 2048
EX v36,v36
MM a[12:15],a[140:143],v[76:79],a[12:15]
s_xor_b32 s61, s62, 2
CV v5,v34,v35
MM a[4:7],a[140:143],v[68:71],a[4:7]
s_mul_i32 s61, s61, 33792
EX v37,v37
MM a[4:7],a[136:139],v[64:67],a[4:7]
s_add_u32 s61, s61, s60
buffer_load_dwordx4 v229, s[8:11], s64 offen offset:1024 lds
MM a[32:35],a[144:147],v[64:67],a[32:35]
s_add_u32 s37, s61, 4096
EX v38,v38
MM a[32:35],a[148:151],v[68:71],a[32:35]
s_xor_b32 s61, s62, 3
RD a[204:207],v227 offset:49152
CV v6,v36,v37
MM a[40:43],a[148:151],v[76:79],a[40:43]
s_mul_i32 s61, s61, 33792
EX v39,v39
MM a[40:43],a[144:147],v[72:75],a[40:43]
s_add_u32 s61, s61, s60
RD a[208:211],v224 offset:53248
CV v7,v38,v39
MM a[48:51],a[144:147],v[80:83],a[48:51]
s_add_u32 s38, s61, 6144
EX v40,v40
MM a[48:51],a[148:151],v[84:87],a[48:51]
s_lshl_b32 s61, s62, 9
EX v41,v41
MM a[56:59],a[148:151],v[92:95],a[56:59]
s_add_u32 s39, s60, s61
RD a[212:215],v225 offset:53248
MM a[56:59],a[144:147],v[88:91],a[56:59]
s_mov_b32 s17, 15
buffer_load_dwordx4 v230, s[8:11], s64 offen offset:2048 lds
MM a[60:63],a[152:155],v[88:91],a[60:63]
s_mov_b32 s58, 0
EX v42,v42
MM a[60:63],a[156:159],v[92:95],a[60:63]
s_lshl_b32 s60, s50, 1
EX v43,v43
MM a[52:55],a[156:159],v[84:87],a[52:55]
s_bfe_u32 s61, s49, 65538
RD a[216:219],v226 offset:53248
CV v12,v40,v41
MM a[52:55],a[152:155],v[80:83],a[52:55]
s_add_u32 s60, s60, s61
EX v44,v44
MM a[44:47],a[152:155],v[72:75],a[44:47]
s_lshl_b32 s60, s60, 2
CV v13,v42,v43
MM a[44:47],a[156:159],v[76:79],a[44:47]
s_add_u32 s60, s60, s84
EX v45,v45
MM a[36:39],a[156:159],v[68:71],a[36:39]
s_lshl_b32 s60, s60, 2
buffer_load_dwordx4 v231, s[8:11], s64 offen offset:3072 lds
MM a[36:39],a[152:155],v[64:67],a[36:39]
s_add_u32 s68, s60, %[swave]
EX v46,v46
MM a[64:67],a[160:163],v[64:67],a[64:67]
s_lshl_b32 s68, s68, 6
RD a[220:223],v227 offset:53248
CV v14,v44,v45
MM a[64:67],a[164:167],v[68:71],a[64:67]
s_add_u32 s68, s68, 16384
EX v47,v47
MM a[72:75],a[164:167],v[76:79],a[72:75]
s_lshr_b32 s61, s49, 3
EX v48,v48
MM a[72:75],a[160:163],v[72:75],a[72:75]
s_lshl_b32 s19, s61, 4
RD a[224:227],v224 offset:57344
CV v15,v46,v47
MM a[80:83],a[160:163],v[80:83],a[80:83]
s_add_u32 s19, s19, s68
EX v49,v49
MM a[80:83],a[164:167],v[84:87],a[80:83]
s_lshr_b32 s96, s49, 2
s_add_u32 m0, s65, 81920
CV v20,v48,v49
MM a[88:91],a[164:167],v[92:95],a[88:91]
s_lshl_b32 s97, s50, 3
EX v50,v50
MM a[88:91],a[160:163],v[88:91],a[88:91]
s_add_u32 s96, s96, s97
buffer_load_dwordx4 v235, s[12:15], s81 offen offset:0 lds
MM a[92:95],a[168:171],v[88:91],a[92:95]
s_lshl_b32 s96, s96, 2
EX v51,v51
MM a[92:95],a[172:175],v[92:95],a[92:95]
s_add_u32 s96, s96, %[swave]
RD a[228:231],v225 offset:57344
CV v21,v50,v51
MM a[84:87],a[172:175],v[84:87],a[84:87]
s_lshl_b32 s96, s96, 6
EX v52,v52
MM a[84:87],a[168:171],v[80:83],a[84:87]
s_add_u32 s96, s96, 0
RD a[232:235],v226 offset:57344
MM a[76:79],a[168:171],v[72:75],a[76:79]
s_xor_b32 s44, s96, 256
EX v53,v53
MM a[76:79],a[172:175],v[76:79],a[76:79]
s_xor_b32 s66, s94, 5
RD a[236:239],v227 offset:57344
CV v22,v52,v53
MM a[68:71],a[172:175],v[68:71],a[68:71]
s_mul_i32 s66, s66, 163840
EX v54,v54
MM a[68:71],a[168:171],v[64:67],a[68:71]
s_add_u32 s66, s66, s88
s_add_u32 m0, s89, 139264
s_nop 0
buffer_load_dwordx4 v252, s[24:27], s66 offen offset:0 lds
MM a[96:99],a[176:179],v[64:67],a[96:99]
s_add_u32 s66, s66, 19456
EX v55,v55
MM a[96:99],a[180:183],v[68:71],a[96:99]
s_add_u32 m0, s65, 81920
s_nop 0
buffer_load_dwordx4 v235, s[12:15], s64 offen offset:1024 lds
MM a[104:107],a[180:183],v[76:79],a[104:107]
EX v56,v56
MM a[104:107],a[176:179],v[72:75],a[104:107]
)ASM"
R"ASM(
RD a[240:243],v224 offset:61440
CV v23,v54,v55
MM a[112:115],a[176:179],v[80:83],a[112:115]
EX v57,v57
MM a[112:115],a[180:183],v[84:87],a[112:115]
EX v58,v58
MM a[120:123],a[180:183],v[92:95],a[120:123]
RD a[244:247],v225 offset:61440
CV v28,v56,v57
MM a[120:123],a[176:179],v[88:91],a[120:123]
EX v59,v59
MM a[124:127],a[184:187],v[88:91],a[124:127]
v_add_f32 v244, v144, v145
v_add_f32 v245, v148, v149
MM a[124:127],a[188:191],v[92:95],a[124:127]
v_add_f32 v246, v152, v153
v_add_f32 v247, v156, v157
MM a[116:119],a[188:191],v[84:87],a[116:119]
s_add_u32 m0, s89, 139264
s_nop 0
buffer_load_dwordx4 v252, s[24:27], s66 offen offset:1024 lds
MM a[116:119],a[184:187],v[80:83],a[116:119]
EX v60,v60
MM a[108:111],a[184:187],v[72:75],a[108:111]
EX v61,v61
MM a[108:111],a[188:191],v[76:79],a[108:111]
s_add_u32 m0, s65, 81920
s_nop 0
buffer_load_dwordx4 v235, s[12:15], s81 offen offset:2048 lds
MM a[100:103],a[188:191],v[68:71],a[100:103]
EX v62,v62
MM a[100:103],a[184:187],v[64:67],a[100:103]
RD a[248:251],v226 offset:61440
CV v29,v58,v59
v_smfmac_f32_16x16x64_bf16 v[144:147], v[236:239], v[0:7], v234
RD a[252:255],v227 offset:61440
EX v63,v63
v_smfmac_f32_16x16x64_bf16 v[148:151], v[236:239], v[8:15], v234
CV v30,v60,v61
v_smfmac_f32_16x16x64_bf16 v[152:155], v[236:239], v[16:23], v234
CV v31,v62,v63
s_nop 1
v_smfmac_f32_16x16x64_bf16 v[156:159], v[236:239], v[24:31], v234
s_waitcnt lgkmcnt(0)
buffer_load_dwordx4 v235, s[12:15], s64 offen offset:3072 lds
MM v[64:67],a[192:195],v[160:163],v[128:131]
s_add_u32 s64, s64, 16384
TR a[128:129],v232 offset:32768
s_add_u32 s81, s64, 896
MM v[64:67],a[196:199],v[176:179],v[64:67]
TR a[130:131],v232 offset:36864
v_max3_f32 v249, v144, v145, v148
MM v[64:67],a[200:203],v[192:195],v[64:67]
TR a[132:133],v232 offset:40960
v_max3_f32 v249, v249, v149, v152
MM v[64:67],a[204:207],v[208:211],v[64:67]
TR a[134:135],v232 offset:45056
v_max3_f32 v249, v249, v153, v156
MM v[68:71],a[220:223],v[208:211],v[128:131]
TR a[136:137],v233 offset:32768
v_max_f32 v249, v249, v157
MM v[68:71],a[216:219],v[192:195],v[68:71]
TR a[138:139],v233 offset:36864
v_cmp_lt_f32 vcc, s71, v249
MM v[68:71],a[212:215],v[176:179],v[68:71]
TR a[140:141],v233 offset:40960
TR a[142:143],v233 offset:45056
MM v[68:71],a[208:211],v[160:163],v[68:71]
s_cbranch_vccnz LSITE34_%=
LBACK34_%=:
MM v[76:79],a[208:211],v[164:167],v[132:135]
buffer_load_dword v242, off, s[20:23], s96 sc1
MM v[76:79],a[212:215],v[180:183],v[76:79]
buffer_load_dword v243, off, s[20:23], s19 sc1
MM v[76:79],a[216:219],v[196:199],v[76:79]
s_xor_b32 s66, s94, 6
v_and_b32 v32, 496, v248
v_mul_u32_u24 v32, 80, v32
MM v[76:79],a[220:223],v[212:215],v[76:79]
s_mul_i32 s66, s66, 163840
EX v64,v64
MM v[72:75],a[204:207],v[212:215],v[132:135]
s_add_u32 s66, s66, s88
s_add_u32 m0, s89, 147456
s_nop 0
buffer_load_dwordx4 v252, s[24:27], s66 offen offset:0 lds
MM v[72:75],a[200:203],v[196:199],v[72:75]
s_add_u32 s66, s66, 19456
EX v65,v65
MM v[72:75],a[196:199],v[180:183],v[72:75]
s_sub_u32 s60, s63, s18
v_add_u32 v32, s18, v32
CV v64,v64,v65
MM v[72:75],a[192:195],v[164:167],v[72:75]
s_mov_b32 s61, 131072
s_mov_b32 exec_lo, 0
v_add_u32 v32, s60, v32
s_mov_b64 exec, -1
MM v[80:83],a[192:195],v[168:171],v[136:139]
s_mov_b32 exec_hi, 0xff
buffer_load_dword v36, v32, s[32:35], 0 offen offset:0
s_mov_b64 exec, -1
EX v66,v66
MM v[80:83],a[196:199],v[184:187],v[80:83]
EX v67,v67
MM v[80:83],a[200:203],v[200:203],v[80:83]
EX v68,v68
MM v[80:83],a[204:207],v[216:219],v[80:83]
s_mov_b32 exec_hi, 0xff
buffer_load_dword v37, v32, s[20:23], s61 offen offset:0 sc1
s_mov_b64 exec, -1
MM v[84:87],a[220:223],v[216:219],v[136:139]
TR a[144:145],v232 offset:33280
TR a[146:147],v232 offset:37376
MM v[84:87],a[216:219],v[200:203],v[84:87]
s_add_u32 m0, s89, 147456
s_nop 0
buffer_load_dwordx4 v252, s[24:27], s66 offen offset:1024 lds
EX v69,v69
MM v[84:87],a[212:215],v[184:187],v[84:87]
EX v70,v70
MM v[84:87],a[208:211],v[168:171],v[84:87]
TR a[148:149],v232 offset:41472
CV v65,v66,v67
MM v[92:95],a[208:211],v[172:175],v[140:143]
EX v71,v71
MM v[92:95],a[212:215],v[188:191],v[92:95]
TR a[150:151],v232 offset:45568
TR a[152:153],v233 offset:33280
MM v[92:95],a[216:219],v[204:207],v[92:95]
EX v72,v72
MM v[92:95],a[220:223],v[220:223],v[92:95]
EX v73,v73
MM v[88:91],a[204:207],v[220:223],v[140:143]
TR a[154:155],v233 offset:37376
CV v66,v68,v69
MM v[88:91],a[200:203],v[204:207],v[88:91]
s_xor_b32 s66, s94, 7
EX v74,v74
MM v[88:91],a[196:199],v[188:191],v[88:91]
s_mul_i32 s66, s66, 163840
TR a[156:157],v233 offset:41472
CV v67,v70,v71
MM v[88:91],a[192:195],v[172:175],v[88:91]
s_add_u32 s66, s66, s88
s_add_u32 m0, s89, 155648
s_nop 0
buffer_load_dwordx4 v252, s[24:27], s66 offen offset:0 lds
MM v[96:99],a[224:227],v[160:163],v[128:131]
s_add_u32 s66, s66, 19456
EX v75,v75
MM v[96:99],a[228:231],v[176:179],v[96:99]
TR a[158:159],v233 offset:45568
TR a[160:161],v232 offset:33792
MM v[96:99],a[232:235],v[192:195],v[96:99]
EX v76,v76
MM v[96:99],a[236:239],v[208:211],v[96:99]
EX v77,v77
MM v[100:103],a[252:255],v[208:211],v[128:131]
TR a[162:163],v232 offset:37888
CV v72,v72,v73
MM v[100:103],a[248:251],v[192:195],v[100:103]
EX v78,v78
MM v[100:103],a[244:247],v[176:179],v[100:103]
TR a[164:165],v232 offset:41984
CV v73,v74,v75
MM v[100:103],a[240:243],v[160:163],v[100:103]
EX v79,v79
MM v[108:111],a[240:243],v[164:167],v[132:135]
s_add_u32 m0, s89, 155648
s_nop 0
buffer_load_dwordx4 v252, s[24:27], s66 offen offset:1024 lds
MM v[108:111],a[244:247],v[180:183],v[108:111]
TR a[166:167],v232 offset:46080
TR a[168:169],v233 offset:33792
MM v[108:111],a[248:251],v[196:199],v[108:111]
EX v80,v80
MM v[108:111],a[252:255],v[212:215],v[108:111]
EX v81,v81
MM v[104:107],a[236:239],v[212:215],v[132:135]
TR a[170:171],v233 offset:37888
CV v74,v76,v77
MM v[104:107],a[232:235],v[196:199],v[104:107]
EX v82,v82
MM v[104:107],a[228:231],v[180:183],v[104:107]
TR a[172:173],v233 offset:41984
TR a[174:175],v233 offset:46080
MM v[104:107],a[224:227],v[164:167],v[104:107]
EX v83,v83
MM v[112:115],a[224:227],v[168:171],v[136:139]
TR a[176:177],v232 offset:34304
CV v75,v78,v79
MM v[112:115],a[228:231],v[184:187],v[112:115]
EX v84,v84
MM v[112:115],a[232:235],v[200:203],v[112:115]
EX v85,v85
MM v[112:115],a[236:239],v[216:219],v[112:115]
TR a[178:179],v232 offset:38400
CV v80,v80,v81
MM v[116:119],a[252:255],v[216:219],v[136:139]
EX v86,v86
MM v[116:119],a[248:251],v[200:203],v[116:119]
TR a[180:181],v232 offset:42496
TR a[182:183],v232 offset:46592
MM v[116:119],a[244:247],v[184:187],v[116:119]
EX v87,v87
MM v[116:119],a[240:243],v[168:171],v[116:119]
TR a[184:185],v233 offset:34304
CV v81,v82,v83
MM v[124:127],a[240:243],v[172:175],v[140:143]
EX v88,v88
MM v[124:127],a[244:247],v[188:191],v[124:127]
TR a[186:187],v233 offset:38400
CV v82,v84,v85
MM v[124:127],a[248:251],v[204:207],v[124:127]
TR a[188:189],v233 offset:42496
TR a[190:191],v233 offset:46592
EX v89,v89
MM v[124:127],a[252:255],v[220:223],v[124:127]
EX v90,v90
MM v[120:123],a[236:239],v[220:223],v[140:143]
CV v83,v86,v87
CV v88,v88,v89
MM v[120:123],a[232:235],v[204:207],v[120:123]
EX v91,v91
MM v[120:123],a[228:231],v[188:191],v[120:123]
EX v92,v92
MM v[120:123],a[224:227],v[172:175],v[120:123]
s_waitcnt vmcnt(24) lgkmcnt(0)
s_barrier
MM a[0:3],a[128:131],v[0:3],a[0:3]
s_add_u32 m0, s65, 32768
EX v93,v93
MM a[0:3],a[132:135],v[4:7],a[0:3]
CV v89,v90,v91
CV v90,v92,v93
MM a[8:11],a[132:135],v[12:15],a[8:11]
EX v94,v94
MM a[8:11],a[128:131],v[8:11],a[8:11]
EX v95,v95
MM a[16:19],a[128:131],v[16:19],a[16:19]
RD a[192:195],v224 offset:0
LPG24_%=:
s_cbranch_execz LPG28_%=
CV v91,v94,v95
MM a[16:19],a[132:135],v[20:23],a[16:19]
EX v96,v96
MM a[24:27],a[132:135],v[28:31],a[24:27]
buffer_load_dwordx4 v228, s[8:11], s64 offen offset:0 lds
MM a[24:27],a[128:131],v[24:27],a[24:27]
EX v97,v97
MM a[28:31],a[136:139],v[24:27],a[28:31]
RD a[196:199],v225 offset:0
CV v68,v96,v97
MM a[28:31],a[140:143],v[28:31],a[28:31]
EX v98,v98
MM a[20:23],a[140:143],v[20:23],a[20:23]
EX v99,v99
MM a[20:23],a[136:139],v[16:19],a[20:23]
TR a[128:129],v232 offset:49152
RD a[200:203],v226 offset:0
MM a[12:15],a[136:139],v[8:11],a[12:15]
EX v100,v100
MM a[12:15],a[140:143],v[12:15],a[12:15]
TR a[130:131],v232 offset:53248
CV v69,v98,v99
MM a[4:7],a[140:143],v[4:7],a[4:7]
EX v101,v101
MM a[4:7],a[136:139],v[0:3],a[4:7]
TR a[132:133],v232 offset:57344
RD a[204:207],v227 offset:0
MM a[32:35],a[144:147],v[0:3],a[32:35]
EX v102,v102
MM a[32:35],a[148:151],v[4:7],a[32:35]
buffer_load_dwordx4 v229, s[8:11], s64 offen offset:1024 lds
MM a[40:43],a[148:151],v[12:15],a[40:43]
TR a[134:135],v232 offset:61440
CV v70,v100,v101
MM a[40:43],a[144:147],v[8:11],a[40:43]
EX v103,v103
MM a[48:51],a[144:147],v[16:19],a[48:51]
TR a[136:137],v233 offset:49152
TR a[138:139],v233 offset:53248
MM a[48:51],a[148:151],v[20:23],a[48:51]
EX v104,v104
MM a[56:59],a[148:151],v[28:31],a[56:59]
EX v105,v105
MM a[56:59],a[144:147],v[24:27],a[56:59]
TR a[140:141],v233 offset:57344
CV v71,v102,v103
MM a[60:63],a[152:155],v[24:27],a[60:63]
EX v106,v106
MM a[60:63],a[156:159],v[28:31],a[60:63]
RD a[208:211],v224 offset:4096
TR a[142:143],v233 offset:61440
MM a[52:55],a[156:159],v[20:23],a[52:55]
EX v107,v107
MM a[52:55],a[152:155],v[16:19],a[52:55]
buffer_load_dwordx4 v230, s[8:11], s64 offen offset:2048 lds
MM a[44:47],a[152:155],v[8:11],a[44:47]
TR a[144:145],v232 offset:49664
RD a[212:215],v225 offset:4096
MM a[44:47],a[156:159],v[12:15],a[44:47]
EX v108,v108
MM a[36:39],a[156:159],v[4:7],a[36:39]
TR a[146:147],v232 offset:53760
CV v76,v104,v105
MM a[36:39],a[152:155],v[0:3],a[36:39]
EX v109,v109
MM a[64:67],a[160:163],v[0:3],a[64:67]
TR a[148:149],v232 offset:57856
CV v77,v106,v107
MM a[64:67],a[164:167],v[4:7],a[64:67]
EX v110,v110
MM a[72:75],a[164:167],v[12:15],a[72:75]
EX v111,v111
MM a[72:75],a[160:163],v[8:11],a[72:75]
TR a[150:151],v232 offset:61952
RD a[216:219],v226 offset:4096
MM a[80:83],a[160:163],v[16:19],a[80:83]
TR a[152:153],v233 offset:49664
CV v78,v108,v109
MM a[80:83],a[164:167],v[20:23],a[80:83]
EX v112,v112
MM a[88:91],a[164:167],v[28:31],a[88:91]
EX v113,v113
MM a[88:91],a[160:163],v[24:27],a[88:91]
TR a[154:155],v233 offset:53760
RD a[220:223],v227 offset:4096
MM a[92:95],a[168:171],v[24:27],a[92:95]
buffer_load_dwordx4 v231, s[8:11], s64 offen offset:3072 lds
MM a[92:95],a[172:175],v[28:31],a[92:95]
s_add_u32 m0, s65, 98304
EX v114,v114
MM a[84:87],a[172:175],v[20:23],a[84:87]
TR a[156:157],v233 offset:57856
TR a[158:159],v233 offset:61952
MM a[84:87],a[168:171],v[16:19],a[84:87]
EX v115,v115
MM a[76:79],a[168:171],v[8:11],a[76:79]
EX v116,v116
MM a[76:79],a[172:175],v[12:15],a[76:79]
TR a[160:161],v232 offset:50176
CV v79,v110,v111
MM a[68:71],a[172:175],v[4:7],a[68:71]
EX v117,v117
MM a[68:71],a[168:171],v[0:3],a[68:71]
TR a[162:163],v232 offset:54272
RD a[224:227],v224 offset:8192
MM a[96:99],a[176:179],v[0:3],a[96:99]
EX v118,v118
MM a[96:99],a[180:183],v[4:7],a[96:99]
TR a[164:165],v232 offset:58368
CV v84,v112,v113
MM a[104:107],a[180:183],v[12:15],a[104:107]
buffer_load_dwordx4 v235, s[12:15], s81 offen offset:0 lds
TR a[166:167],v232 offset:62464
EX v119,v119
MM a[104:107],a[176:179],v[8:11],a[104:107]
RD a[228:231],v225 offset:8192
TR a[168:169],v233 offset:50176
MM a[112:115],a[176:179],v[16:19],a[112:115]
EX v120,v120
MM a[112:115],a[180:183],v[20:23],a[112:115]
TR a[170:171],v233 offset:54272
CV v85,v114,v115
MM a[120:123],a[180:183],v[28:31],a[120:123]
s_waitcnt vmcnt(8)
EX v121,v121
MM a[120:123],a[176:179],v[24:27],a[120:123]
s_lshr_b32 s77, %[sb], 3
RD a[232:235],v226 offset:8192
TR a[172:173],v233 offset:58368
MM a[124:127],a[184:187],v[24:27],a[124:127]
s_lshl_b32 s77, s77, 2
v_add_f32 v244, v144, v145
v_add_f32 v245, v148, v149
EX v122,v122
MM a[124:127],a[188:191],v[28:31],a[124:127]
s_add_u32 s77, s77, %[swave]
v_add_f32 v246, v152, v153
v_add_f32 v247, v156, v157
CV v86,v116,v117
MM a[116:119],a[188:191],v[20:23],a[116:119]
s_lshl_b32 s77, s77, 9
TR a[174:175],v233 offset:62464
buffer_load_dwordx4 v235, s[12:15], s64 offen offset:1024 lds
)ASM"
R"ASM(
EX v123,v123
MM a[116:119],a[184:187],v[16:19],a[116:119]
s_add_u32 s77, s77, 0
TR a[176:177],v232 offset:50688
v_lshrrev_b32 v253, 1, v248
v_add_u32 v253, s77, v253
MM a[108:111],a[184:187],v[8:11],a[108:111]
s_mov_b32 s77, LKEND_ka-LPD2_%=-16
RD a[236:239],v227 offset:8192
v_min_u32 v253, s77, v253
EX v124,v124
CV v87,v118,v119
MM a[108:111],a[188:191],v[12:15],a[108:111]
s_getpc_b64 s[98:99]
LPD2_%=:
global_load_dword v253, v253, s[98:99]
TR a[178:179],v232 offset:54784
EX v125,v125
MM a[100:103],a[188:191],v[4:7],a[100:103]
TR a[180:181],v232 offset:58880
RD a[240:243],v224 offset:12288
MM a[100:103],a[184:187],v[0:3],a[100:103]
EX v126,v126
v_smfmac_f32_16x16x64_bf16 v[144:147], v[236:239], v[64:71], v234
RD a[244:247],v225 offset:12288
RD a[248:251],v226 offset:12288
RD a[252:255],v227 offset:12288
EX v127,v127
v_smfmac_f32_16x16x64_bf16 v[148:151], v[236:239], v[72:79], v234
TR a[182:183],v232 offset:62976
CV v92,v120,v121
v_smfmac_f32_16x16x64_bf16 v[152:155], v[236:239], v[80:87], v234
buffer_load_dwordx4 v235, s[12:15], s81 offen offset:2048 lds
CV v93,v122,v123
CV v94,v124,v125
CV v95,v126,v127
s_nop 1
v_smfmac_f32_16x16x64_bf16 v[156:159], v[236:239], v[88:95], v234
TR a[184:185],v233 offset:50688
TR a[186:187],v233 offset:54784
TR a[188:189],v233 offset:58880
TR a[190:191],v233 offset:62976
s_waitcnt lgkmcnt(5)
buffer_load_dwordx4 v235, s[12:15], s64 offen offset:3072 lds
MM v[16:19],a[192:195],v[168:171],v[136:139]
MM v[16:19],a[196:199],v[184:187],v[16:19]
MM v[16:19],a[200:203],v[200:203],v[16:19]
v_max3_f32 v249, v144, v145, v148
v_max3_f32 v249, v249, v149, v152
MM v[16:19],a[204:207],v[216:219],v[16:19]
v_max3_f32 v249, v249, v153, v156
v_max_f32 v249, v249, v157
MM v[20:23],a[220:223],v[216:219],v[136:139]
v_cmp_lt_f32 vcc, s71, v249
MM v[20:23],a[216:219],v[200:203],v[20:23]
s_cbranch_vccnz LSITE35_%=
LBACK35_%=:
v_add_f32 v244, v144, v145
MM v[20:23],a[212:215],v[184:187],v[20:23]
v_add_f32 v245, v148, v149
v_add_f32 v246, v152, v153
MM v[20:23],a[208:211],v[168:171],v[20:23]
s_waitcnt lgkmcnt(0)
v_add_f32 v247, v156, v157
MM a[0:3],a[128:131],v[64:67],a[0:3]
EX v16,v16
MM a[0:3],a[132:135],v[68:71],a[0:3]
v_rcp_f32 v145, v244
MM a[8:11],a[132:135],v[76:79],a[8:11]
EX v17,v17
MM a[8:11],a[128:131],v[72:75],a[8:11]
EX v18,v18
MM a[12:15],a[136:139],v[72:75],a[12:15]
v_rcp_f32 v149, v245
MM a[12:15],a[140:143],v[76:79],a[12:15]
CV v16,v16,v17
v_bfe_u32 v144, v241, 0, 8
MM a[4:7],a[140:143],v[68:71],a[4:7]
EX v19,v19
MM a[4:7],a[136:139],v[64:67],a[4:7]
CV v17,v18,v19
v_lshl_or_b32 v144, v144, 1, 1
MM a[32:35],a[144:147],v[64:67],a[32:35]
EX v20,v20
MM a[32:35],a[148:151],v[68:71],a[32:35]
v_lshlrev_b32 v144, 22, v144
v_mul_f32 v144, -v144, v145
MM a[40:43],a[148:151],v[76:79],a[40:43]
v_bfe_u32 v148, v241, 8, 8
v_lshl_or_b32 v148, v148, 1, 1
MM a[40:43],a[144:147],v[72:75],a[40:43]
v_lshlrev_b32 v148, 22, v148
v_mul_f32 v148, -v148, v149
MM a[44:47],a[152:155],v[72:75],a[44:47]
s_mul_i32 s78, s49, 17408
s_add_u32 s78, s83, s78
s_add_u32 s78, s78, 0
s_add_u32 s79, s78, 4096
AR v0,a0
AR v1,a1
AR v2,a2
MM a[44:47],a[156:159],v[76:79],a[44:47]
AR v3,a3
AR v4,a4
AR v5,a5
MM a[36:39],a[156:159],v[68:71],a[36:39]
AR v6,a6
AR v7,a7
v_fma_f32 v0, v0, v145, v144
v_fma_f32 v1, v1, v145, v144
MM a[36:39],a[152:155],v[64:67],a[36:39]
v_fma_f32 v2, v2, v145, v144
v_fma_f32 v3, v3, v145, v144
v_fma_f32 v4, v4, v145, v144
v_fma_f32 v5, v5, v145, v144
MM a[64:67],a[160:163],v[64:67],a[64:67]
v_fma_f32 v6, v6, v145, v144
v_fma_f32 v7, v7, v145, v144
CV v160,v0,v1
CV v161,v2,v3
MM a[64:67],a[164:167],v[68:71],a[64:67]
CV v162,v4,v5
CV v163,v6,v7
MM a[72:75],a[164:167],v[76:79],a[72:75]
buffer_store_dwordx4 v[160:163], v248, s[20:23], s78 offen offset:0 sc1
AR v8,a8
AR v9,a9
AR v10,a10
MM a[72:75],a[160:163],v[72:75],a[72:75]
AR v11,a11
AR v12,a12
AR v13,a13
EX v21,v21
AR v14,a14
MM a[76:79],a[168:171],v[72:75],a[76:79]
AR v15,a15
v_fma_f32 v8, v8, v149, v148
v_fma_f32 v9, v9, v149, v148
v_fma_f32 v10, v10, v149, v148
MM a[76:79],a[172:175],v[76:79],a[76:79]
v_fma_f32 v11, v11, v149, v148
v_fma_f32 v12, v12, v149, v148
v_fma_f32 v13, v13, v149, v148
v_fma_f32 v14, v14, v149, v148
EX v22,v22
MM a[68:71],a[172:175],v[68:71],a[68:71]
v_fma_f32 v15, v15, v149, v148
CV v164,v8,v9
CV v165,v10,v11
CV v166,v12,v13
MM a[68:71],a[168:171],v[64:67],a[68:71]
CV v167,v14,v15
CV v18,v20,v21
MM a[96:99],a[176:179],v[64:67],a[96:99]
buffer_store_dwordx4 v[164:167], v248, s[20:23], s79 offen offset:0 sc1
AR v32,a32
AR v33,a33
AR v34,a34
EX v23,v23
AR v35,a35
MM a[96:99],a[180:183],v[68:71],a[96:99]
AR v36,a36
AR v37,a37
AR v38,a38
AR v39,a39
MM a[104:107],a[180:183],v[76:79],a[104:107]
v_fma_f32 v32, v32, v145, v144
v_fma_f32 v33, v33, v145, v144
v_fma_f32 v34, v34, v145, v144
v_fma_f32 v35, v35, v145, v144
MM a[104:107],a[176:179],v[72:75],a[104:107]
v_fma_f32 v36, v36, v145, v144
v_fma_f32 v37, v37, v145, v144
v_fma_f32 v38, v38, v145, v144
MM a[108:111],a[184:187],v[72:75],a[108:111]
v_fma_f32 v39, v39, v145, v144
CV v176,v32,v33
CV v177,v34,v35
CV v178,v36,v37
MM a[108:111],a[188:191],v[76:79],a[108:111]
CV v179,v38,v39
CV v19,v22,v23
MM a[100:103],a[188:191],v[68:71],a[100:103]
buffer_store_dwordx4 v[176:179], v248, s[20:23], s78 offen offset:1024 sc1
AR v40,a40
AR v41,a41
AR v42,a42
AR v43,a43
AR v44,a44
MM a[100:103],a[184:187],v[64:67],a[100:103]
AR v45,a45
AR v46,a46
AR v47,a47
v_fma_f32 v40, v40, v149, v148
MM v[28:31],a[208:211],v[172:175],v[140:143]
v_fma_f32 v41, v41, v149, v148
v_fma_f32 v42, v42, v149, v148
v_fma_f32 v43, v43, v149, v148
MM v[28:31],a[212:215],v[188:191],v[28:31]
v_fma_f32 v44, v44, v149, v148
v_fma_f32 v45, v45, v149, v148
v_fma_f32 v46, v46, v149, v148
v_fma_f32 v47, v47, v149, v148
MM v[28:31],a[216:219],v[204:207],v[28:31]
CV v180,v40,v41
CV v181,v42,v43
CV v182,v44,v45
CV v183,v46,v47
MM v[28:31],a[220:223],v[220:223],v[28:31]
buffer_store_dwordx4 v[180:183], v248, s[20:23], s79 offen offset:1024 sc1
AR v0,a64
AR v1,a65
MM v[24:27],a[204:207],v[220:223],v[140:143]
AR v2,a66
AR v3,a67
AR v4,a68
MM v[24:27],a[200:203],v[204:207],v[24:27]
AR v5,a69
AR v6,a70
AR v7,a71
v_fma_f32 v0, v0, v145, v144
MM v[24:27],a[196:199],v[188:191],v[24:27]
v_fma_f32 v1, v1, v145, v144
v_fma_f32 v2, v2, v145, v144
v_fma_f32 v3, v3, v145, v144
v_fma_f32 v4, v4, v145, v144
MM v[24:27],a[192:195],v[172:175],v[24:27]
v_fma_f32 v5, v5, v145, v144
v_fma_f32 v6, v6, v145, v144
v_fma_f32 v7, v7, v145, v144
CV v192,v0,v1
MM v[48:51],a[224:227],v[168:171],v[136:139]
CV v193,v2,v3
CV v194,v4,v5
CV v195,v6,v7
MM v[48:51],a[228:231],v[184:187],v[48:51]
buffer_store_dwordx4 v[192:195], v248, s[20:23], s78 offen offset:2048 sc1
AR v8,a72
AR v9,a73
MM v[48:51],a[232:235],v[200:203],v[48:51]
AR v10,a74
AR v11,a75
AR v12,a76
AR v13,a77
MM v[48:51],a[236:239],v[216:219],v[48:51]
AR v14,a78
AR v15,a79
v_fma_f32 v8, v8, v149, v148
v_fma_f32 v9, v9, v149, v148
MM v[52:55],a[252:255],v[216:219],v[136:139]
EX v24,v24
EX v25,v25
EX v26,v26
EX v27,v27
EX v28,v28
EX v29,v29
v_fma_f32 v10, v10, v149, v148
v_fma_f32 v11, v11, v149, v148
EX v30,v30
v_fma_f32 v12, v12, v149, v148
v_fma_f32 v13, v13, v149, v148
MM v[52:55],a[248:251],v[200:203],v[52:55]
v_fma_f32 v14, v14, v149, v148
v_fma_f32 v15, v15, v149, v148
CV v196,v8,v9
EX v31,v31
CV v24,v24,v25
CV v197,v10,v11
MM v[52:55],a[244:247],v[184:187],v[52:55]
CV v198,v12,v13
CV v199,v14,v15
MM v[52:55],a[240:243],v[168:171],v[52:55]
buffer_store_dwordx4 v[196:199], v248, s[20:23], s79 offen offset:2048 sc1
AR v32,a96
AR v33,a97
AR v34,a98
MM v[60:63],a[240:243],v[172:175],v[140:143]
AR v35,a99
AR v36,a100
AR v37,a101
EX v48,v48
AR v38,a102
MM v[60:63],a[244:247],v[188:191],v[60:63]
AR v39,a103
v_fma_f32 v32, v32, v145, v144
v_fma_f32 v33, v33, v145, v144
v_fma_f32 v34, v34, v145, v144
CV v25,v26,v27
MM v[60:63],a[248:251],v[204:207],v[60:63]
v_fma_f32 v35, v35, v145, v144
v_fma_f32 v36, v36, v145, v144
v_fma_f32 v37, v37, v145, v144
MM v[60:63],a[252:255],v[220:223],v[60:63]
v_fma_f32 v38, v38, v145, v144
v_fma_f32 v39, v39, v145, v144
CV v208,v32,v33
EX v49,v49
CV v209,v34,v35
MM v[56:59],a[236:239],v[220:223],v[140:143]
CV v210,v36,v37
CV v211,v38,v39
MM v[56:59],a[232:235],v[204:207],v[56:59]
buffer_store_dwordx4 v[208:211], v248, s[20:23], s78 offen offset:3072 sc1
AR v40,a104
AR v41,a105
AR v42,a106
AR v43,a107
EX v50,v50
MM v[56:59],a[228:231],v[188:191],v[56:59]
AR v44,a108
AR v45,a109
AR v46,a110
AR v47,a111
CV v26,v28,v29
MM v[56:59],a[224:227],v[172:175],v[56:59]
s_waitcnt lgkmcnt(0)
s_barrier
v_fma_f32 v40, v40, v149, v148
v_fma_f32 v41, v41, v149, v148
v_fma_f32 v42, v42, v149, v148
MM a[16:19],a[128:131],v[80:83],a[16:19]
s_xor_b32 s66, s94, 4
v_fma_f32 v43, v43, v149, v148
v_fma_f32 v44, v44, v149, v148
v_fma_f32 v45, v45, v149, v148
v_fma_f32 v46, v46, v149, v148
EX v51,v51
MM a[16:19],a[132:135],v[84:87],a[16:19]
s_mul_i32 s66, s66, 163840
v_fma_f32 v47, v47, v149, v148
CV v212,v40,v41
CV v213,v42,v43
CV v214,v44,v45
MM a[24:27],a[132:135],v[92:95],a[24:27]
s_add_u32 s66, s66, s87
CV v215,v46,v47
CV v27,v30,v31
MM a[24:27],a[128:131],v[88:91],a[24:27]
s_add_u32 m0, s65, 114688
buffer_store_dwordx4 v[212:215], v248, s[20:23], s79 offen offset:3072 sc1
EX v52,v52
MM a[28:31],a[136:139],v[88:91],a[28:31]
EX v53,v53
MM a[28:31],a[140:143],v[92:95],a[28:31]
RD a[192:195],v224 offset:16384
CV v20,v48,v49
MM a[20:23],a[140:143],v[84:87],a[20:23]
buffer_load_dwordx4 v240, s[24:27], s66 offen offset:0 lds
MM a[20:23],a[136:139],v[80:83],a[20:23]
s_add_u32 s66, s66, 9216
EX v54,v54
MM a[48:51],a[144:147],v[80:83],a[48:51]
RD a[196:199],v225 offset:16384
CV v21,v50,v51
MM a[48:51],a[148:151],v[84:87],a[48:51]
EX v55,v55
MM a[56:59],a[148:151],v[92:95],a[56:59]
RD a[200:203],v226 offset:16384
RD a[204:207],v227 offset:16384
MM a[56:59],a[144:147],v[88:91],a[56:59]
buffer_load_dwordx4 v240, s[24:27], s66 offen offset:1024 lds
MM a[60:63],a[152:155],v[88:91],a[60:63]
s_add_u32 s66, s66, 9216
EX v56,v56
MM a[60:63],a[156:159],v[92:95],a[60:63]
RD a[208:211],v224 offset:20480
CV v22,v52,v53
MM a[52:55],a[156:159],v[84:87],a[52:55]
buffer_load_dwordx4 v240, s[24:27], s66 offen offset:2048 lds
MM a[52:55],a[152:155],v[80:83],a[52:55]
s_add_u32 s66, s66, 9216
EX v57,v57
MM a[80:83],a[160:163],v[80:83],a[80:83]
RD a[212:215],v225 offset:20480
RD a[216:219],v226 offset:20480
MM a[80:83],a[164:167],v[84:87],a[80:83]
buffer_load_dwordx4 v240, s[24:27], s66 offen offset:3072 lds
MM a[88:91],a[164:167],v[92:95],a[88:91]
s_xor_b32 s66, s94, 4
EX v58,v58
MM a[88:91],a[160:163],v[88:91],a[88:91]
s_mul_i32 s66, s66, 163840
RD a[220:223],v227 offset:20480
CV v23,v54,v55
MM a[92:95],a[168:171],v[88:91],a[92:95]
s_add_u32 s66, s66, s86
EX v59,v59
MM a[92:95],a[172:175],v[92:95],a[92:95]
s_add_u32 m0, s65, 49152
RD a[224:227],v224 offset:24576
RD a[228:231],v225 offset:24576
MM a[84:87],a[172:175],v[84:87],a[84:87]
buffer_load_dwordx4 v240, s[24:27], s66 offen offset:0 lds
MM a[84:87],a[168:171],v[80:83],a[84:87]
s_add_u32 s66, s66, 9216
EX v60,v60
MM a[112:115],a[176:179],v[80:83],a[112:115]
RD a[232:235],v226 offset:24576
CV v28,v56,v57
MM a[112:115],a[180:183],v[84:87],a[112:115]
EX v61,v61
MM a[120:123],a[180:183],v[92:95],a[120:123]
buffer_load_dwordx4 v240, s[24:27], s66 offen offset:1024 lds
MM a[120:123],a[176:179],v[88:91],a[120:123]
s_add_u32 s66, s66, 9216
RD a[236:239],v227 offset:24576
RD a[240:243],v224 offset:28672
MM a[124:127],a[184:187],v[88:91],a[124:127]
EX v62,v62
MM a[124:127],a[188:191],v[92:95],a[124:127]
buffer_load_dwordx4 v240, s[24:27], s66 offen offset:2048 lds
MM a[116:119],a[188:191],v[84:87],a[116:119]
s_add_u32 s66, s66, 9216
RD a[244:247],v225 offset:28672
RD a[248:251],v226 offset:28672
RD a[252:255],v227 offset:28672
EX v63,v63
MM a[116:119],a[184:187],v[80:83],a[116:119]
buffer_load_dwordx4 v240, s[24:27], s66 offen offset:3072 lds
v_smfmac_f32_16x16x64_bf16 v[152:155], v[236:239], v[16:23], v234
s_nop 0
LPG25_%=:
s_cbranch_execz LPG29_%=
CV v29,v58,v59
CV v30,v60,v61
CV v31,v62,v63
s_nop 1
v_smfmac_f32_16x16x64_bf16 v[156:159], v[236:239], v[24:31], v234
)ASM"
R"ASM(
s_waitcnt lgkmcnt(0)
MM v[80:83],a[192:195],v[168:171],v[136:139]
TR a[128:129],v232 offset:0
TR a[130:131],v232 offset:4096
MM v[80:83],a[196:199],v[184:187],v[80:83]
TR a[132:133],v232 offset:8192
TR a[134:135],v232 offset:12288
MM v[80:83],a[200:203],v[200:203],v[80:83]
TR a[136:137],v233 offset:0
TR a[138:139],v233 offset:4096
MM v[80:83],a[204:207],v[216:219],v[80:83]
v_max3_f32 v249, v152, v153, v156
v_max_f32 v249, v249, v157
MM v[84:87],a[220:223],v[216:219],v[136:139]
TR a[140:141],v233 offset:8192
v_cmp_lt_f32 vcc, s71, v249
MM v[84:87],a[216:219],v[200:203],v[84:87]
s_cbranch_vccnz LSITE36_%=
LBACK36_%=:
TR a[142:143],v233 offset:12288
v_add_f32 v246, v152, v153
MM v[84:87],a[212:215],v[184:187],v[84:87]
TR a[144:145],v232 offset:512
v_add_f32 v247, v156, v157
MM v[84:87],a[208:211],v[168:171],v[84:87]
TR a[146:147],v232 offset:4608
TR a[148:149],v232 offset:8704
MM v[92:95],a[208:211],v[172:175],v[140:143]
s_lshl_b32 s97, s84, 2
s_add_u32 s97, s97, s96
s_add_u32 s97, s97, 0
MM v[92:95],a[212:215],v[188:191],v[92:95]
s_waitcnt vmcnt(8)
v_mov_b32 v242, s69
s_mov_b64 exec, 1
buffer_store_dword v242, off, s[20:23], s97 offset:0 sc1
s_mov_b64 exec, -1
MM v[92:95],a[216:219],v[204:207],v[92:95]
v_lshrrev_b32 v98, 1, v248
v_min_u32 v98, 312, v98
MM v[92:95],a[220:223],v[220:223],v[92:95]
s_mov_b32 s62, 94464
buffer_load_dwordx2 v[96:97], v98, s[20:23], s62 offen offset:0 sc1
MM v[88:91],a[204:207],v[220:223],v[140:143]
TR a[150:151],v232 offset:12800
EX v80,v80
MM v[88:91],a[200:203],v[204:207],v[88:91]
TR a[152:153],v233 offset:512
TR a[154:155],v233 offset:4608
EX v81,v81
MM v[88:91],a[196:199],v[188:191],v[88:91]
TR a[156:157],v233 offset:8704
TR a[158:159],v233 offset:12800
MM v[88:91],a[192:195],v[172:175],v[88:91]
EX v82,v82
MM v[112:115],a[224:227],v[168:171],v[136:139]
TR a[160:161],v232 offset:1024
TR a[162:163],v232 offset:5120
EX v83,v83
MM v[112:115],a[228:231],v[184:187],v[112:115]
TR a[164:165],v232 offset:9216
TR a[166:167],v232 offset:13312
MM v[112:115],a[232:235],v[200:203],v[112:115]
EX v84,v84
MM v[112:115],a[236:239],v[216:219],v[112:115]
TR a[168:169],v233 offset:1024
TR a[170:171],v233 offset:5120
MM v[116:119],a[252:255],v[216:219],v[136:139]
EX v85,v85
MM v[116:119],a[248:251],v[200:203],v[116:119]
TR a[172:173],v233 offset:9216
TR a[174:175],v233 offset:13312
MM v[116:119],a[244:247],v[184:187],v[116:119]
TR a[176:177],v232 offset:1536
EX v86,v86
MM v[116:119],a[240:243],v[168:171],v[116:119]
TR a[178:179],v232 offset:5632
CV v80,v80,v81
EX v87,v87
MM v[124:127],a[240:243],v[172:175],v[140:143]
TR a[180:181],v232 offset:9728
TR a[182:183],v232 offset:13824
MM v[124:127],a[244:247],v[188:191],v[124:127]
EX v88,v88
MM v[124:127],a[248:251],v[204:207],v[124:127]
TR a[184:185],v233 offset:1536
TR a[186:187],v233 offset:5632
TR a[188:189],v233 offset:9728
TR a[190:191],v233 offset:13824
EX v89,v89
MM v[124:127],a[252:255],v[220:223],v[124:127]
CV v81,v82,v83
CV v82,v84,v85
MM v[120:123],a[236:239],v[220:223],v[140:143]
EX v90,v90
MM v[120:123],a[232:235],v[204:207],v[120:123]
EX v91,v91
MM v[120:123],a[228:231],v[188:191],v[120:123]
CV v83,v86,v87
CV v88,v88,v89
MM v[120:123],a[224:227],v[172:175],v[120:123]
s_waitcnt lgkmcnt(0)
s_barrier
CV v89,v90,v91
EX v92,v92
MM a[16:19],a[128:131],v[16:19],a[16:19]
s_xor_b32 s66, s94, 5
EX v93,v93
MM a[16:19],a[132:135],v[20:23],a[16:19]
s_mul_i32 s66, s66, 163840
EX v94,v94
MM a[24:27],a[132:135],v[28:31],a[24:27]
s_add_u32 s66, s66, s87
EX v95,v95
MM a[24:27],a[128:131],v[24:27],a[24:27]
s_add_u32 m0, s65, 65536
RD a[192:195],v224 offset:32768
CV v90,v92,v93
MM a[28:31],a[136:139],v[24:27],a[28:31]
EX v112,v112
MM a[28:31],a[140:143],v[28:31],a[28:31]
buffer_load_dwordx4 v240, s[24:27], s66 offen offset:0 lds
MM a[20:23],a[140:143],v[20:23],a[20:23]
s_add_u32 s66, s66, 9216
EX v113,v113
MM a[20:23],a[136:139],v[16:19],a[20:23]
RD a[196:199],v225 offset:32768
CV v91,v94,v95
MM a[48:51],a[144:147],v[16:19],a[48:51]
v_lshrrev_b32 v243, 2, v248
v_min_u32 v243, 12, v243
MM a[48:51],a[148:151],v[20:23],a[48:51]
buffer_load_dword v242, v243, s[20:23], s44 offen offset:0 sc1
EX v114,v114
MM a[56:59],a[148:151],v[28:31],a[56:59]
RD a[200:203],v226 offset:32768
RD a[204:207],v227 offset:32768
MM a[56:59],a[144:147],v[24:27],a[56:59]
EX v115,v115
MM a[60:63],a[152:155],v[24:27],a[60:63]
buffer_load_dwordx4 v240, s[24:27], s66 offen offset:1024 lds
MM a[60:63],a[156:159],v[28:31],a[60:63]
s_add_u32 s66, s66, 9216
EX v116,v116
MM a[52:55],a[156:159],v[20:23],a[52:55]
EX v117,v117
MM a[52:55],a[152:155],v[16:19],a[52:55]
RD a[208:211],v224 offset:36864
RD a[212:215],v225 offset:36864
buffer_load_dwordx4 v240, s[24:27], s66 offen offset:2048 lds
MM a[80:83],a[160:163],v[16:19],a[80:83]
s_add_u32 s66, s66, 9216
EX v118,v118
MM a[80:83],a[164:167],v[20:23],a[80:83]
RD a[216:219],v226 offset:36864
CV v84,v112,v113
MM a[88:91],a[164:167],v[28:31],a[88:91]
EX v119,v119
MM a[88:91],a[160:163],v[24:27],a[88:91]
buffer_load_dwordx4 v240, s[24:27], s66 offen offset:3072 lds
EX v120,v120
MM a[92:95],a[168:171],v[24:27],a[92:95]
s_xor_b32 s66, s94, 5
RD a[220:223],v227 offset:36864
CV v85,v114,v115
MM a[92:95],a[172:175],v[28:31],a[92:95]
s_mul_i32 s66, s66, 163840
EX v121,v121
MM a[84:87],a[172:175],v[20:23],a[84:87]
s_add_u32 s66, s66, s86
RD a[224:227],v224 offset:40960
CV v86,v116,v117
MM a[84:87],a[168:171],v[16:19],a[84:87]
s_add_u32 m0, s65, 0
EX v122,v122
MM a[112:115],a[176:179],v[16:19],a[112:115]
buffer_load_dwordx4 v240, s[24:27], s66 offen offset:0 lds
RD a[228:231],v225 offset:40960
EX v123,v123
MM a[112:115],a[180:183],v[20:23],a[112:115]
s_add_u32 s66, s66, 9216
RD a[232:235],v226 offset:40960
CV v87,v118,v119
MM a[120:123],a[180:183],v[28:31],a[120:123]
EX v124,v124
MM a[120:123],a[176:179],v[24:27],a[120:123]
buffer_load_dwordx4 v240, s[24:27], s66 offen offset:1024 lds
MM a[124:127],a[184:187],v[24:27],a[124:127]
s_add_u32 s66, s66, 9216
RD a[236:239],v227 offset:40960
EX v125,v125
MM a[124:127],a[188:191],v[28:31],a[124:127]
EX v126,v126
MM a[116:119],a[188:191],v[20:23],a[116:119]
RD a[240:243],v224 offset:45056
RD a[244:247],v225 offset:45056
RD a[248:251],v226 offset:45056
RD a[252:255],v227 offset:45056
EX v127,v127
MM a[116:119],a[184:187],v[16:19],a[116:119]
buffer_load_dwordx4 v240, s[24:27], s66 offen offset:2048 lds
CV v92,v120,v121
v_smfmac_f32_16x16x64_bf16 v[152:155], v[236:239], v[80:87], v234
s_add_u32 s66, s66, 9216
CV v93,v122,v123
CV v94,v124,v125
CV v95,v126,v127
s_nop 1
v_smfmac_f32_16x16x64_bf16 v[156:159], v[236:239], v[88:95], v234
s_waitcnt lgkmcnt(0)
buffer_load_dwordx4 v240, s[24:27], s66 offen offset:3072 lds
v_lshrrev_b32 v145, 9, v248
v_mul_u32_u24 v145, 1536, v145
v_sub_u32 v244, v232, v145
v_sub_u32 v235, v233, v145
v_add_u32 v244, 65536, v244
v_add_u32 v235, 65536, v235
v_and_b32 v145, 960, v248
v_lshlrev_b32 v145, 1, v145
v_bfe_u32 v146, v248, 7, 2
v_lshl_add_u32 v145, v146, 5, v145
v_bfe_u32 v146, v248, 4, 2
v_lshl_add_u32 v145, v146, 3, v145
v_add_u32 v228, %[slds], v145
v_xor_b32 v229, 32, v228
v_xor_b32 v230, 64, v228
v_xor_b32 v231, 96, v228
v_add_u32 v144, 65536, v228
v_add_u32 v145, 65536, v229
v_add_u32 v146, 65536, v230
v_add_u32 v147, 65536, v231
MM v[16:19],a[192:195],v[168:171],v[136:139]
TR a[128:129],v232 offset:16384
TR a[130:131],v232 offset:20480
MM v[16:19],a[196:199],v[184:187],v[16:19]
TR a[132:133],v232 offset:24576
TR a[134:135],v232 offset:28672
MM v[16:19],a[200:203],v[200:203],v[16:19]
TR a[136:137],v233 offset:16384
TR a[138:139],v233 offset:20480
MM v[16:19],a[204:207],v[216:219],v[16:19]
v_max3_f32 v249, v152, v153, v156
v_max_f32 v249, v249, v157
MM v[20:23],a[220:223],v[216:219],v[136:139]
TR a[140:141],v233 offset:24576
v_cmp_lt_f32 vcc, s71, v249
MM v[20:23],a[216:219],v[200:203],v[20:23]
s_cbranch_vccnz LSITE37_%=
LBACK37_%=:
TR a[142:143],v233 offset:28672
v_add_f32 v246, v152, v153
MM v[20:23],a[212:215],v[184:187],v[20:23]
TR a[144:145],v232 offset:16896
v_add_f32 v247, v156, v157
MM v[20:23],a[208:211],v[168:171],v[20:23]
TR a[146:147],v232 offset:20992
TR a[148:149],v232 offset:25088
MM v[28:31],a[208:211],v[172:175],v[140:143]
s_waitcnt vmcnt(7)
v_cmp_ne_u32 vcc, s69, v242
s_cbranch_vccnz LRPO0_%=
LRPO0b_%=:
MM v[28:31],a[212:215],v[188:191],v[28:31]
s_xor_b32 s92, s49, 4
s_mul_i32 s92, s92, 17408
s_add_u32 s92, s83, s92
MM v[28:31],a[216:219],v[204:207],v[28:31]
buffer_load_dwordx4 a[0:3], v248, s[20:23], s92 offen offset:0 sc1
MM v[28:31],a[220:223],v[220:223],v[28:31]
buffer_load_dwordx4 a[32:35], v248, s[20:23], s92 offen offset:1024 sc1
MM v[24:27],a[204:207],v[220:223],v[140:143]
TR a[150:151],v232 offset:29184
v_xor_b32 v96, s69, v96
v_xor_b32 v97, s69, v97
EX v16,v16
MM v[24:27],a[200:203],v[204:207],v[24:27]
TR a[152:153],v233 offset:16896
TR a[154:155],v233 offset:20992
v_or_b32 v99, v96, v97
EX v17,v17
MM v[24:27],a[196:199],v[188:191],v[24:27]
buffer_load_dwordx4 a[64:67], v248, s[20:23], s92 offen offset:2048 sc1
TR a[156:157],v233 offset:25088
MM v[24:27],a[192:195],v[172:175],v[24:27]
TR a[158:159],v233 offset:29184
v_cmp_ne_u32 vcc, 0, v99
s_cbranch_vccnz LRPW_%=
LRPWb_%=:
EX v18,v18
MM v[48:51],a[224:227],v[168:171],v[136:139]
TR a[160:161],v232 offset:17408
TR a[162:163],v232 offset:21504
EX v19,v19
MM v[48:51],a[228:231],v[184:187],v[48:51]
buffer_load_dwordx4 a[96:99], v248, s[20:23], s92 offen offset:3072 sc1
TR a[164:165],v232 offset:25600
MM v[48:51],a[232:235],v[200:203],v[48:51]
s_add_u32 s92, s92, 4096
TR a[166:167],v232 offset:29696
TR a[168:169],v233 offset:17408
EX v20,v20
MM v[48:51],a[236:239],v[216:219],v[48:51]
TR a[170:171],v233 offset:21504
TR a[172:173],v233 offset:25600
MM v[52:55],a[252:255],v[216:219],v[136:139]
buffer_load_dwordx4 a[4:7], v248, s[20:23], s92 offen offset:0 sc1
EX v21,v21
MM v[52:55],a[248:251],v[200:203],v[52:55]
TR a[174:175],v233 offset:29696
CV v16,v16,v17
MM v[52:55],a[244:247],v[184:187],v[52:55]
TR a[176:177],v232 offset:17920
EX v22,v22
MM v[52:55],a[240:243],v[168:171],v[52:55]
buffer_load_dwordx4 a[36:39], v248, s[20:23], s92 offen offset:1024 sc1
TR a[178:179],v232 offset:22016
EX v23,v23
MM v[60:63],a[240:243],v[172:175],v[140:143]
TR a[180:181],v232 offset:26112
TR a[182:183],v232 offset:30208
MM v[60:63],a[244:247],v[188:191],v[60:63]
EX v24,v24
MM v[60:63],a[248:251],v[204:207],v[60:63]
buffer_load_dwordx4 a[68:71], v248, s[20:23], s92 offen offset:2048 sc1
TR a[184:185],v233 offset:17920
TR a[186:187],v233 offset:22016
TR a[188:189],v233 offset:26112
TR a[190:191],v233 offset:30208
EX v25,v25
MM v[60:63],a[252:255],v[220:223],v[60:63]
EX v26,v26
CV v17,v18,v19
MM v[56:59],a[236:239],v[220:223],v[140:143]
EX v27,v27
MM v[56:59],a[232:235],v[204:207],v[56:59]
buffer_load_dwordx4 a[100:103], v248, s[20:23], s92 offen offset:3072 sc1
MM v[56:59],a[228:231],v[188:191],v[56:59]
EX v28,v28
CV v18,v20,v21
MM v[56:59],a[224:227],v[172:175],v[56:59]
s_waitcnt lgkmcnt(0)
s_barrier
CV v19,v22,v23
MM a[16:19],a[128:131],v[80:83],a[16:19]
s_xor_b32 s66, s94, 6
EX v29,v29
MM a[16:19],a[132:135],v[84:87],a[16:19]
s_mul_i32 s66, s66, 163840
CV v24,v24,v25
CV v25,v26,v27
MM a[24:27],a[132:135],v[92:95],a[24:27]
s_add_u32 s66, s66, s87
EX v30,v30
MM a[24:27],a[128:131],v[88:91],a[24:27]
s_add_u32 m0, s65, 81920
EX v31,v31
MM a[28:31],a[136:139],v[88:91],a[28:31]
EX v48,v48
MM a[28:31],a[140:143],v[92:95],a[28:31]
CV v26,v28,v29
CV v27,v30,v31
MM a[20:23],a[140:143],v[84:87],a[20:23]
buffer_load_dwordx4 v240, s[24:27], s66 offen offset:0 lds
MM a[20:23],a[136:139],v[80:83],a[20:23]
s_add_u32 s66, s66, 9216
EX v49,v49
MM a[48:51],a[144:147],v[80:83],a[48:51]
EX v50,v50
MM a[48:51],a[148:151],v[84:87],a[48:51]
buffer_load_dwordx4 v240, s[24:27], s66 offen offset:1024 lds
MM a[56:59],a[148:151],v[92:95],a[56:59]
s_add_u32 s66, s66, 9216
EX v51,v51
MM a[56:59],a[144:147],v[88:91],a[56:59]
TR a[128:129],v232 offset:32768
CV v20,v48,v49
MM a[60:63],a[152:155],v[88:91],a[60:63]
EX v52,v52
MM a[60:63],a[156:159],v[92:95],a[60:63]
TR a[130:131],v232 offset:36864
TR a[132:133],v232 offset:40960
MM a[52:55],a[156:159],v[84:87],a[52:55]
)ASM"
R"ASM(
EX v53,v53
MM a[52:55],a[152:155],v[80:83],a[52:55]
TR a[134:135],v232 offset:45056
TR a[136:137],v233 offset:32768
buffer_load_dwordx4 v240, s[24:27], s66 offen offset:2048 lds
MM a[80:83],a[160:163],v[80:83],a[80:83]
s_add_u32 s66, s66, 9216
TR a[138:139],v233 offset:36864
TR a[140:141],v233 offset:40960
EX v54,v54
MM a[80:83],a[164:167],v[84:87],a[80:83]
TR a[142:143],v233 offset:45056
TR a[144:145],v232 offset:33280
MM a[88:91],a[164:167],v[92:95],a[88:91]
s_xor_b32 s92, s49, 5
s_mul_i32 s92, s92, 17408
s_add_u32 s92, s83, s92
EX v55,v55
MM a[88:91],a[160:163],v[88:91],a[88:91]
buffer_load_dwordx4 a[8:11], v248, s[20:23], s92 offen offset:0 sc1
TR a[146:147],v232 offset:37376
EX v56,v56
MM a[92:95],a[168:171],v[88:91],a[92:95]
TR a[148:149],v232 offset:41472
TR a[150:151],v232 offset:45568
buffer_load_dwordx4 v240, s[24:27], s66 offen offset:3072 lds
MM a[92:95],a[172:175],v[92:95],a[92:95]
s_xor_b32 s66, s94, 6
TR a[152:153],v233 offset:33280
EX v57,v57
CV v21,v50,v51
MM a[84:87],a[172:175],v[84:87],a[84:87]
s_mul_i32 s66, s66, 163840
TR a[154:155],v233 offset:37376
TR a[156:157],v233 offset:41472
MM a[84:87],a[168:171],v[80:83],a[84:87]
s_add_u32 s66, s66, s86
buffer_load_dwordx4 a[40:43], v248, s[20:23], s92 offen offset:1024 sc1
TR a[158:159],v233 offset:45568
s_add_u32 m0, s65, 16384
EX v58,v58
MM a[112:115],a[176:179],v[80:83],a[112:115]
TR a[160:161],v232 offset:33792
CV v22,v52,v53
EX v59,v59
MM a[112:115],a[180:183],v[84:87],a[112:115]
TR a[162:163],v232 offset:37888
TR a[164:165],v232 offset:41984
MM a[120:123],a[180:183],v[92:95],a[120:123]
buffer_load_dwordx4 a[72:75], v248, s[20:23], s92 offen offset:2048 sc1
TR a[166:167],v232 offset:46080
EX v60,v60
MM a[120:123],a[176:179],v[88:91],a[120:123]
TR a[168:169],v233 offset:33792
TR a[170:171],v233 offset:37888
CV v23,v54,v55
MM a[124:127],a[184:187],v[88:91],a[124:127]
TR a[172:173],v233 offset:41984
EX v61,v61
MM a[124:127],a[188:191],v[92:95],a[124:127]
buffer_load_dwordx4 a[104:107], v248, s[20:23], s92 offen offset:3072 sc1
TR a[174:175],v233 offset:46080
EX v62,v62
MM a[116:119],a[188:191],v[84:87],a[116:119]
TR a[176:177],v232 offset:34304
TR a[178:179],v232 offset:38400
EX v63,v63
MM a[116:119],a[184:187],v[80:83],a[116:119]
s_add_u32 s92, s92, 4096
TR a[180:181],v232 offset:42496
CV v28,v56,v57
v_smfmac_f32_16x16x64_bf16 v[152:155], v[236:239], v[16:23], v234
buffer_load_dwordx4 v240, s[24:27], s66 offen offset:0 lds
s_add_u32 s66, s66, 9216
TR a[182:183],v232 offset:46592
CV v29,v58,v59
CV v30,v60,v61
CV v31,v62,v63
s_nop 1
v_smfmac_f32_16x16x64_bf16 v[156:159], v[236:239], v[24:31], v234
TR a[184:185],v233 offset:34304
TR a[186:187],v233 offset:38400
TR a[188:189],v233 offset:42496
TR a[190:191],v233 offset:46592
s_waitcnt lgkmcnt(0)
buffer_load_dwordx4 v240, s[24:27], s66 offen offset:1024 lds
s_add_u32 s66, s66, 9216
buffer_load_dwordx4 v240, s[24:27], s66 offen offset:2048 lds
s_add_u32 s66, s66, 9216
buffer_load_dwordx4 v240, s[24:27], s66 offen offset:3072 lds
s_nop 6
v_max3_f32 v249, v152, v153, v156
v_max_f32 v249, v249, v157
v_cmp_lt_f32 vcc, s71, v249
s_cbranch_vccnz LSITE38_%=
LBACK38_%=:
v_add_f32 v246, v152, v153
v_add_f32 v247, v156, v157
s_waitcnt lgkmcnt(0)
MM a[16:19],a[128:131],v[16:19],a[16:19]
s_barrier
MM a[16:19],a[132:135],v[20:23],a[16:19]
buffer_load_dwordx4 a[12:15], v248, s[20:23], s92 offen offset:0 sc1
MM a[24:27],a[132:135],v[28:31],a[24:27]
v_rcp_f32 v153, v246
MM a[24:27],a[128:131],v[24:27],a[24:27]
v_rcp_f32 v157, v247
MM a[28:31],a[136:139],v[24:27],a[28:31]
v_bfe_u32 v152, v241, 16, 8
v_lshl_or_b32 v152, v152, 1, 1
MM a[28:31],a[140:143],v[28:31],a[28:31]
buffer_load_dwordx4 a[44:47], v248, s[20:23], s92 offen offset:1024 sc1
MM a[20:23],a[140:143],v[20:23],a[20:23]
v_lshlrev_b32 v152, 22, v152
v_mul_f32 v152, -v152, v153
MM a[20:23],a[136:139],v[16:19],a[20:23]
v_bfe_u32 v156, v241, 24, 8
v_lshl_or_b32 v156, v156, 1, 1
MM a[48:51],a[144:147],v[16:19],a[48:51]
TR a[128:129],v244 offset:0
TR a[130:131],v244 offset:1024
MM a[48:51],a[148:151],v[20:23],a[48:51]
buffer_load_dwordx4 a[76:79], v248, s[20:23], s92 offen offset:2048 sc1
MM a[56:59],a[148:151],v[28:31],a[56:59]
TR a[132:133],v244 offset:2048
TR a[134:135],v244 offset:3072
MM a[56:59],a[144:147],v[24:27],a[56:59]
v_lshlrev_b32 v156, 22, v156
v_mul_f32 v156, -v156, v157
MM a[60:63],a[152:155],v[24:27],a[60:63]
s_mul_i32 s78, s49, 17408
TR a[136:137],v244 offset:4096
TR a[138:139],v244 offset:5120
MM a[60:63],a[156:159],v[28:31],a[60:63]
s_add_u32 s78, s83, s78
buffer_load_dwordx4 a[108:111], v248, s[20:23], s92 offen offset:3072 sc1
MM a[52:55],a[156:159],v[20:23],a[52:55]
s_add_u32 s78, s78, 8192
TR a[140:141],v244 offset:6144
TR a[142:143],v244 offset:7168
MM a[52:55],a[152:155],v[16:19],a[52:55]
s_add_u32 s79, s78, 4096
AR v80,a16
AR v81,a17
MM a[80:83],a[160:163],v[16:19],a[80:83]
TR a[144:145],v235 offset:0
TR a[146:147],v235 offset:1024
MM a[80:83],a[164:167],v[20:23],a[80:83]
AR v82,a18
AR v83,a19
MM a[88:91],a[164:167],v[28:31],a[88:91]
TR a[148:149],v235 offset:2048
TR a[150:151],v235 offset:3072
MM a[88:91],a[160:163],v[24:27],a[88:91]
AR v84,a20
AR v85,a21
MM a[92:95],a[168:171],v[24:27],a[92:95]
TR a[152:153],v235 offset:4096
TR a[154:155],v235 offset:5120
MM a[92:95],a[172:175],v[28:31],a[92:95]
s_xor_b32 s66, s94, 7
AR v86,a22
AR v87,a23
MM a[84:87],a[172:175],v[20:23],a[84:87]
s_mul_i32 s66, s66, 163840
TR a[156:157],v235 offset:6144
TR a[158:159],v235 offset:7168
v_fma_f32 v80, v80, v153, v152
MM a[84:87],a[168:171],v[16:19],a[84:87]
s_add_u32 s66, s66, s87
v_fma_f32 v81, v81, v153, v152
v_fma_f32 v82, v82, v153, v152
MM a[112:115],a[176:179],v[16:19],a[112:115]
s_add_u32 m0, s65, 98304
TR a[160:161],v144 offset:49152
TR a[162:163],v144 offset:51200
v_fma_f32 v83, v83, v153, v152
MM a[112:115],a[180:183],v[20:23],a[112:115]
buffer_load_dwordx4 v240, s[24:27], s66 offen offset:0 lds
v_fma_f32 v84, v84, v153, v152
v_fma_f32 v85, v85, v153, v152
MM a[120:123],a[180:183],v[28:31],a[120:123]
s_add_u32 s66, s66, 9216
TR a[164:165],v144 offset:53248
TR a[166:167],v144 offset:55296
v_fma_f32 v86, v86, v153, v152
v_fma_f32 v87, v87, v153, v152
MM a[120:123],a[176:179],v[24:27],a[120:123]
CV v168,v80,v81
CV v169,v82,v83
MM a[124:127],a[184:187],v[24:27],a[124:127]
TR a[168:169],v144 offset:57344
TR a[170:171],v144 offset:59392
CV v170,v84,v85
MM a[124:127],a[188:191],v[28:31],a[124:127]
buffer_load_dwordx4 v240, s[24:27], s66 offen offset:1024 lds
CV v171,v86,v87
MM a[116:119],a[188:191],v[20:23],a[116:119]
LPG26_%=:
s_cbranch_execz LPG30_%=
s_add_u32 s66, s66, 9216
TR a[172:173],v144 offset:61440
TR a[174:175],v144 offset:63488
MM a[116:119],a[184:187],v[16:19],a[116:119]
buffer_store_dwordx4 v[168:171], v248, s[20:23], s78 offen offset:0 sc1
AR v88,a24
AR v89,a25
s_waitcnt vmcnt(23)
s_waitcnt lgkmcnt(15)
MM v[128:131],a[128:131],a[0:3],0
TR a[176:177],v145 offset:49152
TR a[178:179],v145 offset:51200
AR v90,a26
AR v91,a27
MM v[128:131],a[132:135],a[32:35],v[128:131]
v_bfe_u32 v249, v248, 6, 2
v_lshlrev_b32 v249, 8, v249
AR v92,a28
AR v93,a29
MM v[128:131],a[136:139],a[64:67],v[128:131]
TR a[180:181],v145 offset:53248
TR a[182:183],v145 offset:55296
AR v94,a30
MM v[128:131],a[140:143],a[96:99],v[128:131]
s_waitcnt vmcnt(19)
v_lshrrev_b32 v250, 8, v248
v_lshl_add_u32 v249, v250, 6, v249
AR v95,a31
v_fma_f32 v88, v88, v157, v156
MM v[136:139],a[140:143],a[100:103],0
TR a[184:185],v145 offset:57344
TR a[186:187],v145 offset:59392
v_fma_f32 v89, v89, v157, v156
v_fma_f32 v90, v90, v157, v156
MM v[136:139],a[136:139],a[68:71],v[136:139]
buffer_load_dwordx4 v240, s[24:27], s66 offen offset:2048 lds
v_and_b32 v251, 48, v248
v_add_u32 v249, v249, v251
v_fma_f32 v91, v91, v157, v156
MM v[136:139],a[132:135],a[36:39],v[136:139]
s_add_u32 s66, s66, 9216
TR a[188:189],v145 offset:61440
TR a[190:191],v145 offset:63488
v_fma_f32 v92, v92, v157, v156
v_fma_f32 v93, v93, v157, v156
MM v[136:139],a[128:131],a[4:7],v[136:139]
s_waitcnt lgkmcnt(15)
v_fma_f32 v94, v94, v157, v156
v_fma_f32 v95, v95, v157, v156
MM v[140:143],a[144:147],a[4:7],0
s_lshr_b32 s60, s49, 3
TR a[128:129],v146 offset:49152
TR a[130:131],v146 offset:51200
CV v172,v88,v89
CV v173,v90,v91
MM v[140:143],a[148:151],a[36:39],v[140:143]
buffer_load_dwordx4 v240, s[24:27], s66 offen offset:3072 lds
CV v174,v92,v93
MM v[140:143],a[152:155],a[68:71],v[140:143]
TR a[132:133],v146 offset:53248
TR a[134:135],v146 offset:55296
CV v175,v94,v95
MM v[140:143],a[156:159],a[100:103],v[140:143]
s_xor_b32 s66, s94, 7
buffer_store_dwordx4 v[172:175], v248, s[20:23], s79 offen offset:0 sc1
MM v[132:135],a[156:159],a[96:99],0
s_mul_i32 s66, s66, 163840
TR a[136:137],v146 offset:57344
TR a[138:139],v146 offset:59392
AR v112,a48
AR v113,a49
MM v[132:135],a[152:155],a[64:67],v[132:135]
s_add_u32 s66, s66, s86
AR v114,a50
AR v115,a51
MM v[132:135],a[148:151],a[32:35],v[132:135]
s_add_u32 m0, s65, 32768
TR a[140:141],v146 offset:61440
TR a[142:143],v146 offset:63488
AR v116,a52
MM v[132:135],a[144:147],a[0:3],v[132:135]
s_waitcnt lgkmcnt(15)
buffer_load_dwordx4 v240, s[24:27], s66 offen offset:0 lds
AR v117,a53
AR v118,a54
MM v[64:67],a[160:163],a[0:3],0
s_xor_b32 s92, s49, 6
TR a[144:145],v147 offset:49152
TR a[146:147],v147 offset:51200
AR v119,a55
v_fma_f32 v112, v112, v153, v152
MM v[64:67],a[164:167],a[32:35],v[64:67]
s_mul_i32 s92, s92, 17408
s_add_u32 s92, s83, s92
v_fma_f32 v113, v113, v153, v152
v_fma_f32 v114, v114, v153, v152
MM v[64:67],a[168:171],a[64:67],v[64:67]
s_add_u32 s66, s66, 9216
TR a[148:149],v147 offset:53248
TR a[150:151],v147 offset:55296
v_fma_f32 v115, v115, v153, v152
MM v[64:67],a[172:175],a[96:99],v[64:67]
buffer_load_dwordx4 v[160:163], v248, s[20:23], s92 offen offset:0 sc1
v_fma_f32 v116, v116, v153, v152
v_fma_f32 v117, v117, v153, v152
MM v[72:75],a[172:175],a[100:103],0
TR a[152:153],v147 offset:57344
TR a[154:155],v147 offset:59392
v_fma_f32 v118, v118, v153, v152
v_fma_f32 v119, v119, v153, v152
MM v[72:75],a[168:171],a[68:71],v[72:75]
CV v184,v112,v113
CV v185,v114,v115
MM v[72:75],a[164:167],a[36:39],v[72:75]
TR a[156:157],v147 offset:61440
TR a[158:159],v147 offset:63488
CV v186,v116,v117
MM v[72:75],a[160:163],a[4:7],v[72:75]
s_waitcnt lgkmcnt(15)
buffer_load_dwordx4 v[176:179], v248, s[20:23], s92 offen offset:1024 sc1
CV v187,v118,v119
MM v[76:79],a[176:179],a[4:7],0
TR a[160:161],v228 offset:49152
TR a[162:163],v228 offset:51200
MM v[76:79],a[180:183],a[36:39],v[76:79]
v_xor_b32 v253, s60, v250
v_and_b32 v238, 240, v248
MM v[76:79],a[184:187],a[68:71],v[76:79]
TR a[164:165],v228 offset:53248
TR a[166:167],v228 offset:55296
MM v[76:79],a[188:191],a[100:103],v[76:79]
buffer_store_dwordx4 v[184:187], v248, s[20:23], s78 offen offset:1024 sc1
AR v120,a56
AR v121,a57
AR v122,a58
AR v123,a59
AR v124,a60
AR v125,a61
MM v[68:71],a[188:191],a[96:99],0
TR a[168:169],v228 offset:57344
TR a[170:171],v228 offset:59392
AR v126,a62
MM v[68:71],a[184:187],a[64:67],v[68:71]
AR v127,a63
v_fma_f32 v120, v120, v157, v156
MM v[68:71],a[180:183],a[32:35],v[68:71]
TR a[172:173],v228 offset:61440
TR a[174:175],v228 offset:63488
v_fma_f32 v121, v121, v157, v156
v_fma_f32 v122, v122, v157, v156
MM v[68:71],a[176:179],a[0:3],v[68:71]
s_waitcnt lgkmcnt(15)
buffer_load_dwordx4 v[192:195], v248, s[20:23], s92 offen offset:2048 sc1
v_fma_f32 v123, v123, v157, v156
MM v[96:99],a[128:131],a[0:3],0
TR a[176:177],v229 offset:49152
TR a[178:179],v229 offset:51200
v_fma_f32 v124, v124, v157, v156
v_fma_f32 v125, v125, v157, v156
MM v[96:99],a[132:135],a[32:35],v[96:99]
v_fma_f32 v126, v126, v157, v156
v_fma_f32 v127, v127, v157, v156
MM v[96:99],a[136:139],a[64:67],v[96:99]
TR a[180:181],v229 offset:53248
TR a[182:183],v229 offset:55296
CV v188,v120,v121
MM v[96:99],a[140:143],a[96:99],v[96:99]
buffer_load_dwordx4 v[208:211], v248, s[20:23], s92 offen offset:3072 sc1
CV v189,v122,v123
CV v190,v124,v125
MM v[104:107],a[140:143],a[100:103],0
TR a[184:185],v229 offset:57344
TR a[186:187],v229 offset:59392
CV v191,v126,v127
MM v[104:107],a[136:139],a[68:71],v[104:107]
)ASM"
R"ASM(
s_add_u32 s92, s92, 4096
v_lshl_add_u32 v232, v250, 2, v238
s_add_u32 s61, s65, 131072
MM v[104:107],a[132:135],a[36:39],v[104:107]
TR a[188:189],v229 offset:61440
TR a[190:191],v229 offset:63488
MM v[104:107],a[128:131],a[4:7],v[104:107]
s_waitcnt lgkmcnt(15)
buffer_store_dwordx4 v[188:191], v248, s[20:23], s79 offen offset:1024 sc1
AR v80,a80
AR v81,a81
AR v82,a82
AR v83,a83
MM v[108:111],a[144:147],a[4:7],0
TR a[128:129],v230 offset:49152
TR a[130:131],v230 offset:51200
AR v84,a84
MM v[108:111],a[148:151],a[36:39],v[108:111]
AR v85,a85
AR v86,a86
MM v[108:111],a[152:155],a[68:71],v[108:111]
TR a[132:133],v230 offset:53248
TR a[134:135],v230 offset:55296
AR v87,a87
v_fma_f32 v80, v80, v153, v152
MM v[108:111],a[156:159],a[100:103],v[108:111]
buffer_load_dwordx4 v[164:167], v248, s[20:23], s92 offen offset:0 sc1
v_fma_f32 v81, v81, v153, v152
v_fma_f32 v82, v82, v153, v152
MM v[100:103],a[156:159],a[96:99],0
TR a[136:137],v230 offset:57344
TR a[138:139],v230 offset:59392
v_fma_f32 v83, v83, v153, v152
MM v[100:103],a[152:155],a[64:67],v[100:103]
v_fma_f32 v84, v84, v153, v152
v_fma_f32 v85, v85, v153, v152
MM v[100:103],a[148:151],a[32:35],v[100:103]
TR a[140:141],v230 offset:61440
TR a[142:143],v230 offset:63488
v_fma_f32 v86, v86, v153, v152
v_fma_f32 v87, v87, v153, v152
MM v[100:103],a[144:147],a[0:3],v[100:103]
s_waitcnt lgkmcnt(15)
buffer_load_dwordx4 v[180:183], v248, s[20:23], s92 offen offset:1024 sc1
CV v200,v80,v81
MM v[0:3],a[160:163],a[0:3],0
TR a[144:145],v231 offset:49152
TR a[146:147],v231 offset:51200
CV v201,v82,v83
CV v202,v84,v85
MM v[0:3],a[164:167],a[32:35],v[0:3]
CV v203,v86,v87
v_mul_u32_u24 v224, 5, v238
MM v[0:3],a[168:171],a[64:67],v[0:3]
TR a[148:149],v231 offset:53248
TR a[150:151],v231 offset:55296
MM v[0:3],a[172:175],a[96:99],v[0:3]
buffer_store_dwordx4 v[200:203], v248, s[20:23], s78 offen offset:2048 sc1
AR v88,a88
AR v89,a89
MM v[8:11],a[172:175],a[100:103],0
TR a[152:153],v231 offset:57344
TR a[154:155],v231 offset:59392
AR v90,a90
AR v91,a91
MM v[8:11],a[168:171],a[68:71],v[8:11]
AR v92,a92
AR v93,a93
MM v[8:11],a[164:167],a[36:39],v[8:11]
TR a[156:157],v231 offset:61440
TR a[158:159],v231 offset:63488
AR v94,a94
MM v[8:11],a[160:163],a[4:7],v[8:11]
s_waitcnt lgkmcnt(15)
buffer_load_dwordx4 v[196:199], v248, s[20:23], s92 offen offset:2048 sc1
AR v95,a95
v_fma_f32 v88, v88, v157, v156
s_barrier
MM v[12:15],a[176:179],a[4:7],0
TR a[160:161],v244 offset:8192
TR a[162:163],v244 offset:9216
v_fma_f32 v89, v89, v157, v156
v_fma_f32 v90, v90, v157, v156
MM v[12:15],a[180:183],a[36:39],v[12:15]
v_fma_f32 v91, v91, v157, v156
v_fma_f32 v92, v92, v157, v156
MM v[12:15],a[184:187],a[68:71],v[12:15]
TR a[164:165],v244 offset:10240
TR a[166:167],v244 offset:11264
v_fma_f32 v93, v93, v157, v156
MM v[12:15],a[188:191],a[100:103],v[12:15]
buffer_load_dwordx4 v[212:215], v248, s[20:23], s92 offen offset:3072 sc1
v_fma_f32 v94, v94, v157, v156
v_fma_f32 v95, v95, v157, v156
MM v[4:7],a[188:191],a[96:99],0
TR a[168:169],v244 offset:12288
TR a[170:171],v244 offset:13312
CV v204,v88,v89
MM v[4:7],a[184:187],a[64:67],v[4:7]
CV v205,v90,v91
CV v206,v92,v93
MM v[4:7],a[180:183],a[32:35],v[4:7]
TR a[172:173],v244 offset:14336
TR a[174:175],v244 offset:15360
CV v207,v94,v95
MM v[4:7],a[176:179],a[0:3],v[4:7]
s_waitcnt lgkmcnt(15)
buffer_store_dwordx4 v[204:207], v248, s[20:23], s79 offen offset:2048 sc1
MM v[32:35],a[128:131],a[0:3],0
TR a[176:177],v235 offset:8192
TR a[178:179],v235 offset:9216
AR v112,a112
AR v113,a113
MM v[32:35],a[132:135],a[32:35],v[32:35]
AR v114,a114
AR v115,a115
MM v[32:35],a[136:139],a[64:67],v[32:35]
TR a[180:181],v235 offset:10240
TR a[182:183],v235 offset:11264
AR v116,a116
MM v[32:35],a[140:143],a[96:99],v[32:35]
buffer_load_dwordx4 v240, s[24:27], s66 offen offset:1024 lds
AR v117,a117
AR v118,a118
MM v[40:43],a[140:143],a[100:103],0
s_add_u32 s66, s66, 9216
TR a[184:185],v235 offset:12288
TR a[186:187],v235 offset:13312
AR v119,a119
v_fma_f32 v112, v112, v153, v152
MM v[40:43],a[136:139],a[68:71],v[40:43]
v_fma_f32 v113, v113, v153, v152
v_fma_f32 v114, v114, v153, v152
MM v[40:43],a[132:135],a[36:39],v[40:43]
TR a[188:189],v235 offset:14336
TR a[190:191],v235 offset:15360
v_fma_f32 v115, v115, v153, v152
MM v[40:43],a[128:131],a[4:7],v[40:43]
s_waitcnt lgkmcnt(15)
buffer_load_dwordx4 v240, s[24:27], s66 offen offset:2048 lds
v_fma_f32 v116, v116, v153, v152
v_fma_f32 v117, v117, v153, v152
MM v[44:47],a[144:147],a[4:7],0
s_add_u32 s66, s66, 9216
TR a[128:129],v144 offset:0
TR a[130:131],v144 offset:2048
v_fma_f32 v118, v118, v153, v152
v_fma_f32 v119, v119, v153, v152
MM v[44:47],a[148:151],a[36:39],v[44:47]
CV v216,v112,v113
CV v217,v114,v115
MM v[44:47],a[152:155],a[68:71],v[44:47]
TR a[132:133],v144 offset:4096
TR a[134:135],v144 offset:6144
CV v218,v116,v117
MM v[44:47],a[156:159],a[100:103],v[44:47]
buffer_load_dwordx4 v240, s[24:27], s66 offen offset:3072 lds
CV v219,v118,v119
MM v[36:39],a[156:159],a[96:99],0
TR a[136:137],v144 offset:8192
TR a[138:139],v144 offset:10240
MM v[36:39],a[152:155],a[64:67],v[36:39]
s_xor_b32 s66, s94, 0
v_lshl_add_u32 v224, v250, 3, v224
v_add_u32 v224, s61, v224
MM v[36:39],a[148:151],a[32:35],v[36:39]
s_mul_i32 s66, s66, 163840
TR a[140:141],v144 offset:12288
TR a[142:143],v144 offset:14336
MM v[36:39],a[144:147],a[0:3],v[36:39]
s_waitcnt lgkmcnt(15)
buffer_store_dwordx4 v[216:219], v248, s[20:23], s78 offen offset:3072 sc1
AR v120,a120
AR v121,a121
AR v122,a122
AR v123,a123
AR v124,a124
s_waitcnt vmcnt(31)
MM v[128:131],a[160:163],a[8:11],v[128:131]
s_add_u32 s66, s66, s88
TR a[144:145],v145 offset:0
TR a[146:147],v145 offset:2048
AR v125,a125
AR v126,a126
MM v[128:131],a[164:167],a[40:43],v[128:131]
s_add_u32 m0, s89, 131072
AR v127,a127
v_fma_f32 v120, v120, v157, v156
MM v[128:131],a[168:171],a[72:75],v[128:131]
TR a[148:149],v145 offset:4096
TR a[150:151],v145 offset:6144
v_fma_f32 v121, v121, v157, v156
v_fma_f32 v122, v122, v157, v156
MM v[128:131],a[172:175],a[104:107],v[128:131]
s_waitcnt vmcnt(23)
buffer_load_dwordx4 v252, s[24:27], s66 offen offset:0 lds
v_fma_f32 v123, v123, v157, v156
MM v[136:139],a[172:175],a[108:111],v[136:139]
s_add_u32 s66, s66, 19456
TR a[152:153],v145 offset:8192
TR a[154:155],v145 offset:10240
v_fma_f32 v124, v124, v157, v156
v_fma_f32 v125, v125, v157, v156
MM v[136:139],a[168:171],a[76:79],v[136:139]
v_fma_f32 v126, v126, v157, v156
v_fma_f32 v127, v127, v157, v156
MM v[136:139],a[164:167],a[44:47],v[136:139]
TR a[156:157],v145 offset:12288
TR a[158:159],v145 offset:14336
CV v220,v120,v121
MM v[136:139],a[160:163],a[12:15],v[136:139]
s_waitcnt lgkmcnt(15)
buffer_load_dwordx4 v252, s[24:27], s66 offen offset:1024 lds
CV v221,v122,v123
CV v222,v124,v125
MM v[140:143],a[176:179],a[12:15],v[140:143]
TR a[160:161],v146 offset:0
TR a[162:163],v146 offset:2048
CV v223,v126,v127
MM v[140:143],a[180:183],a[44:47],v[140:143]
s_xor_b32 s66, s94, 0
v_mul_u32_u24 v254, 1280, v250
v_mul_u32_u24 v250, 33792, v253
MM v[140:143],a[184:187],a[76:79],v[140:143]
s_mul_i32 s66, s66, 163840
TR a[164:165],v146 offset:4096
TR a[166:167],v146 offset:6144
MM v[140:143],a[188:191],a[108:111],v[140:143]
s_add_u32 s66, s66, s87
buffer_store_dwordx4 v[220:223], v248, s[20:23], s79 offen offset:3072 sc1
MM v[132:135],a[188:191],a[104:107],v[132:135]
s_add_u32 m0, s65, 114688
TR a[168:169],v146 offset:8192
TR a[170:171],v146 offset:10240
MM v[132:135],a[184:187],a[72:75],v[132:135]
v_add_u32 v250, v250, v238
v_lshlrev_b32 v253, 4, v253
MM v[132:135],a[180:183],a[40:43],v[132:135]
TR a[172:173],v146 offset:12288
TR a[174:175],v146 offset:14336
MM v[132:135],a[176:179],a[8:11],v[132:135]
s_waitcnt lgkmcnt(15)
buffer_load_dwordx4 v240, s[24:27], s66 offen offset:0 lds
MM v[64:67],a[128:131],a[8:11],v[64:67]
s_xor_b32 s92, s49, 7
TR a[176:177],v147 offset:0
TR a[178:179],v147 offset:2048
MM v[64:67],a[132:135],a[40:43],v[64:67]
s_mul_i32 s92, s92, 17408
s_add_u32 s92, s83, s92
v_bfe_u32 v238, v248, 4, 3
MM v[64:67],a[136:139],a[72:75],v[64:67]
s_add_u32 s66, s66, 9216
TR a[180:181],v147 offset:4096
TR a[182:183],v147 offset:6144
MM v[64:67],a[140:143],a[104:107],v[64:67]
buffer_load_dwordx4 a[192:195], v248, s[20:23], s92 offen offset:0 sc1
MM v[72:75],a[140:143],a[108:111],v[72:75]
TR a[184:185],v147 offset:8192
TR a[186:187],v147 offset:10240
MM v[72:75],a[136:139],a[76:79],v[72:75]
v_lshl_add_u32 v254, v238, 3, v254
v_bfe_u32 v238, v248, 7, 1
MM v[72:75],a[132:135],a[44:47],v[72:75]
TR a[188:189],v147 offset:12288
TR a[190:191],v147 offset:14336
MM v[72:75],a[128:131],a[12:15],v[72:75]
s_waitcnt lgkmcnt(15)
buffer_load_dwordx4 a[208:211], v248, s[20:23], s92 offen offset:1024 sc1
MM v[76:79],a[144:147],a[12:15],v[76:79]
TR a[128:129],v228 offset:0
TR a[130:131],v228 offset:2048
MM v[76:79],a[148:151],a[44:47],v[76:79]
v_mul_u32_u24 v238, 5120, v238
v_add_u32 v254, v254, v238
MM v[76:79],a[152:155],a[76:79],v[76:79]
TR a[132:133],v228 offset:4096
TR a[134:135],v228 offset:6144
MM v[76:79],a[156:159],a[108:111],v[76:79]
buffer_load_dwordx4 a[224:227], v248, s[20:23], s92 offen offset:2048 sc1
MM v[68:71],a[156:159],a[104:107],v[68:71]
TR a[136:137],v228 offset:8192
TR a[138:139],v228 offset:10240
MM v[68:71],a[152:155],a[72:75],v[68:71]
v_lshrrev_b32 v238, 6, v248
v_mul_u32_u24 v225, 80, v238
MM v[68:71],a[148:151],a[40:43],v[68:71]
TR a[140:141],v228 offset:12288
TR a[142:143],v228 offset:14336
MM v[68:71],a[144:147],a[8:11],v[68:71]
s_waitcnt lgkmcnt(15)
buffer_load_dwordx4 a[240:243], v248, s[20:23], s92 offen offset:3072 sc1
MM v[96:99],a[160:163],a[8:11],v[96:99]
TR a[144:145],v229 offset:0
TR a[146:147],v229 offset:2048
MM v[96:99],a[164:167],a[40:43],v[96:99]
s_add_u32 s92, s92, 4096
v_add3_u32 v225, v225, v251, s61
v_mul_u32_u24 v238, 1280, v238
MM v[96:99],a[168:171],a[72:75],v[96:99]
TR a[148:149],v229 offset:4096
TR a[150:151],v229 offset:6144
MM v[96:99],a[172:175],a[104:107],v[96:99]
buffer_load_dwordx4 a[196:199], v248, s[20:23], s92 offen offset:0 sc1
MM v[104:107],a[172:175],a[108:111],v[104:107]
TR a[152:153],v229 offset:8192
TR a[154:155],v229 offset:10240
MM v[104:107],a[168:171],a[76:79],v[104:107]
v_add_u32 v251, v251, v238
MM v[104:107],a[164:167],a[44:47],v[104:107]
TR a[156:157],v229 offset:12288
TR a[158:159],v229 offset:14336
MM v[104:107],a[160:163],a[12:15],v[104:107]
s_waitcnt lgkmcnt(15)
buffer_load_dwordx4 a[212:215], v248, s[20:23], s92 offen offset:1024 sc1
MM v[108:111],a[176:179],a[12:15],v[108:111]
TR a[160:161],v230 offset:0
TR a[162:163],v230 offset:2048
MM v[108:111],a[180:183],a[44:47],v[108:111]
MM v[108:111],a[184:187],a[76:79],v[108:111]
TR a[164:165],v230 offset:4096
TR a[166:167],v230 offset:6144
MM v[108:111],a[188:191],a[108:111],v[108:111]
buffer_load_dwordx4 a[228:231], v248, s[20:23], s92 offen offset:2048 sc1
MM v[100:103],a[188:191],a[104:107],v[100:103]
TR a[168:169],v230 offset:8192
TR a[170:171],v230 offset:10240
MM v[100:103],a[184:187],a[72:75],v[100:103]
MM v[100:103],a[180:183],a[40:43],v[100:103]
TR a[172:173],v230 offset:12288
TR a[174:175],v230 offset:14336
MM v[100:103],a[176:179],a[8:11],v[100:103]
s_waitcnt lgkmcnt(15)
buffer_load_dwordx4 a[244:247], v248, s[20:23], s92 offen offset:3072 sc1
MM v[0:3],a[128:131],a[8:11],v[0:3]
TR a[176:177],v231 offset:0
TR a[178:179],v231 offset:2048
MM v[0:3],a[132:135],a[40:43],v[0:3]
MM v[0:3],a[136:139],a[72:75],v[0:3]
TR a[180:181],v231 offset:4096
TR a[182:183],v231 offset:6144
MM v[0:3],a[140:143],a[104:107],v[0:3]
buffer_load_dwordx4 v240, s[24:27], s66 offen offset:1024 lds
MM v[8:11],a[140:143],a[108:111],v[8:11]
s_add_u32 s66, s66, 9216
TR a[184:185],v231 offset:8192
TR a[186:187],v231 offset:10240
MM v[8:11],a[136:139],a[76:79],v[8:11]
MM v[8:11],a[132:135],a[44:47],v[8:11]
TR a[188:189],v231 offset:12288
TR a[190:191],v231 offset:14336
MM v[8:11],a[128:131],a[12:15],v[8:11]
s_waitcnt lgkmcnt(15)
buffer_load_dwordx4 v240, s[24:27], s66 offen offset:2048 lds
s_barrier
MM v[12:15],a[144:147],a[12:15],v[12:15]
s_add_u32 s66, s66, 9216
TR a[128:129],v244 offset:16384
TR a[130:131],v244 offset:17408
)ASM"
R"ASM(
MM v[12:15],a[148:151],a[44:47],v[12:15]
LPG27_%=:
s_cbranch_execz LTR1_%=
MM v[12:15],a[152:155],a[76:79],v[12:15]
TR a[132:133],v244 offset:18432
TR a[134:135],v244 offset:19456
MM v[12:15],a[156:159],a[108:111],v[12:15]
buffer_load_dwordx4 v240, s[24:27], s66 offen offset:3072 lds
MM v[4:7],a[156:159],a[104:107],v[4:7]
TR a[136:137],v244 offset:20480
TR a[138:139],v244 offset:21504
MM v[4:7],a[152:155],a[72:75],v[4:7]
s_xor_b32 s66, s94, 0
s_mul_i32 s66, s66, 163840
s_add_u32 s66, s66, s86
MM v[4:7],a[148:151],a[40:43],v[4:7]
s_add_u32 m0, s65, 49152
TR a[140:141],v244 offset:22528
TR a[142:143],v244 offset:23552
MM v[4:7],a[144:147],a[8:11],v[4:7]
s_waitcnt lgkmcnt(15)
buffer_load_dwordx4 v240, s[24:27], s66 offen offset:0 lds
MM v[32:35],a[160:163],a[8:11],v[32:35]
s_add_u32 s66, s66, 9216
TR a[144:145],v235 offset:16384
TR a[146:147],v235 offset:17408
MM v[32:35],a[164:167],a[40:43],v[32:35]
MM v[32:35],a[168:171],a[72:75],v[32:35]
TR a[148:149],v235 offset:18432
TR a[150:151],v235 offset:19456
MM v[32:35],a[172:175],a[104:107],v[32:35]
buffer_load_dwordx4 v240, s[24:27], s66 offen offset:1024 lds
MM v[40:43],a[172:175],a[108:111],v[40:43]
s_add_u32 s66, s66, 9216
TR a[152:153],v235 offset:20480
TR a[154:155],v235 offset:21504
MM v[40:43],a[168:171],a[76:79],v[40:43]
MM v[40:43],a[164:167],a[44:47],v[40:43]
TR a[156:157],v235 offset:22528
TR a[158:159],v235 offset:23552
MM v[40:43],a[160:163],a[12:15],v[40:43]
s_waitcnt lgkmcnt(15)
buffer_load_dwordx4 v240, s[24:27], s66 offen offset:2048 lds
MM v[44:47],a[176:179],a[12:15],v[44:47]
s_lshl_b32 s97, s84, 2
TR a[160:161],v144 offset:16384
TR a[162:163],v144 offset:18432
MM v[44:47],a[180:183],a[44:47],v[44:47]
s_add_u32 s97, s97, s96
s_add_u32 s97, s97, 16
s_waitcnt vmcnt(15)
MM v[44:47],a[184:187],a[76:79],v[44:47]
s_add_u32 s66, s66, 9216
TR a[164:165],v144 offset:20480
TR a[166:167],v144 offset:22528
MM v[44:47],a[188:191],a[108:111],v[44:47]
v_mov_b32 v242, s69
s_mov_b64 exec, 1
buffer_store_dword v242, off, s[20:23], s97 offset:0 sc1
s_mov_b64 exec, -1
MM v[36:39],a[188:191],a[104:107],v[36:39]
TR a[168:169],v144 offset:24576
TR a[170:171],v144 offset:26624
MM v[36:39],a[184:187],a[72:75],v[36:39]
buffer_load_dwordx4 v240, s[24:27], s66 offen offset:3072 lds
MM v[36:39],a[180:183],a[40:43],v[36:39]
TR a[172:173],v144 offset:28672
TR a[174:175],v144 offset:30720
MM v[36:39],a[176:179],a[8:11],v[36:39]
s_waitcnt lgkmcnt(15)
s_xor_b32 s66, s94, 1
s_mul_i32 s66, s66, 163840
MM v[128:131],a[128:131],v[160:163],v[128:131]
s_add_u32 s66, s66, s88
TR a[176:177],v145 offset:16384
TR a[178:179],v145 offset:18432
MM v[128:131],a[132:135],v[176:179],v[128:131]
s_add_u32 m0, s89, 139264
MM v[128:131],a[136:139],v[192:195],v[128:131]
TR a[180:181],v145 offset:20480
TR a[182:183],v145 offset:22528
MM v[128:131],a[140:143],v[208:211],v[128:131]
buffer_load_dwordx4 v252, s[24:27], s66 offen offset:0 lds
MM v[136:139],a[140:143],v[212:215],v[136:139]
s_add_u32 s66, s66, 19456
TR a[184:185],v145 offset:24576
TR a[186:187],v145 offset:26624
MM v[136:139],a[136:139],v[196:199],v[136:139]
MM v[136:139],a[132:135],v[180:183],v[136:139]
TR a[188:189],v145 offset:28672
TR a[190:191],v145 offset:30720
MM v[136:139],a[128:131],v[164:167],v[136:139]
s_waitcnt lgkmcnt(15)
buffer_load_dwordx4 v252, s[24:27], s66 offen offset:1024 lds
MM v[140:143],a[144:147],v[164:167],v[140:143]
TR a[128:129],v146 offset:16384
TR a[130:131],v146 offset:18432
MM v[140:143],a[148:151],v[180:183],v[140:143]
s_xor_b32 s66, s94, 1
s_mul_i32 s66, s66, 163840
s_add_u32 s66, s66, s87
MM v[140:143],a[152:155],v[196:199],v[140:143]
s_add_u32 m0, s65, 65536
TR a[132:133],v146 offset:20480
TR a[134:135],v146 offset:22528
MM v[140:143],a[156:159],v[212:215],v[140:143]
buffer_load_dwordx4 v240, s[24:27], s66 offen offset:0 lds
MM v[132:135],a[156:159],v[208:211],v[132:135]
s_add_u32 s66, s66, 9216
TR a[136:137],v146 offset:24576
TR a[138:139],v146 offset:26624
MM v[132:135],a[152:155],v[192:195],v[132:135]
MM v[132:135],a[148:151],v[176:179],v[132:135]
TR a[140:141],v146 offset:28672
TR a[142:143],v146 offset:30720
MM v[132:135],a[144:147],v[160:163],v[132:135]
s_waitcnt lgkmcnt(15)
buffer_load_dwordx4 v240, s[24:27], s66 offen offset:1024 lds
MM v[64:67],a[160:163],v[160:163],v[64:67]
s_add_u32 s66, s66, 9216
TR a[144:145],v147 offset:16384
TR a[146:147],v147 offset:18432
MM v[64:67],a[164:167],v[176:179],v[64:67]
MM v[64:67],a[168:171],v[192:195],v[64:67]
TR a[148:149],v147 offset:20480
TR a[150:151],v147 offset:22528
MM v[64:67],a[172:175],v[208:211],v[64:67]
buffer_load_dwordx4 v240, s[24:27], s66 offen offset:2048 lds
MM v[72:75],a[172:175],v[212:215],v[72:75]
s_add_u32 s66, s66, 9216
TR a[152:153],v147 offset:24576
TR a[154:155],v147 offset:26624
MM v[72:75],a[168:171],v[196:199],v[72:75]
MM v[72:75],a[164:167],v[180:183],v[72:75]
TR a[156:157],v147 offset:28672
TR a[158:159],v147 offset:30720
MM v[72:75],a[160:163],v[164:167],v[72:75]
s_waitcnt lgkmcnt(15)
buffer_load_dwordx4 v240, s[24:27], s66 offen offset:3072 lds
MM v[76:79],a[176:179],v[164:167],v[76:79]
TR a[160:161],v228 offset:16384
TR a[162:163],v228 offset:18432
MM v[76:79],a[180:183],v[180:183],v[76:79]
MM v[76:79],a[184:187],v[196:199],v[76:79]
TR a[164:165],v228 offset:20480
TR a[166:167],v228 offset:22528
MM v[76:79],a[188:191],v[212:215],v[76:79]
MM v[68:71],a[188:191],v[208:211],v[68:71]
TR a[168:169],v228 offset:24576
TR a[170:171],v228 offset:26624
MM v[68:71],a[184:187],v[192:195],v[68:71]
MM v[68:71],a[180:183],v[176:179],v[68:71]
TR a[172:173],v228 offset:28672
TR a[174:175],v228 offset:30720
MM v[68:71],a[176:179],v[160:163],v[68:71]
s_waitcnt lgkmcnt(15)
MM v[96:99],a[128:131],v[160:163],v[96:99]
s_add_u32 s80, s18, 131072
TR a[176:177],v229 offset:16384
TR a[178:179],v229 offset:18432
MM v[96:99],a[132:135],v[176:179],v[96:99]
buffer_load_dwordx4 v[16:19], v251, s[20:23], s80 offen offset:0 sc1
MM v[96:99],a[136:139],v[192:195],v[96:99]
TR a[180:181],v229 offset:20480
TR a[182:183],v229 offset:22528
MM v[96:99],a[140:143],v[208:211],v[96:99]
v_mov_b32 v237, 0x3f80
v_and_b32 v236, 16, v248
MM v[104:107],a[140:143],v[212:215],v[104:107]
TR a[184:185],v229 offset:24576
TR a[186:187],v229 offset:26624
MM v[104:107],a[136:139],v[196:199],v[104:107]
s_add_u32 s80, s18, 151552
buffer_load_dwordx4 v[20:23], v251, s[20:23], s80 offen offset:0 sc1
MM v[104:107],a[132:135],v[180:183],v[104:107]
TR a[188:189],v229 offset:28672
TR a[190:191],v229 offset:30720
MM v[104:107],a[128:131],v[164:167],v[104:107]
s_waitcnt lgkmcnt(15)
v_lshlrev_b32 v236, v236, v237
s_mov_b32 s28, 0x30003
MM v[108:111],a[144:147],v[164:167],v[108:111]
s_mov_b32 s29, s28
TR a[128:129],v230 offset:16384
TR a[130:131],v230 offset:18432
MM v[108:111],a[148:151],v[180:183],v[108:111]
s_add_u32 s80, s63, 131072
v_lshrrev_b32 v243, 2, v248
v_min_u32 v243, 12, v243
MM v[108:111],a[152:155],v[196:199],v[108:111]
TR a[132:133],v230 offset:20480
TR a[134:135],v230 offset:22528
MM v[108:111],a[156:159],v[212:215],v[108:111]
buffer_load_dword v242, v243, s[20:23], s96 offen offset:16 sc1
buffer_load_dwordx2 v[150:151], v254, s[20:23], s80 offen offset:0 sc1
MM v[100:103],a[156:159],v[208:211],v[100:103]
TR a[136:137],v230 offset:24576
TR a[138:139],v230 offset:26624
MM v[100:103],a[152:155],v[192:195],v[100:103]
v_cndmask_b32 v48, 0, v236, s[28:29]
s_lshl_b64 s[28:29], s[28:29], 2
MM v[100:103],a[148:151],v[176:179],v[100:103]
TR a[140:141],v230 offset:28672
TR a[142:143],v230 offset:30720
MM v[100:103],a[144:147],v[160:163],v[100:103]
s_waitcnt lgkmcnt(15)
v_cndmask_b32 v49, 0, v236, s[28:29]
s_lshl_b64 s[28:29], s[28:29], 2
MM v[0:3],a[160:163],v[160:163],v[0:3]
TR a[144:145],v231 offset:16384
TR a[146:147],v231 offset:18432
MM v[0:3],a[164:167],v[176:179],v[0:3]
v_cndmask_b32 v50, 0, v236, s[28:29]
s_lshl_b64 s[28:29], s[28:29], 2
MM v[0:3],a[168:171],v[192:195],v[0:3]
TR a[148:149],v231 offset:20480
TR a[150:151],v231 offset:22528
MM v[0:3],a[172:175],v[208:211],v[0:3]
v_cndmask_b32 v51, 0, v236, s[28:29]
s_lshl_b64 s[28:29], s[28:29], 2
MM v[8:11],a[172:175],v[212:215],v[8:11]
TR a[152:153],v231 offset:24576
TR a[154:155],v231 offset:26624
MM v[8:11],a[168:171],v[196:199],v[8:11]
v_cndmask_b32 v52, 0, v236, s[28:29]
s_lshl_b64 s[28:29], s[28:29], 2
MM v[8:11],a[164:167],v[180:183],v[8:11]
TR a[156:157],v231 offset:28672
TR a[158:159],v231 offset:30720
MM v[8:11],a[160:163],v[164:167],v[8:11]
s_waitcnt lgkmcnt(15)
v_cndmask_b32 v53, 0, v236, s[28:29]
s_lshl_b64 s[28:29], s[28:29], 2
s_barrier
MM v[12:15],a[176:179],v[164:167],v[12:15]
s_xor_b32 s66, s94, 2
TR a[160:161],v244 offset:24576
TR a[162:163],v244 offset:25600
MM v[12:15],a[180:183],v[180:183],v[12:15]
s_mul_i32 s66, s66, 163840
v_cndmask_b32 v54, 0, v236, s[28:29]
s_lshl_b64 s[28:29], s[28:29], 2
MM v[12:15],a[184:187],v[196:199],v[12:15]
s_add_u32 s66, s66, s88
TR a[164:165],v244 offset:26624
TR a[166:167],v244 offset:27648
MM v[12:15],a[188:191],v[212:215],v[12:15]
s_add_u32 m0, s89, 147456
v_cndmask_b32 v55, 0, v236, s[28:29]
s_mov_b32 s28, 0x300003
MM v[4:7],a[188:191],v[208:211],v[4:7]
s_mov_b32 s29, 0x30000300
TR a[168:169],v244 offset:28672
TR a[170:171],v244 offset:29696
MM v[4:7],a[184:187],v[192:195],v[4:7]
buffer_load_dwordx4 v252, s[24:27], s66 offen offset:0 lds
MM v[4:7],a[180:183],v[176:179],v[4:7]
s_add_u32 s66, s66, 19456
TR a[172:173],v244 offset:30720
TR a[174:175],v244 offset:31744
MM v[4:7],a[176:179],v[160:163],v[4:7]
s_waitcnt lgkmcnt(15)
v_mov_b32 v56, 0
v_mov_b32 v57, 0
MM v[32:35],a[128:131],v[160:163],v[32:35]
TR a[176:177],v235 offset:24576
TR a[178:179],v235 offset:25600
MM v[32:35],a[132:135],v[176:179],v[32:35]
buffer_load_dwordx4 v252, s[24:27], s66 offen offset:1024 lds
MM v[32:35],a[136:139],v[192:195],v[32:35]
TR a[180:181],v235 offset:26624
TR a[182:183],v235 offset:27648
MM v[32:35],a[140:143],v[208:211],v[32:35]
s_xor_b32 s66, s94, 2
v_mov_b32 v60, 0
v_mov_b32 v61, 0
MM v[40:43],a[140:143],v[212:215],v[40:43]
s_mul_i32 s66, s66, 163840
TR a[184:185],v235 offset:28672
TR a[186:187],v235 offset:29696
MM v[40:43],a[136:139],v[196:199],v[40:43]
s_add_u32 s66, s66, s87
v_cndmask_b32 v58, 0, v236, s[28:29]
s_lshl_b64 s[28:29], s[28:29], 2
MM v[40:43],a[132:135],v[180:183],v[40:43]
s_add_u32 m0, s65, 81920
TR a[188:189],v235 offset:30720
TR a[190:191],v235 offset:31744
MM v[40:43],a[128:131],v[164:167],v[40:43]
s_waitcnt lgkmcnt(15)
buffer_load_dwordx4 v240, s[24:27], s66 offen offset:0 lds
MM v[44:47],a[144:147],v[164:167],v[44:47]
s_add_u32 s66, s66, 9216
TR a[128:129],v144 offset:32768
TR a[130:131],v144 offset:34816
MM v[44:47],a[148:151],v[180:183],v[44:47]
v_cndmask_b32 v59, 0, v236, s[28:29]
MM v[44:47],a[152:155],v[196:199],v[44:47]
TR a[132:133],v144 offset:36864
TR a[134:135],v144 offset:38912
MM v[44:47],a[156:159],v[212:215],v[44:47]
buffer_load_dwordx4 v240, s[24:27], s66 offen offset:1024 lds
MM v[36:39],a[156:159],v[208:211],v[36:39]
s_add_u32 s66, s66, 9216
TR a[136:137],v144 offset:40960
TR a[138:139],v144 offset:43008
MM v[36:39],a[152:155],v[192:195],v[36:39]
MM v[36:39],a[148:151],v[176:179],v[36:39]
TR a[140:141],v144 offset:45056
TR a[142:143],v144 offset:47104
MM v[36:39],a[144:147],v[160:163],v[36:39]
s_waitcnt lgkmcnt(15)
buffer_load_dwordx4 v240, s[24:27], s66 offen offset:2048 lds
s_waitcnt vmcnt(27)
MM v[128:131],a[160:163],a[192:195],v[128:131]
s_add_u32 s80, s18, 0
TR a[144:145],v145 offset:32768
TR a[146:147],v145 offset:34816
MM v[128:131],a[164:167],a[208:211],v[128:131]
s_add_u32 s66, s66, 9216
buffer_load_dwordx4 v[24:27], v251, s[32:35], s80 offen offset:0
MM v[128:131],a[168:171],a[224:227],v[128:131]
TR a[148:149],v145 offset:36864
TR a[150:151],v145 offset:38912
MM v[128:131],a[172:175],a[240:243],v[128:131]
s_waitcnt vmcnt(24)
MM v[136:139],a[172:175],a[244:247],v[136:139]
TR a[152:153],v145 offset:40960
TR a[154:155],v145 offset:43008
MM v[136:139],a[168:171],a[228:231],v[136:139]
s_add_u32 s80, s18, 20480
buffer_load_dwordx4 v[28:31], v251, s[32:35], s80 offen offset:0
MM v[136:139],a[164:167],a[212:215],v[136:139]
)ASM"
R"ASM(
TR a[156:157],v145 offset:45056
TR a[158:159],v145 offset:47104
MM v[136:139],a[160:163],a[196:199],v[136:139]
s_waitcnt lgkmcnt(15)
MM v[140:143],a[176:179],a[196:199],v[140:143]
TR a[160:161],v146 offset:32768
TR a[162:163],v146 offset:34816
MM v[140:143],a[180:183],a[212:215],v[140:143]
s_add_u32 s80, s63, 0
buffer_load_dwordx2 v[148:149], v254, s[32:35], s80 offen offset:0
MM v[140:143],a[184:187],a[228:231],v[140:143]
TR a[164:165],v146 offset:36864
TR a[166:167],v146 offset:38912
MM v[140:143],a[188:191],a[244:247],v[140:143]
buffer_load_dwordx4 v240, s[24:27], s66 offen offset:3072 lds
MM v[132:135],a[188:191],a[240:243],v[132:135]
TR a[168:169],v146 offset:40960
TR a[170:171],v146 offset:43008
MM v[132:135],a[184:187],a[224:227],v[132:135]
MM v[132:135],a[180:183],a[208:211],v[132:135]
TR a[172:173],v146 offset:45056
TR a[174:175],v146 offset:47104
MM v[132:135],a[176:179],a[192:195],v[132:135]
s_waitcnt lgkmcnt(15)
MM v[64:67],a[128:131],a[192:195],v[64:67]
s_waitcnt vmcnt(10)
TR a[176:177],v147 offset:32768
TR a[178:179],v147 offset:34816
MM v[64:67],a[132:135],a[208:211],v[64:67]
v_cmp_ne_u32 vcc, s69, v242
s_cbranch_vccnz LRPO1_%=
LRPO1b_%=:
MM v[64:67],a[136:139],a[224:227],v[64:67]
s_xor_b32 s92, s49, 1
TR a[180:181],v147 offset:36864
TR a[182:183],v147 offset:38912
MM v[64:67],a[140:143],a[240:243],v[64:67]
s_mul_i32 s92, s92, 17408
s_add_u32 s92, s83, s92
s_add_u32 s92, s92, 8192
MM v[72:75],a[140:143],a[244:247],v[72:75]
TR a[184:185],v147 offset:40960
TR a[186:187],v147 offset:43008
MM v[72:75],a[136:139],a[228:231],v[72:75]
buffer_load_dwordx4 a[16:19], v248, s[20:23], s92 offen offset:0 sc1
MM v[72:75],a[132:135],a[212:215],v[72:75]
TR a[188:189],v147 offset:45056
TR a[190:191],v147 offset:47104
MM v[72:75],a[128:131],a[196:199],v[72:75]
s_waitcnt lgkmcnt(15)
buffer_load_dwordx4 a[48:51], v248, s[20:23], s92 offen offset:1024 sc1
MM v[76:79],a[144:147],a[196:199],v[76:79]
TR a[128:129],v228 offset:32768
TR a[130:131],v228 offset:34816
MM v[76:79],a[148:151],a[212:215],v[76:79]
MM v[76:79],a[152:155],a[228:231],v[76:79]
TR a[132:133],v228 offset:36864
TR a[134:135],v228 offset:38912
MM v[76:79],a[156:159],a[244:247],v[76:79]
buffer_load_dwordx4 a[80:83], v248, s[20:23], s92 offen offset:2048 sc1
MM v[68:71],a[156:159],a[240:243],v[68:71]
TR a[136:137],v228 offset:40960
TR a[138:139],v228 offset:43008
MM v[68:71],a[152:155],a[224:227],v[68:71]
MM v[68:71],a[148:151],a[208:211],v[68:71]
TR a[140:141],v228 offset:45056
TR a[142:143],v228 offset:47104
MM v[68:71],a[144:147],a[192:195],v[68:71]
s_waitcnt lgkmcnt(15)
buffer_load_dwordx4 a[112:115], v248, s[20:23], s92 offen offset:3072 sc1
MM v[96:99],a[160:163],a[192:195],v[96:99]
s_add_u32 s92, s92, 4096
TR a[144:145],v229 offset:32768
TR a[146:147],v229 offset:34816
MM v[96:99],a[164:167],a[208:211],v[96:99]
MM v[96:99],a[168:171],a[224:227],v[96:99]
TR a[148:149],v229 offset:36864
TR a[150:151],v229 offset:38912
MM v[96:99],a[172:175],a[240:243],v[96:99]
buffer_load_dwordx4 a[20:23], v248, s[20:23], s92 offen offset:0 sc1
MM v[104:107],a[172:175],a[244:247],v[104:107]
TR a[152:153],v229 offset:40960
TR a[154:155],v229 offset:43008
MM v[104:107],a[168:171],a[228:231],v[104:107]
MM v[104:107],a[164:167],a[212:215],v[104:107]
TR a[156:157],v229 offset:45056
TR a[158:159],v229 offset:47104
MM v[104:107],a[160:163],a[196:199],v[104:107]
s_waitcnt lgkmcnt(15)
buffer_load_dwordx4 a[52:55], v248, s[20:23], s92 offen offset:1024 sc1
MM v[108:111],a[176:179],a[196:199],v[108:111]
TR a[160:161],v230 offset:32768
TR a[162:163],v230 offset:34816
MM v[108:111],a[180:183],a[212:215],v[108:111]
MM v[108:111],a[184:187],a[228:231],v[108:111]
TR a[164:165],v230 offset:36864
TR a[166:167],v230 offset:38912
MM v[108:111],a[188:191],a[244:247],v[108:111]
buffer_load_dwordx4 a[84:87], v248, s[20:23], s92 offen offset:2048 sc1
MM v[100:103],a[188:191],a[240:243],v[100:103]
TR a[168:169],v230 offset:40960
TR a[170:171],v230 offset:43008
MM v[100:103],a[184:187],a[224:227],v[100:103]
MM v[100:103],a[180:183],a[208:211],v[100:103]
TR a[172:173],v230 offset:45056
TR a[174:175],v230 offset:47104
MM v[100:103],a[176:179],a[192:195],v[100:103]
s_waitcnt lgkmcnt(15)
buffer_load_dwordx4 a[116:119], v248, s[20:23], s92 offen offset:3072 sc1
MM v[0:3],a[128:131],a[192:195],v[0:3]
TR a[176:177],v231 offset:32768
TR a[178:179],v231 offset:34816
MM v[0:3],a[132:135],a[208:211],v[0:3]
MM v[0:3],a[136:139],a[224:227],v[0:3]
TR a[180:181],v231 offset:36864
TR a[182:183],v231 offset:38912
MM v[0:3],a[140:143],a[240:243],v[0:3]
MM v[8:11],a[140:143],a[244:247],v[8:11]
TR a[184:185],v231 offset:40960
TR a[186:187],v231 offset:43008
MM v[8:11],a[136:139],a[228:231],v[8:11]
MM v[8:11],a[132:135],a[212:215],v[8:11]
TR a[188:189],v231 offset:45056
TR a[190:191],v231 offset:47104
MM v[8:11],a[128:131],a[196:199],v[8:11]
s_waitcnt lgkmcnt(15)
s_barrier
MM v[12:15],a[144:147],a[196:199],v[12:15]
s_xor_b32 s66, s94, 3
TR a[128:129],v244 offset:0
TR a[130:131],v244 offset:1024
MM v[12:15],a[148:151],a[212:215],v[12:15]
s_mul_i32 s66, s66, 163840
s_add_u32 s66, s66, s88
s_add_u32 m0, s89, 155648
MM v[12:15],a[152:155],a[228:231],v[12:15]
TR a[132:133],v244 offset:2048
TR a[134:135],v244 offset:3072
MM v[12:15],a[156:159],a[244:247],v[12:15]
buffer_load_dwordx4 v252, s[24:27], s66 offen offset:0 lds
MM v[4:7],a[156:159],a[240:243],v[4:7]
s_add_u32 s66, s66, 19456
TR a[136:137],v244 offset:4096
TR a[138:139],v244 offset:5120
MM v[4:7],a[152:155],a[224:227],v[4:7]
MM v[4:7],a[148:151],a[208:211],v[4:7]
TR a[140:141],v244 offset:6144
TR a[142:143],v244 offset:7168
MM v[4:7],a[144:147],a[192:195],v[4:7]
s_waitcnt lgkmcnt(15)
buffer_load_dwordx4 v252, s[24:27], s66 offen offset:1024 lds
MM v[32:35],a[160:163],a[192:195],v[32:35]
TR a[144:145],v235 offset:0
TR a[146:147],v235 offset:1024
MM v[32:35],a[164:167],a[208:211],v[32:35]
s_xor_b32 s66, s94, 3
s_mul_i32 s66, s66, 163840
s_add_u32 s66, s66, s87
MM v[32:35],a[168:171],a[224:227],v[32:35]
s_add_u32 m0, s65, 98304
TR a[148:149],v235 offset:2048
TR a[150:151],v235 offset:3072
MM v[32:35],a[172:175],a[240:243],v[32:35]
buffer_load_dwordx4 v240, s[24:27], s66 offen offset:0 lds
MM v[40:43],a[172:175],a[244:247],v[40:43]
s_add_u32 s66, s66, 9216
TR a[152:153],v235 offset:4096
TR a[154:155],v235 offset:5120
MM v[40:43],a[168:171],a[228:231],v[40:43]
MM v[40:43],a[164:167],a[212:215],v[40:43]
TR a[156:157],v235 offset:6144
TR a[158:159],v235 offset:7168
MM v[40:43],a[160:163],a[196:199],v[40:43]
s_waitcnt lgkmcnt(15)
buffer_load_dwordx4 v240, s[24:27], s66 offen offset:1024 lds
MM v[44:47],a[176:179],a[196:199],v[44:47]
s_add_u32 s66, s66, 9216
TR a[160:161],v144 offset:49152
TR a[162:163],v144 offset:51200
MM v[44:47],a[180:183],a[212:215],v[44:47]
MM v[44:47],a[184:187],a[228:231],v[44:47]
TR a[164:165],v144 offset:53248
TR a[166:167],v144 offset:55296
MM v[44:47],a[188:191],a[244:247],v[44:47]
buffer_load_dwordx4 v240, s[24:27], s66 offen offset:2048 lds
MM v[36:39],a[188:191],a[240:243],v[36:39]
s_add_u32 s66, s66, 9216
TR a[168:169],v144 offset:57344
TR a[170:171],v144 offset:59392
MM v[36:39],a[184:187],a[224:227],v[36:39]
MM v[36:39],a[180:183],a[208:211],v[36:39]
TR a[172:173],v144 offset:61440
TR a[174:175],v144 offset:63488
MM v[36:39],a[176:179],a[192:195],v[36:39]
s_waitcnt lgkmcnt(15)
buffer_load_dwordx4 v240, s[24:27], s66 offen offset:3072 lds
MM v[128:131],a[128:131],v[168:171],v[128:131]
TR a[176:177],v145 offset:49152
TR a[178:179],v145 offset:51200
MM v[128:131],a[132:135],v[184:187],v[128:131]
MM v[128:131],a[136:139],v[200:203],v[128:131]
TR a[180:181],v145 offset:53248
TR a[182:183],v145 offset:55296
MM v[128:131],a[140:143],v[216:219],v[128:131]
MM v[136:139],a[140:143],v[220:223],v[136:139]
TR a[184:185],v145 offset:57344
TR a[186:187],v145 offset:59392
MM v[136:139],a[136:139],v[204:207],v[136:139]
MM v[136:139],a[132:135],v[188:191],v[136:139]
TR a[188:189],v145 offset:61440
TR a[190:191],v145 offset:63488
MM v[136:139],a[128:131],v[172:175],v[136:139]
s_waitcnt lgkmcnt(15)
MM v[140:143],a[144:147],v[172:175],v[140:143]
s_xor_b32 s92, s49, 2
TR a[128:129],v146 offset:49152
TR a[130:131],v146 offset:51200
MM v[140:143],a[148:151],v[188:191],v[140:143]
s_mul_i32 s92, s92, 17408
s_add_u32 s92, s83, s92
LPG28_%=:
s_cbranch_execz LTR1_%=
s_add_u32 s92, s92, 8192
MM v[140:143],a[152:155],v[204:207],v[140:143]
TR a[132:133],v146 offset:53248
TR a[134:135],v146 offset:55296
MM v[140:143],a[156:159],v[220:223],v[140:143]
buffer_load_dwordx4 a[24:27], v248, s[20:23], s92 offen offset:0 sc1
MM v[132:135],a[156:159],v[216:219],v[132:135]
TR a[136:137],v146 offset:57344
TR a[138:139],v146 offset:59392
MM v[132:135],a[152:155],v[200:203],v[132:135]
buffer_load_dwordx4 a[56:59], v248, s[20:23], s92 offen offset:1024 sc1
MM v[132:135],a[148:151],v[184:187],v[132:135]
TR a[140:141],v146 offset:61440
TR a[142:143],v146 offset:63488
MM v[132:135],a[144:147],v[168:171],v[132:135]
s_waitcnt lgkmcnt(15)
MM v[64:67],a[160:163],v[168:171],v[64:67]
TR a[144:145],v147 offset:49152
TR a[146:147],v147 offset:51200
MM v[64:67],a[164:167],v[184:187],v[64:67]
buffer_load_dwordx4 a[88:91], v248, s[20:23], s92 offen offset:2048 sc1
MM v[64:67],a[168:171],v[200:203],v[64:67]
TR a[148:149],v147 offset:53248
TR a[150:151],v147 offset:55296
MM v[64:67],a[172:175],v[216:219],v[64:67]
MM v[72:75],a[172:175],v[220:223],v[72:75]
TR a[152:153],v147 offset:57344
TR a[154:155],v147 offset:59392
MM v[72:75],a[168:171],v[204:207],v[72:75]
buffer_load_dwordx4 a[120:123], v248, s[20:23], s92 offen offset:3072 sc1
MM v[72:75],a[164:167],v[188:191],v[72:75]
s_add_u32 s92, s92, 4096
TR a[156:157],v147 offset:61440
TR a[158:159],v147 offset:63488
MM v[72:75],a[160:163],v[172:175],v[72:75]
s_waitcnt lgkmcnt(15)
MM v[76:79],a[176:179],v[172:175],v[76:79]
TR a[160:161],v230 offset:49152
TR a[162:163],v230 offset:51200
MM v[76:79],a[180:183],v[188:191],v[76:79]
buffer_load_dwordx4 a[28:31], v248, s[20:23], s92 offen offset:0 sc1
MM v[76:79],a[184:187],v[204:207],v[76:79]
TR a[164:165],v230 offset:53248
TR a[166:167],v230 offset:55296
MM v[76:79],a[188:191],v[220:223],v[76:79]
MM v[68:71],a[188:191],v[216:219],v[68:71]
TR a[168:169],v230 offset:57344
TR a[170:171],v230 offset:59392
MM v[68:71],a[184:187],v[200:203],v[68:71]
buffer_load_dwordx4 a[60:63], v248, s[20:23], s92 offen offset:1024 sc1
MM v[68:71],a[180:183],v[184:187],v[68:71]
TR a[172:173],v230 offset:61440
TR a[174:175],v230 offset:63488
MM v[68:71],a[176:179],v[168:171],v[68:71]
s_waitcnt lgkmcnt(15)
MM v[96:99],a[128:131],v[168:171],v[96:99]
TR a[176:177],v231 offset:49152
TR a[178:179],v231 offset:51200
MM v[96:99],a[132:135],v[184:187],v[96:99]
buffer_load_dwordx4 a[92:95], v248, s[20:23], s92 offen offset:2048 sc1
MM v[96:99],a[136:139],v[200:203],v[96:99]
TR a[180:181],v231 offset:53248
TR a[182:183],v231 offset:55296
MM v[96:99],a[140:143],v[216:219],v[96:99]
MM v[104:107],a[140:143],v[220:223],v[104:107]
TR a[184:185],v231 offset:57344
TR a[186:187],v231 offset:59392
MM v[104:107],a[136:139],v[204:207],v[104:107]
buffer_load_dwordx4 a[124:127], v248, s[20:23], s92 offen offset:3072 sc1
MM v[104:107],a[132:135],v[188:191],v[104:107]
s_xor_b32 s92, s49, 3
TR a[188:189],v231 offset:61440
TR a[190:191],v231 offset:63488
MM v[104:107],a[128:131],v[172:175],v[104:107]
s_waitcnt lgkmcnt(15)
s_mul_i32 s92, s92, 17408
s_add_u32 s92, s83, s92
s_barrier
MM v[108:111],a[144:147],v[172:175],v[108:111]
s_add_u32 s92, s92, 8192
TR a[128:129],v244 offset:8192
TR a[130:131],v244 offset:9216
MM v[108:111],a[148:151],v[188:191],v[108:111]
buffer_load_dwordx4 a[200:203], v248, s[20:23], s92 offen offset:0 sc1
MM v[108:111],a[152:155],v[204:207],v[108:111]
TR a[132:133],v244 offset:10240
TR a[134:135],v244 offset:11264
MM v[108:111],a[156:159],v[220:223],v[108:111]
MM v[100:103],a[156:159],v[216:219],v[100:103]
TR a[136:137],v244 offset:12288
TR a[138:139],v244 offset:13312
MM v[100:103],a[152:155],v[200:203],v[100:103]
buffer_load_dwordx4 a[216:219], v248, s[20:23], s92 offen offset:1024 sc1
MM v[100:103],a[148:151],v[184:187],v[100:103]
)ASM"
R"ASM(
TR a[140:141],v244 offset:14336
TR a[142:143],v244 offset:15360
MM v[100:103],a[144:147],v[168:171],v[100:103]
s_waitcnt lgkmcnt(15)
MM v[32:35],a[160:163],v[168:171],v[32:35]
TR a[144:145],v235 offset:8192
TR a[146:147],v235 offset:9216
MM v[32:35],a[164:167],v[184:187],v[32:35]
buffer_load_dwordx4 a[232:235], v248, s[20:23], s92 offen offset:2048 sc1
MM v[32:35],a[168:171],v[200:203],v[32:35]
TR a[148:149],v235 offset:10240
TR a[150:151],v235 offset:11264
MM v[32:35],a[172:175],v[216:219],v[32:35]
MM v[40:43],a[172:175],v[220:223],v[40:43]
TR a[152:153],v235 offset:12288
TR a[154:155],v235 offset:13312
MM v[40:43],a[168:171],v[204:207],v[40:43]
buffer_load_dwordx4 a[248:251], v248, s[20:23], s92 offen offset:3072 sc1
MM v[40:43],a[164:167],v[188:191],v[40:43]
s_add_u32 s92, s92, 4096
TR a[156:157],v235 offset:14336
TR a[158:159],v235 offset:15360
MM v[40:43],a[160:163],v[172:175],v[40:43]
s_waitcnt lgkmcnt(15)
MM v[44:47],a[176:179],v[172:175],v[44:47]
TR a[160:161],v144 offset:0
TR a[162:163],v144 offset:2048
MM v[44:47],a[180:183],v[188:191],v[44:47]
buffer_load_dwordx4 a[204:207], v248, s[20:23], s92 offen offset:0 sc1
MM v[44:47],a[184:187],v[204:207],v[44:47]
TR a[164:165],v144 offset:4096
TR a[166:167],v144 offset:6144
MM v[44:47],a[188:191],v[220:223],v[44:47]
MM v[36:39],a[188:191],v[216:219],v[36:39]
TR a[168:169],v144 offset:8192
TR a[170:171],v144 offset:10240
MM v[36:39],a[184:187],v[200:203],v[36:39]
buffer_load_dwordx4 a[220:223], v248, s[20:23], s92 offen offset:1024 sc1
MM v[36:39],a[180:183],v[184:187],v[36:39]
TR a[172:173],v144 offset:12288
TR a[174:175],v144 offset:14336
MM v[36:39],a[176:179],v[168:171],v[36:39]
s_waitcnt lgkmcnt(15)
s_waitcnt vmcnt(24)
MM v[128:131],a[128:131],a[16:19],v[128:131]
TR a[176:177],v145 offset:0
TR a[178:179],v145 offset:2048
MM v[128:131],a[132:135],a[48:51],v[128:131]
buffer_load_dwordx4 a[236:239], v248, s[20:23], s92 offen offset:2048 sc1
MM v[128:131],a[136:139],a[80:83],v[128:131]
TR a[180:181],v145 offset:4096
TR a[182:183],v145 offset:6144
MM v[128:131],a[140:143],a[112:115],v[128:131]
s_waitcnt vmcnt(21)
MM v[136:139],a[140:143],a[116:119],v[136:139]
TR a[184:185],v145 offset:8192
TR a[186:187],v145 offset:10240
MM v[136:139],a[136:139],a[84:87],v[136:139]
buffer_load_dwordx4 a[252:255], v248, s[20:23], s92 offen offset:3072 sc1
MM v[136:139],a[132:135],a[52:55],v[136:139]
TR a[188:189],v145 offset:12288
TR a[190:191],v145 offset:14336
MM v[136:139],a[128:131],a[20:23],v[136:139]
s_waitcnt lgkmcnt(15)
MM v[140:143],a[144:147],a[20:23],v[140:143]
TR a[128:129],v146 offset:0
TR a[130:131],v146 offset:2048
MM v[140:143],a[148:151],a[52:55],v[140:143]
MM v[140:143],a[152:155],a[84:87],v[140:143]
TR a[132:133],v146 offset:4096
TR a[134:135],v146 offset:6144
MM v[140:143],a[156:159],a[116:119],v[140:143]
MM v[132:135],a[156:159],a[112:115],v[132:135]
TR a[136:137],v146 offset:8192
TR a[138:139],v146 offset:10240
MM v[132:135],a[152:155],a[80:83],v[132:135]
MM v[132:135],a[148:151],a[48:51],v[132:135]
TR a[140:141],v146 offset:12288
TR a[142:143],v146 offset:14336
MM v[132:135],a[144:147],a[16:19],v[132:135]
s_waitcnt lgkmcnt(15)
MM v[64:67],a[160:163],a[16:19],v[64:67]
TR a[144:145],v147 offset:0
TR a[146:147],v147 offset:2048
MM v[64:67],a[164:167],a[48:51],v[64:67]
MM v[64:67],a[168:171],a[80:83],v[64:67]
TR a[148:149],v147 offset:4096
TR a[150:151],v147 offset:6144
MM v[64:67],a[172:175],a[112:115],v[64:67]
MM v[72:75],a[172:175],a[116:119],v[72:75]
TR a[152:153],v147 offset:8192
TR a[154:155],v147 offset:10240
MM v[72:75],a[168:171],a[84:87],v[72:75]
MM v[72:75],a[164:167],a[52:55],v[72:75]
TR a[156:157],v147 offset:12288
TR a[158:159],v147 offset:14336
MM v[72:75],a[160:163],a[20:23],v[72:75]
s_waitcnt lgkmcnt(15)
s_waitcnt vmcnt(16)
s_barrier
MM v[76:79],a[176:179],a[20:23],v[76:79]
s_xor_b32 s66, s94, 1
TR a[160:161],v244 offset:16384
TR a[162:163],v244 offset:17408
MM v[76:79],a[180:183],a[52:55],v[76:79]
s_mul_i32 s66, s66, 163840
s_add_u32 s66, s66, s86
s_add_u32 m0, s65, 0
MM v[76:79],a[184:187],a[84:87],v[76:79]
TR a[164:165],v244 offset:18432
TR a[166:167],v244 offset:19456
MM v[76:79],a[188:191],a[116:119],v[76:79]
buffer_load_dwordx4 v240, s[24:27], s66 offen offset:0 lds
MM v[68:71],a[188:191],a[112:115],v[68:71]
s_add_u32 s66, s66, 9216
TR a[168:169],v244 offset:20480
TR a[170:171],v244 offset:21504
MM v[68:71],a[184:187],a[80:83],v[68:71]
MM v[68:71],a[180:183],a[48:51],v[68:71]
TR a[172:173],v244 offset:22528
TR a[174:175],v244 offset:23552
MM v[68:71],a[176:179],a[16:19],v[68:71]
s_waitcnt lgkmcnt(15)
buffer_load_dwordx4 v240, s[24:27], s66 offen offset:1024 lds
MM v[96:99],a[128:131],a[16:19],v[96:99]
s_add_u32 s66, s66, 9216
TR a[176:177],v244 offset:24576
TR a[178:179],v244 offset:25600
MM v[96:99],a[132:135],a[48:51],v[96:99]
MM v[96:99],a[136:139],a[80:83],v[96:99]
TR a[180:181],v244 offset:26624
TR a[182:183],v244 offset:27648
MM v[96:99],a[140:143],a[112:115],v[96:99]
buffer_load_dwordx4 v240, s[24:27], s66 offen offset:2048 lds
MM v[104:107],a[140:143],a[116:119],v[104:107]
s_add_u32 s66, s66, 9216
TR a[184:185],v244 offset:28672
TR a[186:187],v244 offset:29696
MM v[104:107],a[136:139],a[84:87],v[104:107]
MM v[104:107],a[132:135],a[52:55],v[104:107]
TR a[188:189],v244 offset:30720
TR a[190:191],v244 offset:31744
MM v[104:107],a[128:131],a[20:23],v[104:107]
s_waitcnt lgkmcnt(15)
buffer_load_dwordx4 v240, s[24:27], s66 offen offset:3072 lds
MM v[108:111],a[144:147],a[20:23],v[108:111]
TR a[128:129],v235 offset:16384
TR a[130:131],v235 offset:17408
MM v[108:111],a[148:151],a[52:55],v[108:111]
s_xor_b32 s66, s94, 2
s_mul_i32 s66, s66, 163840
s_add_u32 s66, s66, s86
MM v[108:111],a[152:155],a[84:87],v[108:111]
s_add_u32 m0, s65, 16384
TR a[132:133],v235 offset:18432
TR a[134:135],v235 offset:19456
MM v[108:111],a[156:159],a[116:119],v[108:111]
buffer_load_dwordx4 v240, s[24:27], s66 offen offset:0 lds
MM v[100:103],a[156:159],a[112:115],v[100:103]
s_add_u32 s66, s66, 9216
TR a[136:137],v235 offset:20480
TR a[138:139],v235 offset:21504
MM v[100:103],a[152:155],a[80:83],v[100:103]
MM v[100:103],a[148:151],a[48:51],v[100:103]
TR a[140:141],v235 offset:22528
TR a[142:143],v235 offset:23552
MM v[100:103],a[144:147],a[16:19],v[100:103]
s_waitcnt lgkmcnt(15)
buffer_load_dwordx4 v240, s[24:27], s66 offen offset:1024 lds
s_waitcnt vmcnt(18)
MM v[128:131],a[160:163],a[24:27],v[128:131]
s_add_u32 s66, s66, 9216
TR a[144:145],v235 offset:24576
TR a[146:147],v235 offset:25600
MM v[128:131],a[164:167],a[56:59],v[128:131]
MM v[128:131],a[168:171],a[88:91],v[128:131]
TR a[148:149],v235 offset:26624
TR a[150:151],v235 offset:27648
MM v[128:131],a[172:175],a[120:123],v[128:131]
s_waitcnt vmcnt(14)
buffer_load_dwordx4 v240, s[24:27], s66 offen offset:2048 lds
MM v[136:139],a[172:175],a[124:127],v[136:139]
s_add_u32 s66, s66, 9216
TR a[152:153],v235 offset:28672
TR a[154:155],v235 offset:29696
MM v[136:139],a[168:171],a[92:95],v[136:139]
MM v[136:139],a[164:167],a[60:63],v[136:139]
TR a[156:157],v235 offset:30720
TR a[158:159],v235 offset:31744
MM v[136:139],a[160:163],a[28:31],v[136:139]
s_waitcnt lgkmcnt(15)
buffer_load_dwordx4 v240, s[24:27], s66 offen offset:3072 lds
s_waitcnt vmcnt(12)
MM v[128:131],a[176:179],a[200:203],v[128:131]
s_memrealtime s[56:57]
TR a[160:161],v144 offset:16384
TR a[162:163],v144 offset:18432
MM v[128:131],a[180:183],a[216:219],v[128:131]
s_xor_b32 s66, s94, 3
s_mul_i32 s66, s66, 163840
s_add_u32 s66, s66, s86
MM v[128:131],a[184:187],a[232:235],v[128:131]
s_add_u32 m0, s65, 32768
TR a[164:165],v144 offset:20480
TR a[166:167],v144 offset:22528
MM v[128:131],a[188:191],a[248:251],v[128:131]
s_waitcnt vmcnt(8)
buffer_load_dwordx4 v240, s[24:27], s66 offen offset:0 lds
MM v[136:139],a[188:191],a[252:255],v[136:139]
s_add_u32 s66, s66, 9216
TR a[168:169],v144 offset:24576
TR a[170:171],v144 offset:26624
MM v[136:139],a[184:187],a[236:239],v[136:139]
MM v[136:139],a[180:183],a[220:223],v[136:139]
TR a[172:173],v144 offset:28672
TR a[174:175],v144 offset:30720
MM v[136:139],a[176:179],a[204:207],v[136:139]
s_waitcnt lgkmcnt(0)
buffer_load_dwordx4 v240, s[24:27], s66 offen offset:1024 lds
MM v[140:143],a[128:131],a[28:31],v[140:143]
s_add_u32 s66, s66, 9216
TR a[176:177],v144 offset:32768
TR a[178:179],v144 offset:34816
MM v[140:143],a[132:135],a[60:63],v[140:143]
v_mov_b32 v158, s56
v_mov_b32 v159, s69
MM v[140:143],a[136:139],a[92:95],v[140:143]
TR a[180:181],v144 offset:36864
TR a[182:183],v144 offset:38912
MM v[140:143],a[140:143],a[124:127],v[140:143]
s_add_u32 s67, s95, 0
CV v128,v128,v129
CV v129,v130,v131
MM v[132:135],a[140:143],a[120:123],v[132:135]
buffer_load_dwordx4 v240, s[24:27], s66 offen offset:2048 lds
s_mov_b64 exec, 1
buffer_store_dwordx2 v[158:159], off, s[20:23], s19 offset:8 sc1
s_mov_b64 exec, -1
s_nop 0
TR a[184:185],v144 offset:40960
TR a[186:187],v144 offset:43008
MM v[132:135],a[136:139],a[88:91],v[132:135]
s_add_u32 s66, s66, 9216
CV v136,v136,v137
CV v137,v138,v139
MM v[132:135],a[132:135],a[56:59],v[132:135]
TR a[188:189],v144 offset:45056
TR a[190:191],v144 offset:47104
MM v[132:135],a[128:131],a[24:27],v[132:135]
buffer_load_dwordx4 v240, s[24:27], s66 offen offset:3072 lds
MM v[132:135],a[144:147],a[200:203],v[132:135]
TR a[128:129],v145 offset:16384
TR a[130:131],v145 offset:18432
MM v[132:135],a[148:151],a[216:219],v[132:135]
MM v[132:135],a[152:155],a[232:235],v[132:135]
TR a[132:133],v145 offset:20480
TR a[134:135],v145 offset:22528
MM v[132:135],a[156:159],a[248:251],v[132:135]
MM v[140:143],a[156:159],a[252:255],v[140:143]
TR a[136:137],v145 offset:24576
TR a[138:139],v145 offset:26624
MM v[140:143],a[152:155],a[236:239],v[140:143]
MM v[140:143],a[148:151],a[220:223],v[140:143]
TR a[140:141],v145 offset:28672
TR a[142:143],v145 offset:30720
MM v[140:143],a[144:147],a[204:207],v[140:143]
CV v130,v132,v133
CV v131,v134,v135
MM v[72:75],a[160:163],a[28:31],v[72:75]
TR a[144:145],v145 offset:32768
TR a[146:147],v145 offset:34816
MM v[72:75],a[164:167],a[60:63],v[72:75]
v_min_u32 v131, -2, v131
MM v[72:75],a[168:171],a[92:95],v[72:75]
TR a[148:149],v145 offset:36864
TR a[150:151],v145 offset:38912
MM v[72:75],a[172:175],a[124:127],v[72:75]
buffer_store_dwordx4 v[128:131], v249, s[20:23], s67 offen offset:0 sc1
MM v[64:67],a[172:175],a[120:123],v[64:67]
TR a[152:153],v145 offset:40960
TR a[154:155],v145 offset:43008
MM v[64:67],a[168:171],a[88:91],v[64:67]
CV v138,v140,v141
CV v139,v142,v143
MM v[64:67],a[164:167],a[56:59],v[64:67]
TR a[156:157],v145 offset:45056
TR a[158:159],v145 offset:47104
MM v[64:67],a[160:163],a[24:27],v[64:67]
s_waitcnt lgkmcnt(15)
v_min_u32 v139, -2, v139
MM v[64:67],a[176:179],a[200:203],v[64:67]
TR a[160:161],v146 offset:16384
TR a[162:163],v146 offset:18432
MM v[64:67],a[180:183],a[216:219],v[64:67]
buffer_store_dwordx4 v[136:139], v249, s[20:23], s67 offen offset:1024 sc1
MM v[64:67],a[184:187],a[232:235],v[64:67]
TR a[164:165],v146 offset:20480
TR a[166:167],v146 offset:22528
MM v[64:67],a[188:191],a[248:251],v[64:67]
MM v[72:75],a[188:191],a[252:255],v[72:75]
TR a[168:169],v146 offset:24576
TR a[170:171],v146 offset:26624
MM v[72:75],a[184:187],a[236:239],v[72:75]
MM v[72:75],a[180:183],a[220:223],v[72:75]
TR a[172:173],v146 offset:28672
TR a[174:175],v146 offset:30720
MM v[72:75],a[176:179],a[204:207],v[72:75]
s_waitcnt lgkmcnt(15)
MM v[76:79],a[128:131],a[28:31],v[76:79]
TR a[176:177],v146 offset:32768
TR a[178:179],v146 offset:34816
MM v[76:79],a[132:135],a[60:63],v[76:79]
v_lshlrev_b32 v136, 16, v150
v_and_b32 v137, 0xffff0000, v150
MM v[76:79],a[136:139],a[92:95],v[76:79]
TR a[180:181],v146 offset:36864
TR a[182:183],v146 offset:38912
MM v[76:79],a[140:143],a[124:127],v[76:79]
s_add_u32 s67, s95, 4096
CV v64,v64,v65
CV v65,v66,v67
MM v[68:71],a[140:143],a[120:123],v[68:71]
TR a[184:185],v146 offset:40960
TR a[186:187],v146 offset:43008
MM v[68:71],a[136:139],a[88:91],v[68:71]
CV v72,v72,v73
CV v73,v74,v75
MM v[68:71],a[132:135],a[56:59],v[68:71]
TR a[188:189],v146 offset:45056
TR a[190:191],v146 offset:47104
MM v[68:71],a[128:131],a[24:27],v[68:71]
)ASM"
R"ASM(
s_waitcnt lgkmcnt(15)
v_lshlrev_b32 v138, 16, v151
v_and_b32 v139, 0xffff0000, v151
MM v[68:71],a[144:147],a[200:203],v[68:71]
TR a[128:129],v147 offset:16384
TR a[130:131],v147 offset:18432
MM v[68:71],a[148:151],a[216:219],v[68:71]
buffer_load_dwordx2 v[156:157], v253, s[20:23], s68 offen offset:8 sc1
MM v[68:71],a[152:155],a[232:235],v[68:71]
TR a[132:133],v147 offset:20480
TR a[134:135],v147 offset:22528
MM v[68:71],a[156:159],a[248:251],v[68:71]
v_mul_f32 v140, 0xbfb8aa3b, v136
v_mul_f32 v141, 0xbfb8aa3b, v137
MM v[76:79],a[156:159],a[252:255],v[76:79]
TR a[136:137],v147 offset:24576
TR a[138:139],v147 offset:26624
MM v[76:79],a[152:155],a[236:239],v[76:79]
v_mul_f32 v142, 0xbfb8aa3b, v138
v_mul_f32 v143, 0xbfb8aa3b, v139
MM v[76:79],a[148:151],a[220:223],v[76:79]
TR a[140:141],v147 offset:28672
TR a[142:143],v147 offset:30720
MM v[76:79],a[144:147],a[204:207],v[76:79]
s_waitcnt lgkmcnt(15)
CV v66,v68,v69
CV v67,v70,v71
s_waitcnt vmcnt(3)
s_barrier
MM v[104:107],a[160:163],a[28:31],v[104:107]
TR a[144:145],v147 offset:32768
TR a[146:147],v147 offset:34816
MM v[104:107],a[164:167],a[60:63],v[104:107]
EX v140,v140
v_min_u32 v67, -2, v67
MM v[104:107],a[168:171],a[92:95],v[104:107]
TR a[148:149],v147 offset:36864
TR a[150:151],v147 offset:38912
MM v[104:107],a[172:175],a[124:127],v[104:107]
buffer_store_dwordx4 v[64:67], v248, s[20:23], s67 offen offset:0 sc1
MM v[96:99],a[172:175],a[120:123],v[96:99]
TR a[152:153],v147 offset:40960
TR a[154:155],v147 offset:43008
MM v[96:99],a[168:171],a[88:91],v[96:99]
CV v74,v76,v77
CV v75,v78,v79
MM v[96:99],a[164:167],a[56:59],v[96:99]
TR a[156:157],v147 offset:45056
TR a[158:159],v147 offset:47104
MM v[96:99],a[160:163],a[24:27],v[96:99]
s_waitcnt lgkmcnt(15)
EX v141,v141
v_min_u32 v75, -2, v75
MM v[96:99],a[176:179],a[200:203],v[96:99]
TR a[160:161],v230 offset:0
TR a[162:163],v230 offset:2048
MM v[96:99],a[180:183],a[216:219],v[96:99]
buffer_store_dwordx4 v[72:75], v248, s[20:23], s67 offen offset:1024 sc1
MM v[96:99],a[184:187],a[232:235],v[96:99]
TR a[164:165],v230 offset:4096
TR a[166:167],v230 offset:6144
MM v[96:99],a[188:191],a[248:251],v[96:99]
EX v142,v142
MM v[104:107],a[188:191],a[252:255],v[104:107]
TR a[168:169],v230 offset:8192
TR a[170:171],v230 offset:10240
MM v[104:107],a[184:187],a[236:239],v[104:107]
EX v143,v143
MM v[104:107],a[180:183],a[220:223],v[104:107]
TR a[172:173],v230 offset:12288
TR a[174:175],v230 offset:14336
MM v[104:107],a[176:179],a[204:207],v[104:107]
s_waitcnt lgkmcnt(15)
v_add_f32 v140, 1.0, v140
v_add_f32 v141, 1.0, v141
MM v[108:111],a[128:131],a[28:31],v[108:111]
TR a[176:177],v230 offset:16384
TR a[178:179],v230 offset:18432
MM v[108:111],a[132:135],a[60:63],v[108:111]
v_add_f32 v142, 1.0, v142
v_add_f32 v143, 1.0, v143
MM v[108:111],a[136:139],a[92:95],v[108:111]
TR a[180:181],v230 offset:20480
TR a[182:183],v230 offset:22528
MM v[108:111],a[140:143],a[124:127],v[108:111]
s_add_u32 s67, s95, 4096
CV v96,v96,v97
CV v97,v98,v99
MM v[100:103],a[140:143],a[120:123],v[100:103]
TR a[184:185],v230 offset:24576
TR a[186:187],v230 offset:26624
MM v[100:103],a[136:139],a[88:91],v[100:103]
CV v104,v104,v105
CV v105,v106,v107
MM v[100:103],a[132:135],a[56:59],v[100:103]
TR a[188:189],v230 offset:28672
TR a[190:191],v230 offset:30720
MM v[100:103],a[128:131],a[24:27],v[100:103]
s_waitcnt lgkmcnt(15)
v_rcp_f32 v140, v140
MM v[100:103],a[144:147],a[200:203],v[100:103]
TR a[128:129],v230 offset:32768
TR a[130:131],v230 offset:34816
MM v[100:103],a[148:151],a[216:219],v[100:103]
v_rcp_f32 v141, v141
MM v[100:103],a[152:155],a[232:235],v[100:103]
TR a[132:133],v230 offset:36864
TR a[134:135],v230 offset:38912
MM v[100:103],a[156:159],a[248:251],v[100:103]
v_rcp_f32 v142, v142
MM v[108:111],a[156:159],a[252:255],v[108:111]
TR a[136:137],v230 offset:40960
TR a[138:139],v230 offset:43008
MM v[108:111],a[152:155],a[236:239],v[108:111]
v_rcp_f32 v143, v143
MM v[108:111],a[148:151],a[220:223],v[108:111]
TR a[140:141],v230 offset:45056
TR a[142:143],v230 offset:47104
MM v[108:111],a[144:147],a[204:207],v[108:111]
s_waitcnt lgkmcnt(15)
LPG29_%=:
s_cbranch_execz LTR1_%=
CV v98,v100,v101
CV v99,v102,v103
MM v[40:43],a[160:163],a[20:23],v[40:43]
TR a[144:145],v231 offset:0
TR a[146:147],v231 offset:2048
MM v[40:43],a[164:167],a[52:55],v[40:43]
v_min_u32 v99, -2, v99
v_mul_f32 v140, v136, v140
MM v[40:43],a[168:171],a[84:87],v[40:43]
TR a[148:149],v231 offset:4096
TR a[150:151],v231 offset:6144
MM v[40:43],a[172:175],a[116:119],v[40:43]
buffer_store_dwordx4 v[96:99], v248, s[20:23], s67 offen offset:2048 sc1
MM v[32:35],a[172:175],a[112:115],v[32:35]
TR a[152:153],v231 offset:8192
TR a[154:155],v231 offset:10240
MM v[32:35],a[168:171],a[80:83],v[32:35]
CV v106,v108,v109
CV v107,v110,v111
MM v[32:35],a[164:167],a[48:51],v[32:35]
TR a[156:157],v231 offset:12288
TR a[158:159],v231 offset:14336
MM v[32:35],a[160:163],a[16:19],v[32:35]
s_waitcnt lgkmcnt(15)
v_min_u32 v107, -2, v107
v_mul_f32 v141, v137, v141
MM v[32:35],a[176:179],a[24:27],v[32:35]
TR a[160:161],v231 offset:16384
TR a[162:163],v231 offset:18432
MM v[32:35],a[180:183],a[56:59],v[32:35]
buffer_store_dwordx4 v[104:107], v248, s[20:23], s67 offen offset:3072 sc1
MM v[32:35],a[184:187],a[88:91],v[32:35]
TR a[164:165],v231 offset:20480
TR a[166:167],v231 offset:22528
MM v[32:35],a[188:191],a[120:123],v[32:35]
v_mul_f32 v142, v138, v142
v_mul_f32 v143, v139, v143
MM v[40:43],a[188:191],a[124:127],v[40:43]
TR a[168:169],v231 offset:24576
TR a[170:171],v231 offset:26624
MM v[40:43],a[184:187],a[92:95],v[40:43]
CV v150,v140,v141
CV v151,v142,v143
MM v[40:43],a[180:183],a[60:63],v[40:43]
TR a[172:173],v231 offset:28672
TR a[174:175],v231 offset:30720
MM v[40:43],a[176:179],a[28:31],v[40:43]
s_waitcnt lgkmcnt(15)
v_lshlrev_b32 v160, 16, v16
v_and_b32 v161, 0xffff0000, v16
MM v[40:43],a[128:131],a[204:207],v[40:43]
TR a[176:177],v231 offset:32768
TR a[178:179],v231 offset:34816
MM v[40:43],a[132:135],a[220:223],v[40:43]
v_lshlrev_b32 v162, 16, v17
v_and_b32 v163, 0xffff0000, v17
MM v[40:43],a[136:139],a[236:239],v[40:43]
TR a[180:181],v231 offset:36864
TR a[182:183],v231 offset:38912
MM v[40:43],a[140:143],a[252:255],v[40:43]
v_lshlrev_b32 v164, 16, v18
v_and_b32 v165, 0xffff0000, v18
MM v[32:35],a[140:143],a[248:251],v[32:35]
TR a[184:185],v231 offset:40960
TR a[186:187],v231 offset:43008
MM v[32:35],a[136:139],a[232:235],v[32:35]
v_lshlrev_b32 v166, 16, v19
v_and_b32 v167, 0xffff0000, v19
MM v[32:35],a[132:135],a[216:219],v[32:35]
TR a[188:189],v231 offset:45056
TR a[190:191],v231 offset:47104
MM v[32:35],a[128:131],a[200:203],v[32:35]
s_waitcnt lgkmcnt(15)
v_mul_f32 v136, 0xbfb8aa3b, v160
v_mul_f32 v137, 0xbfb8aa3b, v161
MM v[36:39],a[144:147],a[16:19],v[36:39]
TR a[128:129],v228 offset:49152
TR a[130:131],v228 offset:51200
MM v[36:39],a[148:151],a[48:51],v[36:39]
v_mul_f32 v138, 0xbfb8aa3b, v162
v_mul_f32 v139, 0xbfb8aa3b, v163
MM v[36:39],a[152:155],a[80:83],v[36:39]
TR a[132:133],v228 offset:53248
TR a[134:135],v228 offset:55296
MM v[36:39],a[156:159],a[112:115],v[36:39]
s_add_u32 s67, s95, 0
CV v32,v32,v33
CV v33,v34,v35
MM v[44:47],a[156:159],a[116:119],v[44:47]
TR a[136:137],v228 offset:57344
TR a[138:139],v228 offset:59392
MM v[44:47],a[152:155],a[84:87],v[44:47]
CV v40,v40,v41
CV v41,v42,v43
MM v[44:47],a[148:151],a[52:55],v[44:47]
TR a[140:141],v228 offset:61440
TR a[142:143],v228 offset:63488
MM v[44:47],a[144:147],a[20:23],v[44:47]
s_waitcnt lgkmcnt(15)
v_mul_f32 v140, 0xbfb8aa3b, v164
v_mul_f32 v141, 0xbfb8aa3b, v165
MM v[44:47],a[160:163],a[28:31],v[44:47]
s_waitcnt vmcnt(4)
TR a[144:145],v228 offset:0
TR a[146:147],v228 offset:2048
MM v[44:47],a[164:167],a[60:63],v[44:47]
v_cmp_eq_u32 s[8:9], s69, v157
v_subrev_u32 v226, s56, v156
MM v[44:47],a[168:171],a[92:95],v[44:47]
TR a[148:149],v228 offset:4096
TR a[150:151],v228 offset:6144
MM v[44:47],a[172:175],a[124:127],v[44:47]
v_cndmask_b32 v226, 4.0, v226, s[8:9]
v_mul_f32 v142, 0xbfb8aa3b, v166
MM v[36:39],a[172:175],a[120:123],v[36:39]
TR a[152:153],v228 offset:8192
TR a[154:155],v228 offset:10240
MM v[36:39],a[168:171],a[88:91],v[36:39]
v_readlane_b32 s13, v226, 16
v_readlane_b32 s14, v226, 32
MM v[36:39],a[164:167],a[56:59],v[36:39]
TR a[156:157],v228 offset:12288
TR a[158:159],v228 offset:14336
MM v[36:39],a[160:163],a[24:27],v[36:39]
s_waitcnt lgkmcnt(15)
v_readlane_b32 s15, v226, 48
v_mul_f32 v143, 0xbfb8aa3b, v167
MM v[36:39],a[176:179],a[200:203],v[36:39]
s_max_i32 s12, s13, s14
TR a[160:161],v228 offset:16384
TR a[162:163],v228 offset:18432
MM v[36:39],a[180:183],a[216:219],v[36:39]
s_max_i32 s12, s12, s15
EX v136,v136
MM v[36:39],a[184:187],a[232:235],v[36:39]
s_max_i32 s12, s12, 0
TR a[164:165],v228 offset:20480
TR a[166:167],v228 offset:22528
MM v[36:39],a[188:191],a[248:251],v[36:39]
buffer_load_dwordx2 v[156:157], v253, s[20:23], s68 offen offset:8 sc1
MM v[44:47],a[188:191],a[252:255],v[44:47]
TR a[168:169],v228 offset:24576
TR a[170:171],v228 offset:26624
MM v[44:47],a[184:187],a[236:239],v[44:47]
EX v137,v137
MM v[44:47],a[180:183],a[220:223],v[44:47]
TR a[172:173],v228 offset:28672
TR a[174:175],v228 offset:30720
MM v[44:47],a[176:179],a[204:207],v[44:47]
s_waitcnt lgkmcnt(15)
CV v34,v36,v37
CV v35,v38,v39
v_min_u32 v35, -2, v35
buffer_store_dwordx4 v[32:35], v248, s[20:23], s67 offen offset:2048 sc1
MM v[8:11],a[128:131],v[172:175],v[8:11]
TR a[176:177],v228 offset:32768
TR a[178:179],v228 offset:34816
MM v[8:11],a[132:135],v[188:191],v[8:11]
EX v138,v138
MM v[8:11],a[136:139],v[204:207],v[8:11]
TR a[180:181],v228 offset:36864
TR a[182:183],v228 offset:38912
MM v[8:11],a[140:143],v[220:223],v[8:11]
CV v42,v44,v45
CV v43,v46,v47
v_min_u32 v43, -2, v43
buffer_store_dwordx4 v[40:43], v248, s[20:23], s67 offen offset:3072 sc1
MM v[0:3],a[140:143],v[216:219],v[0:3]
TR a[184:185],v228 offset:40960
TR a[186:187],v228 offset:43008
MM v[0:3],a[136:139],v[200:203],v[0:3]
EX v139,v139
MM v[0:3],a[132:135],v[184:187],v[0:3]
TR a[188:189],v228 offset:45056
TR a[190:191],v228 offset:47104
MM v[0:3],a[128:131],v[168:171],v[0:3]
s_waitcnt lgkmcnt(15)
EX v140,v140
MM v[0:3],a[144:147],a[16:19],v[0:3]
TR a[128:129],v229 offset:49152
TR a[130:131],v229 offset:51200
MM v[0:3],a[148:151],a[48:51],v[0:3]
s_cmp_le_i32 s13, -42
s_cselect_b32 s60, 2, 0
s_cselect_b64 s[8:9], -1, 0
MM v[0:3],a[152:155],a[80:83],v[0:3]
s_or_b32 s58, s58, s60
TR a[132:133],v229 offset:53248
TR a[134:135],v229 offset:55296
MM v[0:3],a[156:159],a[112:115],v[0:3]
s_mov_b64 exec, s[8:9]
buffer_load_dwordx4 v[80:83], v248, s[20:23], s36 offen offset:0 sc1
s_mov_b64 exec, -1
MM v[8:11],a[156:159],a[116:119],v[8:11]
TR a[136:137],v229 offset:57344
TR a[138:139],v229 offset:59392
MM v[8:11],a[152:155],a[84:87],v[8:11]
s_mov_b64 exec, s[8:9]
buffer_load_dwordx4 v[84:87], v248, s[20:23], s36 offen offset:1024 sc1
s_mov_b64 exec, -1
MM v[8:11],a[148:151],a[52:55],v[8:11]
TR a[140:141],v229 offset:61440
TR a[142:143],v229 offset:63488
MM v[8:11],a[144:147],a[20:23],v[8:11]
s_waitcnt lgkmcnt(15)
EX v141,v141
MM v[8:11],a[160:163],a[28:31],v[8:11]
TR a[144:145],v229 offset:0
TR a[146:147],v229 offset:2048
MM v[8:11],a[164:167],a[60:63],v[8:11]
s_cmp_le_i32 s15, 17
s_cselect_b32 s60, 8, 0
s_cselect_b64 s[8:9], -1, 0
MM v[8:11],a[168:171],a[92:95],v[8:11]
s_or_b32 s58, s58, s60
TR a[148:149],v229 offset:4096
TR a[150:151],v229 offset:6144
MM v[8:11],a[172:175],a[124:127],v[8:11]
s_mov_b64 exec, s[8:9]
buffer_load_dwordx4 v[112:115], v248, s[20:23], s38 offen offset:0 sc1
s_mov_b64 exec, -1
MM v[0:3],a[172:175],a[120:123],v[0:3]
TR a[152:153],v229 offset:8192
TR a[154:155],v229 offset:10240
MM v[0:3],a[168:171],a[88:91],v[0:3]
s_mov_b64 exec, s[8:9]
buffer_load_dwordx4 v[116:119], v248, s[20:23], s38 offen offset:1024 sc1
s_mov_b64 exec, -1
MM v[0:3],a[164:167],a[56:59],v[0:3]
TR a[156:157],v229 offset:12288
TR a[158:159],v229 offset:14336
MM v[0:3],a[160:163],a[24:27],v[0:3]
s_waitcnt lgkmcnt(15)
EX v142,v142
MM v[0:3],a[176:179],a[200:203],v[0:3]
TR a[160:161],v229 offset:16384
TR a[162:163],v229 offset:18432
MM v[0:3],a[180:183],a[216:219],v[0:3]
)ASM"
R"ASM(
s_cmp_le_i32 s14, 59
s_cselect_b32 s60, 4, 0
s_cselect_b64 s[8:9], -1, 0
MM v[0:3],a[184:187],a[232:235],v[0:3]
s_or_b32 s58, s58, s60
TR a[164:165],v229 offset:20480
TR a[166:167],v229 offset:22528
MM v[0:3],a[188:191],a[248:251],v[0:3]
s_mov_b64 exec, s[8:9]
buffer_load_dwordx4 v[88:91], v248, s[20:23], s37 offen offset:0 sc1
s_mov_b64 exec, -1
MM v[8:11],a[188:191],a[252:255],v[8:11]
TR a[168:169],v229 offset:24576
TR a[170:171],v229 offset:26624
MM v[8:11],a[184:187],a[236:239],v[8:11]
s_mov_b64 exec, s[8:9]
buffer_load_dwordx4 v[92:95], v248, s[20:23], s37 offen offset:1024 sc1
s_mov_b64 exec, -1
MM v[8:11],a[180:183],a[220:223],v[8:11]
TR a[172:173],v229 offset:28672
TR a[174:175],v229 offset:30720
MM v[8:11],a[176:179],a[204:207],v[8:11]
s_waitcnt lgkmcnt(15)
EX v143,v143
MM v[12:15],a[128:131],v[172:175],v[12:15]
TR a[176:177],v229 offset:32768
TR a[178:179],v229 offset:34816
MM v[12:15],a[132:135],v[188:191],v[12:15]
s_cmp_le_i32 s12, 102
s_cselect_b32 s60, 1, 0
s_cselect_b64 s[8:9], -1, 0
MM v[12:15],a[136:139],v[204:207],v[12:15]
s_or_b32 s58, s58, s60
TR a[180:181],v229 offset:36864
TR a[182:183],v229 offset:38912
MM v[12:15],a[140:143],v[220:223],v[12:15]
s_mov_b64 exec, s[8:9]
buffer_load_dwordx4 v[120:123], v250, s[20:23], s39 offen offset:0 sc1
s_mov_b64 exec, -1
MM v[4:7],a[140:143],v[216:219],v[4:7]
TR a[184:185],v229 offset:40960
TR a[186:187],v229 offset:43008
MM v[4:7],a[136:139],v[200:203],v[4:7]
s_mov_b64 exec, s[8:9]
buffer_load_dwordx4 v[124:127], v250, s[20:23], s39 offen offset:256 sc1
s_mov_b64 exec, -1
MM v[4:7],a[132:135],v[184:187],v[4:7]
TR a[188:189],v229 offset:45056
TR a[190:191],v229 offset:47104
MM v[4:7],a[128:131],v[168:171],v[4:7]
s_waitcnt lgkmcnt(15)
v_add_f32 v136, 1.0, v136
v_add_f32 v137, 1.0, v137
MM v[4:7],a[144:147],a[16:19],v[4:7]
v_add_f32 v138, 1.0, v138
v_add_f32 v139, 1.0, v139
MM v[4:7],a[148:151],a[48:51],v[4:7]
v_add_f32 v140, 1.0, v140
v_add_f32 v141, 1.0, v141
MM v[4:7],a[152:155],a[80:83],v[4:7]
v_add_f32 v142, 1.0, v142
v_add_f32 v143, 1.0, v143
MM v[4:7],a[156:159],a[112:115],v[4:7]
v_rcp_f32 v136, v136
MM v[12:15],a[156:159],a[116:119],v[12:15]
v_rcp_f32 v137, v137
MM v[12:15],a[152:155],a[84:87],v[12:15]
v_rcp_f32 v138, v138
MM v[12:15],a[148:151],a[52:55],v[12:15]
v_rcp_f32 v139, v139
MM v[12:15],a[144:147],a[20:23],v[12:15]
s_waitcnt lgkmcnt(8)
v_rcp_f32 v140, v140
MM v[12:15],a[160:163],a[28:31],v[12:15]
s_cmp_le_i32 s13, 0
s_cselect_b32 s60, 2, 0
s_andn2_b32 s60, s60, s58
s_cselect_b64 s[8:9], -1, 0
MM v[12:15],a[164:167],a[60:63],v[12:15]
s_or_b32 s58, s58, s60
s_mov_b64 exec, s[8:9]
buffer_load_dwordx4 v[80:83], v248, s[20:23], s36 offen offset:0 sc1
s_mov_b64 exec, -1
MM v[12:15],a[168:171],a[92:95],v[12:15]
v_rcp_f32 v141, v141
MM v[12:15],a[172:175],a[124:127],v[12:15]
s_mov_b64 exec, s[8:9]
buffer_load_dwordx4 v[84:87], v248, s[20:23], s36 offen offset:1024 sc1
s_mov_b64 exec, -1
MM v[4:7],a[172:175],a[120:123],v[4:7]
v_rcp_f32 v142, v142
MM v[4:7],a[168:171],a[88:91],v[4:7]
v_rcp_f32 v143, v143
MM v[4:7],a[164:167],a[56:59],v[4:7]
v_mul_f32 v136, v160, v136
v_mul_f32 v137, v161, v137
MM v[4:7],a[160:163],a[24:27],v[4:7]
s_waitcnt lgkmcnt(0)
v_mul_f32 v138, v162, v138
v_mul_f32 v139, v163, v139
MM v[4:7],a[176:179],a[200:203],v[4:7]
v_mul_f32 v140, v164, v140
v_mul_f32 v141, v165, v141
MM v[4:7],a[180:183],a[216:219],v[4:7]
v_mul_f32 v142, v166, v142
v_mul_f32 v143, v167, v143
MM v[4:7],a[184:187],a[232:235],v[4:7]
CV v16,v136,v137
CV v17,v138,v139
MM v[4:7],a[188:191],a[248:251],v[4:7]
CV v18,v140,v141
CV v19,v142,v143
MM v[12:15],a[188:191],a[252:255],v[12:15]
v_lshlrev_b32 v160, 16, v16
v_and_b32 v161, 0xffff0000, v16
MM v[12:15],a[184:187],a[236:239],v[12:15]
v_lshlrev_b32 v162, 16, v17
v_and_b32 v163, 0xffff0000, v17
MM v[12:15],a[180:183],a[220:223],v[12:15]
v_lshlrev_b32 v164, 16, v18
v_and_b32 v165, 0xffff0000, v18
MM v[12:15],a[176:179],a[204:207],v[12:15]
v_lshlrev_b32 v166, 16, v19
v_and_b32 v167, 0xffff0000, v19
s_memrealtime s[54:55]
s_cmp_eq_u32 s58, 15
s_cbranch_scc1 LSK1_%=
s_memrealtime s[98:99]
s_andn2_b32 s91, s17, s58
s_mov_b32 s77, 0x7fffffff
s_waitcnt lgkmcnt(0)
s_sub_u32 s76, s98, s54
s_bitcmp0_b32 s91, 1
s_cbranch_scc1 LSM11_%=
s_sub_u32 s90, s13, s76
s_sub_u32 s90, s90, 12
s_cmp_gt_i32 s90, 0
s_cbranch_scc1 LSM11_%=
buffer_load_dwordx4 v[80:83], v248, s[20:23], s36 offen offset:0 sc1
buffer_load_dwordx4 v[84:87], v248, s[20:23], s36 offen offset:1024 sc1
s_bitset1_b32 s58, 1
LSM11_%=:
s_bitcmp0_b32 s91, 3
s_cbranch_scc1 LSM13_%=
s_sub_u32 s90, s15, s76
s_sub_u32 s90, s90, 63
s_cmp_gt_i32 s90, 0
s_cbranch_scc1 LSM13_%=
buffer_load_dwordx4 v[112:115], v248, s[20:23], s38 offen offset:0 sc1
buffer_load_dwordx4 v[116:119], v248, s[20:23], s38 offen offset:1024 sc1
s_bitset1_b32 s58, 3
LSM13_%=:
s_bitcmp0_b32 s91, 2
s_cbranch_scc1 LSM12_%=
s_sub_u32 s90, s14, s76
s_sub_u32 s90, s90, 97
s_cmp_gt_i32 s90, 0
s_cbranch_scc1 LSM12_%=
buffer_load_dwordx4 v[88:91], v248, s[20:23], s37 offen offset:0 sc1
buffer_load_dwordx4 v[92:95], v248, s[20:23], s37 offen offset:1024 sc1
s_bitset1_b32 s58, 2
LSM12_%=:
s_bitcmp0_b32 s91, 0
s_cbranch_scc1 LSM10_%=
s_sub_u32 s90, s12, s76
s_sub_u32 s90, s90, 131
s_cmp_gt_i32 s90, 0
s_cbranch_scc1 LSM10_%=
buffer_load_dwordx4 v[120:123], v250, s[20:23], s39 offen offset:0 sc1
buffer_load_dwordx4 v[124:127], v250, s[20:23], s39 offen offset:256 sc1
s_bitset1_b32 s58, 0
LSM10_%=:
LSK1_%=:
v_lshlrev_b32 v176, 16, v20
v_and_b32 v177, 0xffff0000, v20
v_lshlrev_b32 v178, 16, v21
v_and_b32 v179, 0xffff0000, v21
v_lshlrev_b32 v180, 16, v22
v_and_b32 v181, 0xffff0000, v22
v_lshlrev_b32 v182, 16, v23
v_and_b32 v183, 0xffff0000, v23
v_mul_f32 v136, 0xbfb8aa3b, v176
v_mul_f32 v137, 0xbfb8aa3b, v177
v_mul_f32 v138, 0xbfb8aa3b, v178
v_mul_f32 v139, 0xbfb8aa3b, v179
v_mul_f32 v140, 0xbfb8aa3b, v180
v_mul_f32 v141, 0xbfb8aa3b, v181
v_mul_f32 v142, 0xbfb8aa3b, v182
v_mul_f32 v143, 0xbfb8aa3b, v183
EX v136,v136
EX v137,v137
EX v138,v138
EX v139,v139
EX v140,v140
EX v141,v141
EX v142,v142
EX v143,v143
v_add_f32 v136, 1.0, v136
v_add_f32 v137, 1.0, v137
v_add_f32 v138, 1.0, v138
v_add_f32 v139, 1.0, v139
v_add_f32 v140, 1.0, v140
v_add_f32 v141, 1.0, v141
v_add_f32 v142, 1.0, v142
v_add_f32 v143, 1.0, v143
v_rcp_f32 v136, v136
v_rcp_f32 v137, v137
v_rcp_f32 v138, v138
v_rcp_f32 v139, v139
s_waitcnt vmcnt(12)
v_cmp_eq_u32 s[8:9], s69, v157
v_subrev_u32 v226, s56, v156
s_nop 0
v_cndmask_b32 v226, 4.0, v226, s[8:9]
s_nop 0
v_readlane_b32 s13, v226, 16
v_readlane_b32 s14, v226, 32
v_readlane_b32 s15, v226, 48
s_max_i32 s12, s13, s14
s_max_i32 s12, s12, s15
s_max_i32 s12, s12, 0
s_cmp_eq_u32 s58, 15
s_cbranch_scc1 LSK2_%=
s_memrealtime s[98:99]
s_andn2_b32 s91, s17, s58
s_mov_b32 s77, 0x7fffffff
s_waitcnt lgkmcnt(0)
s_sub_u32 s76, s98, s54
s_bitcmp0_b32 s91, 1
s_cbranch_scc1 LSM21_%=
s_sub_u32 s90, s13, s76
s_sub_u32 s90, s90, 12
s_cmp_gt_i32 s90, 0
s_cbranch_scc1 LSM21_%=
buffer_load_dwordx4 v[80:83], v248, s[20:23], s36 offen offset:0 sc1
buffer_load_dwordx4 v[84:87], v248, s[20:23], s36 offen offset:1024 sc1
s_bitset1_b32 s58, 1
LSM21_%=:
s_bitcmp0_b32 s91, 3
s_cbranch_scc1 LSM23_%=
s_sub_u32 s90, s15, s76
s_sub_u32 s90, s90, 63
s_cmp_gt_i32 s90, 0
s_cbranch_scc1 LSM23_%=
buffer_load_dwordx4 v[112:115], v248, s[20:23], s38 offen offset:0 sc1
buffer_load_dwordx4 v[116:119], v248, s[20:23], s38 offen offset:1024 sc1
s_bitset1_b32 s58, 3
LSM23_%=:
s_bitcmp0_b32 s91, 2
s_cbranch_scc1 LSM22_%=
s_sub_u32 s90, s14, s76
s_sub_u32 s90, s90, 97
s_cmp_gt_i32 s90, 0
s_cbranch_scc1 LSM22_%=
buffer_load_dwordx4 v[88:91], v248, s[20:23], s37 offen offset:0 sc1
buffer_load_dwordx4 v[92:95], v248, s[20:23], s37 offen offset:1024 sc1
s_bitset1_b32 s58, 2
LSM22_%=:
s_bitcmp0_b32 s91, 0
s_cbranch_scc1 LSM20_%=
s_sub_u32 s90, s12, s76
s_sub_u32 s90, s90, 131
s_cmp_gt_i32 s90, 0
s_cbranch_scc1 LSM20_%=
buffer_load_dwordx4 v[120:123], v250, s[20:23], s39 offen offset:0 sc1
buffer_load_dwordx4 v[124:127], v250, s[20:23], s39 offen offset:256 sc1
s_bitset1_b32 s58, 0
LSM20_%=:
LSK2_%=:
v_rcp_f32 v140, v140
v_rcp_f32 v141, v141
v_rcp_f32 v142, v142
v_rcp_f32 v143, v143
v_mul_f32 v136, v176, v136
v_mul_f32 v137, v177, v137
v_mul_f32 v138, v178, v138
v_mul_f32 v139, v179, v139
v_mul_f32 v140, v180, v140
v_mul_f32 v141, v181, v141
v_mul_f32 v142, v182, v142
v_mul_f32 v143, v183, v143
CV v20,v136,v137
CV v21,v138,v139
CV v22,v140,v141
CV v23,v142,v143
v_lshlrev_b32 v176, 16, v20
v_and_b32 v177, 0xffff0000, v20
v_lshlrev_b32 v178, 16, v21
v_and_b32 v179, 0xffff0000, v21
v_lshlrev_b32 v180, 16, v22
v_and_b32 v181, 0xffff0000, v22
v_lshlrev_b32 v182, 16, v23
v_and_b32 v183, 0xffff0000, v23
v_lshlrev_b32 v192, 16, v24
v_and_b32 v193, 0xffff0000, v24
v_lshlrev_b32 v194, 16, v25
v_and_b32 v195, 0xffff0000, v25
v_lshlrev_b32 v196, 16, v26
v_and_b32 v197, 0xffff0000, v26
v_lshlrev_b32 v198, 16, v27
v_and_b32 v199, 0xffff0000, v27
v_lshlrev_b32 v208, 16, v28
v_and_b32 v209, 0xffff0000, v28
v_lshlrev_b32 v210, 16, v29
v_and_b32 v211, 0xffff0000, v29
v_lshlrev_b32 v212, 16, v30
v_and_b32 v213, 0xffff0000, v30
v_lshlrev_b32 v214, 16, v31
v_and_b32 v215, 0xffff0000, v31
s_mov_b32 s73, 30000
s_cmp_lg_u32 s58, 15
s_cbranch_scc1 LSI_%=
s_waitcnt vmcnt(0)
v_max3_u32 v236, v123, v127, v83
v_max3_u32 v237, v87, v91, v95
v_max3_u32 v236, v236, v115, v119
v_max_u32 v236, v236, v237
v_cmp_eq_u32 vcc, -1, v236
s_cbranch_vccnz LSI_%=
LSF_%=:
s_waitcnt lgkmcnt(0)
MM v[152:155],v[48:51],v[120:123],0
MM v[152:155],v[52:55],v[124:127],v[152:155]
MM v[0:3],v[58:61],v[88:91],v[0:3]
v_lshlrev_b32 v128, 16, v150
v_and_b32 v129, 0xffff0000, v150
MM v[0:3],v[58:61],v[112:115],v[0:3]
v_lshlrev_b32 v130, 16, v151
v_and_b32 v131, 0xffff0000, v151
MM v[0:3],v[58:61],v[80:83],v[0:3]
v_lshlrev_b32 v132, 16, v148
v_and_b32 v133, 0xffff0000, v148
MM v[4:7],v[56:59],v[88:91],v[4:7]
v_lshlrev_b32 v134, 16, v149
v_and_b32 v135, 0xffff0000, v149
MM v[4:7],v[56:59],v[112:115],v[4:7]
CV v236,v152,v153
CV v237,v154,v155
MM v[4:7],v[56:59],v[80:83],v[4:7]
ds_bpermute_b32 v236, v232, v236
ds_bpermute_b32 v237, v232, v237
MM v[8:11],v[58:61],v[92:95],v[8:11]
MM v[8:11],v[58:61],v[116:119],v[8:11]
MM v[8:11],v[58:61],v[84:87],v[8:11]
s_add_u32 s80, s18, 20480
MM v[12:15],v[56:59],v[92:95],v[12:15]
MM v[12:15],v[56:59],v[116:119],v[12:15]
CV v32,v0,v1
CV v33,v2,v3
MM v[12:15],v[56:59],v[84:87],v[12:15]
CV v34,v4,v5
CV v35,v6,v7
ds_write2_b64 v224, v[32:33], v[34:35] offset0:0 offset1:4
RD v[40:43],v225 offset:0
CV v36,v8,v9
CV v37,v10,v11
s_nop 1
CV v38,v12,v13
CV v39,v14,v15
ds_write2_b64 v224, v[36:37], v[38:39] offset0:160 offset1:164
RD v[44:47],v225 offset:1280
s_waitcnt lgkmcnt(4)
v_lshlrev_b32 v152, 16, v236
v_and_b32 v153, 0xffff0000, v236
v_lshlrev_b32 v154, 16, v237
v_and_b32 v155, 0xffff0000, v237
v_pk_mul_f32 v[152:153], v[152:153], v[128:129]
v_pk_mul_f32 v[154:155], v[154:155], v[130:131]
CV v236,v152,v153
CV v237,v154,v155
v_lshlrev_b32 v152, 16, v236
v_and_b32 v153, 0xffff0000, v236
v_lshlrev_b32 v154, 16, v237
v_and_b32 v155, 0xffff0000, v237
v_pk_add_f32 v[152:153], v[152:153], v[132:133]
v_pk_add_f32 v[154:155], v[154:155], v[134:135]
CV v238,v152,v153
CV v239,v154,v155
buffer_store_dwordx2 v[238:239], v254, s[40:43], s63 offen offset:0 sc1
s_nop 0
s_waitcnt lgkmcnt(2)
v_lshlrev_b32 v0, 16, v40
v_and_b32 v1, 0xffff0000, v40
v_lshlrev_b32 v2, 16, v41
v_and_b32 v3, 0xffff0000, v41
v_lshlrev_b32 v4, 16, v42
v_and_b32 v5, 0xffff0000, v42
v_lshlrev_b32 v6, 16, v43
v_and_b32 v7, 0xffff0000, v43
v_pk_mul_f32 v[0:1], v[0:1], v[160:161]
v_pk_mul_f32 v[2:3], v[2:3], v[162:163]
v_pk_mul_f32 v[4:5], v[4:5], v[164:165]
v_pk_mul_f32 v[6:7], v[6:7], v[166:167]
CV v40,v0,v1
CV v41,v2,v3
CV v42,v4,v5
CV v43,v6,v7
v_lshlrev_b32 v0, 16, v40
v_and_b32 v1, 0xffff0000, v40
v_lshlrev_b32 v2, 16, v41
v_and_b32 v3, 0xffff0000, v41
v_lshlrev_b32 v4, 16, v42
v_and_b32 v5, 0xffff0000, v42
v_lshlrev_b32 v6, 16, v43
v_and_b32 v7, 0xffff0000, v43
v_pk_add_f32 v[0:1], v[0:1], v[192:193]
v_pk_add_f32 v[2:3], v[2:3], v[194:195]
v_pk_add_f32 v[4:5], v[4:5], v[196:197]
v_pk_add_f32 v[6:7], v[6:7], v[198:199]
)ASM"
R"ASM(
s_nop 0
LPG30_%=:
s_cbranch_execz LTR1_%=
CV v40,v0,v1
CV v41,v2,v3
CV v42,v4,v5
CV v43,v6,v7
buffer_store_dwordx4 v[40:43], v251, s[40:43], s18 offen offset:0 sc1
s_waitcnt lgkmcnt(0)
v_lshlrev_b32 v8, 16, v44
v_and_b32 v9, 0xffff0000, v44
v_lshlrev_b32 v10, 16, v45
v_and_b32 v11, 0xffff0000, v45
v_lshlrev_b32 v12, 16, v46
v_and_b32 v13, 0xffff0000, v46
v_lshlrev_b32 v14, 16, v47
v_and_b32 v15, 0xffff0000, v47
v_pk_mul_f32 v[8:9], v[8:9], v[176:177]
v_pk_mul_f32 v[10:11], v[10:11], v[178:179]
v_pk_mul_f32 v[12:13], v[12:13], v[180:181]
v_pk_mul_f32 v[14:15], v[14:15], v[182:183]
CV v44,v8,v9
CV v45,v10,v11
CV v46,v12,v13
CV v47,v14,v15
v_lshlrev_b32 v8, 16, v44
v_and_b32 v9, 0xffff0000, v44
v_lshlrev_b32 v10, 16, v45
v_and_b32 v11, 0xffff0000, v45
v_lshlrev_b32 v12, 16, v46
v_and_b32 v13, 0xffff0000, v46
v_lshlrev_b32 v14, 16, v47
v_and_b32 v15, 0xffff0000, v47
v_pk_add_f32 v[8:9], v[8:9], v[208:209]
v_pk_add_f32 v[10:11], v[10:11], v[210:211]
v_pk_add_f32 v[12:13], v[12:13], v[212:213]
v_pk_add_f32 v[14:15], v[14:15], v[214:215]
CV v44,v8,v9
CV v45,v10,v11
CV v46,v12,v13
CV v47,v14,v15
buffer_store_dwordx4 v[44:47], v251, s[40:43], s80 offen offset:0 sc1
s_waitcnt vmcnt(0)
s_endpgm
LSI_%=:
s_memrealtime s[98:99]
s_andn2_b32 s91, s17, s58
s_mov_b32 s77, 0x7fffffff
s_waitcnt lgkmcnt(0)
s_sub_u32 s76, s98, s54
s_bitcmp0_b32 s91, 1
s_cbranch_scc1 LSM1_%=
s_sub_u32 s90, s13, s76
s_sub_u32 s90, s90, 12
s_cmp_gt_i32 s90, 0
s_cbranch_scc1 LSN1_%=
buffer_load_dwordx4 v[80:83], v248, s[20:23], s36 offen offset:0 sc1
buffer_load_dwordx4 v[84:87], v248, s[20:23], s36 offen offset:1024 sc1
s_bitset1_b32 s58, 1
s_branch LSM1_%=
LSN1_%=:
s_min_i32 s77, s77, s90
LSM1_%=:
s_bitcmp0_b32 s91, 3
s_cbranch_scc1 LSM3_%=
s_sub_u32 s90, s15, s76
s_sub_u32 s90, s90, 63
s_cmp_gt_i32 s90, 0
s_cbranch_scc1 LSN3_%=
buffer_load_dwordx4 v[112:115], v248, s[20:23], s38 offen offset:0 sc1
buffer_load_dwordx4 v[116:119], v248, s[20:23], s38 offen offset:1024 sc1
s_bitset1_b32 s58, 3
s_branch LSM3_%=
LSN3_%=:
s_min_i32 s77, s77, s90
LSM3_%=:
s_bitcmp0_b32 s91, 2
s_cbranch_scc1 LSM2_%=
s_sub_u32 s90, s14, s76
s_sub_u32 s90, s90, 97
s_cmp_gt_i32 s90, 0
s_cbranch_scc1 LSN2_%=
buffer_load_dwordx4 v[88:91], v248, s[20:23], s37 offen offset:0 sc1
buffer_load_dwordx4 v[92:95], v248, s[20:23], s37 offen offset:1024 sc1
s_bitset1_b32 s58, 2
s_branch LSM2_%=
LSN2_%=:
s_min_i32 s77, s77, s90
LSM2_%=:
s_bitcmp0_b32 s91, 0
s_cbranch_scc1 LSM0_%=
s_sub_u32 s90, s12, s76
s_sub_u32 s90, s90, 131
s_cmp_gt_i32 s90, 0
s_cbranch_scc1 LSN0_%=
buffer_load_dwordx4 v[120:123], v250, s[20:23], s39 offen offset:0 sc1
buffer_load_dwordx4 v[124:127], v250, s[20:23], s39 offen offset:256 sc1
s_bitset1_b32 s58, 0
s_branch LSM0_%=
LSN0_%=:
s_min_i32 s77, s77, s90
LSM0_%=:
s_cmp_eq_u32 s12, 4.0
s_cselect_b32 s90, 30, 45
s_cmp_lt_i32 s77, s90
s_cbranch_scc1 LSI_%=
buffer_load_dwordx2 v[156:157], v253, s[20:23], s68 offen offset:8 sc1
s_waitcnt vmcnt(1)
s_bitcmp0_b32 s58, 1
s_cbranch_scc1 LSB1_%=
v_max_u32 v237, v83, v87
v_cmp_eq_u32 vcc, -1, v237
s_cbranch_vccnz LSB1_%=
s_bitset0_b32 s17, 1
LSB1_%=:
s_bitcmp0_b32 s58, 3
s_cbranch_scc1 LSB3_%=
v_max_u32 v239, v115, v119
v_cmp_eq_u32 vcc, -1, v239
s_cbranch_vccnz LSB3_%=
s_bitset0_b32 s17, 3
LSB3_%=:
s_bitcmp0_b32 s58, 2
s_cbranch_scc1 LSB2_%=
v_max_u32 v238, v91, v95
v_cmp_eq_u32 vcc, -1, v238
s_cbranch_vccnz LSB2_%=
s_bitset0_b32 s17, 2
LSB2_%=:
s_bitcmp0_b32 s58, 0
s_cbranch_scc1 LSB0_%=
v_max_u32 v236, v123, v127
v_cmp_eq_u32 vcc, -1, v236
s_cbranch_vccnz LSB0_%=
s_bitset0_b32 s17, 0
LSB0_%=:
s_cmp_eq_u32 s17, 0
s_cbranch_scc1 LSF_%=
s_mov_b32 s58, 0
s_waitcnt vmcnt(0)
v_cmp_eq_u32 s[8:9], s69, v157
v_subrev_u32 v226, s56, v156
s_nop 0
v_cndmask_b32 v226, 4.0, v226, s[8:9]
s_nop 0
v_readlane_b32 s13, v226, 16
v_readlane_b32 s14, v226, 32
v_readlane_b32 s15, v226, 48
s_max_i32 s12, s13, s14
s_max_i32 s12, s12, s15
s_max_i32 s12, s12, 0
s_sub_u32 s73, s73, 1
s_cbranch_scc0 LSI_%=
LSE_%=:
s_waitcnt vmcnt(0)
s_endpgm
LRG1o_%=:
s_mov_b32 s70, 30000
LRG1ol_%=:
s_sleep 2
buffer_load_dwordx4 v[200:203], v208, s[20:23], s62 offen offset:0 sc1
buffer_load_dwordx4 v[204:207], v208, s[20:23], s93 offen offset:0 sc1
s_waitcnt vmcnt(0)
v_xor_b32 v201, s69, v201
v_xor_b32 v203, s69, v203
v_xor_b32 v205, s69, v205
v_xor_b32 v207, s69, v207
v_or3_b32 v209, v201, v203, v205
v_or_b32 v209, v209, v207
v_cmp_ne_u32 vcc, 0, v209
s_cbranch_vccz LRG1ob_%=
s_sub_u32 s70, s70, 1
s_cbranch_scc0 LRG1ol_%=
s_branch LRG1ob_%=
LRFR_%=:
s_mov_b32 s91, 30000
LRFRl_%=:
s_sleep 2
buffer_load_dwordx4 v[56:59], v248, s[20:23], s90 offen offset:0 sc1
s_waitcnt vmcnt(0)
v_xor_b32 v56, s69, v56
v_xor_b32 v57, s69, v57
v_xor_b32 v58, s69, v58
v_xor_b32 v59, s69, v59
v_or3_b32 v252, v56, v57, v58
v_or_b32 v252, v252, v59
v_cmp_ne_u32 vcc, 0, v252
s_cbranch_vccz LRFRb_%=
s_sub_u32 s91, s91, 1
s_cbranch_scc0 LRFRl_%=
s_branch LRFRb_%=
LRRK_%=:
s_mov_b32 s91, 30000
LRRKl_%=:
s_sleep 2
buffer_load_dwordx2 v[32:33], v34, s[20:23], s90 offen offset:0 sc1
s_waitcnt vmcnt(0)
v_cmp_ne_u32 vcc, s69, v33
s_cbranch_vccz LRRKb_%=
s_sub_u32 s91, s91, 1
s_cbranch_scc0 LRRKl_%=
s_branch LRRKb_%=
LRK_%=:
s_cmp_ge_u32 s60, 8
s_cbranch_scc1 LRK0_%=
v_mov_b32 v253, 0
v_cmp_lt_u32 vcc, s52, v96
v_cmp_lt_u32 s[98:99], s52, v97
s_nop 0
v_addc_co_u32 v253, vcc, 0, v253, vcc
v_addc_co_u32 v253, s[98:99], 0, v253, s[98:99]
v_cmp_lt_u32 vcc, s52, v98
v_cmp_lt_u32 s[98:99], s52, v99
s_nop 0
v_addc_co_u32 v253, vcc, 0, v253, vcc
v_addc_co_u32 v253, s[98:99], 0, v253, s[98:99]
v_cmp_lt_u32 vcc, s52, v100
v_cmp_lt_u32 s[98:99], s52, v101
s_nop 0
v_addc_co_u32 v253, vcc, 0, v253, vcc
v_addc_co_u32 v253, s[98:99], 0, v253, s[98:99]
v_cmp_lt_u32 vcc, s52, v102
v_cmp_lt_u32 s[98:99], s52, v103
s_nop 0
v_addc_co_u32 v253, vcc, 0, v253, vcc
v_addc_co_u32 v253, s[98:99], 0, v253, s[98:99]
v_cmp_lt_u32 vcc, s52, v104
v_cmp_lt_u32 s[98:99], s52, v105
s_nop 0
v_addc_co_u32 v253, vcc, 0, v253, vcc
v_addc_co_u32 v253, s[98:99], 0, v253, s[98:99]
v_cmp_lt_u32 vcc, s52, v106
v_cmp_lt_u32 s[98:99], s52, v107
s_nop 0
v_addc_co_u32 v253, vcc, 0, v253, vcc
v_addc_co_u32 v253, s[98:99], 0, v253, s[98:99]
v_cmp_lt_u32 vcc, s52, v108
v_cmp_lt_u32 s[98:99], s52, v109
s_nop 0
v_addc_co_u32 v253, vcc, 0, v253, vcc
v_addc_co_u32 v253, s[98:99], 0, v253, s[98:99]
v_cmp_lt_u32 vcc, s52, v110
v_cmp_lt_u32 s[98:99], s52, v111
s_nop 0
v_addc_co_u32 v253, vcc, 0, v253, vcc
v_addc_co_u32 v253, s[98:99], 0, v253, s[98:99]
v_cmp_lt_u32 vcc, s52, v112
v_cmp_lt_u32 s[98:99], s52, v113
s_nop 0
v_addc_co_u32 v253, vcc, 0, v253, vcc
v_addc_co_u32 v253, s[98:99], 0, v253, s[98:99]
v_cmp_lt_u32 vcc, s52, v114
v_cmp_lt_u32 s[98:99], s52, v115
s_nop 0
v_addc_co_u32 v253, vcc, 0, v253, vcc
v_addc_co_u32 v253, s[98:99], 0, v253, s[98:99]
v_cmp_lt_u32 vcc, s52, v116
v_cmp_lt_u32 s[98:99], s52, v117
s_nop 0
v_addc_co_u32 v253, vcc, 0, v253, vcc
v_addc_co_u32 v253, s[98:99], 0, v253, s[98:99]
v_cmp_lt_u32 vcc, s52, v118
v_cmp_lt_u32 s[98:99], s52, v119
s_nop 0
v_addc_co_u32 v253, vcc, 0, v253, vcc
v_addc_co_u32 v253, s[98:99], 0, v253, s[98:99]
v_cmp_lt_u32 vcc, s52, v120
v_cmp_lt_u32 s[98:99], s52, v121
s_nop 0
v_addc_co_u32 v253, vcc, 0, v253, vcc
v_addc_co_u32 v253, s[98:99], 0, v253, s[98:99]
v_cmp_lt_u32 vcc, s52, v122
v_cmp_lt_u32 s[98:99], s52, v123
s_nop 0
v_addc_co_u32 v253, vcc, 0, v253, vcc
v_addc_co_u32 v253, s[98:99], 0, v253, s[98:99]
v_cmp_lt_u32 vcc, s52, v124
v_cmp_lt_u32 s[98:99], s52, v125
s_nop 0
v_addc_co_u32 v253, vcc, 0, v253, vcc
v_addc_co_u32 v253, s[98:99], 0, v253, s[98:99]
v_cmp_lt_u32 vcc, s52, v126
v_cmp_lt_u32 s[98:99], s52, v127
s_nop 0
v_addc_co_u32 v253, vcc, 0, v253, vcc
v_addc_co_u32 v253, s[98:99], 0, v253, s[98:99]
s_nop 1
v_add_u32_dpp v253, v253, v253 row_ror:8 row_mask:0xf bank_mask:0xf
s_nop 1
v_add_u32_dpp v253, v253, v253 row_ror:4 row_mask:0xf bank_mask:0xf
s_nop 1
v_add_u32_dpp v253, v253, v253 row_ror:2 row_mask:0xf bank_mask:0xf
s_nop 1
v_add_u32_dpp v253, v253, v253 row_ror:1 row_mask:0xf bank_mask:0xf
v_mov_b32 v250, v253
s_nop 1
v_permlane16_swap_b32 v253, v250
v_add_u32 v253, v253, v250
v_mov_b32 v250, v253
s_nop 1
v_permlane32_swap_b32 v253, v250
v_add_u32 v253, v253, v250
v_cmp_gt_u32 vcc, 8, v253
v_mov_b32 v250, s52
v_mov_b32 v251, s69
v_lshlrev_b32 v253, 3, v253
s_mov_b32 s76, 94208
s_cbranch_vccz LRK0_%=
s_mov_b64 exec, 1
buffer_store_dwordx2 v[250:251], v253, s[20:23], s76 offen offset:0 sc1
s_mov_b64 exec, -1
s_nop 1
LRK0_%=:
s_cmp_ge_u32 s62, 8
s_cbranch_scc1 LRK1_%=
v_mov_b32 v253, 0
v_cmp_lt_u32 vcc, s53, v96
v_cmp_lt_u32 s[98:99], s53, v97
s_nop 0
v_addc_co_u32 v253, vcc, 0, v253, vcc
v_addc_co_u32 v253, s[98:99], 0, v253, s[98:99]
v_cmp_lt_u32 vcc, s53, v98
v_cmp_lt_u32 s[98:99], s53, v99
s_nop 0
v_addc_co_u32 v253, vcc, 0, v253, vcc
v_addc_co_u32 v253, s[98:99], 0, v253, s[98:99]
v_cmp_lt_u32 vcc, s53, v100
v_cmp_lt_u32 s[98:99], s53, v101
s_nop 0
v_addc_co_u32 v253, vcc, 0, v253, vcc
v_addc_co_u32 v253, s[98:99], 0, v253, s[98:99]
v_cmp_lt_u32 vcc, s53, v102
v_cmp_lt_u32 s[98:99], s53, v103
s_nop 0
v_addc_co_u32 v253, vcc, 0, v253, vcc
v_addc_co_u32 v253, s[98:99], 0, v253, s[98:99]
v_cmp_lt_u32 vcc, s53, v104
v_cmp_lt_u32 s[98:99], s53, v105
s_nop 0
v_addc_co_u32 v253, vcc, 0, v253, vcc
v_addc_co_u32 v253, s[98:99], 0, v253, s[98:99]
v_cmp_lt_u32 vcc, s53, v106
v_cmp_lt_u32 s[98:99], s53, v107
s_nop 0
v_addc_co_u32 v253, vcc, 0, v253, vcc
v_addc_co_u32 v253, s[98:99], 0, v253, s[98:99]
v_cmp_lt_u32 vcc, s53, v108
v_cmp_lt_u32 s[98:99], s53, v109
s_nop 0
v_addc_co_u32 v253, vcc, 0, v253, vcc
v_addc_co_u32 v253, s[98:99], 0, v253, s[98:99]
v_cmp_lt_u32 vcc, s53, v110
v_cmp_lt_u32 s[98:99], s53, v111
s_nop 0
v_addc_co_u32 v253, vcc, 0, v253, vcc
v_addc_co_u32 v253, s[98:99], 0, v253, s[98:99]
v_cmp_lt_u32 vcc, s53, v112
v_cmp_lt_u32 s[98:99], s53, v113
s_nop 0
v_addc_co_u32 v253, vcc, 0, v253, vcc
v_addc_co_u32 v253, s[98:99], 0, v253, s[98:99]
v_cmp_lt_u32 vcc, s53, v114
v_cmp_lt_u32 s[98:99], s53, v115
s_nop 0
v_addc_co_u32 v253, vcc, 0, v253, vcc
v_addc_co_u32 v253, s[98:99], 0, v253, s[98:99]
v_cmp_lt_u32 vcc, s53, v116
v_cmp_lt_u32 s[98:99], s53, v117
s_nop 0
v_addc_co_u32 v253, vcc, 0, v253, vcc
v_addc_co_u32 v253, s[98:99], 0, v253, s[98:99]
v_cmp_lt_u32 vcc, s53, v118
v_cmp_lt_u32 s[98:99], s53, v119
s_nop 0
v_addc_co_u32 v253, vcc, 0, v253, vcc
v_addc_co_u32 v253, s[98:99], 0, v253, s[98:99]
v_cmp_lt_u32 vcc, s53, v120
v_cmp_lt_u32 s[98:99], s53, v121
s_nop 0
v_addc_co_u32 v253, vcc, 0, v253, vcc
v_addc_co_u32 v253, s[98:99], 0, v253, s[98:99]
v_cmp_lt_u32 vcc, s53, v122
v_cmp_lt_u32 s[98:99], s53, v123
s_nop 0
v_addc_co_u32 v253, vcc, 0, v253, vcc
v_addc_co_u32 v253, s[98:99], 0, v253, s[98:99]
v_cmp_lt_u32 vcc, s53, v124
v_cmp_lt_u32 s[98:99], s53, v125
s_nop 0
v_addc_co_u32 v253, vcc, 0, v253, vcc
v_addc_co_u32 v253, s[98:99], 0, v253, s[98:99]
v_cmp_lt_u32 vcc, s53, v126
v_cmp_lt_u32 s[98:99], s53, v127
s_nop 0
v_addc_co_u32 v253, vcc, 0, v253, vcc
v_addc_co_u32 v253, s[98:99], 0, v253, s[98:99]
s_nop 1
v_add_u32_dpp v253, v253, v253 row_ror:8 row_mask:0xf bank_mask:0xf
s_nop 1
v_add_u32_dpp v253, v253, v253 row_ror:4 row_mask:0xf bank_mask:0xf
s_nop 1
v_add_u32_dpp v253, v253, v253 row_ror:2 row_mask:0xf bank_mask:0xf
s_nop 1
v_add_u32_dpp v253, v253, v253 row_ror:1 row_mask:0xf bank_mask:0xf
v_mov_b32 v250, v253
s_nop 1
v_permlane16_swap_b32 v253, v250
v_add_u32 v253, v253, v250
v_mov_b32 v250, v253
s_nop 1
v_permlane32_swap_b32 v253, v250
v_add_u32 v253, v253, v250
v_cmp_gt_u32 vcc, 8, v253
v_mov_b32 v250, s53
v_mov_b32 v251, s69
v_lshlrev_b32 v253, 3, v253
s_mov_b32 s76, 94208
s_cbranch_vccz LRK1_%=
s_mov_b64 exec, 1
buffer_store_dwordx2 v[250:251], v253, s[20:23], s76 offen offset:0 sc1
s_mov_b64 exec, -1
s_nop 1
LRK1_%=:
s_cmp_ge_u32 s56, 8
s_cbranch_scc1 LRK2_%=
v_mov_b32 v253, 0
v_cmp_lt_u32 vcc, s54, v96
v_cmp_lt_u32 s[98:99], s54, v97
s_nop 0
v_addc_co_u32 v253, vcc, 0, v253, vcc
v_addc_co_u32 v253, s[98:99], 0, v253, s[98:99]
v_cmp_lt_u32 vcc, s54, v98
v_cmp_lt_u32 s[98:99], s54, v99
s_nop 0
v_addc_co_u32 v253, vcc, 0, v253, vcc
)ASM"
R"ASM(
v_addc_co_u32 v253, s[98:99], 0, v253, s[98:99]
v_cmp_lt_u32 vcc, s54, v100
v_cmp_lt_u32 s[98:99], s54, v101
s_nop 0
v_addc_co_u32 v253, vcc, 0, v253, vcc
v_addc_co_u32 v253, s[98:99], 0, v253, s[98:99]
v_cmp_lt_u32 vcc, s54, v102
v_cmp_lt_u32 s[98:99], s54, v103
s_nop 0
v_addc_co_u32 v253, vcc, 0, v253, vcc
v_addc_co_u32 v253, s[98:99], 0, v253, s[98:99]
v_cmp_lt_u32 vcc, s54, v104
v_cmp_lt_u32 s[98:99], s54, v105
s_nop 0
v_addc_co_u32 v253, vcc, 0, v253, vcc
v_addc_co_u32 v253, s[98:99], 0, v253, s[98:99]
v_cmp_lt_u32 vcc, s54, v106
v_cmp_lt_u32 s[98:99], s54, v107
s_nop 0
v_addc_co_u32 v253, vcc, 0, v253, vcc
v_addc_co_u32 v253, s[98:99], 0, v253, s[98:99]
v_cmp_lt_u32 vcc, s54, v108
v_cmp_lt_u32 s[98:99], s54, v109
s_nop 0
v_addc_co_u32 v253, vcc, 0, v253, vcc
v_addc_co_u32 v253, s[98:99], 0, v253, s[98:99]
v_cmp_lt_u32 vcc, s54, v110
v_cmp_lt_u32 s[98:99], s54, v111
s_nop 0
v_addc_co_u32 v253, vcc, 0, v253, vcc
v_addc_co_u32 v253, s[98:99], 0, v253, s[98:99]
v_cmp_lt_u32 vcc, s54, v112
v_cmp_lt_u32 s[98:99], s54, v113
s_nop 0
v_addc_co_u32 v253, vcc, 0, v253, vcc
v_addc_co_u32 v253, s[98:99], 0, v253, s[98:99]
v_cmp_lt_u32 vcc, s54, v114
v_cmp_lt_u32 s[98:99], s54, v115
s_nop 0
v_addc_co_u32 v253, vcc, 0, v253, vcc
v_addc_co_u32 v253, s[98:99], 0, v253, s[98:99]
v_cmp_lt_u32 vcc, s54, v116
v_cmp_lt_u32 s[98:99], s54, v117
s_nop 0
v_addc_co_u32 v253, vcc, 0, v253, vcc
v_addc_co_u32 v253, s[98:99], 0, v253, s[98:99]
v_cmp_lt_u32 vcc, s54, v118
v_cmp_lt_u32 s[98:99], s54, v119
s_nop 0
v_addc_co_u32 v253, vcc, 0, v253, vcc
v_addc_co_u32 v253, s[98:99], 0, v253, s[98:99]
v_cmp_lt_u32 vcc, s54, v120
v_cmp_lt_u32 s[98:99], s54, v121
s_nop 0
v_addc_co_u32 v253, vcc, 0, v253, vcc
v_addc_co_u32 v253, s[98:99], 0, v253, s[98:99]
v_cmp_lt_u32 vcc, s54, v122
v_cmp_lt_u32 s[98:99], s54, v123
s_nop 0
v_addc_co_u32 v253, vcc, 0, v253, vcc
v_addc_co_u32 v253, s[98:99], 0, v253, s[98:99]
v_cmp_lt_u32 vcc, s54, v124
v_cmp_lt_u32 s[98:99], s54, v125
s_nop 0
v_addc_co_u32 v253, vcc, 0, v253, vcc
v_addc_co_u32 v253, s[98:99], 0, v253, s[98:99]
v_cmp_lt_u32 vcc, s54, v126
v_cmp_lt_u32 s[98:99], s54, v127
s_nop 0
v_addc_co_u32 v253, vcc, 0, v253, vcc
v_addc_co_u32 v253, s[98:99], 0, v253, s[98:99]
s_nop 1
v_add_u32_dpp v253, v253, v253 row_ror:8 row_mask:0xf bank_mask:0xf
s_nop 1
v_add_u32_dpp v253, v253, v253 row_ror:4 row_mask:0xf bank_mask:0xf
s_nop 1
v_add_u32_dpp v253, v253, v253 row_ror:2 row_mask:0xf bank_mask:0xf
s_nop 1
v_add_u32_dpp v253, v253, v253 row_ror:1 row_mask:0xf bank_mask:0xf
v_mov_b32 v250, v253
s_nop 1
v_permlane16_swap_b32 v253, v250
v_add_u32 v253, v253, v250
v_mov_b32 v250, v253
s_nop 1
v_permlane32_swap_b32 v253, v250
v_add_u32 v253, v253, v250
v_cmp_gt_u32 vcc, 8, v253
v_mov_b32 v250, s54
v_mov_b32 v251, s69
v_lshlrev_b32 v253, 3, v253
s_mov_b32 s76, 94208
s_cbranch_vccz LRK2_%=
s_mov_b64 exec, 1
buffer_store_dwordx2 v[250:251], v253, s[20:23], s76 offen offset:0 sc1
s_mov_b64 exec, -1
s_nop 1
LRK2_%=:
s_cmp_ge_u32 s58, 8
s_cbranch_scc1 LRK3_%=
v_mov_b32 v253, 0
v_cmp_lt_u32 vcc, s55, v96
v_cmp_lt_u32 s[98:99], s55, v97
s_nop 0
v_addc_co_u32 v253, vcc, 0, v253, vcc
v_addc_co_u32 v253, s[98:99], 0, v253, s[98:99]
v_cmp_lt_u32 vcc, s55, v98
v_cmp_lt_u32 s[98:99], s55, v99
s_nop 0
v_addc_co_u32 v253, vcc, 0, v253, vcc
v_addc_co_u32 v253, s[98:99], 0, v253, s[98:99]
v_cmp_lt_u32 vcc, s55, v100
v_cmp_lt_u32 s[98:99], s55, v101
s_nop 0
v_addc_co_u32 v253, vcc, 0, v253, vcc
v_addc_co_u32 v253, s[98:99], 0, v253, s[98:99]
v_cmp_lt_u32 vcc, s55, v102
v_cmp_lt_u32 s[98:99], s55, v103
s_nop 0
v_addc_co_u32 v253, vcc, 0, v253, vcc
v_addc_co_u32 v253, s[98:99], 0, v253, s[98:99]
v_cmp_lt_u32 vcc, s55, v104
v_cmp_lt_u32 s[98:99], s55, v105
s_nop 0
v_addc_co_u32 v253, vcc, 0, v253, vcc
v_addc_co_u32 v253, s[98:99], 0, v253, s[98:99]
v_cmp_lt_u32 vcc, s55, v106
v_cmp_lt_u32 s[98:99], s55, v107
s_nop 0
v_addc_co_u32 v253, vcc, 0, v253, vcc
v_addc_co_u32 v253, s[98:99], 0, v253, s[98:99]
v_cmp_lt_u32 vcc, s55, v108
v_cmp_lt_u32 s[98:99], s55, v109
s_nop 0
v_addc_co_u32 v253, vcc, 0, v253, vcc
v_addc_co_u32 v253, s[98:99], 0, v253, s[98:99]
v_cmp_lt_u32 vcc, s55, v110
v_cmp_lt_u32 s[98:99], s55, v111
s_nop 0
v_addc_co_u32 v253, vcc, 0, v253, vcc
v_addc_co_u32 v253, s[98:99], 0, v253, s[98:99]
v_cmp_lt_u32 vcc, s55, v112
v_cmp_lt_u32 s[98:99], s55, v113
s_nop 0
v_addc_co_u32 v253, vcc, 0, v253, vcc
v_addc_co_u32 v253, s[98:99], 0, v253, s[98:99]
v_cmp_lt_u32 vcc, s55, v114
v_cmp_lt_u32 s[98:99], s55, v115
s_nop 0
v_addc_co_u32 v253, vcc, 0, v253, vcc
v_addc_co_u32 v253, s[98:99], 0, v253, s[98:99]
v_cmp_lt_u32 vcc, s55, v116
v_cmp_lt_u32 s[98:99], s55, v117
s_nop 0
v_addc_co_u32 v253, vcc, 0, v253, vcc
v_addc_co_u32 v253, s[98:99], 0, v253, s[98:99]
v_cmp_lt_u32 vcc, s55, v118
v_cmp_lt_u32 s[98:99], s55, v119
s_nop 0
v_addc_co_u32 v253, vcc, 0, v253, vcc
v_addc_co_u32 v253, s[98:99], 0, v253, s[98:99]
v_cmp_lt_u32 vcc, s55, v120
v_cmp_lt_u32 s[98:99], s55, v121
s_nop 0
v_addc_co_u32 v253, vcc, 0, v253, vcc
v_addc_co_u32 v253, s[98:99], 0, v253, s[98:99]
v_cmp_lt_u32 vcc, s55, v122
v_cmp_lt_u32 s[98:99], s55, v123
s_nop 0
v_addc_co_u32 v253, vcc, 0, v253, vcc
v_addc_co_u32 v253, s[98:99], 0, v253, s[98:99]
v_cmp_lt_u32 vcc, s55, v124
v_cmp_lt_u32 s[98:99], s55, v125
s_nop 0
v_addc_co_u32 v253, vcc, 0, v253, vcc
v_addc_co_u32 v253, s[98:99], 0, v253, s[98:99]
v_cmp_lt_u32 vcc, s55, v126
v_cmp_lt_u32 s[98:99], s55, v127
s_nop 0
v_addc_co_u32 v253, vcc, 0, v253, vcc
v_addc_co_u32 v253, s[98:99], 0, v253, s[98:99]
s_nop 1
v_add_u32_dpp v253, v253, v253 row_ror:8 row_mask:0xf bank_mask:0xf
s_nop 1
v_add_u32_dpp v253, v253, v253 row_ror:4 row_mask:0xf bank_mask:0xf
s_nop 1
v_add_u32_dpp v253, v253, v253 row_ror:2 row_mask:0xf bank_mask:0xf
s_nop 1
v_add_u32_dpp v253, v253, v253 row_ror:1 row_mask:0xf bank_mask:0xf
v_mov_b32 v250, v253
s_nop 1
v_permlane16_swap_b32 v253, v250
v_add_u32 v253, v253, v250
v_mov_b32 v250, v253
s_nop 1
v_permlane32_swap_b32 v253, v250
v_add_u32 v253, v253, v250
v_cmp_gt_u32 vcc, 8, v253
v_mov_b32 v250, s55
v_mov_b32 v251, s69
v_lshlrev_b32 v253, 3, v253
s_mov_b32 s76, 94208
s_cbranch_vccz LRK3_%=
s_mov_b64 exec, 1
buffer_store_dwordx2 v[250:251], v253, s[20:23], s76 offen offset:0 sc1
s_mov_b64 exec, -1
s_nop 1
LRK3_%=:
s_branch LRKb_%=
LRIX_%=:
s_mov_b32 s91, 30000
LRIXl_%=:
s_sleep 2
v_bfe_u32 v251, v248, 4, 3
v_lshlrev_b32 v251, 3, v251
buffer_load_dwordx2 v[252:253], v251, s[20:23], s86 offen offset:0 sc1
s_waitcnt vmcnt(0)
v_cmp_ne_u32 vcc, s69, v253
s_cbranch_vccz LRIXb_%=
s_sub_u32 s91, s91, 1
s_cbranch_scc0 LRIXl_%=
s_branch LRIXb_%=
LRFG_%=:
s_mov_b32 s91, 30000
LRFGl_%=:
s_sleep 2
buffer_load_dword v252, v253, s[20:23], s76 offen offset:0 sc1
s_waitcnt vmcnt(0)
v_cmp_ne_u32 vcc, s69, v252
s_cbranch_vccz LRFGb_%=
s_sub_u32 s91, s91, 1
s_cbranch_scc0 LRFGl_%=
s_branch LRFGb_%=
LRFH_%=:
s_mov_b32 s91, 30000
LRFHl_%=:
s_sleep 2
buffer_load_dword v252, v253, s[20:23], s76 offen offset:0 sc1
s_waitcnt vmcnt(0)
v_cmp_ne_u32 vcc, s69, v252
s_cbranch_vccz LRFHb_%=
s_sub_u32 s91, s91, 1
s_cbranch_scc0 LRFHl_%=
s_branch LRFHb_%=
LRFY_%=:
s_mov_b32 s91, 30000
LRFYl_%=:
s_sleep 2
buffer_load_dword v252, v253, s[20:23], s76 offen offset:0 sc1
s_waitcnt vmcnt(0)
v_cmp_ne_u32 vcc, s69, v252
s_cbranch_vccz LRFYb_%=
s_sub_u32 s91, s91, 1
s_cbranch_scc0 LRFYl_%=
s_branch LRFYb_%=
LRPO0_%=:
s_mov_b32 s97, 30000
buffer_load_dword v242, v243, s[20:23], s44 offen offset:0 sc1
s_sleep 4
LRPO0l_%=:
buffer_load_dword v245, v243, s[20:23], s44 offen offset:0 sc1
s_waitcnt vmcnt(1)
v_cmp_ne_u32 vcc, s69, v242
s_cbranch_vccz LRPO0b_%=
buffer_load_dword v242, v243, s[20:23], s44 offen offset:0 sc1
s_waitcnt vmcnt(1)
v_cmp_ne_u32 vcc, s69, v245
s_cbranch_vccz LRPO0b_%=
s_sub_u32 s97, s97, 1
s_cbranch_scc0 LRPO0l_%=
s_branch LRPO0b_%=
LRPW_%=:
s_mov_b32 s61, 30000
LRPWl_%=:
s_sleep 2
buffer_load_dwordx2 v[96:97], v98, s[20:23], s62 offen offset:0 sc1
s_waitcnt vmcnt(0)
v_xor_b32 v96, s69, v96
v_xor_b32 v97, s69, v97
v_or_b32 v99, v96, v97
v_cmp_ne_u32 vcc, 0, v99
s_cbranch_vccz LRPWb_%=
s_sub_u32 s61, s61, 1
s_cbranch_scc0 LRPWl_%=
s_branch LRPWb_%=
LRPO1_%=:
s_mov_b32 s97, 30000
LRPO1l_%=:
s_sleep 2
buffer_load_dword v242, v243, s[20:23], s96 offen offset:16 sc1
s_waitcnt vmcnt(0)
v_cmp_ne_u32 vcc, s69, v242
s_cbranch_vccz LRPO1b_%=
s_sub_u32 s97, s97, 1
s_cbranch_scc0 LRPO1l_%=
s_branch LRPO1b_%=
LKEND_ka:
LRET_%=:
s_cmp_eq_u32 s73, 0
s_cbranch_scc1 LBACK0_%=
s_cmp_eq_u32 s73, 2
s_cbranch_scc1 LBACK2_%=
s_cmp_eq_u32 s73, 1
s_cbranch_scc1 LBACK1_%=
s_cmp_eq_u32 s73, 3
s_cbranch_scc1 LBACK3_%=
s_cmp_eq_u32 s73, 5
s_cbranch_scc1 LBACK5_%=
s_cmp_eq_u32 s73, 7
s_cbranch_scc1 LBACK7_%=
s_cmp_eq_u32 s73, 9
s_cbranch_scc1 LBACK9_%=
s_cmp_eq_u32 s73, 11
s_cbranch_scc1 LBACK11_%=
s_cmp_eq_u32 s73, 13
s_cbranch_scc1 LBACK13_%=
s_cmp_eq_u32 s73, 15
s_cbranch_scc1 LBACK15_%=
s_cmp_eq_u32 s73, 17
s_cbranch_scc1 LBACK17_%=
s_cmp_eq_u32 s73, 19
s_cbranch_scc1 LBACK19_%=
s_cmp_eq_u32 s73, 21
s_cbranch_scc1 LBACK21_%=
s_cmp_eq_u32 s73, 23
s_cbranch_scc1 LBACK23_%=
s_cmp_eq_u32 s73, 25
s_cbranch_scc1 LBACK25_%=
s_cmp_eq_u32 s73, 27
s_cbranch_scc1 LBACK27_%=
s_cmp_eq_u32 s73, 29
s_cbranch_scc1 LBACK29_%=
s_cmp_eq_u32 s73, 31
s_cbranch_scc1 LBACK31_%=
s_cmp_eq_u32 s73, 33
s_cbranch_scc1 LBACK33_%=
s_cmp_eq_u32 s73, 35
s_cbranch_scc1 LBACK35_%=
s_cmp_eq_u32 s73, 4
s_cbranch_scc1 LBACK4_%=
s_cmp_eq_u32 s73, 6
s_cbranch_scc1 LBACK6_%=
s_cmp_eq_u32 s73, 8
s_cbranch_scc1 LBACK8_%=
s_cmp_eq_u32 s73, 10
s_cbranch_scc1 LBACK10_%=
s_cmp_eq_u32 s73, 12
s_cbranch_scc1 LBACK12_%=
s_cmp_eq_u32 s73, 14
s_cbranch_scc1 LBACK14_%=
s_cmp_eq_u32 s73, 16
s_cbranch_scc1 LBACK16_%=
s_cmp_eq_u32 s73, 18
s_cbranch_scc1 LBACK18_%=
s_cmp_eq_u32 s73, 20
s_cbranch_scc1 LBACK20_%=
s_cmp_eq_u32 s73, 22
s_cbranch_scc1 LBACK22_%=
s_cmp_eq_u32 s73, 24
s_cbranch_scc1 LBACK24_%=
s_cmp_eq_u32 s73, 26
s_cbranch_scc1 LBACK26_%=
s_cmp_eq_u32 s73, 28
s_cbranch_scc1 LBACK28_%=
s_cmp_eq_u32 s73, 30
s_cbranch_scc1 LBACK30_%=
s_cmp_eq_u32 s73, 32
s_cbranch_scc1 LBACK32_%=
s_cmp_eq_u32 s73, 34
s_cbranch_scc1 LBACK34_%=
s_cmp_eq_u32 s73, 36
s_cbranch_scc1 LBACK36_%=
s_cmp_eq_u32 s73, 38
s_cbranch_scc1 LBACK38_%=
s_cmp_eq_u32 s73, 37
s_cbranch_scc1 LBACK37_%=
s_endpgm
LSITE0_%=:
s_mov_b32 s73, 0
s_mov_b32 s74, 0
s_mov_b32 s75, 16384
s_branch LRARE002_%=
LSITE2_%=:
s_mov_b32 s73, 2
s_mov_b32 s74, 32768
s_mov_b32 s75, 49152
s_branch LRARE002_%=
LSITE1_%=:
s_mov_b32 s73, 1
s_mov_b32 s74, 16384
s_mov_b32 s75, 32768
s_branch LRARE102_%=
LSITE3_%=:
s_mov_b32 s73, 3
s_mov_b32 s74, 49152
s_mov_b32 s75, 0
s_branch LRARE104_%=
LSITE5_%=:
s_mov_b32 s73, 5
s_mov_b32 s74, 16384
s_mov_b32 s75, 32768
s_branch LRARE104_%=
LSITE7_%=:
s_mov_b32 s73, 7
s_mov_b32 s74, 49152
s_mov_b32 s75, 0
s_branch LRARE104_%=
LSITE9_%=:
s_mov_b32 s73, 9
s_mov_b32 s74, 16384
s_mov_b32 s75, 32768
s_branch LRARE104_%=
LSITE11_%=:
s_mov_b32 s73, 11
s_mov_b32 s74, 49152
s_mov_b32 s75, 0
s_branch LRARE104_%=
LSITE13_%=:
s_mov_b32 s73, 13
s_mov_b32 s74, 16384
s_mov_b32 s75, 32768
s_branch LRARE104_%=
LSITE15_%=:
s_mov_b32 s73, 15
s_mov_b32 s74, 49152
s_mov_b32 s75, 0
s_branch LRARE104_%=
LSITE17_%=:
s_mov_b32 s73, 17
s_mov_b32 s74, 16384
s_mov_b32 s75, 32768
s_branch LRARE104_%=
LSITE19_%=:
s_mov_b32 s73, 19
s_mov_b32 s74, 49152
s_mov_b32 s75, 0
s_branch LRARE104_%=
LSITE21_%=:
s_mov_b32 s73, 21
s_mov_b32 s74, 16384
s_mov_b32 s75, 32768
s_branch LRARE104_%=
LSITE23_%=:
s_mov_b32 s73, 23
s_mov_b32 s74, 49152
s_mov_b32 s75, 0
s_branch LRARE104_%=
LSITE25_%=:
s_mov_b32 s73, 25
s_mov_b32 s74, 16384
s_mov_b32 s75, 32768
s_branch LRARE104_%=
LSITE27_%=:
s_mov_b32 s73, 27
s_mov_b32 s74, 49152
s_mov_b32 s75, 0
s_branch LRARE104_%=
LSITE29_%=:
s_mov_b32 s73, 29
s_mov_b32 s74, 16384
s_mov_b32 s75, 32768
s_branch LRARE104_%=
LSITE31_%=:
s_mov_b32 s73, 31
s_mov_b32 s74, 49152
s_mov_b32 s75, 0
s_branch LRARE104_%=
LSITE33_%=:
s_mov_b32 s73, 33
s_mov_b32 s74, 16384
s_mov_b32 s75, 32768
s_branch LRARE104_%=
LSITE35_%=:
s_mov_b32 s73, 35
s_mov_b32 s74, 49152
s_mov_b32 s75, 0
s_branch LRARE104_%=
LSITE4_%=:
)ASM"
R"ASM(
s_mov_b32 s73, 4
s_mov_b32 s74, 0
s_mov_b32 s75, 16384
s_branch LRARE004_%=
LSITE6_%=:
s_mov_b32 s73, 6
s_mov_b32 s74, 32768
s_mov_b32 s75, 49152
s_branch LRARE004_%=
LSITE8_%=:
s_mov_b32 s73, 8
s_mov_b32 s74, 0
s_mov_b32 s75, 16384
s_branch LRARE004_%=
LSITE10_%=:
s_mov_b32 s73, 10
s_mov_b32 s74, 32768
s_mov_b32 s75, 49152
s_branch LRARE004_%=
LSITE12_%=:
s_mov_b32 s73, 12
s_mov_b32 s74, 0
s_mov_b32 s75, 16384
s_branch LRARE004_%=
LSITE14_%=:
s_mov_b32 s73, 14
s_mov_b32 s74, 32768
s_mov_b32 s75, 49152
s_branch LRARE004_%=
LSITE16_%=:
s_mov_b32 s73, 16
s_mov_b32 s74, 0
s_mov_b32 s75, 16384
s_branch LRARE004_%=
LSITE18_%=:
s_mov_b32 s73, 18
s_mov_b32 s74, 32768
s_mov_b32 s75, 49152
s_branch LRARE004_%=
LSITE20_%=:
s_mov_b32 s73, 20
s_mov_b32 s74, 0
s_mov_b32 s75, 16384
s_branch LRARE004_%=
LSITE22_%=:
s_mov_b32 s73, 22
s_mov_b32 s74, 32768
s_mov_b32 s75, 49152
s_branch LRARE004_%=
LSITE24_%=:
s_mov_b32 s73, 24
s_mov_b32 s74, 0
s_mov_b32 s75, 16384
s_branch LRARE004_%=
LSITE26_%=:
s_mov_b32 s73, 26
s_mov_b32 s74, 32768
s_mov_b32 s75, 49152
s_branch LRARE004_%=
LSITE28_%=:
s_mov_b32 s73, 28
s_mov_b32 s74, 0
s_mov_b32 s75, 16384
s_branch LRARE004_%=
LSITE30_%=:
s_mov_b32 s73, 30
s_mov_b32 s74, 32768
s_mov_b32 s75, 49152
s_branch LRARE004_%=
LSITE32_%=:
s_mov_b32 s73, 32
s_mov_b32 s74, 0
s_mov_b32 s75, 16384
s_branch LRARE004_%=
LSITE34_%=:
s_mov_b32 s73, 34
s_mov_b32 s74, 32768
s_mov_b32 s75, 49152
s_branch LRARE004_%=
LSITE36_%=:
s_mov_b32 s73, 36
s_mov_b32 s74, 0
s_mov_b32 s75, 16384
s_branch LRARE022_%=
LSITE38_%=:
s_mov_b32 s73, 38
s_mov_b32 s74, 32768
s_mov_b32 s75, 49152
s_branch LRARE022_%=
LSITE37_%=:
s_mov_b32 s73, 37
s_mov_b32 s74, 16384
s_mov_b32 s75, 32768
s_branch LRARE122_%=
LRARE002_%=:
s_nop 15
s_waitcnt vmcnt(0) lgkmcnt(0)
v_add_u32 v249, s74, v224
RD a[192:195],v249 offset:0
RD a[208:211],v249 offset:4096
RD a[224:227],v249 offset:8192
RD a[240:243],v249 offset:12288
v_add_u32 v250, s74, v225
RD a[196:199],v250 offset:0
RD a[212:215],v250 offset:4096
RD a[228:231],v250 offset:8192
RD a[244:247],v250 offset:12288
v_add_u32 v251, s74, v226
RD a[200:203],v251 offset:0
RD a[216:219],v251 offset:4096
RD a[232:235],v251 offset:8192
RD a[248:251],v251 offset:12288
v_add_u32 v254, s74, v227
RD a[204:207],v254 offset:0
RD a[220:223],v254 offset:4096
RD a[236:239],v254 offset:8192
RD a[252:255],v254 offset:12288
s_waitcnt lgkmcnt(0)
MM v[0:3],a[192:195],v[160:163],v[128:131]
MM v[0:3],a[196:199],v[176:179],v[0:3]
MM v[0:3],a[200:203],v[192:195],v[0:3]
MM v[0:3],a[204:207],v[208:211],v[0:3]
MM v[4:7],a[220:223],v[208:211],v[128:131]
MM v[4:7],a[216:219],v[192:195],v[4:7]
MM v[4:7],a[212:215],v[176:179],v[4:7]
MM v[4:7],a[208:211],v[160:163],v[4:7]
MM v[12:15],a[208:211],v[164:167],v[132:135]
MM v[12:15],a[212:215],v[180:183],v[12:15]
MM v[12:15],a[216:219],v[196:199],v[12:15]
MM v[12:15],a[220:223],v[212:215],v[12:15]
MM v[8:11],a[204:207],v[212:215],v[132:135]
MM v[8:11],a[200:203],v[196:199],v[8:11]
MM v[8:11],a[196:199],v[180:183],v[8:11]
MM v[8:11],a[192:195],v[164:167],v[8:11]
MM v[32:35],a[224:227],v[160:163],v[128:131]
MM v[32:35],a[228:231],v[176:179],v[32:35]
MM v[32:35],a[232:235],v[192:195],v[32:35]
MM v[32:35],a[236:239],v[208:211],v[32:35]
MM v[36:39],a[252:255],v[208:211],v[128:131]
MM v[36:39],a[248:251],v[192:195],v[36:39]
MM v[36:39],a[244:247],v[176:179],v[36:39]
MM v[36:39],a[240:243],v[160:163],v[36:39]
MM v[44:47],a[240:243],v[164:167],v[132:135]
MM v[44:47],a[244:247],v[180:183],v[44:47]
MM v[44:47],a[248:251],v[196:199],v[44:47]
MM v[44:47],a[252:255],v[212:215],v[44:47]
MM v[40:43],a[236:239],v[212:215],v[132:135]
MM v[40:43],a[232:235],v[196:199],v[40:43]
MM v[40:43],a[228:231],v[180:183],v[40:43]
MM v[40:43],a[224:227],v[164:167],v[40:43]
s_nop 15
v_add_u32 v249, s75, v224
RD a[192:195],v249 offset:0
RD a[208:211],v249 offset:4096
RD a[224:227],v249 offset:8192
RD a[240:243],v249 offset:12288
v_add_u32 v250, s75, v225
RD a[196:199],v250 offset:0
RD a[212:215],v250 offset:4096
RD a[228:231],v250 offset:8192
RD a[244:247],v250 offset:12288
v_add_u32 v251, s75, v226
RD a[200:203],v251 offset:0
RD a[216:219],v251 offset:4096
RD a[232:235],v251 offset:8192
RD a[248:251],v251 offset:12288
v_add_u32 v254, s75, v227
RD a[204:207],v254 offset:0
RD a[220:223],v254 offset:4096
RD a[236:239],v254 offset:8192
RD a[252:255],v254 offset:12288
s_waitcnt lgkmcnt(0)
v_max3_f32 v249, v0, v1, v2
v_max3_f32 v251, v32, v33, v34
v_max3_f32 v249, v249, v3, v4
v_max3_f32 v251, v251, v35, v36
v_max3_f32 v249, v249, v5, v6
v_max3_f32 v251, v251, v37, v38
v_max3_f32 v249, v249, v7, v39
v_max_f32 v249, v249, v251
v_mov_b32 v251, v249
s_nop 1
v_permlane16_swap_b32 v249, v251
v_max_f32 v249, v249, v251
v_mov_b32 v251, v249
s_nop 1
v_permlane32_swap_b32 v249, v251
v_max_f32 v249, v249, v251
v_log_f32 v250, v244
s_nop 0
v_max3_f32 v249, 0, v249, v250
v_sub_f32 v250, 0, v249
EX v250,v250
v_sub_f32 v128, v128, v249
v_sub_f32 v129, v129, v249
v_sub_f32 v130, v130, v249
v_sub_f32 v131, v131, v249
v_sub_f32 v64, v64, v249
v_sub_f32 v0, v0, v249
v_sub_f32 v65, v65, v249
v_sub_f32 v1, v1, v249
v_sub_f32 v66, v66, v249
v_sub_f32 v2, v2, v249
v_sub_f32 v67, v67, v249
v_sub_f32 v3, v3, v249
v_sub_f32 v68, v68, v249
v_sub_f32 v4, v4, v249
v_sub_f32 v69, v69, v249
v_sub_f32 v5, v5, v249
v_sub_f32 v70, v70, v249
v_sub_f32 v6, v6, v249
v_sub_f32 v71, v71, v249
v_sub_f32 v7, v7, v249
v_sub_f32 v96, v96, v249
v_sub_f32 v32, v32, v249
v_sub_f32 v97, v97, v249
v_sub_f32 v33, v33, v249
v_sub_f32 v98, v98, v249
v_sub_f32 v34, v34, v249
v_sub_f32 v99, v99, v249
v_sub_f32 v35, v35, v249
v_sub_f32 v100, v100, v249
v_sub_f32 v36, v36, v249
v_sub_f32 v101, v101, v249
v_sub_f32 v37, v37, v249
v_sub_f32 v102, v102, v249
v_sub_f32 v38, v38, v249
v_sub_f32 v103, v103, v249
v_sub_f32 v39, v39, v249
v_mul_f32 v144, v244, v250
v_mov_b32 v145, 0
v_bfe_u32 v249, v241, 0, 8
v_lshl_or_b32 v249, v249, 1, 1
v_lshlrev_b32 v249, 22, v249
v_and_b32 v241, 0xffffff00, v241
AR v254,a0
v_sub_f32 v254, v254, v249
v_mul_f32 v254, v254, v250
AW a0,v254
AR v251,a1
v_sub_f32 v251, v251, v249
v_mul_f32 v251, v251, v250
AW a1,v251
AR v254,a2
v_sub_f32 v254, v254, v249
v_mul_f32 v254, v254, v250
AW a2,v254
AR v251,a3
v_sub_f32 v251, v251, v249
v_mul_f32 v251, v251, v250
AW a3,v251
AR v254,a4
v_sub_f32 v254, v254, v249
v_mul_f32 v254, v254, v250
AW a4,v254
AR v251,a5
v_sub_f32 v251, v251, v249
v_mul_f32 v251, v251, v250
AW a5,v251
AR v254,a6
v_sub_f32 v254, v254, v249
v_mul_f32 v254, v254, v250
AW a6,v254
AR v251,a7
v_sub_f32 v251, v251, v249
v_mul_f32 v251, v251, v250
AW a7,v251
AR v254,a32
v_sub_f32 v254, v254, v249
v_mul_f32 v254, v254, v250
AW a32,v254
AR v251,a33
v_sub_f32 v251, v251, v249
v_mul_f32 v251, v251, v250
AW a33,v251
AR v254,a34
v_sub_f32 v254, v254, v249
v_mul_f32 v254, v254, v250
AW a34,v254
AR v251,a35
v_sub_f32 v251, v251, v249
v_mul_f32 v251, v251, v250
AW a35,v251
AR v254,a36
v_sub_f32 v254, v254, v249
v_mul_f32 v254, v254, v250
AW a36,v254
AR v251,a37
v_sub_f32 v251, v251, v249
v_mul_f32 v251, v251, v250
AW a37,v251
AR v254,a38
v_sub_f32 v254, v254, v249
v_mul_f32 v254, v254, v250
AW a38,v254
AR v251,a39
v_sub_f32 v251, v251, v249
v_mul_f32 v251, v251, v250
AW a39,v251
AR v254,a64
v_sub_f32 v254, v254, v249
v_mul_f32 v254, v254, v250
AW a64,v254
AR v251,a65
v_sub_f32 v251, v251, v249
v_mul_f32 v251, v251, v250
AW a65,v251
AR v254,a66
v_sub_f32 v254, v254, v249
v_mul_f32 v254, v254, v250
AW a66,v254
AR v251,a67
v_sub_f32 v251, v251, v249
v_mul_f32 v251, v251, v250
AW a67,v251
AR v254,a68
v_sub_f32 v254, v254, v249
v_mul_f32 v254, v254, v250
AW a68,v254
AR v251,a69
v_sub_f32 v251, v251, v249
v_mul_f32 v251, v251, v250
AW a69,v251
AR v254,a70
v_sub_f32 v254, v254, v249
v_mul_f32 v254, v254, v250
AW a70,v254
AR v251,a71
v_sub_f32 v251, v251, v249
v_mul_f32 v251, v251, v250
AW a71,v251
AR v254,a96
v_sub_f32 v254, v254, v249
v_mul_f32 v254, v254, v250
AW a96,v254
AR v251,a97
v_sub_f32 v251, v251, v249
v_mul_f32 v251, v251, v250
AW a97,v251
AR v254,a98
v_sub_f32 v254, v254, v249
v_mul_f32 v254, v254, v250
AW a98,v254
AR v251,a99
v_sub_f32 v251, v251, v249
v_mul_f32 v251, v251, v250
AW a99,v251
AR v254,a100
v_sub_f32 v254, v254, v249
v_mul_f32 v254, v254, v250
AW a100,v254
AR v251,a101
v_sub_f32 v251, v251, v249
v_mul_f32 v251, v251, v250
AW a101,v251
AR v254,a102
v_sub_f32 v254, v254, v249
v_mul_f32 v254, v254, v250
AW a102,v254
AR v251,a103
v_sub_f32 v251, v251, v249
v_mul_f32 v251, v251, v250
AW a103,v251
v_max3_f32 v249, v8, v9, v10
v_max3_f32 v251, v40, v41, v42
v_max3_f32 v249, v249, v11, v12
v_max3_f32 v251, v251, v43, v44
v_max3_f32 v249, v249, v13, v14
v_max3_f32 v251, v251, v45, v46
v_max3_f32 v249, v249, v15, v47
v_max_f32 v249, v249, v251
v_mov_b32 v251, v249
s_nop 1
v_permlane16_swap_b32 v249, v251
v_max_f32 v249, v249, v251
v_mov_b32 v251, v249
s_nop 1
v_permlane32_swap_b32 v249, v251
v_max_f32 v249, v249, v251
v_log_f32 v250, v245
s_nop 0
v_max3_f32 v249, 0, v249, v250
v_sub_f32 v250, 0, v249
EX v250,v250
v_sub_f32 v132, v132, v249
v_sub_f32 v133, v133, v249
v_sub_f32 v134, v134, v249
v_sub_f32 v135, v135, v249
v_sub_f32 v72, v72, v249
v_sub_f32 v8, v8, v249
v_sub_f32 v73, v73, v249
v_sub_f32 v9, v9, v249
v_sub_f32 v74, v74, v249
v_sub_f32 v10, v10, v249
v_sub_f32 v75, v75, v249
v_sub_f32 v11, v11, v249
v_sub_f32 v76, v76, v249
v_sub_f32 v12, v12, v249
v_sub_f32 v77, v77, v249
v_sub_f32 v13, v13, v249
v_sub_f32 v78, v78, v249
v_sub_f32 v14, v14, v249
v_sub_f32 v79, v79, v249
v_sub_f32 v15, v15, v249
v_sub_f32 v104, v104, v249
v_sub_f32 v40, v40, v249
v_sub_f32 v105, v105, v249
v_sub_f32 v41, v41, v249
v_sub_f32 v106, v106, v249
v_sub_f32 v42, v42, v249
v_sub_f32 v107, v107, v249
v_sub_f32 v43, v43, v249
v_sub_f32 v108, v108, v249
v_sub_f32 v44, v44, v249
v_sub_f32 v109, v109, v249
v_sub_f32 v45, v45, v249
v_sub_f32 v110, v110, v249
v_sub_f32 v46, v46, v249
v_sub_f32 v111, v111, v249
v_sub_f32 v47, v47, v249
v_mul_f32 v148, v245, v250
v_mov_b32 v149, 0
v_bfe_u32 v249, v241, 8, 8
v_lshl_or_b32 v249, v249, 1, 1
v_lshlrev_b32 v249, 22, v249
v_and_b32 v241, 0xffff00ff, v241
AR v254,a8
v_sub_f32 v254, v254, v249
v_mul_f32 v254, v254, v250
AW a8,v254
AR v251,a9
v_sub_f32 v251, v251, v249
v_mul_f32 v251, v251, v250
AW a9,v251
AR v254,a10
v_sub_f32 v254, v254, v249
v_mul_f32 v254, v254, v250
AW a10,v254
AR v251,a11
v_sub_f32 v251, v251, v249
v_mul_f32 v251, v251, v250
AW a11,v251
AR v254,a12
v_sub_f32 v254, v254, v249
v_mul_f32 v254, v254, v250
AW a12,v254
AR v251,a13
v_sub_f32 v251, v251, v249
v_mul_f32 v251, v251, v250
AW a13,v251
AR v254,a14
v_sub_f32 v254, v254, v249
v_mul_f32 v254, v254, v250
AW a14,v254
AR v251,a15
v_sub_f32 v251, v251, v249
v_mul_f32 v251, v251, v250
AW a15,v251
AR v254,a40
v_sub_f32 v254, v254, v249
v_mul_f32 v254, v254, v250
AW a40,v254
AR v251,a41
v_sub_f32 v251, v251, v249
v_mul_f32 v251, v251, v250
AW a41,v251
AR v254,a42
v_sub_f32 v254, v254, v249
v_mul_f32 v254, v254, v250
AW a42,v254
AR v251,a43
v_sub_f32 v251, v251, v249
v_mul_f32 v251, v251, v250
AW a43,v251
AR v254,a44
v_sub_f32 v254, v254, v249
v_mul_f32 v254, v254, v250
AW a44,v254
AR v251,a45
v_sub_f32 v251, v251, v249
v_mul_f32 v251, v251, v250
AW a45,v251
AR v254,a46
v_sub_f32 v254, v254, v249
v_mul_f32 v254, v254, v250
AW a46,v254
AR v251,a47
v_sub_f32 v251, v251, v249
v_mul_f32 v251, v251, v250
AW a47,v251
AR v254,a72
v_sub_f32 v254, v254, v249
v_mul_f32 v254, v254, v250
AW a72,v254
AR v251,a73
v_sub_f32 v251, v251, v249
v_mul_f32 v251, v251, v250
AW a73,v251
AR v254,a74
v_sub_f32 v254, v254, v249
v_mul_f32 v254, v254, v250
AW a74,v254
AR v251,a75
v_sub_f32 v251, v251, v249
v_mul_f32 v251, v251, v250
AW a75,v251
AR v254,a76
v_sub_f32 v254, v254, v249
v_mul_f32 v254, v254, v250
AW a76,v254
AR v251,a77
v_sub_f32 v251, v251, v249
v_mul_f32 v251, v251, v250
AW a77,v251
AR v254,a78
v_sub_f32 v254, v254, v249
v_mul_f32 v254, v254, v250
AW a78,v254
AR v251,a79
v_sub_f32 v251, v251, v249
v_mul_f32 v251, v251, v250
AW a79,v251
AR v254,a104
v_sub_f32 v254, v254, v249
v_mul_f32 v254, v254, v250
AW a104,v254
AR v251,a105
v_sub_f32 v251, v251, v249
)ASM"
R"ASM(
v_mul_f32 v251, v251, v250
AW a105,v251
AR v254,a106
v_sub_f32 v254, v254, v249
v_mul_f32 v254, v254, v250
AW a106,v254
AR v251,a107
v_sub_f32 v251, v251, v249
v_mul_f32 v251, v251, v250
AW a107,v251
AR v254,a108
v_sub_f32 v254, v254, v249
v_mul_f32 v254, v254, v250
AW a108,v254
AR v251,a109
v_sub_f32 v251, v251, v249
v_mul_f32 v251, v251, v250
AW a109,v251
AR v254,a110
v_sub_f32 v254, v254, v249
v_mul_f32 v254, v254, v250
AW a110,v254
AR v251,a111
v_sub_f32 v251, v251, v249
v_mul_f32 v251, v251, v250
AW a111,v251
EX v0,v0
EX v1,v1
EX v2,v2
EX v3,v3
EX v4,v4
EX v5,v5
EX v6,v6
EX v7,v7
EX v8,v8
EX v9,v9
EX v10,v10
EX v11,v11
EX v12,v12
EX v13,v13
EX v14,v14
EX v15,v15
EX v32,v32
EX v33,v33
EX v34,v34
EX v35,v35
EX v36,v36
EX v37,v37
EX v38,v38
EX v39,v39
EX v40,v40
EX v41,v41
EX v42,v42
EX v43,v43
EX v44,v44
EX v45,v45
EX v46,v46
EX v47,v47
CV v0,v0,v1
CV v1,v2,v3
CV v2,v4,v5
CV v3,v6,v7
CV v8,v8,v9
CV v9,v10,v11
CV v10,v12,v13
CV v11,v14,v15
CV v4,v32,v33
CV v5,v34,v35
CV v6,v36,v37
CV v7,v38,v39
CV v12,v40,v41
CV v13,v42,v43
CV v14,v44,v45
CV v15,v46,v47
v_smfmac_f32_16x16x64_bf16 v[144:147], v[236:239], v[0:7], v234
s_nop 0
v_smfmac_f32_16x16x64_bf16 v[148:151], v[236:239], v[8:15], v234
s_nop 9
s_branch LRET_%=
LRARE102_%=:
s_nop 15
s_waitcnt vmcnt(0) lgkmcnt(0)
v_add_u32 v249, s74, v224
RD a[192:195],v249 offset:0
RD a[208:211],v249 offset:4096
RD a[224:227],v249 offset:8192
RD a[240:243],v249 offset:12288
v_add_u32 v250, s74, v225
RD a[196:199],v250 offset:0
RD a[212:215],v250 offset:4096
RD a[228:231],v250 offset:8192
RD a[244:247],v250 offset:12288
v_add_u32 v251, s74, v226
RD a[200:203],v251 offset:0
RD a[216:219],v251 offset:4096
RD a[232:235],v251 offset:8192
RD a[248:251],v251 offset:12288
v_add_u32 v254, s74, v227
RD a[204:207],v254 offset:0
RD a[220:223],v254 offset:4096
RD a[236:239],v254 offset:8192
RD a[252:255],v254 offset:12288
s_waitcnt lgkmcnt(0)
MM v[64:67],a[192:195],v[160:163],v[128:131]
MM v[64:67],a[196:199],v[176:179],v[64:67]
MM v[64:67],a[200:203],v[192:195],v[64:67]
MM v[64:67],a[204:207],v[208:211],v[64:67]
MM v[68:71],a[220:223],v[208:211],v[128:131]
MM v[68:71],a[216:219],v[192:195],v[68:71]
MM v[68:71],a[212:215],v[176:179],v[68:71]
MM v[68:71],a[208:211],v[160:163],v[68:71]
MM v[76:79],a[208:211],v[164:167],v[132:135]
MM v[76:79],a[212:215],v[180:183],v[76:79]
MM v[76:79],a[216:219],v[196:199],v[76:79]
MM v[76:79],a[220:223],v[212:215],v[76:79]
MM v[72:75],a[204:207],v[212:215],v[132:135]
MM v[72:75],a[200:203],v[196:199],v[72:75]
MM v[72:75],a[196:199],v[180:183],v[72:75]
MM v[72:75],a[192:195],v[164:167],v[72:75]
MM v[96:99],a[224:227],v[160:163],v[128:131]
MM v[96:99],a[228:231],v[176:179],v[96:99]
MM v[96:99],a[232:235],v[192:195],v[96:99]
MM v[96:99],a[236:239],v[208:211],v[96:99]
MM v[100:103],a[252:255],v[208:211],v[128:131]
MM v[100:103],a[248:251],v[192:195],v[100:103]
MM v[100:103],a[244:247],v[176:179],v[100:103]
MM v[100:103],a[240:243],v[160:163],v[100:103]
MM v[108:111],a[240:243],v[164:167],v[132:135]
MM v[108:111],a[244:247],v[180:183],v[108:111]
MM v[108:111],a[248:251],v[196:199],v[108:111]
MM v[108:111],a[252:255],v[212:215],v[108:111]
MM v[104:107],a[236:239],v[212:215],v[132:135]
MM v[104:107],a[232:235],v[196:199],v[104:107]
MM v[104:107],a[228:231],v[180:183],v[104:107]
MM v[104:107],a[224:227],v[164:167],v[104:107]
s_nop 15
v_add_u32 v249, s75, v224
RD a[192:195],v249 offset:0
RD a[208:211],v249 offset:4096
RD a[224:227],v249 offset:8192
RD a[240:243],v249 offset:12288
v_add_u32 v250, s75, v225
RD a[196:199],v250 offset:0
RD a[212:215],v250 offset:4096
RD a[228:231],v250 offset:8192
RD a[244:247],v250 offset:12288
v_add_u32 v251, s75, v226
RD a[200:203],v251 offset:0
RD a[216:219],v251 offset:4096
RD a[232:235],v251 offset:8192
RD a[248:251],v251 offset:12288
v_add_u32 v254, s75, v227
RD a[204:207],v254 offset:0
RD a[220:223],v254 offset:4096
RD a[236:239],v254 offset:8192
RD a[252:255],v254 offset:12288
s_waitcnt lgkmcnt(0)
v_max3_f32 v249, v64, v65, v66
v_max3_f32 v251, v96, v97, v98
v_max3_f32 v249, v249, v67, v68
v_max3_f32 v251, v251, v99, v100
v_max3_f32 v249, v249, v69, v70
v_max3_f32 v251, v251, v101, v102
v_max3_f32 v249, v249, v71, v103
v_max_f32 v249, v249, v251
v_mov_b32 v251, v249
s_nop 1
v_permlane16_swap_b32 v249, v251
v_max_f32 v249, v249, v251
v_mov_b32 v251, v249
s_nop 1
v_permlane32_swap_b32 v249, v251
v_max_f32 v249, v249, v251
v_log_f32 v250, v244
s_nop 0
v_max3_f32 v249, 0, v249, v250
v_sub_f32 v250, 0, v249
EX v250,v250
v_sub_f32 v128, v128, v249
v_sub_f32 v129, v129, v249
v_sub_f32 v130, v130, v249
v_sub_f32 v131, v131, v249
v_sub_f32 v0, v0, v249
v_sub_f32 v64, v64, v249
v_sub_f32 v1, v1, v249
v_sub_f32 v65, v65, v249
v_sub_f32 v2, v2, v249
v_sub_f32 v66, v66, v249
v_sub_f32 v3, v3, v249
v_sub_f32 v67, v67, v249
v_sub_f32 v4, v4, v249
v_sub_f32 v68, v68, v249
v_sub_f32 v5, v5, v249
v_sub_f32 v69, v69, v249
v_sub_f32 v6, v6, v249
v_sub_f32 v70, v70, v249
v_sub_f32 v7, v7, v249
v_sub_f32 v71, v71, v249
v_sub_f32 v32, v32, v249
v_sub_f32 v96, v96, v249
v_sub_f32 v33, v33, v249
v_sub_f32 v97, v97, v249
v_sub_f32 v34, v34, v249
v_sub_f32 v98, v98, v249
v_sub_f32 v35, v35, v249
v_sub_f32 v99, v99, v249
v_sub_f32 v36, v36, v249
v_sub_f32 v100, v100, v249
v_sub_f32 v37, v37, v249
v_sub_f32 v101, v101, v249
v_sub_f32 v38, v38, v249
v_sub_f32 v102, v102, v249
v_sub_f32 v39, v39, v249
v_sub_f32 v103, v103, v249
v_mul_f32 v144, v244, v250
v_mov_b32 v145, 0
v_bfe_u32 v249, v241, 0, 8
v_lshl_or_b32 v249, v249, 1, 1
v_lshlrev_b32 v249, 22, v249
v_and_b32 v241, 0xffffff00, v241
AR v254,a0
v_sub_f32 v254, v254, v249
v_mul_f32 v254, v254, v250
AW a0,v254
AR v251,a1
v_sub_f32 v251, v251, v249
v_mul_f32 v251, v251, v250
AW a1,v251
AR v254,a2
v_sub_f32 v254, v254, v249
v_mul_f32 v254, v254, v250
AW a2,v254
AR v251,a3
v_sub_f32 v251, v251, v249
v_mul_f32 v251, v251, v250
AW a3,v251
AR v254,a4
v_sub_f32 v254, v254, v249
v_mul_f32 v254, v254, v250
AW a4,v254
AR v251,a5
v_sub_f32 v251, v251, v249
v_mul_f32 v251, v251, v250
AW a5,v251
AR v254,a6
v_sub_f32 v254, v254, v249
v_mul_f32 v254, v254, v250
AW a6,v254
AR v251,a7
v_sub_f32 v251, v251, v249
v_mul_f32 v251, v251, v250
AW a7,v251
AR v254,a32
v_sub_f32 v254, v254, v249
v_mul_f32 v254, v254, v250
AW a32,v254
AR v251,a33
v_sub_f32 v251, v251, v249
v_mul_f32 v251, v251, v250
AW a33,v251
AR v254,a34
v_sub_f32 v254, v254, v249
v_mul_f32 v254, v254, v250
AW a34,v254
AR v251,a35
v_sub_f32 v251, v251, v249
v_mul_f32 v251, v251, v250
AW a35,v251
AR v254,a36
v_sub_f32 v254, v254, v249
v_mul_f32 v254, v254, v250
AW a36,v254
AR v251,a37
v_sub_f32 v251, v251, v249
v_mul_f32 v251, v251, v250
AW a37,v251
AR v254,a38
v_sub_f32 v254, v254, v249
v_mul_f32 v254, v254, v250
AW a38,v254
AR v251,a39
v_sub_f32 v251, v251, v249
v_mul_f32 v251, v251, v250
AW a39,v251
AR v254,a64
v_sub_f32 v254, v254, v249
v_mul_f32 v254, v254, v250
AW a64,v254
AR v251,a65
v_sub_f32 v251, v251, v249
v_mul_f32 v251, v251, v250
AW a65,v251
AR v254,a66
v_sub_f32 v254, v254, v249
v_mul_f32 v254, v254, v250
AW a66,v254
AR v251,a67
v_sub_f32 v251, v251, v249
v_mul_f32 v251, v251, v250
AW a67,v251
AR v254,a68
v_sub_f32 v254, v254, v249
v_mul_f32 v254, v254, v250
AW a68,v254
AR v251,a69
v_sub_f32 v251, v251, v249
v_mul_f32 v251, v251, v250
AW a69,v251
AR v254,a70
v_sub_f32 v254, v254, v249
v_mul_f32 v254, v254, v250
AW a70,v254
AR v251,a71
v_sub_f32 v251, v251, v249
v_mul_f32 v251, v251, v250
AW a71,v251
AR v254,a96
v_sub_f32 v254, v254, v249
v_mul_f32 v254, v254, v250
AW a96,v254
AR v251,a97
v_sub_f32 v251, v251, v249
v_mul_f32 v251, v251, v250
AW a97,v251
AR v254,a98
v_sub_f32 v254, v254, v249
v_mul_f32 v254, v254, v250
AW a98,v254
AR v251,a99
v_sub_f32 v251, v251, v249
v_mul_f32 v251, v251, v250
AW a99,v251
AR v254,a100
v_sub_f32 v254, v254, v249
v_mul_f32 v254, v254, v250
AW a100,v254
AR v251,a101
v_sub_f32 v251, v251, v249
v_mul_f32 v251, v251, v250
AW a101,v251
AR v254,a102
v_sub_f32 v254, v254, v249
v_mul_f32 v254, v254, v250
AW a102,v254
AR v251,a103
v_sub_f32 v251, v251, v249
v_mul_f32 v251, v251, v250
AW a103,v251
v_max3_f32 v249, v72, v73, v74
v_max3_f32 v251, v104, v105, v106
v_max3_f32 v249, v249, v75, v76
v_max3_f32 v251, v251, v107, v108
v_max3_f32 v249, v249, v77, v78
v_max3_f32 v251, v251, v109, v110
v_max3_f32 v249, v249, v79, v111
v_max_f32 v249, v249, v251
v_mov_b32 v251, v249
s_nop 1
v_permlane16_swap_b32 v249, v251
v_max_f32 v249, v249, v251
v_mov_b32 v251, v249
s_nop 1
v_permlane32_swap_b32 v249, v251
v_max_f32 v249, v249, v251
v_log_f32 v250, v245
s_nop 0
v_max3_f32 v249, 0, v249, v250
v_sub_f32 v250, 0, v249
EX v250,v250
v_sub_f32 v132, v132, v249
v_sub_f32 v133, v133, v249
v_sub_f32 v134, v134, v249
v_sub_f32 v135, v135, v249
v_sub_f32 v8, v8, v249
v_sub_f32 v72, v72, v249
v_sub_f32 v9, v9, v249
v_sub_f32 v73, v73, v249
v_sub_f32 v10, v10, v249
v_sub_f32 v74, v74, v249
v_sub_f32 v11, v11, v249
v_sub_f32 v75, v75, v249
v_sub_f32 v12, v12, v249
v_sub_f32 v76, v76, v249
v_sub_f32 v13, v13, v249
v_sub_f32 v77, v77, v249
v_sub_f32 v14, v14, v249
v_sub_f32 v78, v78, v249
v_sub_f32 v15, v15, v249
v_sub_f32 v79, v79, v249
v_sub_f32 v40, v40, v249
v_sub_f32 v104, v104, v249
v_sub_f32 v41, v41, v249
v_sub_f32 v105, v105, v249
v_sub_f32 v42, v42, v249
v_sub_f32 v106, v106, v249
v_sub_f32 v43, v43, v249
v_sub_f32 v107, v107, v249
v_sub_f32 v44, v44, v249
v_sub_f32 v108, v108, v249
v_sub_f32 v45, v45, v249
v_sub_f32 v109, v109, v249
v_sub_f32 v46, v46, v249
v_sub_f32 v110, v110, v249
v_sub_f32 v47, v47, v249
v_sub_f32 v111, v111, v249
v_mul_f32 v148, v245, v250
v_mov_b32 v149, 0
v_bfe_u32 v249, v241, 8, 8
v_lshl_or_b32 v249, v249, 1, 1
v_lshlrev_b32 v249, 22, v249
v_and_b32 v241, 0xffff00ff, v241
AR v254,a8
v_sub_f32 v254, v254, v249
v_mul_f32 v254, v254, v250
AW a8,v254
AR v251,a9
v_sub_f32 v251, v251, v249
v_mul_f32 v251, v251, v250
AW a9,v251
AR v254,a10
v_sub_f32 v254, v254, v249
v_mul_f32 v254, v254, v250
AW a10,v254
AR v251,a11
v_sub_f32 v251, v251, v249
v_mul_f32 v251, v251, v250
AW a11,v251
AR v254,a12
v_sub_f32 v254, v254, v249
v_mul_f32 v254, v254, v250
AW a12,v254
AR v251,a13
v_sub_f32 v251, v251, v249
v_mul_f32 v251, v251, v250
AW a13,v251
AR v254,a14
v_sub_f32 v254, v254, v249
v_mul_f32 v254, v254, v250
AW a14,v254
AR v251,a15
v_sub_f32 v251, v251, v249
v_mul_f32 v251, v251, v250
AW a15,v251
AR v254,a40
v_sub_f32 v254, v254, v249
v_mul_f32 v254, v254, v250
AW a40,v254
AR v251,a41
v_sub_f32 v251, v251, v249
v_mul_f32 v251, v251, v250
AW a41,v251
AR v254,a42
v_sub_f32 v254, v254, v249
v_mul_f32 v254, v254, v250
AW a42,v254
AR v251,a43
v_sub_f32 v251, v251, v249
v_mul_f32 v251, v251, v250
AW a43,v251
AR v254,a44
v_sub_f32 v254, v254, v249
v_mul_f32 v254, v254, v250
AW a44,v254
AR v251,a45
v_sub_f32 v251, v251, v249
v_mul_f32 v251, v251, v250
AW a45,v251
AR v254,a46
v_sub_f32 v254, v254, v249
v_mul_f32 v254, v254, v250
AW a46,v254
AR v251,a47
v_sub_f32 v251, v251, v249
v_mul_f32 v251, v251, v250
AW a47,v251
AR v254,a72
v_sub_f32 v254, v254, v249
v_mul_f32 v254, v254, v250
AW a72,v254
AR v251,a73
v_sub_f32 v251, v251, v249
v_mul_f32 v251, v251, v250
AW a73,v251
AR v254,a74
v_sub_f32 v254, v254, v249
v_mul_f32 v254, v254, v250
AW a74,v254
AR v251,a75
v_sub_f32 v251, v251, v249
v_mul_f32 v251, v251, v250
AW a75,v251
AR v254,a76
v_sub_f32 v254, v254, v249
v_mul_f32 v254, v254, v250
AW a76,v254
AR v251,a77
v_sub_f32 v251, v251, v249
v_mul_f32 v251, v251, v250
AW a77,v251
AR v254,a78
v_sub_f32 v254, v254, v249
v_mul_f32 v254, v254, v250
AW a78,v254
AR v251,a79
v_sub_f32 v251, v251, v249
v_mul_f32 v251, v251, v250
AW a79,v251
AR v254,a104
v_sub_f32 v254, v254, v249
v_mul_f32 v254, v254, v250
AW a104,v254
AR v251,a105
v_sub_f32 v251, v251, v249
v_mul_f32 v251, v251, v250
AW a105,v251
AR v254,a106
v_sub_f32 v254, v254, v249
v_mul_f32 v254, v254, v250
AW a106,v254
AR v251,a107
v_sub_f32 v251, v251, v249
v_mul_f32 v251, v251, v250
AW a107,v251
AR v254,a108
v_sub_f32 v254, v254, v249
v_mul_f32 v254, v254, v250
AW a108,v254
AR v251,a109
v_sub_f32 v251, v251, v249
v_mul_f32 v251, v251, v250
AW a109,v251
)ASM"
R"ASM(
AR v254,a110
v_sub_f32 v254, v254, v249
v_mul_f32 v254, v254, v250
AW a110,v254
AR v251,a111
v_sub_f32 v251, v251, v249
v_mul_f32 v251, v251, v250
AW a111,v251
EX v64,v64
EX v65,v65
EX v66,v66
EX v67,v67
EX v68,v68
EX v69,v69
EX v70,v70
EX v71,v71
EX v72,v72
EX v73,v73
EX v74,v74
EX v75,v75
EX v76,v76
EX v77,v77
EX v78,v78
EX v79,v79
EX v96,v96
EX v97,v97
EX v98,v98
EX v99,v99
EX v100,v100
EX v101,v101
EX v102,v102
EX v103,v103
EX v104,v104
EX v105,v105
EX v106,v106
EX v107,v107
EX v108,v108
EX v109,v109
EX v110,v110
EX v111,v111
CV v64,v64,v65
CV v65,v66,v67
CV v66,v68,v69
CV v67,v70,v71
CV v72,v72,v73
CV v73,v74,v75
CV v74,v76,v77
CV v75,v78,v79
CV v68,v96,v97
CV v69,v98,v99
CV v70,v100,v101
CV v71,v102,v103
CV v76,v104,v105
CV v77,v106,v107
CV v78,v108,v109
CV v79,v110,v111
v_smfmac_f32_16x16x64_bf16 v[144:147], v[236:239], v[64:71], v234
s_nop 0
v_smfmac_f32_16x16x64_bf16 v[148:151], v[236:239], v[72:79], v234
s_nop 9
s_branch LRET_%=
LRARE104_%=:
s_nop 15
s_waitcnt vmcnt(0) lgkmcnt(0)
v_add_u32 v249, s74, v224
RD a[192:195],v249 offset:0
RD a[208:211],v249 offset:4096
RD a[224:227],v249 offset:8192
RD a[240:243],v249 offset:12288
v_add_u32 v250, s74, v225
RD a[196:199],v250 offset:0
RD a[212:215],v250 offset:4096
RD a[228:231],v250 offset:8192
RD a[244:247],v250 offset:12288
v_add_u32 v251, s74, v226
RD a[200:203],v251 offset:0
RD a[216:219],v251 offset:4096
RD a[232:235],v251 offset:8192
RD a[248:251],v251 offset:12288
v_add_u32 v254, s74, v227
RD a[204:207],v254 offset:0
RD a[220:223],v254 offset:4096
RD a[236:239],v254 offset:8192
RD a[252:255],v254 offset:12288
s_waitcnt lgkmcnt(0)
MM v[64:67],a[192:195],v[160:163],v[128:131]
MM v[64:67],a[196:199],v[176:179],v[64:67]
MM v[64:67],a[200:203],v[192:195],v[64:67]
MM v[64:67],a[204:207],v[208:211],v[64:67]
MM v[68:71],a[220:223],v[208:211],v[128:131]
MM v[68:71],a[216:219],v[192:195],v[68:71]
MM v[68:71],a[212:215],v[176:179],v[68:71]
MM v[68:71],a[208:211],v[160:163],v[68:71]
MM v[76:79],a[208:211],v[164:167],v[132:135]
MM v[76:79],a[212:215],v[180:183],v[76:79]
MM v[76:79],a[216:219],v[196:199],v[76:79]
MM v[76:79],a[220:223],v[212:215],v[76:79]
MM v[72:75],a[204:207],v[212:215],v[132:135]
MM v[72:75],a[200:203],v[196:199],v[72:75]
MM v[72:75],a[196:199],v[180:183],v[72:75]
MM v[72:75],a[192:195],v[164:167],v[72:75]
MM v[80:83],a[192:195],v[168:171],v[136:139]
MM v[80:83],a[196:199],v[184:187],v[80:83]
MM v[80:83],a[200:203],v[200:203],v[80:83]
MM v[80:83],a[204:207],v[216:219],v[80:83]
MM v[84:87],a[220:223],v[216:219],v[136:139]
MM v[84:87],a[216:219],v[200:203],v[84:87]
MM v[84:87],a[212:215],v[184:187],v[84:87]
MM v[84:87],a[208:211],v[168:171],v[84:87]
MM v[92:95],a[208:211],v[172:175],v[140:143]
MM v[92:95],a[212:215],v[188:191],v[92:95]
MM v[92:95],a[216:219],v[204:207],v[92:95]
MM v[92:95],a[220:223],v[220:223],v[92:95]
MM v[88:91],a[204:207],v[220:223],v[140:143]
MM v[88:91],a[200:203],v[204:207],v[88:91]
MM v[88:91],a[196:199],v[188:191],v[88:91]
MM v[88:91],a[192:195],v[172:175],v[88:91]
MM v[96:99],a[224:227],v[160:163],v[128:131]
MM v[96:99],a[228:231],v[176:179],v[96:99]
MM v[96:99],a[232:235],v[192:195],v[96:99]
MM v[96:99],a[236:239],v[208:211],v[96:99]
MM v[100:103],a[252:255],v[208:211],v[128:131]
MM v[100:103],a[248:251],v[192:195],v[100:103]
MM v[100:103],a[244:247],v[176:179],v[100:103]
MM v[100:103],a[240:243],v[160:163],v[100:103]
MM v[108:111],a[240:243],v[164:167],v[132:135]
MM v[108:111],a[244:247],v[180:183],v[108:111]
MM v[108:111],a[248:251],v[196:199],v[108:111]
MM v[108:111],a[252:255],v[212:215],v[108:111]
MM v[104:107],a[236:239],v[212:215],v[132:135]
MM v[104:107],a[232:235],v[196:199],v[104:107]
MM v[104:107],a[228:231],v[180:183],v[104:107]
MM v[104:107],a[224:227],v[164:167],v[104:107]
MM v[112:115],a[224:227],v[168:171],v[136:139]
MM v[112:115],a[228:231],v[184:187],v[112:115]
MM v[112:115],a[232:235],v[200:203],v[112:115]
MM v[112:115],a[236:239],v[216:219],v[112:115]
MM v[116:119],a[252:255],v[216:219],v[136:139]
MM v[116:119],a[248:251],v[200:203],v[116:119]
MM v[116:119],a[244:247],v[184:187],v[116:119]
MM v[116:119],a[240:243],v[168:171],v[116:119]
MM v[124:127],a[240:243],v[172:175],v[140:143]
MM v[124:127],a[244:247],v[188:191],v[124:127]
MM v[124:127],a[248:251],v[204:207],v[124:127]
MM v[124:127],a[252:255],v[220:223],v[124:127]
MM v[120:123],a[236:239],v[220:223],v[140:143]
MM v[120:123],a[232:235],v[204:207],v[120:123]
MM v[120:123],a[228:231],v[188:191],v[120:123]
MM v[120:123],a[224:227],v[172:175],v[120:123]
s_nop 15
v_add_u32 v249, s75, v224
RD a[192:195],v249 offset:0
RD a[208:211],v249 offset:4096
RD a[224:227],v249 offset:8192
RD a[240:243],v249 offset:12288
v_add_u32 v250, s75, v225
RD a[196:199],v250 offset:0
RD a[212:215],v250 offset:4096
RD a[228:231],v250 offset:8192
RD a[244:247],v250 offset:12288
v_add_u32 v251, s75, v226
RD a[200:203],v251 offset:0
RD a[216:219],v251 offset:4096
RD a[232:235],v251 offset:8192
RD a[248:251],v251 offset:12288
v_add_u32 v254, s75, v227
RD a[204:207],v254 offset:0
RD a[220:223],v254 offset:4096
RD a[236:239],v254 offset:8192
RD a[252:255],v254 offset:12288
s_waitcnt lgkmcnt(0)
v_max3_f32 v249, v64, v65, v66
v_max3_f32 v251, v96, v97, v98
v_max3_f32 v249, v249, v67, v68
v_max3_f32 v251, v251, v99, v100
v_max3_f32 v249, v249, v69, v70
v_max3_f32 v251, v251, v101, v102
v_max3_f32 v249, v249, v71, v103
v_max_f32 v249, v249, v251
v_mov_b32 v251, v249
s_nop 1
v_permlane16_swap_b32 v249, v251
v_max_f32 v249, v249, v251
v_mov_b32 v251, v249
s_nop 1
v_permlane32_swap_b32 v249, v251
v_max_f32 v249, v249, v251
v_log_f32 v250, v244
s_nop 0
v_max3_f32 v249, 0, v249, v250
v_sub_f32 v250, 0, v249
EX v250,v250
v_sub_f32 v128, v128, v249
v_sub_f32 v129, v129, v249
v_sub_f32 v130, v130, v249
v_sub_f32 v131, v131, v249
v_sub_f32 v0, v0, v249
v_sub_f32 v64, v64, v249
v_sub_f32 v1, v1, v249
v_sub_f32 v65, v65, v249
v_sub_f32 v2, v2, v249
v_sub_f32 v66, v66, v249
v_sub_f32 v3, v3, v249
v_sub_f32 v67, v67, v249
v_sub_f32 v4, v4, v249
v_sub_f32 v68, v68, v249
v_sub_f32 v5, v5, v249
v_sub_f32 v69, v69, v249
v_sub_f32 v6, v6, v249
v_sub_f32 v70, v70, v249
v_sub_f32 v7, v7, v249
v_sub_f32 v71, v71, v249
v_sub_f32 v32, v32, v249
v_sub_f32 v96, v96, v249
v_sub_f32 v33, v33, v249
v_sub_f32 v97, v97, v249
v_sub_f32 v34, v34, v249
v_sub_f32 v98, v98, v249
v_sub_f32 v35, v35, v249
v_sub_f32 v99, v99, v249
v_sub_f32 v36, v36, v249
v_sub_f32 v100, v100, v249
v_sub_f32 v37, v37, v249
v_sub_f32 v101, v101, v249
v_sub_f32 v38, v38, v249
v_sub_f32 v102, v102, v249
v_sub_f32 v39, v39, v249
v_sub_f32 v103, v103, v249
v_mul_f32 v144, v244, v250
v_mov_b32 v145, 0
v_bfe_u32 v249, v241, 0, 8
v_lshl_or_b32 v249, v249, 1, 1
v_lshlrev_b32 v249, 22, v249
v_and_b32 v241, 0xffffff00, v241
AR v254,a0
v_sub_f32 v254, v254, v249
v_mul_f32 v254, v254, v250
AW a0,v254
AR v251,a1
v_sub_f32 v251, v251, v249
v_mul_f32 v251, v251, v250
AW a1,v251
AR v254,a2
v_sub_f32 v254, v254, v249
v_mul_f32 v254, v254, v250
AW a2,v254
AR v251,a3
v_sub_f32 v251, v251, v249
v_mul_f32 v251, v251, v250
AW a3,v251
AR v254,a4
v_sub_f32 v254, v254, v249
v_mul_f32 v254, v254, v250
AW a4,v254
AR v251,a5
v_sub_f32 v251, v251, v249
v_mul_f32 v251, v251, v250
AW a5,v251
AR v254,a6
v_sub_f32 v254, v254, v249
v_mul_f32 v254, v254, v250
AW a6,v254
AR v251,a7
v_sub_f32 v251, v251, v249
v_mul_f32 v251, v251, v250
AW a7,v251
AR v254,a32
v_sub_f32 v254, v254, v249
v_mul_f32 v254, v254, v250
AW a32,v254
AR v251,a33
v_sub_f32 v251, v251, v249
v_mul_f32 v251, v251, v250
AW a33,v251
AR v254,a34
v_sub_f32 v254, v254, v249
v_mul_f32 v254, v254, v250
AW a34,v254
AR v251,a35
v_sub_f32 v251, v251, v249
v_mul_f32 v251, v251, v250
AW a35,v251
AR v254,a36
v_sub_f32 v254, v254, v249
v_mul_f32 v254, v254, v250
AW a36,v254
AR v251,a37
v_sub_f32 v251, v251, v249
v_mul_f32 v251, v251, v250
AW a37,v251
AR v254,a38
v_sub_f32 v254, v254, v249
v_mul_f32 v254, v254, v250
AW a38,v254
AR v251,a39
v_sub_f32 v251, v251, v249
v_mul_f32 v251, v251, v250
AW a39,v251
AR v254,a64
v_sub_f32 v254, v254, v249
v_mul_f32 v254, v254, v250
AW a64,v254
AR v251,a65
v_sub_f32 v251, v251, v249
v_mul_f32 v251, v251, v250
AW a65,v251
AR v254,a66
v_sub_f32 v254, v254, v249
v_mul_f32 v254, v254, v250
AW a66,v254
AR v251,a67
v_sub_f32 v251, v251, v249
v_mul_f32 v251, v251, v250
AW a67,v251
AR v254,a68
v_sub_f32 v254, v254, v249
v_mul_f32 v254, v254, v250
AW a68,v254
AR v251,a69
v_sub_f32 v251, v251, v249
v_mul_f32 v251, v251, v250
AW a69,v251
AR v254,a70
v_sub_f32 v254, v254, v249
v_mul_f32 v254, v254, v250
AW a70,v254
AR v251,a71
v_sub_f32 v251, v251, v249
v_mul_f32 v251, v251, v250
AW a71,v251
AR v254,a96
v_sub_f32 v254, v254, v249
v_mul_f32 v254, v254, v250
AW a96,v254
AR v251,a97
v_sub_f32 v251, v251, v249
v_mul_f32 v251, v251, v250
AW a97,v251
AR v254,a98
v_sub_f32 v254, v254, v249
v_mul_f32 v254, v254, v250
AW a98,v254
AR v251,a99
v_sub_f32 v251, v251, v249
v_mul_f32 v251, v251, v250
AW a99,v251
AR v254,a100
v_sub_f32 v254, v254, v249
v_mul_f32 v254, v254, v250
AW a100,v254
AR v251,a101
v_sub_f32 v251, v251, v249
v_mul_f32 v251, v251, v250
AW a101,v251
AR v254,a102
v_sub_f32 v254, v254, v249
v_mul_f32 v254, v254, v250
AW a102,v254
AR v251,a103
v_sub_f32 v251, v251, v249
v_mul_f32 v251, v251, v250
AW a103,v251
v_max3_f32 v249, v72, v73, v74
v_max3_f32 v251, v104, v105, v106
v_max3_f32 v249, v249, v75, v76
v_max3_f32 v251, v251, v107, v108
v_max3_f32 v249, v249, v77, v78
v_max3_f32 v251, v251, v109, v110
v_max3_f32 v249, v249, v79, v111
v_max_f32 v249, v249, v251
v_mov_b32 v251, v249
s_nop 1
v_permlane16_swap_b32 v249, v251
v_max_f32 v249, v249, v251
v_mov_b32 v251, v249
s_nop 1
v_permlane32_swap_b32 v249, v251
v_max_f32 v249, v249, v251
v_log_f32 v250, v245
s_nop 0
v_max3_f32 v249, 0, v249, v250
v_sub_f32 v250, 0, v249
EX v250,v250
v_sub_f32 v132, v132, v249
v_sub_f32 v133, v133, v249
v_sub_f32 v134, v134, v249
v_sub_f32 v135, v135, v249
v_sub_f32 v8, v8, v249
v_sub_f32 v72, v72, v249
v_sub_f32 v9, v9, v249
v_sub_f32 v73, v73, v249
v_sub_f32 v10, v10, v249
v_sub_f32 v74, v74, v249
v_sub_f32 v11, v11, v249
v_sub_f32 v75, v75, v249
v_sub_f32 v12, v12, v249
v_sub_f32 v76, v76, v249
v_sub_f32 v13, v13, v249
v_sub_f32 v77, v77, v249
v_sub_f32 v14, v14, v249
v_sub_f32 v78, v78, v249
v_sub_f32 v15, v15, v249
v_sub_f32 v79, v79, v249
v_sub_f32 v40, v40, v249
v_sub_f32 v104, v104, v249
v_sub_f32 v41, v41, v249
v_sub_f32 v105, v105, v249
v_sub_f32 v42, v42, v249
v_sub_f32 v106, v106, v249
v_sub_f32 v43, v43, v249
v_sub_f32 v107, v107, v249
v_sub_f32 v44, v44, v249
v_sub_f32 v108, v108, v249
v_sub_f32 v45, v45, v249
v_sub_f32 v109, v109, v249
v_sub_f32 v46, v46, v249
v_sub_f32 v110, v110, v249
v_sub_f32 v47, v47, v249
v_sub_f32 v111, v111, v249
v_mul_f32 v148, v245, v250
v_mov_b32 v149, 0
v_bfe_u32 v249, v241, 8, 8
v_lshl_or_b32 v249, v249, 1, 1
v_lshlrev_b32 v249, 22, v249
v_and_b32 v241, 0xffff00ff, v241
AR v254,a8
v_sub_f32 v254, v254, v249
v_mul_f32 v254, v254, v250
AW a8,v254
AR v251,a9
v_sub_f32 v251, v251, v249
v_mul_f32 v251, v251, v250
AW a9,v251
AR v254,a10
v_sub_f32 v254, v254, v249
v_mul_f32 v254, v254, v250
AW a10,v254
AR v251,a11
v_sub_f32 v251, v251, v249
v_mul_f32 v251, v251, v250
AW a11,v251
AR v254,a12
v_sub_f32 v254, v254, v249
v_mul_f32 v254, v254, v250
AW a12,v254
AR v251,a13
v_sub_f32 v251, v251, v249
v_mul_f32 v251, v251, v250
AW a13,v251
AR v254,a14
v_sub_f32 v254, v254, v249
v_mul_f32 v254, v254, v250
AW a14,v254
AR v251,a15
v_sub_f32 v251, v251, v249
v_mul_f32 v251, v251, v250
AW a15,v251
AR v254,a40
v_sub_f32 v254, v254, v249
v_mul_f32 v254, v254, v250
AW a40,v254
AR v251,a41
v_sub_f32 v251, v251, v249
v_mul_f32 v251, v251, v250
AW a41,v251
AR v254,a42
v_sub_f32 v254, v254, v249
v_mul_f32 v254, v254, v250
AW a42,v254
AR v251,a43
v_sub_f32 v251, v251, v249
v_mul_f32 v251, v251, v250
AW a43,v251
AR v254,a44
v_sub_f32 v254, v254, v249
v_mul_f32 v254, v254, v250
AW a44,v254
AR v251,a45
v_sub_f32 v251, v251, v249
v_mul_f32 v251, v251, v250
AW a45,v251
AR v254,a46
v_sub_f32 v254, v254, v249
v_mul_f32 v254, v254, v250
AW a46,v254
AR v251,a47
v_sub_f32 v251, v251, v249
)ASM"
R"ASM(
v_mul_f32 v251, v251, v250
AW a47,v251
AR v254,a72
v_sub_f32 v254, v254, v249
v_mul_f32 v254, v254, v250
AW a72,v254
AR v251,a73
v_sub_f32 v251, v251, v249
v_mul_f32 v251, v251, v250
AW a73,v251
AR v254,a74
v_sub_f32 v254, v254, v249
v_mul_f32 v254, v254, v250
AW a74,v254
AR v251,a75
v_sub_f32 v251, v251, v249
v_mul_f32 v251, v251, v250
AW a75,v251
AR v254,a76
v_sub_f32 v254, v254, v249
v_mul_f32 v254, v254, v250
AW a76,v254
AR v251,a77
v_sub_f32 v251, v251, v249
v_mul_f32 v251, v251, v250
AW a77,v251
AR v254,a78
v_sub_f32 v254, v254, v249
v_mul_f32 v254, v254, v250
AW a78,v254
AR v251,a79
v_sub_f32 v251, v251, v249
v_mul_f32 v251, v251, v250
AW a79,v251
AR v254,a104
v_sub_f32 v254, v254, v249
v_mul_f32 v254, v254, v250
AW a104,v254
AR v251,a105
v_sub_f32 v251, v251, v249
v_mul_f32 v251, v251, v250
AW a105,v251
AR v254,a106
v_sub_f32 v254, v254, v249
v_mul_f32 v254, v254, v250
AW a106,v254
AR v251,a107
v_sub_f32 v251, v251, v249
v_mul_f32 v251, v251, v250
AW a107,v251
AR v254,a108
v_sub_f32 v254, v254, v249
v_mul_f32 v254, v254, v250
AW a108,v254
AR v251,a109
v_sub_f32 v251, v251, v249
v_mul_f32 v251, v251, v250
AW a109,v251
AR v254,a110
v_sub_f32 v254, v254, v249
v_mul_f32 v254, v254, v250
AW a110,v254
AR v251,a111
v_sub_f32 v251, v251, v249
v_mul_f32 v251, v251, v250
AW a111,v251
v_max3_f32 v249, v80, v81, v82
v_max3_f32 v251, v112, v113, v114
v_max3_f32 v249, v249, v83, v84
v_max3_f32 v251, v251, v115, v116
v_max3_f32 v249, v249, v85, v86
v_max3_f32 v251, v251, v117, v118
v_max3_f32 v249, v249, v87, v119
v_max_f32 v249, v249, v251
v_mov_b32 v251, v249
s_nop 1
v_permlane16_swap_b32 v249, v251
v_max_f32 v249, v249, v251
v_mov_b32 v251, v249
s_nop 1
v_permlane32_swap_b32 v249, v251
v_max_f32 v249, v249, v251
v_log_f32 v250, v246
s_nop 0
v_max3_f32 v249, 0, v249, v250
v_sub_f32 v250, 0, v249
EX v250,v250
v_sub_f32 v136, v136, v249
v_sub_f32 v137, v137, v249
v_sub_f32 v138, v138, v249
v_sub_f32 v139, v139, v249
v_sub_f32 v16, v16, v249
v_sub_f32 v80, v80, v249
v_sub_f32 v17, v17, v249
v_sub_f32 v81, v81, v249
v_sub_f32 v18, v18, v249
v_sub_f32 v82, v82, v249
v_sub_f32 v19, v19, v249
v_sub_f32 v83, v83, v249
v_sub_f32 v20, v20, v249
v_sub_f32 v84, v84, v249
v_sub_f32 v21, v21, v249
v_sub_f32 v85, v85, v249
v_sub_f32 v22, v22, v249
v_sub_f32 v86, v86, v249
v_sub_f32 v23, v23, v249
v_sub_f32 v87, v87, v249
v_sub_f32 v48, v48, v249
v_sub_f32 v112, v112, v249
v_sub_f32 v49, v49, v249
v_sub_f32 v113, v113, v249
v_sub_f32 v50, v50, v249
v_sub_f32 v114, v114, v249
v_sub_f32 v51, v51, v249
v_sub_f32 v115, v115, v249
v_sub_f32 v52, v52, v249
v_sub_f32 v116, v116, v249
v_sub_f32 v53, v53, v249
v_sub_f32 v117, v117, v249
v_sub_f32 v54, v54, v249
v_sub_f32 v118, v118, v249
v_sub_f32 v55, v55, v249
v_sub_f32 v119, v119, v249
v_mul_f32 v152, v246, v250
v_mov_b32 v153, 0
v_bfe_u32 v249, v241, 16, 8
v_lshl_or_b32 v249, v249, 1, 1
v_lshlrev_b32 v249, 22, v249
v_and_b32 v241, 0xff00ffff, v241
AR v254,a16
v_sub_f32 v254, v254, v249
v_mul_f32 v254, v254, v250
AW a16,v254
AR v251,a17
v_sub_f32 v251, v251, v249
v_mul_f32 v251, v251, v250
AW a17,v251
AR v254,a18
v_sub_f32 v254, v254, v249
v_mul_f32 v254, v254, v250
AW a18,v254
AR v251,a19
v_sub_f32 v251, v251, v249
v_mul_f32 v251, v251, v250
AW a19,v251
AR v254,a20
v_sub_f32 v254, v254, v249
v_mul_f32 v254, v254, v250
AW a20,v254
AR v251,a21
v_sub_f32 v251, v251, v249
v_mul_f32 v251, v251, v250
AW a21,v251
AR v254,a22
v_sub_f32 v254, v254, v249
v_mul_f32 v254, v254, v250
AW a22,v254
AR v251,a23
v_sub_f32 v251, v251, v249
v_mul_f32 v251, v251, v250
AW a23,v251
AR v254,a48
v_sub_f32 v254, v254, v249
v_mul_f32 v254, v254, v250
AW a48,v254
AR v251,a49
v_sub_f32 v251, v251, v249
v_mul_f32 v251, v251, v250
AW a49,v251
AR v254,a50
v_sub_f32 v254, v254, v249
v_mul_f32 v254, v254, v250
AW a50,v254
AR v251,a51
v_sub_f32 v251, v251, v249
v_mul_f32 v251, v251, v250
AW a51,v251
AR v254,a52
v_sub_f32 v254, v254, v249
v_mul_f32 v254, v254, v250
AW a52,v254
AR v251,a53
v_sub_f32 v251, v251, v249
v_mul_f32 v251, v251, v250
AW a53,v251
AR v254,a54
v_sub_f32 v254, v254, v249
v_mul_f32 v254, v254, v250
AW a54,v254
AR v251,a55
v_sub_f32 v251, v251, v249
v_mul_f32 v251, v251, v250
AW a55,v251
AR v254,a80
v_sub_f32 v254, v254, v249
v_mul_f32 v254, v254, v250
AW a80,v254
AR v251,a81
v_sub_f32 v251, v251, v249
v_mul_f32 v251, v251, v250
AW a81,v251
AR v254,a82
v_sub_f32 v254, v254, v249
v_mul_f32 v254, v254, v250
AW a82,v254
AR v251,a83
v_sub_f32 v251, v251, v249
v_mul_f32 v251, v251, v250
AW a83,v251
AR v254,a84
v_sub_f32 v254, v254, v249
v_mul_f32 v254, v254, v250
AW a84,v254
AR v251,a85
v_sub_f32 v251, v251, v249
v_mul_f32 v251, v251, v250
AW a85,v251
AR v254,a86
v_sub_f32 v254, v254, v249
v_mul_f32 v254, v254, v250
AW a86,v254
AR v251,a87
v_sub_f32 v251, v251, v249
v_mul_f32 v251, v251, v250
AW a87,v251
AR v254,a112
v_sub_f32 v254, v254, v249
v_mul_f32 v254, v254, v250
AW a112,v254
AR v251,a113
v_sub_f32 v251, v251, v249
v_mul_f32 v251, v251, v250
AW a113,v251
AR v254,a114
v_sub_f32 v254, v254, v249
v_mul_f32 v254, v254, v250
AW a114,v254
AR v251,a115
v_sub_f32 v251, v251, v249
v_mul_f32 v251, v251, v250
AW a115,v251
AR v254,a116
v_sub_f32 v254, v254, v249
v_mul_f32 v254, v254, v250
AW a116,v254
AR v251,a117
v_sub_f32 v251, v251, v249
v_mul_f32 v251, v251, v250
AW a117,v251
AR v254,a118
v_sub_f32 v254, v254, v249
v_mul_f32 v254, v254, v250
AW a118,v254
AR v251,a119
v_sub_f32 v251, v251, v249
v_mul_f32 v251, v251, v250
AW a119,v251
v_max3_f32 v249, v88, v89, v90
v_max3_f32 v251, v120, v121, v122
v_max3_f32 v249, v249, v91, v92
v_max3_f32 v251, v251, v123, v124
v_max3_f32 v249, v249, v93, v94
v_max3_f32 v251, v251, v125, v126
v_max3_f32 v249, v249, v95, v127
v_max_f32 v249, v249, v251
v_mov_b32 v251, v249
s_nop 1
v_permlane16_swap_b32 v249, v251
v_max_f32 v249, v249, v251
v_mov_b32 v251, v249
s_nop 1
v_permlane32_swap_b32 v249, v251
v_max_f32 v249, v249, v251
v_log_f32 v250, v247
s_nop 0
v_max3_f32 v249, 0, v249, v250
v_sub_f32 v250, 0, v249
EX v250,v250
v_sub_f32 v140, v140, v249
v_sub_f32 v141, v141, v249
v_sub_f32 v142, v142, v249
v_sub_f32 v143, v143, v249
v_sub_f32 v24, v24, v249
v_sub_f32 v88, v88, v249
v_sub_f32 v25, v25, v249
v_sub_f32 v89, v89, v249
v_sub_f32 v26, v26, v249
v_sub_f32 v90, v90, v249
v_sub_f32 v27, v27, v249
v_sub_f32 v91, v91, v249
v_sub_f32 v28, v28, v249
v_sub_f32 v92, v92, v249
v_sub_f32 v29, v29, v249
v_sub_f32 v93, v93, v249
v_sub_f32 v30, v30, v249
v_sub_f32 v94, v94, v249
v_sub_f32 v31, v31, v249
v_sub_f32 v95, v95, v249
v_sub_f32 v56, v56, v249
v_sub_f32 v120, v120, v249
v_sub_f32 v57, v57, v249
v_sub_f32 v121, v121, v249
v_sub_f32 v58, v58, v249
v_sub_f32 v122, v122, v249
v_sub_f32 v59, v59, v249
v_sub_f32 v123, v123, v249
v_sub_f32 v60, v60, v249
v_sub_f32 v124, v124, v249
v_sub_f32 v61, v61, v249
v_sub_f32 v125, v125, v249
v_sub_f32 v62, v62, v249
v_sub_f32 v126, v126, v249
v_sub_f32 v63, v63, v249
v_sub_f32 v127, v127, v249
v_mul_f32 v156, v247, v250
v_mov_b32 v157, 0
v_bfe_u32 v249, v241, 24, 8
v_lshl_or_b32 v249, v249, 1, 1
v_lshlrev_b32 v249, 22, v249
v_and_b32 v241, 0xffffff, v241
AR v254,a24
v_sub_f32 v254, v254, v249
v_mul_f32 v254, v254, v250
AW a24,v254
AR v251,a25
v_sub_f32 v251, v251, v249
v_mul_f32 v251, v251, v250
AW a25,v251
AR v254,a26
v_sub_f32 v254, v254, v249
v_mul_f32 v254, v254, v250
AW a26,v254
AR v251,a27
v_sub_f32 v251, v251, v249
v_mul_f32 v251, v251, v250
AW a27,v251
AR v254,a28
v_sub_f32 v254, v254, v249
v_mul_f32 v254, v254, v250
AW a28,v254
AR v251,a29
v_sub_f32 v251, v251, v249
v_mul_f32 v251, v251, v250
AW a29,v251
AR v254,a30
v_sub_f32 v254, v254, v249
v_mul_f32 v254, v254, v250
AW a30,v254
AR v251,a31
v_sub_f32 v251, v251, v249
v_mul_f32 v251, v251, v250
AW a31,v251
AR v254,a56
v_sub_f32 v254, v254, v249
v_mul_f32 v254, v254, v250
AW a56,v254
AR v251,a57
v_sub_f32 v251, v251, v249
v_mul_f32 v251, v251, v250
AW a57,v251
AR v254,a58
v_sub_f32 v254, v254, v249
v_mul_f32 v254, v254, v250
AW a58,v254
AR v251,a59
v_sub_f32 v251, v251, v249
v_mul_f32 v251, v251, v250
AW a59,v251
AR v254,a60
v_sub_f32 v254, v254, v249
v_mul_f32 v254, v254, v250
AW a60,v254
AR v251,a61
v_sub_f32 v251, v251, v249
v_mul_f32 v251, v251, v250
AW a61,v251
AR v254,a62
v_sub_f32 v254, v254, v249
v_mul_f32 v254, v254, v250
AW a62,v254
AR v251,a63
v_sub_f32 v251, v251, v249
v_mul_f32 v251, v251, v250
AW a63,v251
AR v254,a88
v_sub_f32 v254, v254, v249
v_mul_f32 v254, v254, v250
AW a88,v254
AR v251,a89
v_sub_f32 v251, v251, v249
v_mul_f32 v251, v251, v250
AW a89,v251
AR v254,a90
v_sub_f32 v254, v254, v249
v_mul_f32 v254, v254, v250
AW a90,v254
AR v251,a91
v_sub_f32 v251, v251, v249
v_mul_f32 v251, v251, v250
AW a91,v251
AR v254,a92
v_sub_f32 v254, v254, v249
v_mul_f32 v254, v254, v250
AW a92,v254
AR v251,a93
v_sub_f32 v251, v251, v249
v_mul_f32 v251, v251, v250
AW a93,v251
AR v254,a94
v_sub_f32 v254, v254, v249
v_mul_f32 v254, v254, v250
AW a94,v254
AR v251,a95
v_sub_f32 v251, v251, v249
v_mul_f32 v251, v251, v250
AW a95,v251
AR v254,a120
v_sub_f32 v254, v254, v249
v_mul_f32 v254, v254, v250
AW a120,v254
AR v251,a121
v_sub_f32 v251, v251, v249
v_mul_f32 v251, v251, v250
AW a121,v251
AR v254,a122
v_sub_f32 v254, v254, v249
v_mul_f32 v254, v254, v250
AW a122,v254
AR v251,a123
v_sub_f32 v251, v251, v249
v_mul_f32 v251, v251, v250
AW a123,v251
AR v254,a124
v_sub_f32 v254, v254, v249
v_mul_f32 v254, v254, v250
AW a124,v254
AR v251,a125
v_sub_f32 v251, v251, v249
v_mul_f32 v251, v251, v250
AW a125,v251
AR v254,a126
v_sub_f32 v254, v254, v249
v_mul_f32 v254, v254, v250
AW a126,v254
AR v251,a127
v_sub_f32 v251, v251, v249
v_mul_f32 v251, v251, v250
AW a127,v251
EX v64,v64
EX v65,v65
EX v66,v66
EX v67,v67
EX v68,v68
EX v69,v69
EX v70,v70
EX v71,v71
EX v72,v72
EX v73,v73
EX v74,v74
EX v75,v75
EX v76,v76
EX v77,v77
EX v78,v78
EX v79,v79
EX v80,v80
EX v81,v81
EX v82,v82
EX v83,v83
EX v84,v84
EX v85,v85
EX v86,v86
EX v87,v87
EX v88,v88
EX v89,v89
EX v90,v90
EX v91,v91
EX v92,v92
EX v93,v93
EX v94,v94
EX v95,v95
EX v96,v96
EX v97,v97
EX v98,v98
EX v99,v99
EX v100,v100
EX v101,v101
EX v102,v102
EX v103,v103
EX v104,v104
EX v105,v105
EX v106,v106
EX v107,v107
EX v108,v108
EX v109,v109
EX v110,v110
EX v111,v111
EX v112,v112
EX v113,v113
EX v114,v114
EX v115,v115
EX v116,v116
EX v117,v117
EX v118,v118
EX v119,v119
EX v120,v120
EX v121,v121
EX v122,v122
EX v123,v123
EX v124,v124
EX v125,v125
EX v126,v126
EX v127,v127
CV v64,v64,v65
CV v65,v66,v67
CV v66,v68,v69
CV v67,v70,v71
CV v72,v72,v73
CV v73,v74,v75
CV v74,v76,v77
CV v75,v78,v79
CV v80,v80,v81
CV v81,v82,v83
CV v82,v84,v85
CV v83,v86,v87
CV v88,v88,v89
CV v89,v90,v91
CV v90,v92,v93
CV v91,v94,v95
CV v68,v96,v97
CV v69,v98,v99
CV v70,v100,v101
CV v71,v102,v103
CV v76,v104,v105
CV v77,v106,v107
CV v78,v108,v109
CV v79,v110,v111
CV v84,v112,v113
CV v85,v114,v115
CV v86,v116,v117
CV v87,v118,v119
CV v92,v120,v121
CV v93,v122,v123
CV v94,v124,v125
CV v95,v126,v127
v_smfmac_f32_16x16x64_bf16 v[144:147], v[236:239], v[64:71], v234
v_smfmac_f32_16x16x64_bf16 v[148:151], v[236:239], v[72:79], v234
v_smfmac_f32_16x16x64_bf16 v[152:155], v[236:239], v[80:87], v234
v_smfmac_f32_16x16x64_bf16 v[156:159], v[236:239], v[88:95], v234
s_nop 9
s_branch LRET_%=
LRARE004_%=:
s_nop 15
s_waitcnt vmcnt(0) lgkmcnt(0)
v_add_u32 v249, s74, v224
RD a[192:195],v249 offset:0
RD a[208:211],v249 offset:4096
RD a[224:227],v249 offset:8192
RD a[240:243],v249 offset:12288
v_add_u32 v250, s74, v225
RD a[196:199],v250 offset:0
RD a[212:215],v250 offset:4096
RD a[228:231],v250 offset:8192
RD a[244:247],v250 offset:12288
v_add_u32 v251, s74, v226
RD a[200:203],v251 offset:0
RD a[216:219],v251 offset:4096
RD a[232:235],v251 offset:8192
RD a[248:251],v251 offset:12288
v_add_u32 v254, s74, v227
RD a[204:207],v254 offset:0
RD a[220:223],v254 offset:4096
RD a[236:239],v254 offset:8192
RD a[252:255],v254 offset:12288
s_waitcnt lgkmcnt(0)
MM v[0:3],a[192:195],v[160:163],v[128:131]
MM v[0:3],a[196:199],v[176:179],v[0:3]
MM v[0:3],a[200:203],v[192:195],v[0:3]
MM v[0:3],a[204:207],v[208:211],v[0:3]
MM v[4:7],a[220:223],v[208:211],v[128:131]
MM v[4:7],a[216:219],v[192:195],v[4:7]
)ASM"
R"ASM(
MM v[4:7],a[212:215],v[176:179],v[4:7]
MM v[4:7],a[208:211],v[160:163],v[4:7]
MM v[12:15],a[208:211],v[164:167],v[132:135]
MM v[12:15],a[212:215],v[180:183],v[12:15]
MM v[12:15],a[216:219],v[196:199],v[12:15]
MM v[12:15],a[220:223],v[212:215],v[12:15]
MM v[8:11],a[204:207],v[212:215],v[132:135]
MM v[8:11],a[200:203],v[196:199],v[8:11]
MM v[8:11],a[196:199],v[180:183],v[8:11]
MM v[8:11],a[192:195],v[164:167],v[8:11]
MM v[16:19],a[192:195],v[168:171],v[136:139]
MM v[16:19],a[196:199],v[184:187],v[16:19]
MM v[16:19],a[200:203],v[200:203],v[16:19]
MM v[16:19],a[204:207],v[216:219],v[16:19]
MM v[20:23],a[220:223],v[216:219],v[136:139]
MM v[20:23],a[216:219],v[200:203],v[20:23]
MM v[20:23],a[212:215],v[184:187],v[20:23]
MM v[20:23],a[208:211],v[168:171],v[20:23]
MM v[28:31],a[208:211],v[172:175],v[140:143]
MM v[28:31],a[212:215],v[188:191],v[28:31]
MM v[28:31],a[216:219],v[204:207],v[28:31]
MM v[28:31],a[220:223],v[220:223],v[28:31]
MM v[24:27],a[204:207],v[220:223],v[140:143]
MM v[24:27],a[200:203],v[204:207],v[24:27]
MM v[24:27],a[196:199],v[188:191],v[24:27]
MM v[24:27],a[192:195],v[172:175],v[24:27]
MM v[32:35],a[224:227],v[160:163],v[128:131]
MM v[32:35],a[228:231],v[176:179],v[32:35]
MM v[32:35],a[232:235],v[192:195],v[32:35]
MM v[32:35],a[236:239],v[208:211],v[32:35]
MM v[36:39],a[252:255],v[208:211],v[128:131]
MM v[36:39],a[248:251],v[192:195],v[36:39]
MM v[36:39],a[244:247],v[176:179],v[36:39]
MM v[36:39],a[240:243],v[160:163],v[36:39]
MM v[44:47],a[240:243],v[164:167],v[132:135]
MM v[44:47],a[244:247],v[180:183],v[44:47]
MM v[44:47],a[248:251],v[196:199],v[44:47]
MM v[44:47],a[252:255],v[212:215],v[44:47]
MM v[40:43],a[236:239],v[212:215],v[132:135]
MM v[40:43],a[232:235],v[196:199],v[40:43]
MM v[40:43],a[228:231],v[180:183],v[40:43]
MM v[40:43],a[224:227],v[164:167],v[40:43]
MM v[48:51],a[224:227],v[168:171],v[136:139]
MM v[48:51],a[228:231],v[184:187],v[48:51]
MM v[48:51],a[232:235],v[200:203],v[48:51]
MM v[48:51],a[236:239],v[216:219],v[48:51]
MM v[52:55],a[252:255],v[216:219],v[136:139]
MM v[52:55],a[248:251],v[200:203],v[52:55]
MM v[52:55],a[244:247],v[184:187],v[52:55]
MM v[52:55],a[240:243],v[168:171],v[52:55]
MM v[60:63],a[240:243],v[172:175],v[140:143]
MM v[60:63],a[244:247],v[188:191],v[60:63]
MM v[60:63],a[248:251],v[204:207],v[60:63]
MM v[60:63],a[252:255],v[220:223],v[60:63]
MM v[56:59],a[236:239],v[220:223],v[140:143]
MM v[56:59],a[232:235],v[204:207],v[56:59]
MM v[56:59],a[228:231],v[188:191],v[56:59]
MM v[56:59],a[224:227],v[172:175],v[56:59]
s_nop 15
v_add_u32 v249, s75, v224
RD a[192:195],v249 offset:0
RD a[208:211],v249 offset:4096
RD a[224:227],v249 offset:8192
RD a[240:243],v249 offset:12288
v_add_u32 v250, s75, v225
RD a[196:199],v250 offset:0
RD a[212:215],v250 offset:4096
RD a[228:231],v250 offset:8192
RD a[244:247],v250 offset:12288
v_add_u32 v251, s75, v226
RD a[200:203],v251 offset:0
RD a[216:219],v251 offset:4096
RD a[232:235],v251 offset:8192
RD a[248:251],v251 offset:12288
v_add_u32 v254, s75, v227
RD a[204:207],v254 offset:0
RD a[220:223],v254 offset:4096
RD a[236:239],v254 offset:8192
RD a[252:255],v254 offset:12288
s_waitcnt lgkmcnt(0)
v_max3_f32 v249, v0, v1, v2
v_max3_f32 v251, v32, v33, v34
v_max3_f32 v249, v249, v3, v4
v_max3_f32 v251, v251, v35, v36
v_max3_f32 v249, v249, v5, v6
v_max3_f32 v251, v251, v37, v38
v_max3_f32 v249, v249, v7, v39
v_max_f32 v249, v249, v251
v_mov_b32 v251, v249
s_nop 1
v_permlane16_swap_b32 v249, v251
v_max_f32 v249, v249, v251
v_mov_b32 v251, v249
s_nop 1
v_permlane32_swap_b32 v249, v251
v_max_f32 v249, v249, v251
v_log_f32 v250, v244
s_nop 0
v_max3_f32 v249, 0, v249, v250
v_sub_f32 v250, 0, v249
EX v250,v250
v_sub_f32 v128, v128, v249
v_sub_f32 v129, v129, v249
v_sub_f32 v130, v130, v249
v_sub_f32 v131, v131, v249
v_sub_f32 v64, v64, v249
v_sub_f32 v0, v0, v249
v_sub_f32 v65, v65, v249
v_sub_f32 v1, v1, v249
v_sub_f32 v66, v66, v249
v_sub_f32 v2, v2, v249
v_sub_f32 v67, v67, v249
v_sub_f32 v3, v3, v249
v_sub_f32 v68, v68, v249
v_sub_f32 v4, v4, v249
v_sub_f32 v69, v69, v249
v_sub_f32 v5, v5, v249
v_sub_f32 v70, v70, v249
v_sub_f32 v6, v6, v249
v_sub_f32 v71, v71, v249
v_sub_f32 v7, v7, v249
v_sub_f32 v96, v96, v249
v_sub_f32 v32, v32, v249
v_sub_f32 v97, v97, v249
v_sub_f32 v33, v33, v249
v_sub_f32 v98, v98, v249
v_sub_f32 v34, v34, v249
v_sub_f32 v99, v99, v249
v_sub_f32 v35, v35, v249
v_sub_f32 v100, v100, v249
v_sub_f32 v36, v36, v249
v_sub_f32 v101, v101, v249
v_sub_f32 v37, v37, v249
v_sub_f32 v102, v102, v249
v_sub_f32 v38, v38, v249
v_sub_f32 v103, v103, v249
v_sub_f32 v39, v39, v249
v_mul_f32 v144, v244, v250
v_mov_b32 v145, 0
v_bfe_u32 v249, v241, 0, 8
v_lshl_or_b32 v249, v249, 1, 1
v_lshlrev_b32 v249, 22, v249
v_and_b32 v241, 0xffffff00, v241
AR v254,a0
v_sub_f32 v254, v254, v249
v_mul_f32 v254, v254, v250
AW a0,v254
AR v251,a1
v_sub_f32 v251, v251, v249
v_mul_f32 v251, v251, v250
AW a1,v251
AR v254,a2
v_sub_f32 v254, v254, v249
v_mul_f32 v254, v254, v250
AW a2,v254
AR v251,a3
v_sub_f32 v251, v251, v249
v_mul_f32 v251, v251, v250
AW a3,v251
AR v254,a4
v_sub_f32 v254, v254, v249
v_mul_f32 v254, v254, v250
AW a4,v254
AR v251,a5
v_sub_f32 v251, v251, v249
v_mul_f32 v251, v251, v250
AW a5,v251
AR v254,a6
v_sub_f32 v254, v254, v249
v_mul_f32 v254, v254, v250
AW a6,v254
AR v251,a7
v_sub_f32 v251, v251, v249
v_mul_f32 v251, v251, v250
AW a7,v251
AR v254,a32
v_sub_f32 v254, v254, v249
v_mul_f32 v254, v254, v250
AW a32,v254
AR v251,a33
v_sub_f32 v251, v251, v249
v_mul_f32 v251, v251, v250
AW a33,v251
AR v254,a34
v_sub_f32 v254, v254, v249
v_mul_f32 v254, v254, v250
AW a34,v254
AR v251,a35
v_sub_f32 v251, v251, v249
v_mul_f32 v251, v251, v250
AW a35,v251
AR v254,a36
v_sub_f32 v254, v254, v249
v_mul_f32 v254, v254, v250
AW a36,v254
AR v251,a37
v_sub_f32 v251, v251, v249
v_mul_f32 v251, v251, v250
AW a37,v251
AR v254,a38
v_sub_f32 v254, v254, v249
v_mul_f32 v254, v254, v250
AW a38,v254
AR v251,a39
v_sub_f32 v251, v251, v249
v_mul_f32 v251, v251, v250
AW a39,v251
AR v254,a64
v_sub_f32 v254, v254, v249
v_mul_f32 v254, v254, v250
AW a64,v254
AR v251,a65
v_sub_f32 v251, v251, v249
v_mul_f32 v251, v251, v250
AW a65,v251
AR v254,a66
v_sub_f32 v254, v254, v249
v_mul_f32 v254, v254, v250
AW a66,v254
AR v251,a67
v_sub_f32 v251, v251, v249
v_mul_f32 v251, v251, v250
AW a67,v251
AR v254,a68
v_sub_f32 v254, v254, v249
v_mul_f32 v254, v254, v250
AW a68,v254
AR v251,a69
v_sub_f32 v251, v251, v249
v_mul_f32 v251, v251, v250
AW a69,v251
AR v254,a70
v_sub_f32 v254, v254, v249
v_mul_f32 v254, v254, v250
AW a70,v254
AR v251,a71
v_sub_f32 v251, v251, v249
v_mul_f32 v251, v251, v250
AW a71,v251
AR v254,a96
v_sub_f32 v254, v254, v249
v_mul_f32 v254, v254, v250
AW a96,v254
AR v251,a97
v_sub_f32 v251, v251, v249
v_mul_f32 v251, v251, v250
AW a97,v251
AR v254,a98
v_sub_f32 v254, v254, v249
v_mul_f32 v254, v254, v250
AW a98,v254
AR v251,a99
v_sub_f32 v251, v251, v249
v_mul_f32 v251, v251, v250
AW a99,v251
AR v254,a100
v_sub_f32 v254, v254, v249
v_mul_f32 v254, v254, v250
AW a100,v254
AR v251,a101
v_sub_f32 v251, v251, v249
v_mul_f32 v251, v251, v250
AW a101,v251
AR v254,a102
v_sub_f32 v254, v254, v249
v_mul_f32 v254, v254, v250
AW a102,v254
AR v251,a103
v_sub_f32 v251, v251, v249
v_mul_f32 v251, v251, v250
AW a103,v251
v_max3_f32 v249, v8, v9, v10
v_max3_f32 v251, v40, v41, v42
v_max3_f32 v249, v249, v11, v12
v_max3_f32 v251, v251, v43, v44
v_max3_f32 v249, v249, v13, v14
v_max3_f32 v251, v251, v45, v46
v_max3_f32 v249, v249, v15, v47
v_max_f32 v249, v249, v251
v_mov_b32 v251, v249
s_nop 1
v_permlane16_swap_b32 v249, v251
v_max_f32 v249, v249, v251
v_mov_b32 v251, v249
s_nop 1
v_permlane32_swap_b32 v249, v251
v_max_f32 v249, v249, v251
v_log_f32 v250, v245
s_nop 0
v_max3_f32 v249, 0, v249, v250
v_sub_f32 v250, 0, v249
EX v250,v250
v_sub_f32 v132, v132, v249
v_sub_f32 v133, v133, v249
v_sub_f32 v134, v134, v249
v_sub_f32 v135, v135, v249
v_sub_f32 v72, v72, v249
v_sub_f32 v8, v8, v249
v_sub_f32 v73, v73, v249
v_sub_f32 v9, v9, v249
v_sub_f32 v74, v74, v249
v_sub_f32 v10, v10, v249
v_sub_f32 v75, v75, v249
v_sub_f32 v11, v11, v249
v_sub_f32 v76, v76, v249
v_sub_f32 v12, v12, v249
v_sub_f32 v77, v77, v249
v_sub_f32 v13, v13, v249
v_sub_f32 v78, v78, v249
v_sub_f32 v14, v14, v249
v_sub_f32 v79, v79, v249
v_sub_f32 v15, v15, v249
v_sub_f32 v104, v104, v249
v_sub_f32 v40, v40, v249
v_sub_f32 v105, v105, v249
v_sub_f32 v41, v41, v249
v_sub_f32 v106, v106, v249
v_sub_f32 v42, v42, v249
v_sub_f32 v107, v107, v249
v_sub_f32 v43, v43, v249
v_sub_f32 v108, v108, v249
v_sub_f32 v44, v44, v249
v_sub_f32 v109, v109, v249
v_sub_f32 v45, v45, v249
v_sub_f32 v110, v110, v249
v_sub_f32 v46, v46, v249
v_sub_f32 v111, v111, v249
v_sub_f32 v47, v47, v249
v_mul_f32 v148, v245, v250
v_mov_b32 v149, 0
v_bfe_u32 v249, v241, 8, 8
v_lshl_or_b32 v249, v249, 1, 1
v_lshlrev_b32 v249, 22, v249
v_and_b32 v241, 0xffff00ff, v241
AR v254,a8
v_sub_f32 v254, v254, v249
v_mul_f32 v254, v254, v250
AW a8,v254
AR v251,a9
v_sub_f32 v251, v251, v249
v_mul_f32 v251, v251, v250
AW a9,v251
AR v254,a10
v_sub_f32 v254, v254, v249
v_mul_f32 v254, v254, v250
AW a10,v254
AR v251,a11
v_sub_f32 v251, v251, v249
v_mul_f32 v251, v251, v250
AW a11,v251
AR v254,a12
v_sub_f32 v254, v254, v249
v_mul_f32 v254, v254, v250
AW a12,v254
AR v251,a13
v_sub_f32 v251, v251, v249
v_mul_f32 v251, v251, v250
AW a13,v251
AR v254,a14
v_sub_f32 v254, v254, v249
v_mul_f32 v254, v254, v250
AW a14,v254
AR v251,a15
v_sub_f32 v251, v251, v249
v_mul_f32 v251, v251, v250
AW a15,v251
AR v254,a40
v_sub_f32 v254, v254, v249
v_mul_f32 v254, v254, v250
AW a40,v254
AR v251,a41
v_sub_f32 v251, v251, v249
v_mul_f32 v251, v251, v250
AW a41,v251
AR v254,a42
v_sub_f32 v254, v254, v249
v_mul_f32 v254, v254, v250
AW a42,v254
AR v251,a43
v_sub_f32 v251, v251, v249
v_mul_f32 v251, v251, v250
AW a43,v251
AR v254,a44
v_sub_f32 v254, v254, v249
v_mul_f32 v254, v254, v250
AW a44,v254
AR v251,a45
v_sub_f32 v251, v251, v249
v_mul_f32 v251, v251, v250
AW a45,v251
AR v254,a46
v_sub_f32 v254, v254, v249
v_mul_f32 v254, v254, v250
AW a46,v254
AR v251,a47
v_sub_f32 v251, v251, v249
v_mul_f32 v251, v251, v250
AW a47,v251
AR v254,a72
v_sub_f32 v254, v254, v249
v_mul_f32 v254, v254, v250
AW a72,v254
AR v251,a73
v_sub_f32 v251, v251, v249
v_mul_f32 v251, v251, v250
AW a73,v251
AR v254,a74
v_sub_f32 v254, v254, v249
v_mul_f32 v254, v254, v250
AW a74,v254
AR v251,a75
v_sub_f32 v251, v251, v249
v_mul_f32 v251, v251, v250
AW a75,v251
AR v254,a76
v_sub_f32 v254, v254, v249
v_mul_f32 v254, v254, v250
AW a76,v254
AR v251,a77
v_sub_f32 v251, v251, v249
v_mul_f32 v251, v251, v250
AW a77,v251
AR v254,a78
v_sub_f32 v254, v254, v249
v_mul_f32 v254, v254, v250
AW a78,v254
AR v251,a79
v_sub_f32 v251, v251, v249
v_mul_f32 v251, v251, v250
AW a79,v251
AR v254,a104
v_sub_f32 v254, v254, v249
v_mul_f32 v254, v254, v250
AW a104,v254
AR v251,a105
v_sub_f32 v251, v251, v249
v_mul_f32 v251, v251, v250
AW a105,v251
AR v254,a106
v_sub_f32 v254, v254, v249
v_mul_f32 v254, v254, v250
AW a106,v254
AR v251,a107
v_sub_f32 v251, v251, v249
v_mul_f32 v251, v251, v250
AW a107,v251
AR v254,a108
v_sub_f32 v254, v254, v249
v_mul_f32 v254, v254, v250
AW a108,v254
AR v251,a109
v_sub_f32 v251, v251, v249
v_mul_f32 v251, v251, v250
AW a109,v251
AR v254,a110
v_sub_f32 v254, v254, v249
v_mul_f32 v254, v254, v250
AW a110,v254
AR v251,a111
v_sub_f32 v251, v251, v249
v_mul_f32 v251, v251, v250
AW a111,v251
v_max3_f32 v249, v16, v17, v18
v_max3_f32 v251, v48, v49, v50
v_max3_f32 v249, v249, v19, v20
v_max3_f32 v251, v251, v51, v52
v_max3_f32 v249, v249, v21, v22
v_max3_f32 v251, v251, v53, v54
v_max3_f32 v249, v249, v23, v55
v_max_f32 v249, v249, v251
v_mov_b32 v251, v249
s_nop 1
v_permlane16_swap_b32 v249, v251
v_max_f32 v249, v249, v251
v_mov_b32 v251, v249
s_nop 1
v_permlane32_swap_b32 v249, v251
v_max_f32 v249, v249, v251
v_log_f32 v250, v246
s_nop 0
v_max3_f32 v249, 0, v249, v250
v_sub_f32 v250, 0, v249
EX v250,v250
v_sub_f32 v136, v136, v249
v_sub_f32 v137, v137, v249
v_sub_f32 v138, v138, v249
v_sub_f32 v139, v139, v249
v_sub_f32 v80, v80, v249
v_sub_f32 v16, v16, v249
v_sub_f32 v81, v81, v249
)ASM"
R"ASM(
v_sub_f32 v17, v17, v249
v_sub_f32 v82, v82, v249
v_sub_f32 v18, v18, v249
v_sub_f32 v83, v83, v249
v_sub_f32 v19, v19, v249
v_sub_f32 v84, v84, v249
v_sub_f32 v20, v20, v249
v_sub_f32 v85, v85, v249
v_sub_f32 v21, v21, v249
v_sub_f32 v86, v86, v249
v_sub_f32 v22, v22, v249
v_sub_f32 v87, v87, v249
v_sub_f32 v23, v23, v249
v_sub_f32 v112, v112, v249
v_sub_f32 v48, v48, v249
v_sub_f32 v113, v113, v249
v_sub_f32 v49, v49, v249
v_sub_f32 v114, v114, v249
v_sub_f32 v50, v50, v249
v_sub_f32 v115, v115, v249
v_sub_f32 v51, v51, v249
v_sub_f32 v116, v116, v249
v_sub_f32 v52, v52, v249
v_sub_f32 v117, v117, v249
v_sub_f32 v53, v53, v249
v_sub_f32 v118, v118, v249
v_sub_f32 v54, v54, v249
v_sub_f32 v119, v119, v249
v_sub_f32 v55, v55, v249
v_mul_f32 v152, v246, v250
v_mov_b32 v153, 0
v_bfe_u32 v249, v241, 16, 8
v_lshl_or_b32 v249, v249, 1, 1
v_lshlrev_b32 v249, 22, v249
v_and_b32 v241, 0xff00ffff, v241
AR v254,a16
v_sub_f32 v254, v254, v249
v_mul_f32 v254, v254, v250
AW a16,v254
AR v251,a17
v_sub_f32 v251, v251, v249
v_mul_f32 v251, v251, v250
AW a17,v251
AR v254,a18
v_sub_f32 v254, v254, v249
v_mul_f32 v254, v254, v250
AW a18,v254
AR v251,a19
v_sub_f32 v251, v251, v249
v_mul_f32 v251, v251, v250
AW a19,v251
AR v254,a20
v_sub_f32 v254, v254, v249
v_mul_f32 v254, v254, v250
AW a20,v254
AR v251,a21
v_sub_f32 v251, v251, v249
v_mul_f32 v251, v251, v250
AW a21,v251
AR v254,a22
v_sub_f32 v254, v254, v249
v_mul_f32 v254, v254, v250
AW a22,v254
AR v251,a23
v_sub_f32 v251, v251, v249
v_mul_f32 v251, v251, v250
AW a23,v251
AR v254,a48
v_sub_f32 v254, v254, v249
v_mul_f32 v254, v254, v250
AW a48,v254
AR v251,a49
v_sub_f32 v251, v251, v249
v_mul_f32 v251, v251, v250
AW a49,v251
AR v254,a50
v_sub_f32 v254, v254, v249
v_mul_f32 v254, v254, v250
AW a50,v254
AR v251,a51
v_sub_f32 v251, v251, v249
v_mul_f32 v251, v251, v250
AW a51,v251
AR v254,a52
v_sub_f32 v254, v254, v249
v_mul_f32 v254, v254, v250
AW a52,v254
AR v251,a53
v_sub_f32 v251, v251, v249
v_mul_f32 v251, v251, v250
AW a53,v251
AR v254,a54
v_sub_f32 v254, v254, v249
v_mul_f32 v254, v254, v250
AW a54,v254
AR v251,a55
v_sub_f32 v251, v251, v249
v_mul_f32 v251, v251, v250
AW a55,v251
AR v254,a80
v_sub_f32 v254, v254, v249
v_mul_f32 v254, v254, v250
AW a80,v254
AR v251,a81
v_sub_f32 v251, v251, v249
v_mul_f32 v251, v251, v250
AW a81,v251
AR v254,a82
v_sub_f32 v254, v254, v249
v_mul_f32 v254, v254, v250
AW a82,v254
AR v251,a83
v_sub_f32 v251, v251, v249
v_mul_f32 v251, v251, v250
AW a83,v251
AR v254,a84
v_sub_f32 v254, v254, v249
v_mul_f32 v254, v254, v250
AW a84,v254
AR v251,a85
v_sub_f32 v251, v251, v249
v_mul_f32 v251, v251, v250
AW a85,v251
AR v254,a86
v_sub_f32 v254, v254, v249
v_mul_f32 v254, v254, v250
AW a86,v254
AR v251,a87
v_sub_f32 v251, v251, v249
v_mul_f32 v251, v251, v250
AW a87,v251
AR v254,a112
v_sub_f32 v254, v254, v249
v_mul_f32 v254, v254, v250
AW a112,v254
AR v251,a113
v_sub_f32 v251, v251, v249
v_mul_f32 v251, v251, v250
AW a113,v251
AR v254,a114
v_sub_f32 v254, v254, v249
v_mul_f32 v254, v254, v250
AW a114,v254
AR v251,a115
v_sub_f32 v251, v251, v249
v_mul_f32 v251, v251, v250
AW a115,v251
AR v254,a116
v_sub_f32 v254, v254, v249
v_mul_f32 v254, v254, v250
AW a116,v254
AR v251,a117
v_sub_f32 v251, v251, v249
v_mul_f32 v251, v251, v250
AW a117,v251
AR v254,a118
v_sub_f32 v254, v254, v249
v_mul_f32 v254, v254, v250
AW a118,v254
AR v251,a119
v_sub_f32 v251, v251, v249
v_mul_f32 v251, v251, v250
AW a119,v251
v_max3_f32 v249, v24, v25, v26
v_max3_f32 v251, v56, v57, v58
v_max3_f32 v249, v249, v27, v28
v_max3_f32 v251, v251, v59, v60
v_max3_f32 v249, v249, v29, v30
v_max3_f32 v251, v251, v61, v62
v_max3_f32 v249, v249, v31, v63
v_max_f32 v249, v249, v251
v_mov_b32 v251, v249
s_nop 1
v_permlane16_swap_b32 v249, v251
v_max_f32 v249, v249, v251
v_mov_b32 v251, v249
s_nop 1
v_permlane32_swap_b32 v249, v251
v_max_f32 v249, v249, v251
v_log_f32 v250, v247
s_nop 0
v_max3_f32 v249, 0, v249, v250
v_sub_f32 v250, 0, v249
EX v250,v250
v_sub_f32 v140, v140, v249
v_sub_f32 v141, v141, v249
v_sub_f32 v142, v142, v249
v_sub_f32 v143, v143, v249
v_sub_f32 v88, v88, v249
v_sub_f32 v24, v24, v249
v_sub_f32 v89, v89, v249
v_sub_f32 v25, v25, v249
v_sub_f32 v90, v90, v249
v_sub_f32 v26, v26, v249
v_sub_f32 v91, v91, v249
v_sub_f32 v27, v27, v249
v_sub_f32 v92, v92, v249
v_sub_f32 v28, v28, v249
v_sub_f32 v93, v93, v249
v_sub_f32 v29, v29, v249
v_sub_f32 v94, v94, v249
v_sub_f32 v30, v30, v249
v_sub_f32 v95, v95, v249
v_sub_f32 v31, v31, v249
v_sub_f32 v120, v120, v249
v_sub_f32 v56, v56, v249
v_sub_f32 v121, v121, v249
v_sub_f32 v57, v57, v249
v_sub_f32 v122, v122, v249
v_sub_f32 v58, v58, v249
v_sub_f32 v123, v123, v249
v_sub_f32 v59, v59, v249
v_sub_f32 v124, v124, v249
v_sub_f32 v60, v60, v249
v_sub_f32 v125, v125, v249
v_sub_f32 v61, v61, v249
v_sub_f32 v126, v126, v249
v_sub_f32 v62, v62, v249
v_sub_f32 v127, v127, v249
v_sub_f32 v63, v63, v249
v_mul_f32 v156, v247, v250
v_mov_b32 v157, 0
v_bfe_u32 v249, v241, 24, 8
v_lshl_or_b32 v249, v249, 1, 1
v_lshlrev_b32 v249, 22, v249
v_and_b32 v241, 0xffffff, v241
AR v254,a24
v_sub_f32 v254, v254, v249
v_mul_f32 v254, v254, v250
AW a24,v254
AR v251,a25
v_sub_f32 v251, v251, v249
v_mul_f32 v251, v251, v250
AW a25,v251
AR v254,a26
v_sub_f32 v254, v254, v249
v_mul_f32 v254, v254, v250
AW a26,v254
AR v251,a27
v_sub_f32 v251, v251, v249
v_mul_f32 v251, v251, v250
AW a27,v251
AR v254,a28
v_sub_f32 v254, v254, v249
v_mul_f32 v254, v254, v250
AW a28,v254
AR v251,a29
v_sub_f32 v251, v251, v249
v_mul_f32 v251, v251, v250
AW a29,v251
AR v254,a30
v_sub_f32 v254, v254, v249
v_mul_f32 v254, v254, v250
AW a30,v254
AR v251,a31
v_sub_f32 v251, v251, v249
v_mul_f32 v251, v251, v250
AW a31,v251
AR v254,a56
v_sub_f32 v254, v254, v249
v_mul_f32 v254, v254, v250
AW a56,v254
AR v251,a57
v_sub_f32 v251, v251, v249
v_mul_f32 v251, v251, v250
AW a57,v251
AR v254,a58
v_sub_f32 v254, v254, v249
v_mul_f32 v254, v254, v250
AW a58,v254
AR v251,a59
v_sub_f32 v251, v251, v249
v_mul_f32 v251, v251, v250
AW a59,v251
AR v254,a60
v_sub_f32 v254, v254, v249
v_mul_f32 v254, v254, v250
AW a60,v254
AR v251,a61
v_sub_f32 v251, v251, v249
v_mul_f32 v251, v251, v250
AW a61,v251
AR v254,a62
v_sub_f32 v254, v254, v249
v_mul_f32 v254, v254, v250
AW a62,v254
AR v251,a63
v_sub_f32 v251, v251, v249
v_mul_f32 v251, v251, v250
AW a63,v251
AR v254,a88
v_sub_f32 v254, v254, v249
v_mul_f32 v254, v254, v250
AW a88,v254
AR v251,a89
v_sub_f32 v251, v251, v249
v_mul_f32 v251, v251, v250
AW a89,v251
AR v254,a90
v_sub_f32 v254, v254, v249
v_mul_f32 v254, v254, v250
AW a90,v254
AR v251,a91
v_sub_f32 v251, v251, v249
v_mul_f32 v251, v251, v250
AW a91,v251
AR v254,a92
v_sub_f32 v254, v254, v249
v_mul_f32 v254, v254, v250
AW a92,v254
AR v251,a93
v_sub_f32 v251, v251, v249
v_mul_f32 v251, v251, v250
AW a93,v251
AR v254,a94
v_sub_f32 v254, v254, v249
v_mul_f32 v254, v254, v250
AW a94,v254
AR v251,a95
v_sub_f32 v251, v251, v249
v_mul_f32 v251, v251, v250
AW a95,v251
AR v254,a120
v_sub_f32 v254, v254, v249
v_mul_f32 v254, v254, v250
AW a120,v254
AR v251,a121
v_sub_f32 v251, v251, v249
v_mul_f32 v251, v251, v250
AW a121,v251
AR v254,a122
v_sub_f32 v254, v254, v249
v_mul_f32 v254, v254, v250
AW a122,v254
AR v251,a123
v_sub_f32 v251, v251, v249
v_mul_f32 v251, v251, v250
AW a123,v251
AR v254,a124
v_sub_f32 v254, v254, v249
v_mul_f32 v254, v254, v250
AW a124,v254
AR v251,a125
v_sub_f32 v251, v251, v249
v_mul_f32 v251, v251, v250
AW a125,v251
AR v254,a126
v_sub_f32 v254, v254, v249
v_mul_f32 v254, v254, v250
AW a126,v254
AR v251,a127
v_sub_f32 v251, v251, v249
v_mul_f32 v251, v251, v250
AW a127,v251
EX v0,v0
EX v1,v1
EX v2,v2
EX v3,v3
EX v4,v4
EX v5,v5
EX v6,v6
EX v7,v7
EX v8,v8
EX v9,v9
EX v10,v10
EX v11,v11
EX v12,v12
EX v13,v13
EX v14,v14
EX v15,v15
EX v16,v16
EX v17,v17
EX v18,v18
EX v19,v19
EX v20,v20
EX v21,v21
EX v22,v22
EX v23,v23
EX v24,v24
EX v25,v25
EX v26,v26
EX v27,v27
EX v28,v28
EX v29,v29
EX v30,v30
EX v31,v31
EX v32,v32
EX v33,v33
EX v34,v34
EX v35,v35
EX v36,v36
EX v37,v37
EX v38,v38
EX v39,v39
EX v40,v40
EX v41,v41
EX v42,v42
EX v43,v43
EX v44,v44
EX v45,v45
EX v46,v46
EX v47,v47
EX v48,v48
EX v49,v49
EX v50,v50
EX v51,v51
EX v52,v52
EX v53,v53
EX v54,v54
EX v55,v55
EX v56,v56
EX v57,v57
EX v58,v58
EX v59,v59
EX v60,v60
EX v61,v61
EX v62,v62
EX v63,v63
CV v0,v0,v1
CV v1,v2,v3
CV v2,v4,v5
CV v3,v6,v7
CV v8,v8,v9
CV v9,v10,v11
CV v10,v12,v13
CV v11,v14,v15
CV v16,v16,v17
CV v17,v18,v19
CV v18,v20,v21
CV v19,v22,v23
CV v24,v24,v25
CV v25,v26,v27
CV v26,v28,v29
CV v27,v30,v31
CV v4,v32,v33
CV v5,v34,v35
CV v6,v36,v37
CV v7,v38,v39
CV v12,v40,v41
CV v13,v42,v43
CV v14,v44,v45
CV v15,v46,v47
CV v20,v48,v49
CV v21,v50,v51
CV v22,v52,v53
CV v23,v54,v55
CV v28,v56,v57
CV v29,v58,v59
CV v30,v60,v61
CV v31,v62,v63
v_smfmac_f32_16x16x64_bf16 v[144:147], v[236:239], v[0:7], v234
v_smfmac_f32_16x16x64_bf16 v[148:151], v[236:239], v[8:15], v234
v_smfmac_f32_16x16x64_bf16 v[152:155], v[236:239], v[16:23], v234
v_smfmac_f32_16x16x64_bf16 v[156:159], v[236:239], v[24:31], v234
s_nop 9
s_branch LRET_%=
LRARE022_%=:
s_nop 15
s_waitcnt vmcnt(0) lgkmcnt(0)
v_add_u32 v249, s74, v224
RD a[192:195],v249 offset:0
RD a[208:211],v249 offset:4096
RD a[224:227],v249 offset:8192
RD a[240:243],v249 offset:12288
v_add_u32 v250, s74, v225
RD a[196:199],v250 offset:0
RD a[212:215],v250 offset:4096
RD a[228:231],v250 offset:8192
RD a[244:247],v250 offset:12288
v_add_u32 v251, s74, v226
RD a[200:203],v251 offset:0
RD a[216:219],v251 offset:4096
RD a[232:235],v251 offset:8192
RD a[248:251],v251 offset:12288
v_add_u32 v254, s74, v227
RD a[204:207],v254 offset:0
RD a[220:223],v254 offset:4096
RD a[236:239],v254 offset:8192
RD a[252:255],v254 offset:12288
s_waitcnt lgkmcnt(0)
MM v[16:19],a[192:195],v[168:171],v[136:139]
MM v[16:19],a[196:199],v[184:187],v[16:19]
MM v[16:19],a[200:203],v[200:203],v[16:19]
MM v[16:19],a[204:207],v[216:219],v[16:19]
MM v[20:23],a[220:223],v[216:219],v[136:139]
MM v[20:23],a[216:219],v[200:203],v[20:23]
MM v[20:23],a[212:215],v[184:187],v[20:23]
MM v[20:23],a[208:211],v[168:171],v[20:23]
MM v[28:31],a[208:211],v[172:175],v[140:143]
MM v[28:31],a[212:215],v[188:191],v[28:31]
MM v[28:31],a[216:219],v[204:207],v[28:31]
MM v[28:31],a[220:223],v[220:223],v[28:31]
MM v[24:27],a[204:207],v[220:223],v[140:143]
MM v[24:27],a[200:203],v[204:207],v[24:27]
MM v[24:27],a[196:199],v[188:191],v[24:27]
MM v[24:27],a[192:195],v[172:175],v[24:27]
MM v[48:51],a[224:227],v[168:171],v[136:139]
MM v[48:51],a[228:231],v[184:187],v[48:51]
MM v[48:51],a[232:235],v[200:203],v[48:51]
MM v[48:51],a[236:239],v[216:219],v[48:51]
MM v[52:55],a[252:255],v[216:219],v[136:139]
MM v[52:55],a[248:251],v[200:203],v[52:55]
MM v[52:55],a[244:247],v[184:187],v[52:55]
MM v[52:55],a[240:243],v[168:171],v[52:55]
MM v[60:63],a[240:243],v[172:175],v[140:143]
MM v[60:63],a[244:247],v[188:191],v[60:63]
MM v[60:63],a[248:251],v[204:207],v[60:63]
MM v[60:63],a[252:255],v[220:223],v[60:63]
MM v[56:59],a[236:239],v[220:223],v[140:143]
MM v[56:59],a[232:235],v[204:207],v[56:59]
MM v[56:59],a[228:231],v[188:191],v[56:59]
MM v[56:59],a[224:227],v[172:175],v[56:59]
s_nop 15
v_add_u32 v249, s75, v224
RD a[192:195],v249 offset:0
RD a[208:211],v249 offset:4096
RD a[224:227],v249 offset:8192
RD a[240:243],v249 offset:12288
v_add_u32 v250, s75, v225
RD a[196:199],v250 offset:0
RD a[212:215],v250 offset:4096
RD a[228:231],v250 offset:8192
RD a[244:247],v250 offset:12288
v_add_u32 v251, s75, v226
RD a[200:203],v251 offset:0
RD a[216:219],v251 offset:4096
RD a[232:235],v251 offset:8192
RD a[248:251],v251 offset:12288
v_add_u32 v254, s75, v227
RD a[204:207],v254 offset:0
RD a[220:223],v254 offset:4096
RD a[236:239],v254 offset:8192
RD a[252:255],v254 offset:12288
s_waitcnt lgkmcnt(0)
v_max3_f32 v249, v16, v17, v18
v_max3_f32 v251, v48, v49, v50
v_max3_f32 v249, v249, v19, v20
v_max3_f32 v251, v251, v51, v52
v_max3_f32 v249, v249, v21, v22
v_max3_f32 v251, v251, v53, v54
v_max3_f32 v249, v249, v23, v55
v_max_f32 v249, v249, v251
v_mov_b32 v251, v249
s_nop 1
v_permlane16_swap_b32 v249, v251
v_max_f32 v249, v249, v251
v_mov_b32 v251, v249
)ASM"
R"ASM(
s_nop 1
v_permlane32_swap_b32 v249, v251
v_max_f32 v249, v249, v251
v_log_f32 v250, v246
s_nop 0
v_max3_f32 v249, 0, v249, v250
v_sub_f32 v250, 0, v249
EX v250,v250
v_sub_f32 v136, v136, v249
v_sub_f32 v137, v137, v249
v_sub_f32 v138, v138, v249
v_sub_f32 v139, v139, v249
v_sub_f32 v80, v80, v249
v_sub_f32 v16, v16, v249
v_sub_f32 v81, v81, v249
v_sub_f32 v17, v17, v249
v_sub_f32 v82, v82, v249
v_sub_f32 v18, v18, v249
v_sub_f32 v83, v83, v249
v_sub_f32 v19, v19, v249
v_sub_f32 v84, v84, v249
v_sub_f32 v20, v20, v249
v_sub_f32 v85, v85, v249
v_sub_f32 v21, v21, v249
v_sub_f32 v86, v86, v249
v_sub_f32 v22, v22, v249
v_sub_f32 v87, v87, v249
v_sub_f32 v23, v23, v249
v_sub_f32 v112, v112, v249
v_sub_f32 v48, v48, v249
v_sub_f32 v113, v113, v249
v_sub_f32 v49, v49, v249
v_sub_f32 v114, v114, v249
v_sub_f32 v50, v50, v249
v_sub_f32 v115, v115, v249
v_sub_f32 v51, v51, v249
v_sub_f32 v116, v116, v249
v_sub_f32 v52, v52, v249
v_sub_f32 v117, v117, v249
v_sub_f32 v53, v53, v249
v_sub_f32 v118, v118, v249
v_sub_f32 v54, v54, v249
v_sub_f32 v119, v119, v249
v_sub_f32 v55, v55, v249
v_mul_f32 v152, v246, v250
v_mov_b32 v153, 0
v_bfe_u32 v249, v241, 16, 8
v_lshl_or_b32 v249, v249, 1, 1
v_lshlrev_b32 v249, 22, v249
v_and_b32 v241, 0xff00ffff, v241
AR v254,a16
v_sub_f32 v254, v254, v249
v_mul_f32 v254, v254, v250
AW a16,v254
AR v251,a17
v_sub_f32 v251, v251, v249
v_mul_f32 v251, v251, v250
AW a17,v251
AR v254,a18
v_sub_f32 v254, v254, v249
v_mul_f32 v254, v254, v250
AW a18,v254
AR v251,a19
v_sub_f32 v251, v251, v249
v_mul_f32 v251, v251, v250
AW a19,v251
AR v254,a20
v_sub_f32 v254, v254, v249
v_mul_f32 v254, v254, v250
AW a20,v254
AR v251,a21
v_sub_f32 v251, v251, v249
v_mul_f32 v251, v251, v250
AW a21,v251
AR v254,a22
v_sub_f32 v254, v254, v249
v_mul_f32 v254, v254, v250
AW a22,v254
AR v251,a23
v_sub_f32 v251, v251, v249
v_mul_f32 v251, v251, v250
AW a23,v251
AR v254,a48
v_sub_f32 v254, v254, v249
v_mul_f32 v254, v254, v250
AW a48,v254
AR v251,a49
v_sub_f32 v251, v251, v249
v_mul_f32 v251, v251, v250
AW a49,v251
AR v254,a50
v_sub_f32 v254, v254, v249
v_mul_f32 v254, v254, v250
AW a50,v254
AR v251,a51
v_sub_f32 v251, v251, v249
v_mul_f32 v251, v251, v250
AW a51,v251
AR v254,a52
v_sub_f32 v254, v254, v249
v_mul_f32 v254, v254, v250
AW a52,v254
AR v251,a53
v_sub_f32 v251, v251, v249
v_mul_f32 v251, v251, v250
AW a53,v251
AR v254,a54
v_sub_f32 v254, v254, v249
v_mul_f32 v254, v254, v250
AW a54,v254
AR v251,a55
v_sub_f32 v251, v251, v249
v_mul_f32 v251, v251, v250
AW a55,v251
AR v254,a80
v_sub_f32 v254, v254, v249
v_mul_f32 v254, v254, v250
AW a80,v254
AR v251,a81
v_sub_f32 v251, v251, v249
v_mul_f32 v251, v251, v250
AW a81,v251
AR v254,a82
v_sub_f32 v254, v254, v249
v_mul_f32 v254, v254, v250
AW a82,v254
AR v251,a83
v_sub_f32 v251, v251, v249
v_mul_f32 v251, v251, v250
AW a83,v251
AR v254,a84
v_sub_f32 v254, v254, v249
v_mul_f32 v254, v254, v250
AW a84,v254
AR v251,a85
v_sub_f32 v251, v251, v249
v_mul_f32 v251, v251, v250
AW a85,v251
AR v254,a86
v_sub_f32 v254, v254, v249
v_mul_f32 v254, v254, v250
AW a86,v254
AR v251,a87
v_sub_f32 v251, v251, v249
v_mul_f32 v251, v251, v250
AW a87,v251
AR v254,a112
v_sub_f32 v254, v254, v249
v_mul_f32 v254, v254, v250
AW a112,v254
AR v251,a113
v_sub_f32 v251, v251, v249
v_mul_f32 v251, v251, v250
AW a113,v251
AR v254,a114
v_sub_f32 v254, v254, v249
v_mul_f32 v254, v254, v250
AW a114,v254
AR v251,a115
v_sub_f32 v251, v251, v249
v_mul_f32 v251, v251, v250
AW a115,v251
AR v254,a116
v_sub_f32 v254, v254, v249
v_mul_f32 v254, v254, v250
AW a116,v254
AR v251,a117
v_sub_f32 v251, v251, v249
v_mul_f32 v251, v251, v250
AW a117,v251
AR v254,a118
v_sub_f32 v254, v254, v249
v_mul_f32 v254, v254, v250
AW a118,v254
AR v251,a119
v_sub_f32 v251, v251, v249
v_mul_f32 v251, v251, v250
AW a119,v251
v_max3_f32 v249, v24, v25, v26
v_max3_f32 v251, v56, v57, v58
v_max3_f32 v249, v249, v27, v28
v_max3_f32 v251, v251, v59, v60
v_max3_f32 v249, v249, v29, v30
v_max3_f32 v251, v251, v61, v62
v_max3_f32 v249, v249, v31, v63
v_max_f32 v249, v249, v251
v_mov_b32 v251, v249
s_nop 1
v_permlane16_swap_b32 v249, v251
v_max_f32 v249, v249, v251
v_mov_b32 v251, v249
s_nop 1
v_permlane32_swap_b32 v249, v251
v_max_f32 v249, v249, v251
v_log_f32 v250, v247
s_nop 0
v_max3_f32 v249, 0, v249, v250
v_sub_f32 v250, 0, v249
EX v250,v250
v_sub_f32 v140, v140, v249
v_sub_f32 v141, v141, v249
v_sub_f32 v142, v142, v249
v_sub_f32 v143, v143, v249
v_sub_f32 v88, v88, v249
v_sub_f32 v24, v24, v249
v_sub_f32 v89, v89, v249
v_sub_f32 v25, v25, v249
v_sub_f32 v90, v90, v249
v_sub_f32 v26, v26, v249
v_sub_f32 v91, v91, v249
v_sub_f32 v27, v27, v249
v_sub_f32 v92, v92, v249
v_sub_f32 v28, v28, v249
v_sub_f32 v93, v93, v249
v_sub_f32 v29, v29, v249
v_sub_f32 v94, v94, v249
v_sub_f32 v30, v30, v249
v_sub_f32 v95, v95, v249
v_sub_f32 v31, v31, v249
v_sub_f32 v120, v120, v249
v_sub_f32 v56, v56, v249
v_sub_f32 v121, v121, v249
v_sub_f32 v57, v57, v249
v_sub_f32 v122, v122, v249
v_sub_f32 v58, v58, v249
v_sub_f32 v123, v123, v249
v_sub_f32 v59, v59, v249
v_sub_f32 v124, v124, v249
v_sub_f32 v60, v60, v249
v_sub_f32 v125, v125, v249
v_sub_f32 v61, v61, v249
v_sub_f32 v126, v126, v249
v_sub_f32 v62, v62, v249
v_sub_f32 v127, v127, v249
v_sub_f32 v63, v63, v249
v_mul_f32 v156, v247, v250
v_mov_b32 v157, 0
v_bfe_u32 v249, v241, 24, 8
v_lshl_or_b32 v249, v249, 1, 1
v_lshlrev_b32 v249, 22, v249
v_and_b32 v241, 0xffffff, v241
AR v254,a24
v_sub_f32 v254, v254, v249
v_mul_f32 v254, v254, v250
AW a24,v254
AR v251,a25
v_sub_f32 v251, v251, v249
v_mul_f32 v251, v251, v250
AW a25,v251
AR v254,a26
v_sub_f32 v254, v254, v249
v_mul_f32 v254, v254, v250
AW a26,v254
AR v251,a27
v_sub_f32 v251, v251, v249
v_mul_f32 v251, v251, v250
AW a27,v251
AR v254,a28
v_sub_f32 v254, v254, v249
v_mul_f32 v254, v254, v250
AW a28,v254
AR v251,a29
v_sub_f32 v251, v251, v249
v_mul_f32 v251, v251, v250
AW a29,v251
AR v254,a30
v_sub_f32 v254, v254, v249
v_mul_f32 v254, v254, v250
AW a30,v254
AR v251,a31
v_sub_f32 v251, v251, v249
v_mul_f32 v251, v251, v250
AW a31,v251
AR v254,a56
v_sub_f32 v254, v254, v249
v_mul_f32 v254, v254, v250
AW a56,v254
AR v251,a57
v_sub_f32 v251, v251, v249
v_mul_f32 v251, v251, v250
AW a57,v251
AR v254,a58
v_sub_f32 v254, v254, v249
v_mul_f32 v254, v254, v250
AW a58,v254
AR v251,a59
v_sub_f32 v251, v251, v249
v_mul_f32 v251, v251, v250
AW a59,v251
AR v254,a60
v_sub_f32 v254, v254, v249
v_mul_f32 v254, v254, v250
AW a60,v254
AR v251,a61
v_sub_f32 v251, v251, v249
v_mul_f32 v251, v251, v250
AW a61,v251
AR v254,a62
v_sub_f32 v254, v254, v249
v_mul_f32 v254, v254, v250
AW a62,v254
AR v251,a63
v_sub_f32 v251, v251, v249
v_mul_f32 v251, v251, v250
AW a63,v251
AR v254,a88
v_sub_f32 v254, v254, v249
v_mul_f32 v254, v254, v250
AW a88,v254
AR v251,a89
v_sub_f32 v251, v251, v249
v_mul_f32 v251, v251, v250
AW a89,v251
AR v254,a90
v_sub_f32 v254, v254, v249
v_mul_f32 v254, v254, v250
AW a90,v254
AR v251,a91
v_sub_f32 v251, v251, v249
v_mul_f32 v251, v251, v250
AW a91,v251
AR v254,a92
v_sub_f32 v254, v254, v249
v_mul_f32 v254, v254, v250
AW a92,v254
AR v251,a93
v_sub_f32 v251, v251, v249
v_mul_f32 v251, v251, v250
AW a93,v251
AR v254,a94
v_sub_f32 v254, v254, v249
v_mul_f32 v254, v254, v250
AW a94,v254
AR v251,a95
v_sub_f32 v251, v251, v249
v_mul_f32 v251, v251, v250
AW a95,v251
AR v254,a120
v_sub_f32 v254, v254, v249
v_mul_f32 v254, v254, v250
AW a120,v254
AR v251,a121
v_sub_f32 v251, v251, v249
v_mul_f32 v251, v251, v250
AW a121,v251
AR v254,a122
v_sub_f32 v254, v254, v249
v_mul_f32 v254, v254, v250
AW a122,v254
AR v251,a123
v_sub_f32 v251, v251, v249
v_mul_f32 v251, v251, v250
AW a123,v251
AR v254,a124
v_sub_f32 v254, v254, v249
v_mul_f32 v254, v254, v250
AW a124,v254
AR v251,a125
v_sub_f32 v251, v251, v249
v_mul_f32 v251, v251, v250
AW a125,v251
AR v254,a126
v_sub_f32 v254, v254, v249
v_mul_f32 v254, v254, v250
AW a126,v254
AR v251,a127
v_sub_f32 v251, v251, v249
v_mul_f32 v251, v251, v250
AW a127,v251
EX v16,v16
EX v17,v17
EX v18,v18
EX v19,v19
EX v20,v20
EX v21,v21
EX v22,v22
EX v23,v23
EX v24,v24
EX v25,v25
EX v26,v26
EX v27,v27
EX v28,v28
EX v29,v29
EX v30,v30
EX v31,v31
EX v48,v48
EX v49,v49
EX v50,v50
EX v51,v51
EX v52,v52
EX v53,v53
EX v54,v54
EX v55,v55
EX v56,v56
EX v57,v57
EX v58,v58
EX v59,v59
EX v60,v60
EX v61,v61
EX v62,v62
EX v63,v63
CV v16,v16,v17
CV v17,v18,v19
CV v18,v20,v21
CV v19,v22,v23
CV v24,v24,v25
CV v25,v26,v27
CV v26,v28,v29
CV v27,v30,v31
CV v20,v48,v49
CV v21,v50,v51
CV v22,v52,v53
CV v23,v54,v55
CV v28,v56,v57
CV v29,v58,v59
CV v30,v60,v61
CV v31,v62,v63
v_smfmac_f32_16x16x64_bf16 v[152:155], v[236:239], v[16:23], v234
s_nop 0
v_smfmac_f32_16x16x64_bf16 v[156:159], v[236:239], v[24:31], v234
s_nop 9
s_branch LRET_%=
LRARE122_%=:
s_nop 15
s_waitcnt vmcnt(0) lgkmcnt(0)
v_add_u32 v249, s74, v224
RD a[192:195],v249 offset:0
RD a[208:211],v249 offset:4096
RD a[224:227],v249 offset:8192
RD a[240:243],v249 offset:12288
v_add_u32 v250, s74, v225
RD a[196:199],v250 offset:0
RD a[212:215],v250 offset:4096
RD a[228:231],v250 offset:8192
RD a[244:247],v250 offset:12288
v_add_u32 v251, s74, v226
RD a[200:203],v251 offset:0
RD a[216:219],v251 offset:4096
RD a[232:235],v251 offset:8192
RD a[248:251],v251 offset:12288
v_add_u32 v254, s74, v227
RD a[204:207],v254 offset:0
RD a[220:223],v254 offset:4096
RD a[236:239],v254 offset:8192
RD a[252:255],v254 offset:12288
s_waitcnt lgkmcnt(0)
MM v[80:83],a[192:195],v[168:171],v[136:139]
MM v[80:83],a[196:199],v[184:187],v[80:83]
MM v[80:83],a[200:203],v[200:203],v[80:83]
MM v[80:83],a[204:207],v[216:219],v[80:83]
MM v[84:87],a[220:223],v[216:219],v[136:139]
MM v[84:87],a[216:219],v[200:203],v[84:87]
MM v[84:87],a[212:215],v[184:187],v[84:87]
MM v[84:87],a[208:211],v[168:171],v[84:87]
MM v[92:95],a[208:211],v[172:175],v[140:143]
MM v[92:95],a[212:215],v[188:191],v[92:95]
MM v[92:95],a[216:219],v[204:207],v[92:95]
MM v[92:95],a[220:223],v[220:223],v[92:95]
MM v[88:91],a[204:207],v[220:223],v[140:143]
MM v[88:91],a[200:203],v[204:207],v[88:91]
MM v[88:91],a[196:199],v[188:191],v[88:91]
MM v[88:91],a[192:195],v[172:175],v[88:91]
MM v[112:115],a[224:227],v[168:171],v[136:139]
MM v[112:115],a[228:231],v[184:187],v[112:115]
MM v[112:115],a[232:235],v[200:203],v[112:115]
MM v[112:115],a[236:239],v[216:219],v[112:115]
MM v[116:119],a[252:255],v[216:219],v[136:139]
MM v[116:119],a[248:251],v[200:203],v[116:119]
MM v[116:119],a[244:247],v[184:187],v[116:119]
MM v[116:119],a[240:243],v[168:171],v[116:119]
MM v[124:127],a[240:243],v[172:175],v[140:143]
MM v[124:127],a[244:247],v[188:191],v[124:127]
MM v[124:127],a[248:251],v[204:207],v[124:127]
MM v[124:127],a[252:255],v[220:223],v[124:127]
MM v[120:123],a[236:239],v[220:223],v[140:143]
MM v[120:123],a[232:235],v[204:207],v[120:123]
MM v[120:123],a[228:231],v[188:191],v[120:123]
MM v[120:123],a[224:227],v[172:175],v[120:123]
s_nop 15
v_add_u32 v249, s75, v224
RD a[192:195],v249 offset:0
RD a[208:211],v249 offset:4096
RD a[224:227],v249 offset:8192
RD a[240:243],v249 offset:12288
v_add_u32 v250, s75, v225
RD a[196:199],v250 offset:0
RD a[212:215],v250 offset:4096
RD a[228:231],v250 offset:8192
RD a[244:247],v250 offset:12288
v_add_u32 v251, s75, v226
RD a[200:203],v251 offset:0
RD a[216:219],v251 offset:4096
RD a[232:235],v251 offset:8192
RD a[248:251],v251 offset:12288
v_add_u32 v254, s75, v227
RD a[204:207],v254 offset:0
RD a[220:223],v254 offset:4096
RD a[236:239],v254 offset:8192
RD a[252:255],v254 offset:12288
s_waitcnt lgkmcnt(0)
v_max3_f32 v249, v80, v81, v82
v_max3_f32 v251, v112, v113, v114
v_max3_f32 v249, v249, v83, v84
v_max3_f32 v251, v251, v115, v116
v_max3_f32 v249, v249, v85, v86
v_max3_f32 v251, v251, v117, v118
v_max3_f32 v249, v249, v87, v119
v_max_f32 v249, v249, v251
v_mov_b32 v251, v249
s_nop 1
v_permlane16_swap_b32 v249, v251
v_max_f32 v249, v249, v251
v_mov_b32 v251, v249
s_nop 1
v_permlane32_swap_b32 v249, v251
v_max_f32 v249, v249, v251
v_log_f32 v250, v246
s_nop 0
v_max3_f32 v249, 0, v249, v250
v_sub_f32 v250, 0, v249
EX v250,v250
v_sub_f32 v136, v136, v249
v_sub_f32 v137, v137, v249
v_sub_f32 v138, v138, v249
v_sub_f32 v139, v139, v249
)ASM"
R"ASM(
v_sub_f32 v16, v16, v249
v_sub_f32 v80, v80, v249
v_sub_f32 v17, v17, v249
v_sub_f32 v81, v81, v249
v_sub_f32 v18, v18, v249
v_sub_f32 v82, v82, v249
v_sub_f32 v19, v19, v249
v_sub_f32 v83, v83, v249
v_sub_f32 v20, v20, v249
v_sub_f32 v84, v84, v249
v_sub_f32 v21, v21, v249
v_sub_f32 v85, v85, v249
v_sub_f32 v22, v22, v249
v_sub_f32 v86, v86, v249
v_sub_f32 v23, v23, v249
v_sub_f32 v87, v87, v249
v_sub_f32 v48, v48, v249
v_sub_f32 v112, v112, v249
v_sub_f32 v49, v49, v249
v_sub_f32 v113, v113, v249
v_sub_f32 v50, v50, v249
v_sub_f32 v114, v114, v249
v_sub_f32 v51, v51, v249
v_sub_f32 v115, v115, v249
v_sub_f32 v52, v52, v249
v_sub_f32 v116, v116, v249
v_sub_f32 v53, v53, v249
v_sub_f32 v117, v117, v249
v_sub_f32 v54, v54, v249
v_sub_f32 v118, v118, v249
v_sub_f32 v55, v55, v249
v_sub_f32 v119, v119, v249
v_mul_f32 v152, v246, v250
v_mov_b32 v153, 0
v_bfe_u32 v249, v241, 16, 8
v_lshl_or_b32 v249, v249, 1, 1
v_lshlrev_b32 v249, 22, v249
v_and_b32 v241, 0xff00ffff, v241
AR v254,a16
v_sub_f32 v254, v254, v249
v_mul_f32 v254, v254, v250
AW a16,v254
AR v251,a17
v_sub_f32 v251, v251, v249
v_mul_f32 v251, v251, v250
AW a17,v251
AR v254,a18
v_sub_f32 v254, v254, v249
v_mul_f32 v254, v254, v250
AW a18,v254
AR v251,a19
v_sub_f32 v251, v251, v249
v_mul_f32 v251, v251, v250
AW a19,v251
AR v254,a20
v_sub_f32 v254, v254, v249
v_mul_f32 v254, v254, v250
AW a20,v254
AR v251,a21
v_sub_f32 v251, v251, v249
v_mul_f32 v251, v251, v250
AW a21,v251
AR v254,a22
v_sub_f32 v254, v254, v249
v_mul_f32 v254, v254, v250
AW a22,v254
AR v251,a23
v_sub_f32 v251, v251, v249
v_mul_f32 v251, v251, v250
AW a23,v251
AR v254,a48
v_sub_f32 v254, v254, v249
v_mul_f32 v254, v254, v250
AW a48,v254
AR v251,a49
v_sub_f32 v251, v251, v249
v_mul_f32 v251, v251, v250
AW a49,v251
AR v254,a50
v_sub_f32 v254, v254, v249
v_mul_f32 v254, v254, v250
AW a50,v254
AR v251,a51
v_sub_f32 v251, v251, v249
v_mul_f32 v251, v251, v250
AW a51,v251
AR v254,a52
v_sub_f32 v254, v254, v249
v_mul_f32 v254, v254, v250
AW a52,v254
AR v251,a53
v_sub_f32 v251, v251, v249
v_mul_f32 v251, v251, v250
AW a53,v251
AR v254,a54
v_sub_f32 v254, v254, v249
v_mul_f32 v254, v254, v250
AW a54,v254
AR v251,a55
v_sub_f32 v251, v251, v249
v_mul_f32 v251, v251, v250
AW a55,v251
AR v254,a80
v_sub_f32 v254, v254, v249
v_mul_f32 v254, v254, v250
AW a80,v254
AR v251,a81
v_sub_f32 v251, v251, v249
v_mul_f32 v251, v251, v250
AW a81,v251
AR v254,a82
v_sub_f32 v254, v254, v249
v_mul_f32 v254, v254, v250
AW a82,v254
AR v251,a83
v_sub_f32 v251, v251, v249
v_mul_f32 v251, v251, v250
AW a83,v251
AR v254,a84
v_sub_f32 v254, v254, v249
v_mul_f32 v254, v254, v250
AW a84,v254
AR v251,a85
v_sub_f32 v251, v251, v249
v_mul_f32 v251, v251, v250
AW a85,v251
AR v254,a86
v_sub_f32 v254, v254, v249
v_mul_f32 v254, v254, v250
AW a86,v254
AR v251,a87
v_sub_f32 v251, v251, v249
v_mul_f32 v251, v251, v250
AW a87,v251
AR v254,a112
v_sub_f32 v254, v254, v249
v_mul_f32 v254, v254, v250
AW a112,v254
AR v251,a113
v_sub_f32 v251, v251, v249
v_mul_f32 v251, v251, v250
AW a113,v251
AR v254,a114
v_sub_f32 v254, v254, v249
v_mul_f32 v254, v254, v250
AW a114,v254
AR v251,a115
v_sub_f32 v251, v251, v249
v_mul_f32 v251, v251, v250
AW a115,v251
AR v254,a116
v_sub_f32 v254, v254, v249
v_mul_f32 v254, v254, v250
AW a116,v254
AR v251,a117
v_sub_f32 v251, v251, v249
v_mul_f32 v251, v251, v250
AW a117,v251
AR v254,a118
v_sub_f32 v254, v254, v249
v_mul_f32 v254, v254, v250
AW a118,v254
AR v251,a119
v_sub_f32 v251, v251, v249
v_mul_f32 v251, v251, v250
AW a119,v251
v_max3_f32 v249, v88, v89, v90
v_max3_f32 v251, v120, v121, v122
v_max3_f32 v249, v249, v91, v92
v_max3_f32 v251, v251, v123, v124
v_max3_f32 v249, v249, v93, v94
v_max3_f32 v251, v251, v125, v126
v_max3_f32 v249, v249, v95, v127
v_max_f32 v249, v249, v251
v_mov_b32 v251, v249
s_nop 1
v_permlane16_swap_b32 v249, v251
v_max_f32 v249, v249, v251
v_mov_b32 v251, v249
s_nop 1
v_permlane32_swap_b32 v249, v251
v_max_f32 v249, v249, v251
v_log_f32 v250, v247
s_nop 0
v_max3_f32 v249, 0, v249, v250
v_sub_f32 v250, 0, v249
EX v250,v250
v_sub_f32 v140, v140, v249
v_sub_f32 v141, v141, v249
v_sub_f32 v142, v142, v249
v_sub_f32 v143, v143, v249
v_sub_f32 v24, v24, v249
v_sub_f32 v88, v88, v249
v_sub_f32 v25, v25, v249
v_sub_f32 v89, v89, v249
v_sub_f32 v26, v26, v249
v_sub_f32 v90, v90, v249
v_sub_f32 v27, v27, v249
v_sub_f32 v91, v91, v249
v_sub_f32 v28, v28, v249
v_sub_f32 v92, v92, v249
v_sub_f32 v29, v29, v249
v_sub_f32 v93, v93, v249
v_sub_f32 v30, v30, v249
v_sub_f32 v94, v94, v249
v_sub_f32 v31, v31, v249
v_sub_f32 v95, v95, v249
v_sub_f32 v56, v56, v249
v_sub_f32 v120, v120, v249
v_sub_f32 v57, v57, v249
v_sub_f32 v121, v121, v249
v_sub_f32 v58, v58, v249
v_sub_f32 v122, v122, v249
v_sub_f32 v59, v59, v249
v_sub_f32 v123, v123, v249
v_sub_f32 v60, v60, v249
v_sub_f32 v124, v124, v249
v_sub_f32 v61, v61, v249
v_sub_f32 v125, v125, v249
v_sub_f32 v62, v62, v249
v_sub_f32 v126, v126, v249
v_sub_f32 v63, v63, v249
v_sub_f32 v127, v127, v249
v_mul_f32 v156, v247, v250
v_mov_b32 v157, 0
v_bfe_u32 v249, v241, 24, 8
v_lshl_or_b32 v249, v249, 1, 1
v_lshlrev_b32 v249, 22, v249
v_and_b32 v241, 0xffffff, v241
AR v254,a24
v_sub_f32 v254, v254, v249
v_mul_f32 v254, v254, v250
AW a24,v254
AR v251,a25
v_sub_f32 v251, v251, v249
v_mul_f32 v251, v251, v250
AW a25,v251
AR v254,a26
v_sub_f32 v254, v254, v249
v_mul_f32 v254, v254, v250
AW a26,v254
AR v251,a27
v_sub_f32 v251, v251, v249
v_mul_f32 v251, v251, v250
AW a27,v251
AR v254,a28
v_sub_f32 v254, v254, v249
v_mul_f32 v254, v254, v250
AW a28,v254
AR v251,a29
v_sub_f32 v251, v251, v249
v_mul_f32 v251, v251, v250
AW a29,v251
AR v254,a30
v_sub_f32 v254, v254, v249
v_mul_f32 v254, v254, v250
AW a30,v254
AR v251,a31
v_sub_f32 v251, v251, v249
v_mul_f32 v251, v251, v250
AW a31,v251
AR v254,a56
v_sub_f32 v254, v254, v249
v_mul_f32 v254, v254, v250
AW a56,v254
AR v251,a57
v_sub_f32 v251, v251, v249
v_mul_f32 v251, v251, v250
AW a57,v251
AR v254,a58
v_sub_f32 v254, v254, v249
v_mul_f32 v254, v254, v250
AW a58,v254
AR v251,a59
v_sub_f32 v251, v251, v249
v_mul_f32 v251, v251, v250
AW a59,v251
AR v254,a60
v_sub_f32 v254, v254, v249
v_mul_f32 v254, v254, v250
AW a60,v254
AR v251,a61
v_sub_f32 v251, v251, v249
v_mul_f32 v251, v251, v250
AW a61,v251
AR v254,a62
v_sub_f32 v254, v254, v249
v_mul_f32 v254, v254, v250
AW a62,v254
AR v251,a63
v_sub_f32 v251, v251, v249
v_mul_f32 v251, v251, v250
AW a63,v251
AR v254,a88
v_sub_f32 v254, v254, v249
v_mul_f32 v254, v254, v250
AW a88,v254
AR v251,a89
v_sub_f32 v251, v251, v249
v_mul_f32 v251, v251, v250
AW a89,v251
AR v254,a90
v_sub_f32 v254, v254, v249
v_mul_f32 v254, v254, v250
AW a90,v254
AR v251,a91
v_sub_f32 v251, v251, v249
v_mul_f32 v251, v251, v250
AW a91,v251
AR v254,a92
v_sub_f32 v254, v254, v249
v_mul_f32 v254, v254, v250
AW a92,v254
AR v251,a93
v_sub_f32 v251, v251, v249
v_mul_f32 v251, v251, v250
AW a93,v251
AR v254,a94
v_sub_f32 v254, v254, v249
v_mul_f32 v254, v254, v250
AW a94,v254
AR v251,a95
v_sub_f32 v251, v251, v249
v_mul_f32 v251, v251, v250
AW a95,v251
AR v254,a120
v_sub_f32 v254, v254, v249
v_mul_f32 v254, v254, v250
AW a120,v254
AR v251,a121
v_sub_f32 v251, v251, v249
v_mul_f32 v251, v251, v250
AW a121,v251
AR v254,a122
v_sub_f32 v254, v254, v249
v_mul_f32 v254, v254, v250
AW a122,v254
AR v251,a123
v_sub_f32 v251, v251, v249
v_mul_f32 v251, v251, v250
AW a123,v251
AR v254,a124
v_sub_f32 v254, v254, v249
v_mul_f32 v254, v254, v250
AW a124,v254
AR v251,a125
v_sub_f32 v251, v251, v249
v_mul_f32 v251, v251, v250
AW a125,v251
AR v254,a126
v_sub_f32 v254, v254, v249
v_mul_f32 v254, v254, v250
AW a126,v254
AR v251,a127
v_sub_f32 v251, v251, v249
v_mul_f32 v251, v251, v250
AW a127,v251
EX v80,v80
EX v81,v81
EX v82,v82
EX v83,v83
EX v84,v84
EX v85,v85
EX v86,v86
EX v87,v87
EX v88,v88
EX v89,v89
EX v90,v90
EX v91,v91
EX v92,v92
EX v93,v93
EX v94,v94
EX v95,v95
EX v112,v112
EX v113,v113
EX v114,v114
EX v115,v115
EX v116,v116
EX v117,v117
EX v118,v118
EX v119,v119
EX v120,v120
EX v121,v121
EX v122,v122
EX v123,v123
EX v124,v124
EX v125,v125
EX v126,v126
EX v127,v127
CV v80,v80,v81
CV v81,v82,v83
CV v82,v84,v85
CV v83,v86,v87
CV v88,v88,v89
CV v89,v90,v91
CV v90,v92,v93
CV v91,v94,v95
CV v84,v112,v113
CV v85,v114,v115
CV v86,v116,v117
CV v87,v118,v119
CV v92,v120,v121
CV v93,v122,v123
CV v94,v124,v125
CV v95,v126,v127
v_smfmac_f32_16x16x64_bf16 v[152:155], v[236:239], v[80:87], v234
s_nop 0
v_smfmac_f32_16x16x64_bf16 v[156:159], v[236:239], v[88:95], v234
s_nop 9
s_branch LRET_%=
LEND_%=:
)ASM"

    :: [ka] "s"(__builtin_amdgcn_kernarg_segment_ptr()), [slds] "s"(slds), [swave] "s"(w), [sb] "s"(blockIdx.x)
    : "memory", "vcc", "scc", CLOB_V, CLOB_A, CLOB_S);
}
constexpr int DYN_LDS = 163840;

extern "C" void kernel_launch(void* const* d_in, const int* in_sizes, int n_in, void* d_out, int out_size, void* d_ws, size_t ws_size, hipStream_t stream) {
#if !defined(__HIP_DEVICE_COMPILE__)
  if (DYN_LDS) hipFuncSetAttribute((const void*)ka_fused, hipFuncAttributeMaxDynamicSharedMemorySize, DYN_LDS);
  ka_fused<<<256, 256, DYN_LDS, stream>>>(d_in[3], d_in[0], d_ws, d_in[1], d_in[6], d_in[2], d_in[4], d_in[7], d_in[8], d_in[9], d_out);
#endif
}
```
